```python
import torch

def _softmax(x, axis=-1):
    return torch.softmax(x, dim=axis)

def _gelu(x):
    return torch.nn.functional.gelu(x, approximate='tanh')

def _silu(x):
    return torch.nn.functional.silu(x)

def _sigmoid(x):
    return torch.sigmoid(x)

def _layernorm(x, axis=-1, eps=1e-05):
    m = x.mean(dim=axis, keepdim=True)
    v = x.var(dim=axis, keepdim=True, unbiased=False)
    return (x - m) / torch.sqrt(v + eps)

def _scatter_add(tgt, idx, src, axis=0):
    return tgt.clone().index_add_(axis, idx, src)

def _segment_sum(src, seg, n):
    out = torch.zeros((n,) + src.shape[1:], dtype=src.dtype)
    return out.index_add_(0, seg, src)

def _pad(x, pw):
    flat = tuple((v for pair in reversed(pw) for v in pair))
    return torch.nn.functional.pad(x, flat)
kernel_name = 'interleave_layers__ssm_conv_h3__memory_aug_recurrent_mem_tr__prompt'

def setup_inputs():

    _w = lambda a: (lambda t: t.to(torch.bfloat16) if t.is_floating_point() else t)(torch.as_tensor(a))

    def _A_setup():
        B, S, D = (1, 2048, 576)
        ks = 4
        return {'q': _w((lambda *s: torch.randn(*s, dtype=torch.bfloat16) * 0.05)(B, S, D)), 'k': _w((lambda *s: torch.randn(*s, dtype=torch.bfloat16) * 0.2)(B, S, D)), 'v': _w((lambda *s: torch.randn(*s, dtype=torch.bfloat16) * 0.2)(B, S, D)), 'shift_k': _w((lambda *s: torch.randn(*s, dtype=torch.bfloat16) * 0.2)(D, ks) * 0.5), 'ssm_k': _w((lambda *s: torch.randn(*s, dtype=torch.bfloat16) * 0.4)(D, S) * (1.0 / S ** 0.5)), 'ssm_D': _w((lambda *s: torch.randn(*s, dtype=torch.bfloat16) * 0.2)(D))}
    _A_in = _A_setup()

    def _B_setup():
        B, S, Dm = (1, 2048, 576)
        H, Dh = (32, 128)
        M, seg = (8, 32)
        seg = min(seg, S)
        S = S // seg * seg
        return {'x': _w((lambda *s: torch.randn(*s, dtype=torch.bfloat16) * 0.05)(B, S, Dm)), 'mem0': _w((lambda *s: torch.randn(*s, dtype=torch.bfloat16) * 0.2)(B, M, Dm)), 'Wq': _w((lambda *s: torch.randn(*s, dtype=torch.bfloat16) * 1.6)(Dm, H, Dh) * Dm ** (-0.5)), 'Wk': _w((lambda *s: torch.randn(*s, dtype=torch.bfloat16) * 1.6)(Dm, H, Dh) * Dm ** (-0.5)), 'Wv': _w((lambda *s: torch.randn(*s, dtype=torch.bfloat16) * 0.2)(Dm, H, Dh) * Dm ** (-0.5)), 'Wo': _w((lambda *s: torch.randn(*s, dtype=torch.bfloat16) * 0.2)(H, Dh, Dm) * (H * Dh) ** (-0.5)), 'seg_len': seg}
    _B_in = _B_setup()
    return {'A_q': _A_in['q'], 'A_k': _A_in['k'], 'A_v': _A_in['v'], 'A_shift_k': _A_in['shift_k'], 'A_ssm_k': _A_in['ssm_k'], 'A_ssm_D': _A_in['ssm_D'], 'B_x': _B_in['x'], 'B_mem0': _B_in['mem0'], 'B_Wq': _B_in['Wq'], 'B_Wk': _B_in['Wk'], 'B_Wv': _B_in['Wv'], 'B_Wo': _B_in['Wo'], 'B_seg_len': _B_in['seg_len']}

def reference(A_q, A_k, A_v, A_shift_k, A_ssm_k, A_ssm_D, B_x, B_mem0, B_Wq, B_Wk, B_Wv, B_Wo, B_seg_len):

    def _A_ref(q, k, v, shift_k, ssm_k, ssm_D):
        B, S, D = q.shape
        ks = shift_k.shape[-1]
        k_pad = _pad(k, ((0, 0), (ks - 1, 0), (0, 0)))
        k_shift = torch.zeros_like(k)
        for t in range(ks):
            k_shift = k_shift + k_pad[:, t:t + S, :] * shift_k[:, ks - 1 - t][None, None, :]
        kv = k_shift * v
        L = 2 * S
        kv_pad = _pad(kv, ((0, 0), (0, S), (0, 0)))
        kf_pad = _pad(ssm_k, ((0, 0), (0, S)))
        kv_f = (lambda x, axes=(-2, -1): torch.fft.rfft2(x.float(), dim=axes))(kv_pad[:, None, :, :], axes=(1, 2))
        kf_f = (lambda x, axes=(-2, -1): torch.fft.rfft2(x.float(), dim=axes))(kf_pad[:, None, :], axes=(1, 2))
        kf_f = (lambda x, *d: x.permute(*d))(kf_f, 1, 2, 0)
        y_f = kv_f * kf_f[None, :, :, :]
        y = (lambda x, s, axes=(-2, -1): torch.fft.irfft2(x, s=s, dim=axes).to(torch.bfloat16))(y_f, (1, L), axes=(1, 2))[:, 0, :S, :]
        y = y + kv * ssm_D[None, None, :]
        return q * y

    def _B_ref(x, mem0, Wq, Wk, Wv, Wo, seg_len):
        B, S, Dm = x.shape
        H, Dh = (Wq.shape[1], Wq.shape[2])
        M = mem0.shape[1]
        scale = Dh ** (-0.5)
        n_seg = S // seg_len
        L = M + seg_len
        idx = torch.arange(L)
        mask = (idx[:, None] < M) | (idx[:, None] >= idx[None, :])
        mem = mem0
        outs = []
        for s in range(n_seg):
            xs = x[:, s * seg_len:(s + 1) * seg_len, :].clone()
            h = torch.concatenate([mem, xs], axis=1)
            q = torch.einsum('btd,dhe->bhte', h, Wq)
            k = torch.einsum('btd,dhe->bhte', h, Wk)
            v = torch.einsum('btd,dhe->bhte', h, Wv)
            scores = torch.einsum('bhqe,bhke->bhqk', q, k) * scale
            scores = torch.where(mask[None, None], scores, -1000000000.0)
            p = _softmax(scores, axis=-1)
            o = torch.einsum('bhqk,bhke->bhqe', p, v)
            o = torch.einsum('bhte,hed->btd', o, Wo)
            mem = o[:, :M, :].clone()
            outs.append(o[:, M:, :])
        return torch.concatenate(outs, axis=1)
    x = A_q
    x = x + _A_ref(_layernorm(x, axis=-1), A_k, A_v, A_shift_k, A_ssm_k, A_ssm_D)
    x = x + _B_ref(_layernorm(x, axis=-1), B_mem0, B_Wq, B_Wk, B_Wv, B_Wo, B_seg_len)
    x = x + _A_ref(_layernorm(x, axis=-1), A_k, A_v, A_shift_k, A_ssm_k, A_ssm_D)
    x = x + _B_ref(_layernorm(x, axis=-1), B_mem0, B_Wq, B_Wk, B_Wv, B_Wo, B_seg_len)
    return x
```

```cpp
#include <hip/hip_runtime.h>
#include <hip/hip_bf16.h>
#include <stdint.h>

typedef unsigned short u16;
using bf16x8 = __attribute__((ext_vector_type(8))) short;
using bf16x4 = __attribute__((ext_vector_type(4))) short;
using f32x4 = __attribute__((ext_vector_type(4))) float;
using f32x2 = __attribute__((ext_vector_type(2))) float;
using f32x16 = __attribute__((ext_vector_type(16))) float;
using bfx2 = __attribute__((ext_vector_type(2))) __bf16;
typedef unsigned v4u __attribute__((ext_vector_type(4)));
typedef unsigned v2u __attribute__((ext_vector_type(2)));
typedef unsigned v8u __attribute__((ext_vector_type(8)));
typedef short v4s __attribute__((ext_vector_type(4)));

#define S_LEN 2048
#define DM 576
#define NH 32
#define DH 128
#define HD (NH * DH)
#define MEMR 8
#define SEG 32
#define NSEG 64

__device__ __forceinline__ float bf2f(u16 x) { return __uint_as_float(((unsigned)x) << 16); }
__device__ __forceinline__ u16 f2bf(float f) {
  unsigned u = __float_as_uint(f);
  u += 0x7FFFu + ((u >> 16) & 1u);
  return (u16)(u >> 16);
}
__device__ __forceinline__ float rbf(float f) { return bf2f(f2bf(f)); }
__device__ __forceinline__ unsigned pk2(float a, float b) { f32x2 v = {a, b}; bfx2 r = __builtin_convertvector(v, bfx2); return __builtin_bit_cast(unsigned, r); }
__device__ __forceinline__ float lo2f(unsigned u) { return __uint_as_float(u << 16); }
__device__ __forceinline__ float hi2f(unsigned u) { return __uint_as_float(u & 0xFFFF0000u); }

__device__ __forceinline__ float wave_sum(float v) {
#pragma unroll
  for (int o = 32; o > 0; o >>= 1) v += __shfl_xor(v, o, 64);
  return v;
}

#define PRE_KV 288
#define PRE_PW 576
#define PRE_PO 256
#define PRE_FILL 128
struct PreP {
  const u16* kk; const u16* vv; const u16* shift_k; u16* kvT; const u16* ssm_k; const u16* ssm_D; u16* yT;
  const u16* Wq; const u16* Wk; const u16* Wv; const u16* Wo; u16* WP; u16* WOP; u16* WOA;
  v4u* xb; const u16* mem0;
};
#define XBLK 2097152u
#define PAIR 16384u
#define SLOT 688128u
#define AGO 589824u
#define AGT 655360u
#define SG0 22
#define TP0 86
#define NXB 95
#define XB_BYTES (NXB * XBLK)
#define SLOT_AT(i) ((unsigned)((i) / 3) * XBLK + (unsigned)((i) % 3) * SLOT)
#define HT_OFF SLOT_AT(65)
#define CTAB_OFF (HT_OFF + 16384u)
#define MF_OFF (HT_OFF + 20480u)
#define TMO_OFF (MF_OFF + 9u * 128u)
#define TRC_OFF (HT_OFF + 32768u)
#define CAL_OFF (XBLK - 16384u)
#define P_KV 96u
#define P_H 112u
#define P_X3 117u
#define P_FA 121u
#define P_FB 122u
#define ARMK 12
#define PKAUX 0
#define PKST(base, idx, val) __builtin_amdgcn_raw_buffer_store_b128(val, __builtin_amdgcn_make_buffer_rsrc((void*)(base), 0, 0x7FFFFFFF, 0x00020000), (unsigned)(idx) * 16u, 0, PKAUX)
#define TRRD(ptr) __builtin_bit_cast(v2u, __builtin_amdgcn_ds_read_tr16_b64_v4i16((__attribute__((address_space(3))) v4s*)(ptr)))
__device__ __forceinline__ void pre_part(const PreP& p, int bid, u16* sh) {
  const int tid = threadIdx.x;
  if (bid < 0) {
    float (*kx)[17] = (float (*)[17])sh;
    float (*cf)[137] = (float (*)[137])(sh + 128 * 34);
    const int d0 = (-1 - bid) * 16;
    {
      const int s = tid >> 1, d = d0 + (tid & 1) * 8;
      v4u kr[4];
#pragma unroll
      for (int a = 0; a < 4; ++a) kr[a] = (s - 3 + a >= 0) ? *(const v4u*)&p.kk[(long)(s - 3 + a) * DM + d] : (v4u){0, 0, 0, 0};
      const v4u vr = *(const v4u*)&p.vv[(long)s * DM + d];
      const v4u c0 = *(const v4u*)&p.ssm_k[(long)(d0 + (tid >> 4)) * S_LEN + (tid & 15) * 8];
#pragma unroll
      for (int j = 0; j < 8; ++j) {
        const v2u ww = *(const v2u*)&p.shift_k[(d + j) * 4];
        const float w[4] = {lo2f(ww.x), hi2f(ww.x), lo2f(ww.y), hi2f(ww.y)};
        float acc = 0.f;
#pragma unroll
        for (int a = 0; a < 4; ++a) {
          const unsigned u = kr[a][j >> 1];
          acc = rbf(acc + rbf(((j & 1) ? hi2f(u) : lo2f(u)) * w[3 - a]));
        }
        const unsigned u = vr[j >> 1];
        kx[s][(tid & 1) * 8 + j] = rbf(acc * ((j & 1) ? hi2f(u) : lo2f(u)));
        cf[tid >> 4][8 + (tid & 15) * 8 + j] = (j & 1) ? hi2f(c0[j >> 1]) : lo2f(c0[j >> 1]);
      }
      if (tid < 128) cf[tid >> 3][tid & 7] = 0.f;
    }
    __syncthreads();
    const int dd = tid & 15, t0 = (tid >> 4) * 8;
    float y[8], cw[8];
#pragma unroll
    for (int i = 0; i < 8; ++i) { y[i] = 0.f; cw[i] = cf[dd][8 + t0 + i]; }
    for (int j = 0; j < t0 + 8; j += 8) {
      float kk8[8], cn[8];
#pragma unroll
      for (int jj = 0; jj < 8; ++jj) { kk8[jj] = kx[j + jj][dd]; cn[jj] = cf[dd][8 + t0 - j - jj - 1]; }
#pragma unroll
      for (int jj = 0; jj < 8; ++jj) {
#pragma unroll
        for (int i = 0; i < 8; ++i) y[i] = __builtin_fmaf(cw[(i + 8 - jj) & 7], kk8[jj], y[i]);
        cw[(7 - jj) & 7] = cn[jj];
      }
    }
    const float Dv = bf2f(p.ssm_D[d0 + dd]);
    unsigned o[4];
#pragma unroll
    for (int i = 0; i < 4; ++i)
      o[i] = (unsigned)f2bf(rbf(y[2 * i]) + rbf(kx[t0 + 2 * i][dd] * Dv)) | ((unsigned)f2bf(rbf(y[2 * i + 1]) + rbf(kx[t0 + 2 * i + 1][dd] * Dv)) << 16);
    *(v4u*)&p.yT[(long)(d0 + dd) * S_LEN + t0] = (v4u){o[0], o[1], o[2], o[3]};
    return;
  }
  if (bid < PRE_KV) {
    u16 (*tile)[72] = (u16 (*)[72])sh;
    const int d0 = (bid % 9) * 64, s0 = (bid / 9) * 64;
    const int dq = tid & 7, sr = tid >> 3;
    const int d = d0 + dq * 8;
    float w[8][4];
#pragma unroll
    for (int j = 0; j < 8; ++j) {
      v2u ww = *(const v2u*)&p.shift_k[(d + j) * 4];
      w[j][0] = lo2f(ww.x); w[j][1] = hi2f(ww.x); w[j][2] = lo2f(ww.y); w[j][3] = hi2f(ww.y);
    }
#pragma unroll
    for (int it = 0; it < 2; ++it) {
      const int s = s0 + sr + 32 * it;
      v4u kr[4];
#pragma unroll
      for (int a = 0; a < 4; ++a) kr[a] = (s - 3 + a >= 0) ? *(const v4u*)&p.kk[(long)(s - 3 + a) * DM + d] : (v4u){0, 0, 0, 0};
      v4u vr = *(const v4u*)&p.vv[(long)s * DM + d];
#pragma unroll
      for (int j = 0; j < 8; ++j) {
        float acc = 0.f;
#pragma unroll
        for (int a = 0; a < 4; ++a) {
          unsigned u = kr[a][j >> 1];
          float kx = (j & 1) ? hi2f(u) : lo2f(u);
          acc = rbf(acc + rbf(kx * w[j][3 - a]));
        }
        unsigned u = vr[j >> 1];
        tile[sr + 32 * it][dq * 8 + j] = f2bf(acc * ((j & 1) ? hi2f(u) : lo2f(u)));
      }
    }
    __syncthreads();
    {
      const int dd = tid >> 2, sq = (tid & 3) * 16;
      unsigned o[8];
#pragma unroll
      for (int i = 0; i < 8; ++i) o[i] = (unsigned)tile[sq + 2 * i][dd] | ((unsigned)tile[sq + 2 * i + 1][dd] << 16);
      *(v4u*)&p.kvT[(long)(d0 + dd) * S_LEN + s0 + sq] = (v4u){o[0], o[1], o[2], o[3]};
      *(v4u*)&p.kvT[(long)(d0 + dd) * S_LEN + s0 + sq + 8] = (v4u){o[4], o[5], o[6], o[7]};
    }
    return;
  }
  bid -= PRE_KV;
  if (bid < PRE_PW) {
    u16 (*t)[136] = (u16 (*)[136])sh;
    const int kq = bid % 6, h = (bid / 6) % NH, which = bid / (6 * NH);
    const u16* W = which == 0 ? p.Wq : (which == 1 ? p.Wk : p.Wv);
    v4u ld[6];
#pragma unroll
    for (int i = 0; i < 6; ++i) {
      int c = tid + 256 * i, r = c >> 4, q = c & 15;
      ld[i] = *reinterpret_cast<const v4u*>(&W[(long)(96 * kq + r) * HD + h * DH + q * 8]);
    }
#pragma unroll
    for (int i = 0; i < 6; ++i) {
      int c = tid + 256 * i, r = c >> 4, q = c & 15;
      *reinterpret_cast<v4u*>(&t[r][q * 8]) = ld[i];
    }
    __syncthreads();
    {
      const int lane = tid & 63, w = tid >> 6, g = lane >> 4, r = (lane >> 2) & 3, c = lane & 3;
#pragma unroll
      for (int i = 0; i < 6; ++i) {
        const int f = 6 * w + i, k3 = f >> 3, tile = f & 7;
        const int col = which < 2 ? 32 * (tile >> 1) + 8 * c + 4 * (tile & 1) : 16 * tile + 4 * c;
        const v2u lo = TRRD(&t[32 * k3 + 8 * g + r][col]), hi = TRRD(&t[32 * k3 + 8 * g + 4 + r][col]);
        PKST(p.WP, (((which * NH + h) * 8 + tile) * 18 + 3 * kq + k3) * 64 + lane, ((v4u){lo.x, lo.y, hi.x, hi.y}));
      }
    }
    return;
  }
  bid -= PRE_PW;
  if (bid < PRE_PO) {
    const int lane = tid & 63, w = tid >> 6, n = lane & 15, g = lane >> 4;
#define PRM(a, b, hi) __builtin_amdgcn_perm(b, a, (hi) ? 0x07060302u : 0x05040100u)
    if (bid < 128) {
      const int run = bid >> 1, kk = 4 * (bid & 1) + w, a = run & 1, ch = (run >> 1) & 1, h = (2 * (run >> 2) + a) ^ (kk >> 2);
      const u16* src = p.Wo + (long)(h * DH + 32 * (kk & 3) + 4 * g) * DM;
      v4u l[8]; unsigned t9[8];
#pragma unroll
      for (int x = 0; x < 8; ++x) { l[x] = *(const v4u*)(src + (x < 4 ? x : 12 + x) * DM + 256 * ch + 128 * a + 8 * n); t9[x] = src[(x < 4 ? x : 12 + x) * DM + 512 + 32 * ch + 16 * a + n]; }
#pragma unroll
      for (int t = 0; t < 8; ++t)
        PKST(p.WOA, (run * 72 + kk * 8 + t) * 64 + lane, ((v4u){PRM(l[0][t >> 1], l[1][t >> 1], t & 1), PRM(l[2][t >> 1], l[3][t >> 1], t & 1), PRM(l[4][t >> 1], l[5][t >> 1], t & 1), PRM(l[6][t >> 1], l[7][t >> 1], t & 1)}));
      PKST(p.WOA, (run * 72 + 64 + kk) * 64 + lane, ((v4u){t9[0] | (t9[1] << 16), t9[2] | (t9[3] << 16), t9[4] | (t9[5] << 16), t9[6] | (t9[7] << 16)}));
    } else {
      const int h = (bid - 128) >> 2, cw = bid & 3, hf = (n >> 1) & 1;
      const u16* src = p.Wo + (long)(h * DH + 16 * (g & 1) + 4 * (g >> 1)) * DM;
      v4u l[2][8]; unsigned t9[8];
#pragma unroll
      for (int x = 0; x < 8; ++x) {
#pragma unroll
        for (int q = 0; q < 2; ++q) l[q][x] = *(const v4u*)(src + (32 * (cw ^ w) + 8 * (x >> 2) + (x & 3)) * DM + 256 * q + 64 * cw + 32 * (n >> 3) + 16 * ((n >> 2) & 1) + 8 * (n & 1));
        t9[x] = src[(64 * (w >> 1) + 32 * (x >> 2) + 8 * (w & 1) + (x & 3)) * DM + 512 + 16 * cw + 2 * (n >> 3) + 8 * ((n >> 2) & 1) + 4 * hf + (n & 1)];
      }
#pragma unroll
      for (int f = 0; f < 8; ++f) {
        const int P = f >> 1, hr = f & 1;
        unsigned d[4];
#pragma unroll
        for (int x = 0; x < 4; ++x) { const v4u u = l[P >> 1][4 * hr + x]; d[x] = hf ? u[2 * (P & 1) + 1] : u[2 * (P & 1)]; }
        PKST(p.WOP, (((h * 4 + cw) * 18 + 4 * P + w) * 2 + hr) * 64 + lane, ((v4u){PRM(d[0], d[1], 0), PRM(d[0], d[1], 1), PRM(d[2], d[3], 0), PRM(d[2], d[3], 1)}));
      }
      PKST(p.WOP, (((h * 4 + cw) * 18 + 16) * 2 + w) * 64 + lane, ((v4u){t9[0] | (t9[1] << 16), t9[4] | (t9[5] << 16), t9[2] | (t9[3] << 16), t9[6] | (t9[7] << 16)}));
    }
    return;
  }
  bid -= PRE_PO;
  {
    const v4u ones = {0xFFFFFFFFu, 0xFFFFFFFFu, 0xFFFFFFFFu, 0xFFFFFFFFu}, zero = {0, 0, 0, 0};
    const __amdgpu_buffer_rsrc_t rx = __builtin_amdgcn_make_buffer_rsrc((void*)p.xb, 0, XB_BYTES, 0x00020000);
    if (bid >= PRE_FILL) {
      if (tid >= 64) return;
      const unsigned cb = (unsigned)(bid - PRE_FILL) * XBLK + CAL_OFF;
      __builtin_amdgcn_raw_buffer_store_b128(zero, rx, (unsigned)(tid & 1) * 8192u + (unsigned)(tid >> 1) * 256u, cb, 16);
      asm volatile("s_waitcnt vmcnt(0)" ::: "memory");
      unsigned mn[2] = {~0u, ~0u};
      for (int i = 0; i < 14; ++i) {
        const unsigned long long c0 = __builtin_amdgcn_s_memtime();
        asm volatile("s_waitcnt lgkmcnt(0)" ::: "memory");
        const unsigned v = __builtin_amdgcn_raw_buffer_load_b32(rx, 0, cb + (unsigned)(i & 1) * 8192u + (unsigned)(i >> 1) * 256u, 16);
        asm volatile("s_waitcnt vmcnt(0)" :: "v"(v) : "memory");
        const unsigned d = (unsigned)(__builtin_amdgcn_s_memtime() - c0);
        if (i & 1) mn[1] = d < mn[1] ? d : mn[1]; else mn[0] = d < mn[0] ? d : mn[0];
      }
      unsigned xcc; asm volatile("s_getreg_b32 %0, hwreg(HW_REG_XCC_ID)" : "=s"(xcc));
      if (tid == 0) __builtin_amdgcn_raw_buffer_store_b32((mn[1] < mn[0] ? 1u : 0u) ^ ((xcc >> 2) & 1u), rx, 0, CTAB_OFF + (unsigned)(bid - PRE_FILL) * 4u, 16);
      return;
    }
#define NARM (ARMK - 1u)
    for (unsigned i = bid * 256 + tid; i < 64u * 2048u + 128u; i += PRE_FILL * 256)
      __builtin_amdgcn_raw_buffer_store_b128(zero, rx, i < 131072u ? (SG0 + (i >> 11)) * XBLK + P_FA * PAIR + (i & 2047u) * 16u : MF_OFF + (i & 127u) * 16u, 0, 16);
    for (unsigned i = bid * 256 + tid; i < (SLOT - PAIR - AGO) / 16u; i += PRE_FILL * 256) {
      const unsigned o = AGO + i * 16u, f = o < AGT ? (o & 8191u) : 8192u + (o & 8191u) % 1152u, s = f >> 10, r = f & 1023u;
      v4u val = ones;
      if (s < 9u) {
        const unsigned gm = s < 8u ? r >> 8 : (r >> 6) & 3u, m3 = (r >> 4) & 3u, ga = s < 8u ? (r >> 6) & 3u : r >> 8, row = 4u * (gm & 1u) + 2u * (gm >> 1) + (m3 & 1u), half = m3 >> 1;
        const unsigned* src = (const unsigned*)(p.mem0 + row * DM + 64u * s + 16u * ga + 2u * half);
        val = (v4u){src[0], src[2], src[4], src[6]};
      }
      __builtin_amdgcn_raw_buffer_store_b128(val, rx, o, 0, 16);
    }
    constexpr unsigned S16 = (SLOT - PAIR) / 16u;
    for (unsigned i = bid * 256 + tid; i < NARM * S16; i += PRE_FILL * 256) __builtin_amdgcn_raw_buffer_store_b128(ones, rx, SLOT_AT(1u + i / S16) + (i % S16) * 16u, 0, 16);
  }
}

__global__ __launch_bounds__(256, 4) void k_pre(PreP p) {
  __shared__ __attribute__((aligned(16))) u16 sh[96 * 136];
  const int b = blockIdx.x;
  pre_part(p, b < NXB ? PRE_KV + PRE_PW + PRE_PO + PRE_FILL + b : b - NXB - 36, sh);
}

#define CREV_LEN 2176
#define CREV_STRIDE (CREV_LEN + 8)
#define CVS(k)
#define CBAR() do { asm volatile("s_waitcnt lgkmcnt(0)" ::: "memory"); __builtin_amdgcn_s_barrier(); asm volatile("" ::: "memory"); } while (0)
__device__ __forceinline__ void conv_task(const u16* __restrict__ kvT, const u16* __restrict__ ssm_k, const u16* __restrict__ ssm_D, u16* yT, u16* shb, int d0, int n, unsigned* cvp = nullptr) {
  v4u cvn = *(const v4u*)&ssm_k[(long)d0 * S_LEN + threadIdx.x * 8], kqn = *(const v4u*)&kvT[(long)d0 * S_LEN + threadIdx.x * 8];
  u16 dn = ssm_D[d0];
  for (int d = d0; d < d0 + n; ++d) {
  CVS(0);
  u16* crev = shb;
  u16* kvs = shb + 8 * CREV_STRIDE;
  u16* rev = kvs + 64 * 72;
  float (*red)[32][68] = (float (*)[32][68])crev;
  const int tid = threadIdx.x, lane = tid & 63, w = tid >> 6;
  const int X0 = 2048 + 63;
  {
    const v4u cv = cvn;
    *(v4u*)&rev[X0 - 7 - tid * 8] = (v4u){__builtin_amdgcn_alignbyte(cv.w, cv.w, 2), __builtin_amdgcn_alignbyte(cv.z, cv.z, 2), __builtin_amdgcn_alignbyte(cv.y, cv.y, 2), __builtin_amdgcn_alignbyte(cv.x, cv.x, 2)};
    if (d == d0) {
      if (tid < 64) rev[tid] = 0;
      if (tid < 80) rev[2112 + tid] = 0;
      *(v4u*)&kvs[(tid >> 3) * 72 + (tid & 7) * 8] = (v4u){0, 0, 0, 0};
    }
    const v4u kq = kqn;
    *(v4u*)&kvs[(32 + (tid >> 3)) * 72 + (tid & 7) * 8] = kq;
  }
  const v4u kq_ = kqn;
  const float Dv = bf2f(dn);
  if (d + 1 < d0 + n) { cvn = *(const v4u*)&ssm_k[(long)(d + 1) * S_LEN + tid * 8]; kqn = *(const v4u*)&kvT[(long)(d + 1) * S_LEN + tid * 8]; dn = ssm_D[d + 1]; }
  CVS(1);
  CBAR();
  CVS(2);
  {
    const int p8 = tid * 8;
    const v4u lo = *(const v4u*)&rev[p8], hi = *(const v4u*)&rev[p8 + 8];
    const int e2 = 256 + (tid >> 3), r2 = tid & 7;
    const u16* q2 = &rev[e2 * 8 + (r2 & ~1)];
    unsigned x2[5];
#pragma unroll
    for (int j = 0; j < 5; ++j) x2[j] = tid < 128 ? *(const unsigned*)&q2[2 * j] : 0u;
    const unsigned dw[8] = {lo.x, lo.y, lo.z, lo.w, hi.x, hi.y, hi.z, hi.w};
#pragma unroll
    for (int rho = 0; rho < 8; ++rho) {
      v4u o;
#pragma unroll
      for (int j = 0; j < 4; ++j) {
        const int q = (rho >> 1) + j;
        o[j] = (rho & 1) ? __builtin_amdgcn_alignbyte(dw[q + 1], dw[q], 2) : dw[q];
      }
      *(v4u*)&crev[rho * CREV_STRIDE + p8] = o;
    }
    if (tid < 128) {
      v4u o;
#pragma unroll
      for (int j = 0; j < 4; ++j) o[j] = __builtin_amdgcn_alignbyte(x2[j + 1], x2[j], (r2 & 1) * 2);
      *(v4u*)&crev[r2 * CREV_STRIDE + e2 * 8] = o;
    }
  }
  CBAR();
  CVS(3);
  const int i = lane & 31, h = lane >> 5;
  const int rho = (X0 - i) & 7;
  f32x16 acc0 = {0}, acc1 = {0};
  {
    const u16* ap = &crev[rho * CREV_STRIDE + (X0 - (512 * w + i - 8 * h) - rho)];
    const u16* bp = &kvs[(32 + i - 8 * w) * 72 + 8 * h];
    bf16x8 af[34];
#pragma unroll
    for (int q = 0; q < 6; ++q) af[q] = *reinterpret_cast<const bf16x8*>(ap - 16 * (q - 3));
    bf16x8 bf[2][4];
#pragma unroll
    for (int kk = 0; kk < 4; ++kk) bf[0][kk] = *reinterpret_cast<const bf16x8*>(bp + 16 * kk);
#pragma unroll
    for (int m = 0; m < 8; ++m) {
#pragma unroll
      for (int kk = 0; kk < 4; ++kk) {
        acc0 = __builtin_amdgcn_mfma_f32_32x32x16_bf16(af[4 * m + 3 - kk], bf[m & 1][kk], acc0, 0, 0, 0);
        if (m < 7) af[4 * m + 6 + kk] = *reinterpret_cast<const bf16x8*>(ap - 16 * (4 * m + 3 + kk));
        __builtin_amdgcn_sched_barrier(0);
        acc1 = __builtin_amdgcn_mfma_f32_32x32x16_bf16(af[4 * m + 5 - kk], bf[m & 1][kk], acc1, 0, 0, 0);
        if (m < 7) bf[(m + 1) & 1][kk] = *reinterpret_cast<const bf16x8*>(bp - (m + 1) * 72 + 16 * kk);
        __builtin_amdgcn_sched_barrier(0);
      }
    }
  }
  CVS(4);
  CBAR();
#pragma unroll
  for (int r = 0; r < 4; ++r) {
    *(f32x4*)&red[w][i][8 * r + 4 * h] = (f32x4){acc0[4 * r], acc0[4 * r + 1], acc0[4 * r + 2], acc0[4 * r + 3]};
    *(f32x4*)&red[w][i][32 + 8 * r + 4 * h] = (f32x4){acc1[4 * r], acc1[4 * r + 1], acc1[4 * r + 2], acc1[4 * r + 3]};
  }
  CBAR();
  CVS(5);
  {
    const int a = tid >> 3, i0 = (tid & 7) * 8;
    f32x4 ya = *(const f32x4*)&red[0][a][i0], yb = *(const f32x4*)&red[0][a][i0 + 4];
#pragma unroll
    for (int q = 1; q < 4; ++q) { ya += *(const f32x4*)&red[q][a][i0]; yb += *(const f32x4*)&red[q][a][i0 + 4]; }
    const float yy[8] = {ya[0], ya[1], ya[2], ya[3], yb[0], yb[1], yb[2], yb[3]};
    unsigned o[4];
#pragma unroll
    for (int j = 0; j < 4; ++j) {
      const unsigned r0 = pk2(yy[2 * j], yy[2 * j + 1]), r1 = pk2(lo2f(kq_[j]) * Dv, hi2f(kq_[j]) * Dv);
      o[j] = pk2(lo2f(r0) + lo2f(r1), hi2f(r0) + hi2f(r1));
    }
    if (tid >= 16) __builtin_amdgcn_raw_buffer_store_b128((v4u){o[0], o[1], o[2], o[3]}, __builtin_amdgcn_make_buffer_rsrc((void*)yT, 0, DM * S_LEN * 2, 0x00020000), tid * 16, d * (S_LEN * 2), 16);
  }
  CVS(6);
  CBAR();
  }
}

#define NCHW 64
#define NTOKW 192
#define PSL2 10
#define PSL 4
#define SENT 0xFFFFFFFFu
#define SPIN_CAP 300000u
struct MP {
  const u16* WP; const u16* WOP; const u16* WOA; const u16* y2; u16* out; unsigned* xb;
  const u16* xin; u16* yT; const u16* kvT; const u16* ssm_k; const u16* ssm_D; unsigned wsb; unsigned pad;
};

__device__ __forceinline__ MP getp() {
  MP r;
#if defined(__HIP_DEVICE_COMPILE__)
  const __attribute__((address_space(4))) unsigned long long* k = (const __attribute__((address_space(4))) unsigned long long*)__builtin_amdgcn_kernarg_segment_ptr();
  asm volatile("" : "+s"(k));
#define GP(T, i) ((T*)(__attribute__((address_space(1))) T*)k[i])
  r.WP = GP(const u16, 0);
  r.WOP = GP(const u16, 1);
  r.WOA = GP(const u16, 2);
  r.y2 = GP(const u16, 3);
  r.out = GP(u16, 4);
  r.xb = GP(unsigned, 5);
  r.xin = GP(const u16, 6);
  r.yT = GP(u16, 7);
  r.kvT = GP(const u16, 8);
  r.ssm_k = GP(const u16, 9);
  r.ssm_D = GP(const u16, 10);
  r.wsb = (unsigned)k[11]; r.pad = 0u;
#endif
  return r;
}
typedef __amdgpu_buffer_rsrc_t rsrc_t;
__device__ __forceinline__ rsrc_t mkr(const void* p, unsigned bytes) { return __builtin_amdgcn_make_buffer_rsrc((void*)p, 0, bytes, 0x00020000); }
__device__ __forceinline__ void st16(rsrc_t r, unsigned vo, unsigned so, v4u v) { __builtin_amdgcn_raw_buffer_store_b128(v, r, vo, so, 16); }
__device__ __forceinline__ void st8(rsrc_t r, unsigned vo, unsigned so, v2u v) { __builtin_amdgcn_raw_buffer_store_b64(v, r, vo, so, 16); }
__device__ __forceinline__ void st4(rsrc_t r, unsigned vo, unsigned so, unsigned v) { __builtin_amdgcn_raw_buffer_store_b32(v, r, vo, so, 16); }
__device__ __forceinline__ v4u ld16(rsrc_t r, unsigned vo, unsigned so) { return __builtin_amdgcn_raw_buffer_load_b128(r, vo, so, 16); }
__device__ __forceinline__ v2u ld8(rsrc_t r, unsigned vo, unsigned so) { return __builtin_amdgcn_raw_buffer_load_b64(r, vo, so, 16); }
__device__ __forceinline__ unsigned ld4(rsrc_t r, unsigned vo, unsigned so) { return __builtin_amdgcn_raw_buffer_load_b32(r, vo, so, 16); }
__device__ __forceinline__ float sum4g(float v) {
  auto r = __builtin_amdgcn_permlane16_swap(__float_as_uint(v), __float_as_uint(v), false, false);
  v = __uint_as_float(r[0]) + __uint_as_float(r[1]);
  auto q = __builtin_amdgcn_permlane32_swap(__float_as_uint(v), __float_as_uint(v), false, false);
  return __uint_as_float(q[0]) + __uint_as_float(q[1]);
}
__device__ __forceinline__ unsigned opq(unsigned x) { asm volatile("" : "+v"(x)); return x; }

__device__ __forceinline__ unsigned long long die_mask(rsrc_t X, unsigned first, int L, int lane) { return __ballot((ld4(X, (unsigned)lane * 4u, CTAB_OFF + first * 4u) & 1u) != (unsigned)L); }
#define SEGB(m, s) ((unsigned)(SG0 + (s)) * XBLK + (unsigned)(((m) >> (s)) & 1ull) * 8192u)
__device__ __forceinline__ bool spin_fail(unsigned& spins, rsrc_t F) {
  ++spins;
  if (spins > SPIN_CAP) { unsigned one = 0x40000000u | (blockIdx.x << 8) | threadIdx.x; asm volatile("" : "+v"(one)); if (__builtin_amdgcn_readfirstlane(ld4(F, 0, TMO_OFF)) == 0u) st4(F, 0, TMO_OFF, one); return true; }
  if ((spins & 1023u) == 0u) return __builtin_amdgcn_readfirstlane(ld4(F, 0, TMO_OFF)) != 0;
  return false;
}
__device__ __forceinline__ unsigned rtc() { return (unsigned)__builtin_amdgcn_s_memrealtime(); }
__device__ __forceinline__ unsigned wait_until(unsigned T) { unsigned n = rtc(); for (int i = 0; i < 8192 && (int)(n - T) < 0; ++i) n = rtc(); return n; }
__device__ __forceinline__ unsigned umax3(unsigned a, unsigned b, unsigned c) { unsigned m = a > b ? a : b; return m > c ? m : c; }
__device__ __forceinline__ float fmx(float a, float b) { float d; asm("v_max_f32 %0, %1, %2\n\ts_nop 1" : "=v"(d) : "v"(a), "v"(b)); return d; }
__device__ __forceinline__ float fmx3(float a, float b, float c) { float d; asm("v_max3_f32 %0, %1, %2, %3" : "=v"(d) : "v"(a), "v"(b), "v"(c)); return d; }
__device__ __forceinline__ float max4gx(float v) {
  auto r = __builtin_amdgcn_permlane16_swap(__float_as_uint(v), __float_as_uint(v), false, false);
  v = fmx(__uint_as_float(r[0]), __uint_as_float(r[1]));
  auto q = __builtin_amdgcn_permlane32_swap(__float_as_uint(v), __float_as_uint(v), false, false);
  return fmx(__uint_as_float(q[0]), __uint_as_float(q[1]));
}
__device__ __forceinline__ float ror8(float v) { return __uint_as_float((unsigned)__builtin_amdgcn_update_dpp(0, (int)__float_as_uint(v), 0x128, 0xf, 0xf, false)); }
__device__ __forceinline__ unsigned ror8u(unsigned v) { return (unsigned)__builtin_amdgcn_update_dpp(0, (int)v, 0x128, 0xf, 0xf, false); }
__device__ __forceinline__ unsigned wave_umax(unsigned v, int lane) {
#pragma unroll
  for (int o = 32; o > 0; o >>= 1) { unsigned x = (unsigned)__builtin_amdgcn_ds_bpermute((lane ^ o) << 2, (int)v); v = x > v ? x : v; }
  return v;
}
#define BAR() do { asm volatile("s_waitcnt lgkmcnt(0)" ::: "memory"); __builtin_amdgcn_s_barrier(); asm volatile("" ::: "memory"); } while (0)
#define SB() __builtin_amdgcn_sched_barrier(0)
#define MF "v_mfma_f32_16x16x32_bf16 "
#define C2X4(d0, d1, a0, a1, b) asm volatile( \
    MF "%0, %2, %10, 0\n\t" MF "%1, %6, %10, 0\n\t" MF "%0, %3, %11, %0\n\t" MF "%1, %7, %11, %1\n\t" \
    MF "%0, %4, %12, %0\n\t" MF "%1, %8, %12, %1\n\t" MF "%0, %5, %13, %0\n\t" MF "%1, %9, %13, %1" \
    : "=&v"(d0), "=&v"(d1) : "v"((a0)[0]), "v"((a0)[1]), "v"((a0)[2]), "v"((a0)[3]), "v"((a1)[0]), "v"((a1)[1]), "v"((a1)[2]), "v"((a1)[3]), \
      "v"((b)[0]), "v"((b)[1]), "v"((b)[2]), "v"((b)[3]))
#define C1X4(d0, a0, b) asm volatile( \
    MF "%0, %1, %5, 0\n\t" MF "%0, %2, %6, %0\n\t" MF "%0, %3, %7, %0\n\t" MF "%0, %4, %8, %0" \
    : "=&v"(d0) : "v"((a0)[0]), "v"((a0)[1]), "v"((a0)[2]), "v"((a0)[3]), "v"((b)[0]), "v"((b)[1]), "v"((b)[2]), "v"((b)[3]))
#define VM0() __builtin_amdgcn_s_waitcnt(0x0F70)
#define DL1 22
#define SCH_B0 54
#define DLL1 6
#define DLL2 4
#define DLF 24
#define SCH_PEN 0
#define DLX 4
#define LATE 10
#define TKL 28
#define UP 1
#define DN 100
#define RSL 2
#define CH_S2(x) #x
#define CH_S(x) CH_S2(x)
#define CH_ST "s_memtime s[62:63]\n\ts_waitcnt lgkmcnt(0)\n\ts_sub_u32 s67, s62, s67\n\t"
#define CH_BASE "s_memtime s[94:95]\n\ts_waitcnt lgkmcnt(0)\n\ts_mov_b32 s67, s94\n\t"
#define CH_DPC
#define CH_TOKCHK "s_cbranch_vccz LG3_%=\n\t"
#define CH_SEGC
#define CH_SG0 CH_S(SG0)
#define CH_ARMD "%[armd]"
#define CH_ARMS "%[arms]"
#define CH_SLOT "688128"
#define CH_SKIP "32768"
#define CH_DL1 CH_S(DL1)
#define CH_DLL1 CH_S(DLL1)
#define CH_DLL2 CH_S(DLL2)
#define CH_DLF CH_S(DLF)
#define CH_B0 CH_S(SCH_B0)
#define CH_PEN CH_S(SCH_PEN)
#define CH_RSL CH_S(RSL)
#define CH_UP CH_S(UP)
#define CH_DN CH_S(DN)
#define CH_DLX CH_S(DLX)
#define CH_LATE CH_S(LATE)
#define CH_DL1L "%[dl1l]"
#define CH_TKL CH_S(TKL)
#define CH_SCHW "656384"
#define CH_CAP "%[cap]"
#define CH_INW "s_sub_u32 s93, s48, 10\n\ts_cmp_lt_u32 s93, 50\n\t"
#define CH_ST1
#define CH_ST2
#define TOUCH 2
#define FDL 6
#define CH_FDL CH_S(FDL)
#define CH_TC0
#define CH_TCH
#define CH_ST3
#define CH_ST4
#define CH_TRC
#define RFL(x) ((unsigned)__builtin_amdgcn_readfirstlane((int)(x)))
#define CH_DP0
#define CH_DP1
#define CH_DP2
#define CH_DP3
#define CH_DP4
#define CH_DP5
#define CH_DP6
#define CH_DP7
#define CH_DP8
#define CH_DP9
#define CH_DP10
#define CH_DP11
#define CH_DP12
#define CH_DP13
#define CH_DP14
#define CH_DP15
#define CH_DP16
#define CH_DP17
#define CH_OPS {1, 2, 3, 4, 5, 6, 7, 8, 17, 18, 20, 22, 23, 24, 64, 65, 67, 68, 69, 70, 71, 80, 81, 82, 83, 84, 85, 86, 87, 88, 128, 129, 130, 131, 132, 133, 134, 135, 136, 144, 145, 146, 147, 149, 150, 151, 195, 209, 210, 211, 227, 241, 242, 256, 0, 16, 19, 21, 66, 72, 148, 152, 208, 240, 192, 224, 193, 225, 194, 226, 243, 257}
#define CH_ASM \
  "s_mov_b32 s84, " CH_CAP "\n\t" \
  "s_mov_b32 s83, " CH_CAP "\n\t" \
  "s_mov_b32 s68, 0\n\t" \
  "s_mov_b32 s74, 0\n\t" \
  "s_mov_b32 s76, 0\n\t" \
  "s_mov_b32 s77, 0\n\t" \
  "s_mov_b32 s78, 0\n\t" \
  "s_mov_b32 s79, 0\n\t" \
  "s_mov_b32 s56, 0xcccccccc\n\t" \
  "s_mov_b32 s57, 0xcccccccc\n\t" \
  "s_mov_b32 s58, 0x33333333\n\t" \
  "s_mov_b32 s59, 0x33333333\n\t" \
  "s_mov_b32 s88, 0x3f803f80\n\t" \
  "s_mov_b32 s89, 0x3f803f80\n\t" \
  "s_mov_b32 s66, s65\n\t" \
  "s_mov_b32 s47, s65\n\t" \
  "s_mov_b32 s72, 0\n\t" \
  "s_mov_b32 s73, 0\n\t" \
  "s_mov_b32 s67, 0\n\t" \
  "s_mul_i32 s60, s40, 36864\n\t" \
  "s_add_u32 s60, s60, 16384\n\t" \
  "v_add_u32 v70, s60, v69\n\t" \
  "v_bfe_i32 v68, v69, 5, 1\n\t" \
  "v_and_b32 v68, 0xaaaaaaaa, v68\n\t" \
  "v_or_b32 v68, 0x44444444, v68\n\t" \
  "s_add_u32 s82, s48, 0\n\t" \
  CH_SEGC \
  "s_lshr_b64 s[60:61], s[90:91], s82\n\t" \
  "s_and_b32 s60, s60, 1\n\t" \
  "s_lshl_b32 s60, s60, 13\n\t" \
  "s_add_u32 s82, s82, " CH_SG0 "\n\t" \
  "s_lshl_b32 s82, s82, 21\n\t" \
  "s_or_b32 s82, s82, s60\n\t" \
  "s_add_u32 s50, s82, s38\n\t" \
  "s_add_u32 s69, s82, s39\n\t" \
  "s_add_u32 s51, s82, s43\n\t" \
  "s_add_u32 s82, s48, 0\n\t" \
  CH_SEGC \
  "s_lshr_b64 s[60:61], s[90:91], s82\n\t" \
  "s_and_b32 s60, s60, 1\n\t" \
  "s_lshl_b32 s60, s60, 13\n\t" \
  "s_add_u32 s82, s82, " CH_SG0 "\n\t" \
  "s_lshl_b32 s82, s82, 21\n\t" \
  "s_or_b32 s82, s82, s60\n\t" \
  "s_add_u32 s50, s82, s38\n\t" \
  "s_add_u32 s69, s82, s39\n\t" \
  "s_add_u32 s82, s48, 1\n\t" \
  CH_SEGC \
  "s_lshr_b64 s[60:61], s[90:91], s82\n\t" \
  "s_and_b32 s60, s60, 1\n\t" \
  "s_lshl_b32 s60, s60, 13\n\t" \
  "s_add_u32 s82, s82, " CH_SG0 "\n\t" \
  "s_lshl_b32 s82, s82, 21\n\t" \
  "s_or_b32 s82, s82, s60\n\t" \
  "s_add_u32 s52, s82, s43\n\t" \
  "s_add_u32 s82, s48, " CH_ARMD "\n\t" \
  CH_SEGC \
  "s_lshr_b64 s[60:61], s[90:91], s82\n\t" \
  "s_and_b32 s60, s60, 1\n\t" \
  "s_lshl_b32 s60, s60, 13\n\t" \
  "s_add_u32 s82, s82, " CH_SG0 "\n\t" \
  "s_lshl_b32 s82, s82, 21\n\t" \
  "s_or_b32 s82, s82, s60\n\t" \
  "s_xor_b32 s85, s82, s45\n\t" \
  "s_cmp_lt_i32 s48, " CH_ARMS "\n\t" \
  "s_cselect_b32 s85, s52, s85\n\t" \
  "s_cmp_eq_u32 s40, 0\n\t" \
  "s_cbranch_scc1 LW0_%=\n\t" \
  "s_barrier\n\t" \
  "s_cmp_eq_u32 s40, 1\n\t" \
  "s_cbranch_scc1 LG1_%=\n\t" \
  "s_cmp_eq_u32 s40, 2\n\t" \
  "s_cbranch_scc1 LG2_%=\n\t" \
  "s_branch LG3_%=\n\t" \
  "LW0_%=:\n\t" \
  "v_lshlrev_b32 v78, 2, v69\n\t" \
  "v_and_b32 v78, 0x300, v78\n\t" \
  "v_lshrrev_b32 v79, 8, v69\n\t" \
  "v_lshl_add_u32 v78, v79, 6, v78\n\t" \
  "v_and_b32 v79, 48, v69\n\t" \
  "v_add_u32 v78, v78, v79\n\t" \
  "s_add_u32 s98, s49, s42\n\t" \
  "s_add_u32 s97, s98, 4096\n\t" \
  "s_add_u32 s99, s49, s44\n\t" \
  "v_mul_u32_u24 v77, 72, v69\n\t" \
  "s_barrier\n\t" \
  "s_memrealtime s[62:63]\n\t" \
  "s_waitcnt lgkmcnt(0)\n\t" \
  "s_add_u32 s62, s62, " CH_DLL2 "\n\t" \
  "s_sub_i32 s63, s62, s65\n\t" \
  "s_add_u32 s62, s62, " CH_DLX "\n\t" \
  "s_cmp_gt_i32 s63, 0\n\t" \
  "s_cselect_b32 s65, s62, s65\n\t" \
  "s_mov_b32 m0, 0\n\t" \
  "s_movk_i32 s60, 8192\n\t" \
  "LWT_%=:\n\t" \
  "s_memrealtime s[62:63]\n\t" \
  "s_sub_u32 s60, s60, 1\n\t" \
  "s_cselect_b32 s61, 0, -1\n\t" \
  "s_waitcnt lgkmcnt(0)\n\t" \
  "s_sub_i32 s63, s62, s65\n\t" \
  "s_and_b32 s63, s63, s61\n\t" \
  "s_cmp_lt_i32 s63, 0\n\t" \
  "s_cbranch_scc1 LWT_%=\n\t" \
  "LDMA_%=:\n\t" \
  CH_DPC \
  "buffer_load_dwordx4 v78, %[X], s98 offen offset:0 sc1 lds\n\t" \
  "buffer_load_dwordx4 v78, %[X], s98 offen offset:1024 sc1 lds\n\t" \
  "buffer_load_dwordx4 v78, %[X], s98 offen offset:2048 sc1 lds\n\t" \
  "buffer_load_dwordx4 v78, %[X], s98 offen offset:3072 sc1 lds\n\t" \
  "s_movk_i32 m0, 4096\n\t" \
  "s_mov_b32 s66, s62\n\t" \
  "buffer_load_dwordx4 v78, %[X], s97 offen offset:0 sc1 lds\n\t" \
  "buffer_load_dwordx4 v78, %[X], s97 offen offset:1024 sc1 lds\n\t" \
  "buffer_load_dwordx4 v78, %[X], s97 offen offset:2048 sc1 lds\n\t" \
  "buffer_load_dwordx4 v78, %[X], s97 offen offset:3072 sc1 lds\n\t" \
  "s_movk_i32 m0, 8192\n\t" \
  "s_nop 0\n\t" \
  "buffer_load_dwordx4 v69, %[X], s99 offen sc1 lds\n\t" \
  CH_ST1 \
  "LG0_%=:\n\t" \
  "s_barrier\n\t" \
  "buffer_load_dwordx4 v[52:55], v69, %[X], s50 offen sc1\n\t" \
  "buffer_load_dwordx4 v[56:59], v69, %[X], s50 offen offset:1024 sc1\n\t" \
  "s_add_u32 s64, s50, 16384\n\t" \
  "s_movk_i32 m0, 11264\n\t" \
  "v_lshlrev_b32 v75, 1, v69\n\t" \
  "buffer_load_dword v75, %[X], s64 offen sc1 lds\n\t" \
  "s_memrealtime s[62:63]\n\t" \
  "s_waitcnt lgkmcnt(0)\n\t" \
  "s_sub_u32 s62, s62, " CH_TKL "\n\t" \
  "s_sub_i32 s63, s62, s65\n\t" \
  "s_cmp_gt_i32 s63, 0\n\t" \
  "s_cselect_b32 s62, s62, s65\n\t" \
  "s_sub_i32 s63, s62, s47\n\t" \
  "s_cmp_gt_i32 s63, " CH_LATE "\n\t" \
  "s_cselect_b32 s71, " CH_SCHW ", s46\n\t" \
  "s_add_u32 s61, s47, " CH_LATE "\n\t" \
  "s_cmp_gt_i32 s63, " CH_LATE "\n\t" \
  "s_cselect_b32 s62, s62, s61\n\t" \
  "s_add_u32 s71, s71, s53\n\t" \
  "s_add_u32 s60, s62, s81\n\t" \
  "s_add_u32 s60, s60, " CH_DL1L "\n\t" \
  "s_lshl_b32 s60, s60, 8\n\t" \
  "s_or_b32 s60, s60, " CH_B0 "\n\t" \
  "s_not_b32 s60, s60\n\t" \
  "v_mov_b32 v76, s60\n\t" \
  "s_mov_b64 exec, 15\n\t" \
  "buffer_atomic_umin v76, v77, %[X], s71 offen sc1\n\t" \
  "s_mov_b64 exec, -1\n\t" \
  "ds_read_b128 v[44:47], v70 offset:0\n\t" \
  "ds_read_b128 v[48:51], v70 offset:1024\n\t" \
  "ds_read_b128 v[60:63], v70 offset:2048\n\t" \
  "ds_read_b128 v[64:67], v70 offset:3072\n\t" \
  "v_mov_b64 v[36:37], 0\n\t" \
  "v_mov_b64 v[38:39], 0\n\t" \
  "v_mov_b64 v[40:41], 0\n\t" \
  "v_mov_b64 v[42:43], 0\n\t" \
  "v_mov_b32 v73, 0\n\t" \
  "v_lshrrev_b32 v71, 1, v69\n\t" \
  "v_lshrrev_b32 v72, 3, v69\n\t" \
  "v_and_b32 v72, 0x78, v72\n\t" \
  "v_and_b32 v74, 0x30, v69\n\t" \
  "v_lshl_add_u32 v72, v74, 3, v72\n\t" \
  "s_add_u32 s54, s53, s36\n\t" \
  "s_add_u32 s55, s54, 524288\n\t" \
  "s_add_u32 s54, s54, 0\n\t" \
  "s_add_u32 s75, s54, 262144\n\t" \
  "s_add_u32 s80, s53, s44\n\t" \
  "s_or_b32 s87, s53, 0x1fff80\n\t" \
  "s_add_u32 s87, s87, 2097152\n\t" \
  CH_TC0 \
  "s_waitcnt vmcnt(9)\n\t" \
  "s_barrier\n\t" \
  CH_DP0 \
  "ds_read_b128 v[0:3], v69 offset:0\n\t" \
  "ds_read_b128 v[4:7], v69 offset:1024\n\t" \
  "ds_read_b128 v[8:11], v69 offset:2048\n\t" \
  "ds_read_b128 v[12:15], v69 offset:3072\n\t" \
  "s_waitcnt lgkmcnt(3)\n\t" \
  "v_smfmac_f32_16x16x64_bf16 v[36:39], v[0:3], v[44:51], v68\n\t" \
  "ds_read_b128 v[44:47], v70 offset:4096\n\t" \
  "v_smfmac_f32_16x16x64_bf16 v[40:43], v[0:3], v[60:67], v68\n\t" \
  "ds_read_b128 v[48:51], v70 offset:5120\n\t" \
  "v_max3_u32 v73, v73, v0, v2\n\t" \
  "s_waitcnt lgkmcnt(4)\n\t" \
  "v_smfmac_f32_16x16x64_bf16 v[36:39], v[4:7], a[0:7], v68\n\t" \
  "s_waitcnt vmcnt(4)\n\t" \
  "s_barrier\n\t" \
  "ds_read_b128 v[16:19], v69 offset:4096\n\t" \
  CH_DP1 \
  "v_smfmac_f32_16x16x64_bf16 v[40:43], v[4:7], a[64:71], v68\n\t" \
  "ds_read_b128 v[20:23], v69 offset:5120\n\t" \
  "v_max3_u32 v73, v73, v4, v6\n\t" \
  "s_waitcnt lgkmcnt(5)\n\t" \
  "v_smfmac_f32_16x16x64_bf16 v[36:39], v[8:11], a[8:15], v68\n\t" \
  "ds_read_b128 v[24:27], v69 offset:6144\n\t" \
  "v_max3_u32 v73, v73, v8, v10\n\t" \
  "v_smfmac_f32_16x16x64_bf16 v[40:43], v[8:11], a[72:79], v68\n\t" \
  "ds_read_b128 v[28:31], v69 offset:7168\n\t" \
  "s_waitcnt lgkmcnt(6)\n\t" \
  "v_max3_u32 v73, v73, v12, v14\n\t" \
  "v_smfmac_f32_16x16x64_bf16 v[36:39], v[12:15], a[16:23], v68\n\t" \
  "ds_read_b128 v[32:35], v69 offset:8192\n\t" \
  "s_waitcnt lgkmcnt(5)\n\t" \
  "v_smfmac_f32_16x16x64_bf16 v[40:43], v[12:15], v[44:51], v68\n\t" \
  "ds_read_b128 v[60:63], v70 offset:6144\n\t" \
  "s_waitcnt lgkmcnt(5)\n\t" \
  "v_smfmac_f32_16x16x64_bf16 v[36:39], v[16:19], a[24:31], v68\n\t" \
  "ds_read_b128 v[64:67], v70 offset:7168\n\t" \
  "v_max3_u32 v73, v73, v16, v18\n\t" \
  "v_smfmac_f32_16x16x64_bf16 v[40:43], v[16:19], a[80:87], v68\n\t" \
  "s_waitcnt lgkmcnt(5)\n\t" \
  "v_max3_u32 v73, v73, v20, v22\n\t" \
  "v_smfmac_f32_16x16x64_bf16 v[36:39], v[20:23], a[32:39], v68\n\t" \
  "s_waitcnt lgkmcnt(4)\n\t" \
  "v_max3_u32 v73, v73, v24, v26\n\t" \
  "v_smfmac_f32_16x16x64_bf16 v[36:39], v[24:27], a[40:47], v68\n\t" \
  "s_waitcnt lgkmcnt(3)\n\t" \
  "v_max3_u32 v73, v73, v28, v30\n\t" \
  "v_smfmac_f32_16x16x64_bf16 v[36:39], v[28:31], a[48:55], v68\n\t" \
  "s_waitcnt lgkmcnt(2)\n\t" \
  "v_max3_u32 v73, v73, v32, v34\n\t" \
  "v_smfmac_f32_16x16x64_bf16 v[36:39], v[32:35], a[56:63], v68\n\t" \
  "v_cmp_eq_u32 vcc, -1, v73\n\t" \
  "s_waitcnt lgkmcnt(0)\n\t" \
  "v_smfmac_f32_16x16x64_bf16 v[40:43], v[20:23], v[60:67], v68\n\t" \
  "ds_read_b128 v[60:63], v70 offset:8192\n\t" \
  "v_mov_b64 v[44:45], 0\n\t" \
  "s_cbranch_vccnz LFAIL0_%=\n\t" \
  "LGO0_%=:\n\t" \
  "v_smfmac_f32_16x16x64_bf16 v[40:43], v[24:27], a[88:95], v68\n\t" \
  "ds_read_b128 v[64:67], v70 offset:9216\n\t" \
  "v_mov_b64 v[46:47], 0\n\t" \
  "v_smfmac_f32_16x16x64_bf16 v[40:43], v[28:31], a[96:103], v68\n\t" \
  "v_add_f32 v74, v36, v38\n\t" \
  "v_smfmac_f32_16x16x64_bf16 v[40:43], v[32:35], a[104:111], v68\n\t" \
  "v_add_f32 v37, v37, v39\n\t" \
  CH_DP2 \
  "v_smfmac_f32_16x16x64_bf16 v[44:47], v[0:3], a[112:119], v68\n\t" \
  "v_cvt_pk_bf16_f32 v74, v74, v37\n\t" \
  "v_smfmac_f32_16x16x64_bf16 v[44:47], v[4:7], a[120:127], v68\n\t" \
  "v_add_f32 v75, v40, v42\n\t" \
  "s_waitcnt lgkmcnt(0)\n\t" \
  "v_smfmac_f32_16x16x64_bf16 v[44:47], v[8:11], v[60:67], v68\n\t" \
  "ds_read_b128 v[60:63], v70 offset:10240\n\t" \
  "v_add_f32 v41, v41, v43\n\t" \
  "v_cvt_pk_bf16_f32 v75, v75, v41\n\t" \
  "v_smfmac_f32_16x16x64_bf16 v[44:47], v[12:15], a[128:135], v68\n\t" \
  "ds_write2st64_b32 v71, v74, v75 offset0:36 offset1:38\n\t" \
  "v_mov_b64 v[48:49], 0\n\t" \
  "v_smfmac_f32_16x16x64_bf16 v[44:47], v[16:19], a[136:143], v68\n\t" \
  "ds_read_b128 v[64:67], v70 offset:11264\n\t" \
  "v_mov_b64 v[50:51], 0\n\t" \
  "v_smfmac_f32_16x16x64_bf16 v[44:47], v[20:23], a[144:151], v68\n\t" \
  "v_smfmac_f32_16x16x64_bf16 v[44:47], v[24:27], a[152:159], v68\n\t" \
  "v_smfmac_f32_16x16x64_bf16 v[44:47], v[28:31], a[160:167], v68\n\t" \
  "s_waitcnt lgkmcnt(0)\n\t" \
  "v_smfmac_f32_16x16x64_bf16 v[44:47], v[32:35], v[60:67], v68\n\t" \
  "buffer_load_dwordx4 v[60:63], v69, %[X], s64 offen sc1\n\t" \
  "s_barrier\n\t" \
  "ds_read_b64_tr_b16 v[76:77], v72 offset:9216\n\t" \
  CH_DP3 \
  "v_smfmac_f32_16x16x64_bf16 v[48:51], v[0:3], a[168:175], v68\n\t" \
  "ds_read_b64_tr_b16 v[78:79], v72 offset:9728\n\t" \
  "v_smfmac_f32_16x16x64_bf16 v[48:51], v[4:7], a[176:183], v68\n\t" \
  "v_add_f32 v74, v44, v46\n\t" \
  "v_smfmac_f32_16x16x64_bf16 v[48:51], v[8:11], a[184:191], v68\n\t" \
  "v_add_f32 v45, v45, v47\n\t" \
  "v_smfmac_f32_16x16x64_bf16 v[48:51], v[12:15], a[192:199], v68\n\t" \
  "v_cvt_pk_bf16_f32 v74, v74, v45\n\t" \
  "s_waitcnt vmcnt(4)\n\t" \
  "s_waitcnt lgkmcnt(0)\n\t" \
  "v_mfma_f32_16x16x32_bf16 v[36:39], v[52:55], v[76:79], 0\n\t" \
  "buffer_load_dwordx4 v[64:67], v69, %[X], s64 offen offset:1024 sc1\n\t" \
  "s_waitcnt vmcnt(4)\n\t" \
  "v_mfma_f32_16x16x32_bf16 v[40:43], v[56:59], v[76:79], 0\n\t" \
  "v_mov_b64 v[52:53], 0\n\t" \
  "v_mov_b64 v[54:55], 0\n\t" \
  "v_smfmac_f32_16x16x64_bf16 v[48:51], v[16:19], a[200:207], v68\n\t" \
  "v_mov_b64 v[56:57], 0\n\t" \
  "v_smfmac_f32_16x16x64_bf16 v[48:51], v[20:23], a[208:215], v68\n\t" \
  "v_add_f32_dpp v36, v40, v36 quad_perm:[2,3,0,1] row_mask:0xf bank_mask:0xf\n\t" \
  "v_smfmac_f32_16x16x64_bf16 v[48:51], v[24:27], a[216:223], v68\n\t" \
  "v_add_f32_dpp v37, v41, v37 quad_perm:[2,3,0,1] row_mask:0xf bank_mask:0xf\n\t" \
  "v_smfmac_f32_16x16x64_bf16 v[48:51], v[28:31], a[224:231], v68\n\t" \
  "v_add_f32_dpp v38, v42, v38 quad_perm:[2,3,0,1] row_mask:0xf bank_mask:0xf\n\t" \
  "v_smfmac_f32_16x16x64_bf16 v[48:51], v[32:35], a[232:239], v68\n\t" \
  "v_add_f32_dpp v39, v43, v39 quad_perm:[2,3,0,1] row_mask:0xf bank_mask:0xf\n\t" \
  CH_DP4 \
  "v_smfmac_f32_16x16x64_bf16 v[52:55], v[0:3], a[240:247], v68\n\t" \
  "s_mov_b64 exec, s[58:59]\n\t" \
  "ds_write_b128 v69, v[36:39] offset:2048\n\t" \
  "s_mov_b64 exec, -1\n\t" \
  "v_mov_b64 v[58:59], 0\n\t" \
  "v_smfmac_f32_16x16x64_bf16 v[52:55], v[4:7], a[248:255], v68\n\t" \
  "buffer_load_dword off, %[X], s87 sc1 lds\n\t" \
  "v_add_f32 v75, v48, v50\n\t" \
  "v_add_f32 v49, v49, v51\n\t" \
  "v_cvt_pk_bf16_f32 v75, v75, v49\n\t" \
  "v_smfmac_f32_16x16x64_bf16 v[56:59], v[0:3], v[136:143], v68\n\t" \
  "ds_write2st64_b32 v71, v74, v75 offset0:0 offset1:2\n\t" \
  "v_smfmac_f32_16x16x64_bf16 v[56:59], v[4:7], v[144:151], v68\n\t" \
  "ds_read_b64_tr_b16 v[0:1], v72 offset:10240\n\t" \
  "ds_read_b64_tr_b16 v[2:3], v72 offset:10752\n\t" \
  "v_smfmac_f32_16x16x64_bf16 v[52:55], v[8:11], v[80:87], v68\n\t" \
  "ds_read_b128 v[36:39], v70 offset:12288\n\t" \
  "v_smfmac_f32_16x16x64_bf16 v[56:59], v[8:11], v[152:159], v68\n\t" \
  "ds_read_b128 v[40:43], v70 offset:13312\n\t" \
  "v_smfmac_f32_16x16x64_bf16 v[52:55], v[12:15], v[88:95], v68\n\t" \
  "s_waitcnt lgkmcnt(4)\n\t" \
  "s_barrier\n\t" \
  CH_DP5 \
  "ds_read_b64_tr_b16 v[44:45], v72 offset:0\n\t" \
  "ds_read_b64_tr_b16 v[46:47], v72 offset:512\n\t" \
  "v_smfmac_f32_16x16x64_bf16 v[56:59], v[12:15], v[160:167], v68\n\t" \
  "ds_read_b64_tr_b16 v[48:49], v72 offset:1024\n\t" \
  "ds_read_b64_tr_b16 v[50:51], v72 offset:1536\n\t" \
  "v_smfmac_f32_16x16x64_bf16 v[52:55], v[16:19], v[96:103], v68\n\t" \
  "ds_read_b128 v[4:7], v69 offset:2048\n\t" \
  "s_waitcnt lgkmcnt(5)\n\t" \
  "v_smfmac_f32_16x16x64_bf16 v[56:59], v[16:19], v[36:43], v68\n\t" \
  "ds_read_b128 v[8:11], v69 offset:3072\n\t" \
  "s_waitcnt lgkmcnt(4)\n\t" \
  "v_mfma_f32_16x16x32_bf16 v[12:15], v[44:47], v[76:79], 0\n\t" \
  "ds_read_b128 v[36:39], v70 offset:14336\n\t" \
  "s_waitcnt lgkmcnt(3)\n\t" \
  "v_mfma_f32_16x16x32_bf16 v[12:15], v[48:51], v[0:3], v[12:15]\n\t" \
  "ds_read_b128 v[40:43], v70 offset:15360\n\t" \
  "v_smfmac_f32_16x16x64_bf16 v[52:55], v[20:23], v[104:111], v68\n\t" \
  "s_waitcnt lgkmcnt(2)\n\t" \
  "v_add_f32 v4, v4, v8\n\t" \
  "v_add_f32 v5, v5, v9\n\t" \
  "v_add_f32 v6, v6, v10\n\t" \
  CH_DP6 \
  "v_cndmask_b32 v78, v13, v14, s[56:57]\n\t" \
  "v_cndmask_b32 v76, v12, v15, s[56:57]\n\t" \
  "v_add_f32 v7, v7, v11\n\t" \
  "v_max3_f32 v77, v4, v5, v6\n\t" \
  "v_add_f32_dpp v76, v78, v76 quad_perm:[2,3,0,1] row_mask:0xf bank_mask:0xf\n\t" \
  "v_max3_f32 v77, v77, v7, v76\n\t" \
  "v_smfmac_f32_16x16x64_bf16 v[56:59], v[20:23], v[168:175], v68\n\t" \
  "ds_read_b128 v[44:47], v70 offset:16384\n\t" \
  "v_max_f32_dpp v77, v77, v77 quad_perm:[2,3,0,1] row_mask:0xf bank_mask:0xf\n\t" \
  "v_mov_b32 v78, v77\n\t" \
  "v_smfmac_f32_16x16x64_bf16 v[52:55], v[24:27], v[112:119], v68\n\t" \
  "ds_read_b128 v[48:51], v70 offset:17408\n\t" \
  "v_permlane16_swap_b32 v77, v78\n\t" \
  "v_max_f32 v77, v77, v78\n\t" \
  "v_mov_b32 v78, v77\n\t" \
  "v_smfmac_f32_16x16x64_bf16 v[52:55], v[28:31], v[120:127], v68\n\t" \
  "ds_read_b128 v[16:19], v70 offset:18432\n\t" \
  "v_permlane32_swap_b32 v77, v78\n\t" \
  "v_max_f32 v77, v77, v78\n\t" \
  "v_smfmac_f32_16x16x64_bf16 v[52:55], v[32:35], v[128:135], v68\n\t" \
  "ds_read_b128 v[20:23], v70 offset:19456\n\t" \
  "v_mul_f32 v77, 0xbe0293ee, v77\n\t" \
  CH_DP7 \
  "v_smfmac_f32_16x16x64_bf16 v[56:59], v[24:27], v[176:183], v68\n\t" \
  "v_fmamk_f32 v4, v4, 0x3e0293ee, v77\n\t" \
  "v_smfmac_f32_16x16x64_bf16 v[56:59], v[28:31], v[184:191], v68\n\t" \
  "ds_read_b128 v[24:27], v70 offset:22528\n\t" \
  "v_fmamk_f32 v5, v5, 0x3e0293ee, v77\n\t" \
  "s_waitcnt lgkmcnt(5)\n\t" \
  "v_smfmac_f32_16x16x64_bf16 v[56:59], v[32:35], v[36:43], v68\n\t" \
  "ds_read_b128 v[28:31], v70 offset:23552\n\t" \
  "v_fmamk_f32 v6, v6, 0x3e0293ee, v77\n\t" \
  "v_fmamk_f32 v7, v7, 0x3e0293ee, v77\n\t" \
  "v_fmamk_f32 v76, v76, 0x3e0293ee, v77\n\t" \
  "v_exp_f32 v4, v4\n\t" \
  "v_exp_f32 v5, v5\n\t" \
  "v_exp_f32 v6, v6\n\t" \
  "v_exp_f32 v7, v7\n\t" \
  "v_cvt_pk_bf16_f32 v4, v4, v5\n\t" \
  "v_exp_f32 v76, v76\n\t" \
  "v_cvt_pk_bf16_f32 v5, v6, v7\n\t" \
  "v_mov_b32_dpp v12, v4 quad_perm:[0,1,0,1] row_mask:0xf bank_mask:0xf\n\t" \
  "v_mov_b32_dpp v14, v4 quad_perm:[2,3,2,3] row_mask:0xf bank_mask:0xf\n\t" \
  "v_mov_b32_dpp v13, v5 quad_perm:[0,1,0,1] row_mask:0xf bank_mask:0xf\n\t" \
  "v_mov_b32_dpp v15, v5 quad_perm:[2,3,2,3] row_mask:0xf bank_mask:0xf\n\t" \
  "v_add_f32 v52, v52, v54\n\t" \
  "v_add_f32 v53, v53, v55\n\t" \
  "s_waitcnt vmcnt(2)\n\t" \
  CH_DP8 \
  "v_mfma_f32_16x16x32_bf16 v[0:3], v[60:63], v[12:15], 0\n\t" \
  "ds_read_b128 v[36:39], v70 offset:20480\n\t" \
  "v_add_f32 v56, v56, v58\n\t" \
  "v_add_f32 v57, v57, v59\n\t" \
  "v_cvt_pk_bf16_f32 v52, v52, v53\n\t" \
  "v_cvt_pk_bf16_f32 v53, v56, v57\n\t" \
  "s_waitcnt vmcnt(1)\n\t" \
  "v_mfma_f32_16x16x32_bf16 v[4:7], v[64:67], v[12:15], 0\n\t" \
  "buffer_store_dwordx2 v[52:53], v71, %[X], s69 offen offset:2048 sc1\n\t" \
  "ds_read_b128 v[40:43], v70 offset:21504\n\t" \
  "v_mov_b64 v[56:57], s[88:89]\n\t" \
  "v_mov_b64 v[58:59], s[88:89]\n\t" \
  "v_mov_b32_dpp v78, v76 quad_perm:[0,1,0,1] row_mask:0xf bank_mask:0xf\n\t" \
  "v_mov_b32_dpp v79, v76 quad_perm:[2,3,2,3] row_mask:0xf bank_mask:0xf\n\t" \
  "v_mfma_f32_16x16x32_bf16 v[32:35], v[56:59], v[12:15], 0\n\t" \
  "ds_read_b128 v[60:63], v70 offset:24576\n\t" \
  "v_cvt_pk_bf16_f32 v11, v78, v79\n\t" \
  "v_cvt_pk_bf16_f32 v8, v78, v79\n\t" \
  "v_mov_b64 v[54:55], 0\n\t" \
  "v_mov_b32 v9, 0\n\t" \
  "v_mov_b32 v10, 0\n\t" \
  "v_mov_b64 v[14:15], 0\n\t" \
  "s_cmp_eq_u32 s48, 63\n\t" \
  "v_mfma_f32_16x16x32_bf16 v[32:35], v[54:57], v[8:11], v[32:35]\n\t" \
  "buffer_load_dword v75, off, %[X], s80 offset:1024 sc1\n\t" \
  "v_mfma_f32_16x16x32_bf16 v[0:3], v[52:55], v[8:11], v[0:3]\n\t" \
  "ds_read_b128 v[64:67], v70 offset:25600\n\t" \
  "v_mov_b64 v[56:57], 0\n\t" \
  "v_mov_b64 v[58:59], 0\n\t" \
  "v_mfma_f32_16x16x32_bf16 v[4:7], v[52:55], v[10:13], v[4:7]\n\t" \
  "s_cbranch_scc1 LLAST0_%=\n\t" \
  "v_rcp_f32 v74, v32\n\t" \
  "v_mov_b64 v[12:13], 0\n\t" \
  "v_mov_b32 v8, 0\n\t" \
  "v_mul_f32 v0, v0, v74\n\t" \
  "v_mul_f32 v1, v1, v74\n\t" \
  "v_mul_f32 v2, v2, v74\n\t" \
  "v_mul_f32 v3, v3, v74\n\t" \
  "v_cvt_pk_bf16_f32 v76, v0, v1\n\t" \
  "v_cvt_pk_bf16_f32 v77, v2, v3\n\t" \
  "v_pk_mul_f32 v[4:5], v[4:5], v[74:75] op_sel_hi:[1,0]\n\t" \
  "v_pk_mul_f32 v[6:7], v[6:7], v[74:75] op_sel_hi:[1,0]\n\t" \
  "v_cvt_pk_bf16_f32 v78, v4, v5\n\t" \
  "v_cvt_pk_bf16_f32 v79, v6, v7\n\t" \
  "ds_write_b128 v69, v[76:79] offset:4096\n\t" \
  CH_DP9 \
  "s_waitcnt lgkmcnt(9)\n\t" \
  "v_smfmac_f32_16x16x64_bf16 v[12:15], v[76:79], v[44:51], v68\n\t" \
  "ds_read_b128 v[44:47], v70 offset:26624\n\t" \
  "v_mov_b32 v11, 0\n\t" \
  "s_waitcnt lgkmcnt(8)\n\t" \
  "v_smfmac_f32_16x16x64_bf16 v[56:59], v[76:79], v[16:23], v68\n\t" \
  "ds_read_b128 v[48:51], v70 offset:27648\n\t" \
  "v_mov_b64 v[52:53], 0\n\t" \
  "s_waitcnt lgkmcnt(5)\n\t" \
  "v_smfmac_f32_16x16x64_bf16 v[8:11], v[76:79], v[36:43], v68\n\t" \
  "ds_read_b128 v[16:19], v70 offset:28672\n\t" \
  "v_and_b32 v73, 32, v69\n\t" \
  "v_smfmac_f32_16x16x64_bf16 v[52:55], v[76:79], v[24:31], v68\n\t" \
  "s_waitcnt lgkmcnt(3)\n\t" \
  "s_barrier\n\t" \
  CH_DP10 \
  "ds_read_b128 v[0:3], v69 offset:5120\n\t" \
  "ds_read_b128 v[4:7], v69 offset:6144\n\t" \
  "v_lshl_add_u32 v73, v73, 5, v69\n\t" \
  "v_lshrrev_b32 v71, 7, v69\n\t" \
  "v_and_b32 v74, 0x70, v69\n\t" \
  "v_lshl_add_u32 v71, v71, 14, v74\n\t" \
  "v_bfe_u32 v72, v69, 2, 7\n\t" \
  "v_and_b32 v74, 0x200, v69\n\t" \
  "s_waitcnt lgkmcnt(1)\n\t" \
  "v_smfmac_f32_16x16x64_bf16 v[8:11], v[0:3], v[60:67], v68\n\t" \
  "ds_read_b128 v[20:23], v70 offset:29696\n\t" \
  "v_mov_b64 v[76:77], 0\n\t" \
  "v_smfmac_f32_16x16x64_bf16 v[12:15], v[0:3], v[200:207], v68\n\t" \
  "ds_read_b128 v[32:35], v69 offset:7168\n\t" \
  "v_mov_b64 v[78:79], 0\n\t" \
  "v_smfmac_f32_16x16x64_bf16 v[52:55], v[0:3], v[44:51], v68\n\t" \
  "ds_read_b128 v[24:27], v70 offset:30720\n\t" \
  "v_lshl_add_u32 v72, v74, 3, v72\n\t" \
  "v_smfmac_f32_16x16x64_bf16 v[56:59], v[0:3], v[232:239], v68\n\t" \
  "ds_read_b128 v[28:31], v70 offset:31744\n\t" \
  "s_waitcnt lgkmcnt(4)\n\t" \
  "v_smfmac_f32_16x16x64_bf16 v[12:15], v[4:7], v[208:215], v68\n\t" \
  "ds_read_b128 v[60:63], v70 offset:32768\n\t" \
  "s_waitcnt lgkmcnt(4)\n\t" \
  "v_smfmac_f32_16x16x64_bf16 v[8:11], v[4:7], v[16:23], v68\n\t" \
  "ds_read_b128 v[64:67], v70 offset:33792\n\t" \
  "s_waitcnt lgkmcnt(4)\n\t" \
  "v_smfmac_f32_16x16x64_bf16 v[8:11], v[32:35], v[192:199], v68\n\t" \
  "ds_read_b128 v[36:39], v73 offset:4096\n\t" \
  "v_smfmac_f32_16x16x64_bf16 v[12:15], v[32:35], v[216:223], v68\n\t" \
  "ds_read_b128 v[40:43], v73 offset:6144\n\t" \
  "s_waitcnt lgkmcnt(4)\n\t" \
  "v_smfmac_f32_16x16x64_bf16 v[52:55], v[4:7], v[24:31], v68\n\t" \
  "ds_read_b128 v[44:47], v70 offset:34816\n\t" \
  "v_cvt_pk_bf16_f32 v8, v8, v9\n\t" \
  "v_smfmac_f32_16x16x64_bf16 v[52:55], v[32:35], v[224:231], v68\n\t" \
  "ds_read_b128 v[48:51], v70 offset:35840\n\t" \
  "v_cvt_pk_bf16_f32 v9, v10, v11\n\t" \
  "v_smfmac_f32_16x16x64_bf16 v[56:59], v[4:7], v[240:247], v68\n\t" \
  "v_cvt_pk_bf16_f32 v10, v12, v13\n\t" \
  "s_waitcnt lgkmcnt(4)\n\t" \
  "v_smfmac_f32_16x16x64_bf16 v[56:59], v[32:35], v[60:67], v68\n\t" \
  "v_cvt_pk_bf16_f32 v11, v14, v15\n\t" \
  "s_waitcnt lgkmcnt(3)\n\t" \
  "v_smfmac_f32_16x16x64_bf16 v[76:79], v[36:39], v[248:255], v68\n\t" \
  "buffer_store_dwordx4 v[8:11], v71, %[X], s54 offen sc1\n\t" \
  CH_DP11 \
  "v_cvt_pk_bf16_f32 v52, v52, v53\n\t" \
  "s_waitcnt lgkmcnt(0)\n\t" \
  "v_smfmac_f32_16x16x64_bf16 v[76:79], v[40:43], v[44:51], v68\n\t" \
  "v_cvt_pk_bf16_f32 v53, v54, v55\n\t" \
  "v_cvt_pk_bf16_f32 v54, v56, v57\n\t" \
  "v_cvt_pk_bf16_f32 v55, v58, v59\n\t" \
  "buffer_store_dwordx4 v[52:55], v71, %[X], s75 offen sc1\n\t" \
  "s_nop 2\n\t" \
  "v_add_f32 v76, v76, v78\n\t" \
  "v_add_f32 v77, v77, v79\n\t" \
  "v_cvt_pk_bf16_f32 v76, v76, v77\n\t" \
  "buffer_store_dword v76, v72, %[X], s55 offen sc1\n\t" \
  CH_DP12 \
  "s_memrealtime s[70:71]\n\t" \
  "s_waitcnt vmcnt(3)\n\t" \
  "v_readfirstlane_b32 s65, v75\n\t" \
  "s_barrier\n\t" \
  "s_waitcnt lgkmcnt(0)\n\t" \
  "s_not_b32 s65, s65\n\t" \
  "s_lshl_b32 s61, s70, 8\n\t" \
  "s_andn2_b32 s64, s65, 0xff\n\t" \
  "s_sub_u32 s64, s64, s61\n\t" \
  "s_ashr_i32 s64, s64, 8\n\t" \
  "s_cmp_eq_u32 s65, 0\n\t" \
  "s_cselect_b32 s64, " CH_DLF ", s64\n\t" \
  "s_cselect_b32 s65, " CH_B0 ", s65\n\t" \
  "s_add_u32 s47, s64, s70\n\t" \
  "s_max_i32 s64, s64, " CH_DLL1 "\n\t" \
  "s_add_u32 s64, s64, s70\n\t" \
  CH_ST2 \
  "s_add_u32 s55, s64, " CH_FDL "\n\t" \
  "s_movk_i32 s60, 8192\n\t" \
  "FWT_%=:\n\t" \
  "s_memrealtime s[72:73]\n\t" \
  "s_sub_u32 s60, s60, 1\n\t" \
  "s_cselect_b32 s61, 0, -1\n\t" \
  "s_waitcnt lgkmcnt(0)\n\t" \
  "s_sub_i32 s73, s72, s55\n\t" \
  "s_and_b32 s73, s73, s61\n\t" \
  "s_cmp_lt_i32 s73, 0\n\t" \
  "s_cbranch_scc1 FWT_%=\n\t" \
  "s_mov_b64 exec, 1\n\t" \
  "buffer_store_dword v68, off, %[X], s51 offset:4064 sc1\n\t" \
  "s_mov_b64 exec, -1\n\t" \
  CH_TCH \
  "s_and_b32 s65, s65, 0xff\n\t" \
  "s_add_u32 s47, s47, s65\n\t" \
  "s_add_u32 s65, s65, s64\n\t" \
  "s_sub_u32 s60, s70, s66\n\t" \
  "s_add_u32 s61, s81, " CH_UP "\n\t" \
  "s_min_u32 s60, s60, s61\n\t" \
  "s_sub_u32 s61, s81, " CH_DN "\n\t" \
  "s_max_u32 s60, s60, s61\n\t" \
  "s_cmp_eq_u32 s68, 0\n\t" \
  "s_cselect_b32 s81, s60, s81\n\t" \
  CH_TRC \
  "s_add_u32 s48, s48, 1\n\t" \
  "s_mov_b32 s49, s53\n\t" \
  "s_add_u32 s53, s53, " CH_SLOT "\n\t" \
  "s_add_u32 s60, s53, " CH_SKIP "\n\t" \
  "s_lshr_b32 s61, s96, 1\n\t" \
  "s_add_u32 s86, s86, 1\n\t" \
  "s_cmp_eq_u32 s86, 3\n\t" \
  "s_cselect_b32 s53, s60, s53\n\t" \
  "s_cselect_b32 s96, s61, s96\n\t" \
  "s_cselect_b32 s86, 0, s86\n\t" \
  "s_andn2_b32 s53, s53, 0x2000\n\t" \
  "s_and_b32 s60, s96, 1\n\t" \
  "s_lshl_b32 s60, s60, 13\n\t" \
  "s_or_b32 s53, s53, s60\n\t" \
  "s_mov_b32 s68, 0\n\t" \
  "s_add_u32 s82, s48, 0\n\t" \
  CH_SEGC \
  "s_lshr_b64 s[60:61], s[90:91], s82\n\t" \
  "s_and_b32 s60, s60, 1\n\t" \
  "s_lshl_b32 s60, s60, 13\n\t" \
  "s_add_u32 s82, s82, " CH_SG0 "\n\t" \
  "s_lshl_b32 s82, s82, 21\n\t" \
  "s_or_b32 s82, s82, s60\n\t" \
  "s_add_u32 s50, s82, s38\n\t" \
  "s_add_u32 s69, s82, s39\n\t" \
  "s_add_u32 s51, s82, s43\n\t" \
  "s_branch LW0_%=\n\t" \
  "LFAIL0_%=:\n\t" \
  "s_add_u32 s68, s68, 1\n\t" \
  "s_waitcnt lgkmcnt(0)\n\t" \
  "s_cmp_gt_u32 s68, s83\n\t" \
  "s_cbranch_scc1 LGV0_%=\n\t" \
  "s_barrier\n\t" \
  "s_memrealtime s[62:63]\n\t" \
  "s_mov_b32 m0, 0\n\t" \
  "s_waitcnt lgkmcnt(0)\n\t" \
  "s_add_u32 s65, s62, " CH_PEN "\n\t" \
  "s_branch LDMA_%=\n\t" \
  "LGV0_%=:\n\t" \
  "s_mov_b32 s83, 0\n\t" \
  "s_lshl_b32 s60, s36, 4\n\t" \
  "s_or_b32 s60, s60, s48\n\t" \
  "s_or_b32 s60, s60, 805306368\n\t" \
  "v_mov_b32 v46, s60\n\t" \
  "v_mov_b32 v47, 0\n\t" \
  "s_mov_b64 exec, 1\n\t" \
  "buffer_atomic_cmpswap v[46:47], off, %[X], s92 sc1\n\t" \
  "s_mov_b64 exec, -1\n\t" \
  "s_branch LGO0_%=\n\t" \
  "LLAST0_%=:\n\t" \
  "s_waitcnt vmcnt(0) lgkmcnt(0)\n\t" \
  "s_barrier\n\t" \
  "s_mov_b64 exec, 1\n\t" \
  "buffer_store_dword v68, off, %[X], s51 offset:4064 sc1\n\t" \
  "s_mov_b64 exec, -1\n\t" \
  "s_branch LEND_%=\n\t" \
  "LG1_%=:\n\t" \
  "s_barrier\n\t" \
  "buffer_load_dwordx4 v[52:55], v69, %[X], s50 offen sc1\n\t" \
  "buffer_load_dwordx4 v[56:59], v69, %[X], s50 offen offset:1024 sc1\n\t" \
  "s_add_u32 s64, s50, 14336\n\t" \
  "s_movk_i32 m0, 11264\n\t" \
  "v_lshlrev_b32 v75, 1, v69\n\t" \
  "buffer_load_dword v75, %[X], s64 offen sc1 lds\n\t" \
  "ds_read_b128 v[44:47], v70 offset:0\n\t" \
  "ds_read_b128 v[48:51], v70 offset:1024\n\t" \
  "ds_read_b128 v[60:63], v70 offset:2048\n\t" \
  "ds_read_b128 v[64:67], v70 offset:3072\n\t" \
  "v_mov_b64 v[36:37], 0\n\t" \
  "v_mov_b64 v[38:39], 0\n\t" \
  "v_mov_b64 v[40:41], 0\n\t" \
  "v_mov_b64 v[42:43], 0\n\t" \
  "v_mov_b32 v73, 0\n\t" \
  "v_lshrrev_b32 v71, 1, v69\n\t" \
  "v_add_u32 v71, 4, v71\n\t" \
  "v_lshrrev_b32 v72, 3, v69\n\t" \
  "v_and_b32 v72, 0x78, v72\n\t" \
  "v_and_b32 v74, 0x30, v69\n\t" \
  "v_lshl_add_u32 v72, v74, 3, v72\n\t" \
  "s_add_u32 s54, s53, s36\n\t" \
  "s_add_u32 s55, s54, 540672\n\t" \
  "s_add_u32 s54, s54, 4096\n\t" \
  "s_add_u32 s75, s54, 262144\n\t" \
  "s_add_u32 s80, s53, s44\n\t" \
  "s_barrier\n\t" \
  CH_DP0 \
  "ds_read_b128 v[0:3], v69 offset:0\n\t" \
  "ds_read_b128 v[4:7], v69 offset:1024\n\t" \
  "ds_read_b128 v[8:11], v69 offset:2048\n\t" \
  "ds_read_b128 v[12:15], v69 offset:3072\n\t" \
  "s_waitcnt lgkmcnt(3)\n\t" \
  "v_smfmac_f32_16x16x64_bf16 v[36:39], v[0:3], v[44:51], v68\n\t" \
  "ds_read_b128 v[44:47], v70 offset:4096\n\t" \
  "v_smfmac_f32_16x16x64_bf16 v[40:43], v[0:3], v[60:67], v68\n\t" \
  "ds_read_b128 v[48:51], v70 offset:5120\n\t" \
  "v_max3_u32 v73, v73, v0, v2\n\t" \
  "s_waitcnt lgkmcnt(4)\n\t" \
  "v_smfmac_f32_16x16x64_bf16 v[36:39], v[4:7], a[0:7], v68\n\t" \
  "s_barrier\n\t" \
  "ds_read_b128 v[16:19], v69 offset:4096\n\t" \
  CH_DP1 \
  "v_smfmac_f32_16x16x64_bf16 v[40:43], v[4:7], a[64:71], v68\n\t" \
  "ds_read_b128 v[20:23], v69 offset:5120\n\t" \
  "v_max3_u32 v73, v73, v4, v6\n\t" \
  "s_waitcnt lgkmcnt(5)\n\t" \
  "v_smfmac_f32_16x16x64_bf16 v[36:39], v[8:11], a[8:15], v68\n\t" \
  "ds_read_b128 v[24:27], v69 offset:6144\n\t" \
  "v_max3_u32 v73, v73, v8, v10\n\t" \
  "v_smfmac_f32_16x16x64_bf16 v[40:43], v[8:11], a[72:79], v68\n\t" \
  "ds_read_b128 v[28:31], v69 offset:7168\n\t" \
  "s_waitcnt lgkmcnt(6)\n\t" \
  "v_max3_u32 v73, v73, v12, v14\n\t" \
  "v_smfmac_f32_16x16x64_bf16 v[36:39], v[12:15], a[16:23], v68\n\t" \
  "ds_read_b128 v[32:35], v69 offset:8192\n\t" \
  "s_waitcnt lgkmcnt(5)\n\t" \
  "v_smfmac_f32_16x16x64_bf16 v[40:43], v[12:15], v[44:51], v68\n\t" \
  "ds_read_b128 v[60:63], v70 offset:6144\n\t" \
  "s_waitcnt lgkmcnt(5)\n\t" \
  "v_smfmac_f32_16x16x64_bf16 v[36:39], v[16:19], a[24:31], v68\n\t" \
  "ds_read_b128 v[64:67], v70 offset:7168\n\t" \
  "v_max3_u32 v73, v73, v16, v18\n\t" \
  "v_smfmac_f32_16x16x64_bf16 v[40:43], v[16:19], a[80:87], v68\n\t" \
  "s_waitcnt lgkmcnt(5)\n\t" \
  "v_max3_u32 v73, v73, v20, v22\n\t" \
  "v_smfmac_f32_16x16x64_bf16 v[36:39], v[20:23], a[32:39], v68\n\t" \
  "s_waitcnt lgkmcnt(4)\n\t" \
  "v_max3_u32 v73, v73, v24, v26\n\t" \
  "v_smfmac_f32_16x16x64_bf16 v[36:39], v[24:27], a[40:47], v68\n\t" \
  "s_waitcnt lgkmcnt(3)\n\t" \
  "v_max3_u32 v73, v73, v28, v30\n\t" \
  "v_smfmac_f32_16x16x64_bf16 v[36:39], v[28:31], a[48:55], v68\n\t" \
  "s_waitcnt lgkmcnt(2)\n\t" \
  "v_max3_u32 v73, v73, v32, v34\n\t" \
  "v_smfmac_f32_16x16x64_bf16 v[36:39], v[32:35], a[56:63], v68\n\t" \
  "v_cmp_eq_u32 vcc, -1, v73\n\t" \
  "s_waitcnt lgkmcnt(0)\n\t" \
  "v_smfmac_f32_16x16x64_bf16 v[40:43], v[20:23], v[60:67], v68\n\t" \
  "ds_read_b128 v[60:63], v70 offset:8192\n\t" \
  "v_mov_b64 v[44:45], 0\n\t" \
  "s_cbranch_vccnz LFAIL1_%=\n\t" \
  "LGO1_%=:\n\t" \
  "v_smfmac_f32_16x16x64_bf16 v[40:43], v[24:27], a[88:95], v68\n\t" \
  "ds_read_b128 v[64:67], v70 offset:9216\n\t" \
  "v_mov_b64 v[46:47], 0\n\t" \
  "v_smfmac_f32_16x16x64_bf16 v[40:43], v[28:31], a[96:103], v68\n\t" \
  "v_add_f32 v74, v36, v38\n\t" \
  "v_smfmac_f32_16x16x64_bf16 v[40:43], v[32:35], a[104:111], v68\n\t" \
  "v_add_f32 v37, v37, v39\n\t" \
  CH_DP2 \
  "v_smfmac_f32_16x16x64_bf16 v[44:47], v[0:3], a[112:119], v68\n\t" \
  "v_cvt_pk_bf16_f32 v74, v74, v37\n\t" \
  "v_smfmac_f32_16x16x64_bf16 v[44:47], v[4:7], a[120:127], v68\n\t" \
  "v_add_f32 v75, v40, v42\n\t" \
  "s_waitcnt lgkmcnt(0)\n\t" \
  "v_smfmac_f32_16x16x64_bf16 v[44:47], v[8:11], v[60:67], v68\n\t" \
  "ds_read_b128 v[60:63], v70 offset:10240\n\t" \
  "v_add_f32 v41, v41, v43\n\t" \
  "v_cvt_pk_bf16_f32 v75, v75, v41\n\t" \
  "v_smfmac_f32_16x16x64_bf16 v[44:47], v[12:15], a[128:135], v68\n\t" \
  "ds_write2st64_b32 v71, v74, v75 offset0:36 offset1:38\n\t" \
  "v_mov_b64 v[48:49], 0\n\t" \
  "v_smfmac_f32_16x16x64_bf16 v[44:47], v[16:19], a[136:143], v68\n\t" \
  "ds_read_b128 v[64:67], v70 offset:11264\n\t" \
  "v_mov_b64 v[50:51], 0\n\t" \
  "v_smfmac_f32_16x16x64_bf16 v[44:47], v[20:23], a[144:151], v68\n\t" \
  "v_smfmac_f32_16x16x64_bf16 v[44:47], v[24:27], a[152:159], v68\n\t" \
  "v_smfmac_f32_16x16x64_bf16 v[44:47], v[28:31], a[160:167], v68\n\t" \
  "s_waitcnt lgkmcnt(0)\n\t" \
  "v_smfmac_f32_16x16x64_bf16 v[44:47], v[32:35], v[60:67], v68\n\t" \
  "s_barrier\n\t" \
  "ds_read_b64_tr_b16 v[76:77], v72 offset:9216\n\t" \
  CH_DP3 \
  "v_smfmac_f32_16x16x64_bf16 v[48:51], v[0:3], a[168:175], v68\n\t" \
  "buffer_load_dwordx4 v[60:63], v69, %[X], s64 offen sc1\n\t" \
  "ds_read_b64_tr_b16 v[78:79], v72 offset:9728\n\t" \
  "v_smfmac_f32_16x16x64_bf16 v[48:51], v[4:7], a[176:183], v68\n\t" \
  "v_add_f32 v74, v44, v46\n\t" \
  "v_smfmac_f32_16x16x64_bf16 v[48:51], v[8:11], a[184:191], v68\n\t" \
  "v_add_f32 v45, v45, v47\n\t" \
  "v_smfmac_f32_16x16x64_bf16 v[48:51], v[12:15], a[192:199], v68\n\t" \
  "v_cvt_pk_bf16_f32 v74, v74, v45\n\t" \
  "s_waitcnt vmcnt(3)\n\t" \
  "s_waitcnt lgkmcnt(0)\n\t" \
  "v_mfma_f32_16x16x32_bf16 v[36:39], v[52:55], v[76:79], 0\n\t" \
  "s_waitcnt vmcnt(2)\n\t" \
  "v_mfma_f32_16x16x32_bf16 v[40:43], v[56:59], v[76:79], 0\n\t" \
  "buffer_load_dwordx4 v[64:67], v69, %[X], s64 offen offset:1024 sc1\n\t" \
  "v_mov_b64 v[52:53], 0\n\t" \
  "v_mov_b64 v[54:55], 0\n\t" \
  "v_smfmac_f32_16x16x64_bf16 v[48:51], v[16:19], a[200:207], v68\n\t" \
  "v_mov_b64 v[56:57], 0\n\t" \
  "v_smfmac_f32_16x16x64_bf16 v[48:51], v[20:23], a[208:215], v68\n\t" \
  "v_add_f32_dpp v36, v40, v36 quad_perm:[2,3,0,1] row_mask:0xf bank_mask:0xf\n\t" \
  "v_smfmac_f32_16x16x64_bf16 v[48:51], v[24:27], a[216:223], v68\n\t" \
  "v_add_f32_dpp v37, v41, v37 quad_perm:[2,3,0,1] row_mask:0xf bank_mask:0xf\n\t" \
  "v_smfmac_f32_16x16x64_bf16 v[48:51], v[28:31], a[224:231], v68\n\t" \
  "v_add_f32_dpp v38, v42, v38 quad_perm:[2,3,0,1] row_mask:0xf bank_mask:0xf\n\t" \
  "v_smfmac_f32_16x16x64_bf16 v[48:51], v[32:35], a[232:239], v68\n\t" \
  "v_add_f32_dpp v39, v43, v39 quad_perm:[2,3,0,1] row_mask:0xf bank_mask:0xf\n\t" \
  CH_DP4 \
  "v_smfmac_f32_16x16x64_bf16 v[52:55], v[0:3], a[240:247], v68\n\t" \
  "s_mov_b64 exec, s[58:59]\n\t" \
  "ds_write_b128 v69, v[36:39] offset:2080\n\t" \
  "s_mov_b64 exec, -1\n\t" \
  "v_mov_b64 v[58:59], 0\n\t" \
  "v_smfmac_f32_16x16x64_bf16 v[52:55], v[4:7], a[248:255], v68\n\t" \
  "v_add_f32 v75, v48, v50\n\t" \
  "v_add_f32 v49, v49, v51\n\t" \
  "v_cvt_pk_bf16_f32 v75, v75, v49\n\t" \
  "v_smfmac_f32_16x16x64_bf16 v[56:59], v[0:3], v[136:143], v68\n\t" \
  "ds_write2st64_b32 v71, v74, v75 offset0:0 offset1:2\n\t" \
  "v_smfmac_f32_16x16x64_bf16 v[56:59], v[4:7], v[144:151], v68\n\t" \
  "ds_read_b64_tr_b16 v[0:1], v72 offset:10240\n\t" \
  "ds_read_b64_tr_b16 v[2:3], v72 offset:10752\n\t" \
  "v_smfmac_f32_16x16x64_bf16 v[52:55], v[8:11], v[80:87], v68\n\t" \
  "ds_read_b128 v[36:39], v70 offset:12288\n\t" \
  "v_smfmac_f32_16x16x64_bf16 v[56:59], v[8:11], v[152:159], v68\n\t" \
  "ds_read_b128 v[40:43], v70 offset:13312\n\t" \
  "v_smfmac_f32_16x16x64_bf16 v[52:55], v[12:15], v[88:95], v68\n\t" \
  "s_waitcnt lgkmcnt(4)\n\t" \
  "s_barrier\n\t" \
  CH_DP5 \
  "ds_read_b64_tr_b16 v[44:45], v72 offset:0\n\t" \
  "ds_read_b64_tr_b16 v[46:47], v72 offset:512\n\t" \
  "v_smfmac_f32_16x16x64_bf16 v[56:59], v[12:15], v[160:167], v68\n\t" \
  "ds_read_b64_tr_b16 v[48:49], v72 offset:1024\n\t" \
  "ds_read_b64_tr_b16 v[50:51], v72 offset:1536\n\t" \
  "v_smfmac_f32_16x16x64_bf16 v[52:55], v[16:19], v[96:103], v68\n\t" \
  "ds_read_b128 v[4:7], v69 offset:2048\n\t" \
  "s_waitcnt lgkmcnt(5)\n\t" \
  "v_smfmac_f32_16x16x64_bf16 v[56:59], v[16:19], v[36:43], v68\n\t" \
  "ds_read_b128 v[8:11], v69 offset:3072\n\t" \
  "s_waitcnt lgkmcnt(4)\n\t" \
  "v_mfma_f32_16x16x32_bf16 v[12:15], v[44:47], v[76:79], 0\n\t" \
  "ds_read_b128 v[36:39], v70 offset:14336\n\t" \
  "s_waitcnt lgkmcnt(3)\n\t" \
  "v_mfma_f32_16x16x32_bf16 v[12:15], v[48:51], v[0:3], v[12:15]\n\t" \
  "buffer_store_dwordx4 v[44:47], v69, %[X], s69 offen offset:0 sc1\n\t" \
  "ds_read_b128 v[40:43], v70 offset:15360\n\t" \
  "v_smfmac_f32_16x16x64_bf16 v[52:55], v[20:23], v[104:111], v68\n\t" \
  "s_waitcnt lgkmcnt(2)\n\t" \
  "v_add_f32 v4, v4, v8\n\t" \
  "v_add_f32 v5, v5, v9\n\t" \
  "v_add_f32 v6, v6, v10\n\t" \
  CH_DP6 \
  "v_cndmask_b32 v78, v13, v14, s[56:57]\n\t" \
  "v_cndmask_b32 v76, v12, v15, s[56:57]\n\t" \
  "v_add_f32 v7, v7, v11\n\t" \
  "v_max3_f32 v77, v4, v5, v6\n\t" \
  "v_add_f32_dpp v76, v78, v76 quad_perm:[2,3,0,1] row_mask:0xf bank_mask:0xf\n\t" \
  "v_max3_f32 v77, v77, v7, v76\n\t" \
  "v_smfmac_f32_16x16x64_bf16 v[56:59], v[20:23], v[168:175], v68\n\t" \
  "ds_read_b128 v[44:47], v70 offset:16384\n\t" \
  "v_max_f32_dpp v77, v77, v77 quad_perm:[2,3,0,1] row_mask:0xf bank_mask:0xf\n\t" \
  "v_mov_b32 v78, v77\n\t" \
  "v_smfmac_f32_16x16x64_bf16 v[52:55], v[24:27], v[112:119], v68\n\t" \
  "ds_read_b128 v[48:51], v70 offset:17408\n\t" \
  "v_permlane16_swap_b32 v77, v78\n\t" \
  "v_max_f32 v77, v77, v78\n\t" \
  "v_mov_b32 v78, v77\n\t" \
  "v_smfmac_f32_16x16x64_bf16 v[52:55], v[28:31], v[120:127], v68\n\t" \
  "ds_read_b128 v[16:19], v70 offset:18432\n\t" \
  "v_permlane32_swap_b32 v77, v78\n\t" \
  "v_max_f32 v77, v77, v78\n\t" \
  "v_smfmac_f32_16x16x64_bf16 v[52:55], v[32:35], v[128:135], v68\n\t" \
  "ds_read_b128 v[20:23], v70 offset:19456\n\t" \
  "v_mul_f32 v77, 0xbe0293ee, v77\n\t" \
  CH_DP7 \
  "v_smfmac_f32_16x16x64_bf16 v[56:59], v[24:27], v[176:183], v68\n\t" \
  "v_fmamk_f32 v4, v4, 0x3e0293ee, v77\n\t" \
  "v_smfmac_f32_16x16x64_bf16 v[56:59], v[28:31], v[184:191], v68\n\t" \
  "ds_read_b128 v[24:27], v70 offset:22528\n\t" \
  "v_fmamk_f32 v5, v5, 0x3e0293ee, v77\n\t" \
  "s_waitcnt lgkmcnt(5)\n\t" \
  "v_smfmac_f32_16x16x64_bf16 v[56:59], v[32:35], v[36:43], v68\n\t" \
  "ds_read_b128 v[28:31], v70 offset:23552\n\t" \
  "v_fmamk_f32 v6, v6, 0x3e0293ee, v77\n\t" \
  "v_fmamk_f32 v7, v7, 0x3e0293ee, v77\n\t" \
  "v_fmamk_f32 v76, v76, 0x3e0293ee, v77\n\t" \
  "v_exp_f32 v4, v4\n\t" \
  "v_exp_f32 v5, v5\n\t" \
  "v_exp_f32 v6, v6\n\t" \
  "v_exp_f32 v7, v7\n\t" \
  "v_cvt_pk_bf16_f32 v4, v4, v5\n\t" \
  "v_exp_f32 v76, v76\n\t" \
  "v_cvt_pk_bf16_f32 v5, v6, v7\n\t" \
  "v_mov_b32_dpp v12, v4 quad_perm:[0,1,0,1] row_mask:0xf bank_mask:0xf\n\t" \
  "v_mov_b32_dpp v14, v4 quad_perm:[2,3,2,3] row_mask:0xf bank_mask:0xf\n\t" \
  "v_mov_b32_dpp v13, v5 quad_perm:[0,1,0,1] row_mask:0xf bank_mask:0xf\n\t" \
  "v_mov_b32_dpp v15, v5 quad_perm:[2,3,2,3] row_mask:0xf bank_mask:0xf\n\t" \
  "v_add_f32 v52, v52, v54\n\t" \
  "v_add_f32 v53, v53, v55\n\t" \
  "s_waitcnt vmcnt(2)\n\t" \
  CH_DP8 \
  "v_mfma_f32_16x16x32_bf16 v[0:3], v[60:63], v[12:15], 0\n\t" \
  "ds_read_b128 v[36:39], v70 offset:20480\n\t" \
  "v_add_f32 v56, v56, v58\n\t" \
  "v_add_f32 v57, v57, v59\n\t" \
  "v_cvt_pk_bf16_f32 v52, v52, v53\n\t" \
  "v_cvt_pk_bf16_f32 v53, v56, v57\n\t" \
  "s_waitcnt vmcnt(1)\n\t" \
  "v_mfma_f32_16x16x32_bf16 v[4:7], v[64:67], v[12:15], 0\n\t" \
  "ds_read_b128 v[40:43], v70 offset:21504\n\t" \
  "v_mov_b64 v[56:57], s[88:89]\n\t" \
  "v_mov_b64 v[58:59], s[88:89]\n\t" \
  "v_mov_b32_dpp v78, v76 quad_perm:[0,1,0,1] row_mask:0xf bank_mask:0xf\n\t" \
  "v_mov_b32_dpp v79, v76 quad_perm:[2,3,2,3] row_mask:0xf bank_mask:0xf\n\t" \
  "v_mfma_f32_16x16x32_bf16 v[32:35], v[56:59], v[12:15], 0\n\t" \
  "buffer_store_dwordx2 v[52:53], v71, %[X], s69 offen offset:2556 sc1\n\t" \
  "ds_read_b128 v[60:63], v70 offset:24576\n\t" \
  "v_cvt_pk_bf16_f32 v11, v78, v79\n\t" \
  "v_cvt_pk_bf16_f32 v8, v78, v79\n\t" \
  "v_mov_b64 v[54:55], 0\n\t" \
  "v_mov_b32 v9, 0\n\t" \
  "v_mov_b32 v10, 0\n\t" \
  "v_mov_b64 v[14:15], 0\n\t" \
  "s_cmp_eq_u32 s48, 63\n\t" \
  "v_mfma_f32_16x16x32_bf16 v[32:35], v[54:57], v[8:11], v[32:35]\n\t" \
  "v_mfma_f32_16x16x32_bf16 v[0:3], v[52:55], v[8:11], v[0:3]\n\t" \
  "buffer_load_dword v75, off, %[X], s80 offset:1024 sc1\n\t" \
  "ds_read_b128 v[64:67], v70 offset:25600\n\t" \
  "v_mov_b64 v[56:57], 0\n\t" \
  "v_mov_b64 v[58:59], 0\n\t" \
  "v_mfma_f32_16x16x32_bf16 v[4:7], v[52:55], v[10:13], v[4:7]\n\t" \
  "s_cbranch_scc1 LLAST1_%=\n\t" \
  "v_rcp_f32 v74, v32\n\t" \
  "v_mov_b64 v[12:13], 0\n\t" \
  "v_mov_b32 v8, 0\n\t" \
  "v_mul_f32 v0, v0, v74\n\t" \
  "v_mul_f32 v1, v1, v74\n\t" \
  "v_mul_f32 v2, v2, v74\n\t" \
  "v_mul_f32 v3, v3, v74\n\t" \
  "v_cvt_pk_bf16_f32 v76, v0, v1\n\t" \
  "v_cvt_pk_bf16_f32 v77, v2, v3\n\t" \
  "v_pk_mul_f32 v[4:5], v[4:5], v[74:75] op_sel_hi:[1,0]\n\t" \
  "v_pk_mul_f32 v[6:7], v[6:7], v[74:75] op_sel_hi:[1,0]\n\t" \
  "v_cvt_pk_bf16_f32 v78, v4, v5\n\t" \
  "v_cvt_pk_bf16_f32 v79, v6, v7\n\t" \
  "ds_write_b128 v69, v[76:79] offset:5120\n\t" \
  CH_DP9 \
  "s_waitcnt lgkmcnt(9)\n\t" \
  "v_smfmac_f32_16x16x64_bf16 v[12:15], v[76:79], v[44:51], v68\n\t" \
  "ds_read_b128 v[44:47], v70 offset:26624\n\t" \
  "v_mov_b32 v11, 0\n\t" \
  "s_waitcnt lgkmcnt(8)\n\t" \
  "v_smfmac_f32_16x16x64_bf16 v[56:59], v[76:79], v[16:23], v68\n\t" \
  "ds_read_b128 v[48:51], v70 offset:27648\n\t" \
  "v_mov_b64 v[52:53], 0\n\t" \
  "s_waitcnt lgkmcnt(5)\n\t" \
  "v_smfmac_f32_16x16x64_bf16 v[8:11], v[76:79], v[36:43], v68\n\t" \
  "ds_read_b128 v[16:19], v70 offset:28672\n\t" \
  "v_and_b32 v73, 32, v69\n\t" \
  "v_smfmac_f32_16x16x64_bf16 v[52:55], v[76:79], v[24:31], v68\n\t" \
  "s_waitcnt lgkmcnt(3)\n\t" \
  "s_barrier\n\t" \
  CH_DP10 \
  "ds_read_b128 v[0:3], v69 offset:4096\n\t" \
  "ds_read_b128 v[4:7], v69 offset:7168\n\t" \
  "v_lshl_add_u32 v73, v73, 5, v69\n\t" \
  "v_lshrrev_b32 v71, 7, v69\n\t" \
  "v_and_b32 v74, 0x70, v69\n\t" \
  "v_lshl_add_u32 v71, v71, 14, v74\n\t" \
  "v_bfe_u32 v72, v69, 2, 7\n\t" \
  "v_and_b32 v74, 0x200, v69\n\t" \
  "s_waitcnt lgkmcnt(1)\n\t" \
  "v_smfmac_f32_16x16x64_bf16 v[8:11], v[0:3], v[60:67], v68\n\t" \
  "ds_read_b128 v[20:23], v70 offset:29696\n\t" \
  "v_mov_b64 v[76:77], 0\n\t" \
  "v_smfmac_f32_16x16x64_bf16 v[12:15], v[0:3], v[200:207], v68\n\t" \
  "ds_read_b128 v[32:35], v69 offset:6144\n\t" \
  "v_mov_b64 v[78:79], 0\n\t" \
  "v_smfmac_f32_16x16x64_bf16 v[52:55], v[0:3], v[44:51], v68\n\t" \
  "ds_read_b128 v[24:27], v70 offset:30720\n\t" \
  "v_lshl_add_u32 v72, v74, 3, v72\n\t" \
  "v_smfmac_f32_16x16x64_bf16 v[56:59], v[0:3], v[232:239], v68\n\t" \
  "ds_read_b128 v[28:31], v70 offset:31744\n\t" \
  "s_waitcnt lgkmcnt(4)\n\t" \
  "v_smfmac_f32_16x16x64_bf16 v[12:15], v[4:7], v[208:215], v68\n\t" \
  "ds_read_b128 v[60:63], v70 offset:32768\n\t" \
  "s_waitcnt lgkmcnt(4)\n\t" \
  "v_smfmac_f32_16x16x64_bf16 v[8:11], v[4:7], v[16:23], v68\n\t" \
  "ds_read_b128 v[64:67], v70 offset:33792\n\t" \
  "s_waitcnt lgkmcnt(4)\n\t" \
  "v_smfmac_f32_16x16x64_bf16 v[8:11], v[32:35], v[192:199], v68\n\t" \
  "ds_read_b128 v[36:39], v73 offset:4096\n\t" \
  "v_smfmac_f32_16x16x64_bf16 v[12:15], v[32:35], v[216:223], v68\n\t" \
  "ds_read_b128 v[40:43], v73 offset:6144\n\t" \
  "s_waitcnt lgkmcnt(4)\n\t" \
  "v_smfmac_f32_16x16x64_bf16 v[52:55], v[4:7], v[24:31], v68\n\t" \
  "ds_read_b128 v[44:47], v70 offset:34816\n\t" \
  "v_cvt_pk_bf16_f32 v8, v8, v9\n\t" \
  "v_smfmac_f32_16x16x64_bf16 v[52:55], v[32:35], v[224:231], v68\n\t" \
  "ds_read_b128 v[48:51], v70 offset:35840\n\t" \
  "v_cvt_pk_bf16_f32 v9, v10, v11\n\t" \
  "v_smfmac_f32_16x16x64_bf16 v[56:59], v[4:7], v[240:247], v68\n\t" \
  "v_cvt_pk_bf16_f32 v10, v12, v13\n\t" \
  "s_waitcnt lgkmcnt(4)\n\t" \
  "v_smfmac_f32_16x16x64_bf16 v[56:59], v[32:35], v[60:67], v68\n\t" \
  "v_cvt_pk_bf16_f32 v11, v14, v15\n\t" \
  "s_waitcnt lgkmcnt(3)\n\t" \
  "v_smfmac_f32_16x16x64_bf16 v[76:79], v[36:39], v[248:255], v68\n\t" \
  "buffer_store_dwordx4 v[8:11], v71, %[X], s54 offen sc1\n\t" \
  CH_DP11 \
  "v_cvt_pk_bf16_f32 v52, v52, v53\n\t" \
  "s_waitcnt lgkmcnt(0)\n\t" \
  "v_smfmac_f32_16x16x64_bf16 v[76:79], v[40:43], v[44:51], v68\n\t" \
  "v_cvt_pk_bf16_f32 v53, v54, v55\n\t" \
  "v_cvt_pk_bf16_f32 v54, v56, v57\n\t" \
  "v_cvt_pk_bf16_f32 v55, v58, v59\n\t" \
  "buffer_store_dwordx4 v[52:55], v71, %[X], s75 offen sc1\n\t" \
  "s_nop 2\n\t" \
  "v_add_f32 v76, v76, v78\n\t" \
  "v_add_f32 v77, v77, v79\n\t" \
  "v_cvt_pk_bf16_f32 v76, v76, v77\n\t" \
  "buffer_store_dword v76, v72, %[X], s55 offen sc1\n\t" \
  CH_DP12 \
  "s_memrealtime s[70:71]\n\t" \
  "s_waitcnt vmcnt(3)\n\t" \
  "v_readfirstlane_b32 s65, v75\n\t" \
  "s_barrier\n\t" \
  "s_waitcnt lgkmcnt(0)\n\t" \
  "s_not_b32 s65, s65\n\t" \
  "s_lshl_b32 s61, s70, 8\n\t" \
  "s_andn2_b32 s64, s65, 0xff\n\t" \
  "s_sub_u32 s64, s64, s61\n\t" \
  "s_ashr_i32 s64, s64, 8\n\t" \
  "s_cmp_eq_u32 s65, 0\n\t" \
  "s_cselect_b32 s64, " CH_DLF ", s64\n\t" \
  "s_max_i32 s64, s64, " CH_DLL1 "\n\t" \
  "s_add_u32 s64, s64, s70\n\t" \
  CH_ST2 \
  "s_add_u32 s55, s41, s53\n\t" \
  "s_mov_b32 s74, 0\n\t" \
  "s_movk_i32 s60, 8192\n\t" \
  "RWT1_%=:\n\t" \
  "s_memrealtime s[72:73]\n\t" \
  "s_sub_u32 s60, s60, 1\n\t" \
  "s_cselect_b32 s61, 0, -1\n\t" \
  "s_waitcnt lgkmcnt(0)\n\t" \
  "s_sub_i32 s73, s72, s64\n\t" \
  "s_and_b32 s73, s73, s61\n\t" \
  "s_cmp_lt_i32 s73, 0\n\t" \
  "s_cbranch_scc1 RWT1_%=\n\t" \
  "RRETRY1_%=:\n\t" \
  CH_DP13 \
  "buffer_load_dwordx4 v[0:3], v69, %[X], s55 offen offset:0 sc1\n\t" \
  "buffer_load_dwordx4 v[4:7], v69, %[X], s55 offen offset:1024 sc1\n\t" \
  "buffer_load_dwordx4 v[8:11], v69, %[X], s55 offen offset:2048 sc1\n\t" \
  "buffer_load_dwordx4 v[12:15], v69, %[X], s55 offen offset:3072 sc1\n\t" \
  "v_lshrrev_b32 v20, 2, v69\n\t" \
  "v_and_b32 v20, 16, v20\n\t" \
  "v_mov_b32 v21, 0x3f80\n\t" \
  "v_lshlrev_b32 v21, v20, v21\n\t" \
  "v_bfe_u32 v20, v69, 4, 2\n\t" \
  "v_cmp_eq_u32 vcc, 0, v20\n\t" \
  "s_nop 1\n\t" \
  "v_cndmask_b32 v32, 0, v21, vcc\n\t" \
  "v_cmp_eq_u32 vcc, 1, v20\n\t" \
  "s_nop 1\n\t" \
  "v_cndmask_b32 v33, 0, v21, vcc\n\t" \
  "v_cmp_eq_u32 vcc, 2, v20\n\t" \
  "s_nop 1\n\t" \
  "v_cndmask_b32 v34, 0, v21, vcc\n\t" \
  "v_cmp_eq_u32 vcc, 3, v20\n\t" \
  "s_nop 1\n\t" \
  "v_cndmask_b32 v35, 0, v21, vcc\n\t" \
  "v_bfe_u32 v38, v69, 7, 1\n\t" \
  "v_bfe_u32 v20, v69, 9, 1\n\t" \
  "v_lshl_add_u32 v38, v20, 1, v38\n\t" \
  "v_mul_u32_u24 v38, 16384, v38\n\t" \
  "v_and_b32 v20, 0x60, v69\n\t" \
  "v_add_u32 v38, v38, v20\n\t" \
  "v_bfe_u32 v20, v69, 8, 1\n\t" \
  "v_lshl_add_u32 v38, v20, 4, v38\n\t" \
  "v_bfe_u32 v20, v69, 4, 1\n\t" \
  "v_lshl_add_u32 v38, v20, 3, v38\n\t" \
  "s_add_u32 s54, s37, s53\n\t" \
  "v_mov_b32 v44, 0\n\t" \
  CH_DP17 \
  "s_waitcnt vmcnt(3)\n\t" \
  CH_DP14 \
  "v_mfma_f32_16x16x32_bf16 v[40:43], v[32:35], v[0:3], 0\n\t" \
  "v_max3_u32 v44, v44, v0, v1\n\t" \
  "v_max3_u32 v44, v44, v2, v3\n\t" \
  "s_waitcnt vmcnt(2)\n\t" \
  "v_mfma_f32_16x16x32_bf16 v[40:43], v[32:35], v[4:7], v[40:43]\n\t" \
  "v_max3_u32 v44, v44, v4, v5\n\t" \
  "v_max3_u32 v44, v44, v6, v7\n\t" \
  "s_waitcnt vmcnt(1)\n\t" \
  "v_mfma_f32_16x16x32_bf16 v[40:43], v[32:35], v[8:11], v[40:43]\n\t" \
  "v_max3_u32 v44, v44, v8, v9\n\t" \
  "v_max3_u32 v44, v44, v10, v11\n\t" \
  "s_waitcnt vmcnt(0)\n\t" \
  CH_DP15 \
  "v_mfma_f32_16x16x32_bf16 v[40:43], v[32:35], v[12:15], v[40:43]\n\t" \
  "v_max3_u32 v44, v44, v12, v13\n\t" \
  "v_max3_u32 v44, v44, v14, v15\n\t" \
  "v_cmp_eq_u32 vcc, -1, v44\n\t" \
  "s_cbranch_vccnz RFAIL1_%=\n\t" \
  "s_nop 3\n\t" \
  "v_add_f32_dpp v40, v40, v40 row_ror:8 row_mask:0xf bank_mask:0xf\n\t" \
  "v_add_f32_dpp v41, v41, v41 row_ror:8 row_mask:0xf bank_mask:0xf\n\t" \
  "v_add_f32_dpp v42, v42, v42 row_ror:8 row_mask:0xf bank_mask:0xf\n\t" \
  "v_add_f32_dpp v43, v43, v43 row_ror:8 row_mask:0xf bank_mask:0xf\n\t" \
  "v_cvt_pk_bf16_f32 v40, v40, v41\n\t" \
  "v_cvt_pk_bf16_f32 v41, v42, v43\n\t" \
  "buffer_store_dwordx2 v[40:41], v38, %[X], s54 offen sc1\n\t" \
  "RDONE1_%=:\n\t" \
  CH_DP16 \
  CH_ST4 \
  "s_barrier\n\t" \
  CH_ST3 \
  CH_TRC \
  "s_add_u32 s48, s48, 1\n\t" \
  "s_mov_b32 s49, s53\n\t" \
  "s_add_u32 s53, s53, " CH_SLOT "\n\t" \
  "s_add_u32 s60, s53, " CH_SKIP "\n\t" \
  "s_lshr_b32 s61, s96, 1\n\t" \
  "s_add_u32 s86, s86, 1\n\t" \
  "s_cmp_eq_u32 s86, 3\n\t" \
  "s_cselect_b32 s53, s60, s53\n\t" \
  "s_cselect_b32 s96, s61, s96\n\t" \
  "s_cselect_b32 s86, 0, s86\n\t" \
  "s_andn2_b32 s53, s53, 0x2000\n\t" \
  "s_and_b32 s60, s96, 1\n\t" \
  "s_lshl_b32 s60, s60, 13\n\t" \
  "s_or_b32 s53, s53, s60\n\t" \
  "s_mov_b32 s68, 0\n\t" \
  "s_add_u32 s82, s48, 0\n\t" \
  CH_SEGC \
  "s_lshr_b64 s[60:61], s[90:91], s82\n\t" \
  "s_and_b32 s60, s60, 1\n\t" \
  "s_lshl_b32 s60, s60, 13\n\t" \
  "s_add_u32 s82, s82, " CH_SG0 "\n\t" \
  "s_lshl_b32 s82, s82, 21\n\t" \
  "s_or_b32 s82, s82, s60\n\t" \
  "s_add_u32 s50, s82, s38\n\t" \
  "s_add_u32 s69, s82, s39\n\t" \
  "s_branch LG1_%=\n\t" \
  "RFAIL1_%=:\n\t" \
  "s_add_u32 s74, s74, 1\n\t" \
  "s_sleep " CH_RSL "\n\t" \
  "s_waitcnt vmcnt(0)\n\t" \
  "s_cmp_gt_u32 s74, s84\n\t" \
  "s_cbranch_scc1 RG1_%=\n\t" \
  "s_and_b32 s60, s74, 1023\n\t" \
  "s_cmp_lg_u32 s60, 0\n\t" \
  "s_cbranch_scc1 RRETRY1_%=\n\t" \
  "buffer_load_dword v46, off, %[X], s92 sc1\n\t" \
  "s_waitcnt vmcnt(0)\n\t" \
  "v_readfirstlane_b32 s60, v46\n\t" \
  "s_nop 0\n\t" \
  "s_cmp_eq_u32 s60, 0\n\t" \
  "s_cbranch_scc1 RRETRY1_%=\n\t" \
  "RG1_%=:\n\t" \
  "s_mov_b32 s84, 0\n\t" \
  "s_lshl_b32 s60, s36, 4\n\t" \
  "s_or_b32 s60, s60, s48\n\t" \
  "s_or_b32 s60, s60, 536870912\n\t" \
  "v_mov_b32 v46, s60\n\t" \
  "v_mov_b32 v47, 0\n\t" \
  "s_mov_b64 exec, 1\n\t" \
  "buffer_atomic_cmpswap v[46:47], off, %[X], s92 sc1\n\t" \
  "s_mov_b64 exec, -1\n\t" \
  "s_branch RDONE1_%=\n\t" \
  "LFAIL1_%=:\n\t" \
  "s_add_u32 s68, s68, 1\n\t" \
  "s_waitcnt lgkmcnt(0)\n\t" \
  "s_cmp_gt_u32 s68, s83\n\t" \
  "s_cbranch_scc1 LGV1_%=\n\t" \
  "s_barrier\n\t" \
  "s_branch LG1_%=\n\t" \
  "LGV1_%=:\n\t" \
  "s_mov_b32 s83, 0\n\t" \
  "s_lshl_b32 s60, s36, 4\n\t" \
  "s_or_b32 s60, s60, s48\n\t" \
  "s_or_b32 s60, s60, 805306368\n\t" \
  "v_mov_b32 v46, s60\n\t" \
  "v_mov_b32 v47, 0\n\t" \
  "s_mov_b64 exec, 1\n\t" \
  "buffer_atomic_cmpswap v[46:47], off, %[X], s92 sc1\n\t" \
  "s_mov_b64 exec, -1\n\t" \
  "s_branch LGO1_%=\n\t" \
  "LLAST1_%=:\n\t" \
  "s_waitcnt vmcnt(0) lgkmcnt(0)\n\t" \
  "s_barrier\n\t" \
  "s_branch LEND_%=\n\t" \
  "LG2_%=:\n\t" \
  "s_barrier\n\t" \
  "buffer_load_dwordx4 v[52:55], v69, %[X], s50 offen sc1\n\t" \
  "buffer_load_dwordx4 v[56:59], v69, %[X], s50 offen offset:1024 sc1\n\t" \
  "s_add_u32 s64, s50, 18432\n\t" \
  "s_movk_i32 m0, 11264\n\t" \
  "v_lshlrev_b32 v75, 1, v69\n\t" \
  "buffer_load_dword v75, %[X], s64 offen sc1 lds\n\t" \
  "ds_read_b128 v[44:47], v70 offset:0\n\t" \
  "ds_read_b128 v[48:51], v70 offset:1024\n\t" \
  "ds_read_b128 v[60:63], v70 offset:2048\n\t" \
  "ds_read_b128 v[64:67], v70 offset:3072\n\t" \
  "v_mov_b64 v[36:37], 0\n\t" \
  "v_mov_b64 v[38:39], 0\n\t" \
  "v_mov_b64 v[40:41], 0\n\t" \
  "v_mov_b64 v[42:43], 0\n\t" \
  "v_mov_b32 v73, 0\n\t" \
  "v_lshrrev_b32 v71, 1, v69\n\t" \
  "v_lshrrev_b32 v72, 3, v69\n\t" \
  "v_and_b32 v72, 0x78, v72\n\t" \
  "v_and_b32 v74, 0x30, v69\n\t" \
  "v_lshl_add_u32 v72, v74, 3, v72\n\t" \
  "s_add_u32 s54, s53, s36\n\t" \
  "s_add_u32 s55, s54, 557056\n\t" \
  "s_add_u32 s54, s54, 131072\n\t" \
  "s_add_u32 s75, s54, 262144\n\t" \
  "s_add_u32 s80, s53, s44\n\t" \
  "s_barrier\n\t" \
  CH_DP0 \
  "ds_read_b128 v[0:3], v69 offset:0\n\t" \
  "ds_read_b128 v[4:7], v69 offset:1024\n\t" \
  "ds_read_b128 v[8:11], v69 offset:2048\n\t" \
  "ds_read_b128 v[12:15], v69 offset:3072\n\t" \
  "s_waitcnt lgkmcnt(3)\n\t" \
  "v_smfmac_f32_16x16x64_bf16 v[36:39], v[0:3], v[44:51], v68\n\t" \
  "ds_read_b128 v[44:47], v70 offset:4096\n\t" \
  "v_smfmac_f32_16x16x64_bf16 v[40:43], v[0:3], v[60:67], v68\n\t" \
  "ds_read_b128 v[48:51], v70 offset:5120\n\t" \
  "v_max3_u32 v73, v73, v0, v2\n\t" \
  "s_waitcnt lgkmcnt(4)\n\t" \
  "v_smfmac_f32_16x16x64_bf16 v[36:39], v[4:7], a[0:7], v68\n\t" \
  "s_barrier\n\t" \
  "ds_read_b128 v[16:19], v69 offset:4096\n\t" \
  CH_DP1 \
  "v_smfmac_f32_16x16x64_bf16 v[40:43], v[4:7], a[64:71], v68\n\t" \
  "ds_read_b128 v[20:23], v69 offset:5120\n\t" \
  "v_max3_u32 v73, v73, v4, v6\n\t" \
  "s_waitcnt lgkmcnt(5)\n\t" \
  "v_smfmac_f32_16x16x64_bf16 v[36:39], v[8:11], a[8:15], v68\n\t" \
  "ds_read_b128 v[24:27], v69 offset:6144\n\t" \
  "v_max3_u32 v73, v73, v8, v10\n\t" \
  "v_smfmac_f32_16x16x64_bf16 v[40:43], v[8:11], a[72:79], v68\n\t" \
  "ds_read_b128 v[28:31], v69 offset:7168\n\t" \
  "s_waitcnt lgkmcnt(6)\n\t" \
  "v_max3_u32 v73, v73, v12, v14\n\t" \
  "v_smfmac_f32_16x16x64_bf16 v[36:39], v[12:15], a[16:23], v68\n\t" \
  "ds_read_b128 v[32:35], v69 offset:8192\n\t" \
  "s_waitcnt lgkmcnt(5)\n\t" \
  "v_smfmac_f32_16x16x64_bf16 v[40:43], v[12:15], v[44:51], v68\n\t" \
  "ds_read_b128 v[60:63], v70 offset:6144\n\t" \
  "s_waitcnt lgkmcnt(5)\n\t" \
  "v_smfmac_f32_16x16x64_bf16 v[36:39], v[16:19], a[24:31], v68\n\t" \
  "ds_read_b128 v[64:67], v70 offset:7168\n\t" \
  "v_max3_u32 v73, v73, v16, v18\n\t" \
  "v_smfmac_f32_16x16x64_bf16 v[40:43], v[16:19], a[80:87], v68\n\t" \
  "s_waitcnt lgkmcnt(5)\n\t" \
  "v_max3_u32 v73, v73, v20, v22\n\t" \
  "v_smfmac_f32_16x16x64_bf16 v[36:39], v[20:23], a[32:39], v68\n\t" \
  "s_waitcnt lgkmcnt(4)\n\t" \
  "v_max3_u32 v73, v73, v24, v26\n\t" \
  "v_smfmac_f32_16x16x64_bf16 v[36:39], v[24:27], a[40:47], v68\n\t" \
  "s_waitcnt lgkmcnt(3)\n\t" \
  "v_max3_u32 v73, v73, v28, v30\n\t" \
  "v_smfmac_f32_16x16x64_bf16 v[36:39], v[28:31], a[48:55], v68\n\t" \
  "s_waitcnt lgkmcnt(2)\n\t" \
  "v_max3_u32 v73, v73, v32, v34\n\t" \
  "v_smfmac_f32_16x16x64_bf16 v[36:39], v[32:35], a[56:63], v68\n\t" \
  "v_cmp_eq_u32 vcc, -1, v73\n\t" \
  "s_waitcnt lgkmcnt(0)\n\t" \
  "v_smfmac_f32_16x16x64_bf16 v[40:43], v[20:23], v[60:67], v68\n\t" \
  "ds_read_b128 v[60:63], v70 offset:8192\n\t" \
  "v_mov_b64 v[44:45], 0\n\t" \
  "s_cbranch_vccnz LFAIL2_%=\n\t" \
  "LGO2_%=:\n\t" \
  "v_smfmac_f32_16x16x64_bf16 v[40:43], v[24:27], a[88:95], v68\n\t" \
  "ds_read_b128 v[64:67], v70 offset:9216\n\t" \
  "v_mov_b64 v[46:47], 0\n\t" \
  "v_smfmac_f32_16x16x64_bf16 v[40:43], v[28:31], a[96:103], v68\n\t" \
  "v_add_f32 v74, v36, v38\n\t" \
  "v_smfmac_f32_16x16x64_bf16 v[40:43], v[32:35], a[104:111], v68\n\t" \
  "v_add_f32 v37, v37, v39\n\t" \
  CH_DP2 \
  "v_smfmac_f32_16x16x64_bf16 v[44:47], v[0:3], a[112:119], v68\n\t" \
  "v_cvt_pk_bf16_f32 v74, v74, v37\n\t" \
  "v_smfmac_f32_16x16x64_bf16 v[44:47], v[4:7], a[120:127], v68\n\t" \
  "v_add_f32 v75, v40, v42\n\t" \
  "s_waitcnt lgkmcnt(0)\n\t" \
  "v_smfmac_f32_16x16x64_bf16 v[44:47], v[8:11], v[60:67], v68\n\t" \
  "ds_read_b128 v[60:63], v70 offset:10240\n\t" \
  "v_add_f32 v41, v41, v43\n\t" \
  "v_cvt_pk_bf16_f32 v75, v75, v41\n\t" \
  "v_smfmac_f32_16x16x64_bf16 v[44:47], v[12:15], a[128:135], v68\n\t" \
  "ds_write2st64_b32 v71, v74, v75 offset0:40 offset1:42\n\t" \
  "v_mov_b64 v[48:49], 0\n\t" \
  "v_smfmac_f32_16x16x64_bf16 v[44:47], v[16:19], a[136:143], v68\n\t" \
  "ds_read_b128 v[64:67], v70 offset:11264\n\t" \
  "v_mov_b64 v[50:51], 0\n\t" \
  "v_smfmac_f32_16x16x64_bf16 v[44:47], v[20:23], a[144:151], v68\n\t" \
  "v_smfmac_f32_16x16x64_bf16 v[44:47], v[24:27], a[152:159], v68\n\t" \
  "v_smfmac_f32_16x16x64_bf16 v[44:47], v[28:31], a[160:167], v68\n\t" \
  "s_waitcnt lgkmcnt(0)\n\t" \
  "v_smfmac_f32_16x16x64_bf16 v[44:47], v[32:35], v[60:67], v68\n\t" \
  "s_barrier\n\t" \
  "ds_read_b64_tr_b16 v[76:77], v72 offset:10240\n\t" \
  CH_DP3 \
  "v_smfmac_f32_16x16x64_bf16 v[48:51], v[0:3], a[168:175], v68\n\t" \
  "ds_read_b64_tr_b16 v[78:79], v72 offset:10752\n\t" \
  "v_smfmac_f32_16x16x64_bf16 v[48:51], v[4:7], a[176:183], v68\n\t" \
  "buffer_load_dwordx4 v[60:63], v69, %[X], s64 offen sc1\n\t" \
  "v_add_f32 v74, v44, v46\n\t" \
  "v_smfmac_f32_16x16x64_bf16 v[48:51], v[8:11], a[184:191], v68\n\t" \
  "v_add_f32 v45, v45, v47\n\t" \
  "v_smfmac_f32_16x16x64_bf16 v[48:51], v[12:15], a[192:199], v68\n\t" \
  "buffer_load_dwordx4 v[64:67], v69, %[X], s64 offen offset:1024 sc1\n\t" \
  "v_cvt_pk_bf16_f32 v74, v74, v45\n\t" \
  "s_waitcnt vmcnt(4)\n\t" \
  "s_waitcnt lgkmcnt(0)\n\t" \
  "v_mfma_f32_16x16x32_bf16 v[36:39], v[52:55], v[76:79], 0\n\t" \
  "s_waitcnt vmcnt(3)\n\t" \
  "v_mfma_f32_16x16x32_bf16 v[40:43], v[56:59], v[76:79], 0\n\t" \
  "v_mov_b64 v[52:53], 0\n\t" \
  "v_mov_b64 v[54:55], 0\n\t" \
  "v_smfmac_f32_16x16x64_bf16 v[48:51], v[16:19], a[200:207], v68\n\t" \
  "v_mov_b64 v[56:57], 0\n\t" \
  "v_smfmac_f32_16x16x64_bf16 v[48:51], v[20:23], a[208:215], v68\n\t" \
  "v_add_f32_dpp v36, v40, v36 quad_perm:[2,3,0,1] row_mask:0xf bank_mask:0xf\n\t" \
  "v_smfmac_f32_16x16x64_bf16 v[48:51], v[24:27], a[216:223], v68\n\t" \
  "v_add_f32_dpp v37, v41, v37 quad_perm:[2,3,0,1] row_mask:0xf bank_mask:0xf\n\t" \
  "v_smfmac_f32_16x16x64_bf16 v[48:51], v[28:31], a[224:231], v68\n\t" \
  "v_add_f32_dpp v38, v42, v38 quad_perm:[2,3,0,1] row_mask:0xf bank_mask:0xf\n\t" \
  "v_smfmac_f32_16x16x64_bf16 v[48:51], v[32:35], a[232:239], v68\n\t" \
  "v_add_f32_dpp v39, v43, v39 quad_perm:[2,3,0,1] row_mask:0xf bank_mask:0xf\n\t" \
  CH_DP4 \
  "v_smfmac_f32_16x16x64_bf16 v[52:55], v[0:3], a[240:247], v68\n\t" \
  "s_mov_b64 exec, s[58:59]\n\t" \
  "ds_write_b128 v69, v[36:39] offset:3072\n\t" \
  "s_mov_b64 exec, -1\n\t" \
  "v_mov_b64 v[58:59], 0\n\t" \
  "v_smfmac_f32_16x16x64_bf16 v[52:55], v[4:7], a[248:255], v68\n\t" \
  "v_add_f32 v75, v48, v50\n\t" \
  "v_add_f32 v49, v49, v51\n\t" \
  "v_cvt_pk_bf16_f32 v75, v75, v49\n\t" \
  "v_smfmac_f32_16x16x64_bf16 v[56:59], v[0:3], v[136:143], v68\n\t" \
  "ds_write2st64_b32 v71, v74, v75 offset0:4 offset1:6\n\t" \
  "v_smfmac_f32_16x16x64_bf16 v[56:59], v[4:7], v[144:151], v68\n\t" \
  "ds_read_b64_tr_b16 v[0:1], v72 offset:9216\n\t" \
  "ds_read_b64_tr_b16 v[2:3], v72 offset:9728\n\t" \
  "v_smfmac_f32_16x16x64_bf16 v[52:55], v[8:11], v[80:87], v68\n\t" \
  "ds_read_b128 v[36:39], v70 offset:12288\n\t" \
  "v_smfmac_f32_16x16x64_bf16 v[56:59], v[8:11], v[152:159], v68\n\t" \
  "ds_read_b128 v[40:43], v70 offset:13312\n\t" \
  "v_smfmac_f32_16x16x64_bf16 v[52:55], v[12:15], v[88:95], v68\n\t" \
  "s_waitcnt lgkmcnt(4)\n\t" \
  "s_barrier\n\t" \
  CH_DP5 \
  "ds_read_b64_tr_b16 v[44:45], v72 offset:1024\n\t" \
  "ds_read_b64_tr_b16 v[46:47], v72 offset:1536\n\t" \
  "v_smfmac_f32_16x16x64_bf16 v[56:59], v[12:15], v[160:167], v68\n\t" \
  "ds_read_b64_tr_b16 v[48:49], v72 offset:0\n\t" \
  "ds_read_b64_tr_b16 v[50:51], v72 offset:512\n\t" \
  "v_smfmac_f32_16x16x64_bf16 v[52:55], v[16:19], v[96:103], v68\n\t" \
  "ds_read_b128 v[4:7], v69 offset:2048\n\t" \
  "s_waitcnt lgkmcnt(5)\n\t" \
  "v_smfmac_f32_16x16x64_bf16 v[56:59], v[16:19], v[36:43], v68\n\t" \
  "ds_read_b128 v[8:11], v69 offset:3072\n\t" \
  "s_waitcnt lgkmcnt(4)\n\t" \
  "v_mfma_f32_16x16x32_bf16 v[12:15], v[44:47], v[76:79], 0\n\t" \
  "ds_read_b128 v[36:39], v70 offset:14336\n\t" \
  "s_waitcnt lgkmcnt(3)\n\t" \
  "v_mfma_f32_16x16x32_bf16 v[12:15], v[48:51], v[0:3], v[12:15]\n\t" \
  "ds_read_b128 v[40:43], v70 offset:15360\n\t" \
  "v_smfmac_f32_16x16x64_bf16 v[52:55], v[20:23], v[104:111], v68\n\t" \
  "buffer_store_dwordx4 v[44:47], v69, %[X], s69 offen offset:1024 sc1\n\t" \
  "s_waitcnt lgkmcnt(2)\n\t" \
  "v_add_f32 v4, v4, v8\n\t" \
  "v_add_f32 v5, v5, v9\n\t" \
  "v_add_f32 v6, v6, v10\n\t" \
  CH_DP6 \
  "v_cndmask_b32 v78, v13, v14, s[56:57]\n\t" \
  "v_cndmask_b32 v76, v12, v15, s[56:57]\n\t" \
  "v_add_f32 v7, v7, v11\n\t" \
  "v_max3_f32 v77, v4, v5, v6\n\t" \
  "v_add_f32_dpp v76, v78, v76 quad_perm:[2,3,0,1] row_mask:0xf bank_mask:0xf\n\t" \
  "v_max3_f32 v77, v77, v7, v76\n\t" \
  "v_smfmac_f32_16x16x64_bf16 v[56:59], v[20:23], v[168:175], v68\n\t" \
  "ds_read_b128 v[44:47], v70 offset:16384\n\t" \
  "v_max_f32_dpp v77, v77, v77 quad_perm:[2,3,0,1] row_mask:0xf bank_mask:0xf\n\t" \
  "v_mov_b32 v78, v77\n\t" \
  "v_smfmac_f32_16x16x64_bf16 v[52:55], v[24:27], v[112:119], v68\n\t" \
  "ds_read_b128 v[48:51], v70 offset:17408\n\t" \
  "v_permlane16_swap_b32 v77, v78\n\t" \
  "v_max_f32 v77, v77, v78\n\t" \
  "v_mov_b32 v78, v77\n\t" \
  "v_smfmac_f32_16x16x64_bf16 v[52:55], v[28:31], v[120:127], v68\n\t" \
  "ds_read_b128 v[16:19], v70 offset:18432\n\t" \
  "v_permlane32_swap_b32 v77, v78\n\t" \
  "v_max_f32 v77, v77, v78\n\t" \
  "v_smfmac_f32_16x16x64_bf16 v[52:55], v[32:35], v[128:135], v68\n\t" \
  "ds_read_b128 v[20:23], v70 offset:19456\n\t" \
  "v_mul_f32 v77, 0xbe0293ee, v77\n\t" \
  CH_DP7 \
  "v_smfmac_f32_16x16x64_bf16 v[56:59], v[24:27], v[176:183], v68\n\t" \
  "v_fmamk_f32 v4, v4, 0x3e0293ee, v77\n\t" \
  "v_smfmac_f32_16x16x64_bf16 v[56:59], v[28:31], v[184:191], v68\n\t" \
  "ds_read_b128 v[24:27], v70 offset:22528\n\t" \
  "v_fmamk_f32 v5, v5, 0x3e0293ee, v77\n\t" \
  "s_waitcnt lgkmcnt(5)\n\t" \
  "v_smfmac_f32_16x16x64_bf16 v[56:59], v[32:35], v[36:43], v68\n\t" \
  "ds_read_b128 v[28:31], v70 offset:23552\n\t" \
  "v_fmamk_f32 v6, v6, 0x3e0293ee, v77\n\t" \
  "v_fmamk_f32 v7, v7, 0x3e0293ee, v77\n\t" \
  "v_fmamk_f32 v76, v76, 0x3e0293ee, v77\n\t" \
  "v_exp_f32 v4, v4\n\t" \
  "v_exp_f32 v5, v5\n\t" \
  "v_exp_f32 v6, v6\n\t" \
  "v_exp_f32 v7, v7\n\t" \
  "v_cvt_pk_bf16_f32 v4, v4, v5\n\t" \
  "v_exp_f32 v76, v76\n\t" \
  "v_cvt_pk_bf16_f32 v5, v6, v7\n\t" \
  "v_mov_b32_dpp v12, v4 quad_perm:[0,1,0,1] row_mask:0xf bank_mask:0xf\n\t" \
  "v_mov_b32_dpp v14, v4 quad_perm:[2,3,2,3] row_mask:0xf bank_mask:0xf\n\t" \
  "v_mov_b32_dpp v13, v5 quad_perm:[0,1,0,1] row_mask:0xf bank_mask:0xf\n\t" \
  "v_mov_b32_dpp v15, v5 quad_perm:[2,3,2,3] row_mask:0xf bank_mask:0xf\n\t" \
  "v_add_f32 v52, v52, v54\n\t" \
  "v_add_f32 v53, v53, v55\n\t" \
  "s_waitcnt vmcnt(2)\n\t" \
  CH_DP8 \
  "v_mfma_f32_16x16x32_bf16 v[0:3], v[60:63], v[12:15], 0\n\t" \
  "ds_read_b128 v[36:39], v70 offset:20480\n\t" \
  "v_add_f32 v56, v56, v58\n\t" \
  "v_add_f32 v57, v57, v59\n\t" \
  "v_cvt_pk_bf16_f32 v52, v52, v53\n\t" \
  "v_cvt_pk_bf16_f32 v53, v56, v57\n\t" \
  "s_waitcnt vmcnt(1)\n\t" \
  "v_mfma_f32_16x16x32_bf16 v[4:7], v[64:67], v[12:15], 0\n\t" \
  "ds_read_b128 v[40:43], v70 offset:21504\n\t" \
  "v_mov_b64 v[56:57], s[88:89]\n\t" \
  "v_mov_b64 v[58:59], s[88:89]\n\t" \
  "v_mov_b32_dpp v78, v76 quad_perm:[0,1,0,1] row_mask:0xf bank_mask:0xf\n\t" \
  "v_mov_b32_dpp v79, v76 quad_perm:[2,3,2,3] row_mask:0xf bank_mask:0xf\n\t" \
  "v_mfma_f32_16x16x32_bf16 v[32:35], v[56:59], v[12:15], 0\n\t" \
  "ds_read_b128 v[60:63], v70 offset:24576\n\t" \
  "v_cvt_pk_bf16_f32 v11, v78, v79\n\t" \
  "v_cvt_pk_bf16_f32 v8, v78, v79\n\t" \
  "v_mov_b64 v[54:55], 0\n\t" \
  "v_mov_b32 v9, 0\n\t" \
  "v_mov_b32 v10, 0\n\t" \
  "v_mov_b64 v[14:15], 0\n\t" \
  "s_cmp_eq_u32 s48, 63\n\t" \
  "v_mfma_f32_16x16x32_bf16 v[32:35], v[54:57], v[8:11], v[32:35]\n\t" \
  "buffer_store_dwordx2 v[52:53], v71, %[X], s69 offen offset:3072 sc1\n\t" \
  "v_mfma_f32_16x16x32_bf16 v[0:3], v[52:55], v[8:11], v[0:3]\n\t" \
  "ds_read_b128 v[64:67], v70 offset:25600\n\t" \
  "v_mov_b64 v[56:57], 0\n\t" \
  "v_mov_b64 v[58:59], 0\n\t" \
  "v_mfma_f32_16x16x32_bf16 v[4:7], v[52:55], v[10:13], v[4:7]\n\t" \
  "buffer_load_dword v75, off, %[X], s80 offset:1024 sc1\n\t" \
  "s_cbranch_scc1 LLAST2_%=\n\t" \
  "v_rcp_f32 v74, v32\n\t" \
  "v_mov_b64 v[12:13], 0\n\t" \
  "v_mov_b32 v8, 0\n\t" \
  "v_mul_f32 v0, v0, v74\n\t" \
  "v_mul_f32 v1, v1, v74\n\t" \
  "v_mul_f32 v2, v2, v74\n\t" \
  "v_mul_f32 v3, v3, v74\n\t" \
  "v_cvt_pk_bf16_f32 v76, v0, v1\n\t" \
  "v_cvt_pk_bf16_f32 v77, v2, v3\n\t" \
  "v_pk_mul_f32 v[4:5], v[4:5], v[74:75] op_sel_hi:[1,0]\n\t" \
  "v_pk_mul_f32 v[6:7], v[6:7], v[74:75] op_sel_hi:[1,0]\n\t" \
  "v_cvt_pk_bf16_f32 v78, v4, v5\n\t" \
  "v_cvt_pk_bf16_f32 v79, v6, v7\n\t" \
  "ds_write_b128 v69, v[76:79] offset:6144\n\t" \
  CH_DP9 \
  "s_waitcnt lgkmcnt(9)\n\t" \
  "v_smfmac_f32_16x16x64_bf16 v[12:15], v[76:79], v[44:51], v68\n\t" \
  "ds_read_b128 v[44:47], v70 offset:26624\n\t" \
  "v_mov_b32 v11, 0\n\t" \
  "s_waitcnt lgkmcnt(8)\n\t" \
  "v_smfmac_f32_16x16x64_bf16 v[56:59], v[76:79], v[16:23], v68\n\t" \
  "ds_read_b128 v[48:51], v70 offset:27648\n\t" \
  "v_mov_b64 v[52:53], 0\n\t" \
  "s_waitcnt lgkmcnt(5)\n\t" \
  "v_smfmac_f32_16x16x64_bf16 v[8:11], v[76:79], v[36:43], v68\n\t" \
  "ds_read_b128 v[16:19], v70 offset:28672\n\t" \
  "v_and_b32 v73, 32, v69\n\t" \
  "v_smfmac_f32_16x16x64_bf16 v[52:55], v[76:79], v[24:31], v68\n\t" \
  "s_waitcnt lgkmcnt(3)\n\t" \
  "s_barrier\n\t" \
  CH_DP10 \
  "ds_read_b128 v[0:3], v69 offset:7168\n\t" \
  "ds_read_b128 v[4:7], v69 offset:4096\n\t" \
  "v_lshl_add_u32 v73, v73, 5, v69\n\t" \
  "v_lshrrev_b32 v71, 7, v69\n\t" \
  "v_and_b32 v74, 0x70, v69\n\t" \
  "v_lshl_add_u32 v71, v71, 14, v74\n\t" \
  "v_bfe_u32 v72, v69, 2, 7\n\t" \
  "v_and_b32 v74, 0x200, v69\n\t" \
  "s_waitcnt lgkmcnt(1)\n\t" \
  "v_smfmac_f32_16x16x64_bf16 v[8:11], v[0:3], v[60:67], v68\n\t" \
  "ds_read_b128 v[20:23], v70 offset:29696\n\t" \
  "v_mov_b64 v[76:77], 0\n\t" \
  "v_smfmac_f32_16x16x64_bf16 v[12:15], v[0:3], v[200:207], v68\n\t" \
  "ds_read_b128 v[32:35], v69 offset:5120\n\t" \
  "v_mov_b64 v[78:79], 0\n\t" \
  "v_smfmac_f32_16x16x64_bf16 v[52:55], v[0:3], v[44:51], v68\n\t" \
  "ds_read_b128 v[24:27], v70 offset:30720\n\t" \
  "v_lshl_add_u32 v72, v74, 3, v72\n\t" \
  "v_smfmac_f32_16x16x64_bf16 v[56:59], v[0:3], v[232:239], v68\n\t" \
  "ds_read_b128 v[28:31], v70 offset:31744\n\t" \
  "s_waitcnt lgkmcnt(4)\n\t" \
  "v_smfmac_f32_16x16x64_bf16 v[12:15], v[4:7], v[208:215], v68\n\t" \
  "ds_read_b128 v[60:63], v70 offset:32768\n\t" \
  "s_waitcnt lgkmcnt(4)\n\t" \
  "v_smfmac_f32_16x16x64_bf16 v[8:11], v[4:7], v[16:23], v68\n\t" \
  "ds_read_b128 v[64:67], v70 offset:33792\n\t" \
  "s_waitcnt lgkmcnt(4)\n\t" \
  "v_smfmac_f32_16x16x64_bf16 v[8:11], v[32:35], v[192:199], v68\n\t" \
  "ds_read_b128 v[36:39], v73 offset:4096\n\t" \
  "v_smfmac_f32_16x16x64_bf16 v[12:15], v[32:35], v[216:223], v68\n\t" \
  "ds_read_b128 v[40:43], v73 offset:6144\n\t" \
  "s_waitcnt lgkmcnt(4)\n\t" \
  "v_smfmac_f32_16x16x64_bf16 v[52:55], v[4:7], v[24:31], v68\n\t" \
  "ds_read_b128 v[44:47], v70 offset:34816\n\t" \
  "v_cvt_pk_bf16_f32 v8, v8, v9\n\t" \
  "v_smfmac_f32_16x16x64_bf16 v[52:55], v[32:35], v[224:231], v68\n\t" \
  "ds_read_b128 v[48:51], v70 offset:35840\n\t" \
  "v_cvt_pk_bf16_f32 v9, v10, v11\n\t" \
  "v_smfmac_f32_16x16x64_bf16 v[56:59], v[4:7], v[240:247], v68\n\t" \
  "v_cvt_pk_bf16_f32 v10, v12, v13\n\t" \
  "s_waitcnt lgkmcnt(4)\n\t" \
  "v_smfmac_f32_16x16x64_bf16 v[56:59], v[32:35], v[60:67], v68\n\t" \
  "v_cvt_pk_bf16_f32 v11, v14, v15\n\t" \
  "s_waitcnt lgkmcnt(3)\n\t" \
  "v_smfmac_f32_16x16x64_bf16 v[76:79], v[36:39], v[248:255], v68\n\t" \
  "buffer_store_dwordx4 v[8:11], v71, %[X], s54 offen sc1\n\t" \
  CH_DP11 \
  "v_cvt_pk_bf16_f32 v52, v52, v53\n\t" \
  "s_waitcnt lgkmcnt(0)\n\t" \
  "v_smfmac_f32_16x16x64_bf16 v[76:79], v[40:43], v[44:51], v68\n\t" \
  "v_cvt_pk_bf16_f32 v53, v54, v55\n\t" \
  "v_cvt_pk_bf16_f32 v54, v56, v57\n\t" \
  "v_cvt_pk_bf16_f32 v55, v58, v59\n\t" \
  "buffer_store_dwordx4 v[52:55], v71, %[X], s75 offen sc1\n\t" \
  "s_nop 2\n\t" \
  "v_add_f32 v76, v76, v78\n\t" \
  "v_add_f32 v77, v77, v79\n\t" \
  "v_cvt_pk_bf16_f32 v76, v76, v77\n\t" \
  "buffer_store_dword v76, v72, %[X], s55 offen sc1\n\t" \
  CH_DP12 \
  "s_memrealtime s[70:71]\n\t" \
  "s_waitcnt vmcnt(3)\n\t" \
  "v_readfirstlane_b32 s65, v75\n\t" \
  "s_barrier\n\t" \
  "s_waitcnt lgkmcnt(0)\n\t" \
  "s_not_b32 s65, s65\n\t" \
  "s_lshl_b32 s61, s70, 8\n\t" \
  "s_andn2_b32 s64, s65, 0xff\n\t" \
  "s_sub_u32 s64, s64, s61\n\t" \
  "s_ashr_i32 s64, s64, 8\n\t" \
  "s_cmp_eq_u32 s65, 0\n\t" \
  "s_cselect_b32 s64, " CH_DLF ", s64\n\t" \
  "s_max_i32 s64, s64, " CH_DLL1 "\n\t" \
  "s_add_u32 s64, s64, s70\n\t" \
  CH_ST2 \
  "s_add_u32 s55, s41, s53\n\t" \
  "s_mov_b32 s74, 0\n\t" \
  "s_movk_i32 s60, 8192\n\t" \
  "RWT2_%=:\n\t" \
  "s_memrealtime s[72:73]\n\t" \
  "s_sub_u32 s60, s60, 1\n\t" \
  "s_cselect_b32 s61, 0, -1\n\t" \
  "s_waitcnt lgkmcnt(0)\n\t" \
  "s_sub_i32 s73, s72, s64\n\t" \
  "s_and_b32 s73, s73, s61\n\t" \
  "s_cmp_lt_i32 s73, 0\n\t" \
  "s_cbranch_scc1 RWT2_%=\n\t" \
  "RRETRY2_%=:\n\t" \
  CH_DP13 \
  "buffer_load_dwordx4 v[0:3], v69, %[X], s55 offen offset:0 sc1\n\t" \
  "buffer_load_dwordx4 v[4:7], v69, %[X], s55 offen offset:1024 sc1\n\t" \
  "buffer_load_dwordx4 v[8:11], v69, %[X], s55 offen offset:2048 sc1\n\t" \
  "buffer_load_dwordx4 v[12:15], v69, %[X], s55 offen offset:3072 sc1\n\t" \
  "v_lshrrev_b32 v20, 2, v69\n\t" \
  "v_and_b32 v20, 16, v20\n\t" \
  "v_mov_b32 v21, 0x3f80\n\t" \
  "v_lshlrev_b32 v21, v20, v21\n\t" \
  "v_bfe_u32 v20, v69, 4, 2\n\t" \
  "v_cmp_eq_u32 vcc, 0, v20\n\t" \
  "s_nop 1\n\t" \
  "v_cndmask_b32 v32, 0, v21, vcc\n\t" \
  "v_cmp_eq_u32 vcc, 1, v20\n\t" \
  "s_nop 1\n\t" \
  "v_cndmask_b32 v33, 0, v21, vcc\n\t" \
  "v_cmp_eq_u32 vcc, 2, v20\n\t" \
  "s_nop 1\n\t" \
  "v_cndmask_b32 v34, 0, v21, vcc\n\t" \
  "v_cmp_eq_u32 vcc, 3, v20\n\t" \
  "s_nop 1\n\t" \
  "v_cndmask_b32 v35, 0, v21, vcc\n\t" \
  "v_bfe_u32 v38, v69, 7, 1\n\t" \
  "v_bfe_u32 v20, v69, 9, 1\n\t" \
  "v_lshl_add_u32 v38, v20, 1, v38\n\t" \
  "v_mul_u32_u24 v38, 16384, v38\n\t" \
  "v_and_b32 v20, 0x60, v69\n\t" \
  "v_add_u32 v38, v38, v20\n\t" \
  "v_bfe_u32 v20, v69, 8, 1\n\t" \
  "v_lshl_add_u32 v38, v20, 4, v38\n\t" \
  "v_bfe_u32 v20, v69, 4, 1\n\t" \
  "v_lshl_add_u32 v38, v20, 3, v38\n\t" \
  "s_add_u32 s54, s37, s53\n\t" \
  "v_mov_b32 v44, 0\n\t" \
  CH_DP17 \
  "s_waitcnt vmcnt(3)\n\t" \
  CH_DP14 \
  "v_mfma_f32_16x16x32_bf16 v[40:43], v[32:35], v[0:3], 0\n\t" \
  "v_max3_u32 v44, v44, v0, v1\n\t" \
  "v_max3_u32 v44, v44, v2, v3\n\t" \
  "s_waitcnt vmcnt(2)\n\t" \
  "v_mfma_f32_16x16x32_bf16 v[40:43], v[32:35], v[4:7], v[40:43]\n\t" \
  "v_max3_u32 v44, v44, v4, v5\n\t" \
  "v_max3_u32 v44, v44, v6, v7\n\t" \
  "s_waitcnt vmcnt(1)\n\t" \
  "v_mfma_f32_16x16x32_bf16 v[40:43], v[32:35], v[8:11], v[40:43]\n\t" \
  "v_max3_u32 v44, v44, v8, v9\n\t" \
  "v_max3_u32 v44, v44, v10, v11\n\t" \
  "s_waitcnt vmcnt(0)\n\t" \
  CH_DP15 \
  "v_mfma_f32_16x16x32_bf16 v[40:43], v[32:35], v[12:15], v[40:43]\n\t" \
  "v_max3_u32 v44, v44, v12, v13\n\t" \
  "v_max3_u32 v44, v44, v14, v15\n\t" \
  "v_cmp_eq_u32 vcc, -1, v44\n\t" \
  "s_cbranch_vccnz RFAIL2_%=\n\t" \
  "s_nop 3\n\t" \
  "v_add_f32_dpp v40, v40, v40 row_ror:8 row_mask:0xf bank_mask:0xf\n\t" \
  "v_add_f32_dpp v41, v41, v41 row_ror:8 row_mask:0xf bank_mask:0xf\n\t" \
  "v_add_f32_dpp v42, v42, v42 row_ror:8 row_mask:0xf bank_mask:0xf\n\t" \
  "v_add_f32_dpp v43, v43, v43 row_ror:8 row_mask:0xf bank_mask:0xf\n\t" \
  "v_cvt_pk_bf16_f32 v40, v40, v41\n\t" \
  "v_cvt_pk_bf16_f32 v41, v42, v43\n\t" \
  "buffer_store_dwordx2 v[40:41], v38, %[X], s54 offen sc1\n\t" \
  "RDONE2_%=:\n\t" \
  CH_DP16 \
  CH_ST4 \
  "s_barrier\n\t" \
  CH_ST3 \
  CH_TRC \
  "s_add_u32 s48, s48, 1\n\t" \
  "s_mov_b32 s49, s53\n\t" \
  "s_add_u32 s53, s53, " CH_SLOT "\n\t" \
  "s_add_u32 s60, s53, " CH_SKIP "\n\t" \
  "s_lshr_b32 s61, s96, 1\n\t" \
  "s_add_u32 s86, s86, 1\n\t" \
  "s_cmp_eq_u32 s86, 3\n\t" \
  "s_cselect_b32 s53, s60, s53\n\t" \
  "s_cselect_b32 s96, s61, s96\n\t" \
  "s_cselect_b32 s86, 0, s86\n\t" \
  "s_andn2_b32 s53, s53, 0x2000\n\t" \
  "s_and_b32 s60, s96, 1\n\t" \
  "s_lshl_b32 s60, s60, 13\n\t" \
  "s_or_b32 s53, s53, s60\n\t" \
  "s_mov_b32 s68, 0\n\t" \
  "s_add_u32 s82, s48, 0\n\t" \
  CH_SEGC \
  "s_lshr_b64 s[60:61], s[90:91], s82\n\t" \
  "s_and_b32 s60, s60, 1\n\t" \
  "s_lshl_b32 s60, s60, 13\n\t" \
  "s_add_u32 s82, s82, " CH_SG0 "\n\t" \
  "s_lshl_b32 s82, s82, 21\n\t" \
  "s_or_b32 s82, s82, s60\n\t" \
  "s_add_u32 s50, s82, s38\n\t" \
  "s_add_u32 s69, s82, s39\n\t" \
  "s_branch LG2_%=\n\t" \
  "RFAIL2_%=:\n\t" \
  "s_add_u32 s74, s74, 1\n\t" \
  "s_sleep " CH_RSL "\n\t" \
  "s_waitcnt vmcnt(0)\n\t" \
  "s_cmp_gt_u32 s74, s84\n\t" \
  "s_cbranch_scc1 RG2_%=\n\t" \
  "s_and_b32 s60, s74, 1023\n\t" \
  "s_cmp_lg_u32 s60, 0\n\t" \
  "s_cbranch_scc1 RRETRY2_%=\n\t" \
  "buffer_load_dword v46, off, %[X], s92 sc1\n\t" \
  "s_waitcnt vmcnt(0)\n\t" \
  "v_readfirstlane_b32 s60, v46\n\t" \
  "s_nop 0\n\t" \
  "s_cmp_eq_u32 s60, 0\n\t" \
  "s_cbranch_scc1 RRETRY2_%=\n\t" \
  "RG2_%=:\n\t" \
  "s_mov_b32 s84, 0\n\t" \
  "s_lshl_b32 s60, s36, 4\n\t" \
  "s_or_b32 s60, s60, s48\n\t" \
  "s_or_b32 s60, s60, 536870912\n\t" \
  "v_mov_b32 v46, s60\n\t" \
  "v_mov_b32 v47, 0\n\t" \
  "s_mov_b64 exec, 1\n\t" \
  "buffer_atomic_cmpswap v[46:47], off, %[X], s92 sc1\n\t" \
  "s_mov_b64 exec, -1\n\t" \
  "s_branch RDONE2_%=\n\t" \
  "LFAIL2_%=:\n\t" \
  "s_add_u32 s68, s68, 1\n\t" \
  "s_waitcnt lgkmcnt(0)\n\t" \
  "s_cmp_gt_u32 s68, s83\n\t" \
  "s_cbranch_scc1 LGV2_%=\n\t" \
  "s_barrier\n\t" \
  "s_branch LG2_%=\n\t" \
  "LGV2_%=:\n\t" \
  "s_mov_b32 s83, 0\n\t" \
  "s_lshl_b32 s60, s36, 4\n\t" \
  "s_or_b32 s60, s60, s48\n\t" \
  "s_or_b32 s60, s60, 805306368\n\t" \
  "v_mov_b32 v46, s60\n\t" \
  "v_mov_b32 v47, 0\n\t" \
  "s_mov_b64 exec, 1\n\t" \
  "buffer_atomic_cmpswap v[46:47], off, %[X], s92 sc1\n\t" \
  "s_mov_b64 exec, -1\n\t" \
  "s_branch LGO2_%=\n\t" \
  "LLAST2_%=:\n\t" \
  "s_waitcnt vmcnt(0) lgkmcnt(0)\n\t" \
  "s_barrier\n\t" \
  "s_branch LEND_%=\n\t" \
  "LG3_%=:\n\t" \
  "s_barrier\n\t" \
  "buffer_load_dwordx4 v[52:55], v69, %[X], s50 offen sc1\n\t" \
  "buffer_load_dwordx4 v[56:59], v69, %[X], s50 offen offset:1024 sc1\n\t" \
  "s_add_u32 s64, s50, 16384\n\t" \
  "s_movk_i32 m0, 11264\n\t" \
  "v_lshlrev_b32 v75, 1, v69\n\t" \
  "buffer_load_dword v75, %[X], s64 offen sc1 lds\n\t" \
  "ds_read_b128 v[44:47], v70 offset:0\n\t" \
  "ds_read_b128 v[48:51], v70 offset:1024\n\t" \
  "ds_read_b128 v[60:63], v70 offset:2048\n\t" \
  "ds_read_b128 v[64:67], v70 offset:3072\n\t" \
  "v_mov_b64 v[36:37], 0\n\t" \
  "v_mov_b64 v[38:39], 0\n\t" \
  "v_mov_b64 v[40:41], 0\n\t" \
  "v_mov_b64 v[42:43], 0\n\t" \
  "v_mov_b32 v73, 0\n\t" \
  "v_lshrrev_b32 v71, 1, v69\n\t" \
  "v_add_u32 v71, 4, v71\n\t" \
  "v_lshrrev_b32 v72, 3, v69\n\t" \
  "v_and_b32 v72, 0x78, v72\n\t" \
  "v_and_b32 v74, 0x30, v69\n\t" \
  "v_lshl_add_u32 v72, v74, 3, v72\n\t" \
  "s_add_u32 s54, s53, s36\n\t" \
  "s_add_u32 s55, s54, 573440\n\t" \
  "s_add_u32 s54, s54, 135168\n\t" \
  "s_add_u32 s75, s54, 262144\n\t" \
  "s_add_u32 s80, s53, s44\n\t" \
  "s_or_b32 s87, s50, 0x1fff80\n\t" \
  "s_add_u32 s87, s87, 4194304\n\t" \
  CH_TC0 \
  "s_barrier\n\t" \
  CH_DP0 \
  "ds_read_b128 v[0:3], v69 offset:0\n\t" \
  "ds_read_b128 v[4:7], v69 offset:1024\n\t" \
  "ds_read_b128 v[8:11], v69 offset:2048\n\t" \
  "ds_read_b128 v[12:15], v69 offset:3072\n\t" \
  "s_waitcnt lgkmcnt(3)\n\t" \
  "v_smfmac_f32_16x16x64_bf16 v[36:39], v[0:3], v[44:51], v68\n\t" \
  "ds_read_b128 v[44:47], v70 offset:4096\n\t" \
  "v_smfmac_f32_16x16x64_bf16 v[40:43], v[0:3], v[60:67], v68\n\t" \
  "ds_read_b128 v[48:51], v70 offset:5120\n\t" \
  "v_max3_u32 v73, v73, v0, v2\n\t" \
  "s_waitcnt lgkmcnt(4)\n\t" \
  "v_smfmac_f32_16x16x64_bf16 v[36:39], v[4:7], a[0:7], v68\n\t" \
  "s_barrier\n\t" \
  "ds_read_b128 v[16:19], v69 offset:4096\n\t" \
  CH_DP1 \
  "v_smfmac_f32_16x16x64_bf16 v[40:43], v[4:7], a[64:71], v68\n\t" \
  "ds_read_b128 v[20:23], v69 offset:5120\n\t" \
  "v_max3_u32 v73, v73, v4, v6\n\t" \
  "s_waitcnt lgkmcnt(5)\n\t" \
  "v_smfmac_f32_16x16x64_bf16 v[36:39], v[8:11], a[8:15], v68\n\t" \
  "ds_read_b128 v[24:27], v69 offset:6144\n\t" \
  "v_max3_u32 v73, v73, v8, v10\n\t" \
  "v_smfmac_f32_16x16x64_bf16 v[40:43], v[8:11], a[72:79], v68\n\t" \
  "ds_read_b128 v[28:31], v69 offset:7168\n\t" \
  "s_waitcnt lgkmcnt(6)\n\t" \
  "v_max3_u32 v73, v73, v12, v14\n\t" \
  "v_smfmac_f32_16x16x64_bf16 v[36:39], v[12:15], a[16:23], v68\n\t" \
  "ds_read_b128 v[32:35], v69 offset:8192\n\t" \
  "s_waitcnt lgkmcnt(5)\n\t" \
  "v_smfmac_f32_16x16x64_bf16 v[40:43], v[12:15], v[44:51], v68\n\t" \
  "ds_read_b128 v[60:63], v70 offset:6144\n\t" \
  "s_waitcnt lgkmcnt(5)\n\t" \
  "v_smfmac_f32_16x16x64_bf16 v[36:39], v[16:19], a[24:31], v68\n\t" \
  "ds_read_b128 v[64:67], v70 offset:7168\n\t" \
  "v_max3_u32 v73, v73, v16, v18\n\t" \
  "v_smfmac_f32_16x16x64_bf16 v[40:43], v[16:19], a[80:87], v68\n\t" \
  "s_waitcnt lgkmcnt(5)\n\t" \
  "v_max3_u32 v73, v73, v20, v22\n\t" \
  "v_smfmac_f32_16x16x64_bf16 v[36:39], v[20:23], a[32:39], v68\n\t" \
  "s_waitcnt lgkmcnt(4)\n\t" \
  "v_max3_u32 v73, v73, v24, v26\n\t" \
  "v_smfmac_f32_16x16x64_bf16 v[36:39], v[24:27], a[40:47], v68\n\t" \
  "s_waitcnt lgkmcnt(3)\n\t" \
  "v_max3_u32 v73, v73, v28, v30\n\t" \
  "v_smfmac_f32_16x16x64_bf16 v[36:39], v[28:31], a[48:55], v68\n\t" \
  "s_waitcnt lgkmcnt(2)\n\t" \
  "v_max3_u32 v73, v73, v32, v34\n\t" \
  "v_smfmac_f32_16x16x64_bf16 v[36:39], v[32:35], a[56:63], v68\n\t" \
  "v_cmp_eq_u32 vcc, -1, v73\n\t" \
  "s_waitcnt lgkmcnt(0)\n\t" \
  "v_smfmac_f32_16x16x64_bf16 v[40:43], v[20:23], v[60:67], v68\n\t" \
  "ds_read_b128 v[60:63], v70 offset:8192\n\t" \
  "v_mov_b64 v[44:45], 0\n\t" \
  "s_cbranch_vccnz LFAIL3_%=\n\t" \
  "LGO3_%=:\n\t" \
  "v_smfmac_f32_16x16x64_bf16 v[40:43], v[24:27], a[88:95], v68\n\t" \
  "ds_read_b128 v[64:67], v70 offset:9216\n\t" \
  "v_mov_b64 v[46:47], 0\n\t" \
  "v_smfmac_f32_16x16x64_bf16 v[40:43], v[28:31], a[96:103], v68\n\t" \
  "v_add_f32 v74, v36, v38\n\t" \
  "v_smfmac_f32_16x16x64_bf16 v[40:43], v[32:35], a[104:111], v68\n\t" \
  "v_add_f32 v37, v37, v39\n\t" \
  CH_DP2 \
  "v_smfmac_f32_16x16x64_bf16 v[44:47], v[0:3], a[112:119], v68\n\t" \
  "v_cvt_pk_bf16_f32 v74, v74, v37\n\t" \
  "v_smfmac_f32_16x16x64_bf16 v[44:47], v[4:7], a[120:127], v68\n\t" \
  "v_add_f32 v75, v40, v42\n\t" \
  "s_waitcnt lgkmcnt(0)\n\t" \
  "v_smfmac_f32_16x16x64_bf16 v[44:47], v[8:11], v[60:67], v68\n\t" \
  "ds_read_b128 v[60:63], v70 offset:10240\n\t" \
  "v_add_f32 v41, v41, v43\n\t" \
  "v_cvt_pk_bf16_f32 v75, v75, v41\n\t" \
  "v_smfmac_f32_16x16x64_bf16 v[44:47], v[12:15], a[128:135], v68\n\t" \
  "ds_write2st64_b32 v71, v74, v75 offset0:40 offset1:42\n\t" \
  "v_mov_b64 v[48:49], 0\n\t" \
  "v_smfmac_f32_16x16x64_bf16 v[44:47], v[16:19], a[136:143], v68\n\t" \
  "ds_read_b128 v[64:67], v70 offset:11264\n\t" \
  "v_mov_b64 v[50:51], 0\n\t" \
  "v_smfmac_f32_16x16x64_bf16 v[44:47], v[20:23], a[144:151], v68\n\t" \
  "v_smfmac_f32_16x16x64_bf16 v[44:47], v[24:27], a[152:159], v68\n\t" \
  "v_smfmac_f32_16x16x64_bf16 v[44:47], v[28:31], a[160:167], v68\n\t" \
  "s_waitcnt lgkmcnt(0)\n\t" \
  "v_smfmac_f32_16x16x64_bf16 v[44:47], v[32:35], v[60:67], v68\n\t" \
  "s_barrier\n\t" \
  "ds_read_b64_tr_b16 v[76:77], v72 offset:10240\n\t" \
  CH_DP3 \
  "v_smfmac_f32_16x16x64_bf16 v[48:51], v[0:3], a[168:175], v68\n\t" \
  "ds_read_b64_tr_b16 v[78:79], v72 offset:10752\n\t" \
  "v_smfmac_f32_16x16x64_bf16 v[48:51], v[4:7], a[176:183], v68\n\t" \
  "v_add_f32 v74, v44, v46\n\t" \
  "v_smfmac_f32_16x16x64_bf16 v[48:51], v[8:11], a[184:191], v68\n\t" \
  "buffer_load_dwordx4 v[60:63], v69, %[X], s64 offen sc1\n\t" \
  "v_add_f32 v45, v45, v47\n\t" \
  "v_smfmac_f32_16x16x64_bf16 v[48:51], v[12:15], a[192:199], v68\n\t" \
  "v_cvt_pk_bf16_f32 v74, v74, v45\n\t" \
  "s_waitcnt vmcnt(3)\n\t" \
  "s_waitcnt lgkmcnt(0)\n\t" \
  "v_mfma_f32_16x16x32_bf16 v[36:39], v[52:55], v[76:79], 0\n\t" \
  "s_waitcnt vmcnt(2)\n\t" \
  "v_mfma_f32_16x16x32_bf16 v[40:43], v[56:59], v[76:79], 0\n\t" \
  "v_mov_b64 v[52:53], 0\n\t" \
  "v_mov_b64 v[54:55], 0\n\t" \
  "v_smfmac_f32_16x16x64_bf16 v[48:51], v[16:19], a[200:207], v68\n\t" \
  "buffer_load_dwordx4 v[64:67], v69, %[X], s64 offen offset:1024 sc1\n\t" \
  "v_mov_b64 v[56:57], 0\n\t" \
  "v_smfmac_f32_16x16x64_bf16 v[48:51], v[20:23], a[208:215], v68\n\t" \
  "v_add_f32_dpp v36, v40, v36 quad_perm:[2,3,0,1] row_mask:0xf bank_mask:0xf\n\t" \
  "v_smfmac_f32_16x16x64_bf16 v[48:51], v[24:27], a[216:223], v68\n\t" \
  "v_add_f32_dpp v37, v41, v37 quad_perm:[2,3,0,1] row_mask:0xf bank_mask:0xf\n\t" \
  "v_smfmac_f32_16x16x64_bf16 v[48:51], v[28:31], a[224:231], v68\n\t" \
  "v_add_f32_dpp v38, v42, v38 quad_perm:[2,3,0,1] row_mask:0xf bank_mask:0xf\n\t" \
  "v_smfmac_f32_16x16x64_bf16 v[48:51], v[32:35], a[232:239], v68\n\t" \
  "v_add_f32_dpp v39, v43, v39 quad_perm:[2,3,0,1] row_mask:0xf bank_mask:0xf\n\t" \
  CH_DP4 \
  "v_smfmac_f32_16x16x64_bf16 v[52:55], v[0:3], a[240:247], v68\n\t" \
  "s_mov_b64 exec, s[58:59]\n\t" \
  "ds_write_b128 v69, v[36:39] offset:3104\n\t" \
  "s_mov_b64 exec, -1\n\t" \
  "v_mov_b64 v[58:59], 0\n\t" \
  "v_smfmac_f32_16x16x64_bf16 v[52:55], v[4:7], a[248:255], v68\n\t" \
  "buffer_load_dword off, %[X], s87 sc1 lds\n\t" \
  "v_add_f32 v75, v48, v50\n\t" \
  "v_add_f32 v49, v49, v51\n\t" \
  "v_cvt_pk_bf16_f32 v75, v75, v49\n\t" \
  "v_smfmac_f32_16x16x64_bf16 v[56:59], v[0:3], v[136:143], v68\n\t" \
  "ds_write2st64_b32 v71, v74, v75 offset0:4 offset1:6\n\t" \
  "v_smfmac_f32_16x16x64_bf16 v[56:59], v[4:7], v[144:151], v68\n\t" \
  "ds_read_b64_tr_b16 v[0:1], v72 offset:9216\n\t" \
  "ds_read_b64_tr_b16 v[2:3], v72 offset:9728\n\t" \
  "v_smfmac_f32_16x16x64_bf16 v[52:55], v[8:11], v[80:87], v68\n\t" \
  "ds_read_b128 v[36:39], v70 offset:12288\n\t" \
  "v_smfmac_f32_16x16x64_bf16 v[56:59], v[8:11], v[152:159], v68\n\t" \
  "ds_read_b128 v[40:43], v70 offset:13312\n\t" \
  "v_smfmac_f32_16x16x64_bf16 v[52:55], v[12:15], v[88:95], v68\n\t" \
  "s_waitcnt lgkmcnt(4)\n\t" \
  "s_barrier\n\t" \
  CH_DP5 \
  "ds_read_b64_tr_b16 v[44:45], v72 offset:1024\n\t" \
  "ds_read_b64_tr_b16 v[46:47], v72 offset:1536\n\t" \
  "v_smfmac_f32_16x16x64_bf16 v[56:59], v[12:15], v[160:167], v68\n\t" \
  "ds_read_b64_tr_b16 v[48:49], v72 offset:0\n\t" \
  "ds_read_b64_tr_b16 v[50:51], v72 offset:512\n\t" \
  "v_smfmac_f32_16x16x64_bf16 v[52:55], v[16:19], v[96:103], v68\n\t" \
  "ds_read_b128 v[4:7], v69 offset:2048\n\t" \
  "s_waitcnt lgkmcnt(5)\n\t" \
  "v_smfmac_f32_16x16x64_bf16 v[56:59], v[16:19], v[36:43], v68\n\t" \
  "ds_read_b128 v[8:11], v69 offset:3072\n\t" \
  "s_waitcnt lgkmcnt(4)\n\t" \
  "v_mfma_f32_16x16x32_bf16 v[12:15], v[44:47], v[76:79], 0\n\t" \
  "ds_read_b128 v[36:39], v70 offset:14336\n\t" \
  "s_waitcnt lgkmcnt(3)\n\t" \
  "v_mfma_f32_16x16x32_bf16 v[12:15], v[48:51], v[0:3], v[12:15]\n\t" \
  "ds_read_b128 v[40:43], v70 offset:15360\n\t" \
  "v_smfmac_f32_16x16x64_bf16 v[52:55], v[20:23], v[104:111], v68\n\t" \
  "s_waitcnt lgkmcnt(2)\n\t" \
  "v_add_f32 v4, v4, v8\n\t" \
  "v_add_f32 v5, v5, v9\n\t" \
  "v_add_f32 v6, v6, v10\n\t" \
  CH_DP6 \
  "v_cndmask_b32 v78, v13, v14, s[56:57]\n\t" \
  "v_cndmask_b32 v76, v12, v15, s[56:57]\n\t" \
  "v_add_f32 v7, v7, v11\n\t" \
  "v_max3_f32 v77, v4, v5, v6\n\t" \
  "v_add_f32_dpp v76, v78, v76 quad_perm:[2,3,0,1] row_mask:0xf bank_mask:0xf\n\t" \
  "v_max3_f32 v77, v77, v7, v76\n\t" \
  "v_smfmac_f32_16x16x64_bf16 v[56:59], v[20:23], v[168:175], v68\n\t" \
  "ds_read_b128 v[44:47], v70 offset:16384\n\t" \
  "v_max_f32_dpp v77, v77, v77 quad_perm:[2,3,0,1] row_mask:0xf bank_mask:0xf\n\t" \
  "v_mov_b32 v78, v77\n\t" \
  "v_smfmac_f32_16x16x64_bf16 v[52:55], v[24:27], v[112:119], v68\n\t" \
  "ds_read_b128 v[48:51], v70 offset:17408\n\t" \
  "v_permlane16_swap_b32 v77, v78\n\t" \
  "v_max_f32 v77, v77, v78\n\t" \
  "v_mov_b32 v78, v77\n\t" \
  "v_smfmac_f32_16x16x64_bf16 v[52:55], v[28:31], v[120:127], v68\n\t" \
  "ds_read_b128 v[16:19], v70 offset:18432\n\t" \
  "v_permlane32_swap_b32 v77, v78\n\t" \
  "v_max_f32 v77, v77, v78\n\t" \
  "v_smfmac_f32_16x16x64_bf16 v[52:55], v[32:35], v[128:135], v68\n\t" \
  "ds_read_b128 v[20:23], v70 offset:19456\n\t" \
  "v_mul_f32 v77, 0xbe0293ee, v77\n\t" \
  CH_DP7 \
  "v_smfmac_f32_16x16x64_bf16 v[56:59], v[24:27], v[176:183], v68\n\t" \
  "v_fmamk_f32 v4, v4, 0x3e0293ee, v77\n\t" \
  "v_smfmac_f32_16x16x64_bf16 v[56:59], v[28:31], v[184:191], v68\n\t" \
  "ds_read_b128 v[24:27], v70 offset:22528\n\t" \
  "v_fmamk_f32 v5, v5, 0x3e0293ee, v77\n\t" \
  "s_waitcnt lgkmcnt(5)\n\t" \
  "v_smfmac_f32_16x16x64_bf16 v[56:59], v[32:35], v[36:43], v68\n\t" \
  "ds_read_b128 v[28:31], v70 offset:23552\n\t" \
  "v_fmamk_f32 v6, v6, 0x3e0293ee, v77\n\t" \
  "v_fmamk_f32 v7, v7, 0x3e0293ee, v77\n\t" \
  "v_fmamk_f32 v76, v76, 0x3e0293ee, v77\n\t" \
  "v_exp_f32 v4, v4\n\t" \
  "v_exp_f32 v5, v5\n\t" \
  "v_exp_f32 v6, v6\n\t" \
  "v_exp_f32 v7, v7\n\t" \
  "v_cvt_pk_bf16_f32 v4, v4, v5\n\t" \
  "v_exp_f32 v76, v76\n\t" \
  "v_cvt_pk_bf16_f32 v5, v6, v7\n\t" \
  "v_mov_b32_dpp v12, v4 quad_perm:[0,1,0,1] row_mask:0xf bank_mask:0xf\n\t" \
  "v_mov_b32_dpp v14, v4 quad_perm:[2,3,2,3] row_mask:0xf bank_mask:0xf\n\t" \
  "v_mov_b32_dpp v13, v5 quad_perm:[0,1,0,1] row_mask:0xf bank_mask:0xf\n\t" \
  "v_mov_b32_dpp v15, v5 quad_perm:[2,3,2,3] row_mask:0xf bank_mask:0xf\n\t" \
  "v_add_f32 v52, v52, v54\n\t" \
  "v_add_f32 v53, v53, v55\n\t" \
  "s_waitcnt vmcnt(2)\n\t" \
  CH_DP8 \
  "v_mfma_f32_16x16x32_bf16 v[0:3], v[60:63], v[12:15], 0\n\t" \
  "buffer_load_dword v75, off, %[X], s80 offset:1024 sc1\n\t" \
  "ds_read_b128 v[36:39], v70 offset:20480\n\t" \
  "v_add_f32 v56, v56, v58\n\t" \
  "v_add_f32 v57, v57, v59\n\t" \
  "v_cvt_pk_bf16_f32 v52, v52, v53\n\t" \
  "v_cvt_pk_bf16_f32 v53, v56, v57\n\t" \
  "s_waitcnt vmcnt(2)\n\t" \
  "v_mfma_f32_16x16x32_bf16 v[4:7], v[64:67], v[12:15], 0\n\t" \
  "ds_read_b128 v[40:43], v70 offset:21504\n\t" \
  "v_mov_b64 v[56:57], s[88:89]\n\t" \
  "v_mov_b64 v[58:59], s[88:89]\n\t" \
  "v_mov_b32_dpp v78, v76 quad_perm:[0,1,0,1] row_mask:0xf bank_mask:0xf\n\t" \
  "v_mov_b32_dpp v79, v76 quad_perm:[2,3,2,3] row_mask:0xf bank_mask:0xf\n\t" \
  "v_mfma_f32_16x16x32_bf16 v[32:35], v[56:59], v[12:15], 0\n\t" \
  "ds_read_b128 v[60:63], v70 offset:24576\n\t" \
  "v_cvt_pk_bf16_f32 v11, v78, v79\n\t" \
  "v_cvt_pk_bf16_f32 v8, v78, v79\n\t" \
  "v_mov_b64 v[54:55], 0\n\t" \
  "v_mov_b32 v9, 0\n\t" \
  "v_mov_b32 v10, 0\n\t" \
  "v_mov_b64 v[14:15], 0\n\t" \
  "s_cmp_eq_u32 s48, 63\n\t" \
  "v_mfma_f32_16x16x32_bf16 v[32:35], v[54:57], v[8:11], v[32:35]\n\t" \
  "v_mfma_f32_16x16x32_bf16 v[0:3], v[52:55], v[8:11], v[0:3]\n\t" \
  "buffer_store_dwordx2 v[52:53], v71, %[X], s69 offen offset:3580 sc1\n\t" \
  "ds_read_b128 v[64:67], v70 offset:25600\n\t" \
  "v_mov_b64 v[56:57], 0\n\t" \
  "v_mov_b64 v[58:59], 0\n\t" \
  "v_mfma_f32_16x16x32_bf16 v[4:7], v[52:55], v[10:13], v[4:7]\n\t" \
  "s_cbranch_scc1 LLAST3_%=\n\t" \
  "v_rcp_f32 v74, v32\n\t" \
  "v_mov_b64 v[12:13], 0\n\t" \
  "v_mov_b32 v8, 0\n\t" \
  "v_mul_f32 v0, v0, v74\n\t" \
  "v_mul_f32 v1, v1, v74\n\t" \
  "v_mul_f32 v2, v2, v74\n\t" \
  "v_mul_f32 v3, v3, v74\n\t" \
  "v_cvt_pk_bf16_f32 v76, v0, v1\n\t" \
  "v_cvt_pk_bf16_f32 v77, v2, v3\n\t" \
  "v_pk_mul_f32 v[4:5], v[4:5], v[74:75] op_sel_hi:[1,0]\n\t" \
  "v_pk_mul_f32 v[6:7], v[6:7], v[74:75] op_sel_hi:[1,0]\n\t" \
  "v_cvt_pk_bf16_f32 v78, v4, v5\n\t" \
  "v_cvt_pk_bf16_f32 v79, v6, v7\n\t" \
  "ds_write_b128 v69, v[76:79] offset:7168\n\t" \
  CH_DP9 \
  "s_waitcnt lgkmcnt(9)\n\t" \
  "v_smfmac_f32_16x16x64_bf16 v[12:15], v[76:79], v[44:51], v68\n\t" \
  "ds_read_b128 v[44:47], v70 offset:26624\n\t" \
  "v_mov_b32 v11, 0\n\t" \
  "s_waitcnt lgkmcnt(8)\n\t" \
  "v_smfmac_f32_16x16x64_bf16 v[56:59], v[76:79], v[16:23], v68\n\t" \
  "ds_read_b128 v[48:51], v70 offset:27648\n\t" \
  "v_mov_b64 v[52:53], 0\n\t" \
  "s_waitcnt lgkmcnt(5)\n\t" \
  "v_smfmac_f32_16x16x64_bf16 v[8:11], v[76:79], v[36:43], v68\n\t" \
  "ds_read_b128 v[16:19], v70 offset:28672\n\t" \
  "v_and_b32 v73, 32, v69\n\t" \
  "v_smfmac_f32_16x16x64_bf16 v[52:55], v[76:79], v[24:31], v68\n\t" \
  "s_waitcnt lgkmcnt(3)\n\t" \
  "s_barrier\n\t" \
  CH_DP10 \
  "ds_read_b128 v[0:3], v69 offset:6144\n\t" \
  "ds_read_b128 v[4:7], v69 offset:5120\n\t" \
  "v_lshl_add_u32 v73, v73, 5, v69\n\t" \
  "v_lshrrev_b32 v71, 7, v69\n\t" \
  "v_and_b32 v74, 0x70, v69\n\t" \
  "v_lshl_add_u32 v71, v71, 14, v74\n\t" \
  "v_bfe_u32 v72, v69, 2, 7\n\t" \
  "v_and_b32 v74, 0x200, v69\n\t" \
  "s_waitcnt lgkmcnt(1)\n\t" \
  "v_smfmac_f32_16x16x64_bf16 v[8:11], v[0:3], v[60:67], v68\n\t" \
  "ds_read_b128 v[20:23], v70 offset:29696\n\t" \
  "v_mov_b64 v[76:77], 0\n\t" \
  "v_smfmac_f32_16x16x64_bf16 v[12:15], v[0:3], v[200:207], v68\n\t" \
  "ds_read_b128 v[32:35], v69 offset:4096\n\t" \
  "v_mov_b64 v[78:79], 0\n\t" \
  "v_smfmac_f32_16x16x64_bf16 v[52:55], v[0:3], v[44:51], v68\n\t" \
  "ds_read_b128 v[24:27], v70 offset:30720\n\t" \
  "v_lshl_add_u32 v72, v74, 3, v72\n\t" \
  "v_smfmac_f32_16x16x64_bf16 v[56:59], v[0:3], v[232:239], v68\n\t" \
  "ds_read_b128 v[28:31], v70 offset:31744\n\t" \
  "s_waitcnt lgkmcnt(4)\n\t" \
  "v_smfmac_f32_16x16x64_bf16 v[12:15], v[4:7], v[208:215], v68\n\t" \
  "ds_read_b128 v[60:63], v70 offset:32768\n\t" \
  "s_waitcnt lgkmcnt(4)\n\t" \
  "v_smfmac_f32_16x16x64_bf16 v[8:11], v[4:7], v[16:23], v68\n\t" \
  "ds_read_b128 v[64:67], v70 offset:33792\n\t" \
  "s_waitcnt lgkmcnt(4)\n\t" \
  "v_smfmac_f32_16x16x64_bf16 v[8:11], v[32:35], v[192:199], v68\n\t" \
  "ds_read_b128 v[36:39], v73 offset:4096\n\t" \
  "v_smfmac_f32_16x16x64_bf16 v[12:15], v[32:35], v[216:223], v68\n\t" \
  "ds_read_b128 v[40:43], v73 offset:6144\n\t" \
  "s_waitcnt lgkmcnt(4)\n\t" \
  "v_smfmac_f32_16x16x64_bf16 v[52:55], v[4:7], v[24:31], v68\n\t" \
  "ds_read_b128 v[44:47], v70 offset:34816\n\t" \
  "v_cvt_pk_bf16_f32 v8, v8, v9\n\t" \
  "v_smfmac_f32_16x16x64_bf16 v[52:55], v[32:35], v[224:231], v68\n\t" \
  "ds_read_b128 v[48:51], v70 offset:35840\n\t" \
  "v_cvt_pk_bf16_f32 v9, v10, v11\n\t" \
  "v_smfmac_f32_16x16x64_bf16 v[56:59], v[4:7], v[240:247], v68\n\t" \
  "v_cvt_pk_bf16_f32 v10, v12, v13\n\t" \
  "s_waitcnt lgkmcnt(4)\n\t" \
  "v_smfmac_f32_16x16x64_bf16 v[56:59], v[32:35], v[60:67], v68\n\t" \
  "v_cvt_pk_bf16_f32 v11, v14, v15\n\t" \
  "s_waitcnt lgkmcnt(3)\n\t" \
  "v_smfmac_f32_16x16x64_bf16 v[76:79], v[36:39], v[248:255], v68\n\t" \
  "buffer_store_dwordx4 v[8:11], v71, %[X], s54 offen sc1\n\t" \
  CH_DP11 \
  "v_cvt_pk_bf16_f32 v52, v52, v53\n\t" \
  "s_waitcnt lgkmcnt(0)\n\t" \
  "v_smfmac_f32_16x16x64_bf16 v[76:79], v[40:43], v[44:51], v68\n\t" \
  "v_cvt_pk_bf16_f32 v53, v54, v55\n\t" \
  "v_cvt_pk_bf16_f32 v54, v56, v57\n\t" \
  "v_cvt_pk_bf16_f32 v55, v58, v59\n\t" \
  "buffer_store_dwordx4 v[52:55], v71, %[X], s75 offen sc1\n\t" \
  "s_nop 2\n\t" \
  "v_add_f32 v76, v76, v78\n\t" \
  "v_add_f32 v77, v77, v79\n\t" \
  "v_cvt_pk_bf16_f32 v76, v76, v77\n\t" \
  "buffer_store_dword v76, v72, %[X], s55 offen sc1\n\t" \
  CH_DP12 \
  "s_memrealtime s[70:71]\n\t" \
  "s_waitcnt vmcnt(3)\n\t" \
  "v_readfirstlane_b32 s65, v75\n\t" \
  "s_barrier\n\t" \
  "s_waitcnt lgkmcnt(0)\n\t" \
  "s_not_b32 s65, s65\n\t" \
  "s_lshl_b32 s61, s70, 8\n\t" \
  "s_andn2_b32 s64, s65, 0xff\n\t" \
  "s_sub_u32 s64, s64, s61\n\t" \
  "s_ashr_i32 s64, s64, 8\n\t" \
  "s_cmp_eq_u32 s65, 0\n\t" \
  "s_cselect_b32 s64, " CH_DLF ", s64\n\t" \
  "s_max_i32 s64, s64, " CH_DLL1 "\n\t" \
  "s_add_u32 s64, s64, s70\n\t" \
  CH_ST2 \
  "s_add_u32 s55, s41, s53\n\t" \
  "s_mov_b32 s74, 0\n\t" \
  "v_and_b32 v37, 0x30, v69\n\t" \
  "v_add_u32 v37, s52, v37\n\t" \
  "s_mov_b32 exec_lo, 0\n\t" \
  "v_mov_b32 v37, s85\n\t" \
  "s_mov_b32 exec_lo, -1\n\t" \
  "s_cmp_lt_i32 s41, 0\n\t" \
  "s_cbranch_scc1 LIDLE_%=\n\t" \
  "s_movk_i32 s60, 8192\n\t" \
  "RWT3_%=:\n\t" \
  "s_memrealtime s[72:73]\n\t" \
  "s_sub_u32 s60, s60, 1\n\t" \
  "s_cselect_b32 s61, 0, -1\n\t" \
  "s_waitcnt lgkmcnt(0)\n\t" \
  "s_sub_i32 s73, s72, s64\n\t" \
  "s_and_b32 s73, s73, s61\n\t" \
  "s_cmp_lt_i32 s73, 0\n\t" \
  "s_cbranch_scc1 RWT3_%=\n\t" \
  "RRETRY3_%=:\n\t" \
  CH_DP13 \
  "buffer_load_dwordx4 v[0:3], v69, %[X], s55 offen offset:0 sc1\n\t" \
  "buffer_load_dwordx4 v[4:7], v69, %[X], s55 offen offset:1024 sc1\n\t" \
  "buffer_load_dwordx4 v[8:11], v69, %[X], s55 offen offset:2048 sc1\n\t" \
  "buffer_load_dwordx4 v[12:15], v69, %[X], s55 offen offset:3072 sc1\n\t" \
  "buffer_load_dword v39, v37, %[X], 0 offen sc1\n\t" \
  "v_lshrrev_b32 v20, 2, v69\n\t" \
  "v_and_b32 v20, 16, v20\n\t" \
  "v_mov_b32 v21, 0x3f80\n\t" \
  "v_lshlrev_b32 v21, v20, v21\n\t" \
  "v_bfe_u32 v20, v69, 4, 2\n\t" \
  "v_cmp_eq_u32 vcc, 0, v20\n\t" \
  "s_nop 1\n\t" \
  "v_cndmask_b32 v32, 0, v21, vcc\n\t" \
  "v_cmp_eq_u32 vcc, 1, v20\n\t" \
  "s_nop 1\n\t" \
  "v_cndmask_b32 v33, 0, v21, vcc\n\t" \
  "v_cmp_eq_u32 vcc, 2, v20\n\t" \
  "s_nop 1\n\t" \
  "v_cndmask_b32 v34, 0, v21, vcc\n\t" \
  "v_cmp_eq_u32 vcc, 3, v20\n\t" \
  "s_nop 1\n\t" \
  "v_cndmask_b32 v35, 0, v21, vcc\n\t" \
  "v_bfe_u32 v38, v69, 7, 1\n\t" \
  "v_bfe_u32 v20, v69, 9, 1\n\t" \
  "v_lshl_add_u32 v38, v20, 1, v38\n\t" \
  "v_mul_u32_u24 v38, 1152, v38\n\t" \
  "v_and_b32 v20, 0x60, v69\n\t" \
  "v_add_u32 v38, v38, v20\n\t" \
  "v_bfe_u32 v20, v69, 8, 1\n\t" \
  "v_lshl_add_u32 v38, v20, 4, v38\n\t" \
  "v_bfe_u32 v20, v69, 4, 1\n\t" \
  "v_lshl_add_u32 v38, v20, 3, v38\n\t" \
  "s_add_u32 s54, s37, s53\n\t" \
  "v_mov_b32 v44, 0\n\t" \
  CH_DP17 \
  "s_waitcnt vmcnt(4)\n\t" \
  CH_DP14 \
  "v_mfma_f32_16x16x32_bf16 v[40:43], v[32:35], v[0:3], 0\n\t" \
  "v_max3_u32 v44, v44, v0, v1\n\t" \
  "v_max3_u32 v44, v44, v2, v3\n\t" \
  "s_waitcnt vmcnt(3)\n\t" \
  "v_mfma_f32_16x16x32_bf16 v[40:43], v[32:35], v[4:7], v[40:43]\n\t" \
  "v_max3_u32 v44, v44, v4, v5\n\t" \
  "v_max3_u32 v44, v44, v6, v7\n\t" \
  "s_waitcnt vmcnt(2)\n\t" \
  "v_mfma_f32_16x16x32_bf16 v[40:43], v[32:35], v[8:11], v[40:43]\n\t" \
  "v_max3_u32 v44, v44, v8, v9\n\t" \
  "v_max3_u32 v44, v44, v10, v11\n\t" \
  "s_waitcnt vmcnt(1)\n\t" \
  CH_DP15 \
  "v_mfma_f32_16x16x32_bf16 v[40:43], v[32:35], v[12:15], v[40:43]\n\t" \
  "v_max3_u32 v44, v44, v12, v13\n\t" \
  "v_max3_u32 v44, v44, v14, v15\n\t" \
  "v_cmp_eq_u32 vcc, -1, v44\n\t" \
  "s_cbranch_vccnz RFAIL3_%=\n\t" \
  "s_nop 3\n\t" \
  "v_add_f32_dpp v40, v40, v40 row_ror:8 row_mask:0xf bank_mask:0xf\n\t" \
  "v_add_f32_dpp v41, v41, v41 row_ror:8 row_mask:0xf bank_mask:0xf\n\t" \
  "v_add_f32_dpp v42, v42, v42 row_ror:8 row_mask:0xf bank_mask:0xf\n\t" \
  "v_add_f32_dpp v43, v43, v43 row_ror:8 row_mask:0xf bank_mask:0xf\n\t" \
  "v_cvt_pk_bf16_f32 v40, v40, v41\n\t" \
  "v_cvt_pk_bf16_f32 v41, v42, v43\n\t" \
  "buffer_store_dwordx2 v[40:41], v38, %[X], s54 offen sc1\n\t" \
  "RDONE3_%=:\n\t" \
  CH_DP16 \
  CH_ST4 \
  "s_barrier\n\t" \
  CH_ST3 \
  "s_waitcnt vmcnt(1)\n\t" \
  "v_cmp_eq_u32 vcc, 0, v39\n\t" \
  CH_TRC \
  "s_add_u32 s48, s48, 1\n\t" \
  "s_mov_b32 s49, s53\n\t" \
  "s_add_u32 s53, s53, " CH_SLOT "\n\t" \
  "s_add_u32 s60, s53, " CH_SKIP "\n\t" \
  "s_lshr_b32 s61, s96, 1\n\t" \
  "s_add_u32 s86, s86, 1\n\t" \
  "s_cmp_eq_u32 s86, 3\n\t" \
  "s_cselect_b32 s53, s60, s53\n\t" \
  "s_cselect_b32 s96, s61, s96\n\t" \
  "s_cselect_b32 s86, 0, s86\n\t" \
  "s_andn2_b32 s53, s53, 0x2000\n\t" \
  "s_and_b32 s60, s96, 1\n\t" \
  "s_lshl_b32 s60, s60, 13\n\t" \
  "s_or_b32 s53, s53, s60\n\t" \
  "s_mov_b32 s68, 0\n\t" \
  "s_add_u32 s82, s48, 0\n\t" \
  CH_SEGC \
  "s_lshr_b64 s[60:61], s[90:91], s82\n\t" \
  "s_and_b32 s60, s60, 1\n\t" \
  "s_lshl_b32 s60, s60, 13\n\t" \
  "s_add_u32 s82, s82, " CH_SG0 "\n\t" \
  "s_lshl_b32 s82, s82, 21\n\t" \
  "s_or_b32 s82, s82, s60\n\t" \
  "s_add_u32 s50, s82, s38\n\t" \
  "s_add_u32 s69, s82, s39\n\t" \
  "s_add_u32 s82, s48, 1\n\t" \
  CH_SEGC \
  "s_lshr_b64 s[60:61], s[90:91], s82\n\t" \
  "s_and_b32 s60, s60, 1\n\t" \
  "s_lshl_b32 s60, s60, 13\n\t" \
  "s_add_u32 s82, s82, " CH_SG0 "\n\t" \
  "s_lshl_b32 s82, s82, 21\n\t" \
  "s_or_b32 s82, s82, s60\n\t" \
  "s_add_u32 s52, s82, s43\n\t" \
  "s_add_u32 s82, s48, " CH_ARMD "\n\t" \
  CH_SEGC \
  "s_lshr_b64 s[60:61], s[90:91], s82\n\t" \
  "s_and_b32 s60, s60, 1\n\t" \
  "s_lshl_b32 s60, s60, 13\n\t" \
  "s_add_u32 s82, s82, " CH_SG0 "\n\t" \
  "s_lshl_b32 s82, s82, 21\n\t" \
  "s_or_b32 s82, s82, s60\n\t" \
  "s_xor_b32 s85, s82, s45\n\t" \
  "s_cmp_lt_i32 s48, " CH_ARMS "\n\t" \
  "s_cselect_b32 s85, s52, s85\n\t" \
  CH_TOKCHK \
  "s_mov_b32 s61, 0\n\t" \
  "LTW_%=:\n\t" \
  "buffer_load_dword v1, v37, %[X], 0 offen sc1\n\t" \
  "s_waitcnt vmcnt(0)\n\t" \
  "v_cmp_eq_u32 vcc, 0, v1\n\t" \
  "s_cbranch_vccz LG3_%=\n\t" \
  "s_add_u32 s61, s61, 1\n\t" \
  "s_sleep 2\n\t" \
  "s_cmp_gt_u32 s61, s84\n\t" \
  "s_cbranch_scc1 LTG_%=\n\t" \
  "s_and_b32 s60, s61, 1023\n\t" \
  "s_cmp_lg_u32 s60, 0\n\t" \
  "s_cbranch_scc1 LTW_%=\n\t" \
  "buffer_load_dword v46, off, %[X], s92 sc1\n\t" \
  "s_waitcnt vmcnt(0)\n\t" \
  "v_readfirstlane_b32 s60, v46\n\t" \
  "s_nop 0\n\t" \
  "s_cmp_eq_u32 s60, 0\n\t" \
  "s_cbranch_scc1 LTW_%=\n\t" \
  "LTG_%=:\n\t" \
  "s_mov_b32 s84, 0\n\t" \
  "s_lshl_b32 s60, s36, 4\n\t" \
  "s_or_b32 s60, s60, s48\n\t" \
  "s_or_b32 s60, s60, 268435456\n\t" \
  "v_mov_b32 v46, s60\n\t" \
  "v_mov_b32 v47, 0\n\t" \
  "s_mov_b64 exec, 1\n\t" \
  "buffer_atomic_cmpswap v[46:47], off, %[X], s92 sc1\n\t" \
  "s_mov_b64 exec, -1\n\t" \
  "s_branch LG3_%=\n\t" \
  "LIDLE_%=:\n\t" \
  "s_add_u32 s64, s64, 8\n\t" \
  "s_movk_i32 s60, 8192\n\t" \
  "IWT_%=:\n\t" \
  "s_memrealtime s[72:73]\n\t" \
  "s_sub_u32 s60, s60, 1\n\t" \
  "s_cselect_b32 s61, 0, -1\n\t" \
  "s_waitcnt lgkmcnt(0)\n\t" \
  "s_sub_i32 s73, s72, s64\n\t" \
  "s_and_b32 s73, s73, s61\n\t" \
  "s_cmp_lt_i32 s73, 0\n\t" \
  "s_cbranch_scc1 IWT_%=\n\t" \
  "buffer_load_dword v39, v37, %[X], 0 offen sc1\n\t" \
  "buffer_store_dword v37, off, %[X], s41\n\t" \
  "s_branch RDONE3_%=\n\t" \
  "RFAIL3_%=:\n\t" \
  "s_add_u32 s74, s74, 1\n\t" \
  "s_sleep " CH_RSL "\n\t" \
  "s_waitcnt vmcnt(0)\n\t" \
  "s_cmp_gt_u32 s74, s84\n\t" \
  "s_cbranch_scc1 RG3_%=\n\t" \
  "s_and_b32 s60, s74, 1023\n\t" \
  "s_cmp_lg_u32 s60, 0\n\t" \
  "s_cbranch_scc1 RRETRY3_%=\n\t" \
  "buffer_load_dword v46, off, %[X], s92 sc1\n\t" \
  "s_waitcnt vmcnt(0)\n\t" \
  "v_readfirstlane_b32 s60, v46\n\t" \
  "s_nop 0\n\t" \
  "s_cmp_eq_u32 s60, 0\n\t" \
  "s_cbranch_scc1 RRETRY3_%=\n\t" \
  "RG3_%=:\n\t" \
  "s_mov_b32 s84, 0\n\t" \
  "s_lshl_b32 s60, s36, 4\n\t" \
  "s_or_b32 s60, s60, s48\n\t" \
  "s_or_b32 s60, s60, 536870912\n\t" \
  "v_mov_b32 v46, s60\n\t" \
  "v_mov_b32 v47, 0\n\t" \
  "s_mov_b64 exec, 1\n\t" \
  "buffer_atomic_cmpswap v[46:47], off, %[X], s92 sc1\n\t" \
  "s_mov_b64 exec, -1\n\t" \
  "s_branch RDONE3_%=\n\t" \
  "LFAIL3_%=:\n\t" \
  "s_add_u32 s68, s68, 1\n\t" \
  "s_waitcnt lgkmcnt(0)\n\t" \
  "s_cmp_gt_u32 s68, s83\n\t" \
  "s_cbranch_scc1 LGV3_%=\n\t" \
  "s_barrier\n\t" \
  "s_branch LG3_%=\n\t" \
  "LGV3_%=:\n\t" \
  "s_mov_b32 s83, 0\n\t" \
  "s_lshl_b32 s60, s36, 4\n\t" \
  "s_or_b32 s60, s60, s48\n\t" \
  "s_or_b32 s60, s60, 805306368\n\t" \
  "v_mov_b32 v46, s60\n\t" \
  "v_mov_b32 v47, 0\n\t" \
  "s_mov_b64 exec, 1\n\t" \
  "buffer_atomic_cmpswap v[46:47], off, %[X], s92 sc1\n\t" \
  "s_mov_b64 exec, -1\n\t" \
  "s_branch LGO3_%=\n\t" \
  "LLAST3_%=:\n\t" \
  "s_waitcnt vmcnt(0) lgkmcnt(0)\n\t" \
  "s_barrier\n\t" \
  "LEND_%=:\n\t" \
  ""
typedef unsigned v32u __attribute__((ext_vector_type(32)));
typedef unsigned v16u __attribute__((ext_vector_type(16)));
#define WL_OFF 16384
#define CH_H(b) (8 * ((b) & 3) + ((b) >> 3))
#define CH_L(b) (((b) >> 2) & 1)
__device__ __forceinline__ void start_conv(char* smem, int d0, int n);
__device__ __forceinline__ void chain_wg(char* smem, v2u cv) {
  if (CH_L(blockIdx.x)) { const int h_ = CH_H(blockIdx.x); start_conv(smem, 4 * h_, 4); }
  const MP p = getp();
  const int b = blockIdx.x, h = CH_H(b), L = CH_L(b);
  const int w = __builtin_amdgcn_readfirstlane(threadIdx.x >> 6);
  unsigned lp16 = (threadIdx.x & 63u) * 16u;
  const rsrc_t X = mkr(p.xb, XB_BYTES);
  v4u* wL = (v4u*)(smem + WL_OFF + w * 36864);
  if ((unsigned)(size_t)(__attribute__((address_space(3))) char*)smem != 0u) return;
  v32u wa0, wa1, wa2, wa3, wa4, wa5, wa6, wa7, wb0, wb1, wb2, wb3, wb4;
  v16u wb5;
  {
    const int lane = (int)(lp16 >> 4);
    const v4u* W = (const v4u*)p.WP;
    const v4u* WO = (const v4u*)p.WOP;
    constexpr int ops[72] = CH_OPS;
    auto fr = [&](int f) -> v4u {
      const int o = ops[f >> 1], hr = f & 1, ty = o >> 6, a = (o >> 4) & 3, s = o & 15;
      if (ty < 3) return W[(((ty * NH + h) * 8 + 2 * w + a) * 18 + 2 * s + hr) * 64 + lane];
      return WO[(((h * 4 + w) * 18 + (ty == 3 ? 4 * a : 16) + s) * 2 + hr) * 64 + lane];
    };
#define PUT4(dst, k, val) do { const v4u t_ = (val); dst[4 * (k)] = t_.x; dst[4 * (k) + 1] = t_.y; dst[4 * (k) + 2] = t_.z; dst[4 * (k) + 3] = t_.w; } while (0)
#define FILL8(dst, f0) do { _Pragma("unroll") for (int k_ = 0; k_ < 8; ++k_) PUT4(dst, k_, fr((f0) + k_)); } while (0)
    FILL8(wa0, 0); FILL8(wa1, 8); FILL8(wa2, 16); FILL8(wa3, 24); FILL8(wa4, 32); FILL8(wa5, 40); FILL8(wa6, 48); FILL8(wa7, 56);
    asm volatile("" : "+{a[0:31]}"(wa0), "+{a[32:63]}"(wa1), "+{a[64:95]}"(wa2), "+{a[96:127]}"(wa3), "+{a[128:159]}"(wa4), "+{a[160:191]}"(wa5), "+{a[192:223]}"(wa6), "+{a[224:255]}"(wa7));
#pragma unroll
    for (int f = 0; f < 36; ++f) wL[f * 64 + lane] = fr(108 + f);
    FILL8(wb0, 64); FILL8(wb1, 72); FILL8(wb2, 80); FILL8(wb3, 88); FILL8(wb4, 96);
#pragma unroll
    for (int k_ = 0; k_ < 4; ++k_) PUT4(wb5, k_, fr(104 + k_));
  }
  asm volatile("" :: "v"(cv));
  unsigned xcc; asm volatile("s_getreg_b32 %0, hwreg(HW_REG_XCC_ID)" : "=s"(xcc)); xcc &= 3u;
  unsigned d96 = (unsigned)die_mask(X, 0u, L, (int)(lp16 >> 4));
  const unsigned long long sm = die_mask(X, SG0, L, (int)(lp16 >> 4));
  const unsigned c43 = P_FA * PAIR + (unsigned)h * 128u + 32u;
  {
    unsigned sp = 0;
    while (true) {
      asm volatile("" ::: "memory");
      const unsigned f = ld4(X, lp16 & 0x30u, SEGB(sm, 0) + c43);
      if (__all(f != 0u)) break;
      if (spin_fail(sp, X)) break;
      __builtin_amdgcn_s_sleep(4);
    }
  }
  const unsigned h128 = (unsigned)h * 128u, uw = (unsigned)w, t = uw >> 1, T = uw & 1u, NONE = 0xF0000000u;
  const unsigned u0 = w == 3 ? (unsigned)h >> 2 : (unsigned)h + (w == 2 ? 32u : 0u);
  const bool idle = w == 3 && (h & 3);
  const unsigned c37 = idle ? NONE : u0 * 128u + (w == 3 ? AGT : AGO);
  const unsigned c41 = idle ? NONE : (w == 3 ? 524288u + (u0 >> 1) * 16384u + (u0 & 1u) * 4096u : (u0 & 7u) * 16384u + ((u0 >> 3) & 1u) * 4096u + (u0 >> 4) * 131072u);
  const unsigned c42 = AGO + xcc * 16384u, c44 = AGT + xcc * 1152u;
  const unsigned c46 = h == 30 ? AGT + 1024u : NONE;
  const unsigned c38 = (unsigned)(3 * h + 1) * PAIR + T * 4096u + t * 2048u, c39 = (P_KV + (unsigned)(h >> 1)) * PAIR + (unsigned)(h & 1) * 4096u;
  const unsigned c45 = P_FB * PAIR + 4096u + (unsigned)(L * 16 + (h & 15)) * 128u + (L ? 0u : 8192u);
  unsigned d48 = 0u, d49 = (d96 & 1u) * 8192u, d53 = SLOT + d49, d86 = 1u, d65 = rtc();
  unsigned mn = 120u;
  v4u stt;
  asm volatile("s_waitcnt vmcnt(0) lgkmcnt(0)\n\t" CH_ASM "LCHEND:"
               : "+{s48}"(d48), "+{s49}"(d49), "+{s53}"(d53), "+{s65}"(d65), "+{s81}"(mn), "+{s86}"(d86), "+{s96}"(d96), "=&{s[76:79]}"(stt), "+{v69}"(lp16)
               : [X] "s"(X), "{s36}"(h128), "{s37}"(c37), "{s40}"(w), "{s41}"(c41), "{s42}"(c42), "{s44}"(c44), "{s46}"(c46), "{s92}"(TMO_OFF), "{s38}"(c38), "{s39}"(c39), "{s43}"(c43), "{s45}"(c45), "{s[90:91]}"(sm), [dl1l] "i"(DL1 - LATE), [armd] "i"(3 - ARMK), [arms] "i"(ARMK - 3),
                 [cap] "i"(SPIN_CAP),
                 "{a[0:31]}"(wa0), "{a[32:63]}"(wa1), "{a[64:95]}"(wa2), "{a[96:127]}"(wa3), "{a[128:159]}"(wa4), "{a[160:191]}"(wa5), "{a[192:223]}"(wa6), "{a[224:255]}"(wa7),
                 "{v[80:111]}"(wb0), "{v[112:143]}"(wb1), "{v[144:175]}"(wb2), "{v[176:207]}"(wb3), "{v[208:239]}"(wb4), "{v[240:255]}"(wb5)
               : "v0", "v1", "v2", "v3", "v4", "v5", "v6", "v7", "v8", "v9", "v10", "v11", "v12", "v13", "v14", "v15", "v16", "v17", "v18", "v19",
                 "v20", "v21", "v22", "v23", "v24", "v25", "v26", "v27", "v28", "v29", "v30", "v31", "v32", "v33", "v34", "v35", "v36", "v37", "v38", "v39",
                 "v40", "v41", "v42", "v43", "v44", "v45", "v46", "v47", "v48", "v49", "v50", "v51", "v52", "v53", "v54", "v55", "v56", "v57", "v58", "v59",
                 "v60", "v61", "v62", "v63", "v64", "v65", "v66", "v67", "v68", "v70", "v71", "v72", "v73", "v74", "v75", "v76", "v77", "v78", "v79",
                 "s47", "s87", "s50", "s51", "s52", "s69", "s82", "s85", "s54", "s75", "s80", "s55", "s56", "s57", "s58", "s59", "s60", "s61", "s62", "s63", "s64", "s66", "s67", "s68", "s70", "s71", "s72", "s73", "s74", "s88", "s89",
                 "s83", "s84", "s97", "s98", "s99", "s93", "s94", "s95", "m0", "vcc", "scc", "memory");
}
#define XQ 0u
#define XQL 0u
#define XA 0u
#define XAL 0u
#define XRL 0u
#define QLEAD 4
#define TK_QDL
#define TK_ADL
#define PSR 8
#define RDLY 40
#define TK_ASL
#define TK_QSL
#define ROT 1
#define PACE 1
#define TK_HAVE "s_cmp_eq_u32 vcc_lo, 0\n\t"
#define TK_PACE "s_cmp_gt_i32 s42, -60\n\ts_cbranch_scc1 LPACE_%=\n\t"
#define TK_QDL2
#define TK_QNP
#define PSK 2
#define PSH 2
#define TK_CAP "%[cap]"
#define TK_SG0 CH_S(SG0)
#define TK_PSK CH_S(PSK)
#define TK_PSH CH_S(PSH)
#define TK_PA0
#define TK_PA1
#define TK_PA2
#define TK_PQ0
#define TK_PQ1
#define TK_PQ2
#define TT(s, i)
#define TK_AC(n)
#define TK_ACE
#define TK_AC0 TK_AC(212)
#define TK_AC1 TK_AC(213)
#define TK_AC2 TK_AC(214)
#define TK_AC3 TK_AC(215)
#define TK_AC4 TK_AC(216)
#define TK_AC5 TK_AC(217)
#define TK_AC6 TK_AC(218)
#define TK_AC7 TK_AC(219)
#define TK_AC8 TK_AC(220)
#define AP_ASM \
  "s_mov_b32 s60, 0\n\t" \
  "buffer_load_dwordx4 a[0:3], v0, %[W], s60 offen offset:0\n\t" \
  "buffer_load_dwordx4 a[4:7], v0, %[W], s60 offen offset:1024\n\t" \
  "buffer_load_dwordx4 a[8:11], v0, %[W], s60 offen offset:2048\n\t" \
  "buffer_load_dwordx4 a[12:15], v0, %[W], s60 offen offset:3072\n\t" \
  "s_add_u32 s60, s60, 4096\n\t" \
  "buffer_load_dwordx4 a[16:19], v0, %[W], s60 offen offset:0\n\t" \
  "buffer_load_dwordx4 a[20:23], v0, %[W], s60 offen offset:1024\n\t" \
  "buffer_load_dwordx4 a[24:27], v0, %[W], s60 offen offset:2048\n\t" \
  "buffer_load_dwordx4 a[28:31], v0, %[W], s60 offen offset:3072\n\t" \
  "s_add_u32 s60, s60, 4096\n\t" \
  "buffer_load_dwordx4 a[32:35], v0, %[W], s60 offen offset:0\n\t" \
  "buffer_load_dwordx4 a[36:39], v0, %[W], s60 offen offset:1024\n\t" \
  "buffer_load_dwordx4 a[40:43], v0, %[W], s60 offen offset:2048\n\t" \
  "buffer_load_dwordx4 a[44:47], v0, %[W], s60 offen offset:3072\n\t" \
  "s_add_u32 s60, s60, 4096\n\t" \
  "buffer_load_dwordx4 a[48:51], v0, %[W], s60 offen offset:0\n\t" \
  "buffer_load_dwordx4 a[52:55], v0, %[W], s60 offen offset:1024\n\t" \
  "buffer_load_dwordx4 a[56:59], v0, %[W], s60 offen offset:2048\n\t" \
  "buffer_load_dwordx4 a[60:63], v0, %[W], s60 offen offset:3072\n\t" \
  "s_add_u32 s60, s60, 4096\n\t" \
  "buffer_load_dwordx4 a[64:67], v0, %[W], s60 offen offset:0\n\t" \
  "buffer_load_dwordx4 a[68:71], v0, %[W], s60 offen offset:1024\n\t" \
  "buffer_load_dwordx4 a[72:75], v0, %[W], s60 offen offset:2048\n\t" \
  "buffer_load_dwordx4 a[76:79], v0, %[W], s60 offen offset:3072\n\t" \
  "s_add_u32 s60, s60, 4096\n\t" \
  "buffer_load_dwordx4 a[80:83], v0, %[W], s60 offen offset:0\n\t" \
  "buffer_load_dwordx4 a[84:87], v0, %[W], s60 offen offset:1024\n\t" \
  "buffer_load_dwordx4 a[88:91], v0, %[W], s60 offen offset:2048\n\t" \
  "buffer_load_dwordx4 a[92:95], v0, %[W], s60 offen offset:3072\n\t" \
  "s_add_u32 s60, s60, 4096\n\t" \
  "buffer_load_dwordx4 a[96:99], v0, %[W], s60 offen offset:0\n\t" \
  "buffer_load_dwordx4 a[100:103], v0, %[W], s60 offen offset:1024\n\t" \
  "buffer_load_dwordx4 a[104:107], v0, %[W], s60 offen offset:2048\n\t" \
  "buffer_load_dwordx4 a[108:111], v0, %[W], s60 offen offset:3072\n\t" \
  "s_add_u32 s60, s60, 4096\n\t" \
  "buffer_load_dwordx4 a[112:115], v0, %[W], s60 offen offset:0\n\t" \
  "buffer_load_dwordx4 a[116:119], v0, %[W], s60 offen offset:1024\n\t" \
  "buffer_load_dwordx4 a[120:123], v0, %[W], s60 offen offset:2048\n\t" \
  "buffer_load_dwordx4 a[124:127], v0, %[W], s60 offen offset:3072\n\t" \
  "s_add_u32 s60, s60, 4096\n\t" \
  "buffer_load_dwordx4 a[128:131], v0, %[W], s60 offen offset:0\n\t" \
  "buffer_load_dwordx4 a[132:135], v0, %[W], s60 offen offset:1024\n\t" \
  "buffer_load_dwordx4 a[136:139], v0, %[W], s60 offen offset:2048\n\t" \
  "buffer_load_dwordx4 a[140:143], v0, %[W], s60 offen offset:3072\n\t" \
  "s_add_u32 s60, s60, 4096\n\t" \
  "buffer_load_dwordx4 a[144:147], v0, %[W], s60 offen offset:0\n\t" \
  "buffer_load_dwordx4 a[148:151], v0, %[W], s60 offen offset:1024\n\t" \
  "buffer_load_dwordx4 a[152:155], v0, %[W], s60 offen offset:2048\n\t" \
  "buffer_load_dwordx4 a[156:159], v0, %[W], s60 offen offset:3072\n\t" \
  "s_add_u32 s60, s60, 4096\n\t" \
  "buffer_load_dwordx4 a[160:163], v0, %[W], s60 offen offset:0\n\t" \
  "buffer_load_dwordx4 a[164:167], v0, %[W], s60 offen offset:1024\n\t" \
  "buffer_load_dwordx4 a[168:171], v0, %[W], s60 offen offset:2048\n\t" \
  "buffer_load_dwordx4 a[172:175], v0, %[W], s60 offen offset:3072\n\t" \
  "s_add_u32 s60, s60, 4096\n\t" \
  "buffer_load_dwordx4 a[176:179], v0, %[W], s60 offen offset:0\n\t" \
  "buffer_load_dwordx4 a[180:183], v0, %[W], s60 offen offset:1024\n\t" \
  "buffer_load_dwordx4 a[184:187], v0, %[W], s60 offen offset:2048\n\t" \
  "buffer_load_dwordx4 a[188:191], v0, %[W], s60 offen offset:3072\n\t" \
  "s_add_u32 s60, s60, 4096\n\t" \
  "buffer_load_dwordx4 a[192:195], v0, %[W], s60 offen offset:0\n\t" \
  "buffer_load_dwordx4 a[196:199], v0, %[W], s60 offen offset:1024\n\t" \
  "buffer_load_dwordx4 a[200:203], v0, %[W], s60 offen offset:2048\n\t" \
  "buffer_load_dwordx4 a[204:207], v0, %[W], s60 offen offset:3072\n\t" \
  "s_add_u32 s60, s60, 4096\n\t" \
  "buffer_load_dwordx4 a[208:211], v0, %[W], s60 offen offset:0\n\t" \
  "buffer_load_dwordx4 a[212:215], v0, %[W], s60 offen offset:1024\n\t" \
  "buffer_load_dwordx4 a[216:219], v0, %[W], s60 offen offset:2048\n\t" \
  "buffer_load_dwordx4 a[220:223], v0, %[W], s60 offen offset:3072\n\t" \
  "s_add_u32 s60, s60, 4096\n\t" \
  "buffer_load_dwordx4 a[224:227], v0, %[W], s60 offen offset:0\n\t" \
  "buffer_load_dwordx4 a[228:231], v0, %[W], s60 offen offset:1024\n\t" \
  "buffer_load_dwordx4 a[232:235], v0, %[W], s60 offen offset:2048\n\t" \
  "buffer_load_dwordx4 a[236:239], v0, %[W], s60 offen offset:3072\n\t" \
  "s_add_u32 s60, s60, 4096\n\t" \
  "s_waitcnt vmcnt(40)\n\t" \
  "buffer_load_dwordx4 a[240:243], v0, %[W], s60 offen offset:0\n\t" \
  "buffer_load_dwordx4 a[244:247], v0, %[W], s60 offen offset:1024\n\t" \
  "buffer_load_dwordx4 a[248:251], v0, %[W], s60 offen offset:2048\n\t" \
  "buffer_load_dwordx4 a[252:255], v0, %[W], s60 offen offset:3072\n\t" \
  "s_add_u32 s60, s60, 4096\n\t" \
  "buffer_load_dwordx4 v[224:227], v0, %[W], s60 offen offset:0\n\t" \
  "buffer_load_dwordx4 v[228:231], v0, %[W], s60 offen offset:1024\n\t" \
  "buffer_load_dwordx4 v[232:235], v0, %[W], s60 offen offset:2048\n\t" \
  "buffer_load_dwordx4 v[236:239], v0, %[W], s60 offen offset:3072\n\t" \
  "s_add_u32 s60, s60, 4096\n\t" \
  "buffer_load_dwordx4 v[240:243], v0, %[W], s60 offen offset:0\n\t" \
  "buffer_load_dwordx4 v[244:247], v0, %[W], s60 offen offset:1024\n\t" \
  "buffer_load_dwordx4 v[248:251], v0, %[W], s60 offen offset:2048\n\t" \
  "buffer_load_dwordx4 v[252:255], v0, %[W], s60 offen offset:3072\n\t" \
  "s_add_u32 s60, s60, 4096\n\t" \
  "s_mov_b32 s84, " TK_CAP "\n\t" \
  "s_mov_b32 s48, 0\n\t" \
  "s_mov_b32 s49, 0xf0000000\n\t" \
  "s_mov_b32 s46, s45\n\t" \
  "v_mov_b32 v5, 0\n\t" \
  "s_add_u32 s83, s48, 0\n\t" \
  "s_lshr_b64 s[60:61], s[90:91], s83\n\t" \
  "s_and_b32 s60, s60, 1\n\t" \
  "s_lshl_b32 s60, s60, 13\n\t" \
  "s_add_u32 s83, s83, " TK_SG0 "\n\t" \
  "s_lshl_b32 s83, s83, 21\n\t" \
  "s_or_b32 s83, s83, s60\n\t" \
  "LCOLD_%=:\n\t" \
  "s_add_u32 s62, s83, s41\n\t" \
  "s_mov_b32 s61, 0\n\t" \
  "LPQ_%=:\n\t" \
  "buffer_load_dword v10, v9, %[X], s62 offen sc1\n\t" \
  "s_waitcnt vmcnt(0)\n\t" \
  "v_cmp_eq_u32 vcc, 0, v10\n\t" \
  "s_cbranch_vccz LPQ_%=x\n\t" \
  "s_add_u32 s61, s61, 1\n\t" \
  "s_sleep 4\n\t" \
  "s_cmp_gt_u32 s61, s84\n\t" \
  "s_cbranch_scc1 LPQ_%=g\n\t" \
  "s_and_b32 s60, s61, 1023\n\t" \
  "s_cmp_lg_u32 s60, 0\n\t" \
  "s_cbranch_scc1 LPQ_%=\n\t" \
  "buffer_load_dword v10, off, %[X], s92 sc1\n\t" \
  "s_waitcnt vmcnt(0)\n\t" \
  "v_readfirstlane_b32 s60, v10\n\t" \
  "s_nop 0\n\t" \
  "s_cmp_eq_u32 s60, 0\n\t" \
  "s_cbranch_scc1 LPQ_%=\n\t" \
  "LPQ_%=g:\n\t" \
  "s_mov_b32 s84, 0\n\t" \
  "s_or_b32 s60, s48, 1073741824\n\t" \
  "v_mov_b32 v10, s60\n\t" \
  "s_mov_b64 exec, 1\n\t" \
  "buffer_store_dword v10, off, %[X], s92 sc1\n\t" \
  "s_mov_b64 exec, -1\n\t" \
  "LPQ_%=x:\n\t" \
  "s_add_u32 m0, s46, 0\n\t" \
  "s_add_u32 s47, s83, s36\n\t" \
  "buffer_load_dwordx4 v0, %[X], s47 offen offset:0 sc1 lds\n\t" \
  "buffer_load_dwordx4 v0, %[X], s47 offen offset:1024 sc1 lds\n\t" \
  "buffer_load_dwordx4 v0, %[X], s47 offen offset:2048 sc1 lds\n\t" \
  "buffer_load_dwordx4 v0, %[X], s47 offen offset:3072 sc1 lds\n\t" \
  "s_add_u32 m0, s46, 4096\n\t" \
  "s_add_u32 s47, s83, s37\n\t" \
  "buffer_load_dwordx4 v0, %[X], s47 offen offset:0 sc1 lds\n\t" \
  "buffer_load_dwordx4 v0, %[X], s47 offen offset:1024 sc1 lds\n\t" \
  "buffer_load_dwordx4 v0, %[X], s47 offen offset:2048 sc1 lds\n\t" \
  "buffer_load_dwordx4 v0, %[X], s47 offen offset:3072 sc1 lds\n\t" \
  "s_add_u32 m0, s46, 8192\n\t" \
  "s_add_u32 s47, s83, s38\n\t" \
  "buffer_load_dwordx4 v0, %[X], s47 offen offset:0 sc1 lds\n\t" \
  "buffer_load_dwordx4 v0, %[X], s47 offen offset:1024 sc1 lds\n\t" \
  "buffer_load_dwordx4 v0, %[X], s47 offen offset:2048 sc1 lds\n\t" \
  "buffer_load_dwordx4 v0, %[X], s47 offen offset:3072 sc1 lds\n\t" \
  "s_waitcnt vmcnt(0)\n\t" \
  "LTOP_%=:\n\t" \
  TK_AC0 \
  "s_barrier\n\t" \
  TK_AC1 \
  "ds_read_b128 v[48:51], v2 offset:0\n\t" \
  "ds_read_b128 v[64:67], v1 offset:8192\n\t" \
  "ds_read_b128 v[80:83], v1 offset:12288\n\t" \
  "ds_read_b128 v[52:55], v2 offset:1024\n\t" \
  "ds_read_b128 v[68:71], v1 offset:9216\n\t" \
  "ds_read_b128 v[84:87], v1 offset:13312\n\t" \
  "ds_read_b128 v[56:59], v2 offset:2048\n\t" \
  "ds_read_b128 v[72:75], v1 offset:10240\n\t" \
  "ds_read_b128 v[88:91], v1 offset:14336\n\t" \
  "ds_read_b128 v[60:63], v2 offset:3072\n\t" \
  "ds_read_b128 v[76:79], v1 offset:11264\n\t" \
  "ds_read_b128 v[92:95], v1 offset:15360\n\t" \
  "v_readfirstlane_b32 s57, v5\n\t" \
  "s_mov_b32 s82, s83\n\t" \
  "s_xor_b32 s46, s46, 0x10000\n\t" \
  "s_add_u32 s58, s82, s41\n\t" \
  "s_add_u32 s58, s58, 4096\n\t" \
  "s_add_u32 s59, s82, s39\n\t" \
  "s_cmp_eq_u32 s57, 0\n\t" \
  "s_cselect_b32 s62, 0xf0000000, s59\n\t" \
  "s_waitcnt lgkmcnt(10)\n\t" \
  "v_mfma_f32_16x16x32_bf16 v[36:39], v[64:67], v[48:51], v[16:19]\n\t" \
  "ds_read_b128 v[96:99], v1 offset:16384\n\t" \
  "buffer_load_dwordx4 v[196:199], v0, %[X], s62 offen offset:0 sc1\n\t" \
  "s_waitcnt lgkmcnt(10)\n\t" \
  "v_mfma_f32_16x16x32_bf16 v[40:43], v[80:83], v[48:51], v[20:23]\n\t" \
  "ds_read_b128 v[100:103], v1 offset:17408\n\t" \
  "buffer_load_dwordx4 v[200:203], v0, %[X], s62 offen offset:1024 sc1\n\t" \
  "s_waitcnt lgkmcnt(9)\n\t" \
  "v_mfma_f32_16x16x32_bf16 v[36:39], v[68:71], v[52:55], v[36:39]\n\t" \
  "ds_read_b128 v[104:107], v1 offset:18432\n\t" \
  "buffer_load_dwordx2 v[204:205], v6, %[X], s62 offen offset:2048 sc1\n\t" \
  "s_waitcnt lgkmcnt(9)\n\t" \
  "v_mfma_f32_16x16x32_bf16 v[40:43], v[84:87], v[52:55], v[40:43]\n\t" \
  "ds_read_b128 v[108:111], v1 offset:19456\n\t" \
  "buffer_load_dwordx2 v[206:207], v6, %[X], s62 offen offset:2560 sc1\n\t" \
  "s_waitcnt lgkmcnt(8)\n\t" \
  "v_mfma_f32_16x16x32_bf16 v[36:39], v[72:75], v[56:59], v[36:39]\n\t" \
  "ds_read_b128 v[112:115], v1 offset:20480\n\t" \
  "buffer_load_dwordx2 v[208:209], v6, %[X], s62 offen offset:3072 sc1\n\t" \
  "s_waitcnt lgkmcnt(8)\n\t" \
  "v_mfma_f32_16x16x32_bf16 v[40:43], v[88:91], v[56:59], v[40:43]\n\t" \
  "ds_read_b128 v[116:119], v1 offset:21504\n\t" \
  "buffer_load_dwordx2 v[210:211], v6, %[X], s62 offen offset:3584 sc1\n\t" \
  "s_waitcnt lgkmcnt(7)\n\t" \
  "v_mfma_f32_16x16x32_bf16 v[36:39], v[76:79], v[60:63], v[36:39]\n\t" \
  "ds_read_b128 v[120:123], v1 offset:22528\n\t" \
  "s_waitcnt lgkmcnt(7)\n\t" \
  "v_mfma_f32_16x16x32_bf16 v[40:43], v[92:95], v[60:63], v[40:43]\n\t" \
  "ds_read_b128 v[124:127], v1 offset:23552\n\t" \
  "buffer_load_dword v5, off, %[X], s58 sc1\n\t" \
  "s_add_u32 s83, s48, 1\n\t" \
  "s_lshr_b64 s[60:61], s[90:91], s83\n\t" \
  "s_and_b32 s60, s60, 1\n\t" \
  "s_lshl_b32 s60, s60, 13\n\t" \
  "s_add_u32 s83, s83, " TK_SG0 "\n\t" \
  "s_lshl_b32 s83, s83, 21\n\t" \
  "s_or_b32 s83, s83, s60\n\t" \
  "v_xor_b32 v1, 0x10000, v1\n\t" \
  "v_xor_b32 v2, 0x10000, v2\n\t" \
  "v_max3_f32 v11, v36, v37, v38\n\t" \
  "v_max3_f32 v11, v11, v39, v40\n\t" \
  "v_max3_f32 v13, v41, v42, v43\n\t" \
  "v_max_f32 v11, v11, v13\n\t" \
  "v_mov_b32 v13, v11\n\t" \
  "s_nop 1\n\t" \
  "v_permlane16_swap_b32 v11, v13\n\t" \
  "v_max_f32 v11, v11, v13\n\t" \
  "v_mov_b32 v13, v11\n\t" \
  "s_nop 1\n\t" \
  "v_permlane32_swap_b32 v11, v13\n\t" \
  "v_max_f32 v11, v11, v13\n\t" \
  "v_mul_f32 v13, 0xbe0293ee, v11\n\t" \
  "v_fmamk_f32 v36, v36, 0x3e0293ee, v13\n\t" \
  "v_fmamk_f32 v37, v37, 0x3e0293ee, v13\n\t" \
  "v_fmamk_f32 v38, v38, 0x3e0293ee, v13\n\t" \
  "v_fmamk_f32 v39, v39, 0x3e0293ee, v13\n\t" \
  "v_fmamk_f32 v40, v40, 0x3e0293ee, v13\n\t" \
  "v_fmamk_f32 v41, v41, 0x3e0293ee, v13\n\t" \
  "v_fmamk_f32 v42, v42, 0x3e0293ee, v13\n\t" \
  "v_fmamk_f32 v43, v43, 0x3e0293ee, v13\n\t" \
  "v_exp_f32 v36, v36\n\t" \
  "v_exp_f32 v37, v37\n\t" \
  "v_exp_f32 v38, v38\n\t" \
  "v_exp_f32 v39, v39\n\t" \
  "v_exp_f32 v40, v40\n\t" \
  "v_exp_f32 v41, v41\n\t" \
  "v_exp_f32 v42, v42\n\t" \
  "v_exp_f32 v43, v43\n\t" \
  "v_cvt_pk_bf16_f32 v44, v36, v37\n\t" \
  "v_cvt_pk_bf16_f32 v45, v38, v39\n\t" \
  "v_cvt_pk_bf16_f32 v46, v40, v41\n\t" \
  "v_cvt_pk_bf16_f32 v47, v42, v43\n\t" \
  "v_add_f32 v36, v36, v37\n\t" \
  "v_add_f32 v38, v38, v39\n\t" \
  "s_waitcnt lgkmcnt(7)\n\t" \
  "v_mfma_f32_16x16x32_bf16 v[128:131], v[96:99], v[44:47], 0\n\t" \
  "v_add_f32 v40, v40, v41\n\t" \
  "s_and_b32 s62, s48, 15\n\t" \
  "s_lshr_b32 s63, s62, 1\n\t" \
  "s_waitcnt lgkmcnt(6)\n\t" \
  "v_mfma_f32_16x16x32_bf16 v[132:135], v[100:103], v[44:47], 0\n\t" \
  "v_add_f32 v42, v42, v43\n\t" \
  "s_lshr_b32 s60, s88, s63\n\t" \
  "s_and_b32 s60, s60, 1\n\t" \
  "s_waitcnt lgkmcnt(5)\n\t" \
  "v_mfma_f32_16x16x32_bf16 v[136:139], v[104:107], v[44:47], 0\n\t" \
  "v_add_f32 v36, v36, v38\n\t" \
  "s_lshl_b32 s60, s60, 13\n\t" \
  "s_lshl_b32 s61, s62, 20\n\t" \
  "s_waitcnt lgkmcnt(4)\n\t" \
  "v_mfma_f32_16x16x32_bf16 v[140:143], v[108:111], v[44:47], 0\n\t" \
  "v_add_f32 v40, v40, v42\n\t" \
  "s_add_u32 s50, s61, s60\n\t" \
  "s_add_u32 s50, s50, s43\n\t" \
  "s_waitcnt lgkmcnt(3)\n\t" \
  "v_mfma_f32_16x16x32_bf16 v[144:147], v[112:115], v[44:47], 0\n\t" \
  "v_add_f32 v12, v36, v40\n\t" \
  "s_add_u32 s51, s50, 32768\n\t" \
  "s_add_u32 s52, s51, 32768\n\t" \
  "s_waitcnt lgkmcnt(2)\n\t" \
  "v_mfma_f32_16x16x32_bf16 v[148:151], v[116:119], v[44:47], 0\n\t" \
  "s_add_u32 s53, s52, 32768\n\t" \
  "s_lshl_b32 s61, s62, 17\n\t" \
  "s_waitcnt lgkmcnt(1)\n\t" \
  "v_mfma_f32_16x16x32_bf16 v[152:155], v[120:123], v[44:47], 0\n\t" \
  "s_add_u32 s54, s61, s44\n\t" \
  "s_add_u32 s63, s83, s41\n\t" \
  "s_waitcnt lgkmcnt(0)\n\t" \
  "v_mfma_f32_16x16x32_bf16 v[156:159], v[124:127], v[44:47], 0\n\t" \
  TK_AC2 \
  "s_waitcnt vmcnt(0)\n\t" \
  "v_readfirstlane_b32 s60, v5\n\t" \
  "s_barrier\n\t" \
  TK_AC3 \
  TK_PA0 \
  "s_cmp_lg_u32 s57, 0\n\t" \
  "s_cbranch_scc1 LGOT_%=\n\t" \
  "s_cmp_lg_u32 s60, 0\n\t" \
  "s_cbranch_scc1 LPK_%=x\n\t" \
  "s_mov_b64 exec, 1\n\t" \
  "buffer_store_dword v3, off, %[X], s49 sc1\n\t" \
  "s_mov_b64 exec, -1\n\t" \
  "s_mov_b32 s49, 0xf0000000\n\t" \
  "s_mov_b32 s61, 0\n\t" \
  "LPK_%=:\n\t" \
  "buffer_load_dword v5, off, %[X], s58 sc1\n\t" \
  "s_waitcnt vmcnt(0)\n\t" \
  "v_cmp_eq_u32 vcc, 0, v5\n\t" \
  "s_cbranch_vccz LPK_%=x\n\t" \
  "s_add_u32 s61, s61, 1\n\t" \
  "s_sleep " TK_PSK "\n\t" \
  "s_cmp_gt_u32 s61, s84\n\t" \
  "s_cbranch_scc1 LPK_%=g\n\t" \
  "s_and_b32 s60, s61, 1023\n\t" \
  "s_cmp_lg_u32 s60, 0\n\t" \
  "s_cbranch_scc1 LPK_%=\n\t" \
  "buffer_load_dword v5, off, %[X], s92 sc1\n\t" \
  "s_waitcnt vmcnt(0)\n\t" \
  "v_readfirstlane_b32 s60, v5\n\t" \
  "s_nop 0\n\t" \
  "s_cmp_eq_u32 s60, 0\n\t" \
  "s_cbranch_scc1 LPK_%=\n\t" \
  "LPK_%=g:\n\t" \
  "s_mov_b32 s84, 0\n\t" \
  "s_or_b32 s60, s48, 1342177280\n\t" \
  "v_mov_b32 v5, s60\n\t" \
  "s_mov_b64 exec, 1\n\t" \
  "buffer_store_dword v5, off, %[X], s92 sc1\n\t" \
  "s_mov_b64 exec, -1\n\t" \
  "LPK_%=x:\n\t" \
  TK_ADL \
  "buffer_load_dwordx4 v[196:199], v0, %[X], s59 offen offset:0 sc1\n\t" \
  "buffer_load_dwordx4 v[200:203], v0, %[X], s59 offen offset:1024 sc1\n\t" \
  "buffer_load_dwordx2 v[204:205], v6, %[X], s59 offen offset:2048 sc1\n\t" \
  "buffer_load_dwordx2 v[206:207], v6, %[X], s59 offen offset:2560 sc1\n\t" \
  "buffer_load_dwordx2 v[208:209], v6, %[X], s59 offen offset:3072 sc1\n\t" \
  "buffer_load_dwordx2 v[210:211], v6, %[X], s59 offen offset:3584 sc1\n\t" \
  "LGOT_%=:\n\t" \
  TK_PA1 \
  TK_AC4 \
  "buffer_load_dword v10, v9, %[X], s63 offen sc1\n\t" \
  "s_mov_b64 exec, 1\n\t" \
  "buffer_store_dword v3, off, %[X], s49 sc1\n\t" \
  "s_mov_b64 exec, -1\n\t" \
  "s_mov_b32 s49, 0xf0000000\n\t" \
  "s_add_u32 s49, s82, s42\n\t" \
  "s_waitcnt vmcnt(7)\n\t" \
  "v_mfma_f32_16x16x32_bf16 v[36:39], v[196:199], v[48:51], 0\n\t" \
  "v_mfma_f32_16x16x32_bf16 v[40:43], v[196:199], v[52:55], 0\n\t" \
  "s_waitcnt vmcnt(6)\n\t" \
  "v_mfma_f32_16x16x32_bf16 v[36:39], v[200:203], v[56:59], v[36:39]\n\t" \
  "v_mfma_f32_16x16x32_bf16 v[40:43], v[200:203], v[60:63], v[40:43]\n\t" \
  TK_AC5 \
  "s_nop 8\n\t" \
  "v_add_f32 v36, v36, v42\n\t" \
  "v_add_f32 v37, v37, v43\n\t" \
  "v_max3_f32 v14, v36, v37, v11\n\t" \
  "v_mov_b32 v13, v14\n\t" \
  "s_nop 1\n\t" \
  "v_permlane16_swap_b32 v14, v13\n\t" \
  "v_max_f32 v14, v14, v13\n\t" \
  "v_mov_b32 v13, v14\n\t" \
  "s_nop 1\n\t" \
  "v_permlane32_swap_b32 v14, v13\n\t" \
  "v_max_f32 v14, v14, v13\n\t" \
  "v_mul_f32 v14, 0xbe0293ee, v14\n\t" \
  "v_fmamk_f32 v11, v11, 0x3e0293ee, v14\n\t" \
  "v_fmamk_f32 v36, v36, 0x3e0293ee, v14\n\t" \
  "v_fmamk_f32 v37, v37, 0x3e0293ee, v14\n\t" \
  "v_exp_f32 v11, v11\n\t" \
  "v_exp_f32 v36, v36\n\t" \
  "v_exp_f32 v37, v37\n\t" \
  "v_fma_f32 v12, v12, v11, v36\n\t" \
  "v_add_f32 v12, v12, v37\n\t" \
  "v_mov_b32 v13, v12\n\t" \
  "s_nop 1\n\t" \
  "v_permlane16_swap_b32 v12, v13\n\t" \
  "v_add_f32 v12, v12, v13\n\t" \
  "v_mov_b32 v13, v12\n\t" \
  "s_nop 1\n\t" \
  "v_permlane32_swap_b32 v12, v13\n\t" \
  "v_add_f32 v12, v12, v13\n\t" \
  "v_rcp_f32 v12, v12\n\t" \
  "s_nop 0\n\t" \
  "v_mul_f32 v14, v11, v12\n\t" \
  "v_mul_f32 v36, v36, v12\n\t" \
  "v_mul_f32 v37, v37, v12\n\t" \
  "v_cvt_pk_bf16_f32 v26, v36, v37\n\t" \
  "v_cvt_pk_bf16_f32 v33, v36, v37\n\t" \
  "v_pk_mul_f32 v[128:129], v[128:129], v[14:15] op_sel_hi:[1,0]\n\t" \
  "v_pk_mul_f32 v[130:131], v[130:131], v[14:15] op_sel_hi:[1,0]\n\t" \
  "v_pk_mul_f32 v[132:133], v[132:133], v[14:15] op_sel_hi:[1,0]\n\t" \
  "v_pk_mul_f32 v[134:135], v[134:135], v[14:15] op_sel_hi:[1,0]\n\t" \
  "v_pk_mul_f32 v[136:137], v[136:137], v[14:15] op_sel_hi:[1,0]\n\t" \
  "v_pk_mul_f32 v[138:139], v[138:139], v[14:15] op_sel_hi:[1,0]\n\t" \
  "v_pk_mul_f32 v[140:141], v[140:141], v[14:15] op_sel_hi:[1,0]\n\t" \
  "v_pk_mul_f32 v[142:143], v[142:143], v[14:15] op_sel_hi:[1,0]\n\t" \
  "s_waitcnt vmcnt(4)\n\t" \
  "v_mfma_f32_16x16x32_bf16 v[128:131], v[204:207], v[26:29], v[128:131]\n\t" \
  "v_mul_f32 v144, v144, v14\n\t" \
  "v_mul_f32 v145, v145, v14\n\t" \
  "v_mfma_f32_16x16x32_bf16 v[132:135], v[204:207], v[32:35], v[132:135]\n\t" \
  "v_mul_f32 v146, v146, v14\n\t" \
  "v_mul_f32 v147, v147, v14\n\t" \
  "v_mfma_f32_16x16x32_bf16 v[136:139], v[204:207], v[24:27], v[136:139]\n\t" \
  "v_mul_f32 v148, v148, v14\n\t" \
  "v_mul_f32 v149, v149, v14\n\t" \
  "v_mfma_f32_16x16x32_bf16 v[140:143], v[204:207], v[30:33], v[140:143]\n\t" \
  "v_cvt_pk_bf16_f32 v64, v128, v129\n\t" \
  "v_cvt_pk_bf16_f32 v65, v130, v131\n\t" \
  "v_cvt_pk_bf16_f32 v66, v132, v133\n\t" \
  "v_cvt_pk_bf16_f32 v67, v134, v135\n\t" \
  "v_mul_f32 v150, v150, v14\n\t" \
  "v_mul_f32 v151, v151, v14\n\t" \
  TK_AC6 \
  "v_mfma_f32_16x16x32_bf16 v[192:195], v[224:227], v[64:67], 0\n\t" \
  "ds_write_b128 v3, v[64:67] offset:0\n\t" \
  "v_cvt_pk_bf16_f32 v68, v136, v137\n\t" \
  "v_cvt_pk_bf16_f32 v69, v138, v139\n\t" \
  "v_mfma_f32_16x16x32_bf16 v[160:163], v[64:67], a[0:3], 0\n\t" \
  "v_cvt_pk_bf16_f32 v70, v140, v141\n\t" \
  "v_cvt_pk_bf16_f32 v71, v142, v143\n\t" \
  "s_waitcnt vmcnt(2)\n\t" \
  "v_mfma_f32_16x16x32_bf16 v[144:147], v[208:211], v[26:29], v[144:147]\n\t" \
  "v_mul_f32 v152, v152, v14\n\t" \
  "v_mul_f32 v153, v153, v14\n\t" \
  "v_mfma_f32_16x16x32_bf16 v[148:151], v[208:211], v[32:35], v[148:151]\n\t" \
  "v_mul_f32 v154, v154, v14\n\t" \
  "v_mul_f32 v155, v155, v14\n\t" \
  "v_mfma_f32_16x16x32_bf16 v[164:167], v[64:67], a[4:7], 0\n\t" \
  "ds_write_b128 v3, v[68:71] offset:1024\n\t" \
  "v_mul_f32 v156, v156, v14\n\t" \
  "v_mul_f32 v157, v157, v14\n\t" \
  "v_mfma_f32_16x16x32_bf16 v[168:171], v[64:67], a[8:11], 0\n\t" \
  "v_mul_f32 v158, v158, v14\n\t" \
  "v_mul_f32 v159, v159, v14\n\t" \
  "v_mfma_f32_16x16x32_bf16 v[172:175], v[64:67], a[12:15], 0\n\t" \
  "v_cvt_pk_bf16_f32 v72, v144, v145\n\t" \
  "v_cvt_pk_bf16_f32 v73, v146, v147\n\t" \
  "v_mfma_f32_16x16x32_bf16 v[152:155], v[208:211], v[24:27], v[152:155]\n\t" \
  "v_cvt_pk_bf16_f32 v74, v148, v149\n\t" \
  "v_cvt_pk_bf16_f32 v75, v150, v151\n\t" \
  "v_mfma_f32_16x16x32_bf16 v[156:159], v[208:211], v[30:33], v[156:159]\n\t" \
  "v_mfma_f32_16x16x32_bf16 v[176:179], v[64:67], a[16:19], 0\n\t" \
  "ds_write_b128 v3, v[72:75] offset:2048\n\t" \
  "v_mfma_f32_16x16x32_bf16 v[180:183], v[64:67], a[20:23], 0\n\t" \
  "v_mfma_f32_16x16x32_bf16 v[184:187], v[64:67], a[24:27], 0\n\t" \
  "v_cvt_pk_bf16_f32 v76, v152, v153\n\t" \
  "v_cvt_pk_bf16_f32 v77, v154, v155\n\t" \
  "v_mfma_f32_16x16x32_bf16 v[188:191], v[64:67], a[28:31], 0\n\t" \
  "v_cvt_pk_bf16_f32 v78, v156, v157\n\t" \
  "v_cvt_pk_bf16_f32 v79, v158, v159\n\t" \
  "v_mfma_f32_16x16x32_bf16 v[192:195], v[228:231], v[68:71], v[192:195]\n\t" \
  "ds_write_b128 v3, v[76:79] offset:3072\n\t" \
  "v_mfma_f32_16x16x32_bf16 v[160:163], v[68:71], a[32:35], v[160:163]\n\t" \
  "s_waitcnt vmcnt(1)\n\t" \
  "v_cmp_eq_u32 vcc, 0, v10\n\t" \
  "v_mfma_f32_16x16x32_bf16 v[164:167], v[68:71], a[36:39], v[164:167]\n\t" \
  "s_cmp_eq_u64 vcc, 0\n\t" \
  "s_cselect_b32 s55, s83, 0xf0000000\n\t" \
  "s_cmp_eq_u32 s48, 63\n\t" \
  "v_mfma_f32_16x16x32_bf16 v[168:171], v[68:71], a[40:43], v[168:171]\n\t" \
  "s_cselect_b32 s55, 0xf0000000, s55\n\t" \
  "s_mov_b32 s56, s83\n\t" \
  "s_mov_b32 s83, s55\n\t" \
  "v_mfma_f32_16x16x32_bf16 v[172:175], v[68:71], a[44:47], v[172:175]\n\t" \
  "v_mfma_f32_16x16x32_bf16 v[176:179], v[68:71], a[48:51], v[176:179]\n\t" \
  "s_add_u32 m0, s46, 0\n\t" \
  "s_add_u32 s47, s83, s36\n\t" \
  "buffer_load_dwordx4 v0, %[X], s47 offen offset:0 sc1 lds\n\t" \
  "v_mfma_f32_16x16x32_bf16 v[180:183], v[68:71], a[52:55], v[180:183]\n\t" \
  "v_mfma_f32_16x16x32_bf16 v[184:187], v[68:71], a[56:59], v[184:187]\n\t" \
  "s_waitcnt lgkmcnt(0)\n\t" \
  "s_barrier\n\t" \
  "v_mfma_f32_16x16x32_bf16 v[188:191], v[68:71], a[60:63], v[188:191]\n\t" \
  "ds_read_b128 v[80:83], v4 offset:0\n\t" \
  "v_mfma_f32_16x16x32_bf16 v[192:195], v[232:235], v[72:75], v[192:195]\n\t" \
  "buffer_load_dwordx4 v0, %[X], s47 offen offset:1024 sc1 lds\n\t" \
  "v_mfma_f32_16x16x32_bf16 v[160:163], v[72:75], a[64:67], v[160:163]\n\t" \
  "ds_read_b128 v[84:87], v4 offset:1024\n\t" \
  "v_mfma_f32_16x16x32_bf16 v[164:167], v[72:75], a[68:71], v[164:167]\n\t" \
  "v_mfma_f32_16x16x32_bf16 v[168:171], v[72:75], a[72:75], v[168:171]\n\t" \
  "ds_read_b128 v[88:91], v4 offset:2048\n\t" \
  "v_mfma_f32_16x16x32_bf16 v[172:175], v[72:75], a[76:79], v[172:175]\n\t" \
  "buffer_load_dwordx4 v0, %[X], s47 offen offset:2048 sc1 lds\n\t" \
  "v_mfma_f32_16x16x32_bf16 v[176:179], v[72:75], a[80:83], v[176:179]\n\t" \
  "ds_read_b128 v[92:95], v4 offset:3072\n\t" \
  "v_mfma_f32_16x16x32_bf16 v[180:183], v[72:75], a[84:87], v[180:183]\n\t" \
  "v_mfma_f32_16x16x32_bf16 v[184:187], v[72:75], a[88:91], v[184:187]\n\t" \
  "v_mfma_f32_16x16x32_bf16 v[188:191], v[72:75], a[92:95], v[188:191]\n\t" \
  "buffer_load_dwordx4 v0, %[X], s47 offen offset:3072 sc1 lds\n\t" \
  "v_mfma_f32_16x16x32_bf16 v[192:195], v[236:239], v[76:79], v[192:195]\n\t" \
  "v_mfma_f32_16x16x32_bf16 v[160:163], v[76:79], a[96:99], v[160:163]\n\t" \
  "v_mfma_f32_16x16x32_bf16 v[164:167], v[76:79], a[100:103], v[164:167]\n\t" \
  "v_mfma_f32_16x16x32_bf16 v[168:171], v[76:79], a[104:107], v[168:171]\n\t" \
  "s_add_u32 m0, s46, 4096\n\t" \
  "s_add_u32 s47, s83, s37\n\t" \
  "buffer_load_dwordx4 v0, %[X], s47 offen offset:0 sc1 lds\n\t" \
  "v_mfma_f32_16x16x32_bf16 v[172:175], v[76:79], a[108:111], v[172:175]\n\t" \
  "v_mfma_f32_16x16x32_bf16 v[176:179], v[76:79], a[112:115], v[176:179]\n\t" \
  "v_mfma_f32_16x16x32_bf16 v[180:183], v[76:79], a[116:119], v[180:183]\n\t" \
  "v_mfma_f32_16x16x32_bf16 v[184:187], v[76:79], a[120:123], v[184:187]\n\t" \
  "buffer_load_dwordx4 v0, %[X], s47 offen offset:1024 sc1 lds\n\t" \
  "v_mfma_f32_16x16x32_bf16 v[188:191], v[76:79], a[124:127], v[188:191]\n\t" \
  "s_waitcnt lgkmcnt(3)\n\t" \
  "v_mfma_f32_16x16x32_bf16 v[192:195], v[240:243], v[80:83], v[192:195]\n\t" \
  "v_mfma_f32_16x16x32_bf16 v[160:163], v[80:83], a[128:131], v[160:163]\n\t" \
  "v_mfma_f32_16x16x32_bf16 v[164:167], v[80:83], a[132:135], v[164:167]\n\t" \
  "buffer_load_dwordx4 v0, %[X], s47 offen offset:2048 sc1 lds\n\t" \
  "v_mfma_f32_16x16x32_bf16 v[168:171], v[80:83], a[136:139], v[168:171]\n\t" \
  "v_mfma_f32_16x16x32_bf16 v[172:175], v[80:83], a[140:143], v[172:175]\n\t" \
  "v_mfma_f32_16x16x32_bf16 v[176:179], v[80:83], a[144:147], v[176:179]\n\t" \
  "v_mfma_f32_16x16x32_bf16 v[180:183], v[80:83], a[148:151], v[180:183]\n\t" \
  "buffer_load_dwordx4 v0, %[X], s47 offen offset:3072 sc1 lds\n\t" \
  "v_mfma_f32_16x16x32_bf16 v[184:187], v[80:83], a[152:155], v[184:187]\n\t" \
  "v_mfma_f32_16x16x32_bf16 v[188:191], v[80:83], a[156:159], v[188:191]\n\t" \
  "s_waitcnt lgkmcnt(2)\n\t" \
  "v_mfma_f32_16x16x32_bf16 v[192:195], v[244:247], v[84:87], v[192:195]\n\t" \
  "v_mfma_f32_16x16x32_bf16 v[160:163], v[84:87], a[160:163], v[160:163]\n\t" \
  "s_add_u32 m0, s46, 8192\n\t" \
  "s_add_u32 s47, s83, s38\n\t" \
  "buffer_load_dwordx4 v0, %[X], s47 offen offset:0 sc1 lds\n\t" \
  "v_mfma_f32_16x16x32_bf16 v[164:167], v[84:87], a[164:167], v[164:167]\n\t" \
  "v_mfma_f32_16x16x32_bf16 v[168:171], v[84:87], a[168:171], v[168:171]\n\t" \
  "v_mfma_f32_16x16x32_bf16 v[172:175], v[84:87], a[172:175], v[172:175]\n\t" \
  "v_mfma_f32_16x16x32_bf16 v[176:179], v[84:87], a[176:179], v[176:179]\n\t" \
  "buffer_load_dwordx4 v0, %[X], s47 offen offset:1024 sc1 lds\n\t" \
  "v_mfma_f32_16x16x32_bf16 v[180:183], v[84:87], a[180:183], v[180:183]\n\t" \
  "v_mfma_f32_16x16x32_bf16 v[184:187], v[84:87], a[184:187], v[184:187]\n\t" \
  "v_mfma_f32_16x16x32_bf16 v[188:191], v[84:87], a[188:191], v[188:191]\n\t" \
  "s_waitcnt lgkmcnt(1)\n\t" \
  "v_mfma_f32_16x16x32_bf16 v[192:195], v[248:251], v[88:91], v[192:195]\n\t" \
  "buffer_load_dwordx4 v0, %[X], s47 offen offset:2048 sc1 lds\n\t" \
  "v_mfma_f32_16x16x32_bf16 v[160:163], v[88:91], a[192:195], v[160:163]\n\t" \
  "v_mfma_f32_16x16x32_bf16 v[164:167], v[88:91], a[196:199], v[164:167]\n\t" \
  "v_mfma_f32_16x16x32_bf16 v[168:171], v[88:91], a[200:203], v[168:171]\n\t" \
  "v_mfma_f32_16x16x32_bf16 v[172:175], v[88:91], a[204:207], v[172:175]\n\t" \
  "buffer_load_dwordx4 v0, %[X], s47 offen offset:3072 sc1 lds\n\t" \
  "v_mfma_f32_16x16x32_bf16 v[176:179], v[88:91], a[208:211], v[176:179]\n\t" \
  "v_mfma_f32_16x16x32_bf16 v[180:183], v[88:91], a[212:215], v[180:183]\n\t" \
  "v_mfma_f32_16x16x32_bf16 v[184:187], v[88:91], a[216:219], v[184:187]\n\t" \
  "s_add_u32 s63, s63, 4096\n\t" \
  "v_mfma_f32_16x16x32_bf16 v[188:191], v[88:91], a[220:223], v[188:191]\n\t" \
  "buffer_load_dword v5, off, %[X], s63 sc1\n\t" \
  "s_waitcnt lgkmcnt(0)\n\t" \
  "v_mfma_f32_16x16x32_bf16 v[192:195], v[252:255], v[92:95], v[192:195]\n\t" \
  "v_mfma_f32_16x16x32_bf16 v[160:163], v[92:95], a[224:227], v[160:163]\n\t" \
  "v_mfma_f32_16x16x32_bf16 v[164:167], v[92:95], a[228:231], v[164:167]\n\t" \
  "v_mfma_f32_16x16x32_bf16 v[168:171], v[92:95], a[232:235], v[168:171]\n\t" \
  "v_cvt_pk_bf16_f32 v112, v192, v193\n\t" \
  "v_cvt_pk_bf16_f32 v113, v194, v195\n\t" \
  "v_mfma_f32_16x16x32_bf16 v[172:175], v[92:95], a[236:239], v[172:175]\n\t" \
  "buffer_store_dwordx2 v[112:113], v8, %[X], s54 offen sc1\n\t" \
  "v_mfma_f32_16x16x32_bf16 v[176:179], v[92:95], a[240:243], v[176:179]\n\t" \
  "v_cvt_pk_bf16_f32 v96, v160, v164\n\t" \
  "v_cvt_pk_bf16_f32 v100, v161, v165\n\t" \
  "v_mfma_f32_16x16x32_bf16 v[180:183], v[92:95], a[244:247], v[180:183]\n\t" \
  "v_cvt_pk_bf16_f32 v104, v162, v166\n\t" \
  "v_cvt_pk_bf16_f32 v108, v163, v167\n\t" \
  "v_mfma_f32_16x16x32_bf16 v[184:187], v[92:95], a[248:251], v[184:187]\n\t" \
  "v_cvt_pk_bf16_f32 v97, v168, v172\n\t" \
  "v_cvt_pk_bf16_f32 v101, v169, v173\n\t" \
  "v_mfma_f32_16x16x32_bf16 v[188:191], v[92:95], a[252:255], v[188:191]\n\t" \
  "v_cvt_pk_bf16_f32 v105, v170, v174\n\t" \
  "v_cvt_pk_bf16_f32 v109, v171, v175\n\t" \
  "v_cvt_pk_bf16_f32 v98, v176, v180\n\t" \
  "v_cvt_pk_bf16_f32 v102, v177, v181\n\t" \
  "v_cvt_pk_bf16_f32 v106, v178, v182\n\t" \
  "v_cvt_pk_bf16_f32 v110, v179, v183\n\t" \
  TK_AC7 \
  "s_nop 1\n\t" \
  "v_cvt_pk_bf16_f32 v99, v184, v188\n\t" \
  "buffer_store_dwordx4 v[96:99], v7, %[X], s50 offen sc1\n\t" \
  TK_ASL \
  "v_cvt_pk_bf16_f32 v103, v185, v189\n\t" \
  "buffer_store_dwordx4 v[100:103], v7, %[X], s51 offen sc1\n\t" \
  TK_ASL \
  "v_cvt_pk_bf16_f32 v107, v186, v190\n\t" \
  "buffer_store_dwordx4 v[104:107], v7, %[X], s52 offen sc1\n\t" \
  TK_ASL \
  "v_cvt_pk_bf16_f32 v111, v187, v191\n\t" \
  "buffer_store_dwordx4 v[108:111], v7, %[X], s53 offen sc1\n\t" \
  TK_PA2 \
  TK_AC8 \
  "s_cmp_eq_u32 s48, 63\n\t" \
  "s_cbranch_scc1 LEND_%=\n\t" \
  "s_add_u32 s48, s48, 1\n\t" \
  "s_cmp_eq_u32 s83, s56\n\t" \
  "s_mov_b32 s83, s56\n\t" \
  "s_cbranch_scc0 LCOLD_%=\n\t" \
  "s_waitcnt vmcnt(5)\n\t" \
  "s_branch LTOP_%=\n\t" \
  "LEND_%=:\n\t" \
  "s_waitcnt vmcnt(0)\n\t" \
  "s_barrier\n\t" \
  "s_mov_b64 exec, 1\n\t" \
  "buffer_store_dword v3, off, %[X], s49 sc1\n\t" \
  "s_mov_b64 exec, -1\n\t" \
  "s_mov_b32 s49, 0xf0000000\n\t" \
  TK_PA0 \
  TK_ACE \
  ""
#define QK_ASM \
  "s_lshl_b32 s61, s44, 11\n\t" \
  "buffer_load_dwordx4 a[0:3], v0, %[W], s61 offen offset:0\n\t" \
  "buffer_load_dwordx4 a[4:7], v0, %[W], s61 offen offset:1024\n\t" \
  "s_add_u32 s61, s61, 18432\n\t" \
  "buffer_load_dwordx4 a[72:75], v0, %[W], s61 offen offset:0\n\t" \
  "buffer_load_dwordx4 a[76:79], v0, %[W], s61 offen offset:1024\n\t" \
  "s_add_u32 s61, s61, 18432\n\t" \
  "buffer_load_dwordx4 a[144:147], v0, %[W], s61 offen offset:0\n\t" \
  "buffer_load_dwordx4 a[148:151], v0, %[W], s61 offen offset:1024\n\t" \
  "s_add_u32 s61, s61, 18432\n\t" \
  "buffer_load_dwordx4 a[216:219], v0, %[W], s61 offen offset:0\n\t" \
  "buffer_load_dwordx4 a[220:223], v0, %[W], s61 offen offset:1024\n\t" \
  "s_lshr_b32 s60, s44, 1\n\t" \
  "s_lshl_b32 s52, s60, 14\n\t" \
  "s_and_b32 s60, s44, 1\n\t" \
  "s_lshl_b32 s60, s60, 12\n\t" \
  "s_add_u32 s52, s52, s60\n\t" \
  "s_add_u32 s44, s44, 1\n\t" \
  "s_cmp_eq_u32 s44, 9\n\t" \
  "s_cselect_b32 s44, 0, s44\n\t" \
  "s_lshl_b32 s61, s44, 11\n\t" \
  "buffer_load_dwordx4 a[8:11], v0, %[W], s61 offen offset:0\n\t" \
  "buffer_load_dwordx4 a[12:15], v0, %[W], s61 offen offset:1024\n\t" \
  "s_add_u32 s61, s61, 18432\n\t" \
  "buffer_load_dwordx4 a[80:83], v0, %[W], s61 offen offset:0\n\t" \
  "buffer_load_dwordx4 a[84:87], v0, %[W], s61 offen offset:1024\n\t" \
  "s_add_u32 s61, s61, 18432\n\t" \
  "buffer_load_dwordx4 a[152:155], v0, %[W], s61 offen offset:0\n\t" \
  "buffer_load_dwordx4 a[156:159], v0, %[W], s61 offen offset:1024\n\t" \
  "s_add_u32 s61, s61, 18432\n\t" \
  "buffer_load_dwordx4 a[224:227], v0, %[W], s61 offen offset:0\n\t" \
  "buffer_load_dwordx4 a[228:231], v0, %[W], s61 offen offset:1024\n\t" \
  "s_lshr_b32 s60, s44, 1\n\t" \
  "s_lshl_b32 s53, s60, 14\n\t" \
  "s_and_b32 s60, s44, 1\n\t" \
  "s_lshl_b32 s60, s60, 12\n\t" \
  "s_add_u32 s53, s53, s60\n\t" \
  "s_add_u32 s44, s44, 1\n\t" \
  "s_cmp_eq_u32 s44, 9\n\t" \
  "s_cselect_b32 s44, 0, s44\n\t" \
  "s_lshl_b32 s61, s44, 11\n\t" \
  "buffer_load_dwordx4 a[16:19], v0, %[W], s61 offen offset:0\n\t" \
  "buffer_load_dwordx4 a[20:23], v0, %[W], s61 offen offset:1024\n\t" \
  "s_add_u32 s61, s61, 18432\n\t" \
  "buffer_load_dwordx4 a[88:91], v0, %[W], s61 offen offset:0\n\t" \
  "buffer_load_dwordx4 a[92:95], v0, %[W], s61 offen offset:1024\n\t" \
  "s_add_u32 s61, s61, 18432\n\t" \
  "buffer_load_dwordx4 a[160:163], v0, %[W], s61 offen offset:0\n\t" \
  "buffer_load_dwordx4 a[164:167], v0, %[W], s61 offen offset:1024\n\t" \
  "s_add_u32 s61, s61, 18432\n\t" \
  "buffer_load_dwordx4 a[232:235], v0, %[W], s61 offen offset:0\n\t" \
  "buffer_load_dwordx4 a[236:239], v0, %[W], s61 offen offset:1024\n\t" \
  "s_lshr_b32 s60, s44, 1\n\t" \
  "s_lshl_b32 s54, s60, 14\n\t" \
  "s_and_b32 s60, s44, 1\n\t" \
  "s_lshl_b32 s60, s60, 12\n\t" \
  "s_add_u32 s54, s54, s60\n\t" \
  "s_add_u32 s44, s44, 1\n\t" \
  "s_cmp_eq_u32 s44, 9\n\t" \
  "s_cselect_b32 s44, 0, s44\n\t" \
  "s_lshl_b32 s61, s44, 11\n\t" \
  "buffer_load_dwordx4 a[24:27], v0, %[W], s61 offen offset:0\n\t" \
  "buffer_load_dwordx4 a[28:31], v0, %[W], s61 offen offset:1024\n\t" \
  "s_add_u32 s61, s61, 18432\n\t" \
  "buffer_load_dwordx4 a[96:99], v0, %[W], s61 offen offset:0\n\t" \
  "buffer_load_dwordx4 a[100:103], v0, %[W], s61 offen offset:1024\n\t" \
  "s_add_u32 s61, s61, 18432\n\t" \
  "buffer_load_dwordx4 a[168:171], v0, %[W], s61 offen offset:0\n\t" \
  "buffer_load_dwordx4 a[172:175], v0, %[W], s61 offen offset:1024\n\t" \
  "s_add_u32 s61, s61, 18432\n\t" \
  "buffer_load_dwordx4 a[240:243], v0, %[W], s61 offen offset:0\n\t" \
  "buffer_load_dwordx4 a[244:247], v0, %[W], s61 offen offset:1024\n\t" \
  "s_lshr_b32 s60, s44, 1\n\t" \
  "s_lshl_b32 s57, s60, 14\n\t" \
  "s_and_b32 s60, s44, 1\n\t" \
  "s_lshl_b32 s60, s60, 12\n\t" \
  "s_add_u32 s57, s57, s60\n\t" \
  "s_add_u32 s44, s44, 1\n\t" \
  "s_cmp_eq_u32 s44, 9\n\t" \
  "s_cselect_b32 s44, 0, s44\n\t" \
  "s_lshl_b32 s61, s44, 11\n\t" \
  "buffer_load_dwordx4 a[32:35], v0, %[W], s61 offen offset:0\n\t" \
  "buffer_load_dwordx4 a[36:39], v0, %[W], s61 offen offset:1024\n\t" \
  "s_add_u32 s61, s61, 18432\n\t" \
  "buffer_load_dwordx4 a[104:107], v0, %[W], s61 offen offset:0\n\t" \
  "buffer_load_dwordx4 a[108:111], v0, %[W], s61 offen offset:1024\n\t" \
  "s_add_u32 s61, s61, 18432\n\t" \
  "buffer_load_dwordx4 a[176:179], v0, %[W], s61 offen offset:0\n\t" \
  "buffer_load_dwordx4 a[180:183], v0, %[W], s61 offen offset:1024\n\t" \
  "s_add_u32 s61, s61, 18432\n\t" \
  "buffer_load_dwordx4 a[248:251], v0, %[W], s61 offen offset:0\n\t" \
  "buffer_load_dwordx4 a[252:255], v0, %[W], s61 offen offset:1024\n\t" \
  "s_lshr_b32 s60, s44, 1\n\t" \
  "s_lshl_b32 s58, s60, 14\n\t" \
  "s_and_b32 s60, s44, 1\n\t" \
  "s_lshl_b32 s60, s60, 12\n\t" \
  "s_add_u32 s58, s58, s60\n\t" \
  "s_add_u32 s44, s44, 1\n\t" \
  "s_cmp_eq_u32 s44, 9\n\t" \
  "s_cselect_b32 s44, 0, s44\n\t" \
  "s_lshl_b32 s61, s44, 11\n\t" \
  "buffer_load_dwordx4 a[40:43], v0, %[W], s61 offen offset:0\n\t" \
  "buffer_load_dwordx4 a[44:47], v0, %[W], s61 offen offset:1024\n\t" \
  "s_add_u32 s61, s61, 18432\n\t" \
  "buffer_load_dwordx4 a[112:115], v0, %[W], s61 offen offset:0\n\t" \
  "buffer_load_dwordx4 a[116:119], v0, %[W], s61 offen offset:1024\n\t" \
  "s_add_u32 s61, s61, 18432\n\t" \
  "buffer_load_dwordx4 a[184:187], v0, %[W], s61 offen offset:0\n\t" \
  "buffer_load_dwordx4 a[188:191], v0, %[W], s61 offen offset:1024\n\t" \
  "s_add_u32 s61, s61, 18432\n\t" \
  "buffer_load_dwordx4 v[224:227], v0, %[W], s61 offen offset:0\n\t" \
  "buffer_load_dwordx4 v[228:231], v0, %[W], s61 offen offset:1024\n\t" \
  "s_lshr_b32 s60, s44, 1\n\t" \
  "s_lshl_b32 s59, s60, 14\n\t" \
  "s_and_b32 s60, s44, 1\n\t" \
  "s_lshl_b32 s60, s60, 12\n\t" \
  "s_add_u32 s59, s59, s60\n\t" \
  "s_add_u32 s44, s44, 1\n\t" \
  "s_cmp_eq_u32 s44, 9\n\t" \
  "s_cselect_b32 s44, 0, s44\n\t" \
  "s_lshl_b32 s61, s44, 11\n\t" \
  "buffer_load_dwordx4 a[48:51], v0, %[W], s61 offen offset:0\n\t" \
  "buffer_load_dwordx4 a[52:55], v0, %[W], s61 offen offset:1024\n\t" \
  "s_add_u32 s61, s61, 18432\n\t" \
  "buffer_load_dwordx4 a[120:123], v0, %[W], s61 offen offset:0\n\t" \
  "buffer_load_dwordx4 a[124:127], v0, %[W], s61 offen offset:1024\n\t" \
  "s_add_u32 s61, s61, 18432\n\t" \
  "buffer_load_dwordx4 a[192:195], v0, %[W], s61 offen offset:0\n\t" \
  "buffer_load_dwordx4 a[196:199], v0, %[W], s61 offen offset:1024\n\t" \
  "s_add_u32 s61, s61, 18432\n\t" \
  "buffer_load_dwordx4 v[232:235], v0, %[W], s61 offen offset:0\n\t" \
  "buffer_load_dwordx4 v[236:239], v0, %[W], s61 offen offset:1024\n\t" \
  "s_lshr_b32 s60, s44, 1\n\t" \
  "s_lshl_b32 s64, s60, 14\n\t" \
  "s_and_b32 s60, s44, 1\n\t" \
  "s_lshl_b32 s60, s60, 12\n\t" \
  "s_add_u32 s64, s64, s60\n\t" \
  "s_add_u32 s44, s44, 1\n\t" \
  "s_cmp_eq_u32 s44, 9\n\t" \
  "s_cselect_b32 s44, 0, s44\n\t" \
  "s_waitcnt vmcnt(40)\n\t" \
  "s_lshl_b32 s61, s44, 11\n\t" \
  "buffer_load_dwordx4 a[56:59], v0, %[W], s61 offen offset:0\n\t" \
  "buffer_load_dwordx4 a[60:63], v0, %[W], s61 offen offset:1024\n\t" \
  "s_add_u32 s61, s61, 18432\n\t" \
  "buffer_load_dwordx4 a[128:131], v0, %[W], s61 offen offset:0\n\t" \
  "buffer_load_dwordx4 a[132:135], v0, %[W], s61 offen offset:1024\n\t" \
  "s_add_u32 s61, s61, 18432\n\t" \
  "buffer_load_dwordx4 a[200:203], v0, %[W], s61 offen offset:0\n\t" \
  "buffer_load_dwordx4 a[204:207], v0, %[W], s61 offen offset:1024\n\t" \
  "s_add_u32 s61, s61, 18432\n\t" \
  "buffer_load_dwordx4 v[240:243], v0, %[W], s61 offen offset:0\n\t" \
  "buffer_load_dwordx4 v[244:247], v0, %[W], s61 offen offset:1024\n\t" \
  "s_lshr_b32 s60, s44, 1\n\t" \
  "s_lshl_b32 s65, s60, 14\n\t" \
  "s_and_b32 s60, s44, 1\n\t" \
  "s_lshl_b32 s60, s60, 12\n\t" \
  "s_add_u32 s65, s65, s60\n\t" \
  "s_add_u32 s44, s44, 1\n\t" \
  "s_cmp_eq_u32 s44, 9\n\t" \
  "s_cselect_b32 s44, 0, s44\n\t" \
  "s_lshl_b32 s61, s44, 11\n\t" \
  "buffer_load_dwordx4 a[64:67], v0, %[W], s61 offen offset:0\n\t" \
  "buffer_load_dwordx4 a[68:71], v0, %[W], s61 offen offset:1024\n\t" \
  "s_add_u32 s61, s61, 18432\n\t" \
  "buffer_load_dwordx4 a[136:139], v0, %[W], s61 offen offset:0\n\t" \
  "buffer_load_dwordx4 a[140:143], v0, %[W], s61 offen offset:1024\n\t" \
  "s_add_u32 s61, s61, 18432\n\t" \
  "buffer_load_dwordx4 a[208:211], v0, %[W], s61 offen offset:0\n\t" \
  "buffer_load_dwordx4 a[212:215], v0, %[W], s61 offen offset:1024\n\t" \
  "s_add_u32 s61, s61, 18432\n\t" \
  "buffer_load_dwordx4 v[248:251], v0, %[W], s61 offen offset:0\n\t" \
  "buffer_load_dwordx4 v[252:255], v0, %[W], s61 offen offset:1024\n\t" \
  "s_lshr_b32 s60, s44, 1\n\t" \
  "s_lshl_b32 s66, s60, 14\n\t" \
  "s_and_b32 s60, s44, 1\n\t" \
  "s_lshl_b32 s60, s60, 12\n\t" \
  "s_add_u32 s66, s66, s60\n\t" \
  "s_add_u32 s44, s44, 1\n\t" \
  "s_cmp_eq_u32 s44, 9\n\t" \
  "s_cselect_b32 s44, 0, s44\n\t" \
  "s_mov_b32 s84, " TK_CAP "\n\t" \
  "s_mov_b32 s48, 0\n\t" \
  "s_mov_b32 s49, 0xf0000000\n\t" \
  "s_mov_b32 s46, s41\n\t" \
  "s_mov_b32 s56, 0\n\t" \
  "s_add_u32 s83, s48, 0\n\t" \
  "s_lshr_b64 s[60:61], s[90:91], s83\n\t" \
  "s_and_b32 s60, s60, 1\n\t" \
  "s_lshl_b32 s60, s60, 13\n\t" \
  "s_add_u32 s83, s83, " TK_SG0 "\n\t" \
  "s_lshl_b32 s83, s83, 21\n\t" \
  "s_or_b32 s83, s83, s60\n\t" \
  "s_add_u32 s63, s48, s42\n\t" \
  "s_lshr_b64 s[60:61], s[90:91], s63\n\t" \
  "s_and_b32 s60, s60, 1\n\t" \
  "s_lshl_b32 s60, s60, 13\n\t" \
  "s_add_u32 s63, s63, " TK_SG0 "\n\t" \
  "s_lshl_b32 s63, s63, 21\n\t" \
  "s_or_b32 s63, s63, s60\n\t" \
  "s_add_u32 s62, s83, s39\n\t" \
  "s_add_u32 s63, s63, s43\n\t" \
  "s_add_i32 s60, s48, s42\n\t" \
  "s_cmp_lt_i32 s60, 0\n\t" \
  "s_cselect_b32 s63, s62, s63\n\t" \
  "v_add_u32 v7, s62, v3\n\t" \
  "s_mov_b32 exec_lo, 0\n\t" \
  "v_mov_b32 v7, s63\n\t" \
  "s_mov_b32 exec_lo, -1\n\t" \
  "LCOLD_%=:\n\t" \
  "s_waitcnt vmcnt(0)\n\t" \
  "s_mov_b64 exec, 1\n\t" \
  "buffer_store_dword v6, off, %[X], s49 sc1\n\t" \
  "s_mov_b64 exec, -1\n\t" \
  "s_mov_b32 s49, 0xf0000000\n\t" \
  "s_add_u32 s55, s83, s36\n\t" \
  "s_cmp_lg_u32 s40, 0\n\t" \
  "s_cbranch_scc1 LPH_%=x\n\t" \
  "s_mov_b32 s61, 0\n\t" \
  "LPH_%=:\n\t" \
  "buffer_load_dword v4, v7, %[X], 0 offen sc1\n\t" \
  "s_waitcnt vmcnt(0)\n\t" \
  "v_cmp_eq_u32 vcc, 0, v4\n\t" \
  "s_cbranch_vccz LPH_%=x\n\t" \
  "s_add_u32 s61, s61, 1\n\t" \
  "s_sleep " TK_PSH "\n\t" \
  "s_cmp_gt_u32 s61, s84\n\t" \
  "s_cbranch_scc1 LPH_%=g\n\t" \
  "s_and_b32 s60, s61, 1023\n\t" \
  "s_cmp_lg_u32 s60, 0\n\t" \
  "s_cbranch_scc1 LPH_%=\n\t" \
  "buffer_load_dword v4, off, %[X], s92 sc1\n\t" \
  "s_waitcnt vmcnt(0)\n\t" \
  "v_readfirstlane_b32 s60, v4\n\t" \
  "s_nop 0\n\t" \
  "s_cmp_eq_u32 s60, 0\n\t" \
  "s_cbranch_scc1 LPH_%=\n\t" \
  "LPH_%=g:\n\t" \
  "s_mov_b32 s84, 0\n\t" \
  "s_or_b32 s60, s48, 1610612736\n\t" \
  "v_mov_b32 v4, s60\n\t" \
  "s_mov_b64 exec, 1\n\t" \
  "buffer_store_dword v4, off, %[X], s92 sc1\n\t" \
  "s_mov_b64 exec, -1\n\t" \
  "LPH_%=x:\n\t" \
  TK_QDL \
  "s_barrier\n\t" \
  "s_add_u32 m0, s46, 0\n\t" \
  "s_add_u32 s47, s55, s52\n\t" \
  "buffer_load_dwordx4 v0, %[X], s47 offen sc1 lds\n\t" \
  "s_add_u32 m0, s46, 4096\n\t" \
  "s_add_u32 s47, s55, s53\n\t" \
  "buffer_load_dwordx4 v0, %[X], s47 offen sc1 lds\n\t" \
  "s_add_u32 m0, s46, 8192\n\t" \
  "s_add_u32 s47, s55, s54\n\t" \
  "buffer_load_dwordx4 v0, %[X], s47 offen sc1 lds\n\t" \
  "s_add_u32 m0, s46, 12288\n\t" \
  "s_add_u32 s47, s55, s57\n\t" \
  "buffer_load_dwordx4 v0, %[X], s47 offen sc1 lds\n\t" \
  "s_add_u32 m0, s46, 16384\n\t" \
  "s_add_u32 s47, s55, s58\n\t" \
  "buffer_load_dwordx4 v0, %[X], s47 offen sc1 lds\n\t" \
  "s_add_u32 m0, s46, 20480\n\t" \
  "s_add_u32 s47, s55, s59\n\t" \
  "buffer_load_dwordx4 v0, %[X], s47 offen sc1 lds\n\t" \
  "s_add_u32 m0, s46, 24576\n\t" \
  "s_add_u32 s47, s55, s64\n\t" \
  "buffer_load_dwordx4 v0, %[X], s47 offen sc1 lds\n\t" \
  "s_add_u32 m0, s46, 28672\n\t" \
  "s_add_u32 s47, s55, s65\n\t" \
  "buffer_load_dwordx4 v0, %[X], s47 offen sc1 lds\n\t" \
  "s_add_u32 m0, s46, 32768\n\t" \
  "s_add_u32 s47, s55, s66\n\t" \
  "buffer_load_dwordx4 v0, %[X], s47 offen sc1 lds\n\t" \
  "s_waitcnt vmcnt(5)\n\t" \
  "LTOP_%=:\n\t" \
  "s_barrier\n\t" \
  TK_PQ1 \
  "ds_read_b128 v[64:67], v1 offset:0\n\t" \
  "ds_read_b128 v[68:71], v1 offset:2048\n\t" \
  "ds_read_b128 v[72:75], v2 offset:0\n\t" \
  "ds_read_b128 v[76:79], v2 offset:2048\n\t" \
  "ds_read_b128 v[80:83], v1 offset:4096\n\t" \
  "ds_read_b128 v[84:87], v1 offset:6144\n\t" \
  "s_mov_b32 s82, s83\n\t" \
  "s_xor_b32 s46, s46, 0x10000\n\t" \
  "s_waitcnt lgkmcnt(5)\n\t" \
  "v_mfma_f32_16x16x32_bf16 v[16:19], a[0:3], v[64:67], 0\n\t" \
  "ds_read_b128 v[88:91], v2 offset:4096\n\t" \
  "v_mfma_f32_16x16x32_bf16 v[20:23], a[72:75], v[64:67], 0\n\t" \
  "s_add_u32 s83, s48, 1\n\t" \
  "s_lshr_b64 s[60:61], s[90:91], s83\n\t" \
  "s_and_b32 s60, s60, 1\n\t" \
  TK_QNP \
  "v_mfma_f32_16x16x32_bf16 v[24:27], a[144:147], v[64:67], 0\n\t" \
  "ds_read_b128 v[92:95], v2 offset:6144\n\t" \
  "v_mfma_f32_16x16x32_bf16 v[28:31], a[216:219], v[64:67], 0\n\t" \
  "s_lshl_b32 s60, s60, 13\n\t" \
  "s_add_u32 s83, s83, " TK_SG0 "\n\t" \
  "s_lshl_b32 s83, s83, 21\n\t" \
  TK_QNP \
  "s_waitcnt lgkmcnt(5)\n\t" \
  "v_mfma_f32_16x16x32_bf16 v[16:19], a[4:7], v[72:75], v[16:19]\n\t" \
  "ds_read_b128 v[96:99], v1 offset:8192\n\t" \
  "v_mfma_f32_16x16x32_bf16 v[20:23], a[76:79], v[72:75], v[20:23]\n\t" \
  "s_or_b32 s83, s83, s60\n\t" \
  "s_add_u32 s63, s48, s42\n\t" \
  "s_lshr_b64 s[60:61], s[90:91], s63\n\t" \
  TK_QNP \
  "v_mfma_f32_16x16x32_bf16 v[24:27], a[148:151], v[72:75], v[24:27]\n\t" \
  "ds_read_b128 v[100:103], v1 offset:10240\n\t" \
  "v_mfma_f32_16x16x32_bf16 v[28:31], a[220:223], v[72:75], v[28:31]\n\t" \
  "s_and_b32 s60, s60, 1\n\t" \
  "s_lshl_b32 s60, s60, 13\n\t" \
  "s_add_u32 s63, s63, " TK_SG0 "\n\t" \
  TK_QNP \
  "s_waitcnt lgkmcnt(5)\n\t" \
  "v_mfma_f32_16x16x32_bf16 v[16:19], a[8:11], v[80:83], v[16:19]\n\t" \
  "ds_read_b128 v[104:107], v2 offset:8192\n\t" \
  "v_mfma_f32_16x16x32_bf16 v[20:23], a[80:83], v[80:83], v[20:23]\n\t" \
  "s_lshl_b32 s63, s63, 21\n\t" \
  "s_or_b32 s63, s63, s60\n\t" \
  "s_add_u32 s62, s83, s39\n\t" \
  TK_QNP \
  "v_mfma_f32_16x16x32_bf16 v[24:27], a[152:155], v[80:83], v[24:27]\n\t" \
  "ds_read_b128 v[108:111], v2 offset:10240\n\t" \
  "v_mfma_f32_16x16x32_bf16 v[28:31], a[224:227], v[80:83], v[28:31]\n\t" \
  "s_add_u32 s63, s63, s43\n\t" \
  "s_add_i32 s60, s48, s42\n\t" \
  "s_cmp_lt_i32 s60, 0\n\t" \
  TK_QNP \
  "s_waitcnt lgkmcnt(5)\n\t" \
  "v_mfma_f32_16x16x32_bf16 v[16:19], a[12:15], v[88:91], v[16:19]\n\t" \
  "ds_read_b128 v[112:115], v1 offset:12288\n\t" \
  "v_mfma_f32_16x16x32_bf16 v[20:23], a[84:87], v[88:91], v[20:23]\n\t" \
  "s_cselect_b32 s63, s62, s63\n\t" \
  TK_QNP \
  "v_mfma_f32_16x16x32_bf16 v[24:27], a[156:159], v[88:91], v[24:27]\n\t" \
  "ds_read_b128 v[116:119], v1 offset:14336\n\t" \
  "v_mfma_f32_16x16x32_bf16 v[28:31], a[228:231], v[88:91], v[28:31]\n\t" \
  "v_add_u32 v7, s62, v3\n\t" \
  "s_mov_b32 exec_lo, 0\n\t" \
  "v_mov_b32 v7, s63\n\t" \
  "s_mov_b32 exec_lo, -1\n\t" \
  TK_QNP \
  "s_waitcnt lgkmcnt(5)\n\t" \
  "v_mfma_f32_16x16x32_bf16 v[16:19], a[16:19], v[96:99], v[16:19]\n\t" \
  "ds_read_b128 v[120:123], v2 offset:12288\n\t" \
  "v_mfma_f32_16x16x32_bf16 v[20:23], a[88:91], v[96:99], v[20:23]\n\t" \
  "s_add_u32 s50, s82, s37\n\t" \
  "s_add_u32 s51, s50, 4096\n\t" \
  "s_cmp_lg_u32 s56, 0\n\t" \
  TK_QNP \
  "v_mfma_f32_16x16x32_bf16 v[24:27], a[160:163], v[96:99], v[24:27]\n\t" \
  "ds_read_b128 v[124:127], v2 offset:14336\n\t" \
  "v_mfma_f32_16x16x32_bf16 v[28:31], a[232:235], v[96:99], v[28:31]\n\t" \
  "s_cbranch_scc1 LWARM_%=\n\t" \
  "s_waitcnt vmcnt(0)\n\t" \
  "LWARM_%=:\n\t" \
  "s_barrier\n\t" \
  TK_QNP \
  "s_waitcnt lgkmcnt(5)\n\t" \
  "v_mfma_f32_16x16x32_bf16 v[16:19], a[20:23], v[104:107], v[16:19]\n\t" \
  "ds_read_b128 v[128:131], v1 offset:16384\n\t" \
  "v_mfma_f32_16x16x32_bf16 v[20:23], a[92:95], v[104:107], v[20:23]\n\t" \
  "buffer_load_dword v4, v7, %[X], 0 offen sc1\n\t" \
  TK_QNP \
  "v_mfma_f32_16x16x32_bf16 v[24:27], a[164:167], v[104:107], v[24:27]\n\t" \
  "ds_read_b128 v[132:135], v1 offset:18432\n\t" \
  "v_mfma_f32_16x16x32_bf16 v[28:31], a[236:239], v[104:107], v[28:31]\n\t" \
  TK_QNP \
  "s_waitcnt lgkmcnt(5)\n\t" \
  "v_mfma_f32_16x16x32_bf16 v[16:19], a[24:27], v[112:115], v[16:19]\n\t" \
  "ds_read_b128 v[136:139], v2 offset:16384\n\t" \
  "v_mfma_f32_16x16x32_bf16 v[20:23], a[96:99], v[112:115], v[20:23]\n\t" \
  TK_QNP \
  "v_mfma_f32_16x16x32_bf16 v[24:27], a[168:171], v[112:115], v[24:27]\n\t" \
  "ds_read_b128 v[140:143], v2 offset:18432\n\t" \
  "v_mfma_f32_16x16x32_bf16 v[28:31], a[240:243], v[112:115], v[28:31]\n\t" \
  TK_QNP \
  "s_waitcnt lgkmcnt(5)\n\t" \
  "v_mfma_f32_16x16x32_bf16 v[16:19], a[28:31], v[120:123], v[16:19]\n\t" \
  "ds_read_b128 v[144:147], v1 offset:20480\n\t" \
  "v_mfma_f32_16x16x32_bf16 v[20:23], a[100:103], v[120:123], v[20:23]\n\t" \
  TK_QNP \
  "v_mfma_f32_16x16x32_bf16 v[24:27], a[172:175], v[120:123], v[24:27]\n\t" \
  "ds_read_b128 v[148:151], v1 offset:22528\n\t" \
  "v_mfma_f32_16x16x32_bf16 v[28:31], a[244:247], v[120:123], v[28:31]\n\t" \
  TK_QNP \
  "s_waitcnt lgkmcnt(5)\n\t" \
  "v_mfma_f32_16x16x32_bf16 v[16:19], a[32:35], v[128:131], v[16:19]\n\t" \
  "ds_read_b128 v[152:155], v2 offset:20480\n\t" \
  "v_mfma_f32_16x16x32_bf16 v[20:23], a[104:107], v[128:131], v[20:23]\n\t" \
  TK_QNP \
  "v_mfma_f32_16x16x32_bf16 v[24:27], a[176:179], v[128:131], v[24:27]\n\t" \
  "ds_read_b128 v[156:159], v2 offset:22528\n\t" \
  "v_mfma_f32_16x16x32_bf16 v[28:31], a[248:251], v[128:131], v[28:31]\n\t" \
  TK_QNP \
  "s_waitcnt lgkmcnt(5)\n\t" \
  "v_mfma_f32_16x16x32_bf16 v[16:19], a[36:39], v[136:139], v[16:19]\n\t" \
  "ds_read_b128 v[160:163], v1 offset:24576\n\t" \
  "v_mfma_f32_16x16x32_bf16 v[20:23], a[108:111], v[136:139], v[20:23]\n\t" \
  TK_QNP \
  "v_mfma_f32_16x16x32_bf16 v[24:27], a[180:183], v[136:139], v[24:27]\n\t" \
  "ds_read_b128 v[164:167], v1 offset:26624\n\t" \
  "v_mfma_f32_16x16x32_bf16 v[28:31], a[252:255], v[136:139], v[28:31]\n\t" \
  TK_QNP \
  "s_waitcnt lgkmcnt(5)\n\t" \
  "v_mfma_f32_16x16x32_bf16 v[16:19], a[40:43], v[144:147], v[16:19]\n\t" \
  "ds_read_b128 v[168:171], v2 offset:24576\n\t" \
  "v_mfma_f32_16x16x32_bf16 v[20:23], a[112:115], v[144:147], v[20:23]\n\t" \
  TK_QNP \
  "v_mfma_f32_16x16x32_bf16 v[24:27], a[184:187], v[144:147], v[24:27]\n\t" \
  "ds_read_b128 v[172:175], v2 offset:26624\n\t" \
  "v_mfma_f32_16x16x32_bf16 v[28:31], v[224:227], v[144:147], v[28:31]\n\t" \
  TK_QNP \
  "s_waitcnt lgkmcnt(5)\n\t" \
  "v_mfma_f32_16x16x32_bf16 v[16:19], a[44:47], v[152:155], v[16:19]\n\t" \
  "ds_read_b128 v[176:179], v1 offset:28672\n\t" \
  "v_mfma_f32_16x16x32_bf16 v[20:23], a[116:119], v[152:155], v[20:23]\n\t" \
  TK_QNP \
  "v_mfma_f32_16x16x32_bf16 v[24:27], a[188:191], v[152:155], v[24:27]\n\t" \
  "ds_read_b128 v[180:183], v1 offset:30720\n\t" \
  "v_mfma_f32_16x16x32_bf16 v[28:31], v[228:231], v[152:155], v[28:31]\n\t" \
  "s_waitcnt vmcnt(0)\n\t" \
  "v_cmp_eq_u32 vcc, 0, v4\n\t" \
  TK_QNP \
  "s_waitcnt lgkmcnt(5)\n\t" \
  "v_mfma_f32_16x16x32_bf16 v[16:19], a[48:51], v[160:163], v[16:19]\n\t" \
  "ds_read_b128 v[184:187], v2 offset:28672\n\t" \
  "v_mfma_f32_16x16x32_bf16 v[20:23], a[120:123], v[160:163], v[20:23]\n\t" \
  "s_mov_b64 exec, 1\n\t" \
  "buffer_store_dword v6, off, %[X], s49 sc1\n\t" \
  "s_mov_b64 exec, -1\n\t" \
  "s_mov_b32 s49, 0xf0000000\n\t" \
  TK_PQ0 \
  TK_QNP \
  "v_mfma_f32_16x16x32_bf16 v[24:27], a[192:195], v[160:163], v[24:27]\n\t" \
  "ds_read_b128 v[188:191], v2 offset:30720\n\t" \
  "v_mfma_f32_16x16x32_bf16 v[28:31], v[232:235], v[160:163], v[28:31]\n\t" \
  TK_HAVE \
  "s_cselect_b32 s60, 1, 0\n\t" \
  "v_mov_b32 v4, s60\n\t" \
  TK_QNP \
  "s_waitcnt lgkmcnt(5)\n\t" \
  "v_mfma_f32_16x16x32_bf16 v[16:19], a[52:55], v[168:171], v[16:19]\n\t" \
  "ds_read_b128 v[192:195], v1 offset:32768\n\t" \
  "v_mfma_f32_16x16x32_bf16 v[20:23], a[124:127], v[168:171], v[20:23]\n\t" \
  "ds_write_b32 v5, v4\n\t" \
  TK_QNP \
  "v_mfma_f32_16x16x32_bf16 v[24:27], a[196:199], v[168:171], v[24:27]\n\t" \
  "ds_read_b128 v[196:199], v1 offset:34816\n\t" \
  "v_mfma_f32_16x16x32_bf16 v[28:31], v[236:239], v[168:171], v[28:31]\n\t" \
  TK_QNP \
  "s_waitcnt lgkmcnt(6)\n\t" \
  "v_mfma_f32_16x16x32_bf16 v[16:19], a[56:59], v[176:179], v[16:19]\n\t" \
  "ds_read_b128 v[200:203], v2 offset:32768\n\t" \
  "v_mfma_f32_16x16x32_bf16 v[20:23], a[128:131], v[176:179], v[20:23]\n\t" \
  TK_QNP \
  "v_mfma_f32_16x16x32_bf16 v[24:27], a[200:203], v[176:179], v[24:27]\n\t" \
  "ds_read_b128 v[204:207], v2 offset:34816\n\t" \
  "v_mfma_f32_16x16x32_bf16 v[28:31], v[240:243], v[176:179], v[28:31]\n\t" \
  TK_QNP \
  "s_waitcnt lgkmcnt(6)\n\t" \
  "v_mfma_f32_16x16x32_bf16 v[16:19], a[60:63], v[184:187], v[16:19]\n\t" \
  "v_mfma_f32_16x16x32_bf16 v[20:23], a[132:135], v[184:187], v[20:23]\n\t" \
  TK_QNP \
  "v_mfma_f32_16x16x32_bf16 v[24:27], a[204:207], v[184:187], v[24:27]\n\t" \
  "v_mfma_f32_16x16x32_bf16 v[28:31], v[244:247], v[184:187], v[28:31]\n\t" \
  "s_waitcnt lgkmcnt(0)\n\t" \
  "s_barrier\n\t" \
  TK_QNP \
  "v_mfma_f32_16x16x32_bf16 v[16:19], a[64:67], v[192:195], v[16:19]\n\t" \
  "ds_read_b32 v4, v6 offset:40959\n\t" \
  "v_xor_b32 v1, 0x10000, v1\n\t" \
  "v_xor_b32 v2, 0x10000, v2\n\t" \
  "v_mfma_f32_16x16x32_bf16 v[20:23], a[136:139], v[192:195], v[20:23]\n\t" \
  TK_QNP \
  "v_mfma_f32_16x16x32_bf16 v[24:27], a[208:211], v[192:195], v[24:27]\n\t" \
  "v_mfma_f32_16x16x32_bf16 v[28:31], v[248:251], v[192:195], v[28:31]\n\t" \
  TK_QNP \
  "v_mfma_f32_16x16x32_bf16 v[16:19], a[68:71], v[200:203], v[16:19]\n\t" \
  "v_mfma_f32_16x16x32_bf16 v[20:23], a[140:143], v[200:203], v[20:23]\n\t" \
  "s_waitcnt lgkmcnt(0)\n\t" \
  "v_readfirstlane_b32 s60, v4\n\t" \
  "s_add_u32 s55, s83, s36\n\t" \
  TK_QNP \
  "v_mfma_f32_16x16x32_bf16 v[24:27], a[212:215], v[200:203], v[24:27]\n\t" \
  "s_cmp_eq_u32 s48, 63\n\t" \
  "s_cselect_b32 s56, 0, s60\n\t" \
  "v_mfma_f32_16x16x32_bf16 v[28:31], v[252:255], v[200:203], v[28:31]\n\t" \
  "s_cmp_eq_u32 s56, 0\n\t" \
  "s_cselect_b32 s55, 0xf0000000, s55\n\t" \
  TK_QNP \
  "v_mfma_f32_16x16x32_bf16 v[32:35], a[0:3], v[68:71], 0\n\t" \
  "v_cvt_pk_bf16_f32 v48, v16, v17\n\t" \
  "v_cvt_pk_bf16_f32 v49, v18, v19\n\t" \
  "v_mfma_f32_16x16x32_bf16 v[36:39], a[72:75], v[68:71], 0\n\t" \
  "v_cvt_pk_bf16_f32 v50, v20, v21\n\t" \
  "v_cvt_pk_bf16_f32 v51, v22, v23\n\t" \
  TK_QNP \
  "v_mfma_f32_16x16x32_bf16 v[32:35], a[4:7], v[76:79], v[32:35]\n\t" \
  "v_cvt_pk_bf16_f32 v52, v24, v25\n\t" \
  "v_cvt_pk_bf16_f32 v53, v26, v27\n\t" \
  "v_mfma_f32_16x16x32_bf16 v[36:39], a[76:79], v[76:79], v[36:39]\n\t" \
  "v_cvt_pk_bf16_f32 v54, v28, v29\n\t" \
  "v_cvt_pk_bf16_f32 v55, v30, v31\n\t" \
  TK_QNP \
  "v_mfma_f32_16x16x32_bf16 v[32:35], a[8:11], v[84:87], v[32:35]\n\t" \
  "buffer_store_dwordx4 v[48:51], v0, %[X], s50 offen offset:0 sc1\n\t" \
  "v_mfma_f32_16x16x32_bf16 v[36:39], a[80:83], v[84:87], v[36:39]\n\t" \
  TK_QNP \
  "v_mfma_f32_16x16x32_bf16 v[32:35], a[12:15], v[92:95], v[32:35]\n\t" \
  "s_add_u32 m0, s46, 0\n\t" \
  "s_add_u32 s47, s55, s52\n\t" \
  "buffer_load_dwordx4 v0, %[X], s47 offen sc1 lds\n\t" \
  "v_mfma_f32_16x16x32_bf16 v[36:39], a[84:87], v[92:95], v[36:39]\n\t" \
  TK_QNP \
  "v_mfma_f32_16x16x32_bf16 v[32:35], a[16:19], v[100:103], v[32:35]\n\t" \
  "v_mfma_f32_16x16x32_bf16 v[36:39], a[88:91], v[100:103], v[36:39]\n\t" \
  "s_add_u32 m0, s46, 4096\n\t" \
  "s_add_u32 s47, s55, s53\n\t" \
  "buffer_load_dwordx4 v0, %[X], s47 offen sc1 lds\n\t" \
  TK_QNP \
  "v_mfma_f32_16x16x32_bf16 v[32:35], a[20:23], v[108:111], v[32:35]\n\t" \
  "v_mfma_f32_16x16x32_bf16 v[36:39], a[92:95], v[108:111], v[36:39]\n\t" \
  TK_QNP \
  "v_mfma_f32_16x16x32_bf16 v[32:35], a[24:27], v[116:119], v[32:35]\n\t" \
  "s_add_u32 m0, s46, 8192\n\t" \
  "s_add_u32 s47, s55, s54\n\t" \
  "buffer_load_dwordx4 v0, %[X], s47 offen sc1 lds\n\t" \
  "v_mfma_f32_16x16x32_bf16 v[36:39], a[96:99], v[116:119], v[36:39]\n\t" \
  TK_QNP \
  "v_mfma_f32_16x16x32_bf16 v[32:35], a[28:31], v[124:127], v[32:35]\n\t" \
  "v_mfma_f32_16x16x32_bf16 v[36:39], a[100:103], v[124:127], v[36:39]\n\t" \
  "s_add_u32 m0, s46, 12288\n\t" \
  "s_add_u32 s47, s55, s57\n\t" \
  "buffer_load_dwordx4 v0, %[X], s47 offen sc1 lds\n\t" \
  TK_QNP \
  "v_mfma_f32_16x16x32_bf16 v[32:35], a[32:35], v[132:135], v[32:35]\n\t" \
  "v_mfma_f32_16x16x32_bf16 v[36:39], a[104:107], v[132:135], v[36:39]\n\t" \
  TK_QNP \
  "v_mfma_f32_16x16x32_bf16 v[32:35], a[36:39], v[140:143], v[32:35]\n\t" \
  "s_add_u32 m0, s46, 16384\n\t" \
  "s_add_u32 s47, s55, s58\n\t" \
  "buffer_load_dwordx4 v0, %[X], s47 offen sc1 lds\n\t" \
  "v_mfma_f32_16x16x32_bf16 v[36:39], a[108:111], v[140:143], v[36:39]\n\t" \
  TK_QNP \
  "v_mfma_f32_16x16x32_bf16 v[32:35], a[40:43], v[148:151], v[32:35]\n\t" \
  "v_mfma_f32_16x16x32_bf16 v[36:39], a[112:115], v[148:151], v[36:39]\n\t" \
  TK_QNP \
  "v_mfma_f32_16x16x32_bf16 v[32:35], a[44:47], v[156:159], v[32:35]\n\t" \
  "buffer_store_dwordx4 v[52:55], v0, %[X], s50 offen offset:1024 sc1\n\t" \
  "v_mfma_f32_16x16x32_bf16 v[36:39], a[116:119], v[156:159], v[36:39]\n\t" \
  TK_QNP \
  "v_mfma_f32_16x16x32_bf16 v[32:35], a[48:51], v[164:167], v[32:35]\n\t" \
  "v_mfma_f32_16x16x32_bf16 v[36:39], a[120:123], v[164:167], v[36:39]\n\t" \
  "s_add_u32 m0, s46, 20480\n\t" \
  "s_add_u32 s47, s55, s59\n\t" \
  "buffer_load_dwordx4 v0, %[X], s47 offen sc1 lds\n\t" \
  TK_QNP \
  "v_mfma_f32_16x16x32_bf16 v[32:35], a[52:55], v[172:175], v[32:35]\n\t" \
  "v_mfma_f32_16x16x32_bf16 v[36:39], a[124:127], v[172:175], v[36:39]\n\t" \
  TK_QNP \
  "v_mfma_f32_16x16x32_bf16 v[32:35], a[56:59], v[180:183], v[32:35]\n\t" \
  "s_add_u32 m0, s46, 24576\n\t" \
  "s_add_u32 s47, s55, s64\n\t" \
  "buffer_load_dwordx4 v0, %[X], s47 offen sc1 lds\n\t" \
  "v_mfma_f32_16x16x32_bf16 v[36:39], a[128:131], v[180:183], v[36:39]\n\t" \
  TK_QNP \
  "v_mfma_f32_16x16x32_bf16 v[32:35], a[60:63], v[188:191], v[32:35]\n\t" \
  "v_mfma_f32_16x16x32_bf16 v[36:39], a[132:135], v[188:191], v[36:39]\n\t" \
  "s_add_u32 m0, s46, 28672\n\t" \
  "s_add_u32 s47, s55, s65\n\t" \
  "buffer_load_dwordx4 v0, %[X], s47 offen sc1 lds\n\t" \
  TK_QNP \
  "v_mfma_f32_16x16x32_bf16 v[32:35], a[64:67], v[196:199], v[32:35]\n\t" \
  "v_mfma_f32_16x16x32_bf16 v[36:39], a[136:139], v[196:199], v[36:39]\n\t" \
  TK_QNP \
  "v_mfma_f32_16x16x32_bf16 v[32:35], a[68:71], v[204:207], v[32:35]\n\t" \
  "s_add_u32 m0, s46, 32768\n\t" \
  "s_add_u32 s47, s55, s66\n\t" \
  "buffer_load_dwordx4 v0, %[X], s47 offen sc1 lds\n\t" \
  "v_mfma_f32_16x16x32_bf16 v[36:39], a[140:143], v[204:207], v[36:39]\n\t" \
  TK_QNP \
  "v_mfma_f32_16x16x32_bf16 v[40:43], a[144:147], v[68:71], 0\n\t" \
  "v_mfma_f32_16x16x32_bf16 v[44:47], a[216:219], v[68:71], 0\n\t" \
  TK_QNP \
  "v_mfma_f32_16x16x32_bf16 v[40:43], a[148:151], v[76:79], v[40:43]\n\t" \
  "v_cvt_pk_bf16_f32 v56, v32, v33\n\t" \
  "v_cvt_pk_bf16_f32 v57, v34, v35\n\t" \
  "v_mfma_f32_16x16x32_bf16 v[44:47], a[220:223], v[76:79], v[44:47]\n\t" \
  "v_cvt_pk_bf16_f32 v58, v36, v37\n\t" \
  "v_cvt_pk_bf16_f32 v59, v38, v39\n\t" \
  TK_QNP \
  "v_mfma_f32_16x16x32_bf16 v[40:43], a[152:155], v[84:87], v[40:43]\n\t" \
  "v_mfma_f32_16x16x32_bf16 v[44:47], a[224:227], v[84:87], v[44:47]\n\t" \
  TK_QNP \
  "v_mfma_f32_16x16x32_bf16 v[40:43], a[156:159], v[92:95], v[40:43]\n\t" \
  "buffer_store_dwordx4 v[56:59], v0, %[X], s51 offen offset:0 sc1\n\t" \
  "v_mfma_f32_16x16x32_bf16 v[44:47], a[228:231], v[92:95], v[44:47]\n\t" \
  TK_QNP \
  "v_mfma_f32_16x16x32_bf16 v[40:43], a[160:163], v[100:103], v[40:43]\n\t" \
  "v_mfma_f32_16x16x32_bf16 v[44:47], a[232:235], v[100:103], v[44:47]\n\t" \
  TK_QNP \
  "v_mfma_f32_16x16x32_bf16 v[40:43], a[164:167], v[108:111], v[40:43]\n\t" \
  "v_mfma_f32_16x16x32_bf16 v[44:47], a[236:239], v[108:111], v[44:47]\n\t" \
  TK_QNP \
  "v_mfma_f32_16x16x32_bf16 v[40:43], a[168:171], v[116:119], v[40:43]\n\t" \
  "v_mfma_f32_16x16x32_bf16 v[44:47], a[240:243], v[116:119], v[44:47]\n\t" \
  TK_QNP \
  "v_mfma_f32_16x16x32_bf16 v[40:43], a[172:175], v[124:127], v[40:43]\n\t" \
  "v_mfma_f32_16x16x32_bf16 v[44:47], a[244:247], v[124:127], v[44:47]\n\t" \
  TK_QNP \
  "v_mfma_f32_16x16x32_bf16 v[40:43], a[176:179], v[132:135], v[40:43]\n\t" \
  "v_mfma_f32_16x16x32_bf16 v[44:47], a[248:251], v[132:135], v[44:47]\n\t" \
  TK_QNP \
  "v_mfma_f32_16x16x32_bf16 v[40:43], a[180:183], v[140:143], v[40:43]\n\t" \
  "v_mfma_f32_16x16x32_bf16 v[44:47], a[252:255], v[140:143], v[44:47]\n\t" \
  TK_QNP \
  "v_mfma_f32_16x16x32_bf16 v[40:43], a[184:187], v[148:151], v[40:43]\n\t" \
  "v_mfma_f32_16x16x32_bf16 v[44:47], v[224:227], v[148:151], v[44:47]\n\t" \
  TK_QNP \
  "v_mfma_f32_16x16x32_bf16 v[40:43], a[188:191], v[156:159], v[40:43]\n\t" \
  "v_mfma_f32_16x16x32_bf16 v[44:47], v[228:231], v[156:159], v[44:47]\n\t" \
  TK_QNP \
  "v_mfma_f32_16x16x32_bf16 v[40:43], a[192:195], v[164:167], v[40:43]\n\t" \
  "v_mfma_f32_16x16x32_bf16 v[44:47], v[232:235], v[164:167], v[44:47]\n\t" \
  TK_QNP \
  "v_mfma_f32_16x16x32_bf16 v[40:43], a[196:199], v[172:175], v[40:43]\n\t" \
  "v_mfma_f32_16x16x32_bf16 v[44:47], v[236:239], v[172:175], v[44:47]\n\t" \
  TK_QNP \
  "v_mfma_f32_16x16x32_bf16 v[40:43], a[200:203], v[180:183], v[40:43]\n\t" \
  "v_mfma_f32_16x16x32_bf16 v[44:47], v[240:243], v[180:183], v[44:47]\n\t" \
  TK_QNP \
  "v_mfma_f32_16x16x32_bf16 v[40:43], a[204:207], v[188:191], v[40:43]\n\t" \
  "v_mfma_f32_16x16x32_bf16 v[44:47], v[244:247], v[188:191], v[44:47]\n\t" \
  TK_QNP \
  "v_mfma_f32_16x16x32_bf16 v[40:43], a[208:211], v[196:199], v[40:43]\n\t" \
  "v_mfma_f32_16x16x32_bf16 v[44:47], v[248:251], v[196:199], v[44:47]\n\t" \
  TK_QNP \
  "v_mfma_f32_16x16x32_bf16 v[40:43], a[212:215], v[204:207], v[40:43]\n\t" \
  "v_mfma_f32_16x16x32_bf16 v[44:47], v[252:255], v[204:207], v[44:47]\n\t" \
  TK_QNP \
  "s_nop 4\n\t" \
  "v_cvt_pk_bf16_f32 v60, v40, v41\n\t" \
  "v_cvt_pk_bf16_f32 v61, v42, v43\n\t" \
  "s_nop 1\n\t" \
  "v_cvt_pk_bf16_f32 v62, v44, v45\n\t" \
  "v_cvt_pk_bf16_f32 v63, v46, v47\n\t" \
  "buffer_store_dwordx4 v[60:63], v0, %[X], s51 offen offset:1024 sc1\n\t" \
  TK_PQ2 \
  "s_add_u32 s49, s82, s38\n\t" \
  "s_cmp_eq_u32 s48, 63\n\t" \
  "s_cbranch_scc1 LEND_%=\n\t" \
  "s_add_u32 s48, s48, 1\n\t" \
  "s_cmp_eq_u32 s56, 0\n\t" \
  "s_cbranch_scc1 LCOLD_%=\n\t" \
  "s_waitcnt vmcnt(2)\n\t" \
  TK_PACE \
  "s_branch LTOP_%=\n\t" \
  "LPACE_%=:\n\t" \
  "s_waitcnt vmcnt(0)\n\t" \
  "s_mov_b64 exec, 1\n\t" \
  "buffer_store_dword v6, off, %[X], s49 sc1\n\t" \
  "s_mov_b64 exec, -1\n\t" \
  "s_mov_b32 s49, 0xf0000000\n\t" \
  "s_cmp_lg_u32 s40, 0\n\t" \
  "s_cbranch_scc1 LTOP_%=\n\t" \
  "s_mov_b32 s61, 0\n\t" \
  "LPP_%=:\n\t" \
  "buffer_load_dword v4, v7, %[X], 0 offen sc1\n\t" \
  "s_waitcnt vmcnt(0)\n\t" \
  "v_cmp_eq_u32 vcc, 0, v4\n\t" \
  "s_cbranch_vccz LPP_%=x\n\t" \
  "s_add_u32 s61, s61, 1\n\t" \
  "s_sleep " TK_PSH "\n\t" \
  "s_cmp_gt_u32 s61, s84\n\t" \
  "s_cbranch_scc1 LPP_%=g\n\t" \
  "s_and_b32 s60, s61, 1023\n\t" \
  "s_cmp_lg_u32 s60, 0\n\t" \
  "s_cbranch_scc1 LPP_%=\n\t" \
  "buffer_load_dword v4, off, %[X], s92 sc1\n\t" \
  "s_waitcnt vmcnt(0)\n\t" \
  "v_readfirstlane_b32 s60, v4\n\t" \
  "s_nop 0\n\t" \
  "s_cmp_eq_u32 s60, 0\n\t" \
  "s_cbranch_scc1 LPP_%=\n\t" \
  "LPP_%=g:\n\t" \
  "s_mov_b32 s84, 0\n\t" \
  "s_or_b32 s60, s48, 1879048192\n\t" \
  "v_mov_b32 v4, s60\n\t" \
  "s_mov_b64 exec, 1\n\t" \
  "buffer_store_dword v4, off, %[X], s92 sc1\n\t" \
  "s_mov_b64 exec, -1\n\t" \
  "LPP_%=x:\n\t" \
  TK_QDL2 \
  "s_branch LTOP_%=\n\t" \
  "LEND_%=:\n\t" \
  "s_waitcnt vmcnt(0)\n\t" \
  "s_mov_b64 exec, 1\n\t" \
  "buffer_store_dword v6, off, %[X], s49 sc1\n\t" \
  "s_mov_b64 exec, -1\n\t" \
  "s_mov_b32 s49, 0xf0000000\n\t" \
  TK_PQ0 \
  ""
#define QV_ASM \
  "s_lshl_b32 s61, s44, 11\n\t" \
  "buffer_load_dwordx4 a[0:3], v0, %[W], s61 offen offset:0\n\t" \
  "buffer_load_dwordx4 a[4:7], v0, %[W], s61 offen offset:1024\n\t" \
  "s_add_u32 s61, s61, 18432\n\t" \
  "buffer_load_dwordx4 a[72:75], v0, %[W], s61 offen offset:0\n\t" \
  "buffer_load_dwordx4 a[76:79], v0, %[W], s61 offen offset:1024\n\t" \
  "s_add_u32 s61, s61, 18432\n\t" \
  "buffer_load_dwordx4 a[144:147], v0, %[W], s61 offen offset:0\n\t" \
  "buffer_load_dwordx4 a[148:151], v0, %[W], s61 offen offset:1024\n\t" \
  "s_add_u32 s61, s61, 18432\n\t" \
  "buffer_load_dwordx4 a[216:219], v0, %[W], s61 offen offset:0\n\t" \
  "buffer_load_dwordx4 a[220:223], v0, %[W], s61 offen offset:1024\n\t" \
  "s_lshr_b32 s60, s44, 1\n\t" \
  "s_lshl_b32 s52, s60, 14\n\t" \
  "s_and_b32 s60, s44, 1\n\t" \
  "s_lshl_b32 s60, s60, 12\n\t" \
  "s_add_u32 s52, s52, s60\n\t" \
  "s_add_u32 s44, s44, 1\n\t" \
  "s_cmp_eq_u32 s44, 9\n\t" \
  "s_cselect_b32 s44, 0, s44\n\t" \
  "s_lshl_b32 s61, s44, 11\n\t" \
  "buffer_load_dwordx4 a[8:11], v0, %[W], s61 offen offset:0\n\t" \
  "buffer_load_dwordx4 a[12:15], v0, %[W], s61 offen offset:1024\n\t" \
  "s_add_u32 s61, s61, 18432\n\t" \
  "buffer_load_dwordx4 a[80:83], v0, %[W], s61 offen offset:0\n\t" \
  "buffer_load_dwordx4 a[84:87], v0, %[W], s61 offen offset:1024\n\t" \
  "s_add_u32 s61, s61, 18432\n\t" \
  "buffer_load_dwordx4 a[152:155], v0, %[W], s61 offen offset:0\n\t" \
  "buffer_load_dwordx4 a[156:159], v0, %[W], s61 offen offset:1024\n\t" \
  "s_add_u32 s61, s61, 18432\n\t" \
  "buffer_load_dwordx4 a[224:227], v0, %[W], s61 offen offset:0\n\t" \
  "buffer_load_dwordx4 a[228:231], v0, %[W], s61 offen offset:1024\n\t" \
  "s_lshr_b32 s60, s44, 1\n\t" \
  "s_lshl_b32 s53, s60, 14\n\t" \
  "s_and_b32 s60, s44, 1\n\t" \
  "s_lshl_b32 s60, s60, 12\n\t" \
  "s_add_u32 s53, s53, s60\n\t" \
  "s_add_u32 s44, s44, 1\n\t" \
  "s_cmp_eq_u32 s44, 9\n\t" \
  "s_cselect_b32 s44, 0, s44\n\t" \
  "s_lshl_b32 s61, s44, 11\n\t" \
  "buffer_load_dwordx4 a[16:19], v0, %[W], s61 offen offset:0\n\t" \
  "buffer_load_dwordx4 a[20:23], v0, %[W], s61 offen offset:1024\n\t" \
  "s_add_u32 s61, s61, 18432\n\t" \
  "buffer_load_dwordx4 a[88:91], v0, %[W], s61 offen offset:0\n\t" \
  "buffer_load_dwordx4 a[92:95], v0, %[W], s61 offen offset:1024\n\t" \
  "s_add_u32 s61, s61, 18432\n\t" \
  "buffer_load_dwordx4 a[160:163], v0, %[W], s61 offen offset:0\n\t" \
  "buffer_load_dwordx4 a[164:167], v0, %[W], s61 offen offset:1024\n\t" \
  "s_add_u32 s61, s61, 18432\n\t" \
  "buffer_load_dwordx4 a[232:235], v0, %[W], s61 offen offset:0\n\t" \
  "buffer_load_dwordx4 a[236:239], v0, %[W], s61 offen offset:1024\n\t" \
  "s_lshr_b32 s60, s44, 1\n\t" \
  "s_lshl_b32 s54, s60, 14\n\t" \
  "s_and_b32 s60, s44, 1\n\t" \
  "s_lshl_b32 s60, s60, 12\n\t" \
  "s_add_u32 s54, s54, s60\n\t" \
  "s_add_u32 s44, s44, 1\n\t" \
  "s_cmp_eq_u32 s44, 9\n\t" \
  "s_cselect_b32 s44, 0, s44\n\t" \
  "s_lshl_b32 s61, s44, 11\n\t" \
  "buffer_load_dwordx4 a[24:27], v0, %[W], s61 offen offset:0\n\t" \
  "buffer_load_dwordx4 a[28:31], v0, %[W], s61 offen offset:1024\n\t" \
  "s_add_u32 s61, s61, 18432\n\t" \
  "buffer_load_dwordx4 a[96:99], v0, %[W], s61 offen offset:0\n\t" \
  "buffer_load_dwordx4 a[100:103], v0, %[W], s61 offen offset:1024\n\t" \
  "s_add_u32 s61, s61, 18432\n\t" \
  "buffer_load_dwordx4 a[168:171], v0, %[W], s61 offen offset:0\n\t" \
  "buffer_load_dwordx4 a[172:175], v0, %[W], s61 offen offset:1024\n\t" \
  "s_add_u32 s61, s61, 18432\n\t" \
  "buffer_load_dwordx4 a[240:243], v0, %[W], s61 offen offset:0\n\t" \
  "buffer_load_dwordx4 a[244:247], v0, %[W], s61 offen offset:1024\n\t" \
  "s_lshr_b32 s60, s44, 1\n\t" \
  "s_lshl_b32 s57, s60, 14\n\t" \
  "s_and_b32 s60, s44, 1\n\t" \
  "s_lshl_b32 s60, s60, 12\n\t" \
  "s_add_u32 s57, s57, s60\n\t" \
  "s_add_u32 s44, s44, 1\n\t" \
  "s_cmp_eq_u32 s44, 9\n\t" \
  "s_cselect_b32 s44, 0, s44\n\t" \
  "s_lshl_b32 s61, s44, 11\n\t" \
  "buffer_load_dwordx4 a[32:35], v0, %[W], s61 offen offset:0\n\t" \
  "buffer_load_dwordx4 a[36:39], v0, %[W], s61 offen offset:1024\n\t" \
  "s_add_u32 s61, s61, 18432\n\t" \
  "buffer_load_dwordx4 a[104:107], v0, %[W], s61 offen offset:0\n\t" \
  "buffer_load_dwordx4 a[108:111], v0, %[W], s61 offen offset:1024\n\t" \
  "s_add_u32 s61, s61, 18432\n\t" \
  "buffer_load_dwordx4 a[176:179], v0, %[W], s61 offen offset:0\n\t" \
  "buffer_load_dwordx4 a[180:183], v0, %[W], s61 offen offset:1024\n\t" \
  "s_add_u32 s61, s61, 18432\n\t" \
  "buffer_load_dwordx4 a[248:251], v0, %[W], s61 offen offset:0\n\t" \
  "buffer_load_dwordx4 a[252:255], v0, %[W], s61 offen offset:1024\n\t" \
  "s_lshr_b32 s60, s44, 1\n\t" \
  "s_lshl_b32 s58, s60, 14\n\t" \
  "s_and_b32 s60, s44, 1\n\t" \
  "s_lshl_b32 s60, s60, 12\n\t" \
  "s_add_u32 s58, s58, s60\n\t" \
  "s_add_u32 s44, s44, 1\n\t" \
  "s_cmp_eq_u32 s44, 9\n\t" \
  "s_cselect_b32 s44, 0, s44\n\t" \
  "s_lshl_b32 s61, s44, 11\n\t" \
  "buffer_load_dwordx4 a[40:43], v0, %[W], s61 offen offset:0\n\t" \
  "buffer_load_dwordx4 a[44:47], v0, %[W], s61 offen offset:1024\n\t" \
  "s_add_u32 s61, s61, 18432\n\t" \
  "buffer_load_dwordx4 a[112:115], v0, %[W], s61 offen offset:0\n\t" \
  "buffer_load_dwordx4 a[116:119], v0, %[W], s61 offen offset:1024\n\t" \
  "s_add_u32 s61, s61, 18432\n\t" \
  "buffer_load_dwordx4 a[184:187], v0, %[W], s61 offen offset:0\n\t" \
  "buffer_load_dwordx4 a[188:191], v0, %[W], s61 offen offset:1024\n\t" \
  "s_add_u32 s61, s61, 18432\n\t" \
  "buffer_load_dwordx4 v[224:227], v0, %[W], s61 offen offset:0\n\t" \
  "buffer_load_dwordx4 v[228:231], v0, %[W], s61 offen offset:1024\n\t" \
  "s_lshr_b32 s60, s44, 1\n\t" \
  "s_lshl_b32 s59, s60, 14\n\t" \
  "s_and_b32 s60, s44, 1\n\t" \
  "s_lshl_b32 s60, s60, 12\n\t" \
  "s_add_u32 s59, s59, s60\n\t" \
  "s_add_u32 s44, s44, 1\n\t" \
  "s_cmp_eq_u32 s44, 9\n\t" \
  "s_cselect_b32 s44, 0, s44\n\t" \
  "s_lshl_b32 s61, s44, 11\n\t" \
  "buffer_load_dwordx4 a[48:51], v0, %[W], s61 offen offset:0\n\t" \
  "buffer_load_dwordx4 a[52:55], v0, %[W], s61 offen offset:1024\n\t" \
  "s_add_u32 s61, s61, 18432\n\t" \
  "buffer_load_dwordx4 a[120:123], v0, %[W], s61 offen offset:0\n\t" \
  "buffer_load_dwordx4 a[124:127], v0, %[W], s61 offen offset:1024\n\t" \
  "s_add_u32 s61, s61, 18432\n\t" \
  "buffer_load_dwordx4 a[192:195], v0, %[W], s61 offen offset:0\n\t" \
  "buffer_load_dwordx4 a[196:199], v0, %[W], s61 offen offset:1024\n\t" \
  "s_add_u32 s61, s61, 18432\n\t" \
  "buffer_load_dwordx4 v[232:235], v0, %[W], s61 offen offset:0\n\t" \
  "buffer_load_dwordx4 v[236:239], v0, %[W], s61 offen offset:1024\n\t" \
  "s_lshr_b32 s60, s44, 1\n\t" \
  "s_lshl_b32 s64, s60, 14\n\t" \
  "s_and_b32 s60, s44, 1\n\t" \
  "s_lshl_b32 s60, s60, 12\n\t" \
  "s_add_u32 s64, s64, s60\n\t" \
  "s_add_u32 s44, s44, 1\n\t" \
  "s_cmp_eq_u32 s44, 9\n\t" \
  "s_cselect_b32 s44, 0, s44\n\t" \
  "s_waitcnt vmcnt(40)\n\t" \
  "s_lshl_b32 s61, s44, 11\n\t" \
  "buffer_load_dwordx4 a[56:59], v0, %[W], s61 offen offset:0\n\t" \
  "buffer_load_dwordx4 a[60:63], v0, %[W], s61 offen offset:1024\n\t" \
  "s_add_u32 s61, s61, 18432\n\t" \
  "buffer_load_dwordx4 a[128:131], v0, %[W], s61 offen offset:0\n\t" \
  "buffer_load_dwordx4 a[132:135], v0, %[W], s61 offen offset:1024\n\t" \
  "s_add_u32 s61, s61, 18432\n\t" \
  "buffer_load_dwordx4 a[200:203], v0, %[W], s61 offen offset:0\n\t" \
  "buffer_load_dwordx4 a[204:207], v0, %[W], s61 offen offset:1024\n\t" \
  "s_add_u32 s61, s61, 18432\n\t" \
  "buffer_load_dwordx4 v[240:243], v0, %[W], s61 offen offset:0\n\t" \
  "buffer_load_dwordx4 v[244:247], v0, %[W], s61 offen offset:1024\n\t" \
  "s_lshr_b32 s60, s44, 1\n\t" \
  "s_lshl_b32 s65, s60, 14\n\t" \
  "s_and_b32 s60, s44, 1\n\t" \
  "s_lshl_b32 s60, s60, 12\n\t" \
  "s_add_u32 s65, s65, s60\n\t" \
  "s_add_u32 s44, s44, 1\n\t" \
  "s_cmp_eq_u32 s44, 9\n\t" \
  "s_cselect_b32 s44, 0, s44\n\t" \
  "s_lshl_b32 s61, s44, 11\n\t" \
  "buffer_load_dwordx4 a[64:67], v0, %[W], s61 offen offset:0\n\t" \
  "buffer_load_dwordx4 a[68:71], v0, %[W], s61 offen offset:1024\n\t" \
  "s_add_u32 s61, s61, 18432\n\t" \
  "buffer_load_dwordx4 a[136:139], v0, %[W], s61 offen offset:0\n\t" \
  "buffer_load_dwordx4 a[140:143], v0, %[W], s61 offen offset:1024\n\t" \
  "s_add_u32 s61, s61, 18432\n\t" \
  "buffer_load_dwordx4 a[208:211], v0, %[W], s61 offen offset:0\n\t" \
  "buffer_load_dwordx4 a[212:215], v0, %[W], s61 offen offset:1024\n\t" \
  "s_add_u32 s61, s61, 18432\n\t" \
  "buffer_load_dwordx4 v[248:251], v0, %[W], s61 offen offset:0\n\t" \
  "buffer_load_dwordx4 v[252:255], v0, %[W], s61 offen offset:1024\n\t" \
  "s_lshr_b32 s60, s44, 1\n\t" \
  "s_lshl_b32 s66, s60, 14\n\t" \
  "s_and_b32 s60, s44, 1\n\t" \
  "s_lshl_b32 s60, s60, 12\n\t" \
  "s_add_u32 s66, s66, s60\n\t" \
  "s_add_u32 s44, s44, 1\n\t" \
  "s_cmp_eq_u32 s44, 9\n\t" \
  "s_cselect_b32 s44, 0, s44\n\t" \
  "s_mov_b32 s84, " TK_CAP "\n\t" \
  "s_mov_b32 s48, 0\n\t" \
  "s_mov_b32 s49, 0xf0000000\n\t" \
  "s_mov_b32 s46, s41\n\t" \
  "s_mov_b32 s56, 0\n\t" \
  "s_add_u32 s83, s48, 0\n\t" \
  "s_lshr_b64 s[60:61], s[90:91], s83\n\t" \
  "s_and_b32 s60, s60, 1\n\t" \
  "s_lshl_b32 s60, s60, 13\n\t" \
  "s_add_u32 s83, s83, " TK_SG0 "\n\t" \
  "s_lshl_b32 s83, s83, 21\n\t" \
  "s_or_b32 s83, s83, s60\n\t" \
  "s_add_u32 s63, s48, s42\n\t" \
  "s_lshr_b64 s[60:61], s[90:91], s63\n\t" \
  "s_and_b32 s60, s60, 1\n\t" \
  "s_lshl_b32 s60, s60, 13\n\t" \
  "s_add_u32 s63, s63, " TK_SG0 "\n\t" \
  "s_lshl_b32 s63, s63, 21\n\t" \
  "s_or_b32 s63, s63, s60\n\t" \
  "s_add_u32 s62, s83, s39\n\t" \
  "s_add_u32 s63, s63, s43\n\t" \
  "s_add_i32 s60, s48, s42\n\t" \
  "s_cmp_lt_i32 s60, 0\n\t" \
  "s_cselect_b32 s63, s62, s63\n\t" \
  "v_add_u32 v7, s62, v3\n\t" \
  "s_mov_b32 exec_lo, 0\n\t" \
  "v_mov_b32 v7, s63\n\t" \
  "s_mov_b32 exec_lo, -1\n\t" \
  "LCOLD_%=:\n\t" \
  "s_waitcnt vmcnt(0)\n\t" \
  "s_mov_b64 exec, 1\n\t" \
  "buffer_store_dword v6, off, %[X], s49 sc1\n\t" \
  "s_mov_b64 exec, -1\n\t" \
  "s_mov_b32 s49, 0xf0000000\n\t" \
  "s_add_u32 s55, s83, s36\n\t" \
  "s_cmp_lg_u32 s40, 0\n\t" \
  "s_cbranch_scc1 LPH_%=x\n\t" \
  "s_mov_b32 s61, 0\n\t" \
  "LPH_%=:\n\t" \
  "buffer_load_dword v4, v7, %[X], 0 offen sc1\n\t" \
  "s_waitcnt vmcnt(0)\n\t" \
  "v_cmp_eq_u32 vcc, 0, v4\n\t" \
  "s_cbranch_vccz LPH_%=x\n\t" \
  "s_add_u32 s61, s61, 1\n\t" \
  "s_sleep " TK_PSH "\n\t" \
  "s_cmp_gt_u32 s61, s84\n\t" \
  "s_cbranch_scc1 LPH_%=g\n\t" \
  "s_and_b32 s60, s61, 1023\n\t" \
  "s_cmp_lg_u32 s60, 0\n\t" \
  "s_cbranch_scc1 LPH_%=\n\t" \
  "buffer_load_dword v4, off, %[X], s92 sc1\n\t" \
  "s_waitcnt vmcnt(0)\n\t" \
  "v_readfirstlane_b32 s60, v4\n\t" \
  "s_nop 0\n\t" \
  "s_cmp_eq_u32 s60, 0\n\t" \
  "s_cbranch_scc1 LPH_%=\n\t" \
  "LPH_%=g:\n\t" \
  "s_mov_b32 s84, 0\n\t" \
  "s_or_b32 s60, s48, 1610612736\n\t" \
  "v_mov_b32 v4, s60\n\t" \
  "s_mov_b64 exec, 1\n\t" \
  "buffer_store_dword v4, off, %[X], s92 sc1\n\t" \
  "s_mov_b64 exec, -1\n\t" \
  "LPH_%=x:\n\t" \
  TK_QDL \
  "s_barrier\n\t" \
  "s_add_u32 m0, s46, 0\n\t" \
  "s_add_u32 s47, s55, s52\n\t" \
  "buffer_load_dwordx4 v0, %[X], s47 offen sc1 lds\n\t" \
  "s_add_u32 m0, s46, 4096\n\t" \
  "s_add_u32 s47, s55, s53\n\t" \
  "buffer_load_dwordx4 v0, %[X], s47 offen sc1 lds\n\t" \
  "s_add_u32 m0, s46, 8192\n\t" \
  "s_add_u32 s47, s55, s54\n\t" \
  "buffer_load_dwordx4 v0, %[X], s47 offen sc1 lds\n\t" \
  "s_add_u32 m0, s46, 12288\n\t" \
  "s_add_u32 s47, s55, s57\n\t" \
  "buffer_load_dwordx4 v0, %[X], s47 offen sc1 lds\n\t" \
  "s_add_u32 m0, s46, 16384\n\t" \
  "s_add_u32 s47, s55, s58\n\t" \
  "buffer_load_dwordx4 v0, %[X], s47 offen sc1 lds\n\t" \
  "s_add_u32 m0, s46, 20480\n\t" \
  "s_add_u32 s47, s55, s59\n\t" \
  "buffer_load_dwordx4 v0, %[X], s47 offen sc1 lds\n\t" \
  "s_add_u32 m0, s46, 24576\n\t" \
  "s_add_u32 s47, s55, s64\n\t" \
  "buffer_load_dwordx4 v0, %[X], s47 offen sc1 lds\n\t" \
  "s_add_u32 m0, s46, 28672\n\t" \
  "s_add_u32 s47, s55, s65\n\t" \
  "buffer_load_dwordx4 v0, %[X], s47 offen sc1 lds\n\t" \
  "s_add_u32 m0, s46, 32768\n\t" \
  "s_add_u32 s47, s55, s66\n\t" \
  "buffer_load_dwordx4 v0, %[X], s47 offen sc1 lds\n\t" \
  "s_waitcnt vmcnt(5)\n\t" \
  "LTOP_%=:\n\t" \
  "s_barrier\n\t" \
  TK_PQ1 \
  "ds_read_b128 v[64:67], v1 offset:0\n\t" \
  "ds_read_b128 v[68:71], v1 offset:2048\n\t" \
  "ds_read_b128 v[72:75], v2 offset:0\n\t" \
  "ds_read_b128 v[76:79], v2 offset:2048\n\t" \
  "ds_read_b128 v[80:83], v1 offset:4096\n\t" \
  "ds_read_b128 v[84:87], v1 offset:6144\n\t" \
  "s_mov_b32 s82, s83\n\t" \
  "s_xor_b32 s46, s46, 0x10000\n\t" \
  "s_waitcnt lgkmcnt(5)\n\t" \
  "v_mfma_f32_16x16x32_bf16 v[16:19], v[64:67], a[0:3], 0\n\t" \
  "ds_read_b128 v[88:91], v2 offset:4096\n\t" \
  "s_waitcnt lgkmcnt(5)\n\t" \
  "v_mfma_f32_16x16x32_bf16 v[20:23], v[68:71], a[0:3], 0\n\t" \
  "s_add_u32 s83, s48, 1\n\t" \
  "s_lshr_b64 s[60:61], s[90:91], s83\n\t" \
  "s_and_b32 s60, s60, 1\n\t" \
  TK_QNP \
  "v_mfma_f32_16x16x32_bf16 v[24:27], v[64:67], a[72:75], 0\n\t" \
  "ds_read_b128 v[92:95], v2 offset:6144\n\t" \
  "v_mfma_f32_16x16x32_bf16 v[28:31], v[68:71], a[72:75], 0\n\t" \
  "s_lshl_b32 s60, s60, 13\n\t" \
  "s_add_u32 s83, s83, " TK_SG0 "\n\t" \
  "s_lshl_b32 s83, s83, 21\n\t" \
  TK_QNP \
  "s_waitcnt lgkmcnt(5)\n\t" \
  "v_mfma_f32_16x16x32_bf16 v[16:19], v[72:75], a[4:7], v[16:19]\n\t" \
  "ds_read_b128 v[96:99], v1 offset:8192\n\t" \
  "s_waitcnt lgkmcnt(5)\n\t" \
  "v_mfma_f32_16x16x32_bf16 v[20:23], v[76:79], a[4:7], v[20:23]\n\t" \
  "s_or_b32 s83, s83, s60\n\t" \
  "s_add_u32 s63, s48, s42\n\t" \
  "s_lshr_b64 s[60:61], s[90:91], s63\n\t" \
  TK_QNP \
  "v_mfma_f32_16x16x32_bf16 v[24:27], v[72:75], a[76:79], v[24:27]\n\t" \
  "ds_read_b128 v[100:103], v1 offset:10240\n\t" \
  "v_mfma_f32_16x16x32_bf16 v[28:31], v[76:79], a[76:79], v[28:31]\n\t" \
  "s_and_b32 s60, s60, 1\n\t" \
  "s_lshl_b32 s60, s60, 13\n\t" \
  "s_add_u32 s63, s63, " TK_SG0 "\n\t" \
  TK_QNP \
  "s_waitcnt lgkmcnt(5)\n\t" \
  "v_mfma_f32_16x16x32_bf16 v[16:19], v[80:83], a[8:11], v[16:19]\n\t" \
  "ds_read_b128 v[104:107], v2 offset:8192\n\t" \
  "s_waitcnt lgkmcnt(5)\n\t" \
  "v_mfma_f32_16x16x32_bf16 v[20:23], v[84:87], a[8:11], v[20:23]\n\t" \
  "s_lshl_b32 s63, s63, 21\n\t" \
  "s_or_b32 s63, s63, s60\n\t" \
  "s_add_u32 s62, s83, s39\n\t" \
  TK_QNP \
  "v_mfma_f32_16x16x32_bf16 v[24:27], v[80:83], a[80:83], v[24:27]\n\t" \
  "ds_read_b128 v[108:111], v2 offset:10240\n\t" \
  "v_mfma_f32_16x16x32_bf16 v[28:31], v[84:87], a[80:83], v[28:31]\n\t" \
  "s_add_u32 s63, s63, s43\n\t" \
  "s_add_i32 s60, s48, s42\n\t" \
  "s_cmp_lt_i32 s60, 0\n\t" \
  TK_QNP \
  "s_waitcnt lgkmcnt(5)\n\t" \
  "v_mfma_f32_16x16x32_bf16 v[16:19], v[88:91], a[12:15], v[16:19]\n\t" \
  "ds_read_b128 v[112:115], v1 offset:12288\n\t" \
  "s_waitcnt lgkmcnt(5)\n\t" \
  "v_mfma_f32_16x16x32_bf16 v[20:23], v[92:95], a[12:15], v[20:23]\n\t" \
  "s_cselect_b32 s63, s62, s63\n\t" \
  TK_QNP \
  "v_mfma_f32_16x16x32_bf16 v[24:27], v[88:91], a[84:87], v[24:27]\n\t" \
  "ds_read_b128 v[116:119], v1 offset:14336\n\t" \
  "v_mfma_f32_16x16x32_bf16 v[28:31], v[92:95], a[84:87], v[28:31]\n\t" \
  "v_add_u32 v7, s62, v3\n\t" \
  "s_mov_b32 exec_lo, 0\n\t" \
  "v_mov_b32 v7, s63\n\t" \
  "s_mov_b32 exec_lo, -1\n\t" \
  TK_QNP \
  "s_waitcnt lgkmcnt(5)\n\t" \
  "v_mfma_f32_16x16x32_bf16 v[16:19], v[96:99], a[16:19], v[16:19]\n\t" \
  "ds_read_b128 v[120:123], v2 offset:12288\n\t" \
  "s_waitcnt lgkmcnt(5)\n\t" \
  "v_mfma_f32_16x16x32_bf16 v[20:23], v[100:103], a[16:19], v[20:23]\n\t" \
  "s_add_u32 s50, s82, s37\n\t" \
  "s_add_u32 s51, s50, 4096\n\t" \
  "s_cmp_lg_u32 s56, 0\n\t" \
  TK_QNP \
  "v_mfma_f32_16x16x32_bf16 v[24:27], v[96:99], a[88:91], v[24:27]\n\t" \
  "ds_read_b128 v[124:127], v2 offset:14336\n\t" \
  "v_mfma_f32_16x16x32_bf16 v[28:31], v[100:103], a[88:91], v[28:31]\n\t" \
  "s_cbranch_scc1 LWARM_%=\n\t" \
  "s_waitcnt vmcnt(0)\n\t" \
  "LWARM_%=:\n\t" \
  "s_barrier\n\t" \
  TK_QNP \
  "s_waitcnt lgkmcnt(5)\n\t" \
  "v_mfma_f32_16x16x32_bf16 v[16:19], v[104:107], a[20:23], v[16:19]\n\t" \
  "ds_read_b128 v[128:131], v1 offset:16384\n\t" \
  "s_waitcnt lgkmcnt(5)\n\t" \
  "v_mfma_f32_16x16x32_bf16 v[20:23], v[108:111], a[20:23], v[20:23]\n\t" \
  "buffer_load_dword v4, v7, %[X], 0 offen sc1\n\t" \
  TK_QNP \
  "v_mfma_f32_16x16x32_bf16 v[24:27], v[104:107], a[92:95], v[24:27]\n\t" \
  "ds_read_b128 v[132:135], v1 offset:18432\n\t" \
  "v_mfma_f32_16x16x32_bf16 v[28:31], v[108:111], a[92:95], v[28:31]\n\t" \
  TK_QNP \
  "s_waitcnt lgkmcnt(5)\n\t" \
  "v_mfma_f32_16x16x32_bf16 v[16:19], v[112:115], a[24:27], v[16:19]\n\t" \
  "ds_read_b128 v[136:139], v2 offset:16384\n\t" \
  "s_waitcnt lgkmcnt(5)\n\t" \
  "v_mfma_f32_16x16x32_bf16 v[20:23], v[116:119], a[24:27], v[20:23]\n\t" \
  TK_QNP \
  "v_mfma_f32_16x16x32_bf16 v[24:27], v[112:115], a[96:99], v[24:27]\n\t" \
  "ds_read_b128 v[140:143], v2 offset:18432\n\t" \
  "v_mfma_f32_16x16x32_bf16 v[28:31], v[116:119], a[96:99], v[28:31]\n\t" \
  TK_QNP \
  "s_waitcnt lgkmcnt(5)\n\t" \
  "v_mfma_f32_16x16x32_bf16 v[16:19], v[120:123], a[28:31], v[16:19]\n\t" \
  "ds_read_b128 v[144:147], v1 offset:20480\n\t" \
  "s_waitcnt lgkmcnt(5)\n\t" \
  "v_mfma_f32_16x16x32_bf16 v[20:23], v[124:127], a[28:31], v[20:23]\n\t" \
  TK_QNP \
  "v_mfma_f32_16x16x32_bf16 v[24:27], v[120:123], a[100:103], v[24:27]\n\t" \
  "ds_read_b128 v[148:151], v1 offset:22528\n\t" \
  "v_mfma_f32_16x16x32_bf16 v[28:31], v[124:127], a[100:103], v[28:31]\n\t" \
  TK_QNP \
  "s_waitcnt lgkmcnt(5)\n\t" \
  "v_mfma_f32_16x16x32_bf16 v[16:19], v[128:131], a[32:35], v[16:19]\n\t" \
  "ds_read_b128 v[152:155], v2 offset:20480\n\t" \
  "s_waitcnt lgkmcnt(5)\n\t" \
  "v_mfma_f32_16x16x32_bf16 v[20:23], v[132:135], a[32:35], v[20:23]\n\t" \
  TK_QNP \
  "v_mfma_f32_16x16x32_bf16 v[24:27], v[128:131], a[104:107], v[24:27]\n\t" \
  "ds_read_b128 v[156:159], v2 offset:22528\n\t" \
  "v_mfma_f32_16x16x32_bf16 v[28:31], v[132:135], a[104:107], v[28:31]\n\t" \
  TK_QNP \
  "s_waitcnt lgkmcnt(5)\n\t" \
  "v_mfma_f32_16x16x32_bf16 v[16:19], v[136:139], a[36:39], v[16:19]\n\t" \
  "ds_read_b128 v[160:163], v1 offset:24576\n\t" \
  "s_waitcnt lgkmcnt(5)\n\t" \
  "v_mfma_f32_16x16x32_bf16 v[20:23], v[140:143], a[36:39], v[20:23]\n\t" \
  TK_QNP \
  "v_mfma_f32_16x16x32_bf16 v[24:27], v[136:139], a[108:111], v[24:27]\n\t" \
  "ds_read_b128 v[164:167], v1 offset:26624\n\t" \
  "v_mfma_f32_16x16x32_bf16 v[28:31], v[140:143], a[108:111], v[28:31]\n\t" \
  TK_QNP \
  "s_waitcnt lgkmcnt(5)\n\t" \
  "v_mfma_f32_16x16x32_bf16 v[16:19], v[144:147], a[40:43], v[16:19]\n\t" \
  "ds_read_b128 v[168:171], v2 offset:24576\n\t" \
  "s_waitcnt lgkmcnt(5)\n\t" \
  "v_mfma_f32_16x16x32_bf16 v[20:23], v[148:151], a[40:43], v[20:23]\n\t" \
  TK_QNP \
  "v_mfma_f32_16x16x32_bf16 v[24:27], v[144:147], a[112:115], v[24:27]\n\t" \
  "ds_read_b128 v[172:175], v2 offset:26624\n\t" \
  "v_mfma_f32_16x16x32_bf16 v[28:31], v[148:151], a[112:115], v[28:31]\n\t" \
  TK_QNP \
  "s_waitcnt lgkmcnt(5)\n\t" \
  "v_mfma_f32_16x16x32_bf16 v[16:19], v[152:155], a[44:47], v[16:19]\n\t" \
  "ds_read_b128 v[176:179], v1 offset:28672\n\t" \
  "s_waitcnt lgkmcnt(5)\n\t" \
  "v_mfma_f32_16x16x32_bf16 v[20:23], v[156:159], a[44:47], v[20:23]\n\t" \
  TK_QNP \
  "v_mfma_f32_16x16x32_bf16 v[24:27], v[152:155], a[116:119], v[24:27]\n\t" \
  "ds_read_b128 v[180:183], v1 offset:30720\n\t" \
  "v_mfma_f32_16x16x32_bf16 v[28:31], v[156:159], a[116:119], v[28:31]\n\t" \
  "s_waitcnt vmcnt(0)\n\t" \
  "v_cmp_eq_u32 vcc, 0, v4\n\t" \
  TK_QNP \
  "s_waitcnt lgkmcnt(5)\n\t" \
  "v_mfma_f32_16x16x32_bf16 v[16:19], v[160:163], a[48:51], v[16:19]\n\t" \
  "ds_read_b128 v[184:187], v2 offset:28672\n\t" \
  "s_waitcnt lgkmcnt(5)\n\t" \
  "v_mfma_f32_16x16x32_bf16 v[20:23], v[164:167], a[48:51], v[20:23]\n\t" \
  "s_mov_b64 exec, 1\n\t" \
  "buffer_store_dword v6, off, %[X], s49 sc1\n\t" \
  "s_mov_b64 exec, -1\n\t" \
  "s_mov_b32 s49, 0xf0000000\n\t" \
  TK_PQ0 \
  TK_QNP \
  "v_mfma_f32_16x16x32_bf16 v[24:27], v[160:163], a[120:123], v[24:27]\n\t" \
  "ds_read_b128 v[188:191], v2 offset:30720\n\t" \
  "v_mfma_f32_16x16x32_bf16 v[28:31], v[164:167], a[120:123], v[28:31]\n\t" \
  TK_HAVE \
  "s_cselect_b32 s60, 1, 0\n\t" \
  "v_mov_b32 v4, s60\n\t" \
  TK_QNP \
  "s_waitcnt lgkmcnt(5)\n\t" \
  "v_mfma_f32_16x16x32_bf16 v[16:19], v[168:171], a[52:55], v[16:19]\n\t" \
  "ds_read_b128 v[192:195], v1 offset:32768\n\t" \
  "s_waitcnt lgkmcnt(5)\n\t" \
  "v_mfma_f32_16x16x32_bf16 v[20:23], v[172:175], a[52:55], v[20:23]\n\t" \
  "ds_write_b32 v5, v4\n\t" \
  TK_QNP \
  "v_mfma_f32_16x16x32_bf16 v[24:27], v[168:171], a[124:127], v[24:27]\n\t" \
  "ds_read_b128 v[196:199], v1 offset:34816\n\t" \
  "v_mfma_f32_16x16x32_bf16 v[28:31], v[172:175], a[124:127], v[28:31]\n\t" \
  TK_QNP \
  "s_waitcnt lgkmcnt(6)\n\t" \
  "v_mfma_f32_16x16x32_bf16 v[16:19], v[176:179], a[56:59], v[16:19]\n\t" \
  "ds_read_b128 v[200:203], v2 offset:32768\n\t" \
  "s_waitcnt lgkmcnt(6)\n\t" \
  "v_mfma_f32_16x16x32_bf16 v[20:23], v[180:183], a[56:59], v[20:23]\n\t" \
  TK_QNP \
  "v_mfma_f32_16x16x32_bf16 v[24:27], v[176:179], a[128:131], v[24:27]\n\t" \
  "ds_read_b128 v[204:207], v2 offset:34816\n\t" \
  "v_mfma_f32_16x16x32_bf16 v[28:31], v[180:183], a[128:131], v[28:31]\n\t" \
  TK_QNP \
  "s_waitcnt lgkmcnt(6)\n\t" \
  "v_mfma_f32_16x16x32_bf16 v[16:19], v[184:187], a[60:63], v[16:19]\n\t" \
  "s_waitcnt lgkmcnt(5)\n\t" \
  "v_mfma_f32_16x16x32_bf16 v[20:23], v[188:191], a[60:63], v[20:23]\n\t" \
  TK_QNP \
  "v_mfma_f32_16x16x32_bf16 v[24:27], v[184:187], a[132:135], v[24:27]\n\t" \
  "v_mfma_f32_16x16x32_bf16 v[28:31], v[188:191], a[132:135], v[28:31]\n\t" \
  "s_waitcnt lgkmcnt(0)\n\t" \
  "s_barrier\n\t" \
  TK_QNP \
  "v_mfma_f32_16x16x32_bf16 v[16:19], v[192:195], a[64:67], v[16:19]\n\t" \
  "ds_read_b32 v4, v6 offset:40959\n\t" \
  "v_xor_b32 v1, 0x10000, v1\n\t" \
  "v_xor_b32 v2, 0x10000, v2\n\t" \
  "v_mfma_f32_16x16x32_bf16 v[20:23], v[196:199], a[64:67], v[20:23]\n\t" \
  TK_QNP \
  "v_mfma_f32_16x16x32_bf16 v[24:27], v[192:195], a[136:139], v[24:27]\n\t" \
  "v_mfma_f32_16x16x32_bf16 v[28:31], v[196:199], a[136:139], v[28:31]\n\t" \
  TK_QNP \
  "v_mfma_f32_16x16x32_bf16 v[16:19], v[200:203], a[68:71], v[16:19]\n\t" \
  "v_mfma_f32_16x16x32_bf16 v[20:23], v[204:207], a[68:71], v[20:23]\n\t" \
  "s_waitcnt lgkmcnt(0)\n\t" \
  "v_readfirstlane_b32 s60, v4\n\t" \
  "s_add_u32 s55, s83, s36\n\t" \
  TK_QNP \
  "v_mfma_f32_16x16x32_bf16 v[24:27], v[200:203], a[140:143], v[24:27]\n\t" \
  "s_cmp_eq_u32 s48, 63\n\t" \
  "s_cselect_b32 s56, 0, s60\n\t" \
  "v_mfma_f32_16x16x32_bf16 v[28:31], v[204:207], a[140:143], v[28:31]\n\t" \
  "s_cmp_eq_u32 s56, 0\n\t" \
  "s_cselect_b32 s55, 0xf0000000, s55\n\t" \
  TK_QNP \
  "v_mfma_f32_16x16x32_bf16 v[32:35], v[64:67], a[144:147], 0\n\t" \
  "v_cvt_pk_bf16_f32 v48, v16, v17\n\t" \
  "v_cvt_pk_bf16_f32 v49, v18, v19\n\t" \
  "v_mfma_f32_16x16x32_bf16 v[36:39], v[68:71], a[144:147], 0\n\t" \
  "v_cvt_pk_bf16_f32 v50, v20, v21\n\t" \
  "v_cvt_pk_bf16_f32 v51, v22, v23\n\t" \
  TK_QNP \
  "v_mfma_f32_16x16x32_bf16 v[32:35], v[72:75], a[148:151], v[32:35]\n\t" \
  "v_cvt_pk_bf16_f32 v52, v24, v25\n\t" \
  "v_cvt_pk_bf16_f32 v53, v26, v27\n\t" \
  "v_mfma_f32_16x16x32_bf16 v[36:39], v[76:79], a[148:151], v[36:39]\n\t" \
  "v_cvt_pk_bf16_f32 v54, v28, v29\n\t" \
  "v_cvt_pk_bf16_f32 v55, v30, v31\n\t" \
  TK_QNP \
  "v_mfma_f32_16x16x32_bf16 v[32:35], v[80:83], a[152:155], v[32:35]\n\t" \
  "buffer_store_dwordx4 v[48:51], v0, %[X], s50 offen offset:0 sc1\n\t" \
  "v_mfma_f32_16x16x32_bf16 v[36:39], v[84:87], a[152:155], v[36:39]\n\t" \
  TK_QNP \
  "v_mfma_f32_16x16x32_bf16 v[32:35], v[88:91], a[156:159], v[32:35]\n\t" \
  "s_add_u32 m0, s46, 0\n\t" \
  "s_add_u32 s47, s55, s52\n\t" \
  "buffer_load_dwordx4 v0, %[X], s47 offen sc1 lds\n\t" \
  "v_mfma_f32_16x16x32_bf16 v[36:39], v[92:95], a[156:159], v[36:39]\n\t" \
  TK_QNP \
  "v_mfma_f32_16x16x32_bf16 v[32:35], v[96:99], a[160:163], v[32:35]\n\t" \
  "v_mfma_f32_16x16x32_bf16 v[36:39], v[100:103], a[160:163], v[36:39]\n\t" \
  "s_add_u32 m0, s46, 4096\n\t" \
  "s_add_u32 s47, s55, s53\n\t" \
  "buffer_load_dwordx4 v0, %[X], s47 offen sc1 lds\n\t" \
  TK_QNP \
  "v_mfma_f32_16x16x32_bf16 v[32:35], v[104:107], a[164:167], v[32:35]\n\t" \
  "v_mfma_f32_16x16x32_bf16 v[36:39], v[108:111], a[164:167], v[36:39]\n\t" \
  TK_QNP \
  "v_mfma_f32_16x16x32_bf16 v[32:35], v[112:115], a[168:171], v[32:35]\n\t" \
  "s_add_u32 m0, s46, 8192\n\t" \
  "s_add_u32 s47, s55, s54\n\t" \
  "buffer_load_dwordx4 v0, %[X], s47 offen sc1 lds\n\t" \
  "v_mfma_f32_16x16x32_bf16 v[36:39], v[116:119], a[168:171], v[36:39]\n\t" \
  TK_QNP \
  "v_mfma_f32_16x16x32_bf16 v[32:35], v[120:123], a[172:175], v[32:35]\n\t" \
  "v_mfma_f32_16x16x32_bf16 v[36:39], v[124:127], a[172:175], v[36:39]\n\t" \
  "s_add_u32 m0, s46, 12288\n\t" \
  "s_add_u32 s47, s55, s57\n\t" \
  "buffer_load_dwordx4 v0, %[X], s47 offen sc1 lds\n\t" \
  TK_QNP \
  "v_mfma_f32_16x16x32_bf16 v[32:35], v[128:131], a[176:179], v[32:35]\n\t" \
  "v_mfma_f32_16x16x32_bf16 v[36:39], v[132:135], a[176:179], v[36:39]\n\t" \
  TK_QNP \
  "v_mfma_f32_16x16x32_bf16 v[32:35], v[136:139], a[180:183], v[32:35]\n\t" \
  "s_add_u32 m0, s46, 16384\n\t" \
  "s_add_u32 s47, s55, s58\n\t" \
  "buffer_load_dwordx4 v0, %[X], s47 offen sc1 lds\n\t" \
  "v_mfma_f32_16x16x32_bf16 v[36:39], v[140:143], a[180:183], v[36:39]\n\t" \
  TK_QNP \
  "v_mfma_f32_16x16x32_bf16 v[32:35], v[144:147], a[184:187], v[32:35]\n\t" \
  "v_mfma_f32_16x16x32_bf16 v[36:39], v[148:151], a[184:187], v[36:39]\n\t" \
  TK_QNP \
  "v_mfma_f32_16x16x32_bf16 v[32:35], v[152:155], a[188:191], v[32:35]\n\t" \
  "buffer_store_dwordx4 v[52:55], v0, %[X], s50 offen offset:1024 sc1\n\t" \
  "v_mfma_f32_16x16x32_bf16 v[36:39], v[156:159], a[188:191], v[36:39]\n\t" \
  TK_QNP \
  "v_mfma_f32_16x16x32_bf16 v[32:35], v[160:163], a[192:195], v[32:35]\n\t" \
  "v_mfma_f32_16x16x32_bf16 v[36:39], v[164:167], a[192:195], v[36:39]\n\t" \
  "s_add_u32 m0, s46, 20480\n\t" \
  "s_add_u32 s47, s55, s59\n\t" \
  "buffer_load_dwordx4 v0, %[X], s47 offen sc1 lds\n\t" \
  TK_QNP \
  "v_mfma_f32_16x16x32_bf16 v[32:35], v[168:171], a[196:199], v[32:35]\n\t" \
  "v_mfma_f32_16x16x32_bf16 v[36:39], v[172:175], a[196:199], v[36:39]\n\t" \
  TK_QNP \
  "v_mfma_f32_16x16x32_bf16 v[32:35], v[176:179], a[200:203], v[32:35]\n\t" \
  "s_add_u32 m0, s46, 24576\n\t" \
  "s_add_u32 s47, s55, s64\n\t" \
  "buffer_load_dwordx4 v0, %[X], s47 offen sc1 lds\n\t" \
  "v_mfma_f32_16x16x32_bf16 v[36:39], v[180:183], a[200:203], v[36:39]\n\t" \
  TK_QNP \
  "v_mfma_f32_16x16x32_bf16 v[32:35], v[184:187], a[204:207], v[32:35]\n\t" \
  "v_mfma_f32_16x16x32_bf16 v[36:39], v[188:191], a[204:207], v[36:39]\n\t" \
  "s_add_u32 m0, s46, 28672\n\t" \
  "s_add_u32 s47, s55, s65\n\t" \
  "buffer_load_dwordx4 v0, %[X], s47 offen sc1 lds\n\t" \
  TK_QNP \
  "v_mfma_f32_16x16x32_bf16 v[32:35], v[192:195], a[208:211], v[32:35]\n\t" \
  "v_mfma_f32_16x16x32_bf16 v[36:39], v[196:199], a[208:211], v[36:39]\n\t" \
  TK_QNP \
  "v_mfma_f32_16x16x32_bf16 v[32:35], v[200:203], a[212:215], v[32:35]\n\t" \
  "s_add_u32 m0, s46, 32768\n\t" \
  "s_add_u32 s47, s55, s66\n\t" \
  "buffer_load_dwordx4 v0, %[X], s47 offen sc1 lds\n\t" \
  "v_mfma_f32_16x16x32_bf16 v[36:39], v[204:207], a[212:215], v[36:39]\n\t" \
  TK_QNP \
  "v_mfma_f32_16x16x32_bf16 v[40:43], v[64:67], a[216:219], 0\n\t" \
  "v_mfma_f32_16x16x32_bf16 v[44:47], v[68:71], a[216:219], 0\n\t" \
  TK_QNP \
  "v_mfma_f32_16x16x32_bf16 v[40:43], v[72:75], a[220:223], v[40:43]\n\t" \
  "v_cvt_pk_bf16_f32 v56, v32, v33\n\t" \
  "v_cvt_pk_bf16_f32 v57, v34, v35\n\t" \
  "v_mfma_f32_16x16x32_bf16 v[44:47], v[76:79], a[220:223], v[44:47]\n\t" \
  "v_cvt_pk_bf16_f32 v58, v36, v37\n\t" \
  "v_cvt_pk_bf16_f32 v59, v38, v39\n\t" \
  TK_QNP \
  "v_mfma_f32_16x16x32_bf16 v[40:43], v[80:83], a[224:227], v[40:43]\n\t" \
  "v_mfma_f32_16x16x32_bf16 v[44:47], v[84:87], a[224:227], v[44:47]\n\t" \
  TK_QNP \
  "v_mfma_f32_16x16x32_bf16 v[40:43], v[88:91], a[228:231], v[40:43]\n\t" \
  "buffer_store_dwordx4 v[56:59], v0, %[X], s50 offen offset:2048 sc1\n\t" \
  "v_mfma_f32_16x16x32_bf16 v[44:47], v[92:95], a[228:231], v[44:47]\n\t" \
  TK_QNP \
  "v_mfma_f32_16x16x32_bf16 v[40:43], v[96:99], a[232:235], v[40:43]\n\t" \
  "v_mfma_f32_16x16x32_bf16 v[44:47], v[100:103], a[232:235], v[44:47]\n\t" \
  TK_QNP \
  "v_mfma_f32_16x16x32_bf16 v[40:43], v[104:107], a[236:239], v[40:43]\n\t" \
  "v_mfma_f32_16x16x32_bf16 v[44:47], v[108:111], a[236:239], v[44:47]\n\t" \
  TK_QNP \
  "v_mfma_f32_16x16x32_bf16 v[40:43], v[112:115], a[240:243], v[40:43]\n\t" \
  "v_mfma_f32_16x16x32_bf16 v[44:47], v[116:119], a[240:243], v[44:47]\n\t" \
  TK_QNP \
  "v_mfma_f32_16x16x32_bf16 v[40:43], v[120:123], a[244:247], v[40:43]\n\t" \
  "v_mfma_f32_16x16x32_bf16 v[44:47], v[124:127], a[244:247], v[44:47]\n\t" \
  TK_QNP \
  "v_mfma_f32_16x16x32_bf16 v[40:43], v[128:131], a[248:251], v[40:43]\n\t" \
  "v_mfma_f32_16x16x32_bf16 v[44:47], v[132:135], a[248:251], v[44:47]\n\t" \
  TK_QNP \
  "v_mfma_f32_16x16x32_bf16 v[40:43], v[136:139], a[252:255], v[40:43]\n\t" \
  "v_mfma_f32_16x16x32_bf16 v[44:47], v[140:143], a[252:255], v[44:47]\n\t" \
  TK_QNP \
  "v_mfma_f32_16x16x32_bf16 v[40:43], v[144:147], v[224:227], v[40:43]\n\t" \
  "v_mfma_f32_16x16x32_bf16 v[44:47], v[148:151], v[224:227], v[44:47]\n\t" \
  TK_QNP \
  "v_mfma_f32_16x16x32_bf16 v[40:43], v[152:155], v[228:231], v[40:43]\n\t" \
  "v_mfma_f32_16x16x32_bf16 v[44:47], v[156:159], v[228:231], v[44:47]\n\t" \
  TK_QNP \
  "v_mfma_f32_16x16x32_bf16 v[40:43], v[160:163], v[232:235], v[40:43]\n\t" \
  "v_mfma_f32_16x16x32_bf16 v[44:47], v[164:167], v[232:235], v[44:47]\n\t" \
  TK_QNP \
  "v_mfma_f32_16x16x32_bf16 v[40:43], v[168:171], v[236:239], v[40:43]\n\t" \
  "v_mfma_f32_16x16x32_bf16 v[44:47], v[172:175], v[236:239], v[44:47]\n\t" \
  TK_QNP \
  "v_mfma_f32_16x16x32_bf16 v[40:43], v[176:179], v[240:243], v[40:43]\n\t" \
  "v_mfma_f32_16x16x32_bf16 v[44:47], v[180:183], v[240:243], v[44:47]\n\t" \
  TK_QNP \
  "v_mfma_f32_16x16x32_bf16 v[40:43], v[184:187], v[244:247], v[40:43]\n\t" \
  "v_mfma_f32_16x16x32_bf16 v[44:47], v[188:191], v[244:247], v[44:47]\n\t" \
  TK_QNP \
  "v_mfma_f32_16x16x32_bf16 v[40:43], v[192:195], v[248:251], v[40:43]\n\t" \
  "v_mfma_f32_16x16x32_bf16 v[44:47], v[196:199], v[248:251], v[44:47]\n\t" \
  TK_QNP \
  "v_mfma_f32_16x16x32_bf16 v[40:43], v[200:203], v[252:255], v[40:43]\n\t" \
  "v_mfma_f32_16x16x32_bf16 v[44:47], v[204:207], v[252:255], v[44:47]\n\t" \
  TK_QNP \
  "s_nop 4\n\t" \
  "v_cvt_pk_bf16_f32 v60, v40, v41\n\t" \
  "v_cvt_pk_bf16_f32 v61, v42, v43\n\t" \
  "s_nop 1\n\t" \
  "v_cvt_pk_bf16_f32 v62, v44, v45\n\t" \
  "v_cvt_pk_bf16_f32 v63, v46, v47\n\t" \
  "buffer_store_dwordx4 v[60:63], v0, %[X], s50 offen offset:3072 sc1\n\t" \
  TK_PQ2 \
  "s_add_u32 s49, s82, s38\n\t" \
  "s_cmp_eq_u32 s48, 63\n\t" \
  "s_cbranch_scc1 LEND_%=\n\t" \
  "s_add_u32 s48, s48, 1\n\t" \
  "s_cmp_eq_u32 s56, 0\n\t" \
  "s_cbranch_scc1 LCOLD_%=\n\t" \
  "s_waitcnt vmcnt(2)\n\t" \
  TK_PACE \
  "s_branch LTOP_%=\n\t" \
  "LPACE_%=:\n\t" \
  "s_waitcnt vmcnt(0)\n\t" \
  "s_mov_b64 exec, 1\n\t" \
  "buffer_store_dword v6, off, %[X], s49 sc1\n\t" \
  "s_mov_b64 exec, -1\n\t" \
  "s_mov_b32 s49, 0xf0000000\n\t" \
  "s_cmp_lg_u32 s40, 0\n\t" \
  "s_cbranch_scc1 LTOP_%=\n\t" \
  "s_mov_b32 s61, 0\n\t" \
  "LPP_%=:\n\t" \
  "buffer_load_dword v4, v7, %[X], 0 offen sc1\n\t" \
  "s_waitcnt vmcnt(0)\n\t" \
  "v_cmp_eq_u32 vcc, 0, v4\n\t" \
  "s_cbranch_vccz LPP_%=x\n\t" \
  "s_add_u32 s61, s61, 1\n\t" \
  "s_sleep " TK_PSH "\n\t" \
  "s_cmp_gt_u32 s61, s84\n\t" \
  "s_cbranch_scc1 LPP_%=g\n\t" \
  "s_and_b32 s60, s61, 1023\n\t" \
  "s_cmp_lg_u32 s60, 0\n\t" \
  "s_cbranch_scc1 LPP_%=\n\t" \
  "buffer_load_dword v4, off, %[X], s92 sc1\n\t" \
  "s_waitcnt vmcnt(0)\n\t" \
  "v_readfirstlane_b32 s60, v4\n\t" \
  "s_nop 0\n\t" \
  "s_cmp_eq_u32 s60, 0\n\t" \
  "s_cbranch_scc1 LPP_%=\n\t" \
  "LPP_%=g:\n\t" \
  "s_mov_b32 s84, 0\n\t" \
  "s_or_b32 s60, s48, 1879048192\n\t" \
  "v_mov_b32 v4, s60\n\t" \
  "s_mov_b64 exec, 1\n\t" \
  "buffer_store_dword v4, off, %[X], s92 sc1\n\t" \
  "s_mov_b64 exec, -1\n\t" \
  "LPP_%=x:\n\t" \
  TK_QDL2 \
  "s_branch LTOP_%=\n\t" \
  "LEND_%=:\n\t" \
  "s_waitcnt vmcnt(0)\n\t" \
  "s_mov_b64 exec, 1\n\t" \
  "buffer_store_dword v6, off, %[X], s49 sc1\n\t" \
  "s_mov_b64 exec, -1\n\t" \
  "s_mov_b32 s49, 0xf0000000\n\t" \
  TK_PQ0 \
  ""
__device__ __forceinline__ unsigned hoff(int r, int piece) {
  const int m = 4 * (r >> 3) + (r & 3), kp = piece >> 3;
  return (P_H + (unsigned)(kp >> 1)) * PAIR + (unsigned)((kp & 1) * 4096 + (16 * ((r >> 2) & 1) + m) * 128 + (((piece ^ m) & 7) << 4));
}
template <int SLEEP>
__device__ __forceinline__ void wait_all(rsrc_t X, unsigned vo, unsigned so) {
  unsigned spins = 0;
  while (true) {
    asm volatile("" ::: "memory");
    if (__all(ld4(X, vo, so) != 0u)) break;
    if (spin_fail(spins, X)) break;
    __builtin_amdgcn_s_sleep(SLEEP);
  }
  asm volatile("" ::: "memory");
}
#define VQ_CLOB "v16", "v17", "v18", "v19", "v20", "v21", "v22", "v23", "v24", "v25", "v26", "v27", "v28", "v29", "v30", "v31", "v32", "v33", "v34", "v35", "v36", "v37", "v38", "v39", "v40", "v41", "v42", "v43", "v44", "v45", "v46", "v47", "v48", "v49", "v50", "v51", "v52", "v53", "v54", "v55", "v56", "v57", "v58", "v59", "v60", "v61", "v62", "v63", "v64", "v65", "v66", "v67", "v68", "v69", "v70", "v71", "v72", "v73", "v74", "v75", "v76", "v77", "v78", "v79", "v80", "v81", "v82", "v83", "v84", "v85", "v86", "v87", "v88", "v89", "v90", "v91", "v92", "v93", "v94", "v95", "v96", "v97", "v98", "v99", "v100", "v101", "v102", "v103", "v104", "v105", "v106", "v107", "v108", "v109", "v110", "v111", "v112", "v113", "v114", "v115", "v116", "v117", "v118", "v119", "v120", "v121", "v122", "v123", "v124", "v125", "v126", "v127", "v128", "v129", "v130", "v131", "v132", "v133", "v134", "v135", "v136", "v137", "v138", "v139", "v140", "v141", "v142", "v143", "v144", "v145", "v146", "v147", "v148", "v149", "v150", "v151", "v152", "v153", "v154", "v155", "v156", "v157", "v158", "v159", "v160", "v161", "v162", "v163", "v164", "v165", "v166", "v167", "v168", "v169", "v170", "v171", "v172", "v173", "v174", "v175", "v176", "v177", "v178", "v179", "v180", "v181", "v182", "v183", "v184", "v185", "v186", "v187", "v188", "v189", "v190", "v191", "v192", "v193", "v194", "v195", "v196", "v197", "v198", "v199", "v200", "v201", "v202", "v203", "v204", "v205", "v206", "v207", "v220", "v221"
#define VA_CLOB "v40", "v41", "v42", "v43", "v44", "v45", "v46", "v47", "v48", "v49", "v50", "v51", "v52", "v53", "v54", "v55", "v56", "v57", "v58", "v59", "v60", "v61", "v62", "v63", "v64", "v65", "v66", "v67", "v68", "v69", "v70", "v71", "v72", "v73", "v74", "v75", "v76", "v77", "v78", "v79", "v80", "v81", "v82", "v83", "v84", "v85", "v86", "v87", "v88", "v89", "v90", "v91", "v92", "v93", "v94", "v95", "v96", "v97", "v98", "v99", "v100", "v101", "v102", "v103", "v104", "v105", "v106", "v107", "v108", "v109", "v110", "v111", "v112", "v113", "v114", "v115", "v116", "v117", "v118", "v119", "v120", "v121", "v122", "v123", "v124", "v125", "v126", "v127", "v128", "v129", "v130", "v131", "v132", "v133", "v134", "v135", "v136", "v137", "v138", "v139", "v140", "v141", "v142", "v143", "v144", "v145", "v146", "v147", "v148", "v149", "v150", "v151", "v152", "v153", "v154", "v155", "v156", "v157", "v158", "v159", "v160", "v161", "v162", "v163", "v164", "v165", "v166", "v167", "v168", "v169", "v170", "v171", "v172", "v173", "v174", "v175", "v176", "v177", "v178", "v179", "v180", "v181", "v182", "v183", "v184", "v185", "v186", "v187", "v188", "v189", "v190", "v191", "v192", "v193", "v194", "v195", "v196", "v197", "v198", "v199", "v200", "v201", "v202", "v203", "v204", "v205", "v206", "v207", "v208", "v209", "v210", "v211", "v220", "v221"
#define TOK_COMMON() \
  const MP p = getp(); \
  const int c = blockIdx.x - NCHW, xq = c & 3, L = (c >> 2) & 1, i = c >> 3; \
  const int tid = threadIdx.x, lane = tid & 63, w = __builtin_amdgcn_readfirstlane(tid >> 6); \
  const rsrc_t X = mkr(p.xb, XB_BYTES)
#define W_IN [W] "s"(WR)
#define W_CLOB "a0", "a1", "a2", "a3", "a4", "a5", "a6", "a7", "a8", "a9", "a10", "a11", "a12", "a13", "a14", "a15", "a16", "a17", "a18", "a19", "a20", "a21", "a22", "a23", "a24", "a25", "a26", "a27", "a28", "a29", "a30", "a31", "a32", "a33", "a34", "a35", "a36", "a37", "a38", "a39", "a40", "a41", "a42", "a43", "a44", "a45", "a46", "a47", "a48", "a49", "a50", "a51", "a52", "a53", "a54", "a55", "a56", "a57", "a58", "a59", "a60", "a61", "a62", "a63", "a64", "a65", "a66", "a67", "a68", "a69", "a70", "a71", "a72", "a73", "a74", "a75", "a76", "a77", "a78", "a79", "a80", "a81", "a82", "a83", "a84", "a85", "a86", "a87", "a88", "a89", "a90", "a91", "a92", "a93", "a94", "a95", "a96", "a97", "a98", "a99", "a100", "a101", "a102", "a103", "a104", "a105", "a106", "a107", "a108", "a109", "a110", "a111", "a112", "a113", "a114", "a115", "a116", "a117", "a118", "a119", "a120", "a121", "a122", "a123", "a124", "a125", "a126", "a127", "a128", "a129", "a130", "a131", "a132", "a133", "a134", "a135", "a136", "a137", "a138", "a139", "a140", "a141", "a142", "a143", "a144", "a145", "a146", "a147", "a148", "a149", "a150", "a151", "a152", "a153", "a154", "a155", "a156", "a157", "a158", "a159", "a160", "a161", "a162", "a163", "a164", "a165", "a166", "a167", "a168", "a169", "a170", "a171", "a172", "a173", "a174", "a175", "a176", "a177", "a178", "a179", "a180", "a181", "a182", "a183", "a184", "a185", "a186", "a187", "a188", "a189", "a190", "a191", "a192", "a193", "a194", "a195", "a196", "a197", "a198", "a199", "a200", "a201", "a202", "a203", "a204", "a205", "a206", "a207", "a208", "a209", "a210", "a211", "a212", "a213", "a214", "a215", "a216", "a217", "a218", "a219", "a220", "a221", "a222", "a223", "a224", "a225", "a226", "a227", "a228", "a229", "a230", "a231", "a232", "a233", "a234", "a235", "a236", "a237", "a238", "a239", "a240", "a241", "a242", "a243", "a244", "a245", "a246", "a247", "a248", "a249", "a250", "a251", "a252", "a253", "a254", "a255", "v224", "v225", "v226", "v227", "v228", "v229", "v230", "v231", "v232", "v233", "v234", "v235", "v236", "v237", "v238", "v239", "v240", "v241", "v242", "v243", "v244", "v245", "v246", "v247", "v248", "v249", "v250", "v251", "v252", "v253", "v254", "v255"
#define S_CLOB "s46", "s47", "s48", "s49", "s50", "s51", "s52", "s53", "s54", "s55", "s56", "s57", "s58", "s59", "s60", "s61", "s62", "s63", "s64", "s65", "s66", "s68", "s69", "s70", "s71", "s72", "s73", "s82", "s83", "s84", "m0", "vcc", "scc", "memory"
typedef unsigned v8w __attribute__((ext_vector_type(8)));

__device__ __forceinline__ void qkv_role(v2u cv) {
  TOK_COMMON();
  const int role = i * 4 + w, hh = 8 * xq + role / 6, part = role % 6, which = part >> 1, half = part & 1;
  const rsrc_t WR = mkr(p.WP + (long)((which * NH + hh) * 8 + 4 * half) * 18 * 512, 73728u);
  asm volatile("" :: "v"(cv));
  const unsigned long long sm = die_mask(X, SG0, L, lane);
  const int m = lane & 15, g = lane >> 4;
  const unsigned a0 = (unsigned)(m * 128 + ((g ^ (m & 7)) << 4));
  v8w lv = {(unsigned)lane * 16u, a0, a0 ^ 64u, (unsigned)(lane & (L ? 15 : 3)) * 128u, 0u, 40960u + 4u * (unsigned)w, 1u, 0u};
  const unsigned c36 = XQL + P_H * PAIR + (unsigned)w * 1024u, c37 = XQ + (unsigned)(3 * hh + which) * PAIR + (unsigned)half * (which == 2 ? 4096u : 2048u);
  const unsigned c38 = P_FA * PAIR + (unsigned)(hh * 128 + part * 16), c39 = w == 0 ? P_FB * PAIR + 4096u : 0xF0000000u, c41 = (unsigned)w * 1024u;
  const int c42 = L ? -64 : 1 - QLEAD;
  const unsigned c43 = w == 0 ? P_FA * PAIR + 4096u + (unsigned)hh * 128u : 0xF0000000u;
  unsigned d74 = TRC_OFF + 262144u + (unsigned)((c * 4 + w) * 66) * 8u, d44 = ROT ? (unsigned)(i * ROT) % 9u : 0u;
  TT(64, 0);
#define Q_CALL(A) asm volatile("s_waitcnt vmcnt(0) lgkmcnt(0)\n\t" A : "+{v[0:7]}"(lv), "+{s74}"(d74), "+{s44}"(d44) \
               : [X] "s"(X), "{s36}"(c36), "{s37}"(c37), "{s38}"(c38), "{s39}"(c39), "{s40}"(w), "{s41}"(c41), "{s42}"(c42), "{s43}"(c43), "{s[90:91]}"(sm), "{s92}"(TMO_OFF), [cap] "i"(SPIN_CAP), W_IN : VQ_CLOB, W_CLOB, S_CLOB)
  if (which == 2) { Q_CALL(QV_ASM); __builtin_amdgcn_endpgm(); }
  Q_CALL(QK_ASM);
  __builtin_amdgcn_endpgm();
}

__device__ __forceinline__ void ap_role(v2u cv) {
  TOK_COMMON();
  const int hp = 4 * xq + ((i - 12) >> 1), ch = i & 1, a = w >> 1, rt = w & 1, h = 2 * hp + a;
  const int n = lane & 15, g = lane >> 4;
  const rsrc_t WR = mkr(p.WOA + (long)((hp * 2 + ch) * 2 + a) * 72 * 512, 73728u);
  asm volatile("" :: "v"(cv));
  const unsigned long long sm = die_mask(X, SG0, L, lane);
  const unsigned tm = (unsigned)die_mask(X, TP0, L, lane);
  const unsigned l16 = (unsigned)lane * 16u, ab = (unsigned)a * 24576u + l16;
  v16u lv = {l16, ab, ab + (unsigned)rt * 4096u, 49152u + (unsigned)w * 4096u + l16, 49152u + (unsigned)(w ^ 2) * 4096u + l16, l16 & ~32u, l16 >> 1,
             (unsigned)(g * 262144 + n * 16), (unsigned)(n * 32 + g * 8), (unsigned)((lane & 7) % 6) * 16u, 0u, 0u, 0u, 0u, 0u, 0u};
  v8w mk;
  const int qi = 8 * (n >> 2) + (n & 3) + 4 * rt;
#pragma unroll
  for (int k = 0; k < 8; ++k) mk[k] = 8 * g + k > qi ? 0xFF800000u : 0u;
  v16u zz = {0u, 0u, 0u, 0u, 0u, 0u, 0u, 0u, 0u, 0u, 0u, 0u, 0u, 0u, 0u, 0u};
  const unsigned tb = XAL + (unsigned)(3 * h) * PAIR;
  const unsigned c36 = tb + (rt ? PAIR + 4096u : 0u), c37 = tb + (rt ? 2u * PAIR : 4096u), c38 = tb + (rt ? 2u * PAIR + 4096u : PAIR);
  const unsigned c39 = (P_KV + (unsigned)(h >> 1)) * PAIR + (unsigned)(h & 1) * 4096u, c41 = P_FA * PAIR + (unsigned)h * 128u;
  const unsigned c42 = w == 0 ? P_FB * PAIR + (unsigned)(2 * hp + ch) * 128u : 0xF0000000u;
  const unsigned c43 = XA + TP0 * XBLK + (unsigned)(8 * rt + (hp >> 3)) * PAIR + (unsigned)((hp & 7) * 1024 + ch * 512 + a * 256);
  const unsigned c44 = (TP0 + 8u) * XBLK + ((tm >> 8) & 1u) * 8192u + (unsigned)(hp >> 1) * PAIR + (unsigned)((((hp & 1) * 4 + 2 * ch + a) * 2 + rt) * 512);
  const unsigned c45 = (unsigned)(a * 24576 + rt * 12288);
  unsigned d74 = TRC_OFF + 262144u + (unsigned)((c * 4 + w) * 66) * 8u;
  asm volatile("s_waitcnt vmcnt(0) lgkmcnt(0)\n\t" AP_ASM : "+{v[0:15]}"(lv), "+{v[24:39]}"(zz), "+{s74}"(d74)
               : [X] "s"(X), "{s36}"(c36), "{s37}"(c37), "{s38}"(c38), "{s39}"(c39), "{s41}"(c41), "{s42}"(c42), "{s43}"(c43), "{s44}"(c44), "{s45}"(c45), "{s88}"(tm), "{s[90:91]}"(sm),
                 "{s92}"(TMO_OFF), [cap] "i"(SPIN_CAP), "{v[16:23]}"(mk), W_IN : VA_CLOB, W_CLOB, S_CLOB);
  __builtin_amdgcn_endpgm();
}

template <int N> __device__ __forceinline__ float rorN(float v) { return __uint_as_float((unsigned)__builtin_amdgcn_update_dpp(0, (int)__float_as_uint(v), 0x120 + N, 0xf, 0xf, false)); }
__device__ __forceinline__ float wsum64(float v) {
  v += rorN<8>(v); v += rorN<4>(v); v += rorN<2>(v); v += rorN<1>(v);
  return sum4g(v);
}
template <int N> __device__ __forceinline__ void rbN(float (&x)[N]) {
#pragma unroll
  for (int i = 0; i < N; i += 2) { const unsigned u = pk2(x[i], x[i + 1]); x[i] = lo2f(u); x[i + 1] = hi2f(u); }
}
__device__ __forceinline__ void ln16(const float (&x)[16], bool v2, float (&o)[16]) {
  float s = 0.f, s2 = 0.f;
#pragma unroll
  for (int i = 0; i < 8; ++i) { s += x[i]; s2 += x[8 + i]; }
  const float mean = wsum64(v2 ? s + s2 : s) * (1.0f / DM);
  float q = 0.f, q2 = 0.f;
#pragma unroll
  for (int i = 0; i < 8; ++i) { const float d = x[i] - mean, e = x[8 + i] - mean; q = __builtin_fmaf(d, d, q); q2 = __builtin_fmaf(e, e, q2); }
  const unsigned mv = pk2(mean, wsum64(v2 ? q + q2 : q) * (1.0f / DM));
  const float m = lo2f(mv), var = hi2f(mv);
  const float ve = lo2f(pk2(var + 1e-5f, 0.f));
  const float den = lo2f(pk2(__builtin_amdgcn_sqrtf(ve), 0.f));
  const float inv = 1.0f / den;
#pragma unroll
  for (int i = 0; i < 16; ++i) o[i] = x[i] - m;
  rbN<16>(o);
#pragma unroll
  for (int i = 0; i < 16; ++i) o[i] *= inv;
}
__device__ __forceinline__ void up8(v4u v, float* x) {
  x[0] = lo2f(v.x); x[1] = hi2f(v.x); x[2] = lo2f(v.y); x[3] = hi2f(v.y); x[4] = lo2f(v.z); x[5] = hi2f(v.z); x[6] = lo2f(v.w); x[7] = hi2f(v.w);
}
__device__ __forceinline__ v4u pk8(const float* x) { return (v4u){pk2(x[0], x[1]), pk2(x[2], x[3]), pk2(x[4], x[5]), pk2(x[6], x[7])}; }

__device__ __forceinline__ void rb9(float (&x)[9]) {
#pragma unroll
  for (int i = 0; i < 8; i += 2) { const unsigned u = pk2(x[i], x[i + 1]); x[i] = lo2f(u); x[i + 1] = hi2f(u); }
  x[8] = lo2f(pk2(x[8], x[8]));
}
__device__ __forceinline__ void ln9(const float (&x)[9], float (&o)[9]) {
  const float mean = wsum64((((x[0] + x[1]) + (x[2] + x[3])) + ((x[4] + x[5]) + (x[6] + x[7]))) + x[8]) * (1.0f / DM);
  float q = 0.f, q2 = 0.f;
#pragma unroll
  for (int i = 0; i < 8; i += 2) { const float d = x[i] - mean, e = x[i + 1] - mean; q = __builtin_fmaf(d, d, q); q2 = __builtin_fmaf(e, e, q2); }
  { const float d = x[8] - mean; q = __builtin_fmaf(d, d, q); }
  const unsigned mv = pk2(mean, wsum64(q + q2) * (1.0f / DM));
  const float m = lo2f(mv), var = hi2f(mv);
  const float ve = lo2f(pk2(var + 1e-5f, 0.f));
  const float den = lo2f(pk2(__builtin_amdgcn_sqrtf(ve), 0.f));
  const float inv = __builtin_amdgcn_rcpf(den);
#pragma unroll
  for (int i = 0; i < 9; ++i) o[i] = x[i] - m;
  rb9(o);
#pragma unroll
  for (int i = 0; i < 9; ++i) o[i] *= inv;
}
__device__ __forceinline__ v4u gat8(float v) {
  const unsigned d = pk2(v, __uint_as_float((unsigned)__builtin_amdgcn_mov_dpp((int)__float_as_uint(v), 0xB1, 0xf, 0xf, true)));
  return (v4u){d, (unsigned)__builtin_amdgcn_mov_dpp((int)d, 0x102, 0xf, 0xf, true), (unsigned)__builtin_amdgcn_mov_dpp((int)d, 0x104, 0xf, 0xf, true),
               (unsigned)__builtin_amdgcn_mov_dpp((int)d, 0x106, 0xf, 0xf, true)};
}
__device__ __forceinline__ void tok_row(const MP& p, const rsrc_t X, unsigned sb1, int L, int s, int r, int lane, const v4u (&v)[18], v4u px, v4u py, unsigned px9, unsigned py9) {
  const long tok = (long)s * SEG + r;
  const int m = lane & 15, g = lane >> 4;
  const int xp = 4 * (g & 1) + 2 * (g >> 1) + (m >> 3);
  const unsigned zr = opq(0u);
  const unsigned one = (g == (m >> 2)) ? (opq(0x3F80u) << (16 * (m & 1))) : zr;
  const unsigned ax = (m & 2) ? zr : one, ay = (m & 2) ? one : zr;
  const v4u A1 = {ax, ay, zr, zr}, A2 = {zr, zr, ax, ay};
  const unsigned on2 = opq(0x3F80u) << (16 * (m & 1));
  const int q2 = (m >> 1) & 3;
  const v4u sel = {q2 == 0 ? on2 : zr, q2 == 1 ? on2 : zr, q2 == 2 ? on2 : zr, q2 == 3 ? on2 : zr};
  f32x4 d1, d2, e1;
  asm volatile("s_nop 1\n\t" MF "%0, %1, %3, 0\n\t" MF "%0, %2, %3, %0" : "=&v"(e1) : "v"(v[0]), "v"(v[1]), "v"(sel));
#pragma unroll
  for (int i = 0; i < 16; ++i) {
    if (i == 0) asm volatile("s_nop 1\n\t" MF "%0, %2, %4, 0\n\t" MF "%1, %3, %4, 0" : "=&v"(d1), "=&v"(d2) : "v"(A1), "v"(A2), "v"(v[2]));
    else asm volatile(MF "%0, %2, %4, %0\n\t" MF "%1, %3, %4, %1" : "+v"(d1), "+v"(d2) : "v"(A1), "v"(A2), "v"(v[2 + i]));
  }
  asm volatile("s_nop 7" : "+v"(d1), "+v"(d2), "+v"(e1));
  float o[9];
#pragma unroll
  for (int i = 0; i < 4; ++i) { o[i] = d1[i]; o[4 + i] = d2[i]; }
  {
    auto a02 = __builtin_amdgcn_permlane32_swap(__float_as_uint(e1[0]), __float_as_uint(e1[2]), false, false);
    auto a13 = __builtin_amdgcn_permlane32_swap(__float_as_uint(e1[1]), __float_as_uint(e1[3]), false, false);
    o[8] = m >= 8 ? __uint_as_float(a13[0]) + __uint_as_float(a13[1]) : __uint_as_float(a02[0]) + __uint_as_float(a02[1]);
  }
  rb9(o);
  float x[9];
  up8(px, x); x[8] = lo2f(px9);
#pragma unroll
  for (int i = 0; i < 9; ++i) x[i] += o[i];
  if (L == 0) {
    float y[9], ln[9];
    up8(py, y); y[8] = lo2f(py9);
    rb9(x);
    ln9(x, ln);
    rb9(ln);
#pragma unroll
    for (int i = 0; i < 9; ++i) ln[i] *= y[i];
    rb9(ln);
#pragma unroll
    for (int i = 0; i < 9; ++i) x[i] += ln[i];
    rb9(x);
    st16(X, (unsigned)lane * 16u, sb1 + (P_X3 + (unsigned)(r >> 3)) * PAIR + (unsigned)(r & 7) * 1024u, pk8(x));
    const v4u x9 = gat8(x[8]);
    if ((lane & 7) == 0) st16(X, (unsigned)xp * 16u, sb1 + (P_H + 4u) * PAIR + 4096u + (unsigned)r * 128u, x9);
    ln9(x, ln);
    st16(X, hoff(r, lane), sb1, pk8(ln));
    const v4u h9 = gat8(ln[8]);
    if ((lane & 7) == 0) st16(X, hoff(r, 64 + xp), sb1, h9);
  } else {
    *(v4u*)&p.out[tok * DM + 8 * lane] = pk8(x);
    const v4u o9 = gat8(x[8]);
    if ((lane & 7) == 0) *(v4u*)&p.out[tok * DM + 512 + 8 * xp] = o9;
  }
}

__device__ __forceinline__ void row_role(char* smem) {
  TOK_COMMON();
  const int rw = 4 * xq + (i - 20), r = 2 * rw + (w >> 1);
  const rsrc_t XY = mkr(p.y2, 2u * 2359296u);
  const unsigned long long sm = die_mask(X, SG0, L, lane);
  const unsigned tm = (unsigned)die_mask(X, TP0, L, lane), dm = (unsigned)die_mask(X, 0u, L, lane);
  __attribute__((address_space(3))) unsigned* cnt = (__attribute__((address_space(3))) unsigned*)smem;
  BAR();
  if (tid < 64) cnt[tid] = 0u;
  BAR();
  for (int s = w & 1; s < NSEG; s += 2) {
    int ln_ = lane; asm volatile("" : "+v"(ln_));
    const int m = ln_ & 15, g = ln_ >> 4;
    const unsigned l16 = (unsigned)ln_ * 16u, xo = (unsigned)(8 * (4 * (g & 1) + 2 * (g >> 1) + (m >> 3)) + (m & 7)) * 2u;
    const unsigned sb = SEGB(sm, s), sb1 = sb ^ (L ? 0u : 8192u);
    if (s + ARMK < NSEG) {
      const unsigned sv = opq(SENT), sl = SLOT_AT(s + ARMK) + ((dm >> ((s + ARMK) / 3)) & 1u) * 8192u;
      for (int j = r; j < 82; j += 32) {
#pragma unroll
        for (int k = 0; k < 4; ++k) st16(X, l16 + (unsigned)k * 1024u, sl + (unsigned)(j >> 1) * PAIR + (unsigned)(j & 1) * 4096u, (v4u){sv, sv, sv, sv});
      }
    }
    wait_all<PSL>(X, 0u, sb + P_FB * PAIR + 4096u + (unsigned)(L ? rw : r >> 3) * 128u);
    v4u px, py = {0u, 0u, 0u, 0u};
    unsigned px9, py9 = 0u;
    if (L == 0) {
      const unsigned ro = (unsigned)(s * SEG + r) * (DM * 2u);
      px = ld16(XY, l16, ro + 2359296u); py = ld16(XY, l16, ro);
      px9 = __builtin_amdgcn_raw_buffer_load_b16(XY, 1024u + xo, ro + 2359296u, 16); py9 = __builtin_amdgcn_raw_buffer_load_b16(XY, 1024u + xo, ro, 16);
    } else {
      px = ld16(X, l16, sb + (P_X3 + (unsigned)(r >> 3)) * PAIR + (unsigned)(r & 7) * 1024u);
      px9 = __builtin_amdgcn_raw_buffer_load_b16(X, xo, sb + (P_H + 4u) * PAIR + 4096u + (unsigned)r * 128u, 16);
    }
    if (!__all(ld4(X, (unsigned)(ln_ & 31) * 128u, sb + P_FB * PAIR) != 0u)) {
      if (__builtin_amdgcn_readfirstlane(ld4(X, 0u, sb + P_FA * PAIR + 4096u + (unsigned)r * 128u)) == 0u) {
        wait_all<PSR>(X, 0u, sb + P_FA * PAIR + 4096u + (unsigned)r * 128u);
        __builtin_amdgcn_s_sleep(RDLY);
      }
      wait_all<PSL>(X, (unsigned)(ln_ & 31) * 128u, sb + P_FB * PAIR);
    }
    asm volatile("" ::: "memory");
    TT(s, 0);
    const unsigned j = (unsigned)s & 15u, to = XRL + TP0 * XBLK + (j << 20) + ((tm >> (j >> 1)) & 1u) * 8192u + (unsigned)r * 2u * PAIR;
    const unsigned t9 = (TP0 + 8u) * XBLK + ((tm >> 8) & 1u) * 8192u + (j << 17) + (unsigned)(((r >> 2) & 1) * 512 + (4 * (r >> 3) + (r & 3)) * 32);
    const unsigned v9 = (unsigned)(g * (int)PAIR + (m >> 1) * 1024 + (m & 1) * 16);
    v4u v[18];
#pragma unroll
    for (int k = 0; k < 2; ++k) v[k] = ld16(X, v9, t9 + (unsigned)k * 4u * PAIR);
#pragma unroll
    for (int k = 0; k < 16; ++k) v[2 + k] = ld16(X, l16 + (unsigned)(k & 3) * 1024u, to + (unsigned)(k >> 3) * PAIR + (unsigned)(k & 4) * 1024u);
    SB();
    tok_row(p, X, sb1, L, s, r, ln_, v, px, py, px9, py9);
    VM0();
    unsigned o1 = 0u;
    if (ln_ == 0) o1 = __hip_atomic_fetch_add(cnt + s, 1u, __ATOMIC_RELAXED, __HIP_MEMORY_SCOPE_WORKGROUP);
    if (__builtin_amdgcn_readfirstlane(o1) == 1u && ln_ == 0) st4(X, 0, sb1 + P_FB * PAIR + 4096u + (unsigned)(L * 16 + rw) * 128u, opq(1u));
    TT(s, 1);
  }
}

__device__ __forceinline__ void rows_task(const MP& p, const rsrc_t X, unsigned long long sm, u16* ys, int grp) {
  const int tid = threadIdx.x, lane = tid & 63, w = __builtin_amdgcn_readfirstlane(tid >> 6);
  const rsrc_t XY = mkr(p.y2, 2u * 2359296u);
  const int t0 = grp * 8;
  const bool v2 = lane < 8;
  const unsigned sb = SEGB(sm, grp >> 2);
  v4u yv[3], xa[2][2];
#pragma unroll
  for (int i = 0; i < 3; ++i) { const int d = tid + 256 * i; yv[i] = ld16(mkr(p.yT, DM * S_LEN * 2u), (unsigned)d * (S_LEN * 2u), (unsigned)t0 * 2u); }
#pragma unroll
  for (int rr = 0; rr < 2; ++rr) {
    const long t = t0 + w + 4 * rr;
    xa[rr][0] = *(const v4u*)&p.xin[t * DM + 8 * lane]; xa[rr][1] = *(const v4u*)&p.xin[t * DM + 512 + 8 * (lane & 7)];
  }
#pragma unroll
  for (int i = 0; i < 3; ++i) {
    const int d = tid + 256 * i;
    if (d < DM) {
#pragma unroll
      for (int j = 0; j < 8; ++j) ys[j * (DM + 8) + d] = (u16)((j & 1) ? (yv[i][j >> 1] >> 16) : (yv[i][j >> 1] & 0xFFFFu));
    }
  }
  BAR();
#pragma unroll
  for (int rr = 0; rr < 2; ++rr) {
    const int row = w + 4 * rr, t = t0 + row;
    const v4u y0 = *(const v4u*)&ys[row * (DM + 8) + 8 * lane], y1 = *(const v4u*)&ys[row * (DM + 8) + 512 + 8 * (lane & 7)];
    float x[16], y[16], ln[16];
    up8(xa[rr][0], x); up8(xa[rr][1], x + 8); up8(y0, y); up8(y1, y + 8);
    ln16(x, v2, ln);
    rbN<16>(ln);
#pragma unroll
    for (int i = 0; i < 16; ++i) ln[i] *= y[i];
    rbN<16>(ln);
#pragma unroll
    for (int i = 0; i < 16; ++i) x[i] += ln[i];
    const v4u x10 = pk8(x), x11 = pk8(x + 8);
    up8(x10, x); up8(x11, x + 8);
    ln16(x, v2, ln);
    const unsigned ro = (unsigned)t * (DM * 2u);
    st16(XY, (unsigned)lane * 16u, ro, y0); st16(XY, (unsigned)lane * 16u, ro + 2359296u, x10);
    if (v2) { st16(XY, 1024u + (unsigned)lane * 16u, ro, y1); st16(XY, 1024u + (unsigned)lane * 16u, ro + 2359296u, x11); }
    st16(X, hoff(t & 31, lane), sb, pk8(ln));
    if (v2) st16(X, hoff(t & 31, 64 + lane), sb, pk8(ln + 8));
  }
  VM0();
  BAR();
  if (tid == 0) st4(X, 0, sb + P_FB * PAIR + 4096u + (unsigned)(grp & 3) * 128u, opq(1u));
}

__device__ __forceinline__ void start_conv(char* smem, int d0, int n) {
  const MP p = getp();
  conv_task(p.kvT, p.ssm_k, p.ssm_D, p.yT, (u16*)smem, d0, n);
  VM0();
  BAR();
  if (threadIdx.x < 64) {
    unsigned old = 0u;
    if (threadIdx.x == 0) old = __hip_atomic_fetch_add(p.xb + MF_OFF / 4u, 1u, __ATOMIC_RELAXED, __HIP_MEMORY_SCOPE_AGENT);
    if (__builtin_amdgcn_readfirstlane(old) == 159u && threadIdx.x < 8) st4(mkr(p.xb, XB_BYTES), threadIdx.x * 128u, MF_OFF + 128u, opq(1u));
  }
}
__device__ __forceinline__ void token_start(char* smem) {
  TOK_COMMON();
  (void)w;
  const int k = 4 * i + xq;
  if (L) start_conv(smem, 128 + 4 * k, 4);
  else if (i >= 12 && i < 20) start_conv(smem, 512 + 2 * (k - 48), 2);
  if (i >= (L ? 12 : 20)) {
    const unsigned long long sm = die_mask(X, SG0, 0, lane);
    if (L) { if (tid < 64) wait_all<PSL>(X, 0u, MF_OFF + 128u + (unsigned)(c & 7) * 128u); BAR(); }
    for (int g = L ? k - 32 : k - 80; g < (L ? 256 : 16); g += 48) rows_task(p, X, sm, (u16*)(smem + 150016), g);
  }
}
__device__ __forceinline__ void token_wg(char* smem, v2u cv) {
  if ((unsigned)(size_t)(__attribute__((address_space(3))) char*)smem != 0u) return;
  token_start(smem);
  const int i = ((int)blockIdx.x - NCHW) >> 3;
  if (i < 12) qkv_role(cv);
  else if (i < 20) ap_role(cv);
  else { asm volatile("" :: "v"(cv)); row_role(smem); }
}

__global__ __launch_bounds__(256, 1) void k_main(MP) {
  extern __shared__ __attribute__((aligned(16))) char smem[];
  v2u cv;
  {
    unsigned long long pc; unsigned len;
    asm volatile("s_getpc_b64 %0\nLKBEG:\n\ts_mov_b32 %1, LCHEND-LKBEG" : "=s"(pc), "=s"(len));
    const unsigned o = ((blockIdx.x >> 3) * 32u + (threadIdx.x & 31u)) * 128u;
    cv.x = *(const __attribute__((address_space(1))) unsigned*)(pc + (o < len ? o : 0u));
    const MP p = getp();
    cv.y = ld4(mkr(p.xb, (blockIdx.x >> 3) == 31u ? p.wsb : 0u), threadIdx.x * XBLK + CAL_OFF + 8064u, 0u);
  }
  if (blockIdx.x < NCHW) chain_wg(smem, cv);
  else token_wg(smem, cv);
}

extern "C" void kernel_launch(void* const* d_in, const int* in_sizes, int n_in, void* d_out, int out_size, void* d_ws,
                              size_t ws_size, hipStream_t stream) {
  const u16* A_q = (const u16*)d_in[0];
  const u16* A_k = (const u16*)d_in[1];
  const u16* A_v = (const u16*)d_in[2];
  const u16* A_shift = (const u16*)d_in[3];
  const u16* A_ssmk = (const u16*)d_in[4];
  const u16* A_ssmD = (const u16*)d_in[5];
  const u16* B_mem0 = (const u16*)d_in[7];
  const u16* B_Wq = (const u16*)d_in[8];
  const u16* B_Wk = (const u16*)d_in[9];
  const u16* B_Wv = (const u16*)d_in[10];
  const u16* B_Wo = (const u16*)d_in[11];

  char* ws = (char*)d_ws;
  size_t off = (size_t)(-(uintptr_t)ws) & (XBLK - 1u);
  auto alloc = [&](size_t bytes) { char* q = ws + off; off += (bytes + 4095) & ~(size_t)4095; return q; };
  unsigned* xb = (unsigned*)alloc((size_t)XB_BYTES);
  u16* WP = (u16*)alloc((size_t)3 * HD * DM * 2);
  u16* WOP = (u16*)alloc((size_t)HD * DM * 2);
  u16* WOA = (u16*)alloc((size_t)HD * DM * 2);
  u16* kvT = (u16*)alloc((size_t)DM * S_LEN * 2);
  u16* yT = (u16*)alloc((size_t)DM * S_LEN * 2);
  u16* y2 = (u16*)alloc((size_t)S_LEN * DM * 2);
  (void)alloc((size_t)S_LEN * DM * 2);

  PreP pp;
  pp.kk = A_k; pp.vv = A_v; pp.shift_k = A_shift; pp.kvT = kvT; pp.ssm_k = A_ssmk; pp.ssm_D = A_ssmD; pp.yT = yT;
  pp.Wq = B_Wq; pp.Wk = B_Wk; pp.Wv = B_Wv; pp.Wo = B_Wo; pp.WP = WP; pp.WOP = WOP; pp.WOA = WOA;
  pp.xb = (v4u*)xb; pp.mem0 = B_mem0;
  k_pre<<<NXB + 36 + PRE_KV + PRE_PW + PRE_PO + PRE_FILL, 256, 0, stream>>>(pp);
  MP mp;
  mp.WP = WP; mp.WOP = WOP; mp.WOA = WOA; mp.y2 = y2; mp.out = (u16*)d_out; mp.xb = xb;
  mp.xin = A_q; mp.yT = yT; mp.kvT = kvT; mp.ssm_k = A_ssmk; mp.ssm_D = A_ssmD; mp.wsb = (unsigned)((ws + off) - (char*)xb); mp.pad = 0u;
  const int smem_bytes = 160 * 1024;
  (void)hipFuncSetAttribute((const void*)k_main, hipFuncAttributeMaxDynamicSharedMemorySize, smem_bytes);
  k_main<<<NCHW + NTOKW, 256, smem_bytes, stream>>>(mp);
}
```

```cpp
#include <hip/hip_runtime.h>
#include <hip/hip_bf16.h>
#include <stdint.h>

typedef unsigned short u16;
using bf16x8 = __attribute__((ext_vector_type(8))) short;
using bf16x4 = __attribute__((ext_vector_type(4))) short;
using f32x4 = __attribute__((ext_vector_type(4))) float;
using f32x2 = __attribute__((ext_vector_type(2))) float;
using f32x16 = __attribute__((ext_vector_type(16))) float;
using bfx8 = __attribute__((ext_vector_type(8))) __bf16;
using bfx2 = __attribute__((ext_vector_type(2))) __bf16;
typedef unsigned v4u __attribute__((ext_vector_type(4)));
typedef unsigned v2u __attribute__((ext_vector_type(2)));
typedef unsigned v8u __attribute__((ext_vector_type(8)));
typedef short v4s __attribute__((ext_vector_type(4)));

#define S_LEN 2048
#define DM 576
#define NH 32
#define DH 128
#define HD (NH * DH)
#define MEMR 8
#define SEG 32
#define NSEG 64

__device__ __forceinline__ float bf2f(u16 x) { return __uint_as_float(((unsigned)x) << 16); }
__device__ __forceinline__ u16 f2bf(float f) {
  unsigned u = __float_as_uint(f);
  u += 0x7FFFu + ((u >> 16) & 1u);
  return (u16)(u >> 16);
}
__device__ __forceinline__ float rbf(float f) { return bf2f(f2bf(f)); }
__device__ __forceinline__ unsigned pk2(float a, float b) { f32x2 v = {a, b}; bfx2 r = __builtin_convertvector(v, bfx2); return __builtin_bit_cast(unsigned, r); }
__device__ __forceinline__ float lo2f(unsigned u) { return __uint_as_float(u << 16); }
__device__ __forceinline__ float hi2f(unsigned u) { return __uint_as_float(u & 0xFFFF0000u); }

__device__ __forceinline__ float wave_sum(float v) {
#pragma unroll
  for (int o = 32; o > 0; o >>= 1) v += __shfl_xor(v, o, 64);
  return v;
}

#define NFC 128
#define PRE_KV 288
#define PRE_PW 216
#define PRE_FILL 128
struct PreP {
  const u16* kk; const u16* vv; const u16* shift_k; u16* kvT; const u16* ssm_k; const u16* ssm_D; u16* yT;
  const u16* Wq; const u16* Wk; const u16* Wv; const u16* Wo;
  v4u* xb; const u16* mem0;
};
#define XBLK 2097152u
#define PAIR 16384u
#define SLOT 688128u
#define AGO 589824u
#define AGT 655360u
#define SG0 22
#define TP0 86
#define NXB 95
#define XB_BYTES (NXB * XBLK)
#define SLOT_AT(i) ((unsigned)((i) / 3) * XBLK + (unsigned)((i) % 3) * SLOT)
#define HT_OFF SLOT_AT(65)
#define CTAB_OFF (HT_OFF + 16384u)
#define MF_OFF (HT_OFF + 20480u)
#define TMO_OFF (MF_OFF + 9u * 128u)
#define CVR_OFF (MF_OFF + 16u * 128u)
#define TRC_OFF (HT_OFF + 32768u)
#define CAL_OFF (XBLK - 16384u)
#define P_KV 96u
#define P_H 112u
#define P_X3 117u
#define P_FA 121u
#define P_FB 122u
#define ARMK 8
#define KPM 0
__device__ __forceinline__ void pre_part(const PreP& p, int bid, u16* sh) {
  const int tid = threadIdx.x;
  if (bid < 0) {
    if (KPM & 1) return;
    float (*kx)[17] = (float (*)[17])sh;
    float (*cf)[137] = (float (*)[137])(sh + 128 * 34);
    const int d0 = (-1 - bid) * 16;
    {
      const int s = tid >> 1, d = d0 + (tid & 1) * 8;
      v4u kr[4];
#pragma unroll
      for (int a = 0; a < 4; ++a) kr[a] = (s - 3 + a >= 0) ? *(const v4u*)&p.kk[(long)(s - 3 + a) * DM + d] : (v4u){0, 0, 0, 0};
      const v4u vr = *(const v4u*)&p.vv[(long)s * DM + d];
      const v4u c0 = *(const v4u*)&p.ssm_k[(long)(d0 + (tid >> 4)) * S_LEN + (tid & 15) * 8];
#pragma unroll
      for (int j = 0; j < 8; ++j) {
        const v2u ww = *(const v2u*)&p.shift_k[(d + j) * 4];
        const float w[4] = {lo2f(ww.x), hi2f(ww.x), lo2f(ww.y), hi2f(ww.y)};
        float acc = 0.f;
#pragma unroll
        for (int a = 0; a < 4; ++a) {
          const unsigned u = kr[a][j >> 1];
          acc = rbf(acc + rbf(((j & 1) ? hi2f(u) : lo2f(u)) * w[3 - a]));
        }
        const unsigned u = vr[j >> 1];
        kx[s][(tid & 1) * 8 + j] = rbf(acc * ((j & 1) ? hi2f(u) : lo2f(u)));
        cf[tid >> 4][8 + (tid & 15) * 8 + j] = (j & 1) ? hi2f(c0[j >> 1]) : lo2f(c0[j >> 1]);
      }
      if (tid < 128) cf[tid >> 3][tid & 7] = 0.f;
    }
    __syncthreads();
    const int dd = tid & 15, t0 = (tid >> 4) * 8;
    float y[8], cw[8];
#pragma unroll
    for (int i = 0; i < 8; ++i) { y[i] = 0.f; cw[i] = cf[dd][8 + t0 + i]; }
    for (int j = 0; j < t0 + 8; j += 8) {
      float kk8[8], cn[8];
#pragma unroll
      for (int jj = 0; jj < 8; ++jj) { kk8[jj] = kx[j + jj][dd]; cn[jj] = cf[dd][8 + t0 - j - jj - 1]; }
#pragma unroll
      for (int jj = 0; jj < 8; ++jj) {
#pragma unroll
        for (int i = 0; i < 8; ++i) y[i] = __builtin_fmaf(cw[(i + 8 - jj) & 7], kk8[jj], y[i]);
        cw[(7 - jj) & 7] = cn[jj];
      }
    }
    const float Dv = bf2f(p.ssm_D[d0 + dd]);
    unsigned o[4];
#pragma unroll
    for (int i = 0; i < 4; ++i)
      o[i] = (unsigned)f2bf(rbf(y[2 * i]) + rbf(kx[t0 + 2 * i][dd] * Dv)) | ((unsigned)f2bf(rbf(y[2 * i + 1]) + rbf(kx[t0 + 2 * i + 1][dd] * Dv)) << 16);
#pragma unroll
    for (int i = 0; i < 8; ++i)
      p.yT[(t0 >> 4) * 9216 + ((d0 + dd) >> 2) * 64 + ((((t0 & 15) + i) ^ (((d0 + dd) >> 3) & 15)) << 2) + (dd & 3)] = (u16)((i & 1) ? o[i >> 1] >> 16 : o[i >> 1] & 0xFFFFu);
    return;
  }
  if (bid < PRE_KV) {
    if (KPM & 2) return;
    u16 (*tile)[72] = (u16 (*)[72])sh;
    const int d0 = (bid % 9) * 64, s0 = (bid / 9) * 64;
    const int dq = tid & 7, sr = tid >> 3;
    const int d = d0 + dq * 8;
    float w[8][4];
#pragma unroll
    for (int j = 0; j < 8; ++j) {
      v2u ww = *(const v2u*)&p.shift_k[(d + j) * 4];
      w[j][0] = lo2f(ww.x); w[j][1] = hi2f(ww.x); w[j][2] = lo2f(ww.y); w[j][3] = hi2f(ww.y);
    }
#pragma unroll
    for (int it = 0; it < 2; ++it) {
      const int s = s0 + sr + 32 * it;
      v4u kr[4];
#pragma unroll
      for (int a = 0; a < 4; ++a) kr[a] = (s - 3 + a >= 0) ? *(const v4u*)&p.kk[(long)(s - 3 + a) * DM + d] : (v4u){0, 0, 0, 0};
      v4u vr = *(const v4u*)&p.vv[(long)s * DM + d];
#pragma unroll
      for (int j = 0; j < 8; ++j) {
        float acc = 0.f;
#pragma unroll
        for (int a = 0; a < 4; ++a) {
          unsigned u = kr[a][j >> 1];
          float kx = (j & 1) ? hi2f(u) : lo2f(u);
          acc = rbf(acc + rbf(kx * w[j][3 - a]));
        }
        unsigned u = vr[j >> 1];
        tile[sr + 32 * it][dq * 8 + j] = f2bf(acc * ((j & 1) ? hi2f(u) : lo2f(u)));
      }
    }
    __syncthreads();
    {
      const int dd = tid >> 2, sq = (tid & 3) * 16;
      unsigned o[8];
#pragma unroll
      for (int i = 0; i < 8; ++i) o[i] = (unsigned)tile[sq + 2 * i][dd] | ((unsigned)tile[sq + 2 * i + 1][dd] << 16);
      *(v4u*)&p.kvT[(long)(d0 + dd) * S_LEN + s0 + sq] = (v4u){o[0], o[1], o[2], o[3]};
      *(v4u*)&p.kvT[(long)(d0 + dd) * S_LEN + s0 + sq + 8] = (v4u){o[4], o[5], o[6], o[7]};
    }
    return;
  }
  bid -= PRE_KV;
  if (bid < PRE_PW) {
    if (KPM & 4) return;
    const int x = blockIdx.x & 7, k = bid >> 3;
    unsigned acc = 0u;
    if (k < 8) {
      unsigned long long pc; unsigned l0, l1, l2, l3; int rel;
      asm volatile("s_getpc_b64 %0\nLPRE:\n\ts_mov_b32 %1, LKBEG-LPRE\n\ts_mov_b32 %2, LCHEND-LKBEG\n\ts_mov_b32 %3, LQVEND-LKBEG\n\ts_mov_b32 %4, LQKEND-LKBEG\n\ts_mov_b32 %5, LAPEND-LKBEG"
                   : "=s"(pc), "=s"(rel), "=s"(l0), "=s"(l1), "=s"(l2), "=s"(l3));
      l0 = l0 > l1 ? l0 : l1; l2 = l2 > l3 ? l2 : l3; l0 = l0 > l2 ? l0 : l2;
      const unsigned o = (unsigned)(k * 256 + tid) * 64u;
      acc = *(const __attribute__((address_space(1))) unsigned*)(pc + rel + (o < l0 ? o : 0u));
    }
#pragma unroll
    for (int i = 0; i < 8; ++i) {
      const int q = (k * 8 + i) * 256 + tid;
      if (x < 4) { const int row = q >> 5; if (row < 3 * DM) acc += *(const unsigned*)((row < DM ? p.Wq : (row < 2 * DM ? p.Wk - (long)DM * HD : p.Wv - 2L * DM * HD)) + (long)row * HD + x * 1024 + (q & 31) * 32); }
      else if (q < 18432) acc += *(const unsigned*)(p.Wo + ((long)(x - 4) * 18432 + q) * 32);
    }
    asm volatile("" :: "v"(acc));
    return;
  }
  bid -= PRE_PW;
  {
    const v4u ones = {0xFFFFFFFFu, 0xFFFFFFFFu, 0xFFFFFFFFu, 0xFFFFFFFFu}, zero = {0, 0, 0, 0};
    const __amdgpu_buffer_rsrc_t rx = __builtin_amdgcn_make_buffer_rsrc((void*)p.xb, 0, XB_BYTES, 0x00020000);
    if (bid >= PRE_FILL) {
      if (tid >= 64 || (KPM & 16)) return;
      const unsigned cb = (unsigned)(bid - PRE_FILL) * XBLK + CAL_OFF;
      __builtin_amdgcn_raw_buffer_store_b128(zero, rx, (unsigned)(tid & 1) * 8192u + (unsigned)(tid >> 1) * 256u, cb, 16);
      asm volatile("s_waitcnt vmcnt(0)" ::: "memory");
      unsigned mn[2] = {~0u, ~0u};
      for (int i = 0; i < 14; ++i) {
        const unsigned long long c0 = __builtin_amdgcn_s_memtime();
        asm volatile("s_waitcnt lgkmcnt(0)" ::: "memory");
        const unsigned v = __builtin_amdgcn_raw_buffer_load_b32(rx, 0, cb + (unsigned)(i & 1) * 8192u + (unsigned)(i >> 1) * 256u, 16);
        asm volatile("s_waitcnt vmcnt(0)" :: "v"(v) : "memory");
        const unsigned d = (unsigned)(__builtin_amdgcn_s_memtime() - c0);
        if (i & 1) mn[1] = d < mn[1] ? d : mn[1]; else mn[0] = d < mn[0] ? d : mn[0];
      }
      unsigned xcc; asm volatile("s_getreg_b32 %0, hwreg(HW_REG_XCC_ID)" : "=s"(xcc));
      if (tid == 0) __builtin_amdgcn_raw_buffer_store_b32((mn[1] < mn[0] ? 1u : 0u) ^ ((xcc >> 2) & 1u), rx, 0, CTAB_OFF + (unsigned)(bid - PRE_FILL) * 4u, 16);
      return;
    }
#define NARM (ARMK - 1u)
    if (KPM & 32) return;
    for (unsigned i = bid * 256 + tid; i < 64u * 2048u + 768u; i += PRE_FILL * 256)
      __builtin_amdgcn_raw_buffer_store_b128(zero, rx, i < 131072u ? (SG0 + (i >> 11)) * XBLK + P_FA * PAIR + (i & 2047u) * 16u : MF_OFF + (i & 1023u) * 16u, 0, 16);
    for (unsigned i = bid * 256 + tid; i < (SLOT - PAIR - AGO) / 16u; i += PRE_FILL * 256) {
      const unsigned o = AGO + i * 16u, f = o < AGT ? (o & 8191u) : 8192u + (o & 8191u) % 1152u, s = f >> 10, r = f & 1023u;
      v4u val = ones;
      if (s < 9u) {
        const unsigned gm = s < 8u ? r >> 8 : (r >> 6) & 3u, m3 = (r >> 4) & 3u, ga = s < 8u ? (r >> 6) & 3u : r >> 8, row = 4u * (gm & 1u) + 2u * (gm >> 1) + (m3 & 1u), half = m3 >> 1;
        const unsigned* src = (const unsigned*)(p.mem0 + row * DM + 64u * s + 16u * ga + 2u * half);
        val = (v4u){src[0], src[2], src[4], src[6]};
      }
      __builtin_amdgcn_raw_buffer_store_b128(val, rx, o, 0, 16);
    }
    constexpr unsigned S16 = (SLOT - PAIR) / 16u;
    for (unsigned i = bid * 256 + tid; i < NARM * S16; i += PRE_FILL * 256) __builtin_amdgcn_raw_buffer_store_b128(ones, rx, SLOT_AT(1u + i / S16) + (i % S16) * 16u, 0, 16);
  }
}

__global__ __launch_bounds__(256, 4) void k_pre(PreP p) {
  __shared__ __attribute__((aligned(16))) u16 sh[128 * 34 + 16 * 137 * 2];
  const int b = blockIdx.x;
  pre_part(p, b < NXB ? PRE_KV + PRE_PW + PRE_FILL + b : b - NXB - 36, sh);
}

#define CREV_STRIDE 2208
#define SP(k)
#define LWAIT() asm volatile("s_waitcnt lgkmcnt(0)" ::: "memory")
__device__ __forceinline__ void conv_task(const u16* __restrict__ kvT, const u16* __restrict__ ssm_k, const u16* __restrict__ ssm_D, u16* yT, u16* shb, int d) {
  const int lane = threadIdx.x & 63;
  u16* crev = shb + 64;
  u16* kvs = crev + 4 * CREV_STRIDE;
  const int X0 = 2048 + 63;
  SP(0);
  v4u cv[4], kq[4];
#pragma unroll
  for (int j = 0; j < 4; ++j) { cv[j] = *(const v4u*)&ssm_k[(long)d * S_LEN + lane * 32 + 8 * j]; kq[j] = *(const v4u*)&kvT[(long)d * S_LEN + (j * 64 + lane) * 8]; }
  const float Dv = bf2f(ssm_D[d]);
  const v4u z4 = {0u, 0u, 0u, 0u};
  *(v4u*)&kvs[(lane >> 2) * 40 + (lane & 3) * 8] = z4;
  if (lane < 32) *(v4u*)&crev[(lane >> 3) * CREV_STRIDE + (lane & 7) * 8] = z4;
  if (lane < 36) *(v4u*)&crev[(lane / 9) * CREV_STRIDE + 2112 + (lane % 9) * 8] = z4;
  {
    unsigned r[18];
#pragma unroll
    for (int k = 0; k < 16; ++k) { const unsigned u = cv[3 - (k >> 2)][3 - (k & 3)]; r[k] = __builtin_amdgcn_alignbyte(u, u, 2); }
#pragma unroll
    for (int k = 0; k < 2; ++k) r[16 + k] = (unsigned)__builtin_amdgcn_update_dpp(0, (int)r[k], 0x138, 0xf, 0xf, true);
    LWAIT();
#pragma unroll
    for (int rho = 0; rho < 4; ++rho)
#pragma unroll
      for (int j = 0; j < 4; ++j) {
        v4u o;
#pragma unroll
        for (int k = 0; k < 4; ++k) { const int q = 4 * j + k + (rho >> 1); o[k] = (rho & 1) ? __builtin_amdgcn_alignbyte(r[q + 1], r[q], 2) : r[q]; }
        *(v4u*)&crev[rho * CREV_STRIDE + 2080 - 32 * lane + 8 * j] = o;
      }
    if (lane == 63) {
      *(v2u*)&crev[1 * CREV_STRIDE + 60] = (v2u){0u, r[0] << 16};
      *(v2u*)&crev[2 * CREV_STRIDE + 60] = (v2u){0u, r[0]};
      *(v2u*)&crev[3 * CREV_STRIDE + 60] = (v2u){r[0] << 16, __builtin_amdgcn_alignbyte(r[1], r[0], 2)};
    }
#pragma unroll
    for (int j = 0; j < 4; ++j) *(v4u*)&kvs[(16 + j * 16 + (lane >> 2)) * 40 + (lane & 3) * 8] = kq[j];
  }
  LWAIT();
  SP(1);
  const int n = lane & 15, g = lane >> 4;
  const int rho = (X0 - n) & 3;
  f32x4 acc[4][2];
#pragma unroll
  for (int q = 0; q < 8; ++q) acc[q >> 1][q & 1] = (f32x4){0.f, 0.f, 0.f, 0.f};
  {
    typedef __attribute__((address_space(3))) const v2u* l2p;
    typedef __attribute__((address_space(3))) const bf16x8* l4p;
    unsigned a0 = (unsigned)(size_t)(__attribute__((address_space(3))) u16*)&crev[rho * CREV_STRIDE + (X0 - (n - 8 * g) - rho)] - 3232u, a1 = a0 + 8u;
    unsigned b0 = (unsigned)(size_t)(__attribute__((address_space(3))) u16*)&kvs[(14 + n) * 40 + 8 * g];
    asm volatile("" : "+v"(a1));
#define AFR(m, f) ({ const unsigned o_ = 3232u - 64u * (m) - 1024u * ((f) >> 1) - 32u * ((f) & 1); const v2u lo_ = *(l2p)(size_t)(a0 + o_), hi_ = *(l2p)(size_t)(a1 + o_); \
                     __builtin_bit_cast(bf16x8, (v4u){lo_.x, lo_.y, hi_.x, hi_.y}); })
#define BFR(m, j) (*(l4p)(size_t)(b0 + 80u * (2 - (m)) + 1280u * (j)))
    bf16x8 af[2][8], bf[2][4];
#pragma unroll
    for (int f = 0; f < 8; ++f) { af[0][f] = AFR(0, f); af[1][f] = af[0][f]; __builtin_amdgcn_sched_barrier(0); }
#pragma unroll
    for (int j = 0; j < 4; ++j) { bf[0][j] = BFR(0, j); bf[1][j] = bf[0][j]; }
#pragma unroll 1
    for (int mo = 0; mo < 8; ++mo) {
#pragma unroll
      for (int m = 0; m < 2; ++m) {
        int k = 0;
#pragma unroll
        for (int a = 0; a < 4; ++a)
#pragma unroll
          for (int ct = a; ct < 4; ++ct)
#pragma unroll
            for (int ti = 0; ti < 2; ++ti) {
              acc[ct][ti] = __builtin_amdgcn_mfma_f32_16x16x32_bf16(__builtin_bit_cast(bfx8, af[m][2 * a + ti]), __builtin_bit_cast(bfx8, bf[m][ct - a]), acc[ct][ti], 0, 0, 0);
              if (k < 2) af[m ^ 1][k] = AFR(m + 1, k);
              else if (k < 6) bf[m ^ 1][k - 2] = BFR(m + 1, k - 2);
              else if (k < 12) af[m ^ 1][k - 4] = AFR(m + 1, k - 4);
              __builtin_amdgcn_sched_barrier(0);
              ++k;
            }
      }
      a0 -= 128u; a1 -= 128u; b0 -= 160u;
    }
  }
  SP(2);
  u16* yo = crev;
  LWAIT();
#pragma unroll
  for (int q = 0; q < 8; ++q) {
    const int o = (16 * (q >> 1) + n) * 40 + 16 * (q & 1) + 4 * g;
    const v2u kx = *(const v2u*)&kvs[16 * 40 + o];
    const f32x4 y = acc[q >> 1][q & 1];
    const unsigned y0 = pk2(y[0], y[1]), y1 = pk2(y[2], y[3]), r0 = pk2(lo2f(kx.x) * Dv, hi2f(kx.x) * Dv), r1 = pk2(lo2f(kx.y) * Dv, hi2f(kx.y) * Dv);
    *(v2u*)&yo[o] = (v2u){pk2(lo2f(y0) + lo2f(r0), hi2f(y0) + hi2f(r0)), pk2(lo2f(y1) + lo2f(r1), hi2f(y1) + hi2f(r1))};
  }
  LWAIT();
  __builtin_amdgcn_s_barrier();
  asm volatile("" ::: "memory");
  {
    const int tid = threadIdx.x, f = (d >> 3) & 15;
    const u16* y0 = shb - (tid >> 6) * 16384 + 64;
#pragma unroll
    for (int k = 0; k < 4; ++k) {
      const int tile = 32 * k + (tid >> 3), t = 16 * tile + 2 * ((tid & 7) ^ (f >> 1));
      unsigned u[4];
#pragma unroll
      for (int c = 0; c < 4; ++c) u[c] = *(const unsigned*)&y0[c * 16384 + (t >> 5) * 40 + (t & 31)];
      const v2u e = {__builtin_amdgcn_perm(u[1], u[0], 0x05040100u), __builtin_amdgcn_perm(u[3], u[2], 0x05040100u)}, o = {__builtin_amdgcn_perm(u[1], u[0], 0x07060302u), __builtin_amdgcn_perm(u[3], u[2], 0x07060302u)};
      if (tile >= NFC / 16) __builtin_amdgcn_raw_buffer_store_b128((f & 1) ? (v4u){o.x, o.y, e.x, e.y} : (v4u){e.x, e.y, o.x, o.y}, __builtin_amdgcn_make_buffer_rsrc((void*)yT, 0, DM * S_LEN * 2, 0x00020000),
                                                                   tile * 18432 + (tid & 7) * 16, (d >> 2) * 128, 16);
    }
  }
}

#define NCHW 64
#define NTOKW 192
#define PSL2 10
#define PSL 4
#define SENT 0xFFFFFFFFu
#define SPIN_CAP 300000u
struct MP {
  const u16* Wq; const u16* Wk; const u16* Wv; const u16* Wo; u16* out; unsigned* xb;
  const u16* xin; u16* yT; const u16* kvT; const u16* ssm_k; const u16* ssm_D; unsigned wsb; unsigned pad;
};

__device__ __forceinline__ MP getp() {
  MP r;
#if defined(__HIP_DEVICE_COMPILE__)
  const __attribute__((address_space(4))) unsigned long long* k = (const __attribute__((address_space(4))) unsigned long long*)__builtin_amdgcn_kernarg_segment_ptr();
  asm volatile("" : "+s"(k));
#define GP(T, i) ((T*)(__attribute__((address_space(1))) T*)k[i])
  r.Wq = GP(const u16, 0);
  r.Wk = GP(const u16, 1);
  r.Wv = GP(const u16, 2);
  r.Wo = GP(const u16, 3);
  r.out = GP(u16, 4);
  r.xb = GP(unsigned, 5);
  r.xin = GP(const u16, 6);
  r.yT = GP(u16, 7);
  r.kvT = GP(const u16, 8);
  r.ssm_k = GP(const u16, 9);
  r.ssm_D = GP(const u16, 10);
  r.wsb = (unsigned)k[11]; r.pad = 0u;
#endif
  return r;
}
typedef __amdgpu_buffer_rsrc_t rsrc_t;
__device__ __forceinline__ rsrc_t mkr(const void* p, unsigned bytes) { return __builtin_amdgcn_make_buffer_rsrc((void*)p, 0, bytes, 0x00020000); }
__device__ __forceinline__ void st16(rsrc_t r, unsigned vo, unsigned so, v4u v) { __builtin_amdgcn_raw_buffer_store_b128(v, r, vo, so, 16); }
__device__ __forceinline__ void st8(rsrc_t r, unsigned vo, unsigned so, v2u v) { __builtin_amdgcn_raw_buffer_store_b64(v, r, vo, so, 16); }
__device__ __forceinline__ void st4(rsrc_t r, unsigned vo, unsigned so, unsigned v) { __builtin_amdgcn_raw_buffer_store_b32(v, r, vo, so, 16); }
__device__ __forceinline__ v4u ld16(rsrc_t r, unsigned vo, unsigned so) { return __builtin_amdgcn_raw_buffer_load_b128(r, vo, so, 16); }
__device__ __forceinline__ v2u ld8(rsrc_t r, unsigned vo, unsigned so) { return __builtin_amdgcn_raw_buffer_load_b64(r, vo, so, 16); }
__device__ __forceinline__ unsigned ld4(rsrc_t r, unsigned vo, unsigned so) { return __builtin_amdgcn_raw_buffer_load_b32(r, vo, so, 16); }
__device__ __forceinline__ float sum4g(float v) {
  auto r = __builtin_amdgcn_permlane16_swap(__float_as_uint(v), __float_as_uint(v), false, false);
  v = __uint_as_float(r[0]) + __uint_as_float(r[1]);
  auto q = __builtin_amdgcn_permlane32_swap(__float_as_uint(v), __float_as_uint(v), false, false);
  return __uint_as_float(q[0]) + __uint_as_float(q[1]);
}
__device__ __forceinline__ unsigned opq(unsigned x) { asm volatile("" : "+v"(x)); return x; }

__device__ __forceinline__ unsigned long long die_mask(rsrc_t X, unsigned first, int L, int lane) { return __ballot((ld4(X, (unsigned)lane * 4u, CTAB_OFF + first * 4u) & 1u) != (unsigned)L); }
#define SEGB(m, s) ((unsigned)(SG0 + (s)) * XBLK + (unsigned)(((m) >> (s)) & 1ull) * 8192u)
__device__ __forceinline__ bool spin_fail(unsigned& spins, rsrc_t F) {
  ++spins;
  if (spins > SPIN_CAP) { unsigned one = 0x40000000u | (blockIdx.x << 8) | threadIdx.x; asm volatile("" : "+v"(one)); if (__builtin_amdgcn_readfirstlane(ld4(F, 0, TMO_OFF)) == 0u) st4(F, 0, TMO_OFF, one); return true; }
  if ((spins & 1023u) == 0u) return __builtin_amdgcn_readfirstlane(ld4(F, 0, TMO_OFF)) != 0;
  return false;
}
__device__ __forceinline__ unsigned rtc() { return (unsigned)__builtin_amdgcn_s_memrealtime(); }
__device__ __forceinline__ unsigned wait_until(unsigned T) { unsigned n = rtc(); for (int i = 0; i < 8192 && (int)(n - T) < 0; ++i) n = rtc(); return n; }
__device__ __forceinline__ unsigned umax3(unsigned a, unsigned b, unsigned c) { unsigned m = a > b ? a : b; return m > c ? m : c; }
__device__ __forceinline__ float fmx(float a, float b) { float d; asm("v_max_f32 %0, %1, %2\n\ts_nop 1" : "=v"(d) : "v"(a), "v"(b)); return d; }
__device__ __forceinline__ float fmx3(float a, float b, float c) { float d; asm("v_max3_f32 %0, %1, %2, %3" : "=v"(d) : "v"(a), "v"(b), "v"(c)); return d; }
__device__ __forceinline__ float max4gx(float v) {
  auto r = __builtin_amdgcn_permlane16_swap(__float_as_uint(v), __float_as_uint(v), false, false);
  v = fmx(__uint_as_float(r[0]), __uint_as_float(r[1]));
  auto q = __builtin_amdgcn_permlane32_swap(__float_as_uint(v), __float_as_uint(v), false, false);
  return fmx(__uint_as_float(q[0]), __uint_as_float(q[1]));
}
__device__ __forceinline__ float ror8(float v) { return __uint_as_float((unsigned)__builtin_amdgcn_update_dpp(0, (int)__float_as_uint(v), 0x128, 0xf, 0xf, false)); }
__device__ __forceinline__ unsigned ror8u(unsigned v) { return (unsigned)__builtin_amdgcn_update_dpp(0, (int)v, 0x128, 0xf, 0xf, false); }
__device__ __forceinline__ unsigned wave_umax(unsigned v, int lane) {
#pragma unroll
  for (int o = 32; o > 0; o >>= 1) { unsigned x = (unsigned)__builtin_amdgcn_ds_bpermute((lane ^ o) << 2, (int)v); v = x > v ? x : v; }
  return v;
}
#define BAR() do { asm volatile("s_waitcnt lgkmcnt(0)" ::: "memory"); __builtin_amdgcn_s_barrier(); asm volatile("" ::: "memory"); } while (0)
#define SB() __builtin_amdgcn_sched_barrier(0)
#define MF "v_mfma_f32_16x16x32_bf16 "
#define C2X4(d0, d1, a0, a1, b) asm volatile( \
    MF "%0, %2, %10, 0\n\t" MF "%1, %6, %10, 0\n\t" MF "%0, %3, %11, %0\n\t" MF "%1, %7, %11, %1\n\t" \
    MF "%0, %4, %12, %0\n\t" MF "%1, %8, %12, %1\n\t" MF "%0, %5, %13, %0\n\t" MF "%1, %9, %13, %1" \
    : "=&v"(d0), "=&v"(d1) : "v"((a0)[0]), "v"((a0)[1]), "v"((a0)[2]), "v"((a0)[3]), "v"((a1)[0]), "v"((a1)[1]), "v"((a1)[2]), "v"((a1)[3]), \
      "v"((b)[0]), "v"((b)[1]), "v"((b)[2]), "v"((b)[3]))
#define C1X4(d0, a0, b) asm volatile( \
    MF "%0, %1, %5, 0\n\t" MF "%0, %2, %6, %0\n\t" MF "%0, %3, %7, %0\n\t" MF "%0, %4, %8, %0" \
    : "=&v"(d0) : "v"((a0)[0]), "v"((a0)[1]), "v"((a0)[2]), "v"((a0)[3]), "v"((b)[0]), "v"((b)[1]), "v"((b)[2]), "v"((b)[3]))
#define VM0() __builtin_amdgcn_s_waitcnt(0x0F70)
#define DL1 22
#define SCH_B0 54
#define DLL1 6
#define DLL2 4
#define DLF 24
#define SCH_PEN 0
#define DLX 4
#define LATE 10
#define TKL 28
#define UP 1
#define DN 100
#define RSL 2
#define CH_S2(x) #x
#define CH_S(x) CH_S2(x)
#define CH_ST "s_memtime s[62:63]\n\ts_waitcnt lgkmcnt(0)\n\ts_sub_u32 s67, s62, s67\n\t"
#define CH_BASE "s_memtime s[94:95]\n\ts_waitcnt lgkmcnt(0)\n\ts_mov_b32 s67, s94\n\t"
#define CH_DPC
#define CH_TOKCHK "s_cbranch_vccz LG3_%=\n\t"
#define CH_SEGC
#define CH_SG0 CH_S(SG0)
#define CH_ARMD "%[armd]"
#define CH_ARMS "%[arms]"
#define CH_SLOT "688128"
#define CH_SKIP "32768"
#define CH_DL1 CH_S(DL1)
#define CH_DLL1 CH_S(DLL1)
#define CH_DLL2 CH_S(DLL2)
#define CH_DLF CH_S(DLF)
#define CH_B0 CH_S(SCH_B0)
#define CH_PEN CH_S(SCH_PEN)
#define CH_RSL CH_S(RSL)
#define CH_UP CH_S(UP)
#define CH_DN CH_S(DN)
#define CH_DLX CH_S(DLX)
#define CH_LATE CH_S(LATE)
#define CH_DL1L "%[dl1l]"
#define CH_TKL CH_S(TKL)
#define CH_SCHW "656384"
#define CH_CAP "%[cap]"
#define CH_INW "s_sub_u32 s93, s48, 10\n\ts_cmp_lt_u32 s93, 50\n\t"
#define CH_ST1
#define CH_ST2
#define TOUCH 2
#define FDL 6
#define CH_FDL CH_S(FDL)
#define CH_TC0
#define CH_TCH
#define CH_ST3
#define CH_ST4
#define CH_TRC
#define CH_STW
#define CH_DRY "s_branch LLOAD_%=\n\t"
#define RFL(x) ((unsigned)__builtin_amdgcn_readfirstlane((int)(x)))
#define CH_DP0
#define CH_DP1
#define CH_DP2
#define CH_DP3
#define CH_DP4
#define CH_DP5
#define CH_DP6
#define CH_DP7
#define CH_DP8
#define CH_DP9
#define CH_DP10
#define CH_DP11
#define CH_DP12
#define CH_DP13
#define CH_DP14
#define CH_DP15
#define CH_DP16
#define CH_DP17
#define CH_OPS {1, 2, 3, 4, 5, 6, 7, 8, 17, 18, 20, 22, 23, 24, 64, 65, 67, 68, 69, 70, 71, 80, 81, 82, 83, 84, 85, 86, 87, 88, 128, 129, 130, 131, 132, 133, 134, 135, 136, 144, 145, 146, 147, 149, 150, 151, 195, 209, 210, 211, 227, 241, 242, 256, 0, 16, 19, 21, 66, 72, 148, 152, 208, 240, 192, 224, 193, 225, 194, 226, 243, 257}
#define CH_ASM \
  "s_mov_b32 s84, " CH_CAP "\n\t" \
  "s_mov_b32 s83, " CH_CAP "\n\t" \
  "s_mov_b32 s68, 0\n\t" \
  "s_mov_b32 s74, 0\n\t" \
  "s_mov_b32 s76, 0\n\t" \
  "s_mov_b32 s77, 0\n\t" \
  "s_mov_b32 s78, 0\n\t" \
  "s_mov_b32 s79, 0\n\t" \
  "s_mov_b32 s56, 0xcccccccc\n\t" \
  "s_mov_b32 s57, 0xcccccccc\n\t" \
  "s_mov_b32 s58, 0x33333333\n\t" \
  "s_mov_b32 s59, 0x33333333\n\t" \
  "s_mov_b32 s88, 0x3f803f80\n\t" \
  "s_mov_b32 s89, 0x3f803f80\n\t" \
  "s_mov_b32 s66, s65\n\t" \
  "s_mov_b32 s47, s65\n\t" \
  "s_mov_b32 s72, 0\n\t" \
  "s_mov_b32 s73, 0\n\t" \
  "s_mov_b32 s67, 0\n\t" \
  "s_mul_i32 s60, s40, 36864\n\t" \
  "s_add_u32 s60, s60, 16384\n\t" \
  "v_add_u32 v70, s60, v69\n\t" \
  "v_bfe_i32 v68, v69, 5, 1\n\t" \
  "v_and_b32 v68, 0xaaaaaaaa, v68\n\t" \
  "v_or_b32 v68, 0x44444444, v68\n\t" \
  "s_mov_b32 s35, s30\n\t" \
  "s_mov_b32 s30, 0\n\t" \
  "s_mov_b32 s34, 0\n\t" \
  CH_DRY \
  "s_cmp_eq_u32 s40, 0\n\t" \
  "s_cbranch_scc1 LW0_%=\n\t" \
  "s_barrier\n\t" \
  "s_cmp_eq_u32 s40, 1\n\t" \
  "s_cbranch_scc1 LG1_%=\n\t" \
  "s_cmp_eq_u32 s40, 2\n\t" \
  "s_cbranch_scc1 LG2_%=\n\t" \
  "s_branch LG3_%=\n\t" \
  "LLOAD_%=:\n\t" \
  "s_waitcnt vmcnt(0) lgkmcnt(0)\n\t" \
  "s_mov_b32 s30, s35\n\t" \
  "s_movk_i32 s34, 1000\n\t" \
  "v_bfe_u32 v0, v69, 8, 1\n\t" \
  "v_bfe_u32 v1, v69, 9, 1\n\t" \
  "v_lshlrev_b32 v0, 2, v0\n\t" \
  "v_add_u32 v0, v0, v1\n\t" \
  "v_mul_u32_u24 v0, 4608, v0\n\t" \
  "v_lshrrev_b32 v1, 1, v69\n\t" \
  "v_and_b32 v1, 0x60, v1\n\t" \
  "v_and_b32 v2, 16, v69\n\t" \
  "v_bfe_u32 v3, v69, 5, 1\n\t" \
  "v_add3_u32 v64, v0, v1, v2\n\t" \
  "v_lshl_add_u32 v64, v3, 2, v64\n\t" \
  "v_lshl_add_u32 v67, v3, 3, v0\n\t" \
  "v_bfe_u32 v1, v69, 7, 1\n\t" \
  "v_lshl_add_u32 v67, v1, 2, v67\n\t" \
  "v_bfe_u32 v1, v69, 6, 1\n\t" \
  "v_lshl_add_u32 v67, v1, 4, v67\n\t" \
  "v_bfe_u32 v1, v69, 4, 1\n\t" \
  "v_lshl_add_u32 v67, v1, 1, v67\n\t" \
  "v_lshrrev_b32 v71, 6, v69\n\t" \
  "v_lshlrev_b32 v71, 13, v71\n\t" \
  "v_and_b32 v1, 48, v69\n\t" \
  "v_add_u32 v71, v71, v1\n\t" \
  "v_and_b32 v1, 0x300, v69\n\t" \
  "v_add_u32 v65, v70, v1\n\t" \
  "v_and_b32 v1, 48, v69\n\t" \
  "v_lshrrev_b32 v1, 1, v1\n\t" \
  "v_sub_u32 v66, v65, v1\n\t" \
  "s_mul_i32 s54, s40, 36864\n\t" \
  "s_add_u32 s54, s54, 16384\n\t" \
  "s_lshl_b32 s55, s40, 7\n\t" \
  "s_lshl_b32 s75, s40, 5\n\t" \
  "s_add_u32 s75, s75, 1024\n\t" \
  "s_mov_b32 s62, 0x05040100\n\t" \
  "s_mov_b32 s63, 0x07060302\n\t" \
  "s_add_u32 m0, s54, 0\n\t" \
  "s_mov_b32 s60, 0\n\t" \
  "buffer_load_dwordx4 v71, %[Wq], s60 offen lds\n\t" \
  "s_add_u32 m0, s54, 1024\n\t" \
  "s_mov_b32 s60, 131072\n\t" \
  "buffer_load_dwordx4 v71, %[Wq], s60 offen lds\n\t" \
  "s_add_u32 m0, s54, 2048\n\t" \
  "s_mov_b32 s60, 262144\n\t" \
  "buffer_load_dwordx4 v71, %[Wq], s60 offen lds\n\t" \
  "s_add_u32 m0, s54, 3072\n\t" \
  "s_mov_b32 s60, 393216\n\t" \
  "buffer_load_dwordx4 v71, %[Wq], s60 offen lds\n\t" \
  "s_add_u32 m0, s54, 4096\n\t" \
  "s_mov_b32 s60, 0\n\t" \
  "buffer_load_dwordx4 v71, %[Wk], s60 offen lds\n\t" \
  "s_add_u32 m0, s54, 5120\n\t" \
  "s_mov_b32 s60, 131072\n\t" \
  "buffer_load_dwordx4 v71, %[Wk], s60 offen lds\n\t" \
  "s_add_u32 m0, s54, 6144\n\t" \
  "s_mov_b32 s60, 262144\n\t" \
  "buffer_load_dwordx4 v71, %[Wk], s60 offen lds\n\t" \
  "s_add_u32 m0, s54, 7168\n\t" \
  "s_mov_b32 s60, 393216\n\t" \
  "buffer_load_dwordx4 v71, %[Wk], s60 offen lds\n\t" \
  "s_add_u32 m0, s54, 8192\n\t" \
  "s_mov_b32 s60, 0\n\t" \
  "buffer_load_dwordx4 v71, %[Wv], s60 offen lds\n\t" \
  "s_add_u32 m0, s54, 9216\n\t" \
  "s_mov_b32 s60, 131072\n\t" \
  "buffer_load_dwordx4 v71, %[Wv], s60 offen lds\n\t" \
  "s_add_u32 m0, s54, 10240\n\t" \
  "s_mov_b32 s60, 262144\n\t" \
  "buffer_load_dwordx4 v71, %[Wv], s60 offen lds\n\t" \
  "s_add_u32 m0, s54, 11264\n\t" \
  "s_mov_b32 s60, 393216\n\t" \
  "buffer_load_dwordx4 v71, %[Wv], s60 offen lds\n\t" \
  "s_add_u32 m0, s54, 12288\n\t" \
  "s_mov_b32 s60, 524288\n\t" \
  "buffer_load_dwordx4 v71, %[Wq], s60 offen lds\n\t" \
  "s_add_u32 m0, s54, 13312\n\t" \
  "s_mov_b32 s60, 655360\n\t" \
  "buffer_load_dwordx4 v71, %[Wq], s60 offen lds\n\t" \
  "s_add_u32 m0, s54, 14336\n\t" \
  "s_mov_b32 s60, 786432\n\t" \
  "buffer_load_dwordx4 v71, %[Wq], s60 offen lds\n\t" \
  "s_add_u32 m0, s54, 15360\n\t" \
  "s_mov_b32 s60, 917504\n\t" \
  "buffer_load_dwordx4 v71, %[Wq], s60 offen lds\n\t" \
  "s_add_u32 m0, s54, 16384\n\t" \
  "s_mov_b32 s60, 524288\n\t" \
  "buffer_load_dwordx4 v71, %[Wk], s60 offen lds\n\t" \
  "s_add_u32 m0, s54, 17408\n\t" \
  "s_mov_b32 s60, 655360\n\t" \
  "buffer_load_dwordx4 v71, %[Wk], s60 offen lds\n\t" \
  "s_add_u32 m0, s54, 18432\n\t" \
  "s_mov_b32 s60, 786432\n\t" \
  "buffer_load_dwordx4 v71, %[Wk], s60 offen lds\n\t" \
  "s_add_u32 m0, s54, 19456\n\t" \
  "s_mov_b32 s60, 917504\n\t" \
  "buffer_load_dwordx4 v71, %[Wk], s60 offen lds\n\t" \
  "s_add_u32 m0, s54, 20480\n\t" \
  "s_mov_b32 s60, 524288\n\t" \
  "buffer_load_dwordx4 v71, %[Wv], s60 offen lds\n\t" \
  "s_add_u32 m0, s54, 21504\n\t" \
  "s_mov_b32 s60, 655360\n\t" \
  "buffer_load_dwordx4 v71, %[Wv], s60 offen lds\n\t" \
  "s_add_u32 m0, s54, 22528\n\t" \
  "s_mov_b32 s60, 786432\n\t" \
  "buffer_load_dwordx4 v71, %[Wv], s60 offen lds\n\t" \
  "s_add_u32 m0, s54, 23552\n\t" \
  "s_mov_b32 s60, 917504\n\t" \
  "buffer_load_dwordx4 v71, %[Wv], s60 offen lds\n\t" \
  "s_add_u32 m0, s54, 24576\n\t" \
  "s_mov_b32 s60, 1048576\n\t" \
  "buffer_load_dwordx4 v71, %[Wq], s60 offen lds\n\t" \
  "s_add_u32 m0, s54, 25600\n\t" \
  "s_mov_b32 s60, 1179648\n\t" \
  "buffer_load_dwordx4 v71, %[Wq], s60 offen lds\n\t" \
  "s_add_u32 m0, s54, 26624\n\t" \
  "s_mov_b32 s60, 1310720\n\t" \
  "buffer_load_dwordx4 v71, %[Wq], s60 offen lds\n\t" \
  "s_add_u32 m0, s54, 27648\n\t" \
  "s_mov_b32 s60, 1441792\n\t" \
  "buffer_load_dwordx4 v71, %[Wq], s60 offen lds\n\t" \
  "s_add_u32 m0, s54, 28672\n\t" \
  "s_mov_b32 s60, 1048576\n\t" \
  "buffer_load_dwordx4 v71, %[Wk], s60 offen lds\n\t" \
  "s_add_u32 m0, s54, 29696\n\t" \
  "s_mov_b32 s60, 1179648\n\t" \
  "buffer_load_dwordx4 v71, %[Wk], s60 offen lds\n\t" \
  "s_add_u32 m0, s54, 30720\n\t" \
  "s_mov_b32 s60, 1310720\n\t" \
  "buffer_load_dwordx4 v71, %[Wk], s60 offen lds\n\t" \
  "s_add_u32 m0, s54, 31744\n\t" \
  "s_mov_b32 s60, 1441792\n\t" \
  "buffer_load_dwordx4 v71, %[Wk], s60 offen lds\n\t" \
  "s_add_u32 m0, s54, 32768\n\t" \
  "s_mov_b32 s60, 1048576\n\t" \
  "buffer_load_dwordx4 v71, %[Wv], s60 offen lds\n\t" \
  "s_add_u32 m0, s54, 33792\n\t" \
  "s_mov_b32 s60, 1179648\n\t" \
  "buffer_load_dwordx4 v71, %[Wv], s60 offen lds\n\t" \
  "s_add_u32 m0, s54, 34816\n\t" \
  "s_mov_b32 s60, 1310720\n\t" \
  "buffer_load_dwordx4 v71, %[Wv], s60 offen lds\n\t" \
  "s_add_u32 m0, s54, 35840\n\t" \
  "s_mov_b32 s60, 1441792\n\t" \
  "buffer_load_dwordx4 v71, %[Wv], s60 offen lds\n\t" \
  "s_waitcnt vmcnt(32)\n\t" \
  "ds_read_b64_tr_b16 v[0:1], v65 offset:0\n\t" \
  "ds_read_b64_tr_b16 v[2:3], v65 offset:256\n\t" \
  "ds_read_b64_tr_b16 v[4:5], v65 offset:2048\n\t" \
  "ds_read_b64_tr_b16 v[6:7], v65 offset:2304\n\t" \
  "ds_read_b64_tr_b16 v[8:9], v65 offset:8\n\t" \
  "ds_read_b64_tr_b16 v[10:11], v65 offset:264\n\t" \
  "ds_read_b64_tr_b16 v[12:13], v65 offset:2056\n\t" \
  "ds_read_b64_tr_b16 v[14:15], v65 offset:2312\n\t" \
  "s_waitcnt lgkmcnt(0)\n\t" \
  "s_add_u32 m0, s54, 0\n\t" \
  "s_mov_b32 s60, 1572864\n\t" \
  "buffer_load_dwordx4 v71, %[Wq], s60 offen lds\n\t" \
  "s_add_u32 m0, s54, 1024\n\t" \
  "s_mov_b32 s60, 1703936\n\t" \
  "buffer_load_dwordx4 v71, %[Wq], s60 offen lds\n\t" \
  "s_add_u32 m0, s54, 2048\n\t" \
  "s_mov_b32 s60, 1835008\n\t" \
  "buffer_load_dwordx4 v71, %[Wq], s60 offen lds\n\t" \
  "s_add_u32 m0, s54, 3072\n\t" \
  "s_mov_b32 s60, 1966080\n\t" \
  "buffer_load_dwordx4 v71, %[Wq], s60 offen lds\n\t" \
  "s_waitcnt vmcnt(32)\n\t" \
  "ds_read_b64_tr_b16 a[112:113], v65 offset:4096\n\t" \
  "ds_read_b64_tr_b16 a[114:115], v65 offset:4352\n\t" \
  "ds_read_b64_tr_b16 a[116:117], v65 offset:6144\n\t" \
  "ds_read_b64_tr_b16 a[118:119], v65 offset:6400\n\t" \
  "ds_read_b64_tr_b16 a[168:169], v65 offset:4104\n\t" \
  "ds_read_b64_tr_b16 a[170:171], v65 offset:4360\n\t" \
  "ds_read_b64_tr_b16 a[172:173], v65 offset:6152\n\t" \
  "ds_read_b64_tr_b16 a[174:175], v65 offset:6408\n\t" \
  "s_waitcnt lgkmcnt(0)\n\t" \
  "s_add_u32 m0, s54, 4096\n\t" \
  "s_mov_b32 s60, 1572864\n\t" \
  "buffer_load_dwordx4 v71, %[Wk], s60 offen lds\n\t" \
  "s_add_u32 m0, s54, 5120\n\t" \
  "s_mov_b32 s60, 1703936\n\t" \
  "buffer_load_dwordx4 v71, %[Wk], s60 offen lds\n\t" \
  "s_add_u32 m0, s54, 6144\n\t" \
  "s_mov_b32 s60, 1835008\n\t" \
  "buffer_load_dwordx4 v71, %[Wk], s60 offen lds\n\t" \
  "s_add_u32 m0, s54, 7168\n\t" \
  "s_mov_b32 s60, 1966080\n\t" \
  "buffer_load_dwordx4 v71, %[Wk], s60 offen lds\n\t" \
  "s_waitcnt vmcnt(32)\n\t" \
  "ds_read_b64_tr_b16 a[240:241], v66 offset:8192\n\t" \
  "ds_read_b64_tr_b16 a[242:243], v66 offset:8448\n\t" \
  "ds_read_b64_tr_b16 a[244:245], v66 offset:10240\n\t" \
  "ds_read_b64_tr_b16 a[246:247], v66 offset:10496\n\t" \
  "ds_read_b64_tr_b16 v[136:137], v66 offset:8224\n\t" \
  "ds_read_b64_tr_b16 v[138:139], v66 offset:8480\n\t" \
  "ds_read_b64_tr_b16 v[140:141], v66 offset:10272\n\t" \
  "ds_read_b64_tr_b16 v[142:143], v66 offset:10528\n\t" \
  "s_waitcnt lgkmcnt(0)\n\t" \
  "s_add_u32 m0, s54, 8192\n\t" \
  "s_mov_b32 s60, 1572864\n\t" \
  "buffer_load_dwordx4 v71, %[Wv], s60 offen lds\n\t" \
  "s_add_u32 m0, s54, 9216\n\t" \
  "s_mov_b32 s60, 1703936\n\t" \
  "buffer_load_dwordx4 v71, %[Wv], s60 offen lds\n\t" \
  "s_add_u32 m0, s54, 10240\n\t" \
  "s_mov_b32 s60, 1835008\n\t" \
  "buffer_load_dwordx4 v71, %[Wv], s60 offen lds\n\t" \
  "s_add_u32 m0, s54, 11264\n\t" \
  "s_mov_b32 s60, 1966080\n\t" \
  "buffer_load_dwordx4 v71, %[Wv], s60 offen lds\n\t" \
  "s_waitcnt vmcnt(32)\n\t" \
  "ds_read_b64_tr_b16 a[0:1], v65 offset:12288\n\t" \
  "ds_read_b64_tr_b16 a[2:3], v65 offset:12544\n\t" \
  "ds_read_b64_tr_b16 a[4:5], v65 offset:14336\n\t" \
  "ds_read_b64_tr_b16 a[6:7], v65 offset:14592\n\t" \
  "ds_read_b64_tr_b16 a[64:65], v65 offset:12296\n\t" \
  "ds_read_b64_tr_b16 a[66:67], v65 offset:12552\n\t" \
  "ds_read_b64_tr_b16 a[68:69], v65 offset:14344\n\t" \
  "ds_read_b64_tr_b16 a[70:71], v65 offset:14600\n\t" \
  "s_waitcnt lgkmcnt(0)\n\t" \
  "s_add_u32 m0, s54, 12288\n\t" \
  "s_mov_b32 s60, 2097152\n\t" \
  "buffer_load_dwordx4 v71, %[Wq], s60 offen lds\n\t" \
  "s_add_u32 m0, s54, 13312\n\t" \
  "s_mov_b32 s60, 2228224\n\t" \
  "buffer_load_dwordx4 v71, %[Wq], s60 offen lds\n\t" \
  "s_add_u32 m0, s54, 14336\n\t" \
  "s_mov_b32 s60, 2359296\n\t" \
  "buffer_load_dwordx4 v71, %[Wq], s60 offen lds\n\t" \
  "s_add_u32 m0, s54, 15360\n\t" \
  "s_mov_b32 s60, 2490368\n\t" \
  "buffer_load_dwordx4 v71, %[Wq], s60 offen lds\n\t" \
  "s_waitcnt vmcnt(32)\n\t" \
  "ds_read_b64_tr_b16 a[120:121], v65 offset:16384\n\t" \
  "ds_read_b64_tr_b16 a[122:123], v65 offset:16640\n\t" \
  "ds_read_b64_tr_b16 a[124:125], v65 offset:18432\n\t" \
  "ds_read_b64_tr_b16 a[126:127], v65 offset:18688\n\t" \
  "ds_read_b64_tr_b16 a[176:177], v65 offset:16392\n\t" \
  "ds_read_b64_tr_b16 a[178:179], v65 offset:16648\n\t" \
  "ds_read_b64_tr_b16 a[180:181], v65 offset:18440\n\t" \
  "ds_read_b64_tr_b16 a[182:183], v65 offset:18696\n\t" \
  "s_waitcnt lgkmcnt(0)\n\t" \
  "s_add_u32 m0, s54, 16384\n\t" \
  "s_mov_b32 s60, 2097152\n\t" \
  "buffer_load_dwordx4 v71, %[Wk], s60 offen lds\n\t" \
  "s_add_u32 m0, s54, 17408\n\t" \
  "s_mov_b32 s60, 2228224\n\t" \
  "buffer_load_dwordx4 v71, %[Wk], s60 offen lds\n\t" \
  "s_add_u32 m0, s54, 18432\n\t" \
  "s_mov_b32 s60, 2359296\n\t" \
  "buffer_load_dwordx4 v71, %[Wk], s60 offen lds\n\t" \
  "s_add_u32 m0, s54, 19456\n\t" \
  "s_mov_b32 s60, 2490368\n\t" \
  "buffer_load_dwordx4 v71, %[Wk], s60 offen lds\n\t" \
  "s_waitcnt vmcnt(32)\n\t" \
  "ds_read_b64_tr_b16 a[248:249], v66 offset:20480\n\t" \
  "ds_read_b64_tr_b16 a[250:251], v66 offset:20736\n\t" \
  "ds_read_b64_tr_b16 a[252:253], v66 offset:22528\n\t" \
  "ds_read_b64_tr_b16 a[254:255], v66 offset:22784\n\t" \
  "ds_read_b64_tr_b16 v[144:145], v66 offset:20512\n\t" \
  "ds_read_b64_tr_b16 v[146:147], v66 offset:20768\n\t" \
  "ds_read_b64_tr_b16 v[148:149], v66 offset:22560\n\t" \
  "ds_read_b64_tr_b16 v[150:151], v66 offset:22816\n\t" \
  "s_waitcnt lgkmcnt(0)\n\t" \
  "s_add_u32 m0, s54, 20480\n\t" \
  "s_mov_b32 s60, 2097152\n\t" \
  "buffer_load_dwordx4 v71, %[Wv], s60 offen lds\n\t" \
  "s_add_u32 m0, s54, 21504\n\t" \
  "s_mov_b32 s60, 2228224\n\t" \
  "buffer_load_dwordx4 v71, %[Wv], s60 offen lds\n\t" \
  "s_add_u32 m0, s54, 22528\n\t" \
  "s_mov_b32 s60, 2359296\n\t" \
  "buffer_load_dwordx4 v71, %[Wv], s60 offen lds\n\t" \
  "s_add_u32 m0, s54, 23552\n\t" \
  "s_mov_b32 s60, 2490368\n\t" \
  "buffer_load_dwordx4 v71, %[Wv], s60 offen lds\n\t" \
  "s_waitcnt vmcnt(32)\n\t" \
  "ds_read_b64_tr_b16 a[8:9], v65 offset:24576\n\t" \
  "ds_read_b64_tr_b16 a[10:11], v65 offset:24832\n\t" \
  "ds_read_b64_tr_b16 a[12:13], v65 offset:26624\n\t" \
  "ds_read_b64_tr_b16 a[14:15], v65 offset:26880\n\t" \
  "ds_read_b64_tr_b16 a[72:73], v65 offset:24584\n\t" \
  "ds_read_b64_tr_b16 a[74:75], v65 offset:24840\n\t" \
  "ds_read_b64_tr_b16 a[76:77], v65 offset:26632\n\t" \
  "ds_read_b64_tr_b16 a[78:79], v65 offset:26888\n\t" \
  "s_waitcnt lgkmcnt(0)\n\t" \
  "s_add_u32 m0, s54, 24576\n\t" \
  "s_mov_b32 s60, 2621440\n\t" \
  "buffer_load_dwordx4 v71, %[Wq], s60 offen lds\n\t" \
  "s_add_u32 m0, s54, 25600\n\t" \
  "s_mov_b32 s60, 2752512\n\t" \
  "buffer_load_dwordx4 v71, %[Wq], s60 offen lds\n\t" \
  "s_add_u32 m0, s54, 26624\n\t" \
  "s_mov_b32 s60, 2883584\n\t" \
  "buffer_load_dwordx4 v71, %[Wq], s60 offen lds\n\t" \
  "s_add_u32 m0, s54, 27648\n\t" \
  "s_mov_b32 s60, 3014656\n\t" \
  "buffer_load_dwordx4 v71, %[Wq], s60 offen lds\n\t" \
  "s_waitcnt vmcnt(32)\n\t" \
  "ds_read_b64_tr_b16 v[32:33], v65 offset:28672\n\t" \
  "ds_read_b64_tr_b16 v[34:35], v65 offset:28928\n\t" \
  "ds_read_b64_tr_b16 v[36:37], v65 offset:30720\n\t" \
  "ds_read_b64_tr_b16 v[38:39], v65 offset:30976\n\t" \
  "ds_read_b64_tr_b16 a[184:185], v65 offset:28680\n\t" \
  "ds_read_b64_tr_b16 a[186:187], v65 offset:28936\n\t" \
  "ds_read_b64_tr_b16 a[188:189], v65 offset:30728\n\t" \
  "ds_read_b64_tr_b16 a[190:191], v65 offset:30984\n\t" \
  "s_waitcnt lgkmcnt(0)\n\t" \
  "s_add_u32 m0, s54, 28672\n\t" \
  "s_mov_b32 s60, 2621440\n\t" \
  "buffer_load_dwordx4 v71, %[Wk], s60 offen lds\n\t" \
  "s_add_u32 m0, s54, 29696\n\t" \
  "s_mov_b32 s60, 2752512\n\t" \
  "buffer_load_dwordx4 v71, %[Wk], s60 offen lds\n\t" \
  "s_add_u32 m0, s54, 30720\n\t" \
  "s_mov_b32 s60, 2883584\n\t" \
  "buffer_load_dwordx4 v71, %[Wk], s60 offen lds\n\t" \
  "s_add_u32 m0, s54, 31744\n\t" \
  "s_mov_b32 s60, 3014656\n\t" \
  "buffer_load_dwordx4 v71, %[Wk], s60 offen lds\n\t" \
  "s_waitcnt vmcnt(32)\n\t" \
  "ds_read_b64_tr_b16 v[80:81], v66 offset:32768\n\t" \
  "ds_read_b64_tr_b16 v[82:83], v66 offset:33024\n\t" \
  "ds_read_b64_tr_b16 v[84:85], v66 offset:34816\n\t" \
  "ds_read_b64_tr_b16 v[86:87], v66 offset:35072\n\t" \
  "ds_read_b64_tr_b16 v[152:153], v66 offset:32800\n\t" \
  "ds_read_b64_tr_b16 v[154:155], v66 offset:33056\n\t" \
  "ds_read_b64_tr_b16 v[156:157], v66 offset:34848\n\t" \
  "ds_read_b64_tr_b16 v[158:159], v66 offset:35104\n\t" \
  "s_waitcnt lgkmcnt(0)\n\t" \
  "s_add_u32 m0, s54, 32768\n\t" \
  "s_mov_b32 s60, 2621440\n\t" \
  "buffer_load_dwordx4 v71, %[Wv], s60 offen lds\n\t" \
  "s_add_u32 m0, s54, 33792\n\t" \
  "s_mov_b32 s60, 2752512\n\t" \
  "buffer_load_dwordx4 v71, %[Wv], s60 offen lds\n\t" \
  "s_add_u32 m0, s54, 34816\n\t" \
  "s_mov_b32 s60, 2883584\n\t" \
  "buffer_load_dwordx4 v71, %[Wv], s60 offen lds\n\t" \
  "s_add_u32 m0, s54, 35840\n\t" \
  "s_mov_b32 s60, 3014656\n\t" \
  "buffer_load_dwordx4 v71, %[Wv], s60 offen lds\n\t" \
  "s_waitcnt vmcnt(32)\n\t" \
  "ds_read_b64_tr_b16 a[16:17], v65 offset:0\n\t" \
  "ds_read_b64_tr_b16 a[18:19], v65 offset:256\n\t" \
  "ds_read_b64_tr_b16 a[20:21], v65 offset:2048\n\t" \
  "ds_read_b64_tr_b16 a[22:23], v65 offset:2304\n\t" \
  "ds_read_b64_tr_b16 v[16:17], v65 offset:8\n\t" \
  "ds_read_b64_tr_b16 v[18:19], v65 offset:264\n\t" \
  "ds_read_b64_tr_b16 v[20:21], v65 offset:2056\n\t" \
  "ds_read_b64_tr_b16 v[22:23], v65 offset:2312\n\t" \
  "s_waitcnt lgkmcnt(0)\n\t" \
  "s_add_u32 m0, s54, 0\n\t" \
  "s_mov_b32 s60, 3145728\n\t" \
  "buffer_load_dwordx4 v71, %[Wq], s60 offen lds\n\t" \
  "s_add_u32 m0, s54, 1024\n\t" \
  "s_mov_b32 s60, 3276800\n\t" \
  "buffer_load_dwordx4 v71, %[Wq], s60 offen lds\n\t" \
  "s_add_u32 m0, s54, 2048\n\t" \
  "s_mov_b32 s60, 3407872\n\t" \
  "buffer_load_dwordx4 v71, %[Wq], s60 offen lds\n\t" \
  "s_add_u32 m0, s54, 3072\n\t" \
  "s_mov_b32 s60, 3538944\n\t" \
  "buffer_load_dwordx4 v71, %[Wq], s60 offen lds\n\t" \
  "s_waitcnt vmcnt(32)\n\t" \
  "ds_read_b64_tr_b16 a[128:129], v65 offset:4096\n\t" \
  "ds_read_b64_tr_b16 a[130:131], v65 offset:4352\n\t" \
  "ds_read_b64_tr_b16 a[132:133], v65 offset:6144\n\t" \
  "ds_read_b64_tr_b16 a[134:135], v65 offset:6400\n\t" \
  "ds_read_b64_tr_b16 a[192:193], v65 offset:4104\n\t" \
  "ds_read_b64_tr_b16 a[194:195], v65 offset:4360\n\t" \
  "ds_read_b64_tr_b16 a[196:197], v65 offset:6152\n\t" \
  "ds_read_b64_tr_b16 a[198:199], v65 offset:6408\n\t" \
  "s_waitcnt lgkmcnt(0)\n\t" \
  "s_add_u32 m0, s54, 4096\n\t" \
  "s_mov_b32 s60, 3145728\n\t" \
  "buffer_load_dwordx4 v71, %[Wk], s60 offen lds\n\t" \
  "s_add_u32 m0, s54, 5120\n\t" \
  "s_mov_b32 s60, 3276800\n\t" \
  "buffer_load_dwordx4 v71, %[Wk], s60 offen lds\n\t" \
  "s_add_u32 m0, s54, 6144\n\t" \
  "s_mov_b32 s60, 3407872\n\t" \
  "buffer_load_dwordx4 v71, %[Wk], s60 offen lds\n\t" \
  "s_add_u32 m0, s54, 7168\n\t" \
  "s_mov_b32 s60, 3538944\n\t" \
  "buffer_load_dwordx4 v71, %[Wk], s60 offen lds\n\t" \
  "s_waitcnt vmcnt(32)\n\t" \
  "ds_read_b64_tr_b16 v[88:89], v66 offset:8192\n\t" \
  "ds_read_b64_tr_b16 v[90:91], v66 offset:8448\n\t" \
  "ds_read_b64_tr_b16 v[92:93], v66 offset:10240\n\t" \
  "ds_read_b64_tr_b16 v[94:95], v66 offset:10496\n\t" \
  "ds_read_b64_tr_b16 v[160:161], v66 offset:8224\n\t" \
  "ds_read_b64_tr_b16 v[162:163], v66 offset:8480\n\t" \
  "ds_read_b64_tr_b16 v[164:165], v66 offset:10272\n\t" \
  "ds_read_b64_tr_b16 v[166:167], v66 offset:10528\n\t" \
  "s_waitcnt lgkmcnt(0)\n\t" \
  "s_add_u32 m0, s54, 8192\n\t" \
  "s_mov_b32 s60, 3145728\n\t" \
  "buffer_load_dwordx4 v71, %[Wv], s60 offen lds\n\t" \
  "s_add_u32 m0, s54, 9216\n\t" \
  "s_mov_b32 s60, 3276800\n\t" \
  "buffer_load_dwordx4 v71, %[Wv], s60 offen lds\n\t" \
  "s_add_u32 m0, s54, 10240\n\t" \
  "s_mov_b32 s60, 3407872\n\t" \
  "buffer_load_dwordx4 v71, %[Wv], s60 offen lds\n\t" \
  "s_add_u32 m0, s54, 11264\n\t" \
  "s_mov_b32 s60, 3538944\n\t" \
  "buffer_load_dwordx4 v71, %[Wv], s60 offen lds\n\t" \
  "s_waitcnt vmcnt(32)\n\t" \
  "ds_read_b64_tr_b16 a[24:25], v65 offset:12288\n\t" \
  "ds_read_b64_tr_b16 a[26:27], v65 offset:12544\n\t" \
  "ds_read_b64_tr_b16 a[28:29], v65 offset:14336\n\t" \
  "ds_read_b64_tr_b16 a[30:31], v65 offset:14592\n\t" \
  "ds_read_b64_tr_b16 a[80:81], v65 offset:12296\n\t" \
  "ds_read_b64_tr_b16 a[82:83], v65 offset:12552\n\t" \
  "ds_read_b64_tr_b16 a[84:85], v65 offset:14344\n\t" \
  "ds_read_b64_tr_b16 a[86:87], v65 offset:14600\n\t" \
  "s_waitcnt lgkmcnt(0)\n\t" \
  "s_add_u32 m0, s54, 12288\n\t" \
  "s_mov_b32 s60, 3670016\n\t" \
  "buffer_load_dwordx4 v71, %[Wq], s60 offen lds\n\t" \
  "s_add_u32 m0, s54, 13312\n\t" \
  "s_mov_b32 s60, 3801088\n\t" \
  "buffer_load_dwordx4 v71, %[Wq], s60 offen lds\n\t" \
  "s_add_u32 m0, s54, 14336\n\t" \
  "s_mov_b32 s60, 3932160\n\t" \
  "buffer_load_dwordx4 v71, %[Wq], s60 offen lds\n\t" \
  "s_add_u32 m0, s54, 15360\n\t" \
  "s_mov_b32 s60, 4063232\n\t" \
  "buffer_load_dwordx4 v71, %[Wq], s60 offen lds\n\t" \
  "s_waitcnt vmcnt(32)\n\t" \
  "ds_read_b64_tr_b16 a[136:137], v65 offset:16384\n\t" \
  "ds_read_b64_tr_b16 a[138:139], v65 offset:16640\n\t" \
  "ds_read_b64_tr_b16 a[140:141], v65 offset:18432\n\t" \
  "ds_read_b64_tr_b16 a[142:143], v65 offset:18688\n\t" \
  "ds_read_b64_tr_b16 a[200:201], v65 offset:16392\n\t" \
  "ds_read_b64_tr_b16 a[202:203], v65 offset:16648\n\t" \
  "ds_read_b64_tr_b16 a[204:205], v65 offset:18440\n\t" \
  "ds_read_b64_tr_b16 a[206:207], v65 offset:18696\n\t" \
  "s_waitcnt lgkmcnt(0)\n\t" \
  "s_add_u32 m0, s54, 16384\n\t" \
  "s_mov_b32 s60, 3670016\n\t" \
  "buffer_load_dwordx4 v71, %[Wk], s60 offen lds\n\t" \
  "s_add_u32 m0, s54, 17408\n\t" \
  "s_mov_b32 s60, 3801088\n\t" \
  "buffer_load_dwordx4 v71, %[Wk], s60 offen lds\n\t" \
  "s_add_u32 m0, s54, 18432\n\t" \
  "s_mov_b32 s60, 3932160\n\t" \
  "buffer_load_dwordx4 v71, %[Wk], s60 offen lds\n\t" \
  "s_add_u32 m0, s54, 19456\n\t" \
  "s_mov_b32 s60, 4063232\n\t" \
  "buffer_load_dwordx4 v71, %[Wk], s60 offen lds\n\t" \
  "s_waitcnt vmcnt(32)\n\t" \
  "ds_read_b64_tr_b16 v[96:97], v66 offset:20480\n\t" \
  "ds_read_b64_tr_b16 v[98:99], v66 offset:20736\n\t" \
  "ds_read_b64_tr_b16 v[100:101], v66 offset:22528\n\t" \
  "ds_read_b64_tr_b16 v[102:103], v66 offset:22784\n\t" \
  "ds_read_b64_tr_b16 v[48:49], v66 offset:20512\n\t" \
  "ds_read_b64_tr_b16 v[50:51], v66 offset:20768\n\t" \
  "ds_read_b64_tr_b16 v[52:53], v66 offset:22560\n\t" \
  "ds_read_b64_tr_b16 v[54:55], v66 offset:22816\n\t" \
  "s_waitcnt lgkmcnt(0)\n\t" \
  "s_add_u32 m0, s54, 20480\n\t" \
  "s_mov_b32 s60, 3670016\n\t" \
  "buffer_load_dwordx4 v71, %[Wv], s60 offen lds\n\t" \
  "s_add_u32 m0, s54, 21504\n\t" \
  "s_mov_b32 s60, 3801088\n\t" \
  "buffer_load_dwordx4 v71, %[Wv], s60 offen lds\n\t" \
  "s_add_u32 m0, s54, 22528\n\t" \
  "s_mov_b32 s60, 3932160\n\t" \
  "buffer_load_dwordx4 v71, %[Wv], s60 offen lds\n\t" \
  "s_add_u32 m0, s54, 23552\n\t" \
  "s_mov_b32 s60, 4063232\n\t" \
  "buffer_load_dwordx4 v71, %[Wv], s60 offen lds\n\t" \
  "s_waitcnt vmcnt(32)\n\t" \
  "ds_read_b64_tr_b16 a[32:33], v65 offset:24576\n\t" \
  "ds_read_b64_tr_b16 a[34:35], v65 offset:24832\n\t" \
  "ds_read_b64_tr_b16 a[36:37], v65 offset:26624\n\t" \
  "ds_read_b64_tr_b16 a[38:39], v65 offset:26880\n\t" \
  "ds_read_b64_tr_b16 v[24:25], v65 offset:24584\n\t" \
  "ds_read_b64_tr_b16 v[26:27], v65 offset:24840\n\t" \
  "ds_read_b64_tr_b16 v[28:29], v65 offset:26632\n\t" \
  "ds_read_b64_tr_b16 v[30:31], v65 offset:26888\n\t" \
  "s_waitcnt lgkmcnt(0)\n\t" \
  "s_add_u32 m0, s54, 24576\n\t" \
  "s_mov_b32 s60, 4194304\n\t" \
  "buffer_load_dwordx4 v71, %[Wq], s60 offen lds\n\t" \
  "s_add_u32 m0, s54, 25600\n\t" \
  "s_mov_b32 s60, 4325376\n\t" \
  "buffer_load_dwordx4 v71, %[Wq], s60 offen lds\n\t" \
  "s_add_u32 m0, s54, 26624\n\t" \
  "s_mov_b32 s60, 4456448\n\t" \
  "buffer_load_dwordx4 v71, %[Wq], s60 offen lds\n\t" \
  "s_add_u32 m0, s54, 27648\n\t" \
  "s_mov_b32 s60, 4587520\n\t" \
  "buffer_load_dwordx4 v71, %[Wq], s60 offen lds\n\t" \
  "s_waitcnt vmcnt(32)\n\t" \
  "ds_read_b64_tr_b16 a[144:145], v65 offset:28672\n\t" \
  "ds_read_b64_tr_b16 a[146:147], v65 offset:28928\n\t" \
  "ds_read_b64_tr_b16 a[148:149], v65 offset:30720\n\t" \
  "ds_read_b64_tr_b16 a[150:151], v65 offset:30976\n\t" \
  "ds_read_b64_tr_b16 a[208:209], v65 offset:28680\n\t" \
  "ds_read_b64_tr_b16 a[210:211], v65 offset:28936\n\t" \
  "ds_read_b64_tr_b16 a[212:213], v65 offset:30728\n\t" \
  "ds_read_b64_tr_b16 a[214:215], v65 offset:30984\n\t" \
  "s_waitcnt lgkmcnt(0)\n\t" \
  "s_add_u32 m0, s54, 28672\n\t" \
  "s_mov_b32 s60, 4194304\n\t" \
  "buffer_load_dwordx4 v71, %[Wk], s60 offen lds\n\t" \
  "s_add_u32 m0, s54, 29696\n\t" \
  "s_mov_b32 s60, 4325376\n\t" \
  "buffer_load_dwordx4 v71, %[Wk], s60 offen lds\n\t" \
  "s_add_u32 m0, s54, 30720\n\t" \
  "s_mov_b32 s60, 4456448\n\t" \
  "buffer_load_dwordx4 v71, %[Wk], s60 offen lds\n\t" \
  "s_add_u32 m0, s54, 31744\n\t" \
  "s_mov_b32 s60, 4587520\n\t" \
  "buffer_load_dwordx4 v71, %[Wk], s60 offen lds\n\t" \
  "s_waitcnt vmcnt(32)\n\t" \
  "ds_read_b64_tr_b16 v[104:105], v66 offset:32768\n\t" \
  "ds_read_b64_tr_b16 v[106:107], v66 offset:33024\n\t" \
  "ds_read_b64_tr_b16 v[108:109], v66 offset:34816\n\t" \
  "ds_read_b64_tr_b16 v[110:111], v66 offset:35072\n\t" \
  "ds_read_b64_tr_b16 v[168:169], v66 offset:32800\n\t" \
  "ds_read_b64_tr_b16 v[170:171], v66 offset:33056\n\t" \
  "ds_read_b64_tr_b16 v[172:173], v66 offset:34848\n\t" \
  "ds_read_b64_tr_b16 v[174:175], v66 offset:35104\n\t" \
  "s_waitcnt lgkmcnt(0)\n\t" \
  "s_add_u32 m0, s54, 32768\n\t" \
  "s_mov_b32 s60, 4194304\n\t" \
  "buffer_load_dwordx4 v71, %[Wv], s60 offen lds\n\t" \
  "s_add_u32 m0, s54, 33792\n\t" \
  "s_mov_b32 s60, 4325376\n\t" \
  "buffer_load_dwordx4 v71, %[Wv], s60 offen lds\n\t" \
  "s_add_u32 m0, s54, 34816\n\t" \
  "s_mov_b32 s60, 4456448\n\t" \
  "buffer_load_dwordx4 v71, %[Wv], s60 offen lds\n\t" \
  "s_add_u32 m0, s54, 35840\n\t" \
  "s_mov_b32 s60, 4587520\n\t" \
  "buffer_load_dwordx4 v71, %[Wv], s60 offen lds\n\t" \
  "s_waitcnt vmcnt(32)\n\t" \
  "ds_read_b64_tr_b16 a[40:41], v65 offset:0\n\t" \
  "ds_read_b64_tr_b16 a[42:43], v65 offset:256\n\t" \
  "ds_read_b64_tr_b16 a[44:45], v65 offset:2048\n\t" \
  "ds_read_b64_tr_b16 a[46:47], v65 offset:2304\n\t" \
  "ds_read_b64_tr_b16 a[88:89], v65 offset:8\n\t" \
  "ds_read_b64_tr_b16 a[90:91], v65 offset:264\n\t" \
  "ds_read_b64_tr_b16 a[92:93], v65 offset:2056\n\t" \
  "ds_read_b64_tr_b16 a[94:95], v65 offset:2312\n\t" \
  "s_waitcnt vmcnt(28)\n\t" \
  "ds_read_b64_tr_b16 a[152:153], v65 offset:4096\n\t" \
  "ds_read_b64_tr_b16 a[154:155], v65 offset:4352\n\t" \
  "ds_read_b64_tr_b16 a[156:157], v65 offset:6144\n\t" \
  "ds_read_b64_tr_b16 a[158:159], v65 offset:6400\n\t" \
  "ds_read_b64_tr_b16 a[216:217], v65 offset:4104\n\t" \
  "ds_read_b64_tr_b16 a[218:219], v65 offset:4360\n\t" \
  "ds_read_b64_tr_b16 a[220:221], v65 offset:6152\n\t" \
  "ds_read_b64_tr_b16 a[222:223], v65 offset:6408\n\t" \
  "s_waitcnt vmcnt(24)\n\t" \
  "ds_read_b64_tr_b16 v[112:113], v66 offset:8192\n\t" \
  "ds_read_b64_tr_b16 v[114:115], v66 offset:8448\n\t" \
  "ds_read_b64_tr_b16 v[116:117], v66 offset:10240\n\t" \
  "ds_read_b64_tr_b16 v[118:119], v66 offset:10496\n\t" \
  "ds_read_b64_tr_b16 v[176:177], v66 offset:8224\n\t" \
  "ds_read_b64_tr_b16 v[178:179], v66 offset:8480\n\t" \
  "ds_read_b64_tr_b16 v[180:181], v66 offset:10272\n\t" \
  "ds_read_b64_tr_b16 v[182:183], v66 offset:10528\n\t" \
  "s_xor_b32 s60, s40, 0\n\t" \
  "s_mul_i32 s60, s60, 36864\n\t" \
  "s_add_u32 s60, s60, s55\n\t" \
  "s_add_u32 s60, s60, 0\n\t" \
  "s_add_u32 s61, s60, 9216\n\t" \
  "buffer_load_dwordx3 v[192:194], v64, %[Wo], s60 offen offset:0\n\t" \
  "buffer_load_dwordx3 v[196:198], v64, %[Wo], s60 offen offset:1152\n\t" \
  "buffer_load_dwordx3 v[200:202], v64, %[Wo], s60 offen offset:2304\n\t" \
  "buffer_load_dwordx3 v[204:206], v64, %[Wo], s60 offen offset:3456\n\t" \
  "buffer_load_dwordx3 v[208:210], v64, %[Wo], s61 offen offset:0\n\t" \
  "buffer_load_dwordx3 v[212:214], v64, %[Wo], s61 offen offset:1152\n\t" \
  "buffer_load_dwordx3 v[216:218], v64, %[Wo], s61 offen offset:2304\n\t" \
  "buffer_load_dwordx3 v[220:222], v64, %[Wo], s61 offen offset:3456\n\t" \
  "s_waitcnt vmcnt(28)\n\t" \
  "ds_read_b64_tr_b16 a[48:49], v65 offset:12288\n\t" \
  "ds_read_b64_tr_b16 a[50:51], v65 offset:12544\n\t" \
  "ds_read_b64_tr_b16 a[52:53], v65 offset:14336\n\t" \
  "ds_read_b64_tr_b16 a[54:55], v65 offset:14592\n\t" \
  "ds_read_b64_tr_b16 a[96:97], v65 offset:12296\n\t" \
  "ds_read_b64_tr_b16 a[98:99], v65 offset:12552\n\t" \
  "ds_read_b64_tr_b16 a[100:101], v65 offset:14344\n\t" \
  "ds_read_b64_tr_b16 a[102:103], v65 offset:14600\n\t" \
  "s_waitcnt vmcnt(24)\n\t" \
  "ds_read_b64_tr_b16 a[160:161], v65 offset:16384\n\t" \
  "ds_read_b64_tr_b16 a[162:163], v65 offset:16640\n\t" \
  "ds_read_b64_tr_b16 a[164:165], v65 offset:18432\n\t" \
  "ds_read_b64_tr_b16 a[166:167], v65 offset:18688\n\t" \
  "ds_read_b64_tr_b16 a[224:225], v65 offset:16392\n\t" \
  "ds_read_b64_tr_b16 a[226:227], v65 offset:16648\n\t" \
  "ds_read_b64_tr_b16 a[228:229], v65 offset:18440\n\t" \
  "ds_read_b64_tr_b16 a[230:231], v65 offset:18696\n\t" \
  "s_xor_b32 s60, s40, 0\n\t" \
  "s_mul_i32 s60, s60, 36864\n\t" \
  "s_add_u32 s60, s60, s55\n\t" \
  "s_add_u32 s60, s60, 512\n\t" \
  "s_add_u32 s61, s60, 9216\n\t" \
  "buffer_load_dwordx3 v[224:226], v64, %[Wo], s60 offen offset:0\n\t" \
  "buffer_load_dwordx3 v[228:230], v64, %[Wo], s60 offen offset:1152\n\t" \
  "buffer_load_dwordx3 v[232:234], v64, %[Wo], s60 offen offset:2304\n\t" \
  "buffer_load_dwordx3 v[236:238], v64, %[Wo], s60 offen offset:3456\n\t" \
  "buffer_load_dwordx3 v[240:242], v64, %[Wo], s61 offen offset:0\n\t" \
  "buffer_load_dwordx3 v[244:246], v64, %[Wo], s61 offen offset:1152\n\t" \
  "buffer_load_dwordx3 v[248:250], v64, %[Wo], s61 offen offset:2304\n\t" \
  "buffer_load_dwordx3 v[252:254], v64, %[Wo], s61 offen offset:3456\n\t" \
  "s_waitcnt vmcnt(28)\n\t" \
  "ds_read_b64_tr_b16 v[120:121], v66 offset:20480\n\t" \
  "ds_read_b64_tr_b16 v[122:123], v66 offset:20736\n\t" \
  "ds_read_b64_tr_b16 v[124:125], v66 offset:22528\n\t" \
  "ds_read_b64_tr_b16 v[126:127], v66 offset:22784\n\t" \
  "ds_read_b64_tr_b16 v[184:185], v66 offset:20512\n\t" \
  "ds_read_b64_tr_b16 v[186:187], v66 offset:20768\n\t" \
  "ds_read_b64_tr_b16 v[188:189], v66 offset:22560\n\t" \
  "ds_read_b64_tr_b16 v[190:191], v66 offset:22816\n\t" \
  "s_waitcnt vmcnt(24)\n\t" \
  "ds_read_b64_tr_b16 a[56:57], v65 offset:24576\n\t" \
  "ds_read_b64_tr_b16 a[58:59], v65 offset:24832\n\t" \
  "ds_read_b64_tr_b16 a[60:61], v65 offset:26624\n\t" \
  "ds_read_b64_tr_b16 a[62:63], v65 offset:26880\n\t" \
  "ds_read_b64_tr_b16 a[104:105], v65 offset:24584\n\t" \
  "ds_read_b64_tr_b16 a[106:107], v65 offset:24840\n\t" \
  "ds_read_b64_tr_b16 a[108:109], v65 offset:26632\n\t" \
  "ds_read_b64_tr_b16 a[110:111], v65 offset:26888\n\t" \
  "s_add_u32 s60, s75, 73728\n\t" \
  "buffer_load_ushort v72, v67, %[Wo], s60 offen\n\t" \
  "buffer_load_ushort v195, v67, %[Wo], s60 offen offset:1152\n\t" \
  "s_add_u32 s60, s75, 110592\n\t" \
  "buffer_load_ushort v73, v67, %[Wo], s60 offen\n\t" \
  "buffer_load_ushort v199, v67, %[Wo], s60 offen offset:1152\n\t" \
  "s_add_u32 s60, s75, 76032\n\t" \
  "buffer_load_ushort v74, v67, %[Wo], s60 offen\n\t" \
  "buffer_load_ushort v203, v67, %[Wo], s60 offen offset:1152\n\t" \
  "s_add_u32 s60, s75, 112896\n\t" \
  "buffer_load_ushort v75, v67, %[Wo], s60 offen\n\t" \
  "buffer_load_ushort v207, v67, %[Wo], s60 offen offset:1152\n\t" \
  "s_add_u32 s60, s75, 82944\n\t" \
  "buffer_load_ushort v76, v67, %[Wo], s60 offen\n\t" \
  "buffer_load_ushort v211, v67, %[Wo], s60 offen offset:1152\n\t" \
  "s_add_u32 s60, s75, 119808\n\t" \
  "buffer_load_ushort v77, v67, %[Wo], s60 offen\n\t" \
  "buffer_load_ushort v215, v67, %[Wo], s60 offen offset:1152\n\t" \
  "s_add_u32 s60, s75, 85248\n\t" \
  "buffer_load_ushort v78, v67, %[Wo], s60 offen\n\t" \
  "buffer_load_ushort v219, v67, %[Wo], s60 offen offset:1152\n\t" \
  "s_add_u32 s60, s75, 122112\n\t" \
  "buffer_load_ushort v79, v67, %[Wo], s60 offen\n\t" \
  "buffer_load_ushort v223, v67, %[Wo], s60 offen offset:1152\n\t" \
  "s_waitcnt vmcnt(36)\n\t" \
  "ds_read_b64_tr_b16 v[40:41], v65 offset:28672\n\t" \
  "ds_read_b64_tr_b16 v[42:43], v65 offset:28928\n\t" \
  "ds_read_b64_tr_b16 v[44:45], v65 offset:30720\n\t" \
  "ds_read_b64_tr_b16 v[46:47], v65 offset:30976\n\t" \
  "ds_read_b64_tr_b16 a[232:233], v65 offset:28680\n\t" \
  "ds_read_b64_tr_b16 a[234:235], v65 offset:28936\n\t" \
  "ds_read_b64_tr_b16 a[236:237], v65 offset:30728\n\t" \
  "ds_read_b64_tr_b16 a[238:239], v65 offset:30984\n\t" \
  "s_waitcnt vmcnt(32)\n\t" \
  "ds_read_b64_tr_b16 v[128:129], v66 offset:32768\n\t" \
  "ds_read_b64_tr_b16 v[130:131], v66 offset:33024\n\t" \
  "ds_read_b64_tr_b16 v[132:133], v66 offset:34816\n\t" \
  "ds_read_b64_tr_b16 v[134:135], v66 offset:35072\n\t" \
  "ds_read_b64_tr_b16 v[56:57], v66 offset:32800\n\t" \
  "ds_read_b64_tr_b16 v[58:59], v66 offset:33056\n\t" \
  "ds_read_b64_tr_b16 v[60:61], v66 offset:34848\n\t" \
  "ds_read_b64_tr_b16 v[62:63], v66 offset:35104\n\t" \
  "s_waitcnt lgkmcnt(0)\n\t" \
  "ds_write_b128 v70, v[0:3] offset:0\n\t" \
  "ds_write_b128 v70, v[4:7] offset:1024\n\t" \
  "ds_write_b128 v70, v[8:11] offset:2048\n\t" \
  "ds_write_b128 v70, v[12:15] offset:3072\n\t" \
  "ds_write_b128 v70, v[16:19] offset:4096\n\t" \
  "ds_write_b128 v70, v[20:23] offset:5120\n\t" \
  "ds_write_b128 v70, v[24:27] offset:6144\n\t" \
  "ds_write_b128 v70, v[28:31] offset:7168\n\t" \
  "ds_write_b128 v70, v[32:35] offset:8192\n\t" \
  "ds_write_b128 v70, v[36:39] offset:9216\n\t" \
  "ds_write_b128 v70, v[40:43] offset:10240\n\t" \
  "ds_write_b128 v70, v[44:47] offset:11264\n\t" \
  "ds_write_b128 v70, v[48:51] offset:12288\n\t" \
  "ds_write_b128 v70, v[52:55] offset:13312\n\t" \
  "ds_write_b128 v70, v[56:59] offset:14336\n\t" \
  "ds_write_b128 v70, v[60:63] offset:15360\n\t" \
  "s_waitcnt lgkmcnt(0)\n\t" \
  "s_xor_b32 s60, s40, 1\n\t" \
  "s_mul_i32 s60, s60, 36864\n\t" \
  "s_add_u32 s60, s60, s55\n\t" \
  "s_add_u32 s60, s60, 0\n\t" \
  "s_add_u32 s61, s60, 9216\n\t" \
  "buffer_load_dwordx3 v[0:2], v64, %[Wo], s60 offen offset:0\n\t" \
  "buffer_load_dwordx3 v[4:6], v64, %[Wo], s60 offen offset:1152\n\t" \
  "buffer_load_dwordx3 v[8:10], v64, %[Wo], s60 offen offset:2304\n\t" \
  "buffer_load_dwordx3 v[12:14], v64, %[Wo], s60 offen offset:3456\n\t" \
  "buffer_load_dwordx3 v[16:18], v64, %[Wo], s61 offen offset:0\n\t" \
  "buffer_load_dwordx3 v[20:22], v64, %[Wo], s61 offen offset:1152\n\t" \
  "buffer_load_dwordx3 v[24:26], v64, %[Wo], s61 offen offset:2304\n\t" \
  "buffer_load_dwordx3 v[28:30], v64, %[Wo], s61 offen offset:3456\n\t" \
  "s_xor_b32 s60, s40, 1\n\t" \
  "s_mul_i32 s60, s60, 36864\n\t" \
  "s_add_u32 s60, s60, s55\n\t" \
  "s_add_u32 s60, s60, 512\n\t" \
  "s_add_u32 s61, s60, 9216\n\t" \
  "buffer_load_dwordx3 v[32:34], v64, %[Wo], s60 offen offset:0\n\t" \
  "buffer_load_dwordx3 v[36:38], v64, %[Wo], s60 offen offset:1152\n\t" \
  "buffer_load_dwordx3 v[40:42], v64, %[Wo], s60 offen offset:2304\n\t" \
  "buffer_load_dwordx3 v[44:46], v64, %[Wo], s60 offen offset:3456\n\t" \
  "buffer_load_dwordx3 v[48:50], v64, %[Wo], s61 offen offset:0\n\t" \
  "buffer_load_dwordx3 v[52:54], v64, %[Wo], s61 offen offset:1152\n\t" \
  "buffer_load_dwordx3 v[56:58], v64, %[Wo], s61 offen offset:2304\n\t" \
  "buffer_load_dwordx3 v[60:62], v64, %[Wo], s61 offen offset:3456\n\t" \
  "s_waitcnt vmcnt(30)\n\t" \
  "v_lshl_or_b32 v72, v195, 16, v72\n\t" \
  "s_waitcnt vmcnt(28)\n\t" \
  "v_lshl_or_b32 v73, v199, 16, v73\n\t" \
  "s_waitcnt vmcnt(26)\n\t" \
  "v_lshl_or_b32 v74, v203, 16, v74\n\t" \
  "s_waitcnt vmcnt(24)\n\t" \
  "v_lshl_or_b32 v75, v207, 16, v75\n\t" \
  "s_waitcnt vmcnt(22)\n\t" \
  "v_lshl_or_b32 v76, v211, 16, v76\n\t" \
  "s_waitcnt vmcnt(20)\n\t" \
  "v_lshl_or_b32 v77, v215, 16, v77\n\t" \
  "s_waitcnt vmcnt(18)\n\t" \
  "v_lshl_or_b32 v78, v219, 16, v78\n\t" \
  "s_waitcnt vmcnt(16)\n\t" \
  "v_lshl_or_b32 v79, v223, 16, v79\n\t" \
  "ds_write_b128 v70, v[72:75] offset:34816\n\t" \
  "ds_write_b128 v70, v[76:79] offset:35840\n\t" \
  "s_waitcnt lgkmcnt(0)\n\t" \
  "v_perm_b32 v72, v196, v192, s62\n\t" \
  "v_perm_b32 v73, v196, v192, s63\n\t" \
  "v_perm_b32 v74, v204, v200, s62\n\t" \
  "v_perm_b32 v75, v204, v200, s63\n\t" \
  "v_perm_b32 v76, v212, v208, s62\n\t" \
  "v_perm_b32 v77, v212, v208, s63\n\t" \
  "v_perm_b32 v78, v220, v216, s62\n\t" \
  "v_perm_b32 v79, v220, v216, s63\n\t" \
  "ds_write_b128 v70, v[72:75] offset:20480\n\t" \
  "ds_write_b128 v70, v[76:79] offset:21504\n\t" \
  "s_waitcnt lgkmcnt(0)\n\t" \
  "v_perm_b32 v72, v198, v194, s62\n\t" \
  "v_perm_b32 v73, v198, v194, s63\n\t" \
  "v_perm_b32 v74, v206, v202, s62\n\t" \
  "v_perm_b32 v75, v206, v202, s63\n\t" \
  "v_perm_b32 v76, v214, v210, s62\n\t" \
  "v_perm_b32 v77, v214, v210, s63\n\t" \
  "v_perm_b32 v78, v222, v218, s62\n\t" \
  "v_perm_b32 v79, v222, v218, s63\n\t" \
  "ds_write_b128 v70, v[72:75] offset:16384\n\t" \
  "ds_write_b128 v70, v[76:79] offset:17408\n\t" \
  "s_waitcnt lgkmcnt(0)\n\t" \
  "v_perm_b32 v72, v228, v224, s62\n\t" \
  "v_perm_b32 v73, v228, v224, s63\n\t" \
  "v_perm_b32 v74, v236, v232, s62\n\t" \
  "v_perm_b32 v75, v236, v232, s63\n\t" \
  "v_perm_b32 v76, v244, v240, s62\n\t" \
  "v_perm_b32 v77, v244, v240, s63\n\t" \
  "v_perm_b32 v78, v252, v248, s62\n\t" \
  "v_perm_b32 v79, v252, v248, s63\n\t" \
  "ds_write_b128 v70, v[72:75] offset:22528\n\t" \
  "ds_write_b128 v70, v[76:79] offset:23552\n\t" \
  "s_waitcnt lgkmcnt(0)\n\t" \
  "v_perm_b32 v72, v230, v226, s62\n\t" \
  "v_perm_b32 v73, v230, v226, s63\n\t" \
  "v_perm_b32 v74, v238, v234, s62\n\t" \
  "v_perm_b32 v75, v238, v234, s63\n\t" \
  "v_perm_b32 v76, v246, v242, s62\n\t" \
  "v_perm_b32 v77, v246, v242, s63\n\t" \
  "v_perm_b32 v78, v254, v250, s62\n\t" \
  "v_perm_b32 v79, v254, v250, s63\n\t" \
  "ds_write_b128 v70, v[72:75] offset:18432\n\t" \
  "ds_write_b128 v70, v[76:79] offset:19456\n\t" \
  "s_add_u32 s60, s75, 0\n\t" \
  "buffer_load_ushort v248, v67, %[Wo], s60 offen\n\t" \
  "buffer_load_ushort v3, v67, %[Wo], s60 offen offset:1152\n\t" \
  "s_add_u32 s60, s75, 36864\n\t" \
  "buffer_load_ushort v249, v67, %[Wo], s60 offen\n\t" \
  "buffer_load_ushort v7, v67, %[Wo], s60 offen offset:1152\n\t" \
  "s_add_u32 s60, s75, 2304\n\t" \
  "buffer_load_ushort v250, v67, %[Wo], s60 offen\n\t" \
  "buffer_load_ushort v11, v67, %[Wo], s60 offen offset:1152\n\t" \
  "s_add_u32 s60, s75, 39168\n\t" \
  "buffer_load_ushort v251, v67, %[Wo], s60 offen\n\t" \
  "buffer_load_ushort v15, v67, %[Wo], s60 offen offset:1152\n\t" \
  "s_add_u32 s60, s75, 9216\n\t" \
  "buffer_load_ushort v252, v67, %[Wo], s60 offen\n\t" \
  "buffer_load_ushort v19, v67, %[Wo], s60 offen offset:1152\n\t" \
  "s_add_u32 s60, s75, 46080\n\t" \
  "buffer_load_ushort v253, v67, %[Wo], s60 offen\n\t" \
  "buffer_load_ushort v23, v67, %[Wo], s60 offen offset:1152\n\t" \
  "s_add_u32 s60, s75, 11520\n\t" \
  "buffer_load_ushort v254, v67, %[Wo], s60 offen\n\t" \
  "buffer_load_ushort v27, v67, %[Wo], s60 offen offset:1152\n\t" \
  "s_add_u32 s60, s75, 48384\n\t" \
  "buffer_load_ushort v255, v67, %[Wo], s60 offen\n\t" \
  "buffer_load_ushort v31, v67, %[Wo], s60 offen offset:1152\n\t" \
  "s_waitcnt lgkmcnt(0)\n\t" \
  "s_waitcnt vmcnt(30)\n\t" \
  "v_perm_b32 v72, v4, v0, s62\n\t" \
  "v_perm_b32 v73, v4, v0, s63\n\t" \
  "s_waitcnt vmcnt(28)\n\t" \
  "v_perm_b32 v74, v12, v8, s62\n\t" \
  "v_perm_b32 v75, v12, v8, s63\n\t" \
  "s_waitcnt vmcnt(26)\n\t" \
  "v_perm_b32 v76, v20, v16, s62\n\t" \
  "v_perm_b32 v77, v20, v16, s63\n\t" \
  "s_waitcnt vmcnt(24)\n\t" \
  "v_perm_b32 v78, v28, v24, s62\n\t" \
  "v_perm_b32 v79, v28, v24, s63\n\t" \
  "ds_write_b128 v70, v[72:75] offset:24576\n\t" \
  "ds_write_b128 v70, v[76:79] offset:25600\n\t" \
  "v_perm_b32 v200, v6, v2, s62\n\t" \
  "v_perm_b32 v201, v6, v2, s63\n\t" \
  "v_perm_b32 v202, v14, v10, s62\n\t" \
  "v_perm_b32 v203, v14, v10, s63\n\t" \
  "v_perm_b32 v204, v22, v18, s62\n\t" \
  "v_perm_b32 v205, v22, v18, s63\n\t" \
  "v_perm_b32 v206, v30, v26, s62\n\t" \
  "v_perm_b32 v207, v30, v26, s63\n\t" \
  "s_xor_b32 s60, s40, 2\n\t" \
  "s_mul_i32 s60, s60, 36864\n\t" \
  "s_add_u32 s60, s60, s55\n\t" \
  "s_add_u32 s60, s60, 0\n\t" \
  "s_add_u32 s61, s60, 9216\n\t" \
  "buffer_load_dwordx3 v[0:2], v64, %[Wo], s60 offen offset:0\n\t" \
  "buffer_load_dwordx3 v[4:6], v64, %[Wo], s60 offen offset:1152\n\t" \
  "buffer_load_dwordx3 v[8:10], v64, %[Wo], s60 offen offset:2304\n\t" \
  "buffer_load_dwordx3 v[12:14], v64, %[Wo], s60 offen offset:3456\n\t" \
  "buffer_load_dwordx3 v[16:18], v64, %[Wo], s61 offen offset:0\n\t" \
  "buffer_load_dwordx3 v[20:22], v64, %[Wo], s61 offen offset:1152\n\t" \
  "buffer_load_dwordx3 v[24:26], v64, %[Wo], s61 offen offset:2304\n\t" \
  "buffer_load_dwordx3 v[28:30], v64, %[Wo], s61 offen offset:3456\n\t" \
  "s_waitcnt lgkmcnt(0)\n\t" \
  "s_waitcnt vmcnt(30)\n\t" \
  "v_perm_b32 v72, v36, v32, s62\n\t" \
  "v_perm_b32 v73, v36, v32, s63\n\t" \
  "s_waitcnt vmcnt(28)\n\t" \
  "v_perm_b32 v74, v44, v40, s62\n\t" \
  "v_perm_b32 v75, v44, v40, s63\n\t" \
  "s_waitcnt vmcnt(26)\n\t" \
  "v_perm_b32 v76, v52, v48, s62\n\t" \
  "v_perm_b32 v77, v52, v48, s63\n\t" \
  "s_waitcnt vmcnt(24)\n\t" \
  "v_perm_b32 v78, v60, v56, s62\n\t" \
  "v_perm_b32 v79, v60, v56, s63\n\t" \
  "ds_write_b128 v70, v[72:75] offset:26624\n\t" \
  "ds_write_b128 v70, v[76:79] offset:27648\n\t" \
  "v_perm_b32 v232, v38, v34, s62\n\t" \
  "v_perm_b32 v233, v38, v34, s63\n\t" \
  "v_perm_b32 v234, v46, v42, s62\n\t" \
  "v_perm_b32 v235, v46, v42, s63\n\t" \
  "v_perm_b32 v236, v54, v50, s62\n\t" \
  "v_perm_b32 v237, v54, v50, s63\n\t" \
  "v_perm_b32 v238, v62, v58, s62\n\t" \
  "v_perm_b32 v239, v62, v58, s63\n\t" \
  "s_xor_b32 s60, s40, 2\n\t" \
  "s_mul_i32 s60, s60, 36864\n\t" \
  "s_add_u32 s60, s60, s55\n\t" \
  "s_add_u32 s60, s60, 512\n\t" \
  "s_add_u32 s61, s60, 9216\n\t" \
  "buffer_load_dwordx3 v[32:34], v64, %[Wo], s60 offen offset:0\n\t" \
  "buffer_load_dwordx3 v[36:38], v64, %[Wo], s60 offen offset:1152\n\t" \
  "buffer_load_dwordx3 v[40:42], v64, %[Wo], s60 offen offset:2304\n\t" \
  "buffer_load_dwordx3 v[44:46], v64, %[Wo], s60 offen offset:3456\n\t" \
  "buffer_load_dwordx3 v[48:50], v64, %[Wo], s61 offen offset:0\n\t" \
  "buffer_load_dwordx3 v[52:54], v64, %[Wo], s61 offen offset:1152\n\t" \
  "buffer_load_dwordx3 v[56:58], v64, %[Wo], s61 offen offset:2304\n\t" \
  "buffer_load_dwordx3 v[60:62], v64, %[Wo], s61 offen offset:3456\n\t" \
  "s_waitcnt lgkmcnt(0)\n\t" \
  "s_waitcnt vmcnt(14)\n\t" \
  "v_perm_b32 v72, v4, v0, s62\n\t" \
  "v_perm_b32 v73, v4, v0, s63\n\t" \
  "s_waitcnt vmcnt(12)\n\t" \
  "v_perm_b32 v74, v12, v8, s62\n\t" \
  "v_perm_b32 v75, v12, v8, s63\n\t" \
  "s_waitcnt vmcnt(10)\n\t" \
  "v_perm_b32 v76, v20, v16, s62\n\t" \
  "v_perm_b32 v77, v20, v16, s63\n\t" \
  "s_waitcnt vmcnt(8)\n\t" \
  "v_perm_b32 v78, v28, v24, s62\n\t" \
  "v_perm_b32 v79, v28, v24, s63\n\t" \
  "ds_write_b128 v70, v[72:75] offset:28672\n\t" \
  "ds_write_b128 v70, v[76:79] offset:29696\n\t" \
  "v_perm_b32 v208, v6, v2, s62\n\t" \
  "v_perm_b32 v209, v6, v2, s63\n\t" \
  "v_perm_b32 v210, v14, v10, s62\n\t" \
  "v_perm_b32 v211, v14, v10, s63\n\t" \
  "v_perm_b32 v212, v22, v18, s62\n\t" \
  "v_perm_b32 v213, v22, v18, s63\n\t" \
  "v_perm_b32 v214, v30, v26, s62\n\t" \
  "v_perm_b32 v215, v30, v26, s63\n\t" \
  "s_xor_b32 s60, s40, 3\n\t" \
  "s_mul_i32 s60, s60, 36864\n\t" \
  "s_add_u32 s60, s60, s55\n\t" \
  "s_add_u32 s60, s60, 0\n\t" \
  "s_add_u32 s61, s60, 9216\n\t" \
  "buffer_load_dwordx3 v[0:2], v64, %[Wo], s60 offen offset:0\n\t" \
  "buffer_load_dwordx3 v[4:6], v64, %[Wo], s60 offen offset:1152\n\t" \
  "buffer_load_dwordx3 v[8:10], v64, %[Wo], s60 offen offset:2304\n\t" \
  "buffer_load_dwordx3 v[12:14], v64, %[Wo], s60 offen offset:3456\n\t" \
  "buffer_load_dwordx3 v[16:18], v64, %[Wo], s61 offen offset:0\n\t" \
  "buffer_load_dwordx3 v[20:22], v64, %[Wo], s61 offen offset:1152\n\t" \
  "buffer_load_dwordx3 v[24:26], v64, %[Wo], s61 offen offset:2304\n\t" \
  "buffer_load_dwordx3 v[28:30], v64, %[Wo], s61 offen offset:3456\n\t" \
  "s_waitcnt lgkmcnt(0)\n\t" \
  "s_waitcnt vmcnt(14)\n\t" \
  "v_perm_b32 v72, v36, v32, s62\n\t" \
  "v_perm_b32 v73, v36, v32, s63\n\t" \
  "s_waitcnt vmcnt(12)\n\t" \
  "v_perm_b32 v74, v44, v40, s62\n\t" \
  "v_perm_b32 v75, v44, v40, s63\n\t" \
  "s_waitcnt vmcnt(10)\n\t" \
  "v_perm_b32 v76, v52, v48, s62\n\t" \
  "v_perm_b32 v77, v52, v48, s63\n\t" \
  "s_waitcnt vmcnt(8)\n\t" \
  "v_perm_b32 v78, v60, v56, s62\n\t" \
  "v_perm_b32 v79, v60, v56, s63\n\t" \
  "ds_write_b128 v70, v[72:75] offset:30720\n\t" \
  "ds_write_b128 v70, v[76:79] offset:31744\n\t" \
  "v_perm_b32 v240, v38, v34, s62\n\t" \
  "v_perm_b32 v241, v38, v34, s63\n\t" \
  "v_perm_b32 v242, v46, v42, s62\n\t" \
  "v_perm_b32 v243, v46, v42, s63\n\t" \
  "v_perm_b32 v244, v54, v50, s62\n\t" \
  "v_perm_b32 v245, v54, v50, s63\n\t" \
  "v_perm_b32 v246, v62, v58, s62\n\t" \
  "v_perm_b32 v247, v62, v58, s63\n\t" \
  "s_xor_b32 s60, s40, 3\n\t" \
  "s_mul_i32 s60, s60, 36864\n\t" \
  "s_add_u32 s60, s60, s55\n\t" \
  "s_add_u32 s60, s60, 512\n\t" \
  "s_add_u32 s61, s60, 9216\n\t" \
  "buffer_load_dwordx3 v[32:34], v64, %[Wo], s60 offen offset:0\n\t" \
  "buffer_load_dwordx3 v[36:38], v64, %[Wo], s60 offen offset:1152\n\t" \
  "buffer_load_dwordx3 v[40:42], v64, %[Wo], s60 offen offset:2304\n\t" \
  "buffer_load_dwordx3 v[44:46], v64, %[Wo], s60 offen offset:3456\n\t" \
  "buffer_load_dwordx3 v[48:50], v64, %[Wo], s61 offen offset:0\n\t" \
  "buffer_load_dwordx3 v[52:54], v64, %[Wo], s61 offen offset:1152\n\t" \
  "buffer_load_dwordx3 v[56:58], v64, %[Wo], s61 offen offset:2304\n\t" \
  "buffer_load_dwordx3 v[60:62], v64, %[Wo], s61 offen offset:3456\n\t" \
  "s_waitcnt vmcnt(14)\n\t" \
  "v_perm_b32 v192, v4, v0, s62\n\t" \
  "v_perm_b32 v193, v4, v0, s63\n\t" \
  "s_waitcnt vmcnt(12)\n\t" \
  "v_perm_b32 v194, v12, v8, s62\n\t" \
  "v_perm_b32 v195, v12, v8, s63\n\t" \
  "s_waitcnt vmcnt(10)\n\t" \
  "v_perm_b32 v196, v20, v16, s62\n\t" \
  "v_perm_b32 v197, v20, v16, s63\n\t" \
  "s_waitcnt vmcnt(8)\n\t" \
  "v_perm_b32 v198, v28, v24, s62\n\t" \
  "v_perm_b32 v199, v28, v24, s63\n\t" \
  "v_perm_b32 v216, v6, v2, s62\n\t" \
  "v_perm_b32 v217, v6, v2, s63\n\t" \
  "v_perm_b32 v218, v14, v10, s62\n\t" \
  "v_perm_b32 v219, v14, v10, s63\n\t" \
  "v_perm_b32 v220, v22, v18, s62\n\t" \
  "v_perm_b32 v221, v22, v18, s63\n\t" \
  "v_perm_b32 v222, v30, v26, s62\n\t" \
  "v_perm_b32 v223, v30, v26, s63\n\t" \
  "s_waitcnt vmcnt(6)\n\t" \
  "v_perm_b32 v224, v36, v32, s62\n\t" \
  "v_perm_b32 v225, v36, v32, s63\n\t" \
  "s_waitcnt vmcnt(4)\n\t" \
  "v_perm_b32 v226, v44, v40, s62\n\t" \
  "v_perm_b32 v227, v44, v40, s63\n\t" \
  "s_waitcnt vmcnt(2)\n\t" \
  "v_perm_b32 v228, v52, v48, s62\n\t" \
  "v_perm_b32 v229, v52, v48, s63\n\t" \
  "s_waitcnt vmcnt(0)\n\t" \
  "v_perm_b32 v230, v60, v56, s62\n\t" \
  "v_perm_b32 v231, v60, v56, s63\n\t" \
  "s_waitcnt lgkmcnt(0)\n\t" \
  "v_perm_b32 v72, v38, v34, s62\n\t" \
  "v_perm_b32 v73, v38, v34, s63\n\t" \
  "v_perm_b32 v74, v46, v42, s62\n\t" \
  "v_perm_b32 v75, v46, v42, s63\n\t" \
  "v_perm_b32 v76, v54, v50, s62\n\t" \
  "v_perm_b32 v77, v54, v50, s63\n\t" \
  "v_perm_b32 v78, v62, v58, s62\n\t" \
  "v_perm_b32 v79, v62, v58, s63\n\t" \
  "ds_write_b128 v70, v[72:75] offset:32768\n\t" \
  "ds_write_b128 v70, v[76:79] offset:33792\n\t" \
  "v_lshl_or_b32 v248, v3, 16, v248\n\t" \
  "v_lshl_or_b32 v249, v7, 16, v249\n\t" \
  "v_lshl_or_b32 v250, v11, 16, v250\n\t" \
  "v_lshl_or_b32 v251, v15, 16, v251\n\t" \
  "v_lshl_or_b32 v252, v19, 16, v252\n\t" \
  "v_lshl_or_b32 v253, v23, 16, v253\n\t" \
  "v_lshl_or_b32 v254, v27, 16, v254\n\t" \
  "v_lshl_or_b32 v255, v31, 16, v255\n\t" \
  "s_waitcnt lgkmcnt(0)\n\t" \
  CH_STW \
  "s_mov_b32 s48, -1\n\t" \
  "s_add_u32 s82, s48, 0\n\t" \
  CH_SEGC \
  "s_lshr_b64 s[60:61], s[90:91], s82\n\t" \
  "s_and_b32 s60, s60, 1\n\t" \
  "s_lshl_b32 s60, s60, 13\n\t" \
  "s_add_u32 s82, s82, " CH_SG0 "\n\t" \
  "s_lshl_b32 s82, s82, 21\n\t" \
  "s_or_b32 s82, s82, s60\n\t" \
  "s_add_u32 s50, s82, s38\n\t" \
  "s_add_u32 s69, s82, s39\n\t" \
  "s_add_u32 s82, s48, 1\n\t" \
  CH_SEGC \
  "s_lshr_b64 s[60:61], s[90:91], s82\n\t" \
  "s_and_b32 s60, s60, 1\n\t" \
  "s_lshl_b32 s60, s60, 13\n\t" \
  "s_add_u32 s82, s82, " CH_SG0 "\n\t" \
  "s_lshl_b32 s82, s82, 21\n\t" \
  "s_or_b32 s82, s82, s60\n\t" \
  "s_add_u32 s52, s82, s43\n\t" \
  "s_add_u32 s82, s48, " CH_ARMD "\n\t" \
  CH_SEGC \
  "s_lshr_b64 s[60:61], s[90:91], s82\n\t" \
  "s_and_b32 s60, s60, 1\n\t" \
  "s_lshl_b32 s60, s60, 13\n\t" \
  "s_add_u32 s82, s82, " CH_SG0 "\n\t" \
  "s_lshl_b32 s82, s82, 21\n\t" \
  "s_or_b32 s82, s82, s60\n\t" \
  "s_xor_b32 s85, s82, s45\n\t" \
  "s_cmp_lt_i32 s48, " CH_ARMS "\n\t" \
  "s_cselect_b32 s85, s52, s85\n\t" \
  "v_and_b32 v37, 0x30, v69\n\t" \
  "v_add_u32 v37, s52, v37\n\t" \
  "s_mov_b32 s48, 0\n\t" \
  "s_mov_b32 s86, 1\n\t" \
  "s_and_b32 s49, s96, 1\n\t" \
  "s_lshl_b32 s49, s49, 13\n\t" \
  "s_add_u32 s53, s49, " CH_SLOT "\n\t" \
  "s_movk_i32 s81, 124\n\t" \
  "s_add_u32 s82, s48, 0\n\t" \
  CH_SEGC \
  "s_lshr_b64 s[60:61], s[90:91], s82\n\t" \
  "s_and_b32 s60, s60, 1\n\t" \
  "s_lshl_b32 s60, s60, 13\n\t" \
  "s_add_u32 s82, s82, " CH_SG0 "\n\t" \
  "s_lshl_b32 s82, s82, 21\n\t" \
  "s_or_b32 s82, s82, s60\n\t" \
  "s_add_u32 s50, s82, s38\n\t" \
  "s_add_u32 s69, s82, s39\n\t" \
  "s_add_u32 s51, s82, s43\n\t" \
  "s_add_u32 s82, s48, 0\n\t" \
  CH_SEGC \
  "s_lshr_b64 s[60:61], s[90:91], s82\n\t" \
  "s_and_b32 s60, s60, 1\n\t" \
  "s_lshl_b32 s60, s60, 13\n\t" \
  "s_add_u32 s82, s82, " CH_SG0 "\n\t" \
  "s_lshl_b32 s82, s82, 21\n\t" \
  "s_or_b32 s82, s82, s60\n\t" \
  "s_add_u32 s50, s82, s38\n\t" \
  "s_add_u32 s69, s82, s39\n\t" \
  "s_add_u32 s82, s48, 1\n\t" \
  CH_SEGC \
  "s_lshr_b64 s[60:61], s[90:91], s82\n\t" \
  "s_and_b32 s60, s60, 1\n\t" \
  "s_lshl_b32 s60, s60, 13\n\t" \
  "s_add_u32 s82, s82, " CH_SG0 "\n\t" \
  "s_lshl_b32 s82, s82, 21\n\t" \
  "s_or_b32 s82, s82, s60\n\t" \
  "s_add_u32 s52, s82, s43\n\t" \
  "s_add_u32 s82, s48, " CH_ARMD "\n\t" \
  CH_SEGC \
  "s_lshr_b64 s[60:61], s[90:91], s82\n\t" \
  "s_and_b32 s60, s60, 1\n\t" \
  "s_lshl_b32 s60, s60, 13\n\t" \
  "s_add_u32 s82, s82, " CH_SG0 "\n\t" \
  "s_lshl_b32 s82, s82, 21\n\t" \
  "s_or_b32 s82, s82, s60\n\t" \
  "s_xor_b32 s85, s82, s45\n\t" \
  "s_cmp_lt_i32 s48, " CH_ARMS "\n\t" \
  "s_cselect_b32 s85, s52, s85\n\t" \
  "s_memrealtime s[62:63]\n\t" \
  "s_waitcnt lgkmcnt(0)\n\t" \
  "s_mov_b32 s65, s62\n\t" \
  "s_sub_u32 s47, s62, 1000\n\t" \
  "s_mov_b64 vcc, -1\n\t" \
  "s_cmp_eq_u32 s40, 0\n\t" \
  "s_cbranch_scc1 LW0_%=\n\t" \
  "s_barrier\n\t" \
  "s_cmp_eq_u32 s40, 1\n\t" \
  "s_cbranch_scc1 LG1_%=\n\t" \
  "s_cmp_eq_u32 s40, 2\n\t" \
  "s_cbranch_scc1 LG2_%=\n\t" \
  "s_branch LTK_%=\n\t" \
  ".p2align 6\n\t" \
  "LW0_%=:\n\t" \
  "v_lshlrev_b32 v78, 2, v69\n\t" \
  "v_and_b32 v78, 0x300, v78\n\t" \
  "v_lshrrev_b32 v79, 8, v69\n\t" \
  "v_lshl_add_u32 v78, v79, 6, v78\n\t" \
  "v_and_b32 v79, 48, v69\n\t" \
  "v_add_u32 v78, v78, v79\n\t" \
  "s_add_u32 s98, s49, s42\n\t" \
  "s_add_u32 s97, s98, 4096\n\t" \
  "s_add_u32 s99, s49, s44\n\t" \
  "v_mul_u32_u24 v77, 72, v69\n\t" \
  "s_barrier\n\t" \
  "s_memrealtime s[62:63]\n\t" \
  "s_waitcnt lgkmcnt(0)\n\t" \
  "s_add_u32 s62, s62, " CH_DLL2 "\n\t" \
  "s_sub_i32 s63, s62, s65\n\t" \
  "s_add_u32 s62, s62, " CH_DLX "\n\t" \
  "s_cmp_gt_i32 s63, 0\n\t" \
  "s_cselect_b32 s65, s62, s65\n\t" \
  "s_mov_b32 m0, 0\n\t" \
  "s_movk_i32 s60, 8192\n\t" \
  "LWT_%=:\n\t" \
  "s_memrealtime s[62:63]\n\t" \
  "s_sub_u32 s60, s60, 1\n\t" \
  "s_cselect_b32 s61, 0, -1\n\t" \
  "s_waitcnt lgkmcnt(0)\n\t" \
  "s_sub_i32 s63, s62, s65\n\t" \
  "s_and_b32 s63, s63, s61\n\t" \
  "s_cmp_lt_i32 s63, 0\n\t" \
  "s_cbranch_scc1 LWT_%=\n\t" \
  "LDMA_%=:\n\t" \
  CH_DPC \
  "buffer_load_dwordx4 v78, s[28:31], s98 offen offset:0 sc1 lds\n\t" \
  "buffer_load_dwordx4 v78, s[28:31], s98 offen offset:1024 sc1 lds\n\t" \
  "buffer_load_dwordx4 v78, s[28:31], s98 offen offset:2048 sc1 lds\n\t" \
  "buffer_load_dwordx4 v78, s[28:31], s98 offen offset:3072 sc1 lds\n\t" \
  "s_movk_i32 m0, 4096\n\t" \
  "s_mov_b32 s66, s62\n\t" \
  "buffer_load_dwordx4 v78, s[28:31], s97 offen offset:0 sc1 lds\n\t" \
  "buffer_load_dwordx4 v78, s[28:31], s97 offen offset:1024 sc1 lds\n\t" \
  "buffer_load_dwordx4 v78, s[28:31], s97 offen offset:2048 sc1 lds\n\t" \
  "buffer_load_dwordx4 v78, s[28:31], s97 offen offset:3072 sc1 lds\n\t" \
  "s_movk_i32 m0, 8192\n\t" \
  "s_nop 0\n\t" \
  "buffer_load_dwordx4 v69, s[28:31], s99 offen sc1 lds\n\t" \
  CH_ST1 \
  ".p2align 6\n\t" \
  "LG0_%=:\n\t" \
  "s_barrier\n\t" \
  "buffer_load_dwordx4 v[52:55], v69, s[28:31], s50 offen sc1\n\t" \
  "buffer_load_dwordx4 v[56:59], v69, s[28:31], s50 offen offset:1024 sc1\n\t" \
  "s_add_u32 s64, s50, 16384\n\t" \
  "s_movk_i32 m0, 11264\n\t" \
  "v_lshlrev_b32 v75, 1, v69\n\t" \
  "buffer_load_dword v75, s[28:31], s64 offen sc1 lds\n\t" \
  "s_memrealtime s[62:63]\n\t" \
  "s_waitcnt lgkmcnt(0)\n\t" \
  "s_sub_u32 s62, s62, " CH_TKL "\n\t" \
  "s_sub_i32 s63, s62, s65\n\t" \
  "s_cmp_gt_i32 s63, 0\n\t" \
  "s_cselect_b32 s62, s62, s65\n\t" \
  "s_sub_i32 s63, s62, s47\n\t" \
  "s_cmp_gt_i32 s63, " CH_LATE "\n\t" \
  "s_cselect_b32 s71, " CH_SCHW ", s46\n\t" \
  "s_add_u32 s61, s47, " CH_LATE "\n\t" \
  "s_cmp_gt_i32 s63, " CH_LATE "\n\t" \
  "s_cselect_b32 s62, s62, s61\n\t" \
  "s_add_u32 s71, s71, s53\n\t" \
  "s_add_u32 s60, s62, s81\n\t" \
  "s_add_u32 s60, s60, " CH_DL1L "\n\t" \
  "s_lshl_b32 s60, s60, 8\n\t" \
  "s_or_b32 s60, s60, " CH_B0 "\n\t" \
  "s_not_b32 s60, s60\n\t" \
  "v_mov_b32 v76, s60\n\t" \
  "s_mov_b64 exec, 15\n\t" \
  "buffer_atomic_umin v76, v77, s[28:31], s71 offen sc1\n\t" \
  "s_mov_b64 exec, -1\n\t" \
  "ds_read_b128 v[44:47], v70 offset:0\n\t" \
  "ds_read_b128 v[48:51], v70 offset:1024\n\t" \
  "ds_read_b128 v[60:63], v70 offset:2048\n\t" \
  "ds_read_b128 v[64:67], v70 offset:3072\n\t" \
  "v_mov_b64 v[36:37], 0\n\t" \
  "v_mov_b64 v[38:39], 0\n\t" \
  "v_mov_b64 v[40:41], 0\n\t" \
  "v_mov_b64 v[42:43], 0\n\t" \
  "v_mov_b32 v73, 0\n\t" \
  "v_lshrrev_b32 v71, 1, v69\n\t" \
  "v_lshrrev_b32 v72, 3, v69\n\t" \
  "v_and_b32 v72, 0x78, v72\n\t" \
  "v_and_b32 v74, 0x30, v69\n\t" \
  "v_lshl_add_u32 v72, v74, 3, v72\n\t" \
  "s_add_u32 s54, s53, s36\n\t" \
  "s_add_u32 s55, s54, 524288\n\t" \
  "s_add_u32 s54, s54, 0\n\t" \
  "s_add_u32 s75, s54, 262144\n\t" \
  "s_add_u32 s80, s53, s44\n\t" \
  "s_or_b32 s87, s53, 0x1fff80\n\t" \
  "s_add_u32 s87, s87, 2097152\n\t" \
  CH_TC0 \
  "s_waitcnt vmcnt(9)\n\t" \
  "s_barrier\n\t" \
  CH_DP0 \
  "ds_read_b128 v[0:3], v69 offset:0\n\t" \
  "ds_read_b128 v[4:7], v69 offset:1024\n\t" \
  "ds_read_b128 v[8:11], v69 offset:2048\n\t" \
  "ds_read_b128 v[12:15], v69 offset:3072\n\t" \
  "s_waitcnt lgkmcnt(3)\n\t" \
  "v_smfmac_f32_16x16x64_bf16 v[36:39], v[0:3], v[44:51], v68\n\t" \
  "ds_read_b128 v[44:47], v70 offset:4096\n\t" \
  "v_smfmac_f32_16x16x64_bf16 v[40:43], v[0:3], v[60:67], v68\n\t" \
  "ds_read_b128 v[48:51], v70 offset:5120\n\t" \
  "v_max3_u32 v73, v73, v0, v2\n\t" \
  "s_waitcnt lgkmcnt(4)\n\t" \
  "v_smfmac_f32_16x16x64_bf16 v[36:39], v[4:7], a[0:7], v68\n\t" \
  "s_waitcnt vmcnt(4)\n\t" \
  "s_barrier\n\t" \
  "ds_read_b128 v[16:19], v69 offset:4096\n\t" \
  CH_DP1 \
  "v_smfmac_f32_16x16x64_bf16 v[40:43], v[4:7], a[64:71], v68\n\t" \
  "ds_read_b128 v[20:23], v69 offset:5120\n\t" \
  "v_max3_u32 v73, v73, v4, v6\n\t" \
  "s_waitcnt lgkmcnt(5)\n\t" \
  "v_smfmac_f32_16x16x64_bf16 v[36:39], v[8:11], a[8:15], v68\n\t" \
  "ds_read_b128 v[24:27], v69 offset:6144\n\t" \
  "v_max3_u32 v73, v73, v8, v10\n\t" \
  "v_smfmac_f32_16x16x64_bf16 v[40:43], v[8:11], a[72:79], v68\n\t" \
  "ds_read_b128 v[28:31], v69 offset:7168\n\t" \
  "s_waitcnt lgkmcnt(6)\n\t" \
  "v_max3_u32 v73, v73, v12, v14\n\t" \
  "v_smfmac_f32_16x16x64_bf16 v[36:39], v[12:15], a[16:23], v68\n\t" \
  "ds_read_b128 v[32:35], v69 offset:8192\n\t" \
  "s_waitcnt lgkmcnt(5)\n\t" \
  "v_smfmac_f32_16x16x64_bf16 v[40:43], v[12:15], v[44:51], v68\n\t" \
  "ds_read_b128 v[60:63], v70 offset:6144\n\t" \
  "s_waitcnt lgkmcnt(5)\n\t" \
  "v_smfmac_f32_16x16x64_bf16 v[36:39], v[16:19], a[24:31], v68\n\t" \
  "ds_read_b128 v[64:67], v70 offset:7168\n\t" \
  "v_max3_u32 v73, v73, v16, v18\n\t" \
  "v_smfmac_f32_16x16x64_bf16 v[40:43], v[16:19], a[80:87], v68\n\t" \
  "s_waitcnt lgkmcnt(5)\n\t" \
  "v_max3_u32 v73, v73, v20, v22\n\t" \
  "v_smfmac_f32_16x16x64_bf16 v[36:39], v[20:23], a[32:39], v68\n\t" \
  "s_waitcnt lgkmcnt(4)\n\t" \
  "v_max3_u32 v73, v73, v24, v26\n\t" \
  "v_smfmac_f32_16x16x64_bf16 v[36:39], v[24:27], a[40:47], v68\n\t" \
  "s_waitcnt lgkmcnt(3)\n\t" \
  "v_max3_u32 v73, v73, v28, v30\n\t" \
  "v_smfmac_f32_16x16x64_bf16 v[36:39], v[28:31], a[48:55], v68\n\t" \
  "s_waitcnt lgkmcnt(2)\n\t" \
  "v_max3_u32 v73, v73, v32, v34\n\t" \
  "v_smfmac_f32_16x16x64_bf16 v[36:39], v[32:35], a[56:63], v68\n\t" \
  "v_cmp_eq_u32 vcc, -1, v73\n\t" \
  "s_waitcnt lgkmcnt(0)\n\t" \
  "v_smfmac_f32_16x16x64_bf16 v[40:43], v[20:23], v[60:67], v68\n\t" \
  "ds_read_b128 v[60:63], v70 offset:8192\n\t" \
  "v_mov_b64 v[44:45], 0\n\t" \
  "s_cbranch_vccnz LFAIL0_%=\n\t" \
  "LGO0_%=:\n\t" \
  "v_smfmac_f32_16x16x64_bf16 v[40:43], v[24:27], a[88:95], v68\n\t" \
  "ds_read_b128 v[64:67], v70 offset:9216\n\t" \
  "v_mov_b64 v[46:47], 0\n\t" \
  "v_smfmac_f32_16x16x64_bf16 v[40:43], v[28:31], a[96:103], v68\n\t" \
  "v_add_f32 v74, v36, v38\n\t" \
  "v_smfmac_f32_16x16x64_bf16 v[40:43], v[32:35], a[104:111], v68\n\t" \
  "v_add_f32 v37, v37, v39\n\t" \
  CH_DP2 \
  "v_smfmac_f32_16x16x64_bf16 v[44:47], v[0:3], a[112:119], v68\n\t" \
  "v_cvt_pk_bf16_f32 v74, v74, v37\n\t" \
  "v_smfmac_f32_16x16x64_bf16 v[44:47], v[4:7], a[120:127], v68\n\t" \
  "v_add_f32 v75, v40, v42\n\t" \
  "s_waitcnt lgkmcnt(0)\n\t" \
  "v_smfmac_f32_16x16x64_bf16 v[44:47], v[8:11], v[60:67], v68\n\t" \
  "ds_read_b128 v[60:63], v70 offset:10240\n\t" \
  "v_add_f32 v41, v41, v43\n\t" \
  "v_cvt_pk_bf16_f32 v75, v75, v41\n\t" \
  "v_smfmac_f32_16x16x64_bf16 v[44:47], v[12:15], a[128:135], v68\n\t" \
  "ds_write2st64_b32 v71, v74, v75 offset0:36 offset1:38\n\t" \
  "v_mov_b64 v[48:49], 0\n\t" \
  "v_smfmac_f32_16x16x64_bf16 v[44:47], v[16:19], a[136:143], v68\n\t" \
  "ds_read_b128 v[64:67], v70 offset:11264\n\t" \
  "v_mov_b64 v[50:51], 0\n\t" \
  "v_smfmac_f32_16x16x64_bf16 v[44:47], v[20:23], a[144:151], v68\n\t" \
  "v_smfmac_f32_16x16x64_bf16 v[44:47], v[24:27], a[152:159], v68\n\t" \
  "v_smfmac_f32_16x16x64_bf16 v[44:47], v[28:31], a[160:167], v68\n\t" \
  "s_waitcnt lgkmcnt(0)\n\t" \
  "v_smfmac_f32_16x16x64_bf16 v[44:47], v[32:35], v[60:67], v68\n\t" \
  "buffer_load_dwordx4 v[60:63], v69, s[28:31], s64 offen sc1\n\t" \
  "s_barrier\n\t" \
  "ds_read_b64_tr_b16 v[76:77], v72 offset:9216\n\t" \
  CH_DP3 \
  "v_smfmac_f32_16x16x64_bf16 v[48:51], v[0:3], a[168:175], v68\n\t" \
  "ds_read_b64_tr_b16 v[78:79], v72 offset:9728\n\t" \
  "v_smfmac_f32_16x16x64_bf16 v[48:51], v[4:7], a[176:183], v68\n\t" \
  "v_add_f32 v74, v44, v46\n\t" \
  "v_smfmac_f32_16x16x64_bf16 v[48:51], v[8:11], a[184:191], v68\n\t" \
  "v_add_f32 v45, v45, v47\n\t" \
  "v_smfmac_f32_16x16x64_bf16 v[48:51], v[12:15], a[192:199], v68\n\t" \
  "v_cvt_pk_bf16_f32 v74, v74, v45\n\t" \
  "s_waitcnt vmcnt(4)\n\t" \
  "s_waitcnt lgkmcnt(0)\n\t" \
  "v_mfma_f32_16x16x32_bf16 v[36:39], v[52:55], v[76:79], 0\n\t" \
  "buffer_load_dwordx4 v[64:67], v69, s[28:31], s64 offen offset:1024 sc1\n\t" \
  "s_waitcnt vmcnt(4)\n\t" \
  "v_mfma_f32_16x16x32_bf16 v[40:43], v[56:59], v[76:79], 0\n\t" \
  "v_mov_b64 v[52:53], 0\n\t" \
  "v_mov_b64 v[54:55], 0\n\t" \
  "v_smfmac_f32_16x16x64_bf16 v[48:51], v[16:19], a[200:207], v68\n\t" \
  "v_mov_b64 v[56:57], 0\n\t" \
  "v_smfmac_f32_16x16x64_bf16 v[48:51], v[20:23], a[208:215], v68\n\t" \
  "v_add_f32_dpp v36, v40, v36 quad_perm:[2,3,0,1] row_mask:0xf bank_mask:0xf\n\t" \
  "v_smfmac_f32_16x16x64_bf16 v[48:51], v[24:27], a[216:223], v68\n\t" \
  "v_add_f32_dpp v37, v41, v37 quad_perm:[2,3,0,1] row_mask:0xf bank_mask:0xf\n\t" \
  "v_smfmac_f32_16x16x64_bf16 v[48:51], v[28:31], a[224:231], v68\n\t" \
  "v_add_f32_dpp v38, v42, v38 quad_perm:[2,3,0,1] row_mask:0xf bank_mask:0xf\n\t" \
  "v_smfmac_f32_16x16x64_bf16 v[48:51], v[32:35], a[232:239], v68\n\t" \
  "v_add_f32_dpp v39, v43, v39 quad_perm:[2,3,0,1] row_mask:0xf bank_mask:0xf\n\t" \
  CH_DP4 \
  "v_smfmac_f32_16x16x64_bf16 v[52:55], v[0:3], a[240:247], v68\n\t" \
  "s_mov_b64 exec, s[58:59]\n\t" \
  "ds_write_b128 v69, v[36:39] offset:2048\n\t" \
  "s_mov_b64 exec, -1\n\t" \
  "v_mov_b64 v[58:59], 0\n\t" \
  "v_smfmac_f32_16x16x64_bf16 v[52:55], v[4:7], a[248:255], v68\n\t" \
  "buffer_load_dword off, s[28:31], s87 sc1 lds\n\t" \
  "v_add_f32 v75, v48, v50\n\t" \
  "v_add_f32 v49, v49, v51\n\t" \
  "v_cvt_pk_bf16_f32 v75, v75, v49\n\t" \
  "v_smfmac_f32_16x16x64_bf16 v[56:59], v[0:3], v[136:143], v68\n\t" \
  "ds_write2st64_b32 v71, v74, v75 offset0:0 offset1:2\n\t" \
  "v_smfmac_f32_16x16x64_bf16 v[56:59], v[4:7], v[144:151], v68\n\t" \
  "ds_read_b64_tr_b16 v[0:1], v72 offset:10240\n\t" \
  "ds_read_b64_tr_b16 v[2:3], v72 offset:10752\n\t" \
  "v_smfmac_f32_16x16x64_bf16 v[52:55], v[8:11], v[80:87], v68\n\t" \
  "ds_read_b128 v[36:39], v70 offset:12288\n\t" \
  "v_smfmac_f32_16x16x64_bf16 v[56:59], v[8:11], v[152:159], v68\n\t" \
  "ds_read_b128 v[40:43], v70 offset:13312\n\t" \
  "v_smfmac_f32_16x16x64_bf16 v[52:55], v[12:15], v[88:95], v68\n\t" \
  "s_waitcnt lgkmcnt(4)\n\t" \
  "s_barrier\n\t" \
  CH_DP5 \
  "ds_read_b64_tr_b16 v[44:45], v72 offset:0\n\t" \
  "ds_read_b64_tr_b16 v[46:47], v72 offset:512\n\t" \
  "v_smfmac_f32_16x16x64_bf16 v[56:59], v[12:15], v[160:167], v68\n\t" \
  "ds_read_b64_tr_b16 v[48:49], v72 offset:1024\n\t" \
  "ds_read_b64_tr_b16 v[50:51], v72 offset:1536\n\t" \
  "v_smfmac_f32_16x16x64_bf16 v[52:55], v[16:19], v[96:103], v68\n\t" \
  "ds_read_b128 v[4:7], v69 offset:2048\n\t" \
  "s_waitcnt lgkmcnt(5)\n\t" \
  "v_smfmac_f32_16x16x64_bf16 v[56:59], v[16:19], v[36:43], v68\n\t" \
  "ds_read_b128 v[8:11], v69 offset:3072\n\t" \
  "s_waitcnt lgkmcnt(4)\n\t" \
  "v_mfma_f32_16x16x32_bf16 v[12:15], v[44:47], v[76:79], 0\n\t" \
  "ds_read_b128 v[36:39], v70 offset:14336\n\t" \
  "s_waitcnt lgkmcnt(3)\n\t" \
  "v_mfma_f32_16x16x32_bf16 v[12:15], v[48:51], v[0:3], v[12:15]\n\t" \
  "ds_read_b128 v[40:43], v70 offset:15360\n\t" \
  "v_smfmac_f32_16x16x64_bf16 v[52:55], v[20:23], v[104:111], v68\n\t" \
  "s_waitcnt lgkmcnt(2)\n\t" \
  "v_add_f32 v4, v4, v8\n\t" \
  "v_add_f32 v5, v5, v9\n\t" \
  "v_add_f32 v6, v6, v10\n\t" \
  CH_DP6 \
  "v_cndmask_b32 v78, v13, v14, s[56:57]\n\t" \
  "v_cndmask_b32 v76, v12, v15, s[56:57]\n\t" \
  "v_add_f32 v7, v7, v11\n\t" \
  "v_max3_f32 v77, v4, v5, v6\n\t" \
  "v_add_f32_dpp v76, v78, v76 quad_perm:[2,3,0,1] row_mask:0xf bank_mask:0xf\n\t" \
  "v_max3_f32 v77, v77, v7, v76\n\t" \
  "v_smfmac_f32_16x16x64_bf16 v[56:59], v[20:23], v[168:175], v68\n\t" \
  "ds_read_b128 v[44:47], v70 offset:16384\n\t" \
  "v_max_f32_dpp v77, v77, v77 quad_perm:[2,3,0,1] row_mask:0xf bank_mask:0xf\n\t" \
  "v_mov_b32 v78, v77\n\t" \
  "v_smfmac_f32_16x16x64_bf16 v[52:55], v[24:27], v[112:119], v68\n\t" \
  "ds_read_b128 v[48:51], v70 offset:17408\n\t" \
  "v_permlane16_swap_b32 v77, v78\n\t" \
  "v_max_f32 v77, v77, v78\n\t" \
  "v_mov_b32 v78, v77\n\t" \
  "v_smfmac_f32_16x16x64_bf16 v[52:55], v[28:31], v[120:127], v68\n\t" \
  "ds_read_b128 v[16:19], v70 offset:18432\n\t" \
  "v_permlane32_swap_b32 v77, v78\n\t" \
  "v_max_f32 v77, v77, v78\n\t" \
  "v_smfmac_f32_16x16x64_bf16 v[52:55], v[32:35], v[128:135], v68\n\t" \
  "ds_read_b128 v[20:23], v70 offset:19456\n\t" \
  "v_mul_f32 v77, 0xbe0293ee, v77\n\t" \
  CH_DP7 \
  "v_smfmac_f32_16x16x64_bf16 v[56:59], v[24:27], v[176:183], v68\n\t" \
  "v_fmamk_f32 v4, v4, 0x3e0293ee, v77\n\t" \
  "v_smfmac_f32_16x16x64_bf16 v[56:59], v[28:31], v[184:191], v68\n\t" \
  "ds_read_b128 v[24:27], v70 offset:22528\n\t" \
  "v_fmamk_f32 v5, v5, 0x3e0293ee, v77\n\t" \
  "s_waitcnt lgkmcnt(5)\n\t" \
  "v_smfmac_f32_16x16x64_bf16 v[56:59], v[32:35], v[36:43], v68\n\t" \
  "ds_read_b128 v[28:31], v70 offset:23552\n\t" \
  "v_fmamk_f32 v6, v6, 0x3e0293ee, v77\n\t" \
  "v_fmamk_f32 v7, v7, 0x3e0293ee, v77\n\t" \
  "v_fmamk_f32 v76, v76, 0x3e0293ee, v77\n\t" \
  "v_exp_f32 v4, v4\n\t" \
  "v_exp_f32 v5, v5\n\t" \
  "v_exp_f32 v6, v6\n\t" \
  "v_exp_f32 v7, v7\n\t" \
  "v_cvt_pk_bf16_f32 v4, v4, v5\n\t" \
  "v_exp_f32 v76, v76\n\t" \
  "v_cvt_pk_bf16_f32 v5, v6, v7\n\t" \
  "v_mov_b32_dpp v12, v4 quad_perm:[0,1,0,1] row_mask:0xf bank_mask:0xf\n\t" \
  "v_mov_b32_dpp v14, v4 quad_perm:[2,3,2,3] row_mask:0xf bank_mask:0xf\n\t" \
  "v_mov_b32_dpp v13, v5 quad_perm:[0,1,0,1] row_mask:0xf bank_mask:0xf\n\t" \
  "v_mov_b32_dpp v15, v5 quad_perm:[2,3,2,3] row_mask:0xf bank_mask:0xf\n\t" \
  "v_add_f32 v52, v52, v54\n\t" \
  "v_add_f32 v53, v53, v55\n\t" \
  "s_waitcnt vmcnt(2)\n\t" \
  CH_DP8 \
  "v_mfma_f32_16x16x32_bf16 v[0:3], v[60:63], v[12:15], 0\n\t" \
  "ds_read_b128 v[36:39], v70 offset:20480\n\t" \
  "v_add_f32 v56, v56, v58\n\t" \
  "v_add_f32 v57, v57, v59\n\t" \
  "v_cvt_pk_bf16_f32 v52, v52, v53\n\t" \
  "v_cvt_pk_bf16_f32 v53, v56, v57\n\t" \
  "s_waitcnt vmcnt(1)\n\t" \
  "v_mfma_f32_16x16x32_bf16 v[4:7], v[64:67], v[12:15], 0\n\t" \
  "buffer_store_dwordx2 v[52:53], v71, s[28:31], s69 offen offset:2048 sc1\n\t" \
  "ds_read_b128 v[40:43], v70 offset:21504\n\t" \
  "v_mov_b64 v[56:57], s[88:89]\n\t" \
  "v_mov_b64 v[58:59], s[88:89]\n\t" \
  "v_mov_b32_dpp v78, v76 quad_perm:[0,1,0,1] row_mask:0xf bank_mask:0xf\n\t" \
  "v_mov_b32_dpp v79, v76 quad_perm:[2,3,2,3] row_mask:0xf bank_mask:0xf\n\t" \
  "v_mfma_f32_16x16x32_bf16 v[32:35], v[56:59], v[12:15], 0\n\t" \
  "ds_read_b128 v[60:63], v70 offset:24576\n\t" \
  "v_cvt_pk_bf16_f32 v11, v78, v79\n\t" \
  "v_cvt_pk_bf16_f32 v8, v78, v79\n\t" \
  "v_mov_b64 v[54:55], 0\n\t" \
  "v_mov_b32 v9, 0\n\t" \
  "v_mov_b32 v10, 0\n\t" \
  "v_mov_b64 v[14:15], 0\n\t" \
  "s_cmp_eq_u32 s48, 63\n\t" \
  "v_mfma_f32_16x16x32_bf16 v[32:35], v[54:57], v[8:11], v[32:35]\n\t" \
  "buffer_load_dword v75, off, s[28:31], s80 offset:1024 sc1\n\t" \
  "v_mfma_f32_16x16x32_bf16 v[0:3], v[52:55], v[8:11], v[0:3]\n\t" \
  "ds_read_b128 v[64:67], v70 offset:25600\n\t" \
  "v_mov_b64 v[56:57], 0\n\t" \
  "v_mov_b64 v[58:59], 0\n\t" \
  "v_mfma_f32_16x16x32_bf16 v[4:7], v[52:55], v[10:13], v[4:7]\n\t" \
  "s_cbranch_scc1 LLAST0_%=\n\t" \
  "v_rcp_f32 v74, v32\n\t" \
  "v_mov_b64 v[12:13], 0\n\t" \
  "v_mov_b32 v8, 0\n\t" \
  "v_mul_f32 v0, v0, v74\n\t" \
  "v_mul_f32 v1, v1, v74\n\t" \
  "v_mul_f32 v2, v2, v74\n\t" \
  "v_mul_f32 v3, v3, v74\n\t" \
  "v_cvt_pk_bf16_f32 v76, v0, v1\n\t" \
  "v_cvt_pk_bf16_f32 v77, v2, v3\n\t" \
  "v_pk_mul_f32 v[4:5], v[4:5], v[74:75] op_sel_hi:[1,0]\n\t" \
  "v_pk_mul_f32 v[6:7], v[6:7], v[74:75] op_sel_hi:[1,0]\n\t" \
  "v_cvt_pk_bf16_f32 v78, v4, v5\n\t" \
  "v_cvt_pk_bf16_f32 v79, v6, v7\n\t" \
  "ds_write_b128 v69, v[76:79] offset:4096\n\t" \
  CH_DP9 \
  "s_waitcnt lgkmcnt(9)\n\t" \
  "v_smfmac_f32_16x16x64_bf16 v[12:15], v[76:79], v[44:51], v68\n\t" \
  "ds_read_b128 v[44:47], v70 offset:26624\n\t" \
  "v_mov_b32 v11, 0\n\t" \
  "s_waitcnt lgkmcnt(8)\n\t" \
  "v_smfmac_f32_16x16x64_bf16 v[56:59], v[76:79], v[16:23], v68\n\t" \
  "ds_read_b128 v[48:51], v70 offset:27648\n\t" \
  "v_mov_b64 v[52:53], 0\n\t" \
  "s_waitcnt lgkmcnt(5)\n\t" \
  "v_smfmac_f32_16x16x64_bf16 v[8:11], v[76:79], v[36:43], v68\n\t" \
  "ds_read_b128 v[16:19], v70 offset:28672\n\t" \
  "v_and_b32 v73, 32, v69\n\t" \
  "v_smfmac_f32_16x16x64_bf16 v[52:55], v[76:79], v[24:31], v68\n\t" \
  "s_waitcnt lgkmcnt(3)\n\t" \
  "s_barrier\n\t" \
  CH_DP10 \
  "ds_read_b128 v[0:3], v69 offset:5120\n\t" \
  "ds_read_b128 v[4:7], v69 offset:6144\n\t" \
  "v_lshl_add_u32 v73, v73, 5, v69\n\t" \
  "v_lshrrev_b32 v71, 7, v69\n\t" \
  "v_and_b32 v74, 0x70, v69\n\t" \
  "v_lshl_add_u32 v71, v71, 14, v74\n\t" \
  "v_bfe_u32 v72, v69, 2, 7\n\t" \
  "v_and_b32 v74, 0x200, v69\n\t" \
  "s_waitcnt lgkmcnt(1)\n\t" \
  "v_smfmac_f32_16x16x64_bf16 v[8:11], v[0:3], v[60:67], v68\n\t" \
  "ds_read_b128 v[20:23], v70 offset:29696\n\t" \
  "v_mov_b64 v[76:77], 0\n\t" \
  "v_smfmac_f32_16x16x64_bf16 v[12:15], v[0:3], v[200:207], v68\n\t" \
  "ds_read_b128 v[32:35], v69 offset:7168\n\t" \
  "v_mov_b64 v[78:79], 0\n\t" \
  "v_smfmac_f32_16x16x64_bf16 v[52:55], v[0:3], v[44:51], v68\n\t" \
  "ds_read_b128 v[24:27], v70 offset:30720\n\t" \
  "v_lshl_add_u32 v72, v74, 3, v72\n\t" \
  "v_smfmac_f32_16x16x64_bf16 v[56:59], v[0:3], v[232:239], v68\n\t" \
  "ds_read_b128 v[28:31], v70 offset:31744\n\t" \
  "s_waitcnt lgkmcnt(4)\n\t" \
  "v_smfmac_f32_16x16x64_bf16 v[12:15], v[4:7], v[208:215], v68\n\t" \
  "ds_read_b128 v[60:63], v70 offset:32768\n\t" \
  "s_waitcnt lgkmcnt(4)\n\t" \
  "v_smfmac_f32_16x16x64_bf16 v[8:11], v[4:7], v[16:23], v68\n\t" \
  "ds_read_b128 v[64:67], v70 offset:33792\n\t" \
  "s_waitcnt lgkmcnt(4)\n\t" \
  "v_smfmac_f32_16x16x64_bf16 v[8:11], v[32:35], v[192:199], v68\n\t" \
  "ds_read_b128 v[36:39], v73 offset:4096\n\t" \
  "v_smfmac_f32_16x16x64_bf16 v[12:15], v[32:35], v[216:223], v68\n\t" \
  "ds_read_b128 v[40:43], v73 offset:6144\n\t" \
  "s_waitcnt lgkmcnt(4)\n\t" \
  "v_smfmac_f32_16x16x64_bf16 v[52:55], v[4:7], v[24:31], v68\n\t" \
  "ds_read_b128 v[44:47], v70 offset:34816\n\t" \
  "v_cvt_pk_bf16_f32 v8, v8, v9\n\t" \
  "v_smfmac_f32_16x16x64_bf16 v[52:55], v[32:35], v[224:231], v68\n\t" \
  "ds_read_b128 v[48:51], v70 offset:35840\n\t" \
  "v_cvt_pk_bf16_f32 v9, v10, v11\n\t" \
  "v_smfmac_f32_16x16x64_bf16 v[56:59], v[4:7], v[240:247], v68\n\t" \
  "v_cvt_pk_bf16_f32 v10, v12, v13\n\t" \
  "s_waitcnt lgkmcnt(4)\n\t" \
  "v_smfmac_f32_16x16x64_bf16 v[56:59], v[32:35], v[60:67], v68\n\t" \
  "v_cvt_pk_bf16_f32 v11, v14, v15\n\t" \
  "s_waitcnt lgkmcnt(3)\n\t" \
  "v_smfmac_f32_16x16x64_bf16 v[76:79], v[36:39], v[248:255], v68\n\t" \
  "buffer_store_dwordx4 v[8:11], v71, s[28:31], s54 offen sc1\n\t" \
  CH_DP11 \
  "v_cvt_pk_bf16_f32 v52, v52, v53\n\t" \
  "s_waitcnt lgkmcnt(0)\n\t" \
  "v_smfmac_f32_16x16x64_bf16 v[76:79], v[40:43], v[44:51], v68\n\t" \
  "v_cvt_pk_bf16_f32 v53, v54, v55\n\t" \
  "v_cvt_pk_bf16_f32 v54, v56, v57\n\t" \
  "v_cvt_pk_bf16_f32 v55, v58, v59\n\t" \
  "buffer_store_dwordx4 v[52:55], v71, s[28:31], s75 offen sc1\n\t" \
  "s_nop 2\n\t" \
  "v_add_f32 v76, v76, v78\n\t" \
  "v_add_f32 v77, v77, v79\n\t" \
  "v_cvt_pk_bf16_f32 v76, v76, v77\n\t" \
  "buffer_store_dword v76, v72, s[28:31], s55 offen sc1\n\t" \
  CH_DP12 \
  "s_memrealtime s[70:71]\n\t" \
  "s_waitcnt vmcnt(3)\n\t" \
  "v_readfirstlane_b32 s65, v75\n\t" \
  "s_barrier\n\t" \
  "s_waitcnt lgkmcnt(0)\n\t" \
  "s_not_b32 s65, s65\n\t" \
  "s_lshl_b32 s61, s70, 8\n\t" \
  "s_andn2_b32 s64, s65, 0xff\n\t" \
  "s_sub_u32 s64, s64, s61\n\t" \
  "s_ashr_i32 s64, s64, 8\n\t" \
  "s_cmp_eq_u32 s65, 0\n\t" \
  "s_cselect_b32 s64, " CH_DLF ", s64\n\t" \
  "s_cselect_b32 s65, " CH_B0 ", s65\n\t" \
  "s_add_u32 s47, s64, s70\n\t" \
  "s_min_i32 s64, s64, s34\n\t" \
  "s_max_i32 s64, s64, " CH_DLL1 "\n\t" \
  "s_add_u32 s64, s64, s70\n\t" \
  CH_ST2 \
  "s_add_u32 s55, s64, " CH_FDL "\n\t" \
  "s_movk_i32 s60, 8192\n\t" \
  "FWT_%=:\n\t" \
  "s_memrealtime s[72:73]\n\t" \
  "s_sub_u32 s60, s60, 1\n\t" \
  "s_cselect_b32 s61, 0, -1\n\t" \
  "s_waitcnt lgkmcnt(0)\n\t" \
  "s_sub_i32 s73, s72, s55\n\t" \
  "s_and_b32 s73, s73, s61\n\t" \
  "s_cmp_lt_i32 s73, 0\n\t" \
  "s_cbranch_scc1 FWT_%=\n\t" \
  "s_mov_b64 exec, 1\n\t" \
  "buffer_store_dword v68, off, s[28:31], s51 offset:4064 sc1\n\t" \
  "s_mov_b64 exec, -1\n\t" \
  CH_TCH \
  "s_and_b32 s65, s65, 0xff\n\t" \
  "s_add_u32 s47, s47, s65\n\t" \
  "s_add_u32 s65, s65, s64\n\t" \
  "s_sub_u32 s60, s70, s66\n\t" \
  "s_add_u32 s61, s81, " CH_UP "\n\t" \
  "s_min_u32 s60, s60, s61\n\t" \
  "s_sub_u32 s61, s81, " CH_DN "\n\t" \
  "s_max_u32 s60, s60, s61\n\t" \
  "s_cmp_eq_u32 s68, 0\n\t" \
  "s_cselect_b32 s81, s60, s81\n\t" \
  CH_TRC \
  "s_add_u32 s48, s48, 1\n\t" \
  "s_mov_b32 s49, s53\n\t" \
  "s_add_u32 s53, s53, " CH_SLOT "\n\t" \
  "s_add_u32 s60, s53, " CH_SKIP "\n\t" \
  "s_lshr_b32 s61, s96, 1\n\t" \
  "s_add_u32 s86, s86, 1\n\t" \
  "s_cmp_eq_u32 s86, 3\n\t" \
  "s_cselect_b32 s53, s60, s53\n\t" \
  "s_cselect_b32 s96, s61, s96\n\t" \
  "s_cselect_b32 s86, 0, s86\n\t" \
  "s_andn2_b32 s53, s53, 0x2000\n\t" \
  "s_and_b32 s60, s96, 1\n\t" \
  "s_lshl_b32 s60, s60, 13\n\t" \
  "s_or_b32 s53, s53, s60\n\t" \
  "s_mov_b32 s68, 0\n\t" \
  "s_add_u32 s82, s48, 0\n\t" \
  CH_SEGC \
  "s_lshr_b64 s[60:61], s[90:91], s82\n\t" \
  "s_and_b32 s60, s60, 1\n\t" \
  "s_lshl_b32 s60, s60, 13\n\t" \
  "s_add_u32 s82, s82, " CH_SG0 "\n\t" \
  "s_lshl_b32 s82, s82, 21\n\t" \
  "s_or_b32 s82, s82, s60\n\t" \
  "s_add_u32 s50, s82, s38\n\t" \
  "s_add_u32 s69, s82, s39\n\t" \
  "s_add_u32 s51, s82, s43\n\t" \
  "s_cmp_lg_u32 s34, 0\n\t" \
  "s_cbranch_scc1 LW0_%=\n\t" \
  "s_barrier\n\t" \
  "s_branch LLOAD_%=\n\t" \
  "LFAIL0_%=:\n\t" \
  "s_add_u32 s68, s68, 1\n\t" \
  "s_waitcnt lgkmcnt(0)\n\t" \
  "s_cmp_gt_u32 s68, s83\n\t" \
  "s_cbranch_scc1 LGV0_%=\n\t" \
  "s_barrier\n\t" \
  "s_memrealtime s[62:63]\n\t" \
  "s_mov_b32 m0, 0\n\t" \
  "s_waitcnt lgkmcnt(0)\n\t" \
  "s_add_u32 s65, s62, " CH_PEN "\n\t" \
  "s_branch LDMA_%=\n\t" \
  "LGV0_%=:\n\t" \
  "s_mov_b32 s83, 0\n\t" \
  "s_lshl_b32 s60, s36, 4\n\t" \
  "s_or_b32 s60, s60, s48\n\t" \
  "s_or_b32 s60, s60, 805306368\n\t" \
  "v_mov_b32 v46, s60\n\t" \
  "v_mov_b32 v47, 0\n\t" \
  "s_mov_b64 exec, 1\n\t" \
  "buffer_atomic_cmpswap v[46:47], off, s[28:31], s92 sc1\n\t" \
  "s_mov_b64 exec, -1\n\t" \
  "s_branch LGO0_%=\n\t" \
  "LLAST0_%=:\n\t" \
  "s_waitcnt vmcnt(0) lgkmcnt(0)\n\t" \
  "s_barrier\n\t" \
  "s_mov_b64 exec, 1\n\t" \
  "buffer_store_dword v68, off, s[28:31], s51 offset:4064 sc1\n\t" \
  "s_mov_b64 exec, -1\n\t" \
  "s_branch LEND_%=\n\t" \
  ".p2align 6\n\t" \
  "LG1_%=:\n\t" \
  "s_barrier\n\t" \
  "buffer_load_dwordx4 v[52:55], v69, s[28:31], s50 offen sc1\n\t" \
  "buffer_load_dwordx4 v[56:59], v69, s[28:31], s50 offen offset:1024 sc1\n\t" \
  "s_add_u32 s64, s50, 14336\n\t" \
  "s_movk_i32 m0, 11264\n\t" \
  "v_lshlrev_b32 v75, 1, v69\n\t" \
  "buffer_load_dword v75, s[28:31], s64 offen sc1 lds\n\t" \
  "ds_read_b128 v[44:47], v70 offset:0\n\t" \
  "ds_read_b128 v[48:51], v70 offset:1024\n\t" \
  "ds_read_b128 v[60:63], v70 offset:2048\n\t" \
  "ds_read_b128 v[64:67], v70 offset:3072\n\t" \
  "v_mov_b64 v[36:37], 0\n\t" \
  "v_mov_b64 v[38:39], 0\n\t" \
  "v_mov_b64 v[40:41], 0\n\t" \
  "v_mov_b64 v[42:43], 0\n\t" \
  "v_mov_b32 v73, 0\n\t" \
  "v_lshrrev_b32 v71, 1, v69\n\t" \
  "v_add_u32 v71, 4, v71\n\t" \
  "v_lshrrev_b32 v72, 3, v69\n\t" \
  "v_and_b32 v72, 0x78, v72\n\t" \
  "v_and_b32 v74, 0x30, v69\n\t" \
  "v_lshl_add_u32 v72, v74, 3, v72\n\t" \
  "s_add_u32 s54, s53, s36\n\t" \
  "s_add_u32 s55, s54, 540672\n\t" \
  "s_add_u32 s54, s54, 4096\n\t" \
  "s_add_u32 s75, s54, 262144\n\t" \
  "s_add_u32 s80, s53, s44\n\t" \
  "s_barrier\n\t" \
  CH_DP0 \
  "ds_read_b128 v[0:3], v69 offset:0\n\t" \
  "ds_read_b128 v[4:7], v69 offset:1024\n\t" \
  "ds_read_b128 v[8:11], v69 offset:2048\n\t" \
  "ds_read_b128 v[12:15], v69 offset:3072\n\t" \
  "s_waitcnt lgkmcnt(3)\n\t" \
  "v_smfmac_f32_16x16x64_bf16 v[36:39], v[0:3], v[44:51], v68\n\t" \
  "ds_read_b128 v[44:47], v70 offset:4096\n\t" \
  "v_smfmac_f32_16x16x64_bf16 v[40:43], v[0:3], v[60:67], v68\n\t" \
  "ds_read_b128 v[48:51], v70 offset:5120\n\t" \
  "v_max3_u32 v73, v73, v0, v2\n\t" \
  "s_waitcnt lgkmcnt(4)\n\t" \
  "v_smfmac_f32_16x16x64_bf16 v[36:39], v[4:7], a[0:7], v68\n\t" \
  "s_barrier\n\t" \
  "ds_read_b128 v[16:19], v69 offset:4096\n\t" \
  CH_DP1 \
  "v_smfmac_f32_16x16x64_bf16 v[40:43], v[4:7], a[64:71], v68\n\t" \
  "ds_read_b128 v[20:23], v69 offset:5120\n\t" \
  "v_max3_u32 v73, v73, v4, v6\n\t" \
  "s_waitcnt lgkmcnt(5)\n\t" \
  "v_smfmac_f32_16x16x64_bf16 v[36:39], v[8:11], a[8:15], v68\n\t" \
  "ds_read_b128 v[24:27], v69 offset:6144\n\t" \
  "v_max3_u32 v73, v73, v8, v10\n\t" \
  "v_smfmac_f32_16x16x64_bf16 v[40:43], v[8:11], a[72:79], v68\n\t" \
  "ds_read_b128 v[28:31], v69 offset:7168\n\t" \
  "s_waitcnt lgkmcnt(6)\n\t" \
  "v_max3_u32 v73, v73, v12, v14\n\t" \
  "v_smfmac_f32_16x16x64_bf16 v[36:39], v[12:15], a[16:23], v68\n\t" \
  "ds_read_b128 v[32:35], v69 offset:8192\n\t" \
  "s_waitcnt lgkmcnt(5)\n\t" \
  "v_smfmac_f32_16x16x64_bf16 v[40:43], v[12:15], v[44:51], v68\n\t" \
  "ds_read_b128 v[60:63], v70 offset:6144\n\t" \
  "s_waitcnt lgkmcnt(5)\n\t" \
  "v_smfmac_f32_16x16x64_bf16 v[36:39], v[16:19], a[24:31], v68\n\t" \
  "ds_read_b128 v[64:67], v70 offset:7168\n\t" \
  "v_max3_u32 v73, v73, v16, v18\n\t" \
  "v_smfmac_f32_16x16x64_bf16 v[40:43], v[16:19], a[80:87], v68\n\t" \
  "s_waitcnt lgkmcnt(5)\n\t" \
  "v_max3_u32 v73, v73, v20, v22\n\t" \
  "v_smfmac_f32_16x16x64_bf16 v[36:39], v[20:23], a[32:39], v68\n\t" \
  "s_waitcnt lgkmcnt(4)\n\t" \
  "v_max3_u32 v73, v73, v24, v26\n\t" \
  "v_smfmac_f32_16x16x64_bf16 v[36:39], v[24:27], a[40:47], v68\n\t" \
  "s_waitcnt lgkmcnt(3)\n\t" \
  "v_max3_u32 v73, v73, v28, v30\n\t" \
  "v_smfmac_f32_16x16x64_bf16 v[36:39], v[28:31], a[48:55], v68\n\t" \
  "s_waitcnt lgkmcnt(2)\n\t" \
  "v_max3_u32 v73, v73, v32, v34\n\t" \
  "v_smfmac_f32_16x16x64_bf16 v[36:39], v[32:35], a[56:63], v68\n\t" \
  "v_cmp_eq_u32 vcc, -1, v73\n\t" \
  "s_waitcnt lgkmcnt(0)\n\t" \
  "v_smfmac_f32_16x16x64_bf16 v[40:43], v[20:23], v[60:67], v68\n\t" \
  "ds_read_b128 v[60:63], v70 offset:8192\n\t" \
  "v_mov_b64 v[44:45], 0\n\t" \
  "s_cbranch_vccnz LFAIL1_%=\n\t" \
  "LGO1_%=:\n\t" \
  "v_smfmac_f32_16x16x64_bf16 v[40:43], v[24:27], a[88:95], v68\n\t" \
  "ds_read_b128 v[64:67], v70 offset:9216\n\t" \
  "v_mov_b64 v[46:47], 0\n\t" \
  "v_smfmac_f32_16x16x64_bf16 v[40:43], v[28:31], a[96:103], v68\n\t" \
  "v_add_f32 v74, v36, v38\n\t" \
  "v_smfmac_f32_16x16x64_bf16 v[40:43], v[32:35], a[104:111], v68\n\t" \
  "v_add_f32 v37, v37, v39\n\t" \
  CH_DP2 \
  "v_smfmac_f32_16x16x64_bf16 v[44:47], v[0:3], a[112:119], v68\n\t" \
  "v_cvt_pk_bf16_f32 v74, v74, v37\n\t" \
  "v_smfmac_f32_16x16x64_bf16 v[44:47], v[4:7], a[120:127], v68\n\t" \
  "v_add_f32 v75, v40, v42\n\t" \
  "s_waitcnt lgkmcnt(0)\n\t" \
  "v_smfmac_f32_16x16x64_bf16 v[44:47], v[8:11], v[60:67], v68\n\t" \
  "ds_read_b128 v[60:63], v70 offset:10240\n\t" \
  "v_add_f32 v41, v41, v43\n\t" \
  "v_cvt_pk_bf16_f32 v75, v75, v41\n\t" \
  "v_smfmac_f32_16x16x64_bf16 v[44:47], v[12:15], a[128:135], v68\n\t" \
  "ds_write2st64_b32 v71, v74, v75 offset0:36 offset1:38\n\t" \
  "v_mov_b64 v[48:49], 0\n\t" \
  "v_smfmac_f32_16x16x64_bf16 v[44:47], v[16:19], a[136:143], v68\n\t" \
  "ds_read_b128 v[64:67], v70 offset:11264\n\t" \
  "v_mov_b64 v[50:51], 0\n\t" \
  "v_smfmac_f32_16x16x64_bf16 v[44:47], v[20:23], a[144:151], v68\n\t" \
  "v_smfmac_f32_16x16x64_bf16 v[44:47], v[24:27], a[152:159], v68\n\t" \
  "v_smfmac_f32_16x16x64_bf16 v[44:47], v[28:31], a[160:167], v68\n\t" \
  "s_waitcnt lgkmcnt(0)\n\t" \
  "v_smfmac_f32_16x16x64_bf16 v[44:47], v[32:35], v[60:67], v68\n\t" \
  "s_barrier\n\t" \
  "ds_read_b64_tr_b16 v[76:77], v72 offset:9216\n\t" \
  CH_DP3 \
  "v_smfmac_f32_16x16x64_bf16 v[48:51], v[0:3], a[168:175], v68\n\t" \
  "buffer_load_dwordx4 v[60:63], v69, s[28:31], s64 offen sc1\n\t" \
  "ds_read_b64_tr_b16 v[78:79], v72 offset:9728\n\t" \
  "v_smfmac_f32_16x16x64_bf16 v[48:51], v[4:7], a[176:183], v68\n\t" \
  "v_add_f32 v74, v44, v46\n\t" \
  "v_smfmac_f32_16x16x64_bf16 v[48:51], v[8:11], a[184:191], v68\n\t" \
  "v_add_f32 v45, v45, v47\n\t" \
  "v_smfmac_f32_16x16x64_bf16 v[48:51], v[12:15], a[192:199], v68\n\t" \
  "v_cvt_pk_bf16_f32 v74, v74, v45\n\t" \
  "s_waitcnt vmcnt(3)\n\t" \
  "s_waitcnt lgkmcnt(0)\n\t" \
  "v_mfma_f32_16x16x32_bf16 v[36:39], v[52:55], v[76:79], 0\n\t" \
  "s_waitcnt vmcnt(2)\n\t" \
  "v_mfma_f32_16x16x32_bf16 v[40:43], v[56:59], v[76:79], 0\n\t" \
  "buffer_load_dwordx4 v[64:67], v69, s[28:31], s64 offen offset:1024 sc1\n\t" \
  "v_mov_b64 v[52:53], 0\n\t" \
  "v_mov_b64 v[54:55], 0\n\t" \
  "v_smfmac_f32_16x16x64_bf16 v[48:51], v[16:19], a[200:207], v68\n\t" \
  "v_mov_b64 v[56:57], 0\n\t" \
  "v_smfmac_f32_16x16x64_bf16 v[48:51], v[20:23], a[208:215], v68\n\t" \
  "v_add_f32_dpp v36, v40, v36 quad_perm:[2,3,0,1] row_mask:0xf bank_mask:0xf\n\t" \
  "v_smfmac_f32_16x16x64_bf16 v[48:51], v[24:27], a[216:223], v68\n\t" \
  "v_add_f32_dpp v37, v41, v37 quad_perm:[2,3,0,1] row_mask:0xf bank_mask:0xf\n\t" \
  "v_smfmac_f32_16x16x64_bf16 v[48:51], v[28:31], a[224:231], v68\n\t" \
  "v_add_f32_dpp v38, v42, v38 quad_perm:[2,3,0,1] row_mask:0xf bank_mask:0xf\n\t" \
  "v_smfmac_f32_16x16x64_bf16 v[48:51], v[32:35], a[232:239], v68\n\t" \
  "v_add_f32_dpp v39, v43, v39 quad_perm:[2,3,0,1] row_mask:0xf bank_mask:0xf\n\t" \
  CH_DP4 \
  "v_smfmac_f32_16x16x64_bf16 v[52:55], v[0:3], a[240:247], v68\n\t" \
  "s_mov_b64 exec, s[58:59]\n\t" \
  "ds_write_b128 v69, v[36:39] offset:2080\n\t" \
  "s_mov_b64 exec, -1\n\t" \
  "v_mov_b64 v[58:59], 0\n\t" \
  "v_smfmac_f32_16x16x64_bf16 v[52:55], v[4:7], a[248:255], v68\n\t" \
  "v_add_f32 v75, v48, v50\n\t" \
  "v_add_f32 v49, v49, v51\n\t" \
  "v_cvt_pk_bf16_f32 v75, v75, v49\n\t" \
  "v_smfmac_f32_16x16x64_bf16 v[56:59], v[0:3], v[136:143], v68\n\t" \
  "ds_write2st64_b32 v71, v74, v75 offset0:0 offset1:2\n\t" \
  "v_smfmac_f32_16x16x64_bf16 v[56:59], v[4:7], v[144:151], v68\n\t" \
  "ds_read_b64_tr_b16 v[0:1], v72 offset:10240\n\t" \
  "ds_read_b64_tr_b16 v[2:3], v72 offset:10752\n\t" \
  "v_smfmac_f32_16x16x64_bf16 v[52:55], v[8:11], v[80:87], v68\n\t" \
  "ds_read_b128 v[36:39], v70 offset:12288\n\t" \
  "v_smfmac_f32_16x16x64_bf16 v[56:59], v[8:11], v[152:159], v68\n\t" \
  "ds_read_b128 v[40:43], v70 offset:13312\n\t" \
  "v_smfmac_f32_16x16x64_bf16 v[52:55], v[12:15], v[88:95], v68\n\t" \
  "s_waitcnt lgkmcnt(4)\n\t" \
  "s_barrier\n\t" \
  CH_DP5 \
  "ds_read_b64_tr_b16 v[44:45], v72 offset:0\n\t" \
  "ds_read_b64_tr_b16 v[46:47], v72 offset:512\n\t" \
  "v_smfmac_f32_16x16x64_bf16 v[56:59], v[12:15], v[160:167], v68\n\t" \
  "ds_read_b64_tr_b16 v[48:49], v72 offset:1024\n\t" \
  "ds_read_b64_tr_b16 v[50:51], v72 offset:1536\n\t" \
  "v_smfmac_f32_16x16x64_bf16 v[52:55], v[16:19], v[96:103], v68\n\t" \
  "ds_read_b128 v[4:7], v69 offset:2048\n\t" \
  "s_waitcnt lgkmcnt(5)\n\t" \
  "v_smfmac_f32_16x16x64_bf16 v[56:59], v[16:19], v[36:43], v68\n\t" \
  "ds_read_b128 v[8:11], v69 offset:3072\n\t" \
  "s_waitcnt lgkmcnt(4)\n\t" \
  "v_mfma_f32_16x16x32_bf16 v[12:15], v[44:47], v[76:79], 0\n\t" \
  "ds_read_b128 v[36:39], v70 offset:14336\n\t" \
  "s_waitcnt lgkmcnt(3)\n\t" \
  "v_mfma_f32_16x16x32_bf16 v[12:15], v[48:51], v[0:3], v[12:15]\n\t" \
  "buffer_store_dwordx4 v[44:47], v69, s[28:31], s69 offen offset:0 sc1\n\t" \
  "ds_read_b128 v[40:43], v70 offset:15360\n\t" \
  "v_smfmac_f32_16x16x64_bf16 v[52:55], v[20:23], v[104:111], v68\n\t" \
  "s_waitcnt lgkmcnt(2)\n\t" \
  "v_add_f32 v4, v4, v8\n\t" \
  "v_add_f32 v5, v5, v9\n\t" \
  "v_add_f32 v6, v6, v10\n\t" \
  CH_DP6 \
  "v_cndmask_b32 v78, v13, v14, s[56:57]\n\t" \
  "v_cndmask_b32 v76, v12, v15, s[56:57]\n\t" \
  "v_add_f32 v7, v7, v11\n\t" \
  "v_max3_f32 v77, v4, v5, v6\n\t" \
  "v_add_f32_dpp v76, v78, v76 quad_perm:[2,3,0,1] row_mask:0xf bank_mask:0xf\n\t" \
  "v_max3_f32 v77, v77, v7, v76\n\t" \
  "v_smfmac_f32_16x16x64_bf16 v[56:59], v[20:23], v[168:175], v68\n\t" \
  "ds_read_b128 v[44:47], v70 offset:16384\n\t" \
  "v_max_f32_dpp v77, v77, v77 quad_perm:[2,3,0,1] row_mask:0xf bank_mask:0xf\n\t" \
  "v_mov_b32 v78, v77\n\t" \
  "v_smfmac_f32_16x16x64_bf16 v[52:55], v[24:27], v[112:119], v68\n\t" \
  "ds_read_b128 v[48:51], v70 offset:17408\n\t" \
  "v_permlane16_swap_b32 v77, v78\n\t" \
  "v_max_f32 v77, v77, v78\n\t" \
  "v_mov_b32 v78, v77\n\t" \
  "v_smfmac_f32_16x16x64_bf16 v[52:55], v[28:31], v[120:127], v68\n\t" \
  "ds_read_b128 v[16:19], v70 offset:18432\n\t" \
  "v_permlane32_swap_b32 v77, v78\n\t" \
  "v_max_f32 v77, v77, v78\n\t" \
  "v_smfmac_f32_16x16x64_bf16 v[52:55], v[32:35], v[128:135], v68\n\t" \
  "ds_read_b128 v[20:23], v70 offset:19456\n\t" \
  "v_mul_f32 v77, 0xbe0293ee, v77\n\t" \
  CH_DP7 \
  "v_smfmac_f32_16x16x64_bf16 v[56:59], v[24:27], v[176:183], v68\n\t" \
  "v_fmamk_f32 v4, v4, 0x3e0293ee, v77\n\t" \
  "v_smfmac_f32_16x16x64_bf16 v[56:59], v[28:31], v[184:191], v68\n\t" \
  "ds_read_b128 v[24:27], v70 offset:22528\n\t" \
  "v_fmamk_f32 v5, v5, 0x3e0293ee, v77\n\t" \
  "s_waitcnt lgkmcnt(5)\n\t" \
  "v_smfmac_f32_16x16x64_bf16 v[56:59], v[32:35], v[36:43], v68\n\t" \
  "ds_read_b128 v[28:31], v70 offset:23552\n\t" \
  "v_fmamk_f32 v6, v6, 0x3e0293ee, v77\n\t" \
  "v_fmamk_f32 v7, v7, 0x3e0293ee, v77\n\t" \
  "v_fmamk_f32 v76, v76, 0x3e0293ee, v77\n\t" \
  "v_exp_f32 v4, v4\n\t" \
  "v_exp_f32 v5, v5\n\t" \
  "v_exp_f32 v6, v6\n\t" \
  "v_exp_f32 v7, v7\n\t" \
  "v_cvt_pk_bf16_f32 v4, v4, v5\n\t" \
  "v_exp_f32 v76, v76\n\t" \
  "v_cvt_pk_bf16_f32 v5, v6, v7\n\t" \
  "v_mov_b32_dpp v12, v4 quad_perm:[0,1,0,1] row_mask:0xf bank_mask:0xf\n\t" \
  "v_mov_b32_dpp v14, v4 quad_perm:[2,3,2,3] row_mask:0xf bank_mask:0xf\n\t" \
  "v_mov_b32_dpp v13, v5 quad_perm:[0,1,0,1] row_mask:0xf bank_mask:0xf\n\t" \
  "v_mov_b32_dpp v15, v5 quad_perm:[2,3,2,3] row_mask:0xf bank_mask:0xf\n\t" \
  "v_add_f32 v52, v52, v54\n\t" \
  "v_add_f32 v53, v53, v55\n\t" \
  "s_waitcnt vmcnt(2)\n\t" \
  CH_DP8 \
  "v_mfma_f32_16x16x32_bf16 v[0:3], v[60:63], v[12:15], 0\n\t" \
  "ds_read_b128 v[36:39], v70 offset:20480\n\t" \
  "v_add_f32 v56, v56, v58\n\t" \
  "v_add_f32 v57, v57, v59\n\t" \
  "v_cvt_pk_bf16_f32 v52, v52, v53\n\t" \
  "v_cvt_pk_bf16_f32 v53, v56, v57\n\t" \
  "s_waitcnt vmcnt(1)\n\t" \
  "v_mfma_f32_16x16x32_bf16 v[4:7], v[64:67], v[12:15], 0\n\t" \
  "ds_read_b128 v[40:43], v70 offset:21504\n\t" \
  "v_mov_b64 v[56:57], s[88:89]\n\t" \
  "v_mov_b64 v[58:59], s[88:89]\n\t" \
  "v_mov_b32_dpp v78, v76 quad_perm:[0,1,0,1] row_mask:0xf bank_mask:0xf\n\t" \
  "v_mov_b32_dpp v79, v76 quad_perm:[2,3,2,3] row_mask:0xf bank_mask:0xf\n\t" \
  "v_mfma_f32_16x16x32_bf16 v[32:35], v[56:59], v[12:15], 0\n\t" \
  "buffer_store_dwordx2 v[52:53], v71, s[28:31], s69 offen offset:2556 sc1\n\t" \
  "ds_read_b128 v[60:63], v70 offset:24576\n\t" \
  "v_cvt_pk_bf16_f32 v11, v78, v79\n\t" \
  "v_cvt_pk_bf16_f32 v8, v78, v79\n\t" \
  "v_mov_b64 v[54:55], 0\n\t" \
  "v_mov_b32 v9, 0\n\t" \
  "v_mov_b32 v10, 0\n\t" \
  "v_mov_b64 v[14:15], 0\n\t" \
  "s_cmp_eq_u32 s48, 63\n\t" \
  "v_mfma_f32_16x16x32_bf16 v[32:35], v[54:57], v[8:11], v[32:35]\n\t" \
  "v_mfma_f32_16x16x32_bf16 v[0:3], v[52:55], v[8:11], v[0:3]\n\t" \
  "buffer_load_dword v75, off, s[28:31], s80 offset:1024 sc1\n\t" \
  "ds_read_b128 v[64:67], v70 offset:25600\n\t" \
  "v_mov_b64 v[56:57], 0\n\t" \
  "v_mov_b64 v[58:59], 0\n\t" \
  "v_mfma_f32_16x16x32_bf16 v[4:7], v[52:55], v[10:13], v[4:7]\n\t" \
  "s_cbranch_scc1 LLAST1_%=\n\t" \
  "v_rcp_f32 v74, v32\n\t" \
  "v_mov_b64 v[12:13], 0\n\t" \
  "v_mov_b32 v8, 0\n\t" \
  "v_mul_f32 v0, v0, v74\n\t" \
  "v_mul_f32 v1, v1, v74\n\t" \
  "v_mul_f32 v2, v2, v74\n\t" \
  "v_mul_f32 v3, v3, v74\n\t" \
  "v_cvt_pk_bf16_f32 v76, v0, v1\n\t" \
  "v_cvt_pk_bf16_f32 v77, v2, v3\n\t" \
  "v_pk_mul_f32 v[4:5], v[4:5], v[74:75] op_sel_hi:[1,0]\n\t" \
  "v_pk_mul_f32 v[6:7], v[6:7], v[74:75] op_sel_hi:[1,0]\n\t" \
  "v_cvt_pk_bf16_f32 v78, v4, v5\n\t" \
  "v_cvt_pk_bf16_f32 v79, v6, v7\n\t" \
  "ds_write_b128 v69, v[76:79] offset:5120\n\t" \
  CH_DP9 \
  "s_waitcnt lgkmcnt(9)\n\t" \
  "v_smfmac_f32_16x16x64_bf16 v[12:15], v[76:79], v[44:51], v68\n\t" \
  "ds_read_b128 v[44:47], v70 offset:26624\n\t" \
  "v_mov_b32 v11, 0\n\t" \
  "s_waitcnt lgkmcnt(8)\n\t" \
  "v_smfmac_f32_16x16x64_bf16 v[56:59], v[76:79], v[16:23], v68\n\t" \
  "ds_read_b128 v[48:51], v70 offset:27648\n\t" \
  "v_mov_b64 v[52:53], 0\n\t" \
  "s_waitcnt lgkmcnt(5)\n\t" \
  "v_smfmac_f32_16x16x64_bf16 v[8:11], v[76:79], v[36:43], v68\n\t" \
  "ds_read_b128 v[16:19], v70 offset:28672\n\t" \
  "v_and_b32 v73, 32, v69\n\t" \
  "v_smfmac_f32_16x16x64_bf16 v[52:55], v[76:79], v[24:31], v68\n\t" \
  "s_waitcnt lgkmcnt(3)\n\t" \
  "s_barrier\n\t" \
  CH_DP10 \
  "ds_read_b128 v[0:3], v69 offset:4096\n\t" \
  "ds_read_b128 v[4:7], v69 offset:7168\n\t" \
  "v_lshl_add_u32 v73, v73, 5, v69\n\t" \
  "v_lshrrev_b32 v71, 7, v69\n\t" \
  "v_and_b32 v74, 0x70, v69\n\t" \
  "v_lshl_add_u32 v71, v71, 14, v74\n\t" \
  "v_bfe_u32 v72, v69, 2, 7\n\t" \
  "v_and_b32 v74, 0x200, v69\n\t" \
  "s_waitcnt lgkmcnt(1)\n\t" \
  "v_smfmac_f32_16x16x64_bf16 v[8:11], v[0:3], v[60:67], v68\n\t" \
  "ds_read_b128 v[20:23], v70 offset:29696\n\t" \
  "v_mov_b64 v[76:77], 0\n\t" \
  "v_smfmac_f32_16x16x64_bf16 v[12:15], v[0:3], v[200:207], v68\n\t" \
  "ds_read_b128 v[32:35], v69 offset:6144\n\t" \
  "v_mov_b64 v[78:79], 0\n\t" \
  "v_smfmac_f32_16x16x64_bf16 v[52:55], v[0:3], v[44:51], v68\n\t" \
  "ds_read_b128 v[24:27], v70 offset:30720\n\t" \
  "v_lshl_add_u32 v72, v74, 3, v72\n\t" \
  "v_smfmac_f32_16x16x64_bf16 v[56:59], v[0:3], v[232:239], v68\n\t" \
  "ds_read_b128 v[28:31], v70 offset:31744\n\t" \
  "s_waitcnt lgkmcnt(4)\n\t" \
  "v_smfmac_f32_16x16x64_bf16 v[12:15], v[4:7], v[208:215], v68\n\t" \
  "ds_read_b128 v[60:63], v70 offset:32768\n\t" \
  "s_waitcnt lgkmcnt(4)\n\t" \
  "v_smfmac_f32_16x16x64_bf16 v[8:11], v[4:7], v[16:23], v68\n\t" \
  "ds_read_b128 v[64:67], v70 offset:33792\n\t" \
  "s_waitcnt lgkmcnt(4)\n\t" \
  "v_smfmac_f32_16x16x64_bf16 v[8:11], v[32:35], v[192:199], v68\n\t" \
  "ds_read_b128 v[36:39], v73 offset:4096\n\t" \
  "v_smfmac_f32_16x16x64_bf16 v[12:15], v[32:35], v[216:223], v68\n\t" \
  "ds_read_b128 v[40:43], v73 offset:6144\n\t" \
  "s_waitcnt lgkmcnt(4)\n\t" \
  "v_smfmac_f32_16x16x64_bf16 v[52:55], v[4:7], v[24:31], v68\n\t" \
  "ds_read_b128 v[44:47], v70 offset:34816\n\t" \
  "v_cvt_pk_bf16_f32 v8, v8, v9\n\t" \
  "v_smfmac_f32_16x16x64_bf16 v[52:55], v[32:35], v[224:231], v68\n\t" \
  "ds_read_b128 v[48:51], v70 offset:35840\n\t" \
  "v_cvt_pk_bf16_f32 v9, v10, v11\n\t" \
  "v_smfmac_f32_16x16x64_bf16 v[56:59], v[4:7], v[240:247], v68\n\t" \
  "v_cvt_pk_bf16_f32 v10, v12, v13\n\t" \
  "s_waitcnt lgkmcnt(4)\n\t" \
  "v_smfmac_f32_16x16x64_bf16 v[56:59], v[32:35], v[60:67], v68\n\t" \
  "v_cvt_pk_bf16_f32 v11, v14, v15\n\t" \
  "s_waitcnt lgkmcnt(3)\n\t" \
  "v_smfmac_f32_16x16x64_bf16 v[76:79], v[36:39], v[248:255], v68\n\t" \
  "buffer_store_dwordx4 v[8:11], v71, s[28:31], s54 offen sc1\n\t" \
  CH_DP11 \
  "v_cvt_pk_bf16_f32 v52, v52, v53\n\t" \
  "s_waitcnt lgkmcnt(0)\n\t" \
  "v_smfmac_f32_16x16x64_bf16 v[76:79], v[40:43], v[44:51], v68\n\t" \
  "v_cvt_pk_bf16_f32 v53, v54, v55\n\t" \
  "v_cvt_pk_bf16_f32 v54, v56, v57\n\t" \
  "v_cvt_pk_bf16_f32 v55, v58, v59\n\t" \
  "buffer_store_dwordx4 v[52:55], v71, s[28:31], s75 offen sc1\n\t" \
  "s_nop 2\n\t" \
  "v_add_f32 v76, v76, v78\n\t" \
  "v_add_f32 v77, v77, v79\n\t" \
  "v_cvt_pk_bf16_f32 v76, v76, v77\n\t" \
  "buffer_store_dword v76, v72, s[28:31], s55 offen sc1\n\t" \
  CH_DP12 \
  "s_memrealtime s[70:71]\n\t" \
  "s_waitcnt vmcnt(3)\n\t" \
  "v_readfirstlane_b32 s65, v75\n\t" \
  "s_barrier\n\t" \
  "s_waitcnt lgkmcnt(0)\n\t" \
  "s_not_b32 s65, s65\n\t" \
  "s_lshl_b32 s61, s70, 8\n\t" \
  "s_andn2_b32 s64, s65, 0xff\n\t" \
  "s_sub_u32 s64, s64, s61\n\t" \
  "s_ashr_i32 s64, s64, 8\n\t" \
  "s_cmp_eq_u32 s65, 0\n\t" \
  "s_cselect_b32 s64, " CH_DLF ", s64\n\t" \
  "s_min_i32 s64, s64, s34\n\t" \
  "s_max_i32 s64, s64, " CH_DLL1 "\n\t" \
  "s_add_u32 s64, s64, s70\n\t" \
  CH_ST2 \
  "s_add_u32 s55, s41, s53\n\t" \
  "s_mov_b32 s74, 0\n\t" \
  "s_movk_i32 s60, 8192\n\t" \
  "RWT1_%=:\n\t" \
  "s_memrealtime s[72:73]\n\t" \
  "s_sub_u32 s60, s60, 1\n\t" \
  "s_cselect_b32 s61, 0, -1\n\t" \
  "s_waitcnt lgkmcnt(0)\n\t" \
  "s_sub_i32 s73, s72, s64\n\t" \
  "s_and_b32 s73, s73, s61\n\t" \
  "s_cmp_lt_i32 s73, 0\n\t" \
  "s_cbranch_scc1 RWT1_%=\n\t" \
  "RRETRY1_%=:\n\t" \
  CH_DP13 \
  "buffer_load_dwordx4 v[0:3], v69, s[28:31], s55 offen offset:0 sc1\n\t" \
  "buffer_load_dwordx4 v[4:7], v69, s[28:31], s55 offen offset:1024 sc1\n\t" \
  "buffer_load_dwordx4 v[8:11], v69, s[28:31], s55 offen offset:2048 sc1\n\t" \
  "buffer_load_dwordx4 v[12:15], v69, s[28:31], s55 offen offset:3072 sc1\n\t" \
  "v_lshrrev_b32 v20, 2, v69\n\t" \
  "v_and_b32 v20, 16, v20\n\t" \
  "v_mov_b32 v21, 0x3f80\n\t" \
  "v_lshlrev_b32 v21, v20, v21\n\t" \
  "v_bfe_u32 v20, v69, 4, 2\n\t" \
  "v_cmp_eq_u32 vcc, 0, v20\n\t" \
  "s_nop 1\n\t" \
  "v_cndmask_b32 v32, 0, v21, vcc\n\t" \
  "v_cmp_eq_u32 vcc, 1, v20\n\t" \
  "s_nop 1\n\t" \
  "v_cndmask_b32 v33, 0, v21, vcc\n\t" \
  "v_cmp_eq_u32 vcc, 2, v20\n\t" \
  "s_nop 1\n\t" \
  "v_cndmask_b32 v34, 0, v21, vcc\n\t" \
  "v_cmp_eq_u32 vcc, 3, v20\n\t" \
  "s_nop 1\n\t" \
  "v_cndmask_b32 v35, 0, v21, vcc\n\t" \
  "v_bfe_u32 v38, v69, 7, 1\n\t" \
  "v_bfe_u32 v20, v69, 9, 1\n\t" \
  "v_lshl_add_u32 v38, v20, 1, v38\n\t" \
  "v_mul_u32_u24 v38, 16384, v38\n\t" \
  "v_and_b32 v20, 0x60, v69\n\t" \
  "v_add_u32 v38, v38, v20\n\t" \
  "v_bfe_u32 v20, v69, 8, 1\n\t" \
  "v_lshl_add_u32 v38, v20, 4, v38\n\t" \
  "v_bfe_u32 v20, v69, 4, 1\n\t" \
  "v_lshl_add_u32 v38, v20, 3, v38\n\t" \
  "s_add_u32 s54, s37, s53\n\t" \
  "v_mov_b32 v44, 0\n\t" \
  CH_DP17 \
  "s_waitcnt vmcnt(3)\n\t" \
  CH_DP14 \
  "v_mfma_f32_16x16x32_bf16 v[40:43], v[32:35], v[0:3], 0\n\t" \
  "v_max3_u32 v44, v44, v0, v1\n\t" \
  "v_max3_u32 v44, v44, v2, v3\n\t" \
  "s_waitcnt vmcnt(2)\n\t" \
  "v_mfma_f32_16x16x32_bf16 v[40:43], v[32:35], v[4:7], v[40:43]\n\t" \
  "v_max3_u32 v44, v44, v4, v5\n\t" \
  "v_max3_u32 v44, v44, v6, v7\n\t" \
  "s_waitcnt vmcnt(1)\n\t" \
  "v_mfma_f32_16x16x32_bf16 v[40:43], v[32:35], v[8:11], v[40:43]\n\t" \
  "v_max3_u32 v44, v44, v8, v9\n\t" \
  "v_max3_u32 v44, v44, v10, v11\n\t" \
  "s_waitcnt vmcnt(0)\n\t" \
  CH_DP15 \
  "v_mfma_f32_16x16x32_bf16 v[40:43], v[32:35], v[12:15], v[40:43]\n\t" \
  "v_max3_u32 v44, v44, v12, v13\n\t" \
  "v_max3_u32 v44, v44, v14, v15\n\t" \
  "v_cmp_eq_u32 vcc, -1, v44\n\t" \
  "s_cbranch_vccnz RFAIL1_%=\n\t" \
  "s_nop 3\n\t" \
  "v_add_f32_dpp v40, v40, v40 row_ror:8 row_mask:0xf bank_mask:0xf\n\t" \
  "v_add_f32_dpp v41, v41, v41 row_ror:8 row_mask:0xf bank_mask:0xf\n\t" \
  "v_add_f32_dpp v42, v42, v42 row_ror:8 row_mask:0xf bank_mask:0xf\n\t" \
  "v_add_f32_dpp v43, v43, v43 row_ror:8 row_mask:0xf bank_mask:0xf\n\t" \
  "v_cvt_pk_bf16_f32 v40, v40, v41\n\t" \
  "v_cvt_pk_bf16_f32 v41, v42, v43\n\t" \
  "buffer_store_dwordx2 v[40:41], v38, s[28:31], s54 offen sc1\n\t" \
  "RDONE1_%=:\n\t" \
  CH_DP16 \
  CH_ST4 \
  "s_barrier\n\t" \
  CH_ST3 \
  CH_TRC \
  "s_add_u32 s48, s48, 1\n\t" \
  "s_mov_b32 s49, s53\n\t" \
  "s_add_u32 s53, s53, " CH_SLOT "\n\t" \
  "s_add_u32 s60, s53, " CH_SKIP "\n\t" \
  "s_lshr_b32 s61, s96, 1\n\t" \
  "s_add_u32 s86, s86, 1\n\t" \
  "s_cmp_eq_u32 s86, 3\n\t" \
  "s_cselect_b32 s53, s60, s53\n\t" \
  "s_cselect_b32 s96, s61, s96\n\t" \
  "s_cselect_b32 s86, 0, s86\n\t" \
  "s_andn2_b32 s53, s53, 0x2000\n\t" \
  "s_and_b32 s60, s96, 1\n\t" \
  "s_lshl_b32 s60, s60, 13\n\t" \
  "s_or_b32 s53, s53, s60\n\t" \
  "s_mov_b32 s68, 0\n\t" \
  "s_add_u32 s82, s48, 0\n\t" \
  CH_SEGC \
  "s_lshr_b64 s[60:61], s[90:91], s82\n\t" \
  "s_and_b32 s60, s60, 1\n\t" \
  "s_lshl_b32 s60, s60, 13\n\t" \
  "s_add_u32 s82, s82, " CH_SG0 "\n\t" \
  "s_lshl_b32 s82, s82, 21\n\t" \
  "s_or_b32 s82, s82, s60\n\t" \
  "s_add_u32 s50, s82, s38\n\t" \
  "s_add_u32 s69, s82, s39\n\t" \
  "s_cmp_eq_u32 s34, 0\n\t" \
  "s_cbranch_scc1 LLOAD_%=\n\t" \
  "s_branch LG1_%=\n\t" \
  "RFAIL1_%=:\n\t" \
  "s_add_u32 s74, s74, 1\n\t" \
  "s_sleep " CH_RSL "\n\t" \
  "s_waitcnt vmcnt(0)\n\t" \
  "s_cmp_gt_u32 s74, s84\n\t" \
  "s_cbranch_scc1 RG1_%=\n\t" \
  "s_and_b32 s60, s74, 1023\n\t" \
  "s_cmp_lg_u32 s60, 0\n\t" \
  "s_cbranch_scc1 RRETRY1_%=\n\t" \
  "buffer_load_dword v46, off, s[28:31], s92 sc1\n\t" \
  "s_waitcnt vmcnt(0)\n\t" \
  "v_readfirstlane_b32 s60, v46\n\t" \
  "s_nop 0\n\t" \
  "s_cmp_eq_u32 s60, 0\n\t" \
  "s_cbranch_scc1 RRETRY1_%=\n\t" \
  "RG1_%=:\n\t" \
  "s_mov_b32 s84, 0\n\t" \
  "s_lshl_b32 s60, s36, 4\n\t" \
  "s_or_b32 s60, s60, s48\n\t" \
  "s_or_b32 s60, s60, 536870912\n\t" \
  "v_mov_b32 v46, s60\n\t" \
  "v_mov_b32 v47, 0\n\t" \
  "s_mov_b64 exec, 1\n\t" \
  "buffer_atomic_cmpswap v[46:47], off, s[28:31], s92 sc1\n\t" \
  "s_mov_b64 exec, -1\n\t" \
  "s_branch RDONE1_%=\n\t" \
  "LFAIL1_%=:\n\t" \
  "s_add_u32 s68, s68, 1\n\t" \
  "s_waitcnt lgkmcnt(0)\n\t" \
  "s_cmp_gt_u32 s68, s83\n\t" \
  "s_cbranch_scc1 LGV1_%=\n\t" \
  "s_barrier\n\t" \
  "s_waitcnt vmcnt(8)\n\t" \
  "s_branch LG1_%=\n\t" \
  "LGV1_%=:\n\t" \
  "s_mov_b32 s83, 0\n\t" \
  "s_lshl_b32 s60, s36, 4\n\t" \
  "s_or_b32 s60, s60, s48\n\t" \
  "s_or_b32 s60, s60, 805306368\n\t" \
  "v_mov_b32 v46, s60\n\t" \
  "v_mov_b32 v47, 0\n\t" \
  "s_mov_b64 exec, 1\n\t" \
  "buffer_atomic_cmpswap v[46:47], off, s[28:31], s92 sc1\n\t" \
  "s_mov_b64 exec, -1\n\t" \
  "s_branch LGO1_%=\n\t" \
  "LLAST1_%=:\n\t" \
  "s_waitcnt vmcnt(0) lgkmcnt(0)\n\t" \
  "s_barrier\n\t" \
  "s_branch LEND_%=\n\t" \
  ".p2align 6\n\t" \
  "LG2_%=:\n\t" \
  "s_barrier\n\t" \
  "buffer_load_dwordx4 v[52:55], v69, s[28:31], s50 offen sc1\n\t" \
  "buffer_load_dwordx4 v[56:59], v69, s[28:31], s50 offen offset:1024 sc1\n\t" \
  "s_add_u32 s64, s50, 18432\n\t" \
  "s_movk_i32 m0, 11264\n\t" \
  "v_lshlrev_b32 v75, 1, v69\n\t" \
  "buffer_load_dword v75, s[28:31], s64 offen sc1 lds\n\t" \
  "ds_read_b128 v[44:47], v70 offset:0\n\t" \
  "ds_read_b128 v[48:51], v70 offset:1024\n\t" \
  "ds_read_b128 v[60:63], v70 offset:2048\n\t" \
  "ds_read_b128 v[64:67], v70 offset:3072\n\t" \
  "v_mov_b64 v[36:37], 0\n\t" \
  "v_mov_b64 v[38:39], 0\n\t" \
  "v_mov_b64 v[40:41], 0\n\t" \
  "v_mov_b64 v[42:43], 0\n\t" \
  "v_mov_b32 v73, 0\n\t" \
  "v_lshrrev_b32 v71, 1, v69\n\t" \
  "v_lshrrev_b32 v72, 3, v69\n\t" \
  "v_and_b32 v72, 0x78, v72\n\t" \
  "v_and_b32 v74, 0x30, v69\n\t" \
  "v_lshl_add_u32 v72, v74, 3, v72\n\t" \
  "s_add_u32 s54, s53, s36\n\t" \
  "s_add_u32 s55, s54, 557056\n\t" \
  "s_add_u32 s54, s54, 131072\n\t" \
  "s_add_u32 s75, s54, 262144\n\t" \
  "s_add_u32 s80, s53, s44\n\t" \
  "s_barrier\n\t" \
  CH_DP0 \
  "ds_read_b128 v[0:3], v69 offset:0\n\t" \
  "ds_read_b128 v[4:7], v69 offset:1024\n\t" \
  "ds_read_b128 v[8:11], v69 offset:2048\n\t" \
  "ds_read_b128 v[12:15], v69 offset:3072\n\t" \
  "s_waitcnt lgkmcnt(3)\n\t" \
  "v_smfmac_f32_16x16x64_bf16 v[36:39], v[0:3], v[44:51], v68\n\t" \
  "ds_read_b128 v[44:47], v70 offset:4096\n\t" \
  "v_smfmac_f32_16x16x64_bf16 v[40:43], v[0:3], v[60:67], v68\n\t" \
  "ds_read_b128 v[48:51], v70 offset:5120\n\t" \
  "v_max3_u32 v73, v73, v0, v2\n\t" \
  "s_waitcnt lgkmcnt(4)\n\t" \
  "v_smfmac_f32_16x16x64_bf16 v[36:39], v[4:7], a[0:7], v68\n\t" \
  "s_barrier\n\t" \
  "ds_read_b128 v[16:19], v69 offset:4096\n\t" \
  CH_DP1 \
  "v_smfmac_f32_16x16x64_bf16 v[40:43], v[4:7], a[64:71], v68\n\t" \
  "ds_read_b128 v[20:23], v69 offset:5120\n\t" \
  "v_max3_u32 v73, v73, v4, v6\n\t" \
  "s_waitcnt lgkmcnt(5)\n\t" \
  "v_smfmac_f32_16x16x64_bf16 v[36:39], v[8:11], a[8:15], v68\n\t" \
  "ds_read_b128 v[24:27], v69 offset:6144\n\t" \
  "v_max3_u32 v73, v73, v8, v10\n\t" \
  "v_smfmac_f32_16x16x64_bf16 v[40:43], v[8:11], a[72:79], v68\n\t" \
  "ds_read_b128 v[28:31], v69 offset:7168\n\t" \
  "s_waitcnt lgkmcnt(6)\n\t" \
  "v_max3_u32 v73, v73, v12, v14\n\t" \
  "v_smfmac_f32_16x16x64_bf16 v[36:39], v[12:15], a[16:23], v68\n\t" \
  "ds_read_b128 v[32:35], v69 offset:8192\n\t" \
  "s_waitcnt lgkmcnt(5)\n\t" \
  "v_smfmac_f32_16x16x64_bf16 v[40:43], v[12:15], v[44:51], v68\n\t" \
  "ds_read_b128 v[60:63], v70 offset:6144\n\t" \
  "s_waitcnt lgkmcnt(5)\n\t" \
  "v_smfmac_f32_16x16x64_bf16 v[36:39], v[16:19], a[24:31], v68\n\t" \
  "ds_read_b128 v[64:67], v70 offset:7168\n\t" \
  "v_max3_u32 v73, v73, v16, v18\n\t" \
  "v_smfmac_f32_16x16x64_bf16 v[40:43], v[16:19], a[80:87], v68\n\t" \
  "s_waitcnt lgkmcnt(5)\n\t" \
  "v_max3_u32 v73, v73, v20, v22\n\t" \
  "v_smfmac_f32_16x16x64_bf16 v[36:39], v[20:23], a[32:39], v68\n\t" \
  "s_waitcnt lgkmcnt(4)\n\t" \
  "v_max3_u32 v73, v73, v24, v26\n\t" \
  "v_smfmac_f32_16x16x64_bf16 v[36:39], v[24:27], a[40:47], v68\n\t" \
  "s_waitcnt lgkmcnt(3)\n\t" \
  "v_max3_u32 v73, v73, v28, v30\n\t" \
  "v_smfmac_f32_16x16x64_bf16 v[36:39], v[28:31], a[48:55], v68\n\t" \
  "s_waitcnt lgkmcnt(2)\n\t" \
  "v_max3_u32 v73, v73, v32, v34\n\t" \
  "v_smfmac_f32_16x16x64_bf16 v[36:39], v[32:35], a[56:63], v68\n\t" \
  "v_cmp_eq_u32 vcc, -1, v73\n\t" \
  "s_waitcnt lgkmcnt(0)\n\t" \
  "v_smfmac_f32_16x16x64_bf16 v[40:43], v[20:23], v[60:67], v68\n\t" \
  "ds_read_b128 v[60:63], v70 offset:8192\n\t" \
  "v_mov_b64 v[44:45], 0\n\t" \
  "s_cbranch_vccnz LFAIL2_%=\n\t" \
  "LGO2_%=:\n\t" \
  "v_smfmac_f32_16x16x64_bf16 v[40:43], v[24:27], a[88:95], v68\n\t" \
  "ds_read_b128 v[64:67], v70 offset:9216\n\t" \
  "v_mov_b64 v[46:47], 0\n\t" \
  "v_smfmac_f32_16x16x64_bf16 v[40:43], v[28:31], a[96:103], v68\n\t" \
  "v_add_f32 v74, v36, v38\n\t" \
  "v_smfmac_f32_16x16x64_bf16 v[40:43], v[32:35], a[104:111], v68\n\t" \
  "v_add_f32 v37, v37, v39\n\t" \
  CH_DP2 \
  "v_smfmac_f32_16x16x64_bf16 v[44:47], v[0:3], a[112:119], v68\n\t" \
  "v_cvt_pk_bf16_f32 v74, v74, v37\n\t" \
  "v_smfmac_f32_16x16x64_bf16 v[44:47], v[4:7], a[120:127], v68\n\t" \
  "v_add_f32 v75, v40, v42\n\t" \
  "s_waitcnt lgkmcnt(0)\n\t" \
  "v_smfmac_f32_16x16x64_bf16 v[44:47], v[8:11], v[60:67], v68\n\t" \
  "ds_read_b128 v[60:63], v70 offset:10240\n\t" \
  "v_add_f32 v41, v41, v43\n\t" \
  "v_cvt_pk_bf16_f32 v75, v75, v41\n\t" \
  "v_smfmac_f32_16x16x64_bf16 v[44:47], v[12:15], a[128:135], v68\n\t" \
  "ds_write2st64_b32 v71, v74, v75 offset0:40 offset1:42\n\t" \
  "v_mov_b64 v[48:49], 0\n\t" \
  "v_smfmac_f32_16x16x64_bf16 v[44:47], v[16:19], a[136:143], v68\n\t" \
  "ds_read_b128 v[64:67], v70 offset:11264\n\t" \
  "v_mov_b64 v[50:51], 0\n\t" \
  "v_smfmac_f32_16x16x64_bf16 v[44:47], v[20:23], a[144:151], v68\n\t" \
  "v_smfmac_f32_16x16x64_bf16 v[44:47], v[24:27], a[152:159], v68\n\t" \
  "v_smfmac_f32_16x16x64_bf16 v[44:47], v[28:31], a[160:167], v68\n\t" \
  "s_waitcnt lgkmcnt(0)\n\t" \
  "v_smfmac_f32_16x16x64_bf16 v[44:47], v[32:35], v[60:67], v68\n\t" \
  "s_barrier\n\t" \
  "ds_read_b64_tr_b16 v[76:77], v72 offset:10240\n\t" \
  CH_DP3 \
  "v_smfmac_f32_16x16x64_bf16 v[48:51], v[0:3], a[168:175], v68\n\t" \
  "ds_read_b64_tr_b16 v[78:79], v72 offset:10752\n\t" \
  "v_smfmac_f32_16x16x64_bf16 v[48:51], v[4:7], a[176:183], v68\n\t" \
  "buffer_load_dwordx4 v[60:63], v69, s[28:31], s64 offen sc1\n\t" \
  "v_add_f32 v74, v44, v46\n\t" \
  "v_smfmac_f32_16x16x64_bf16 v[48:51], v[8:11], a[184:191], v68\n\t" \
  "v_add_f32 v45, v45, v47\n\t" \
  "v_smfmac_f32_16x16x64_bf16 v[48:51], v[12:15], a[192:199], v68\n\t" \
  "buffer_load_dwordx4 v[64:67], v69, s[28:31], s64 offen offset:1024 sc1\n\t" \
  "v_cvt_pk_bf16_f32 v74, v74, v45\n\t" \
  "s_waitcnt vmcnt(4)\n\t" \
  "s_waitcnt lgkmcnt(0)\n\t" \
  "v_mfma_f32_16x16x32_bf16 v[36:39], v[52:55], v[76:79], 0\n\t" \
  "s_waitcnt vmcnt(3)\n\t" \
  "v_mfma_f32_16x16x32_bf16 v[40:43], v[56:59], v[76:79], 0\n\t" \
  "v_mov_b64 v[52:53], 0\n\t" \
  "v_mov_b64 v[54:55], 0\n\t" \
  "v_smfmac_f32_16x16x64_bf16 v[48:51], v[16:19], a[200:207], v68\n\t" \
  "v_mov_b64 v[56:57], 0\n\t" \
  "v_smfmac_f32_16x16x64_bf16 v[48:51], v[20:23], a[208:215], v68\n\t" \
  "v_add_f32_dpp v36, v40, v36 quad_perm:[2,3,0,1] row_mask:0xf bank_mask:0xf\n\t" \
  "v_smfmac_f32_16x16x64_bf16 v[48:51], v[24:27], a[216:223], v68\n\t" \
  "v_add_f32_dpp v37, v41, v37 quad_perm:[2,3,0,1] row_mask:0xf bank_mask:0xf\n\t" \
  "v_smfmac_f32_16x16x64_bf16 v[48:51], v[28:31], a[224:231], v68\n\t" \
  "v_add_f32_dpp v38, v42, v38 quad_perm:[2,3,0,1] row_mask:0xf bank_mask:0xf\n\t" \
  "v_smfmac_f32_16x16x64_bf16 v[48:51], v[32:35], a[232:239], v68\n\t" \
  "v_add_f32_dpp v39, v43, v39 quad_perm:[2,3,0,1] row_mask:0xf bank_mask:0xf\n\t" \
  CH_DP4 \
  "v_smfmac_f32_16x16x64_bf16 v[52:55], v[0:3], a[240:247], v68\n\t" \
  "s_mov_b64 exec, s[58:59]\n\t" \
  "ds_write_b128 v69, v[36:39] offset:3072\n\t" \
  "s_mov_b64 exec, -1\n\t" \
  "v_mov_b64 v[58:59], 0\n\t" \
  "v_smfmac_f32_16x16x64_bf16 v[52:55], v[4:7], a[248:255], v68\n\t" \
  "v_add_f32 v75, v48, v50\n\t" \
  "v_add_f32 v49, v49, v51\n\t" \
  "v_cvt_pk_bf16_f32 v75, v75, v49\n\t" \
  "v_smfmac_f32_16x16x64_bf16 v[56:59], v[0:3], v[136:143], v68\n\t" \
  "ds_write2st64_b32 v71, v74, v75 offset0:4 offset1:6\n\t" \
  "v_smfmac_f32_16x16x64_bf16 v[56:59], v[4:7], v[144:151], v68\n\t" \
  "ds_read_b64_tr_b16 v[0:1], v72 offset:9216\n\t" \
  "ds_read_b64_tr_b16 v[2:3], v72 offset:9728\n\t" \
  "v_smfmac_f32_16x16x64_bf16 v[52:55], v[8:11], v[80:87], v68\n\t" \
  "ds_read_b128 v[36:39], v70 offset:12288\n\t" \
  "v_smfmac_f32_16x16x64_bf16 v[56:59], v[8:11], v[152:159], v68\n\t" \
  "ds_read_b128 v[40:43], v70 offset:13312\n\t" \
  "v_smfmac_f32_16x16x64_bf16 v[52:55], v[12:15], v[88:95], v68\n\t" \
  "s_waitcnt lgkmcnt(4)\n\t" \
  "s_barrier\n\t" \
  CH_DP5 \
  "ds_read_b64_tr_b16 v[44:45], v72 offset:1024\n\t" \
  "ds_read_b64_tr_b16 v[46:47], v72 offset:1536\n\t" \
  "v_smfmac_f32_16x16x64_bf16 v[56:59], v[12:15], v[160:167], v68\n\t" \
  "ds_read_b64_tr_b16 v[48:49], v72 offset:0\n\t" \
  "ds_read_b64_tr_b16 v[50:51], v72 offset:512\n\t" \
  "v_smfmac_f32_16x16x64_bf16 v[52:55], v[16:19], v[96:103], v68\n\t" \
  "ds_read_b128 v[4:7], v69 offset:2048\n\t" \
  "s_waitcnt lgkmcnt(5)\n\t" \
  "v_smfmac_f32_16x16x64_bf16 v[56:59], v[16:19], v[36:43], v68\n\t" \
  "ds_read_b128 v[8:11], v69 offset:3072\n\t" \
  "s_waitcnt lgkmcnt(4)\n\t" \
  "v_mfma_f32_16x16x32_bf16 v[12:15], v[44:47], v[76:79], 0\n\t" \
  "ds_read_b128 v[36:39], v70 offset:14336\n\t" \
  "s_waitcnt lgkmcnt(3)\n\t" \
  "v_mfma_f32_16x16x32_bf16 v[12:15], v[48:51], v[0:3], v[12:15]\n\t" \
  "ds_read_b128 v[40:43], v70 offset:15360\n\t" \
  "v_smfmac_f32_16x16x64_bf16 v[52:55], v[20:23], v[104:111], v68\n\t" \
  "buffer_store_dwordx4 v[44:47], v69, s[28:31], s69 offen offset:1024 sc1\n\t" \
  "s_waitcnt lgkmcnt(2)\n\t" \
  "v_add_f32 v4, v4, v8\n\t" \
  "v_add_f32 v5, v5, v9\n\t" \
  "v_add_f32 v6, v6, v10\n\t" \
  CH_DP6 \
  "v_cndmask_b32 v78, v13, v14, s[56:57]\n\t" \
  "v_cndmask_b32 v76, v12, v15, s[56:57]\n\t" \
  "v_add_f32 v7, v7, v11\n\t" \
  "v_max3_f32 v77, v4, v5, v6\n\t" \
  "v_add_f32_dpp v76, v78, v76 quad_perm:[2,3,0,1] row_mask:0xf bank_mask:0xf\n\t" \
  "v_max3_f32 v77, v77, v7, v76\n\t" \
  "v_smfmac_f32_16x16x64_bf16 v[56:59], v[20:23], v[168:175], v68\n\t" \
  "ds_read_b128 v[44:47], v70 offset:16384\n\t" \
  "v_max_f32_dpp v77, v77, v77 quad_perm:[2,3,0,1] row_mask:0xf bank_mask:0xf\n\t" \
  "v_mov_b32 v78, v77\n\t" \
  "v_smfmac_f32_16x16x64_bf16 v[52:55], v[24:27], v[112:119], v68\n\t" \
  "ds_read_b128 v[48:51], v70 offset:17408\n\t" \
  "v_permlane16_swap_b32 v77, v78\n\t" \
  "v_max_f32 v77, v77, v78\n\t" \
  "v_mov_b32 v78, v77\n\t" \
  "v_smfmac_f32_16x16x64_bf16 v[52:55], v[28:31], v[120:127], v68\n\t" \
  "ds_read_b128 v[16:19], v70 offset:18432\n\t" \
  "v_permlane32_swap_b32 v77, v78\n\t" \
  "v_max_f32 v77, v77, v78\n\t" \
  "v_smfmac_f32_16x16x64_bf16 v[52:55], v[32:35], v[128:135], v68\n\t" \
  "ds_read_b128 v[20:23], v70 offset:19456\n\t" \
  "v_mul_f32 v77, 0xbe0293ee, v77\n\t" \
  CH_DP7 \
  "v_smfmac_f32_16x16x64_bf16 v[56:59], v[24:27], v[176:183], v68\n\t" \
  "v_fmamk_f32 v4, v4, 0x3e0293ee, v77\n\t" \
  "v_smfmac_f32_16x16x64_bf16 v[56:59], v[28:31], v[184:191], v68\n\t" \
  "ds_read_b128 v[24:27], v70 offset:22528\n\t" \
  "v_fmamk_f32 v5, v5, 0x3e0293ee, v77\n\t" \
  "s_waitcnt lgkmcnt(5)\n\t" \
  "v_smfmac_f32_16x16x64_bf16 v[56:59], v[32:35], v[36:43], v68\n\t" \
  "ds_read_b128 v[28:31], v70 offset:23552\n\t" \
  "v_fmamk_f32 v6, v6, 0x3e0293ee, v77\n\t" \
  "v_fmamk_f32 v7, v7, 0x3e0293ee, v77\n\t" \
  "v_fmamk_f32 v76, v76, 0x3e0293ee, v77\n\t" \
  "v_exp_f32 v4, v4\n\t" \
  "v_exp_f32 v5, v5\n\t" \
  "v_exp_f32 v6, v6\n\t" \
  "v_exp_f32 v7, v7\n\t" \
  "v_cvt_pk_bf16_f32 v4, v4, v5\n\t" \
  "v_exp_f32 v76, v76\n\t" \
  "v_cvt_pk_bf16_f32 v5, v6, v7\n\t" \
  "v_mov_b32_dpp v12, v4 quad_perm:[0,1,0,1] row_mask:0xf bank_mask:0xf\n\t" \
  "v_mov_b32_dpp v14, v4 quad_perm:[2,3,2,3] row_mask:0xf bank_mask:0xf\n\t" \
  "v_mov_b32_dpp v13, v5 quad_perm:[0,1,0,1] row_mask:0xf bank_mask:0xf\n\t" \
  "v_mov_b32_dpp v15, v5 quad_perm:[2,3,2,3] row_mask:0xf bank_mask:0xf\n\t" \
  "v_add_f32 v52, v52, v54\n\t" \
  "v_add_f32 v53, v53, v55\n\t" \
  "s_waitcnt vmcnt(2)\n\t" \
  CH_DP8 \
  "v_mfma_f32_16x16x32_bf16 v[0:3], v[60:63], v[12:15], 0\n\t" \
  "ds_read_b128 v[36:39], v70 offset:20480\n\t" \
  "v_add_f32 v56, v56, v58\n\t" \
  "v_add_f32 v57, v57, v59\n\t" \
  "v_cvt_pk_bf16_f32 v52, v52, v53\n\t" \
  "v_cvt_pk_bf16_f32 v53, v56, v57\n\t" \
  "s_waitcnt vmcnt(1)\n\t" \
  "v_mfma_f32_16x16x32_bf16 v[4:7], v[64:67], v[12:15], 0\n\t" \
  "ds_read_b128 v[40:43], v70 offset:21504\n\t" \
  "v_mov_b64 v[56:57], s[88:89]\n\t" \
  "v_mov_b64 v[58:59], s[88:89]\n\t" \
  "v_mov_b32_dpp v78, v76 quad_perm:[0,1,0,1] row_mask:0xf bank_mask:0xf\n\t" \
  "v_mov_b32_dpp v79, v76 quad_perm:[2,3,2,3] row_mask:0xf bank_mask:0xf\n\t" \
  "v_mfma_f32_16x16x32_bf16 v[32:35], v[56:59], v[12:15], 0\n\t" \
  "ds_read_b128 v[60:63], v70 offset:24576\n\t" \
  "v_cvt_pk_bf16_f32 v11, v78, v79\n\t" \
  "v_cvt_pk_bf16_f32 v8, v78, v79\n\t" \
  "v_mov_b64 v[54:55], 0\n\t" \
  "v_mov_b32 v9, 0\n\t" \
  "v_mov_b32 v10, 0\n\t" \
  "v_mov_b64 v[14:15], 0\n\t" \
  "s_cmp_eq_u32 s48, 63\n\t" \
  "v_mfma_f32_16x16x32_bf16 v[32:35], v[54:57], v[8:11], v[32:35]\n\t" \
  "buffer_store_dwordx2 v[52:53], v71, s[28:31], s69 offen offset:3072 sc1\n\t" \
  "v_mfma_f32_16x16x32_bf16 v[0:3], v[52:55], v[8:11], v[0:3]\n\t" \
  "ds_read_b128 v[64:67], v70 offset:25600\n\t" \
  "v_mov_b64 v[56:57], 0\n\t" \
  "v_mov_b64 v[58:59], 0\n\t" \
  "v_mfma_f32_16x16x32_bf16 v[4:7], v[52:55], v[10:13], v[4:7]\n\t" \
  "buffer_load_dword v75, off, s[28:31], s80 offset:1024 sc1\n\t" \
  "s_cbranch_scc1 LLAST2_%=\n\t" \
  "v_rcp_f32 v74, v32\n\t" \
  "v_mov_b64 v[12:13], 0\n\t" \
  "v_mov_b32 v8, 0\n\t" \
  "v_mul_f32 v0, v0, v74\n\t" \
  "v_mul_f32 v1, v1, v74\n\t" \
  "v_mul_f32 v2, v2, v74\n\t" \
  "v_mul_f32 v3, v3, v74\n\t" \
  "v_cvt_pk_bf16_f32 v76, v0, v1\n\t" \
  "v_cvt_pk_bf16_f32 v77, v2, v3\n\t" \
  "v_pk_mul_f32 v[4:5], v[4:5], v[74:75] op_sel_hi:[1,0]\n\t" \
  "v_pk_mul_f32 v[6:7], v[6:7], v[74:75] op_sel_hi:[1,0]\n\t" \
  "v_cvt_pk_bf16_f32 v78, v4, v5\n\t" \
  "v_cvt_pk_bf16_f32 v79, v6, v7\n\t" \
  "ds_write_b128 v69, v[76:79] offset:6144\n\t" \
  CH_DP9 \
  "s_waitcnt lgkmcnt(9)\n\t" \
  "v_smfmac_f32_16x16x64_bf16 v[12:15], v[76:79], v[44:51], v68\n\t" \
  "ds_read_b128 v[44:47], v70 offset:26624\n\t" \
  "v_mov_b32 v11, 0\n\t" \
  "s_waitcnt lgkmcnt(8)\n\t" \
  "v_smfmac_f32_16x16x64_bf16 v[56:59], v[76:79], v[16:23], v68\n\t" \
  "ds_read_b128 v[48:51], v70 offset:27648\n\t" \
  "v_mov_b64 v[52:53], 0\n\t" \
  "s_waitcnt lgkmcnt(5)\n\t" \
  "v_smfmac_f32_16x16x64_bf16 v[8:11], v[76:79], v[36:43], v68\n\t" \
  "ds_read_b128 v[16:19], v70 offset:28672\n\t" \
  "v_and_b32 v73, 32, v69\n\t" \
  "v_smfmac_f32_16x16x64_bf16 v[52:55], v[76:79], v[24:31], v68\n\t" \
  "s_waitcnt lgkmcnt(3)\n\t" \
  "s_barrier\n\t" \
  CH_DP10 \
  "ds_read_b128 v[0:3], v69 offset:7168\n\t" \
  "ds_read_b128 v[4:7], v69 offset:4096\n\t" \
  "v_lshl_add_u32 v73, v73, 5, v69\n\t" \
  "v_lshrrev_b32 v71, 7, v69\n\t" \
  "v_and_b32 v74, 0x70, v69\n\t" \
  "v_lshl_add_u32 v71, v71, 14, v74\n\t" \
  "v_bfe_u32 v72, v69, 2, 7\n\t" \
  "v_and_b32 v74, 0x200, v69\n\t" \
  "s_waitcnt lgkmcnt(1)\n\t" \
  "v_smfmac_f32_16x16x64_bf16 v[8:11], v[0:3], v[60:67], v68\n\t" \
  "ds_read_b128 v[20:23], v70 offset:29696\n\t" \
  "v_mov_b64 v[76:77], 0\n\t" \
  "v_smfmac_f32_16x16x64_bf16 v[12:15], v[0:3], v[200:207], v68\n\t" \
  "ds_read_b128 v[32:35], v69 offset:5120\n\t" \
  "v_mov_b64 v[78:79], 0\n\t" \
  "v_smfmac_f32_16x16x64_bf16 v[52:55], v[0:3], v[44:51], v68\n\t" \
  "ds_read_b128 v[24:27], v70 offset:30720\n\t" \
  "v_lshl_add_u32 v72, v74, 3, v72\n\t" \
  "v_smfmac_f32_16x16x64_bf16 v[56:59], v[0:3], v[232:239], v68\n\t" \
  "ds_read_b128 v[28:31], v70 offset:31744\n\t" \
  "s_waitcnt lgkmcnt(4)\n\t" \
  "v_smfmac_f32_16x16x64_bf16 v[12:15], v[4:7], v[208:215], v68\n\t" \
  "ds_read_b128 v[60:63], v70 offset:32768\n\t" \
  "s_waitcnt lgkmcnt(4)\n\t" \
  "v_smfmac_f32_16x16x64_bf16 v[8:11], v[4:7], v[16:23], v68\n\t" \
  "ds_read_b128 v[64:67], v70 offset:33792\n\t" \
  "s_waitcnt lgkmcnt(4)\n\t" \
  "v_smfmac_f32_16x16x64_bf16 v[8:11], v[32:35], v[192:199], v68\n\t" \
  "ds_read_b128 v[36:39], v73 offset:4096\n\t" \
  "v_smfmac_f32_16x16x64_bf16 v[12:15], v[32:35], v[216:223], v68\n\t" \
  "ds_read_b128 v[40:43], v73 offset:6144\n\t" \
  "s_waitcnt lgkmcnt(4)\n\t" \
  "v_smfmac_f32_16x16x64_bf16 v[52:55], v[4:7], v[24:31], v68\n\t" \
  "ds_read_b128 v[44:47], v70 offset:34816\n\t" \
  "v_cvt_pk_bf16_f32 v8, v8, v9\n\t" \
  "v_smfmac_f32_16x16x64_bf16 v[52:55], v[32:35], v[224:231], v68\n\t" \
  "ds_read_b128 v[48:51], v70 offset:35840\n\t" \
  "v_cvt_pk_bf16_f32 v9, v10, v11\n\t" \
  "v_smfmac_f32_16x16x64_bf16 v[56:59], v[4:7], v[240:247], v68\n\t" \
  "v_cvt_pk_bf16_f32 v10, v12, v13\n\t" \
  "s_waitcnt lgkmcnt(4)\n\t" \
  "v_smfmac_f32_16x16x64_bf16 v[56:59], v[32:35], v[60:67], v68\n\t" \
  "v_cvt_pk_bf16_f32 v11, v14, v15\n\t" \
  "s_waitcnt lgkmcnt(3)\n\t" \
  "v_smfmac_f32_16x16x64_bf16 v[76:79], v[36:39], v[248:255], v68\n\t" \
  "buffer_store_dwordx4 v[8:11], v71, s[28:31], s54 offen sc1\n\t" \
  CH_DP11 \
  "v_cvt_pk_bf16_f32 v52, v52, v53\n\t" \
  "s_waitcnt lgkmcnt(0)\n\t" \
  "v_smfmac_f32_16x16x64_bf16 v[76:79], v[40:43], v[44:51], v68\n\t" \
  "v_cvt_pk_bf16_f32 v53, v54, v55\n\t" \
  "v_cvt_pk_bf16_f32 v54, v56, v57\n\t" \
  "v_cvt_pk_bf16_f32 v55, v58, v59\n\t" \
  "buffer_store_dwordx4 v[52:55], v71, s[28:31], s75 offen sc1\n\t" \
  "s_nop 2\n\t" \
  "v_add_f32 v76, v76, v78\n\t" \
  "v_add_f32 v77, v77, v79\n\t" \
  "v_cvt_pk_bf16_f32 v76, v76, v77\n\t" \
  "buffer_store_dword v76, v72, s[28:31], s55 offen sc1\n\t" \
  CH_DP12 \
  "s_memrealtime s[70:71]\n\t" \
  "s_waitcnt vmcnt(3)\n\t" \
  "v_readfirstlane_b32 s65, v75\n\t" \
  "s_barrier\n\t" \
  "s_waitcnt lgkmcnt(0)\n\t" \
  "s_not_b32 s65, s65\n\t" \
  "s_lshl_b32 s61, s70, 8\n\t" \
  "s_andn2_b32 s64, s65, 0xff\n\t" \
  "s_sub_u32 s64, s64, s61\n\t" \
  "s_ashr_i32 s64, s64, 8\n\t" \
  "s_cmp_eq_u32 s65, 0\n\t" \
  "s_cselect_b32 s64, " CH_DLF ", s64\n\t" \
  "s_min_i32 s64, s64, s34\n\t" \
  "s_max_i32 s64, s64, " CH_DLL1 "\n\t" \
  "s_add_u32 s64, s64, s70\n\t" \
  CH_ST2 \
  "s_add_u32 s55, s41, s53\n\t" \
  "s_mov_b32 s74, 0\n\t" \
  "s_movk_i32 s60, 8192\n\t" \
  "RWT2_%=:\n\t" \
  "s_memrealtime s[72:73]\n\t" \
  "s_sub_u32 s60, s60, 1\n\t" \
  "s_cselect_b32 s61, 0, -1\n\t" \
  "s_waitcnt lgkmcnt(0)\n\t" \
  "s_sub_i32 s73, s72, s64\n\t" \
  "s_and_b32 s73, s73, s61\n\t" \
  "s_cmp_lt_i32 s73, 0\n\t" \
  "s_cbranch_scc1 RWT2_%=\n\t" \
  "RRETRY2_%=:\n\t" \
  CH_DP13 \
  "buffer_load_dwordx4 v[0:3], v69, s[28:31], s55 offen offset:0 sc1\n\t" \
  "buffer_load_dwordx4 v[4:7], v69, s[28:31], s55 offen offset:1024 sc1\n\t" \
  "buffer_load_dwordx4 v[8:11], v69, s[28:31], s55 offen offset:2048 sc1\n\t" \
  "buffer_load_dwordx4 v[12:15], v69, s[28:31], s55 offen offset:3072 sc1\n\t" \
  "v_lshrrev_b32 v20, 2, v69\n\t" \
  "v_and_b32 v20, 16, v20\n\t" \
  "v_mov_b32 v21, 0x3f80\n\t" \
  "v_lshlrev_b32 v21, v20, v21\n\t" \
  "v_bfe_u32 v20, v69, 4, 2\n\t" \
  "v_cmp_eq_u32 vcc, 0, v20\n\t" \
  "s_nop 1\n\t" \
  "v_cndmask_b32 v32, 0, v21, vcc\n\t" \
  "v_cmp_eq_u32 vcc, 1, v20\n\t" \
  "s_nop 1\n\t" \
  "v_cndmask_b32 v33, 0, v21, vcc\n\t" \
  "v_cmp_eq_u32 vcc, 2, v20\n\t" \
  "s_nop 1\n\t" \
  "v_cndmask_b32 v34, 0, v21, vcc\n\t" \
  "v_cmp_eq_u32 vcc, 3, v20\n\t" \
  "s_nop 1\n\t" \
  "v_cndmask_b32 v35, 0, v21, vcc\n\t" \
  "v_bfe_u32 v38, v69, 7, 1\n\t" \
  "v_bfe_u32 v20, v69, 9, 1\n\t" \
  "v_lshl_add_u32 v38, v20, 1, v38\n\t" \
  "v_mul_u32_u24 v38, 16384, v38\n\t" \
  "v_and_b32 v20, 0x60, v69\n\t" \
  "v_add_u32 v38, v38, v20\n\t" \
  "v_bfe_u32 v20, v69, 8, 1\n\t" \
  "v_lshl_add_u32 v38, v20, 4, v38\n\t" \
  "v_bfe_u32 v20, v69, 4, 1\n\t" \
  "v_lshl_add_u32 v38, v20, 3, v38\n\t" \
  "s_add_u32 s54, s37, s53\n\t" \
  "v_mov_b32 v44, 0\n\t" \
  CH_DP17 \
  "s_waitcnt vmcnt(3)\n\t" \
  CH_DP14 \
  "v_mfma_f32_16x16x32_bf16 v[40:43], v[32:35], v[0:3], 0\n\t" \
  "v_max3_u32 v44, v44, v0, v1\n\t" \
  "v_max3_u32 v44, v44, v2, v3\n\t" \
  "s_waitcnt vmcnt(2)\n\t" \
  "v_mfma_f32_16x16x32_bf16 v[40:43], v[32:35], v[4:7], v[40:43]\n\t" \
  "v_max3_u32 v44, v44, v4, v5\n\t" \
  "v_max3_u32 v44, v44, v6, v7\n\t" \
  "s_waitcnt vmcnt(1)\n\t" \
  "v_mfma_f32_16x16x32_bf16 v[40:43], v[32:35], v[8:11], v[40:43]\n\t" \
  "v_max3_u32 v44, v44, v8, v9\n\t" \
  "v_max3_u32 v44, v44, v10, v11\n\t" \
  "s_waitcnt vmcnt(0)\n\t" \
  CH_DP15 \
  "v_mfma_f32_16x16x32_bf16 v[40:43], v[32:35], v[12:15], v[40:43]\n\t" \
  "v_max3_u32 v44, v44, v12, v13\n\t" \
  "v_max3_u32 v44, v44, v14, v15\n\t" \
  "v_cmp_eq_u32 vcc, -1, v44\n\t" \
  "s_cbranch_vccnz RFAIL2_%=\n\t" \
  "s_nop 3\n\t" \
  "v_add_f32_dpp v40, v40, v40 row_ror:8 row_mask:0xf bank_mask:0xf\n\t" \
  "v_add_f32_dpp v41, v41, v41 row_ror:8 row_mask:0xf bank_mask:0xf\n\t" \
  "v_add_f32_dpp v42, v42, v42 row_ror:8 row_mask:0xf bank_mask:0xf\n\t" \
  "v_add_f32_dpp v43, v43, v43 row_ror:8 row_mask:0xf bank_mask:0xf\n\t" \
  "v_cvt_pk_bf16_f32 v40, v40, v41\n\t" \
  "v_cvt_pk_bf16_f32 v41, v42, v43\n\t" \
  "buffer_store_dwordx2 v[40:41], v38, s[28:31], s54 offen sc1\n\t" \
  "RDONE2_%=:\n\t" \
  CH_DP16 \
  CH_ST4 \
  "s_barrier\n\t" \
  CH_ST3 \
  CH_TRC \
  "s_add_u32 s48, s48, 1\n\t" \
  "s_mov_b32 s49, s53\n\t" \
  "s_add_u32 s53, s53, " CH_SLOT "\n\t" \
  "s_add_u32 s60, s53, " CH_SKIP "\n\t" \
  "s_lshr_b32 s61, s96, 1\n\t" \
  "s_add_u32 s86, s86, 1\n\t" \
  "s_cmp_eq_u32 s86, 3\n\t" \
  "s_cselect_b32 s53, s60, s53\n\t" \
  "s_cselect_b32 s96, s61, s96\n\t" \
  "s_cselect_b32 s86, 0, s86\n\t" \
  "s_andn2_b32 s53, s53, 0x2000\n\t" \
  "s_and_b32 s60, s96, 1\n\t" \
  "s_lshl_b32 s60, s60, 13\n\t" \
  "s_or_b32 s53, s53, s60\n\t" \
  "s_mov_b32 s68, 0\n\t" \
  "s_add_u32 s82, s48, 0\n\t" \
  CH_SEGC \
  "s_lshr_b64 s[60:61], s[90:91], s82\n\t" \
  "s_and_b32 s60, s60, 1\n\t" \
  "s_lshl_b32 s60, s60, 13\n\t" \
  "s_add_u32 s82, s82, " CH_SG0 "\n\t" \
  "s_lshl_b32 s82, s82, 21\n\t" \
  "s_or_b32 s82, s82, s60\n\t" \
  "s_add_u32 s50, s82, s38\n\t" \
  "s_add_u32 s69, s82, s39\n\t" \
  "s_cmp_eq_u32 s34, 0\n\t" \
  "s_cbranch_scc1 LLOAD_%=\n\t" \
  "s_branch LG2_%=\n\t" \
  "RFAIL2_%=:\n\t" \
  "s_add_u32 s74, s74, 1\n\t" \
  "s_sleep " CH_RSL "\n\t" \
  "s_waitcnt vmcnt(0)\n\t" \
  "s_cmp_gt_u32 s74, s84\n\t" \
  "s_cbranch_scc1 RG2_%=\n\t" \
  "s_and_b32 s60, s74, 1023\n\t" \
  "s_cmp_lg_u32 s60, 0\n\t" \
  "s_cbranch_scc1 RRETRY2_%=\n\t" \
  "buffer_load_dword v46, off, s[28:31], s92 sc1\n\t" \
  "s_waitcnt vmcnt(0)\n\t" \
  "v_readfirstlane_b32 s60, v46\n\t" \
  "s_nop 0\n\t" \
  "s_cmp_eq_u32 s60, 0\n\t" \
  "s_cbranch_scc1 RRETRY2_%=\n\t" \
  "RG2_%=:\n\t" \
  "s_mov_b32 s84, 0\n\t" \
  "s_lshl_b32 s60, s36, 4\n\t" \
  "s_or_b32 s60, s60, s48\n\t" \
  "s_or_b32 s60, s60, 536870912\n\t" \
  "v_mov_b32 v46, s60\n\t" \
  "v_mov_b32 v47, 0\n\t" \
  "s_mov_b64 exec, 1\n\t" \
  "buffer_atomic_cmpswap v[46:47], off, s[28:31], s92 sc1\n\t" \
  "s_mov_b64 exec, -1\n\t" \
  "s_branch RDONE2_%=\n\t" \
  "LFAIL2_%=:\n\t" \
  "s_add_u32 s68, s68, 1\n\t" \
  "s_waitcnt lgkmcnt(0)\n\t" \
  "s_cmp_gt_u32 s68, s83\n\t" \
  "s_cbranch_scc1 LGV2_%=\n\t" \
  "s_barrier\n\t" \
  "s_waitcnt vmcnt(8)\n\t" \
  "s_branch LG2_%=\n\t" \
  "LGV2_%=:\n\t" \
  "s_mov_b32 s83, 0\n\t" \
  "s_lshl_b32 s60, s36, 4\n\t" \
  "s_or_b32 s60, s60, s48\n\t" \
  "s_or_b32 s60, s60, 805306368\n\t" \
  "v_mov_b32 v46, s60\n\t" \
  "v_mov_b32 v47, 0\n\t" \
  "s_mov_b64 exec, 1\n\t" \
  "buffer_atomic_cmpswap v[46:47], off, s[28:31], s92 sc1\n\t" \
  "s_mov_b64 exec, -1\n\t" \
  "s_branch LGO2_%=\n\t" \
  "LLAST2_%=:\n\t" \
  "s_waitcnt vmcnt(0) lgkmcnt(0)\n\t" \
  "s_barrier\n\t" \
  "s_branch LEND_%=\n\t" \
  ".p2align 6\n\t" \
  "LG3_%=:\n\t" \
  "s_barrier\n\t" \
  "buffer_load_dwordx4 v[52:55], v69, s[28:31], s50 offen sc1\n\t" \
  "buffer_load_dwordx4 v[56:59], v69, s[28:31], s50 offen offset:1024 sc1\n\t" \
  "s_add_u32 s64, s50, 16384\n\t" \
  "s_movk_i32 m0, 11264\n\t" \
  "v_lshlrev_b32 v75, 1, v69\n\t" \
  "buffer_load_dword v75, s[28:31], s64 offen sc1 lds\n\t" \
  "ds_read_b128 v[44:47], v70 offset:0\n\t" \
  "ds_read_b128 v[48:51], v70 offset:1024\n\t" \
  "ds_read_b128 v[60:63], v70 offset:2048\n\t" \
  "ds_read_b128 v[64:67], v70 offset:3072\n\t" \
  "v_mov_b64 v[36:37], 0\n\t" \
  "v_mov_b64 v[38:39], 0\n\t" \
  "v_mov_b64 v[40:41], 0\n\t" \
  "v_mov_b64 v[42:43], 0\n\t" \
  "v_mov_b32 v73, 0\n\t" \
  "v_lshrrev_b32 v71, 1, v69\n\t" \
  "v_add_u32 v71, 4, v71\n\t" \
  "v_lshrrev_b32 v72, 3, v69\n\t" \
  "v_and_b32 v72, 0x78, v72\n\t" \
  "v_and_b32 v74, 0x30, v69\n\t" \
  "v_lshl_add_u32 v72, v74, 3, v72\n\t" \
  "s_add_u32 s54, s53, s36\n\t" \
  "s_add_u32 s55, s54, 573440\n\t" \
  "s_add_u32 s54, s54, 135168\n\t" \
  "s_add_u32 s75, s54, 262144\n\t" \
  "s_add_u32 s80, s53, s44\n\t" \
  "s_or_b32 s87, s50, 0x1fff80\n\t" \
  "s_add_u32 s87, s87, 4194304\n\t" \
  CH_TC0 \
  "s_barrier\n\t" \
  CH_DP0 \
  "ds_read_b128 v[0:3], v69 offset:0\n\t" \
  "ds_read_b128 v[4:7], v69 offset:1024\n\t" \
  "ds_read_b128 v[8:11], v69 offset:2048\n\t" \
  "ds_read_b128 v[12:15], v69 offset:3072\n\t" \
  "s_waitcnt lgkmcnt(3)\n\t" \
  "v_smfmac_f32_16x16x64_bf16 v[36:39], v[0:3], v[44:51], v68\n\t" \
  "ds_read_b128 v[44:47], v70 offset:4096\n\t" \
  "v_smfmac_f32_16x16x64_bf16 v[40:43], v[0:3], v[60:67], v68\n\t" \
  "ds_read_b128 v[48:51], v70 offset:5120\n\t" \
  "v_max3_u32 v73, v73, v0, v2\n\t" \
  "s_waitcnt lgkmcnt(4)\n\t" \
  "v_smfmac_f32_16x16x64_bf16 v[36:39], v[4:7], a[0:7], v68\n\t" \
  "s_barrier\n\t" \
  "ds_read_b128 v[16:19], v69 offset:4096\n\t" \
  CH_DP1 \
  "v_smfmac_f32_16x16x64_bf16 v[40:43], v[4:7], a[64:71], v68\n\t" \
  "ds_read_b128 v[20:23], v69 offset:5120\n\t" \
  "v_max3_u32 v73, v73, v4, v6\n\t" \
  "s_waitcnt lgkmcnt(5)\n\t" \
  "v_smfmac_f32_16x16x64_bf16 v[36:39], v[8:11], a[8:15], v68\n\t" \
  "ds_read_b128 v[24:27], v69 offset:6144\n\t" \
  "v_max3_u32 v73, v73, v8, v10\n\t" \
  "v_smfmac_f32_16x16x64_bf16 v[40:43], v[8:11], a[72:79], v68\n\t" \
  "ds_read_b128 v[28:31], v69 offset:7168\n\t" \
  "s_waitcnt lgkmcnt(6)\n\t" \
  "v_max3_u32 v73, v73, v12, v14\n\t" \
  "v_smfmac_f32_16x16x64_bf16 v[36:39], v[12:15], a[16:23], v68\n\t" \
  "ds_read_b128 v[32:35], v69 offset:8192\n\t" \
  "s_waitcnt lgkmcnt(5)\n\t" \
  "v_smfmac_f32_16x16x64_bf16 v[40:43], v[12:15], v[44:51], v68\n\t" \
  "ds_read_b128 v[60:63], v70 offset:6144\n\t" \
  "s_waitcnt lgkmcnt(5)\n\t" \
  "v_smfmac_f32_16x16x64_bf16 v[36:39], v[16:19], a[24:31], v68\n\t" \
  "ds_read_b128 v[64:67], v70 offset:7168\n\t" \
  "v_max3_u32 v73, v73, v16, v18\n\t" \
  "v_smfmac_f32_16x16x64_bf16 v[40:43], v[16:19], a[80:87], v68\n\t" \
  "s_waitcnt lgkmcnt(5)\n\t" \
  "v_max3_u32 v73, v73, v20, v22\n\t" \
  "v_smfmac_f32_16x16x64_bf16 v[36:39], v[20:23], a[32:39], v68\n\t" \
  "s_waitcnt lgkmcnt(4)\n\t" \
  "v_max3_u32 v73, v73, v24, v26\n\t" \
  "v_smfmac_f32_16x16x64_bf16 v[36:39], v[24:27], a[40:47], v68\n\t" \
  "s_waitcnt lgkmcnt(3)\n\t" \
  "v_max3_u32 v73, v73, v28, v30\n\t" \
  "v_smfmac_f32_16x16x64_bf16 v[36:39], v[28:31], a[48:55], v68\n\t" \
  "s_waitcnt lgkmcnt(2)\n\t" \
  "v_max3_u32 v73, v73, v32, v34\n\t" \
  "v_smfmac_f32_16x16x64_bf16 v[36:39], v[32:35], a[56:63], v68\n\t" \
  "v_cmp_eq_u32 vcc, -1, v73\n\t" \
  "s_waitcnt lgkmcnt(0)\n\t" \
  "v_smfmac_f32_16x16x64_bf16 v[40:43], v[20:23], v[60:67], v68\n\t" \
  "ds_read_b128 v[60:63], v70 offset:8192\n\t" \
  "v_mov_b64 v[44:45], 0\n\t" \
  "s_cbranch_vccnz LFAIL3_%=\n\t" \
  "LGO3_%=:\n\t" \
  "v_smfmac_f32_16x16x64_bf16 v[40:43], v[24:27], a[88:95], v68\n\t" \
  "ds_read_b128 v[64:67], v70 offset:9216\n\t" \
  "v_mov_b64 v[46:47], 0\n\t" \
  "v_smfmac_f32_16x16x64_bf16 v[40:43], v[28:31], a[96:103], v68\n\t" \
  "v_add_f32 v74, v36, v38\n\t" \
  "v_smfmac_f32_16x16x64_bf16 v[40:43], v[32:35], a[104:111], v68\n\t" \
  "v_add_f32 v37, v37, v39\n\t" \
  CH_DP2 \
  "v_smfmac_f32_16x16x64_bf16 v[44:47], v[0:3], a[112:119], v68\n\t" \
  "v_cvt_pk_bf16_f32 v74, v74, v37\n\t" \
  "v_smfmac_f32_16x16x64_bf16 v[44:47], v[4:7], a[120:127], v68\n\t" \
  "v_add_f32 v75, v40, v42\n\t" \
  "s_waitcnt lgkmcnt(0)\n\t" \
  "v_smfmac_f32_16x16x64_bf16 v[44:47], v[8:11], v[60:67], v68\n\t" \
  "ds_read_b128 v[60:63], v70 offset:10240\n\t" \
  "v_add_f32 v41, v41, v43\n\t" \
  "v_cvt_pk_bf16_f32 v75, v75, v41\n\t" \
  "v_smfmac_f32_16x16x64_bf16 v[44:47], v[12:15], a[128:135], v68\n\t" \
  "ds_write2st64_b32 v71, v74, v75 offset0:40 offset1:42\n\t" \
  "v_mov_b64 v[48:49], 0\n\t" \
  "v_smfmac_f32_16x16x64_bf16 v[44:47], v[16:19], a[136:143], v68\n\t" \
  "ds_read_b128 v[64:67], v70 offset:11264\n\t" \
  "v_mov_b64 v[50:51], 0\n\t" \
  "v_smfmac_f32_16x16x64_bf16 v[44:47], v[20:23], a[144:151], v68\n\t" \
  "v_smfmac_f32_16x16x64_bf16 v[44:47], v[24:27], a[152:159], v68\n\t" \
  "v_smfmac_f32_16x16x64_bf16 v[44:47], v[28:31], a[160:167], v68\n\t" \
  "s_waitcnt lgkmcnt(0)\n\t" \
  "v_smfmac_f32_16x16x64_bf16 v[44:47], v[32:35], v[60:67], v68\n\t" \
  "s_barrier\n\t" \
  "ds_read_b64_tr_b16 v[76:77], v72 offset:10240\n\t" \
  CH_DP3 \
  "v_smfmac_f32_16x16x64_bf16 v[48:51], v[0:3], a[168:175], v68\n\t" \
  "ds_read_b64_tr_b16 v[78:79], v72 offset:10752\n\t" \
  "v_smfmac_f32_16x16x64_bf16 v[48:51], v[4:7], a[176:183], v68\n\t" \
  "v_add_f32 v74, v44, v46\n\t" \
  "v_smfmac_f32_16x16x64_bf16 v[48:51], v[8:11], a[184:191], v68\n\t" \
  "buffer_load_dwordx4 v[60:63], v69, s[28:31], s64 offen sc1\n\t" \
  "v_add_f32 v45, v45, v47\n\t" \
  "v_smfmac_f32_16x16x64_bf16 v[48:51], v[12:15], a[192:199], v68\n\t" \
  "v_cvt_pk_bf16_f32 v74, v74, v45\n\t" \
  "s_waitcnt vmcnt(3)\n\t" \
  "s_waitcnt lgkmcnt(0)\n\t" \
  "v_mfma_f32_16x16x32_bf16 v[36:39], v[52:55], v[76:79], 0\n\t" \
  "s_waitcnt vmcnt(2)\n\t" \
  "v_mfma_f32_16x16x32_bf16 v[40:43], v[56:59], v[76:79], 0\n\t" \
  "v_mov_b64 v[52:53], 0\n\t" \
  "v_mov_b64 v[54:55], 0\n\t" \
  "v_smfmac_f32_16x16x64_bf16 v[48:51], v[16:19], a[200:207], v68\n\t" \
  "buffer_load_dwordx4 v[64:67], v69, s[28:31], s64 offen offset:1024 sc1\n\t" \
  "v_mov_b64 v[56:57], 0\n\t" \
  "v_smfmac_f32_16x16x64_bf16 v[48:51], v[20:23], a[208:215], v68\n\t" \
  "v_add_f32_dpp v36, v40, v36 quad_perm:[2,3,0,1] row_mask:0xf bank_mask:0xf\n\t" \
  "v_smfmac_f32_16x16x64_bf16 v[48:51], v[24:27], a[216:223], v68\n\t" \
  "v_add_f32_dpp v37, v41, v37 quad_perm:[2,3,0,1] row_mask:0xf bank_mask:0xf\n\t" \
  "v_smfmac_f32_16x16x64_bf16 v[48:51], v[28:31], a[224:231], v68\n\t" \
  "v_add_f32_dpp v38, v42, v38 quad_perm:[2,3,0,1] row_mask:0xf bank_mask:0xf\n\t" \
  "v_smfmac_f32_16x16x64_bf16 v[48:51], v[32:35], a[232:239], v68\n\t" \
  "v_add_f32_dpp v39, v43, v39 quad_perm:[2,3,0,1] row_mask:0xf bank_mask:0xf\n\t" \
  CH_DP4 \
  "v_smfmac_f32_16x16x64_bf16 v[52:55], v[0:3], a[240:247], v68\n\t" \
  "s_mov_b64 exec, s[58:59]\n\t" \
  "ds_write_b128 v69, v[36:39] offset:3104\n\t" \
  "s_mov_b64 exec, -1\n\t" \
  "v_mov_b64 v[58:59], 0\n\t" \
  "v_smfmac_f32_16x16x64_bf16 v[52:55], v[4:7], a[248:255], v68\n\t" \
  "buffer_load_dword off, s[28:31], s87 sc1 lds\n\t" \
  "v_add_f32 v75, v48, v50\n\t" \
  "v_add_f32 v49, v49, v51\n\t" \
  "v_cvt_pk_bf16_f32 v75, v75, v49\n\t" \
  "v_smfmac_f32_16x16x64_bf16 v[56:59], v[0:3], v[136:143], v68\n\t" \
  "ds_write2st64_b32 v71, v74, v75 offset0:4 offset1:6\n\t" \
  "v_smfmac_f32_16x16x64_bf16 v[56:59], v[4:7], v[144:151], v68\n\t" \
  "ds_read_b64_tr_b16 v[0:1], v72 offset:9216\n\t" \
  "ds_read_b64_tr_b16 v[2:3], v72 offset:9728\n\t" \
  "v_smfmac_f32_16x16x64_bf16 v[52:55], v[8:11], v[80:87], v68\n\t" \
  "ds_read_b128 v[36:39], v70 offset:12288\n\t" \
  "v_smfmac_f32_16x16x64_bf16 v[56:59], v[8:11], v[152:159], v68\n\t" \
  "ds_read_b128 v[40:43], v70 offset:13312\n\t" \
  "v_smfmac_f32_16x16x64_bf16 v[52:55], v[12:15], v[88:95], v68\n\t" \
  "s_waitcnt lgkmcnt(4)\n\t" \
  "s_barrier\n\t" \
  CH_DP5 \
  "ds_read_b64_tr_b16 v[44:45], v72 offset:1024\n\t" \
  "ds_read_b64_tr_b16 v[46:47], v72 offset:1536\n\t" \
  "v_smfmac_f32_16x16x64_bf16 v[56:59], v[12:15], v[160:167], v68\n\t" \
  "ds_read_b64_tr_b16 v[48:49], v72 offset:0\n\t" \
  "ds_read_b64_tr_b16 v[50:51], v72 offset:512\n\t" \
  "v_smfmac_f32_16x16x64_bf16 v[52:55], v[16:19], v[96:103], v68\n\t" \
  "ds_read_b128 v[4:7], v69 offset:2048\n\t" \
  "s_waitcnt lgkmcnt(5)\n\t" \
  "v_smfmac_f32_16x16x64_bf16 v[56:59], v[16:19], v[36:43], v68\n\t" \
  "ds_read_b128 v[8:11], v69 offset:3072\n\t" \
  "s_waitcnt lgkmcnt(4)\n\t" \
  "v_mfma_f32_16x16x32_bf16 v[12:15], v[44:47], v[76:79], 0\n\t" \
  "ds_read_b128 v[36:39], v70 offset:14336\n\t" \
  "s_waitcnt lgkmcnt(3)\n\t" \
  "v_mfma_f32_16x16x32_bf16 v[12:15], v[48:51], v[0:3], v[12:15]\n\t" \
  "ds_read_b128 v[40:43], v70 offset:15360\n\t" \
  "v_smfmac_f32_16x16x64_bf16 v[52:55], v[20:23], v[104:111], v68\n\t" \
  "s_waitcnt lgkmcnt(2)\n\t" \
  "v_add_f32 v4, v4, v8\n\t" \
  "v_add_f32 v5, v5, v9\n\t" \
  "v_add_f32 v6, v6, v10\n\t" \
  CH_DP6 \
  "v_cndmask_b32 v78, v13, v14, s[56:57]\n\t" \
  "v_cndmask_b32 v76, v12, v15, s[56:57]\n\t" \
  "v_add_f32 v7, v7, v11\n\t" \
  "v_max3_f32 v77, v4, v5, v6\n\t" \
  "v_add_f32_dpp v76, v78, v76 quad_perm:[2,3,0,1] row_mask:0xf bank_mask:0xf\n\t" \
  "v_max3_f32 v77, v77, v7, v76\n\t" \
  "v_smfmac_f32_16x16x64_bf16 v[56:59], v[20:23], v[168:175], v68\n\t" \
  "ds_read_b128 v[44:47], v70 offset:16384\n\t" \
  "v_max_f32_dpp v77, v77, v77 quad_perm:[2,3,0,1] row_mask:0xf bank_mask:0xf\n\t" \
  "v_mov_b32 v78, v77\n\t" \
  "v_smfmac_f32_16x16x64_bf16 v[52:55], v[24:27], v[112:119], v68\n\t" \
  "ds_read_b128 v[48:51], v70 offset:17408\n\t" \
  "v_permlane16_swap_b32 v77, v78\n\t" \
  "v_max_f32 v77, v77, v78\n\t" \
  "v_mov_b32 v78, v77\n\t" \
  "v_smfmac_f32_16x16x64_bf16 v[52:55], v[28:31], v[120:127], v68\n\t" \
  "ds_read_b128 v[16:19], v70 offset:18432\n\t" \
  "v_permlane32_swap_b32 v77, v78\n\t" \
  "v_max_f32 v77, v77, v78\n\t" \
  "v_smfmac_f32_16x16x64_bf16 v[52:55], v[32:35], v[128:135], v68\n\t" \
  "ds_read_b128 v[20:23], v70 offset:19456\n\t" \
  "v_mul_f32 v77, 0xbe0293ee, v77\n\t" \
  CH_DP7 \
  "v_smfmac_f32_16x16x64_bf16 v[56:59], v[24:27], v[176:183], v68\n\t" \
  "v_fmamk_f32 v4, v4, 0x3e0293ee, v77\n\t" \
  "v_smfmac_f32_16x16x64_bf16 v[56:59], v[28:31], v[184:191], v68\n\t" \
  "ds_read_b128 v[24:27], v70 offset:22528\n\t" \
  "v_fmamk_f32 v5, v5, 0x3e0293ee, v77\n\t" \
  "s_waitcnt lgkmcnt(5)\n\t" \
  "v_smfmac_f32_16x16x64_bf16 v[56:59], v[32:35], v[36:43], v68\n\t" \
  "ds_read_b128 v[28:31], v70 offset:23552\n\t" \
  "v_fmamk_f32 v6, v6, 0x3e0293ee, v77\n\t" \
  "v_fmamk_f32 v7, v7, 0x3e0293ee, v77\n\t" \
  "v_fmamk_f32 v76, v76, 0x3e0293ee, v77\n\t" \
  "v_exp_f32 v4, v4\n\t" \
  "v_exp_f32 v5, v5\n\t" \
  "v_exp_f32 v6, v6\n\t" \
  "v_exp_f32 v7, v7\n\t" \
  "v_cvt_pk_bf16_f32 v4, v4, v5\n\t" \
  "v_exp_f32 v76, v76\n\t" \
  "v_cvt_pk_bf16_f32 v5, v6, v7\n\t" \
  "v_mov_b32_dpp v12, v4 quad_perm:[0,1,0,1] row_mask:0xf bank_mask:0xf\n\t" \
  "v_mov_b32_dpp v14, v4 quad_perm:[2,3,2,3] row_mask:0xf bank_mask:0xf\n\t" \
  "v_mov_b32_dpp v13, v5 quad_perm:[0,1,0,1] row_mask:0xf bank_mask:0xf\n\t" \
  "v_mov_b32_dpp v15, v5 quad_perm:[2,3,2,3] row_mask:0xf bank_mask:0xf\n\t" \
  "v_add_f32 v52, v52, v54\n\t" \
  "v_add_f32 v53, v53, v55\n\t" \
  "s_waitcnt vmcnt(2)\n\t" \
  CH_DP8 \
  "v_mfma_f32_16x16x32_bf16 v[0:3], v[60:63], v[12:15], 0\n\t" \
  "buffer_load_dword v75, off, s[28:31], s80 offset:1024 sc1\n\t" \
  "ds_read_b128 v[36:39], v70 offset:20480\n\t" \
  "v_add_f32 v56, v56, v58\n\t" \
  "v_add_f32 v57, v57, v59\n\t" \
  "v_cvt_pk_bf16_f32 v52, v52, v53\n\t" \
  "v_cvt_pk_bf16_f32 v53, v56, v57\n\t" \
  "s_waitcnt vmcnt(2)\n\t" \
  "v_mfma_f32_16x16x32_bf16 v[4:7], v[64:67], v[12:15], 0\n\t" \
  "ds_read_b128 v[40:43], v70 offset:21504\n\t" \
  "v_mov_b64 v[56:57], s[88:89]\n\t" \
  "v_mov_b64 v[58:59], s[88:89]\n\t" \
  "v_mov_b32_dpp v78, v76 quad_perm:[0,1,0,1] row_mask:0xf bank_mask:0xf\n\t" \
  "v_mov_b32_dpp v79, v76 quad_perm:[2,3,2,3] row_mask:0xf bank_mask:0xf\n\t" \
  "v_mfma_f32_16x16x32_bf16 v[32:35], v[56:59], v[12:15], 0\n\t" \
  "ds_read_b128 v[60:63], v70 offset:24576\n\t" \
  "v_cvt_pk_bf16_f32 v11, v78, v79\n\t" \
  "v_cvt_pk_bf16_f32 v8, v78, v79\n\t" \
  "v_mov_b64 v[54:55], 0\n\t" \
  "v_mov_b32 v9, 0\n\t" \
  "v_mov_b32 v10, 0\n\t" \
  "v_mov_b64 v[14:15], 0\n\t" \
  "s_cmp_eq_u32 s48, 63\n\t" \
  "v_mfma_f32_16x16x32_bf16 v[32:35], v[54:57], v[8:11], v[32:35]\n\t" \
  "v_mfma_f32_16x16x32_bf16 v[0:3], v[52:55], v[8:11], v[0:3]\n\t" \
  "buffer_store_dwordx2 v[52:53], v71, s[28:31], s69 offen offset:3580 sc1\n\t" \
  "ds_read_b128 v[64:67], v70 offset:25600\n\t" \
  "v_mov_b64 v[56:57], 0\n\t" \
  "v_mov_b64 v[58:59], 0\n\t" \
  "v_mfma_f32_16x16x32_bf16 v[4:7], v[52:55], v[10:13], v[4:7]\n\t" \
  "s_cbranch_scc1 LLAST3_%=\n\t" \
  "v_rcp_f32 v74, v32\n\t" \
  "v_mov_b64 v[12:13], 0\n\t" \
  "v_mov_b32 v8, 0\n\t" \
  "v_mul_f32 v0, v0, v74\n\t" \
  "v_mul_f32 v1, v1, v74\n\t" \
  "v_mul_f32 v2, v2, v74\n\t" \
  "v_mul_f32 v3, v3, v74\n\t" \
  "v_cvt_pk_bf16_f32 v76, v0, v1\n\t" \
  "v_cvt_pk_bf16_f32 v77, v2, v3\n\t" \
  "v_pk_mul_f32 v[4:5], v[4:5], v[74:75] op_sel_hi:[1,0]\n\t" \
  "v_pk_mul_f32 v[6:7], v[6:7], v[74:75] op_sel_hi:[1,0]\n\t" \
  "v_cvt_pk_bf16_f32 v78, v4, v5\n\t" \
  "v_cvt_pk_bf16_f32 v79, v6, v7\n\t" \
  "ds_write_b128 v69, v[76:79] offset:7168\n\t" \
  CH_DP9 \
  "s_waitcnt lgkmcnt(9)\n\t" \
  "v_smfmac_f32_16x16x64_bf16 v[12:15], v[76:79], v[44:51], v68\n\t" \
  "ds_read_b128 v[44:47], v70 offset:26624\n\t" \
  "v_mov_b32 v11, 0\n\t" \
  "s_waitcnt lgkmcnt(8)\n\t" \
  "v_smfmac_f32_16x16x64_bf16 v[56:59], v[76:79], v[16:23], v68\n\t" \
  "ds_read_b128 v[48:51], v70 offset:27648\n\t" \
  "v_mov_b64 v[52:53], 0\n\t" \
  "s_waitcnt lgkmcnt(5)\n\t" \
  "v_smfmac_f32_16x16x64_bf16 v[8:11], v[76:79], v[36:43], v68\n\t" \
  "ds_read_b128 v[16:19], v70 offset:28672\n\t" \
  "v_and_b32 v73, 32, v69\n\t" \
  "v_smfmac_f32_16x16x64_bf16 v[52:55], v[76:79], v[24:31], v68\n\t" \
  "s_waitcnt lgkmcnt(3)\n\t" \
  "s_barrier\n\t" \
  CH_DP10 \
  "ds_read_b128 v[0:3], v69 offset:6144\n\t" \
  "ds_read_b128 v[4:7], v69 offset:5120\n\t" \
  "v_lshl_add_u32 v73, v73, 5, v69\n\t" \
  "v_lshrrev_b32 v71, 7, v69\n\t" \
  "v_and_b32 v74, 0x70, v69\n\t" \
  "v_lshl_add_u32 v71, v71, 14, v74\n\t" \
  "v_bfe_u32 v72, v69, 2, 7\n\t" \
  "v_and_b32 v74, 0x200, v69\n\t" \
  "s_waitcnt lgkmcnt(1)\n\t" \
  "v_smfmac_f32_16x16x64_bf16 v[8:11], v[0:3], v[60:67], v68\n\t" \
  "ds_read_b128 v[20:23], v70 offset:29696\n\t" \
  "v_mov_b64 v[76:77], 0\n\t" \
  "v_smfmac_f32_16x16x64_bf16 v[12:15], v[0:3], v[200:207], v68\n\t" \
  "ds_read_b128 v[32:35], v69 offset:4096\n\t" \
  "v_mov_b64 v[78:79], 0\n\t" \
  "v_smfmac_f32_16x16x64_bf16 v[52:55], v[0:3], v[44:51], v68\n\t" \
  "ds_read_b128 v[24:27], v70 offset:30720\n\t" \
  "v_lshl_add_u32 v72, v74, 3, v72\n\t" \
  "v_smfmac_f32_16x16x64_bf16 v[56:59], v[0:3], v[232:239], v68\n\t" \
  "ds_read_b128 v[28:31], v70 offset:31744\n\t" \
  "s_waitcnt lgkmcnt(4)\n\t" \
  "v_smfmac_f32_16x16x64_bf16 v[12:15], v[4:7], v[208:215], v68\n\t" \
  "ds_read_b128 v[60:63], v70 offset:32768\n\t" \
  "s_waitcnt lgkmcnt(4)\n\t" \
  "v_smfmac_f32_16x16x64_bf16 v[8:11], v[4:7], v[16:23], v68\n\t" \
  "ds_read_b128 v[64:67], v70 offset:33792\n\t" \
  "s_waitcnt lgkmcnt(4)\n\t" \
  "v_smfmac_f32_16x16x64_bf16 v[8:11], v[32:35], v[192:199], v68\n\t" \
  "ds_read_b128 v[36:39], v73 offset:4096\n\t" \
  "v_smfmac_f32_16x16x64_bf16 v[12:15], v[32:35], v[216:223], v68\n\t" \
  "ds_read_b128 v[40:43], v73 offset:6144\n\t" \
  "s_waitcnt lgkmcnt(4)\n\t" \
  "v_smfmac_f32_16x16x64_bf16 v[52:55], v[4:7], v[24:31], v68\n\t" \
  "ds_read_b128 v[44:47], v70 offset:34816\n\t" \
  "v_cvt_pk_bf16_f32 v8, v8, v9\n\t" \
  "v_smfmac_f32_16x16x64_bf16 v[52:55], v[32:35], v[224:231], v68\n\t" \
  "ds_read_b128 v[48:51], v70 offset:35840\n\t" \
  "v_cvt_pk_bf16_f32 v9, v10, v11\n\t" \
  "v_smfmac_f32_16x16x64_bf16 v[56:59], v[4:7], v[240:247], v68\n\t" \
  "v_cvt_pk_bf16_f32 v10, v12, v13\n\t" \
  "s_waitcnt lgkmcnt(4)\n\t" \
  "v_smfmac_f32_16x16x64_bf16 v[56:59], v[32:35], v[60:67], v68\n\t" \
  "v_cvt_pk_bf16_f32 v11, v14, v15\n\t" \
  "s_waitcnt lgkmcnt(3)\n\t" \
  "v_smfmac_f32_16x16x64_bf16 v[76:79], v[36:39], v[248:255], v68\n\t" \
  "buffer_store_dwordx4 v[8:11], v71, s[28:31], s54 offen sc1\n\t" \
  CH_DP11 \
  "v_cvt_pk_bf16_f32 v52, v52, v53\n\t" \
  "s_waitcnt lgkmcnt(0)\n\t" \
  "v_smfmac_f32_16x16x64_bf16 v[76:79], v[40:43], v[44:51], v68\n\t" \
  "v_cvt_pk_bf16_f32 v53, v54, v55\n\t" \
  "v_cvt_pk_bf16_f32 v54, v56, v57\n\t" \
  "v_cvt_pk_bf16_f32 v55, v58, v59\n\t" \
  "buffer_store_dwordx4 v[52:55], v71, s[28:31], s75 offen sc1\n\t" \
  "s_nop 2\n\t" \
  "v_add_f32 v76, v76, v78\n\t" \
  "v_add_f32 v77, v77, v79\n\t" \
  "v_cvt_pk_bf16_f32 v76, v76, v77\n\t" \
  "buffer_store_dword v76, v72, s[28:31], s55 offen sc1\n\t" \
  CH_DP12 \
  "s_memrealtime s[70:71]\n\t" \
  "s_waitcnt vmcnt(3)\n\t" \
  "v_readfirstlane_b32 s65, v75\n\t" \
  "s_barrier\n\t" \
  "s_waitcnt lgkmcnt(0)\n\t" \
  "s_not_b32 s65, s65\n\t" \
  "s_lshl_b32 s61, s70, 8\n\t" \
  "s_andn2_b32 s64, s65, 0xff\n\t" \
  "s_sub_u32 s64, s64, s61\n\t" \
  "s_ashr_i32 s64, s64, 8\n\t" \
  "s_cmp_eq_u32 s65, 0\n\t" \
  "s_cselect_b32 s64, " CH_DLF ", s64\n\t" \
  "s_min_i32 s64, s64, s34\n\t" \
  "s_max_i32 s64, s64, " CH_DLL1 "\n\t" \
  "s_add_u32 s64, s64, s70\n\t" \
  CH_ST2 \
  "s_add_u32 s55, s41, s53\n\t" \
  "s_mov_b32 s74, 0\n\t" \
  "v_and_b32 v37, 0x30, v69\n\t" \
  "v_add_u32 v37, s52, v37\n\t" \
  "s_mov_b32 exec_lo, 0\n\t" \
  "v_mov_b32 v37, s85\n\t" \
  "s_mov_b32 exec_lo, -1\n\t" \
  "s_cmp_lt_i32 s41, 0\n\t" \
  "s_cbranch_scc1 LIDLE_%=\n\t" \
  "s_movk_i32 s60, 8192\n\t" \
  "RWT3_%=:\n\t" \
  "s_memrealtime s[72:73]\n\t" \
  "s_sub_u32 s60, s60, 1\n\t" \
  "s_cselect_b32 s61, 0, -1\n\t" \
  "s_waitcnt lgkmcnt(0)\n\t" \
  "s_sub_i32 s73, s72, s64\n\t" \
  "s_and_b32 s73, s73, s61\n\t" \
  "s_cmp_lt_i32 s73, 0\n\t" \
  "s_cbranch_scc1 RWT3_%=\n\t" \
  "RRETRY3_%=:\n\t" \
  CH_DP13 \
  "buffer_load_dwordx4 v[0:3], v69, s[28:31], s55 offen offset:0 sc1\n\t" \
  "buffer_load_dwordx4 v[4:7], v69, s[28:31], s55 offen offset:1024 sc1\n\t" \
  "buffer_load_dwordx4 v[8:11], v69, s[28:31], s55 offen offset:2048 sc1\n\t" \
  "buffer_load_dwordx4 v[12:15], v69, s[28:31], s55 offen offset:3072 sc1\n\t" \
  "buffer_load_dword v39, v37, s[28:31], 0 offen sc1\n\t" \
  "v_lshrrev_b32 v20, 2, v69\n\t" \
  "v_and_b32 v20, 16, v20\n\t" \
  "v_mov_b32 v21, 0x3f80\n\t" \
  "v_lshlrev_b32 v21, v20, v21\n\t" \
  "v_bfe_u32 v20, v69, 4, 2\n\t" \
  "v_cmp_eq_u32 vcc, 0, v20\n\t" \
  "s_nop 1\n\t" \
  "v_cndmask_b32 v32, 0, v21, vcc\n\t" \
  "v_cmp_eq_u32 vcc, 1, v20\n\t" \
  "s_nop 1\n\t" \
  "v_cndmask_b32 v33, 0, v21, vcc\n\t" \
  "v_cmp_eq_u32 vcc, 2, v20\n\t" \
  "s_nop 1\n\t" \
  "v_cndmask_b32 v34, 0, v21, vcc\n\t" \
  "v_cmp_eq_u32 vcc, 3, v20\n\t" \
  "s_nop 1\n\t" \
  "v_cndmask_b32 v35, 0, v21, vcc\n\t" \
  "v_bfe_u32 v38, v69, 7, 1\n\t" \
  "v_bfe_u32 v20, v69, 9, 1\n\t" \
  "v_lshl_add_u32 v38, v20, 1, v38\n\t" \
  "v_mul_u32_u24 v38, 1152, v38\n\t" \
  "v_and_b32 v20, 0x60, v69\n\t" \
  "v_add_u32 v38, v38, v20\n\t" \
  "v_bfe_u32 v20, v69, 8, 1\n\t" \
  "v_lshl_add_u32 v38, v20, 4, v38\n\t" \
  "v_bfe_u32 v20, v69, 4, 1\n\t" \
  "v_lshl_add_u32 v38, v20, 3, v38\n\t" \
  "s_add_u32 s54, s37, s53\n\t" \
  "v_mov_b32 v44, 0\n\t" \
  CH_DP17 \
  "s_waitcnt vmcnt(4)\n\t" \
  CH_DP14 \
  "v_mfma_f32_16x16x32_bf16 v[40:43], v[32:35], v[0:3], 0\n\t" \
  "v_max3_u32 v44, v44, v0, v1\n\t" \
  "v_max3_u32 v44, v44, v2, v3\n\t" \
  "s_waitcnt vmcnt(3)\n\t" \
  "v_mfma_f32_16x16x32_bf16 v[40:43], v[32:35], v[4:7], v[40:43]\n\t" \
  "v_max3_u32 v44, v44, v4, v5\n\t" \
  "v_max3_u32 v44, v44, v6, v7\n\t" \
  "s_waitcnt vmcnt(2)\n\t" \
  "v_mfma_f32_16x16x32_bf16 v[40:43], v[32:35], v[8:11], v[40:43]\n\t" \
  "v_max3_u32 v44, v44, v8, v9\n\t" \
  "v_max3_u32 v44, v44, v10, v11\n\t" \
  "s_waitcnt vmcnt(1)\n\t" \
  CH_DP15 \
  "v_mfma_f32_16x16x32_bf16 v[40:43], v[32:35], v[12:15], v[40:43]\n\t" \
  "v_max3_u32 v44, v44, v12, v13\n\t" \
  "v_max3_u32 v44, v44, v14, v15\n\t" \
  "v_cmp_eq_u32 vcc, -1, v44\n\t" \
  "s_cbranch_vccnz RFAIL3_%=\n\t" \
  "s_nop 3\n\t" \
  "v_add_f32_dpp v40, v40, v40 row_ror:8 row_mask:0xf bank_mask:0xf\n\t" \
  "v_add_f32_dpp v41, v41, v41 row_ror:8 row_mask:0xf bank_mask:0xf\n\t" \
  "v_add_f32_dpp v42, v42, v42 row_ror:8 row_mask:0xf bank_mask:0xf\n\t" \
  "v_add_f32_dpp v43, v43, v43 row_ror:8 row_mask:0xf bank_mask:0xf\n\t" \
  "v_cvt_pk_bf16_f32 v40, v40, v41\n\t" \
  "v_cvt_pk_bf16_f32 v41, v42, v43\n\t" \
  "buffer_store_dwordx2 v[40:41], v38, s[28:31], s54 offen sc1\n\t" \
  "RDONE3_%=:\n\t" \
  CH_DP16 \
  CH_ST4 \
  "s_barrier\n\t" \
  CH_ST3 \
  "s_waitcnt vmcnt(1)\n\t" \
  "v_cmp_eq_u32 vcc, 0, v39\n\t" \
  CH_TRC \
  "s_add_u32 s48, s48, 1\n\t" \
  "s_mov_b32 s49, s53\n\t" \
  "s_add_u32 s53, s53, " CH_SLOT "\n\t" \
  "s_add_u32 s60, s53, " CH_SKIP "\n\t" \
  "s_lshr_b32 s61, s96, 1\n\t" \
  "s_add_u32 s86, s86, 1\n\t" \
  "s_cmp_eq_u32 s86, 3\n\t" \
  "s_cselect_b32 s53, s60, s53\n\t" \
  "s_cselect_b32 s96, s61, s96\n\t" \
  "s_cselect_b32 s86, 0, s86\n\t" \
  "s_andn2_b32 s53, s53, 0x2000\n\t" \
  "s_and_b32 s60, s96, 1\n\t" \
  "s_lshl_b32 s60, s60, 13\n\t" \
  "s_or_b32 s53, s53, s60\n\t" \
  "s_mov_b32 s68, 0\n\t" \
  "s_add_u32 s82, s48, 0\n\t" \
  CH_SEGC \
  "s_lshr_b64 s[60:61], s[90:91], s82\n\t" \
  "s_and_b32 s60, s60, 1\n\t" \
  "s_lshl_b32 s60, s60, 13\n\t" \
  "s_add_u32 s82, s82, " CH_SG0 "\n\t" \
  "s_lshl_b32 s82, s82, 21\n\t" \
  "s_or_b32 s82, s82, s60\n\t" \
  "s_add_u32 s50, s82, s38\n\t" \
  "s_add_u32 s69, s82, s39\n\t" \
  "s_add_u32 s82, s48, 1\n\t" \
  CH_SEGC \
  "s_lshr_b64 s[60:61], s[90:91], s82\n\t" \
  "s_and_b32 s60, s60, 1\n\t" \
  "s_lshl_b32 s60, s60, 13\n\t" \
  "s_add_u32 s82, s82, " CH_SG0 "\n\t" \
  "s_lshl_b32 s82, s82, 21\n\t" \
  "s_or_b32 s82, s82, s60\n\t" \
  "s_add_u32 s52, s82, s43\n\t" \
  "s_add_u32 s82, s48, " CH_ARMD "\n\t" \
  CH_SEGC \
  "s_lshr_b64 s[60:61], s[90:91], s82\n\t" \
  "s_and_b32 s60, s60, 1\n\t" \
  "s_lshl_b32 s60, s60, 13\n\t" \
  "s_add_u32 s82, s82, " CH_SG0 "\n\t" \
  "s_lshl_b32 s82, s82, 21\n\t" \
  "s_or_b32 s82, s82, s60\n\t" \
  "s_xor_b32 s85, s82, s45\n\t" \
  "s_cmp_lt_i32 s48, " CH_ARMS "\n\t" \
  "s_cselect_b32 s85, s52, s85\n\t" \
  "s_cmp_eq_u32 s34, 0\n\t" \
  "s_cbranch_scc1 LLOAD_%=\n\t" \
  "LTK_%=:\n\t" \
  CH_TOKCHK \
  "s_mov_b32 s61, 0\n\t" \
  "LTW_%=:\n\t" \
  "buffer_load_dword v1, v37, s[28:31], 0 offen sc1\n\t" \
  "s_waitcnt vmcnt(0)\n\t" \
  "v_cmp_eq_u32 vcc, 0, v1\n\t" \
  "s_cbranch_vccz LG3_%=\n\t" \
  "s_add_u32 s61, s61, 1\n\t" \
  "s_sleep 2\n\t" \
  "s_cmp_gt_u32 s61, s84\n\t" \
  "s_cbranch_scc1 LTG_%=\n\t" \
  "s_and_b32 s60, s61, 1023\n\t" \
  "s_cmp_lg_u32 s60, 0\n\t" \
  "s_cbranch_scc1 LTW_%=\n\t" \
  "buffer_load_dword v46, off, s[28:31], s92 sc1\n\t" \
  "s_waitcnt vmcnt(0)\n\t" \
  "v_readfirstlane_b32 s60, v46\n\t" \
  "s_nop 0\n\t" \
  "s_cmp_eq_u32 s60, 0\n\t" \
  "s_cbranch_scc1 LTW_%=\n\t" \
  "LTG_%=:\n\t" \
  "s_mov_b32 s84, 0\n\t" \
  "s_lshl_b32 s60, s36, 4\n\t" \
  "s_or_b32 s60, s60, s48\n\t" \
  "s_or_b32 s60, s60, 268435456\n\t" \
  "v_mov_b32 v46, s60\n\t" \
  "v_mov_b32 v47, 0\n\t" \
  "s_mov_b64 exec, 1\n\t" \
  "buffer_atomic_cmpswap v[46:47], off, s[28:31], s92 sc1\n\t" \
  "s_mov_b64 exec, -1\n\t" \
  "s_branch LG3_%=\n\t" \
  "LIDLE_%=:\n\t" \
  "s_add_u32 s64, s64, 8\n\t" \
  "s_movk_i32 s60, 8192\n\t" \
  "IWT_%=:\n\t" \
  "s_memrealtime s[72:73]\n\t" \
  "s_sub_u32 s60, s60, 1\n\t" \
  "s_cselect_b32 s61, 0, -1\n\t" \
  "s_waitcnt lgkmcnt(0)\n\t" \
  "s_sub_i32 s73, s72, s64\n\t" \
  "s_and_b32 s73, s73, s61\n\t" \
  "s_cmp_lt_i32 s73, 0\n\t" \
  "s_cbranch_scc1 IWT_%=\n\t" \
  "buffer_load_dword v39, v37, s[28:31], 0 offen sc1\n\t" \
  "buffer_store_dword v37, off, s[28:31], s41\n\t" \
  "s_branch RDONE3_%=\n\t" \
  "RFAIL3_%=:\n\t" \
  "s_add_u32 s74, s74, 1\n\t" \
  "s_sleep " CH_RSL "\n\t" \
  "s_waitcnt vmcnt(0)\n\t" \
  "s_cmp_gt_u32 s74, s84\n\t" \
  "s_cbranch_scc1 RG3_%=\n\t" \
  "s_and_b32 s60, s74, 1023\n\t" \
  "s_cmp_lg_u32 s60, 0\n\t" \
  "s_cbranch_scc1 RRETRY3_%=\n\t" \
  "buffer_load_dword v46, off, s[28:31], s92 sc1\n\t" \
  "s_waitcnt vmcnt(0)\n\t" \
  "v_readfirstlane_b32 s60, v46\n\t" \
  "s_nop 0\n\t" \
  "s_cmp_eq_u32 s60, 0\n\t" \
  "s_cbranch_scc1 RRETRY3_%=\n\t" \
  "RG3_%=:\n\t" \
  "s_mov_b32 s84, 0\n\t" \
  "s_lshl_b32 s60, s36, 4\n\t" \
  "s_or_b32 s60, s60, s48\n\t" \
  "s_or_b32 s60, s60, 536870912\n\t" \
  "v_mov_b32 v46, s60\n\t" \
  "v_mov_b32 v47, 0\n\t" \
  "s_mov_b64 exec, 1\n\t" \
  "buffer_atomic_cmpswap v[46:47], off, s[28:31], s92 sc1\n\t" \
  "s_mov_b64 exec, -1\n\t" \
  "s_branch RDONE3_%=\n\t" \
  "LFAIL3_%=:\n\t" \
  "s_add_u32 s68, s68, 1\n\t" \
  "s_waitcnt lgkmcnt(0)\n\t" \
  "s_cmp_gt_u32 s68, s83\n\t" \
  "s_cbranch_scc1 LGV3_%=\n\t" \
  "s_barrier\n\t" \
  "s_waitcnt vmcnt(8)\n\t" \
  "s_branch LG3_%=\n\t" \
  "LGV3_%=:\n\t" \
  "s_mov_b32 s83, 0\n\t" \
  "s_lshl_b32 s60, s36, 4\n\t" \
  "s_or_b32 s60, s60, s48\n\t" \
  "s_or_b32 s60, s60, 805306368\n\t" \
  "v_mov_b32 v46, s60\n\t" \
  "v_mov_b32 v47, 0\n\t" \
  "s_mov_b64 exec, 1\n\t" \
  "buffer_atomic_cmpswap v[46:47], off, s[28:31], s92 sc1\n\t" \
  "s_mov_b64 exec, -1\n\t" \
  "s_branch LGO3_%=\n\t" \
  "LLAST3_%=:\n\t" \
  "s_waitcnt vmcnt(0) lgkmcnt(0)\n\t" \
  "s_barrier\n\t" \
  "LEND_%=:\n\t" \
  ""
typedef unsigned v32u __attribute__((ext_vector_type(32)));
typedef unsigned v16u __attribute__((ext_vector_type(16)));
#define CH_WCLOB "a0", "a1", "a2", "a3", "a4", "a5", "a6", "a7", "a8", "a9", "a10", "a11", "a12", "a13", "a14", "a15", "a16", "a17", "a18", "a19", "a20", "a21", "a22", "a23", "a24", "a25", "a26", "a27", "a28", "a29", "a30", "a31", "a32", "a33", "a34", "a35", "a36", "a37", "a38", "a39", "a40", "a41", "a42", "a43", "a44", "a45", "a46", "a47", "a48", "a49", "a50", "a51", "a52", "a53", "a54", "a55", "a56", "a57", "a58", "a59", "a60", "a61", "a62", "a63", "a64", "a65", "a66", "a67", "a68", "a69", "a70", "a71", "a72", "a73", "a74", "a75", "a76", "a77", "a78", "a79", "a80", "a81", "a82", "a83", "a84", "a85", "a86", "a87", "a88", "a89", "a90", "a91", "a92", "a93", "a94", "a95", "a96", "a97", "a98", "a99", "a100", "a101", "a102", "a103", "a104", "a105", "a106", "a107", "a108", "a109", "a110", "a111", "a112", "a113", "a114", "a115", "a116", "a117", "a118", "a119", "a120", "a121", "a122", "a123", "a124", "a125", "a126", "a127", "a128", "a129", "a130", "a131", "a132", "a133", "a134", "a135", "a136", "a137", "a138", "a139", "a140", "a141", "a142", "a143", "a144", "a145", "a146", "a147", "a148", "a149", "a150", "a151", "a152", "a153", "a154", "a155", "a156", "a157", "a158", "a159", "a160", "a161", "a162", "a163", "a164", "a165", "a166", "a167", "a168", "a169", "a170", "a171", "a172", "a173", "a174", "a175", "a176", "a177", "a178", "a179", "a180", "a181", "a182", "a183", "a184", "a185", "a186", "a187", "a188", "a189", "a190", "a191", "a192", "a193", "a194", "a195", "a196", "a197", "a198", "a199", "a200", "a201", "a202", "a203", "a204", "a205", "a206", "a207", "a208", "a209", "a210", "a211", "a212", "a213", "a214", "a215", "a216", "a217", "a218", "a219", "a220", "a221", "a222", "a223", "a224", "a225", "a226", "a227", "a228", "a229", "a230", "a231", "a232", "a233", "a234", "a235", "a236", "a237", "a238", "a239", "a240", "a241", "a242", "a243", "a244", "a245", "a246", "a247", "a248", "a249", "a250", "a251", "a252", "a253", "a254", "a255", "v80", "v81", "v82", "v83", "v84", "v85", "v86", "v87", "v88", "v89", "v90", "v91", "v92", "v93", "v94", "v95", "v96", "v97", "v98", "v99", "v100", "v101", "v102", "v103", "v104", "v105", "v106", "v107", "v108", "v109", "v110", "v111", "v112", "v113", "v114", "v115", "v116", "v117", "v118", "v119", "v120", "v121", "v122", "v123", "v124", "v125", "v126", "v127", "v128", "v129", "v130", "v131", "v132", "v133", "v134", "v135", "v136", "v137", "v138", "v139", "v140", "v141", "v142", "v143", "v144", "v145", "v146", "v147", "v148", "v149", "v150", "v151", "v152", "v153", "v154", "v155", "v156", "v157", "v158", "v159", "v160", "v161", "v162", "v163", "v164", "v165", "v166", "v167", "v168", "v169", "v170", "v171", "v172", "v173", "v174", "v175", "v176", "v177", "v178", "v179", "v180", "v181", "v182", "v183", "v184", "v185", "v186", "v187", "v188", "v189", "v190", "v191", "v192", "v193", "v194", "v195", "v196", "v197", "v198", "v199", "v200", "v201", "v202", "v203", "v204", "v205", "v206", "v207", "v208", "v209", "v210", "v211", "v212", "v213", "v214", "v215", "v216", "v217", "v218", "v219", "v220", "v221", "v222", "v223", "v224", "v225", "v226", "v227", "v228", "v229", "v230", "v231", "v232", "v233", "v234", "v235", "v236", "v237", "v238", "v239", "v240", "v241", "v242", "v243", "v244", "v245", "v246", "v247", "v248", "v249", "v250", "v251", "v252", "v253", "v254", "v255"
#define CH_H(b) (8 * ((b) & 3) + ((b) >> 3))
#define CH_L(b) (((b) >> 2) & 1)
__device__ __forceinline__ void start_conv(char* smem, int d0);
__device__ __forceinline__ void chain_wg(char* smem, v2u cv) {
  if (CH_L(blockIdx.x)) { const int h_ = CH_H(blockIdx.x); start_conv(smem, 4 * h_); }
  const MP p = getp();
  const int b = blockIdx.x, h = CH_H(b), L = CH_L(b);
  const int w = __builtin_amdgcn_readfirstlane(threadIdx.x >> 6);
  unsigned lp16 = (threadIdx.x & 63u) * 16u;
  rsrc_t X = mkr(p.xb, XB_BYTES);
  if ((unsigned)(size_t)(__attribute__((address_space(3))) char*)smem != 0u) return;
  const unsigned wsz = DM * HD * 2u - (unsigned)(h * DH + 32 * w) * 2u;
  const rsrc_t RQ = mkr(p.Wq + h * DH + 32 * w, wsz), RK = mkr(p.Wk + h * DH + 32 * w, wsz), RV = mkr(p.Wv + h * DH + 32 * w, wsz), RO = mkr(p.Wo + (long)h * DH * DM, DH * DM * 2u);
  asm volatile("" :: "v"(cv));
  unsigned xcc; asm volatile("s_getreg_b32 %0, hwreg(HW_REG_XCC_ID)" : "=s"(xcc)); xcc &= 3u;
  unsigned d96 = (unsigned)die_mask(X, 0u, L, (int)(lp16 >> 4));
  const unsigned long long sm = die_mask(X, SG0, L, (int)(lp16 >> 4));
  const unsigned c43 = P_FA * PAIR + (unsigned)h * 128u + 32u;
  const unsigned h128 = (unsigned)h * 128u, uw = (unsigned)w, t = uw >> 1, T = uw & 1u, NONE = 0xF0000000u;
  const unsigned u0 = w == 3 ? (unsigned)h >> 2 : (unsigned)h + (w == 2 ? 32u : 0u);
  const bool idle = w == 3 && (h & 3);
  const unsigned c37 = idle ? NONE : u0 * 128u + (w == 3 ? AGT : AGO);
  const unsigned c41 = idle ? NONE : (w == 3 ? 524288u + (u0 >> 1) * 16384u + (u0 & 1u) * 4096u : (u0 & 7u) * 16384u + ((u0 >> 3) & 1u) * 4096u + (u0 >> 4) * 131072u);
  const unsigned c42 = AGO + xcc * 16384u, c44 = AGT + xcc * 1152u;
  const unsigned c46 = h == 30 ? AGT + 1024u : NONE;
  const unsigned c38 = (unsigned)(3 * h + 1) * PAIR + T * 4096u + t * 2048u, c39 = (P_KV + (unsigned)(h >> 1)) * PAIR + (unsigned)(h & 1) * 4096u;
  const unsigned c45 = P_FB * PAIR + 4096u + (unsigned)(L * 16 + (h & 15)) * 128u + (L ? 0u : 8192u);
  unsigned d48 = 0u, d49 = (d96 & 1u) * 8192u, d53 = SLOT + d49, d86 = 1u, d65 = rtc();
  unsigned mn = 120u;
  v4u stt;
  asm volatile("s_waitcnt vmcnt(0) lgkmcnt(0)\n\t" CH_ASM "LCHEND:"
               : "+{s48}"(d48), "+{s49}"(d49), "+{s53}"(d53), "+{s65}"(d65), "+{s81}"(mn), "+{s86}"(d86), "+{s96}"(d96), "=&{s[76:79]}"(stt), "+{v69}"(lp16), "+{s[28:31]}"(X)
               : [Wq] "s"(RQ), [Wk] "s"(RK), [Wv] "s"(RV), [Wo] "s"(RO), "{s33}"(HT_OFF + 8192u + 256u + (unsigned)b * 4u), "{s36}"(h128), "{s37}"(c37), "{s40}"(w), "{s41}"(c41), "{s42}"(c42), "{s44}"(c44), "{s46}"(c46), "{s92}"(TMO_OFF), "{s38}"(c38), "{s39}"(c39), "{s43}"(c43), "{s45}"(c45), "{s[90:91]}"(sm), [dl1l] "i"(DL1 - LATE), [armd] "i"(3 - ARMK), [arms] "i"(ARMK - 3),
                 [cap] "i"(SPIN_CAP)
               : "v0", "v1", "v2", "v3", "v4", "v5", "v6", "v7", "v8", "v9", "v10", "v11", "v12", "v13", "v14", "v15", "v16", "v17", "v18", "v19",
                 "v20", "v21", "v22", "v23", "v24", "v25", "v26", "v27", "v28", "v29", "v30", "v31", "v32", "v33", "v34", "v35", "v36", "v37", "v38", "v39",
                 "v40", "v41", "v42", "v43", "v44", "v45", "v46", "v47", "v48", "v49", "v50", "v51", "v52", "v53", "v54", "v55", "v56", "v57", "v58", "v59",
                 "v60", "v61", "v62", "v63", "v64", "v65", "v66", "v67", "v68", "v70", "v71", "v72", "v73", "v74", "v75", "v76", "v77", "v78", "v79",
                 CH_WCLOB, "s34", "s35", "s47", "s87", "s50", "s51", "s52", "s69", "s82", "s85", "s54", "s75", "s80", "s55", "s56", "s57", "s58", "s59", "s60", "s61", "s62", "s63", "s64", "s66", "s67", "s68", "s70", "s71", "s72", "s73", "s74", "s88", "s89",
                 "s83", "s84", "s97", "s98", "s99", "s93", "s94", "s95", "m0", "vcc", "scc", "memory");
}
#define XQ 0u
#define XQL 0u
#define XA 0u
#define XAL 0u
#define XRL 0u
#define QLEAD 4
#define TK_QDL
#define TK_ADL
#define PSR 8
#define RDLY 40
#define TK_ASL
#define TK_QSL
#define ROT 1
#define PACE 1
#define TK_HAVE "s_cmp_eq_u32 vcc_lo, 0\n\t"
#define TK_PACE "s_cmp_gt_i32 s42, -60\n\ts_cbranch_scc1 LPACE_%=\n\t"
#define TK_QDL2
#define TK_QNP
#define PSK 2
#define PSH 2
#define TK_CAP "%[cap]"
#define TK_SG0 CH_S(SG0)
#define TK_PSK CH_S(PSK)
#define TK_PSH CH_S(PSH)
#define TK_PA0
#define TK_PA1
#define TK_PA2
#define TK_PQ0
#define TK_PQ1
#define TK_PQ2
#define TT(s, i)
#define TK_AC(n)
#define TK_ACE
#define TK_AC0 TK_AC(212)
#define TK_AC1 TK_AC(213)
#define TK_AC2 TK_AC(214)
#define TK_AC3 TK_AC(215)
#define TK_AC4 TK_AC(216)
#define TK_AC5 TK_AC(217)
#define TK_AC6 TK_AC(218)
#define TK_AC7 TK_AC(219)
#define TK_AC8 TK_AC(220)
#define AP_ASM \
  "v_lshrrev_b32 v40, 8, v0\n\t" \
  "v_mul_u32_u24 v40, 4608, v40\n\t" \
  "v_and_b32 v41, 0xf0, v0\n\t" \
  "v_lshrrev_b32 v42, 3, v41\n\t" \
  "v_add3_u32 v41, v41, v40, s64\n\t" \
  "v_add3_u32 v42, v42, v40, s65\n\t" \
  "s_mov_b32 s62, 0x05040100\n\t" \
  "s_mov_b32 s63, 0x07060302\n\t" \
  "s_xor_b32 s60, s66, 0\n\t" \
  "s_add_u32 s60, s60, 0\n\t" \
  "s_add_u32 s61, s60, 18432\n\t" \
  "buffer_load_dwordx4 v[48:51], v41, %[W], s60 offen offset:0\n\t" \
  "buffer_load_dwordx4 v[52:55], v41, %[W], s60 offen offset:1152\n\t" \
  "buffer_load_dwordx4 v[56:59], v41, %[W], s60 offen offset:2304\n\t" \
  "buffer_load_dwordx4 v[60:63], v41, %[W], s60 offen offset:3456\n\t" \
  "buffer_load_dwordx4 v[64:67], v41, %[W], s61 offen offset:0\n\t" \
  "buffer_load_dwordx4 v[68:71], v41, %[W], s61 offen offset:1152\n\t" \
  "buffer_load_dwordx4 v[72:75], v41, %[W], s61 offen offset:2304\n\t" \
  "buffer_load_dwordx4 v[76:79], v41, %[W], s61 offen offset:3456\n\t" \
  "s_xor_b32 s60, s66, 0\n\t" \
  "s_add_u32 s60, s60, 36864\n\t" \
  "s_add_u32 s61, s60, 18432\n\t" \
  "buffer_load_dwordx4 v[80:83], v41, %[W], s60 offen offset:0\n\t" \
  "buffer_load_dwordx4 v[84:87], v41, %[W], s60 offen offset:1152\n\t" \
  "buffer_load_dwordx4 v[88:91], v41, %[W], s60 offen offset:2304\n\t" \
  "buffer_load_dwordx4 v[92:95], v41, %[W], s60 offen offset:3456\n\t" \
  "buffer_load_dwordx4 v[96:99], v41, %[W], s61 offen offset:0\n\t" \
  "buffer_load_dwordx4 v[100:103], v41, %[W], s61 offen offset:1152\n\t" \
  "buffer_load_dwordx4 v[104:107], v41, %[W], s61 offen offset:2304\n\t" \
  "buffer_load_dwordx4 v[108:111], v41, %[W], s61 offen offset:3456\n\t" \
  "s_xor_b32 s60, s66, 0\n\t" \
  "s_add_u32 s60, s60, 73728\n\t" \
  "s_add_u32 s61, s60, 18432\n\t" \
  "buffer_load_dwordx4 v[112:115], v41, %[W], s60 offen offset:0\n\t" \
  "buffer_load_dwordx4 v[116:119], v41, %[W], s60 offen offset:1152\n\t" \
  "buffer_load_dwordx4 v[120:123], v41, %[W], s60 offen offset:2304\n\t" \
  "buffer_load_dwordx4 v[124:127], v41, %[W], s60 offen offset:3456\n\t" \
  "buffer_load_dwordx4 v[128:131], v41, %[W], s61 offen offset:0\n\t" \
  "buffer_load_dwordx4 v[132:135], v41, %[W], s61 offen offset:1152\n\t" \
  "buffer_load_dwordx4 v[136:139], v41, %[W], s61 offen offset:2304\n\t" \
  "buffer_load_dwordx4 v[140:143], v41, %[W], s61 offen offset:3456\n\t" \
  "s_xor_b32 s60, s66, 0\n\t" \
  "s_add_u32 s60, s60, 110592\n\t" \
  "s_add_u32 s61, s60, 18432\n\t" \
  "buffer_load_dwordx4 v[144:147], v41, %[W], s60 offen offset:0\n\t" \
  "buffer_load_dwordx4 v[148:151], v41, %[W], s60 offen offset:1152\n\t" \
  "buffer_load_dwordx4 v[152:155], v41, %[W], s60 offen offset:2304\n\t" \
  "buffer_load_dwordx4 v[156:159], v41, %[W], s60 offen offset:3456\n\t" \
  "buffer_load_dwordx4 v[160:163], v41, %[W], s61 offen offset:0\n\t" \
  "buffer_load_dwordx4 v[164:167], v41, %[W], s61 offen offset:1152\n\t" \
  "buffer_load_dwordx4 v[168:171], v41, %[W], s61 offen offset:2304\n\t" \
  "buffer_load_dwordx4 v[172:175], v41, %[W], s61 offen offset:3456\n\t" \
  "s_xor_b32 s60, s66, 147456\n\t" \
  "s_add_u32 s60, s60, 0\n\t" \
  "s_add_u32 s61, s60, 18432\n\t" \
  "buffer_load_dwordx4 v[176:179], v41, %[W], s60 offen offset:0\n\t" \
  "buffer_load_dwordx4 v[180:183], v41, %[W], s60 offen offset:1152\n\t" \
  "buffer_load_dwordx4 v[184:187], v41, %[W], s60 offen offset:2304\n\t" \
  "buffer_load_dwordx4 v[188:191], v41, %[W], s60 offen offset:3456\n\t" \
  "buffer_load_dwordx4 v[192:195], v41, %[W], s61 offen offset:0\n\t" \
  "buffer_load_dwordx4 v[196:199], v41, %[W], s61 offen offset:1152\n\t" \
  "buffer_load_dwordx4 v[200:203], v41, %[W], s61 offen offset:2304\n\t" \
  "buffer_load_dwordx4 v[204:207], v41, %[W], s61 offen offset:3456\n\t" \
  "s_waitcnt vmcnt(38)\n\t" \
  "v_perm_b32 v44, v52, v48, s62\n\t" \
  "s_waitcnt vmcnt(36)\n\t" \
  "v_perm_b32 v45, v60, v56, s62\n\t" \
  "s_waitcnt vmcnt(34)\n\t" \
  "v_perm_b32 v46, v68, v64, s62\n\t" \
  "s_waitcnt vmcnt(32)\n\t" \
  "v_perm_b32 v47, v76, v72, s62\n\t" \
  "v_accvgpr_write_b32 a0, v44\n\t" \
  "v_accvgpr_write_b32 a1, v45\n\t" \
  "v_accvgpr_write_b32 a2, v46\n\t" \
  "v_accvgpr_write_b32 a3, v47\n\t" \
  "v_perm_b32 v44, v52, v48, s63\n\t" \
  "v_perm_b32 v45, v60, v56, s63\n\t" \
  "v_perm_b32 v46, v68, v64, s63\n\t" \
  "v_perm_b32 v47, v76, v72, s63\n\t" \
  "v_accvgpr_write_b32 a4, v44\n\t" \
  "v_accvgpr_write_b32 a5, v45\n\t" \
  "v_accvgpr_write_b32 a6, v46\n\t" \
  "v_accvgpr_write_b32 a7, v47\n\t" \
  "v_perm_b32 v44, v53, v49, s62\n\t" \
  "v_perm_b32 v45, v61, v57, s62\n\t" \
  "v_perm_b32 v46, v69, v65, s62\n\t" \
  "v_perm_b32 v47, v77, v73, s62\n\t" \
  "v_accvgpr_write_b32 a8, v44\n\t" \
  "v_accvgpr_write_b32 a9, v45\n\t" \
  "v_accvgpr_write_b32 a10, v46\n\t" \
  "v_accvgpr_write_b32 a11, v47\n\t" \
  "v_perm_b32 v44, v53, v49, s63\n\t" \
  "v_perm_b32 v45, v61, v57, s63\n\t" \
  "v_perm_b32 v46, v69, v65, s63\n\t" \
  "v_perm_b32 v47, v77, v73, s63\n\t" \
  "v_accvgpr_write_b32 a12, v44\n\t" \
  "v_accvgpr_write_b32 a13, v45\n\t" \
  "v_accvgpr_write_b32 a14, v46\n\t" \
  "v_accvgpr_write_b32 a15, v47\n\t" \
  "v_perm_b32 v44, v54, v50, s62\n\t" \
  "v_perm_b32 v45, v62, v58, s62\n\t" \
  "v_perm_b32 v46, v70, v66, s62\n\t" \
  "v_perm_b32 v47, v78, v74, s62\n\t" \
  "v_accvgpr_write_b32 a16, v44\n\t" \
  "v_accvgpr_write_b32 a17, v45\n\t" \
  "v_accvgpr_write_b32 a18, v46\n\t" \
  "v_accvgpr_write_b32 a19, v47\n\t" \
  "v_perm_b32 v44, v54, v50, s63\n\t" \
  "v_perm_b32 v45, v62, v58, s63\n\t" \
  "v_perm_b32 v46, v70, v66, s63\n\t" \
  "v_perm_b32 v47, v78, v74, s63\n\t" \
  "v_accvgpr_write_b32 a20, v44\n\t" \
  "v_accvgpr_write_b32 a21, v45\n\t" \
  "v_accvgpr_write_b32 a22, v46\n\t" \
  "v_accvgpr_write_b32 a23, v47\n\t" \
  "v_perm_b32 v44, v55, v51, s62\n\t" \
  "v_perm_b32 v45, v63, v59, s62\n\t" \
  "v_perm_b32 v46, v71, v67, s62\n\t" \
  "v_perm_b32 v47, v79, v75, s62\n\t" \
  "v_accvgpr_write_b32 a24, v44\n\t" \
  "v_accvgpr_write_b32 a25, v45\n\t" \
  "v_accvgpr_write_b32 a26, v46\n\t" \
  "v_accvgpr_write_b32 a27, v47\n\t" \
  "v_perm_b32 v44, v55, v51, s63\n\t" \
  "v_perm_b32 v45, v63, v59, s63\n\t" \
  "v_perm_b32 v46, v71, v67, s63\n\t" \
  "v_perm_b32 v47, v79, v75, s63\n\t" \
  "v_accvgpr_write_b32 a28, v44\n\t" \
  "v_accvgpr_write_b32 a29, v45\n\t" \
  "v_accvgpr_write_b32 a30, v46\n\t" \
  "v_accvgpr_write_b32 a31, v47\n\t" \
  "s_xor_b32 s60, s66, 147456\n\t" \
  "s_add_u32 s60, s60, 36864\n\t" \
  "s_add_u32 s61, s60, 18432\n\t" \
  "buffer_load_dwordx4 v[48:51], v41, %[W], s60 offen offset:0\n\t" \
  "buffer_load_dwordx4 v[52:55], v41, %[W], s60 offen offset:1152\n\t" \
  "buffer_load_dwordx4 v[56:59], v41, %[W], s60 offen offset:2304\n\t" \
  "buffer_load_dwordx4 v[60:63], v41, %[W], s60 offen offset:3456\n\t" \
  "buffer_load_dwordx4 v[64:67], v41, %[W], s61 offen offset:0\n\t" \
  "buffer_load_dwordx4 v[68:71], v41, %[W], s61 offen offset:1152\n\t" \
  "buffer_load_dwordx4 v[72:75], v41, %[W], s61 offen offset:2304\n\t" \
  "buffer_load_dwordx4 v[76:79], v41, %[W], s61 offen offset:3456\n\t" \
  "s_waitcnt vmcnt(38)\n\t" \
  "v_perm_b32 v44, v84, v80, s62\n\t" \
  "s_waitcnt vmcnt(36)\n\t" \
  "v_perm_b32 v45, v92, v88, s62\n\t" \
  "s_waitcnt vmcnt(34)\n\t" \
  "v_perm_b32 v46, v100, v96, s62\n\t" \
  "s_waitcnt vmcnt(32)\n\t" \
  "v_perm_b32 v47, v108, v104, s62\n\t" \
  "v_accvgpr_write_b32 a32, v44\n\t" \
  "v_accvgpr_write_b32 a33, v45\n\t" \
  "v_accvgpr_write_b32 a34, v46\n\t" \
  "v_accvgpr_write_b32 a35, v47\n\t" \
  "v_perm_b32 v44, v84, v80, s63\n\t" \
  "v_perm_b32 v45, v92, v88, s63\n\t" \
  "v_perm_b32 v46, v100, v96, s63\n\t" \
  "v_perm_b32 v47, v108, v104, s63\n\t" \
  "v_accvgpr_write_b32 a36, v44\n\t" \
  "v_accvgpr_write_b32 a37, v45\n\t" \
  "v_accvgpr_write_b32 a38, v46\n\t" \
  "v_accvgpr_write_b32 a39, v47\n\t" \
  "v_perm_b32 v44, v85, v81, s62\n\t" \
  "v_perm_b32 v45, v93, v89, s62\n\t" \
  "v_perm_b32 v46, v101, v97, s62\n\t" \
  "v_perm_b32 v47, v109, v105, s62\n\t" \
  "v_accvgpr_write_b32 a40, v44\n\t" \
  "v_accvgpr_write_b32 a41, v45\n\t" \
  "v_accvgpr_write_b32 a42, v46\n\t" \
  "v_accvgpr_write_b32 a43, v47\n\t" \
  "v_perm_b32 v44, v85, v81, s63\n\t" \
  "v_perm_b32 v45, v93, v89, s63\n\t" \
  "v_perm_b32 v46, v101, v97, s63\n\t" \
  "v_perm_b32 v47, v109, v105, s63\n\t" \
  "v_accvgpr_write_b32 a44, v44\n\t" \
  "v_accvgpr_write_b32 a45, v45\n\t" \
  "v_accvgpr_write_b32 a46, v46\n\t" \
  "v_accvgpr_write_b32 a47, v47\n\t" \
  "v_perm_b32 v44, v86, v82, s62\n\t" \
  "v_perm_b32 v45, v94, v90, s62\n\t" \
  "v_perm_b32 v46, v102, v98, s62\n\t" \
  "v_perm_b32 v47, v110, v106, s62\n\t" \
  "v_accvgpr_write_b32 a48, v44\n\t" \
  "v_accvgpr_write_b32 a49, v45\n\t" \
  "v_accvgpr_write_b32 a50, v46\n\t" \
  "v_accvgpr_write_b32 a51, v47\n\t" \
  "v_perm_b32 v44, v86, v82, s63\n\t" \
  "v_perm_b32 v45, v94, v90, s63\n\t" \
  "v_perm_b32 v46, v102, v98, s63\n\t" \
  "v_perm_b32 v47, v110, v106, s63\n\t" \
  "v_accvgpr_write_b32 a52, v44\n\t" \
  "v_accvgpr_write_b32 a53, v45\n\t" \
  "v_accvgpr_write_b32 a54, v46\n\t" \
  "v_accvgpr_write_b32 a55, v47\n\t" \
  "v_perm_b32 v44, v87, v83, s62\n\t" \
  "v_perm_b32 v45, v95, v91, s62\n\t" \
  "v_perm_b32 v46, v103, v99, s62\n\t" \
  "v_perm_b32 v47, v111, v107, s62\n\t" \
  "v_accvgpr_write_b32 a56, v44\n\t" \
  "v_accvgpr_write_b32 a57, v45\n\t" \
  "v_accvgpr_write_b32 a58, v46\n\t" \
  "v_accvgpr_write_b32 a59, v47\n\t" \
  "v_perm_b32 v44, v87, v83, s63\n\t" \
  "v_perm_b32 v45, v95, v91, s63\n\t" \
  "v_perm_b32 v46, v103, v99, s63\n\t" \
  "v_perm_b32 v47, v111, v107, s63\n\t" \
  "v_accvgpr_write_b32 a60, v44\n\t" \
  "v_accvgpr_write_b32 a61, v45\n\t" \
  "v_accvgpr_write_b32 a62, v46\n\t" \
  "v_accvgpr_write_b32 a63, v47\n\t" \
  "s_xor_b32 s60, s66, 147456\n\t" \
  "s_add_u32 s60, s60, 73728\n\t" \
  "s_add_u32 s61, s60, 18432\n\t" \
  "buffer_load_dwordx4 v[80:83], v41, %[W], s60 offen offset:0\n\t" \
  "buffer_load_dwordx4 v[84:87], v41, %[W], s60 offen offset:1152\n\t" \
  "buffer_load_dwordx4 v[88:91], v41, %[W], s60 offen offset:2304\n\t" \
  "buffer_load_dwordx4 v[92:95], v41, %[W], s60 offen offset:3456\n\t" \
  "buffer_load_dwordx4 v[96:99], v41, %[W], s61 offen offset:0\n\t" \
  "buffer_load_dwordx4 v[100:103], v41, %[W], s61 offen offset:1152\n\t" \
  "buffer_load_dwordx4 v[104:107], v41, %[W], s61 offen offset:2304\n\t" \
  "buffer_load_dwordx4 v[108:111], v41, %[W], s61 offen offset:3456\n\t" \
  "s_waitcnt vmcnt(38)\n\t" \
  "v_perm_b32 v44, v116, v112, s62\n\t" \
  "s_waitcnt vmcnt(36)\n\t" \
  "v_perm_b32 v45, v124, v120, s62\n\t" \
  "s_waitcnt vmcnt(34)\n\t" \
  "v_perm_b32 v46, v132, v128, s62\n\t" \
  "s_waitcnt vmcnt(32)\n\t" \
  "v_perm_b32 v47, v140, v136, s62\n\t" \
  "v_accvgpr_write_b32 a64, v44\n\t" \
  "v_accvgpr_write_b32 a65, v45\n\t" \
  "v_accvgpr_write_b32 a66, v46\n\t" \
  "v_accvgpr_write_b32 a67, v47\n\t" \
  "v_perm_b32 v44, v116, v112, s63\n\t" \
  "v_perm_b32 v45, v124, v120, s63\n\t" \
  "v_perm_b32 v46, v132, v128, s63\n\t" \
  "v_perm_b32 v47, v140, v136, s63\n\t" \
  "v_accvgpr_write_b32 a68, v44\n\t" \
  "v_accvgpr_write_b32 a69, v45\n\t" \
  "v_accvgpr_write_b32 a70, v46\n\t" \
  "v_accvgpr_write_b32 a71, v47\n\t" \
  "v_perm_b32 v44, v117, v113, s62\n\t" \
  "v_perm_b32 v45, v125, v121, s62\n\t" \
  "v_perm_b32 v46, v133, v129, s62\n\t" \
  "v_perm_b32 v47, v141, v137, s62\n\t" \
  "v_accvgpr_write_b32 a72, v44\n\t" \
  "v_accvgpr_write_b32 a73, v45\n\t" \
  "v_accvgpr_write_b32 a74, v46\n\t" \
  "v_accvgpr_write_b32 a75, v47\n\t" \
  "v_perm_b32 v44, v117, v113, s63\n\t" \
  "v_perm_b32 v45, v125, v121, s63\n\t" \
  "v_perm_b32 v46, v133, v129, s63\n\t" \
  "v_perm_b32 v47, v141, v137, s63\n\t" \
  "v_accvgpr_write_b32 a76, v44\n\t" \
  "v_accvgpr_write_b32 a77, v45\n\t" \
  "v_accvgpr_write_b32 a78, v46\n\t" \
  "v_accvgpr_write_b32 a79, v47\n\t" \
  "v_perm_b32 v44, v118, v114, s62\n\t" \
  "v_perm_b32 v45, v126, v122, s62\n\t" \
  "v_perm_b32 v46, v134, v130, s62\n\t" \
  "v_perm_b32 v47, v142, v138, s62\n\t" \
  "v_accvgpr_write_b32 a80, v44\n\t" \
  "v_accvgpr_write_b32 a81, v45\n\t" \
  "v_accvgpr_write_b32 a82, v46\n\t" \
  "v_accvgpr_write_b32 a83, v47\n\t" \
  "v_perm_b32 v44, v118, v114, s63\n\t" \
  "v_perm_b32 v45, v126, v122, s63\n\t" \
  "v_perm_b32 v46, v134, v130, s63\n\t" \
  "v_perm_b32 v47, v142, v138, s63\n\t" \
  "v_accvgpr_write_b32 a84, v44\n\t" \
  "v_accvgpr_write_b32 a85, v45\n\t" \
  "v_accvgpr_write_b32 a86, v46\n\t" \
  "v_accvgpr_write_b32 a87, v47\n\t" \
  "v_perm_b32 v44, v119, v115, s62\n\t" \
  "v_perm_b32 v45, v127, v123, s62\n\t" \
  "v_perm_b32 v46, v135, v131, s62\n\t" \
  "v_perm_b32 v47, v143, v139, s62\n\t" \
  "v_accvgpr_write_b32 a88, v44\n\t" \
  "v_accvgpr_write_b32 a89, v45\n\t" \
  "v_accvgpr_write_b32 a90, v46\n\t" \
  "v_accvgpr_write_b32 a91, v47\n\t" \
  "v_perm_b32 v44, v119, v115, s63\n\t" \
  "v_perm_b32 v45, v127, v123, s63\n\t" \
  "v_perm_b32 v46, v135, v131, s63\n\t" \
  "v_perm_b32 v47, v143, v139, s63\n\t" \
  "v_accvgpr_write_b32 a92, v44\n\t" \
  "v_accvgpr_write_b32 a93, v45\n\t" \
  "v_accvgpr_write_b32 a94, v46\n\t" \
  "v_accvgpr_write_b32 a95, v47\n\t" \
  "s_xor_b32 s60, s66, 147456\n\t" \
  "s_add_u32 s60, s60, 110592\n\t" \
  "s_add_u32 s61, s60, 18432\n\t" \
  "buffer_load_dwordx4 v[112:115], v41, %[W], s60 offen offset:0\n\t" \
  "buffer_load_dwordx4 v[116:119], v41, %[W], s60 offen offset:1152\n\t" \
  "buffer_load_dwordx4 v[120:123], v41, %[W], s60 offen offset:2304\n\t" \
  "buffer_load_dwordx4 v[124:127], v41, %[W], s60 offen offset:3456\n\t" \
  "buffer_load_dwordx4 v[128:131], v41, %[W], s61 offen offset:0\n\t" \
  "buffer_load_dwordx4 v[132:135], v41, %[W], s61 offen offset:1152\n\t" \
  "buffer_load_dwordx4 v[136:139], v41, %[W], s61 offen offset:2304\n\t" \
  "buffer_load_dwordx4 v[140:143], v41, %[W], s61 offen offset:3456\n\t" \
  "s_waitcnt vmcnt(38)\n\t" \
  "v_perm_b32 v44, v148, v144, s62\n\t" \
  "s_waitcnt vmcnt(36)\n\t" \
  "v_perm_b32 v45, v156, v152, s62\n\t" \
  "s_waitcnt vmcnt(34)\n\t" \
  "v_perm_b32 v46, v164, v160, s62\n\t" \
  "s_waitcnt vmcnt(32)\n\t" \
  "v_perm_b32 v47, v172, v168, s62\n\t" \
  "v_accvgpr_write_b32 a96, v44\n\t" \
  "v_accvgpr_write_b32 a97, v45\n\t" \
  "v_accvgpr_write_b32 a98, v46\n\t" \
  "v_accvgpr_write_b32 a99, v47\n\t" \
  "v_perm_b32 v44, v148, v144, s63\n\t" \
  "v_perm_b32 v45, v156, v152, s63\n\t" \
  "v_perm_b32 v46, v164, v160, s63\n\t" \
  "v_perm_b32 v47, v172, v168, s63\n\t" \
  "v_accvgpr_write_b32 a100, v44\n\t" \
  "v_accvgpr_write_b32 a101, v45\n\t" \
  "v_accvgpr_write_b32 a102, v46\n\t" \
  "v_accvgpr_write_b32 a103, v47\n\t" \
  "v_perm_b32 v44, v149, v145, s62\n\t" \
  "v_perm_b32 v45, v157, v153, s62\n\t" \
  "v_perm_b32 v46, v165, v161, s62\n\t" \
  "v_perm_b32 v47, v173, v169, s62\n\t" \
  "v_accvgpr_write_b32 a104, v44\n\t" \
  "v_accvgpr_write_b32 a105, v45\n\t" \
  "v_accvgpr_write_b32 a106, v46\n\t" \
  "v_accvgpr_write_b32 a107, v47\n\t" \
  "v_perm_b32 v44, v149, v145, s63\n\t" \
  "v_perm_b32 v45, v157, v153, s63\n\t" \
  "v_perm_b32 v46, v165, v161, s63\n\t" \
  "v_perm_b32 v47, v173, v169, s63\n\t" \
  "v_accvgpr_write_b32 a108, v44\n\t" \
  "v_accvgpr_write_b32 a109, v45\n\t" \
  "v_accvgpr_write_b32 a110, v46\n\t" \
  "v_accvgpr_write_b32 a111, v47\n\t" \
  "v_perm_b32 v44, v150, v146, s62\n\t" \
  "v_perm_b32 v45, v158, v154, s62\n\t" \
  "v_perm_b32 v46, v166, v162, s62\n\t" \
  "v_perm_b32 v47, v174, v170, s62\n\t" \
  "v_accvgpr_write_b32 a112, v44\n\t" \
  "v_accvgpr_write_b32 a113, v45\n\t" \
  "v_accvgpr_write_b32 a114, v46\n\t" \
  "v_accvgpr_write_b32 a115, v47\n\t" \
  "v_perm_b32 v44, v150, v146, s63\n\t" \
  "v_perm_b32 v45, v158, v154, s63\n\t" \
  "v_perm_b32 v46, v166, v162, s63\n\t" \
  "v_perm_b32 v47, v174, v170, s63\n\t" \
  "v_accvgpr_write_b32 a116, v44\n\t" \
  "v_accvgpr_write_b32 a117, v45\n\t" \
  "v_accvgpr_write_b32 a118, v46\n\t" \
  "v_accvgpr_write_b32 a119, v47\n\t" \
  "v_perm_b32 v44, v151, v147, s62\n\t" \
  "v_perm_b32 v45, v159, v155, s62\n\t" \
  "v_perm_b32 v46, v167, v163, s62\n\t" \
  "v_perm_b32 v47, v175, v171, s62\n\t" \
  "v_accvgpr_write_b32 a120, v44\n\t" \
  "v_accvgpr_write_b32 a121, v45\n\t" \
  "v_accvgpr_write_b32 a122, v46\n\t" \
  "v_accvgpr_write_b32 a123, v47\n\t" \
  "v_perm_b32 v44, v151, v147, s63\n\t" \
  "v_perm_b32 v45, v159, v155, s63\n\t" \
  "v_perm_b32 v46, v167, v163, s63\n\t" \
  "v_perm_b32 v47, v175, v171, s63\n\t" \
  "v_accvgpr_write_b32 a124, v44\n\t" \
  "v_accvgpr_write_b32 a125, v45\n\t" \
  "v_accvgpr_write_b32 a126, v46\n\t" \
  "v_accvgpr_write_b32 a127, v47\n\t" \
  "s_xor_b32 s60, s66, 0\n\t" \
  "s_add_u32 s60, s60, 0\n\t" \
  "s_add_u32 s61, s60, 18432\n\t" \
  "buffer_load_ushort v224, v42, %[W], s60 offen offset:0\n\t" \
  "buffer_load_ushort v144, v42, %[W], s60 offen offset:1152\n\t" \
  "buffer_load_ushort v225, v42, %[W], s60 offen offset:2304\n\t" \
  "buffer_load_ushort v145, v42, %[W], s60 offen offset:3456\n\t" \
  "buffer_load_ushort v226, v42, %[W], s61 offen offset:0\n\t" \
  "buffer_load_ushort v146, v42, %[W], s61 offen offset:1152\n\t" \
  "buffer_load_ushort v227, v42, %[W], s61 offen offset:2304\n\t" \
  "buffer_load_ushort v147, v42, %[W], s61 offen offset:3456\n\t" \
  "s_xor_b32 s60, s66, 0\n\t" \
  "s_add_u32 s60, s60, 36864\n\t" \
  "s_add_u32 s61, s60, 18432\n\t" \
  "buffer_load_ushort v228, v42, %[W], s60 offen offset:0\n\t" \
  "buffer_load_ushort v148, v42, %[W], s60 offen offset:1152\n\t" \
  "buffer_load_ushort v229, v42, %[W], s60 offen offset:2304\n\t" \
  "buffer_load_ushort v149, v42, %[W], s60 offen offset:3456\n\t" \
  "buffer_load_ushort v230, v42, %[W], s61 offen offset:0\n\t" \
  "buffer_load_ushort v150, v42, %[W], s61 offen offset:1152\n\t" \
  "buffer_load_ushort v231, v42, %[W], s61 offen offset:2304\n\t" \
  "buffer_load_ushort v151, v42, %[W], s61 offen offset:3456\n\t" \
  "s_waitcnt vmcnt(46)\n\t" \
  "v_perm_b32 v44, v180, v176, s62\n\t" \
  "s_waitcnt vmcnt(44)\n\t" \
  "v_perm_b32 v45, v188, v184, s62\n\t" \
  "s_waitcnt vmcnt(42)\n\t" \
  "v_perm_b32 v46, v196, v192, s62\n\t" \
  "s_waitcnt vmcnt(40)\n\t" \
  "v_perm_b32 v47, v204, v200, s62\n\t" \
  "v_accvgpr_write_b32 a128, v44\n\t" \
  "v_accvgpr_write_b32 a129, v45\n\t" \
  "v_accvgpr_write_b32 a130, v46\n\t" \
  "v_accvgpr_write_b32 a131, v47\n\t" \
  "v_perm_b32 v44, v180, v176, s63\n\t" \
  "v_perm_b32 v45, v188, v184, s63\n\t" \
  "v_perm_b32 v46, v196, v192, s63\n\t" \
  "v_perm_b32 v47, v204, v200, s63\n\t" \
  "v_accvgpr_write_b32 a132, v44\n\t" \
  "v_accvgpr_write_b32 a133, v45\n\t" \
  "v_accvgpr_write_b32 a134, v46\n\t" \
  "v_accvgpr_write_b32 a135, v47\n\t" \
  "v_perm_b32 v44, v181, v177, s62\n\t" \
  "v_perm_b32 v45, v189, v185, s62\n\t" \
  "v_perm_b32 v46, v197, v193, s62\n\t" \
  "v_perm_b32 v47, v205, v201, s62\n\t" \
  "v_accvgpr_write_b32 a136, v44\n\t" \
  "v_accvgpr_write_b32 a137, v45\n\t" \
  "v_accvgpr_write_b32 a138, v46\n\t" \
  "v_accvgpr_write_b32 a139, v47\n\t" \
  "v_perm_b32 v44, v181, v177, s63\n\t" \
  "v_perm_b32 v45, v189, v185, s63\n\t" \
  "v_perm_b32 v46, v197, v193, s63\n\t" \
  "v_perm_b32 v47, v205, v201, s63\n\t" \
  "v_accvgpr_write_b32 a140, v44\n\t" \
  "v_accvgpr_write_b32 a141, v45\n\t" \
  "v_accvgpr_write_b32 a142, v46\n\t" \
  "v_accvgpr_write_b32 a143, v47\n\t" \
  "v_perm_b32 v44, v182, v178, s62\n\t" \
  "v_perm_b32 v45, v190, v186, s62\n\t" \
  "v_perm_b32 v46, v198, v194, s62\n\t" \
  "v_perm_b32 v47, v206, v202, s62\n\t" \
  "v_accvgpr_write_b32 a144, v44\n\t" \
  "v_accvgpr_write_b32 a145, v45\n\t" \
  "v_accvgpr_write_b32 a146, v46\n\t" \
  "v_accvgpr_write_b32 a147, v47\n\t" \
  "v_perm_b32 v44, v182, v178, s63\n\t" \
  "v_perm_b32 v45, v190, v186, s63\n\t" \
  "v_perm_b32 v46, v198, v194, s63\n\t" \
  "v_perm_b32 v47, v206, v202, s63\n\t" \
  "v_accvgpr_write_b32 a148, v44\n\t" \
  "v_accvgpr_write_b32 a149, v45\n\t" \
  "v_accvgpr_write_b32 a150, v46\n\t" \
  "v_accvgpr_write_b32 a151, v47\n\t" \
  "v_perm_b32 v44, v183, v179, s62\n\t" \
  "v_perm_b32 v45, v191, v187, s62\n\t" \
  "v_perm_b32 v46, v199, v195, s62\n\t" \
  "v_perm_b32 v47, v207, v203, s62\n\t" \
  "v_accvgpr_write_b32 a152, v44\n\t" \
  "v_accvgpr_write_b32 a153, v45\n\t" \
  "v_accvgpr_write_b32 a154, v46\n\t" \
  "v_accvgpr_write_b32 a155, v47\n\t" \
  "v_perm_b32 v44, v183, v179, s63\n\t" \
  "v_perm_b32 v45, v191, v187, s63\n\t" \
  "v_perm_b32 v46, v199, v195, s63\n\t" \
  "v_perm_b32 v47, v207, v203, s63\n\t" \
  "v_accvgpr_write_b32 a156, v44\n\t" \
  "v_accvgpr_write_b32 a157, v45\n\t" \
  "v_accvgpr_write_b32 a158, v46\n\t" \
  "v_accvgpr_write_b32 a159, v47\n\t" \
  "s_xor_b32 s60, s66, 0\n\t" \
  "s_add_u32 s60, s60, 73728\n\t" \
  "s_add_u32 s61, s60, 18432\n\t" \
  "buffer_load_ushort v232, v42, %[W], s60 offen offset:0\n\t" \
  "buffer_load_ushort v152, v42, %[W], s60 offen offset:1152\n\t" \
  "buffer_load_ushort v233, v42, %[W], s60 offen offset:2304\n\t" \
  "buffer_load_ushort v153, v42, %[W], s60 offen offset:3456\n\t" \
  "buffer_load_ushort v234, v42, %[W], s61 offen offset:0\n\t" \
  "buffer_load_ushort v154, v42, %[W], s61 offen offset:1152\n\t" \
  "buffer_load_ushort v235, v42, %[W], s61 offen offset:2304\n\t" \
  "buffer_load_ushort v155, v42, %[W], s61 offen offset:3456\n\t" \
  "s_xor_b32 s60, s66, 0\n\t" \
  "s_add_u32 s60, s60, 110592\n\t" \
  "s_add_u32 s61, s60, 18432\n\t" \
  "buffer_load_ushort v236, v42, %[W], s60 offen offset:0\n\t" \
  "buffer_load_ushort v156, v42, %[W], s60 offen offset:1152\n\t" \
  "buffer_load_ushort v237, v42, %[W], s60 offen offset:2304\n\t" \
  "buffer_load_ushort v157, v42, %[W], s60 offen offset:3456\n\t" \
  "buffer_load_ushort v238, v42, %[W], s61 offen offset:0\n\t" \
  "buffer_load_ushort v158, v42, %[W], s61 offen offset:1152\n\t" \
  "buffer_load_ushort v239, v42, %[W], s61 offen offset:2304\n\t" \
  "buffer_load_ushort v159, v42, %[W], s61 offen offset:3456\n\t" \
  "s_waitcnt vmcnt(54)\n\t" \
  "v_perm_b32 v44, v52, v48, s62\n\t" \
  "s_waitcnt vmcnt(52)\n\t" \
  "v_perm_b32 v45, v60, v56, s62\n\t" \
  "s_waitcnt vmcnt(50)\n\t" \
  "v_perm_b32 v46, v68, v64, s62\n\t" \
  "s_waitcnt vmcnt(48)\n\t" \
  "v_perm_b32 v47, v76, v72, s62\n\t" \
  "v_accvgpr_write_b32 a160, v44\n\t" \
  "v_accvgpr_write_b32 a161, v45\n\t" \
  "v_accvgpr_write_b32 a162, v46\n\t" \
  "v_accvgpr_write_b32 a163, v47\n\t" \
  "v_perm_b32 v44, v52, v48, s63\n\t" \
  "v_perm_b32 v45, v60, v56, s63\n\t" \
  "v_perm_b32 v46, v68, v64, s63\n\t" \
  "v_perm_b32 v47, v76, v72, s63\n\t" \
  "v_accvgpr_write_b32 a164, v44\n\t" \
  "v_accvgpr_write_b32 a165, v45\n\t" \
  "v_accvgpr_write_b32 a166, v46\n\t" \
  "v_accvgpr_write_b32 a167, v47\n\t" \
  "v_perm_b32 v44, v53, v49, s62\n\t" \
  "v_perm_b32 v45, v61, v57, s62\n\t" \
  "v_perm_b32 v46, v69, v65, s62\n\t" \
  "v_perm_b32 v47, v77, v73, s62\n\t" \
  "v_accvgpr_write_b32 a168, v44\n\t" \
  "v_accvgpr_write_b32 a169, v45\n\t" \
  "v_accvgpr_write_b32 a170, v46\n\t" \
  "v_accvgpr_write_b32 a171, v47\n\t" \
  "v_perm_b32 v44, v53, v49, s63\n\t" \
  "v_perm_b32 v45, v61, v57, s63\n\t" \
  "v_perm_b32 v46, v69, v65, s63\n\t" \
  "v_perm_b32 v47, v77, v73, s63\n\t" \
  "v_accvgpr_write_b32 a172, v44\n\t" \
  "v_accvgpr_write_b32 a173, v45\n\t" \
  "v_accvgpr_write_b32 a174, v46\n\t" \
  "v_accvgpr_write_b32 a175, v47\n\t" \
  "v_perm_b32 v44, v54, v50, s62\n\t" \
  "v_perm_b32 v45, v62, v58, s62\n\t" \
  "v_perm_b32 v46, v70, v66, s62\n\t" \
  "v_perm_b32 v47, v78, v74, s62\n\t" \
  "v_accvgpr_write_b32 a176, v44\n\t" \
  "v_accvgpr_write_b32 a177, v45\n\t" \
  "v_accvgpr_write_b32 a178, v46\n\t" \
  "v_accvgpr_write_b32 a179, v47\n\t" \
  "v_perm_b32 v44, v54, v50, s63\n\t" \
  "v_perm_b32 v45, v62, v58, s63\n\t" \
  "v_perm_b32 v46, v70, v66, s63\n\t" \
  "v_perm_b32 v47, v78, v74, s63\n\t" \
  "v_accvgpr_write_b32 a180, v44\n\t" \
  "v_accvgpr_write_b32 a181, v45\n\t" \
  "v_accvgpr_write_b32 a182, v46\n\t" \
  "v_accvgpr_write_b32 a183, v47\n\t" \
  "v_perm_b32 v44, v55, v51, s62\n\t" \
  "v_perm_b32 v45, v63, v59, s62\n\t" \
  "v_perm_b32 v46, v71, v67, s62\n\t" \
  "v_perm_b32 v47, v79, v75, s62\n\t" \
  "v_accvgpr_write_b32 a184, v44\n\t" \
  "v_accvgpr_write_b32 a185, v45\n\t" \
  "v_accvgpr_write_b32 a186, v46\n\t" \
  "v_accvgpr_write_b32 a187, v47\n\t" \
  "v_perm_b32 v44, v55, v51, s63\n\t" \
  "v_perm_b32 v45, v63, v59, s63\n\t" \
  "v_perm_b32 v46, v71, v67, s63\n\t" \
  "v_perm_b32 v47, v79, v75, s63\n\t" \
  "v_accvgpr_write_b32 a188, v44\n\t" \
  "v_accvgpr_write_b32 a189, v45\n\t" \
  "v_accvgpr_write_b32 a190, v46\n\t" \
  "v_accvgpr_write_b32 a191, v47\n\t" \
  "s_xor_b32 s60, s66, 147456\n\t" \
  "s_add_u32 s60, s60, 0\n\t" \
  "s_add_u32 s61, s60, 18432\n\t" \
  "buffer_load_ushort v240, v42, %[W], s60 offen offset:0\n\t" \
  "buffer_load_ushort v160, v42, %[W], s60 offen offset:1152\n\t" \
  "buffer_load_ushort v241, v42, %[W], s60 offen offset:2304\n\t" \
  "buffer_load_ushort v161, v42, %[W], s60 offen offset:3456\n\t" \
  "buffer_load_ushort v242, v42, %[W], s61 offen offset:0\n\t" \
  "buffer_load_ushort v162, v42, %[W], s61 offen offset:1152\n\t" \
  "buffer_load_ushort v243, v42, %[W], s61 offen offset:2304\n\t" \
  "buffer_load_ushort v163, v42, %[W], s61 offen offset:3456\n\t" \
  "s_waitcnt vmcnt(54)\n\t" \
  "v_perm_b32 v44, v84, v80, s62\n\t" \
  "s_waitcnt vmcnt(52)\n\t" \
  "v_perm_b32 v45, v92, v88, s62\n\t" \
  "s_waitcnt vmcnt(50)\n\t" \
  "v_perm_b32 v46, v100, v96, s62\n\t" \
  "s_waitcnt vmcnt(48)\n\t" \
  "v_perm_b32 v47, v108, v104, s62\n\t" \
  "v_accvgpr_write_b32 a192, v44\n\t" \
  "v_accvgpr_write_b32 a193, v45\n\t" \
  "v_accvgpr_write_b32 a194, v46\n\t" \
  "v_accvgpr_write_b32 a195, v47\n\t" \
  "v_perm_b32 v44, v84, v80, s63\n\t" \
  "v_perm_b32 v45, v92, v88, s63\n\t" \
  "v_perm_b32 v46, v100, v96, s63\n\t" \
  "v_perm_b32 v47, v108, v104, s63\n\t" \
  "v_accvgpr_write_b32 a196, v44\n\t" \
  "v_accvgpr_write_b32 a197, v45\n\t" \
  "v_accvgpr_write_b32 a198, v46\n\t" \
  "v_accvgpr_write_b32 a199, v47\n\t" \
  "v_perm_b32 v44, v85, v81, s62\n\t" \
  "v_perm_b32 v45, v93, v89, s62\n\t" \
  "v_perm_b32 v46, v101, v97, s62\n\t" \
  "v_perm_b32 v47, v109, v105, s62\n\t" \
  "v_accvgpr_write_b32 a200, v44\n\t" \
  "v_accvgpr_write_b32 a201, v45\n\t" \
  "v_accvgpr_write_b32 a202, v46\n\t" \
  "v_accvgpr_write_b32 a203, v47\n\t" \
  "v_perm_b32 v44, v85, v81, s63\n\t" \
  "v_perm_b32 v45, v93, v89, s63\n\t" \
  "v_perm_b32 v46, v101, v97, s63\n\t" \
  "v_perm_b32 v47, v109, v105, s63\n\t" \
  "v_accvgpr_write_b32 a204, v44\n\t" \
  "v_accvgpr_write_b32 a205, v45\n\t" \
  "v_accvgpr_write_b32 a206, v46\n\t" \
  "v_accvgpr_write_b32 a207, v47\n\t" \
  "v_perm_b32 v44, v86, v82, s62\n\t" \
  "v_perm_b32 v45, v94, v90, s62\n\t" \
  "v_perm_b32 v46, v102, v98, s62\n\t" \
  "v_perm_b32 v47, v110, v106, s62\n\t" \
  "v_accvgpr_write_b32 a208, v44\n\t" \
  "v_accvgpr_write_b32 a209, v45\n\t" \
  "v_accvgpr_write_b32 a210, v46\n\t" \
  "v_accvgpr_write_b32 a211, v47\n\t" \
  "v_perm_b32 v44, v86, v82, s63\n\t" \
  "v_perm_b32 v45, v94, v90, s63\n\t" \
  "v_perm_b32 v46, v102, v98, s63\n\t" \
  "v_perm_b32 v47, v110, v106, s63\n\t" \
  "v_accvgpr_write_b32 a212, v44\n\t" \
  "v_accvgpr_write_b32 a213, v45\n\t" \
  "v_accvgpr_write_b32 a214, v46\n\t" \
  "v_accvgpr_write_b32 a215, v47\n\t" \
  "v_perm_b32 v44, v87, v83, s62\n\t" \
  "v_perm_b32 v45, v95, v91, s62\n\t" \
  "v_perm_b32 v46, v103, v99, s62\n\t" \
  "v_perm_b32 v47, v111, v107, s62\n\t" \
  "v_accvgpr_write_b32 a216, v44\n\t" \
  "v_accvgpr_write_b32 a217, v45\n\t" \
  "v_accvgpr_write_b32 a218, v46\n\t" \
  "v_accvgpr_write_b32 a219, v47\n\t" \
  "v_perm_b32 v44, v87, v83, s63\n\t" \
  "v_perm_b32 v45, v95, v91, s63\n\t" \
  "v_perm_b32 v46, v103, v99, s63\n\t" \
  "v_perm_b32 v47, v111, v107, s63\n\t" \
  "v_accvgpr_write_b32 a220, v44\n\t" \
  "v_accvgpr_write_b32 a221, v45\n\t" \
  "v_accvgpr_write_b32 a222, v46\n\t" \
  "v_accvgpr_write_b32 a223, v47\n\t" \
  "s_xor_b32 s60, s66, 147456\n\t" \
  "s_add_u32 s60, s60, 36864\n\t" \
  "s_add_u32 s61, s60, 18432\n\t" \
  "buffer_load_ushort v244, v42, %[W], s60 offen offset:0\n\t" \
  "buffer_load_ushort v164, v42, %[W], s60 offen offset:1152\n\t" \
  "buffer_load_ushort v245, v42, %[W], s60 offen offset:2304\n\t" \
  "buffer_load_ushort v165, v42, %[W], s60 offen offset:3456\n\t" \
  "buffer_load_ushort v246, v42, %[W], s61 offen offset:0\n\t" \
  "buffer_load_ushort v166, v42, %[W], s61 offen offset:1152\n\t" \
  "buffer_load_ushort v247, v42, %[W], s61 offen offset:2304\n\t" \
  "buffer_load_ushort v167, v42, %[W], s61 offen offset:3456\n\t" \
  "s_waitcnt vmcnt(54)\n\t" \
  "v_perm_b32 v44, v116, v112, s62\n\t" \
  "s_waitcnt vmcnt(52)\n\t" \
  "v_perm_b32 v45, v124, v120, s62\n\t" \
  "s_waitcnt vmcnt(50)\n\t" \
  "v_perm_b32 v46, v132, v128, s62\n\t" \
  "s_waitcnt vmcnt(48)\n\t" \
  "v_perm_b32 v47, v140, v136, s62\n\t" \
  "v_accvgpr_write_b32 a224, v44\n\t" \
  "v_accvgpr_write_b32 a225, v45\n\t" \
  "v_accvgpr_write_b32 a226, v46\n\t" \
  "v_accvgpr_write_b32 a227, v47\n\t" \
  "v_perm_b32 v44, v116, v112, s63\n\t" \
  "v_perm_b32 v45, v124, v120, s63\n\t" \
  "v_perm_b32 v46, v132, v128, s63\n\t" \
  "v_perm_b32 v47, v140, v136, s63\n\t" \
  "v_accvgpr_write_b32 a228, v44\n\t" \
  "v_accvgpr_write_b32 a229, v45\n\t" \
  "v_accvgpr_write_b32 a230, v46\n\t" \
  "v_accvgpr_write_b32 a231, v47\n\t" \
  "v_perm_b32 v44, v117, v113, s62\n\t" \
  "v_perm_b32 v45, v125, v121, s62\n\t" \
  "v_perm_b32 v46, v133, v129, s62\n\t" \
  "v_perm_b32 v47, v141, v137, s62\n\t" \
  "v_accvgpr_write_b32 a232, v44\n\t" \
  "v_accvgpr_write_b32 a233, v45\n\t" \
  "v_accvgpr_write_b32 a234, v46\n\t" \
  "v_accvgpr_write_b32 a235, v47\n\t" \
  "v_perm_b32 v44, v117, v113, s63\n\t" \
  "v_perm_b32 v45, v125, v121, s63\n\t" \
  "v_perm_b32 v46, v133, v129, s63\n\t" \
  "v_perm_b32 v47, v141, v137, s63\n\t" \
  "v_accvgpr_write_b32 a236, v44\n\t" \
  "v_accvgpr_write_b32 a237, v45\n\t" \
  "v_accvgpr_write_b32 a238, v46\n\t" \
  "v_accvgpr_write_b32 a239, v47\n\t" \
  "v_perm_b32 v44, v118, v114, s62\n\t" \
  "v_perm_b32 v45, v126, v122, s62\n\t" \
  "v_perm_b32 v46, v134, v130, s62\n\t" \
  "v_perm_b32 v47, v142, v138, s62\n\t" \
  "v_accvgpr_write_b32 a240, v44\n\t" \
  "v_accvgpr_write_b32 a241, v45\n\t" \
  "v_accvgpr_write_b32 a242, v46\n\t" \
  "v_accvgpr_write_b32 a243, v47\n\t" \
  "v_perm_b32 v44, v118, v114, s63\n\t" \
  "v_perm_b32 v45, v126, v122, s63\n\t" \
  "v_perm_b32 v46, v134, v130, s63\n\t" \
  "v_perm_b32 v47, v142, v138, s63\n\t" \
  "v_accvgpr_write_b32 a244, v44\n\t" \
  "v_accvgpr_write_b32 a245, v45\n\t" \
  "v_accvgpr_write_b32 a246, v46\n\t" \
  "v_accvgpr_write_b32 a247, v47\n\t" \
  "v_perm_b32 v44, v119, v115, s62\n\t" \
  "v_perm_b32 v45, v127, v123, s62\n\t" \
  "v_perm_b32 v46, v135, v131, s62\n\t" \
  "v_perm_b32 v47, v143, v139, s62\n\t" \
  "v_accvgpr_write_b32 a248, v44\n\t" \
  "v_accvgpr_write_b32 a249, v45\n\t" \
  "v_accvgpr_write_b32 a250, v46\n\t" \
  "v_accvgpr_write_b32 a251, v47\n\t" \
  "v_perm_b32 v44, v119, v115, s63\n\t" \
  "v_perm_b32 v45, v127, v123, s63\n\t" \
  "v_perm_b32 v46, v135, v131, s63\n\t" \
  "v_perm_b32 v47, v143, v139, s63\n\t" \
  "v_accvgpr_write_b32 a252, v44\n\t" \
  "v_accvgpr_write_b32 a253, v45\n\t" \
  "v_accvgpr_write_b32 a254, v46\n\t" \
  "v_accvgpr_write_b32 a255, v47\n\t" \
  "s_xor_b32 s60, s66, 147456\n\t" \
  "s_add_u32 s60, s60, 73728\n\t" \
  "s_add_u32 s61, s60, 18432\n\t" \
  "buffer_load_ushort v248, v42, %[W], s60 offen offset:0\n\t" \
  "buffer_load_ushort v168, v42, %[W], s60 offen offset:1152\n\t" \
  "buffer_load_ushort v249, v42, %[W], s60 offen offset:2304\n\t" \
  "buffer_load_ushort v169, v42, %[W], s60 offen offset:3456\n\t" \
  "buffer_load_ushort v250, v42, %[W], s61 offen offset:0\n\t" \
  "buffer_load_ushort v170, v42, %[W], s61 offen offset:1152\n\t" \
  "buffer_load_ushort v251, v42, %[W], s61 offen offset:2304\n\t" \
  "buffer_load_ushort v171, v42, %[W], s61 offen offset:3456\n\t" \
  "s_xor_b32 s60, s66, 147456\n\t" \
  "s_add_u32 s60, s60, 110592\n\t" \
  "s_add_u32 s61, s60, 18432\n\t" \
  "s_waitcnt vmcnt(40)\n\t" \
  "buffer_load_ushort v252, v42, %[W], s60 offen offset:0\n\t" \
  "buffer_load_ushort v172, v42, %[W], s60 offen offset:1152\n\t" \
  "buffer_load_ushort v253, v42, %[W], s60 offen offset:2304\n\t" \
  "buffer_load_ushort v173, v42, %[W], s60 offen offset:3456\n\t" \
  "buffer_load_ushort v254, v42, %[W], s61 offen offset:0\n\t" \
  "buffer_load_ushort v174, v42, %[W], s61 offen offset:1152\n\t" \
  "buffer_load_ushort v255, v42, %[W], s61 offen offset:2304\n\t" \
  "buffer_load_ushort v175, v42, %[W], s61 offen offset:3456\n\t" \
  "v_lshl_or_b32 v224, v144, 16, v224\n\t" \
  "v_lshl_or_b32 v225, v145, 16, v225\n\t" \
  "v_lshl_or_b32 v226, v146, 16, v226\n\t" \
  "v_lshl_or_b32 v227, v147, 16, v227\n\t" \
  "v_lshl_or_b32 v228, v148, 16, v228\n\t" \
  "v_lshl_or_b32 v229, v149, 16, v229\n\t" \
  "v_lshl_or_b32 v230, v150, 16, v230\n\t" \
  "v_lshl_or_b32 v231, v151, 16, v231\n\t" \
  "s_waitcnt vmcnt(46)\n\t" \
  "v_lshl_or_b32 v232, v152, 16, v232\n\t" \
  "s_waitcnt vmcnt(44)\n\t" \
  "v_lshl_or_b32 v233, v153, 16, v233\n\t" \
  "s_waitcnt vmcnt(42)\n\t" \
  "v_lshl_or_b32 v234, v154, 16, v234\n\t" \
  "s_waitcnt vmcnt(40)\n\t" \
  "v_lshl_or_b32 v235, v155, 16, v235\n\t" \
  "s_waitcnt vmcnt(38)\n\t" \
  "v_lshl_or_b32 v236, v156, 16, v236\n\t" \
  "s_waitcnt vmcnt(36)\n\t" \
  "v_lshl_or_b32 v237, v157, 16, v237\n\t" \
  "s_waitcnt vmcnt(34)\n\t" \
  "v_lshl_or_b32 v238, v158, 16, v238\n\t" \
  "s_waitcnt vmcnt(32)\n\t" \
  "v_lshl_or_b32 v239, v159, 16, v239\n\t" \
  "s_waitcnt vmcnt(30)\n\t" \
  "v_lshl_or_b32 v240, v160, 16, v240\n\t" \
  "s_waitcnt vmcnt(28)\n\t" \
  "v_lshl_or_b32 v241, v161, 16, v241\n\t" \
  "s_waitcnt vmcnt(26)\n\t" \
  "v_lshl_or_b32 v242, v162, 16, v242\n\t" \
  "s_waitcnt vmcnt(24)\n\t" \
  "v_lshl_or_b32 v243, v163, 16, v243\n\t" \
  "s_waitcnt vmcnt(22)\n\t" \
  "v_lshl_or_b32 v244, v164, 16, v244\n\t" \
  "s_waitcnt vmcnt(20)\n\t" \
  "v_lshl_or_b32 v245, v165, 16, v245\n\t" \
  "s_waitcnt vmcnt(18)\n\t" \
  "v_lshl_or_b32 v246, v166, 16, v246\n\t" \
  "s_waitcnt vmcnt(16)\n\t" \
  "v_lshl_or_b32 v247, v167, 16, v247\n\t" \
  "s_waitcnt vmcnt(14)\n\t" \
  "v_lshl_or_b32 v248, v168, 16, v248\n\t" \
  "s_waitcnt vmcnt(12)\n\t" \
  "v_lshl_or_b32 v249, v169, 16, v249\n\t" \
  "s_waitcnt vmcnt(10)\n\t" \
  "v_lshl_or_b32 v250, v170, 16, v250\n\t" \
  "s_waitcnt vmcnt(8)\n\t" \
  "v_lshl_or_b32 v251, v171, 16, v251\n\t" \
  "s_waitcnt vmcnt(6)\n\t" \
  "v_lshl_or_b32 v252, v172, 16, v252\n\t" \
  "s_waitcnt vmcnt(4)\n\t" \
  "v_lshl_or_b32 v253, v173, 16, v253\n\t" \
  "s_waitcnt vmcnt(2)\n\t" \
  "v_lshl_or_b32 v254, v174, 16, v254\n\t" \
  "s_waitcnt vmcnt(0)\n\t" \
  "v_lshl_or_b32 v255, v175, 16, v255\n\t" \
  "s_mov_b32 s84, " TK_CAP "\n\t" \
  "s_mov_b32 s48, 0\n\t" \
  "s_mov_b32 s49, 0xf0000000\n\t" \
  "s_mov_b32 s46, s45\n\t" \
  "v_mov_b32 v5, 0\n\t" \
  "s_add_u32 s83, s48, 0\n\t" \
  "s_lshr_b64 s[60:61], s[90:91], s83\n\t" \
  "s_and_b32 s60, s60, 1\n\t" \
  "s_lshl_b32 s60, s60, 13\n\t" \
  "s_add_u32 s83, s83, " TK_SG0 "\n\t" \
  "s_lshl_b32 s83, s83, 21\n\t" \
  "s_or_b32 s83, s83, s60\n\t" \
  "LCOLD_%=:\n\t" \
  "s_add_u32 s62, s83, s41\n\t" \
  "s_mov_b32 s61, 0\n\t" \
  "LPQ_%=:\n\t" \
  "buffer_load_dword v10, v9, %[X], s62 offen sc1\n\t" \
  "s_waitcnt vmcnt(0)\n\t" \
  "v_cmp_eq_u32 vcc, 0, v10\n\t" \
  "s_cbranch_vccz LPQ_%=x\n\t" \
  "s_add_u32 s61, s61, 1\n\t" \
  "s_sleep 4\n\t" \
  "s_cmp_gt_u32 s61, s84\n\t" \
  "s_cbranch_scc1 LPQ_%=g\n\t" \
  "s_and_b32 s60, s61, 1023\n\t" \
  "s_cmp_lg_u32 s60, 0\n\t" \
  "s_cbranch_scc1 LPQ_%=\n\t" \
  "buffer_load_dword v10, off, %[X], s92 sc1\n\t" \
  "s_waitcnt vmcnt(0)\n\t" \
  "v_readfirstlane_b32 s60, v10\n\t" \
  "s_nop 0\n\t" \
  "s_cmp_eq_u32 s60, 0\n\t" \
  "s_cbranch_scc1 LPQ_%=\n\t" \
  "LPQ_%=g:\n\t" \
  "s_mov_b32 s84, 0\n\t" \
  "s_or_b32 s60, s48, 1073741824\n\t" \
  "v_mov_b32 v10, s60\n\t" \
  "s_mov_b64 exec, 1\n\t" \
  "buffer_store_dword v10, off, %[X], s92 sc1\n\t" \
  "s_mov_b64 exec, -1\n\t" \
  "LPQ_%=x:\n\t" \
  "s_add_u32 m0, s46, 0\n\t" \
  "s_add_u32 s47, s83, s36\n\t" \
  "buffer_load_dwordx4 v0, %[X], s47 offen offset:0 sc1 lds\n\t" \
  "buffer_load_dwordx4 v0, %[X], s47 offen offset:1024 sc1 lds\n\t" \
  "buffer_load_dwordx4 v0, %[X], s47 offen offset:2048 sc1 lds\n\t" \
  "buffer_load_dwordx4 v0, %[X], s47 offen offset:3072 sc1 lds\n\t" \
  "s_add_u32 m0, s46, 4096\n\t" \
  "s_add_u32 s47, s83, s37\n\t" \
  "buffer_load_dwordx4 v0, %[X], s47 offen offset:0 sc1 lds\n\t" \
  "buffer_load_dwordx4 v0, %[X], s47 offen offset:1024 sc1 lds\n\t" \
  "buffer_load_dwordx4 v0, %[X], s47 offen offset:2048 sc1 lds\n\t" \
  "buffer_load_dwordx4 v0, %[X], s47 offen offset:3072 sc1 lds\n\t" \
  "s_add_u32 m0, s46, 8192\n\t" \
  "s_add_u32 s47, s83, s38\n\t" \
  "buffer_load_dwordx4 v0, %[X], s47 offen offset:0 sc1 lds\n\t" \
  "buffer_load_dwordx4 v0, %[X], s47 offen offset:1024 sc1 lds\n\t" \
  "buffer_load_dwordx4 v0, %[X], s47 offen offset:2048 sc1 lds\n\t" \
  "buffer_load_dwordx4 v0, %[X], s47 offen offset:3072 sc1 lds\n\t" \
  "s_waitcnt vmcnt(0)\n\t" \
  "LTOP_%=:\n\t" \
  TK_AC0 \
  "s_barrier\n\t" \
  TK_AC1 \
  "ds_read_b128 v[48:51], v2 offset:0\n\t" \
  "ds_read_b128 v[64:67], v1 offset:8192\n\t" \
  "ds_read_b128 v[80:83], v1 offset:12288\n\t" \
  "ds_read_b128 v[52:55], v2 offset:1024\n\t" \
  "ds_read_b128 v[68:71], v1 offset:9216\n\t" \
  "ds_read_b128 v[84:87], v1 offset:13312\n\t" \
  "ds_read_b128 v[56:59], v2 offset:2048\n\t" \
  "ds_read_b128 v[72:75], v1 offset:10240\n\t" \
  "ds_read_b128 v[88:91], v1 offset:14336\n\t" \
  "ds_read_b128 v[60:63], v2 offset:3072\n\t" \
  "ds_read_b128 v[76:79], v1 offset:11264\n\t" \
  "ds_read_b128 v[92:95], v1 offset:15360\n\t" \
  "v_readfirstlane_b32 s57, v5\n\t" \
  "s_mov_b32 s82, s83\n\t" \
  "s_xor_b32 s46, s46, 0x10000\n\t" \
  "s_add_u32 s58, s82, s41\n\t" \
  "s_add_u32 s58, s58, 4096\n\t" \
  "s_add_u32 s59, s82, s39\n\t" \
  "s_cmp_eq_u32 s57, 0\n\t" \
  "s_cselect_b32 s62, 0xf0000000, s59\n\t" \
  "s_waitcnt lgkmcnt(10)\n\t" \
  "v_mfma_f32_16x16x32_bf16 v[36:39], v[64:67], v[48:51], v[16:19]\n\t" \
  "ds_read_b128 v[96:99], v1 offset:16384\n\t" \
  "buffer_load_dwordx4 v[196:199], v0, %[X], s62 offen offset:0 sc1\n\t" \
  "s_waitcnt lgkmcnt(10)\n\t" \
  "v_mfma_f32_16x16x32_bf16 v[40:43], v[80:83], v[48:51], v[20:23]\n\t" \
  "ds_read_b128 v[100:103], v1 offset:17408\n\t" \
  "buffer_load_dwordx4 v[200:203], v0, %[X], s62 offen offset:1024 sc1\n\t" \
  "s_waitcnt lgkmcnt(9)\n\t" \
  "v_mfma_f32_16x16x32_bf16 v[36:39], v[68:71], v[52:55], v[36:39]\n\t" \
  "ds_read_b128 v[104:107], v1 offset:18432\n\t" \
  "buffer_load_dwordx2 v[204:205], v6, %[X], s62 offen offset:2048 sc1\n\t" \
  "s_waitcnt lgkmcnt(9)\n\t" \
  "v_mfma_f32_16x16x32_bf16 v[40:43], v[84:87], v[52:55], v[40:43]\n\t" \
  "ds_read_b128 v[108:111], v1 offset:19456\n\t" \
  "buffer_load_dwordx2 v[206:207], v6, %[X], s62 offen offset:2560 sc1\n\t" \
  "s_waitcnt lgkmcnt(8)\n\t" \
  "v_mfma_f32_16x16x32_bf16 v[36:39], v[72:75], v[56:59], v[36:39]\n\t" \
  "ds_read_b128 v[112:115], v1 offset:20480\n\t" \
  "buffer_load_dwordx2 v[208:209], v6, %[X], s62 offen offset:3072 sc1\n\t" \
  "s_waitcnt lgkmcnt(8)\n\t" \
  "v_mfma_f32_16x16x32_bf16 v[40:43], v[88:91], v[56:59], v[40:43]\n\t" \
  "ds_read_b128 v[116:119], v1 offset:21504\n\t" \
  "buffer_load_dwordx2 v[210:211], v6, %[X], s62 offen offset:3584 sc1\n\t" \
  "s_waitcnt lgkmcnt(7)\n\t" \
  "v_mfma_f32_16x16x32_bf16 v[36:39], v[76:79], v[60:63], v[36:39]\n\t" \
  "ds_read_b128 v[120:123], v1 offset:22528\n\t" \
  "s_waitcnt lgkmcnt(7)\n\t" \
  "v_mfma_f32_16x16x32_bf16 v[40:43], v[92:95], v[60:63], v[40:43]\n\t" \
  "ds_read_b128 v[124:127], v1 offset:23552\n\t" \
  "buffer_load_dword v5, off, %[X], s58 sc1\n\t" \
  "s_add_u32 s83, s48, 1\n\t" \
  "s_lshr_b64 s[60:61], s[90:91], s83\n\t" \
  "s_and_b32 s60, s60, 1\n\t" \
  "s_lshl_b32 s60, s60, 13\n\t" \
  "s_add_u32 s83, s83, " TK_SG0 "\n\t" \
  "s_lshl_b32 s83, s83, 21\n\t" \
  "s_or_b32 s83, s83, s60\n\t" \
  "v_xor_b32 v1, 0x10000, v1\n\t" \
  "v_xor_b32 v2, 0x10000, v2\n\t" \
  "v_max3_f32 v11, v36, v37, v38\n\t" \
  "v_max3_f32 v11, v11, v39, v40\n\t" \
  "v_max3_f32 v13, v41, v42, v43\n\t" \
  "v_max_f32 v11, v11, v13\n\t" \
  "v_mov_b32 v13, v11\n\t" \
  "s_nop 1\n\t" \
  "v_permlane16_swap_b32 v11, v13\n\t" \
  "v_max_f32 v11, v11, v13\n\t" \
  "v_mov_b32 v13, v11\n\t" \
  "s_nop 1\n\t" \
  "v_permlane32_swap_b32 v11, v13\n\t" \
  "v_max_f32 v11, v11, v13\n\t" \
  "v_mul_f32 v13, 0xbe0293ee, v11\n\t" \
  "v_fmamk_f32 v36, v36, 0x3e0293ee, v13\n\t" \
  "v_fmamk_f32 v37, v37, 0x3e0293ee, v13\n\t" \
  "v_fmamk_f32 v38, v38, 0x3e0293ee, v13\n\t" \
  "v_fmamk_f32 v39, v39, 0x3e0293ee, v13\n\t" \
  "v_fmamk_f32 v40, v40, 0x3e0293ee, v13\n\t" \
  "v_fmamk_f32 v41, v41, 0x3e0293ee, v13\n\t" \
  "v_fmamk_f32 v42, v42, 0x3e0293ee, v13\n\t" \
  "v_fmamk_f32 v43, v43, 0x3e0293ee, v13\n\t" \
  "v_exp_f32 v36, v36\n\t" \
  "v_exp_f32 v37, v37\n\t" \
  "v_exp_f32 v38, v38\n\t" \
  "v_exp_f32 v39, v39\n\t" \
  "v_exp_f32 v40, v40\n\t" \
  "v_exp_f32 v41, v41\n\t" \
  "v_exp_f32 v42, v42\n\t" \
  "v_exp_f32 v43, v43\n\t" \
  "v_cvt_pk_bf16_f32 v44, v36, v37\n\t" \
  "v_cvt_pk_bf16_f32 v45, v38, v39\n\t" \
  "v_cvt_pk_bf16_f32 v46, v40, v41\n\t" \
  "v_cvt_pk_bf16_f32 v47, v42, v43\n\t" \
  "v_add_f32 v36, v36, v37\n\t" \
  "v_add_f32 v38, v38, v39\n\t" \
  "s_waitcnt lgkmcnt(7)\n\t" \
  "v_mfma_f32_16x16x32_bf16 v[128:131], v[96:99], v[44:47], 0\n\t" \
  "v_add_f32 v40, v40, v41\n\t" \
  "s_and_b32 s62, s48, 15\n\t" \
  "s_lshr_b32 s63, s62, 1\n\t" \
  "s_waitcnt lgkmcnt(6)\n\t" \
  "v_mfma_f32_16x16x32_bf16 v[132:135], v[100:103], v[44:47], 0\n\t" \
  "v_add_f32 v42, v42, v43\n\t" \
  "s_lshr_b32 s60, s88, s63\n\t" \
  "s_and_b32 s60, s60, 1\n\t" \
  "s_waitcnt lgkmcnt(5)\n\t" \
  "v_mfma_f32_16x16x32_bf16 v[136:139], v[104:107], v[44:47], 0\n\t" \
  "v_add_f32 v36, v36, v38\n\t" \
  "s_lshl_b32 s60, s60, 13\n\t" \
  "s_lshl_b32 s61, s62, 20\n\t" \
  "s_waitcnt lgkmcnt(4)\n\t" \
  "v_mfma_f32_16x16x32_bf16 v[140:143], v[108:111], v[44:47], 0\n\t" \
  "v_add_f32 v40, v40, v42\n\t" \
  "s_add_u32 s50, s61, s60\n\t" \
  "s_add_u32 s50, s50, s43\n\t" \
  "s_waitcnt lgkmcnt(3)\n\t" \
  "v_mfma_f32_16x16x32_bf16 v[144:147], v[112:115], v[44:47], 0\n\t" \
  "v_add_f32 v12, v36, v40\n\t" \
  "s_add_u32 s51, s50, 32768\n\t" \
  "s_add_u32 s52, s51, 32768\n\t" \
  "s_waitcnt lgkmcnt(2)\n\t" \
  "v_mfma_f32_16x16x32_bf16 v[148:151], v[116:119], v[44:47], 0\n\t" \
  "s_add_u32 s53, s52, 32768\n\t" \
  "s_lshl_b32 s61, s62, 17\n\t" \
  "s_waitcnt lgkmcnt(1)\n\t" \
  "v_mfma_f32_16x16x32_bf16 v[152:155], v[120:123], v[44:47], 0\n\t" \
  "s_add_u32 s54, s61, s44\n\t" \
  "s_add_u32 s63, s83, s41\n\t" \
  "s_waitcnt lgkmcnt(0)\n\t" \
  "v_mfma_f32_16x16x32_bf16 v[156:159], v[124:127], v[44:47], 0\n\t" \
  TK_AC2 \
  "s_waitcnt vmcnt(0)\n\t" \
  "v_readfirstlane_b32 s60, v5\n\t" \
  "s_barrier\n\t" \
  TK_AC3 \
  TK_PA0 \
  "s_cmp_lg_u32 s57, 0\n\t" \
  "s_cbranch_scc1 LGOT_%=\n\t" \
  "s_cmp_lg_u32 s60, 0\n\t" \
  "s_cbranch_scc1 LPK_%=x\n\t" \
  "s_mov_b64 exec, 1\n\t" \
  "buffer_store_dword v3, off, %[X], s49 sc1\n\t" \
  "s_mov_b64 exec, -1\n\t" \
  "s_mov_b32 s49, 0xf0000000\n\t" \
  "s_mov_b32 s61, 0\n\t" \
  "LPK_%=:\n\t" \
  "buffer_load_dword v5, off, %[X], s58 sc1\n\t" \
  "s_waitcnt vmcnt(0)\n\t" \
  "v_cmp_eq_u32 vcc, 0, v5\n\t" \
  "s_cbranch_vccz LPK_%=x\n\t" \
  "s_add_u32 s61, s61, 1\n\t" \
  "s_sleep " TK_PSK "\n\t" \
  "s_cmp_gt_u32 s61, s84\n\t" \
  "s_cbranch_scc1 LPK_%=g\n\t" \
  "s_and_b32 s60, s61, 1023\n\t" \
  "s_cmp_lg_u32 s60, 0\n\t" \
  "s_cbranch_scc1 LPK_%=\n\t" \
  "buffer_load_dword v5, off, %[X], s92 sc1\n\t" \
  "s_waitcnt vmcnt(0)\n\t" \
  "v_readfirstlane_b32 s60, v5\n\t" \
  "s_nop 0\n\t" \
  "s_cmp_eq_u32 s60, 0\n\t" \
  "s_cbranch_scc1 LPK_%=\n\t" \
  "LPK_%=g:\n\t" \
  "s_mov_b32 s84, 0\n\t" \
  "s_or_b32 s60, s48, 1342177280\n\t" \
  "v_mov_b32 v5, s60\n\t" \
  "s_mov_b64 exec, 1\n\t" \
  "buffer_store_dword v5, off, %[X], s92 sc1\n\t" \
  "s_mov_b64 exec, -1\n\t" \
  "LPK_%=x:\n\t" \
  TK_ADL \
  "buffer_load_dwordx4 v[196:199], v0, %[X], s59 offen offset:0 sc1\n\t" \
  "buffer_load_dwordx4 v[200:203], v0, %[X], s59 offen offset:1024 sc1\n\t" \
  "buffer_load_dwordx2 v[204:205], v6, %[X], s59 offen offset:2048 sc1\n\t" \
  "buffer_load_dwordx2 v[206:207], v6, %[X], s59 offen offset:2560 sc1\n\t" \
  "buffer_load_dwordx2 v[208:209], v6, %[X], s59 offen offset:3072 sc1\n\t" \
  "buffer_load_dwordx2 v[210:211], v6, %[X], s59 offen offset:3584 sc1\n\t" \
  "LGOT_%=:\n\t" \
  TK_PA1 \
  TK_AC4 \
  "buffer_load_dword v10, v9, %[X], s63 offen sc1\n\t" \
  "s_mov_b64 exec, 1\n\t" \
  "buffer_store_dword v3, off, %[X], s49 sc1\n\t" \
  "s_mov_b64 exec, -1\n\t" \
  "s_mov_b32 s49, 0xf0000000\n\t" \
  "s_add_u32 s49, s82, s42\n\t" \
  "s_waitcnt vmcnt(7)\n\t" \
  "v_mfma_f32_16x16x32_bf16 v[36:39], v[196:199], v[48:51], 0\n\t" \
  "v_mfma_f32_16x16x32_bf16 v[40:43], v[196:199], v[52:55], 0\n\t" \
  "s_waitcnt vmcnt(6)\n\t" \
  "v_mfma_f32_16x16x32_bf16 v[36:39], v[200:203], v[56:59], v[36:39]\n\t" \
  "v_mfma_f32_16x16x32_bf16 v[40:43], v[200:203], v[60:63], v[40:43]\n\t" \
  TK_AC5 \
  "s_nop 8\n\t" \
  "v_add_f32 v36, v36, v42\n\t" \
  "v_add_f32 v37, v37, v43\n\t" \
  "v_max3_f32 v14, v36, v37, v11\n\t" \
  "v_mov_b32 v13, v14\n\t" \
  "s_nop 1\n\t" \
  "v_permlane16_swap_b32 v14, v13\n\t" \
  "v_max_f32 v14, v14, v13\n\t" \
  "v_mov_b32 v13, v14\n\t" \
  "s_nop 1\n\t" \
  "v_permlane32_swap_b32 v14, v13\n\t" \
  "v_max_f32 v14, v14, v13\n\t" \
  "v_mul_f32 v14, 0xbe0293ee, v14\n\t" \
  "v_fmamk_f32 v11, v11, 0x3e0293ee, v14\n\t" \
  "v_fmamk_f32 v36, v36, 0x3e0293ee, v14\n\t" \
  "v_fmamk_f32 v37, v37, 0x3e0293ee, v14\n\t" \
  "v_exp_f32 v11, v11\n\t" \
  "v_exp_f32 v36, v36\n\t" \
  "v_exp_f32 v37, v37\n\t" \
  "v_fma_f32 v12, v12, v11, v36\n\t" \
  "v_add_f32 v12, v12, v37\n\t" \
  "v_mov_b32 v13, v12\n\t" \
  "s_nop 1\n\t" \
  "v_permlane16_swap_b32 v12, v13\n\t" \
  "v_add_f32 v12, v12, v13\n\t" \
  "v_mov_b32 v13, v12\n\t" \
  "s_nop 1\n\t" \
  "v_permlane32_swap_b32 v12, v13\n\t" \
  "v_add_f32 v12, v12, v13\n\t" \
  "v_rcp_f32 v12, v12\n\t" \
  "s_nop 0\n\t" \
  "v_mul_f32 v14, v11, v12\n\t" \
  "v_mul_f32 v36, v36, v12\n\t" \
  "v_mul_f32 v37, v37, v12\n\t" \
  "v_cvt_pk_bf16_f32 v26, v36, v37\n\t" \
  "v_cvt_pk_bf16_f32 v33, v36, v37\n\t" \
  "v_pk_mul_f32 v[128:129], v[128:129], v[14:15] op_sel_hi:[1,0]\n\t" \
  "v_pk_mul_f32 v[130:131], v[130:131], v[14:15] op_sel_hi:[1,0]\n\t" \
  "v_pk_mul_f32 v[132:133], v[132:133], v[14:15] op_sel_hi:[1,0]\n\t" \
  "v_pk_mul_f32 v[134:135], v[134:135], v[14:15] op_sel_hi:[1,0]\n\t" \
  "v_pk_mul_f32 v[136:137], v[136:137], v[14:15] op_sel_hi:[1,0]\n\t" \
  "v_pk_mul_f32 v[138:139], v[138:139], v[14:15] op_sel_hi:[1,0]\n\t" \
  "v_pk_mul_f32 v[140:141], v[140:141], v[14:15] op_sel_hi:[1,0]\n\t" \
  "v_pk_mul_f32 v[142:143], v[142:143], v[14:15] op_sel_hi:[1,0]\n\t" \
  "s_waitcnt vmcnt(4)\n\t" \
  "v_mfma_f32_16x16x32_bf16 v[128:131], v[204:207], v[26:29], v[128:131]\n\t" \
  "v_mul_f32 v144, v144, v14\n\t" \
  "v_mul_f32 v145, v145, v14\n\t" \
  "v_mfma_f32_16x16x32_bf16 v[132:135], v[204:207], v[32:35], v[132:135]\n\t" \
  "v_mul_f32 v146, v146, v14\n\t" \
  "v_mul_f32 v147, v147, v14\n\t" \
  "v_mfma_f32_16x16x32_bf16 v[136:139], v[204:207], v[24:27], v[136:139]\n\t" \
  "v_mul_f32 v148, v148, v14\n\t" \
  "v_mul_f32 v149, v149, v14\n\t" \
  "v_mfma_f32_16x16x32_bf16 v[140:143], v[204:207], v[30:33], v[140:143]\n\t" \
  "v_cvt_pk_bf16_f32 v64, v128, v129\n\t" \
  "v_cvt_pk_bf16_f32 v65, v130, v131\n\t" \
  "v_cvt_pk_bf16_f32 v66, v132, v133\n\t" \
  "v_cvt_pk_bf16_f32 v67, v134, v135\n\t" \
  "v_mul_f32 v150, v150, v14\n\t" \
  "v_mul_f32 v151, v151, v14\n\t" \
  TK_AC6 \
  "v_mfma_f32_16x16x32_bf16 v[192:195], v[224:227], v[64:67], 0\n\t" \
  "ds_write_b128 v3, v[64:67] offset:0\n\t" \
  "v_cvt_pk_bf16_f32 v68, v136, v137\n\t" \
  "v_cvt_pk_bf16_f32 v69, v138, v139\n\t" \
  "v_mfma_f32_16x16x32_bf16 v[160:163], v[64:67], a[0:3], 0\n\t" \
  "v_cvt_pk_bf16_f32 v70, v140, v141\n\t" \
  "v_cvt_pk_bf16_f32 v71, v142, v143\n\t" \
  "s_waitcnt vmcnt(2)\n\t" \
  "v_mfma_f32_16x16x32_bf16 v[144:147], v[208:211], v[26:29], v[144:147]\n\t" \
  "v_mul_f32 v152, v152, v14\n\t" \
  "v_mul_f32 v153, v153, v14\n\t" \
  "v_mfma_f32_16x16x32_bf16 v[148:151], v[208:211], v[32:35], v[148:151]\n\t" \
  "v_mul_f32 v154, v154, v14\n\t" \
  "v_mul_f32 v155, v155, v14\n\t" \
  "v_mfma_f32_16x16x32_bf16 v[164:167], v[64:67], a[4:7], 0\n\t" \
  "ds_write_b128 v3, v[68:71] offset:1024\n\t" \
  "v_mul_f32 v156, v156, v14\n\t" \
  "v_mul_f32 v157, v157, v14\n\t" \
  "v_mfma_f32_16x16x32_bf16 v[168:171], v[64:67], a[8:11], 0\n\t" \
  "v_mul_f32 v158, v158, v14\n\t" \
  "v_mul_f32 v159, v159, v14\n\t" \
  "v_mfma_f32_16x16x32_bf16 v[172:175], v[64:67], a[12:15], 0\n\t" \
  "v_cvt_pk_bf16_f32 v72, v144, v145\n\t" \
  "v_cvt_pk_bf16_f32 v73, v146, v147\n\t" \
  "v_mfma_f32_16x16x32_bf16 v[152:155], v[208:211], v[24:27], v[152:155]\n\t" \
  "v_cvt_pk_bf16_f32 v74, v148, v149\n\t" \
  "v_cvt_pk_bf16_f32 v75, v150, v151\n\t" \
  "v_mfma_f32_16x16x32_bf16 v[156:159], v[208:211], v[30:33], v[156:159]\n\t" \
  "v_mfma_f32_16x16x32_bf16 v[176:179], v[64:67], a[16:19], 0\n\t" \
  "ds_write_b128 v3, v[72:75] offset:2048\n\t" \
  "v_mfma_f32_16x16x32_bf16 v[180:183], v[64:67], a[20:23], 0\n\t" \
  "v_mfma_f32_16x16x32_bf16 v[184:187], v[64:67], a[24:27], 0\n\t" \
  "v_cvt_pk_bf16_f32 v76, v152, v153\n\t" \
  "v_cvt_pk_bf16_f32 v77, v154, v155\n\t" \
  "v_mfma_f32_16x16x32_bf16 v[188:191], v[64:67], a[28:31], 0\n\t" \
  "v_cvt_pk_bf16_f32 v78, v156, v157\n\t" \
  "v_cvt_pk_bf16_f32 v79, v158, v159\n\t" \
  "v_mfma_f32_16x16x32_bf16 v[192:195], v[228:231], v[68:71], v[192:195]\n\t" \
  "ds_write_b128 v3, v[76:79] offset:3072\n\t" \
  "v_mfma_f32_16x16x32_bf16 v[160:163], v[68:71], a[32:35], v[160:163]\n\t" \
  "s_waitcnt vmcnt(1)\n\t" \
  "v_cmp_eq_u32 vcc, 0, v10\n\t" \
  "v_mfma_f32_16x16x32_bf16 v[164:167], v[68:71], a[36:39], v[164:167]\n\t" \
  "s_cmp_eq_u64 vcc, 0\n\t" \
  "s_cselect_b32 s55, s83, 0xf0000000\n\t" \
  "s_cmp_eq_u32 s48, 63\n\t" \
  "v_mfma_f32_16x16x32_bf16 v[168:171], v[68:71], a[40:43], v[168:171]\n\t" \
  "s_cselect_b32 s55, 0xf0000000, s55\n\t" \
  "s_mov_b32 s56, s83\n\t" \
  "s_mov_b32 s83, s55\n\t" \
  "v_mfma_f32_16x16x32_bf16 v[172:175], v[68:71], a[44:47], v[172:175]\n\t" \
  "v_mfma_f32_16x16x32_bf16 v[176:179], v[68:71], a[48:51], v[176:179]\n\t" \
  "s_add_u32 m0, s46, 0\n\t" \
  "s_add_u32 s47, s83, s36\n\t" \
  "buffer_load_dwordx4 v0, %[X], s47 offen offset:0 sc1 lds\n\t" \
  "v_mfma_f32_16x16x32_bf16 v[180:183], v[68:71], a[52:55], v[180:183]\n\t" \
  "v_mfma_f32_16x16x32_bf16 v[184:187], v[68:71], a[56:59], v[184:187]\n\t" \
  "s_waitcnt lgkmcnt(0)\n\t" \
  "s_barrier\n\t" \
  "v_mfma_f32_16x16x32_bf16 v[188:191], v[68:71], a[60:63], v[188:191]\n\t" \
  "ds_read_b128 v[80:83], v4 offset:0\n\t" \
  "v_mfma_f32_16x16x32_bf16 v[192:195], v[232:235], v[72:75], v[192:195]\n\t" \
  "buffer_load_dwordx4 v0, %[X], s47 offen offset:1024 sc1 lds\n\t" \
  "v_mfma_f32_16x16x32_bf16 v[160:163], v[72:75], a[64:67], v[160:163]\n\t" \
  "ds_read_b128 v[84:87], v4 offset:1024\n\t" \
  "v_mfma_f32_16x16x32_bf16 v[164:167], v[72:75], a[68:71], v[164:167]\n\t" \
  "v_mfma_f32_16x16x32_bf16 v[168:171], v[72:75], a[72:75], v[168:171]\n\t" \
  "ds_read_b128 v[88:91], v4 offset:2048\n\t" \
  "v_mfma_f32_16x16x32_bf16 v[172:175], v[72:75], a[76:79], v[172:175]\n\t" \
  "buffer_load_dwordx4 v0, %[X], s47 offen offset:2048 sc1 lds\n\t" \
  "v_mfma_f32_16x16x32_bf16 v[176:179], v[72:75], a[80:83], v[176:179]\n\t" \
  "ds_read_b128 v[92:95], v4 offset:3072\n\t" \
  "v_mfma_f32_16x16x32_bf16 v[180:183], v[72:75], a[84:87], v[180:183]\n\t" \
  "v_mfma_f32_16x16x32_bf16 v[184:187], v[72:75], a[88:91], v[184:187]\n\t" \
  "v_mfma_f32_16x16x32_bf16 v[188:191], v[72:75], a[92:95], v[188:191]\n\t" \
  "buffer_load_dwordx4 v0, %[X], s47 offen offset:3072 sc1 lds\n\t" \
  "v_mfma_f32_16x16x32_bf16 v[192:195], v[236:239], v[76:79], v[192:195]\n\t" \
  "v_mfma_f32_16x16x32_bf16 v[160:163], v[76:79], a[96:99], v[160:163]\n\t" \
  "v_mfma_f32_16x16x32_bf16 v[164:167], v[76:79], a[100:103], v[164:167]\n\t" \
  "v_mfma_f32_16x16x32_bf16 v[168:171], v[76:79], a[104:107], v[168:171]\n\t" \
  "s_add_u32 m0, s46, 4096\n\t" \
  "s_add_u32 s47, s83, s37\n\t" \
  "buffer_load_dwordx4 v0, %[X], s47 offen offset:0 sc1 lds\n\t" \
  "v_mfma_f32_16x16x32_bf16 v[172:175], v[76:79], a[108:111], v[172:175]\n\t" \
  "v_mfma_f32_16x16x32_bf16 v[176:179], v[76:79], a[112:115], v[176:179]\n\t" \
  "v_mfma_f32_16x16x32_bf16 v[180:183], v[76:79], a[116:119], v[180:183]\n\t" \
  "v_mfma_f32_16x16x32_bf16 v[184:187], v[76:79], a[120:123], v[184:187]\n\t" \
  "buffer_load_dwordx4 v0, %[X], s47 offen offset:1024 sc1 lds\n\t" \
  "v_mfma_f32_16x16x32_bf16 v[188:191], v[76:79], a[124:127], v[188:191]\n\t" \
  "s_waitcnt lgkmcnt(3)\n\t" \
  "v_mfma_f32_16x16x32_bf16 v[192:195], v[240:243], v[80:83], v[192:195]\n\t" \
  "v_mfma_f32_16x16x32_bf16 v[160:163], v[80:83], a[128:131], v[160:163]\n\t" \
  "v_mfma_f32_16x16x32_bf16 v[164:167], v[80:83], a[132:135], v[164:167]\n\t" \
  "buffer_load_dwordx4 v0, %[X], s47 offen offset:2048 sc1 lds\n\t" \
  "v_mfma_f32_16x16x32_bf16 v[168:171], v[80:83], a[136:139], v[168:171]\n\t" \
  "v_mfma_f32_16x16x32_bf16 v[172:175], v[80:83], a[140:143], v[172:175]\n\t" \
  "v_mfma_f32_16x16x32_bf16 v[176:179], v[80:83], a[144:147], v[176:179]\n\t" \
  "v_mfma_f32_16x16x32_bf16 v[180:183], v[80:83], a[148:151], v[180:183]\n\t" \
  "buffer_load_dwordx4 v0, %[X], s47 offen offset:3072 sc1 lds\n\t" \
  "v_mfma_f32_16x16x32_bf16 v[184:187], v[80:83], a[152:155], v[184:187]\n\t" \
  "v_mfma_f32_16x16x32_bf16 v[188:191], v[80:83], a[156:159], v[188:191]\n\t" \
  "s_waitcnt lgkmcnt(2)\n\t" \
  "v_mfma_f32_16x16x32_bf16 v[192:195], v[244:247], v[84:87], v[192:195]\n\t" \
  "v_mfma_f32_16x16x32_bf16 v[160:163], v[84:87], a[160:163], v[160:163]\n\t" \
  "s_add_u32 m0, s46, 8192\n\t" \
  "s_add_u32 s47, s83, s38\n\t" \
  "buffer_load_dwordx4 v0, %[X], s47 offen offset:0 sc1 lds\n\t" \
  "v_mfma_f32_16x16x32_bf16 v[164:167], v[84:87], a[164:167], v[164:167]\n\t" \
  "v_mfma_f32_16x16x32_bf16 v[168:171], v[84:87], a[168:171], v[168:171]\n\t" \
  "v_mfma_f32_16x16x32_bf16 v[172:175], v[84:87], a[172:175], v[172:175]\n\t" \
  "v_mfma_f32_16x16x32_bf16 v[176:179], v[84:87], a[176:179], v[176:179]\n\t" \
  "buffer_load_dwordx4 v0, %[X], s47 offen offset:1024 sc1 lds\n\t" \
  "v_mfma_f32_16x16x32_bf16 v[180:183], v[84:87], a[180:183], v[180:183]\n\t" \
  "v_mfma_f32_16x16x32_bf16 v[184:187], v[84:87], a[184:187], v[184:187]\n\t" \
  "v_mfma_f32_16x16x32_bf16 v[188:191], v[84:87], a[188:191], v[188:191]\n\t" \
  "s_waitcnt lgkmcnt(1)\n\t" \
  "v_mfma_f32_16x16x32_bf16 v[192:195], v[248:251], v[88:91], v[192:195]\n\t" \
  "buffer_load_dwordx4 v0, %[X], s47 offen offset:2048 sc1 lds\n\t" \
  "v_mfma_f32_16x16x32_bf16 v[160:163], v[88:91], a[192:195], v[160:163]\n\t" \
  "v_mfma_f32_16x16x32_bf16 v[164:167], v[88:91], a[196:199], v[164:167]\n\t" \
  "v_mfma_f32_16x16x32_bf16 v[168:171], v[88:91], a[200:203], v[168:171]\n\t" \
  "v_mfma_f32_16x16x32_bf16 v[172:175], v[88:91], a[204:207], v[172:175]\n\t" \
  "buffer_load_dwordx4 v0, %[X], s47 offen offset:3072 sc1 lds\n\t" \
  "v_mfma_f32_16x16x32_bf16 v[176:179], v[88:91], a[208:211], v[176:179]\n\t" \
  "v_mfma_f32_16x16x32_bf16 v[180:183], v[88:91], a[212:215], v[180:183]\n\t" \
  "v_mfma_f32_16x16x32_bf16 v[184:187], v[88:91], a[216:219], v[184:187]\n\t" \
  "s_add_u32 s63, s63, 4096\n\t" \
  "v_mfma_f32_16x16x32_bf16 v[188:191], v[88:91], a[220:223], v[188:191]\n\t" \
  "buffer_load_dword v5, off, %[X], s63 sc1\n\t" \
  "s_waitcnt lgkmcnt(0)\n\t" \
  "v_mfma_f32_16x16x32_bf16 v[192:195], v[252:255], v[92:95], v[192:195]\n\t" \
  "v_mfma_f32_16x16x32_bf16 v[160:163], v[92:95], a[224:227], v[160:163]\n\t" \
  "v_mfma_f32_16x16x32_bf16 v[164:167], v[92:95], a[228:231], v[164:167]\n\t" \
  "v_mfma_f32_16x16x32_bf16 v[168:171], v[92:95], a[232:235], v[168:171]\n\t" \
  "v_cvt_pk_bf16_f32 v112, v192, v193\n\t" \
  "v_cvt_pk_bf16_f32 v113, v194, v195\n\t" \
  "v_mfma_f32_16x16x32_bf16 v[172:175], v[92:95], a[236:239], v[172:175]\n\t" \
  "buffer_store_dwordx2 v[112:113], v8, %[X], s54 offen sc1\n\t" \
  "v_mfma_f32_16x16x32_bf16 v[176:179], v[92:95], a[240:243], v[176:179]\n\t" \
  "v_cvt_pk_bf16_f32 v96, v160, v164\n\t" \
  "v_cvt_pk_bf16_f32 v100, v161, v165\n\t" \
  "v_mfma_f32_16x16x32_bf16 v[180:183], v[92:95], a[244:247], v[180:183]\n\t" \
  "v_cvt_pk_bf16_f32 v104, v162, v166\n\t" \
  "v_cvt_pk_bf16_f32 v108, v163, v167\n\t" \
  "v_mfma_f32_16x16x32_bf16 v[184:187], v[92:95], a[248:251], v[184:187]\n\t" \
  "v_cvt_pk_bf16_f32 v97, v168, v172\n\t" \
  "v_cvt_pk_bf16_f32 v101, v169, v173\n\t" \
  "v_mfma_f32_16x16x32_bf16 v[188:191], v[92:95], a[252:255], v[188:191]\n\t" \
  "v_cvt_pk_bf16_f32 v105, v170, v174\n\t" \
  "v_cvt_pk_bf16_f32 v109, v171, v175\n\t" \
  "v_cvt_pk_bf16_f32 v98, v176, v180\n\t" \
  "v_cvt_pk_bf16_f32 v102, v177, v181\n\t" \
  "v_cvt_pk_bf16_f32 v106, v178, v182\n\t" \
  "v_cvt_pk_bf16_f32 v110, v179, v183\n\t" \
  TK_AC7 \
  "s_nop 1\n\t" \
  "v_cvt_pk_bf16_f32 v99, v184, v188\n\t" \
  "buffer_store_dwordx4 v[96:99], v7, %[X], s50 offen sc1\n\t" \
  TK_ASL \
  "v_cvt_pk_bf16_f32 v103, v185, v189\n\t" \
  "buffer_store_dwordx4 v[100:103], v7, %[X], s51 offen sc1\n\t" \
  TK_ASL \
  "v_cvt_pk_bf16_f32 v107, v186, v190\n\t" \
  "buffer_store_dwordx4 v[104:107], v7, %[X], s52 offen sc1\n\t" \
  TK_ASL \
  "v_cvt_pk_bf16_f32 v111, v187, v191\n\t" \
  "buffer_store_dwordx4 v[108:111], v7, %[X], s53 offen sc1\n\t" \
  TK_PA2 \
  TK_AC8 \
  "s_cmp_eq_u32 s48, 63\n\t" \
  "s_cbranch_scc1 LEND_%=\n\t" \
  "s_add_u32 s48, s48, 1\n\t" \
  "s_cmp_eq_u32 s83, s56\n\t" \
  "s_mov_b32 s83, s56\n\t" \
  "s_cbranch_scc0 LCOLD_%=\n\t" \
  "s_waitcnt vmcnt(5)\n\t" \
  "s_branch LTOP_%=\n\t" \
  "LEND_%=:\n\t" \
  "s_waitcnt vmcnt(0)\n\t" \
  "s_barrier\n\t" \
  "s_mov_b64 exec, 1\n\t" \
  "buffer_store_dword v3, off, %[X], s49 sc1\n\t" \
  "s_mov_b64 exec, -1\n\t" \
  "s_mov_b32 s49, 0xf0000000\n\t" \
  TK_PA0 \
  TK_ACE \
  ""
#define QK_ASM \
  "v_lshrrev_b32 v16, 7, v0\n\t" \
  "v_lshlrev_b32 v16, 13, v16\n\t" \
  "v_lshrrev_b32 v19, 2, v0\n\t" \
  "v_and_b32 v19, 64, v19\n\t" \
  "v_xor_b32 v19, v19, v0\n\t" \
  "v_and_b32 v19, 0x70, v19\n\t" \
  "v_add_u32 v16, v16, v19\n\t" \
  "s_mul_i32 s63, s40, 40960\n\t" \
  "v_and_b32 v19, 0x300, v0\n\t" \
  "v_lshlrev_b32 v17, 2, v19\n\t" \
  "v_and_b32 v19, 0xc0, v0\n\t" \
  "v_lshl_add_u32 v17, v19, 1, v17\n\t" \
  "v_and_b32 v19, 0x30, v0\n\t" \
  "v_add3_u32 v17, v17, v19, s63\n\t" \
  "v_lshrrev_b32 v19, 1, v0\n\t" \
  "v_and_b32 v19, 64, v19\n\t" \
  "v_add_u32 v17, v17, v19\n\t" \
  "v_xor_b32 v18, 64, v17\n\t" \
  "s_add_u32 m0, s63, 0\n\t" \
  "s_mov_b32 s60, 0\n\t" \
  "buffer_load_dwordx4 v16, %[W], s60 offen lds\n\t" \
  "s_add_u32 m0, s63, 1024\n\t" \
  "s_mov_b32 s60, 65536\n\t" \
  "buffer_load_dwordx4 v16, %[W], s60 offen lds\n\t" \
  "s_add_u32 m0, s63, 2048\n\t" \
  "s_mov_b32 s60, 131072\n\t" \
  "buffer_load_dwordx4 v16, %[W], s60 offen lds\n\t" \
  "s_add_u32 m0, s63, 3072\n\t" \
  "s_mov_b32 s60, 196608\n\t" \
  "buffer_load_dwordx4 v16, %[W], s60 offen lds\n\t" \
  "s_add_u32 m0, s63, 4096\n\t" \
  "s_mov_b32 s60, 262144\n\t" \
  "buffer_load_dwordx4 v16, %[W], s60 offen lds\n\t" \
  "s_add_u32 m0, s63, 5120\n\t" \
  "s_mov_b32 s60, 327680\n\t" \
  "buffer_load_dwordx4 v16, %[W], s60 offen lds\n\t" \
  "s_add_u32 m0, s63, 6144\n\t" \
  "s_mov_b32 s60, 393216\n\t" \
  "buffer_load_dwordx4 v16, %[W], s60 offen lds\n\t" \
  "s_add_u32 m0, s63, 7168\n\t" \
  "s_mov_b32 s60, 458752\n\t" \
  "buffer_load_dwordx4 v16, %[W], s60 offen lds\n\t" \
  "s_add_u32 m0, s63, 8192\n\t" \
  "s_mov_b32 s60, 524288\n\t" \
  "buffer_load_dwordx4 v16, %[W], s60 offen lds\n\t" \
  "s_add_u32 m0, s63, 9216\n\t" \
  "s_mov_b32 s60, 589824\n\t" \
  "buffer_load_dwordx4 v16, %[W], s60 offen lds\n\t" \
  "s_add_u32 m0, s63, 10240\n\t" \
  "s_mov_b32 s60, 655360\n\t" \
  "buffer_load_dwordx4 v16, %[W], s60 offen lds\n\t" \
  "s_add_u32 m0, s63, 11264\n\t" \
  "s_mov_b32 s60, 720896\n\t" \
  "buffer_load_dwordx4 v16, %[W], s60 offen lds\n\t" \
  "s_add_u32 m0, s63, 12288\n\t" \
  "s_mov_b32 s60, 786432\n\t" \
  "buffer_load_dwordx4 v16, %[W], s60 offen lds\n\t" \
  "s_add_u32 m0, s63, 13312\n\t" \
  "s_mov_b32 s60, 851968\n\t" \
  "buffer_load_dwordx4 v16, %[W], s60 offen lds\n\t" \
  "s_add_u32 m0, s63, 14336\n\t" \
  "s_mov_b32 s60, 917504\n\t" \
  "buffer_load_dwordx4 v16, %[W], s60 offen lds\n\t" \
  "s_add_u32 m0, s63, 15360\n\t" \
  "s_mov_b32 s60, 983040\n\t" \
  "buffer_load_dwordx4 v16, %[W], s60 offen lds\n\t" \
  "s_add_u32 m0, s63, 16384\n\t" \
  "s_mov_b32 s60, 1048576\n\t" \
  "buffer_load_dwordx4 v16, %[W], s60 offen lds\n\t" \
  "s_add_u32 m0, s63, 17408\n\t" \
  "s_mov_b32 s60, 1114112\n\t" \
  "buffer_load_dwordx4 v16, %[W], s60 offen lds\n\t" \
  "s_add_u32 m0, s63, 18432\n\t" \
  "s_mov_b32 s60, 1179648\n\t" \
  "buffer_load_dwordx4 v16, %[W], s60 offen lds\n\t" \
  "s_add_u32 m0, s63, 19456\n\t" \
  "s_mov_b32 s60, 1245184\n\t" \
  "buffer_load_dwordx4 v16, %[W], s60 offen lds\n\t" \
  "s_add_u32 m0, s63, 20480\n\t" \
  "s_mov_b32 s60, 1310720\n\t" \
  "buffer_load_dwordx4 v16, %[W], s60 offen lds\n\t" \
  "s_add_u32 m0, s63, 21504\n\t" \
  "s_mov_b32 s60, 1376256\n\t" \
  "buffer_load_dwordx4 v16, %[W], s60 offen lds\n\t" \
  "s_add_u32 m0, s63, 22528\n\t" \
  "s_mov_b32 s60, 1441792\n\t" \
  "buffer_load_dwordx4 v16, %[W], s60 offen lds\n\t" \
  "s_add_u32 m0, s63, 23552\n\t" \
  "s_mov_b32 s60, 1507328\n\t" \
  "buffer_load_dwordx4 v16, %[W], s60 offen lds\n\t" \
  "s_add_u32 m0, s63, 24576\n\t" \
  "s_mov_b32 s60, 1572864\n\t" \
  "buffer_load_dwordx4 v16, %[W], s60 offen lds\n\t" \
  "s_add_u32 m0, s63, 25600\n\t" \
  "s_mov_b32 s60, 1638400\n\t" \
  "buffer_load_dwordx4 v16, %[W], s60 offen lds\n\t" \
  "s_add_u32 m0, s63, 26624\n\t" \
  "s_mov_b32 s60, 1703936\n\t" \
  "buffer_load_dwordx4 v16, %[W], s60 offen lds\n\t" \
  "s_add_u32 m0, s63, 27648\n\t" \
  "s_mov_b32 s60, 1769472\n\t" \
  "buffer_load_dwordx4 v16, %[W], s60 offen lds\n\t" \
  "s_add_u32 m0, s63, 28672\n\t" \
  "s_mov_b32 s60, 1835008\n\t" \
  "buffer_load_dwordx4 v16, %[W], s60 offen lds\n\t" \
  "s_add_u32 m0, s63, 29696\n\t" \
  "s_mov_b32 s60, 1900544\n\t" \
  "buffer_load_dwordx4 v16, %[W], s60 offen lds\n\t" \
  "s_add_u32 m0, s63, 30720\n\t" \
  "s_mov_b32 s60, 1966080\n\t" \
  "buffer_load_dwordx4 v16, %[W], s60 offen lds\n\t" \
  "s_add_u32 m0, s63, 31744\n\t" \
  "s_mov_b32 s60, 2031616\n\t" \
  "buffer_load_dwordx4 v16, %[W], s60 offen lds\n\t" \
  "s_add_u32 m0, s63, 32768\n\t" \
  "s_mov_b32 s60, 2097152\n\t" \
  "buffer_load_dwordx4 v16, %[W], s60 offen lds\n\t" \
  "s_add_u32 m0, s63, 33792\n\t" \
  "s_mov_b32 s60, 2162688\n\t" \
  "buffer_load_dwordx4 v16, %[W], s60 offen lds\n\t" \
  "s_add_u32 m0, s63, 34816\n\t" \
  "s_mov_b32 s60, 2228224\n\t" \
  "buffer_load_dwordx4 v16, %[W], s60 offen lds\n\t" \
  "s_add_u32 m0, s63, 35840\n\t" \
  "s_mov_b32 s60, 2293760\n\t" \
  "buffer_load_dwordx4 v16, %[W], s60 offen lds\n\t" \
  "s_add_u32 m0, s63, 36864\n\t" \
  "s_mov_b32 s60, 2359296\n\t" \
  "buffer_load_dwordx4 v16, %[W], s60 offen lds\n\t" \
  "s_add_u32 m0, s63, 37888\n\t" \
  "s_mov_b32 s60, 2424832\n\t" \
  "buffer_load_dwordx4 v16, %[W], s60 offen lds\n\t" \
  "s_add_u32 m0, s63, 38912\n\t" \
  "s_mov_b32 s60, 2490368\n\t" \
  "buffer_load_dwordx4 v16, %[W], s60 offen lds\n\t" \
  "s_add_u32 m0, s63, 39936\n\t" \
  "s_mov_b32 s60, 2555904\n\t" \
  "buffer_load_dwordx4 v16, %[W], s60 offen lds\n\t" \
  "s_waitcnt vmcnt(32)\n\t" \
  "ds_read_b64_tr_b16 a[0:1], v17 offset:0\n\t" \
  "ds_read_b64_tr_b16 a[2:3], v17 offset:512\n\t" \
  "ds_read_b64_tr_b16 a[4:5], v17 offset:4096\n\t" \
  "ds_read_b64_tr_b16 a[6:7], v17 offset:4608\n\t" \
  "ds_read_b64_tr_b16 a[72:73], v17 offset:8\n\t" \
  "ds_read_b64_tr_b16 a[74:75], v17 offset:520\n\t" \
  "ds_read_b64_tr_b16 a[76:77], v17 offset:4104\n\t" \
  "ds_read_b64_tr_b16 a[78:79], v17 offset:4616\n\t" \
  "ds_read_b64_tr_b16 a[144:145], v18 offset:0\n\t" \
  "ds_read_b64_tr_b16 a[146:147], v18 offset:512\n\t" \
  "ds_read_b64_tr_b16 a[148:149], v18 offset:4096\n\t" \
  "ds_read_b64_tr_b16 a[150:151], v18 offset:4608\n\t" \
  "ds_read_b64_tr_b16 a[216:217], v18 offset:8\n\t" \
  "ds_read_b64_tr_b16 a[218:219], v18 offset:520\n\t" \
  "ds_read_b64_tr_b16 a[220:221], v18 offset:4104\n\t" \
  "ds_read_b64_tr_b16 a[222:223], v18 offset:4616\n\t" \
  "s_waitcnt lgkmcnt(0)\n\t" \
  "s_add_u32 m0, s63, 0\n\t" \
  "s_mov_b32 s60, 2621440\n\t" \
  "buffer_load_dwordx4 v16, %[W], s60 offen lds\n\t" \
  "s_add_u32 m0, s63, 1024\n\t" \
  "s_mov_b32 s60, 2686976\n\t" \
  "buffer_load_dwordx4 v16, %[W], s60 offen lds\n\t" \
  "s_add_u32 m0, s63, 2048\n\t" \
  "s_mov_b32 s60, 2752512\n\t" \
  "buffer_load_dwordx4 v16, %[W], s60 offen lds\n\t" \
  "s_add_u32 m0, s63, 3072\n\t" \
  "s_mov_b32 s60, 2818048\n\t" \
  "buffer_load_dwordx4 v16, %[W], s60 offen lds\n\t" \
  "s_add_u32 m0, s63, 4096\n\t" \
  "s_mov_b32 s60, 2883584\n\t" \
  "buffer_load_dwordx4 v16, %[W], s60 offen lds\n\t" \
  "s_add_u32 m0, s63, 5120\n\t" \
  "s_mov_b32 s60, 2949120\n\t" \
  "buffer_load_dwordx4 v16, %[W], s60 offen lds\n\t" \
  "s_add_u32 m0, s63, 6144\n\t" \
  "s_mov_b32 s60, 3014656\n\t" \
  "buffer_load_dwordx4 v16, %[W], s60 offen lds\n\t" \
  "s_add_u32 m0, s63, 7168\n\t" \
  "s_mov_b32 s60, 3080192\n\t" \
  "buffer_load_dwordx4 v16, %[W], s60 offen lds\n\t" \
  "s_waitcnt vmcnt(32)\n\t" \
  "ds_read_b64_tr_b16 a[8:9], v17 offset:8192\n\t" \
  "ds_read_b64_tr_b16 a[10:11], v17 offset:8704\n\t" \
  "ds_read_b64_tr_b16 a[12:13], v17 offset:12288\n\t" \
  "ds_read_b64_tr_b16 a[14:15], v17 offset:12800\n\t" \
  "ds_read_b64_tr_b16 a[80:81], v17 offset:8200\n\t" \
  "ds_read_b64_tr_b16 a[82:83], v17 offset:8712\n\t" \
  "ds_read_b64_tr_b16 a[84:85], v17 offset:12296\n\t" \
  "ds_read_b64_tr_b16 a[86:87], v17 offset:12808\n\t" \
  "ds_read_b64_tr_b16 a[152:153], v18 offset:8192\n\t" \
  "ds_read_b64_tr_b16 a[154:155], v18 offset:8704\n\t" \
  "ds_read_b64_tr_b16 a[156:157], v18 offset:12288\n\t" \
  "ds_read_b64_tr_b16 a[158:159], v18 offset:12800\n\t" \
  "ds_read_b64_tr_b16 a[224:225], v18 offset:8200\n\t" \
  "ds_read_b64_tr_b16 a[226:227], v18 offset:8712\n\t" \
  "ds_read_b64_tr_b16 a[228:229], v18 offset:12296\n\t" \
  "ds_read_b64_tr_b16 a[230:231], v18 offset:12808\n\t" \
  "s_waitcnt lgkmcnt(0)\n\t" \
  "s_add_u32 m0, s63, 8192\n\t" \
  "s_mov_b32 s60, 3145728\n\t" \
  "buffer_load_dwordx4 v16, %[W], s60 offen lds\n\t" \
  "s_add_u32 m0, s63, 9216\n\t" \
  "s_mov_b32 s60, 3211264\n\t" \
  "buffer_load_dwordx4 v16, %[W], s60 offen lds\n\t" \
  "s_add_u32 m0, s63, 10240\n\t" \
  "s_mov_b32 s60, 3276800\n\t" \
  "buffer_load_dwordx4 v16, %[W], s60 offen lds\n\t" \
  "s_add_u32 m0, s63, 11264\n\t" \
  "s_mov_b32 s60, 3342336\n\t" \
  "buffer_load_dwordx4 v16, %[W], s60 offen lds\n\t" \
  "s_add_u32 m0, s63, 12288\n\t" \
  "s_mov_b32 s60, 3407872\n\t" \
  "buffer_load_dwordx4 v16, %[W], s60 offen lds\n\t" \
  "s_add_u32 m0, s63, 13312\n\t" \
  "s_mov_b32 s60, 3473408\n\t" \
  "buffer_load_dwordx4 v16, %[W], s60 offen lds\n\t" \
  "s_add_u32 m0, s63, 14336\n\t" \
  "s_mov_b32 s60, 3538944\n\t" \
  "buffer_load_dwordx4 v16, %[W], s60 offen lds\n\t" \
  "s_add_u32 m0, s63, 15360\n\t" \
  "s_mov_b32 s60, 3604480\n\t" \
  "buffer_load_dwordx4 v16, %[W], s60 offen lds\n\t" \
  "s_waitcnt vmcnt(32)\n\t" \
  "ds_read_b64_tr_b16 a[16:17], v17 offset:16384\n\t" \
  "ds_read_b64_tr_b16 a[18:19], v17 offset:16896\n\t" \
  "ds_read_b64_tr_b16 a[20:21], v17 offset:20480\n\t" \
  "ds_read_b64_tr_b16 a[22:23], v17 offset:20992\n\t" \
  "ds_read_b64_tr_b16 a[88:89], v17 offset:16392\n\t" \
  "ds_read_b64_tr_b16 a[90:91], v17 offset:16904\n\t" \
  "ds_read_b64_tr_b16 a[92:93], v17 offset:20488\n\t" \
  "ds_read_b64_tr_b16 a[94:95], v17 offset:21000\n\t" \
  "ds_read_b64_tr_b16 a[160:161], v18 offset:16384\n\t" \
  "ds_read_b64_tr_b16 a[162:163], v18 offset:16896\n\t" \
  "ds_read_b64_tr_b16 a[164:165], v18 offset:20480\n\t" \
  "ds_read_b64_tr_b16 a[166:167], v18 offset:20992\n\t" \
  "ds_read_b64_tr_b16 a[232:233], v18 offset:16392\n\t" \
  "ds_read_b64_tr_b16 a[234:235], v18 offset:16904\n\t" \
  "ds_read_b64_tr_b16 a[236:237], v18 offset:20488\n\t" \
  "ds_read_b64_tr_b16 a[238:239], v18 offset:21000\n\t" \
  "s_waitcnt lgkmcnt(0)\n\t" \
  "s_add_u32 m0, s63, 16384\n\t" \
  "s_mov_b32 s60, 3670016\n\t" \
  "buffer_load_dwordx4 v16, %[W], s60 offen lds\n\t" \
  "s_add_u32 m0, s63, 17408\n\t" \
  "s_mov_b32 s60, 3735552\n\t" \
  "buffer_load_dwordx4 v16, %[W], s60 offen lds\n\t" \
  "s_add_u32 m0, s63, 18432\n\t" \
  "s_mov_b32 s60, 3801088\n\t" \
  "buffer_load_dwordx4 v16, %[W], s60 offen lds\n\t" \
  "s_add_u32 m0, s63, 19456\n\t" \
  "s_mov_b32 s60, 3866624\n\t" \
  "buffer_load_dwordx4 v16, %[W], s60 offen lds\n\t" \
  "s_add_u32 m0, s63, 20480\n\t" \
  "s_mov_b32 s60, 3932160\n\t" \
  "buffer_load_dwordx4 v16, %[W], s60 offen lds\n\t" \
  "s_add_u32 m0, s63, 21504\n\t" \
  "s_mov_b32 s60, 3997696\n\t" \
  "buffer_load_dwordx4 v16, %[W], s60 offen lds\n\t" \
  "s_add_u32 m0, s63, 22528\n\t" \
  "s_mov_b32 s60, 4063232\n\t" \
  "buffer_load_dwordx4 v16, %[W], s60 offen lds\n\t" \
  "s_add_u32 m0, s63, 23552\n\t" \
  "s_mov_b32 s60, 4128768\n\t" \
  "buffer_load_dwordx4 v16, %[W], s60 offen lds\n\t" \
  "s_waitcnt vmcnt(32)\n\t" \
  "ds_read_b64_tr_b16 a[24:25], v17 offset:24576\n\t" \
  "ds_read_b64_tr_b16 a[26:27], v17 offset:25088\n\t" \
  "ds_read_b64_tr_b16 a[28:29], v17 offset:28672\n\t" \
  "ds_read_b64_tr_b16 a[30:31], v17 offset:29184\n\t" \
  "ds_read_b64_tr_b16 a[96:97], v17 offset:24584\n\t" \
  "ds_read_b64_tr_b16 a[98:99], v17 offset:25096\n\t" \
  "ds_read_b64_tr_b16 a[100:101], v17 offset:28680\n\t" \
  "ds_read_b64_tr_b16 a[102:103], v17 offset:29192\n\t" \
  "ds_read_b64_tr_b16 a[168:169], v18 offset:24576\n\t" \
  "ds_read_b64_tr_b16 a[170:171], v18 offset:25088\n\t" \
  "ds_read_b64_tr_b16 a[172:173], v18 offset:28672\n\t" \
  "ds_read_b64_tr_b16 a[174:175], v18 offset:29184\n\t" \
  "ds_read_b64_tr_b16 a[240:241], v18 offset:24584\n\t" \
  "ds_read_b64_tr_b16 a[242:243], v18 offset:25096\n\t" \
  "ds_read_b64_tr_b16 a[244:245], v18 offset:28680\n\t" \
  "ds_read_b64_tr_b16 a[246:247], v18 offset:29192\n\t" \
  "s_waitcnt lgkmcnt(0)\n\t" \
  "s_add_u32 m0, s63, 24576\n\t" \
  "s_mov_b32 s60, 4194304\n\t" \
  "buffer_load_dwordx4 v16, %[W], s60 offen lds\n\t" \
  "s_add_u32 m0, s63, 25600\n\t" \
  "s_mov_b32 s60, 4259840\n\t" \
  "buffer_load_dwordx4 v16, %[W], s60 offen lds\n\t" \
  "s_add_u32 m0, s63, 26624\n\t" \
  "s_mov_b32 s60, 4325376\n\t" \
  "buffer_load_dwordx4 v16, %[W], s60 offen lds\n\t" \
  "s_add_u32 m0, s63, 27648\n\t" \
  "s_mov_b32 s60, 4390912\n\t" \
  "buffer_load_dwordx4 v16, %[W], s60 offen lds\n\t" \
  "s_add_u32 m0, s63, 28672\n\t" \
  "s_mov_b32 s60, 4456448\n\t" \
  "buffer_load_dwordx4 v16, %[W], s60 offen lds\n\t" \
  "s_add_u32 m0, s63, 29696\n\t" \
  "s_mov_b32 s60, 4521984\n\t" \
  "buffer_load_dwordx4 v16, %[W], s60 offen lds\n\t" \
  "s_add_u32 m0, s63, 30720\n\t" \
  "s_mov_b32 s60, 4587520\n\t" \
  "buffer_load_dwordx4 v16, %[W], s60 offen lds\n\t" \
  "s_add_u32 m0, s63, 31744\n\t" \
  "s_mov_b32 s60, 4653056\n\t" \
  "buffer_load_dwordx4 v16, %[W], s60 offen lds\n\t" \
  "s_waitcnt vmcnt(32)\n\t" \
  "ds_read_b64_tr_b16 a[32:33], v17 offset:32768\n\t" \
  "ds_read_b64_tr_b16 a[34:35], v17 offset:33280\n\t" \
  "ds_read_b64_tr_b16 a[36:37], v17 offset:36864\n\t" \
  "ds_read_b64_tr_b16 a[38:39], v17 offset:37376\n\t" \
  "ds_read_b64_tr_b16 a[104:105], v17 offset:32776\n\t" \
  "ds_read_b64_tr_b16 a[106:107], v17 offset:33288\n\t" \
  "ds_read_b64_tr_b16 a[108:109], v17 offset:36872\n\t" \
  "ds_read_b64_tr_b16 a[110:111], v17 offset:37384\n\t" \
  "ds_read_b64_tr_b16 a[176:177], v18 offset:32768\n\t" \
  "ds_read_b64_tr_b16 a[178:179], v18 offset:33280\n\t" \
  "ds_read_b64_tr_b16 a[180:181], v18 offset:36864\n\t" \
  "ds_read_b64_tr_b16 a[182:183], v18 offset:37376\n\t" \
  "ds_read_b64_tr_b16 a[248:249], v18 offset:32776\n\t" \
  "ds_read_b64_tr_b16 a[250:251], v18 offset:33288\n\t" \
  "ds_read_b64_tr_b16 a[252:253], v18 offset:36872\n\t" \
  "ds_read_b64_tr_b16 a[254:255], v18 offset:37384\n\t" \
  "s_waitcnt lgkmcnt(0)\n\t" \
  "s_waitcnt vmcnt(24)\n\t" \
  "ds_read_b64_tr_b16 a[40:41], v17 offset:0\n\t" \
  "ds_read_b64_tr_b16 a[42:43], v17 offset:512\n\t" \
  "ds_read_b64_tr_b16 a[44:45], v17 offset:4096\n\t" \
  "ds_read_b64_tr_b16 a[46:47], v17 offset:4608\n\t" \
  "ds_read_b64_tr_b16 a[112:113], v17 offset:8\n\t" \
  "ds_read_b64_tr_b16 a[114:115], v17 offset:520\n\t" \
  "ds_read_b64_tr_b16 a[116:117], v17 offset:4104\n\t" \
  "ds_read_b64_tr_b16 a[118:119], v17 offset:4616\n\t" \
  "ds_read_b64_tr_b16 a[184:185], v18 offset:0\n\t" \
  "ds_read_b64_tr_b16 a[186:187], v18 offset:512\n\t" \
  "ds_read_b64_tr_b16 a[188:189], v18 offset:4096\n\t" \
  "ds_read_b64_tr_b16 a[190:191], v18 offset:4608\n\t" \
  "ds_read_b64_tr_b16 v[224:225], v18 offset:8\n\t" \
  "ds_read_b64_tr_b16 v[226:227], v18 offset:520\n\t" \
  "ds_read_b64_tr_b16 v[228:229], v18 offset:4104\n\t" \
  "ds_read_b64_tr_b16 v[230:231], v18 offset:4616\n\t" \
  "s_waitcnt lgkmcnt(0)\n\t" \
  "s_waitcnt vmcnt(16)\n\t" \
  "ds_read_b64_tr_b16 a[48:49], v17 offset:8192\n\t" \
  "ds_read_b64_tr_b16 a[50:51], v17 offset:8704\n\t" \
  "ds_read_b64_tr_b16 a[52:53], v17 offset:12288\n\t" \
  "ds_read_b64_tr_b16 a[54:55], v17 offset:12800\n\t" \
  "ds_read_b64_tr_b16 a[120:121], v17 offset:8200\n\t" \
  "ds_read_b64_tr_b16 a[122:123], v17 offset:8712\n\t" \
  "ds_read_b64_tr_b16 a[124:125], v17 offset:12296\n\t" \
  "ds_read_b64_tr_b16 a[126:127], v17 offset:12808\n\t" \
  "ds_read_b64_tr_b16 a[192:193], v18 offset:8192\n\t" \
  "ds_read_b64_tr_b16 a[194:195], v18 offset:8704\n\t" \
  "ds_read_b64_tr_b16 a[196:197], v18 offset:12288\n\t" \
  "ds_read_b64_tr_b16 a[198:199], v18 offset:12800\n\t" \
  "ds_read_b64_tr_b16 v[232:233], v18 offset:8200\n\t" \
  "ds_read_b64_tr_b16 v[234:235], v18 offset:8712\n\t" \
  "ds_read_b64_tr_b16 v[236:237], v18 offset:12296\n\t" \
  "ds_read_b64_tr_b16 v[238:239], v18 offset:12808\n\t" \
  "s_waitcnt lgkmcnt(0)\n\t" \
  "s_waitcnt vmcnt(8)\n\t" \
  "ds_read_b64_tr_b16 a[56:57], v17 offset:16384\n\t" \
  "ds_read_b64_tr_b16 a[58:59], v17 offset:16896\n\t" \
  "ds_read_b64_tr_b16 a[60:61], v17 offset:20480\n\t" \
  "ds_read_b64_tr_b16 a[62:63], v17 offset:20992\n\t" \
  "ds_read_b64_tr_b16 a[128:129], v17 offset:16392\n\t" \
  "ds_read_b64_tr_b16 a[130:131], v17 offset:16904\n\t" \
  "ds_read_b64_tr_b16 a[132:133], v17 offset:20488\n\t" \
  "ds_read_b64_tr_b16 a[134:135], v17 offset:21000\n\t" \
  "ds_read_b64_tr_b16 a[200:201], v18 offset:16384\n\t" \
  "ds_read_b64_tr_b16 a[202:203], v18 offset:16896\n\t" \
  "ds_read_b64_tr_b16 a[204:205], v18 offset:20480\n\t" \
  "ds_read_b64_tr_b16 a[206:207], v18 offset:20992\n\t" \
  "ds_read_b64_tr_b16 v[240:241], v18 offset:16392\n\t" \
  "ds_read_b64_tr_b16 v[242:243], v18 offset:16904\n\t" \
  "ds_read_b64_tr_b16 v[244:245], v18 offset:20488\n\t" \
  "ds_read_b64_tr_b16 v[246:247], v18 offset:21000\n\t" \
  "s_waitcnt lgkmcnt(0)\n\t" \
  "s_waitcnt vmcnt(0)\n\t" \
  "ds_read_b64_tr_b16 a[64:65], v17 offset:24576\n\t" \
  "ds_read_b64_tr_b16 a[66:67], v17 offset:25088\n\t" \
  "ds_read_b64_tr_b16 a[68:69], v17 offset:28672\n\t" \
  "ds_read_b64_tr_b16 a[70:71], v17 offset:29184\n\t" \
  "ds_read_b64_tr_b16 a[136:137], v17 offset:24584\n\t" \
  "ds_read_b64_tr_b16 a[138:139], v17 offset:25096\n\t" \
  "ds_read_b64_tr_b16 a[140:141], v17 offset:28680\n\t" \
  "ds_read_b64_tr_b16 a[142:143], v17 offset:29192\n\t" \
  "ds_read_b64_tr_b16 a[208:209], v18 offset:24576\n\t" \
  "ds_read_b64_tr_b16 a[210:211], v18 offset:25088\n\t" \
  "ds_read_b64_tr_b16 a[212:213], v18 offset:28672\n\t" \
  "ds_read_b64_tr_b16 a[214:215], v18 offset:29184\n\t" \
  "ds_read_b64_tr_b16 v[248:249], v18 offset:24584\n\t" \
  "ds_read_b64_tr_b16 v[250:251], v18 offset:25096\n\t" \
  "ds_read_b64_tr_b16 v[252:253], v18 offset:28680\n\t" \
  "ds_read_b64_tr_b16 v[254:255], v18 offset:29192\n\t" \
  "s_waitcnt lgkmcnt(0)\n\t" \
  "s_mov_b32 s84, " TK_CAP "\n\t" \
  "s_mov_b32 s48, 0\n\t" \
  "s_mov_b32 s49, 0xf0000000\n\t" \
  "s_mov_b32 s46, s41\n\t" \
  "s_mov_b32 s56, 0\n\t" \
  "s_add_u32 s83, s48, 0\n\t" \
  "s_lshr_b64 s[60:61], s[90:91], s83\n\t" \
  "s_and_b32 s60, s60, 1\n\t" \
  "s_lshl_b32 s60, s60, 13\n\t" \
  "s_add_u32 s83, s83, " TK_SG0 "\n\t" \
  "s_lshl_b32 s83, s83, 21\n\t" \
  "s_or_b32 s83, s83, s60\n\t" \
  "s_add_u32 s63, s48, s42\n\t" \
  "s_lshr_b64 s[60:61], s[90:91], s63\n\t" \
  "s_and_b32 s60, s60, 1\n\t" \
  "s_lshl_b32 s60, s60, 13\n\t" \
  "s_add_u32 s63, s63, " TK_SG0 "\n\t" \
  "s_lshl_b32 s63, s63, 21\n\t" \
  "s_or_b32 s63, s63, s60\n\t" \
  "s_add_u32 s62, s83, s39\n\t" \
  "s_add_u32 s63, s63, s43\n\t" \
  "s_add_i32 s60, s48, s42\n\t" \
  "s_cmp_lt_i32 s60, 0\n\t" \
  "s_cselect_b32 s63, s62, s63\n\t" \
  "v_add_u32 v7, s62, v3\n\t" \
  "s_mov_b32 exec_lo, 0\n\t" \
  "v_mov_b32 v7, s63\n\t" \
  "s_mov_b32 exec_lo, -1\n\t" \
  "LCOLD_%=:\n\t" \
  "s_waitcnt vmcnt(0)\n\t" \
  "s_mov_b64 exec, 1\n\t" \
  "buffer_store_dword v6, off, %[X], s49 sc1\n\t" \
  "s_mov_b64 exec, -1\n\t" \
  "s_mov_b32 s49, 0xf0000000\n\t" \
  "s_add_u32 s55, s83, s36\n\t" \
  "s_cmp_lg_u32 s40, 0\n\t" \
  "s_cbranch_scc1 LPH_%=x\n\t" \
  "s_mov_b32 s61, 0\n\t" \
  "LPH_%=:\n\t" \
  "buffer_load_dword v4, v7, %[X], 0 offen sc1\n\t" \
  "s_waitcnt vmcnt(0)\n\t" \
  "v_cmp_eq_u32 vcc, 0, v4\n\t" \
  "s_cbranch_vccz LPH_%=x\n\t" \
  "s_add_u32 s61, s61, 1\n\t" \
  "s_sleep " TK_PSH "\n\t" \
  "s_cmp_gt_u32 s61, s84\n\t" \
  "s_cbranch_scc1 LPH_%=g\n\t" \
  "s_and_b32 s60, s61, 1023\n\t" \
  "s_cmp_lg_u32 s60, 0\n\t" \
  "s_cbranch_scc1 LPH_%=\n\t" \
  "buffer_load_dword v4, off, %[X], s92 sc1\n\t" \
  "s_waitcnt vmcnt(0)\n\t" \
  "v_readfirstlane_b32 s60, v4\n\t" \
  "s_nop 0\n\t" \
  "s_cmp_eq_u32 s60, 0\n\t" \
  "s_cbranch_scc1 LPH_%=\n\t" \
  "LPH_%=g:\n\t" \
  "s_mov_b32 s84, 0\n\t" \
  "s_or_b32 s60, s48, 1610612736\n\t" \
  "v_mov_b32 v4, s60\n\t" \
  "s_mov_b64 exec, 1\n\t" \
  "buffer_store_dword v4, off, %[X], s92 sc1\n\t" \
  "s_mov_b64 exec, -1\n\t" \
  "LPH_%=x:\n\t" \
  TK_QDL \
  "s_barrier\n\t" \
  "s_add_u32 m0, s46, 0\n\t" \
  "s_add_u32 s47, s55, 0\n\t" \
  "buffer_load_dwordx4 v0, %[X], s47 offen sc1 lds\n\t" \
  "s_add_u32 m0, s46, 4096\n\t" \
  "s_add_u32 s47, s55, 4096\n\t" \
  "buffer_load_dwordx4 v0, %[X], s47 offen sc1 lds\n\t" \
  "s_add_u32 m0, s46, 8192\n\t" \
  "s_add_u32 s47, s55, 16384\n\t" \
  "buffer_load_dwordx4 v0, %[X], s47 offen sc1 lds\n\t" \
  "s_add_u32 m0, s46, 12288\n\t" \
  "s_add_u32 s47, s55, 20480\n\t" \
  "buffer_load_dwordx4 v0, %[X], s47 offen sc1 lds\n\t" \
  "s_add_u32 m0, s46, 16384\n\t" \
  "s_add_u32 s47, s55, 32768\n\t" \
  "buffer_load_dwordx4 v0, %[X], s47 offen sc1 lds\n\t" \
  "s_add_u32 m0, s46, 20480\n\t" \
  "s_add_u32 s47, s55, 36864\n\t" \
  "buffer_load_dwordx4 v0, %[X], s47 offen sc1 lds\n\t" \
  "s_add_u32 m0, s46, 24576\n\t" \
  "s_add_u32 s47, s55, 49152\n\t" \
  "buffer_load_dwordx4 v0, %[X], s47 offen sc1 lds\n\t" \
  "s_add_u32 m0, s46, 28672\n\t" \
  "s_add_u32 s47, s55, 53248\n\t" \
  "buffer_load_dwordx4 v0, %[X], s47 offen sc1 lds\n\t" \
  "s_add_u32 m0, s46, 32768\n\t" \
  "s_add_u32 s47, s55, 65536\n\t" \
  "buffer_load_dwordx4 v0, %[X], s47 offen sc1 lds\n\t" \
  "s_waitcnt vmcnt(5)\n\t" \
  "LTOP_%=:\n\t" \
  "s_barrier\n\t" \
  TK_PQ1 \
  "ds_read_b128 v[64:67], v1 offset:0\n\t" \
  "ds_read_b128 v[68:71], v1 offset:2048\n\t" \
  "ds_read_b128 v[72:75], v2 offset:0\n\t" \
  "ds_read_b128 v[76:79], v2 offset:2048\n\t" \
  "ds_read_b128 v[80:83], v1 offset:4096\n\t" \
  "ds_read_b128 v[84:87], v1 offset:6144\n\t" \
  "s_mov_b32 s82, s83\n\t" \
  "s_xor_b32 s46, s46, 0x10000\n\t" \
  "s_waitcnt lgkmcnt(5)\n\t" \
  "v_mfma_f32_16x16x32_bf16 v[16:19], a[0:3], v[64:67], 0\n\t" \
  "ds_read_b128 v[88:91], v2 offset:4096\n\t" \
  "v_mfma_f32_16x16x32_bf16 v[20:23], a[72:75], v[64:67], 0\n\t" \
  "s_add_u32 s83, s48, 1\n\t" \
  "s_lshr_b64 s[60:61], s[90:91], s83\n\t" \
  "s_and_b32 s60, s60, 1\n\t" \
  TK_QNP \
  "v_mfma_f32_16x16x32_bf16 v[24:27], a[144:147], v[64:67], 0\n\t" \
  "ds_read_b128 v[92:95], v2 offset:6144\n\t" \
  "v_mfma_f32_16x16x32_bf16 v[28:31], a[216:219], v[64:67], 0\n\t" \
  "s_lshl_b32 s60, s60, 13\n\t" \
  "s_add_u32 s83, s83, " TK_SG0 "\n\t" \
  "s_lshl_b32 s83, s83, 21\n\t" \
  TK_QNP \
  "s_waitcnt lgkmcnt(5)\n\t" \
  "v_mfma_f32_16x16x32_bf16 v[16:19], a[4:7], v[72:75], v[16:19]\n\t" \
  "ds_read_b128 v[96:99], v1 offset:8192\n\t" \
  "v_mfma_f32_16x16x32_bf16 v[20:23], a[76:79], v[72:75], v[20:23]\n\t" \
  "s_or_b32 s83, s83, s60\n\t" \
  "s_add_u32 s63, s48, s42\n\t" \
  "s_lshr_b64 s[60:61], s[90:91], s63\n\t" \
  TK_QNP \
  "v_mfma_f32_16x16x32_bf16 v[24:27], a[148:151], v[72:75], v[24:27]\n\t" \
  "ds_read_b128 v[100:103], v1 offset:10240\n\t" \
  "v_mfma_f32_16x16x32_bf16 v[28:31], a[220:223], v[72:75], v[28:31]\n\t" \
  "s_and_b32 s60, s60, 1\n\t" \
  "s_lshl_b32 s60, s60, 13\n\t" \
  "s_add_u32 s63, s63, " TK_SG0 "\n\t" \
  TK_QNP \
  "s_waitcnt lgkmcnt(5)\n\t" \
  "v_mfma_f32_16x16x32_bf16 v[16:19], a[8:11], v[80:83], v[16:19]\n\t" \
  "ds_read_b128 v[104:107], v2 offset:8192\n\t" \
  "v_mfma_f32_16x16x32_bf16 v[20:23], a[80:83], v[80:83], v[20:23]\n\t" \
  "s_lshl_b32 s63, s63, 21\n\t" \
  "s_or_b32 s63, s63, s60\n\t" \
  "s_add_u32 s62, s83, s39\n\t" \
  TK_QNP \
  "v_mfma_f32_16x16x32_bf16 v[24:27], a[152:155], v[80:83], v[24:27]\n\t" \
  "ds_read_b128 v[108:111], v2 offset:10240\n\t" \
  "v_mfma_f32_16x16x32_bf16 v[28:31], a[224:227], v[80:83], v[28:31]\n\t" \
  "s_add_u32 s63, s63, s43\n\t" \
  "s_add_i32 s60, s48, s42\n\t" \
  "s_cmp_lt_i32 s60, 0\n\t" \
  TK_QNP \
  "s_waitcnt lgkmcnt(5)\n\t" \
  "v_mfma_f32_16x16x32_bf16 v[16:19], a[12:15], v[88:91], v[16:19]\n\t" \
  "ds_read_b128 v[112:115], v1 offset:12288\n\t" \
  "v_mfma_f32_16x16x32_bf16 v[20:23], a[84:87], v[88:91], v[20:23]\n\t" \
  "s_cselect_b32 s63, s62, s63\n\t" \
  TK_QNP \
  "v_mfma_f32_16x16x32_bf16 v[24:27], a[156:159], v[88:91], v[24:27]\n\t" \
  "ds_read_b128 v[116:119], v1 offset:14336\n\t" \
  "v_mfma_f32_16x16x32_bf16 v[28:31], a[228:231], v[88:91], v[28:31]\n\t" \
  "v_add_u32 v7, s62, v3\n\t" \
  "s_mov_b32 exec_lo, 0\n\t" \
  "v_mov_b32 v7, s63\n\t" \
  "s_mov_b32 exec_lo, -1\n\t" \
  TK_QNP \
  "s_waitcnt lgkmcnt(5)\n\t" \
  "v_mfma_f32_16x16x32_bf16 v[16:19], a[16:19], v[96:99], v[16:19]\n\t" \
  "ds_read_b128 v[120:123], v2 offset:12288\n\t" \
  "v_mfma_f32_16x16x32_bf16 v[20:23], a[88:91], v[96:99], v[20:23]\n\t" \
  "s_add_u32 s50, s82, s37\n\t" \
  "s_add_u32 s51, s50, 4096\n\t" \
  "s_cmp_lg_u32 s56, 0\n\t" \
  TK_QNP \
  "v_mfma_f32_16x16x32_bf16 v[24:27], a[160:163], v[96:99], v[24:27]\n\t" \
  "ds_read_b128 v[124:127], v2 offset:14336\n\t" \
  "v_mfma_f32_16x16x32_bf16 v[28:31], a[232:235], v[96:99], v[28:31]\n\t" \
  "s_cbranch_scc1 LWARM_%=\n\t" \
  "s_waitcnt vmcnt(0)\n\t" \
  "LWARM_%=:\n\t" \
  "s_barrier\n\t" \
  TK_QNP \
  "s_waitcnt lgkmcnt(5)\n\t" \
  "v_mfma_f32_16x16x32_bf16 v[16:19], a[20:23], v[104:107], v[16:19]\n\t" \
  "ds_read_b128 v[128:131], v1 offset:16384\n\t" \
  "v_mfma_f32_16x16x32_bf16 v[20:23], a[92:95], v[104:107], v[20:23]\n\t" \
  "buffer_load_dword v4, v7, %[X], 0 offen sc1\n\t" \
  TK_QNP \
  "v_mfma_f32_16x16x32_bf16 v[24:27], a[164:167], v[104:107], v[24:27]\n\t" \
  "ds_read_b128 v[132:135], v1 offset:18432\n\t" \
  "v_mfma_f32_16x16x32_bf16 v[28:31], a[236:239], v[104:107], v[28:31]\n\t" \
  TK_QNP \
  "s_waitcnt lgkmcnt(5)\n\t" \
  "v_mfma_f32_16x16x32_bf16 v[16:19], a[24:27], v[112:115], v[16:19]\n\t" \
  "ds_read_b128 v[136:139], v2 offset:16384\n\t" \
  "v_mfma_f32_16x16x32_bf16 v[20:23], a[96:99], v[112:115], v[20:23]\n\t" \
  TK_QNP \
  "v_mfma_f32_16x16x32_bf16 v[24:27], a[168:171], v[112:115], v[24:27]\n\t" \
  "ds_read_b128 v[140:143], v2 offset:18432\n\t" \
  "v_mfma_f32_16x16x32_bf16 v[28:31], a[240:243], v[112:115], v[28:31]\n\t" \
  TK_QNP \
  "s_waitcnt lgkmcnt(5)\n\t" \
  "v_mfma_f32_16x16x32_bf16 v[16:19], a[28:31], v[120:123], v[16:19]\n\t" \
  "ds_read_b128 v[144:147], v1 offset:20480\n\t" \
  "v_mfma_f32_16x16x32_bf16 v[20:23], a[100:103], v[120:123], v[20:23]\n\t" \
  TK_QNP \
  "v_mfma_f32_16x16x32_bf16 v[24:27], a[172:175], v[120:123], v[24:27]\n\t" \
  "ds_read_b128 v[148:151], v1 offset:22528\n\t" \
  "v_mfma_f32_16x16x32_bf16 v[28:31], a[244:247], v[120:123], v[28:31]\n\t" \
  TK_QNP \
  "s_waitcnt lgkmcnt(5)\n\t" \
  "v_mfma_f32_16x16x32_bf16 v[16:19], a[32:35], v[128:131], v[16:19]\n\t" \
  "ds_read_b128 v[152:155], v2 offset:20480\n\t" \
  "v_mfma_f32_16x16x32_bf16 v[20:23], a[104:107], v[128:131], v[20:23]\n\t" \
  TK_QNP \
  "v_mfma_f32_16x16x32_bf16 v[24:27], a[176:179], v[128:131], v[24:27]\n\t" \
  "ds_read_b128 v[156:159], v2 offset:22528\n\t" \
  "v_mfma_f32_16x16x32_bf16 v[28:31], a[248:251], v[128:131], v[28:31]\n\t" \
  TK_QNP \
  "s_waitcnt lgkmcnt(5)\n\t" \
  "v_mfma_f32_16x16x32_bf16 v[16:19], a[36:39], v[136:139], v[16:19]\n\t" \
  "ds_read_b128 v[160:163], v1 offset:24576\n\t" \
  "v_mfma_f32_16x16x32_bf16 v[20:23], a[108:111], v[136:139], v[20:23]\n\t" \
  TK_QNP \
  "v_mfma_f32_16x16x32_bf16 v[24:27], a[180:183], v[136:139], v[24:27]\n\t" \
  "ds_read_b128 v[164:167], v1 offset:26624\n\t" \
  "v_mfma_f32_16x16x32_bf16 v[28:31], a[252:255], v[136:139], v[28:31]\n\t" \
  TK_QNP \
  "s_waitcnt lgkmcnt(5)\n\t" \
  "v_mfma_f32_16x16x32_bf16 v[16:19], a[40:43], v[144:147], v[16:19]\n\t" \
  "ds_read_b128 v[168:171], v2 offset:24576\n\t" \
  "v_mfma_f32_16x16x32_bf16 v[20:23], a[112:115], v[144:147], v[20:23]\n\t" \
  TK_QNP \
  "v_mfma_f32_16x16x32_bf16 v[24:27], a[184:187], v[144:147], v[24:27]\n\t" \
  "ds_read_b128 v[172:175], v2 offset:26624\n\t" \
  "v_mfma_f32_16x16x32_bf16 v[28:31], v[224:227], v[144:147], v[28:31]\n\t" \
  TK_QNP \
  "s_waitcnt lgkmcnt(5)\n\t" \
  "v_mfma_f32_16x16x32_bf16 v[16:19], a[44:47], v[152:155], v[16:19]\n\t" \
  "ds_read_b128 v[176:179], v1 offset:28672\n\t" \
  "v_mfma_f32_16x16x32_bf16 v[20:23], a[116:119], v[152:155], v[20:23]\n\t" \
  TK_QNP \
  "v_mfma_f32_16x16x32_bf16 v[24:27], a[188:191], v[152:155], v[24:27]\n\t" \
  "ds_read_b128 v[180:183], v1 offset:30720\n\t" \
  "v_mfma_f32_16x16x32_bf16 v[28:31], v[228:231], v[152:155], v[28:31]\n\t" \
  "s_waitcnt vmcnt(0)\n\t" \
  "v_cmp_eq_u32 vcc, 0, v4\n\t" \
  TK_QNP \
  "s_waitcnt lgkmcnt(5)\n\t" \
  "v_mfma_f32_16x16x32_bf16 v[16:19], a[48:51], v[160:163], v[16:19]\n\t" \
  "ds_read_b128 v[184:187], v2 offset:28672\n\t" \
  "v_mfma_f32_16x16x32_bf16 v[20:23], a[120:123], v[160:163], v[20:23]\n\t" \
  "s_mov_b64 exec, 1\n\t" \
  "buffer_store_dword v6, off, %[X], s49 sc1\n\t" \
  "s_mov_b64 exec, -1\n\t" \
  "s_mov_b32 s49, 0xf0000000\n\t" \
  TK_PQ0 \
  TK_QNP \
  "v_mfma_f32_16x16x32_bf16 v[24:27], a[192:195], v[160:163], v[24:27]\n\t" \
  "ds_read_b128 v[188:191], v2 offset:30720\n\t" \
  "v_mfma_f32_16x16x32_bf16 v[28:31], v[232:235], v[160:163], v[28:31]\n\t" \
  TK_HAVE \
  "s_cselect_b32 s60, 1, 0\n\t" \
  "v_mov_b32 v4, s60\n\t" \
  TK_QNP \
  "s_waitcnt lgkmcnt(5)\n\t" \
  "v_mfma_f32_16x16x32_bf16 v[16:19], a[52:55], v[168:171], v[16:19]\n\t" \
  "ds_read_b128 v[192:195], v1 offset:32768\n\t" \
  "v_mfma_f32_16x16x32_bf16 v[20:23], a[124:127], v[168:171], v[20:23]\n\t" \
  "ds_write_b32 v5, v4\n\t" \
  TK_QNP \
  "v_mfma_f32_16x16x32_bf16 v[24:27], a[196:199], v[168:171], v[24:27]\n\t" \
  "ds_read_b128 v[196:199], v1 offset:34816\n\t" \
  "v_mfma_f32_16x16x32_bf16 v[28:31], v[236:239], v[168:171], v[28:31]\n\t" \
  TK_QNP \
  "s_waitcnt lgkmcnt(6)\n\t" \
  "v_mfma_f32_16x16x32_bf16 v[16:19], a[56:59], v[176:179], v[16:19]\n\t" \
  "ds_read_b128 v[200:203], v2 offset:32768\n\t" \
  "v_mfma_f32_16x16x32_bf16 v[20:23], a[128:131], v[176:179], v[20:23]\n\t" \
  TK_QNP \
  "v_mfma_f32_16x16x32_bf16 v[24:27], a[200:203], v[176:179], v[24:27]\n\t" \
  "ds_read_b128 v[204:207], v2 offset:34816\n\t" \
  "v_mfma_f32_16x16x32_bf16 v[28:31], v[240:243], v[176:179], v[28:31]\n\t" \
  TK_QNP \
  "s_waitcnt lgkmcnt(6)\n\t" \
  "v_mfma_f32_16x16x32_bf16 v[16:19], a[60:63], v[184:187], v[16:19]\n\t" \
  "v_mfma_f32_16x16x32_bf16 v[20:23], a[132:135], v[184:187], v[20:23]\n\t" \
  TK_QNP \
  "v_mfma_f32_16x16x32_bf16 v[24:27], a[204:207], v[184:187], v[24:27]\n\t" \
  "v_mfma_f32_16x16x32_bf16 v[28:31], v[244:247], v[184:187], v[28:31]\n\t" \
  "s_waitcnt lgkmcnt(0)\n\t" \
  "s_barrier\n\t" \
  TK_QNP \
  "v_mfma_f32_16x16x32_bf16 v[16:19], a[64:67], v[192:195], v[16:19]\n\t" \
  "ds_read_b32 v4, v6 offset:40959\n\t" \
  "v_xor_b32 v1, 0x10000, v1\n\t" \
  "v_xor_b32 v2, 0x10000, v2\n\t" \
  "v_mfma_f32_16x16x32_bf16 v[20:23], a[136:139], v[192:195], v[20:23]\n\t" \
  TK_QNP \
  "v_mfma_f32_16x16x32_bf16 v[24:27], a[208:211], v[192:195], v[24:27]\n\t" \
  "v_mfma_f32_16x16x32_bf16 v[28:31], v[248:251], v[192:195], v[28:31]\n\t" \
  TK_QNP \
  "v_mfma_f32_16x16x32_bf16 v[16:19], a[68:71], v[200:203], v[16:19]\n\t" \
  "v_mfma_f32_16x16x32_bf16 v[20:23], a[140:143], v[200:203], v[20:23]\n\t" \
  "s_waitcnt lgkmcnt(0)\n\t" \
  "v_readfirstlane_b32 s60, v4\n\t" \
  "s_add_u32 s55, s83, s36\n\t" \
  TK_QNP \
  "v_mfma_f32_16x16x32_bf16 v[24:27], a[212:215], v[200:203], v[24:27]\n\t" \
  "s_cmp_eq_u32 s48, 63\n\t" \
  "s_cselect_b32 s56, 0, s60\n\t" \
  "v_mfma_f32_16x16x32_bf16 v[28:31], v[252:255], v[200:203], v[28:31]\n\t" \
  "s_cmp_eq_u32 s56, 0\n\t" \
  "s_cselect_b32 s55, 0xf0000000, s55\n\t" \
  TK_QNP \
  "v_mfma_f32_16x16x32_bf16 v[32:35], a[0:3], v[68:71], 0\n\t" \
  "v_cvt_pk_bf16_f32 v48, v16, v17\n\t" \
  "v_cvt_pk_bf16_f32 v49, v18, v19\n\t" \
  "v_mfma_f32_16x16x32_bf16 v[36:39], a[72:75], v[68:71], 0\n\t" \
  "v_cvt_pk_bf16_f32 v50, v20, v21\n\t" \
  "v_cvt_pk_bf16_f32 v51, v22, v23\n\t" \
  TK_QNP \
  "v_mfma_f32_16x16x32_bf16 v[32:35], a[4:7], v[76:79], v[32:35]\n\t" \
  "v_cvt_pk_bf16_f32 v52, v24, v25\n\t" \
  "v_cvt_pk_bf16_f32 v53, v26, v27\n\t" \
  "v_mfma_f32_16x16x32_bf16 v[36:39], a[76:79], v[76:79], v[36:39]\n\t" \
  "v_cvt_pk_bf16_f32 v54, v28, v29\n\t" \
  "v_cvt_pk_bf16_f32 v55, v30, v31\n\t" \
  TK_QNP \
  "v_mfma_f32_16x16x32_bf16 v[32:35], a[8:11], v[84:87], v[32:35]\n\t" \
  "buffer_store_dwordx4 v[48:51], v0, %[X], s50 offen offset:0 sc1\n\t" \
  "v_mfma_f32_16x16x32_bf16 v[36:39], a[80:83], v[84:87], v[36:39]\n\t" \
  TK_QNP \
  "v_mfma_f32_16x16x32_bf16 v[32:35], a[12:15], v[92:95], v[32:35]\n\t" \
  "s_add_u32 m0, s46, 0\n\t" \
  "s_add_u32 s47, s55, 0\n\t" \
  "buffer_load_dwordx4 v0, %[X], s47 offen sc1 lds\n\t" \
  "v_mfma_f32_16x16x32_bf16 v[36:39], a[84:87], v[92:95], v[36:39]\n\t" \
  TK_QNP \
  "v_mfma_f32_16x16x32_bf16 v[32:35], a[16:19], v[100:103], v[32:35]\n\t" \
  "v_mfma_f32_16x16x32_bf16 v[36:39], a[88:91], v[100:103], v[36:39]\n\t" \
  "s_add_u32 m0, s46, 4096\n\t" \
  "s_add_u32 s47, s55, 4096\n\t" \
  "buffer_load_dwordx4 v0, %[X], s47 offen sc1 lds\n\t" \
  TK_QNP \
  "v_mfma_f32_16x16x32_bf16 v[32:35], a[20:23], v[108:111], v[32:35]\n\t" \
  "v_mfma_f32_16x16x32_bf16 v[36:39], a[92:95], v[108:111], v[36:39]\n\t" \
  TK_QNP \
  "v_mfma_f32_16x16x32_bf16 v[32:35], a[24:27], v[116:119], v[32:35]\n\t" \
  "s_add_u32 m0, s46, 8192\n\t" \
  "s_add_u32 s47, s55, 16384\n\t" \
  "buffer_load_dwordx4 v0, %[X], s47 offen sc1 lds\n\t" \
  "v_mfma_f32_16x16x32_bf16 v[36:39], a[96:99], v[116:119], v[36:39]\n\t" \
  TK_QNP \
  "v_mfma_f32_16x16x32_bf16 v[32:35], a[28:31], v[124:127], v[32:35]\n\t" \
  "v_mfma_f32_16x16x32_bf16 v[36:39], a[100:103], v[124:127], v[36:39]\n\t" \
  "s_add_u32 m0, s46, 12288\n\t" \
  "s_add_u32 s47, s55, 20480\n\t" \
  "buffer_load_dwordx4 v0, %[X], s47 offen sc1 lds\n\t" \
  TK_QNP \
  "v_mfma_f32_16x16x32_bf16 v[32:35], a[32:35], v[132:135], v[32:35]\n\t" \
  "v_mfma_f32_16x16x32_bf16 v[36:39], a[104:107], v[132:135], v[36:39]\n\t" \
  TK_QNP \
  "v_mfma_f32_16x16x32_bf16 v[32:35], a[36:39], v[140:143], v[32:35]\n\t" \
  "s_add_u32 m0, s46, 16384\n\t" \
  "s_add_u32 s47, s55, 32768\n\t" \
  "buffer_load_dwordx4 v0, %[X], s47 offen sc1 lds\n\t" \
  "v_mfma_f32_16x16x32_bf16 v[36:39], a[108:111], v[140:143], v[36:39]\n\t" \
  TK_QNP \
  "v_mfma_f32_16x16x32_bf16 v[32:35], a[40:43], v[148:151], v[32:35]\n\t" \
  "v_mfma_f32_16x16x32_bf16 v[36:39], a[112:115], v[148:151], v[36:39]\n\t" \
  TK_QNP \
  "v_mfma_f32_16x16x32_bf16 v[32:35], a[44:47], v[156:159], v[32:35]\n\t" \
  "buffer_store_dwordx4 v[52:55], v0, %[X], s50 offen offset:1024 sc1\n\t" \
  "v_mfma_f32_16x16x32_bf16 v[36:39], a[116:119], v[156:159], v[36:39]\n\t" \
  TK_QNP \
  "v_mfma_f32_16x16x32_bf16 v[32:35], a[48:51], v[164:167], v[32:35]\n\t" \
  "v_mfma_f32_16x16x32_bf16 v[36:39], a[120:123], v[164:167], v[36:39]\n\t" \
  "s_add_u32 m0, s46, 20480\n\t" \
  "s_add_u32 s47, s55, 36864\n\t" \
  "buffer_load_dwordx4 v0, %[X], s47 offen sc1 lds\n\t" \
  TK_QNP \
  "v_mfma_f32_16x16x32_bf16 v[32:35], a[52:55], v[172:175], v[32:35]\n\t" \
  "v_mfma_f32_16x16x32_bf16 v[36:39], a[124:127], v[172:175], v[36:39]\n\t" \
  TK_QNP \
  "v_mfma_f32_16x16x32_bf16 v[32:35], a[56:59], v[180:183], v[32:35]\n\t" \
  "s_add_u32 m0, s46, 24576\n\t" \
  "s_add_u32 s47, s55, 49152\n\t" \
  "buffer_load_dwordx4 v0, %[X], s47 offen sc1 lds\n\t" \
  "v_mfma_f32_16x16x32_bf16 v[36:39], a[128:131], v[180:183], v[36:39]\n\t" \
  TK_QNP \
  "v_mfma_f32_16x16x32_bf16 v[32:35], a[60:63], v[188:191], v[32:35]\n\t" \
  "v_mfma_f32_16x16x32_bf16 v[36:39], a[132:135], v[188:191], v[36:39]\n\t" \
  "s_add_u32 m0, s46, 28672\n\t" \
  "s_add_u32 s47, s55, 53248\n\t" \
  "buffer_load_dwordx4 v0, %[X], s47 offen sc1 lds\n\t" \
  TK_QNP \
  "v_mfma_f32_16x16x32_bf16 v[32:35], a[64:67], v[196:199], v[32:35]\n\t" \
  "v_mfma_f32_16x16x32_bf16 v[36:39], a[136:139], v[196:199], v[36:39]\n\t" \
  TK_QNP \
  "v_mfma_f32_16x16x32_bf16 v[32:35], a[68:71], v[204:207], v[32:35]\n\t" \
  "s_add_u32 m0, s46, 32768\n\t" \
  "s_add_u32 s47, s55, 65536\n\t" \
  "buffer_load_dwordx4 v0, %[X], s47 offen sc1 lds\n\t" \
  "v_mfma_f32_16x16x32_bf16 v[36:39], a[140:143], v[204:207], v[36:39]\n\t" \
  TK_QNP \
  "v_mfma_f32_16x16x32_bf16 v[40:43], a[144:147], v[68:71], 0\n\t" \
  "v_mfma_f32_16x16x32_bf16 v[44:47], a[216:219], v[68:71], 0\n\t" \
  TK_QNP \
  "v_mfma_f32_16x16x32_bf16 v[40:43], a[148:151], v[76:79], v[40:43]\n\t" \
  "v_cvt_pk_bf16_f32 v56, v32, v33\n\t" \
  "v_cvt_pk_bf16_f32 v57, v34, v35\n\t" \
  "v_mfma_f32_16x16x32_bf16 v[44:47], a[220:223], v[76:79], v[44:47]\n\t" \
  "v_cvt_pk_bf16_f32 v58, v36, v37\n\t" \
  "v_cvt_pk_bf16_f32 v59, v38, v39\n\t" \
  TK_QNP \
  "v_mfma_f32_16x16x32_bf16 v[40:43], a[152:155], v[84:87], v[40:43]\n\t" \
  "v_mfma_f32_16x16x32_bf16 v[44:47], a[224:227], v[84:87], v[44:47]\n\t" \
  TK_QNP \
  "v_mfma_f32_16x16x32_bf16 v[40:43], a[156:159], v[92:95], v[40:43]\n\t" \
  "buffer_store_dwordx4 v[56:59], v0, %[X], s51 offen offset:0 sc1\n\t" \
  "v_mfma_f32_16x16x32_bf16 v[44:47], a[228:231], v[92:95], v[44:47]\n\t" \
  TK_QNP \
  "v_mfma_f32_16x16x32_bf16 v[40:43], a[160:163], v[100:103], v[40:43]\n\t" \
  "v_mfma_f32_16x16x32_bf16 v[44:47], a[232:235], v[100:103], v[44:47]\n\t" \
  TK_QNP \
  "v_mfma_f32_16x16x32_bf16 v[40:43], a[164:167], v[108:111], v[40:43]\n\t" \
  "v_mfma_f32_16x16x32_bf16 v[44:47], a[236:239], v[108:111], v[44:47]\n\t" \
  TK_QNP \
  "v_mfma_f32_16x16x32_bf16 v[40:43], a[168:171], v[116:119], v[40:43]\n\t" \
  "v_mfma_f32_16x16x32_bf16 v[44:47], a[240:243], v[116:119], v[44:47]\n\t" \
  TK_QNP \
  "v_mfma_f32_16x16x32_bf16 v[40:43], a[172:175], v[124:127], v[40:43]\n\t" \
  "v_mfma_f32_16x16x32_bf16 v[44:47], a[244:247], v[124:127], v[44:47]\n\t" \
  TK_QNP \
  "v_mfma_f32_16x16x32_bf16 v[40:43], a[176:179], v[132:135], v[40:43]\n\t" \
  "v_mfma_f32_16x16x32_bf16 v[44:47], a[248:251], v[132:135], v[44:47]\n\t" \
  TK_QNP \
  "v_mfma_f32_16x16x32_bf16 v[40:43], a[180:183], v[140:143], v[40:43]\n\t" \
  "v_mfma_f32_16x16x32_bf16 v[44:47], a[252:255], v[140:143], v[44:47]\n\t" \
  TK_QNP \
  "v_mfma_f32_16x16x32_bf16 v[40:43], a[184:187], v[148:151], v[40:43]\n\t" \
  "v_mfma_f32_16x16x32_bf16 v[44:47], v[224:227], v[148:151], v[44:47]\n\t" \
  TK_QNP \
  "v_mfma_f32_16x16x32_bf16 v[40:43], a[188:191], v[156:159], v[40:43]\n\t" \
  "v_mfma_f32_16x16x32_bf16 v[44:47], v[228:231], v[156:159], v[44:47]\n\t" \
  TK_QNP \
  "v_mfma_f32_16x16x32_bf16 v[40:43], a[192:195], v[164:167], v[40:43]\n\t" \
  "v_mfma_f32_16x16x32_bf16 v[44:47], v[232:235], v[164:167], v[44:47]\n\t" \
  TK_QNP \
  "v_mfma_f32_16x16x32_bf16 v[40:43], a[196:199], v[172:175], v[40:43]\n\t" \
  "v_mfma_f32_16x16x32_bf16 v[44:47], v[236:239], v[172:175], v[44:47]\n\t" \
  TK_QNP \
  "v_mfma_f32_16x16x32_bf16 v[40:43], a[200:203], v[180:183], v[40:43]\n\t" \
  "v_mfma_f32_16x16x32_bf16 v[44:47], v[240:243], v[180:183], v[44:47]\n\t" \
  TK_QNP \
  "v_mfma_f32_16x16x32_bf16 v[40:43], a[204:207], v[188:191], v[40:43]\n\t" \
  "v_mfma_f32_16x16x32_bf16 v[44:47], v[244:247], v[188:191], v[44:47]\n\t" \
  TK_QNP \
  "v_mfma_f32_16x16x32_bf16 v[40:43], a[208:211], v[196:199], v[40:43]\n\t" \
  "v_mfma_f32_16x16x32_bf16 v[44:47], v[248:251], v[196:199], v[44:47]\n\t" \
  TK_QNP \
  "v_mfma_f32_16x16x32_bf16 v[40:43], a[212:215], v[204:207], v[40:43]\n\t" \
  "v_mfma_f32_16x16x32_bf16 v[44:47], v[252:255], v[204:207], v[44:47]\n\t" \
  TK_QNP \
  "s_nop 4\n\t" \
  "v_cvt_pk_bf16_f32 v60, v40, v41\n\t" \
  "v_cvt_pk_bf16_f32 v61, v42, v43\n\t" \
  "s_nop 1\n\t" \
  "v_cvt_pk_bf16_f32 v62, v44, v45\n\t" \
  "v_cvt_pk_bf16_f32 v63, v46, v47\n\t" \
  "buffer_store_dwordx4 v[60:63], v0, %[X], s51 offen offset:1024 sc1\n\t" \
  TK_PQ2 \
  "s_add_u32 s49, s82, s38\n\t" \
  "s_cmp_eq_u32 s48, 63\n\t" \
  "s_cbranch_scc1 LEND_%=\n\t" \
  "s_add_u32 s48, s48, 1\n\t" \
  "s_cmp_eq_u32 s56, 0\n\t" \
  "s_cbranch_scc1 LCOLD_%=\n\t" \
  "s_waitcnt vmcnt(2)\n\t" \
  TK_PACE \
  "s_branch LTOP_%=\n\t" \
  "LPACE_%=:\n\t" \
  "s_waitcnt vmcnt(0)\n\t" \
  "s_mov_b64 exec, 1\n\t" \
  "buffer_store_dword v6, off, %[X], s49 sc1\n\t" \
  "s_mov_b64 exec, -1\n\t" \
  "s_mov_b32 s49, 0xf0000000\n\t" \
  "s_cmp_lg_u32 s40, 0\n\t" \
  "s_cbranch_scc1 LTOP_%=\n\t" \
  "s_mov_b32 s61, 0\n\t" \
  "LPP_%=:\n\t" \
  "buffer_load_dword v4, v7, %[X], 0 offen sc1\n\t" \
  "s_waitcnt vmcnt(0)\n\t" \
  "v_cmp_eq_u32 vcc, 0, v4\n\t" \
  "s_cbranch_vccz LPP_%=x\n\t" \
  "s_add_u32 s61, s61, 1\n\t" \
  "s_sleep " TK_PSH "\n\t" \
  "s_cmp_gt_u32 s61, s84\n\t" \
  "s_cbranch_scc1 LPP_%=g\n\t" \
  "s_and_b32 s60, s61, 1023\n\t" \
  "s_cmp_lg_u32 s60, 0\n\t" \
  "s_cbranch_scc1 LPP_%=\n\t" \
  "buffer_load_dword v4, off, %[X], s92 sc1\n\t" \
  "s_waitcnt vmcnt(0)\n\t" \
  "v_readfirstlane_b32 s60, v4\n\t" \
  "s_nop 0\n\t" \
  "s_cmp_eq_u32 s60, 0\n\t" \
  "s_cbranch_scc1 LPP_%=\n\t" \
  "LPP_%=g:\n\t" \
  "s_mov_b32 s84, 0\n\t" \
  "s_or_b32 s60, s48, 1879048192\n\t" \
  "v_mov_b32 v4, s60\n\t" \
  "s_mov_b64 exec, 1\n\t" \
  "buffer_store_dword v4, off, %[X], s92 sc1\n\t" \
  "s_mov_b64 exec, -1\n\t" \
  "LPP_%=x:\n\t" \
  TK_QDL2 \
  "s_branch LTOP_%=\n\t" \
  "LEND_%=:\n\t" \
  "s_waitcnt vmcnt(0)\n\t" \
  "s_mov_b64 exec, 1\n\t" \
  "buffer_store_dword v6, off, %[X], s49 sc1\n\t" \
  "s_mov_b64 exec, -1\n\t" \
  "s_mov_b32 s49, 0xf0000000\n\t" \
  TK_PQ0 \
  ""
#define QV_ASM \
  "v_lshrrev_b32 v16, 7, v0\n\t" \
  "v_lshlrev_b32 v16, 13, v16\n\t" \
  "v_lshrrev_b32 v19, 2, v0\n\t" \
  "v_and_b32 v19, 64, v19\n\t" \
  "v_xor_b32 v19, v19, v0\n\t" \
  "v_and_b32 v19, 0x70, v19\n\t" \
  "v_add_u32 v16, v16, v19\n\t" \
  "s_mul_i32 s63, s40, 40960\n\t" \
  "v_and_b32 v19, 0x300, v0\n\t" \
  "v_lshlrev_b32 v17, 2, v19\n\t" \
  "v_and_b32 v19, 0xc0, v0\n\t" \
  "v_lshl_add_u32 v17, v19, 1, v17\n\t" \
  "v_and_b32 v19, 0x30, v0\n\t" \
  "v_lshrrev_b32 v19, 1, v19\n\t" \
  "v_add3_u32 v17, v17, v19, s63\n\t" \
  "v_lshrrev_b32 v19, 1, v0\n\t" \
  "v_and_b32 v19, 64, v19\n\t" \
  "v_add_u32 v17, v17, v19\n\t" \
  "v_xor_b32 v18, 64, v17\n\t" \
  "s_add_u32 m0, s63, 0\n\t" \
  "s_mov_b32 s60, 0\n\t" \
  "buffer_load_dwordx4 v16, %[W], s60 offen lds\n\t" \
  "s_add_u32 m0, s63, 1024\n\t" \
  "s_mov_b32 s60, 65536\n\t" \
  "buffer_load_dwordx4 v16, %[W], s60 offen lds\n\t" \
  "s_add_u32 m0, s63, 2048\n\t" \
  "s_mov_b32 s60, 131072\n\t" \
  "buffer_load_dwordx4 v16, %[W], s60 offen lds\n\t" \
  "s_add_u32 m0, s63, 3072\n\t" \
  "s_mov_b32 s60, 196608\n\t" \
  "buffer_load_dwordx4 v16, %[W], s60 offen lds\n\t" \
  "s_add_u32 m0, s63, 4096\n\t" \
  "s_mov_b32 s60, 262144\n\t" \
  "buffer_load_dwordx4 v16, %[W], s60 offen lds\n\t" \
  "s_add_u32 m0, s63, 5120\n\t" \
  "s_mov_b32 s60, 327680\n\t" \
  "buffer_load_dwordx4 v16, %[W], s60 offen lds\n\t" \
  "s_add_u32 m0, s63, 6144\n\t" \
  "s_mov_b32 s60, 393216\n\t" \
  "buffer_load_dwordx4 v16, %[W], s60 offen lds\n\t" \
  "s_add_u32 m0, s63, 7168\n\t" \
  "s_mov_b32 s60, 458752\n\t" \
  "buffer_load_dwordx4 v16, %[W], s60 offen lds\n\t" \
  "s_add_u32 m0, s63, 8192\n\t" \
  "s_mov_b32 s60, 524288\n\t" \
  "buffer_load_dwordx4 v16, %[W], s60 offen lds\n\t" \
  "s_add_u32 m0, s63, 9216\n\t" \
  "s_mov_b32 s60, 589824\n\t" \
  "buffer_load_dwordx4 v16, %[W], s60 offen lds\n\t" \
  "s_add_u32 m0, s63, 10240\n\t" \
  "s_mov_b32 s60, 655360\n\t" \
  "buffer_load_dwordx4 v16, %[W], s60 offen lds\n\t" \
  "s_add_u32 m0, s63, 11264\n\t" \
  "s_mov_b32 s60, 720896\n\t" \
  "buffer_load_dwordx4 v16, %[W], s60 offen lds\n\t" \
  "s_add_u32 m0, s63, 12288\n\t" \
  "s_mov_b32 s60, 786432\n\t" \
  "buffer_load_dwordx4 v16, %[W], s60 offen lds\n\t" \
  "s_add_u32 m0, s63, 13312\n\t" \
  "s_mov_b32 s60, 851968\n\t" \
  "buffer_load_dwordx4 v16, %[W], s60 offen lds\n\t" \
  "s_add_u32 m0, s63, 14336\n\t" \
  "s_mov_b32 s60, 917504\n\t" \
  "buffer_load_dwordx4 v16, %[W], s60 offen lds\n\t" \
  "s_add_u32 m0, s63, 15360\n\t" \
  "s_mov_b32 s60, 983040\n\t" \
  "buffer_load_dwordx4 v16, %[W], s60 offen lds\n\t" \
  "s_add_u32 m0, s63, 16384\n\t" \
  "s_mov_b32 s60, 1048576\n\t" \
  "buffer_load_dwordx4 v16, %[W], s60 offen lds\n\t" \
  "s_add_u32 m0, s63, 17408\n\t" \
  "s_mov_b32 s60, 1114112\n\t" \
  "buffer_load_dwordx4 v16, %[W], s60 offen lds\n\t" \
  "s_add_u32 m0, s63, 18432\n\t" \
  "s_mov_b32 s60, 1179648\n\t" \
  "buffer_load_dwordx4 v16, %[W], s60 offen lds\n\t" \
  "s_add_u32 m0, s63, 19456\n\t" \
  "s_mov_b32 s60, 1245184\n\t" \
  "buffer_load_dwordx4 v16, %[W], s60 offen lds\n\t" \
  "s_add_u32 m0, s63, 20480\n\t" \
  "s_mov_b32 s60, 1310720\n\t" \
  "buffer_load_dwordx4 v16, %[W], s60 offen lds\n\t" \
  "s_add_u32 m0, s63, 21504\n\t" \
  "s_mov_b32 s60, 1376256\n\t" \
  "buffer_load_dwordx4 v16, %[W], s60 offen lds\n\t" \
  "s_add_u32 m0, s63, 22528\n\t" \
  "s_mov_b32 s60, 1441792\n\t" \
  "buffer_load_dwordx4 v16, %[W], s60 offen lds\n\t" \
  "s_add_u32 m0, s63, 23552\n\t" \
  "s_mov_b32 s60, 1507328\n\t" \
  "buffer_load_dwordx4 v16, %[W], s60 offen lds\n\t" \
  "s_add_u32 m0, s63, 24576\n\t" \
  "s_mov_b32 s60, 1572864\n\t" \
  "buffer_load_dwordx4 v16, %[W], s60 offen lds\n\t" \
  "s_add_u32 m0, s63, 25600\n\t" \
  "s_mov_b32 s60, 1638400\n\t" \
  "buffer_load_dwordx4 v16, %[W], s60 offen lds\n\t" \
  "s_add_u32 m0, s63, 26624\n\t" \
  "s_mov_b32 s60, 1703936\n\t" \
  "buffer_load_dwordx4 v16, %[W], s60 offen lds\n\t" \
  "s_add_u32 m0, s63, 27648\n\t" \
  "s_mov_b32 s60, 1769472\n\t" \
  "buffer_load_dwordx4 v16, %[W], s60 offen lds\n\t" \
  "s_add_u32 m0, s63, 28672\n\t" \
  "s_mov_b32 s60, 1835008\n\t" \
  "buffer_load_dwordx4 v16, %[W], s60 offen lds\n\t" \
  "s_add_u32 m0, s63, 29696\n\t" \
  "s_mov_b32 s60, 1900544\n\t" \
  "buffer_load_dwordx4 v16, %[W], s60 offen lds\n\t" \
  "s_add_u32 m0, s63, 30720\n\t" \
  "s_mov_b32 s60, 1966080\n\t" \
  "buffer_load_dwordx4 v16, %[W], s60 offen lds\n\t" \
  "s_add_u32 m0, s63, 31744\n\t" \
  "s_mov_b32 s60, 2031616\n\t" \
  "buffer_load_dwordx4 v16, %[W], s60 offen lds\n\t" \
  "s_add_u32 m0, s63, 32768\n\t" \
  "s_mov_b32 s60, 2097152\n\t" \
  "buffer_load_dwordx4 v16, %[W], s60 offen lds\n\t" \
  "s_add_u32 m0, s63, 33792\n\t" \
  "s_mov_b32 s60, 2162688\n\t" \
  "buffer_load_dwordx4 v16, %[W], s60 offen lds\n\t" \
  "s_add_u32 m0, s63, 34816\n\t" \
  "s_mov_b32 s60, 2228224\n\t" \
  "buffer_load_dwordx4 v16, %[W], s60 offen lds\n\t" \
  "s_add_u32 m0, s63, 35840\n\t" \
  "s_mov_b32 s60, 2293760\n\t" \
  "buffer_load_dwordx4 v16, %[W], s60 offen lds\n\t" \
  "s_add_u32 m0, s63, 36864\n\t" \
  "s_mov_b32 s60, 2359296\n\t" \
  "buffer_load_dwordx4 v16, %[W], s60 offen lds\n\t" \
  "s_add_u32 m0, s63, 37888\n\t" \
  "s_mov_b32 s60, 2424832\n\t" \
  "buffer_load_dwordx4 v16, %[W], s60 offen lds\n\t" \
  "s_add_u32 m0, s63, 38912\n\t" \
  "s_mov_b32 s60, 2490368\n\t" \
  "buffer_load_dwordx4 v16, %[W], s60 offen lds\n\t" \
  "s_add_u32 m0, s63, 39936\n\t" \
  "s_mov_b32 s60, 2555904\n\t" \
  "buffer_load_dwordx4 v16, %[W], s60 offen lds\n\t" \
  "s_waitcnt vmcnt(32)\n\t" \
  "ds_read_b64_tr_b16 a[0:1], v17 offset:0\n\t" \
  "ds_read_b64_tr_b16 a[2:3], v17 offset:512\n\t" \
  "ds_read_b64_tr_b16 a[4:5], v17 offset:4096\n\t" \
  "ds_read_b64_tr_b16 a[6:7], v17 offset:4608\n\t" \
  "ds_read_b64_tr_b16 a[72:73], v17 offset:32\n\t" \
  "ds_read_b64_tr_b16 a[74:75], v17 offset:544\n\t" \
  "ds_read_b64_tr_b16 a[76:77], v17 offset:4128\n\t" \
  "ds_read_b64_tr_b16 a[78:79], v17 offset:4640\n\t" \
  "ds_read_b64_tr_b16 a[144:145], v18 offset:0\n\t" \
  "ds_read_b64_tr_b16 a[146:147], v18 offset:512\n\t" \
  "ds_read_b64_tr_b16 a[148:149], v18 offset:4096\n\t" \
  "ds_read_b64_tr_b16 a[150:151], v18 offset:4608\n\t" \
  "ds_read_b64_tr_b16 a[216:217], v18 offset:32\n\t" \
  "ds_read_b64_tr_b16 a[218:219], v18 offset:544\n\t" \
  "ds_read_b64_tr_b16 a[220:221], v18 offset:4128\n\t" \
  "ds_read_b64_tr_b16 a[222:223], v18 offset:4640\n\t" \
  "s_waitcnt lgkmcnt(0)\n\t" \
  "s_add_u32 m0, s63, 0\n\t" \
  "s_mov_b32 s60, 2621440\n\t" \
  "buffer_load_dwordx4 v16, %[W], s60 offen lds\n\t" \
  "s_add_u32 m0, s63, 1024\n\t" \
  "s_mov_b32 s60, 2686976\n\t" \
  "buffer_load_dwordx4 v16, %[W], s60 offen lds\n\t" \
  "s_add_u32 m0, s63, 2048\n\t" \
  "s_mov_b32 s60, 2752512\n\t" \
  "buffer_load_dwordx4 v16, %[W], s60 offen lds\n\t" \
  "s_add_u32 m0, s63, 3072\n\t" \
  "s_mov_b32 s60, 2818048\n\t" \
  "buffer_load_dwordx4 v16, %[W], s60 offen lds\n\t" \
  "s_add_u32 m0, s63, 4096\n\t" \
  "s_mov_b32 s60, 2883584\n\t" \
  "buffer_load_dwordx4 v16, %[W], s60 offen lds\n\t" \
  "s_add_u32 m0, s63, 5120\n\t" \
  "s_mov_b32 s60, 2949120\n\t" \
  "buffer_load_dwordx4 v16, %[W], s60 offen lds\n\t" \
  "s_add_u32 m0, s63, 6144\n\t" \
  "s_mov_b32 s60, 3014656\n\t" \
  "buffer_load_dwordx4 v16, %[W], s60 offen lds\n\t" \
  "s_add_u32 m0, s63, 7168\n\t" \
  "s_mov_b32 s60, 3080192\n\t" \
  "buffer_load_dwordx4 v16, %[W], s60 offen lds\n\t" \
  "s_waitcnt vmcnt(32)\n\t" \
  "ds_read_b64_tr_b16 a[8:9], v17 offset:8192\n\t" \
  "ds_read_b64_tr_b16 a[10:11], v17 offset:8704\n\t" \
  "ds_read_b64_tr_b16 a[12:13], v17 offset:12288\n\t" \
  "ds_read_b64_tr_b16 a[14:15], v17 offset:12800\n\t" \
  "ds_read_b64_tr_b16 a[80:81], v17 offset:8224\n\t" \
  "ds_read_b64_tr_b16 a[82:83], v17 offset:8736\n\t" \
  "ds_read_b64_tr_b16 a[84:85], v17 offset:12320\n\t" \
  "ds_read_b64_tr_b16 a[86:87], v17 offset:12832\n\t" \
  "ds_read_b64_tr_b16 a[152:153], v18 offset:8192\n\t" \
  "ds_read_b64_tr_b16 a[154:155], v18 offset:8704\n\t" \
  "ds_read_b64_tr_b16 a[156:157], v18 offset:12288\n\t" \
  "ds_read_b64_tr_b16 a[158:159], v18 offset:12800\n\t" \
  "ds_read_b64_tr_b16 a[224:225], v18 offset:8224\n\t" \
  "ds_read_b64_tr_b16 a[226:227], v18 offset:8736\n\t" \
  "ds_read_b64_tr_b16 a[228:229], v18 offset:12320\n\t" \
  "ds_read_b64_tr_b16 a[230:231], v18 offset:12832\n\t" \
  "s_waitcnt lgkmcnt(0)\n\t" \
  "s_add_u32 m0, s63, 8192\n\t" \
  "s_mov_b32 s60, 3145728\n\t" \
  "buffer_load_dwordx4 v16, %[W], s60 offen lds\n\t" \
  "s_add_u32 m0, s63, 9216\n\t" \
  "s_mov_b32 s60, 3211264\n\t" \
  "buffer_load_dwordx4 v16, %[W], s60 offen lds\n\t" \
  "s_add_u32 m0, s63, 10240\n\t" \
  "s_mov_b32 s60, 3276800\n\t" \
  "buffer_load_dwordx4 v16, %[W], s60 offen lds\n\t" \
  "s_add_u32 m0, s63, 11264\n\t" \
  "s_mov_b32 s60, 3342336\n\t" \
  "buffer_load_dwordx4 v16, %[W], s60 offen lds\n\t" \
  "s_add_u32 m0, s63, 12288\n\t" \
  "s_mov_b32 s60, 3407872\n\t" \
  "buffer_load_dwordx4 v16, %[W], s60 offen lds\n\t" \
  "s_add_u32 m0, s63, 13312\n\t" \
  "s_mov_b32 s60, 3473408\n\t" \
  "buffer_load_dwordx4 v16, %[W], s60 offen lds\n\t" \
  "s_add_u32 m0, s63, 14336\n\t" \
  "s_mov_b32 s60, 3538944\n\t" \
  "buffer_load_dwordx4 v16, %[W], s60 offen lds\n\t" \
  "s_add_u32 m0, s63, 15360\n\t" \
  "s_mov_b32 s60, 3604480\n\t" \
  "buffer_load_dwordx4 v16, %[W], s60 offen lds\n\t" \
  "s_waitcnt vmcnt(32)\n\t" \
  "ds_read_b64_tr_b16 a[16:17], v17 offset:16384\n\t" \
  "ds_read_b64_tr_b16 a[18:19], v17 offset:16896\n\t" \
  "ds_read_b64_tr_b16 a[20:21], v17 offset:20480\n\t" \
  "ds_read_b64_tr_b16 a[22:23], v17 offset:20992\n\t" \
  "ds_read_b64_tr_b16 a[88:89], v17 offset:16416\n\t" \
  "ds_read_b64_tr_b16 a[90:91], v17 offset:16928\n\t" \
  "ds_read_b64_tr_b16 a[92:93], v17 offset:20512\n\t" \
  "ds_read_b64_tr_b16 a[94:95], v17 offset:21024\n\t" \
  "ds_read_b64_tr_b16 a[160:161], v18 offset:16384\n\t" \
  "ds_read_b64_tr_b16 a[162:163], v18 offset:16896\n\t" \
  "ds_read_b64_tr_b16 a[164:165], v18 offset:20480\n\t" \
  "ds_read_b64_tr_b16 a[166:167], v18 offset:20992\n\t" \
  "ds_read_b64_tr_b16 a[232:233], v18 offset:16416\n\t" \
  "ds_read_b64_tr_b16 a[234:235], v18 offset:16928\n\t" \
  "ds_read_b64_tr_b16 a[236:237], v18 offset:20512\n\t" \
  "ds_read_b64_tr_b16 a[238:239], v18 offset:21024\n\t" \
  "s_waitcnt lgkmcnt(0)\n\t" \
  "s_add_u32 m0, s63, 16384\n\t" \
  "s_mov_b32 s60, 3670016\n\t" \
  "buffer_load_dwordx4 v16, %[W], s60 offen lds\n\t" \
  "s_add_u32 m0, s63, 17408\n\t" \
  "s_mov_b32 s60, 3735552\n\t" \
  "buffer_load_dwordx4 v16, %[W], s60 offen lds\n\t" \
  "s_add_u32 m0, s63, 18432\n\t" \
  "s_mov_b32 s60, 3801088\n\t" \
  "buffer_load_dwordx4 v16, %[W], s60 offen lds\n\t" \
  "s_add_u32 m0, s63, 19456\n\t" \
  "s_mov_b32 s60, 3866624\n\t" \
  "buffer_load_dwordx4 v16, %[W], s60 offen lds\n\t" \
  "s_add_u32 m0, s63, 20480\n\t" \
  "s_mov_b32 s60, 3932160\n\t" \
  "buffer_load_dwordx4 v16, %[W], s60 offen lds\n\t" \
  "s_add_u32 m0, s63, 21504\n\t" \
  "s_mov_b32 s60, 3997696\n\t" \
  "buffer_load_dwordx4 v16, %[W], s60 offen lds\n\t" \
  "s_add_u32 m0, s63, 22528\n\t" \
  "s_mov_b32 s60, 4063232\n\t" \
  "buffer_load_dwordx4 v16, %[W], s60 offen lds\n\t" \
  "s_add_u32 m0, s63, 23552\n\t" \
  "s_mov_b32 s60, 4128768\n\t" \
  "buffer_load_dwordx4 v16, %[W], s60 offen lds\n\t" \
  "s_waitcnt vmcnt(32)\n\t" \
  "ds_read_b64_tr_b16 a[24:25], v17 offset:24576\n\t" \
  "ds_read_b64_tr_b16 a[26:27], v17 offset:25088\n\t" \
  "ds_read_b64_tr_b16 a[28:29], v17 offset:28672\n\t" \
  "ds_read_b64_tr_b16 a[30:31], v17 offset:29184\n\t" \
  "ds_read_b64_tr_b16 a[96:97], v17 offset:24608\n\t" \
  "ds_read_b64_tr_b16 a[98:99], v17 offset:25120\n\t" \
  "ds_read_b64_tr_b16 a[100:101], v17 offset:28704\n\t" \
  "ds_read_b64_tr_b16 a[102:103], v17 offset:29216\n\t" \
  "ds_read_b64_tr_b16 a[168:169], v18 offset:24576\n\t" \
  "ds_read_b64_tr_b16 a[170:171], v18 offset:25088\n\t" \
  "ds_read_b64_tr_b16 a[172:173], v18 offset:28672\n\t" \
  "ds_read_b64_tr_b16 a[174:175], v18 offset:29184\n\t" \
  "ds_read_b64_tr_b16 a[240:241], v18 offset:24608\n\t" \
  "ds_read_b64_tr_b16 a[242:243], v18 offset:25120\n\t" \
  "ds_read_b64_tr_b16 a[244:245], v18 offset:28704\n\t" \
  "ds_read_b64_tr_b16 a[246:247], v18 offset:29216\n\t" \
  "s_waitcnt lgkmcnt(0)\n\t" \
  "s_add_u32 m0, s63, 24576\n\t" \
  "s_mov_b32 s60, 4194304\n\t" \
  "buffer_load_dwordx4 v16, %[W], s60 offen lds\n\t" \
  "s_add_u32 m0, s63, 25600\n\t" \
  "s_mov_b32 s60, 4259840\n\t" \
  "buffer_load_dwordx4 v16, %[W], s60 offen lds\n\t" \
  "s_add_u32 m0, s63, 26624\n\t" \
  "s_mov_b32 s60, 4325376\n\t" \
  "buffer_load_dwordx4 v16, %[W], s60 offen lds\n\t" \
  "s_add_u32 m0, s63, 27648\n\t" \
  "s_mov_b32 s60, 4390912\n\t" \
  "buffer_load_dwordx4 v16, %[W], s60 offen lds\n\t" \
  "s_add_u32 m0, s63, 28672\n\t" \
  "s_mov_b32 s60, 4456448\n\t" \
  "buffer_load_dwordx4 v16, %[W], s60 offen lds\n\t" \
  "s_add_u32 m0, s63, 29696\n\t" \
  "s_mov_b32 s60, 4521984\n\t" \
  "buffer_load_dwordx4 v16, %[W], s60 offen lds\n\t" \
  "s_add_u32 m0, s63, 30720\n\t" \
  "s_mov_b32 s60, 4587520\n\t" \
  "buffer_load_dwordx4 v16, %[W], s60 offen lds\n\t" \
  "s_add_u32 m0, s63, 31744\n\t" \
  "s_mov_b32 s60, 4653056\n\t" \
  "buffer_load_dwordx4 v16, %[W], s60 offen lds\n\t" \
  "s_waitcnt vmcnt(32)\n\t" \
  "ds_read_b64_tr_b16 a[32:33], v17 offset:32768\n\t" \
  "ds_read_b64_tr_b16 a[34:35], v17 offset:33280\n\t" \
  "ds_read_b64_tr_b16 a[36:37], v17 offset:36864\n\t" \
  "ds_read_b64_tr_b16 a[38:39], v17 offset:37376\n\t" \
  "ds_read_b64_tr_b16 a[104:105], v17 offset:32800\n\t" \
  "ds_read_b64_tr_b16 a[106:107], v17 offset:33312\n\t" \
  "ds_read_b64_tr_b16 a[108:109], v17 offset:36896\n\t" \
  "ds_read_b64_tr_b16 a[110:111], v17 offset:37408\n\t" \
  "ds_read_b64_tr_b16 a[176:177], v18 offset:32768\n\t" \
  "ds_read_b64_tr_b16 a[178:179], v18 offset:33280\n\t" \
  "ds_read_b64_tr_b16 a[180:181], v18 offset:36864\n\t" \
  "ds_read_b64_tr_b16 a[182:183], v18 offset:37376\n\t" \
  "ds_read_b64_tr_b16 a[248:249], v18 offset:32800\n\t" \
  "ds_read_b64_tr_b16 a[250:251], v18 offset:33312\n\t" \
  "ds_read_b64_tr_b16 a[252:253], v18 offset:36896\n\t" \
  "ds_read_b64_tr_b16 a[254:255], v18 offset:37408\n\t" \
  "s_waitcnt lgkmcnt(0)\n\t" \
  "s_waitcnt vmcnt(24)\n\t" \
  "ds_read_b64_tr_b16 a[40:41], v17 offset:0\n\t" \
  "ds_read_b64_tr_b16 a[42:43], v17 offset:512\n\t" \
  "ds_read_b64_tr_b16 a[44:45], v17 offset:4096\n\t" \
  "ds_read_b64_tr_b16 a[46:47], v17 offset:4608\n\t" \
  "ds_read_b64_tr_b16 a[112:113], v17 offset:32\n\t" \
  "ds_read_b64_tr_b16 a[114:115], v17 offset:544\n\t" \
  "ds_read_b64_tr_b16 a[116:117], v17 offset:4128\n\t" \
  "ds_read_b64_tr_b16 a[118:119], v17 offset:4640\n\t" \
  "ds_read_b64_tr_b16 a[184:185], v18 offset:0\n\t" \
  "ds_read_b64_tr_b16 a[186:187], v18 offset:512\n\t" \
  "ds_read_b64_tr_b16 a[188:189], v18 offset:4096\n\t" \
  "ds_read_b64_tr_b16 a[190:191], v18 offset:4608\n\t" \
  "ds_read_b64_tr_b16 v[224:225], v18 offset:32\n\t" \
  "ds_read_b64_tr_b16 v[226:227], v18 offset:544\n\t" \
  "ds_read_b64_tr_b16 v[228:229], v18 offset:4128\n\t" \
  "ds_read_b64_tr_b16 v[230:231], v18 offset:4640\n\t" \
  "s_waitcnt lgkmcnt(0)\n\t" \
  "s_waitcnt vmcnt(16)\n\t" \
  "ds_read_b64_tr_b16 a[48:49], v17 offset:8192\n\t" \
  "ds_read_b64_tr_b16 a[50:51], v17 offset:8704\n\t" \
  "ds_read_b64_tr_b16 a[52:53], v17 offset:12288\n\t" \
  "ds_read_b64_tr_b16 a[54:55], v17 offset:12800\n\t" \
  "ds_read_b64_tr_b16 a[120:121], v17 offset:8224\n\t" \
  "ds_read_b64_tr_b16 a[122:123], v17 offset:8736\n\t" \
  "ds_read_b64_tr_b16 a[124:125], v17 offset:12320\n\t" \
  "ds_read_b64_tr_b16 a[126:127], v17 offset:12832\n\t" \
  "ds_read_b64_tr_b16 a[192:193], v18 offset:8192\n\t" \
  "ds_read_b64_tr_b16 a[194:195], v18 offset:8704\n\t" \
  "ds_read_b64_tr_b16 a[196:197], v18 offset:12288\n\t" \
  "ds_read_b64_tr_b16 a[198:199], v18 offset:12800\n\t" \
  "ds_read_b64_tr_b16 v[232:233], v18 offset:8224\n\t" \
  "ds_read_b64_tr_b16 v[234:235], v18 offset:8736\n\t" \
  "ds_read_b64_tr_b16 v[236:237], v18 offset:12320\n\t" \
  "ds_read_b64_tr_b16 v[238:239], v18 offset:12832\n\t" \
  "s_waitcnt lgkmcnt(0)\n\t" \
  "s_waitcnt vmcnt(8)\n\t" \
  "ds_read_b64_tr_b16 a[56:57], v17 offset:16384\n\t" \
  "ds_read_b64_tr_b16 a[58:59], v17 offset:16896\n\t" \
  "ds_read_b64_tr_b16 a[60:61], v17 offset:20480\n\t" \
  "ds_read_b64_tr_b16 a[62:63], v17 offset:20992\n\t" \
  "ds_read_b64_tr_b16 a[128:129], v17 offset:16416\n\t" \
  "ds_read_b64_tr_b16 a[130:131], v17 offset:16928\n\t" \
  "ds_read_b64_tr_b16 a[132:133], v17 offset:20512\n\t" \
  "ds_read_b64_tr_b16 a[134:135], v17 offset:21024\n\t" \
  "ds_read_b64_tr_b16 a[200:201], v18 offset:16384\n\t" \
  "ds_read_b64_tr_b16 a[202:203], v18 offset:16896\n\t" \
  "ds_read_b64_tr_b16 a[204:205], v18 offset:20480\n\t" \
  "ds_read_b64_tr_b16 a[206:207], v18 offset:20992\n\t" \
  "ds_read_b64_tr_b16 v[240:241], v18 offset:16416\n\t" \
  "ds_read_b64_tr_b16 v[242:243], v18 offset:16928\n\t" \
  "ds_read_b64_tr_b16 v[244:245], v18 offset:20512\n\t" \
  "ds_read_b64_tr_b16 v[246:247], v18 offset:21024\n\t" \
  "s_waitcnt lgkmcnt(0)\n\t" \
  "s_waitcnt vmcnt(0)\n\t" \
  "ds_read_b64_tr_b16 a[64:65], v17 offset:24576\n\t" \
  "ds_read_b64_tr_b16 a[66:67], v17 offset:25088\n\t" \
  "ds_read_b64_tr_b16 a[68:69], v17 offset:28672\n\t" \
  "ds_read_b64_tr_b16 a[70:71], v17 offset:29184\n\t" \
  "ds_read_b64_tr_b16 a[136:137], v17 offset:24608\n\t" \
  "ds_read_b64_tr_b16 a[138:139], v17 offset:25120\n\t" \
  "ds_read_b64_tr_b16 a[140:141], v17 offset:28704\n\t" \
  "ds_read_b64_tr_b16 a[142:143], v17 offset:29216\n\t" \
  "ds_read_b64_tr_b16 a[208:209], v18 offset:24576\n\t" \
  "ds_read_b64_tr_b16 a[210:211], v18 offset:25088\n\t" \
  "ds_read_b64_tr_b16 a[212:213], v18 offset:28672\n\t" \
  "ds_read_b64_tr_b16 a[214:215], v18 offset:29184\n\t" \
  "ds_read_b64_tr_b16 v[248:249], v18 offset:24608\n\t" \
  "ds_read_b64_tr_b16 v[250:251], v18 offset:25120\n\t" \
  "ds_read_b64_tr_b16 v[252:253], v18 offset:28704\n\t" \
  "ds_read_b64_tr_b16 v[254:255], v18 offset:29216\n\t" \
  "s_waitcnt lgkmcnt(0)\n\t" \
  "s_mov_b32 s84, " TK_CAP "\n\t" \
  "s_mov_b32 s48, 0\n\t" \
  "s_mov_b32 s49, 0xf0000000\n\t" \
  "s_mov_b32 s46, s41\n\t" \
  "s_mov_b32 s56, 0\n\t" \
  "s_add_u32 s83, s48, 0\n\t" \
  "s_lshr_b64 s[60:61], s[90:91], s83\n\t" \
  "s_and_b32 s60, s60, 1\n\t" \
  "s_lshl_b32 s60, s60, 13\n\t" \
  "s_add_u32 s83, s83, " TK_SG0 "\n\t" \
  "s_lshl_b32 s83, s83, 21\n\t" \
  "s_or_b32 s83, s83, s60\n\t" \
  "s_add_u32 s63, s48, s42\n\t" \
  "s_lshr_b64 s[60:61], s[90:91], s63\n\t" \
  "s_and_b32 s60, s60, 1\n\t" \
  "s_lshl_b32 s60, s60, 13\n\t" \
  "s_add_u32 s63, s63, " TK_SG0 "\n\t" \
  "s_lshl_b32 s63, s63, 21\n\t" \
  "s_or_b32 s63, s63, s60\n\t" \
  "s_add_u32 s62, s83, s39\n\t" \
  "s_add_u32 s63, s63, s43\n\t" \
  "s_add_i32 s60, s48, s42\n\t" \
  "s_cmp_lt_i32 s60, 0\n\t" \
  "s_cselect_b32 s63, s62, s63\n\t" \
  "v_add_u32 v7, s62, v3\n\t" \
  "s_mov_b32 exec_lo, 0\n\t" \
  "v_mov_b32 v7, s63\n\t" \
  "s_mov_b32 exec_lo, -1\n\t" \
  "LCOLD_%=:\n\t" \
  "s_waitcnt vmcnt(0)\n\t" \
  "s_mov_b64 exec, 1\n\t" \
  "buffer_store_dword v6, off, %[X], s49 sc1\n\t" \
  "s_mov_b64 exec, -1\n\t" \
  "s_mov_b32 s49, 0xf0000000\n\t" \
  "s_add_u32 s55, s83, s36\n\t" \
  "s_cmp_lg_u32 s40, 0\n\t" \
  "s_cbranch_scc1 LPH_%=x\n\t" \
  "s_mov_b32 s61, 0\n\t" \
  "LPH_%=:\n\t" \
  "buffer_load_dword v4, v7, %[X], 0 offen sc1\n\t" \
  "s_waitcnt vmcnt(0)\n\t" \
  "v_cmp_eq_u32 vcc, 0, v4\n\t" \
  "s_cbranch_vccz LPH_%=x\n\t" \
  "s_add_u32 s61, s61, 1\n\t" \
  "s_sleep " TK_PSH "\n\t" \
  "s_cmp_gt_u32 s61, s84\n\t" \
  "s_cbranch_scc1 LPH_%=g\n\t" \
  "s_and_b32 s60, s61, 1023\n\t" \
  "s_cmp_lg_u32 s60, 0\n\t" \
  "s_cbranch_scc1 LPH_%=\n\t" \
  "buffer_load_dword v4, off, %[X], s92 sc1\n\t" \
  "s_waitcnt vmcnt(0)\n\t" \
  "v_readfirstlane_b32 s60, v4\n\t" \
  "s_nop 0\n\t" \
  "s_cmp_eq_u32 s60, 0\n\t" \
  "s_cbranch_scc1 LPH_%=\n\t" \
  "LPH_%=g:\n\t" \
  "s_mov_b32 s84, 0\n\t" \
  "s_or_b32 s60, s48, 1610612736\n\t" \
  "v_mov_b32 v4, s60\n\t" \
  "s_mov_b64 exec, 1\n\t" \
  "buffer_store_dword v4, off, %[X], s92 sc1\n\t" \
  "s_mov_b64 exec, -1\n\t" \
  "LPH_%=x:\n\t" \
  TK_QDL \
  "s_barrier\n\t" \
  "s_add_u32 m0, s46, 0\n\t" \
  "s_add_u32 s47, s55, 0\n\t" \
  "buffer_load_dwordx4 v0, %[X], s47 offen sc1 lds\n\t" \
  "s_add_u32 m0, s46, 4096\n\t" \
  "s_add_u32 s47, s55, 4096\n\t" \
  "buffer_load_dwordx4 v0, %[X], s47 offen sc1 lds\n\t" \
  "s_add_u32 m0, s46, 8192\n\t" \
  "s_add_u32 s47, s55, 16384\n\t" \
  "buffer_load_dwordx4 v0, %[X], s47 offen sc1 lds\n\t" \
  "s_add_u32 m0, s46, 12288\n\t" \
  "s_add_u32 s47, s55, 20480\n\t" \
  "buffer_load_dwordx4 v0, %[X], s47 offen sc1 lds\n\t" \
  "s_add_u32 m0, s46, 16384\n\t" \
  "s_add_u32 s47, s55, 32768\n\t" \
  "buffer_load_dwordx4 v0, %[X], s47 offen sc1 lds\n\t" \
  "s_add_u32 m0, s46, 20480\n\t" \
  "s_add_u32 s47, s55, 36864\n\t" \
  "buffer_load_dwordx4 v0, %[X], s47 offen sc1 lds\n\t" \
  "s_add_u32 m0, s46, 24576\n\t" \
  "s_add_u32 s47, s55, 49152\n\t" \
  "buffer_load_dwordx4 v0, %[X], s47 offen sc1 lds\n\t" \
  "s_add_u32 m0, s46, 28672\n\t" \
  "s_add_u32 s47, s55, 53248\n\t" \
  "buffer_load_dwordx4 v0, %[X], s47 offen sc1 lds\n\t" \
  "s_add_u32 m0, s46, 32768\n\t" \
  "s_add_u32 s47, s55, 65536\n\t" \
  "buffer_load_dwordx4 v0, %[X], s47 offen sc1 lds\n\t" \
  "s_waitcnt vmcnt(5)\n\t" \
  "LTOP_%=:\n\t" \
  "s_barrier\n\t" \
  TK_PQ1 \
  "ds_read_b128 v[64:67], v1 offset:0\n\t" \
  "ds_read_b128 v[68:71], v1 offset:2048\n\t" \
  "ds_read_b128 v[72:75], v2 offset:0\n\t" \
  "ds_read_b128 v[76:79], v2 offset:2048\n\t" \
  "ds_read_b128 v[80:83], v1 offset:4096\n\t" \
  "ds_read_b128 v[84:87], v1 offset:6144\n\t" \
  "s_mov_b32 s82, s83\n\t" \
  "s_xor_b32 s46, s46, 0x10000\n\t" \
  "s_waitcnt lgkmcnt(5)\n\t" \
  "v_mfma_f32_16x16x32_bf16 v[16:19], v[64:67], a[0:3], 0\n\t" \
  "ds_read_b128 v[88:91], v2 offset:4096\n\t" \
  "s_waitcnt lgkmcnt(5)\n\t" \
  "v_mfma_f32_16x16x32_bf16 v[20:23], v[68:71], a[0:3], 0\n\t" \
  "s_add_u32 s83, s48, 1\n\t" \
  "s_lshr_b64 s[60:61], s[90:91], s83\n\t" \
  "s_and_b32 s60, s60, 1\n\t" \
  TK_QNP \
  "v_mfma_f32_16x16x32_bf16 v[24:27], v[64:67], a[72:75], 0\n\t" \
  "ds_read_b128 v[92:95], v2 offset:6144\n\t" \
  "v_mfma_f32_16x16x32_bf16 v[28:31], v[68:71], a[72:75], 0\n\t" \
  "s_lshl_b32 s60, s60, 13\n\t" \
  "s_add_u32 s83, s83, " TK_SG0 "\n\t" \
  "s_lshl_b32 s83, s83, 21\n\t" \
  TK_QNP \
  "s_waitcnt lgkmcnt(5)\n\t" \
  "v_mfma_f32_16x16x32_bf16 v[16:19], v[72:75], a[4:7], v[16:19]\n\t" \
  "ds_read_b128 v[96:99], v1 offset:8192\n\t" \
  "s_waitcnt lgkmcnt(5)\n\t" \
  "v_mfma_f32_16x16x32_bf16 v[20:23], v[76:79], a[4:7], v[20:23]\n\t" \
  "s_or_b32 s83, s83, s60\n\t" \
  "s_add_u32 s63, s48, s42\n\t" \
  "s_lshr_b64 s[60:61], s[90:91], s63\n\t" \
  TK_QNP \
  "v_mfma_f32_16x16x32_bf16 v[24:27], v[72:75], a[76:79], v[24:27]\n\t" \
  "ds_read_b128 v[100:103], v1 offset:10240\n\t" \
  "v_mfma_f32_16x16x32_bf16 v[28:31], v[76:79], a[76:79], v[28:31]\n\t" \
  "s_and_b32 s60, s60, 1\n\t" \
  "s_lshl_b32 s60, s60, 13\n\t" \
  "s_add_u32 s63, s63, " TK_SG0 "\n\t" \
  TK_QNP \
  "s_waitcnt lgkmcnt(5)\n\t" \
  "v_mfma_f32_16x16x32_bf16 v[16:19], v[80:83], a[8:11], v[16:19]\n\t" \
  "ds_read_b128 v[104:107], v2 offset:8192\n\t" \
  "s_waitcnt lgkmcnt(5)\n\t" \
  "v_mfma_f32_16x16x32_bf16 v[20:23], v[84:87], a[8:11], v[20:23]\n\t" \
  "s_lshl_b32 s63, s63, 21\n\t" \
  "s_or_b32 s63, s63, s60\n\t" \
  "s_add_u32 s62, s83, s39\n\t" \
  TK_QNP \
  "v_mfma_f32_16x16x32_bf16 v[24:27], v[80:83], a[80:83], v[24:27]\n\t" \
  "ds_read_b128 v[108:111], v2 offset:10240\n\t" \
  "v_mfma_f32_16x16x32_bf16 v[28:31], v[84:87], a[80:83], v[28:31]\n\t" \
  "s_add_u32 s63, s63, s43\n\t" \
  "s_add_i32 s60, s48, s42\n\t" \
  "s_cmp_lt_i32 s60, 0\n\t" \
  TK_QNP \
  "s_waitcnt lgkmcnt(5)\n\t" \
  "v_mfma_f32_16x16x32_bf16 v[16:19], v[88:91], a[12:15], v[16:19]\n\t" \
  "ds_read_b128 v[112:115], v1 offset:12288\n\t" \
  "s_waitcnt lgkmcnt(5)\n\t" \
  "v_mfma_f32_16x16x32_bf16 v[20:23], v[92:95], a[12:15], v[20:23]\n\t" \
  "s_cselect_b32 s63, s62, s63\n\t" \
  TK_QNP \
  "v_mfma_f32_16x16x32_bf16 v[24:27], v[88:91], a[84:87], v[24:27]\n\t" \
  "ds_read_b128 v[116:119], v1 offset:14336\n\t" \
  "v_mfma_f32_16x16x32_bf16 v[28:31], v[92:95], a[84:87], v[28:31]\n\t" \
  "v_add_u32 v7, s62, v3\n\t" \
  "s_mov_b32 exec_lo, 0\n\t" \
  "v_mov_b32 v7, s63\n\t" \
  "s_mov_b32 exec_lo, -1\n\t" \
  TK_QNP \
  "s_waitcnt lgkmcnt(5)\n\t" \
  "v_mfma_f32_16x16x32_bf16 v[16:19], v[96:99], a[16:19], v[16:19]\n\t" \
  "ds_read_b128 v[120:123], v2 offset:12288\n\t" \
  "s_waitcnt lgkmcnt(5)\n\t" \
  "v_mfma_f32_16x16x32_bf16 v[20:23], v[100:103], a[16:19], v[20:23]\n\t" \
  "s_add_u32 s50, s82, s37\n\t" \
  "s_add_u32 s51, s50, 4096\n\t" \
  "s_cmp_lg_u32 s56, 0\n\t" \
  TK_QNP \
  "v_mfma_f32_16x16x32_bf16 v[24:27], v[96:99], a[88:91], v[24:27]\n\t" \
  "ds_read_b128 v[124:127], v2 offset:14336\n\t" \
  "v_mfma_f32_16x16x32_bf16 v[28:31], v[100:103], a[88:91], v[28:31]\n\t" \
  "s_cbranch_scc1 LWARM_%=\n\t" \
  "s_waitcnt vmcnt(0)\n\t" \
  "LWARM_%=:\n\t" \
  "s_barrier\n\t" \
  TK_QNP \
  "s_waitcnt lgkmcnt(5)\n\t" \
  "v_mfma_f32_16x16x32_bf16 v[16:19], v[104:107], a[20:23], v[16:19]\n\t" \
  "ds_read_b128 v[128:131], v1 offset:16384\n\t" \
  "s_waitcnt lgkmcnt(5)\n\t" \
  "v_mfma_f32_16x16x32_bf16 v[20:23], v[108:111], a[20:23], v[20:23]\n\t" \
  "buffer_load_dword v4, v7, %[X], 0 offen sc1\n\t" \
  TK_QNP \
  "v_mfma_f32_16x16x32_bf16 v[24:27], v[104:107], a[92:95], v[24:27]\n\t" \
  "ds_read_b128 v[132:135], v1 offset:18432\n\t" \
  "v_mfma_f32_16x16x32_bf16 v[28:31], v[108:111], a[92:95], v[28:31]\n\t" \
  TK_QNP \
  "s_waitcnt lgkmcnt(5)\n\t" \
  "v_mfma_f32_16x16x32_bf16 v[16:19], v[112:115], a[24:27], v[16:19]\n\t" \
  "ds_read_b128 v[136:139], v2 offset:16384\n\t" \
  "s_waitcnt lgkmcnt(5)\n\t" \
  "v_mfma_f32_16x16x32_bf16 v[20:23], v[116:119], a[24:27], v[20:23]\n\t" \
  TK_QNP \
  "v_mfma_f32_16x16x32_bf16 v[24:27], v[112:115], a[96:99], v[24:27]\n\t" \
  "ds_read_b128 v[140:143], v2 offset:18432\n\t" \
  "v_mfma_f32_16x16x32_bf16 v[28:31], v[116:119], a[96:99], v[28:31]\n\t" \
  TK_QNP \
  "s_waitcnt lgkmcnt(5)\n\t" \
  "v_mfma_f32_16x16x32_bf16 v[16:19], v[120:123], a[28:31], v[16:19]\n\t" \
  "ds_read_b128 v[144:147], v1 offset:20480\n\t" \
  "s_waitcnt lgkmcnt(5)\n\t" \
  "v_mfma_f32_16x16x32_bf16 v[20:23], v[124:127], a[28:31], v[20:23]\n\t" \
  TK_QNP \
  "v_mfma_f32_16x16x32_bf16 v[24:27], v[120:123], a[100:103], v[24:27]\n\t" \
  "ds_read_b128 v[148:151], v1 offset:22528\n\t" \
  "v_mfma_f32_16x16x32_bf16 v[28:31], v[124:127], a[100:103], v[28:31]\n\t" \
  TK_QNP \
  "s_waitcnt lgkmcnt(5)\n\t" \
  "v_mfma_f32_16x16x32_bf16 v[16:19], v[128:131], a[32:35], v[16:19]\n\t" \
  "ds_read_b128 v[152:155], v2 offset:20480\n\t" \
  "s_waitcnt lgkmcnt(5)\n\t" \
  "v_mfma_f32_16x16x32_bf16 v[20:23], v[132:135], a[32:35], v[20:23]\n\t" \
  TK_QNP \
  "v_mfma_f32_16x16x32_bf16 v[24:27], v[128:131], a[104:107], v[24:27]\n\t" \
  "ds_read_b128 v[156:159], v2 offset:22528\n\t" \
  "v_mfma_f32_16x16x32_bf16 v[28:31], v[132:135], a[104:107], v[28:31]\n\t" \
  TK_QNP \
  "s_waitcnt lgkmcnt(5)\n\t" \
  "v_mfma_f32_16x16x32_bf16 v[16:19], v[136:139], a[36:39], v[16:19]\n\t" \
  "ds_read_b128 v[160:163], v1 offset:24576\n\t" \
  "s_waitcnt lgkmcnt(5)\n\t" \
  "v_mfma_f32_16x16x32_bf16 v[20:23], v[140:143], a[36:39], v[20:23]\n\t" \
  TK_QNP \
  "v_mfma_f32_16x16x32_bf16 v[24:27], v[136:139], a[108:111], v[24:27]\n\t" \
  "ds_read_b128 v[164:167], v1 offset:26624\n\t" \
  "v_mfma_f32_16x16x32_bf16 v[28:31], v[140:143], a[108:111], v[28:31]\n\t" \
  TK_QNP \
  "s_waitcnt lgkmcnt(5)\n\t" \
  "v_mfma_f32_16x16x32_bf16 v[16:19], v[144:147], a[40:43], v[16:19]\n\t" \
  "ds_read_b128 v[168:171], v2 offset:24576\n\t" \
  "s_waitcnt lgkmcnt(5)\n\t" \
  "v_mfma_f32_16x16x32_bf16 v[20:23], v[148:151], a[40:43], v[20:23]\n\t" \
  TK_QNP \
  "v_mfma_f32_16x16x32_bf16 v[24:27], v[144:147], a[112:115], v[24:27]\n\t" \
  "ds_read_b128 v[172:175], v2 offset:26624\n\t" \
  "v_mfma_f32_16x16x32_bf16 v[28:31], v[148:151], a[112:115], v[28:31]\n\t" \
  TK_QNP \
  "s_waitcnt lgkmcnt(5)\n\t" \
  "v_mfma_f32_16x16x32_bf16 v[16:19], v[152:155], a[44:47], v[16:19]\n\t" \
  "ds_read_b128 v[176:179], v1 offset:28672\n\t" \
  "s_waitcnt lgkmcnt(5)\n\t" \
  "v_mfma_f32_16x16x32_bf16 v[20:23], v[156:159], a[44:47], v[20:23]\n\t" \
  TK_QNP \
  "v_mfma_f32_16x16x32_bf16 v[24:27], v[152:155], a[116:119], v[24:27]\n\t" \
  "ds_read_b128 v[180:183], v1 offset:30720\n\t" \
  "v_mfma_f32_16x16x32_bf16 v[28:31], v[156:159], a[116:119], v[28:31]\n\t" \
  "s_waitcnt vmcnt(0)\n\t" \
  "v_cmp_eq_u32 vcc, 0, v4\n\t" \
  TK_QNP \
  "s_waitcnt lgkmcnt(5)\n\t" \
  "v_mfma_f32_16x16x32_bf16 v[16:19], v[160:163], a[48:51], v[16:19]\n\t" \
  "ds_read_b128 v[184:187], v2 offset:28672\n\t" \
  "s_waitcnt lgkmcnt(5)\n\t" \
  "v_mfma_f32_16x16x32_bf16 v[20:23], v[164:167], a[48:51], v[20:23]\n\t" \
  "s_mov_b64 exec, 1\n\t" \
  "buffer_store_dword v6, off, %[X], s49 sc1\n\t" \
  "s_mov_b64 exec, -1\n\t" \
  "s_mov_b32 s49, 0xf0000000\n\t" \
  TK_PQ0 \
  TK_QNP \
  "v_mfma_f32_16x16x32_bf16 v[24:27], v[160:163], a[120:123], v[24:27]\n\t" \
  "ds_read_b128 v[188:191], v2 offset:30720\n\t" \
  "v_mfma_f32_16x16x32_bf16 v[28:31], v[164:167], a[120:123], v[28:31]\n\t" \
  TK_HAVE \
  "s_cselect_b32 s60, 1, 0\n\t" \
  "v_mov_b32 v4, s60\n\t" \
  TK_QNP \
  "s_waitcnt lgkmcnt(5)\n\t" \
  "v_mfma_f32_16x16x32_bf16 v[16:19], v[168:171], a[52:55], v[16:19]\n\t" \
  "ds_read_b128 v[192:195], v1 offset:32768\n\t" \
  "s_waitcnt lgkmcnt(5)\n\t" \
  "v_mfma_f32_16x16x32_bf16 v[20:23], v[172:175], a[52:55], v[20:23]\n\t" \
  "ds_write_b32 v5, v4\n\t" \
  TK_QNP \
  "v_mfma_f32_16x16x32_bf16 v[24:27], v[168:171], a[124:127], v[24:27]\n\t" \
  "ds_read_b128 v[196:199], v1 offset:34816\n\t" \
  "v_mfma_f32_16x16x32_bf16 v[28:31], v[172:175], a[124:127], v[28:31]\n\t" \
  TK_QNP \
  "s_waitcnt lgkmcnt(6)\n\t" \
  "v_mfma_f32_16x16x32_bf16 v[16:19], v[176:179], a[56:59], v[16:19]\n\t" \
  "ds_read_b128 v[200:203], v2 offset:32768\n\t" \
  "s_waitcnt lgkmcnt(6)\n\t" \
  "v_mfma_f32_16x16x32_bf16 v[20:23], v[180:183], a[56:59], v[20:23]\n\t" \
  TK_QNP \
  "v_mfma_f32_16x16x32_bf16 v[24:27], v[176:179], a[128:131], v[24:27]\n\t" \
  "ds_read_b128 v[204:207], v2 offset:34816\n\t" \
  "v_mfma_f32_16x16x32_bf16 v[28:31], v[180:183], a[128:131], v[28:31]\n\t" \
  TK_QNP \
  "s_waitcnt lgkmcnt(6)\n\t" \
  "v_mfma_f32_16x16x32_bf16 v[16:19], v[184:187], a[60:63], v[16:19]\n\t" \
  "s_waitcnt lgkmcnt(5)\n\t" \
  "v_mfma_f32_16x16x32_bf16 v[20:23], v[188:191], a[60:63], v[20:23]\n\t" \
  TK_QNP \
  "v_mfma_f32_16x16x32_bf16 v[24:27], v[184:187], a[132:135], v[24:27]\n\t" \
  "v_mfma_f32_16x16x32_bf16 v[28:31], v[188:191], a[132:135], v[28:31]\n\t" \
  "s_waitcnt lgkmcnt(0)\n\t" \
  "s_barrier\n\t" \
  TK_QNP \
  "v_mfma_f32_16x16x32_bf16 v[16:19], v[192:195], a[64:67], v[16:19]\n\t" \
  "ds_read_b32 v4, v6 offset:40959\n\t" \
  "v_xor_b32 v1, 0x10000, v1\n\t" \
  "v_xor_b32 v2, 0x10000, v2\n\t" \
  "v_mfma_f32_16x16x32_bf16 v[20:23], v[196:199], a[64:67], v[20:23]\n\t" \
  TK_QNP \
  "v_mfma_f32_16x16x32_bf16 v[24:27], v[192:195], a[136:139], v[24:27]\n\t" \
  "v_mfma_f32_16x16x32_bf16 v[28:31], v[196:199], a[136:139], v[28:31]\n\t" \
  TK_QNP \
  "v_mfma_f32_16x16x32_bf16 v[16:19], v[200:203], a[68:71], v[16:19]\n\t" \
  "v_mfma_f32_16x16x32_bf16 v[20:23], v[204:207], a[68:71], v[20:23]\n\t" \
  "s_waitcnt lgkmcnt(0)\n\t" \
  "v_readfirstlane_b32 s60, v4\n\t" \
  "s_add_u32 s55, s83, s36\n\t" \
  TK_QNP \
  "v_mfma_f32_16x16x32_bf16 v[24:27], v[200:203], a[140:143], v[24:27]\n\t" \
  "s_cmp_eq_u32 s48, 63\n\t" \
  "s_cselect_b32 s56, 0, s60\n\t" \
  "v_mfma_f32_16x16x32_bf16 v[28:31], v[204:207], a[140:143], v[28:31]\n\t" \
  "s_cmp_eq_u32 s56, 0\n\t" \
  "s_cselect_b32 s55, 0xf0000000, s55\n\t" \
  TK_QNP \
  "v_mfma_f32_16x16x32_bf16 v[32:35], v[64:67], a[144:147], 0\n\t" \
  "v_cvt_pk_bf16_f32 v48, v16, v17\n\t" \
  "v_cvt_pk_bf16_f32 v49, v18, v19\n\t" \
  "v_mfma_f32_16x16x32_bf16 v[36:39], v[68:71], a[144:147], 0\n\t" \
  "v_cvt_pk_bf16_f32 v50, v20, v21\n\t" \
  "v_cvt_pk_bf16_f32 v51, v22, v23\n\t" \
  TK_QNP \
  "v_mfma_f32_16x16x32_bf16 v[32:35], v[72:75], a[148:151], v[32:35]\n\t" \
  "v_cvt_pk_bf16_f32 v52, v24, v25\n\t" \
  "v_cvt_pk_bf16_f32 v53, v26, v27\n\t" \
  "v_mfma_f32_16x16x32_bf16 v[36:39], v[76:79], a[148:151], v[36:39]\n\t" \
  "v_cvt_pk_bf16_f32 v54, v28, v29\n\t" \
  "v_cvt_pk_bf16_f32 v55, v30, v31\n\t" \
  TK_QNP \
  "v_mfma_f32_16x16x32_bf16 v[32:35], v[80:83], a[152:155], v[32:35]\n\t" \
  "buffer_store_dwordx4 v[48:51], v0, %[X], s50 offen offset:0 sc1\n\t" \
  "v_mfma_f32_16x16x32_bf16 v[36:39], v[84:87], a[152:155], v[36:39]\n\t" \
  TK_QNP \
  "v_mfma_f32_16x16x32_bf16 v[32:35], v[88:91], a[156:159], v[32:35]\n\t" \
  "s_add_u32 m0, s46, 0\n\t" \
  "s_add_u32 s47, s55, 0\n\t" \
  "buffer_load_dwordx4 v0, %[X], s47 offen sc1 lds\n\t" \
  "v_mfma_f32_16x16x32_bf16 v[36:39], v[92:95], a[156:159], v[36:39]\n\t" \
  TK_QNP \
  "v_mfma_f32_16x16x32_bf16 v[32:35], v[96:99], a[160:163], v[32:35]\n\t" \
  "v_mfma_f32_16x16x32_bf16 v[36:39], v[100:103], a[160:163], v[36:39]\n\t" \
  "s_add_u32 m0, s46, 4096\n\t" \
  "s_add_u32 s47, s55, 4096\n\t" \
  "buffer_load_dwordx4 v0, %[X], s47 offen sc1 lds\n\t" \
  TK_QNP \
  "v_mfma_f32_16x16x32_bf16 v[32:35], v[104:107], a[164:167], v[32:35]\n\t" \
  "v_mfma_f32_16x16x32_bf16 v[36:39], v[108:111], a[164:167], v[36:39]\n\t" \
  TK_QNP \
  "v_mfma_f32_16x16x32_bf16 v[32:35], v[112:115], a[168:171], v[32:35]\n\t" \
  "s_add_u32 m0, s46, 8192\n\t" \
  "s_add_u32 s47, s55, 16384\n\t" \
  "buffer_load_dwordx4 v0, %[X], s47 offen sc1 lds\n\t" \
  "v_mfma_f32_16x16x32_bf16 v[36:39], v[116:119], a[168:171], v[36:39]\n\t" \
  TK_QNP \
  "v_mfma_f32_16x16x32_bf16 v[32:35], v[120:123], a[172:175], v[32:35]\n\t" \
  "v_mfma_f32_16x16x32_bf16 v[36:39], v[124:127], a[172:175], v[36:39]\n\t" \
  "s_add_u32 m0, s46, 12288\n\t" \
  "s_add_u32 s47, s55, 20480\n\t" \
  "buffer_load_dwordx4 v0, %[X], s47 offen sc1 lds\n\t" \
  TK_QNP \
  "v_mfma_f32_16x16x32_bf16 v[32:35], v[128:131], a[176:179], v[32:35]\n\t" \
  "v_mfma_f32_16x16x32_bf16 v[36:39], v[132:135], a[176:179], v[36:39]\n\t" \
  TK_QNP \
  "v_mfma_f32_16x16x32_bf16 v[32:35], v[136:139], a[180:183], v[32:35]\n\t" \
  "s_add_u32 m0, s46, 16384\n\t" \
  "s_add_u32 s47, s55, 32768\n\t" \
  "buffer_load_dwordx4 v0, %[X], s47 offen sc1 lds\n\t" \
  "v_mfma_f32_16x16x32_bf16 v[36:39], v[140:143], a[180:183], v[36:39]\n\t" \
  TK_QNP \
  "v_mfma_f32_16x16x32_bf16 v[32:35], v[144:147], a[184:187], v[32:35]\n\t" \
  "v_mfma_f32_16x16x32_bf16 v[36:39], v[148:151], a[184:187], v[36:39]\n\t" \
  TK_QNP \
  "v_mfma_f32_16x16x32_bf16 v[32:35], v[152:155], a[188:191], v[32:35]\n\t" \
  "buffer_store_dwordx4 v[52:55], v0, %[X], s50 offen offset:1024 sc1\n\t" \
  "v_mfma_f32_16x16x32_bf16 v[36:39], v[156:159], a[188:191], v[36:39]\n\t" \
  TK_QNP \
  "v_mfma_f32_16x16x32_bf16 v[32:35], v[160:163], a[192:195], v[32:35]\n\t" \
  "v_mfma_f32_16x16x32_bf16 v[36:39], v[164:167], a[192:195], v[36:39]\n\t" \
  "s_add_u32 m0, s46, 20480\n\t" \
  "s_add_u32 s47, s55, 36864\n\t" \
  "buffer_load_dwordx4 v0, %[X], s47 offen sc1 lds\n\t" \
  TK_QNP \
  "v_mfma_f32_16x16x32_bf16 v[32:35], v[168:171], a[196:199], v[32:35]\n\t" \
  "v_mfma_f32_16x16x32_bf16 v[36:39], v[172:175], a[196:199], v[36:39]\n\t" \
  TK_QNP \
  "v_mfma_f32_16x16x32_bf16 v[32:35], v[176:179], a[200:203], v[32:35]\n\t" \
  "s_add_u32 m0, s46, 24576\n\t" \
  "s_add_u32 s47, s55, 49152\n\t" \
  "buffer_load_dwordx4 v0, %[X], s47 offen sc1 lds\n\t" \
  "v_mfma_f32_16x16x32_bf16 v[36:39], v[180:183], a[200:203], v[36:39]\n\t" \
  TK_QNP \
  "v_mfma_f32_16x16x32_bf16 v[32:35], v[184:187], a[204:207], v[32:35]\n\t" \
  "v_mfma_f32_16x16x32_bf16 v[36:39], v[188:191], a[204:207], v[36:39]\n\t" \
  "s_add_u32 m0, s46, 28672\n\t" \
  "s_add_u32 s47, s55, 53248\n\t" \
  "buffer_load_dwordx4 v0, %[X], s47 offen sc1 lds\n\t" \
  TK_QNP \
  "v_mfma_f32_16x16x32_bf16 v[32:35], v[192:195], a[208:211], v[32:35]\n\t" \
  "v_mfma_f32_16x16x32_bf16 v[36:39], v[196:199], a[208:211], v[36:39]\n\t" \
  TK_QNP \
  "v_mfma_f32_16x16x32_bf16 v[32:35], v[200:203], a[212:215], v[32:35]\n\t" \
  "s_add_u32 m0, s46, 32768\n\t" \
  "s_add_u32 s47, s55, 65536\n\t" \
  "buffer_load_dwordx4 v0, %[X], s47 offen sc1 lds\n\t" \
  "v_mfma_f32_16x16x32_bf16 v[36:39], v[204:207], a[212:215], v[36:39]\n\t" \
  TK_QNP \
  "v_mfma_f32_16x16x32_bf16 v[40:43], v[64:67], a[216:219], 0\n\t" \
  "v_mfma_f32_16x16x32_bf16 v[44:47], v[68:71], a[216:219], 0\n\t" \
  TK_QNP \
  "v_mfma_f32_16x16x32_bf16 v[40:43], v[72:75], a[220:223], v[40:43]\n\t" \
  "v_cvt_pk_bf16_f32 v56, v32, v33\n\t" \
  "v_cvt_pk_bf16_f32 v57, v34, v35\n\t" \
  "v_mfma_f32_16x16x32_bf16 v[44:47], v[76:79], a[220:223], v[44:47]\n\t" \
  "v_cvt_pk_bf16_f32 v58, v36, v37\n\t" \
  "v_cvt_pk_bf16_f32 v59, v38, v39\n\t" \
  TK_QNP \
  "v_mfma_f32_16x16x32_bf16 v[40:43], v[80:83], a[224:227], v[40:43]\n\t" \
  "v_mfma_f32_16x16x32_bf16 v[44:47], v[84:87], a[224:227], v[44:47]\n\t" \
  TK_QNP \
  "v_mfma_f32_16x16x32_bf16 v[40:43], v[88:91], a[228:231], v[40:43]\n\t" \
  "buffer_store_dwordx4 v[56:59], v0, %[X], s50 offen offset:2048 sc1\n\t" \
  "v_mfma_f32_16x16x32_bf16 v[44:47], v[92:95], a[228:231], v[44:47]\n\t" \
  TK_QNP \
  "v_mfma_f32_16x16x32_bf16 v[40:43], v[96:99], a[232:235], v[40:43]\n\t" \
  "v_mfma_f32_16x16x32_bf16 v[44:47], v[100:103], a[232:235], v[44:47]\n\t" \
  TK_QNP \
  "v_mfma_f32_16x16x32_bf16 v[40:43], v[104:107], a[236:239], v[40:43]\n\t" \
  "v_mfma_f32_16x16x32_bf16 v[44:47], v[108:111], a[236:239], v[44:47]\n\t" \
  TK_QNP \
  "v_mfma_f32_16x16x32_bf16 v[40:43], v[112:115], a[240:243], v[40:43]\n\t" \
  "v_mfma_f32_16x16x32_bf16 v[44:47], v[116:119], a[240:243], v[44:47]\n\t" \
  TK_QNP \
  "v_mfma_f32_16x16x32_bf16 v[40:43], v[120:123], a[244:247], v[40:43]\n\t" \
  "v_mfma_f32_16x16x32_bf16 v[44:47], v[124:127], a[244:247], v[44:47]\n\t" \
  TK_QNP \
  "v_mfma_f32_16x16x32_bf16 v[40:43], v[128:131], a[248:251], v[40:43]\n\t" \
  "v_mfma_f32_16x16x32_bf16 v[44:47], v[132:135], a[248:251], v[44:47]\n\t" \
  TK_QNP \
  "v_mfma_f32_16x16x32_bf16 v[40:43], v[136:139], a[252:255], v[40:43]\n\t" \
  "v_mfma_f32_16x16x32_bf16 v[44:47], v[140:143], a[252:255], v[44:47]\n\t" \
  TK_QNP \
  "v_mfma_f32_16x16x32_bf16 v[40:43], v[144:147], v[224:227], v[40:43]\n\t" \
  "v_mfma_f32_16x16x32_bf16 v[44:47], v[148:151], v[224:227], v[44:47]\n\t" \
  TK_QNP \
  "v_mfma_f32_16x16x32_bf16 v[40:43], v[152:155], v[228:231], v[40:43]\n\t" \
  "v_mfma_f32_16x16x32_bf16 v[44:47], v[156:159], v[228:231], v[44:47]\n\t" \
  TK_QNP \
  "v_mfma_f32_16x16x32_bf16 v[40:43], v[160:163], v[232:235], v[40:43]\n\t" \
  "v_mfma_f32_16x16x32_bf16 v[44:47], v[164:167], v[232:235], v[44:47]\n\t" \
  TK_QNP \
  "v_mfma_f32_16x16x32_bf16 v[40:43], v[168:171], v[236:239], v[40:43]\n\t" \
  "v_mfma_f32_16x16x32_bf16 v[44:47], v[172:175], v[236:239], v[44:47]\n\t" \
  TK_QNP \
  "v_mfma_f32_16x16x32_bf16 v[40:43], v[176:179], v[240:243], v[40:43]\n\t" \
  "v_mfma_f32_16x16x32_bf16 v[44:47], v[180:183], v[240:243], v[44:47]\n\t" \
  TK_QNP \
  "v_mfma_f32_16x16x32_bf16 v[40:43], v[184:187], v[244:247], v[40:43]\n\t" \
  "v_mfma_f32_16x16x32_bf16 v[44:47], v[188:191], v[244:247], v[44:47]\n\t" \
  TK_QNP \
  "v_mfma_f32_16x16x32_bf16 v[40:43], v[192:195], v[248:251], v[40:43]\n\t" \
  "v_mfma_f32_16x16x32_bf16 v[44:47], v[196:199], v[248:251], v[44:47]\n\t" \
  TK_QNP \
  "v_mfma_f32_16x16x32_bf16 v[40:43], v[200:203], v[252:255], v[40:43]\n\t" \
  "v_mfma_f32_16x16x32_bf16 v[44:47], v[204:207], v[252:255], v[44:47]\n\t" \
  TK_QNP \
  "s_nop 4\n\t" \
  "v_cvt_pk_bf16_f32 v60, v40, v41\n\t" \
  "v_cvt_pk_bf16_f32 v61, v42, v43\n\t" \
  "s_nop 1\n\t" \
  "v_cvt_pk_bf16_f32 v62, v44, v45\n\t" \
  "v_cvt_pk_bf16_f32 v63, v46, v47\n\t" \
  "buffer_store_dwordx4 v[60:63], v0, %[X], s50 offen offset:3072 sc1\n\t" \
  TK_PQ2 \
  "s_add_u32 s49, s82, s38\n\t" \
  "s_cmp_eq_u32 s48, 63\n\t" \
  "s_cbranch_scc1 LEND_%=\n\t" \
  "s_add_u32 s48, s48, 1\n\t" \
  "s_cmp_eq_u32 s56, 0\n\t" \
  "s_cbranch_scc1 LCOLD_%=\n\t" \
  "s_waitcnt vmcnt(2)\n\t" \
  TK_PACE \
  "s_branch LTOP_%=\n\t" \
  "LPACE_%=:\n\t" \
  "s_waitcnt vmcnt(0)\n\t" \
  "s_mov_b64 exec, 1\n\t" \
  "buffer_store_dword v6, off, %[X], s49 sc1\n\t" \
  "s_mov_b64 exec, -1\n\t" \
  "s_mov_b32 s49, 0xf0000000\n\t" \
  "s_cmp_lg_u32 s40, 0\n\t" \
  "s_cbranch_scc1 LTOP_%=\n\t" \
  "s_mov_b32 s61, 0\n\t" \
  "LPP_%=:\n\t" \
  "buffer_load_dword v4, v7, %[X], 0 offen sc1\n\t" \
  "s_waitcnt vmcnt(0)\n\t" \
  "v_cmp_eq_u32 vcc, 0, v4\n\t" \
  "s_cbranch_vccz LPP_%=x\n\t" \
  "s_add_u32 s61, s61, 1\n\t" \
  "s_sleep " TK_PSH "\n\t" \
  "s_cmp_gt_u32 s61, s84\n\t" \
  "s_cbranch_scc1 LPP_%=g\n\t" \
  "s_and_b32 s60, s61, 1023\n\t" \
  "s_cmp_lg_u32 s60, 0\n\t" \
  "s_cbranch_scc1 LPP_%=\n\t" \
  "buffer_load_dword v4, off, %[X], s92 sc1\n\t" \
  "s_waitcnt vmcnt(0)\n\t" \
  "v_readfirstlane_b32 s60, v4\n\t" \
  "s_nop 0\n\t" \
  "s_cmp_eq_u32 s60, 0\n\t" \
  "s_cbranch_scc1 LPP_%=\n\t" \
  "LPP_%=g:\n\t" \
  "s_mov_b32 s84, 0\n\t" \
  "s_or_b32 s60, s48, 1879048192\n\t" \
  "v_mov_b32 v4, s60\n\t" \
  "s_mov_b64 exec, 1\n\t" \
  "buffer_store_dword v4, off, %[X], s92 sc1\n\t" \
  "s_mov_b64 exec, -1\n\t" \
  "LPP_%=x:\n\t" \
  TK_QDL2 \
  "s_branch LTOP_%=\n\t" \
  "LEND_%=:\n\t" \
  "s_waitcnt vmcnt(0)\n\t" \
  "s_mov_b64 exec, 1\n\t" \
  "buffer_store_dword v6, off, %[X], s49 sc1\n\t" \
  "s_mov_b64 exec, -1\n\t" \
  "s_mov_b32 s49, 0xf0000000\n\t" \
  TK_PQ0 \
  ""
__device__ __forceinline__ unsigned hoff(int r, int piece) {
  const int m = 4 * (r >> 3) + (r & 3), kp = piece >> 3;
  return (P_H + (unsigned)(kp >> 1)) * PAIR + (unsigned)((kp & 1) * 4096 + (16 * ((r >> 2) & 1) + m) * 128 + (((piece ^ m) & 7) << 4));
}
template <int SLEEP>
__device__ __forceinline__ void wait_all(rsrc_t X, unsigned vo, unsigned so) {
  unsigned spins = 0;
  while (true) {
    asm volatile("" ::: "memory");
    if (__all(ld4(X, vo, so) != 0u)) break;
    if (spin_fail(spins, X)) break;
    __builtin_amdgcn_s_sleep(SLEEP);
  }
  asm volatile("" ::: "memory");
}
#define VQ_CLOB "v16", "v17", "v18", "v19", "v20", "v21", "v22", "v23", "v24", "v25", "v26", "v27", "v28", "v29", "v30", "v31", "v32", "v33", "v34", "v35", "v36", "v37", "v38", "v39", "v40", "v41", "v42", "v43", "v44", "v45", "v46", "v47", "v48", "v49", "v50", "v51", "v52", "v53", "v54", "v55", "v56", "v57", "v58", "v59", "v60", "v61", "v62", "v63", "v64", "v65", "v66", "v67", "v68", "v69", "v70", "v71", "v72", "v73", "v74", "v75", "v76", "v77", "v78", "v79", "v80", "v81", "v82", "v83", "v84", "v85", "v86", "v87", "v88", "v89", "v90", "v91", "v92", "v93", "v94", "v95", "v96", "v97", "v98", "v99", "v100", "v101", "v102", "v103", "v104", "v105", "v106", "v107", "v108", "v109", "v110", "v111", "v112", "v113", "v114", "v115", "v116", "v117", "v118", "v119", "v120", "v121", "v122", "v123", "v124", "v125", "v126", "v127", "v128", "v129", "v130", "v131", "v132", "v133", "v134", "v135", "v136", "v137", "v138", "v139", "v140", "v141", "v142", "v143", "v144", "v145", "v146", "v147", "v148", "v149", "v150", "v151", "v152", "v153", "v154", "v155", "v156", "v157", "v158", "v159", "v160", "v161", "v162", "v163", "v164", "v165", "v166", "v167", "v168", "v169", "v170", "v171", "v172", "v173", "v174", "v175", "v176", "v177", "v178", "v179", "v180", "v181", "v182", "v183", "v184", "v185", "v186", "v187", "v188", "v189", "v190", "v191", "v192", "v193", "v194", "v195", "v196", "v197", "v198", "v199", "v200", "v201", "v202", "v203", "v204", "v205", "v206", "v207", "v220", "v221"
#define VA_CLOB "v40", "v41", "v42", "v43", "v44", "v45", "v46", "v47", "v48", "v49", "v50", "v51", "v52", "v53", "v54", "v55", "v56", "v57", "v58", "v59", "v60", "v61", "v62", "v63", "v64", "v65", "v66", "v67", "v68", "v69", "v70", "v71", "v72", "v73", "v74", "v75", "v76", "v77", "v78", "v79", "v80", "v81", "v82", "v83", "v84", "v85", "v86", "v87", "v88", "v89", "v90", "v91", "v92", "v93", "v94", "v95", "v96", "v97", "v98", "v99", "v100", "v101", "v102", "v103", "v104", "v105", "v106", "v107", "v108", "v109", "v110", "v111", "v112", "v113", "v114", "v115", "v116", "v117", "v118", "v119", "v120", "v121", "v122", "v123", "v124", "v125", "v126", "v127", "v128", "v129", "v130", "v131", "v132", "v133", "v134", "v135", "v136", "v137", "v138", "v139", "v140", "v141", "v142", "v143", "v144", "v145", "v146", "v147", "v148", "v149", "v150", "v151", "v152", "v153", "v154", "v155", "v156", "v157", "v158", "v159", "v160", "v161", "v162", "v163", "v164", "v165", "v166", "v167", "v168", "v169", "v170", "v171", "v172", "v173", "v174", "v175", "v176", "v177", "v178", "v179", "v180", "v181", "v182", "v183", "v184", "v185", "v186", "v187", "v188", "v189", "v190", "v191", "v192", "v193", "v194", "v195", "v196", "v197", "v198", "v199", "v200", "v201", "v202", "v203", "v204", "v205", "v206", "v207", "v208", "v209", "v210", "v211", "v220", "v221"
#define TOK_COMMON() \
  const MP p = getp(); \
  const int c = blockIdx.x - NCHW, xq = c & 3, L = (c >> 2) & 1, i = c >> 3; \
  const int tid = threadIdx.x, lane = tid & 63, w = __builtin_amdgcn_readfirstlane(tid >> 6); \
  const rsrc_t X = mkr(p.xb, XB_BYTES)
#define W_IN [W] "s"(WR)
#define W_CLOB "a0", "a1", "a2", "a3", "a4", "a5", "a6", "a7", "a8", "a9", "a10", "a11", "a12", "a13", "a14", "a15", "a16", "a17", "a18", "a19", "a20", "a21", "a22", "a23", "a24", "a25", "a26", "a27", "a28", "a29", "a30", "a31", "a32", "a33", "a34", "a35", "a36", "a37", "a38", "a39", "a40", "a41", "a42", "a43", "a44", "a45", "a46", "a47", "a48", "a49", "a50", "a51", "a52", "a53", "a54", "a55", "a56", "a57", "a58", "a59", "a60", "a61", "a62", "a63", "a64", "a65", "a66", "a67", "a68", "a69", "a70", "a71", "a72", "a73", "a74", "a75", "a76", "a77", "a78", "a79", "a80", "a81", "a82", "a83", "a84", "a85", "a86", "a87", "a88", "a89", "a90", "a91", "a92", "a93", "a94", "a95", "a96", "a97", "a98", "a99", "a100", "a101", "a102", "a103", "a104", "a105", "a106", "a107", "a108", "a109", "a110", "a111", "a112", "a113", "a114", "a115", "a116", "a117", "a118", "a119", "a120", "a121", "a122", "a123", "a124", "a125", "a126", "a127", "a128", "a129", "a130", "a131", "a132", "a133", "a134", "a135", "a136", "a137", "a138", "a139", "a140", "a141", "a142", "a143", "a144", "a145", "a146", "a147", "a148", "a149", "a150", "a151", "a152", "a153", "a154", "a155", "a156", "a157", "a158", "a159", "a160", "a161", "a162", "a163", "a164", "a165", "a166", "a167", "a168", "a169", "a170", "a171", "a172", "a173", "a174", "a175", "a176", "a177", "a178", "a179", "a180", "a181", "a182", "a183", "a184", "a185", "a186", "a187", "a188", "a189", "a190", "a191", "a192", "a193", "a194", "a195", "a196", "a197", "a198", "a199", "a200", "a201", "a202", "a203", "a204", "a205", "a206", "a207", "a208", "a209", "a210", "a211", "a212", "a213", "a214", "a215", "a216", "a217", "a218", "a219", "a220", "a221", "a222", "a223", "a224", "a225", "a226", "a227", "a228", "a229", "a230", "a231", "a232", "a233", "a234", "a235", "a236", "a237", "a238", "a239", "a240", "a241", "a242", "a243", "a244", "a245", "a246", "a247", "a248", "a249", "a250", "a251", "a252", "a253", "a254", "a255", "v224", "v225", "v226", "v227", "v228", "v229", "v230", "v231", "v232", "v233", "v234", "v235", "v236", "v237", "v238", "v239", "v240", "v241", "v242", "v243", "v244", "v245", "v246", "v247", "v248", "v249", "v250", "v251", "v252", "v253", "v254", "v255"
#define S_CLOB "s46", "s47", "s48", "s49", "s50", "s51", "s52", "s53", "s54", "s55", "s56", "s57", "s58", "s59", "s60", "s61", "s62", "s63", "s68", "s69", "s70", "s71", "s72", "s73", "s82", "s83", "s84", "m0", "vcc", "scc", "memory"
typedef unsigned v8w __attribute__((ext_vector_type(8)));

__device__ __forceinline__ void qkv_role(v2u cv) {
  TOK_COMMON();
  const int role = i * 4 + w, hh = 8 * xq + role / 6, part = role % 6, which = part >> 1, half = part & 1;
  const rsrc_t WR = mkr((which == 0 ? p.Wq : (which == 1 ? p.Wk : p.Wv)) + hh * DH + 64 * half, DM * HD * 2u - (unsigned)(hh * DH + 64 * half) * 2u);
  asm volatile("" :: "v"(cv));
  const unsigned long long sm = die_mask(X, SG0, L, lane);
  const int m = lane & 15, g = lane >> 4;
  const unsigned a0 = (unsigned)(m * 128 + ((g ^ (m & 7)) << 4));
  v8w lv = {(unsigned)lane * 16u, a0, a0 ^ 64u, (unsigned)(lane & (L ? 15 : 3)) * 128u, 0u, 40960u + 4u * (unsigned)w, 1u, 0u};
  const unsigned c36 = XQL + P_H * PAIR + (unsigned)w * 1024u, c37 = XQ + (unsigned)(3 * hh + which) * PAIR + (unsigned)half * (which == 2 ? 4096u : 2048u);
  const unsigned c38 = P_FA * PAIR + (unsigned)(hh * 128 + part * 16), c39 = w == 0 ? P_FB * PAIR + 4096u : 0xF0000000u, c41 = (unsigned)w * 1024u;
  const int c42 = L ? -64 : 1 - QLEAD;
  const unsigned c43 = w == 0 ? P_FA * PAIR + 4096u + (unsigned)hh * 128u : 0xF0000000u;
  unsigned d74 = TRC_OFF + 262144u + (unsigned)((c * 4 + w) * 66) * 8u;
  TT(64, 0);
#define Q_CALL(A) asm volatile("s_waitcnt vmcnt(0) lgkmcnt(0)\n\t" A : "+{v[0:7]}"(lv), "+{s74}"(d74) \
               : [X] "s"(X), "{s36}"(c36), "{s37}"(c37), "{s38}"(c38), "{s39}"(c39), "{s40}"(w), "{s41}"(c41), "{s42}"(c42), "{s43}"(c43), "{s[90:91]}"(sm), "{s92}"(TMO_OFF), [cap] "i"(SPIN_CAP), W_IN : VQ_CLOB, W_CLOB, S_CLOB)
  if (which == 2) { Q_CALL(QV_ASM "LQVEND:"); __builtin_amdgcn_endpgm(); }
  Q_CALL(QK_ASM "LQKEND:");
  __builtin_amdgcn_endpgm();
}

__device__ __forceinline__ void ap_role(v2u cv) {
  TOK_COMMON();
  const int hp = 4 * xq + ((i - 12) >> 1), ch = i & 1, a = w >> 1, rt = w & 1, h = 2 * hp + a;
  const int n = lane & 15, g = lane >> 4;
  const rsrc_t WR = mkr(p.Wo + (long)hp * 2 * DH * DM, 2u * DH * DM * 2u);
  asm volatile("" :: "v"(cv));
  const unsigned long long sm = die_mask(X, SG0, L, lane);
  const unsigned tm = (unsigned)die_mask(X, TP0, L, lane);
  const unsigned l16 = (unsigned)lane * 16u, ab = (unsigned)a * 24576u + l16;
  v16u lv = {l16, ab, ab + (unsigned)rt * 4096u, 49152u + (unsigned)w * 4096u + l16, 49152u + (unsigned)(w ^ 2) * 4096u + l16, l16 & ~32u, l16 >> 1,
             (unsigned)(g * 262144 + n * 16), (unsigned)(n * 32 + g * 8), (unsigned)((lane & 7) % 6) * 16u, 0u, 0u, 0u, 0u, 0u, 0u};
  v8w mk;
  const int qi = 8 * (n >> 2) + (n & 3) + 4 * rt;
#pragma unroll
  for (int k = 0; k < 8; ++k) mk[k] = 8 * g + k > qi ? 0xFF800000u : 0u;
  v16u zz = {0u, 0u, 0u, 0u, 0u, 0u, 0u, 0u, 0u, 0u, 0u, 0u, 0u, 0u, 0u, 0u};
  const unsigned tb = XAL + (unsigned)(3 * h) * PAIR;
  const unsigned c36 = tb + (rt ? PAIR + 4096u : 0u), c37 = tb + (rt ? 2u * PAIR : 4096u), c38 = tb + (rt ? 2u * PAIR + 4096u : PAIR);
  const unsigned c39 = (P_KV + (unsigned)(h >> 1)) * PAIR + (unsigned)(h & 1) * 4096u, c41 = P_FA * PAIR + (unsigned)h * 128u;
  const unsigned c42 = w == 0 ? P_FB * PAIR + (unsigned)(2 * hp + ch) * 128u : 0xF0000000u;
  const unsigned c43 = XA + TP0 * XBLK + (unsigned)(8 * rt + (hp >> 3)) * PAIR + (unsigned)((hp & 7) * 1024 + ch * 512 + a * 256);
  const unsigned c44 = (TP0 + 8u) * XBLK + ((tm >> 8) & 1u) * 8192u + (unsigned)(hp >> 1) * PAIR + (unsigned)((((hp & 1) * 4 + 2 * ch + a) * 2 + rt) * 512);
  const unsigned c45 = (unsigned)(a * 24576 + rt * 12288);
  unsigned d74 = TRC_OFF + 262144u + (unsigned)((c * 4 + w) * 66) * 8u;
  asm volatile("s_waitcnt vmcnt(0) lgkmcnt(0)\n\t" AP_ASM "LAPEND:" : "+{v[0:15]}"(lv), "+{v[24:39]}"(zz), "+{s74}"(d74)
               : [X] "s"(X), "{s36}"(c36), "{s37}"(c37), "{s38}"(c38), "{s39}"(c39), "{s41}"(c41), "{s42}"(c42), "{s43}"(c43), "{s44}"(c44), "{s45}"(c45), "{s64}"(512 * ch + 256 * a), "{s65}"(1024 + 64 * ch + 32 * a), "{s66}"(147456 * a), "{s88}"(tm), "{s[90:91]}"(sm),
                 "{s92}"(TMO_OFF), [cap] "i"(SPIN_CAP), "{v[16:23]}"(mk), W_IN : VA_CLOB, W_CLOB, S_CLOB);
  __builtin_amdgcn_endpgm();
}

template <int N> __device__ __forceinline__ float rorN(float v) { return __uint_as_float((unsigned)__builtin_amdgcn_update_dpp(0, (int)__float_as_uint(v), 0x120 + N, 0xf, 0xf, false)); }
__device__ __forceinline__ void up8(v4u v, float* x) {
  x[0] = lo2f(v.x); x[1] = hi2f(v.x); x[2] = lo2f(v.y); x[3] = hi2f(v.y); x[4] = lo2f(v.z); x[5] = hi2f(v.z); x[6] = lo2f(v.w); x[7] = hi2f(v.w);
}
__device__ __forceinline__ v4u pk8(const float* x) { return (v4u){pk2(x[0], x[1]), pk2(x[2], x[3]), pk2(x[4], x[5]), pk2(x[6], x[7])}; }

__device__ __forceinline__ void rb9(float (&x)[9]) {
#pragma unroll
  for (int i = 0; i < 8; i += 2) { const unsigned u = pk2(x[i], x[i + 1]); x[i] = lo2f(u); x[i + 1] = hi2f(u); }
  x[8] = lo2f(pk2(x[8], x[8]));
}
template <int R>
__device__ __forceinline__ void lnR(const float (&x)[R][9], float (&o)[R][9]) {
  float s[2 * R], pv[R];
#pragma unroll
  for (int r = 0; r < R; ++r) {
    pv[r] = __uint_as_float(__builtin_amdgcn_readfirstlane(__float_as_uint(x[r][0])));
    const f32x2 p2 = {pv[r], pv[r]};
    f32x2 d[4];
#pragma unroll
    for (int i = 0; i < 4; ++i) d[i] = (f32x2){x[r][2 * i], x[r][2 * i + 1]} - p2;
    const float d8 = x[r][8] - pv[r];
    const f32x2 a = (d[0] + d[1]) + (d[2] + d[3]);
    const f32x2 q = __builtin_elementwise_fma(d[3], d[3], __builtin_elementwise_fma(d[2], d[2], __builtin_elementwise_fma(d[1], d[1], d[0] * d[0])));
    s[2 * r] = (a[0] + d8) + a[1];
    s[2 * r + 1] = __builtin_fmaf(d8, d8, q[0]) + q[1];
  }
#pragma unroll
  for (int k = 0; k < 2 * R; ++k) s[k] += rorN<8>(s[k]);
#pragma unroll
  for (int k = 0; k < 2 * R; ++k) s[k] += rorN<4>(s[k]);
#pragma unroll
  for (int k = 0; k < 2 * R; ++k) s[k] += rorN<2>(s[k]);
#pragma unroll
  for (int k = 0; k < 2 * R; ++k) s[k] += rorN<1>(s[k]);
#pragma unroll
  for (int r = 0; r < R; ++r) {
    auto u = __builtin_amdgcn_permlane16_swap(__float_as_uint(s[2 * r]), __float_as_uint(s[2 * r + 1]), false, false);
    s[2 * r] = __uint_as_float(u[0]) + __uint_as_float(u[1]);
  }
#pragma unroll
  for (int r = 0; r < R; ++r) {
    const unsigned u = __float_as_uint(s[2 * r]);
    const float md = (__uint_as_float(__builtin_amdgcn_readlane(u, 0)) + __uint_as_float(__builtin_amdgcn_readlane(u, 32))) * (1.0f / DM);
    const float sq = (__uint_as_float(__builtin_amdgcn_readlane(u, 16)) + __uint_as_float(__builtin_amdgcn_readlane(u, 48))) * (1.0f / DM);
    const unsigned mv = pk2(pv[r] + md, __builtin_fmaxf(__builtin_fmaf(-md, md, sq), 0.f));
    const float m = lo2f(mv), var = hi2f(mv);
    const float ve = lo2f(pk2(var + 1e-5f, 0.f));
    const float den = lo2f(pk2(__builtin_amdgcn_sqrtf(ve), 0.f));
    const float inv = __builtin_amdgcn_rcpf(den);
#pragma unroll
    for (int i = 0; i < 9; ++i) o[r][i] = (x[r][i] - m) * inv;
  }
}
__device__ __forceinline__ void ln9(const float (&x)[9], float (&o)[9]) { lnR<1>((const float (&)[1][9])x, (float (&)[1][9])o); }
__device__ __forceinline__ v4u gat8(float v) {
  const unsigned d = pk2(v, __uint_as_float((unsigned)__builtin_amdgcn_mov_dpp((int)__float_as_uint(v), 0xB1, 0xf, 0xf, true)));
  return (v4u){d, (unsigned)__builtin_amdgcn_mov_dpp((int)d, 0x102, 0xf, 0xf, true), (unsigned)__builtin_amdgcn_mov_dpp((int)d, 0x104, 0xf, 0xf, true),
               (unsigned)__builtin_amdgcn_mov_dpp((int)d, 0x106, 0xf, 0xf, true)};
}
__device__ __forceinline__ void tok_row(const MP& p, const rsrc_t X, unsigned sb1, int L, int s, int r, int lane, const v4u (&v)[18], v4u px, v4u py, unsigned px9, unsigned py9) {
  const long tok = (long)s * SEG + r;
  const int m = lane & 15, g = lane >> 4;
  const int xp = 4 * (g & 1) + 2 * (g >> 1) + (m >> 3);
  const unsigned zr = opq(0u);
  const unsigned one = (g == (m >> 2)) ? (opq(0x3F80u) << (16 * (m & 1))) : zr;
  const unsigned ax = (m & 2) ? zr : one, ay = (m & 2) ? one : zr;
  const v4u A1 = {ax, ay, zr, zr}, A2 = {zr, zr, ax, ay};
  const unsigned on2 = opq(0x3F80u) << (16 * (m & 1));
  const int q2 = (m >> 1) & 3;
  const v4u sel = {q2 == 0 ? on2 : zr, q2 == 1 ? on2 : zr, q2 == 2 ? on2 : zr, q2 == 3 ? on2 : zr};
  f32x4 d1, d2, e1;
  asm volatile("s_nop 1\n\t" MF "%0, %1, %3, 0\n\t" MF "%0, %2, %3, %0" : "=&v"(e1) : "v"(v[0]), "v"(v[1]), "v"(sel));
#pragma unroll
  for (int i = 0; i < 16; ++i) {
    if (i == 0) asm volatile("s_nop 1\n\t" MF "%0, %2, %4, 0\n\t" MF "%1, %3, %4, 0" : "=&v"(d1), "=&v"(d2) : "v"(A1), "v"(A2), "v"(v[2]));
    else asm volatile(MF "%0, %2, %4, %0\n\t" MF "%1, %3, %4, %1" : "+v"(d1), "+v"(d2) : "v"(A1), "v"(A2), "v"(v[2 + i]));
  }
  asm volatile("s_nop 7" : "+v"(d1), "+v"(d2), "+v"(e1));
  float o[9];
#pragma unroll
  for (int i = 0; i < 4; ++i) { o[i] = d1[i]; o[4 + i] = d2[i]; }
  {
    auto a02 = __builtin_amdgcn_permlane32_swap(__float_as_uint(e1[0]), __float_as_uint(e1[2]), false, false);
    auto a13 = __builtin_amdgcn_permlane32_swap(__float_as_uint(e1[1]), __float_as_uint(e1[3]), false, false);
    o[8] = m >= 8 ? __uint_as_float(a13[0]) + __uint_as_float(a13[1]) : __uint_as_float(a02[0]) + __uint_as_float(a02[1]);
  }
  float x[9];
  up8(px, x); x[8] = lo2f(px9);
#pragma unroll
  for (int i = 0; i < 9; ++i) x[i] += o[i];
  if (L == 0) {
    float y[9], ln[9];
    up8(py, y); y[8] = lo2f(py9);
    rb9(x);
    ln9(x, ln);
#pragma unroll
    for (int i = 0; i < 9; ++i) x[i] = __builtin_fmaf(ln[i], y[i], x[i]);
    rb9(x);
    st16(X, (unsigned)lane * 16u, sb1 + (P_X3 + (unsigned)(r >> 3)) * PAIR + (unsigned)(r & 7) * 1024u, pk8(x));
    const v4u x9 = gat8(x[8]);
    if ((lane & 7) == 0) st16(X, (unsigned)xp * 16u, sb1 + (P_H + 4u) * PAIR + 4096u + (unsigned)r * 128u, x9);
    ln9(x, ln);
    st16(X, hoff(r, lane), sb1, pk8(ln));
    const v4u h9 = gat8(ln[8]);
    if ((lane & 7) == 0) st16(X, hoff(r, 64 + xp), sb1, h9);
  } else {
    *(v4u*)&p.out[tok * DM + 8 * lane] = pk8(x);
    const v4u o9 = gat8(x[8]);
    if ((lane & 7) == 0) *(v4u*)&p.out[tok * DM + 512 + 8 * xp] = o9;
  }
}

__device__ __forceinline__ void row_role(char* smem) {
  TOK_COMMON();
  const int rw = 4 * xq + (i - 20), r = 2 * rw + (w >> 1);
  const unsigned long long sm = die_mask(X, SG0, L, lane);
  const unsigned tm = (unsigned)die_mask(X, TP0, L, lane), dm = (unsigned)die_mask(X, 0u, L, lane);
  __attribute__((address_space(3))) unsigned* cnt = (__attribute__((address_space(3))) unsigned*)smem;
  BAR();
  if (tid < 64) cnt[tid] = 0u;
  BAR();
  for (int s = w & 1; s < NSEG; s += 2) {
    int ln_ = lane; asm volatile("" : "+v"(ln_));
    const int m = ln_ & 15, g = ln_ >> 4;
    const unsigned l16 = (unsigned)ln_ * 16u, xo = (unsigned)(8 * (4 * (g & 1) + 2 * (g >> 1) + (m >> 3)) + (m & 7)) * 2u;
    const unsigned sb = SEGB(sm, s), sb1 = sb ^ (L ? 0u : 8192u);
    if (s + ARMK < NSEG) {
      const unsigned sv = opq(SENT), sl = SLOT_AT(s + ARMK) + ((dm >> ((s + ARMK) / 3)) & 1u) * 8192u;
      for (int j = r; j < 82; j += 32) {
#pragma unroll
        for (int k = 0; k < 4; ++k) st16(X, l16 + (unsigned)k * 1024u, sl + (unsigned)(j >> 1) * PAIR + (unsigned)(j & 1) * 4096u, (v4u){sv, sv, sv, sv});
      }
    }
    wait_all<PSL>(X, 0u, sb + P_FB * PAIR + 4096u + (unsigned)(L ? rw : r >> 3) * 128u);
    const v4u px = ld16(X, l16, sb + (P_X3 + (unsigned)(r >> 3)) * PAIR + (unsigned)(r & 7) * 1024u);
    const unsigned px9 = __builtin_amdgcn_raw_buffer_load_b16(X, xo, sb + (P_H + 4u) * PAIR + 4096u + (unsigned)r * 128u, 16);
    const rsrc_t YX = mkr(p.yT + (2 * s + (r >> 4)) * 9216, L ? 0u : 18432u);
    const v2u ya = ld8(YX, (unsigned)(ln_ * 256 + ((r ^ m) & 15) * 8), 0u), yb = ld8(YX, (unsigned)(ln_ * 256 + 128 + ((r ^ m) & 15) * 8), 0u);
    const v4u py = {ya.x, ya.y, yb.x, yb.y};
    const unsigned py9 = __builtin_amdgcn_raw_buffer_load_b16(YX, 16384u + (xo >> 3) * 128u + (((unsigned)r ^ (xo >> 4)) & 15u) * 8u + (xo & 6u), 0u, 16);
    if (!__all(ld4(X, (unsigned)(ln_ & 31) * 128u, sb + P_FB * PAIR) != 0u)) {
      if (__builtin_amdgcn_readfirstlane(ld4(X, 0u, sb + P_FA * PAIR + 4096u + (unsigned)r * 128u)) == 0u) {
        wait_all<PSR>(X, 0u, sb + P_FA * PAIR + 4096u + (unsigned)r * 128u);
        __builtin_amdgcn_s_sleep(RDLY);
      }
      wait_all<PSL>(X, (unsigned)(ln_ & 31) * 128u, sb + P_FB * PAIR);
    }
    asm volatile("" ::: "memory");
    TT(s, 0);
    const unsigned j = (unsigned)s & 15u, to = XRL + TP0 * XBLK + (j << 20) + ((tm >> (j >> 1)) & 1u) * 8192u + (unsigned)r * 2u * PAIR;
    const unsigned t9 = (TP0 + 8u) * XBLK + ((tm >> 8) & 1u) * 8192u + (j << 17) + (unsigned)(((r >> 2) & 1) * 512 + (4 * (r >> 3) + (r & 3)) * 32);
    const unsigned v9 = (unsigned)(g * (int)PAIR + (m >> 1) * 1024 + (m & 1) * 16);
    v4u v[18];
#pragma unroll
    for (int k = 0; k < 2; ++k) v[k] = ld16(X, v9, t9 + (unsigned)k * 4u * PAIR);
#pragma unroll
    for (int k = 0; k < 16; ++k) v[2 + k] = ld16(X, l16 + (unsigned)(k & 3) * 1024u, to + (unsigned)(k >> 3) * PAIR + (unsigned)(k & 4) * 1024u);
    SB();
    tok_row(p, X, sb1, L, s, r, ln_, v, px, py, px9, py9);
    VM0();
    unsigned o1 = 0u;
    if (ln_ == 0) o1 = __hip_atomic_fetch_add(cnt + s, 1u, __ATOMIC_RELAXED, __HIP_MEMORY_SCOPE_WORKGROUP);
    if (__builtin_amdgcn_readfirstlane(o1) == 1u && ln_ == 0) st4(X, 0, sb1 + P_FB * PAIR + 4096u + (unsigned)(L * 16 + rw) * 128u, opq(1u));
    TT(s, 1);
  }
}

__device__ __forceinline__ void rows_task(const MP& p, const rsrc_t X, unsigned long long sm, u16* ys, int grp, int ng) {
  const int tid = threadIdx.x, lane = tid & 63, w = __builtin_amdgcn_readfirstlane(tid >> 6);
  const int m = lane & 15, g = lane >> 4;
  const int xp = 4 * (g & 1) + 2 * (g >> 1) + (m >> 3), c9 = 512 + 8 * xp + (m & 7);
  const unsigned sb = SEGB(sm, grp >> 2);
  v4u yv[5], xa[4];
  unsigned x9[4];
#pragma unroll
  for (int i = 0; i < 5; ++i) yv[i] = ld16(mkr(p.yT + (grp >> 1) * 9216, 18432u), (unsigned)(tid + 256 * i) * 16u, 0u);
#pragma unroll
  for (int rr = 0; rr < 4; ++rr) {
    const long t = grp * 8 + w + 4 * (rr & (2 * ng - 1));
    xa[rr] = *(const v4u*)&p.xin[t * DM + 8 * lane]; x9[rr] = p.xin[t * DM + c9];
  }
#pragma unroll
  for (int i = 0; i < 5; ++i) ((v4u*)ys)[tid + 256 * i] = yv[i];
  BAR();
  SP(13 - 3 * ng);
  const unsigned o9 = (lane & 7) == 0 ? 0u : 0xF0000000u;
#pragma unroll 1
  for (int k = 0; k < ng; ++k) {
    float x[2][9], y[2][9], ln[2][9];
    const int t0 = (grp + k) * 8 + w;
#pragma unroll
    for (int rr = 0; rr < 2; ++rr) {
      const int j = (t0 + 4 * rr) & 15;
      const v2u ya = *(const v2u*)&ys[lane * 128 + (j ^ m) * 4], yb = *(const v2u*)&ys[lane * 128 + 64 + (j ^ m) * 4];
      up8(xa[rr], x[rr]); x[rr][8] = lo2f(x9[rr]); up8((v4u){ya.x, ya.y, yb.x, yb.y}, y[rr]); y[rr][8] = bf2f(ys[(c9 >> 2) * 64 + (j ^ xp) * 4 + (m & 3)]);
      xa[rr] = xa[2 + rr]; x9[rr] = x9[2 + rr];
    }
    lnR<2>(x, ln);
#pragma unroll
    for (int rr = 0; rr < 2; ++rr) {
#pragma unroll
      for (int i = 0; i < 9; ++i) x[rr][i] = __builtin_fmaf(ln[rr][i], y[rr][i], x[rr][i]);
      rb9(x[rr]);
      const int r = (t0 + 4 * rr) & 31;
      st16(X, (unsigned)lane * 16u, sb + (P_X3 + (unsigned)(r >> 3)) * PAIR + (unsigned)(r & 7) * 1024u, pk8(x[rr]));
      st16(X, (unsigned)xp * 16u + o9, sb + (P_H + 4u) * PAIR + 4096u + (unsigned)r * 128u, gat8(x[rr][8]));
    }
    lnR<2>(x, ln);
#pragma unroll
    for (int rr = 0; rr < 2; ++rr) {
      const int r = (t0 + 4 * rr) & 31;
      st16(X, hoff(r, lane), sb, pk8(ln[rr]));
      st16(X, hoff(r, 64 + xp) + o9, sb, gat8(ln[rr][8]));
    }
  }
  SP(14 - 3 * ng);
  VM0();
  BAR();
  SP(15 - 3 * ng);
  if (tid < ng) st4(X, (unsigned)tid * 128u, sb + P_FB * PAIR + 4096u + (unsigned)(grp & 3) * 128u, opq(1u));
}

__device__ __forceinline__ void start_conv(char* smem, int d0) {
  const MP p = getp();
  const int w = __builtin_amdgcn_readfirstlane(threadIdx.x >> 6);
  conv_task(p.kvT, p.ssm_k, p.ssm_D, p.yT, (u16*)(smem + w * 32768), d0 + w);
  SP(3);
  VM0();
  BAR();
  SP(4);
  if (threadIdx.x < 64) {
    unsigned old = 0u;
    if (threadIdx.x == 0) old = __hip_atomic_fetch_add(p.xb + MF_OFF / 4u + (d0 >> 6) * 32, 1u, __ATOMIC_RELAXED, __HIP_MEMORY_SCOPE_AGENT);
    if (__builtin_amdgcn_readfirstlane(old) == 15u && threadIdx.x < 8) st4(mkr(p.xb, XB_BYTES), threadIdx.x * 128u, CVR_OFF + (unsigned)(d0 >> 6) * 1024u, opq(1u));
  }
}
__device__ __forceinline__ void token_start(char* smem) {
  TOK_COMMON();
  (void)w;
  const int k = 4 * i + xq;
  if (L || (i >= 12 && i < 16)) start_conv(smem, L ? 128 + 4 * k : 320 + 4 * k);
  if (L || i >= 20) {
    const unsigned long long sm = die_mask(X, SG0, 0, lane);
    int g = L ? 16 + k : k - 96;
    while (g < 120) {
      if (g >= 0 && g < 112) {
        SP(5);
        if (tid < 64) wait_all<PSL>(X, (unsigned)(lane % 9) * 1024u, CVR_OFF + (unsigned)(c & 7) * 128u);
        BAR();
        SP(6);
      }
      rows_task(p, X, sm, (u16*)smem, g < 0 ? g + 16 : 16 + 2 * g, g < 0 ? 1 : 2);
      g += g < 0 ? 16 : (g < 104 ? 120 : 8);
    }
  }
}
__device__ __forceinline__ void token_wg(char* smem, v2u cv) {
  if ((unsigned)(size_t)(__attribute__((address_space(3))) char*)smem != 0u) return;
  token_start(smem);
  const int i = ((int)blockIdx.x - NCHW) >> 3;
  if (i < 12) qkv_role(cv);
  else if (i < 20) ap_role(cv);
  else { asm volatile("" :: "v"(cv)); row_role(smem); }
}

__global__ __launch_bounds__(256, 1) void k_main(MP) {
  extern __shared__ __attribute__((aligned(16))) char smem[];
  v2u cv;
  {
    asm volatile("LKBEG:");
    cv.x = 0u;
    SP(13);
    const MP p = getp();
    cv.y = ld4(mkr(p.xb, (blockIdx.x >> 3) == 31u ? p.wsb : 0u), threadIdx.x * XBLK + CAL_OFF + 8064u, 0u);
  }
  if (blockIdx.x < NCHW) chain_wg(smem, cv);
  else token_wg(smem, cv);
}

extern "C" void kernel_launch(void* const* d_in, const int* in_sizes, int n_in, void* d_out, int out_size, void* d_ws,
                              size_t ws_size, hipStream_t stream) {
  const u16* A_q = (const u16*)d_in[0];
  const u16* A_k = (const u16*)d_in[1];
  const u16* A_v = (const u16*)d_in[2];
  const u16* A_shift = (const u16*)d_in[3];
  const u16* A_ssmk = (const u16*)d_in[4];
  const u16* A_ssmD = (const u16*)d_in[5];
  const u16* B_mem0 = (const u16*)d_in[7];
  const u16* B_Wq = (const u16*)d_in[8];
  const u16* B_Wk = (const u16*)d_in[9];
  const u16* B_Wv = (const u16*)d_in[10];
  const u16* B_Wo = (const u16*)d_in[11];

  char* ws = (char*)d_ws;
  size_t off = (size_t)(-(uintptr_t)ws) & (XBLK - 1u);
  auto alloc = [&](size_t bytes) { char* q = ws + off; off += (bytes + 4095) & ~(size_t)4095; return q; };
  unsigned* xb = (unsigned*)alloc((size_t)XB_BYTES);
  u16* kvT = (u16*)alloc((size_t)DM * S_LEN * 2);
  u16* yT = (u16*)alloc((size_t)DM * S_LEN * 2);

  PreP pp;
  pp.kk = A_k; pp.vv = A_v; pp.shift_k = A_shift; pp.kvT = kvT; pp.ssm_k = A_ssmk; pp.ssm_D = A_ssmD; pp.yT = yT;
  pp.Wq = B_Wq; pp.Wk = B_Wk; pp.Wv = B_Wv; pp.Wo = B_Wo;
  pp.xb = (v4u*)xb; pp.mem0 = B_mem0;
  k_pre<<<NXB + 36 + PRE_KV + PRE_PW + PRE_FILL, 256, 0, stream>>>(pp);
  MP mp;
  mp.out = (u16*)d_out; mp.xb = xb;
  mp.xin = A_q; mp.yT = yT; mp.kvT = kvT; mp.ssm_k = A_ssmk; mp.ssm_D = A_ssmD; mp.wsb = (unsigned)((ws + off) - (char*)xb); mp.pad = 0u;
  mp.Wq = B_Wq; mp.Wk = B_Wk; mp.Wv = B_Wv; mp.Wo = B_Wo;
  const int smem_bytes = 160 * 1024;
  (void)hipFuncSetAttribute((const void*)k_main, hipFuncAttributeMaxDynamicSharedMemorySize, smem_bytes);
  k_main<<<NCHW + NTOKW, 256, smem_bytes, stream>>>(mp);
}
```

```cpp
#include <hip/hip_runtime.h>
#include <hip/hip_bf16.h>
#include <stdint.h>

typedef unsigned short u16;
using bf16x8 = __attribute__((ext_vector_type(8))) short;
using bf16x4 = __attribute__((ext_vector_type(4))) short;
using f32x4 = __attribute__((ext_vector_type(4))) float;
using f32x2 = __attribute__((ext_vector_type(2))) float;
using f32x16 = __attribute__((ext_vector_type(16))) float;
using bfx8 = __attribute__((ext_vector_type(8))) __bf16;
using bfx2 = __attribute__((ext_vector_type(2))) __bf16;
typedef unsigned v4u __attribute__((ext_vector_type(4)));
typedef unsigned v2u __attribute__((ext_vector_type(2)));
typedef unsigned v8u __attribute__((ext_vector_type(8)));
typedef short v4s __attribute__((ext_vector_type(4)));

#define S_LEN 2048
#define DM 576
#define NH 32
#define DH 128
#define HD (NH * DH)
#define MEMR 8
#define SEG 32
#define NSEG 64

__device__ __forceinline__ float bf2f(u16 x) { return __uint_as_float(((unsigned)x) << 16); }
__device__ __forceinline__ u16 f2bf(float f) {
  unsigned u = __float_as_uint(f);
  u += 0x7FFFu + ((u >> 16) & 1u);
  return (u16)(u >> 16);
}
__device__ __forceinline__ float rbf(float f) { return bf2f(f2bf(f)); }
__device__ __forceinline__ unsigned pk2(float a, float b) { f32x2 v = {a, b}; bfx2 r = __builtin_convertvector(v, bfx2); return __builtin_bit_cast(unsigned, r); }
__device__ __forceinline__ float lo2f(unsigned u) { return __uint_as_float(u << 16); }
__device__ __forceinline__ float hi2f(unsigned u) { return __uint_as_float(u & 0xFFFF0000u); }

__device__ __forceinline__ float wave_sum(float v) {
#pragma unroll
  for (int o = 32; o > 0; o >>= 1) v += __shfl_xor(v, o, 64);
  return v;
}

#define NFC 128
#define XBLK 2097152u
#define PAIR 16384u
#define SLOT 688128u
#define AGO 589824u
#define AGT 655360u
#define SG0 22
#define TP0 86
#define NXB 95
#define XB_BYTES (NXB * XBLK)
#define SLOT_AT(i) ((unsigned)((i) / 3) * XBLK + (unsigned)((i) % 3) * SLOT)
#define HT_OFF SLOT_AT(65)
#define CTAB_OFF (HT_OFF + 16384u)
#define MF_OFF (HT_OFF + 20480u)
#define TMO_OFF (MF_OFF + 15u * 128u)
#define RDY_OFF(k) (MF_OFF + 4096u + (k) * 10240u)
#define MF_BYTES 16384u
#define TRC_OFF SLOT_AT(64)
#define CAL_OFF (XBLK - 16384u)
#define P_KV 96u
#define P_H 112u
#define P_X3 117u
#define P_FA 121u
#define P_FB 122u
#define ARMK 8
#define NCAL 8
#define KPM 0
__device__ __forceinline__ unsigned kv2(const unsigned (&u)[4], unsigned v, v2u w0, v2u w1) {
  const float a = __builtin_fmaf(lo2f(u[3]), lo2f(w0.x), __builtin_fmaf(lo2f(u[2]), hi2f(w0.x), __builtin_fmaf(lo2f(u[1]), lo2f(w0.y), lo2f(u[0]) * hi2f(w0.y))));
  const float b = __builtin_fmaf(hi2f(u[3]), lo2f(w1.x), __builtin_fmaf(hi2f(u[2]), hi2f(w1.x), __builtin_fmaf(hi2f(u[1]), lo2f(w1.y), hi2f(u[0]) * hi2f(w1.y))));
  return pk2(a * lo2f(v), b * hi2f(v));
}
struct PreP {
  const u16* kk; const u16* vv; const u16* shift_k; const u16* ssm_k; const u16* ssm_D; u16* yT; u16* kvT;
  const u16* Wq; const u16* Wk; const u16* Wv; const u16* Wo; unsigned* xb; const u16* mem0;
};
#define PRE_FILL 112
#define PFN 5
__global__ __launch_bounds__(256, 4) void k_pre(PreP p) {
  __shared__ __attribute__((aligned(16))) float sh[64 * 36];
  const int tid = threadIdx.x, b0 = blockIdx.x, b = b0 < 240 ? b0 : (b0 < 528 ? b0 + PRE_FILL + 256 : b0 - 288);
  const __amdgpu_buffer_rsrc_t rx = __builtin_amdgcn_make_buffer_rsrc((void*)p.xb, 0, XB_BYTES, 0x00020000);
  const v4u ones = {0xFFFFFFFFu, 0xFFFFFFFFu, 0xFFFFFFFFu, 0xFFFFFFFFu}, zero = {0, 0, 0, 0};
  if (b < 96) {
    if (tid >= 64 || (KPM & 16)) return;
    const unsigned cb = (unsigned)b * XBLK + CAL_OFF;
    __builtin_amdgcn_raw_buffer_store_b128(zero, rx, (unsigned)(tid & 1) * 8192u + (unsigned)(tid >> 1) * 256u, cb, 16);
    asm volatile("s_waitcnt vmcnt(0)" ::: "memory");
    unsigned mn[2] = {~0u, ~0u};
    for (int i = 0; i < NCAL; ++i) {
      const unsigned long long c0 = __builtin_amdgcn_s_memtime();
      asm volatile("s_waitcnt lgkmcnt(0)" ::: "memory");
      const unsigned v = __builtin_amdgcn_raw_buffer_load_b32(rx, 0, cb + (unsigned)(i & 1) * 8192u + (unsigned)(i >> 1) * 256u, 16);
      asm volatile("s_waitcnt vmcnt(0)" :: "v"(v) : "memory");
      const unsigned d = (unsigned)(__builtin_amdgcn_s_memtime() - c0);
      if (i & 1) mn[1] = d < mn[1] ? d : mn[1]; else mn[0] = d < mn[0] ? d : mn[0];
    }
    unsigned xcc; asm volatile("s_getreg_b32 %0, hwreg(HW_REG_XCC_ID)" : "=s"(xcc));
    if (tid == 0) __builtin_amdgcn_raw_buffer_store_b32((mn[1] < mn[0] ? 1u : 0u) ^ ((xcc >> 2) & 1u), rx, 0, CTAB_OFF + (unsigned)b * 4u, 16);
  } else if (b < 240) {
    if (KPM & 1) return;
    const __amdgpu_buffer_rsrc_t rk = __builtin_amdgcn_make_buffer_rsrc((void*)p.kk, 0, S_LEN * DM * 2, 0x00020000);
    float (*kx)[132] = (float (*)[132])sh;
    float (*cf)[140] = (float (*)[140])(sh + 4 * 132);
    const int d0 = (b - 96) * 4, q = tid & 3, dd = (tid >> 2) & 3, t0 = (tid >> 4) * 8;
    const float Dv = bf2f(p.ssm_D[d0 + dd]);
    {
      const int s = tid >> 1, d = d0 + (tid & 1) * 2;
      unsigned u[4];
#pragma unroll
      for (int a = 0; a < 4; ++a) u[a] = __builtin_amdgcn_raw_buffer_load_b32(rk, (unsigned)((s - 3 + a) * DM + d) * 2u, 0, 0);
      const unsigned vr = *(const unsigned*)&p.vv[s * DM + d], c0 = *(const unsigned*)&p.ssm_k[(long)(d0 + (tid >> 6)) * S_LEN + (tid & 63) * 2];
      const v4u w = *(const v4u*)&p.shift_k[d * 4];
      const unsigned r = kv2(u, vr, (v2u){w.x, w.y}, (v2u){w.z, w.w});
      kx[(tid & 1) * 2][s] = lo2f(r); kx[(tid & 1) * 2 + 1][s] = hi2f(r);
      *(f32x2*)&cf[tid >> 6][8 + (tid & 63) * 2] = (f32x2){lo2f(c0), hi2f(c0)};
      if (tid < 32) cf[tid >> 3][tid & 7] = 0.f;
    }
    __syncthreads();
    float y[8];
#pragma unroll
    for (int i = 0; i < 8; ++i) y[i] = 0.f;
#pragma unroll 1
    for (int j = 8 * q; j <= t0; j += 32) {
      f32x4 kk[2], cw[4];
#pragma unroll
      for (int x = 0; x < 2; ++x) kk[x] = *(const f32x4*)&kx[dd][j + 4 * x];
#pragma unroll
      for (int x = 0; x < 4; ++x) cw[x] = *(const f32x4*)&cf[dd][t0 - j + 4 * x];
#pragma unroll
      for (int jj = 0; jj < 8; ++jj)
#pragma unroll
        for (int i = 0; i < 8; ++i) y[i] = __builtin_fmaf(cw[(8 + i - jj) >> 2][(8 + i - jj) & 3], kk[jj >> 2][jj & 3], y[i]);
    }
#pragma unroll
    for (int i = 0; i < 8; ++i) {
      y[i] += __uint_as_float((unsigned)__builtin_amdgcn_mov_dpp((int)__float_as_uint(y[i]), 0xB1, 0xf, 0xf, true));
      y[i] += __uint_as_float((unsigned)__builtin_amdgcn_mov_dpp((int)__float_as_uint(y[i]), 0x4E, 0xf, 0xf, true));
    }
    const float ya = q == 0 ? y[0] : (q == 1 ? y[2] : (q == 2 ? y[4] : y[6])), yb = q == 0 ? y[1] : (q == 1 ? y[3] : (q == 2 ? y[5] : y[7]));
    const int t = t0 + 2 * q, f = (d0 >> 3) & 15;
    const unsigned r0 = pk2(ya, yb), r1 = pk2(kx[dd][t] * Dv, kx[dd][t + 1] * Dv), o = pk2(lo2f(r0) + lo2f(r1), hi2f(r0) + hi2f(r1));
    u16* dst = p.yT + (t >> 4) * 9216 + (d0 >> 2) * 64 + dd;
    dst[((t & 15) ^ f) << 2] = (u16)(o & 0xFFFFu); dst[(((t + 1) & 15) ^ f) << 2] = (u16)(o >> 16);
  } else if (b < 240 + PRE_FILL) {
    if (KPM & 32) return;
    const unsigned i0 = (unsigned)(b - 240) * 256u + tid;
    if (i0 < (SLOT - PAIR - AGO) / 16u) {
      const unsigned o = AGO + i0 * 16u, f = o < AGT ? (o & 8191u) : 8192u + (o & 8191u) % 1152u, s = f >> 10, r = f & 1023u;
      const unsigned gm = s < 8u ? r >> 8 : (r >> 6) & 3u, m3 = (r >> 4) & 3u, ga = s < 8u ? (r >> 6) & 3u : r >> 8, row = 4u * (gm & 1u) + 2u * (gm >> 1) + (m3 & 1u), half = m3 >> 1;
      const unsigned* src = (const unsigned*)(p.mem0 + row * DM + 64u * (s < 9u ? s : 0u) + 16u * ga + 2u * half);
      __builtin_amdgcn_raw_buffer_store_b128((v4u){src[0], src[2], src[4], src[6]}, rx, o, 0, 16);
    }
    for (unsigned i = i0; i < 131072u + MF_BYTES / 16u; i += PRE_FILL * 256)
      __builtin_amdgcn_raw_buffer_store_b128(zero, rx, i < 131072u ? (SG0 + (i >> 11)) * XBLK + P_FA * PAIR + (i & 2047u) * 16u : MF_OFF + (i & 2047u) * 16u, 0, 16);
    for (unsigned i = i0; i < 2u * SLOT / 16u; i += PRE_FILL * 256) __builtin_amdgcn_raw_buffer_store_b128(ones, rx, SLOT + i * 16u, 0, 16);
  } else if (b >= 240 + PRE_FILL + 256) {
    if (KPM & 2) return;
    const __amdgpu_buffer_rsrc_t rk = __builtin_amdgcn_make_buffer_rsrc((void*)p.kk, 0, S_LEN * DM * 2, 0x00020000);
    u16 (*tile)[72] = (u16 (*)[72])sh;
    const int bid = b - 240 - PRE_FILL - 256, d0 = (bid % 9) * 64, s0 = (bid / 9) * 64;
    const int sr = (tid >> 3) * 2, d = d0 + (tid & 7) * 8;
    v4u kr[5], vr[2], w[4];
#pragma unroll
    for (int a = 0; a < 5; ++a) kr[a] = __builtin_amdgcn_raw_buffer_load_b128(rk, (unsigned)((s0 + sr - 3 + a) * DM + d) * 2u, 0, 0);
#pragma unroll
    for (int a = 0; a < 2; ++a) vr[a] = *(const v4u*)&p.vv[(long)(s0 + sr + a) * DM + d];
#pragma unroll
    for (int a = 0; a < 4; ++a) w[a] = *(const v4u*)&p.shift_k[(d + 2 * a) * 4];
    __builtin_amdgcn_sched_barrier(0);
#pragma unroll
    for (int it = 0; it < 2; ++it) {
      v4u o;
#pragma unroll
      for (int j = 0; j < 4; ++j) { const unsigned u[4] = {kr[it][j], kr[it + 1][j], kr[it + 2][j], kr[it + 3][j]}; o[j] = kv2(u, vr[it][j], (v2u){w[j].x, w[j].y}, (v2u){w[j].z, w[j].w}); }
      *(v4u*)&tile[sr + it][(tid & 7) * 8] = o;
    }
    __syncthreads();
    const int lane = tid & 63, wv = tid >> 6, g = lane >> 4, r = (lane >> 2) & 3, c = lane & 3;
    v2u o[4];
#pragma unroll
    for (int i = 0; i < 4; ++i) o[i] = __builtin_bit_cast(v2u, __builtin_amdgcn_ds_read_tr16_b64_v4i16((__attribute__((address_space(3))) v4s*)&tile[16 * g + 4 * i + r][16 * wv + 4 * c]));
    u16* dst = p.kvT + (long)(d0 + 16 * wv + (lane & 15)) * S_LEN + s0 + 16 * g;
    *(v4u*)dst = (v4u){o[0].x, o[0].y, o[1].x, o[1].y};
    *(v4u*)(dst + 8) = (v4u){o[2].x, o[2].y, o[3].x, o[3].y};
  } else {
    if (KPM & 4) return;
    const int x = b & 7, k = (b - 240 - PRE_FILL) >> 3;
    unsigned long long pc; unsigned l0, l1, l2, l3; int rel;
    asm volatile("s_getpc_b64 %0\nLPRE:\n\ts_mov_b32 %1, LKBEG-LPRE\n\ts_mov_b32 %2, LCHEND-LKBEG\n\ts_mov_b32 %3, LQVEND-LKBEG\n\ts_mov_b32 %4, LQKEND-LKBEG\n\ts_mov_b32 %5, LAPEND-LKBEG"
                 : "=s"(pc), "=s"(rel), "=s"(l0), "=s"(l1), "=s"(l2), "=s"(l3));
    l0 = l0 > l1 ? l0 : l1; l2 = l2 > l3 ? l2 : l3; l0 = l0 > l2 ? l0 : l2;
    const unsigned o = (unsigned)(k * 64 + (tid & 63)) * 64u;
    unsigned a[PFN + 2];
    a[PFN] = *(const __attribute__((address_space(1))) unsigned*)(pc + rel + (o < l0 ? o : 0u));
    a[PFN + 1] = __builtin_amdgcn_raw_buffer_load_b32(rx, k == 31 ? tid * XBLK + CAL_OFF + 8064u : 0xF0000000u, 0, 16);
#pragma unroll
    for (int i = 0; i < PFN; ++i) {
      const int q = (i * 32 + k) * 256 + tid, r2 = q >> 5, gr = r2 >> 5, row = (gr / 3) * 64 + (x >> 2) * 32 + (r2 & 31), off = (q & 31) * 32, ty = gr % 3;
      a[i] = *(const unsigned*)(gr < 27 ? (ty == 0 ? p.Wq : (ty == 1 ? p.Wk : p.Wv)) + (long)row * HD + (x & 3) * 1024 + off
                                        : p.Wo + ((long)(x & 3) * DM + (gr < 36 ? gr - 27 : 0) * 64 + (x >> 2) * 32 + (r2 & 31)) * 1024 + off);
    }
#pragma unroll
    for (int i = 0; i < PFN + 2; ++i) asm volatile("" :: "v"(a[i]));
  }
}

#define CREV_STRIDE 2208
#define SP(k)
#define LWAIT() asm volatile("s_waitcnt lgkmcnt(0)" ::: "memory")
template <class F>
__device__ __forceinline__ void conv_task(const u16* __restrict__ kvT, const u16* __restrict__ ssm_k, const u16* __restrict__ ssm_D, u16* yT, u16* shb, int d, F fill) {
  const int lane = threadIdx.x & 63;
  u16* crev = shb + 64;
  u16* kvs = crev + 4 * CREV_STRIDE;
  const int X0 = 2048 + 63;
  SP(0);
  v4u cv[4], kq[4];
#pragma unroll
  for (int j = 0; j < 4; ++j) { cv[j] = *(const v4u*)&ssm_k[(long)d * S_LEN + lane * 32 + 8 * j]; kq[j] = *(const v4u*)&kvT[(long)d * S_LEN + (j * 64 + lane) * 8]; }
  const float Dv = bf2f(ssm_D[d]);
  const v4u z4 = {0u, 0u, 0u, 0u};
  *(v4u*)&kvs[(lane >> 2) * 40 + (lane & 3) * 8] = z4;
  if (lane < 32) *(v4u*)&crev[(lane >> 3) * CREV_STRIDE + (lane & 7) * 8] = z4;
  if (lane < 36) *(v4u*)&crev[(lane / 9) * CREV_STRIDE + 2112 + (lane % 9) * 8] = z4;
  {
    unsigned r[18];
#pragma unroll
    for (int k = 0; k < 16; ++k) { const unsigned u = cv[3 - (k >> 2)][3 - (k & 3)]; r[k] = __builtin_amdgcn_alignbyte(u, u, 2); }
#pragma unroll
    for (int k = 0; k < 2; ++k) r[16 + k] = (unsigned)__builtin_amdgcn_update_dpp(0, (int)r[k], 0x138, 0xf, 0xf, true);
    LWAIT();
#pragma unroll
    for (int rho = 0; rho < 4; ++rho)
#pragma unroll
      for (int j = 0; j < 4; ++j) {
        v4u o;
#pragma unroll
        for (int k = 0; k < 4; ++k) { const int q = 4 * j + k + (rho >> 1); o[k] = (rho & 1) ? __builtin_amdgcn_alignbyte(r[q + 1], r[q], 2) : r[q]; }
        *(v4u*)&crev[rho * CREV_STRIDE + 2080 - 32 * lane + 8 * j] = o;
      }
    if (lane == 63) {
      *(v2u*)&crev[1 * CREV_STRIDE + 60] = (v2u){0u, r[0] << 16};
      *(v2u*)&crev[2 * CREV_STRIDE + 60] = (v2u){0u, r[0]};
      *(v2u*)&crev[3 * CREV_STRIDE + 60] = (v2u){r[0] << 16, __builtin_amdgcn_alignbyte(r[1], r[0], 2)};
    }
#pragma unroll
    for (int j = 0; j < 4; ++j) *(v4u*)&kvs[(16 + j * 16 + (lane >> 2)) * 40 + (lane & 3) * 8] = kq[j];
  }
  fill();
  LWAIT();
  SP(1);
  const int n = lane & 15, g = lane >> 4;
  const int rho = (X0 - n) & 3;
  f32x4 acc[4][2];
#pragma unroll
  for (int q = 0; q < 8; ++q) acc[q >> 1][q & 1] = (f32x4){0.f, 0.f, 0.f, 0.f};
  {
    typedef __attribute__((address_space(3))) const v2u* l2p;
    typedef __attribute__((address_space(3))) const bf16x8* l4p;
    unsigned a0 = (unsigned)(size_t)(__attribute__((address_space(3))) u16*)&crev[rho * CREV_STRIDE + (X0 - (n - 8 * g) - rho)] - 3232u, a1 = a0 + 8u;
    unsigned b0 = (unsigned)(size_t)(__attribute__((address_space(3))) u16*)&kvs[(14 + n) * 40 + 8 * g];
    asm volatile("" : "+v"(a1));
#define AFR(m, f) ({ const unsigned o_ = 3232u - 64u * (m) - 1024u * ((f) >> 1) - 32u * ((f) & 1); const v2u lo_ = *(l2p)(size_t)(a0 + o_), hi_ = *(l2p)(size_t)(a1 + o_); \
                     __builtin_bit_cast(bf16x8, (v4u){lo_.x, lo_.y, hi_.x, hi_.y}); })
#define BFR(m, j) (*(l4p)(size_t)(b0 + 80u * (2 - (m)) + 1280u * (j)))
    bf16x8 af[2][8], bf[2][4];
#pragma unroll
    for (int f = 0; f < 8; ++f) { af[0][f] = AFR(0, f); af[1][f] = af[0][f]; __builtin_amdgcn_sched_barrier(0); }
#pragma unroll
    for (int j = 0; j < 4; ++j) { bf[0][j] = BFR(0, j); bf[1][j] = bf[0][j]; }
#pragma unroll 1
    for (int mo = 0; mo < 8; ++mo) {
#pragma unroll
      for (int m = 0; m < 2; ++m) {
        int k = 0;
#pragma unroll
        for (int a = 0; a < 4; ++a)
#pragma unroll
          for (int ct = a; ct < 4; ++ct)
#pragma unroll
            for (int ti = 0; ti < 2; ++ti) {
              acc[ct][ti] = __builtin_amdgcn_mfma_f32_16x16x32_bf16(__builtin_bit_cast(bfx8, af[m][2 * a + ti]), __builtin_bit_cast(bfx8, bf[m][ct - a]), acc[ct][ti], 0, 0, 0);
              if (k < 2) af[m ^ 1][k] = AFR(m + 1, k);
              else if (k < 6) bf[m ^ 1][k - 2] = BFR(m + 1, k - 2);
              else if (k < 12) af[m ^ 1][k - 4] = AFR(m + 1, k - 4);
              __builtin_amdgcn_sched_barrier(0);
              ++k;
            }
      }
      a0 -= 128u; a1 -= 128u; b0 -= 160u;
    }
  }
  SP(2);
  u16* yo = crev;
  LWAIT();
#pragma unroll
  for (int q = 0; q < 8; ++q) {
    const int o = (16 * (q >> 1) + n) * 40 + 16 * (q & 1) + 4 * g;
    const v2u kx = *(const v2u*)&kvs[16 * 40 + o];
    const f32x4 y = acc[q >> 1][q & 1];
    const unsigned y0 = pk2(y[0], y[1]), y1 = pk2(y[2], y[3]), r0 = pk2(lo2f(kx.x) * Dv, hi2f(kx.x) * Dv), r1 = pk2(lo2f(kx.y) * Dv, hi2f(kx.y) * Dv);
    *(v2u*)&yo[o] = (v2u){pk2(lo2f(y0) + lo2f(r0), hi2f(y0) + hi2f(r0)), pk2(lo2f(y1) + lo2f(r1), hi2f(y1) + hi2f(r1))};
  }
  LWAIT();
  __builtin_amdgcn_s_barrier();
  asm volatile("" ::: "memory");
  {
    const int tid = threadIdx.x, f = (d >> 3) & 15;
    const u16* y0 = shb - (tid >> 6) * 16384 + 64;
#pragma unroll
    for (int k = 0; k < 4; ++k) {
      const int tile = 32 * k + (tid >> 3), t = 16 * tile + 2 * ((tid & 7) ^ (f >> 1));
      unsigned u[4];
#pragma unroll
      for (int c = 0; c < 4; ++c) u[c] = *(const unsigned*)&y0[c * 16384 + (t >> 5) * 40 + (t & 31)];
      const v2u e = {__builtin_amdgcn_perm(u[1], u[0], 0x05040100u), __builtin_amdgcn_perm(u[3], u[2], 0x05040100u)}, o = {__builtin_amdgcn_perm(u[1], u[0], 0x07060302u), __builtin_amdgcn_perm(u[3], u[2], 0x07060302u)};
      if (tile >= NFC / 16) __builtin_amdgcn_raw_buffer_store_b128((f & 1) ? (v4u){o.x, o.y, e.x, e.y} : (v4u){e.x, e.y, o.x, o.y}, __builtin_amdgcn_make_buffer_rsrc((void*)yT, 0, DM * S_LEN * 2, 0x00020000),
                                                                   tile * 18432 + (tid & 7) * 16, (d >> 2) * 128, 16);
    }
  }
}

#define NCHW 64
#define NTOKW 192
#define PSL2 10
#define PSL 4
#define SENT 0xFFFFFFFFu
#define SPIN_CAP 300000u
struct MP {
  const u16* Wq; const u16* Wk; const u16* Wv; const u16* Wo; u16* out; unsigned* xb;
  const u16* xin; u16* yT; u16* kvT; const u16* ssm_k; const u16* ssm_D;
};

__device__ __forceinline__ MP getp() {
  MP r;
#if defined(__HIP_DEVICE_COMPILE__)
  const __attribute__((address_space(4))) unsigned long long* k = (const __attribute__((address_space(4))) unsigned long long*)__builtin_amdgcn_kernarg_segment_ptr();
  asm volatile("" : "+s"(k));
#define GP(T, i) ((T*)(__attribute__((address_space(1))) T*)k[i])
  r.Wq = GP(const u16, 0);
  r.Wk = GP(const u16, 1);
  r.Wv = GP(const u16, 2);
  r.Wo = GP(const u16, 3);
  r.out = GP(u16, 4);
  r.xb = GP(unsigned, 5);
  r.xin = GP(const u16, 6);
  r.yT = GP(u16, 7);
  r.kvT = GP(u16, 8);
  r.ssm_k = GP(const u16, 9);
  r.ssm_D = GP(const u16, 10);
#endif
  return r;
}
typedef __amdgpu_buffer_rsrc_t rsrc_t;
__device__ __forceinline__ rsrc_t mkr(const void* p, unsigned bytes) { return __builtin_amdgcn_make_buffer_rsrc((void*)p, 0, bytes, 0x00020000); }
__device__ __forceinline__ void st16(rsrc_t r, unsigned vo, unsigned so, v4u v) { __builtin_amdgcn_raw_buffer_store_b128(v, r, vo, so, 16); }
__device__ __forceinline__ void st8(rsrc_t r, unsigned vo, unsigned so, v2u v) { __builtin_amdgcn_raw_buffer_store_b64(v, r, vo, so, 16); }
__device__ __forceinline__ void st4(rsrc_t r, unsigned vo, unsigned so, unsigned v) { __builtin_amdgcn_raw_buffer_store_b32(v, r, vo, so, 16); }
__device__ __forceinline__ v4u ld16(rsrc_t r, unsigned vo, unsigned so) { return __builtin_amdgcn_raw_buffer_load_b128(r, vo, so, 16); }
__device__ __forceinline__ v2u ld8(rsrc_t r, unsigned vo, unsigned so) { return __builtin_amdgcn_raw_buffer_load_b64(r, vo, so, 16); }
__device__ __forceinline__ unsigned ld4(rsrc_t r, unsigned vo, unsigned so) { return __builtin_amdgcn_raw_buffer_load_b32(r, vo, so, 16); }
__device__ __forceinline__ float sum4g(float v) {
  auto r = __builtin_amdgcn_permlane16_swap(__float_as_uint(v), __float_as_uint(v), false, false);
  v = __uint_as_float(r[0]) + __uint_as_float(r[1]);
  auto q = __builtin_amdgcn_permlane32_swap(__float_as_uint(v), __float_as_uint(v), false, false);
  return __uint_as_float(q[0]) + __uint_as_float(q[1]);
}
__device__ __forceinline__ unsigned opq(unsigned x) { asm volatile("" : "+v"(x)); return x; }

__device__ __forceinline__ unsigned long long die_mask(rsrc_t X, unsigned first, int L, int lane) { return __ballot((ld4(X, (unsigned)lane * 4u, CTAB_OFF + first * 4u) & 1u) != (unsigned)L); }
#define SEGB(m, s) ((unsigned)(SG0 + (s)) * XBLK + (unsigned)(((m) >> (s)) & 1ull) * 8192u)
__device__ __forceinline__ bool spin_fail(unsigned& spins, rsrc_t F) {
  ++spins;
  if (spins > SPIN_CAP) { unsigned one = 0x40000000u | (blockIdx.x << 8) | threadIdx.x; asm volatile("" : "+v"(one)); if (__builtin_amdgcn_readfirstlane(ld4(F, 0, TMO_OFF)) == 0u) st4(F, 0, TMO_OFF, one); return true; }
  if ((spins & 1023u) == 0u) return __builtin_amdgcn_readfirstlane(ld4(F, 0, TMO_OFF)) != 0;
  return false;
}
__device__ __forceinline__ unsigned rtc() { return (unsigned)__builtin_amdgcn_s_memrealtime(); }
__device__ __forceinline__ unsigned wait_until(unsigned T) { unsigned n = rtc(); for (int i = 0; i < 8192 && (int)(n - T) < 0; ++i) n = rtc(); return n; }
__device__ __forceinline__ unsigned umax3(unsigned a, unsigned b, unsigned c) { unsigned m = a > b ? a : b; return m > c ? m : c; }
__device__ __forceinline__ float fmx(float a, float b) { float d; asm("v_max_f32 %0, %1, %2\n\ts_nop 1" : "=v"(d) : "v"(a), "v"(b)); return d; }
__device__ __forceinline__ float fmx3(float a, float b, float c) { float d; asm("v_max3_f32 %0, %1, %2, %3" : "=v"(d) : "v"(a), "v"(b), "v"(c)); return d; }
__device__ __forceinline__ float max4gx(float v) {
  auto r = __builtin_amdgcn_permlane16_swap(__float_as_uint(v), __float_as_uint(v), false, false);
  v = fmx(__uint_as_float(r[0]), __uint_as_float(r[1]));
  auto q = __builtin_amdgcn_permlane32_swap(__float_as_uint(v), __float_as_uint(v), false, false);
  return fmx(__uint_as_float(q[0]), __uint_as_float(q[1]));
}
__device__ __forceinline__ float ror8(float v) { return __uint_as_float((unsigned)__builtin_amdgcn_update_dpp(0, (int)__float_as_uint(v), 0x128, 0xf, 0xf, false)); }
__device__ __forceinline__ unsigned ror8u(unsigned v) { return (unsigned)__builtin_amdgcn_update_dpp(0, (int)v, 0x128, 0xf, 0xf, false); }
__device__ __forceinline__ unsigned wave_umax(unsigned v, int lane) {
#pragma unroll
  for (int o = 32; o > 0; o >>= 1) { unsigned x = (unsigned)__builtin_amdgcn_ds_bpermute((lane ^ o) << 2, (int)v); v = x > v ? x : v; }
  return v;
}
#define BAR() do { asm volatile("s_waitcnt lgkmcnt(0)" ::: "memory"); __builtin_amdgcn_s_barrier(); asm volatile("" ::: "memory"); } while (0)
#define SB() __builtin_amdgcn_sched_barrier(0)
#define MF "v_mfma_f32_16x16x32_bf16 "
#define C2X4(d0, d1, a0, a1, b) asm volatile( \
    MF "%0, %2, %10, 0\n\t" MF "%1, %6, %10, 0\n\t" MF "%0, %3, %11, %0\n\t" MF "%1, %7, %11, %1\n\t" \
    MF "%0, %4, %12, %0\n\t" MF "%1, %8, %12, %1\n\t" MF "%0, %5, %13, %0\n\t" MF "%1, %9, %13, %1" \
    : "=&v"(d0), "=&v"(d1) : "v"((a0)[0]), "v"((a0)[1]), "v"((a0)[2]), "v"((a0)[3]), "v"((a1)[0]), "v"((a1)[1]), "v"((a1)[2]), "v"((a1)[3]), \
      "v"((b)[0]), "v"((b)[1]), "v"((b)[2]), "v"((b)[3]))
#define C1X4(d0, a0, b) asm volatile( \
    MF "%0, %1, %5, 0\n\t" MF "%0, %2, %6, %0\n\t" MF "%0, %3, %7, %0\n\t" MF "%0, %4, %8, %0" \
    : "=&v"(d0) : "v"((a0)[0]), "v"((a0)[1]), "v"((a0)[2]), "v"((a0)[3]), "v"((b)[0]), "v"((b)[1]), "v"((b)[2]), "v"((b)[3]))
#define VM0() __builtin_amdgcn_s_waitcnt(0x0F70)
#define DL1 12
#define SCH_B0 54
#define DLL1 6
#define DLL2 4
#define DLF 24
#define SCH_PEN 0
#define DLX 4
#define LATE 2
#define TKL 28
#define UP 1
#define DN 100
#define DL3 16
#define RSL 2
#define CH_S2(x) #x
#define CH_S(x) CH_S2(x)
#define CH_ST "s_memtime s[62:63]\n\ts_waitcnt lgkmcnt(0)\n\ts_sub_u32 s67, s62, s67\n\t"
#define CH_BASE "s_memtime s[94:95]\n\ts_waitcnt lgkmcnt(0)\n\ts_mov_b32 s67, s94\n\t"
#define CH_DPC
#define CH_TOKCHK "s_cbranch_vccz LG3_%=\n\t"
#define CH_SEGC
#define CH_SG0 CH_S(SG0)
#define CH_ARMD "%[armd]"
#define CH_ARMS "%[arms]"
#define CH_SLOT "688128"
#define CH_SKIP "32768"
#define CH_DL1 CH_S(DL1)
#define CH_DLL1 CH_S(DLL1)
#define CH_DLL2 CH_S(DLL2)
#define CH_DLF CH_S(DLF)
#define CH_B0 CH_S(SCH_B0)
#define CH_PEN CH_S(SCH_PEN)
#define CH_RSL CH_S(RSL)
#define CH_DL3 CH_S(DL3)
#define CH_UP CH_S(UP)
#define CH_DN CH_S(DN)
#define CH_DLX CH_S(DLX)
#define CH_LATE CH_S(LATE)
#define CH_DL1L "%[dl1l]"
#define CH_TKL CH_S(TKL)
#define CH_SCHW "656384"
#define CH_CAP "%[cap]"
#define CH_CTAB "%[ctab]"
#define CH_COW
#define CH_INW "s_sub_u32 s93, s48, 10\n\ts_cmp_lt_u32 s93, 50\n\t"
#define CH_ST1
#define CH_ST2
#define TOUCH 2
#define FDL 6
#define CH_FDL CH_S(FDL)
#define CH_TC0
#define CH_TCH
#define CH_ST3
#define CH_ST4
#define CH_TRC
#define CH_STW
#define CH_DRY "s_branch LLOAD_%=\n\t"
#define RFL(x) ((unsigned)__builtin_amdgcn_readfirstlane((int)(x)))
#define CH_DP0
#define CH_DP1
#define CH_DP2
#define CH_DP3
#define CH_DP4
#define CH_DP5
#define CH_DP6
#define CH_DP7
#define CH_DP8
#define CH_DP9
#define CH_DP10
#define CH_DP11
#define CH_DP12
#define CH_DP13
#define CH_DP14
#define CH_DP15
#define CH_DP16
#define CH_DP17
#define CH_OPS {1, 2, 3, 4, 5, 6, 7, 8, 17, 18, 20, 22, 23, 24, 64, 65, 67, 68, 69, 70, 71, 80, 81, 82, 83, 84, 85, 86, 87, 88, 128, 129, 130, 131, 132, 133, 134, 135, 136, 144, 145, 146, 147, 149, 150, 151, 195, 209, 210, 211, 227, 241, 242, 256, 0, 16, 19, 21, 66, 72, 148, 152, 208, 240, 192, 224, 193, 225, 194, 226, 243, 257}
#define CH_ASM \
  "s_mov_b32 s84, " CH_CAP "\n\t" \
  "s_mov_b32 s83, " CH_CAP "\n\t" \
  "s_mov_b32 s68, 0\n\t" \
  "s_mov_b32 s74, 0\n\t" \
  "s_mov_b32 s76, 0\n\t" \
  "s_mov_b32 s77, 0\n\t" \
  "s_mov_b32 s78, 0\n\t" \
  "s_mov_b32 s79, 0\n\t" \
  "s_mov_b32 s56, 0xcccccccc\n\t" \
  "s_mov_b32 s57, 0xcccccccc\n\t" \
  "s_mov_b32 s58, 0x33333333\n\t" \
  "s_mov_b32 s59, 0x33333333\n\t" \
  "s_mov_b32 s88, 0x3f803f80\n\t" \
  "s_mov_b32 s89, 0x3f803f80\n\t" \
  "s_mov_b32 s66, s65\n\t" \
  "s_mov_b32 s47, s65\n\t" \
  "s_mov_b32 s72, 0\n\t" \
  "s_mov_b32 s73, 0\n\t" \
  "s_mov_b32 s67, 0\n\t" \
  "s_mul_i32 s60, s40, 36864\n\t" \
  "s_add_u32 s60, s60, 16384\n\t" \
  "v_add_u32 v70, s60, v69\n\t" \
  "v_bfe_i32 v68, v69, 5, 1\n\t" \
  "v_and_b32 v68, 0xaaaaaaaa, v68\n\t" \
  "v_or_b32 v68, 0x44444444, v68\n\t" \
  "s_mov_b32 s35, s30\n\t" \
  "s_mov_b32 s30, 0\n\t" \
  "s_mov_b32 s34, 0\n\t" \
  CH_DRY \
  "s_cmp_eq_u32 s40, 0\n\t" \
  "s_cbranch_scc1 LW0_%=\n\t" \
  "s_barrier\n\t" \
  "s_cmp_eq_u32 s40, 1\n\t" \
  "s_cbranch_scc1 LG1_%=\n\t" \
  "s_cmp_eq_u32 s40, 2\n\t" \
  "s_cbranch_scc1 LG2_%=\n\t" \
  "s_branch LG3_%=\n\t" \
  "LLOAD_%=:\n\t" \
  "s_waitcnt vmcnt(0) lgkmcnt(0)\n\t" \
  "s_mov_b32 s30, s35\n\t" \
  "s_movk_i32 s34, 1000\n\t" \
  "v_bfe_u32 v0, v69, 8, 1\n\t" \
  "v_bfe_u32 v1, v69, 9, 1\n\t" \
  "v_lshlrev_b32 v0, 2, v0\n\t" \
  "v_add_u32 v0, v0, v1\n\t" \
  "v_mul_u32_u24 v0, 4608, v0\n\t" \
  "v_lshrrev_b32 v1, 1, v69\n\t" \
  "v_and_b32 v1, 0x60, v1\n\t" \
  "v_and_b32 v2, 16, v69\n\t" \
  "v_bfe_u32 v3, v69, 5, 1\n\t" \
  "v_add3_u32 v64, v0, v1, v2\n\t" \
  "v_lshl_add_u32 v64, v3, 2, v64\n\t" \
  "v_lshl_add_u32 v67, v3, 3, v0\n\t" \
  "v_bfe_u32 v1, v69, 7, 1\n\t" \
  "v_lshl_add_u32 v67, v1, 2, v67\n\t" \
  "v_bfe_u32 v1, v69, 6, 1\n\t" \
  "v_lshl_add_u32 v67, v1, 4, v67\n\t" \
  "v_bfe_u32 v1, v69, 4, 1\n\t" \
  "v_lshl_add_u32 v67, v1, 1, v67\n\t" \
  "v_lshrrev_b32 v71, 6, v69\n\t" \
  "v_lshlrev_b32 v71, 13, v71\n\t" \
  "v_and_b32 v1, 48, v69\n\t" \
  "v_add_u32 v71, v71, v1\n\t" \
  "v_and_b32 v1, 0x300, v69\n\t" \
  "v_add_u32 v65, v70, v1\n\t" \
  "v_and_b32 v1, 48, v69\n\t" \
  "v_lshrrev_b32 v1, 1, v1\n\t" \
  "v_sub_u32 v66, v65, v1\n\t" \
  "s_mul_i32 s54, s40, 36864\n\t" \
  "s_add_u32 s54, s54, 16384\n\t" \
  "s_lshl_b32 s55, s40, 7\n\t" \
  "s_lshl_b32 s75, s40, 5\n\t" \
  "s_add_u32 s75, s75, 1024\n\t" \
  "s_mov_b32 s62, 0x05040100\n\t" \
  "s_mov_b32 s63, 0x07060302\n\t" \
  "s_add_u32 m0, s54, 0\n\t" \
  "s_mov_b32 s60, 0\n\t" \
  "buffer_load_dwordx4 v71, %[Wq], s60 offen lds\n\t" \
  "s_add_u32 m0, s54, 1024\n\t" \
  "s_mov_b32 s60, 131072\n\t" \
  "buffer_load_dwordx4 v71, %[Wq], s60 offen lds\n\t" \
  "s_add_u32 m0, s54, 2048\n\t" \
  "s_mov_b32 s60, 262144\n\t" \
  "buffer_load_dwordx4 v71, %[Wq], s60 offen lds\n\t" \
  "s_add_u32 m0, s54, 3072\n\t" \
  "s_mov_b32 s60, 393216\n\t" \
  "buffer_load_dwordx4 v71, %[Wq], s60 offen lds\n\t" \
  "s_add_u32 m0, s54, 4096\n\t" \
  "s_mov_b32 s60, 0\n\t" \
  "buffer_load_dwordx4 v71, %[Wk], s60 offen lds\n\t" \
  "s_add_u32 m0, s54, 5120\n\t" \
  "s_mov_b32 s60, 131072\n\t" \
  "buffer_load_dwordx4 v71, %[Wk], s60 offen lds\n\t" \
  "s_add_u32 m0, s54, 6144\n\t" \
  "s_mov_b32 s60, 262144\n\t" \
  "buffer_load_dwordx4 v71, %[Wk], s60 offen lds\n\t" \
  "s_add_u32 m0, s54, 7168\n\t" \
  "s_mov_b32 s60, 393216\n\t" \
  "buffer_load_dwordx4 v71, %[Wk], s60 offen lds\n\t" \
  "s_add_u32 m0, s54, 8192\n\t" \
  "s_mov_b32 s60, 0\n\t" \
  "buffer_load_dwordx4 v71, %[Wv], s60 offen lds\n\t" \
  "s_add_u32 m0, s54, 9216\n\t" \
  "s_mov_b32 s60, 131072\n\t" \
  "buffer_load_dwordx4 v71, %[Wv], s60 offen lds\n\t" \
  "s_add_u32 m0, s54, 10240\n\t" \
  "s_mov_b32 s60, 262144\n\t" \
  "buffer_load_dwordx4 v71, %[Wv], s60 offen lds\n\t" \
  "s_add_u32 m0, s54, 11264\n\t" \
  "s_mov_b32 s60, 393216\n\t" \
  "buffer_load_dwordx4 v71, %[Wv], s60 offen lds\n\t" \
  "s_add_u32 m0, s54, 12288\n\t" \
  "s_mov_b32 s60, 524288\n\t" \
  "buffer_load_dwordx4 v71, %[Wq], s60 offen lds\n\t" \
  "s_add_u32 m0, s54, 13312\n\t" \
  "s_mov_b32 s60, 655360\n\t" \
  "buffer_load_dwordx4 v71, %[Wq], s60 offen lds\n\t" \
  "s_add_u32 m0, s54, 14336\n\t" \
  "s_mov_b32 s60, 786432\n\t" \
  "buffer_load_dwordx4 v71, %[Wq], s60 offen lds\n\t" \
  "s_add_u32 m0, s54, 15360\n\t" \
  "s_mov_b32 s60, 917504\n\t" \
  "buffer_load_dwordx4 v71, %[Wq], s60 offen lds\n\t" \
  "s_add_u32 m0, s54, 16384\n\t" \
  "s_mov_b32 s60, 524288\n\t" \
  "buffer_load_dwordx4 v71, %[Wk], s60 offen lds\n\t" \
  "s_add_u32 m0, s54, 17408\n\t" \
  "s_mov_b32 s60, 655360\n\t" \
  "buffer_load_dwordx4 v71, %[Wk], s60 offen lds\n\t" \
  "s_add_u32 m0, s54, 18432\n\t" \
  "s_mov_b32 s60, 786432\n\t" \
  "buffer_load_dwordx4 v71, %[Wk], s60 offen lds\n\t" \
  "s_add_u32 m0, s54, 19456\n\t" \
  "s_mov_b32 s60, 917504\n\t" \
  "buffer_load_dwordx4 v71, %[Wk], s60 offen lds\n\t" \
  "s_add_u32 m0, s54, 20480\n\t" \
  "s_mov_b32 s60, 524288\n\t" \
  "buffer_load_dwordx4 v71, %[Wv], s60 offen lds\n\t" \
  "s_add_u32 m0, s54, 21504\n\t" \
  "s_mov_b32 s60, 655360\n\t" \
  "buffer_load_dwordx4 v71, %[Wv], s60 offen lds\n\t" \
  "s_add_u32 m0, s54, 22528\n\t" \
  "s_mov_b32 s60, 786432\n\t" \
  "buffer_load_dwordx4 v71, %[Wv], s60 offen lds\n\t" \
  "s_add_u32 m0, s54, 23552\n\t" \
  "s_mov_b32 s60, 917504\n\t" \
  "buffer_load_dwordx4 v71, %[Wv], s60 offen lds\n\t" \
  "s_add_u32 m0, s54, 24576\n\t" \
  "s_mov_b32 s60, 1048576\n\t" \
  "buffer_load_dwordx4 v71, %[Wq], s60 offen lds\n\t" \
  "s_add_u32 m0, s54, 25600\n\t" \
  "s_mov_b32 s60, 1179648\n\t" \
  "buffer_load_dwordx4 v71, %[Wq], s60 offen lds\n\t" \
  "s_add_u32 m0, s54, 26624\n\t" \
  "s_mov_b32 s60, 1310720\n\t" \
  "buffer_load_dwordx4 v71, %[Wq], s60 offen lds\n\t" \
  "s_add_u32 m0, s54, 27648\n\t" \
  "s_mov_b32 s60, 1441792\n\t" \
  "buffer_load_dwordx4 v71, %[Wq], s60 offen lds\n\t" \
  "s_add_u32 m0, s54, 28672\n\t" \
  "s_mov_b32 s60, 1048576\n\t" \
  "buffer_load_dwordx4 v71, %[Wk], s60 offen lds\n\t" \
  "s_add_u32 m0, s54, 29696\n\t" \
  "s_mov_b32 s60, 1179648\n\t" \
  "buffer_load_dwordx4 v71, %[Wk], s60 offen lds\n\t" \
  "s_add_u32 m0, s54, 30720\n\t" \
  "s_mov_b32 s60, 1310720\n\t" \
  "buffer_load_dwordx4 v71, %[Wk], s60 offen lds\n\t" \
  "s_add_u32 m0, s54, 31744\n\t" \
  "s_mov_b32 s60, 1441792\n\t" \
  "buffer_load_dwordx4 v71, %[Wk], s60 offen lds\n\t" \
  "s_add_u32 m0, s54, 32768\n\t" \
  "s_mov_b32 s60, 1048576\n\t" \
  "buffer_load_dwordx4 v71, %[Wv], s60 offen lds\n\t" \
  "s_add_u32 m0, s54, 33792\n\t" \
  "s_mov_b32 s60, 1179648\n\t" \
  "buffer_load_dwordx4 v71, %[Wv], s60 offen lds\n\t" \
  "s_add_u32 m0, s54, 34816\n\t" \
  "s_mov_b32 s60, 1310720\n\t" \
  "buffer_load_dwordx4 v71, %[Wv], s60 offen lds\n\t" \
  "s_add_u32 m0, s54, 35840\n\t" \
  "s_mov_b32 s60, 1441792\n\t" \
  "buffer_load_dwordx4 v71, %[Wv], s60 offen lds\n\t" \
  "s_waitcnt vmcnt(32)\n\t" \
  "ds_read_b64_tr_b16 v[0:1], v65 offset:0\n\t" \
  "ds_read_b64_tr_b16 v[2:3], v65 offset:256\n\t" \
  "ds_read_b64_tr_b16 v[4:5], v65 offset:2048\n\t" \
  "ds_read_b64_tr_b16 v[6:7], v65 offset:2304\n\t" \
  "ds_read_b64_tr_b16 v[8:9], v65 offset:8\n\t" \
  "ds_read_b64_tr_b16 v[10:11], v65 offset:264\n\t" \
  "ds_read_b64_tr_b16 v[12:13], v65 offset:2056\n\t" \
  "ds_read_b64_tr_b16 v[14:15], v65 offset:2312\n\t" \
  "s_waitcnt lgkmcnt(0)\n\t" \
  "s_add_u32 m0, s54, 0\n\t" \
  "s_mov_b32 s60, 1572864\n\t" \
  "buffer_load_dwordx4 v71, %[Wq], s60 offen lds\n\t" \
  "s_add_u32 m0, s54, 1024\n\t" \
  "s_mov_b32 s60, 1703936\n\t" \
  "buffer_load_dwordx4 v71, %[Wq], s60 offen lds\n\t" \
  "s_add_u32 m0, s54, 2048\n\t" \
  "s_mov_b32 s60, 1835008\n\t" \
  "buffer_load_dwordx4 v71, %[Wq], s60 offen lds\n\t" \
  "s_add_u32 m0, s54, 3072\n\t" \
  "s_mov_b32 s60, 1966080\n\t" \
  "buffer_load_dwordx4 v71, %[Wq], s60 offen lds\n\t" \
  "s_waitcnt vmcnt(32)\n\t" \
  "ds_read_b64_tr_b16 a[112:113], v65 offset:4096\n\t" \
  "ds_read_b64_tr_b16 a[114:115], v65 offset:4352\n\t" \
  "ds_read_b64_tr_b16 a[116:117], v65 offset:6144\n\t" \
  "ds_read_b64_tr_b16 a[118:119], v65 offset:6400\n\t" \
  "ds_read_b64_tr_b16 a[168:169], v65 offset:4104\n\t" \
  "ds_read_b64_tr_b16 a[170:171], v65 offset:4360\n\t" \
  "ds_read_b64_tr_b16 a[172:173], v65 offset:6152\n\t" \
  "ds_read_b64_tr_b16 a[174:175], v65 offset:6408\n\t" \
  "s_waitcnt lgkmcnt(0)\n\t" \
  "s_add_u32 m0, s54, 4096\n\t" \
  "s_mov_b32 s60, 1572864\n\t" \
  "buffer_load_dwordx4 v71, %[Wk], s60 offen lds\n\t" \
  "s_add_u32 m0, s54, 5120\n\t" \
  "s_mov_b32 s60, 1703936\n\t" \
  "buffer_load_dwordx4 v71, %[Wk], s60 offen lds\n\t" \
  "s_add_u32 m0, s54, 6144\n\t" \
  "s_mov_b32 s60, 1835008\n\t" \
  "buffer_load_dwordx4 v71, %[Wk], s60 offen lds\n\t" \
  "s_add_u32 m0, s54, 7168\n\t" \
  "s_mov_b32 s60, 1966080\n\t" \
  "buffer_load_dwordx4 v71, %[Wk], s60 offen lds\n\t" \
  "s_waitcnt vmcnt(32)\n\t" \
  "ds_read_b64_tr_b16 a[240:241], v66 offset:8192\n\t" \
  "ds_read_b64_tr_b16 a[242:243], v66 offset:8448\n\t" \
  "ds_read_b64_tr_b16 a[244:245], v66 offset:10240\n\t" \
  "ds_read_b64_tr_b16 a[246:247], v66 offset:10496\n\t" \
  "ds_read_b64_tr_b16 v[136:137], v66 offset:8224\n\t" \
  "ds_read_b64_tr_b16 v[138:139], v66 offset:8480\n\t" \
  "ds_read_b64_tr_b16 v[140:141], v66 offset:10272\n\t" \
  "ds_read_b64_tr_b16 v[142:143], v66 offset:10528\n\t" \
  "s_waitcnt lgkmcnt(0)\n\t" \
  "s_add_u32 m0, s54, 8192\n\t" \
  "s_mov_b32 s60, 1572864\n\t" \
  "buffer_load_dwordx4 v71, %[Wv], s60 offen lds\n\t" \
  "s_add_u32 m0, s54, 9216\n\t" \
  "s_mov_b32 s60, 1703936\n\t" \
  "buffer_load_dwordx4 v71, %[Wv], s60 offen lds\n\t" \
  "s_add_u32 m0, s54, 10240\n\t" \
  "s_mov_b32 s60, 1835008\n\t" \
  "buffer_load_dwordx4 v71, %[Wv], s60 offen lds\n\t" \
  "s_add_u32 m0, s54, 11264\n\t" \
  "s_mov_b32 s60, 1966080\n\t" \
  "buffer_load_dwordx4 v71, %[Wv], s60 offen lds\n\t" \
  "s_waitcnt vmcnt(32)\n\t" \
  "ds_read_b64_tr_b16 a[0:1], v65 offset:12288\n\t" \
  "ds_read_b64_tr_b16 a[2:3], v65 offset:12544\n\t" \
  "ds_read_b64_tr_b16 a[4:5], v65 offset:14336\n\t" \
  "ds_read_b64_tr_b16 a[6:7], v65 offset:14592\n\t" \
  "ds_read_b64_tr_b16 a[64:65], v65 offset:12296\n\t" \
  "ds_read_b64_tr_b16 a[66:67], v65 offset:12552\n\t" \
  "ds_read_b64_tr_b16 a[68:69], v65 offset:14344\n\t" \
  "ds_read_b64_tr_b16 a[70:71], v65 offset:14600\n\t" \
  "s_waitcnt lgkmcnt(0)\n\t" \
  "s_add_u32 m0, s54, 12288\n\t" \
  "s_mov_b32 s60, 2097152\n\t" \
  "buffer_load_dwordx4 v71, %[Wq], s60 offen lds\n\t" \
  "s_add_u32 m0, s54, 13312\n\t" \
  "s_mov_b32 s60, 2228224\n\t" \
  "buffer_load_dwordx4 v71, %[Wq], s60 offen lds\n\t" \
  "s_add_u32 m0, s54, 14336\n\t" \
  "s_mov_b32 s60, 2359296\n\t" \
  "buffer_load_dwordx4 v71, %[Wq], s60 offen lds\n\t" \
  "s_add_u32 m0, s54, 15360\n\t" \
  "s_mov_b32 s60, 2490368\n\t" \
  "buffer_load_dwordx4 v71, %[Wq], s60 offen lds\n\t" \
  "s_waitcnt vmcnt(32)\n\t" \
  "ds_read_b64_tr_b16 a[120:121], v65 offset:16384\n\t" \
  "ds_read_b64_tr_b16 a[122:123], v65 offset:16640\n\t" \
  "ds_read_b64_tr_b16 a[124:125], v65 offset:18432\n\t" \
  "ds_read_b64_tr_b16 a[126:127], v65 offset:18688\n\t" \
  "ds_read_b64_tr_b16 a[176:177], v65 offset:16392\n\t" \
  "ds_read_b64_tr_b16 a[178:179], v65 offset:16648\n\t" \
  "ds_read_b64_tr_b16 a[180:181], v65 offset:18440\n\t" \
  "ds_read_b64_tr_b16 a[182:183], v65 offset:18696\n\t" \
  "s_waitcnt lgkmcnt(0)\n\t" \
  "s_add_u32 m0, s54, 16384\n\t" \
  "s_mov_b32 s60, 2097152\n\t" \
  "buffer_load_dwordx4 v71, %[Wk], s60 offen lds\n\t" \
  "s_add_u32 m0, s54, 17408\n\t" \
  "s_mov_b32 s60, 2228224\n\t" \
  "buffer_load_dwordx4 v71, %[Wk], s60 offen lds\n\t" \
  "s_add_u32 m0, s54, 18432\n\t" \
  "s_mov_b32 s60, 2359296\n\t" \
  "buffer_load_dwordx4 v71, %[Wk], s60 offen lds\n\t" \
  "s_add_u32 m0, s54, 19456\n\t" \
  "s_mov_b32 s60, 2490368\n\t" \
  "buffer_load_dwordx4 v71, %[Wk], s60 offen lds\n\t" \
  "s_waitcnt vmcnt(32)\n\t" \
  "ds_read_b64_tr_b16 a[248:249], v66 offset:20480\n\t" \
  "ds_read_b64_tr_b16 a[250:251], v66 offset:20736\n\t" \
  "ds_read_b64_tr_b16 a[252:253], v66 offset:22528\n\t" \
  "ds_read_b64_tr_b16 a[254:255], v66 offset:22784\n\t" \
  "ds_read_b64_tr_b16 v[144:145], v66 offset:20512\n\t" \
  "ds_read_b64_tr_b16 v[146:147], v66 offset:20768\n\t" \
  "ds_read_b64_tr_b16 v[148:149], v66 offset:22560\n\t" \
  "ds_read_b64_tr_b16 v[150:151], v66 offset:22816\n\t" \
  "s_waitcnt lgkmcnt(0)\n\t" \
  "s_add_u32 m0, s54, 20480\n\t" \
  "s_mov_b32 s60, 2097152\n\t" \
  "buffer_load_dwordx4 v71, %[Wv], s60 offen lds\n\t" \
  "s_add_u32 m0, s54, 21504\n\t" \
  "s_mov_b32 s60, 2228224\n\t" \
  "buffer_load_dwordx4 v71, %[Wv], s60 offen lds\n\t" \
  "s_add_u32 m0, s54, 22528\n\t" \
  "s_mov_b32 s60, 2359296\n\t" \
  "buffer_load_dwordx4 v71, %[Wv], s60 offen lds\n\t" \
  "s_add_u32 m0, s54, 23552\n\t" \
  "s_mov_b32 s60, 2490368\n\t" \
  "buffer_load_dwordx4 v71, %[Wv], s60 offen lds\n\t" \
  "s_waitcnt vmcnt(32)\n\t" \
  "ds_read_b64_tr_b16 a[8:9], v65 offset:24576\n\t" \
  "ds_read_b64_tr_b16 a[10:11], v65 offset:24832\n\t" \
  "ds_read_b64_tr_b16 a[12:13], v65 offset:26624\n\t" \
  "ds_read_b64_tr_b16 a[14:15], v65 offset:26880\n\t" \
  "ds_read_b64_tr_b16 a[72:73], v65 offset:24584\n\t" \
  "ds_read_b64_tr_b16 a[74:75], v65 offset:24840\n\t" \
  "ds_read_b64_tr_b16 a[76:77], v65 offset:26632\n\t" \
  "ds_read_b64_tr_b16 a[78:79], v65 offset:26888\n\t" \
  "s_waitcnt lgkmcnt(0)\n\t" \
  "s_add_u32 m0, s54, 24576\n\t" \
  "s_mov_b32 s60, 2621440\n\t" \
  "buffer_load_dwordx4 v71, %[Wq], s60 offen lds\n\t" \
  "s_add_u32 m0, s54, 25600\n\t" \
  "s_mov_b32 s60, 2752512\n\t" \
  "buffer_load_dwordx4 v71, %[Wq], s60 offen lds\n\t" \
  "s_add_u32 m0, s54, 26624\n\t" \
  "s_mov_b32 s60, 2883584\n\t" \
  "buffer_load_dwordx4 v71, %[Wq], s60 offen lds\n\t" \
  "s_add_u32 m0, s54, 27648\n\t" \
  "s_mov_b32 s60, 3014656\n\t" \
  "buffer_load_dwordx4 v71, %[Wq], s60 offen lds\n\t" \
  "s_waitcnt vmcnt(32)\n\t" \
  "ds_read_b64_tr_b16 v[32:33], v65 offset:28672\n\t" \
  "ds_read_b64_tr_b16 v[34:35], v65 offset:28928\n\t" \
  "ds_read_b64_tr_b16 v[36:37], v65 offset:30720\n\t" \
  "ds_read_b64_tr_b16 v[38:39], v65 offset:30976\n\t" \
  "ds_read_b64_tr_b16 a[184:185], v65 offset:28680\n\t" \
  "ds_read_b64_tr_b16 a[186:187], v65 offset:28936\n\t" \
  "ds_read_b64_tr_b16 a[188:189], v65 offset:30728\n\t" \
  "ds_read_b64_tr_b16 a[190:191], v65 offset:30984\n\t" \
  "s_waitcnt lgkmcnt(0)\n\t" \
  "s_add_u32 m0, s54, 28672\n\t" \
  "s_mov_b32 s60, 2621440\n\t" \
  "buffer_load_dwordx4 v71, %[Wk], s60 offen lds\n\t" \
  "s_add_u32 m0, s54, 29696\n\t" \
  "s_mov_b32 s60, 2752512\n\t" \
  "buffer_load_dwordx4 v71, %[Wk], s60 offen lds\n\t" \
  "s_add_u32 m0, s54, 30720\n\t" \
  "s_mov_b32 s60, 2883584\n\t" \
  "buffer_load_dwordx4 v71, %[Wk], s60 offen lds\n\t" \
  "s_add_u32 m0, s54, 31744\n\t" \
  "s_mov_b32 s60, 3014656\n\t" \
  "buffer_load_dwordx4 v71, %[Wk], s60 offen lds\n\t" \
  "s_waitcnt vmcnt(32)\n\t" \
  "ds_read_b64_tr_b16 v[80:81], v66 offset:32768\n\t" \
  "ds_read_b64_tr_b16 v[82:83], v66 offset:33024\n\t" \
  "ds_read_b64_tr_b16 v[84:85], v66 offset:34816\n\t" \
  "ds_read_b64_tr_b16 v[86:87], v66 offset:35072\n\t" \
  "ds_read_b64_tr_b16 v[152:153], v66 offset:32800\n\t" \
  "ds_read_b64_tr_b16 v[154:155], v66 offset:33056\n\t" \
  "ds_read_b64_tr_b16 v[156:157], v66 offset:34848\n\t" \
  "ds_read_b64_tr_b16 v[158:159], v66 offset:35104\n\t" \
  "s_waitcnt lgkmcnt(0)\n\t" \
  "s_add_u32 m0, s54, 32768\n\t" \
  "s_mov_b32 s60, 2621440\n\t" \
  "buffer_load_dwordx4 v71, %[Wv], s60 offen lds\n\t" \
  "s_add_u32 m0, s54, 33792\n\t" \
  "s_mov_b32 s60, 2752512\n\t" \
  "buffer_load_dwordx4 v71, %[Wv], s60 offen lds\n\t" \
  "s_add_u32 m0, s54, 34816\n\t" \
  "s_mov_b32 s60, 2883584\n\t" \
  "buffer_load_dwordx4 v71, %[Wv], s60 offen lds\n\t" \
  "s_add_u32 m0, s54, 35840\n\t" \
  "s_mov_b32 s60, 3014656\n\t" \
  "buffer_load_dwordx4 v71, %[Wv], s60 offen lds\n\t" \
  "s_waitcnt vmcnt(32)\n\t" \
  "ds_read_b64_tr_b16 a[16:17], v65 offset:0\n\t" \
  "ds_read_b64_tr_b16 a[18:19], v65 offset:256\n\t" \
  "ds_read_b64_tr_b16 a[20:21], v65 offset:2048\n\t" \
  "ds_read_b64_tr_b16 a[22:23], v65 offset:2304\n\t" \
  "ds_read_b64_tr_b16 v[16:17], v65 offset:8\n\t" \
  "ds_read_b64_tr_b16 v[18:19], v65 offset:264\n\t" \
  "ds_read_b64_tr_b16 v[20:21], v65 offset:2056\n\t" \
  "ds_read_b64_tr_b16 v[22:23], v65 offset:2312\n\t" \
  "s_waitcnt lgkmcnt(0)\n\t" \
  "s_add_u32 m0, s54, 0\n\t" \
  "s_mov_b32 s60, 3145728\n\t" \
  "buffer_load_dwordx4 v71, %[Wq], s60 offen lds\n\t" \
  "s_add_u32 m0, s54, 1024\n\t" \
  "s_mov_b32 s60, 3276800\n\t" \
  "buffer_load_dwordx4 v71, %[Wq], s60 offen lds\n\t" \
  "s_add_u32 m0, s54, 2048\n\t" \
  "s_mov_b32 s60, 3407872\n\t" \
  "buffer_load_dwordx4 v71, %[Wq], s60 offen lds\n\t" \
  "s_add_u32 m0, s54, 3072\n\t" \
  "s_mov_b32 s60, 3538944\n\t" \
  "buffer_load_dwordx4 v71, %[Wq], s60 offen lds\n\t" \
  "s_waitcnt vmcnt(32)\n\t" \
  "ds_read_b64_tr_b16 a[128:129], v65 offset:4096\n\t" \
  "ds_read_b64_tr_b16 a[130:131], v65 offset:4352\n\t" \
  "ds_read_b64_tr_b16 a[132:133], v65 offset:6144\n\t" \
  "ds_read_b64_tr_b16 a[134:135], v65 offset:6400\n\t" \
  "ds_read_b64_tr_b16 a[192:193], v65 offset:4104\n\t" \
  "ds_read_b64_tr_b16 a[194:195], v65 offset:4360\n\t" \
  "ds_read_b64_tr_b16 a[196:197], v65 offset:6152\n\t" \
  "ds_read_b64_tr_b16 a[198:199], v65 offset:6408\n\t" \
  "s_waitcnt lgkmcnt(0)\n\t" \
  "s_add_u32 m0, s54, 4096\n\t" \
  "s_mov_b32 s60, 3145728\n\t" \
  "buffer_load_dwordx4 v71, %[Wk], s60 offen lds\n\t" \
  "s_add_u32 m0, s54, 5120\n\t" \
  "s_mov_b32 s60, 3276800\n\t" \
  "buffer_load_dwordx4 v71, %[Wk], s60 offen lds\n\t" \
  "s_add_u32 m0, s54, 6144\n\t" \
  "s_mov_b32 s60, 3407872\n\t" \
  "buffer_load_dwordx4 v71, %[Wk], s60 offen lds\n\t" \
  "s_add_u32 m0, s54, 7168\n\t" \
  "s_mov_b32 s60, 3538944\n\t" \
  "buffer_load_dwordx4 v71, %[Wk], s60 offen lds\n\t" \
  "s_waitcnt vmcnt(32)\n\t" \
  "ds_read_b64_tr_b16 v[88:89], v66 offset:8192\n\t" \
  "ds_read_b64_tr_b16 v[90:91], v66 offset:8448\n\t" \
  "ds_read_b64_tr_b16 v[92:93], v66 offset:10240\n\t" \
  "ds_read_b64_tr_b16 v[94:95], v66 offset:10496\n\t" \
  "ds_read_b64_tr_b16 v[160:161], v66 offset:8224\n\t" \
  "ds_read_b64_tr_b16 v[162:163], v66 offset:8480\n\t" \
  "ds_read_b64_tr_b16 v[164:165], v66 offset:10272\n\t" \
  "ds_read_b64_tr_b16 v[166:167], v66 offset:10528\n\t" \
  "s_waitcnt lgkmcnt(0)\n\t" \
  "s_add_u32 m0, s54, 8192\n\t" \
  "s_mov_b32 s60, 3145728\n\t" \
  "buffer_load_dwordx4 v71, %[Wv], s60 offen lds\n\t" \
  "s_add_u32 m0, s54, 9216\n\t" \
  "s_mov_b32 s60, 3276800\n\t" \
  "buffer_load_dwordx4 v71, %[Wv], s60 offen lds\n\t" \
  "s_add_u32 m0, s54, 10240\n\t" \
  "s_mov_b32 s60, 3407872\n\t" \
  "buffer_load_dwordx4 v71, %[Wv], s60 offen lds\n\t" \
  "s_add_u32 m0, s54, 11264\n\t" \
  "s_mov_b32 s60, 3538944\n\t" \
  "buffer_load_dwordx4 v71, %[Wv], s60 offen lds\n\t" \
  "s_waitcnt vmcnt(32)\n\t" \
  "ds_read_b64_tr_b16 a[24:25], v65 offset:12288\n\t" \
  "ds_read_b64_tr_b16 a[26:27], v65 offset:12544\n\t" \
  "ds_read_b64_tr_b16 a[28:29], v65 offset:14336\n\t" \
  "ds_read_b64_tr_b16 a[30:31], v65 offset:14592\n\t" \
  "ds_read_b64_tr_b16 a[80:81], v65 offset:12296\n\t" \
  "ds_read_b64_tr_b16 a[82:83], v65 offset:12552\n\t" \
  "ds_read_b64_tr_b16 a[84:85], v65 offset:14344\n\t" \
  "ds_read_b64_tr_b16 a[86:87], v65 offset:14600\n\t" \
  "s_waitcnt lgkmcnt(0)\n\t" \
  "s_add_u32 m0, s54, 12288\n\t" \
  "s_mov_b32 s60, 3670016\n\t" \
  "buffer_load_dwordx4 v71, %[Wq], s60 offen lds\n\t" \
  "s_add_u32 m0, s54, 13312\n\t" \
  "s_mov_b32 s60, 3801088\n\t" \
  "buffer_load_dwordx4 v71, %[Wq], s60 offen lds\n\t" \
  "s_add_u32 m0, s54, 14336\n\t" \
  "s_mov_b32 s60, 3932160\n\t" \
  "buffer_load_dwordx4 v71, %[Wq], s60 offen lds\n\t" \
  "s_add_u32 m0, s54, 15360\n\t" \
  "s_mov_b32 s60, 4063232\n\t" \
  "buffer_load_dwordx4 v71, %[Wq], s60 offen lds\n\t" \
  "s_waitcnt vmcnt(32)\n\t" \
  "ds_read_b64_tr_b16 a[136:137], v65 offset:16384\n\t" \
  "ds_read_b64_tr_b16 a[138:139], v65 offset:16640\n\t" \
  "ds_read_b64_tr_b16 a[140:141], v65 offset:18432\n\t" \
  "ds_read_b64_tr_b16 a[142:143], v65 offset:18688\n\t" \
  "ds_read_b64_tr_b16 a[200:201], v65 offset:16392\n\t" \
  "ds_read_b64_tr_b16 a[202:203], v65 offset:16648\n\t" \
  "ds_read_b64_tr_b16 a[204:205], v65 offset:18440\n\t" \
  "ds_read_b64_tr_b16 a[206:207], v65 offset:18696\n\t" \
  "s_waitcnt lgkmcnt(0)\n\t" \
  "s_add_u32 m0, s54, 16384\n\t" \
  "s_mov_b32 s60, 3670016\n\t" \
  "buffer_load_dwordx4 v71, %[Wk], s60 offen lds\n\t" \
  "s_add_u32 m0, s54, 17408\n\t" \
  "s_mov_b32 s60, 3801088\n\t" \
  "buffer_load_dwordx4 v71, %[Wk], s60 offen lds\n\t" \
  "s_add_u32 m0, s54, 18432\n\t" \
  "s_mov_b32 s60, 3932160\n\t" \
  "buffer_load_dwordx4 v71, %[Wk], s60 offen lds\n\t" \
  "s_add_u32 m0, s54, 19456\n\t" \
  "s_mov_b32 s60, 4063232\n\t" \
  "buffer_load_dwordx4 v71, %[Wk], s60 offen lds\n\t" \
  "s_waitcnt vmcnt(32)\n\t" \
  "ds_read_b64_tr_b16 v[96:97], v66 offset:20480\n\t" \
  "ds_read_b64_tr_b16 v[98:99], v66 offset:20736\n\t" \
  "ds_read_b64_tr_b16 v[100:101], v66 offset:22528\n\t" \
  "ds_read_b64_tr_b16 v[102:103], v66 offset:22784\n\t" \
  "ds_read_b64_tr_b16 v[48:49], v66 offset:20512\n\t" \
  "ds_read_b64_tr_b16 v[50:51], v66 offset:20768\n\t" \
  "ds_read_b64_tr_b16 v[52:53], v66 offset:22560\n\t" \
  "ds_read_b64_tr_b16 v[54:55], v66 offset:22816\n\t" \
  "s_waitcnt lgkmcnt(0)\n\t" \
  "s_add_u32 m0, s54, 20480\n\t" \
  "s_mov_b32 s60, 3670016\n\t" \
  "buffer_load_dwordx4 v71, %[Wv], s60 offen lds\n\t" \
  "s_add_u32 m0, s54, 21504\n\t" \
  "s_mov_b32 s60, 3801088\n\t" \
  "buffer_load_dwordx4 v71, %[Wv], s60 offen lds\n\t" \
  "s_add_u32 m0, s54, 22528\n\t" \
  "s_mov_b32 s60, 3932160\n\t" \
  "buffer_load_dwordx4 v71, %[Wv], s60 offen lds\n\t" \
  "s_add_u32 m0, s54, 23552\n\t" \
  "s_mov_b32 s60, 4063232\n\t" \
  "buffer_load_dwordx4 v71, %[Wv], s60 offen lds\n\t" \
  "s_waitcnt vmcnt(32)\n\t" \
  "ds_read_b64_tr_b16 a[32:33], v65 offset:24576\n\t" \
  "ds_read_b64_tr_b16 a[34:35], v65 offset:24832\n\t" \
  "ds_read_b64_tr_b16 a[36:37], v65 offset:26624\n\t" \
  "ds_read_b64_tr_b16 a[38:39], v65 offset:26880\n\t" \
  "ds_read_b64_tr_b16 v[24:25], v65 offset:24584\n\t" \
  "ds_read_b64_tr_b16 v[26:27], v65 offset:24840\n\t" \
  "ds_read_b64_tr_b16 v[28:29], v65 offset:26632\n\t" \
  "ds_read_b64_tr_b16 v[30:31], v65 offset:26888\n\t" \
  "s_waitcnt lgkmcnt(0)\n\t" \
  "s_add_u32 m0, s54, 24576\n\t" \
  "s_mov_b32 s60, 4194304\n\t" \
  "buffer_load_dwordx4 v71, %[Wq], s60 offen lds\n\t" \
  "s_add_u32 m0, s54, 25600\n\t" \
  "s_mov_b32 s60, 4325376\n\t" \
  "buffer_load_dwordx4 v71, %[Wq], s60 offen lds\n\t" \
  "s_add_u32 m0, s54, 26624\n\t" \
  "s_mov_b32 s60, 4456448\n\t" \
  "buffer_load_dwordx4 v71, %[Wq], s60 offen lds\n\t" \
  "s_add_u32 m0, s54, 27648\n\t" \
  "s_mov_b32 s60, 4587520\n\t" \
  "buffer_load_dwordx4 v71, %[Wq], s60 offen lds\n\t" \
  "s_waitcnt vmcnt(32)\n\t" \
  "ds_read_b64_tr_b16 a[144:145], v65 offset:28672\n\t" \
  "ds_read_b64_tr_b16 a[146:147], v65 offset:28928\n\t" \
  "ds_read_b64_tr_b16 a[148:149], v65 offset:30720\n\t" \
  "ds_read_b64_tr_b16 a[150:151], v65 offset:30976\n\t" \
  "ds_read_b64_tr_b16 a[208:209], v65 offset:28680\n\t" \
  "ds_read_b64_tr_b16 a[210:211], v65 offset:28936\n\t" \
  "ds_read_b64_tr_b16 a[212:213], v65 offset:30728\n\t" \
  "ds_read_b64_tr_b16 a[214:215], v65 offset:30984\n\t" \
  "s_waitcnt lgkmcnt(0)\n\t" \
  "s_add_u32 m0, s54, 28672\n\t" \
  "s_mov_b32 s60, 4194304\n\t" \
  "buffer_load_dwordx4 v71, %[Wk], s60 offen lds\n\t" \
  "s_add_u32 m0, s54, 29696\n\t" \
  "s_mov_b32 s60, 4325376\n\t" \
  "buffer_load_dwordx4 v71, %[Wk], s60 offen lds\n\t" \
  "s_add_u32 m0, s54, 30720\n\t" \
  "s_mov_b32 s60, 4456448\n\t" \
  "buffer_load_dwordx4 v71, %[Wk], s60 offen lds\n\t" \
  "s_add_u32 m0, s54, 31744\n\t" \
  "s_mov_b32 s60, 4587520\n\t" \
  "buffer_load_dwordx4 v71, %[Wk], s60 offen lds\n\t" \
  "s_waitcnt vmcnt(32)\n\t" \
  "ds_read_b64_tr_b16 v[104:105], v66 offset:32768\n\t" \
  "ds_read_b64_tr_b16 v[106:107], v66 offset:33024\n\t" \
  "ds_read_b64_tr_b16 v[108:109], v66 offset:34816\n\t" \
  "ds_read_b64_tr_b16 v[110:111], v66 offset:35072\n\t" \
  "ds_read_b64_tr_b16 v[168:169], v66 offset:32800\n\t" \
  "ds_read_b64_tr_b16 v[170:171], v66 offset:33056\n\t" \
  "ds_read_b64_tr_b16 v[172:173], v66 offset:34848\n\t" \
  "ds_read_b64_tr_b16 v[174:175], v66 offset:35104\n\t" \
  "s_waitcnt lgkmcnt(0)\n\t" \
  "s_add_u32 m0, s54, 32768\n\t" \
  "s_mov_b32 s60, 4194304\n\t" \
  "buffer_load_dwordx4 v71, %[Wv], s60 offen lds\n\t" \
  "s_add_u32 m0, s54, 33792\n\t" \
  "s_mov_b32 s60, 4325376\n\t" \
  "buffer_load_dwordx4 v71, %[Wv], s60 offen lds\n\t" \
  "s_add_u32 m0, s54, 34816\n\t" \
  "s_mov_b32 s60, 4456448\n\t" \
  "buffer_load_dwordx4 v71, %[Wv], s60 offen lds\n\t" \
  "s_add_u32 m0, s54, 35840\n\t" \
  "s_mov_b32 s60, 4587520\n\t" \
  "buffer_load_dwordx4 v71, %[Wv], s60 offen lds\n\t" \
  "s_waitcnt vmcnt(32)\n\t" \
  "ds_read_b64_tr_b16 a[40:41], v65 offset:0\n\t" \
  "ds_read_b64_tr_b16 a[42:43], v65 offset:256\n\t" \
  "ds_read_b64_tr_b16 a[44:45], v65 offset:2048\n\t" \
  "ds_read_b64_tr_b16 a[46:47], v65 offset:2304\n\t" \
  "ds_read_b64_tr_b16 a[88:89], v65 offset:8\n\t" \
  "ds_read_b64_tr_b16 a[90:91], v65 offset:264\n\t" \
  "ds_read_b64_tr_b16 a[92:93], v65 offset:2056\n\t" \
  "ds_read_b64_tr_b16 a[94:95], v65 offset:2312\n\t" \
  "s_waitcnt vmcnt(28)\n\t" \
  "ds_read_b64_tr_b16 a[152:153], v65 offset:4096\n\t" \
  "ds_read_b64_tr_b16 a[154:155], v65 offset:4352\n\t" \
  "ds_read_b64_tr_b16 a[156:157], v65 offset:6144\n\t" \
  "ds_read_b64_tr_b16 a[158:159], v65 offset:6400\n\t" \
  "ds_read_b64_tr_b16 a[216:217], v65 offset:4104\n\t" \
  "ds_read_b64_tr_b16 a[218:219], v65 offset:4360\n\t" \
  "ds_read_b64_tr_b16 a[220:221], v65 offset:6152\n\t" \
  "ds_read_b64_tr_b16 a[222:223], v65 offset:6408\n\t" \
  "s_waitcnt vmcnt(24)\n\t" \
  "ds_read_b64_tr_b16 v[112:113], v66 offset:8192\n\t" \
  "ds_read_b64_tr_b16 v[114:115], v66 offset:8448\n\t" \
  "ds_read_b64_tr_b16 v[116:117], v66 offset:10240\n\t" \
  "ds_read_b64_tr_b16 v[118:119], v66 offset:10496\n\t" \
  "ds_read_b64_tr_b16 v[176:177], v66 offset:8224\n\t" \
  "ds_read_b64_tr_b16 v[178:179], v66 offset:8480\n\t" \
  "ds_read_b64_tr_b16 v[180:181], v66 offset:10272\n\t" \
  "ds_read_b64_tr_b16 v[182:183], v66 offset:10528\n\t" \
  "s_xor_b32 s60, s40, 0\n\t" \
  "s_mul_i32 s60, s60, 36864\n\t" \
  "s_add_u32 s60, s60, s55\n\t" \
  "s_add_u32 s60, s60, 0\n\t" \
  "s_add_u32 s61, s60, 9216\n\t" \
  "buffer_load_dwordx3 v[192:194], v64, %[Wo], s60 offen offset:0\n\t" \
  "buffer_load_dwordx3 v[196:198], v64, %[Wo], s60 offen offset:1152\n\t" \
  "buffer_load_dwordx3 v[200:202], v64, %[Wo], s60 offen offset:2304\n\t" \
  "buffer_load_dwordx3 v[204:206], v64, %[Wo], s60 offen offset:3456\n\t" \
  "buffer_load_dwordx3 v[208:210], v64, %[Wo], s61 offen offset:0\n\t" \
  "buffer_load_dwordx3 v[212:214], v64, %[Wo], s61 offen offset:1152\n\t" \
  "buffer_load_dwordx3 v[216:218], v64, %[Wo], s61 offen offset:2304\n\t" \
  "buffer_load_dwordx3 v[220:222], v64, %[Wo], s61 offen offset:3456\n\t" \
  "s_waitcnt vmcnt(28)\n\t" \
  "ds_read_b64_tr_b16 a[48:49], v65 offset:12288\n\t" \
  "ds_read_b64_tr_b16 a[50:51], v65 offset:12544\n\t" \
  "ds_read_b64_tr_b16 a[52:53], v65 offset:14336\n\t" \
  "ds_read_b64_tr_b16 a[54:55], v65 offset:14592\n\t" \
  "ds_read_b64_tr_b16 a[96:97], v65 offset:12296\n\t" \
  "ds_read_b64_tr_b16 a[98:99], v65 offset:12552\n\t" \
  "ds_read_b64_tr_b16 a[100:101], v65 offset:14344\n\t" \
  "ds_read_b64_tr_b16 a[102:103], v65 offset:14600\n\t" \
  "s_waitcnt vmcnt(24)\n\t" \
  "ds_read_b64_tr_b16 a[160:161], v65 offset:16384\n\t" \
  "ds_read_b64_tr_b16 a[162:163], v65 offset:16640\n\t" \
  "ds_read_b64_tr_b16 a[164:165], v65 offset:18432\n\t" \
  "ds_read_b64_tr_b16 a[166:167], v65 offset:18688\n\t" \
  "ds_read_b64_tr_b16 a[224:225], v65 offset:16392\n\t" \
  "ds_read_b64_tr_b16 a[226:227], v65 offset:16648\n\t" \
  "ds_read_b64_tr_b16 a[228:229], v65 offset:18440\n\t" \
  "ds_read_b64_tr_b16 a[230:231], v65 offset:18696\n\t" \
  "s_xor_b32 s60, s40, 0\n\t" \
  "s_mul_i32 s60, s60, 36864\n\t" \
  "s_add_u32 s60, s60, s55\n\t" \
  "s_add_u32 s60, s60, 512\n\t" \
  "s_add_u32 s61, s60, 9216\n\t" \
  "buffer_load_dwordx3 v[224:226], v64, %[Wo], s60 offen offset:0\n\t" \
  "buffer_load_dwordx3 v[228:230], v64, %[Wo], s60 offen offset:1152\n\t" \
  "buffer_load_dwordx3 v[232:234], v64, %[Wo], s60 offen offset:2304\n\t" \
  "buffer_load_dwordx3 v[236:238], v64, %[Wo], s60 offen offset:3456\n\t" \
  "buffer_load_dwordx3 v[240:242], v64, %[Wo], s61 offen offset:0\n\t" \
  "buffer_load_dwordx3 v[244:246], v64, %[Wo], s61 offen offset:1152\n\t" \
  "buffer_load_dwordx3 v[248:250], v64, %[Wo], s61 offen offset:2304\n\t" \
  "buffer_load_dwordx3 v[252:254], v64, %[Wo], s61 offen offset:3456\n\t" \
  "s_waitcnt vmcnt(28)\n\t" \
  "ds_read_b64_tr_b16 v[120:121], v66 offset:20480\n\t" \
  "ds_read_b64_tr_b16 v[122:123], v66 offset:20736\n\t" \
  "ds_read_b64_tr_b16 v[124:125], v66 offset:22528\n\t" \
  "ds_read_b64_tr_b16 v[126:127], v66 offset:22784\n\t" \
  "ds_read_b64_tr_b16 v[184:185], v66 offset:20512\n\t" \
  "ds_read_b64_tr_b16 v[186:187], v66 offset:20768\n\t" \
  "ds_read_b64_tr_b16 v[188:189], v66 offset:22560\n\t" \
  "ds_read_b64_tr_b16 v[190:191], v66 offset:22816\n\t" \
  "s_waitcnt vmcnt(24)\n\t" \
  "ds_read_b64_tr_b16 a[56:57], v65 offset:24576\n\t" \
  "ds_read_b64_tr_b16 a[58:59], v65 offset:24832\n\t" \
  "ds_read_b64_tr_b16 a[60:61], v65 offset:26624\n\t" \
  "ds_read_b64_tr_b16 a[62:63], v65 offset:26880\n\t" \
  "ds_read_b64_tr_b16 a[104:105], v65 offset:24584\n\t" \
  "ds_read_b64_tr_b16 a[106:107], v65 offset:24840\n\t" \
  "ds_read_b64_tr_b16 a[108:109], v65 offset:26632\n\t" \
  "ds_read_b64_tr_b16 a[110:111], v65 offset:26888\n\t" \
  "s_add_u32 s60, s75, 73728\n\t" \
  "buffer_load_ushort v72, v67, %[Wo], s60 offen\n\t" \
  "buffer_load_ushort v195, v67, %[Wo], s60 offen offset:1152\n\t" \
  "s_add_u32 s60, s75, 110592\n\t" \
  "buffer_load_ushort v73, v67, %[Wo], s60 offen\n\t" \
  "buffer_load_ushort v199, v67, %[Wo], s60 offen offset:1152\n\t" \
  "s_add_u32 s60, s75, 76032\n\t" \
  "buffer_load_ushort v74, v67, %[Wo], s60 offen\n\t" \
  "buffer_load_ushort v203, v67, %[Wo], s60 offen offset:1152\n\t" \
  "s_add_u32 s60, s75, 112896\n\t" \
  "buffer_load_ushort v75, v67, %[Wo], s60 offen\n\t" \
  "buffer_load_ushort v207, v67, %[Wo], s60 offen offset:1152\n\t" \
  "s_add_u32 s60, s75, 82944\n\t" \
  "buffer_load_ushort v76, v67, %[Wo], s60 offen\n\t" \
  "buffer_load_ushort v211, v67, %[Wo], s60 offen offset:1152\n\t" \
  "s_add_u32 s60, s75, 119808\n\t" \
  "buffer_load_ushort v77, v67, %[Wo], s60 offen\n\t" \
  "buffer_load_ushort v215, v67, %[Wo], s60 offen offset:1152\n\t" \
  "s_add_u32 s60, s75, 85248\n\t" \
  "buffer_load_ushort v78, v67, %[Wo], s60 offen\n\t" \
  "buffer_load_ushort v219, v67, %[Wo], s60 offen offset:1152\n\t" \
  "s_add_u32 s60, s75, 122112\n\t" \
  "buffer_load_ushort v79, v67, %[Wo], s60 offen\n\t" \
  "buffer_load_ushort v223, v67, %[Wo], s60 offen offset:1152\n\t" \
  "s_waitcnt vmcnt(36)\n\t" \
  "ds_read_b64_tr_b16 v[40:41], v65 offset:28672\n\t" \
  "ds_read_b64_tr_b16 v[42:43], v65 offset:28928\n\t" \
  "ds_read_b64_tr_b16 v[44:45], v65 offset:30720\n\t" \
  "ds_read_b64_tr_b16 v[46:47], v65 offset:30976\n\t" \
  "ds_read_b64_tr_b16 a[232:233], v65 offset:28680\n\t" \
  "ds_read_b64_tr_b16 a[234:235], v65 offset:28936\n\t" \
  "ds_read_b64_tr_b16 a[236:237], v65 offset:30728\n\t" \
  "ds_read_b64_tr_b16 a[238:239], v65 offset:30984\n\t" \
  "s_waitcnt vmcnt(32)\n\t" \
  "ds_read_b64_tr_b16 v[128:129], v66 offset:32768\n\t" \
  "ds_read_b64_tr_b16 v[130:131], v66 offset:33024\n\t" \
  "ds_read_b64_tr_b16 v[132:133], v66 offset:34816\n\t" \
  "ds_read_b64_tr_b16 v[134:135], v66 offset:35072\n\t" \
  "ds_read_b64_tr_b16 v[56:57], v66 offset:32800\n\t" \
  "ds_read_b64_tr_b16 v[58:59], v66 offset:33056\n\t" \
  "ds_read_b64_tr_b16 v[60:61], v66 offset:34848\n\t" \
  "ds_read_b64_tr_b16 v[62:63], v66 offset:35104\n\t" \
  "s_waitcnt lgkmcnt(0)\n\t" \
  "ds_write_b128 v70, v[0:3] offset:0\n\t" \
  "ds_write_b128 v70, v[4:7] offset:1024\n\t" \
  "ds_write_b128 v70, v[8:11] offset:2048\n\t" \
  "ds_write_b128 v70, v[12:15] offset:3072\n\t" \
  "ds_write_b128 v70, v[16:19] offset:4096\n\t" \
  "ds_write_b128 v70, v[20:23] offset:5120\n\t" \
  "ds_write_b128 v70, v[24:27] offset:6144\n\t" \
  "ds_write_b128 v70, v[28:31] offset:7168\n\t" \
  "ds_write_b128 v70, v[32:35] offset:8192\n\t" \
  "ds_write_b128 v70, v[36:39] offset:9216\n\t" \
  "ds_write_b128 v70, v[40:43] offset:10240\n\t" \
  "ds_write_b128 v70, v[44:47] offset:11264\n\t" \
  "ds_write_b128 v70, v[48:51] offset:12288\n\t" \
  "ds_write_b128 v70, v[52:55] offset:13312\n\t" \
  "ds_write_b128 v70, v[56:59] offset:14336\n\t" \
  "ds_write_b128 v70, v[60:63] offset:15360\n\t" \
  "s_waitcnt lgkmcnt(0)\n\t" \
  "s_xor_b32 s60, s40, 1\n\t" \
  "s_mul_i32 s60, s60, 36864\n\t" \
  "s_add_u32 s60, s60, s55\n\t" \
  "s_add_u32 s60, s60, 0\n\t" \
  "s_add_u32 s61, s60, 9216\n\t" \
  "buffer_load_dwordx3 v[0:2], v64, %[Wo], s60 offen offset:0\n\t" \
  "buffer_load_dwordx3 v[4:6], v64, %[Wo], s60 offen offset:1152\n\t" \
  "buffer_load_dwordx3 v[8:10], v64, %[Wo], s60 offen offset:2304\n\t" \
  "buffer_load_dwordx3 v[12:14], v64, %[Wo], s60 offen offset:3456\n\t" \
  "buffer_load_dwordx3 v[16:18], v64, %[Wo], s61 offen offset:0\n\t" \
  "buffer_load_dwordx3 v[20:22], v64, %[Wo], s61 offen offset:1152\n\t" \
  "buffer_load_dwordx3 v[24:26], v64, %[Wo], s61 offen offset:2304\n\t" \
  "buffer_load_dwordx3 v[28:30], v64, %[Wo], s61 offen offset:3456\n\t" \
  "s_xor_b32 s60, s40, 1\n\t" \
  "s_mul_i32 s60, s60, 36864\n\t" \
  "s_add_u32 s60, s60, s55\n\t" \
  "s_add_u32 s60, s60, 512\n\t" \
  "s_add_u32 s61, s60, 9216\n\t" \
  "buffer_load_dwordx3 v[32:34], v64, %[Wo], s60 offen offset:0\n\t" \
  "buffer_load_dwordx3 v[36:38], v64, %[Wo], s60 offen offset:1152\n\t" \
  "buffer_load_dwordx3 v[40:42], v64, %[Wo], s60 offen offset:2304\n\t" \
  "buffer_load_dwordx3 v[44:46], v64, %[Wo], s60 offen offset:3456\n\t" \
  "buffer_load_dwordx3 v[48:50], v64, %[Wo], s61 offen offset:0\n\t" \
  "buffer_load_dwordx3 v[52:54], v64, %[Wo], s61 offen offset:1152\n\t" \
  "buffer_load_dwordx3 v[56:58], v64, %[Wo], s61 offen offset:2304\n\t" \
  "buffer_load_dwordx3 v[60:62], v64, %[Wo], s61 offen offset:3456\n\t" \
  "s_waitcnt vmcnt(30)\n\t" \
  "v_lshl_or_b32 v72, v195, 16, v72\n\t" \
  "s_waitcnt vmcnt(28)\n\t" \
  "v_lshl_or_b32 v73, v199, 16, v73\n\t" \
  "s_waitcnt vmcnt(26)\n\t" \
  "v_lshl_or_b32 v74, v203, 16, v74\n\t" \
  "s_waitcnt vmcnt(24)\n\t" \
  "v_lshl_or_b32 v75, v207, 16, v75\n\t" \
  "s_waitcnt vmcnt(22)\n\t" \
  "v_lshl_or_b32 v76, v211, 16, v76\n\t" \
  "s_waitcnt vmcnt(20)\n\t" \
  "v_lshl_or_b32 v77, v215, 16, v77\n\t" \
  "s_waitcnt vmcnt(18)\n\t" \
  "v_lshl_or_b32 v78, v219, 16, v78\n\t" \
  "s_waitcnt vmcnt(16)\n\t" \
  "v_lshl_or_b32 v79, v223, 16, v79\n\t" \
  "ds_write_b128 v70, v[72:75] offset:34816\n\t" \
  "ds_write_b128 v70, v[76:79] offset:35840\n\t" \
  "s_waitcnt lgkmcnt(0)\n\t" \
  "v_perm_b32 v72, v196, v192, s62\n\t" \
  "v_perm_b32 v73, v196, v192, s63\n\t" \
  "v_perm_b32 v74, v204, v200, s62\n\t" \
  "v_perm_b32 v75, v204, v200, s63\n\t" \
  "v_perm_b32 v76, v212, v208, s62\n\t" \
  "v_perm_b32 v77, v212, v208, s63\n\t" \
  "v_perm_b32 v78, v220, v216, s62\n\t" \
  "v_perm_b32 v79, v220, v216, s63\n\t" \
  "ds_write_b128 v70, v[72:75] offset:20480\n\t" \
  "ds_write_b128 v70, v[76:79] offset:21504\n\t" \
  "s_waitcnt lgkmcnt(0)\n\t" \
  "v_perm_b32 v72, v198, v194, s62\n\t" \
  "v_perm_b32 v73, v198, v194, s63\n\t" \
  "v_perm_b32 v74, v206, v202, s62\n\t" \
  "v_perm_b32 v75, v206, v202, s63\n\t" \
  "v_perm_b32 v76, v214, v210, s62\n\t" \
  "v_perm_b32 v77, v214, v210, s63\n\t" \
  "v_perm_b32 v78, v222, v218, s62\n\t" \
  "v_perm_b32 v79, v222, v218, s63\n\t" \
  "ds_write_b128 v70, v[72:75] offset:16384\n\t" \
  "ds_write_b128 v70, v[76:79] offset:17408\n\t" \
  "s_waitcnt lgkmcnt(0)\n\t" \
  "v_perm_b32 v72, v228, v224, s62\n\t" \
  "v_perm_b32 v73, v228, v224, s63\n\t" \
  "v_perm_b32 v74, v236, v232, s62\n\t" \
  "v_perm_b32 v75, v236, v232, s63\n\t" \
  "v_perm_b32 v76, v244, v240, s62\n\t" \
  "v_perm_b32 v77, v244, v240, s63\n\t" \
  "v_perm_b32 v78, v252, v248, s62\n\t" \
  "v_perm_b32 v79, v252, v248, s63\n\t" \
  "ds_write_b128 v70, v[72:75] offset:22528\n\t" \
  "ds_write_b128 v70, v[76:79] offset:23552\n\t" \
  "s_waitcnt lgkmcnt(0)\n\t" \
  "v_perm_b32 v72, v230, v226, s62\n\t" \
  "v_perm_b32 v73, v230, v226, s63\n\t" \
  "v_perm_b32 v74, v238, v234, s62\n\t" \
  "v_perm_b32 v75, v238, v234, s63\n\t" \
  "v_perm_b32 v76, v246, v242, s62\n\t" \
  "v_perm_b32 v77, v246, v242, s63\n\t" \
  "v_perm_b32 v78, v254, v250, s62\n\t" \
  "v_perm_b32 v79, v254, v250, s63\n\t" \
  "ds_write_b128 v70, v[72:75] offset:18432\n\t" \
  "ds_write_b128 v70, v[76:79] offset:19456\n\t" \
  "s_add_u32 s60, s75, 0\n\t" \
  "buffer_load_ushort v248, v67, %[Wo], s60 offen\n\t" \
  "buffer_load_ushort v3, v67, %[Wo], s60 offen offset:1152\n\t" \
  "s_add_u32 s60, s75, 36864\n\t" \
  "buffer_load_ushort v249, v67, %[Wo], s60 offen\n\t" \
  "buffer_load_ushort v7, v67, %[Wo], s60 offen offset:1152\n\t" \
  "s_add_u32 s60, s75, 2304\n\t" \
  "buffer_load_ushort v250, v67, %[Wo], s60 offen\n\t" \
  "buffer_load_ushort v11, v67, %[Wo], s60 offen offset:1152\n\t" \
  "s_add_u32 s60, s75, 39168\n\t" \
  "buffer_load_ushort v251, v67, %[Wo], s60 offen\n\t" \
  "buffer_load_ushort v15, v67, %[Wo], s60 offen offset:1152\n\t" \
  "s_add_u32 s60, s75, 9216\n\t" \
  "buffer_load_ushort v252, v67, %[Wo], s60 offen\n\t" \
  "buffer_load_ushort v19, v67, %[Wo], s60 offen offset:1152\n\t" \
  "s_add_u32 s60, s75, 46080\n\t" \
  "buffer_load_ushort v253, v67, %[Wo], s60 offen\n\t" \
  "buffer_load_ushort v23, v67, %[Wo], s60 offen offset:1152\n\t" \
  "s_add_u32 s60, s75, 11520\n\t" \
  "buffer_load_ushort v254, v67, %[Wo], s60 offen\n\t" \
  "buffer_load_ushort v27, v67, %[Wo], s60 offen offset:1152\n\t" \
  "s_add_u32 s60, s75, 48384\n\t" \
  "buffer_load_ushort v255, v67, %[Wo], s60 offen\n\t" \
  "buffer_load_ushort v31, v67, %[Wo], s60 offen offset:1152\n\t" \
  "s_waitcnt lgkmcnt(0)\n\t" \
  "s_waitcnt vmcnt(30)\n\t" \
  "v_perm_b32 v72, v4, v0, s62\n\t" \
  "v_perm_b32 v73, v4, v0, s63\n\t" \
  "s_waitcnt vmcnt(28)\n\t" \
  "v_perm_b32 v74, v12, v8, s62\n\t" \
  "v_perm_b32 v75, v12, v8, s63\n\t" \
  "s_waitcnt vmcnt(26)\n\t" \
  "v_perm_b32 v76, v20, v16, s62\n\t" \
  "v_perm_b32 v77, v20, v16, s63\n\t" \
  "s_waitcnt vmcnt(24)\n\t" \
  "v_perm_b32 v78, v28, v24, s62\n\t" \
  "v_perm_b32 v79, v28, v24, s63\n\t" \
  "ds_write_b128 v70, v[72:75] offset:24576\n\t" \
  "ds_write_b128 v70, v[76:79] offset:25600\n\t" \
  "v_perm_b32 v200, v6, v2, s62\n\t" \
  "v_perm_b32 v201, v6, v2, s63\n\t" \
  "v_perm_b32 v202, v14, v10, s62\n\t" \
  "v_perm_b32 v203, v14, v10, s63\n\t" \
  "v_perm_b32 v204, v22, v18, s62\n\t" \
  "v_perm_b32 v205, v22, v18, s63\n\t" \
  "v_perm_b32 v206, v30, v26, s62\n\t" \
  "v_perm_b32 v207, v30, v26, s63\n\t" \
  "s_xor_b32 s60, s40, 2\n\t" \
  "s_mul_i32 s60, s60, 36864\n\t" \
  "s_add_u32 s60, s60, s55\n\t" \
  "s_add_u32 s60, s60, 0\n\t" \
  "s_add_u32 s61, s60, 9216\n\t" \
  "buffer_load_dwordx3 v[0:2], v64, %[Wo], s60 offen offset:0\n\t" \
  "buffer_load_dwordx3 v[4:6], v64, %[Wo], s60 offen offset:1152\n\t" \
  "buffer_load_dwordx3 v[8:10], v64, %[Wo], s60 offen offset:2304\n\t" \
  "buffer_load_dwordx3 v[12:14], v64, %[Wo], s60 offen offset:3456\n\t" \
  "buffer_load_dwordx3 v[16:18], v64, %[Wo], s61 offen offset:0\n\t" \
  "buffer_load_dwordx3 v[20:22], v64, %[Wo], s61 offen offset:1152\n\t" \
  "buffer_load_dwordx3 v[24:26], v64, %[Wo], s61 offen offset:2304\n\t" \
  "buffer_load_dwordx3 v[28:30], v64, %[Wo], s61 offen offset:3456\n\t" \
  "s_waitcnt lgkmcnt(0)\n\t" \
  "s_waitcnt vmcnt(30)\n\t" \
  "v_perm_b32 v72, v36, v32, s62\n\t" \
  "v_perm_b32 v73, v36, v32, s63\n\t" \
  "s_waitcnt vmcnt(28)\n\t" \
  "v_perm_b32 v74, v44, v40, s62\n\t" \
  "v_perm_b32 v75, v44, v40, s63\n\t" \
  "s_waitcnt vmcnt(26)\n\t" \
  "v_perm_b32 v76, v52, v48, s62\n\t" \
  "v_perm_b32 v77, v52, v48, s63\n\t" \
  "s_waitcnt vmcnt(24)\n\t" \
  "v_perm_b32 v78, v60, v56, s62\n\t" \
  "v_perm_b32 v79, v60, v56, s63\n\t" \
  "ds_write_b128 v70, v[72:75] offset:26624\n\t" \
  "ds_write_b128 v70, v[76:79] offset:27648\n\t" \
  "v_perm_b32 v232, v38, v34, s62\n\t" \
  "v_perm_b32 v233, v38, v34, s63\n\t" \
  "v_perm_b32 v234, v46, v42, s62\n\t" \
  "v_perm_b32 v235, v46, v42, s63\n\t" \
  "v_perm_b32 v236, v54, v50, s62\n\t" \
  "v_perm_b32 v237, v54, v50, s63\n\t" \
  "v_perm_b32 v238, v62, v58, s62\n\t" \
  "v_perm_b32 v239, v62, v58, s63\n\t" \
  "s_xor_b32 s60, s40, 2\n\t" \
  "s_mul_i32 s60, s60, 36864\n\t" \
  "s_add_u32 s60, s60, s55\n\t" \
  "s_add_u32 s60, s60, 512\n\t" \
  "s_add_u32 s61, s60, 9216\n\t" \
  "buffer_load_dwordx3 v[32:34], v64, %[Wo], s60 offen offset:0\n\t" \
  "buffer_load_dwordx3 v[36:38], v64, %[Wo], s60 offen offset:1152\n\t" \
  "buffer_load_dwordx3 v[40:42], v64, %[Wo], s60 offen offset:2304\n\t" \
  "buffer_load_dwordx3 v[44:46], v64, %[Wo], s60 offen offset:3456\n\t" \
  "buffer_load_dwordx3 v[48:50], v64, %[Wo], s61 offen offset:0\n\t" \
  "buffer_load_dwordx3 v[52:54], v64, %[Wo], s61 offen offset:1152\n\t" \
  "buffer_load_dwordx3 v[56:58], v64, %[Wo], s61 offen offset:2304\n\t" \
  "buffer_load_dwordx3 v[60:62], v64, %[Wo], s61 offen offset:3456\n\t" \
  "s_waitcnt lgkmcnt(0)\n\t" \
  "s_waitcnt vmcnt(14)\n\t" \
  "v_perm_b32 v72, v4, v0, s62\n\t" \
  "v_perm_b32 v73, v4, v0, s63\n\t" \
  "s_waitcnt vmcnt(12)\n\t" \
  "v_perm_b32 v74, v12, v8, s62\n\t" \
  "v_perm_b32 v75, v12, v8, s63\n\t" \
  "s_waitcnt vmcnt(10)\n\t" \
  "v_perm_b32 v76, v20, v16, s62\n\t" \
  "v_perm_b32 v77, v20, v16, s63\n\t" \
  "s_waitcnt vmcnt(8)\n\t" \
  "v_perm_b32 v78, v28, v24, s62\n\t" \
  "v_perm_b32 v79, v28, v24, s63\n\t" \
  "ds_write_b128 v70, v[72:75] offset:28672\n\t" \
  "ds_write_b128 v70, v[76:79] offset:29696\n\t" \
  "v_perm_b32 v208, v6, v2, s62\n\t" \
  "v_perm_b32 v209, v6, v2, s63\n\t" \
  "v_perm_b32 v210, v14, v10, s62\n\t" \
  "v_perm_b32 v211, v14, v10, s63\n\t" \
  "v_perm_b32 v212, v22, v18, s62\n\t" \
  "v_perm_b32 v213, v22, v18, s63\n\t" \
  "v_perm_b32 v214, v30, v26, s62\n\t" \
  "v_perm_b32 v215, v30, v26, s63\n\t" \
  "s_xor_b32 s60, s40, 3\n\t" \
  "s_mul_i32 s60, s60, 36864\n\t" \
  "s_add_u32 s60, s60, s55\n\t" \
  "s_add_u32 s60, s60, 0\n\t" \
  "s_add_u32 s61, s60, 9216\n\t" \
  "buffer_load_dwordx3 v[0:2], v64, %[Wo], s60 offen offset:0\n\t" \
  "buffer_load_dwordx3 v[4:6], v64, %[Wo], s60 offen offset:1152\n\t" \
  "buffer_load_dwordx3 v[8:10], v64, %[Wo], s60 offen offset:2304\n\t" \
  "buffer_load_dwordx3 v[12:14], v64, %[Wo], s60 offen offset:3456\n\t" \
  "buffer_load_dwordx3 v[16:18], v64, %[Wo], s61 offen offset:0\n\t" \
  "buffer_load_dwordx3 v[20:22], v64, %[Wo], s61 offen offset:1152\n\t" \
  "buffer_load_dwordx3 v[24:26], v64, %[Wo], s61 offen offset:2304\n\t" \
  "buffer_load_dwordx3 v[28:30], v64, %[Wo], s61 offen offset:3456\n\t" \
  "s_waitcnt lgkmcnt(0)\n\t" \
  "s_waitcnt vmcnt(14)\n\t" \
  "v_perm_b32 v72, v36, v32, s62\n\t" \
  "v_perm_b32 v73, v36, v32, s63\n\t" \
  "s_waitcnt vmcnt(12)\n\t" \
  "v_perm_b32 v74, v44, v40, s62\n\t" \
  "v_perm_b32 v75, v44, v40, s63\n\t" \
  "s_waitcnt vmcnt(10)\n\t" \
  "v_perm_b32 v76, v52, v48, s62\n\t" \
  "v_perm_b32 v77, v52, v48, s63\n\t" \
  "s_waitcnt vmcnt(8)\n\t" \
  "v_perm_b32 v78, v60, v56, s62\n\t" \
  "v_perm_b32 v79, v60, v56, s63\n\t" \
  "ds_write_b128 v70, v[72:75] offset:30720\n\t" \
  "ds_write_b128 v70, v[76:79] offset:31744\n\t" \
  "v_perm_b32 v240, v38, v34, s62\n\t" \
  "v_perm_b32 v241, v38, v34, s63\n\t" \
  "v_perm_b32 v242, v46, v42, s62\n\t" \
  "v_perm_b32 v243, v46, v42, s63\n\t" \
  "v_perm_b32 v244, v54, v50, s62\n\t" \
  "v_perm_b32 v245, v54, v50, s63\n\t" \
  "v_perm_b32 v246, v62, v58, s62\n\t" \
  "v_perm_b32 v247, v62, v58, s63\n\t" \
  "s_xor_b32 s60, s40, 3\n\t" \
  "s_mul_i32 s60, s60, 36864\n\t" \
  "s_add_u32 s60, s60, s55\n\t" \
  "s_add_u32 s60, s60, 512\n\t" \
  "s_add_u32 s61, s60, 9216\n\t" \
  "buffer_load_dwordx3 v[32:34], v64, %[Wo], s60 offen offset:0\n\t" \
  "buffer_load_dwordx3 v[36:38], v64, %[Wo], s60 offen offset:1152\n\t" \
  "buffer_load_dwordx3 v[40:42], v64, %[Wo], s60 offen offset:2304\n\t" \
  "buffer_load_dwordx3 v[44:46], v64, %[Wo], s60 offen offset:3456\n\t" \
  "buffer_load_dwordx3 v[48:50], v64, %[Wo], s61 offen offset:0\n\t" \
  "buffer_load_dwordx3 v[52:54], v64, %[Wo], s61 offen offset:1152\n\t" \
  "buffer_load_dwordx3 v[56:58], v64, %[Wo], s61 offen offset:2304\n\t" \
  "buffer_load_dwordx3 v[60:62], v64, %[Wo], s61 offen offset:3456\n\t" \
  "s_waitcnt vmcnt(14)\n\t" \
  "v_perm_b32 v192, v4, v0, s62\n\t" \
  "v_perm_b32 v193, v4, v0, s63\n\t" \
  "s_waitcnt vmcnt(12)\n\t" \
  "v_perm_b32 v194, v12, v8, s62\n\t" \
  "v_perm_b32 v195, v12, v8, s63\n\t" \
  "s_waitcnt vmcnt(10)\n\t" \
  "v_perm_b32 v196, v20, v16, s62\n\t" \
  "v_perm_b32 v197, v20, v16, s63\n\t" \
  "s_waitcnt vmcnt(8)\n\t" \
  "v_perm_b32 v198, v28, v24, s62\n\t" \
  "v_perm_b32 v199, v28, v24, s63\n\t" \
  "v_perm_b32 v216, v6, v2, s62\n\t" \
  "v_perm_b32 v217, v6, v2, s63\n\t" \
  "v_perm_b32 v218, v14, v10, s62\n\t" \
  "v_perm_b32 v219, v14, v10, s63\n\t" \
  "v_perm_b32 v220, v22, v18, s62\n\t" \
  "v_perm_b32 v221, v22, v18, s63\n\t" \
  "v_perm_b32 v222, v30, v26, s62\n\t" \
  "v_perm_b32 v223, v30, v26, s63\n\t" \
  "s_waitcnt vmcnt(6)\n\t" \
  "v_perm_b32 v224, v36, v32, s62\n\t" \
  "v_perm_b32 v225, v36, v32, s63\n\t" \
  "s_waitcnt vmcnt(4)\n\t" \
  "v_perm_b32 v226, v44, v40, s62\n\t" \
  "v_perm_b32 v227, v44, v40, s63\n\t" \
  "s_waitcnt vmcnt(2)\n\t" \
  "v_perm_b32 v228, v52, v48, s62\n\t" \
  "v_perm_b32 v229, v52, v48, s63\n\t" \
  "s_waitcnt vmcnt(0)\n\t" \
  "v_perm_b32 v230, v60, v56, s62\n\t" \
  "v_perm_b32 v231, v60, v56, s63\n\t" \
  "s_waitcnt lgkmcnt(0)\n\t" \
  "v_perm_b32 v72, v38, v34, s62\n\t" \
  "v_perm_b32 v73, v38, v34, s63\n\t" \
  "v_perm_b32 v74, v46, v42, s62\n\t" \
  "v_perm_b32 v75, v46, v42, s63\n\t" \
  "v_perm_b32 v76, v54, v50, s62\n\t" \
  "v_perm_b32 v77, v54, v50, s63\n\t" \
  "v_perm_b32 v78, v62, v58, s62\n\t" \
  "v_perm_b32 v79, v62, v58, s63\n\t" \
  "ds_write_b128 v70, v[72:75] offset:32768\n\t" \
  "ds_write_b128 v70, v[76:79] offset:33792\n\t" \
  "v_lshl_or_b32 v248, v3, 16, v248\n\t" \
  "v_lshl_or_b32 v249, v7, 16, v249\n\t" \
  "v_lshl_or_b32 v250, v11, 16, v250\n\t" \
  "v_lshl_or_b32 v251, v15, 16, v251\n\t" \
  "v_lshl_or_b32 v252, v19, 16, v252\n\t" \
  "v_lshl_or_b32 v253, v23, 16, v253\n\t" \
  "v_lshl_or_b32 v254, v27, 16, v254\n\t" \
  "v_lshl_or_b32 v255, v31, 16, v255\n\t" \
  "s_waitcnt lgkmcnt(0)\n\t" \
  CH_STW \
  CH_COW \
  "v_lshrrev_b32 v0, 2, v69\n\t" \
  "s_mov_b32 s60, " CH_CTAB "\n\t" \
  "buffer_load_dword v1, v0, s[28:31], s60 offen offset:88 sc1\n\t" \
  "buffer_load_dword v0, v0, s[28:31], s60 offen sc1\n\t" \
  "s_waitcnt vmcnt(0)\n\t" \
  "v_and_b32 v1, 1, v1\n\t" \
  "v_and_b32 v0, 1, v0\n\t" \
  "v_cmp_ne_u32 s[90:91], s32, v1\n\t" \
  "v_cmp_ne_u32 vcc, s32, v0\n\t" \
  "s_nop 1\n\t" \
  "s_mov_b32 s96, vcc_lo\n\t" \
  "s_mov_b32 s48, -1\n\t" \
  "s_add_u32 s82, s48, 0\n\t" \
  CH_SEGC \
  "s_lshr_b64 s[60:61], s[90:91], s82\n\t" \
  "s_and_b32 s60, s60, 1\n\t" \
  "s_lshl_b32 s60, s60, 13\n\t" \
  "s_add_u32 s82, s82, " CH_SG0 "\n\t" \
  "s_lshl_b32 s82, s82, 21\n\t" \
  "s_or_b32 s82, s82, s60\n\t" \
  "s_add_u32 s50, s82, s38\n\t" \
  "s_add_u32 s69, s82, s39\n\t" \
  "s_add_u32 s82, s48, 1\n\t" \
  CH_SEGC \
  "s_lshr_b64 s[60:61], s[90:91], s82\n\t" \
  "s_and_b32 s60, s60, 1\n\t" \
  "s_lshl_b32 s60, s60, 13\n\t" \
  "s_add_u32 s82, s82, " CH_SG0 "\n\t" \
  "s_lshl_b32 s82, s82, 21\n\t" \
  "s_or_b32 s82, s82, s60\n\t" \
  "s_add_u32 s52, s82, s43\n\t" \
  "s_add_u32 s82, s48, " CH_ARMD "\n\t" \
  CH_SEGC \
  "s_lshr_b64 s[60:61], s[90:91], s82\n\t" \
  "s_and_b32 s60, s60, 1\n\t" \
  "s_lshl_b32 s60, s60, 13\n\t" \
  "s_add_u32 s82, s82, " CH_SG0 "\n\t" \
  "s_lshl_b32 s82, s82, 21\n\t" \
  "s_or_b32 s82, s82, s60\n\t" \
  "s_add_u32 s85, s82, s45\n\t" \
  "s_cmp_lt_i32 s48, " CH_ARMS "\n\t" \
  "s_cselect_b32 s85, s27, s85\n\t" \
  "v_and_b32 v37, 0x30, v69\n\t" \
  "v_add_u32 v37, s52, v37\n\t" \
  "s_mov_b32 s48, 0\n\t" \
  "s_mov_b32 s86, 1\n\t" \
  "s_and_b32 s49, s96, 1\n\t" \
  "s_lshl_b32 s49, s49, 13\n\t" \
  "s_add_u32 s53, s49, " CH_SLOT "\n\t" \
  "s_cmp_eq_u32 s40, 3\n\t" \
  "s_cselect_b32 s81, 44, 0x7c\n\t" \
  "s_add_u32 s82, s48, 0\n\t" \
  CH_SEGC \
  "s_lshr_b64 s[60:61], s[90:91], s82\n\t" \
  "s_and_b32 s60, s60, 1\n\t" \
  "s_lshl_b32 s60, s60, 13\n\t" \
  "s_add_u32 s82, s82, " CH_SG0 "\n\t" \
  "s_lshl_b32 s82, s82, 21\n\t" \
  "s_or_b32 s82, s82, s60\n\t" \
  "s_add_u32 s50, s82, s38\n\t" \
  "s_add_u32 s69, s82, s39\n\t" \
  "s_add_u32 s51, s82, s43\n\t" \
  "s_add_u32 s82, s48, 0\n\t" \
  CH_SEGC \
  "s_lshr_b64 s[60:61], s[90:91], s82\n\t" \
  "s_and_b32 s60, s60, 1\n\t" \
  "s_lshl_b32 s60, s60, 13\n\t" \
  "s_add_u32 s82, s82, " CH_SG0 "\n\t" \
  "s_lshl_b32 s82, s82, 21\n\t" \
  "s_or_b32 s82, s82, s60\n\t" \
  "s_add_u32 s50, s82, s38\n\t" \
  "s_add_u32 s69, s82, s39\n\t" \
  "s_add_u32 s82, s48, 1\n\t" \
  CH_SEGC \
  "s_lshr_b64 s[60:61], s[90:91], s82\n\t" \
  "s_and_b32 s60, s60, 1\n\t" \
  "s_lshl_b32 s60, s60, 13\n\t" \
  "s_add_u32 s82, s82, " CH_SG0 "\n\t" \
  "s_lshl_b32 s82, s82, 21\n\t" \
  "s_or_b32 s82, s82, s60\n\t" \
  "s_add_u32 s52, s82, s43\n\t" \
  "s_add_u32 s82, s48, " CH_ARMD "\n\t" \
  CH_SEGC \
  "s_lshr_b64 s[60:61], s[90:91], s82\n\t" \
  "s_and_b32 s60, s60, 1\n\t" \
  "s_lshl_b32 s60, s60, 13\n\t" \
  "s_add_u32 s82, s82, " CH_SG0 "\n\t" \
  "s_lshl_b32 s82, s82, 21\n\t" \
  "s_or_b32 s82, s82, s60\n\t" \
  "s_add_u32 s85, s82, s45\n\t" \
  "s_cmp_lt_i32 s48, " CH_ARMS "\n\t" \
  "s_cselect_b32 s85, s27, s85\n\t" \
  "s_memrealtime s[62:63]\n\t" \
  "s_waitcnt lgkmcnt(0)\n\t" \
  "s_mov_b32 s65, s62\n\t" \
  "s_sub_u32 s47, s62, 1000\n\t" \
  "s_mov_b64 vcc, -1\n\t" \
  "s_cmp_eq_u32 s40, 0\n\t" \
  "s_cbranch_scc1 LW0_%=\n\t" \
  "s_barrier\n\t" \
  "s_cmp_eq_u32 s40, 1\n\t" \
  "s_cbranch_scc1 LG1_%=\n\t" \
  "s_cmp_eq_u32 s40, 2\n\t" \
  "s_cbranch_scc1 LG2_%=\n\t" \
  "s_branch LTK_%=\n\t" \
  ".p2align 6\n\t" \
  "LW0_%=:\n\t" \
  "v_lshlrev_b32 v78, 2, v69\n\t" \
  "v_and_b32 v78, 0x300, v78\n\t" \
  "v_lshrrev_b32 v79, 8, v69\n\t" \
  "v_lshl_add_u32 v78, v79, 6, v78\n\t" \
  "v_and_b32 v79, 48, v69\n\t" \
  "v_add_u32 v78, v78, v79\n\t" \
  "s_add_u32 s98, s49, s42\n\t" \
  "s_add_u32 s97, s98, 4096\n\t" \
  "s_add_u32 s99, s49, s44\n\t" \
  "v_mul_u32_u24 v77, 72, v69\n\t" \
  "s_barrier\n\t" \
  "s_memrealtime s[62:63]\n\t" \
  "s_waitcnt lgkmcnt(0)\n\t" \
  "s_add_u32 s62, s62, " CH_DLL2 "\n\t" \
  "s_sub_i32 s63, s62, s65\n\t" \
  "s_add_u32 s62, s62, " CH_DLX "\n\t" \
  "s_cmp_gt_i32 s63, 0\n\t" \
  "s_cselect_b32 s65, s62, s65\n\t" \
  "s_mov_b32 m0, 0\n\t" \
  "s_movk_i32 s60, 8192\n\t" \
  "LWT_%=:\n\t" \
  "s_memrealtime s[62:63]\n\t" \
  "s_sub_u32 s60, s60, 1\n\t" \
  "s_cselect_b32 s61, 0, -1\n\t" \
  "s_waitcnt lgkmcnt(0)\n\t" \
  "s_sub_i32 s63, s62, s65\n\t" \
  "s_and_b32 s63, s63, s61\n\t" \
  "s_cmp_lt_i32 s63, 0\n\t" \
  "s_cbranch_scc1 LWT_%=\n\t" \
  "LDMA_%=:\n\t" \
  CH_DPC \
  "buffer_load_dwordx4 v78, s[28:31], s98 offen offset:0 sc1 lds\n\t" \
  "buffer_load_dwordx4 v78, s[28:31], s98 offen offset:1024 sc1 lds\n\t" \
  "buffer_load_dwordx4 v78, s[28:31], s98 offen offset:2048 sc1 lds\n\t" \
  "buffer_load_dwordx4 v78, s[28:31], s98 offen offset:3072 sc1 lds\n\t" \
  "s_movk_i32 m0, 4096\n\t" \
  "s_mov_b32 s66, s62\n\t" \
  "buffer_load_dwordx4 v78, s[28:31], s97 offen offset:0 sc1 lds\n\t" \
  "buffer_load_dwordx4 v78, s[28:31], s97 offen offset:1024 sc1 lds\n\t" \
  "buffer_load_dwordx4 v78, s[28:31], s97 offen offset:2048 sc1 lds\n\t" \
  "buffer_load_dwordx4 v78, s[28:31], s97 offen offset:3072 sc1 lds\n\t" \
  "s_movk_i32 m0, 8192\n\t" \
  "s_nop 0\n\t" \
  "buffer_load_dwordx4 v69, s[28:31], s99 offen sc1 lds\n\t" \
  CH_ST1 \
  ".p2align 6\n\t" \
  "LG0_%=:\n\t" \
  "s_barrier\n\t" \
  "buffer_load_dwordx4 v[52:55], v69, s[28:31], s50 offen sc1\n\t" \
  "buffer_load_dwordx4 v[56:59], v69, s[28:31], s50 offen offset:1024 sc1\n\t" \
  "s_add_u32 s64, s50, 16384\n\t" \
  "s_movk_i32 m0, 11264\n\t" \
  "v_lshlrev_b32 v75, 1, v69\n\t" \
  "buffer_load_dword v75, s[28:31], s64 offen sc1 lds\n\t" \
  "s_memrealtime s[62:63]\n\t" \
  "s_waitcnt lgkmcnt(0)\n\t" \
  "s_sub_u32 s62, s62, " CH_TKL "\n\t" \
  "s_sub_i32 s63, s62, s65\n\t" \
  "s_cmp_gt_i32 s63, 0\n\t" \
  "s_cselect_b32 s62, s62, s65\n\t" \
  "s_sub_i32 s63, s62, s47\n\t" \
  "s_cmp_gt_i32 s63, " CH_LATE "\n\t" \
  "s_cselect_b32 s71, " CH_SCHW ", s46\n\t" \
  "s_add_u32 s61, s47, " CH_LATE "\n\t" \
  "s_cmp_gt_i32 s63, " CH_LATE "\n\t" \
  "s_cselect_b32 s62, s62, s61\n\t" \
  "s_add_u32 s71, s71, s53\n\t" \
  "s_add_u32 s60, s62, s81\n\t" \
  "s_add_u32 s60, s60, " CH_DL1L "\n\t" \
  "s_lshl_b32 s60, s60, 8\n\t" \
  "s_or_b32 s60, s60, " CH_B0 "\n\t" \
  "s_not_b32 s60, s60\n\t" \
  "v_mov_b32 v76, s60\n\t" \
  "s_mov_b64 exec, 31\n\t" \
  "buffer_atomic_umin v76, v77, s[28:31], s71 offen sc1\n\t" \
  "s_mov_b64 exec, -1\n\t" \
  "ds_read_b128 v[44:47], v70 offset:0\n\t" \
  "ds_read_b128 v[48:51], v70 offset:1024\n\t" \
  "ds_read_b128 v[60:63], v70 offset:2048\n\t" \
  "ds_read_b128 v[64:67], v70 offset:3072\n\t" \
  "v_mov_b64 v[36:37], 0\n\t" \
  "v_mov_b64 v[38:39], 0\n\t" \
  "v_mov_b64 v[40:41], 0\n\t" \
  "v_mov_b64 v[42:43], 0\n\t" \
  "v_mov_b32 v73, 0\n\t" \
  "v_lshrrev_b32 v71, 1, v69\n\t" \
  "v_lshrrev_b32 v72, 3, v69\n\t" \
  "v_and_b32 v72, 0x78, v72\n\t" \
  "v_and_b32 v74, 0x30, v69\n\t" \
  "v_lshl_add_u32 v72, v74, 3, v72\n\t" \
  "s_add_u32 s54, s53, s36\n\t" \
  "s_add_u32 s55, s54, 524288\n\t" \
  "s_add_u32 s54, s54, 0\n\t" \
  "s_add_u32 s75, s54, 262144\n\t" \
  "s_add_u32 s80, s53, s44\n\t" \
  "s_or_b32 s87, s53, 0x1fff80\n\t" \
  "s_add_u32 s87, s87, 2097152\n\t" \
  CH_TC0 \
  "s_waitcnt vmcnt(9)\n\t" \
  "s_barrier\n\t" \
  CH_DP0 \
  "ds_read_b128 v[0:3], v69 offset:0\n\t" \
  "ds_read_b128 v[4:7], v69 offset:1024\n\t" \
  "ds_read_b128 v[8:11], v69 offset:2048\n\t" \
  "ds_read_b128 v[12:15], v69 offset:3072\n\t" \
  "s_waitcnt lgkmcnt(3)\n\t" \
  "v_smfmac_f32_16x16x64_bf16 v[36:39], v[0:3], v[44:51], v68\n\t" \
  "ds_read_b128 v[44:47], v70 offset:4096\n\t" \
  "v_smfmac_f32_16x16x64_bf16 v[40:43], v[0:3], v[60:67], v68\n\t" \
  "ds_read_b128 v[48:51], v70 offset:5120\n\t" \
  "v_max3_u32 v73, v73, v0, v2\n\t" \
  "s_waitcnt lgkmcnt(4)\n\t" \
  "v_smfmac_f32_16x16x64_bf16 v[36:39], v[4:7], a[0:7], v68\n\t" \
  "s_waitcnt vmcnt(4)\n\t" \
  "s_barrier\n\t" \
  "ds_read_b128 v[16:19], v69 offset:4096\n\t" \
  CH_DP1 \
  "v_smfmac_f32_16x16x64_bf16 v[40:43], v[4:7], a[64:71], v68\n\t" \
  "ds_read_b128 v[20:23], v69 offset:5120\n\t" \
  "v_max3_u32 v73, v73, v4, v6\n\t" \
  "s_waitcnt lgkmcnt(5)\n\t" \
  "v_smfmac_f32_16x16x64_bf16 v[36:39], v[8:11], a[8:15], v68\n\t" \
  "ds_read_b128 v[24:27], v69 offset:6144\n\t" \
  "v_max3_u32 v73, v73, v8, v10\n\t" \
  "v_smfmac_f32_16x16x64_bf16 v[40:43], v[8:11], a[72:79], v68\n\t" \
  "ds_read_b128 v[28:31], v69 offset:7168\n\t" \
  "s_waitcnt lgkmcnt(6)\n\t" \
  "v_max3_u32 v73, v73, v12, v14\n\t" \
  "v_smfmac_f32_16x16x64_bf16 v[36:39], v[12:15], a[16:23], v68\n\t" \
  "ds_read_b128 v[32:35], v69 offset:8192\n\t" \
  "s_waitcnt lgkmcnt(5)\n\t" \
  "v_smfmac_f32_16x16x64_bf16 v[40:43], v[12:15], v[44:51], v68\n\t" \
  "ds_read_b128 v[60:63], v70 offset:6144\n\t" \
  "s_waitcnt lgkmcnt(5)\n\t" \
  "v_smfmac_f32_16x16x64_bf16 v[36:39], v[16:19], a[24:31], v68\n\t" \
  "ds_read_b128 v[64:67], v70 offset:7168\n\t" \
  "v_max3_u32 v73, v73, v16, v18\n\t" \
  "v_smfmac_f32_16x16x64_bf16 v[40:43], v[16:19], a[80:87], v68\n\t" \
  "s_waitcnt lgkmcnt(5)\n\t" \
  "v_max3_u32 v73, v73, v20, v22\n\t" \
  "v_smfmac_f32_16x16x64_bf16 v[36:39], v[20:23], a[32:39], v68\n\t" \
  "s_waitcnt lgkmcnt(4)\n\t" \
  "v_max3_u32 v73, v73, v24, v26\n\t" \
  "v_smfmac_f32_16x16x64_bf16 v[36:39], v[24:27], a[40:47], v68\n\t" \
  "s_waitcnt lgkmcnt(3)\n\t" \
  "v_max3_u32 v73, v73, v28, v30\n\t" \
  "v_smfmac_f32_16x16x64_bf16 v[36:39], v[28:31], a[48:55], v68\n\t" \
  "s_waitcnt lgkmcnt(2)\n\t" \
  "v_max3_u32 v73, v73, v32, v34\n\t" \
  "v_smfmac_f32_16x16x64_bf16 v[36:39], v[32:35], a[56:63], v68\n\t" \
  "v_cmp_eq_u32 vcc, -1, v73\n\t" \
  "s_waitcnt lgkmcnt(0)\n\t" \
  "v_smfmac_f32_16x16x64_bf16 v[40:43], v[20:23], v[60:67], v68\n\t" \
  "ds_read_b128 v[60:63], v70 offset:8192\n\t" \
  "v_mov_b64 v[44:45], 0\n\t" \
  "s_cbranch_vccnz LFAIL0_%=\n\t" \
  "LGO0_%=:\n\t" \
  "v_smfmac_f32_16x16x64_bf16 v[40:43], v[24:27], a[88:95], v68\n\t" \
  "ds_read_b128 v[64:67], v70 offset:9216\n\t" \
  "v_mov_b64 v[46:47], 0\n\t" \
  "v_smfmac_f32_16x16x64_bf16 v[40:43], v[28:31], a[96:103], v68\n\t" \
  "v_add_f32 v74, v36, v38\n\t" \
  "v_smfmac_f32_16x16x64_bf16 v[40:43], v[32:35], a[104:111], v68\n\t" \
  "v_add_f32 v37, v37, v39\n\t" \
  CH_DP2 \
  "v_smfmac_f32_16x16x64_bf16 v[44:47], v[0:3], a[112:119], v68\n\t" \
  "v_cvt_pk_bf16_f32 v74, v74, v37\n\t" \
  "v_smfmac_f32_16x16x64_bf16 v[44:47], v[4:7], a[120:127], v68\n\t" \
  "v_add_f32 v75, v40, v42\n\t" \
  "s_waitcnt lgkmcnt(0)\n\t" \
  "v_smfmac_f32_16x16x64_bf16 v[44:47], v[8:11], v[60:67], v68\n\t" \
  "ds_read_b128 v[60:63], v70 offset:10240\n\t" \
  "v_add_f32 v41, v41, v43\n\t" \
  "v_cvt_pk_bf16_f32 v75, v75, v41\n\t" \
  "v_smfmac_f32_16x16x64_bf16 v[44:47], v[12:15], a[128:135], v68\n\t" \
  "ds_write2st64_b32 v71, v74, v75 offset0:36 offset1:38\n\t" \
  "v_mov_b64 v[48:49], 0\n\t" \
  "v_smfmac_f32_16x16x64_bf16 v[44:47], v[16:19], a[136:143], v68\n\t" \
  "ds_read_b128 v[64:67], v70 offset:11264\n\t" \
  "v_mov_b64 v[50:51], 0\n\t" \
  "v_smfmac_f32_16x16x64_bf16 v[44:47], v[20:23], a[144:151], v68\n\t" \
  "v_smfmac_f32_16x16x64_bf16 v[44:47], v[24:27], a[152:159], v68\n\t" \
  "v_smfmac_f32_16x16x64_bf16 v[44:47], v[28:31], a[160:167], v68\n\t" \
  "s_waitcnt lgkmcnt(0)\n\t" \
  "v_smfmac_f32_16x16x64_bf16 v[44:47], v[32:35], v[60:67], v68\n\t" \
  "buffer_load_dwordx4 v[60:63], v69, s[28:31], s64 offen sc1\n\t" \
  "s_barrier\n\t" \
  "ds_read_b64_tr_b16 v[76:77], v72 offset:9216\n\t" \
  CH_DP3 \
  "v_smfmac_f32_16x16x64_bf16 v[48:51], v[0:3], a[168:175], v68\n\t" \
  "ds_read_b64_tr_b16 v[78:79], v72 offset:9728\n\t" \
  "v_smfmac_f32_16x16x64_bf16 v[48:51], v[4:7], a[176:183], v68\n\t" \
  "v_add_f32 v74, v44, v46\n\t" \
  "v_smfmac_f32_16x16x64_bf16 v[48:51], v[8:11], a[184:191], v68\n\t" \
  "v_add_f32 v45, v45, v47\n\t" \
  "v_smfmac_f32_16x16x64_bf16 v[48:51], v[12:15], a[192:199], v68\n\t" \
  "v_cvt_pk_bf16_f32 v74, v74, v45\n\t" \
  "s_waitcnt vmcnt(4)\n\t" \
  "s_waitcnt lgkmcnt(0)\n\t" \
  "v_mfma_f32_16x16x32_bf16 v[36:39], v[52:55], v[76:79], 0\n\t" \
  "buffer_load_dwordx4 v[64:67], v69, s[28:31], s64 offen offset:1024 sc1\n\t" \
  "s_waitcnt vmcnt(4)\n\t" \
  "v_mfma_f32_16x16x32_bf16 v[40:43], v[56:59], v[76:79], 0\n\t" \
  "v_mov_b64 v[52:53], 0\n\t" \
  "v_mov_b64 v[54:55], 0\n\t" \
  "v_smfmac_f32_16x16x64_bf16 v[48:51], v[16:19], a[200:207], v68\n\t" \
  "v_mov_b64 v[56:57], 0\n\t" \
  "v_smfmac_f32_16x16x64_bf16 v[48:51], v[20:23], a[208:215], v68\n\t" \
  "v_add_f32_dpp v36, v40, v36 quad_perm:[2,3,0,1] row_mask:0xf bank_mask:0xf\n\t" \
  "v_smfmac_f32_16x16x64_bf16 v[48:51], v[24:27], a[216:223], v68\n\t" \
  "v_add_f32_dpp v37, v41, v37 quad_perm:[2,3,0,1] row_mask:0xf bank_mask:0xf\n\t" \
  "v_smfmac_f32_16x16x64_bf16 v[48:51], v[28:31], a[224:231], v68\n\t" \
  "v_add_f32_dpp v38, v42, v38 quad_perm:[2,3,0,1] row_mask:0xf bank_mask:0xf\n\t" \
  "v_smfmac_f32_16x16x64_bf16 v[48:51], v[32:35], a[232:239], v68\n\t" \
  "v_add_f32_dpp v39, v43, v39 quad_perm:[2,3,0,1] row_mask:0xf bank_mask:0xf\n\t" \
  CH_DP4 \
  "v_smfmac_f32_16x16x64_bf16 v[52:55], v[0:3], a[240:247], v68\n\t" \
  "s_mov_b64 exec, s[58:59]\n\t" \
  "ds_write_b128 v69, v[36:39] offset:2048\n\t" \
  "s_mov_b64 exec, -1\n\t" \
  "v_mov_b64 v[58:59], 0\n\t" \
  "v_smfmac_f32_16x16x64_bf16 v[52:55], v[4:7], a[248:255], v68\n\t" \
  "buffer_load_dword off, s[28:31], s87 sc1 lds\n\t" \
  "v_add_f32 v75, v48, v50\n\t" \
  "v_add_f32 v49, v49, v51\n\t" \
  "v_cvt_pk_bf16_f32 v75, v75, v49\n\t" \
  "v_smfmac_f32_16x16x64_bf16 v[56:59], v[0:3], v[136:143], v68\n\t" \
  "ds_write2st64_b32 v71, v74, v75 offset0:0 offset1:2\n\t" \
  "v_smfmac_f32_16x16x64_bf16 v[56:59], v[4:7], v[144:151], v68\n\t" \
  "ds_read_b64_tr_b16 v[0:1], v72 offset:10240\n\t" \
  "ds_read_b64_tr_b16 v[2:3], v72 offset:10752\n\t" \
  "v_smfmac_f32_16x16x64_bf16 v[52:55], v[8:11], v[80:87], v68\n\t" \
  "ds_read_b128 v[36:39], v70 offset:12288\n\t" \
  "v_smfmac_f32_16x16x64_bf16 v[56:59], v[8:11], v[152:159], v68\n\t" \
  "ds_read_b128 v[40:43], v70 offset:13312\n\t" \
  "v_smfmac_f32_16x16x64_bf16 v[52:55], v[12:15], v[88:95], v68\n\t" \
  "s_waitcnt lgkmcnt(4)\n\t" \
  "s_barrier\n\t" \
  CH_DP5 \
  "ds_read_b64_tr_b16 v[44:45], v72 offset:0\n\t" \
  "ds_read_b64_tr_b16 v[46:47], v72 offset:512\n\t" \
  "v_smfmac_f32_16x16x64_bf16 v[56:59], v[12:15], v[160:167], v68\n\t" \
  "ds_read_b64_tr_b16 v[48:49], v72 offset:1024\n\t" \
  "ds_read_b64_tr_b16 v[50:51], v72 offset:1536\n\t" \
  "v_smfmac_f32_16x16x64_bf16 v[52:55], v[16:19], v[96:103], v68\n\t" \
  "ds_read_b128 v[4:7], v69 offset:2048\n\t" \
  "s_waitcnt lgkmcnt(5)\n\t" \
  "v_smfmac_f32_16x16x64_bf16 v[56:59], v[16:19], v[36:43], v68\n\t" \
  "ds_read_b128 v[8:11], v69 offset:3072\n\t" \
  "s_waitcnt lgkmcnt(4)\n\t" \
  "v_mfma_f32_16x16x32_bf16 v[12:15], v[44:47], v[76:79], 0\n\t" \
  "ds_read_b128 v[36:39], v70 offset:14336\n\t" \
  "s_waitcnt lgkmcnt(3)\n\t" \
  "v_mfma_f32_16x16x32_bf16 v[12:15], v[48:51], v[0:3], v[12:15]\n\t" \
  "ds_read_b128 v[40:43], v70 offset:15360\n\t" \
  "v_smfmac_f32_16x16x64_bf16 v[52:55], v[20:23], v[104:111], v68\n\t" \
  "s_waitcnt lgkmcnt(2)\n\t" \
  "v_add_f32 v4, v4, v8\n\t" \
  "v_add_f32 v5, v5, v9\n\t" \
  "v_add_f32 v6, v6, v10\n\t" \
  CH_DP6 \
  "v_cndmask_b32 v78, v13, v14, s[56:57]\n\t" \
  "v_cndmask_b32 v76, v12, v15, s[56:57]\n\t" \
  "v_add_f32 v7, v7, v11\n\t" \
  "v_max3_f32 v77, v4, v5, v6\n\t" \
  "v_add_f32_dpp v76, v78, v76 quad_perm:[2,3,0,1] row_mask:0xf bank_mask:0xf\n\t" \
  "v_max3_f32 v77, v77, v7, v76\n\t" \
  "v_smfmac_f32_16x16x64_bf16 v[56:59], v[20:23], v[168:175], v68\n\t" \
  "ds_read_b128 v[44:47], v70 offset:16384\n\t" \
  "v_max_f32_dpp v77, v77, v77 quad_perm:[2,3,0,1] row_mask:0xf bank_mask:0xf\n\t" \
  "v_mov_b32 v78, v77\n\t" \
  "v_smfmac_f32_16x16x64_bf16 v[52:55], v[24:27], v[112:119], v68\n\t" \
  "ds_read_b128 v[48:51], v70 offset:17408\n\t" \
  "v_permlane16_swap_b32 v77, v78\n\t" \
  "v_max_f32 v77, v77, v78\n\t" \
  "v_mov_b32 v78, v77\n\t" \
  "v_smfmac_f32_16x16x64_bf16 v[52:55], v[28:31], v[120:127], v68\n\t" \
  "ds_read_b128 v[16:19], v70 offset:18432\n\t" \
  "v_permlane32_swap_b32 v77, v78\n\t" \
  "v_max_f32 v77, v77, v78\n\t" \
  "v_smfmac_f32_16x16x64_bf16 v[52:55], v[32:35], v[128:135], v68\n\t" \
  "ds_read_b128 v[20:23], v70 offset:19456\n\t" \
  "v_mul_f32 v77, 0xbe0293ee, v77\n\t" \
  CH_DP7 \
  "v_smfmac_f32_16x16x64_bf16 v[56:59], v[24:27], v[176:183], v68\n\t" \
  "v_fmamk_f32 v4, v4, 0x3e0293ee, v77\n\t" \
  "v_smfmac_f32_16x16x64_bf16 v[56:59], v[28:31], v[184:191], v68\n\t" \
  "ds_read_b128 v[24:27], v70 offset:22528\n\t" \
  "v_fmamk_f32 v5, v5, 0x3e0293ee, v77\n\t" \
  "s_waitcnt lgkmcnt(5)\n\t" \
  "v_smfmac_f32_16x16x64_bf16 v[56:59], v[32:35], v[36:43], v68\n\t" \
  "ds_read_b128 v[28:31], v70 offset:23552\n\t" \
  "v_fmamk_f32 v6, v6, 0x3e0293ee, v77\n\t" \
  "v_fmamk_f32 v7, v7, 0x3e0293ee, v77\n\t" \
  "v_fmamk_f32 v76, v76, 0x3e0293ee, v77\n\t" \
  "v_exp_f32 v4, v4\n\t" \
  "v_exp_f32 v5, v5\n\t" \
  "v_exp_f32 v6, v6\n\t" \
  "v_exp_f32 v7, v7\n\t" \
  "v_cvt_pk_bf16_f32 v4, v4, v5\n\t" \
  "v_exp_f32 v76, v76\n\t" \
  "v_cvt_pk_bf16_f32 v5, v6, v7\n\t" \
  "v_mov_b32_dpp v12, v4 quad_perm:[0,1,0,1] row_mask:0xf bank_mask:0xf\n\t" \
  "v_mov_b32_dpp v14, v4 quad_perm:[2,3,2,3] row_mask:0xf bank_mask:0xf\n\t" \
  "v_mov_b32_dpp v13, v5 quad_perm:[0,1,0,1] row_mask:0xf bank_mask:0xf\n\t" \
  "v_mov_b32_dpp v15, v5 quad_perm:[2,3,2,3] row_mask:0xf bank_mask:0xf\n\t" \
  "v_add_f32 v52, v52, v54\n\t" \
  "v_add_f32 v53, v53, v55\n\t" \
  "s_waitcnt vmcnt(2)\n\t" \
  CH_DP8 \
  "v_mfma_f32_16x16x32_bf16 v[0:3], v[60:63], v[12:15], 0\n\t" \
  "ds_read_b128 v[36:39], v70 offset:20480\n\t" \
  "v_add_f32 v56, v56, v58\n\t" \
  "v_add_f32 v57, v57, v59\n\t" \
  "v_cvt_pk_bf16_f32 v52, v52, v53\n\t" \
  "v_cvt_pk_bf16_f32 v53, v56, v57\n\t" \
  "s_waitcnt vmcnt(1)\n\t" \
  "v_mfma_f32_16x16x32_bf16 v[4:7], v[64:67], v[12:15], 0\n\t" \
  "buffer_store_dwordx2 v[52:53], v71, s[28:31], s69 offen offset:2048 sc1\n\t" \
  "ds_read_b128 v[40:43], v70 offset:21504\n\t" \
  "v_mov_b64 v[56:57], s[88:89]\n\t" \
  "v_mov_b64 v[58:59], s[88:89]\n\t" \
  "v_mov_b32_dpp v78, v76 quad_perm:[0,1,0,1] row_mask:0xf bank_mask:0xf\n\t" \
  "v_mov_b32_dpp v79, v76 quad_perm:[2,3,2,3] row_mask:0xf bank_mask:0xf\n\t" \
  "v_mfma_f32_16x16x32_bf16 v[32:35], v[56:59], v[12:15], 0\n\t" \
  "ds_read_b128 v[60:63], v70 offset:24576\n\t" \
  "v_cvt_pk_bf16_f32 v11, v78, v79\n\t" \
  "v_cvt_pk_bf16_f32 v8, v78, v79\n\t" \
  "v_mov_b64 v[54:55], 0\n\t" \
  "v_mov_b32 v9, 0\n\t" \
  "v_mov_b32 v10, 0\n\t" \
  "v_mov_b64 v[14:15], 0\n\t" \
  "s_cmp_eq_u32 s48, 63\n\t" \
  "v_mfma_f32_16x16x32_bf16 v[32:35], v[54:57], v[8:11], v[32:35]\n\t" \
  "v_mfma_f32_16x16x32_bf16 v[0:3], v[52:55], v[8:11], v[0:3]\n\t" \
  "ds_read_b128 v[64:67], v70 offset:25600\n\t" \
  "v_mov_b64 v[56:57], 0\n\t" \
  "v_mov_b64 v[58:59], 0\n\t" \
  "v_mfma_f32_16x16x32_bf16 v[4:7], v[52:55], v[10:13], v[4:7]\n\t" \
  "s_cbranch_scc1 LLAST0_%=\n\t" \
  "v_rcp_f32 v74, v32\n\t" \
  "v_mov_b64 v[12:13], 0\n\t" \
  "v_mov_b32 v8, 0\n\t" \
  "v_mul_f32 v0, v0, v74\n\t" \
  "v_mul_f32 v1, v1, v74\n\t" \
  "v_mul_f32 v2, v2, v74\n\t" \
  "v_mul_f32 v3, v3, v74\n\t" \
  "v_cvt_pk_bf16_f32 v76, v0, v1\n\t" \
  "v_cvt_pk_bf16_f32 v77, v2, v3\n\t" \
  "v_pk_mul_f32 v[4:5], v[4:5], v[74:75] op_sel_hi:[1,0]\n\t" \
  "v_pk_mul_f32 v[6:7], v[6:7], v[74:75] op_sel_hi:[1,0]\n\t" \
  "v_cvt_pk_bf16_f32 v78, v4, v5\n\t" \
  "v_cvt_pk_bf16_f32 v79, v6, v7\n\t" \
  "ds_write_b128 v69, v[76:79] offset:4096\n\t" \
  CH_DP9 \
  "s_waitcnt lgkmcnt(9)\n\t" \
  "v_smfmac_f32_16x16x64_bf16 v[12:15], v[76:79], v[44:51], v68\n\t" \
  "ds_read_b128 v[44:47], v70 offset:26624\n\t" \
  "v_mov_b32 v11, 0\n\t" \
  "s_waitcnt lgkmcnt(8)\n\t" \
  "v_smfmac_f32_16x16x64_bf16 v[56:59], v[76:79], v[16:23], v68\n\t" \
  "ds_read_b128 v[48:51], v70 offset:27648\n\t" \
  "v_mov_b64 v[52:53], 0\n\t" \
  "s_waitcnt lgkmcnt(5)\n\t" \
  "v_smfmac_f32_16x16x64_bf16 v[8:11], v[76:79], v[36:43], v68\n\t" \
  "ds_read_b128 v[16:19], v70 offset:28672\n\t" \
  "v_and_b32 v73, 32, v69\n\t" \
  "v_smfmac_f32_16x16x64_bf16 v[52:55], v[76:79], v[24:31], v68\n\t" \
  "s_waitcnt lgkmcnt(3)\n\t" \
  "s_barrier\n\t" \
  CH_DP10 \
  "ds_read_b128 v[0:3], v69 offset:5120\n\t" \
  "ds_read_b128 v[4:7], v69 offset:6144\n\t" \
  "v_lshl_add_u32 v73, v73, 5, v69\n\t" \
  "v_lshrrev_b32 v71, 7, v69\n\t" \
  "v_and_b32 v74, 0x70, v69\n\t" \
  "v_lshl_add_u32 v71, v71, 14, v74\n\t" \
  "v_bfe_u32 v72, v69, 2, 7\n\t" \
  "v_and_b32 v74, 0x200, v69\n\t" \
  "s_waitcnt lgkmcnt(1)\n\t" \
  "v_smfmac_f32_16x16x64_bf16 v[8:11], v[0:3], v[60:67], v68\n\t" \
  "ds_read_b128 v[20:23], v70 offset:29696\n\t" \
  "v_mov_b64 v[76:77], 0\n\t" \
  "v_smfmac_f32_16x16x64_bf16 v[12:15], v[0:3], v[200:207], v68\n\t" \
  "ds_read_b128 v[32:35], v69 offset:7168\n\t" \
  "v_mov_b64 v[78:79], 0\n\t" \
  "v_smfmac_f32_16x16x64_bf16 v[52:55], v[0:3], v[44:51], v68\n\t" \
  "ds_read_b128 v[24:27], v70 offset:30720\n\t" \
  "v_lshl_add_u32 v72, v74, 3, v72\n\t" \
  "v_smfmac_f32_16x16x64_bf16 v[56:59], v[0:3], v[232:239], v68\n\t" \
  "ds_read_b128 v[28:31], v70 offset:31744\n\t" \
  "s_waitcnt lgkmcnt(4)\n\t" \
  "v_smfmac_f32_16x16x64_bf16 v[12:15], v[4:7], v[208:215], v68\n\t" \
  "ds_read_b128 v[60:63], v70 offset:32768\n\t" \
  "s_waitcnt lgkmcnt(4)\n\t" \
  "v_smfmac_f32_16x16x64_bf16 v[8:11], v[4:7], v[16:23], v68\n\t" \
  "ds_read_b128 v[64:67], v70 offset:33792\n\t" \
  "s_waitcnt lgkmcnt(4)\n\t" \
  "v_smfmac_f32_16x16x64_bf16 v[8:11], v[32:35], v[192:199], v68\n\t" \
  "ds_read_b128 v[36:39], v73 offset:4096\n\t" \
  "v_smfmac_f32_16x16x64_bf16 v[12:15], v[32:35], v[216:223], v68\n\t" \
  "ds_read_b128 v[40:43], v73 offset:6144\n\t" \
  "s_waitcnt lgkmcnt(4)\n\t" \
  "v_smfmac_f32_16x16x64_bf16 v[52:55], v[4:7], v[24:31], v68\n\t" \
  "buffer_load_dword v75, off, s[28:31], s80 offset:1024 sc1\n\t" \
  "ds_read_b128 v[44:47], v70 offset:34816\n\t" \
  "v_cvt_pk_bf16_f32 v8, v8, v9\n\t" \
  "v_smfmac_f32_16x16x64_bf16 v[52:55], v[32:35], v[224:231], v68\n\t" \
  "ds_read_b128 v[48:51], v70 offset:35840\n\t" \
  "v_cvt_pk_bf16_f32 v9, v10, v11\n\t" \
  "v_smfmac_f32_16x16x64_bf16 v[56:59], v[4:7], v[240:247], v68\n\t" \
  "v_cvt_pk_bf16_f32 v10, v12, v13\n\t" \
  "s_waitcnt lgkmcnt(4)\n\t" \
  "v_smfmac_f32_16x16x64_bf16 v[56:59], v[32:35], v[60:67], v68\n\t" \
  "v_cvt_pk_bf16_f32 v11, v14, v15\n\t" \
  "s_waitcnt lgkmcnt(3)\n\t" \
  "v_smfmac_f32_16x16x64_bf16 v[76:79], v[36:39], v[248:255], v68\n\t" \
  "buffer_store_dwordx4 v[8:11], v71, s[28:31], s54 offen sc1\n\t" \
  CH_DP11 \
  "v_cvt_pk_bf16_f32 v52, v52, v53\n\t" \
  "s_waitcnt lgkmcnt(0)\n\t" \
  "v_smfmac_f32_16x16x64_bf16 v[76:79], v[40:43], v[44:51], v68\n\t" \
  "v_cvt_pk_bf16_f32 v53, v54, v55\n\t" \
  "v_cvt_pk_bf16_f32 v54, v56, v57\n\t" \
  "v_cvt_pk_bf16_f32 v55, v58, v59\n\t" \
  "buffer_store_dwordx4 v[52:55], v71, s[28:31], s75 offen sc1\n\t" \
  "s_nop 2\n\t" \
  "v_add_f32 v76, v76, v78\n\t" \
  "v_add_f32 v77, v77, v79\n\t" \
  "v_cvt_pk_bf16_f32 v76, v76, v77\n\t" \
  "buffer_store_dword v76, v72, s[28:31], s55 offen sc1\n\t" \
  CH_DP12 \
  "s_memrealtime s[70:71]\n\t" \
  "s_waitcnt vmcnt(3)\n\t" \
  "v_readfirstlane_b32 s65, v75\n\t" \
  "s_barrier\n\t" \
  "s_waitcnt lgkmcnt(0)\n\t" \
  "s_not_b32 s65, s65\n\t" \
  "s_lshl_b32 s61, s70, 8\n\t" \
  "s_andn2_b32 s64, s65, 0xff\n\t" \
  "s_sub_u32 s64, s64, s61\n\t" \
  "s_ashr_i32 s64, s64, 8\n\t" \
  "s_cmp_eq_u32 s65, 0\n\t" \
  "s_cselect_b32 s64, " CH_DLF ", s64\n\t" \
  "s_cselect_b32 s65, " CH_B0 ", s65\n\t" \
  "s_add_u32 s47, s64, s70\n\t" \
  "s_min_i32 s64, s64, s34\n\t" \
  "s_max_i32 s64, s64, " CH_DLL1 "\n\t" \
  "s_add_u32 s64, s64, s70\n\t" \
  CH_ST2 \
  "s_add_u32 s55, s64, " CH_FDL "\n\t" \
  "s_movk_i32 s60, 8192\n\t" \
  "FWT_%=:\n\t" \
  "s_memrealtime s[72:73]\n\t" \
  "s_sub_u32 s60, s60, 1\n\t" \
  "s_cselect_b32 s61, 0, -1\n\t" \
  "s_waitcnt lgkmcnt(0)\n\t" \
  "s_sub_i32 s73, s72, s55\n\t" \
  "s_and_b32 s73, s73, s61\n\t" \
  "s_cmp_lt_i32 s73, 0\n\t" \
  "s_cbranch_scc1 FWT_%=\n\t" \
  "s_mov_b64 exec, 1\n\t" \
  "buffer_store_dword v68, off, s[28:31], s51 offset:4064 sc1\n\t" \
  "s_mov_b64 exec, -1\n\t" \
  CH_TCH \
  "s_and_b32 s65, s65, 0xff\n\t" \
  "s_add_u32 s47, s47, s65\n\t" \
  "s_add_u32 s65, s65, s64\n\t" \
  "s_sub_u32 s60, s70, s66\n\t" \
  "s_add_u32 s61, s81, " CH_UP "\n\t" \
  "s_min_u32 s60, s60, s61\n\t" \
  "s_sub_u32 s61, s81, " CH_DN "\n\t" \
  "s_max_u32 s60, s60, s61\n\t" \
  "s_cmp_eq_u32 s68, 0\n\t" \
  "s_cselect_b32 s81, s60, s81\n\t" \
  CH_TRC \
  "s_add_u32 s48, s48, 1\n\t" \
  "s_mov_b32 s49, s53\n\t" \
  "s_add_u32 s53, s53, " CH_SLOT "\n\t" \
  "s_add_u32 s60, s53, " CH_SKIP "\n\t" \
  "s_lshr_b32 s61, s96, 1\n\t" \
  "s_add_u32 s86, s86, 1\n\t" \
  "s_cmp_eq_u32 s86, 3\n\t" \
  "s_cselect_b32 s53, s60, s53\n\t" \
  "s_cselect_b32 s96, s61, s96\n\t" \
  "s_cselect_b32 s86, 0, s86\n\t" \
  "s_andn2_b32 s53, s53, 0x2000\n\t" \
  "s_and_b32 s60, s96, 1\n\t" \
  "s_lshl_b32 s60, s60, 13\n\t" \
  "s_or_b32 s53, s53, s60\n\t" \
  "s_mov_b32 s68, 0\n\t" \
  "s_add_u32 s82, s48, 0\n\t" \
  CH_SEGC \
  "s_lshr_b64 s[60:61], s[90:91], s82\n\t" \
  "s_and_b32 s60, s60, 1\n\t" \
  "s_lshl_b32 s60, s60, 13\n\t" \
  "s_add_u32 s82, s82, " CH_SG0 "\n\t" \
  "s_lshl_b32 s82, s82, 21\n\t" \
  "s_or_b32 s82, s82, s60\n\t" \
  "s_add_u32 s50, s82, s38\n\t" \
  "s_add_u32 s69, s82, s39\n\t" \
  "s_add_u32 s51, s82, s43\n\t" \
  "s_cmp_lg_u32 s34, 0\n\t" \
  "s_cbranch_scc1 LW0_%=\n\t" \
  "s_barrier\n\t" \
  "s_branch LLOAD_%=\n\t" \
  "LFAIL0_%=:\n\t" \
  "s_add_u32 s68, s68, 1\n\t" \
  "s_waitcnt lgkmcnt(0)\n\t" \
  "s_cmp_gt_u32 s68, s83\n\t" \
  "s_cbranch_scc1 LGV0_%=\n\t" \
  "s_barrier\n\t" \
  "s_memrealtime s[62:63]\n\t" \
  "s_mov_b32 m0, 0\n\t" \
  "s_waitcnt lgkmcnt(0)\n\t" \
  "s_add_u32 s65, s62, " CH_PEN "\n\t" \
  "s_branch LDMA_%=\n\t" \
  "LGV0_%=:\n\t" \
  "s_mov_b32 s83, 0\n\t" \
  "s_lshl_b32 s60, s36, 4\n\t" \
  "s_or_b32 s60, s60, s48\n\t" \
  "s_or_b32 s60, s60, 805306368\n\t" \
  "v_mov_b32 v46, s60\n\t" \
  "v_mov_b32 v47, 0\n\t" \
  "s_mov_b64 exec, 1\n\t" \
  "buffer_atomic_cmpswap v[46:47], off, s[28:31], s92 sc1\n\t" \
  "s_mov_b64 exec, -1\n\t" \
  "s_branch LGO0_%=\n\t" \
  "LLAST0_%=:\n\t" \
  "s_waitcnt vmcnt(0) lgkmcnt(0)\n\t" \
  "s_barrier\n\t" \
  "s_mov_b64 exec, 1\n\t" \
  "buffer_store_dword v68, off, s[28:31], s51 offset:4064 sc1\n\t" \
  "s_mov_b64 exec, -1\n\t" \
  "s_branch LEND_%=\n\t" \
  ".p2align 6\n\t" \
  "LG1_%=:\n\t" \
  "s_barrier\n\t" \
  "buffer_load_dwordx4 v[52:55], v69, s[28:31], s50 offen sc1\n\t" \
  "buffer_load_dwordx4 v[56:59], v69, s[28:31], s50 offen offset:1024 sc1\n\t" \
  "s_add_u32 s64, s50, 14336\n\t" \
  "s_movk_i32 m0, 11264\n\t" \
  "v_lshlrev_b32 v75, 1, v69\n\t" \
  "buffer_load_dword v75, s[28:31], s64 offen sc1 lds\n\t" \
  "ds_read_b128 v[44:47], v70 offset:0\n\t" \
  "ds_read_b128 v[48:51], v70 offset:1024\n\t" \
  "ds_read_b128 v[60:63], v70 offset:2048\n\t" \
  "ds_read_b128 v[64:67], v70 offset:3072\n\t" \
  "v_mov_b64 v[36:37], 0\n\t" \
  "v_mov_b64 v[38:39], 0\n\t" \
  "v_mov_b64 v[40:41], 0\n\t" \
  "v_mov_b64 v[42:43], 0\n\t" \
  "v_mov_b32 v73, 0\n\t" \
  "v_lshrrev_b32 v71, 1, v69\n\t" \
  "v_add_u32 v71, 4, v71\n\t" \
  "v_lshrrev_b32 v72, 3, v69\n\t" \
  "v_and_b32 v72, 0x78, v72\n\t" \
  "v_and_b32 v74, 0x30, v69\n\t" \
  "v_lshl_add_u32 v72, v74, 3, v72\n\t" \
  "s_add_u32 s54, s53, s36\n\t" \
  "s_add_u32 s55, s54, 540672\n\t" \
  "s_add_u32 s54, s54, 4096\n\t" \
  "s_add_u32 s75, s54, 262144\n\t" \
  "s_add_u32 s80, s53, s44\n\t" \
  "s_barrier\n\t" \
  CH_DP0 \
  "ds_read_b128 v[0:3], v69 offset:0\n\t" \
  "ds_read_b128 v[4:7], v69 offset:1024\n\t" \
  "ds_read_b128 v[8:11], v69 offset:2048\n\t" \
  "ds_read_b128 v[12:15], v69 offset:3072\n\t" \
  "s_waitcnt lgkmcnt(3)\n\t" \
  "v_smfmac_f32_16x16x64_bf16 v[36:39], v[0:3], v[44:51], v68\n\t" \
  "ds_read_b128 v[44:47], v70 offset:4096\n\t" \
  "v_smfmac_f32_16x16x64_bf16 v[40:43], v[0:3], v[60:67], v68\n\t" \
  "ds_read_b128 v[48:51], v70 offset:5120\n\t" \
  "v_max3_u32 v73, v73, v0, v2\n\t" \
  "s_waitcnt lgkmcnt(4)\n\t" \
  "v_smfmac_f32_16x16x64_bf16 v[36:39], v[4:7], a[0:7], v68\n\t" \
  "s_barrier\n\t" \
  "ds_read_b128 v[16:19], v69 offset:4096\n\t" \
  CH_DP1 \
  "v_smfmac_f32_16x16x64_bf16 v[40:43], v[4:7], a[64:71], v68\n\t" \
  "ds_read_b128 v[20:23], v69 offset:5120\n\t" \
  "v_max3_u32 v73, v73, v4, v6\n\t" \
  "s_waitcnt lgkmcnt(5)\n\t" \
  "v_smfmac_f32_16x16x64_bf16 v[36:39], v[8:11], a[8:15], v68\n\t" \
  "ds_read_b128 v[24:27], v69 offset:6144\n\t" \
  "v_max3_u32 v73, v73, v8, v10\n\t" \
  "v_smfmac_f32_16x16x64_bf16 v[40:43], v[8:11], a[72:79], v68\n\t" \
  "ds_read_b128 v[28:31], v69 offset:7168\n\t" \
  "s_waitcnt lgkmcnt(6)\n\t" \
  "v_max3_u32 v73, v73, v12, v14\n\t" \
  "v_smfmac_f32_16x16x64_bf16 v[36:39], v[12:15], a[16:23], v68\n\t" \
  "ds_read_b128 v[32:35], v69 offset:8192\n\t" \
  "s_waitcnt lgkmcnt(5)\n\t" \
  "v_smfmac_f32_16x16x64_bf16 v[40:43], v[12:15], v[44:51], v68\n\t" \
  "ds_read_b128 v[60:63], v70 offset:6144\n\t" \
  "s_waitcnt lgkmcnt(5)\n\t" \
  "v_smfmac_f32_16x16x64_bf16 v[36:39], v[16:19], a[24:31], v68\n\t" \
  "ds_read_b128 v[64:67], v70 offset:7168\n\t" \
  "v_max3_u32 v73, v73, v16, v18\n\t" \
  "v_smfmac_f32_16x16x64_bf16 v[40:43], v[16:19], a[80:87], v68\n\t" \
  "s_waitcnt lgkmcnt(5)\n\t" \
  "v_max3_u32 v73, v73, v20, v22\n\t" \
  "v_smfmac_f32_16x16x64_bf16 v[36:39], v[20:23], a[32:39], v68\n\t" \
  "s_waitcnt lgkmcnt(4)\n\t" \
  "v_max3_u32 v73, v73, v24, v26\n\t" \
  "v_smfmac_f32_16x16x64_bf16 v[36:39], v[24:27], a[40:47], v68\n\t" \
  "s_waitcnt lgkmcnt(3)\n\t" \
  "v_max3_u32 v73, v73, v28, v30\n\t" \
  "v_smfmac_f32_16x16x64_bf16 v[36:39], v[28:31], a[48:55], v68\n\t" \
  "s_waitcnt lgkmcnt(2)\n\t" \
  "v_max3_u32 v73, v73, v32, v34\n\t" \
  "v_smfmac_f32_16x16x64_bf16 v[36:39], v[32:35], a[56:63], v68\n\t" \
  "v_cmp_eq_u32 vcc, -1, v73\n\t" \
  "s_waitcnt lgkmcnt(0)\n\t" \
  "v_smfmac_f32_16x16x64_bf16 v[40:43], v[20:23], v[60:67], v68\n\t" \
  "ds_read_b128 v[60:63], v70 offset:8192\n\t" \
  "v_mov_b64 v[44:45], 0\n\t" \
  "s_cbranch_vccnz LFAIL1_%=\n\t" \
  "LGO1_%=:\n\t" \
  "v_smfmac_f32_16x16x64_bf16 v[40:43], v[24:27], a[88:95], v68\n\t" \
  "ds_read_b128 v[64:67], v70 offset:9216\n\t" \
  "v_mov_b64 v[46:47], 0\n\t" \
  "v_smfmac_f32_16x16x64_bf16 v[40:43], v[28:31], a[96:103], v68\n\t" \
  "v_add_f32 v74, v36, v38\n\t" \
  "v_smfmac_f32_16x16x64_bf16 v[40:43], v[32:35], a[104:111], v68\n\t" \
  "v_add_f32 v37, v37, v39\n\t" \
  CH_DP2 \
  "v_smfmac_f32_16x16x64_bf16 v[44:47], v[0:3], a[112:119], v68\n\t" \
  "v_cvt_pk_bf16_f32 v74, v74, v37\n\t" \
  "v_smfmac_f32_16x16x64_bf16 v[44:47], v[4:7], a[120:127], v68\n\t" \
  "v_add_f32 v75, v40, v42\n\t" \
  "s_waitcnt lgkmcnt(0)\n\t" \
  "v_smfmac_f32_16x16x64_bf16 v[44:47], v[8:11], v[60:67], v68\n\t" \
  "ds_read_b128 v[60:63], v70 offset:10240\n\t" \
  "v_add_f32 v41, v41, v43\n\t" \
  "v_cvt_pk_bf16_f32 v75, v75, v41\n\t" \
  "v_smfmac_f32_16x16x64_bf16 v[44:47], v[12:15], a[128:135], v68\n\t" \
  "ds_write2st64_b32 v71, v74, v75 offset0:36 offset1:38\n\t" \
  "v_mov_b64 v[48:49], 0\n\t" \
  "v_smfmac_f32_16x16x64_bf16 v[44:47], v[16:19], a[136:143], v68\n\t" \
  "ds_read_b128 v[64:67], v70 offset:11264\n\t" \
  "v_mov_b64 v[50:51], 0\n\t" \
  "v_smfmac_f32_16x16x64_bf16 v[44:47], v[20:23], a[144:151], v68\n\t" \
  "v_smfmac_f32_16x16x64_bf16 v[44:47], v[24:27], a[152:159], v68\n\t" \
  "v_smfmac_f32_16x16x64_bf16 v[44:47], v[28:31], a[160:167], v68\n\t" \
  "s_waitcnt lgkmcnt(0)\n\t" \
  "v_smfmac_f32_16x16x64_bf16 v[44:47], v[32:35], v[60:67], v68\n\t" \
  "s_barrier\n\t" \
  "ds_read_b64_tr_b16 v[76:77], v72 offset:9216\n\t" \
  CH_DP3 \
  "v_smfmac_f32_16x16x64_bf16 v[48:51], v[0:3], a[168:175], v68\n\t" \
  "buffer_load_dwordx4 v[60:63], v69, s[28:31], s64 offen sc1\n\t" \
  "ds_read_b64_tr_b16 v[78:79], v72 offset:9728\n\t" \
  "v_smfmac_f32_16x16x64_bf16 v[48:51], v[4:7], a[176:183], v68\n\t" \
  "v_add_f32 v74, v44, v46\n\t" \
  "v_smfmac_f32_16x16x64_bf16 v[48:51], v[8:11], a[184:191], v68\n\t" \
  "v_add_f32 v45, v45, v47\n\t" \
  "v_smfmac_f32_16x16x64_bf16 v[48:51], v[12:15], a[192:199], v68\n\t" \
  "v_cvt_pk_bf16_f32 v74, v74, v45\n\t" \
  "s_waitcnt vmcnt(3)\n\t" \
  "s_waitcnt lgkmcnt(0)\n\t" \
  "v_mfma_f32_16x16x32_bf16 v[36:39], v[52:55], v[76:79], 0\n\t" \
  "s_waitcnt vmcnt(2)\n\t" \
  "v_mfma_f32_16x16x32_bf16 v[40:43], v[56:59], v[76:79], 0\n\t" \
  "buffer_load_dwordx4 v[64:67], v69, s[28:31], s64 offen offset:1024 sc1\n\t" \
  "v_mov_b64 v[52:53], 0\n\t" \
  "v_mov_b64 v[54:55], 0\n\t" \
  "v_smfmac_f32_16x16x64_bf16 v[48:51], v[16:19], a[200:207], v68\n\t" \
  "v_mov_b64 v[56:57], 0\n\t" \
  "v_smfmac_f32_16x16x64_bf16 v[48:51], v[20:23], a[208:215], v68\n\t" \
  "v_add_f32_dpp v36, v40, v36 quad_perm:[2,3,0,1] row_mask:0xf bank_mask:0xf\n\t" \
  "v_smfmac_f32_16x16x64_bf16 v[48:51], v[24:27], a[216:223], v68\n\t" \
  "v_add_f32_dpp v37, v41, v37 quad_perm:[2,3,0,1] row_mask:0xf bank_mask:0xf\n\t" \
  "v_smfmac_f32_16x16x64_bf16 v[48:51], v[28:31], a[224:231], v68\n\t" \
  "v_add_f32_dpp v38, v42, v38 quad_perm:[2,3,0,1] row_mask:0xf bank_mask:0xf\n\t" \
  "v_smfmac_f32_16x16x64_bf16 v[48:51], v[32:35], a[232:239], v68\n\t" \
  "v_add_f32_dpp v39, v43, v39 quad_perm:[2,3,0,1] row_mask:0xf bank_mask:0xf\n\t" \
  CH_DP4 \
  "v_smfmac_f32_16x16x64_bf16 v[52:55], v[0:3], a[240:247], v68\n\t" \
  "s_mov_b64 exec, s[58:59]\n\t" \
  "ds_write_b128 v69, v[36:39] offset:2080\n\t" \
  "s_mov_b64 exec, -1\n\t" \
  "v_mov_b64 v[58:59], 0\n\t" \
  "v_smfmac_f32_16x16x64_bf16 v[52:55], v[4:7], a[248:255], v68\n\t" \
  "v_add_f32 v75, v48, v50\n\t" \
  "v_add_f32 v49, v49, v51\n\t" \
  "v_cvt_pk_bf16_f32 v75, v75, v49\n\t" \
  "v_smfmac_f32_16x16x64_bf16 v[56:59], v[0:3], v[136:143], v68\n\t" \
  "ds_write2st64_b32 v71, v74, v75 offset0:0 offset1:2\n\t" \
  "v_smfmac_f32_16x16x64_bf16 v[56:59], v[4:7], v[144:151], v68\n\t" \
  "ds_read_b64_tr_b16 v[0:1], v72 offset:10240\n\t" \
  "ds_read_b64_tr_b16 v[2:3], v72 offset:10752\n\t" \
  "v_smfmac_f32_16x16x64_bf16 v[52:55], v[8:11], v[80:87], v68\n\t" \
  "ds_read_b128 v[36:39], v70 offset:12288\n\t" \
  "v_smfmac_f32_16x16x64_bf16 v[56:59], v[8:11], v[152:159], v68\n\t" \
  "ds_read_b128 v[40:43], v70 offset:13312\n\t" \
  "v_smfmac_f32_16x16x64_bf16 v[52:55], v[12:15], v[88:95], v68\n\t" \
  "s_waitcnt lgkmcnt(4)\n\t" \
  "s_barrier\n\t" \
  CH_DP5 \
  "ds_read_b64_tr_b16 v[44:45], v72 offset:0\n\t" \
  "ds_read_b64_tr_b16 v[46:47], v72 offset:512\n\t" \
  "v_smfmac_f32_16x16x64_bf16 v[56:59], v[12:15], v[160:167], v68\n\t" \
  "ds_read_b64_tr_b16 v[48:49], v72 offset:1024\n\t" \
  "ds_read_b64_tr_b16 v[50:51], v72 offset:1536\n\t" \
  "v_smfmac_f32_16x16x64_bf16 v[52:55], v[16:19], v[96:103], v68\n\t" \
  "ds_read_b128 v[4:7], v69 offset:2048\n\t" \
  "s_waitcnt lgkmcnt(5)\n\t" \
  "v_smfmac_f32_16x16x64_bf16 v[56:59], v[16:19], v[36:43], v68\n\t" \
  "ds_read_b128 v[8:11], v69 offset:3072\n\t" \
  "s_waitcnt lgkmcnt(4)\n\t" \
  "v_mfma_f32_16x16x32_bf16 v[12:15], v[44:47], v[76:79], 0\n\t" \
  "ds_read_b128 v[36:39], v70 offset:14336\n\t" \
  "s_waitcnt lgkmcnt(3)\n\t" \
  "v_mfma_f32_16x16x32_bf16 v[12:15], v[48:51], v[0:3], v[12:15]\n\t" \
  "buffer_store_dwordx4 v[44:47], v69, s[28:31], s69 offen offset:0 sc1\n\t" \
  "ds_read_b128 v[40:43], v70 offset:15360\n\t" \
  "v_smfmac_f32_16x16x64_bf16 v[52:55], v[20:23], v[104:111], v68\n\t" \
  "s_waitcnt lgkmcnt(2)\n\t" \
  "v_add_f32 v4, v4, v8\n\t" \
  "v_add_f32 v5, v5, v9\n\t" \
  "v_add_f32 v6, v6, v10\n\t" \
  CH_DP6 \
  "v_cndmask_b32 v78, v13, v14, s[56:57]\n\t" \
  "v_cndmask_b32 v76, v12, v15, s[56:57]\n\t" \
  "v_add_f32 v7, v7, v11\n\t" \
  "v_max3_f32 v77, v4, v5, v6\n\t" \
  "v_add_f32_dpp v76, v78, v76 quad_perm:[2,3,0,1] row_mask:0xf bank_mask:0xf\n\t" \
  "v_max3_f32 v77, v77, v7, v76\n\t" \
  "v_smfmac_f32_16x16x64_bf16 v[56:59], v[20:23], v[168:175], v68\n\t" \
  "ds_read_b128 v[44:47], v70 offset:16384\n\t" \
  "v_max_f32_dpp v77, v77, v77 quad_perm:[2,3,0,1] row_mask:0xf bank_mask:0xf\n\t" \
  "v_mov_b32 v78, v77\n\t" \
  "v_smfmac_f32_16x16x64_bf16 v[52:55], v[24:27], v[112:119], v68\n\t" \
  "ds_read_b128 v[48:51], v70 offset:17408\n\t" \
  "v_permlane16_swap_b32 v77, v78\n\t" \
  "v_max_f32 v77, v77, v78\n\t" \
  "v_mov_b32 v78, v77\n\t" \
  "v_smfmac_f32_16x16x64_bf16 v[52:55], v[28:31], v[120:127], v68\n\t" \
  "ds_read_b128 v[16:19], v70 offset:18432\n\t" \
  "v_permlane32_swap_b32 v77, v78\n\t" \
  "v_max_f32 v77, v77, v78\n\t" \
  "v_smfmac_f32_16x16x64_bf16 v[52:55], v[32:35], v[128:135], v68\n\t" \
  "ds_read_b128 v[20:23], v70 offset:19456\n\t" \
  "v_mul_f32 v77, 0xbe0293ee, v77\n\t" \
  CH_DP7 \
  "v_smfmac_f32_16x16x64_bf16 v[56:59], v[24:27], v[176:183], v68\n\t" \
  "v_fmamk_f32 v4, v4, 0x3e0293ee, v77\n\t" \
  "v_smfmac_f32_16x16x64_bf16 v[56:59], v[28:31], v[184:191], v68\n\t" \
  "ds_read_b128 v[24:27], v70 offset:22528\n\t" \
  "v_fmamk_f32 v5, v5, 0x3e0293ee, v77\n\t" \
  "s_waitcnt lgkmcnt(5)\n\t" \
  "v_smfmac_f32_16x16x64_bf16 v[56:59], v[32:35], v[36:43], v68\n\t" \
  "ds_read_b128 v[28:31], v70 offset:23552\n\t" \
  "v_fmamk_f32 v6, v6, 0x3e0293ee, v77\n\t" \
  "v_fmamk_f32 v7, v7, 0x3e0293ee, v77\n\t" \
  "v_fmamk_f32 v76, v76, 0x3e0293ee, v77\n\t" \
  "v_exp_f32 v4, v4\n\t" \
  "v_exp_f32 v5, v5\n\t" \
  "v_exp_f32 v6, v6\n\t" \
  "v_exp_f32 v7, v7\n\t" \
  "v_cvt_pk_bf16_f32 v4, v4, v5\n\t" \
  "v_exp_f32 v76, v76\n\t" \
  "v_cvt_pk_bf16_f32 v5, v6, v7\n\t" \
  "v_mov_b32_dpp v12, v4 quad_perm:[0,1,0,1] row_mask:0xf bank_mask:0xf\n\t" \
  "v_mov_b32_dpp v14, v4 quad_perm:[2,3,2,3] row_mask:0xf bank_mask:0xf\n\t" \
  "v_mov_b32_dpp v13, v5 quad_perm:[0,1,0,1] row_mask:0xf bank_mask:0xf\n\t" \
  "v_mov_b32_dpp v15, v5 quad_perm:[2,3,2,3] row_mask:0xf bank_mask:0xf\n\t" \
  "v_add_f32 v52, v52, v54\n\t" \
  "v_add_f32 v53, v53, v55\n\t" \
  "s_waitcnt vmcnt(2)\n\t" \
  CH_DP8 \
  "v_mfma_f32_16x16x32_bf16 v[0:3], v[60:63], v[12:15], 0\n\t" \
  "ds_read_b128 v[36:39], v70 offset:20480\n\t" \
  "v_add_f32 v56, v56, v58\n\t" \
  "v_add_f32 v57, v57, v59\n\t" \
  "v_cvt_pk_bf16_f32 v52, v52, v53\n\t" \
  "v_cvt_pk_bf16_f32 v53, v56, v57\n\t" \
  "s_waitcnt vmcnt(1)\n\t" \
  "v_mfma_f32_16x16x32_bf16 v[4:7], v[64:67], v[12:15], 0\n\t" \
  "ds_read_b128 v[40:43], v70 offset:21504\n\t" \
  "v_mov_b64 v[56:57], s[88:89]\n\t" \
  "v_mov_b64 v[58:59], s[88:89]\n\t" \
  "v_mov_b32_dpp v78, v76 quad_perm:[0,1,0,1] row_mask:0xf bank_mask:0xf\n\t" \
  "v_mov_b32_dpp v79, v76 quad_perm:[2,3,2,3] row_mask:0xf bank_mask:0xf\n\t" \
  "v_mfma_f32_16x16x32_bf16 v[32:35], v[56:59], v[12:15], 0\n\t" \
  "buffer_store_dwordx2 v[52:53], v71, s[28:31], s69 offen offset:2556 sc1\n\t" \
  "ds_read_b128 v[60:63], v70 offset:24576\n\t" \
  "v_cvt_pk_bf16_f32 v11, v78, v79\n\t" \
  "v_cvt_pk_bf16_f32 v8, v78, v79\n\t" \
  "v_mov_b64 v[54:55], 0\n\t" \
  "v_mov_b32 v9, 0\n\t" \
  "v_mov_b32 v10, 0\n\t" \
  "v_mov_b64 v[14:15], 0\n\t" \
  "s_cmp_eq_u32 s48, 63\n\t" \
  "v_mfma_f32_16x16x32_bf16 v[32:35], v[54:57], v[8:11], v[32:35]\n\t" \
  "v_mfma_f32_16x16x32_bf16 v[0:3], v[52:55], v[8:11], v[0:3]\n\t" \
  "ds_read_b128 v[64:67], v70 offset:25600\n\t" \
  "v_mov_b64 v[56:57], 0\n\t" \
  "v_mov_b64 v[58:59], 0\n\t" \
  "v_mfma_f32_16x16x32_bf16 v[4:7], v[52:55], v[10:13], v[4:7]\n\t" \
  "s_cbranch_scc1 LLAST1_%=\n\t" \
  "v_rcp_f32 v74, v32\n\t" \
  "v_mov_b64 v[12:13], 0\n\t" \
  "v_mov_b32 v8, 0\n\t" \
  "v_mul_f32 v0, v0, v74\n\t" \
  "v_mul_f32 v1, v1, v74\n\t" \
  "v_mul_f32 v2, v2, v74\n\t" \
  "v_mul_f32 v3, v3, v74\n\t" \
  "v_cvt_pk_bf16_f32 v76, v0, v1\n\t" \
  "v_cvt_pk_bf16_f32 v77, v2, v3\n\t" \
  "v_pk_mul_f32 v[4:5], v[4:5], v[74:75] op_sel_hi:[1,0]\n\t" \
  "v_pk_mul_f32 v[6:7], v[6:7], v[74:75] op_sel_hi:[1,0]\n\t" \
  "v_cvt_pk_bf16_f32 v78, v4, v5\n\t" \
  "v_cvt_pk_bf16_f32 v79, v6, v7\n\t" \
  "ds_write_b128 v69, v[76:79] offset:5120\n\t" \
  CH_DP9 \
  "s_waitcnt lgkmcnt(9)\n\t" \
  "v_smfmac_f32_16x16x64_bf16 v[12:15], v[76:79], v[44:51], v68\n\t" \
  "ds_read_b128 v[44:47], v70 offset:26624\n\t" \
  "v_mov_b32 v11, 0\n\t" \
  "s_waitcnt lgkmcnt(8)\n\t" \
  "v_smfmac_f32_16x16x64_bf16 v[56:59], v[76:79], v[16:23], v68\n\t" \
  "ds_read_b128 v[48:51], v70 offset:27648\n\t" \
  "v_mov_b64 v[52:53], 0\n\t" \
  "s_waitcnt lgkmcnt(5)\n\t" \
  "v_smfmac_f32_16x16x64_bf16 v[8:11], v[76:79], v[36:43], v68\n\t" \
  "ds_read_b128 v[16:19], v70 offset:28672\n\t" \
  "v_and_b32 v73, 32, v69\n\t" \
  "v_smfmac_f32_16x16x64_bf16 v[52:55], v[76:79], v[24:31], v68\n\t" \
  "s_waitcnt lgkmcnt(3)\n\t" \
  "s_barrier\n\t" \
  CH_DP10 \
  "ds_read_b128 v[0:3], v69 offset:4096\n\t" \
  "ds_read_b128 v[4:7], v69 offset:7168\n\t" \
  "v_lshl_add_u32 v73, v73, 5, v69\n\t" \
  "v_lshrrev_b32 v71, 7, v69\n\t" \
  "v_and_b32 v74, 0x70, v69\n\t" \
  "v_lshl_add_u32 v71, v71, 14, v74\n\t" \
  "v_bfe_u32 v72, v69, 2, 7\n\t" \
  "v_and_b32 v74, 0x200, v69\n\t" \
  "s_waitcnt lgkmcnt(1)\n\t" \
  "v_smfmac_f32_16x16x64_bf16 v[8:11], v[0:3], v[60:67], v68\n\t" \
  "ds_read_b128 v[20:23], v70 offset:29696\n\t" \
  "v_mov_b64 v[76:77], 0\n\t" \
  "v_smfmac_f32_16x16x64_bf16 v[12:15], v[0:3], v[200:207], v68\n\t" \
  "ds_read_b128 v[32:35], v69 offset:6144\n\t" \
  "v_mov_b64 v[78:79], 0\n\t" \
  "v_smfmac_f32_16x16x64_bf16 v[52:55], v[0:3], v[44:51], v68\n\t" \
  "ds_read_b128 v[24:27], v70 offset:30720\n\t" \
  "v_lshl_add_u32 v72, v74, 3, v72\n\t" \
  "v_smfmac_f32_16x16x64_bf16 v[56:59], v[0:3], v[232:239], v68\n\t" \
  "ds_read_b128 v[28:31], v70 offset:31744\n\t" \
  "s_waitcnt lgkmcnt(4)\n\t" \
  "v_smfmac_f32_16x16x64_bf16 v[12:15], v[4:7], v[208:215], v68\n\t" \
  "ds_read_b128 v[60:63], v70 offset:32768\n\t" \
  "s_waitcnt lgkmcnt(4)\n\t" \
  "v_smfmac_f32_16x16x64_bf16 v[8:11], v[4:7], v[16:23], v68\n\t" \
  "ds_read_b128 v[64:67], v70 offset:33792\n\t" \
  "s_waitcnt lgkmcnt(4)\n\t" \
  "v_smfmac_f32_16x16x64_bf16 v[8:11], v[32:35], v[192:199], v68\n\t" \
  "ds_read_b128 v[36:39], v73 offset:4096\n\t" \
  "v_smfmac_f32_16x16x64_bf16 v[12:15], v[32:35], v[216:223], v68\n\t" \
  "ds_read_b128 v[40:43], v73 offset:6144\n\t" \
  "s_waitcnt lgkmcnt(4)\n\t" \
  "v_smfmac_f32_16x16x64_bf16 v[52:55], v[4:7], v[24:31], v68\n\t" \
  "ds_read_b128 v[44:47], v70 offset:34816\n\t" \
  "v_cvt_pk_bf16_f32 v8, v8, v9\n\t" \
  "v_smfmac_f32_16x16x64_bf16 v[52:55], v[32:35], v[224:231], v68\n\t" \
  "buffer_load_dword v75, off, s[28:31], s80 offset:1024 sc1\n\t" \
  "ds_read_b128 v[48:51], v70 offset:35840\n\t" \
  "v_cvt_pk_bf16_f32 v9, v10, v11\n\t" \
  "v_smfmac_f32_16x16x64_bf16 v[56:59], v[4:7], v[240:247], v68\n\t" \
  "v_cvt_pk_bf16_f32 v10, v12, v13\n\t" \
  "s_waitcnt lgkmcnt(4)\n\t" \
  "v_smfmac_f32_16x16x64_bf16 v[56:59], v[32:35], v[60:67], v68\n\t" \
  "v_cvt_pk_bf16_f32 v11, v14, v15\n\t" \
  "s_waitcnt lgkmcnt(3)\n\t" \
  "v_smfmac_f32_16x16x64_bf16 v[76:79], v[36:39], v[248:255], v68\n\t" \
  "buffer_store_dwordx4 v[8:11], v71, s[28:31], s54 offen sc1\n\t" \
  CH_DP11 \
  "v_cvt_pk_bf16_f32 v52, v52, v53\n\t" \
  "s_waitcnt lgkmcnt(0)\n\t" \
  "v_smfmac_f32_16x16x64_bf16 v[76:79], v[40:43], v[44:51], v68\n\t" \
  "v_cvt_pk_bf16_f32 v53, v54, v55\n\t" \
  "v_cvt_pk_bf16_f32 v54, v56, v57\n\t" \
  "v_cvt_pk_bf16_f32 v55, v58, v59\n\t" \
  "buffer_store_dwordx4 v[52:55], v71, s[28:31], s75 offen sc1\n\t" \
  "s_nop 2\n\t" \
  "v_add_f32 v76, v76, v78\n\t" \
  "v_add_f32 v77, v77, v79\n\t" \
  "v_cvt_pk_bf16_f32 v76, v76, v77\n\t" \
  "buffer_store_dword v76, v72, s[28:31], s55 offen sc1\n\t" \
  CH_DP12 \
  "s_memrealtime s[70:71]\n\t" \
  "s_waitcnt vmcnt(3)\n\t" \
  "v_readfirstlane_b32 s65, v75\n\t" \
  "s_barrier\n\t" \
  "s_waitcnt lgkmcnt(0)\n\t" \
  "s_not_b32 s65, s65\n\t" \
  "s_lshl_b32 s61, s70, 8\n\t" \
  "s_andn2_b32 s64, s65, 0xff\n\t" \
  "s_sub_u32 s64, s64, s61\n\t" \
  "s_ashr_i32 s64, s64, 8\n\t" \
  "s_cmp_eq_u32 s65, 0\n\t" \
  "s_cselect_b32 s64, " CH_DLF ", s64\n\t" \
  "s_min_i32 s64, s64, s34\n\t" \
  "s_max_i32 s64, s64, " CH_DLL1 "\n\t" \
  "s_add_u32 s64, s64, s70\n\t" \
  CH_ST2 \
  "s_add_u32 s55, s41, s53\n\t" \
  "s_mov_b32 s74, 0\n\t" \
  "s_movk_i32 s60, 8192\n\t" \
  "RWT1_%=:\n\t" \
  "s_memrealtime s[72:73]\n\t" \
  "s_sub_u32 s60, s60, 1\n\t" \
  "s_cselect_b32 s61, 0, -1\n\t" \
  "s_waitcnt lgkmcnt(0)\n\t" \
  "s_sub_i32 s73, s72, s64\n\t" \
  "s_and_b32 s73, s73, s61\n\t" \
  "s_cmp_lt_i32 s73, 0\n\t" \
  "s_cbranch_scc1 RWT1_%=\n\t" \
  "RRETRY1_%=:\n\t" \
  CH_DP13 \
  "buffer_load_dwordx4 v[0:3], v69, s[28:31], s55 offen offset:0 sc1\n\t" \
  "buffer_load_dwordx4 v[4:7], v69, s[28:31], s55 offen offset:1024 sc1\n\t" \
  "buffer_load_dwordx4 v[8:11], v69, s[28:31], s55 offen offset:2048 sc1\n\t" \
  "buffer_load_dwordx4 v[12:15], v69, s[28:31], s55 offen offset:3072 sc1\n\t" \
  "v_lshrrev_b32 v20, 2, v69\n\t" \
  "v_and_b32 v20, 16, v20\n\t" \
  "v_mov_b32 v21, 0x3f80\n\t" \
  "v_lshlrev_b32 v21, v20, v21\n\t" \
  "v_bfe_u32 v20, v69, 4, 2\n\t" \
  "v_cmp_eq_u32 vcc, 0, v20\n\t" \
  "s_nop 1\n\t" \
  "v_cndmask_b32 v32, 0, v21, vcc\n\t" \
  "v_cmp_eq_u32 vcc, 1, v20\n\t" \
  "s_nop 1\n\t" \
  "v_cndmask_b32 v33, 0, v21, vcc\n\t" \
  "v_cmp_eq_u32 vcc, 2, v20\n\t" \
  "s_nop 1\n\t" \
  "v_cndmask_b32 v34, 0, v21, vcc\n\t" \
  "v_cmp_eq_u32 vcc, 3, v20\n\t" \
  "s_nop 1\n\t" \
  "v_cndmask_b32 v35, 0, v21, vcc\n\t" \
  "v_bfe_u32 v38, v69, 7, 1\n\t" \
  "v_bfe_u32 v20, v69, 9, 1\n\t" \
  "v_lshl_add_u32 v38, v20, 1, v38\n\t" \
  "v_mul_u32_u24 v38, 16384, v38\n\t" \
  "v_and_b32 v20, 0x60, v69\n\t" \
  "v_add_u32 v38, v38, v20\n\t" \
  "v_bfe_u32 v20, v69, 8, 1\n\t" \
  "v_lshl_add_u32 v38, v20, 4, v38\n\t" \
  "v_bfe_u32 v20, v69, 4, 1\n\t" \
  "v_lshl_add_u32 v38, v20, 3, v38\n\t" \
  "s_add_u32 s54, s37, s53\n\t" \
  "v_mov_b32 v44, 0\n\t" \
  CH_DP17 \
  "s_waitcnt vmcnt(3)\n\t" \
  CH_DP14 \
  "v_mfma_f32_16x16x32_bf16 v[40:43], v[32:35], v[0:3], 0\n\t" \
  "v_max3_u32 v44, v44, v0, v1\n\t" \
  "v_max3_u32 v44, v44, v2, v3\n\t" \
  "s_waitcnt vmcnt(2)\n\t" \
  "v_mfma_f32_16x16x32_bf16 v[40:43], v[32:35], v[4:7], v[40:43]\n\t" \
  "v_max3_u32 v44, v44, v4, v5\n\t" \
  "v_max3_u32 v44, v44, v6, v7\n\t" \
  "s_waitcnt vmcnt(1)\n\t" \
  "v_mfma_f32_16x16x32_bf16 v[40:43], v[32:35], v[8:11], v[40:43]\n\t" \
  "v_max3_u32 v44, v44, v8, v9\n\t" \
  "v_max3_u32 v44, v44, v10, v11\n\t" \
  "s_waitcnt vmcnt(0)\n\t" \
  CH_DP15 \
  "v_mfma_f32_16x16x32_bf16 v[40:43], v[32:35], v[12:15], v[40:43]\n\t" \
  "v_max3_u32 v44, v44, v12, v13\n\t" \
  "v_max3_u32 v44, v44, v14, v15\n\t" \
  "v_cmp_eq_u32 vcc, -1, v44\n\t" \
  "s_cbranch_vccnz RFAIL1_%=\n\t" \
  "s_nop 3\n\t" \
  "v_add_f32_dpp v40, v40, v40 row_ror:8 row_mask:0xf bank_mask:0xf\n\t" \
  "v_add_f32_dpp v41, v41, v41 row_ror:8 row_mask:0xf bank_mask:0xf\n\t" \
  "v_add_f32_dpp v42, v42, v42 row_ror:8 row_mask:0xf bank_mask:0xf\n\t" \
  "v_add_f32_dpp v43, v43, v43 row_ror:8 row_mask:0xf bank_mask:0xf\n\t" \
  "v_cvt_pk_bf16_f32 v40, v40, v41\n\t" \
  "v_cvt_pk_bf16_f32 v41, v42, v43\n\t" \
  "buffer_store_dwordx2 v[40:41], v38, s[28:31], s54 offen sc1\n\t" \
  "RDONE1_%=:\n\t" \
  CH_DP16 \
  CH_ST4 \
  "s_barrier\n\t" \
  CH_ST3 \
  CH_TRC \
  "s_add_u32 s48, s48, 1\n\t" \
  "s_mov_b32 s49, s53\n\t" \
  "s_add_u32 s53, s53, " CH_SLOT "\n\t" \
  "s_add_u32 s60, s53, " CH_SKIP "\n\t" \
  "s_lshr_b32 s61, s96, 1\n\t" \
  "s_add_u32 s86, s86, 1\n\t" \
  "s_cmp_eq_u32 s86, 3\n\t" \
  "s_cselect_b32 s53, s60, s53\n\t" \
  "s_cselect_b32 s96, s61, s96\n\t" \
  "s_cselect_b32 s86, 0, s86\n\t" \
  "s_andn2_b32 s53, s53, 0x2000\n\t" \
  "s_and_b32 s60, s96, 1\n\t" \
  "s_lshl_b32 s60, s60, 13\n\t" \
  "s_or_b32 s53, s53, s60\n\t" \
  "s_mov_b32 s68, 0\n\t" \
  "s_add_u32 s82, s48, 0\n\t" \
  CH_SEGC \
  "s_lshr_b64 s[60:61], s[90:91], s82\n\t" \
  "s_and_b32 s60, s60, 1\n\t" \
  "s_lshl_b32 s60, s60, 13\n\t" \
  "s_add_u32 s82, s82, " CH_SG0 "\n\t" \
  "s_lshl_b32 s82, s82, 21\n\t" \
  "s_or_b32 s82, s82, s60\n\t" \
  "s_add_u32 s50, s82, s38\n\t" \
  "s_add_u32 s69, s82, s39\n\t" \
  "s_cmp_eq_u32 s34, 0\n\t" \
  "s_cbranch_scc1 LLOAD_%=\n\t" \
  "s_branch LG1_%=\n\t" \
  "RFAIL1_%=:\n\t" \
  "s_add_u32 s74, s74, 1\n\t" \
  "s_sleep " CH_RSL "\n\t" \
  "s_waitcnt vmcnt(0)\n\t" \
  "s_cmp_gt_u32 s74, s84\n\t" \
  "s_cbranch_scc1 RG1_%=\n\t" \
  "s_and_b32 s60, s74, 1023\n\t" \
  "s_cmp_lg_u32 s60, 0\n\t" \
  "s_cbranch_scc1 RRETRY1_%=\n\t" \
  "buffer_load_dword v46, off, s[28:31], s92 sc1\n\t" \
  "s_waitcnt vmcnt(0)\n\t" \
  "v_readfirstlane_b32 s60, v46\n\t" \
  "s_nop 0\n\t" \
  "s_cmp_eq_u32 s60, 0\n\t" \
  "s_cbranch_scc1 RRETRY1_%=\n\t" \
  "RG1_%=:\n\t" \
  "s_mov_b32 s84, 0\n\t" \
  "s_lshl_b32 s60, s36, 4\n\t" \
  "s_or_b32 s60, s60, s48\n\t" \
  "s_or_b32 s60, s60, 536870912\n\t" \
  "v_mov_b32 v46, s60\n\t" \
  "v_mov_b32 v47, 0\n\t" \
  "s_mov_b64 exec, 1\n\t" \
  "buffer_atomic_cmpswap v[46:47], off, s[28:31], s92 sc1\n\t" \
  "s_mov_b64 exec, -1\n\t" \
  "s_branch RDONE1_%=\n\t" \
  "LFAIL1_%=:\n\t" \
  "s_add_u32 s68, s68, 1\n\t" \
  "s_waitcnt lgkmcnt(0)\n\t" \
  "s_cmp_gt_u32 s68, s83\n\t" \
  "s_cbranch_scc1 LGV1_%=\n\t" \
  "s_barrier\n\t" \
  "s_waitcnt vmcnt(8)\n\t" \
  "s_branch LG1_%=\n\t" \
  "LGV1_%=:\n\t" \
  "s_mov_b32 s83, 0\n\t" \
  "s_lshl_b32 s60, s36, 4\n\t" \
  "s_or_b32 s60, s60, s48\n\t" \
  "s_or_b32 s60, s60, 805306368\n\t" \
  "v_mov_b32 v46, s60\n\t" \
  "v_mov_b32 v47, 0\n\t" \
  "s_mov_b64 exec, 1\n\t" \
  "buffer_atomic_cmpswap v[46:47], off, s[28:31], s92 sc1\n\t" \
  "s_mov_b64 exec, -1\n\t" \
  "s_branch LGO1_%=\n\t" \
  "LLAST1_%=:\n\t" \
  "s_waitcnt vmcnt(0) lgkmcnt(0)\n\t" \
  "s_barrier\n\t" \
  "s_branch LEND_%=\n\t" \
  ".p2align 6\n\t" \
  "LG2_%=:\n\t" \
  "s_barrier\n\t" \
  "buffer_load_dwordx4 v[52:55], v69, s[28:31], s50 offen sc1\n\t" \
  "buffer_load_dwordx4 v[56:59], v69, s[28:31], s50 offen offset:1024 sc1\n\t" \
  "s_add_u32 s64, s50, 18432\n\t" \
  "s_movk_i32 m0, 11264\n\t" \
  "v_lshlrev_b32 v75, 1, v69\n\t" \
  "buffer_load_dword v75, s[28:31], s64 offen sc1 lds\n\t" \
  "ds_read_b128 v[44:47], v70 offset:0\n\t" \
  "ds_read_b128 v[48:51], v70 offset:1024\n\t" \
  "ds_read_b128 v[60:63], v70 offset:2048\n\t" \
  "ds_read_b128 v[64:67], v70 offset:3072\n\t" \
  "v_mov_b64 v[36:37], 0\n\t" \
  "v_mov_b64 v[38:39], 0\n\t" \
  "v_mov_b64 v[40:41], 0\n\t" \
  "v_mov_b64 v[42:43], 0\n\t" \
  "v_mov_b32 v73, 0\n\t" \
  "v_lshrrev_b32 v71, 1, v69\n\t" \
  "v_lshrrev_b32 v72, 3, v69\n\t" \
  "v_and_b32 v72, 0x78, v72\n\t" \
  "v_and_b32 v74, 0x30, v69\n\t" \
  "v_lshl_add_u32 v72, v74, 3, v72\n\t" \
  "s_add_u32 s54, s53, s36\n\t" \
  "s_add_u32 s55, s54, 557056\n\t" \
  "s_add_u32 s54, s54, 131072\n\t" \
  "s_add_u32 s75, s54, 262144\n\t" \
  "s_add_u32 s80, s53, s44\n\t" \
  "s_barrier\n\t" \
  CH_DP0 \
  "ds_read_b128 v[0:3], v69 offset:0\n\t" \
  "ds_read_b128 v[4:7], v69 offset:1024\n\t" \
  "ds_read_b128 v[8:11], v69 offset:2048\n\t" \
  "ds_read_b128 v[12:15], v69 offset:3072\n\t" \
  "s_waitcnt lgkmcnt(3)\n\t" \
  "v_smfmac_f32_16x16x64_bf16 v[36:39], v[0:3], v[44:51], v68\n\t" \
  "ds_read_b128 v[44:47], v70 offset:4096\n\t" \
  "v_smfmac_f32_16x16x64_bf16 v[40:43], v[0:3], v[60:67], v68\n\t" \
  "ds_read_b128 v[48:51], v70 offset:5120\n\t" \
  "v_max3_u32 v73, v73, v0, v2\n\t" \
  "s_waitcnt lgkmcnt(4)\n\t" \
  "v_smfmac_f32_16x16x64_bf16 v[36:39], v[4:7], a[0:7], v68\n\t" \
  "s_barrier\n\t" \
  "ds_read_b128 v[16:19], v69 offset:4096\n\t" \
  CH_DP1 \
  "v_smfmac_f32_16x16x64_bf16 v[40:43], v[4:7], a[64:71], v68\n\t" \
  "ds_read_b128 v[20:23], v69 offset:5120\n\t" \
  "v_max3_u32 v73, v73, v4, v6\n\t" \
  "s_waitcnt lgkmcnt(5)\n\t" \
  "v_smfmac_f32_16x16x64_bf16 v[36:39], v[8:11], a[8:15], v68\n\t" \
  "ds_read_b128 v[24:27], v69 offset:6144\n\t" \
  "v_max3_u32 v73, v73, v8, v10\n\t" \
  "v_smfmac_f32_16x16x64_bf16 v[40:43], v[8:11], a[72:79], v68\n\t" \
  "ds_read_b128 v[28:31], v69 offset:7168\n\t" \
  "s_waitcnt lgkmcnt(6)\n\t" \
  "v_max3_u32 v73, v73, v12, v14\n\t" \
  "v_smfmac_f32_16x16x64_bf16 v[36:39], v[12:15], a[16:23], v68\n\t" \
  "ds_read_b128 v[32:35], v69 offset:8192\n\t" \
  "s_waitcnt lgkmcnt(5)\n\t" \
  "v_smfmac_f32_16x16x64_bf16 v[40:43], v[12:15], v[44:51], v68\n\t" \
  "ds_read_b128 v[60:63], v70 offset:6144\n\t" \
  "s_waitcnt lgkmcnt(5)\n\t" \
  "v_smfmac_f32_16x16x64_bf16 v[36:39], v[16:19], a[24:31], v68\n\t" \
  "ds_read_b128 v[64:67], v70 offset:7168\n\t" \
  "v_max3_u32 v73, v73, v16, v18\n\t" \
  "v_smfmac_f32_16x16x64_bf16 v[40:43], v[16:19], a[80:87], v68\n\t" \
  "s_waitcnt lgkmcnt(5)\n\t" \
  "v_max3_u32 v73, v73, v20, v22\n\t" \
  "v_smfmac_f32_16x16x64_bf16 v[36:39], v[20:23], a[32:39], v68\n\t" \
  "s_waitcnt lgkmcnt(4)\n\t" \
  "v_max3_u32 v73, v73, v24, v26\n\t" \
  "v_smfmac_f32_16x16x64_bf16 v[36:39], v[24:27], a[40:47], v68\n\t" \
  "s_waitcnt lgkmcnt(3)\n\t" \
  "v_max3_u32 v73, v73, v28, v30\n\t" \
  "v_smfmac_f32_16x16x64_bf16 v[36:39], v[28:31], a[48:55], v68\n\t" \
  "s_waitcnt lgkmcnt(2)\n\t" \
  "v_max3_u32 v73, v73, v32, v34\n\t" \
  "v_smfmac_f32_16x16x64_bf16 v[36:39], v[32:35], a[56:63], v68\n\t" \
  "v_cmp_eq_u32 vcc, -1, v73\n\t" \
  "s_waitcnt lgkmcnt(0)\n\t" \
  "v_smfmac_f32_16x16x64_bf16 v[40:43], v[20:23], v[60:67], v68\n\t" \
  "ds_read_b128 v[60:63], v70 offset:8192\n\t" \
  "v_mov_b64 v[44:45], 0\n\t" \
  "s_cbranch_vccnz LFAIL2_%=\n\t" \
  "LGO2_%=:\n\t" \
  "v_smfmac_f32_16x16x64_bf16 v[40:43], v[24:27], a[88:95], v68\n\t" \
  "ds_read_b128 v[64:67], v70 offset:9216\n\t" \
  "v_mov_b64 v[46:47], 0\n\t" \
  "v_smfmac_f32_16x16x64_bf16 v[40:43], v[28:31], a[96:103], v68\n\t" \
  "v_add_f32 v74, v36, v38\n\t" \
  "v_smfmac_f32_16x16x64_bf16 v[40:43], v[32:35], a[104:111], v68\n\t" \
  "v_add_f32 v37, v37, v39\n\t" \
  CH_DP2 \
  "v_smfmac_f32_16x16x64_bf16 v[44:47], v[0:3], a[112:119], v68\n\t" \
  "v_cvt_pk_bf16_f32 v74, v74, v37\n\t" \
  "v_smfmac_f32_16x16x64_bf16 v[44:47], v[4:7], a[120:127], v68\n\t" \
  "v_add_f32 v75, v40, v42\n\t" \
  "s_waitcnt lgkmcnt(0)\n\t" \
  "v_smfmac_f32_16x16x64_bf16 v[44:47], v[8:11], v[60:67], v68\n\t" \
  "ds_read_b128 v[60:63], v70 offset:10240\n\t" \
  "v_add_f32 v41, v41, v43\n\t" \
  "v_cvt_pk_bf16_f32 v75, v75, v41\n\t" \
  "v_smfmac_f32_16x16x64_bf16 v[44:47], v[12:15], a[128:135], v68\n\t" \
  "ds_write2st64_b32 v71, v74, v75 offset0:40 offset1:42\n\t" \
  "v_mov_b64 v[48:49], 0\n\t" \
  "v_smfmac_f32_16x16x64_bf16 v[44:47], v[16:19], a[136:143], v68\n\t" \
  "ds_read_b128 v[64:67], v70 offset:11264\n\t" \
  "v_mov_b64 v[50:51], 0\n\t" \
  "v_smfmac_f32_16x16x64_bf16 v[44:47], v[20:23], a[144:151], v68\n\t" \
  "v_smfmac_f32_16x16x64_bf16 v[44:47], v[24:27], a[152:159], v68\n\t" \
  "v_smfmac_f32_16x16x64_bf16 v[44:47], v[28:31], a[160:167], v68\n\t" \
  "s_waitcnt lgkmcnt(0)\n\t" \
  "v_smfmac_f32_16x16x64_bf16 v[44:47], v[32:35], v[60:67], v68\n\t" \
  "s_barrier\n\t" \
  "ds_read_b64_tr_b16 v[76:77], v72 offset:10240\n\t" \
  CH_DP3 \
  "v_smfmac_f32_16x16x64_bf16 v[48:51], v[0:3], a[168:175], v68\n\t" \
  "ds_read_b64_tr_b16 v[78:79], v72 offset:10752\n\t" \
  "v_smfmac_f32_16x16x64_bf16 v[48:51], v[4:7], a[176:183], v68\n\t" \
  "buffer_load_dwordx4 v[60:63], v69, s[28:31], s64 offen sc1\n\t" \
  "v_add_f32 v74, v44, v46\n\t" \
  "v_smfmac_f32_16x16x64_bf16 v[48:51], v[8:11], a[184:191], v68\n\t" \
  "v_add_f32 v45, v45, v47\n\t" \
  "v_smfmac_f32_16x16x64_bf16 v[48:51], v[12:15], a[192:199], v68\n\t" \
  "buffer_load_dwordx4 v[64:67], v69, s[28:31], s64 offen offset:1024 sc1\n\t" \
  "v_cvt_pk_bf16_f32 v74, v74, v45\n\t" \
  "s_waitcnt vmcnt(4)\n\t" \
  "s_waitcnt lgkmcnt(0)\n\t" \
  "v_mfma_f32_16x16x32_bf16 v[36:39], v[52:55], v[76:79], 0\n\t" \
  "s_waitcnt vmcnt(3)\n\t" \
  "v_mfma_f32_16x16x32_bf16 v[40:43], v[56:59], v[76:79], 0\n\t" \
  "v_mov_b64 v[52:53], 0\n\t" \
  "v_mov_b64 v[54:55], 0\n\t" \
  "v_smfmac_f32_16x16x64_bf16 v[48:51], v[16:19], a[200:207], v68\n\t" \
  "v_mov_b64 v[56:57], 0\n\t" \
  "v_smfmac_f32_16x16x64_bf16 v[48:51], v[20:23], a[208:215], v68\n\t" \
  "v_add_f32_dpp v36, v40, v36 quad_perm:[2,3,0,1] row_mask:0xf bank_mask:0xf\n\t" \
  "v_smfmac_f32_16x16x64_bf16 v[48:51], v[24:27], a[216:223], v68\n\t" \
  "v_add_f32_dpp v37, v41, v37 quad_perm:[2,3,0,1] row_mask:0xf bank_mask:0xf\n\t" \
  "v_smfmac_f32_16x16x64_bf16 v[48:51], v[28:31], a[224:231], v68\n\t" \
  "v_add_f32_dpp v38, v42, v38 quad_perm:[2,3,0,1] row_mask:0xf bank_mask:0xf\n\t" \
  "v_smfmac_f32_16x16x64_bf16 v[48:51], v[32:35], a[232:239], v68\n\t" \
  "v_add_f32_dpp v39, v43, v39 quad_perm:[2,3,0,1] row_mask:0xf bank_mask:0xf\n\t" \
  CH_DP4 \
  "v_smfmac_f32_16x16x64_bf16 v[52:55], v[0:3], a[240:247], v68\n\t" \
  "s_mov_b64 exec, s[58:59]\n\t" \
  "ds_write_b128 v69, v[36:39] offset:3072\n\t" \
  "s_mov_b64 exec, -1\n\t" \
  "v_mov_b64 v[58:59], 0\n\t" \
  "v_smfmac_f32_16x16x64_bf16 v[52:55], v[4:7], a[248:255], v68\n\t" \
  "v_add_f32 v75, v48, v50\n\t" \
  "v_add_f32 v49, v49, v51\n\t" \
  "v_cvt_pk_bf16_f32 v75, v75, v49\n\t" \
  "v_smfmac_f32_16x16x64_bf16 v[56:59], v[0:3], v[136:143], v68\n\t" \
  "ds_write2st64_b32 v71, v74, v75 offset0:4 offset1:6\n\t" \
  "v_smfmac_f32_16x16x64_bf16 v[56:59], v[4:7], v[144:151], v68\n\t" \
  "ds_read_b64_tr_b16 v[0:1], v72 offset:9216\n\t" \
  "ds_read_b64_tr_b16 v[2:3], v72 offset:9728\n\t" \
  "v_smfmac_f32_16x16x64_bf16 v[52:55], v[8:11], v[80:87], v68\n\t" \
  "ds_read_b128 v[36:39], v70 offset:12288\n\t" \
  "v_smfmac_f32_16x16x64_bf16 v[56:59], v[8:11], v[152:159], v68\n\t" \
  "ds_read_b128 v[40:43], v70 offset:13312\n\t" \
  "v_smfmac_f32_16x16x64_bf16 v[52:55], v[12:15], v[88:95], v68\n\t" \
  "s_waitcnt lgkmcnt(4)\n\t" \
  "s_barrier\n\t" \
  CH_DP5 \
  "ds_read_b64_tr_b16 v[44:45], v72 offset:1024\n\t" \
  "ds_read_b64_tr_b16 v[46:47], v72 offset:1536\n\t" \
  "v_smfmac_f32_16x16x64_bf16 v[56:59], v[12:15], v[160:167], v68\n\t" \
  "ds_read_b64_tr_b16 v[48:49], v72 offset:0\n\t" \
  "ds_read_b64_tr_b16 v[50:51], v72 offset:512\n\t" \
  "v_smfmac_f32_16x16x64_bf16 v[52:55], v[16:19], v[96:103], v68\n\t" \
  "ds_read_b128 v[4:7], v69 offset:2048\n\t" \
  "s_waitcnt lgkmcnt(5)\n\t" \
  "v_smfmac_f32_16x16x64_bf16 v[56:59], v[16:19], v[36:43], v68\n\t" \
  "ds_read_b128 v[8:11], v69 offset:3072\n\t" \
  "s_waitcnt lgkmcnt(4)\n\t" \
  "v_mfma_f32_16x16x32_bf16 v[12:15], v[44:47], v[76:79], 0\n\t" \
  "ds_read_b128 v[36:39], v70 offset:14336\n\t" \
  "s_waitcnt lgkmcnt(3)\n\t" \
  "v_mfma_f32_16x16x32_bf16 v[12:15], v[48:51], v[0:3], v[12:15]\n\t" \
  "ds_read_b128 v[40:43], v70 offset:15360\n\t" \
  "v_smfmac_f32_16x16x64_bf16 v[52:55], v[20:23], v[104:111], v68\n\t" \
  "buffer_store_dwordx4 v[44:47], v69, s[28:31], s69 offen offset:1024 sc1\n\t" \
  "s_waitcnt lgkmcnt(2)\n\t" \
  "v_add_f32 v4, v4, v8\n\t" \
  "v_add_f32 v5, v5, v9\n\t" \
  "v_add_f32 v6, v6, v10\n\t" \
  CH_DP6 \
  "v_cndmask_b32 v78, v13, v14, s[56:57]\n\t" \
  "v_cndmask_b32 v76, v12, v15, s[56:57]\n\t" \
  "v_add_f32 v7, v7, v11\n\t" \
  "v_max3_f32 v77, v4, v5, v6\n\t" \
  "v_add_f32_dpp v76, v78, v76 quad_perm:[2,3,0,1] row_mask:0xf bank_mask:0xf\n\t" \
  "v_max3_f32 v77, v77, v7, v76\n\t" \
  "v_smfmac_f32_16x16x64_bf16 v[56:59], v[20:23], v[168:175], v68\n\t" \
  "ds_read_b128 v[44:47], v70 offset:16384\n\t" \
  "v_max_f32_dpp v77, v77, v77 quad_perm:[2,3,0,1] row_mask:0xf bank_mask:0xf\n\t" \
  "v_mov_b32 v78, v77\n\t" \
  "v_smfmac_f32_16x16x64_bf16 v[52:55], v[24:27], v[112:119], v68\n\t" \
  "ds_read_b128 v[48:51], v70 offset:17408\n\t" \
  "v_permlane16_swap_b32 v77, v78\n\t" \
  "v_max_f32 v77, v77, v78\n\t" \
  "v_mov_b32 v78, v77\n\t" \
  "v_smfmac_f32_16x16x64_bf16 v[52:55], v[28:31], v[120:127], v68\n\t" \
  "ds_read_b128 v[16:19], v70 offset:18432\n\t" \
  "v_permlane32_swap_b32 v77, v78\n\t" \
  "v_max_f32 v77, v77, v78\n\t" \
  "v_smfmac_f32_16x16x64_bf16 v[52:55], v[32:35], v[128:135], v68\n\t" \
  "ds_read_b128 v[20:23], v70 offset:19456\n\t" \
  "v_mul_f32 v77, 0xbe0293ee, v77\n\t" \
  CH_DP7 \
  "v_smfmac_f32_16x16x64_bf16 v[56:59], v[24:27], v[176:183], v68\n\t" \
  "v_fmamk_f32 v4, v4, 0x3e0293ee, v77\n\t" \
  "v_smfmac_f32_16x16x64_bf16 v[56:59], v[28:31], v[184:191], v68\n\t" \
  "ds_read_b128 v[24:27], v70 offset:22528\n\t" \
  "v_fmamk_f32 v5, v5, 0x3e0293ee, v77\n\t" \
  "s_waitcnt lgkmcnt(5)\n\t" \
  "v_smfmac_f32_16x16x64_bf16 v[56:59], v[32:35], v[36:43], v68\n\t" \
  "ds_read_b128 v[28:31], v70 offset:23552\n\t" \
  "v_fmamk_f32 v6, v6, 0x3e0293ee, v77\n\t" \
  "v_fmamk_f32 v7, v7, 0x3e0293ee, v77\n\t" \
  "v_fmamk_f32 v76, v76, 0x3e0293ee, v77\n\t" \
  "v_exp_f32 v4, v4\n\t" \
  "v_exp_f32 v5, v5\n\t" \
  "v_exp_f32 v6, v6\n\t" \
  "v_exp_f32 v7, v7\n\t" \
  "v_cvt_pk_bf16_f32 v4, v4, v5\n\t" \
  "v_exp_f32 v76, v76\n\t" \
  "v_cvt_pk_bf16_f32 v5, v6, v7\n\t" \
  "v_mov_b32_dpp v12, v4 quad_perm:[0,1,0,1] row_mask:0xf bank_mask:0xf\n\t" \
  "v_mov_b32_dpp v14, v4 quad_perm:[2,3,2,3] row_mask:0xf bank_mask:0xf\n\t" \
  "v_mov_b32_dpp v13, v5 quad_perm:[0,1,0,1] row_mask:0xf bank_mask:0xf\n\t" \
  "v_mov_b32_dpp v15, v5 quad_perm:[2,3,2,3] row_mask:0xf bank_mask:0xf\n\t" \
  "v_add_f32 v52, v52, v54\n\t" \
  "v_add_f32 v53, v53, v55\n\t" \
  "s_waitcnt vmcnt(2)\n\t" \
  CH_DP8 \
  "v_mfma_f32_16x16x32_bf16 v[0:3], v[60:63], v[12:15], 0\n\t" \
  "ds_read_b128 v[36:39], v70 offset:20480\n\t" \
  "v_add_f32 v56, v56, v58\n\t" \
  "v_add_f32 v57, v57, v59\n\t" \
  "v_cvt_pk_bf16_f32 v52, v52, v53\n\t" \
  "v_cvt_pk_bf16_f32 v53, v56, v57\n\t" \
  "s_waitcnt vmcnt(1)\n\t" \
  "v_mfma_f32_16x16x32_bf16 v[4:7], v[64:67], v[12:15], 0\n\t" \
  "ds_read_b128 v[40:43], v70 offset:21504\n\t" \
  "v_mov_b64 v[56:57], s[88:89]\n\t" \
  "v_mov_b64 v[58:59], s[88:89]\n\t" \
  "v_mov_b32_dpp v78, v76 quad_perm:[0,1,0,1] row_mask:0xf bank_mask:0xf\n\t" \
  "v_mov_b32_dpp v79, v76 quad_perm:[2,3,2,3] row_mask:0xf bank_mask:0xf\n\t" \
  "v_mfma_f32_16x16x32_bf16 v[32:35], v[56:59], v[12:15], 0\n\t" \
  "ds_read_b128 v[60:63], v70 offset:24576\n\t" \
  "v_cvt_pk_bf16_f32 v11, v78, v79\n\t" \
  "v_cvt_pk_bf16_f32 v8, v78, v79\n\t" \
  "v_mov_b64 v[54:55], 0\n\t" \
  "v_mov_b32 v9, 0\n\t" \
  "v_mov_b32 v10, 0\n\t" \
  "v_mov_b64 v[14:15], 0\n\t" \
  "s_cmp_eq_u32 s48, 63\n\t" \
  "v_mfma_f32_16x16x32_bf16 v[32:35], v[54:57], v[8:11], v[32:35]\n\t" \
  "buffer_store_dwordx2 v[52:53], v71, s[28:31], s69 offen offset:3072 sc1\n\t" \
  "v_mfma_f32_16x16x32_bf16 v[0:3], v[52:55], v[8:11], v[0:3]\n\t" \
  "ds_read_b128 v[64:67], v70 offset:25600\n\t" \
  "v_mov_b64 v[56:57], 0\n\t" \
  "v_mov_b64 v[58:59], 0\n\t" \
  "v_mfma_f32_16x16x32_bf16 v[4:7], v[52:55], v[10:13], v[4:7]\n\t" \
  "s_cbranch_scc1 LLAST2_%=\n\t" \
  "v_rcp_f32 v74, v32\n\t" \
  "v_mov_b64 v[12:13], 0\n\t" \
  "v_mov_b32 v8, 0\n\t" \
  "v_mul_f32 v0, v0, v74\n\t" \
  "v_mul_f32 v1, v1, v74\n\t" \
  "v_mul_f32 v2, v2, v74\n\t" \
  "v_mul_f32 v3, v3, v74\n\t" \
  "v_cvt_pk_bf16_f32 v76, v0, v1\n\t" \
  "v_cvt_pk_bf16_f32 v77, v2, v3\n\t" \
  "v_pk_mul_f32 v[4:5], v[4:5], v[74:75] op_sel_hi:[1,0]\n\t" \
  "v_pk_mul_f32 v[6:7], v[6:7], v[74:75] op_sel_hi:[1,0]\n\t" \
  "v_cvt_pk_bf16_f32 v78, v4, v5\n\t" \
  "v_cvt_pk_bf16_f32 v79, v6, v7\n\t" \
  "ds_write_b128 v69, v[76:79] offset:6144\n\t" \
  CH_DP9 \
  "s_waitcnt lgkmcnt(9)\n\t" \
  "v_smfmac_f32_16x16x64_bf16 v[12:15], v[76:79], v[44:51], v68\n\t" \
  "ds_read_b128 v[44:47], v70 offset:26624\n\t" \
  "v_mov_b32 v11, 0\n\t" \
  "s_waitcnt lgkmcnt(8)\n\t" \
  "v_smfmac_f32_16x16x64_bf16 v[56:59], v[76:79], v[16:23], v68\n\t" \
  "ds_read_b128 v[48:51], v70 offset:27648\n\t" \
  "v_mov_b64 v[52:53], 0\n\t" \
  "s_waitcnt lgkmcnt(5)\n\t" \
  "v_smfmac_f32_16x16x64_bf16 v[8:11], v[76:79], v[36:43], v68\n\t" \
  "ds_read_b128 v[16:19], v70 offset:28672\n\t" \
  "v_and_b32 v73, 32, v69\n\t" \
  "v_smfmac_f32_16x16x64_bf16 v[52:55], v[76:79], v[24:31], v68\n\t" \
  "s_waitcnt lgkmcnt(3)\n\t" \
  "s_barrier\n\t" \
  CH_DP10 \
  "ds_read_b128 v[0:3], v69 offset:7168\n\t" \
  "ds_read_b128 v[4:7], v69 offset:4096\n\t" \
  "v_lshl_add_u32 v73, v73, 5, v69\n\t" \
  "v_lshrrev_b32 v71, 7, v69\n\t" \
  "v_and_b32 v74, 0x70, v69\n\t" \
  "v_lshl_add_u32 v71, v71, 14, v74\n\t" \
  "v_bfe_u32 v72, v69, 2, 7\n\t" \
  "v_and_b32 v74, 0x200, v69\n\t" \
  "s_waitcnt lgkmcnt(1)\n\t" \
  "v_smfmac_f32_16x16x64_bf16 v[8:11], v[0:3], v[60:67], v68\n\t" \
  "ds_read_b128 v[20:23], v70 offset:29696\n\t" \
  "v_mov_b64 v[76:77], 0\n\t" \
  "v_smfmac_f32_16x16x64_bf16 v[12:15], v[0:3], v[200:207], v68\n\t" \
  "ds_read_b128 v[32:35], v69 offset:5120\n\t" \
  "v_mov_b64 v[78:79], 0\n\t" \
  "v_smfmac_f32_16x16x64_bf16 v[52:55], v[0:3], v[44:51], v68\n\t" \
  "ds_read_b128 v[24:27], v70 offset:30720\n\t" \
  "v_lshl_add_u32 v72, v74, 3, v72\n\t" \
  "v_smfmac_f32_16x16x64_bf16 v[56:59], v[0:3], v[232:239], v68\n\t" \
  "ds_read_b128 v[28:31], v70 offset:31744\n\t" \
  "s_waitcnt lgkmcnt(4)\n\t" \
  "v_smfmac_f32_16x16x64_bf16 v[12:15], v[4:7], v[208:215], v68\n\t" \
  "ds_read_b128 v[60:63], v70 offset:32768\n\t" \
  "s_waitcnt lgkmcnt(4)\n\t" \
  "v_smfmac_f32_16x16x64_bf16 v[8:11], v[4:7], v[16:23], v68\n\t" \
  "ds_read_b128 v[64:67], v70 offset:33792\n\t" \
  "s_waitcnt lgkmcnt(4)\n\t" \
  "v_smfmac_f32_16x16x64_bf16 v[8:11], v[32:35], v[192:199], v68\n\t" \
  "ds_read_b128 v[36:39], v73 offset:4096\n\t" \
  "v_smfmac_f32_16x16x64_bf16 v[12:15], v[32:35], v[216:223], v68\n\t" \
  "ds_read_b128 v[40:43], v73 offset:6144\n\t" \
  "s_waitcnt lgkmcnt(4)\n\t" \
  "v_smfmac_f32_16x16x64_bf16 v[52:55], v[4:7], v[24:31], v68\n\t" \
  "ds_read_b128 v[44:47], v70 offset:34816\n\t" \
  "v_cvt_pk_bf16_f32 v8, v8, v9\n\t" \
  "v_smfmac_f32_16x16x64_bf16 v[52:55], v[32:35], v[224:231], v68\n\t" \
  "ds_read_b128 v[48:51], v70 offset:35840\n\t" \
  "v_cvt_pk_bf16_f32 v9, v10, v11\n\t" \
  "v_smfmac_f32_16x16x64_bf16 v[56:59], v[4:7], v[240:247], v68\n\t" \
  "buffer_load_dword v75, off, s[28:31], s80 offset:1024 sc1\n\t" \
  "v_cvt_pk_bf16_f32 v10, v12, v13\n\t" \
  "s_waitcnt lgkmcnt(4)\n\t" \
  "v_smfmac_f32_16x16x64_bf16 v[56:59], v[32:35], v[60:67], v68\n\t" \
  "v_cvt_pk_bf16_f32 v11, v14, v15\n\t" \
  "s_waitcnt lgkmcnt(3)\n\t" \
  "v_smfmac_f32_16x16x64_bf16 v[76:79], v[36:39], v[248:255], v68\n\t" \
  "buffer_store_dwordx4 v[8:11], v71, s[28:31], s54 offen sc1\n\t" \
  CH_DP11 \
  "v_cvt_pk_bf16_f32 v52, v52, v53\n\t" \
  "s_waitcnt lgkmcnt(0)\n\t" \
  "v_smfmac_f32_16x16x64_bf16 v[76:79], v[40:43], v[44:51], v68\n\t" \
  "v_cvt_pk_bf16_f32 v53, v54, v55\n\t" \
  "v_cvt_pk_bf16_f32 v54, v56, v57\n\t" \
  "v_cvt_pk_bf16_f32 v55, v58, v59\n\t" \
  "buffer_store_dwordx4 v[52:55], v71, s[28:31], s75 offen sc1\n\t" \
  "s_nop 2\n\t" \
  "v_add_f32 v76, v76, v78\n\t" \
  "v_add_f32 v77, v77, v79\n\t" \
  "v_cvt_pk_bf16_f32 v76, v76, v77\n\t" \
  "buffer_store_dword v76, v72, s[28:31], s55 offen sc1\n\t" \
  CH_DP12 \
  "s_memrealtime s[70:71]\n\t" \
  "s_waitcnt vmcnt(3)\n\t" \
  "v_readfirstlane_b32 s65, v75\n\t" \
  "s_barrier\n\t" \
  "s_waitcnt lgkmcnt(0)\n\t" \
  "s_not_b32 s65, s65\n\t" \
  "s_lshl_b32 s61, s70, 8\n\t" \
  "s_andn2_b32 s64, s65, 0xff\n\t" \
  "s_sub_u32 s64, s64, s61\n\t" \
  "s_ashr_i32 s64, s64, 8\n\t" \
  "s_cmp_eq_u32 s65, 0\n\t" \
  "s_cselect_b32 s64, " CH_DLF ", s64\n\t" \
  "s_min_i32 s64, s64, s34\n\t" \
  "s_max_i32 s64, s64, " CH_DLL1 "\n\t" \
  "s_add_u32 s64, s64, s70\n\t" \
  CH_ST2 \
  "s_add_u32 s55, s41, s53\n\t" \
  "s_mov_b32 s74, 0\n\t" \
  "s_movk_i32 s60, 8192\n\t" \
  "RWT2_%=:\n\t" \
  "s_memrealtime s[72:73]\n\t" \
  "s_sub_u32 s60, s60, 1\n\t" \
  "s_cselect_b32 s61, 0, -1\n\t" \
  "s_waitcnt lgkmcnt(0)\n\t" \
  "s_sub_i32 s73, s72, s64\n\t" \
  "s_and_b32 s73, s73, s61\n\t" \
  "s_cmp_lt_i32 s73, 0\n\t" \
  "s_cbranch_scc1 RWT2_%=\n\t" \
  "RRETRY2_%=:\n\t" \
  CH_DP13 \
  "buffer_load_dwordx4 v[0:3], v69, s[28:31], s55 offen offset:0 sc1\n\t" \
  "buffer_load_dwordx4 v[4:7], v69, s[28:31], s55 offen offset:1024 sc1\n\t" \
  "buffer_load_dwordx4 v[8:11], v69, s[28:31], s55 offen offset:2048 sc1\n\t" \
  "buffer_load_dwordx4 v[12:15], v69, s[28:31], s55 offen offset:3072 sc1\n\t" \
  "v_lshrrev_b32 v20, 2, v69\n\t" \
  "v_and_b32 v20, 16, v20\n\t" \
  "v_mov_b32 v21, 0x3f80\n\t" \
  "v_lshlrev_b32 v21, v20, v21\n\t" \
  "v_bfe_u32 v20, v69, 4, 2\n\t" \
  "v_cmp_eq_u32 vcc, 0, v20\n\t" \
  "s_nop 1\n\t" \
  "v_cndmask_b32 v32, 0, v21, vcc\n\t" \
  "v_cmp_eq_u32 vcc, 1, v20\n\t" \
  "s_nop 1\n\t" \
  "v_cndmask_b32 v33, 0, v21, vcc\n\t" \
  "v_cmp_eq_u32 vcc, 2, v20\n\t" \
  "s_nop 1\n\t" \
  "v_cndmask_b32 v34, 0, v21, vcc\n\t" \
  "v_cmp_eq_u32 vcc, 3, v20\n\t" \
  "s_nop 1\n\t" \
  "v_cndmask_b32 v35, 0, v21, vcc\n\t" \
  "v_bfe_u32 v38, v69, 7, 1\n\t" \
  "v_bfe_u32 v20, v69, 9, 1\n\t" \
  "v_lshl_add_u32 v38, v20, 1, v38\n\t" \
  "v_mul_u32_u24 v38, 16384, v38\n\t" \
  "v_and_b32 v20, 0x60, v69\n\t" \
  "v_add_u32 v38, v38, v20\n\t" \
  "v_bfe_u32 v20, v69, 8, 1\n\t" \
  "v_lshl_add_u32 v38, v20, 4, v38\n\t" \
  "v_bfe_u32 v20, v69, 4, 1\n\t" \
  "v_lshl_add_u32 v38, v20, 3, v38\n\t" \
  "s_add_u32 s54, s37, s53\n\t" \
  "v_mov_b32 v44, 0\n\t" \
  CH_DP17 \
  "s_waitcnt vmcnt(3)\n\t" \
  CH_DP14 \
  "v_mfma_f32_16x16x32_bf16 v[40:43], v[32:35], v[0:3], 0\n\t" \
  "v_max3_u32 v44, v44, v0, v1\n\t" \
  "v_max3_u32 v44, v44, v2, v3\n\t" \
  "s_waitcnt vmcnt(2)\n\t" \
  "v_mfma_f32_16x16x32_bf16 v[40:43], v[32:35], v[4:7], v[40:43]\n\t" \
  "v_max3_u32 v44, v44, v4, v5\n\t" \
  "v_max3_u32 v44, v44, v6, v7\n\t" \
  "s_waitcnt vmcnt(1)\n\t" \
  "v_mfma_f32_16x16x32_bf16 v[40:43], v[32:35], v[8:11], v[40:43]\n\t" \
  "v_max3_u32 v44, v44, v8, v9\n\t" \
  "v_max3_u32 v44, v44, v10, v11\n\t" \
  "s_waitcnt vmcnt(0)\n\t" \
  CH_DP15 \
  "v_mfma_f32_16x16x32_bf16 v[40:43], v[32:35], v[12:15], v[40:43]\n\t" \
  "v_max3_u32 v44, v44, v12, v13\n\t" \
  "v_max3_u32 v44, v44, v14, v15\n\t" \
  "v_cmp_eq_u32 vcc, -1, v44\n\t" \
  "s_cbranch_vccnz RFAIL2_%=\n\t" \
  "s_nop 3\n\t" \
  "v_add_f32_dpp v40, v40, v40 row_ror:8 row_mask:0xf bank_mask:0xf\n\t" \
  "v_add_f32_dpp v41, v41, v41 row_ror:8 row_mask:0xf bank_mask:0xf\n\t" \
  "v_add_f32_dpp v42, v42, v42 row_ror:8 row_mask:0xf bank_mask:0xf\n\t" \
  "v_add_f32_dpp v43, v43, v43 row_ror:8 row_mask:0xf bank_mask:0xf\n\t" \
  "v_cvt_pk_bf16_f32 v40, v40, v41\n\t" \
  "v_cvt_pk_bf16_f32 v41, v42, v43\n\t" \
  "buffer_store_dwordx2 v[40:41], v38, s[28:31], s54 offen sc1\n\t" \
  "RDONE2_%=:\n\t" \
  CH_DP16 \
  CH_ST4 \
  "s_barrier\n\t" \
  CH_ST3 \
  CH_TRC \
  "s_add_u32 s48, s48, 1\n\t" \
  "s_mov_b32 s49, s53\n\t" \
  "s_add_u32 s53, s53, " CH_SLOT "\n\t" \
  "s_add_u32 s60, s53, " CH_SKIP "\n\t" \
  "s_lshr_b32 s61, s96, 1\n\t" \
  "s_add_u32 s86, s86, 1\n\t" \
  "s_cmp_eq_u32 s86, 3\n\t" \
  "s_cselect_b32 s53, s60, s53\n\t" \
  "s_cselect_b32 s96, s61, s96\n\t" \
  "s_cselect_b32 s86, 0, s86\n\t" \
  "s_andn2_b32 s53, s53, 0x2000\n\t" \
  "s_and_b32 s60, s96, 1\n\t" \
  "s_lshl_b32 s60, s60, 13\n\t" \
  "s_or_b32 s53, s53, s60\n\t" \
  "s_mov_b32 s68, 0\n\t" \
  "s_add_u32 s82, s48, 0\n\t" \
  CH_SEGC \
  "s_lshr_b64 s[60:61], s[90:91], s82\n\t" \
  "s_and_b32 s60, s60, 1\n\t" \
  "s_lshl_b32 s60, s60, 13\n\t" \
  "s_add_u32 s82, s82, " CH_SG0 "\n\t" \
  "s_lshl_b32 s82, s82, 21\n\t" \
  "s_or_b32 s82, s82, s60\n\t" \
  "s_add_u32 s50, s82, s38\n\t" \
  "s_add_u32 s69, s82, s39\n\t" \
  "s_cmp_eq_u32 s34, 0\n\t" \
  "s_cbranch_scc1 LLOAD_%=\n\t" \
  "s_branch LG2_%=\n\t" \
  "RFAIL2_%=:\n\t" \
  "s_add_u32 s74, s74, 1\n\t" \
  "s_sleep " CH_RSL "\n\t" \
  "s_waitcnt vmcnt(0)\n\t" \
  "s_cmp_gt_u32 s74, s84\n\t" \
  "s_cbranch_scc1 RG2_%=\n\t" \
  "s_and_b32 s60, s74, 1023\n\t" \
  "s_cmp_lg_u32 s60, 0\n\t" \
  "s_cbranch_scc1 RRETRY2_%=\n\t" \
  "buffer_load_dword v46, off, s[28:31], s92 sc1\n\t" \
  "s_waitcnt vmcnt(0)\n\t" \
  "v_readfirstlane_b32 s60, v46\n\t" \
  "s_nop 0\n\t" \
  "s_cmp_eq_u32 s60, 0\n\t" \
  "s_cbranch_scc1 RRETRY2_%=\n\t" \
  "RG2_%=:\n\t" \
  "s_mov_b32 s84, 0\n\t" \
  "s_lshl_b32 s60, s36, 4\n\t" \
  "s_or_b32 s60, s60, s48\n\t" \
  "s_or_b32 s60, s60, 536870912\n\t" \
  "v_mov_b32 v46, s60\n\t" \
  "v_mov_b32 v47, 0\n\t" \
  "s_mov_b64 exec, 1\n\t" \
  "buffer_atomic_cmpswap v[46:47], off, s[28:31], s92 sc1\n\t" \
  "s_mov_b64 exec, -1\n\t" \
  "s_branch RDONE2_%=\n\t" \
  "LFAIL2_%=:\n\t" \
  "s_add_u32 s68, s68, 1\n\t" \
  "s_waitcnt lgkmcnt(0)\n\t" \
  "s_cmp_gt_u32 s68, s83\n\t" \
  "s_cbranch_scc1 LGV2_%=\n\t" \
  "s_barrier\n\t" \
  "s_waitcnt vmcnt(8)\n\t" \
  "s_branch LG2_%=\n\t" \
  "LGV2_%=:\n\t" \
  "s_mov_b32 s83, 0\n\t" \
  "s_lshl_b32 s60, s36, 4\n\t" \
  "s_or_b32 s60, s60, s48\n\t" \
  "s_or_b32 s60, s60, 805306368\n\t" \
  "v_mov_b32 v46, s60\n\t" \
  "v_mov_b32 v47, 0\n\t" \
  "s_mov_b64 exec, 1\n\t" \
  "buffer_atomic_cmpswap v[46:47], off, s[28:31], s92 sc1\n\t" \
  "s_mov_b64 exec, -1\n\t" \
  "s_branch LGO2_%=\n\t" \
  "LLAST2_%=:\n\t" \
  "s_waitcnt vmcnt(0) lgkmcnt(0)\n\t" \
  "s_barrier\n\t" \
  "s_branch LEND_%=\n\t" \
  ".p2align 6\n\t" \
  "LG3_%=:\n\t" \
  "s_barrier\n\t" \
  "buffer_load_dwordx4 v[52:55], v69, s[28:31], s50 offen sc1\n\t" \
  "buffer_load_dwordx4 v[56:59], v69, s[28:31], s50 offen offset:1024 sc1\n\t" \
  "s_add_u32 s64, s50, 16384\n\t" \
  "s_movk_i32 m0, 11264\n\t" \
  "v_lshlrev_b32 v75, 1, v69\n\t" \
  "buffer_load_dword v75, s[28:31], s64 offen sc1 lds\n\t" \
  "s_add_u32 s47, s81, " CH_DL3 "\n\t" \
  "s_lshl_b32 s47, s47, 8\n\t" \
  "s_or_b32 s47, s47, " CH_B0 "\n\t" \
  "s_not_b32 s47, s47\n\t" \
  "s_add_u32 s71, s53, s46\n\t" \
  "ds_read_b128 v[44:47], v70 offset:0\n\t" \
  "ds_read_b128 v[48:51], v70 offset:1024\n\t" \
  "ds_read_b128 v[60:63], v70 offset:2048\n\t" \
  "ds_read_b128 v[64:67], v70 offset:3072\n\t" \
  "v_mov_b64 v[36:37], 0\n\t" \
  "v_mov_b64 v[38:39], 0\n\t" \
  "v_mov_b64 v[40:41], 0\n\t" \
  "v_mov_b64 v[42:43], 0\n\t" \
  "v_mov_b32 v73, 0\n\t" \
  "v_lshrrev_b32 v71, 1, v69\n\t" \
  "v_add_u32 v71, 4, v71\n\t" \
  "v_lshrrev_b32 v72, 3, v69\n\t" \
  "v_and_b32 v72, 0x78, v72\n\t" \
  "v_and_b32 v74, 0x30, v69\n\t" \
  "v_lshl_add_u32 v72, v74, 3, v72\n\t" \
  "s_add_u32 s54, s53, s36\n\t" \
  "s_add_u32 s55, s54, 573440\n\t" \
  "s_add_u32 s54, s54, 135168\n\t" \
  "s_add_u32 s75, s54, 262144\n\t" \
  "s_add_u32 s80, s53, s44\n\t" \
  "s_or_b32 s87, s50, 0x1fff80\n\t" \
  "s_add_u32 s87, s87, 4194304\n\t" \
  CH_TC0 \
  "s_barrier\n\t" \
  CH_DP0 \
  "ds_read_b128 v[0:3], v69 offset:0\n\t" \
  "ds_read_b128 v[4:7], v69 offset:1024\n\t" \
  "ds_read_b128 v[8:11], v69 offset:2048\n\t" \
  "ds_read_b128 v[12:15], v69 offset:3072\n\t" \
  "s_waitcnt lgkmcnt(3)\n\t" \
  "v_smfmac_f32_16x16x64_bf16 v[36:39], v[0:3], v[44:51], v68\n\t" \
  "ds_read_b128 v[44:47], v70 offset:4096\n\t" \
  "v_smfmac_f32_16x16x64_bf16 v[40:43], v[0:3], v[60:67], v68\n\t" \
  "ds_read_b128 v[48:51], v70 offset:5120\n\t" \
  "v_max3_u32 v73, v73, v0, v2\n\t" \
  "s_waitcnt lgkmcnt(4)\n\t" \
  "v_smfmac_f32_16x16x64_bf16 v[36:39], v[4:7], a[0:7], v68\n\t" \
  "s_barrier\n\t" \
  "ds_read_b128 v[16:19], v69 offset:4096\n\t" \
  CH_DP1 \
  "v_smfmac_f32_16x16x64_bf16 v[40:43], v[4:7], a[64:71], v68\n\t" \
  "ds_read_b128 v[20:23], v69 offset:5120\n\t" \
  "v_max3_u32 v73, v73, v4, v6\n\t" \
  "s_waitcnt lgkmcnt(5)\n\t" \
  "v_smfmac_f32_16x16x64_bf16 v[36:39], v[8:11], a[8:15], v68\n\t" \
  "ds_read_b128 v[24:27], v69 offset:6144\n\t" \
  "v_max3_u32 v73, v73, v8, v10\n\t" \
  "v_smfmac_f32_16x16x64_bf16 v[40:43], v[8:11], a[72:79], v68\n\t" \
  "ds_read_b128 v[28:31], v69 offset:7168\n\t" \
  "s_waitcnt lgkmcnt(6)\n\t" \
  "v_max3_u32 v73, v73, v12, v14\n\t" \
  "v_smfmac_f32_16x16x64_bf16 v[36:39], v[12:15], a[16:23], v68\n\t" \
  "ds_read_b128 v[32:35], v69 offset:8192\n\t" \
  "s_waitcnt lgkmcnt(5)\n\t" \
  "v_smfmac_f32_16x16x64_bf16 v[40:43], v[12:15], v[44:51], v68\n\t" \
  "ds_read_b128 v[60:63], v70 offset:6144\n\t" \
  "s_waitcnt lgkmcnt(5)\n\t" \
  "v_smfmac_f32_16x16x64_bf16 v[36:39], v[16:19], a[24:31], v68\n\t" \
  "ds_read_b128 v[64:67], v70 offset:7168\n\t" \
  "v_max3_u32 v73, v73, v16, v18\n\t" \
  "v_smfmac_f32_16x16x64_bf16 v[40:43], v[16:19], a[80:87], v68\n\t" \
  "s_waitcnt lgkmcnt(5)\n\t" \
  "v_max3_u32 v73, v73, v20, v22\n\t" \
  "v_smfmac_f32_16x16x64_bf16 v[36:39], v[20:23], a[32:39], v68\n\t" \
  "s_waitcnt lgkmcnt(4)\n\t" \
  "v_max3_u32 v73, v73, v24, v26\n\t" \
  "v_smfmac_f32_16x16x64_bf16 v[36:39], v[24:27], a[40:47], v68\n\t" \
  "s_waitcnt lgkmcnt(3)\n\t" \
  "v_max3_u32 v73, v73, v28, v30\n\t" \
  "v_smfmac_f32_16x16x64_bf16 v[36:39], v[28:31], a[48:55], v68\n\t" \
  "s_waitcnt lgkmcnt(2)\n\t" \
  "v_max3_u32 v73, v73, v32, v34\n\t" \
  "v_smfmac_f32_16x16x64_bf16 v[36:39], v[32:35], a[56:63], v68\n\t" \
  "v_cmp_eq_u32 vcc, -1, v73\n\t" \
  "s_waitcnt lgkmcnt(0)\n\t" \
  "v_smfmac_f32_16x16x64_bf16 v[40:43], v[20:23], v[60:67], v68\n\t" \
  "ds_read_b128 v[60:63], v70 offset:8192\n\t" \
  "v_mov_b64 v[44:45], 0\n\t" \
  "s_cbranch_vccnz LFAIL3_%=\n\t" \
  "LGO3_%=:\n\t" \
  "v_smfmac_f32_16x16x64_bf16 v[40:43], v[24:27], a[88:95], v68\n\t" \
  "ds_read_b128 v[64:67], v70 offset:9216\n\t" \
  "v_mov_b64 v[46:47], 0\n\t" \
  "v_smfmac_f32_16x16x64_bf16 v[40:43], v[28:31], a[96:103], v68\n\t" \
  "v_add_f32 v74, v36, v38\n\t" \
  "v_smfmac_f32_16x16x64_bf16 v[40:43], v[32:35], a[104:111], v68\n\t" \
  "v_add_f32 v37, v37, v39\n\t" \
  CH_DP2 \
  "v_smfmac_f32_16x16x64_bf16 v[44:47], v[0:3], a[112:119], v68\n\t" \
  "v_cvt_pk_bf16_f32 v74, v74, v37\n\t" \
  "v_smfmac_f32_16x16x64_bf16 v[44:47], v[4:7], a[120:127], v68\n\t" \
  "v_add_f32 v75, v40, v42\n\t" \
  "s_waitcnt lgkmcnt(0)\n\t" \
  "v_smfmac_f32_16x16x64_bf16 v[44:47], v[8:11], v[60:67], v68\n\t" \
  "ds_read_b128 v[60:63], v70 offset:10240\n\t" \
  "v_add_f32 v41, v41, v43\n\t" \
  "v_cvt_pk_bf16_f32 v75, v75, v41\n\t" \
  "v_smfmac_f32_16x16x64_bf16 v[44:47], v[12:15], a[128:135], v68\n\t" \
  "ds_write2st64_b32 v71, v74, v75 offset0:40 offset1:42\n\t" \
  "v_mov_b64 v[48:49], 0\n\t" \
  "v_smfmac_f32_16x16x64_bf16 v[44:47], v[16:19], a[136:143], v68\n\t" \
  "ds_read_b128 v[64:67], v70 offset:11264\n\t" \
  "v_mov_b64 v[50:51], 0\n\t" \
  "v_smfmac_f32_16x16x64_bf16 v[44:47], v[20:23], a[144:151], v68\n\t" \
  "v_mul_u32_u24 v73, 72, v69\n\t" \
  "v_smfmac_f32_16x16x64_bf16 v[44:47], v[24:27], a[152:159], v68\n\t" \
  "v_smfmac_f32_16x16x64_bf16 v[44:47], v[28:31], a[160:167], v68\n\t" \
  "s_waitcnt lgkmcnt(0)\n\t" \
  "v_smfmac_f32_16x16x64_bf16 v[44:47], v[32:35], v[60:67], v68\n\t" \
  "s_barrier\n\t" \
  "ds_read_b64_tr_b16 v[76:77], v72 offset:10240\n\t" \
  CH_DP3 \
  "v_smfmac_f32_16x16x64_bf16 v[48:51], v[0:3], a[168:175], v68\n\t" \
  "ds_read_b64_tr_b16 v[78:79], v72 offset:10752\n\t" \
  "v_smfmac_f32_16x16x64_bf16 v[48:51], v[4:7], a[176:183], v68\n\t" \
  "v_add_f32 v74, v44, v46\n\t" \
  "v_smfmac_f32_16x16x64_bf16 v[48:51], v[8:11], a[184:191], v68\n\t" \
  "buffer_load_dwordx4 v[60:63], v69, s[28:31], s64 offen sc1\n\t" \
  "v_add_f32 v45, v45, v47\n\t" \
  "v_smfmac_f32_16x16x64_bf16 v[48:51], v[12:15], a[192:199], v68\n\t" \
  "v_cvt_pk_bf16_f32 v74, v74, v45\n\t" \
  "s_waitcnt vmcnt(3)\n\t" \
  "s_waitcnt lgkmcnt(0)\n\t" \
  "v_mfma_f32_16x16x32_bf16 v[36:39], v[52:55], v[76:79], 0\n\t" \
  "s_waitcnt vmcnt(2)\n\t" \
  "v_mfma_f32_16x16x32_bf16 v[40:43], v[56:59], v[76:79], 0\n\t" \
  "v_mov_b64 v[52:53], 0\n\t" \
  "v_mov_b64 v[54:55], 0\n\t" \
  "v_smfmac_f32_16x16x64_bf16 v[48:51], v[16:19], a[200:207], v68\n\t" \
  "buffer_load_dwordx4 v[64:67], v69, s[28:31], s64 offen offset:1024 sc1\n\t" \
  "v_mov_b64 v[56:57], 0\n\t" \
  "v_smfmac_f32_16x16x64_bf16 v[48:51], v[20:23], a[208:215], v68\n\t" \
  "v_add_f32_dpp v36, v40, v36 quad_perm:[2,3,0,1] row_mask:0xf bank_mask:0xf\n\t" \
  "v_smfmac_f32_16x16x64_bf16 v[48:51], v[24:27], a[216:223], v68\n\t" \
  "v_add_f32_dpp v37, v41, v37 quad_perm:[2,3,0,1] row_mask:0xf bank_mask:0xf\n\t" \
  "v_smfmac_f32_16x16x64_bf16 v[48:51], v[28:31], a[224:231], v68\n\t" \
  "v_add_f32_dpp v38, v42, v38 quad_perm:[2,3,0,1] row_mask:0xf bank_mask:0xf\n\t" \
  "v_smfmac_f32_16x16x64_bf16 v[48:51], v[32:35], a[232:239], v68\n\t" \
  "v_add_f32_dpp v39, v43, v39 quad_perm:[2,3,0,1] row_mask:0xf bank_mask:0xf\n\t" \
  CH_DP4 \
  "v_smfmac_f32_16x16x64_bf16 v[52:55], v[0:3], a[240:247], v68\n\t" \
  "s_mov_b64 exec, s[58:59]\n\t" \
  "ds_write_b128 v69, v[36:39] offset:3104\n\t" \
  "s_mov_b64 exec, -1\n\t" \
  "v_mov_b64 v[58:59], 0\n\t" \
  "v_smfmac_f32_16x16x64_bf16 v[52:55], v[4:7], a[248:255], v68\n\t" \
  "buffer_load_dword off, s[28:31], s87 sc1 lds\n\t" \
  "v_add_f32 v75, v48, v50\n\t" \
  "v_add_f32 v49, v49, v51\n\t" \
  "v_cvt_pk_bf16_f32 v75, v75, v49\n\t" \
  "v_smfmac_f32_16x16x64_bf16 v[56:59], v[0:3], v[136:143], v68\n\t" \
  "ds_write2st64_b32 v71, v74, v75 offset0:4 offset1:6\n\t" \
  "v_smfmac_f32_16x16x64_bf16 v[56:59], v[4:7], v[144:151], v68\n\t" \
  "ds_read_b64_tr_b16 v[0:1], v72 offset:9216\n\t" \
  "ds_read_b64_tr_b16 v[2:3], v72 offset:9728\n\t" \
  "v_smfmac_f32_16x16x64_bf16 v[52:55], v[8:11], v[80:87], v68\n\t" \
  "ds_read_b128 v[36:39], v70 offset:12288\n\t" \
  "v_smfmac_f32_16x16x64_bf16 v[56:59], v[8:11], v[152:159], v68\n\t" \
  "ds_read_b128 v[40:43], v70 offset:13312\n\t" \
  "v_smfmac_f32_16x16x64_bf16 v[52:55], v[12:15], v[88:95], v68\n\t" \
  "s_waitcnt lgkmcnt(4)\n\t" \
  "s_barrier\n\t" \
  CH_DP5 \
  "ds_read_b64_tr_b16 v[44:45], v72 offset:1024\n\t" \
  "ds_read_b64_tr_b16 v[46:47], v72 offset:1536\n\t" \
  "v_smfmac_f32_16x16x64_bf16 v[56:59], v[12:15], v[160:167], v68\n\t" \
  "ds_read_b64_tr_b16 v[48:49], v72 offset:0\n\t" \
  "ds_read_b64_tr_b16 v[50:51], v72 offset:512\n\t" \
  "v_smfmac_f32_16x16x64_bf16 v[52:55], v[16:19], v[96:103], v68\n\t" \
  "ds_read_b128 v[4:7], v69 offset:2048\n\t" \
  "s_waitcnt lgkmcnt(5)\n\t" \
  "v_smfmac_f32_16x16x64_bf16 v[56:59], v[16:19], v[36:43], v68\n\t" \
  "ds_read_b128 v[8:11], v69 offset:3072\n\t" \
  "s_waitcnt lgkmcnt(4)\n\t" \
  "v_mfma_f32_16x16x32_bf16 v[12:15], v[44:47], v[76:79], 0\n\t" \
  "ds_read_b128 v[36:39], v70 offset:14336\n\t" \
  "s_waitcnt lgkmcnt(3)\n\t" \
  "v_mfma_f32_16x16x32_bf16 v[12:15], v[48:51], v[0:3], v[12:15]\n\t" \
  "ds_read_b128 v[40:43], v70 offset:15360\n\t" \
  "v_smfmac_f32_16x16x64_bf16 v[52:55], v[20:23], v[104:111], v68\n\t" \
  "s_waitcnt lgkmcnt(2)\n\t" \
  "v_add_f32 v4, v4, v8\n\t" \
  "v_add_f32 v5, v5, v9\n\t" \
  "v_add_f32 v6, v6, v10\n\t" \
  CH_DP6 \
  "v_cndmask_b32 v78, v13, v14, s[56:57]\n\t" \
  "v_cndmask_b32 v76, v12, v15, s[56:57]\n\t" \
  "v_add_f32 v7, v7, v11\n\t" \
  "v_max3_f32 v77, v4, v5, v6\n\t" \
  "v_add_f32_dpp v76, v78, v76 quad_perm:[2,3,0,1] row_mask:0xf bank_mask:0xf\n\t" \
  "v_max3_f32 v77, v77, v7, v76\n\t" \
  "v_smfmac_f32_16x16x64_bf16 v[56:59], v[20:23], v[168:175], v68\n\t" \
  "ds_read_b128 v[44:47], v70 offset:16384\n\t" \
  "v_max_f32_dpp v77, v77, v77 quad_perm:[2,3,0,1] row_mask:0xf bank_mask:0xf\n\t" \
  "v_mov_b32 v78, v77\n\t" \
  "v_smfmac_f32_16x16x64_bf16 v[52:55], v[24:27], v[112:119], v68\n\t" \
  "ds_read_b128 v[48:51], v70 offset:17408\n\t" \
  "v_permlane16_swap_b32 v77, v78\n\t" \
  "v_max_f32 v77, v77, v78\n\t" \
  "v_mov_b32 v78, v77\n\t" \
  "v_smfmac_f32_16x16x64_bf16 v[52:55], v[28:31], v[120:127], v68\n\t" \
  "ds_read_b128 v[16:19], v70 offset:18432\n\t" \
  "v_permlane32_swap_b32 v77, v78\n\t" \
  "v_max_f32 v77, v77, v78\n\t" \
  "v_smfmac_f32_16x16x64_bf16 v[52:55], v[32:35], v[128:135], v68\n\t" \
  "ds_read_b128 v[20:23], v70 offset:19456\n\t" \
  "v_mul_f32 v77, 0xbe0293ee, v77\n\t" \
  CH_DP7 \
  "v_smfmac_f32_16x16x64_bf16 v[56:59], v[24:27], v[176:183], v68\n\t" \
  "v_fmamk_f32 v4, v4, 0x3e0293ee, v77\n\t" \
  "v_smfmac_f32_16x16x64_bf16 v[56:59], v[28:31], v[184:191], v68\n\t" \
  "ds_read_b128 v[24:27], v70 offset:22528\n\t" \
  "v_fmamk_f32 v5, v5, 0x3e0293ee, v77\n\t" \
  "s_waitcnt lgkmcnt(5)\n\t" \
  "v_smfmac_f32_16x16x64_bf16 v[56:59], v[32:35], v[36:43], v68\n\t" \
  "ds_read_b128 v[28:31], v70 offset:23552\n\t" \
  "s_memrealtime s[66:67]\n\t" \
  "v_fmamk_f32 v6, v6, 0x3e0293ee, v77\n\t" \
  "v_fmamk_f32 v7, v7, 0x3e0293ee, v77\n\t" \
  "v_fmamk_f32 v76, v76, 0x3e0293ee, v77\n\t" \
  "v_exp_f32 v4, v4\n\t" \
  "v_exp_f32 v5, v5\n\t" \
  "v_exp_f32 v6, v6\n\t" \
  "v_exp_f32 v7, v7\n\t" \
  "v_cvt_pk_bf16_f32 v4, v4, v5\n\t" \
  "v_exp_f32 v76, v76\n\t" \
  "s_waitcnt lgkmcnt(0)\n\t" \
  "v_cvt_pk_bf16_f32 v5, v6, v7\n\t" \
  "v_mov_b32_dpp v12, v4 quad_perm:[0,1,0,1] row_mask:0xf bank_mask:0xf\n\t" \
  "v_mov_b32_dpp v14, v4 quad_perm:[2,3,2,3] row_mask:0xf bank_mask:0xf\n\t" \
  "v_mov_b32_dpp v13, v5 quad_perm:[0,1,0,1] row_mask:0xf bank_mask:0xf\n\t" \
  "v_mov_b32_dpp v15, v5 quad_perm:[2,3,2,3] row_mask:0xf bank_mask:0xf\n\t" \
  "v_add_f32 v52, v52, v54\n\t" \
  "v_add_f32 v53, v53, v55\n\t" \
  "s_waitcnt vmcnt(2)\n\t" \
  CH_DP8 \
  "v_mfma_f32_16x16x32_bf16 v[0:3], v[60:63], v[12:15], 0\n\t" \
  "s_lshl_b32 s60, s66, 8\n\t" \
  "s_sub_u32 s60, s47, s60\n\t" \
  "v_mov_b32 v72, s60\n\t" \
  "ds_read_b128 v[36:39], v70 offset:20480\n\t" \
  "v_add_f32 v56, v56, v58\n\t" \
  "v_add_f32 v57, v57, v59\n\t" \
  "v_cvt_pk_bf16_f32 v52, v52, v53\n\t" \
  "v_cvt_pk_bf16_f32 v53, v56, v57\n\t" \
  "s_waitcnt vmcnt(1)\n\t" \
  "v_mfma_f32_16x16x32_bf16 v[4:7], v[64:67], v[12:15], 0\n\t" \
  "s_mov_b64 exec, 15\n\t" \
  "buffer_atomic_umin v72, v73, s[28:31], s71 offen sc1\n\t" \
  "s_mov_b64 exec, -1\n\t" \
  "ds_read_b128 v[40:43], v70 offset:21504\n\t" \
  "v_mov_b64 v[56:57], s[88:89]\n\t" \
  "v_mov_b64 v[58:59], s[88:89]\n\t" \
  "v_mov_b32_dpp v78, v76 quad_perm:[0,1,0,1] row_mask:0xf bank_mask:0xf\n\t" \
  "v_mov_b32_dpp v79, v76 quad_perm:[2,3,2,3] row_mask:0xf bank_mask:0xf\n\t" \
  "v_mfma_f32_16x16x32_bf16 v[32:35], v[56:59], v[12:15], 0\n\t" \
  "ds_read_b128 v[60:63], v70 offset:24576\n\t" \
  "v_cvt_pk_bf16_f32 v11, v78, v79\n\t" \
  "v_cvt_pk_bf16_f32 v8, v78, v79\n\t" \
  "v_mov_b64 v[54:55], 0\n\t" \
  "v_mov_b32 v9, 0\n\t" \
  "v_mov_b32 v10, 0\n\t" \
  "v_mov_b64 v[14:15], 0\n\t" \
  "s_cmp_eq_u32 s48, 63\n\t" \
  "v_mfma_f32_16x16x32_bf16 v[32:35], v[54:57], v[8:11], v[32:35]\n\t" \
  "v_mfma_f32_16x16x32_bf16 v[0:3], v[52:55], v[8:11], v[0:3]\n\t" \
  "buffer_store_dwordx2 v[52:53], v71, s[28:31], s69 offen offset:3580 sc1\n\t" \
  "ds_read_b128 v[64:67], v70 offset:25600\n\t" \
  "v_mov_b64 v[56:57], 0\n\t" \
  "v_mov_b64 v[58:59], 0\n\t" \
  "v_mfma_f32_16x16x32_bf16 v[4:7], v[52:55], v[10:13], v[4:7]\n\t" \
  "s_cbranch_scc1 LLAST3_%=\n\t" \
  "v_rcp_f32 v74, v32\n\t" \
  "v_mov_b64 v[12:13], 0\n\t" \
  "v_mov_b32 v8, 0\n\t" \
  "v_mul_f32 v0, v0, v74\n\t" \
  "v_mul_f32 v1, v1, v74\n\t" \
  "v_mul_f32 v2, v2, v74\n\t" \
  "v_mul_f32 v3, v3, v74\n\t" \
  "v_cvt_pk_bf16_f32 v76, v0, v1\n\t" \
  "v_cvt_pk_bf16_f32 v77, v2, v3\n\t" \
  "v_pk_mul_f32 v[4:5], v[4:5], v[74:75] op_sel_hi:[1,0]\n\t" \
  "v_pk_mul_f32 v[6:7], v[6:7], v[74:75] op_sel_hi:[1,0]\n\t" \
  "v_cvt_pk_bf16_f32 v78, v4, v5\n\t" \
  "v_cvt_pk_bf16_f32 v79, v6, v7\n\t" \
  "ds_write_b128 v69, v[76:79] offset:7168\n\t" \
  CH_DP9 \
  "s_nop 0\n\t" \
  "v_smfmac_f32_16x16x64_bf16 v[12:15], v[76:79], v[44:51], v68\n\t" \
  "ds_read_b128 v[44:47], v70 offset:26624\n\t" \
  "v_mov_b32 v11, 0\n\t" \
  "v_smfmac_f32_16x16x64_bf16 v[56:59], v[76:79], v[16:23], v68\n\t" \
  "ds_read_b128 v[48:51], v70 offset:27648\n\t" \
  "v_mov_b64 v[52:53], 0\n\t" \
  "s_waitcnt lgkmcnt(5)\n\t" \
  "v_smfmac_f32_16x16x64_bf16 v[8:11], v[76:79], v[36:43], v68\n\t" \
  "ds_read_b128 v[16:19], v70 offset:28672\n\t" \
  "v_and_b32 v73, 32, v69\n\t" \
  "v_smfmac_f32_16x16x64_bf16 v[52:55], v[76:79], v[24:31], v68\n\t" \
  "s_waitcnt lgkmcnt(3)\n\t" \
  "s_barrier\n\t" \
  CH_DP10 \
  "ds_read_b128 v[0:3], v69 offset:6144\n\t" \
  "ds_read_b128 v[4:7], v69 offset:5120\n\t" \
  "v_lshl_add_u32 v73, v73, 5, v69\n\t" \
  "v_lshrrev_b32 v71, 7, v69\n\t" \
  "v_and_b32 v74, 0x70, v69\n\t" \
  "v_lshl_add_u32 v71, v71, 14, v74\n\t" \
  "v_bfe_u32 v72, v69, 2, 7\n\t" \
  "v_and_b32 v74, 0x200, v69\n\t" \
  "s_waitcnt lgkmcnt(1)\n\t" \
  "v_smfmac_f32_16x16x64_bf16 v[8:11], v[0:3], v[60:67], v68\n\t" \
  "ds_read_b128 v[20:23], v70 offset:29696\n\t" \
  "v_mov_b64 v[76:77], 0\n\t" \
  "v_smfmac_f32_16x16x64_bf16 v[12:15], v[0:3], v[200:207], v68\n\t" \
  "ds_read_b128 v[32:35], v69 offset:4096\n\t" \
  "v_mov_b64 v[78:79], 0\n\t" \
  "v_smfmac_f32_16x16x64_bf16 v[52:55], v[0:3], v[44:51], v68\n\t" \
  "ds_read_b128 v[24:27], v70 offset:30720\n\t" \
  "v_lshl_add_u32 v72, v74, 3, v72\n\t" \
  "v_smfmac_f32_16x16x64_bf16 v[56:59], v[0:3], v[232:239], v68\n\t" \
  "ds_read_b128 v[28:31], v70 offset:31744\n\t" \
  "s_waitcnt lgkmcnt(4)\n\t" \
  "v_smfmac_f32_16x16x64_bf16 v[12:15], v[4:7], v[208:215], v68\n\t" \
  "ds_read_b128 v[60:63], v70 offset:32768\n\t" \
  "s_waitcnt lgkmcnt(4)\n\t" \
  "v_smfmac_f32_16x16x64_bf16 v[8:11], v[4:7], v[16:23], v68\n\t" \
  "ds_read_b128 v[64:67], v70 offset:33792\n\t" \
  "s_waitcnt lgkmcnt(4)\n\t" \
  "v_smfmac_f32_16x16x64_bf16 v[8:11], v[32:35], v[192:199], v68\n\t" \
  "ds_read_b128 v[36:39], v73 offset:4096\n\t" \
  "v_smfmac_f32_16x16x64_bf16 v[12:15], v[32:35], v[216:223], v68\n\t" \
  "ds_read_b128 v[40:43], v73 offset:6144\n\t" \
  "s_waitcnt lgkmcnt(4)\n\t" \
  "v_smfmac_f32_16x16x64_bf16 v[52:55], v[4:7], v[24:31], v68\n\t" \
  "ds_read_b128 v[44:47], v70 offset:34816\n\t" \
  "v_cvt_pk_bf16_f32 v8, v8, v9\n\t" \
  "v_smfmac_f32_16x16x64_bf16 v[52:55], v[32:35], v[224:231], v68\n\t" \
  "ds_read_b128 v[48:51], v70 offset:35840\n\t" \
  "v_cvt_pk_bf16_f32 v9, v10, v11\n\t" \
  "v_smfmac_f32_16x16x64_bf16 v[56:59], v[4:7], v[240:247], v68\n\t" \
  "v_cvt_pk_bf16_f32 v10, v12, v13\n\t" \
  "s_waitcnt lgkmcnt(4)\n\t" \
  "v_smfmac_f32_16x16x64_bf16 v[56:59], v[32:35], v[60:67], v68\n\t" \
  "buffer_load_dword v75, off, s[28:31], s80 offset:1024 sc1\n\t" \
  "v_cvt_pk_bf16_f32 v11, v14, v15\n\t" \
  "s_waitcnt lgkmcnt(3)\n\t" \
  "v_smfmac_f32_16x16x64_bf16 v[76:79], v[36:39], v[248:255], v68\n\t" \
  "buffer_store_dwordx4 v[8:11], v71, s[28:31], s54 offen sc1\n\t" \
  CH_DP11 \
  "v_cvt_pk_bf16_f32 v52, v52, v53\n\t" \
  "s_waitcnt lgkmcnt(0)\n\t" \
  "v_smfmac_f32_16x16x64_bf16 v[76:79], v[40:43], v[44:51], v68\n\t" \
  "v_cvt_pk_bf16_f32 v53, v54, v55\n\t" \
  "v_cvt_pk_bf16_f32 v54, v56, v57\n\t" \
  "v_cvt_pk_bf16_f32 v55, v58, v59\n\t" \
  "buffer_store_dwordx4 v[52:55], v71, s[28:31], s75 offen sc1\n\t" \
  "s_nop 2\n\t" \
  "v_add_f32 v76, v76, v78\n\t" \
  "v_add_f32 v77, v77, v79\n\t" \
  "v_cvt_pk_bf16_f32 v76, v76, v77\n\t" \
  "buffer_store_dword v76, v72, s[28:31], s55 offen sc1\n\t" \
  CH_DP12 \
  "s_memrealtime s[70:71]\n\t" \
  "s_waitcnt vmcnt(3)\n\t" \
  "v_readfirstlane_b32 s65, v75\n\t" \
  "s_barrier\n\t" \
  "s_waitcnt lgkmcnt(0)\n\t" \
  "s_not_b32 s65, s65\n\t" \
  "s_lshl_b32 s61, s70, 8\n\t" \
  "s_andn2_b32 s64, s65, 0xff\n\t" \
  "s_sub_u32 s64, s64, s61\n\t" \
  "s_ashr_i32 s64, s64, 8\n\t" \
  "s_cmp_eq_u32 s65, 0\n\t" \
  "s_cselect_b32 s64, " CH_DLF ", s64\n\t" \
  "s_min_i32 s64, s64, s34\n\t" \
  "s_max_i32 s64, s64, " CH_DLL1 "\n\t" \
  "s_add_u32 s64, s64, s70\n\t" \
  CH_ST2 \
  "s_add_u32 s55, s41, s53\n\t" \
  "s_mov_b32 s74, 0\n\t" \
  "v_and_b32 v37, 0x30, v69\n\t" \
  "v_add_u32 v37, s52, v37\n\t" \
  "s_mov_b32 exec_lo, 0\n\t" \
  "v_mov_b32 v37, s85\n\t" \
  "s_mov_b32 exec_lo, -1\n\t" \
  "s_cmp_lt_i32 s41, 0\n\t" \
  "s_cbranch_scc1 LIDLE_%=\n\t" \
  "s_movk_i32 s60, 8192\n\t" \
  "RWT3_%=:\n\t" \
  "s_memrealtime s[72:73]\n\t" \
  "s_sub_u32 s60, s60, 1\n\t" \
  "s_cselect_b32 s61, 0, -1\n\t" \
  "s_waitcnt lgkmcnt(0)\n\t" \
  "s_sub_i32 s73, s72, s64\n\t" \
  "s_and_b32 s73, s73, s61\n\t" \
  "s_cmp_lt_i32 s73, 0\n\t" \
  "s_cbranch_scc1 RWT3_%=\n\t" \
  "RRETRY3_%=:\n\t" \
  CH_DP13 \
  "buffer_load_dwordx4 v[0:3], v69, s[28:31], s55 offen offset:0 sc1\n\t" \
  "buffer_load_dwordx4 v[4:7], v69, s[28:31], s55 offen offset:1024 sc1\n\t" \
  "buffer_load_dwordx4 v[8:11], v69, s[28:31], s55 offen offset:2048 sc1\n\t" \
  "buffer_load_dwordx4 v[12:15], v69, s[28:31], s55 offen offset:3072 sc1\n\t" \
  "v_lshrrev_b32 v20, 2, v69\n\t" \
  "v_and_b32 v20, 16, v20\n\t" \
  "v_mov_b32 v21, 0x3f80\n\t" \
  "v_lshlrev_b32 v21, v20, v21\n\t" \
  "v_bfe_u32 v20, v69, 4, 2\n\t" \
  "v_cmp_eq_u32 vcc, 0, v20\n\t" \
  "s_nop 1\n\t" \
  "v_cndmask_b32 v32, 0, v21, vcc\n\t" \
  "v_cmp_eq_u32 vcc, 1, v20\n\t" \
  "s_nop 1\n\t" \
  "v_cndmask_b32 v33, 0, v21, vcc\n\t" \
  "v_cmp_eq_u32 vcc, 2, v20\n\t" \
  "s_nop 1\n\t" \
  "v_cndmask_b32 v34, 0, v21, vcc\n\t" \
  "v_cmp_eq_u32 vcc, 3, v20\n\t" \
  "s_nop 1\n\t" \
  "v_cndmask_b32 v35, 0, v21, vcc\n\t" \
  "v_bfe_u32 v38, v69, 7, 1\n\t" \
  "v_bfe_u32 v20, v69, 9, 1\n\t" \
  "v_lshl_add_u32 v38, v20, 1, v38\n\t" \
  "v_mul_u32_u24 v38, 1152, v38\n\t" \
  "v_and_b32 v20, 0x60, v69\n\t" \
  "v_add_u32 v38, v38, v20\n\t" \
  "v_bfe_u32 v20, v69, 8, 1\n\t" \
  "v_lshl_add_u32 v38, v20, 4, v38\n\t" \
  "v_bfe_u32 v20, v69, 4, 1\n\t" \
  "v_lshl_add_u32 v38, v20, 3, v38\n\t" \
  "s_add_u32 s54, s37, s53\n\t" \
  "v_mov_b32 v44, 0\n\t" \
  "buffer_load_dword v39, v37, s[28:31], 0 offen sc1\n\t" \
  CH_DP17 \
  "s_waitcnt vmcnt(4)\n\t" \
  CH_DP14 \
  "v_mfma_f32_16x16x32_bf16 v[40:43], v[32:35], v[0:3], 0\n\t" \
  "v_max3_u32 v44, v44, v0, v1\n\t" \
  "v_max3_u32 v44, v44, v2, v3\n\t" \
  "s_waitcnt vmcnt(3)\n\t" \
  "v_mfma_f32_16x16x32_bf16 v[40:43], v[32:35], v[4:7], v[40:43]\n\t" \
  "v_max3_u32 v44, v44, v4, v5\n\t" \
  "v_max3_u32 v44, v44, v6, v7\n\t" \
  "s_waitcnt vmcnt(2)\n\t" \
  "v_mfma_f32_16x16x32_bf16 v[40:43], v[32:35], v[8:11], v[40:43]\n\t" \
  "v_max3_u32 v44, v44, v8, v9\n\t" \
  "v_max3_u32 v44, v44, v10, v11\n\t" \
  "s_waitcnt vmcnt(1)\n\t" \
  CH_DP15 \
  "v_mfma_f32_16x16x32_bf16 v[40:43], v[32:35], v[12:15], v[40:43]\n\t" \
  "v_max3_u32 v44, v44, v12, v13\n\t" \
  "v_max3_u32 v44, v44, v14, v15\n\t" \
  "v_cmp_eq_u32 vcc, -1, v44\n\t" \
  "s_cbranch_vccnz RFAIL3_%=\n\t" \
  "s_nop 3\n\t" \
  "v_add_f32_dpp v40, v40, v40 row_ror:8 row_mask:0xf bank_mask:0xf\n\t" \
  "v_add_f32_dpp v41, v41, v41 row_ror:8 row_mask:0xf bank_mask:0xf\n\t" \
  "v_add_f32_dpp v42, v42, v42 row_ror:8 row_mask:0xf bank_mask:0xf\n\t" \
  "v_add_f32_dpp v43, v43, v43 row_ror:8 row_mask:0xf bank_mask:0xf\n\t" \
  "v_cvt_pk_bf16_f32 v40, v40, v41\n\t" \
  "v_cvt_pk_bf16_f32 v41, v42, v43\n\t" \
  "buffer_store_dwordx2 v[40:41], v38, s[28:31], s54 offen sc1\n\t" \
  "RDONE3_%=:\n\t" \
  CH_DP16 \
  CH_ST4 \
  "s_barrier\n\t" \
  CH_ST3 \
  "s_waitcnt vmcnt(1)\n\t" \
  "v_cmp_eq_u32 vcc, 0, v39\n\t" \
  "s_sub_u32 s60, s70, s66\n\t" \
  "s_add_u32 s81, s81, 1\n\t" \
  "s_min_u32 s81, s81, s60\n\t" \
  CH_TRC \
  "s_add_u32 s48, s48, 1\n\t" \
  "s_mov_b32 s49, s53\n\t" \
  "s_add_u32 s53, s53, " CH_SLOT "\n\t" \
  "s_add_u32 s60, s53, " CH_SKIP "\n\t" \
  "s_lshr_b32 s61, s96, 1\n\t" \
  "s_add_u32 s86, s86, 1\n\t" \
  "s_cmp_eq_u32 s86, 3\n\t" \
  "s_cselect_b32 s53, s60, s53\n\t" \
  "s_cselect_b32 s96, s61, s96\n\t" \
  "s_cselect_b32 s86, 0, s86\n\t" \
  "s_andn2_b32 s53, s53, 0x2000\n\t" \
  "s_and_b32 s60, s96, 1\n\t" \
  "s_lshl_b32 s60, s60, 13\n\t" \
  "s_or_b32 s53, s53, s60\n\t" \
  "s_mov_b32 s68, 0\n\t" \
  "s_add_u32 s82, s48, 0\n\t" \
  CH_SEGC \
  "s_lshr_b64 s[60:61], s[90:91], s82\n\t" \
  "s_and_b32 s60, s60, 1\n\t" \
  "s_lshl_b32 s60, s60, 13\n\t" \
  "s_add_u32 s82, s82, " CH_SG0 "\n\t" \
  "s_lshl_b32 s82, s82, 21\n\t" \
  "s_or_b32 s82, s82, s60\n\t" \
  "s_add_u32 s50, s82, s38\n\t" \
  "s_add_u32 s69, s82, s39\n\t" \
  "s_add_u32 s82, s48, 1\n\t" \
  CH_SEGC \
  "s_lshr_b64 s[60:61], s[90:91], s82\n\t" \
  "s_and_b32 s60, s60, 1\n\t" \
  "s_lshl_b32 s60, s60, 13\n\t" \
  "s_add_u32 s82, s82, " CH_SG0 "\n\t" \
  "s_lshl_b32 s82, s82, 21\n\t" \
  "s_or_b32 s82, s82, s60\n\t" \
  "s_add_u32 s52, s82, s43\n\t" \
  "s_add_u32 s82, s48, " CH_ARMD "\n\t" \
  CH_SEGC \
  "s_lshr_b64 s[60:61], s[90:91], s82\n\t" \
  "s_and_b32 s60, s60, 1\n\t" \
  "s_lshl_b32 s60, s60, 13\n\t" \
  "s_add_u32 s82, s82, " CH_SG0 "\n\t" \
  "s_lshl_b32 s82, s82, 21\n\t" \
  "s_or_b32 s82, s82, s60\n\t" \
  "s_add_u32 s85, s82, s45\n\t" \
  "s_cmp_lt_i32 s48, " CH_ARMS "\n\t" \
  "s_cselect_b32 s85, s27, s85\n\t" \
  "s_cmp_eq_u32 s34, 0\n\t" \
  "s_cbranch_scc1 LLOAD_%=\n\t" \
  "LTK_%=:\n\t" \
  CH_TOKCHK \
  "s_mov_b32 s61, 0\n\t" \
  "LTW_%=:\n\t" \
  "buffer_load_dword v1, v37, s[28:31], 0 offen sc1\n\t" \
  "s_waitcnt vmcnt(0)\n\t" \
  "v_cmp_eq_u32 vcc, 0, v1\n\t" \
  "s_cbranch_vccz LG3_%=\n\t" \
  "s_add_u32 s61, s61, 1\n\t" \
  "s_sleep 2\n\t" \
  "s_cmp_gt_u32 s61, s84\n\t" \
  "s_cbranch_scc1 LTG_%=\n\t" \
  "s_and_b32 s60, s61, 1023\n\t" \
  "s_cmp_lg_u32 s60, 0\n\t" \
  "s_cbranch_scc1 LTW_%=\n\t" \
  "buffer_load_dword v46, off, s[28:31], s92 sc1\n\t" \
  "s_waitcnt vmcnt(0)\n\t" \
  "v_readfirstlane_b32 s60, v46\n\t" \
  "s_nop 0\n\t" \
  "s_cmp_eq_u32 s60, 0\n\t" \
  "s_cbranch_scc1 LTW_%=\n\t" \
  "LTG_%=:\n\t" \
  "s_mov_b32 s84, 0\n\t" \
  "s_lshl_b32 s60, s36, 4\n\t" \
  "s_or_b32 s60, s60, s48\n\t" \
  "s_or_b32 s60, s60, 268435456\n\t" \
  "v_mov_b32 v46, s60\n\t" \
  "v_mov_b32 v47, 0\n\t" \
  "s_mov_b64 exec, 1\n\t" \
  "buffer_atomic_cmpswap v[46:47], off, s[28:31], s92 sc1\n\t" \
  "s_mov_b64 exec, -1\n\t" \
  "s_branch LG3_%=\n\t" \
  "LIDLE_%=:\n\t" \
  "s_add_u32 s64, s64, 8\n\t" \
  "s_movk_i32 s60, 8192\n\t" \
  "IWT_%=:\n\t" \
  "s_memrealtime s[72:73]\n\t" \
  "s_sub_u32 s60, s60, 1\n\t" \
  "s_cselect_b32 s61, 0, -1\n\t" \
  "s_waitcnt lgkmcnt(0)\n\t" \
  "s_sub_i32 s73, s72, s64\n\t" \
  "s_and_b32 s73, s73, s61\n\t" \
  "s_cmp_lt_i32 s73, 0\n\t" \
  "s_cbranch_scc1 IWT_%=\n\t" \
  "buffer_load_dword v39, v37, s[28:31], 0 offen sc1\n\t" \
  "buffer_store_dword v37, off, s[28:31], s41\n\t" \
  "s_branch RDONE3_%=\n\t" \
  "RFAIL3_%=:\n\t" \
  "s_add_u32 s74, s74, 1\n\t" \
  "s_sleep " CH_RSL "\n\t" \
  "s_waitcnt vmcnt(0)\n\t" \
  "s_cmp_gt_u32 s74, s84\n\t" \
  "s_cbranch_scc1 RG3_%=\n\t" \
  "s_and_b32 s60, s74, 1023\n\t" \
  "s_cmp_lg_u32 s60, 0\n\t" \
  "s_cbranch_scc1 RRETRY3_%=\n\t" \
  "buffer_load_dword v46, off, s[28:31], s92 sc1\n\t" \
  "s_waitcnt vmcnt(0)\n\t" \
  "v_readfirstlane_b32 s60, v46\n\t" \
  "s_nop 0\n\t" \
  "s_cmp_eq_u32 s60, 0\n\t" \
  "s_cbranch_scc1 RRETRY3_%=\n\t" \
  "RG3_%=:\n\t" \
  "s_mov_b32 s84, 0\n\t" \
  "s_lshl_b32 s60, s36, 4\n\t" \
  "s_or_b32 s60, s60, s48\n\t" \
  "s_or_b32 s60, s60, 536870912\n\t" \
  "v_mov_b32 v46, s60\n\t" \
  "v_mov_b32 v47, 0\n\t" \
  "s_mov_b64 exec, 1\n\t" \
  "buffer_atomic_cmpswap v[46:47], off, s[28:31], s92 sc1\n\t" \
  "s_mov_b64 exec, -1\n\t" \
  "s_branch RDONE3_%=\n\t" \
  "LFAIL3_%=:\n\t" \
  "s_add_u32 s68, s68, 1\n\t" \
  "s_waitcnt lgkmcnt(0)\n\t" \
  "s_cmp_gt_u32 s68, s83\n\t" \
  "s_cbranch_scc1 LGV3_%=\n\t" \
  "s_barrier\n\t" \
  "s_waitcnt vmcnt(8)\n\t" \
  "s_branch LG3_%=\n\t" \
  "LGV3_%=:\n\t" \
  "s_mov_b32 s83, 0\n\t" \
  "s_lshl_b32 s60, s36, 4\n\t" \
  "s_or_b32 s60, s60, s48\n\t" \
  "s_or_b32 s60, s60, 805306368\n\t" \
  "v_mov_b32 v46, s60\n\t" \
  "v_mov_b32 v47, 0\n\t" \
  "s_mov_b64 exec, 1\n\t" \
  "buffer_atomic_cmpswap v[46:47], off, s[28:31], s92 sc1\n\t" \
  "s_mov_b64 exec, -1\n\t" \
  "s_branch LGO3_%=\n\t" \
  "LLAST3_%=:\n\t" \
  "s_waitcnt vmcnt(0) lgkmcnt(0)\n\t" \
  "s_barrier\n\t" \
  "LEND_%=:\n\t" \
  ""
typedef unsigned v32u __attribute__((ext_vector_type(32)));
typedef unsigned v16u __attribute__((ext_vector_type(16)));
#define CH_WCLOB "a0", "a1", "a2", "a3", "a4", "a5", "a6", "a7", "a8", "a9", "a10", "a11", "a12", "a13", "a14", "a15", "a16", "a17", "a18", "a19", "a20", "a21", "a22", "a23", "a24", "a25", "a26", "a27", "a28", "a29", "a30", "a31", "a32", "a33", "a34", "a35", "a36", "a37", "a38", "a39", "a40", "a41", "a42", "a43", "a44", "a45", "a46", "a47", "a48", "a49", "a50", "a51", "a52", "a53", "a54", "a55", "a56", "a57", "a58", "a59", "a60", "a61", "a62", "a63", "a64", "a65", "a66", "a67", "a68", "a69", "a70", "a71", "a72", "a73", "a74", "a75", "a76", "a77", "a78", "a79", "a80", "a81", "a82", "a83", "a84", "a85", "a86", "a87", "a88", "a89", "a90", "a91", "a92", "a93", "a94", "a95", "a96", "a97", "a98", "a99", "a100", "a101", "a102", "a103", "a104", "a105", "a106", "a107", "a108", "a109", "a110", "a111", "a112", "a113", "a114", "a115", "a116", "a117", "a118", "a119", "a120", "a121", "a122", "a123", "a124", "a125", "a126", "a127", "a128", "a129", "a130", "a131", "a132", "a133", "a134", "a135", "a136", "a137", "a138", "a139", "a140", "a141", "a142", "a143", "a144", "a145", "a146", "a147", "a148", "a149", "a150", "a151", "a152", "a153", "a154", "a155", "a156", "a157", "a158", "a159", "a160", "a161", "a162", "a163", "a164", "a165", "a166", "a167", "a168", "a169", "a170", "a171", "a172", "a173", "a174", "a175", "a176", "a177", "a178", "a179", "a180", "a181", "a182", "a183", "a184", "a185", "a186", "a187", "a188", "a189", "a190", "a191", "a192", "a193", "a194", "a195", "a196", "a197", "a198", "a199", "a200", "a201", "a202", "a203", "a204", "a205", "a206", "a207", "a208", "a209", "a210", "a211", "a212", "a213", "a214", "a215", "a216", "a217", "a218", "a219", "a220", "a221", "a222", "a223", "a224", "a225", "a226", "a227", "a228", "a229", "a230", "a231", "a232", "a233", "a234", "a235", "a236", "a237", "a238", "a239", "a240", "a241", "a242", "a243", "a244", "a245", "a246", "a247", "a248", "a249", "a250", "a251", "a252", "a253", "a254", "a255", "v80", "v81", "v82", "v83", "v84", "v85", "v86", "v87", "v88", "v89", "v90", "v91", "v92", "v93", "v94", "v95", "v96", "v97", "v98", "v99", "v100", "v101", "v102", "v103", "v104", "v105", "v106", "v107", "v108", "v109", "v110", "v111", "v112", "v113", "v114", "v115", "v116", "v117", "v118", "v119", "v120", "v121", "v122", "v123", "v124", "v125", "v126", "v127", "v128", "v129", "v130", "v131", "v132", "v133", "v134", "v135", "v136", "v137", "v138", "v139", "v140", "v141", "v142", "v143", "v144", "v145", "v146", "v147", "v148", "v149", "v150", "v151", "v152", "v153", "v154", "v155", "v156", "v157", "v158", "v159", "v160", "v161", "v162", "v163", "v164", "v165", "v166", "v167", "v168", "v169", "v170", "v171", "v172", "v173", "v174", "v175", "v176", "v177", "v178", "v179", "v180", "v181", "v182", "v183", "v184", "v185", "v186", "v187", "v188", "v189", "v190", "v191", "v192", "v193", "v194", "v195", "v196", "v197", "v198", "v199", "v200", "v201", "v202", "v203", "v204", "v205", "v206", "v207", "v208", "v209", "v210", "v211", "v212", "v213", "v214", "v215", "v216", "v217", "v218", "v219", "v220", "v221", "v222", "v223", "v224", "v225", "v226", "v227", "v228", "v229", "v230", "v231", "v232", "v233", "v234", "v235", "v236", "v237", "v238", "v239", "v240", "v241", "v242", "v243", "v244", "v245", "v246", "v247", "v248", "v249", "v250", "v251", "v252", "v253", "v254", "v255"
#define CH_H(b) (8 * ((b) & 3) + ((b) >> 3))
#define CH_L(b) (((b) >> 2) & 1)
__device__ __forceinline__ void start_work(char* smem, int wk);
__device__ __forceinline__ void chain_wg(char* smem) {
  if (CH_L(blockIdx.x)) start_work(smem, CH_H(blockIdx.x));
  const MP p = getp();
  const int b = blockIdx.x, h = CH_H(b), L = CH_L(b);
  const int w = __builtin_amdgcn_readfirstlane(threadIdx.x >> 6);
  unsigned lp16 = (threadIdx.x & 63u) * 16u;
  rsrc_t X = mkr(p.xb, XB_BYTES);
  if ((unsigned)(size_t)(__attribute__((address_space(3))) char*)smem != 0u) return;
  const unsigned wsz = DM * HD * 2u - (unsigned)(h * DH + 32 * w) * 2u;
  const rsrc_t RQ = mkr(p.Wq + h * DH + 32 * w, wsz), RK = mkr(p.Wk + h * DH + 32 * w, wsz), RV = mkr(p.Wv + h * DH + 32 * w, wsz), RO = mkr(p.Wo + (long)h * DH * DM, DH * DM * 2u);
  unsigned xcc; asm volatile("s_getreg_b32 %0, hwreg(HW_REG_XCC_ID)" : "=s"(xcc)); xcc &= 3u;
  unsigned d96 = 0u;
  const unsigned c43 = P_FA * PAIR + (unsigned)h * 128u + 32u;
  const unsigned h128 = (unsigned)h * 128u, uw = (unsigned)w, t = uw >> 1, T = uw & 1u, NONE = 0xF0000000u;
  const unsigned u0 = w == 3 ? (unsigned)h >> 2 : (unsigned)h + (w == 2 ? 32u : 0u);
  const bool idle = w == 3 && (h & 3);
  const unsigned c37 = idle ? NONE : u0 * 128u + (w == 3 ? AGT : AGO);
  const unsigned c41 = idle ? NONE : (w == 3 ? 524288u + (u0 >> 1) * 16384u + (u0 & 1u) * 4096u : (u0 & 7u) * 16384u + ((u0 >> 3) & 1u) * 4096u + (u0 >> 4) * 131072u);
  const unsigned c42 = AGO + xcc * 16384u, c44 = AGT + xcc * 1152u;
  const unsigned c46 = (w == 3 ? (h & 7) == 5 && DL3 > -50 : h == 30) ? AGT + 1024u : NONE;
  const unsigned c38 = (unsigned)(3 * h + 1) * PAIR + T * 4096u + t * 2048u, c39 = (P_KV + (unsigned)(h >> 1)) * PAIR + (unsigned)(h & 1) * 4096u;
  const unsigned c45 = P_FB * PAIR + 4096u + (unsigned)(16 + (h & 15)) * 128u;
  unsigned d48 = 0u, d49 = 0u, d53 = SLOT, d86 = 1u, d65 = rtc();
  unsigned mn = 120u;
  v4u stt;
  asm volatile("s_waitcnt vmcnt(0) lgkmcnt(0)\n\t" CH_ASM "LCHEND:"
               : "+{s48}"(d48), "+{s49}"(d49), "+{s53}"(d53), "+{s65}"(d65), "+{s81}"(mn), "+{s86}"(d86), "+{s96}"(d96), "=&{s[76:79]}"(stt), "+{v69}"(lp16), "+{s[28:31]}"(X)
               : [Wq] "s"(RQ), [Wk] "s"(RK), [Wv] "s"(RV), [Wo] "s"(RO), "{s33}"(HT_OFF + 8192u + 256u + (unsigned)b * 4u), "{s36}"(h128), "{s37}"(c37), "{s40}"(w), "{s41}"(c41), "{s42}"(c42), "{s44}"(c44), "{s46}"(c46), "{s92}"(TMO_OFF), "{s38}"(c38), "{s39}"(c39), "{s43}"(c43), "{s45}"(c45), "{s32}"(L), "{s27}"(RDY_OFF(0) + (unsigned)((h % 9) * 1024 + (b & 7) * 128)), [ctab] "i"(CTAB_OFF), [dl1l] "i"(DL1 - LATE), [armd] "i"(3 - ARMK), [arms] "i"(ARMK - 3),
                 [cap] "i"(SPIN_CAP)
               : "v0", "v1", "v2", "v3", "v4", "v5", "v6", "v7", "v8", "v9", "v10", "v11", "v12", "v13", "v14", "v15", "v16", "v17", "v18", "v19",
                 "v20", "v21", "v22", "v23", "v24", "v25", "v26", "v27", "v28", "v29", "v30", "v31", "v32", "v33", "v34", "v35", "v36", "v37", "v38", "v39",
                 "v40", "v41", "v42", "v43", "v44", "v45", "v46", "v47", "v48", "v49", "v50", "v51", "v52", "v53", "v54", "v55", "v56", "v57", "v58", "v59",
                 "v60", "v61", "v62", "v63", "v64", "v65", "v66", "v67", "v68", "v70", "v71", "v72", "v73", "v74", "v75", "v76", "v77", "v78", "v79",
                 CH_WCLOB, "s90", "s91", "s34", "s35", "s47", "s87", "s50", "s51", "s52", "s69", "s82", "s85", "s54", "s75", "s80", "s55", "s56", "s57", "s58", "s59", "s60", "s61", "s62", "s63", "s64", "s66", "s67", "s68", "s70", "s71", "s72", "s73", "s74", "s88", "s89",
                 "s83", "s84", "s97", "s98", "s99", "s93", "s94", "s95", "m0", "vcc", "scc", "memory");
}
#define XQ 0u
#define XQL 0u
#define XA 0u
#define XAL 0u
#define XRL 0u
#define QLEAD 4
#define TK_QDL
#define TK_ADL
#define PSR 8
#define RDLY 40
#define TK_ASL
#define TK_QSL
#define ROT 1
#define PACE 1
#define TK_HAVE "s_cmp_eq_u32 vcc_lo, 0\n\t"
#define TK_PACE "s_cmp_gt_i32 s42, -60\n\ts_cbranch_scc1 LPACE_%=\n\t"
#define TK_QDL2
#define TK_QNP
#define QLEAD1 2
#define QXW -30
#define QXC QXW
#define TK_QXW "%[qxw]"
#define TK_QXC "%[qxc]"
#define PSG 16
#define TK_PSG CH_S(PSG)
#define PSK 2
#define PSH 2
#define TK_CAP "%[cap]"
#define TK_CTAB "%[ctab]"
#define TK_SG0 CH_S(SG0)
#define TK_PSK CH_S(PSK)
#define TK_PSH CH_S(PSH)
#define TK_PA0
#define TK_PA1
#define TK_PA2
#define TK_PQ0
#define TK_PQ1
#define TK_PQ2
#define TT(s, i)
#define TK_AC(n)
#define TK_ACE
#define TK_AC0 TK_AC(212)
#define TK_AC1 TK_AC(213)
#define TK_AC2 TK_AC(214)
#define TK_AC3 TK_AC(215)
#define TK_AC4 TK_AC(216)
#define TK_AC5 TK_AC(217)
#define TK_AC6 TK_AC(218)
#define TK_AC7 TK_AC(219)
#define TK_AC8 TK_AC(220)
#define AP_ASM \
  "v_lshrrev_b32 v40, 8, v0\n\t" \
  "v_mul_u32_u24 v40, 4608, v40\n\t" \
  "v_and_b32 v41, 0xf0, v0\n\t" \
  "v_lshrrev_b32 v42, 3, v41\n\t" \
  "v_add3_u32 v41, v41, v40, s64\n\t" \
  "v_add3_u32 v42, v42, v40, s65\n\t" \
  "s_mov_b32 s62, 0x05040100\n\t" \
  "s_mov_b32 s63, 0x07060302\n\t" \
  "s_xor_b32 s60, s66, 0\n\t" \
  "s_add_u32 s60, s60, 0\n\t" \
  "s_add_u32 s61, s60, 18432\n\t" \
  "buffer_load_dwordx4 v[48:51], v41, %[W], s60 offen offset:0\n\t" \
  "buffer_load_dwordx4 v[52:55], v41, %[W], s60 offen offset:1152\n\t" \
  "buffer_load_dwordx4 v[56:59], v41, %[W], s60 offen offset:2304\n\t" \
  "buffer_load_dwordx4 v[60:63], v41, %[W], s60 offen offset:3456\n\t" \
  "buffer_load_dwordx4 v[64:67], v41, %[W], s61 offen offset:0\n\t" \
  "buffer_load_dwordx4 v[68:71], v41, %[W], s61 offen offset:1152\n\t" \
  "buffer_load_dwordx4 v[72:75], v41, %[W], s61 offen offset:2304\n\t" \
  "buffer_load_dwordx4 v[76:79], v41, %[W], s61 offen offset:3456\n\t" \
  "s_xor_b32 s60, s66, 0\n\t" \
  "s_add_u32 s60, s60, 36864\n\t" \
  "s_add_u32 s61, s60, 18432\n\t" \
  "buffer_load_dwordx4 v[80:83], v41, %[W], s60 offen offset:0\n\t" \
  "buffer_load_dwordx4 v[84:87], v41, %[W], s60 offen offset:1152\n\t" \
  "buffer_load_dwordx4 v[88:91], v41, %[W], s60 offen offset:2304\n\t" \
  "buffer_load_dwordx4 v[92:95], v41, %[W], s60 offen offset:3456\n\t" \
  "buffer_load_dwordx4 v[96:99], v41, %[W], s61 offen offset:0\n\t" \
  "buffer_load_dwordx4 v[100:103], v41, %[W], s61 offen offset:1152\n\t" \
  "buffer_load_dwordx4 v[104:107], v41, %[W], s61 offen offset:2304\n\t" \
  "buffer_load_dwordx4 v[108:111], v41, %[W], s61 offen offset:3456\n\t" \
  "s_xor_b32 s60, s66, 0\n\t" \
  "s_add_u32 s60, s60, 73728\n\t" \
  "s_add_u32 s61, s60, 18432\n\t" \
  "buffer_load_dwordx4 v[112:115], v41, %[W], s60 offen offset:0\n\t" \
  "buffer_load_dwordx4 v[116:119], v41, %[W], s60 offen offset:1152\n\t" \
  "buffer_load_dwordx4 v[120:123], v41, %[W], s60 offen offset:2304\n\t" \
  "buffer_load_dwordx4 v[124:127], v41, %[W], s60 offen offset:3456\n\t" \
  "buffer_load_dwordx4 v[128:131], v41, %[W], s61 offen offset:0\n\t" \
  "buffer_load_dwordx4 v[132:135], v41, %[W], s61 offen offset:1152\n\t" \
  "buffer_load_dwordx4 v[136:139], v41, %[W], s61 offen offset:2304\n\t" \
  "buffer_load_dwordx4 v[140:143], v41, %[W], s61 offen offset:3456\n\t" \
  "s_xor_b32 s60, s66, 0\n\t" \
  "s_add_u32 s60, s60, 110592\n\t" \
  "s_add_u32 s61, s60, 18432\n\t" \
  "buffer_load_dwordx4 v[144:147], v41, %[W], s60 offen offset:0\n\t" \
  "buffer_load_dwordx4 v[148:151], v41, %[W], s60 offen offset:1152\n\t" \
  "buffer_load_dwordx4 v[152:155], v41, %[W], s60 offen offset:2304\n\t" \
  "buffer_load_dwordx4 v[156:159], v41, %[W], s60 offen offset:3456\n\t" \
  "buffer_load_dwordx4 v[160:163], v41, %[W], s61 offen offset:0\n\t" \
  "buffer_load_dwordx4 v[164:167], v41, %[W], s61 offen offset:1152\n\t" \
  "buffer_load_dwordx4 v[168:171], v41, %[W], s61 offen offset:2304\n\t" \
  "buffer_load_dwordx4 v[172:175], v41, %[W], s61 offen offset:3456\n\t" \
  "s_xor_b32 s60, s66, 147456\n\t" \
  "s_add_u32 s60, s60, 0\n\t" \
  "s_add_u32 s61, s60, 18432\n\t" \
  "buffer_load_dwordx4 v[176:179], v41, %[W], s60 offen offset:0\n\t" \
  "buffer_load_dwordx4 v[180:183], v41, %[W], s60 offen offset:1152\n\t" \
  "buffer_load_dwordx4 v[184:187], v41, %[W], s60 offen offset:2304\n\t" \
  "buffer_load_dwordx4 v[188:191], v41, %[W], s60 offen offset:3456\n\t" \
  "buffer_load_dwordx4 v[192:195], v41, %[W], s61 offen offset:0\n\t" \
  "buffer_load_dwordx4 v[196:199], v41, %[W], s61 offen offset:1152\n\t" \
  "buffer_load_dwordx4 v[200:203], v41, %[W], s61 offen offset:2304\n\t" \
  "buffer_load_dwordx4 v[204:207], v41, %[W], s61 offen offset:3456\n\t" \
  "s_waitcnt vmcnt(38)\n\t" \
  "v_perm_b32 v44, v52, v48, s62\n\t" \
  "s_waitcnt vmcnt(36)\n\t" \
  "v_perm_b32 v45, v60, v56, s62\n\t" \
  "s_waitcnt vmcnt(34)\n\t" \
  "v_perm_b32 v46, v68, v64, s62\n\t" \
  "s_waitcnt vmcnt(32)\n\t" \
  "v_perm_b32 v47, v76, v72, s62\n\t" \
  "v_accvgpr_write_b32 a0, v44\n\t" \
  "v_accvgpr_write_b32 a1, v45\n\t" \
  "v_accvgpr_write_b32 a2, v46\n\t" \
  "v_accvgpr_write_b32 a3, v47\n\t" \
  "v_perm_b32 v44, v52, v48, s63\n\t" \
  "v_perm_b32 v45, v60, v56, s63\n\t" \
  "v_perm_b32 v46, v68, v64, s63\n\t" \
  "v_perm_b32 v47, v76, v72, s63\n\t" \
  "v_accvgpr_write_b32 a4, v44\n\t" \
  "v_accvgpr_write_b32 a5, v45\n\t" \
  "v_accvgpr_write_b32 a6, v46\n\t" \
  "v_accvgpr_write_b32 a7, v47\n\t" \
  "v_perm_b32 v44, v53, v49, s62\n\t" \
  "v_perm_b32 v45, v61, v57, s62\n\t" \
  "v_perm_b32 v46, v69, v65, s62\n\t" \
  "v_perm_b32 v47, v77, v73, s62\n\t" \
  "v_accvgpr_write_b32 a8, v44\n\t" \
  "v_accvgpr_write_b32 a9, v45\n\t" \
  "v_accvgpr_write_b32 a10, v46\n\t" \
  "v_accvgpr_write_b32 a11, v47\n\t" \
  "v_perm_b32 v44, v53, v49, s63\n\t" \
  "v_perm_b32 v45, v61, v57, s63\n\t" \
  "v_perm_b32 v46, v69, v65, s63\n\t" \
  "v_perm_b32 v47, v77, v73, s63\n\t" \
  "v_accvgpr_write_b32 a12, v44\n\t" \
  "v_accvgpr_write_b32 a13, v45\n\t" \
  "v_accvgpr_write_b32 a14, v46\n\t" \
  "v_accvgpr_write_b32 a15, v47\n\t" \
  "v_perm_b32 v44, v54, v50, s62\n\t" \
  "v_perm_b32 v45, v62, v58, s62\n\t" \
  "v_perm_b32 v46, v70, v66, s62\n\t" \
  "v_perm_b32 v47, v78, v74, s62\n\t" \
  "v_accvgpr_write_b32 a16, v44\n\t" \
  "v_accvgpr_write_b32 a17, v45\n\t" \
  "v_accvgpr_write_b32 a18, v46\n\t" \
  "v_accvgpr_write_b32 a19, v47\n\t" \
  "v_perm_b32 v44, v54, v50, s63\n\t" \
  "v_perm_b32 v45, v62, v58, s63\n\t" \
  "v_perm_b32 v46, v70, v66, s63\n\t" \
  "v_perm_b32 v47, v78, v74, s63\n\t" \
  "v_accvgpr_write_b32 a20, v44\n\t" \
  "v_accvgpr_write_b32 a21, v45\n\t" \
  "v_accvgpr_write_b32 a22, v46\n\t" \
  "v_accvgpr_write_b32 a23, v47\n\t" \
  "v_perm_b32 v44, v55, v51, s62\n\t" \
  "v_perm_b32 v45, v63, v59, s62\n\t" \
  "v_perm_b32 v46, v71, v67, s62\n\t" \
  "v_perm_b32 v47, v79, v75, s62\n\t" \
  "v_accvgpr_write_b32 a24, v44\n\t" \
  "v_accvgpr_write_b32 a25, v45\n\t" \
  "v_accvgpr_write_b32 a26, v46\n\t" \
  "v_accvgpr_write_b32 a27, v47\n\t" \
  "v_perm_b32 v44, v55, v51, s63\n\t" \
  "v_perm_b32 v45, v63, v59, s63\n\t" \
  "v_perm_b32 v46, v71, v67, s63\n\t" \
  "v_perm_b32 v47, v79, v75, s63\n\t" \
  "v_accvgpr_write_b32 a28, v44\n\t" \
  "v_accvgpr_write_b32 a29, v45\n\t" \
  "v_accvgpr_write_b32 a30, v46\n\t" \
  "v_accvgpr_write_b32 a31, v47\n\t" \
  "s_xor_b32 s60, s66, 147456\n\t" \
  "s_add_u32 s60, s60, 36864\n\t" \
  "s_add_u32 s61, s60, 18432\n\t" \
  "buffer_load_dwordx4 v[48:51], v41, %[W], s60 offen offset:0\n\t" \
  "buffer_load_dwordx4 v[52:55], v41, %[W], s60 offen offset:1152\n\t" \
  "buffer_load_dwordx4 v[56:59], v41, %[W], s60 offen offset:2304\n\t" \
  "buffer_load_dwordx4 v[60:63], v41, %[W], s60 offen offset:3456\n\t" \
  "buffer_load_dwordx4 v[64:67], v41, %[W], s61 offen offset:0\n\t" \
  "buffer_load_dwordx4 v[68:71], v41, %[W], s61 offen offset:1152\n\t" \
  "buffer_load_dwordx4 v[72:75], v41, %[W], s61 offen offset:2304\n\t" \
  "buffer_load_dwordx4 v[76:79], v41, %[W], s61 offen offset:3456\n\t" \
  "s_waitcnt vmcnt(38)\n\t" \
  "v_perm_b32 v44, v84, v80, s62\n\t" \
  "s_waitcnt vmcnt(36)\n\t" \
  "v_perm_b32 v45, v92, v88, s62\n\t" \
  "s_waitcnt vmcnt(34)\n\t" \
  "v_perm_b32 v46, v100, v96, s62\n\t" \
  "s_waitcnt vmcnt(32)\n\t" \
  "v_perm_b32 v47, v108, v104, s62\n\t" \
  "v_accvgpr_write_b32 a32, v44\n\t" \
  "v_accvgpr_write_b32 a33, v45\n\t" \
  "v_accvgpr_write_b32 a34, v46\n\t" \
  "v_accvgpr_write_b32 a35, v47\n\t" \
  "v_perm_b32 v44, v84, v80, s63\n\t" \
  "v_perm_b32 v45, v92, v88, s63\n\t" \
  "v_perm_b32 v46, v100, v96, s63\n\t" \
  "v_perm_b32 v47, v108, v104, s63\n\t" \
  "v_accvgpr_write_b32 a36, v44\n\t" \
  "v_accvgpr_write_b32 a37, v45\n\t" \
  "v_accvgpr_write_b32 a38, v46\n\t" \
  "v_accvgpr_write_b32 a39, v47\n\t" \
  "v_perm_b32 v44, v85, v81, s62\n\t" \
  "v_perm_b32 v45, v93, v89, s62\n\t" \
  "v_perm_b32 v46, v101, v97, s62\n\t" \
  "v_perm_b32 v47, v109, v105, s62\n\t" \
  "v_accvgpr_write_b32 a40, v44\n\t" \
  "v_accvgpr_write_b32 a41, v45\n\t" \
  "v_accvgpr_write_b32 a42, v46\n\t" \
  "v_accvgpr_write_b32 a43, v47\n\t" \
  "v_perm_b32 v44, v85, v81, s63\n\t" \
  "v_perm_b32 v45, v93, v89, s63\n\t" \
  "v_perm_b32 v46, v101, v97, s63\n\t" \
  "v_perm_b32 v47, v109, v105, s63\n\t" \
  "v_accvgpr_write_b32 a44, v44\n\t" \
  "v_accvgpr_write_b32 a45, v45\n\t" \
  "v_accvgpr_write_b32 a46, v46\n\t" \
  "v_accvgpr_write_b32 a47, v47\n\t" \
  "v_perm_b32 v44, v86, v82, s62\n\t" \
  "v_perm_b32 v45, v94, v90, s62\n\t" \
  "v_perm_b32 v46, v102, v98, s62\n\t" \
  "v_perm_b32 v47, v110, v106, s62\n\t" \
  "v_accvgpr_write_b32 a48, v44\n\t" \
  "v_accvgpr_write_b32 a49, v45\n\t" \
  "v_accvgpr_write_b32 a50, v46\n\t" \
  "v_accvgpr_write_b32 a51, v47\n\t" \
  "v_perm_b32 v44, v86, v82, s63\n\t" \
  "v_perm_b32 v45, v94, v90, s63\n\t" \
  "v_perm_b32 v46, v102, v98, s63\n\t" \
  "v_perm_b32 v47, v110, v106, s63\n\t" \
  "v_accvgpr_write_b32 a52, v44\n\t" \
  "v_accvgpr_write_b32 a53, v45\n\t" \
  "v_accvgpr_write_b32 a54, v46\n\t" \
  "v_accvgpr_write_b32 a55, v47\n\t" \
  "v_perm_b32 v44, v87, v83, s62\n\t" \
  "v_perm_b32 v45, v95, v91, s62\n\t" \
  "v_perm_b32 v46, v103, v99, s62\n\t" \
  "v_perm_b32 v47, v111, v107, s62\n\t" \
  "v_accvgpr_write_b32 a56, v44\n\t" \
  "v_accvgpr_write_b32 a57, v45\n\t" \
  "v_accvgpr_write_b32 a58, v46\n\t" \
  "v_accvgpr_write_b32 a59, v47\n\t" \
  "v_perm_b32 v44, v87, v83, s63\n\t" \
  "v_perm_b32 v45, v95, v91, s63\n\t" \
  "v_perm_b32 v46, v103, v99, s63\n\t" \
  "v_perm_b32 v47, v111, v107, s63\n\t" \
  "v_accvgpr_write_b32 a60, v44\n\t" \
  "v_accvgpr_write_b32 a61, v45\n\t" \
  "v_accvgpr_write_b32 a62, v46\n\t" \
  "v_accvgpr_write_b32 a63, v47\n\t" \
  "s_xor_b32 s60, s66, 147456\n\t" \
  "s_add_u32 s60, s60, 73728\n\t" \
  "s_add_u32 s61, s60, 18432\n\t" \
  "buffer_load_dwordx4 v[80:83], v41, %[W], s60 offen offset:0\n\t" \
  "buffer_load_dwordx4 v[84:87], v41, %[W], s60 offen offset:1152\n\t" \
  "buffer_load_dwordx4 v[88:91], v41, %[W], s60 offen offset:2304\n\t" \
  "buffer_load_dwordx4 v[92:95], v41, %[W], s60 offen offset:3456\n\t" \
  "buffer_load_dwordx4 v[96:99], v41, %[W], s61 offen offset:0\n\t" \
  "buffer_load_dwordx4 v[100:103], v41, %[W], s61 offen offset:1152\n\t" \
  "buffer_load_dwordx4 v[104:107], v41, %[W], s61 offen offset:2304\n\t" \
  "buffer_load_dwordx4 v[108:111], v41, %[W], s61 offen offset:3456\n\t" \
  "s_waitcnt vmcnt(38)\n\t" \
  "v_perm_b32 v44, v116, v112, s62\n\t" \
  "s_waitcnt vmcnt(36)\n\t" \
  "v_perm_b32 v45, v124, v120, s62\n\t" \
  "s_waitcnt vmcnt(34)\n\t" \
  "v_perm_b32 v46, v132, v128, s62\n\t" \
  "s_waitcnt vmcnt(32)\n\t" \
  "v_perm_b32 v47, v140, v136, s62\n\t" \
  "v_accvgpr_write_b32 a64, v44\n\t" \
  "v_accvgpr_write_b32 a65, v45\n\t" \
  "v_accvgpr_write_b32 a66, v46\n\t" \
  "v_accvgpr_write_b32 a67, v47\n\t" \
  "v_perm_b32 v44, v116, v112, s63\n\t" \
  "v_perm_b32 v45, v124, v120, s63\n\t" \
  "v_perm_b32 v46, v132, v128, s63\n\t" \
  "v_perm_b32 v47, v140, v136, s63\n\t" \
  "v_accvgpr_write_b32 a68, v44\n\t" \
  "v_accvgpr_write_b32 a69, v45\n\t" \
  "v_accvgpr_write_b32 a70, v46\n\t" \
  "v_accvgpr_write_b32 a71, v47\n\t" \
  "v_perm_b32 v44, v117, v113, s62\n\t" \
  "v_perm_b32 v45, v125, v121, s62\n\t" \
  "v_perm_b32 v46, v133, v129, s62\n\t" \
  "v_perm_b32 v47, v141, v137, s62\n\t" \
  "v_accvgpr_write_b32 a72, v44\n\t" \
  "v_accvgpr_write_b32 a73, v45\n\t" \
  "v_accvgpr_write_b32 a74, v46\n\t" \
  "v_accvgpr_write_b32 a75, v47\n\t" \
  "v_perm_b32 v44, v117, v113, s63\n\t" \
  "v_perm_b32 v45, v125, v121, s63\n\t" \
  "v_perm_b32 v46, v133, v129, s63\n\t" \
  "v_perm_b32 v47, v141, v137, s63\n\t" \
  "v_accvgpr_write_b32 a76, v44\n\t" \
  "v_accvgpr_write_b32 a77, v45\n\t" \
  "v_accvgpr_write_b32 a78, v46\n\t" \
  "v_accvgpr_write_b32 a79, v47\n\t" \
  "v_perm_b32 v44, v118, v114, s62\n\t" \
  "v_perm_b32 v45, v126, v122, s62\n\t" \
  "v_perm_b32 v46, v134, v130, s62\n\t" \
  "v_perm_b32 v47, v142, v138, s62\n\t" \
  "v_accvgpr_write_b32 a80, v44\n\t" \
  "v_accvgpr_write_b32 a81, v45\n\t" \
  "v_accvgpr_write_b32 a82, v46\n\t" \
  "v_accvgpr_write_b32 a83, v47\n\t" \
  "v_perm_b32 v44, v118, v114, s63\n\t" \
  "v_perm_b32 v45, v126, v122, s63\n\t" \
  "v_perm_b32 v46, v134, v130, s63\n\t" \
  "v_perm_b32 v47, v142, v138, s63\n\t" \
  "v_accvgpr_write_b32 a84, v44\n\t" \
  "v_accvgpr_write_b32 a85, v45\n\t" \
  "v_accvgpr_write_b32 a86, v46\n\t" \
  "v_accvgpr_write_b32 a87, v47\n\t" \
  "v_perm_b32 v44, v119, v115, s62\n\t" \
  "v_perm_b32 v45, v127, v123, s62\n\t" \
  "v_perm_b32 v46, v135, v131, s62\n\t" \
  "v_perm_b32 v47, v143, v139, s62\n\t" \
  "v_accvgpr_write_b32 a88, v44\n\t" \
  "v_accvgpr_write_b32 a89, v45\n\t" \
  "v_accvgpr_write_b32 a90, v46\n\t" \
  "v_accvgpr_write_b32 a91, v47\n\t" \
  "v_perm_b32 v44, v119, v115, s63\n\t" \
  "v_perm_b32 v45, v127, v123, s63\n\t" \
  "v_perm_b32 v46, v135, v131, s63\n\t" \
  "v_perm_b32 v47, v143, v139, s63\n\t" \
  "v_accvgpr_write_b32 a92, v44\n\t" \
  "v_accvgpr_write_b32 a93, v45\n\t" \
  "v_accvgpr_write_b32 a94, v46\n\t" \
  "v_accvgpr_write_b32 a95, v47\n\t" \
  "s_xor_b32 s60, s66, 147456\n\t" \
  "s_add_u32 s60, s60, 110592\n\t" \
  "s_add_u32 s61, s60, 18432\n\t" \
  "buffer_load_dwordx4 v[112:115], v41, %[W], s60 offen offset:0\n\t" \
  "buffer_load_dwordx4 v[116:119], v41, %[W], s60 offen offset:1152\n\t" \
  "buffer_load_dwordx4 v[120:123], v41, %[W], s60 offen offset:2304\n\t" \
  "buffer_load_dwordx4 v[124:127], v41, %[W], s60 offen offset:3456\n\t" \
  "buffer_load_dwordx4 v[128:131], v41, %[W], s61 offen offset:0\n\t" \
  "buffer_load_dwordx4 v[132:135], v41, %[W], s61 offen offset:1152\n\t" \
  "buffer_load_dwordx4 v[136:139], v41, %[W], s61 offen offset:2304\n\t" \
  "buffer_load_dwordx4 v[140:143], v41, %[W], s61 offen offset:3456\n\t" \
  "s_waitcnt vmcnt(38)\n\t" \
  "v_perm_b32 v44, v148, v144, s62\n\t" \
  "s_waitcnt vmcnt(36)\n\t" \
  "v_perm_b32 v45, v156, v152, s62\n\t" \
  "s_waitcnt vmcnt(34)\n\t" \
  "v_perm_b32 v46, v164, v160, s62\n\t" \
  "s_waitcnt vmcnt(32)\n\t" \
  "v_perm_b32 v47, v172, v168, s62\n\t" \
  "v_accvgpr_write_b32 a96, v44\n\t" \
  "v_accvgpr_write_b32 a97, v45\n\t" \
  "v_accvgpr_write_b32 a98, v46\n\t" \
  "v_accvgpr_write_b32 a99, v47\n\t" \
  "v_perm_b32 v44, v148, v144, s63\n\t" \
  "v_perm_b32 v45, v156, v152, s63\n\t" \
  "v_perm_b32 v46, v164, v160, s63\n\t" \
  "v_perm_b32 v47, v172, v168, s63\n\t" \
  "v_accvgpr_write_b32 a100, v44\n\t" \
  "v_accvgpr_write_b32 a101, v45\n\t" \
  "v_accvgpr_write_b32 a102, v46\n\t" \
  "v_accvgpr_write_b32 a103, v47\n\t" \
  "v_perm_b32 v44, v149, v145, s62\n\t" \
  "v_perm_b32 v45, v157, v153, s62\n\t" \
  "v_perm_b32 v46, v165, v161, s62\n\t" \
  "v_perm_b32 v47, v173, v169, s62\n\t" \
  "v_accvgpr_write_b32 a104, v44\n\t" \
  "v_accvgpr_write_b32 a105, v45\n\t" \
  "v_accvgpr_write_b32 a106, v46\n\t" \
  "v_accvgpr_write_b32 a107, v47\n\t" \
  "v_perm_b32 v44, v149, v145, s63\n\t" \
  "v_perm_b32 v45, v157, v153, s63\n\t" \
  "v_perm_b32 v46, v165, v161, s63\n\t" \
  "v_perm_b32 v47, v173, v169, s63\n\t" \
  "v_accvgpr_write_b32 a108, v44\n\t" \
  "v_accvgpr_write_b32 a109, v45\n\t" \
  "v_accvgpr_write_b32 a110, v46\n\t" \
  "v_accvgpr_write_b32 a111, v47\n\t" \
  "v_perm_b32 v44, v150, v146, s62\n\t" \
  "v_perm_b32 v45, v158, v154, s62\n\t" \
  "v_perm_b32 v46, v166, v162, s62\n\t" \
  "v_perm_b32 v47, v174, v170, s62\n\t" \
  "v_accvgpr_write_b32 a112, v44\n\t" \
  "v_accvgpr_write_b32 a113, v45\n\t" \
  "v_accvgpr_write_b32 a114, v46\n\t" \
  "v_accvgpr_write_b32 a115, v47\n\t" \
  "v_perm_b32 v44, v150, v146, s63\n\t" \
  "v_perm_b32 v45, v158, v154, s63\n\t" \
  "v_perm_b32 v46, v166, v162, s63\n\t" \
  "v_perm_b32 v47, v174, v170, s63\n\t" \
  "v_accvgpr_write_b32 a116, v44\n\t" \
  "v_accvgpr_write_b32 a117, v45\n\t" \
  "v_accvgpr_write_b32 a118, v46\n\t" \
  "v_accvgpr_write_b32 a119, v47\n\t" \
  "v_perm_b32 v44, v151, v147, s62\n\t" \
  "v_perm_b32 v45, v159, v155, s62\n\t" \
  "v_perm_b32 v46, v167, v163, s62\n\t" \
  "v_perm_b32 v47, v175, v171, s62\n\t" \
  "v_accvgpr_write_b32 a120, v44\n\t" \
  "v_accvgpr_write_b32 a121, v45\n\t" \
  "v_accvgpr_write_b32 a122, v46\n\t" \
  "v_accvgpr_write_b32 a123, v47\n\t" \
  "v_perm_b32 v44, v151, v147, s63\n\t" \
  "v_perm_b32 v45, v159, v155, s63\n\t" \
  "v_perm_b32 v46, v167, v163, s63\n\t" \
  "v_perm_b32 v47, v175, v171, s63\n\t" \
  "v_accvgpr_write_b32 a124, v44\n\t" \
  "v_accvgpr_write_b32 a125, v45\n\t" \
  "v_accvgpr_write_b32 a126, v46\n\t" \
  "v_accvgpr_write_b32 a127, v47\n\t" \
  "s_xor_b32 s60, s66, 0\n\t" \
  "s_add_u32 s60, s60, 0\n\t" \
  "s_add_u32 s61, s60, 18432\n\t" \
  "buffer_load_ushort v224, v42, %[W], s60 offen offset:0\n\t" \
  "buffer_load_ushort v144, v42, %[W], s60 offen offset:1152\n\t" \
  "buffer_load_ushort v225, v42, %[W], s60 offen offset:2304\n\t" \
  "buffer_load_ushort v145, v42, %[W], s60 offen offset:3456\n\t" \
  "buffer_load_ushort v226, v42, %[W], s61 offen offset:0\n\t" \
  "buffer_load_ushort v146, v42, %[W], s61 offen offset:1152\n\t" \
  "buffer_load_ushort v227, v42, %[W], s61 offen offset:2304\n\t" \
  "buffer_load_ushort v147, v42, %[W], s61 offen offset:3456\n\t" \
  "s_xor_b32 s60, s66, 0\n\t" \
  "s_add_u32 s60, s60, 36864\n\t" \
  "s_add_u32 s61, s60, 18432\n\t" \
  "buffer_load_ushort v228, v42, %[W], s60 offen offset:0\n\t" \
  "buffer_load_ushort v148, v42, %[W], s60 offen offset:1152\n\t" \
  "buffer_load_ushort v229, v42, %[W], s60 offen offset:2304\n\t" \
  "buffer_load_ushort v149, v42, %[W], s60 offen offset:3456\n\t" \
  "buffer_load_ushort v230, v42, %[W], s61 offen offset:0\n\t" \
  "buffer_load_ushort v150, v42, %[W], s61 offen offset:1152\n\t" \
  "buffer_load_ushort v231, v42, %[W], s61 offen offset:2304\n\t" \
  "buffer_load_ushort v151, v42, %[W], s61 offen offset:3456\n\t" \
  "s_waitcnt vmcnt(46)\n\t" \
  "v_perm_b32 v44, v180, v176, s62\n\t" \
  "s_waitcnt vmcnt(44)\n\t" \
  "v_perm_b32 v45, v188, v184, s62\n\t" \
  "s_waitcnt vmcnt(42)\n\t" \
  "v_perm_b32 v46, v196, v192, s62\n\t" \
  "s_waitcnt vmcnt(40)\n\t" \
  "v_perm_b32 v47, v204, v200, s62\n\t" \
  "v_accvgpr_write_b32 a128, v44\n\t" \
  "v_accvgpr_write_b32 a129, v45\n\t" \
  "v_accvgpr_write_b32 a130, v46\n\t" \
  "v_accvgpr_write_b32 a131, v47\n\t" \
  "v_perm_b32 v44, v180, v176, s63\n\t" \
  "v_perm_b32 v45, v188, v184, s63\n\t" \
  "v_perm_b32 v46, v196, v192, s63\n\t" \
  "v_perm_b32 v47, v204, v200, s63\n\t" \
  "v_accvgpr_write_b32 a132, v44\n\t" \
  "v_accvgpr_write_b32 a133, v45\n\t" \
  "v_accvgpr_write_b32 a134, v46\n\t" \
  "v_accvgpr_write_b32 a135, v47\n\t" \
  "v_perm_b32 v44, v181, v177, s62\n\t" \
  "v_perm_b32 v45, v189, v185, s62\n\t" \
  "v_perm_b32 v46, v197, v193, s62\n\t" \
  "v_perm_b32 v47, v205, v201, s62\n\t" \
  "v_accvgpr_write_b32 a136, v44\n\t" \
  "v_accvgpr_write_b32 a137, v45\n\t" \
  "v_accvgpr_write_b32 a138, v46\n\t" \
  "v_accvgpr_write_b32 a139, v47\n\t" \
  "v_perm_b32 v44, v181, v177, s63\n\t" \
  "v_perm_b32 v45, v189, v185, s63\n\t" \
  "v_perm_b32 v46, v197, v193, s63\n\t" \
  "v_perm_b32 v47, v205, v201, s63\n\t" \
  "v_accvgpr_write_b32 a140, v44\n\t" \
  "v_accvgpr_write_b32 a141, v45\n\t" \
  "v_accvgpr_write_b32 a142, v46\n\t" \
  "v_accvgpr_write_b32 a143, v47\n\t" \
  "v_perm_b32 v44, v182, v178, s62\n\t" \
  "v_perm_b32 v45, v190, v186, s62\n\t" \
  "v_perm_b32 v46, v198, v194, s62\n\t" \
  "v_perm_b32 v47, v206, v202, s62\n\t" \
  "v_accvgpr_write_b32 a144, v44\n\t" \
  "v_accvgpr_write_b32 a145, v45\n\t" \
  "v_accvgpr_write_b32 a146, v46\n\t" \
  "v_accvgpr_write_b32 a147, v47\n\t" \
  "v_perm_b32 v44, v182, v178, s63\n\t" \
  "v_perm_b32 v45, v190, v186, s63\n\t" \
  "v_perm_b32 v46, v198, v194, s63\n\t" \
  "v_perm_b32 v47, v206, v202, s63\n\t" \
  "v_accvgpr_write_b32 a148, v44\n\t" \
  "v_accvgpr_write_b32 a149, v45\n\t" \
  "v_accvgpr_write_b32 a150, v46\n\t" \
  "v_accvgpr_write_b32 a151, v47\n\t" \
  "v_perm_b32 v44, v183, v179, s62\n\t" \
  "v_perm_b32 v45, v191, v187, s62\n\t" \
  "v_perm_b32 v46, v199, v195, s62\n\t" \
  "v_perm_b32 v47, v207, v203, s62\n\t" \
  "v_accvgpr_write_b32 a152, v44\n\t" \
  "v_accvgpr_write_b32 a153, v45\n\t" \
  "v_accvgpr_write_b32 a154, v46\n\t" \
  "v_accvgpr_write_b32 a155, v47\n\t" \
  "v_perm_b32 v44, v183, v179, s63\n\t" \
  "v_perm_b32 v45, v191, v187, s63\n\t" \
  "v_perm_b32 v46, v199, v195, s63\n\t" \
  "v_perm_b32 v47, v207, v203, s63\n\t" \
  "v_accvgpr_write_b32 a156, v44\n\t" \
  "v_accvgpr_write_b32 a157, v45\n\t" \
  "v_accvgpr_write_b32 a158, v46\n\t" \
  "v_accvgpr_write_b32 a159, v47\n\t" \
  "s_xor_b32 s60, s66, 0\n\t" \
  "s_add_u32 s60, s60, 73728\n\t" \
  "s_add_u32 s61, s60, 18432\n\t" \
  "buffer_load_ushort v232, v42, %[W], s60 offen offset:0\n\t" \
  "buffer_load_ushort v152, v42, %[W], s60 offen offset:1152\n\t" \
  "buffer_load_ushort v233, v42, %[W], s60 offen offset:2304\n\t" \
  "buffer_load_ushort v153, v42, %[W], s60 offen offset:3456\n\t" \
  "buffer_load_ushort v234, v42, %[W], s61 offen offset:0\n\t" \
  "buffer_load_ushort v154, v42, %[W], s61 offen offset:1152\n\t" \
  "buffer_load_ushort v235, v42, %[W], s61 offen offset:2304\n\t" \
  "buffer_load_ushort v155, v42, %[W], s61 offen offset:3456\n\t" \
  "s_xor_b32 s60, s66, 0\n\t" \
  "s_add_u32 s60, s60, 110592\n\t" \
  "s_add_u32 s61, s60, 18432\n\t" \
  "buffer_load_ushort v236, v42, %[W], s60 offen offset:0\n\t" \
  "buffer_load_ushort v156, v42, %[W], s60 offen offset:1152\n\t" \
  "buffer_load_ushort v237, v42, %[W], s60 offen offset:2304\n\t" \
  "buffer_load_ushort v157, v42, %[W], s60 offen offset:3456\n\t" \
  "buffer_load_ushort v238, v42, %[W], s61 offen offset:0\n\t" \
  "buffer_load_ushort v158, v42, %[W], s61 offen offset:1152\n\t" \
  "buffer_load_ushort v239, v42, %[W], s61 offen offset:2304\n\t" \
  "buffer_load_ushort v159, v42, %[W], s61 offen offset:3456\n\t" \
  "s_waitcnt vmcnt(54)\n\t" \
  "v_perm_b32 v44, v52, v48, s62\n\t" \
  "s_waitcnt vmcnt(52)\n\t" \
  "v_perm_b32 v45, v60, v56, s62\n\t" \
  "s_waitcnt vmcnt(50)\n\t" \
  "v_perm_b32 v46, v68, v64, s62\n\t" \
  "s_waitcnt vmcnt(48)\n\t" \
  "v_perm_b32 v47, v76, v72, s62\n\t" \
  "v_accvgpr_write_b32 a160, v44\n\t" \
  "v_accvgpr_write_b32 a161, v45\n\t" \
  "v_accvgpr_write_b32 a162, v46\n\t" \
  "v_accvgpr_write_b32 a163, v47\n\t" \
  "v_perm_b32 v44, v52, v48, s63\n\t" \
  "v_perm_b32 v45, v60, v56, s63\n\t" \
  "v_perm_b32 v46, v68, v64, s63\n\t" \
  "v_perm_b32 v47, v76, v72, s63\n\t" \
  "v_accvgpr_write_b32 a164, v44\n\t" \
  "v_accvgpr_write_b32 a165, v45\n\t" \
  "v_accvgpr_write_b32 a166, v46\n\t" \
  "v_accvgpr_write_b32 a167, v47\n\t" \
  "v_perm_b32 v44, v53, v49, s62\n\t" \
  "v_perm_b32 v45, v61, v57, s62\n\t" \
  "v_perm_b32 v46, v69, v65, s62\n\t" \
  "v_perm_b32 v47, v77, v73, s62\n\t" \
  "v_accvgpr_write_b32 a168, v44\n\t" \
  "v_accvgpr_write_b32 a169, v45\n\t" \
  "v_accvgpr_write_b32 a170, v46\n\t" \
  "v_accvgpr_write_b32 a171, v47\n\t" \
  "v_perm_b32 v44, v53, v49, s63\n\t" \
  "v_perm_b32 v45, v61, v57, s63\n\t" \
  "v_perm_b32 v46, v69, v65, s63\n\t" \
  "v_perm_b32 v47, v77, v73, s63\n\t" \
  "v_accvgpr_write_b32 a172, v44\n\t" \
  "v_accvgpr_write_b32 a173, v45\n\t" \
  "v_accvgpr_write_b32 a174, v46\n\t" \
  "v_accvgpr_write_b32 a175, v47\n\t" \
  "v_perm_b32 v44, v54, v50, s62\n\t" \
  "v_perm_b32 v45, v62, v58, s62\n\t" \
  "v_perm_b32 v46, v70, v66, s62\n\t" \
  "v_perm_b32 v47, v78, v74, s62\n\t" \
  "v_accvgpr_write_b32 a176, v44\n\t" \
  "v_accvgpr_write_b32 a177, v45\n\t" \
  "v_accvgpr_write_b32 a178, v46\n\t" \
  "v_accvgpr_write_b32 a179, v47\n\t" \
  "v_perm_b32 v44, v54, v50, s63\n\t" \
  "v_perm_b32 v45, v62, v58, s63\n\t" \
  "v_perm_b32 v46, v70, v66, s63\n\t" \
  "v_perm_b32 v47, v78, v74, s63\n\t" \
  "v_accvgpr_write_b32 a180, v44\n\t" \
  "v_accvgpr_write_b32 a181, v45\n\t" \
  "v_accvgpr_write_b32 a182, v46\n\t" \
  "v_accvgpr_write_b32 a183, v47\n\t" \
  "v_perm_b32 v44, v55, v51, s62\n\t" \
  "v_perm_b32 v45, v63, v59, s62\n\t" \
  "v_perm_b32 v46, v71, v67, s62\n\t" \
  "v_perm_b32 v47, v79, v75, s62\n\t" \
  "v_accvgpr_write_b32 a184, v44\n\t" \
  "v_accvgpr_write_b32 a185, v45\n\t" \
  "v_accvgpr_write_b32 a186, v46\n\t" \
  "v_accvgpr_write_b32 a187, v47\n\t" \
  "v_perm_b32 v44, v55, v51, s63\n\t" \
  "v_perm_b32 v45, v63, v59, s63\n\t" \
  "v_perm_b32 v46, v71, v67, s63\n\t" \
  "v_perm_b32 v47, v79, v75, s63\n\t" \
  "v_accvgpr_write_b32 a188, v44\n\t" \
  "v_accvgpr_write_b32 a189, v45\n\t" \
  "v_accvgpr_write_b32 a190, v46\n\t" \
  "v_accvgpr_write_b32 a191, v47\n\t" \
  "s_xor_b32 s60, s66, 147456\n\t" \
  "s_add_u32 s60, s60, 0\n\t" \
  "s_add_u32 s61, s60, 18432\n\t" \
  "buffer_load_ushort v240, v42, %[W], s60 offen offset:0\n\t" \
  "buffer_load_ushort v160, v42, %[W], s60 offen offset:1152\n\t" \
  "buffer_load_ushort v241, v42, %[W], s60 offen offset:2304\n\t" \
  "buffer_load_ushort v161, v42, %[W], s60 offen offset:3456\n\t" \
  "buffer_load_ushort v242, v42, %[W], s61 offen offset:0\n\t" \
  "buffer_load_ushort v162, v42, %[W], s61 offen offset:1152\n\t" \
  "buffer_load_ushort v243, v42, %[W], s61 offen offset:2304\n\t" \
  "buffer_load_ushort v163, v42, %[W], s61 offen offset:3456\n\t" \
  "s_waitcnt vmcnt(54)\n\t" \
  "v_perm_b32 v44, v84, v80, s62\n\t" \
  "s_waitcnt vmcnt(52)\n\t" \
  "v_perm_b32 v45, v92, v88, s62\n\t" \
  "s_waitcnt vmcnt(50)\n\t" \
  "v_perm_b32 v46, v100, v96, s62\n\t" \
  "s_waitcnt vmcnt(48)\n\t" \
  "v_perm_b32 v47, v108, v104, s62\n\t" \
  "v_accvgpr_write_b32 a192, v44\n\t" \
  "v_accvgpr_write_b32 a193, v45\n\t" \
  "v_accvgpr_write_b32 a194, v46\n\t" \
  "v_accvgpr_write_b32 a195, v47\n\t" \
  "v_perm_b32 v44, v84, v80, s63\n\t" \
  "v_perm_b32 v45, v92, v88, s63\n\t" \
  "v_perm_b32 v46, v100, v96, s63\n\t" \
  "v_perm_b32 v47, v108, v104, s63\n\t" \
  "v_accvgpr_write_b32 a196, v44\n\t" \
  "v_accvgpr_write_b32 a197, v45\n\t" \
  "v_accvgpr_write_b32 a198, v46\n\t" \
  "v_accvgpr_write_b32 a199, v47\n\t" \
  "v_perm_b32 v44, v85, v81, s62\n\t" \
  "v_perm_b32 v45, v93, v89, s62\n\t" \
  "v_perm_b32 v46, v101, v97, s62\n\t" \
  "v_perm_b32 v47, v109, v105, s62\n\t" \
  "v_accvgpr_write_b32 a200, v44\n\t" \
  "v_accvgpr_write_b32 a201, v45\n\t" \
  "v_accvgpr_write_b32 a202, v46\n\t" \
  "v_accvgpr_write_b32 a203, v47\n\t" \
  "v_perm_b32 v44, v85, v81, s63\n\t" \
  "v_perm_b32 v45, v93, v89, s63\n\t" \
  "v_perm_b32 v46, v101, v97, s63\n\t" \
  "v_perm_b32 v47, v109, v105, s63\n\t" \
  "v_accvgpr_write_b32 a204, v44\n\t" \
  "v_accvgpr_write_b32 a205, v45\n\t" \
  "v_accvgpr_write_b32 a206, v46\n\t" \
  "v_accvgpr_write_b32 a207, v47\n\t" \
  "v_perm_b32 v44, v86, v82, s62\n\t" \
  "v_perm_b32 v45, v94, v90, s62\n\t" \
  "v_perm_b32 v46, v102, v98, s62\n\t" \
  "v_perm_b32 v47, v110, v106, s62\n\t" \
  "v_accvgpr_write_b32 a208, v44\n\t" \
  "v_accvgpr_write_b32 a209, v45\n\t" \
  "v_accvgpr_write_b32 a210, v46\n\t" \
  "v_accvgpr_write_b32 a211, v47\n\t" \
  "v_perm_b32 v44, v86, v82, s63\n\t" \
  "v_perm_b32 v45, v94, v90, s63\n\t" \
  "v_perm_b32 v46, v102, v98, s63\n\t" \
  "v_perm_b32 v47, v110, v106, s63\n\t" \
  "v_accvgpr_write_b32 a212, v44\n\t" \
  "v_accvgpr_write_b32 a213, v45\n\t" \
  "v_accvgpr_write_b32 a214, v46\n\t" \
  "v_accvgpr_write_b32 a215, v47\n\t" \
  "v_perm_b32 v44, v87, v83, s62\n\t" \
  "v_perm_b32 v45, v95, v91, s62\n\t" \
  "v_perm_b32 v46, v103, v99, s62\n\t" \
  "v_perm_b32 v47, v111, v107, s62\n\t" \
  "v_accvgpr_write_b32 a216, v44\n\t" \
  "v_accvgpr_write_b32 a217, v45\n\t" \
  "v_accvgpr_write_b32 a218, v46\n\t" \
  "v_accvgpr_write_b32 a219, v47\n\t" \
  "v_perm_b32 v44, v87, v83, s63\n\t" \
  "v_perm_b32 v45, v95, v91, s63\n\t" \
  "v_perm_b32 v46, v103, v99, s63\n\t" \
  "v_perm_b32 v47, v111, v107, s63\n\t" \
  "v_accvgpr_write_b32 a220, v44\n\t" \
  "v_accvgpr_write_b32 a221, v45\n\t" \
  "v_accvgpr_write_b32 a222, v46\n\t" \
  "v_accvgpr_write_b32 a223, v47\n\t" \
  "s_xor_b32 s60, s66, 147456\n\t" \
  "s_add_u32 s60, s60, 36864\n\t" \
  "s_add_u32 s61, s60, 18432\n\t" \
  "buffer_load_ushort v244, v42, %[W], s60 offen offset:0\n\t" \
  "buffer_load_ushort v164, v42, %[W], s60 offen offset:1152\n\t" \
  "buffer_load_ushort v245, v42, %[W], s60 offen offset:2304\n\t" \
  "buffer_load_ushort v165, v42, %[W], s60 offen offset:3456\n\t" \
  "buffer_load_ushort v246, v42, %[W], s61 offen offset:0\n\t" \
  "buffer_load_ushort v166, v42, %[W], s61 offen offset:1152\n\t" \
  "buffer_load_ushort v247, v42, %[W], s61 offen offset:2304\n\t" \
  "buffer_load_ushort v167, v42, %[W], s61 offen offset:3456\n\t" \
  "s_waitcnt vmcnt(54)\n\t" \
  "v_perm_b32 v44, v116, v112, s62\n\t" \
  "s_waitcnt vmcnt(52)\n\t" \
  "v_perm_b32 v45, v124, v120, s62\n\t" \
  "s_waitcnt vmcnt(50)\n\t" \
  "v_perm_b32 v46, v132, v128, s62\n\t" \
  "s_waitcnt vmcnt(48)\n\t" \
  "v_perm_b32 v47, v140, v136, s62\n\t" \
  "v_accvgpr_write_b32 a224, v44\n\t" \
  "v_accvgpr_write_b32 a225, v45\n\t" \
  "v_accvgpr_write_b32 a226, v46\n\t" \
  "v_accvgpr_write_b32 a227, v47\n\t" \
  "v_perm_b32 v44, v116, v112, s63\n\t" \
  "v_perm_b32 v45, v124, v120, s63\n\t" \
  "v_perm_b32 v46, v132, v128, s63\n\t" \
  "v_perm_b32 v47, v140, v136, s63\n\t" \
  "v_accvgpr_write_b32 a228, v44\n\t" \
  "v_accvgpr_write_b32 a229, v45\n\t" \
  "v_accvgpr_write_b32 a230, v46\n\t" \
  "v_accvgpr_write_b32 a231, v47\n\t" \
  "v_perm_b32 v44, v117, v113, s62\n\t" \
  "v_perm_b32 v45, v125, v121, s62\n\t" \
  "v_perm_b32 v46, v133, v129, s62\n\t" \
  "v_perm_b32 v47, v141, v137, s62\n\t" \
  "v_accvgpr_write_b32 a232, v44\n\t" \
  "v_accvgpr_write_b32 a233, v45\n\t" \
  "v_accvgpr_write_b32 a234, v46\n\t" \
  "v_accvgpr_write_b32 a235, v47\n\t" \
  "v_perm_b32 v44, v117, v113, s63\n\t" \
  "v_perm_b32 v45, v125, v121, s63\n\t" \
  "v_perm_b32 v46, v133, v129, s63\n\t" \
  "v_perm_b32 v47, v141, v137, s63\n\t" \
  "v_accvgpr_write_b32 a236, v44\n\t" \
  "v_accvgpr_write_b32 a237, v45\n\t" \
  "v_accvgpr_write_b32 a238, v46\n\t" \
  "v_accvgpr_write_b32 a239, v47\n\t" \
  "v_perm_b32 v44, v118, v114, s62\n\t" \
  "v_perm_b32 v45, v126, v122, s62\n\t" \
  "v_perm_b32 v46, v134, v130, s62\n\t" \
  "v_perm_b32 v47, v142, v138, s62\n\t" \
  "v_accvgpr_write_b32 a240, v44\n\t" \
  "v_accvgpr_write_b32 a241, v45\n\t" \
  "v_accvgpr_write_b32 a242, v46\n\t" \
  "v_accvgpr_write_b32 a243, v47\n\t" \
  "v_perm_b32 v44, v118, v114, s63\n\t" \
  "v_perm_b32 v45, v126, v122, s63\n\t" \
  "v_perm_b32 v46, v134, v130, s63\n\t" \
  "v_perm_b32 v47, v142, v138, s63\n\t" \
  "v_accvgpr_write_b32 a244, v44\n\t" \
  "v_accvgpr_write_b32 a245, v45\n\t" \
  "v_accvgpr_write_b32 a246, v46\n\t" \
  "v_accvgpr_write_b32 a247, v47\n\t" \
  "v_perm_b32 v44, v119, v115, s62\n\t" \
  "v_perm_b32 v45, v127, v123, s62\n\t" \
  "v_perm_b32 v46, v135, v131, s62\n\t" \
  "v_perm_b32 v47, v143, v139, s62\n\t" \
  "v_accvgpr_write_b32 a248, v44\n\t" \
  "v_accvgpr_write_b32 a249, v45\n\t" \
  "v_accvgpr_write_b32 a250, v46\n\t" \
  "v_accvgpr_write_b32 a251, v47\n\t" \
  "v_perm_b32 v44, v119, v115, s63\n\t" \
  "v_perm_b32 v45, v127, v123, s63\n\t" \
  "v_perm_b32 v46, v135, v131, s63\n\t" \
  "v_perm_b32 v47, v143, v139, s63\n\t" \
  "v_accvgpr_write_b32 a252, v44\n\t" \
  "v_accvgpr_write_b32 a253, v45\n\t" \
  "v_accvgpr_write_b32 a254, v46\n\t" \
  "v_accvgpr_write_b32 a255, v47\n\t" \
  "s_xor_b32 s60, s66, 147456\n\t" \
  "s_add_u32 s60, s60, 73728\n\t" \
  "s_add_u32 s61, s60, 18432\n\t" \
  "buffer_load_ushort v248, v42, %[W], s60 offen offset:0\n\t" \
  "buffer_load_ushort v168, v42, %[W], s60 offen offset:1152\n\t" \
  "buffer_load_ushort v249, v42, %[W], s60 offen offset:2304\n\t" \
  "buffer_load_ushort v169, v42, %[W], s60 offen offset:3456\n\t" \
  "buffer_load_ushort v250, v42, %[W], s61 offen offset:0\n\t" \
  "buffer_load_ushort v170, v42, %[W], s61 offen offset:1152\n\t" \
  "buffer_load_ushort v251, v42, %[W], s61 offen offset:2304\n\t" \
  "buffer_load_ushort v171, v42, %[W], s61 offen offset:3456\n\t" \
  "s_xor_b32 s60, s66, 147456\n\t" \
  "s_add_u32 s60, s60, 110592\n\t" \
  "s_add_u32 s61, s60, 18432\n\t" \
  "s_waitcnt vmcnt(40)\n\t" \
  "buffer_load_ushort v252, v42, %[W], s60 offen offset:0\n\t" \
  "buffer_load_ushort v172, v42, %[W], s60 offen offset:1152\n\t" \
  "buffer_load_ushort v253, v42, %[W], s60 offen offset:2304\n\t" \
  "buffer_load_ushort v173, v42, %[W], s60 offen offset:3456\n\t" \
  "buffer_load_ushort v254, v42, %[W], s61 offen offset:0\n\t" \
  "buffer_load_ushort v174, v42, %[W], s61 offen offset:1152\n\t" \
  "buffer_load_ushort v255, v42, %[W], s61 offen offset:2304\n\t" \
  "buffer_load_ushort v175, v42, %[W], s61 offen offset:3456\n\t" \
  "v_lshl_or_b32 v224, v144, 16, v224\n\t" \
  "v_lshl_or_b32 v225, v145, 16, v225\n\t" \
  "v_lshl_or_b32 v226, v146, 16, v226\n\t" \
  "v_lshl_or_b32 v227, v147, 16, v227\n\t" \
  "v_lshl_or_b32 v228, v148, 16, v228\n\t" \
  "v_lshl_or_b32 v229, v149, 16, v229\n\t" \
  "v_lshl_or_b32 v230, v150, 16, v230\n\t" \
  "v_lshl_or_b32 v231, v151, 16, v231\n\t" \
  "s_waitcnt vmcnt(46)\n\t" \
  "v_lshl_or_b32 v232, v152, 16, v232\n\t" \
  "s_waitcnt vmcnt(44)\n\t" \
  "v_lshl_or_b32 v233, v153, 16, v233\n\t" \
  "s_waitcnt vmcnt(42)\n\t" \
  "v_lshl_or_b32 v234, v154, 16, v234\n\t" \
  "s_waitcnt vmcnt(40)\n\t" \
  "v_lshl_or_b32 v235, v155, 16, v235\n\t" \
  "s_waitcnt vmcnt(38)\n\t" \
  "v_lshl_or_b32 v236, v156, 16, v236\n\t" \
  "s_waitcnt vmcnt(36)\n\t" \
  "v_lshl_or_b32 v237, v157, 16, v237\n\t" \
  "s_waitcnt vmcnt(34)\n\t" \
  "v_lshl_or_b32 v238, v158, 16, v238\n\t" \
  "s_waitcnt vmcnt(32)\n\t" \
  "v_lshl_or_b32 v239, v159, 16, v239\n\t" \
  "s_waitcnt vmcnt(30)\n\t" \
  "v_lshl_or_b32 v240, v160, 16, v240\n\t" \
  "s_waitcnt vmcnt(28)\n\t" \
  "v_lshl_or_b32 v241, v161, 16, v241\n\t" \
  "s_waitcnt vmcnt(26)\n\t" \
  "v_lshl_or_b32 v242, v162, 16, v242\n\t" \
  "s_waitcnt vmcnt(24)\n\t" \
  "v_lshl_or_b32 v243, v163, 16, v243\n\t" \
  "s_waitcnt vmcnt(22)\n\t" \
  "v_lshl_or_b32 v244, v164, 16, v244\n\t" \
  "s_waitcnt vmcnt(20)\n\t" \
  "v_lshl_or_b32 v245, v165, 16, v245\n\t" \
  "s_waitcnt vmcnt(18)\n\t" \
  "v_lshl_or_b32 v246, v166, 16, v246\n\t" \
  "s_waitcnt vmcnt(16)\n\t" \
  "v_lshl_or_b32 v247, v167, 16, v247\n\t" \
  "s_waitcnt vmcnt(14)\n\t" \
  "v_lshl_or_b32 v248, v168, 16, v248\n\t" \
  "s_waitcnt vmcnt(12)\n\t" \
  "v_lshl_or_b32 v249, v169, 16, v249\n\t" \
  "s_waitcnt vmcnt(10)\n\t" \
  "v_lshl_or_b32 v250, v170, 16, v250\n\t" \
  "s_waitcnt vmcnt(8)\n\t" \
  "v_lshl_or_b32 v251, v171, 16, v251\n\t" \
  "s_waitcnt vmcnt(6)\n\t" \
  "v_lshl_or_b32 v252, v172, 16, v252\n\t" \
  "s_waitcnt vmcnt(4)\n\t" \
  "v_lshl_or_b32 v253, v173, 16, v253\n\t" \
  "s_waitcnt vmcnt(2)\n\t" \
  "v_lshl_or_b32 v254, v174, 16, v254\n\t" \
  "s_waitcnt vmcnt(0)\n\t" \
  "v_lshl_or_b32 v255, v175, 16, v255\n\t" \
  "s_mov_b32 s84, " TK_CAP "\n\t" \
  "s_mov_b32 s48, 0\n\t" \
  "s_mov_b32 s49, 0xf0000000\n\t" \
  "s_mov_b32 s46, s45\n\t" \
  "v_mov_b32 v5, 0\n\t" \
  "v_lshrrev_b32 v13, 2, v0\n\t" \
  "s_mov_b32 s60, " TK_CTAB "\n\t" \
  "buffer_load_dword v10, v13, %[X], s60 offen offset:88 sc1\n\t" \
  "buffer_load_dword v13, v13, %[X], s60 offen offset:344 sc1\n\t" \
  "s_waitcnt vmcnt(0)\n\t" \
  "v_and_b32 v10, 1, v10\n\t" \
  "v_and_b32 v13, 1, v13\n\t" \
  "v_cmp_ne_u32 s[90:91], s67, v10\n\t" \
  "v_cmp_ne_u32 s[88:89], s67, v13\n\t" \
  "s_nop 1\n\t" \
  "s_and_b32 s60, s88, 256\n\t" \
  "s_lshl_b32 s60, s60, 5\n\t" \
  "s_add_u32 s44, s44, s60\n\t" \
  "s_add_u32 s83, s48, 0\n\t" \
  "s_lshr_b64 s[60:61], s[90:91], s83\n\t" \
  "s_and_b32 s60, s60, 1\n\t" \
  "s_lshl_b32 s60, s60, 13\n\t" \
  "s_add_u32 s83, s83, " TK_SG0 "\n\t" \
  "s_lshl_b32 s83, s83, 21\n\t" \
  "s_or_b32 s83, s83, s60\n\t" \
  "LCOLD_%=:\n\t" \
  "s_add_u32 s62, s83, s41\n\t" \
  "s_mov_b32 s61, 0\n\t" \
  "LPQ_%=:\n\t" \
  "buffer_load_dword v10, v9, %[X], s62 offen sc1\n\t" \
  "s_waitcnt vmcnt(0)\n\t" \
  "v_cmp_eq_u32 vcc, 0, v10\n\t" \
  "s_cbranch_vccz LPQ_%=x\n\t" \
  "s_add_u32 s61, s61, 1\n\t" \
  "s_sleep 4\n\t" \
  "s_cmp_gt_u32 s61, s84\n\t" \
  "s_cbranch_scc1 LPQ_%=g\n\t" \
  "s_and_b32 s60, s61, 1023\n\t" \
  "s_cmp_lg_u32 s60, 0\n\t" \
  "s_cbranch_scc1 LPQ_%=\n\t" \
  "buffer_load_dword v10, off, %[X], s92 sc1\n\t" \
  "s_waitcnt vmcnt(0)\n\t" \
  "v_readfirstlane_b32 s60, v10\n\t" \
  "s_nop 0\n\t" \
  "s_cmp_eq_u32 s60, 0\n\t" \
  "s_cbranch_scc1 LPQ_%=\n\t" \
  "LPQ_%=g:\n\t" \
  "s_mov_b32 s84, 0\n\t" \
  "s_or_b32 s60, s48, 1073741824\n\t" \
  "v_mov_b32 v10, s60\n\t" \
  "s_mov_b64 exec, 1\n\t" \
  "buffer_store_dword v10, off, %[X], s92 sc1\n\t" \
  "s_mov_b64 exec, -1\n\t" \
  "LPQ_%=x:\n\t" \
  "s_add_u32 m0, s46, 0\n\t" \
  "s_add_u32 s47, s83, s36\n\t" \
  "buffer_load_dwordx4 v0, %[X], s47 offen offset:0 sc1 lds\n\t" \
  "buffer_load_dwordx4 v0, %[X], s47 offen offset:1024 sc1 lds\n\t" \
  "buffer_load_dwordx4 v0, %[X], s47 offen offset:2048 sc1 lds\n\t" \
  "buffer_load_dwordx4 v0, %[X], s47 offen offset:3072 sc1 lds\n\t" \
  "s_add_u32 m0, s46, 4096\n\t" \
  "s_add_u32 s47, s83, s37\n\t" \
  "buffer_load_dwordx4 v0, %[X], s47 offen offset:0 sc1 lds\n\t" \
  "buffer_load_dwordx4 v0, %[X], s47 offen offset:1024 sc1 lds\n\t" \
  "buffer_load_dwordx4 v0, %[X], s47 offen offset:2048 sc1 lds\n\t" \
  "buffer_load_dwordx4 v0, %[X], s47 offen offset:3072 sc1 lds\n\t" \
  "s_add_u32 m0, s46, 8192\n\t" \
  "s_add_u32 s47, s83, s38\n\t" \
  "buffer_load_dwordx4 v0, %[X], s47 offen offset:0 sc1 lds\n\t" \
  "buffer_load_dwordx4 v0, %[X], s47 offen offset:1024 sc1 lds\n\t" \
  "buffer_load_dwordx4 v0, %[X], s47 offen offset:2048 sc1 lds\n\t" \
  "buffer_load_dwordx4 v0, %[X], s47 offen offset:3072 sc1 lds\n\t" \
  "s_waitcnt vmcnt(0)\n\t" \
  "LTOP_%=:\n\t" \
  TK_AC0 \
  "s_barrier\n\t" \
  TK_AC1 \
  "ds_read_b128 v[48:51], v2 offset:0\n\t" \
  "ds_read_b128 v[64:67], v1 offset:8192\n\t" \
  "ds_read_b128 v[80:83], v1 offset:12288\n\t" \
  "ds_read_b128 v[52:55], v2 offset:1024\n\t" \
  "ds_read_b128 v[68:71], v1 offset:9216\n\t" \
  "ds_read_b128 v[84:87], v1 offset:13312\n\t" \
  "ds_read_b128 v[56:59], v2 offset:2048\n\t" \
  "ds_read_b128 v[72:75], v1 offset:10240\n\t" \
  "ds_read_b128 v[88:91], v1 offset:14336\n\t" \
  "ds_read_b128 v[60:63], v2 offset:3072\n\t" \
  "ds_read_b128 v[76:79], v1 offset:11264\n\t" \
  "ds_read_b128 v[92:95], v1 offset:15360\n\t" \
  "v_readfirstlane_b32 s57, v5\n\t" \
  "s_mov_b32 s82, s83\n\t" \
  "s_xor_b32 s46, s46, 0x10000\n\t" \
  "s_add_u32 s58, s82, s41\n\t" \
  "s_add_u32 s58, s58, 4096\n\t" \
  "s_add_u32 s59, s82, s39\n\t" \
  "s_cmp_eq_u32 s57, 0\n\t" \
  "s_cselect_b32 s62, 0xf0000000, s59\n\t" \
  "s_waitcnt lgkmcnt(10)\n\t" \
  "v_mfma_f32_16x16x32_bf16 v[36:39], v[64:67], v[48:51], v[16:19]\n\t" \
  "ds_read_b128 v[96:99], v1 offset:16384\n\t" \
  "buffer_load_dwordx4 v[196:199], v0, %[X], s62 offen offset:0 sc1\n\t" \
  "s_waitcnt lgkmcnt(10)\n\t" \
  "v_mfma_f32_16x16x32_bf16 v[40:43], v[80:83], v[48:51], v[20:23]\n\t" \
  "ds_read_b128 v[100:103], v1 offset:17408\n\t" \
  "buffer_load_dwordx4 v[200:203], v0, %[X], s62 offen offset:1024 sc1\n\t" \
  "s_waitcnt lgkmcnt(9)\n\t" \
  "v_mfma_f32_16x16x32_bf16 v[36:39], v[68:71], v[52:55], v[36:39]\n\t" \
  "ds_read_b128 v[104:107], v1 offset:18432\n\t" \
  "buffer_load_dwordx2 v[204:205], v6, %[X], s62 offen offset:2048 sc1\n\t" \
  "s_waitcnt lgkmcnt(9)\n\t" \
  "v_mfma_f32_16x16x32_bf16 v[40:43], v[84:87], v[52:55], v[40:43]\n\t" \
  "ds_read_b128 v[108:111], v1 offset:19456\n\t" \
  "buffer_load_dwordx2 v[206:207], v6, %[X], s62 offen offset:2560 sc1\n\t" \
  "s_waitcnt lgkmcnt(8)\n\t" \
  "v_mfma_f32_16x16x32_bf16 v[36:39], v[72:75], v[56:59], v[36:39]\n\t" \
  "ds_read_b128 v[112:115], v1 offset:20480\n\t" \
  "buffer_load_dwordx2 v[208:209], v6, %[X], s62 offen offset:3072 sc1\n\t" \
  "s_waitcnt lgkmcnt(8)\n\t" \
  "v_mfma_f32_16x16x32_bf16 v[40:43], v[88:91], v[56:59], v[40:43]\n\t" \
  "ds_read_b128 v[116:119], v1 offset:21504\n\t" \
  "buffer_load_dwordx2 v[210:211], v6, %[X], s62 offen offset:3584 sc1\n\t" \
  "s_waitcnt lgkmcnt(7)\n\t" \
  "v_mfma_f32_16x16x32_bf16 v[36:39], v[76:79], v[60:63], v[36:39]\n\t" \
  "ds_read_b128 v[120:123], v1 offset:22528\n\t" \
  "s_waitcnt lgkmcnt(7)\n\t" \
  "v_mfma_f32_16x16x32_bf16 v[40:43], v[92:95], v[60:63], v[40:43]\n\t" \
  "ds_read_b128 v[124:127], v1 offset:23552\n\t" \
  "buffer_load_dword v5, off, %[X], s58 sc1\n\t" \
  "s_add_u32 s83, s48, 1\n\t" \
  "s_lshr_b64 s[60:61], s[90:91], s83\n\t" \
  "s_and_b32 s60, s60, 1\n\t" \
  "s_lshl_b32 s60, s60, 13\n\t" \
  "s_add_u32 s83, s83, " TK_SG0 "\n\t" \
  "s_lshl_b32 s83, s83, 21\n\t" \
  "s_or_b32 s83, s83, s60\n\t" \
  "v_xor_b32 v1, 0x10000, v1\n\t" \
  "v_xor_b32 v2, 0x10000, v2\n\t" \
  "v_max3_f32 v11, v36, v37, v38\n\t" \
  "v_max3_f32 v11, v11, v39, v40\n\t" \
  "v_max3_f32 v13, v41, v42, v43\n\t" \
  "v_max_f32 v11, v11, v13\n\t" \
  "v_mov_b32 v13, v11\n\t" \
  "s_nop 1\n\t" \
  "v_permlane16_swap_b32 v11, v13\n\t" \
  "v_max_f32 v11, v11, v13\n\t" \
  "v_mov_b32 v13, v11\n\t" \
  "s_nop 1\n\t" \
  "v_permlane32_swap_b32 v11, v13\n\t" \
  "v_max_f32 v11, v11, v13\n\t" \
  "v_mul_f32 v13, 0xbe0293ee, v11\n\t" \
  "v_fmamk_f32 v36, v36, 0x3e0293ee, v13\n\t" \
  "v_fmamk_f32 v37, v37, 0x3e0293ee, v13\n\t" \
  "v_fmamk_f32 v38, v38, 0x3e0293ee, v13\n\t" \
  "v_fmamk_f32 v39, v39, 0x3e0293ee, v13\n\t" \
  "v_fmamk_f32 v40, v40, 0x3e0293ee, v13\n\t" \
  "v_fmamk_f32 v41, v41, 0x3e0293ee, v13\n\t" \
  "v_fmamk_f32 v42, v42, 0x3e0293ee, v13\n\t" \
  "v_fmamk_f32 v43, v43, 0x3e0293ee, v13\n\t" \
  "v_exp_f32 v36, v36\n\t" \
  "v_exp_f32 v37, v37\n\t" \
  "v_exp_f32 v38, v38\n\t" \
  "v_exp_f32 v39, v39\n\t" \
  "v_exp_f32 v40, v40\n\t" \
  "v_exp_f32 v41, v41\n\t" \
  "v_exp_f32 v42, v42\n\t" \
  "v_exp_f32 v43, v43\n\t" \
  "v_cvt_pk_bf16_f32 v44, v36, v37\n\t" \
  "v_cvt_pk_bf16_f32 v45, v38, v39\n\t" \
  "v_cvt_pk_bf16_f32 v46, v40, v41\n\t" \
  "v_cvt_pk_bf16_f32 v47, v42, v43\n\t" \
  "v_add_f32 v36, v36, v37\n\t" \
  "v_add_f32 v38, v38, v39\n\t" \
  "s_waitcnt lgkmcnt(7)\n\t" \
  "v_mfma_f32_16x16x32_bf16 v[128:131], v[96:99], v[44:47], 0\n\t" \
  "v_add_f32 v40, v40, v41\n\t" \
  "s_and_b32 s62, s48, 15\n\t" \
  "s_lshr_b32 s63, s62, 1\n\t" \
  "s_waitcnt lgkmcnt(6)\n\t" \
  "v_mfma_f32_16x16x32_bf16 v[132:135], v[100:103], v[44:47], 0\n\t" \
  "v_add_f32 v42, v42, v43\n\t" \
  "s_lshr_b32 s60, s88, s63\n\t" \
  "s_and_b32 s60, s60, 1\n\t" \
  "s_waitcnt lgkmcnt(5)\n\t" \
  "v_mfma_f32_16x16x32_bf16 v[136:139], v[104:107], v[44:47], 0\n\t" \
  "v_add_f32 v36, v36, v38\n\t" \
  "s_lshl_b32 s60, s60, 13\n\t" \
  "s_lshl_b32 s61, s62, 20\n\t" \
  "s_waitcnt lgkmcnt(4)\n\t" \
  "v_mfma_f32_16x16x32_bf16 v[140:143], v[108:111], v[44:47], 0\n\t" \
  "v_add_f32 v40, v40, v42\n\t" \
  "s_add_u32 s50, s61, s60\n\t" \
  "s_add_u32 s50, s50, s43\n\t" \
  "s_waitcnt lgkmcnt(3)\n\t" \
  "v_mfma_f32_16x16x32_bf16 v[144:147], v[112:115], v[44:47], 0\n\t" \
  "v_add_f32 v12, v36, v40\n\t" \
  "s_add_u32 s51, s50, 32768\n\t" \
  "s_add_u32 s52, s51, 32768\n\t" \
  "s_waitcnt lgkmcnt(2)\n\t" \
  "v_mfma_f32_16x16x32_bf16 v[148:151], v[116:119], v[44:47], 0\n\t" \
  "s_add_u32 s53, s52, 32768\n\t" \
  "s_lshl_b32 s61, s62, 17\n\t" \
  "s_waitcnt lgkmcnt(1)\n\t" \
  "v_mfma_f32_16x16x32_bf16 v[152:155], v[120:123], v[44:47], 0\n\t" \
  "s_add_u32 s54, s61, s44\n\t" \
  "s_add_u32 s63, s83, s41\n\t" \
  "s_waitcnt lgkmcnt(0)\n\t" \
  "v_mfma_f32_16x16x32_bf16 v[156:159], v[124:127], v[44:47], 0\n\t" \
  TK_AC2 \
  "s_waitcnt vmcnt(0)\n\t" \
  "v_readfirstlane_b32 s60, v5\n\t" \
  "s_barrier\n\t" \
  TK_AC3 \
  TK_PA0 \
  "s_cmp_lg_u32 s57, 0\n\t" \
  "s_cbranch_scc1 LGOT_%=\n\t" \
  "s_cmp_lg_u32 s60, 0\n\t" \
  "s_cbranch_scc1 LPK_%=x\n\t" \
  "s_mov_b64 exec, 1\n\t" \
  "buffer_store_dword v3, off, %[X], s49 sc1\n\t" \
  "s_mov_b64 exec, -1\n\t" \
  "s_mov_b32 s49, 0xf0000000\n\t" \
  "s_mov_b32 s61, 0\n\t" \
  "LPK_%=:\n\t" \
  "buffer_load_dword v5, off, %[X], s58 sc1\n\t" \
  "s_waitcnt vmcnt(0)\n\t" \
  "v_cmp_eq_u32 vcc, 0, v5\n\t" \
  "s_cbranch_vccz LPK_%=x\n\t" \
  "s_add_u32 s61, s61, 1\n\t" \
  "s_sleep " TK_PSK "\n\t" \
  "s_cmp_gt_u32 s61, s84\n\t" \
  "s_cbranch_scc1 LPK_%=g\n\t" \
  "s_and_b32 s60, s61, 1023\n\t" \
  "s_cmp_lg_u32 s60, 0\n\t" \
  "s_cbranch_scc1 LPK_%=\n\t" \
  "buffer_load_dword v5, off, %[X], s92 sc1\n\t" \
  "s_waitcnt vmcnt(0)\n\t" \
  "v_readfirstlane_b32 s60, v5\n\t" \
  "s_nop 0\n\t" \
  "s_cmp_eq_u32 s60, 0\n\t" \
  "s_cbranch_scc1 LPK_%=\n\t" \
  "LPK_%=g:\n\t" \
  "s_mov_b32 s84, 0\n\t" \
  "s_or_b32 s60, s48, 1342177280\n\t" \
  "v_mov_b32 v5, s60\n\t" \
  "s_mov_b64 exec, 1\n\t" \
  "buffer_store_dword v5, off, %[X], s92 sc1\n\t" \
  "s_mov_b64 exec, -1\n\t" \
  "LPK_%=x:\n\t" \
  TK_ADL \
  "buffer_load_dwordx4 v[196:199], v0, %[X], s59 offen offset:0 sc1\n\t" \
  "buffer_load_dwordx4 v[200:203], v0, %[X], s59 offen offset:1024 sc1\n\t" \
  "buffer_load_dwordx2 v[204:205], v6, %[X], s59 offen offset:2048 sc1\n\t" \
  "buffer_load_dwordx2 v[206:207], v6, %[X], s59 offen offset:2560 sc1\n\t" \
  "buffer_load_dwordx2 v[208:209], v6, %[X], s59 offen offset:3072 sc1\n\t" \
  "buffer_load_dwordx2 v[210:211], v6, %[X], s59 offen offset:3584 sc1\n\t" \
  "LGOT_%=:\n\t" \
  TK_PA1 \
  TK_AC4 \
  "buffer_load_dword v10, v9, %[X], s63 offen sc1\n\t" \
  "s_mov_b64 exec, 1\n\t" \
  "buffer_store_dword v3, off, %[X], s49 sc1\n\t" \
  "s_mov_b64 exec, -1\n\t" \
  "s_mov_b32 s49, 0xf0000000\n\t" \
  "s_add_u32 s49, s82, s42\n\t" \
  "s_waitcnt vmcnt(7)\n\t" \
  "v_mfma_f32_16x16x32_bf16 v[36:39], v[196:199], v[48:51], 0\n\t" \
  "v_mfma_f32_16x16x32_bf16 v[40:43], v[196:199], v[52:55], 0\n\t" \
  "s_waitcnt vmcnt(6)\n\t" \
  "v_mfma_f32_16x16x32_bf16 v[36:39], v[200:203], v[56:59], v[36:39]\n\t" \
  "v_mfma_f32_16x16x32_bf16 v[40:43], v[200:203], v[60:63], v[40:43]\n\t" \
  TK_AC5 \
  "s_nop 8\n\t" \
  "v_add_f32 v36, v36, v42\n\t" \
  "v_add_f32 v37, v37, v43\n\t" \
  "v_max3_f32 v14, v36, v37, v11\n\t" \
  "v_mov_b32 v13, v14\n\t" \
  "s_nop 1\n\t" \
  "v_permlane16_swap_b32 v14, v13\n\t" \
  "v_max_f32 v14, v14, v13\n\t" \
  "v_mov_b32 v13, v14\n\t" \
  "s_nop 1\n\t" \
  "v_permlane32_swap_b32 v14, v13\n\t" \
  "v_max_f32 v14, v14, v13\n\t" \
  "v_mul_f32 v14, 0xbe0293ee, v14\n\t" \
  "v_fmamk_f32 v11, v11, 0x3e0293ee, v14\n\t" \
  "v_fmamk_f32 v36, v36, 0x3e0293ee, v14\n\t" \
  "v_fmamk_f32 v37, v37, 0x3e0293ee, v14\n\t" \
  "v_exp_f32 v11, v11\n\t" \
  "v_exp_f32 v36, v36\n\t" \
  "v_exp_f32 v37, v37\n\t" \
  "v_fma_f32 v12, v12, v11, v36\n\t" \
  "v_add_f32 v12, v12, v37\n\t" \
  "v_mov_b32 v13, v12\n\t" \
  "s_nop 1\n\t" \
  "v_permlane16_swap_b32 v12, v13\n\t" \
  "v_add_f32 v12, v12, v13\n\t" \
  "v_mov_b32 v13, v12\n\t" \
  "s_nop 1\n\t" \
  "v_permlane32_swap_b32 v12, v13\n\t" \
  "v_add_f32 v12, v12, v13\n\t" \
  "v_rcp_f32 v12, v12\n\t" \
  "s_nop 0\n\t" \
  "v_mul_f32 v14, v11, v12\n\t" \
  "v_mul_f32 v36, v36, v12\n\t" \
  "v_mul_f32 v37, v37, v12\n\t" \
  "v_cvt_pk_bf16_f32 v26, v36, v37\n\t" \
  "v_cvt_pk_bf16_f32 v33, v36, v37\n\t" \
  "v_pk_mul_f32 v[128:129], v[128:129], v[14:15] op_sel_hi:[1,0]\n\t" \
  "v_pk_mul_f32 v[130:131], v[130:131], v[14:15] op_sel_hi:[1,0]\n\t" \
  "v_pk_mul_f32 v[132:133], v[132:133], v[14:15] op_sel_hi:[1,0]\n\t" \
  "v_pk_mul_f32 v[134:135], v[134:135], v[14:15] op_sel_hi:[1,0]\n\t" \
  "v_pk_mul_f32 v[136:137], v[136:137], v[14:15] op_sel_hi:[1,0]\n\t" \
  "v_pk_mul_f32 v[138:139], v[138:139], v[14:15] op_sel_hi:[1,0]\n\t" \
  "v_pk_mul_f32 v[140:141], v[140:141], v[14:15] op_sel_hi:[1,0]\n\t" \
  "v_pk_mul_f32 v[142:143], v[142:143], v[14:15] op_sel_hi:[1,0]\n\t" \
  "s_waitcnt vmcnt(4)\n\t" \
  "v_mfma_f32_16x16x32_bf16 v[128:131], v[204:207], v[26:29], v[128:131]\n\t" \
  "v_mul_f32 v144, v144, v14\n\t" \
  "v_mul_f32 v145, v145, v14\n\t" \
  "v_mfma_f32_16x16x32_bf16 v[132:135], v[204:207], v[32:35], v[132:135]\n\t" \
  "v_mul_f32 v146, v146, v14\n\t" \
  "v_mul_f32 v147, v147, v14\n\t" \
  "v_mfma_f32_16x16x32_bf16 v[136:139], v[204:207], v[24:27], v[136:139]\n\t" \
  "v_mul_f32 v148, v148, v14\n\t" \
  "v_mul_f32 v149, v149, v14\n\t" \
  "v_mfma_f32_16x16x32_bf16 v[140:143], v[204:207], v[30:33], v[140:143]\n\t" \
  "v_cvt_pk_bf16_f32 v64, v128, v129\n\t" \
  "v_cvt_pk_bf16_f32 v65, v130, v131\n\t" \
  "v_cvt_pk_bf16_f32 v66, v132, v133\n\t" \
  "v_cvt_pk_bf16_f32 v67, v134, v135\n\t" \
  "v_mul_f32 v150, v150, v14\n\t" \
  "v_mul_f32 v151, v151, v14\n\t" \
  TK_AC6 \
  "v_mfma_f32_16x16x32_bf16 v[192:195], v[224:227], v[64:67], 0\n\t" \
  "ds_write_b128 v3, v[64:67] offset:0\n\t" \
  "v_cvt_pk_bf16_f32 v68, v136, v137\n\t" \
  "v_cvt_pk_bf16_f32 v69, v138, v139\n\t" \
  "v_mfma_f32_16x16x32_bf16 v[160:163], v[64:67], a[0:3], 0\n\t" \
  "v_cvt_pk_bf16_f32 v70, v140, v141\n\t" \
  "v_cvt_pk_bf16_f32 v71, v142, v143\n\t" \
  "s_waitcnt vmcnt(2)\n\t" \
  "v_mfma_f32_16x16x32_bf16 v[144:147], v[208:211], v[26:29], v[144:147]\n\t" \
  "v_mul_f32 v152, v152, v14\n\t" \
  "v_mul_f32 v153, v153, v14\n\t" \
  "v_mfma_f32_16x16x32_bf16 v[148:151], v[208:211], v[32:35], v[148:151]\n\t" \
  "v_mul_f32 v154, v154, v14\n\t" \
  "v_mul_f32 v155, v155, v14\n\t" \
  "v_mfma_f32_16x16x32_bf16 v[164:167], v[64:67], a[4:7], 0\n\t" \
  "ds_write_b128 v3, v[68:71] offset:1024\n\t" \
  "v_mul_f32 v156, v156, v14\n\t" \
  "v_mul_f32 v157, v157, v14\n\t" \
  "v_mfma_f32_16x16x32_bf16 v[168:171], v[64:67], a[8:11], 0\n\t" \
  "v_mul_f32 v158, v158, v14\n\t" \
  "v_mul_f32 v159, v159, v14\n\t" \
  "v_mfma_f32_16x16x32_bf16 v[172:175], v[64:67], a[12:15], 0\n\t" \
  "v_cvt_pk_bf16_f32 v72, v144, v145\n\t" \
  "v_cvt_pk_bf16_f32 v73, v146, v147\n\t" \
  "v_mfma_f32_16x16x32_bf16 v[152:155], v[208:211], v[24:27], v[152:155]\n\t" \
  "v_cvt_pk_bf16_f32 v74, v148, v149\n\t" \
  "v_cvt_pk_bf16_f32 v75, v150, v151\n\t" \
  "v_mfma_f32_16x16x32_bf16 v[156:159], v[208:211], v[30:33], v[156:159]\n\t" \
  "v_mfma_f32_16x16x32_bf16 v[176:179], v[64:67], a[16:19], 0\n\t" \
  "ds_write_b128 v3, v[72:75] offset:2048\n\t" \
  "v_mfma_f32_16x16x32_bf16 v[180:183], v[64:67], a[20:23], 0\n\t" \
  "v_mfma_f32_16x16x32_bf16 v[184:187], v[64:67], a[24:27], 0\n\t" \
  "v_cvt_pk_bf16_f32 v76, v152, v153\n\t" \
  "v_cvt_pk_bf16_f32 v77, v154, v155\n\t" \
  "v_mfma_f32_16x16x32_bf16 v[188:191], v[64:67], a[28:31], 0\n\t" \
  "v_cvt_pk_bf16_f32 v78, v156, v157\n\t" \
  "v_cvt_pk_bf16_f32 v79, v158, v159\n\t" \
  "v_mfma_f32_16x16x32_bf16 v[192:195], v[228:231], v[68:71], v[192:195]\n\t" \
  "ds_write_b128 v3, v[76:79] offset:3072\n\t" \
  "v_mfma_f32_16x16x32_bf16 v[160:163], v[68:71], a[32:35], v[160:163]\n\t" \
  "s_waitcnt vmcnt(1)\n\t" \
  "v_cmp_eq_u32 vcc, 0, v10\n\t" \
  "v_mfma_f32_16x16x32_bf16 v[164:167], v[68:71], a[36:39], v[164:167]\n\t" \
  "s_cmp_eq_u64 vcc, 0\n\t" \
  "s_cselect_b32 s55, s83, 0xf0000000\n\t" \
  "s_cmp_eq_u32 s48, 63\n\t" \
  "v_mfma_f32_16x16x32_bf16 v[168:171], v[68:71], a[40:43], v[168:171]\n\t" \
  "s_cselect_b32 s55, 0xf0000000, s55\n\t" \
  "s_mov_b32 s56, s83\n\t" \
  "s_mov_b32 s83, s55\n\t" \
  "v_mfma_f32_16x16x32_bf16 v[172:175], v[68:71], a[44:47], v[172:175]\n\t" \
  "v_mfma_f32_16x16x32_bf16 v[176:179], v[68:71], a[48:51], v[176:179]\n\t" \
  "s_add_u32 m0, s46, 0\n\t" \
  "s_add_u32 s47, s83, s36\n\t" \
  "buffer_load_dwordx4 v0, %[X], s47 offen offset:0 sc1 lds\n\t" \
  "v_mfma_f32_16x16x32_bf16 v[180:183], v[68:71], a[52:55], v[180:183]\n\t" \
  "v_mfma_f32_16x16x32_bf16 v[184:187], v[68:71], a[56:59], v[184:187]\n\t" \
  "s_waitcnt lgkmcnt(0)\n\t" \
  "s_barrier\n\t" \
  "v_mfma_f32_16x16x32_bf16 v[188:191], v[68:71], a[60:63], v[188:191]\n\t" \
  "ds_read_b128 v[80:83], v4 offset:0\n\t" \
  "v_mfma_f32_16x16x32_bf16 v[192:195], v[232:235], v[72:75], v[192:195]\n\t" \
  "buffer_load_dwordx4 v0, %[X], s47 offen offset:1024 sc1 lds\n\t" \
  "v_mfma_f32_16x16x32_bf16 v[160:163], v[72:75], a[64:67], v[160:163]\n\t" \
  "ds_read_b128 v[84:87], v4 offset:1024\n\t" \
  "v_mfma_f32_16x16x32_bf16 v[164:167], v[72:75], a[68:71], v[164:167]\n\t" \
  "v_mfma_f32_16x16x32_bf16 v[168:171], v[72:75], a[72:75], v[168:171]\n\t" \
  "ds_read_b128 v[88:91], v4 offset:2048\n\t" \
  "v_mfma_f32_16x16x32_bf16 v[172:175], v[72:75], a[76:79], v[172:175]\n\t" \
  "buffer_load_dwordx4 v0, %[X], s47 offen offset:2048 sc1 lds\n\t" \
  "v_mfma_f32_16x16x32_bf16 v[176:179], v[72:75], a[80:83], v[176:179]\n\t" \
  "ds_read_b128 v[92:95], v4 offset:3072\n\t" \
  "v_mfma_f32_16x16x32_bf16 v[180:183], v[72:75], a[84:87], v[180:183]\n\t" \
  "v_mfma_f32_16x16x32_bf16 v[184:187], v[72:75], a[88:91], v[184:187]\n\t" \
  "v_mfma_f32_16x16x32_bf16 v[188:191], v[72:75], a[92:95], v[188:191]\n\t" \
  "buffer_load_dwordx4 v0, %[X], s47 offen offset:3072 sc1 lds\n\t" \
  "v_mfma_f32_16x16x32_bf16 v[192:195], v[236:239], v[76:79], v[192:195]\n\t" \
  "v_mfma_f32_16x16x32_bf16 v[160:163], v[76:79], a[96:99], v[160:163]\n\t" \
  "v_mfma_f32_16x16x32_bf16 v[164:167], v[76:79], a[100:103], v[164:167]\n\t" \
  "v_mfma_f32_16x16x32_bf16 v[168:171], v[76:79], a[104:107], v[168:171]\n\t" \
  "s_add_u32 m0, s46, 4096\n\t" \
  "s_add_u32 s47, s83, s37\n\t" \
  "buffer_load_dwordx4 v0, %[X], s47 offen offset:0 sc1 lds\n\t" \
  "v_mfma_f32_16x16x32_bf16 v[172:175], v[76:79], a[108:111], v[172:175]\n\t" \
  "v_mfma_f32_16x16x32_bf16 v[176:179], v[76:79], a[112:115], v[176:179]\n\t" \
  "v_mfma_f32_16x16x32_bf16 v[180:183], v[76:79], a[116:119], v[180:183]\n\t" \
  "v_mfma_f32_16x16x32_bf16 v[184:187], v[76:79], a[120:123], v[184:187]\n\t" \
  "buffer_load_dwordx4 v0, %[X], s47 offen offset:1024 sc1 lds\n\t" \
  "v_mfma_f32_16x16x32_bf16 v[188:191], v[76:79], a[124:127], v[188:191]\n\t" \
  "s_waitcnt lgkmcnt(3)\n\t" \
  "v_mfma_f32_16x16x32_bf16 v[192:195], v[240:243], v[80:83], v[192:195]\n\t" \
  "v_mfma_f32_16x16x32_bf16 v[160:163], v[80:83], a[128:131], v[160:163]\n\t" \
  "v_mfma_f32_16x16x32_bf16 v[164:167], v[80:83], a[132:135], v[164:167]\n\t" \
  "buffer_load_dwordx4 v0, %[X], s47 offen offset:2048 sc1 lds\n\t" \
  "v_mfma_f32_16x16x32_bf16 v[168:171], v[80:83], a[136:139], v[168:171]\n\t" \
  "v_mfma_f32_16x16x32_bf16 v[172:175], v[80:83], a[140:143], v[172:175]\n\t" \
  "v_mfma_f32_16x16x32_bf16 v[176:179], v[80:83], a[144:147], v[176:179]\n\t" \
  "v_mfma_f32_16x16x32_bf16 v[180:183], v[80:83], a[148:151], v[180:183]\n\t" \
  "buffer_load_dwordx4 v0, %[X], s47 offen offset:3072 sc1 lds\n\t" \
  "v_mfma_f32_16x16x32_bf16 v[184:187], v[80:83], a[152:155], v[184:187]\n\t" \
  "v_mfma_f32_16x16x32_bf16 v[188:191], v[80:83], a[156:159], v[188:191]\n\t" \
  "s_waitcnt lgkmcnt(2)\n\t" \
  "v_mfma_f32_16x16x32_bf16 v[192:195], v[244:247], v[84:87], v[192:195]\n\t" \
  "v_mfma_f32_16x16x32_bf16 v[160:163], v[84:87], a[160:163], v[160:163]\n\t" \
  "s_add_u32 m0, s46, 8192\n\t" \
  "s_add_u32 s47, s83, s38\n\t" \
  "buffer_load_dwordx4 v0, %[X], s47 offen offset:0 sc1 lds\n\t" \
  "v_mfma_f32_16x16x32_bf16 v[164:167], v[84:87], a[164:167], v[164:167]\n\t" \
  "v_mfma_f32_16x16x32_bf16 v[168:171], v[84:87], a[168:171], v[168:171]\n\t" \
  "v_mfma_f32_16x16x32_bf16 v[172:175], v[84:87], a[172:175], v[172:175]\n\t" \
  "v_mfma_f32_16x16x32_bf16 v[176:179], v[84:87], a[176:179], v[176:179]\n\t" \
  "buffer_load_dwordx4 v0, %[X], s47 offen offset:1024 sc1 lds\n\t" \
  "v_mfma_f32_16x16x32_bf16 v[180:183], v[84:87], a[180:183], v[180:183]\n\t" \
  "v_mfma_f32_16x16x32_bf16 v[184:187], v[84:87], a[184:187], v[184:187]\n\t" \
  "v_mfma_f32_16x16x32_bf16 v[188:191], v[84:87], a[188:191], v[188:191]\n\t" \
  "s_waitcnt lgkmcnt(1)\n\t" \
  "v_mfma_f32_16x16x32_bf16 v[192:195], v[248:251], v[88:91], v[192:195]\n\t" \
  "buffer_load_dwordx4 v0, %[X], s47 offen offset:2048 sc1 lds\n\t" \
  "v_mfma_f32_16x16x32_bf16 v[160:163], v[88:91], a[192:195], v[160:163]\n\t" \
  "v_mfma_f32_16x16x32_bf16 v[164:167], v[88:91], a[196:199], v[164:167]\n\t" \
  "v_mfma_f32_16x16x32_bf16 v[168:171], v[88:91], a[200:203], v[168:171]\n\t" \
  "v_mfma_f32_16x16x32_bf16 v[172:175], v[88:91], a[204:207], v[172:175]\n\t" \
  "buffer_load_dwordx4 v0, %[X], s47 offen offset:3072 sc1 lds\n\t" \
  "v_mfma_f32_16x16x32_bf16 v[176:179], v[88:91], a[208:211], v[176:179]\n\t" \
  "v_mfma_f32_16x16x32_bf16 v[180:183], v[88:91], a[212:215], v[180:183]\n\t" \
  "v_mfma_f32_16x16x32_bf16 v[184:187], v[88:91], a[216:219], v[184:187]\n\t" \
  "s_add_u32 s63, s63, 4096\n\t" \
  "v_mfma_f32_16x16x32_bf16 v[188:191], v[88:91], a[220:223], v[188:191]\n\t" \
  "buffer_load_dword v5, off, %[X], s63 sc1\n\t" \
  "s_waitcnt lgkmcnt(0)\n\t" \
  "v_mfma_f32_16x16x32_bf16 v[192:195], v[252:255], v[92:95], v[192:195]\n\t" \
  "v_mfma_f32_16x16x32_bf16 v[160:163], v[92:95], a[224:227], v[160:163]\n\t" \
  "v_mfma_f32_16x16x32_bf16 v[164:167], v[92:95], a[228:231], v[164:167]\n\t" \
  "v_mfma_f32_16x16x32_bf16 v[168:171], v[92:95], a[232:235], v[168:171]\n\t" \
  "v_cvt_pk_bf16_f32 v112, v192, v193\n\t" \
  "v_cvt_pk_bf16_f32 v113, v194, v195\n\t" \
  "v_mfma_f32_16x16x32_bf16 v[172:175], v[92:95], a[236:239], v[172:175]\n\t" \
  "buffer_store_dwordx2 v[112:113], v8, %[X], s54 offen sc1\n\t" \
  "v_mfma_f32_16x16x32_bf16 v[176:179], v[92:95], a[240:243], v[176:179]\n\t" \
  "v_cvt_pk_bf16_f32 v96, v160, v164\n\t" \
  "v_cvt_pk_bf16_f32 v100, v161, v165\n\t" \
  "v_mfma_f32_16x16x32_bf16 v[180:183], v[92:95], a[244:247], v[180:183]\n\t" \
  "v_cvt_pk_bf16_f32 v104, v162, v166\n\t" \
  "v_cvt_pk_bf16_f32 v108, v163, v167\n\t" \
  "v_mfma_f32_16x16x32_bf16 v[184:187], v[92:95], a[248:251], v[184:187]\n\t" \
  "v_cvt_pk_bf16_f32 v97, v168, v172\n\t" \
  "v_cvt_pk_bf16_f32 v101, v169, v173\n\t" \
  "v_mfma_f32_16x16x32_bf16 v[188:191], v[92:95], a[252:255], v[188:191]\n\t" \
  "v_cvt_pk_bf16_f32 v105, v170, v174\n\t" \
  "v_cvt_pk_bf16_f32 v109, v171, v175\n\t" \
  "v_cvt_pk_bf16_f32 v98, v176, v180\n\t" \
  "v_cvt_pk_bf16_f32 v102, v177, v181\n\t" \
  "v_cvt_pk_bf16_f32 v106, v178, v182\n\t" \
  "v_cvt_pk_bf16_f32 v110, v179, v183\n\t" \
  TK_AC7 \
  "s_nop 1\n\t" \
  "v_cvt_pk_bf16_f32 v99, v184, v188\n\t" \
  "buffer_store_dwordx4 v[96:99], v7, %[X], s50 offen sc1\n\t" \
  TK_ASL \
  "v_cvt_pk_bf16_f32 v103, v185, v189\n\t" \
  "buffer_store_dwordx4 v[100:103], v7, %[X], s51 offen sc1\n\t" \
  TK_ASL \
  "v_cvt_pk_bf16_f32 v107, v186, v190\n\t" \
  "buffer_store_dwordx4 v[104:107], v7, %[X], s52 offen sc1\n\t" \
  TK_ASL \
  "v_cvt_pk_bf16_f32 v111, v187, v191\n\t" \
  "buffer_store_dwordx4 v[108:111], v7, %[X], s53 offen sc1\n\t" \
  TK_PA2 \
  TK_AC8 \
  "s_cmp_eq_u32 s48, 63\n\t" \
  "s_cbranch_scc1 LEND_%=\n\t" \
  "s_add_u32 s48, s48, 1\n\t" \
  "s_cmp_eq_u32 s83, s56\n\t" \
  "s_mov_b32 s83, s56\n\t" \
  "s_cbranch_scc0 LCOLD_%=\n\t" \
  "s_waitcnt vmcnt(5)\n\t" \
  "s_branch LTOP_%=\n\t" \
  "LEND_%=:\n\t" \
  "s_waitcnt vmcnt(0)\n\t" \
  "s_barrier\n\t" \
  "s_mov_b64 exec, 1\n\t" \
  "buffer_store_dword v3, off, %[X], s49 sc1\n\t" \
  "s_mov_b64 exec, -1\n\t" \
  "s_mov_b32 s49, 0xf0000000\n\t" \
  TK_PA0 \
  TK_ACE \
  ""
#define QK_ASM \
  "v_lshrrev_b32 v16, 7, v0\n\t" \
  "v_lshlrev_b32 v16, 13, v16\n\t" \
  "v_lshrrev_b32 v19, 2, v0\n\t" \
  "v_and_b32 v19, 64, v19\n\t" \
  "v_xor_b32 v19, v19, v0\n\t" \
  "v_and_b32 v19, 0x70, v19\n\t" \
  "v_add_u32 v16, v16, v19\n\t" \
  "s_mul_i32 s63, s40, 40960\n\t" \
  "v_and_b32 v19, 0x300, v0\n\t" \
  "v_lshlrev_b32 v17, 2, v19\n\t" \
  "v_and_b32 v19, 0xc0, v0\n\t" \
  "v_lshl_add_u32 v17, v19, 1, v17\n\t" \
  "v_and_b32 v19, 0x30, v0\n\t" \
  "v_add3_u32 v17, v17, v19, s63\n\t" \
  "v_lshrrev_b32 v19, 1, v0\n\t" \
  "v_and_b32 v19, 64, v19\n\t" \
  "v_add_u32 v17, v17, v19\n\t" \
  "v_xor_b32 v18, 64, v17\n\t" \
  "s_add_u32 m0, s63, 0\n\t" \
  "s_mov_b32 s60, 0\n\t" \
  "buffer_load_dwordx4 v16, %[W], s60 offen lds\n\t" \
  "s_add_u32 m0, s63, 1024\n\t" \
  "s_mov_b32 s60, 65536\n\t" \
  "buffer_load_dwordx4 v16, %[W], s60 offen lds\n\t" \
  "s_add_u32 m0, s63, 2048\n\t" \
  "s_mov_b32 s60, 131072\n\t" \
  "buffer_load_dwordx4 v16, %[W], s60 offen lds\n\t" \
  "s_add_u32 m0, s63, 3072\n\t" \
  "s_mov_b32 s60, 196608\n\t" \
  "buffer_load_dwordx4 v16, %[W], s60 offen lds\n\t" \
  "s_add_u32 m0, s63, 4096\n\t" \
  "s_mov_b32 s60, 262144\n\t" \
  "buffer_load_dwordx4 v16, %[W], s60 offen lds\n\t" \
  "s_add_u32 m0, s63, 5120\n\t" \
  "s_mov_b32 s60, 327680\n\t" \
  "buffer_load_dwordx4 v16, %[W], s60 offen lds\n\t" \
  "s_add_u32 m0, s63, 6144\n\t" \
  "s_mov_b32 s60, 393216\n\t" \
  "buffer_load_dwordx4 v16, %[W], s60 offen lds\n\t" \
  "s_add_u32 m0, s63, 7168\n\t" \
  "s_mov_b32 s60, 458752\n\t" \
  "buffer_load_dwordx4 v16, %[W], s60 offen lds\n\t" \
  "s_add_u32 m0, s63, 8192\n\t" \
  "s_mov_b32 s60, 524288\n\t" \
  "buffer_load_dwordx4 v16, %[W], s60 offen lds\n\t" \
  "s_add_u32 m0, s63, 9216\n\t" \
  "s_mov_b32 s60, 589824\n\t" \
  "buffer_load_dwordx4 v16, %[W], s60 offen lds\n\t" \
  "s_add_u32 m0, s63, 10240\n\t" \
  "s_mov_b32 s60, 655360\n\t" \
  "buffer_load_dwordx4 v16, %[W], s60 offen lds\n\t" \
  "s_add_u32 m0, s63, 11264\n\t" \
  "s_mov_b32 s60, 720896\n\t" \
  "buffer_load_dwordx4 v16, %[W], s60 offen lds\n\t" \
  "s_add_u32 m0, s63, 12288\n\t" \
  "s_mov_b32 s60, 786432\n\t" \
  "buffer_load_dwordx4 v16, %[W], s60 offen lds\n\t" \
  "s_add_u32 m0, s63, 13312\n\t" \
  "s_mov_b32 s60, 851968\n\t" \
  "buffer_load_dwordx4 v16, %[W], s60 offen lds\n\t" \
  "s_add_u32 m0, s63, 14336\n\t" \
  "s_mov_b32 s60, 917504\n\t" \
  "buffer_load_dwordx4 v16, %[W], s60 offen lds\n\t" \
  "s_add_u32 m0, s63, 15360\n\t" \
  "s_mov_b32 s60, 983040\n\t" \
  "buffer_load_dwordx4 v16, %[W], s60 offen lds\n\t" \
  "s_add_u32 m0, s63, 16384\n\t" \
  "s_mov_b32 s60, 1048576\n\t" \
  "buffer_load_dwordx4 v16, %[W], s60 offen lds\n\t" \
  "s_add_u32 m0, s63, 17408\n\t" \
  "s_mov_b32 s60, 1114112\n\t" \
  "buffer_load_dwordx4 v16, %[W], s60 offen lds\n\t" \
  "s_add_u32 m0, s63, 18432\n\t" \
  "s_mov_b32 s60, 1179648\n\t" \
  "buffer_load_dwordx4 v16, %[W], s60 offen lds\n\t" \
  "s_add_u32 m0, s63, 19456\n\t" \
  "s_mov_b32 s60, 1245184\n\t" \
  "buffer_load_dwordx4 v16, %[W], s60 offen lds\n\t" \
  "s_add_u32 m0, s63, 20480\n\t" \
  "s_mov_b32 s60, 1310720\n\t" \
  "buffer_load_dwordx4 v16, %[W], s60 offen lds\n\t" \
  "s_add_u32 m0, s63, 21504\n\t" \
  "s_mov_b32 s60, 1376256\n\t" \
  "buffer_load_dwordx4 v16, %[W], s60 offen lds\n\t" \
  "s_add_u32 m0, s63, 22528\n\t" \
  "s_mov_b32 s60, 1441792\n\t" \
  "buffer_load_dwordx4 v16, %[W], s60 offen lds\n\t" \
  "s_add_u32 m0, s63, 23552\n\t" \
  "s_mov_b32 s60, 1507328\n\t" \
  "buffer_load_dwordx4 v16, %[W], s60 offen lds\n\t" \
  "s_add_u32 m0, s63, 24576\n\t" \
  "s_mov_b32 s60, 1572864\n\t" \
  "buffer_load_dwordx4 v16, %[W], s60 offen lds\n\t" \
  "s_add_u32 m0, s63, 25600\n\t" \
  "s_mov_b32 s60, 1638400\n\t" \
  "buffer_load_dwordx4 v16, %[W], s60 offen lds\n\t" \
  "s_add_u32 m0, s63, 26624\n\t" \
  "s_mov_b32 s60, 1703936\n\t" \
  "buffer_load_dwordx4 v16, %[W], s60 offen lds\n\t" \
  "s_add_u32 m0, s63, 27648\n\t" \
  "s_mov_b32 s60, 1769472\n\t" \
  "buffer_load_dwordx4 v16, %[W], s60 offen lds\n\t" \
  "s_add_u32 m0, s63, 28672\n\t" \
  "s_mov_b32 s60, 1835008\n\t" \
  "buffer_load_dwordx4 v16, %[W], s60 offen lds\n\t" \
  "s_add_u32 m0, s63, 29696\n\t" \
  "s_mov_b32 s60, 1900544\n\t" \
  "buffer_load_dwordx4 v16, %[W], s60 offen lds\n\t" \
  "s_add_u32 m0, s63, 30720\n\t" \
  "s_mov_b32 s60, 1966080\n\t" \
  "buffer_load_dwordx4 v16, %[W], s60 offen lds\n\t" \
  "s_add_u32 m0, s63, 31744\n\t" \
  "s_mov_b32 s60, 2031616\n\t" \
  "buffer_load_dwordx4 v16, %[W], s60 offen lds\n\t" \
  "s_add_u32 m0, s63, 32768\n\t" \
  "s_mov_b32 s60, 2097152\n\t" \
  "buffer_load_dwordx4 v16, %[W], s60 offen lds\n\t" \
  "s_add_u32 m0, s63, 33792\n\t" \
  "s_mov_b32 s60, 2162688\n\t" \
  "buffer_load_dwordx4 v16, %[W], s60 offen lds\n\t" \
  "s_add_u32 m0, s63, 34816\n\t" \
  "s_mov_b32 s60, 2228224\n\t" \
  "buffer_load_dwordx4 v16, %[W], s60 offen lds\n\t" \
  "s_add_u32 m0, s63, 35840\n\t" \
  "s_mov_b32 s60, 2293760\n\t" \
  "buffer_load_dwordx4 v16, %[W], s60 offen lds\n\t" \
  "s_add_u32 m0, s63, 36864\n\t" \
  "s_mov_b32 s60, 2359296\n\t" \
  "buffer_load_dwordx4 v16, %[W], s60 offen lds\n\t" \
  "s_add_u32 m0, s63, 37888\n\t" \
  "s_mov_b32 s60, 2424832\n\t" \
  "buffer_load_dwordx4 v16, %[W], s60 offen lds\n\t" \
  "s_add_u32 m0, s63, 38912\n\t" \
  "s_mov_b32 s60, 2490368\n\t" \
  "buffer_load_dwordx4 v16, %[W], s60 offen lds\n\t" \
  "s_add_u32 m0, s63, 39936\n\t" \
  "s_mov_b32 s60, 2555904\n\t" \
  "buffer_load_dwordx4 v16, %[W], s60 offen lds\n\t" \
  "s_waitcnt vmcnt(32)\n\t" \
  "ds_read_b64_tr_b16 a[0:1], v17 offset:0\n\t" \
  "ds_read_b64_tr_b16 a[2:3], v17 offset:512\n\t" \
  "ds_read_b64_tr_b16 a[4:5], v17 offset:4096\n\t" \
  "ds_read_b64_tr_b16 a[6:7], v17 offset:4608\n\t" \
  "ds_read_b64_tr_b16 a[72:73], v17 offset:8\n\t" \
  "ds_read_b64_tr_b16 a[74:75], v17 offset:520\n\t" \
  "ds_read_b64_tr_b16 a[76:77], v17 offset:4104\n\t" \
  "ds_read_b64_tr_b16 a[78:79], v17 offset:4616\n\t" \
  "ds_read_b64_tr_b16 a[144:145], v18 offset:0\n\t" \
  "ds_read_b64_tr_b16 a[146:147], v18 offset:512\n\t" \
  "ds_read_b64_tr_b16 a[148:149], v18 offset:4096\n\t" \
  "ds_read_b64_tr_b16 a[150:151], v18 offset:4608\n\t" \
  "ds_read_b64_tr_b16 a[216:217], v18 offset:8\n\t" \
  "ds_read_b64_tr_b16 a[218:219], v18 offset:520\n\t" \
  "ds_read_b64_tr_b16 a[220:221], v18 offset:4104\n\t" \
  "ds_read_b64_tr_b16 a[222:223], v18 offset:4616\n\t" \
  "s_waitcnt lgkmcnt(0)\n\t" \
  "s_add_u32 m0, s63, 0\n\t" \
  "s_mov_b32 s60, 2621440\n\t" \
  "buffer_load_dwordx4 v16, %[W], s60 offen lds\n\t" \
  "s_add_u32 m0, s63, 1024\n\t" \
  "s_mov_b32 s60, 2686976\n\t" \
  "buffer_load_dwordx4 v16, %[W], s60 offen lds\n\t" \
  "s_add_u32 m0, s63, 2048\n\t" \
  "s_mov_b32 s60, 2752512\n\t" \
  "buffer_load_dwordx4 v16, %[W], s60 offen lds\n\t" \
  "s_add_u32 m0, s63, 3072\n\t" \
  "s_mov_b32 s60, 2818048\n\t" \
  "buffer_load_dwordx4 v16, %[W], s60 offen lds\n\t" \
  "s_add_u32 m0, s63, 4096\n\t" \
  "s_mov_b32 s60, 2883584\n\t" \
  "buffer_load_dwordx4 v16, %[W], s60 offen lds\n\t" \
  "s_add_u32 m0, s63, 5120\n\t" \
  "s_mov_b32 s60, 2949120\n\t" \
  "buffer_load_dwordx4 v16, %[W], s60 offen lds\n\t" \
  "s_add_u32 m0, s63, 6144\n\t" \
  "s_mov_b32 s60, 3014656\n\t" \
  "buffer_load_dwordx4 v16, %[W], s60 offen lds\n\t" \
  "s_add_u32 m0, s63, 7168\n\t" \
  "s_mov_b32 s60, 3080192\n\t" \
  "buffer_load_dwordx4 v16, %[W], s60 offen lds\n\t" \
  "s_waitcnt vmcnt(32)\n\t" \
  "ds_read_b64_tr_b16 a[8:9], v17 offset:8192\n\t" \
  "ds_read_b64_tr_b16 a[10:11], v17 offset:8704\n\t" \
  "ds_read_b64_tr_b16 a[12:13], v17 offset:12288\n\t" \
  "ds_read_b64_tr_b16 a[14:15], v17 offset:12800\n\t" \
  "ds_read_b64_tr_b16 a[80:81], v17 offset:8200\n\t" \
  "ds_read_b64_tr_b16 a[82:83], v17 offset:8712\n\t" \
  "ds_read_b64_tr_b16 a[84:85], v17 offset:12296\n\t" \
  "ds_read_b64_tr_b16 a[86:87], v17 offset:12808\n\t" \
  "ds_read_b64_tr_b16 a[152:153], v18 offset:8192\n\t" \
  "ds_read_b64_tr_b16 a[154:155], v18 offset:8704\n\t" \
  "ds_read_b64_tr_b16 a[156:157], v18 offset:12288\n\t" \
  "ds_read_b64_tr_b16 a[158:159], v18 offset:12800\n\t" \
  "ds_read_b64_tr_b16 a[224:225], v18 offset:8200\n\t" \
  "ds_read_b64_tr_b16 a[226:227], v18 offset:8712\n\t" \
  "ds_read_b64_tr_b16 a[228:229], v18 offset:12296\n\t" \
  "ds_read_b64_tr_b16 a[230:231], v18 offset:12808\n\t" \
  "s_waitcnt lgkmcnt(0)\n\t" \
  "s_add_u32 m0, s63, 8192\n\t" \
  "s_mov_b32 s60, 3145728\n\t" \
  "buffer_load_dwordx4 v16, %[W], s60 offen lds\n\t" \
  "s_add_u32 m0, s63, 9216\n\t" \
  "s_mov_b32 s60, 3211264\n\t" \
  "buffer_load_dwordx4 v16, %[W], s60 offen lds\n\t" \
  "s_add_u32 m0, s63, 10240\n\t" \
  "s_mov_b32 s60, 3276800\n\t" \
  "buffer_load_dwordx4 v16, %[W], s60 offen lds\n\t" \
  "s_add_u32 m0, s63, 11264\n\t" \
  "s_mov_b32 s60, 3342336\n\t" \
  "buffer_load_dwordx4 v16, %[W], s60 offen lds\n\t" \
  "s_add_u32 m0, s63, 12288\n\t" \
  "s_mov_b32 s60, 3407872\n\t" \
  "buffer_load_dwordx4 v16, %[W], s60 offen lds\n\t" \
  "s_add_u32 m0, s63, 13312\n\t" \
  "s_mov_b32 s60, 3473408\n\t" \
  "buffer_load_dwordx4 v16, %[W], s60 offen lds\n\t" \
  "s_add_u32 m0, s63, 14336\n\t" \
  "s_mov_b32 s60, 3538944\n\t" \
  "buffer_load_dwordx4 v16, %[W], s60 offen lds\n\t" \
  "s_add_u32 m0, s63, 15360\n\t" \
  "s_mov_b32 s60, 3604480\n\t" \
  "buffer_load_dwordx4 v16, %[W], s60 offen lds\n\t" \
  "s_waitcnt vmcnt(32)\n\t" \
  "ds_read_b64_tr_b16 a[16:17], v17 offset:16384\n\t" \
  "ds_read_b64_tr_b16 a[18:19], v17 offset:16896\n\t" \
  "ds_read_b64_tr_b16 a[20:21], v17 offset:20480\n\t" \
  "ds_read_b64_tr_b16 a[22:23], v17 offset:20992\n\t" \
  "ds_read_b64_tr_b16 a[88:89], v17 offset:16392\n\t" \
  "ds_read_b64_tr_b16 a[90:91], v17 offset:16904\n\t" \
  "ds_read_b64_tr_b16 a[92:93], v17 offset:20488\n\t" \
  "ds_read_b64_tr_b16 a[94:95], v17 offset:21000\n\t" \
  "ds_read_b64_tr_b16 a[160:161], v18 offset:16384\n\t" \
  "ds_read_b64_tr_b16 a[162:163], v18 offset:16896\n\t" \
  "ds_read_b64_tr_b16 a[164:165], v18 offset:20480\n\t" \
  "ds_read_b64_tr_b16 a[166:167], v18 offset:20992\n\t" \
  "ds_read_b64_tr_b16 a[232:233], v18 offset:16392\n\t" \
  "ds_read_b64_tr_b16 a[234:235], v18 offset:16904\n\t" \
  "ds_read_b64_tr_b16 a[236:237], v18 offset:20488\n\t" \
  "ds_read_b64_tr_b16 a[238:239], v18 offset:21000\n\t" \
  "s_waitcnt lgkmcnt(0)\n\t" \
  "s_add_u32 m0, s63, 16384\n\t" \
  "s_mov_b32 s60, 3670016\n\t" \
  "buffer_load_dwordx4 v16, %[W], s60 offen lds\n\t" \
  "s_add_u32 m0, s63, 17408\n\t" \
  "s_mov_b32 s60, 3735552\n\t" \
  "buffer_load_dwordx4 v16, %[W], s60 offen lds\n\t" \
  "s_add_u32 m0, s63, 18432\n\t" \
  "s_mov_b32 s60, 3801088\n\t" \
  "buffer_load_dwordx4 v16, %[W], s60 offen lds\n\t" \
  "s_add_u32 m0, s63, 19456\n\t" \
  "s_mov_b32 s60, 3866624\n\t" \
  "buffer_load_dwordx4 v16, %[W], s60 offen lds\n\t" \
  "s_add_u32 m0, s63, 20480\n\t" \
  "s_mov_b32 s60, 3932160\n\t" \
  "buffer_load_dwordx4 v16, %[W], s60 offen lds\n\t" \
  "s_add_u32 m0, s63, 21504\n\t" \
  "s_mov_b32 s60, 3997696\n\t" \
  "buffer_load_dwordx4 v16, %[W], s60 offen lds\n\t" \
  "s_add_u32 m0, s63, 22528\n\t" \
  "s_mov_b32 s60, 4063232\n\t" \
  "buffer_load_dwordx4 v16, %[W], s60 offen lds\n\t" \
  "s_add_u32 m0, s63, 23552\n\t" \
  "s_mov_b32 s60, 4128768\n\t" \
  "buffer_load_dwordx4 v16, %[W], s60 offen lds\n\t" \
  "s_waitcnt vmcnt(32)\n\t" \
  "ds_read_b64_tr_b16 a[24:25], v17 offset:24576\n\t" \
  "ds_read_b64_tr_b16 a[26:27], v17 offset:25088\n\t" \
  "ds_read_b64_tr_b16 a[28:29], v17 offset:28672\n\t" \
  "ds_read_b64_tr_b16 a[30:31], v17 offset:29184\n\t" \
  "ds_read_b64_tr_b16 a[96:97], v17 offset:24584\n\t" \
  "ds_read_b64_tr_b16 a[98:99], v17 offset:25096\n\t" \
  "ds_read_b64_tr_b16 a[100:101], v17 offset:28680\n\t" \
  "ds_read_b64_tr_b16 a[102:103], v17 offset:29192\n\t" \
  "ds_read_b64_tr_b16 a[168:169], v18 offset:24576\n\t" \
  "ds_read_b64_tr_b16 a[170:171], v18 offset:25088\n\t" \
  "ds_read_b64_tr_b16 a[172:173], v18 offset:28672\n\t" \
  "ds_read_b64_tr_b16 a[174:175], v18 offset:29184\n\t" \
  "ds_read_b64_tr_b16 a[240:241], v18 offset:24584\n\t" \
  "ds_read_b64_tr_b16 a[242:243], v18 offset:25096\n\t" \
  "ds_read_b64_tr_b16 a[244:245], v18 offset:28680\n\t" \
  "ds_read_b64_tr_b16 a[246:247], v18 offset:29192\n\t" \
  "s_waitcnt lgkmcnt(0)\n\t" \
  "s_add_u32 m0, s63, 24576\n\t" \
  "s_mov_b32 s60, 4194304\n\t" \
  "buffer_load_dwordx4 v16, %[W], s60 offen lds\n\t" \
  "s_add_u32 m0, s63, 25600\n\t" \
  "s_mov_b32 s60, 4259840\n\t" \
  "buffer_load_dwordx4 v16, %[W], s60 offen lds\n\t" \
  "s_add_u32 m0, s63, 26624\n\t" \
  "s_mov_b32 s60, 4325376\n\t" \
  "buffer_load_dwordx4 v16, %[W], s60 offen lds\n\t" \
  "s_add_u32 m0, s63, 27648\n\t" \
  "s_mov_b32 s60, 4390912\n\t" \
  "buffer_load_dwordx4 v16, %[W], s60 offen lds\n\t" \
  "s_add_u32 m0, s63, 28672\n\t" \
  "s_mov_b32 s60, 4456448\n\t" \
  "buffer_load_dwordx4 v16, %[W], s60 offen lds\n\t" \
  "s_add_u32 m0, s63, 29696\n\t" \
  "s_mov_b32 s60, 4521984\n\t" \
  "buffer_load_dwordx4 v16, %[W], s60 offen lds\n\t" \
  "s_add_u32 m0, s63, 30720\n\t" \
  "s_mov_b32 s60, 4587520\n\t" \
  "buffer_load_dwordx4 v16, %[W], s60 offen lds\n\t" \
  "s_add_u32 m0, s63, 31744\n\t" \
  "s_mov_b32 s60, 4653056\n\t" \
  "buffer_load_dwordx4 v16, %[W], s60 offen lds\n\t" \
  "s_waitcnt vmcnt(32)\n\t" \
  "ds_read_b64_tr_b16 a[32:33], v17 offset:32768\n\t" \
  "ds_read_b64_tr_b16 a[34:35], v17 offset:33280\n\t" \
  "ds_read_b64_tr_b16 a[36:37], v17 offset:36864\n\t" \
  "ds_read_b64_tr_b16 a[38:39], v17 offset:37376\n\t" \
  "ds_read_b64_tr_b16 a[104:105], v17 offset:32776\n\t" \
  "ds_read_b64_tr_b16 a[106:107], v17 offset:33288\n\t" \
  "ds_read_b64_tr_b16 a[108:109], v17 offset:36872\n\t" \
  "ds_read_b64_tr_b16 a[110:111], v17 offset:37384\n\t" \
  "ds_read_b64_tr_b16 a[176:177], v18 offset:32768\n\t" \
  "ds_read_b64_tr_b16 a[178:179], v18 offset:33280\n\t" \
  "ds_read_b64_tr_b16 a[180:181], v18 offset:36864\n\t" \
  "ds_read_b64_tr_b16 a[182:183], v18 offset:37376\n\t" \
  "ds_read_b64_tr_b16 a[248:249], v18 offset:32776\n\t" \
  "ds_read_b64_tr_b16 a[250:251], v18 offset:33288\n\t" \
  "ds_read_b64_tr_b16 a[252:253], v18 offset:36872\n\t" \
  "ds_read_b64_tr_b16 a[254:255], v18 offset:37384\n\t" \
  "s_waitcnt lgkmcnt(0)\n\t" \
  "s_waitcnt vmcnt(24)\n\t" \
  "ds_read_b64_tr_b16 a[40:41], v17 offset:0\n\t" \
  "ds_read_b64_tr_b16 a[42:43], v17 offset:512\n\t" \
  "ds_read_b64_tr_b16 a[44:45], v17 offset:4096\n\t" \
  "ds_read_b64_tr_b16 a[46:47], v17 offset:4608\n\t" \
  "ds_read_b64_tr_b16 a[112:113], v17 offset:8\n\t" \
  "ds_read_b64_tr_b16 a[114:115], v17 offset:520\n\t" \
  "ds_read_b64_tr_b16 a[116:117], v17 offset:4104\n\t" \
  "ds_read_b64_tr_b16 a[118:119], v17 offset:4616\n\t" \
  "ds_read_b64_tr_b16 a[184:185], v18 offset:0\n\t" \
  "ds_read_b64_tr_b16 a[186:187], v18 offset:512\n\t" \
  "ds_read_b64_tr_b16 a[188:189], v18 offset:4096\n\t" \
  "ds_read_b64_tr_b16 a[190:191], v18 offset:4608\n\t" \
  "ds_read_b64_tr_b16 v[224:225], v18 offset:8\n\t" \
  "ds_read_b64_tr_b16 v[226:227], v18 offset:520\n\t" \
  "ds_read_b64_tr_b16 v[228:229], v18 offset:4104\n\t" \
  "ds_read_b64_tr_b16 v[230:231], v18 offset:4616\n\t" \
  "s_waitcnt lgkmcnt(0)\n\t" \
  "s_waitcnt vmcnt(16)\n\t" \
  "ds_read_b64_tr_b16 a[48:49], v17 offset:8192\n\t" \
  "ds_read_b64_tr_b16 a[50:51], v17 offset:8704\n\t" \
  "ds_read_b64_tr_b16 a[52:53], v17 offset:12288\n\t" \
  "ds_read_b64_tr_b16 a[54:55], v17 offset:12800\n\t" \
  "ds_read_b64_tr_b16 a[120:121], v17 offset:8200\n\t" \
  "ds_read_b64_tr_b16 a[122:123], v17 offset:8712\n\t" \
  "ds_read_b64_tr_b16 a[124:125], v17 offset:12296\n\t" \
  "ds_read_b64_tr_b16 a[126:127], v17 offset:12808\n\t" \
  "ds_read_b64_tr_b16 a[192:193], v18 offset:8192\n\t" \
  "ds_read_b64_tr_b16 a[194:195], v18 offset:8704\n\t" \
  "ds_read_b64_tr_b16 a[196:197], v18 offset:12288\n\t" \
  "ds_read_b64_tr_b16 a[198:199], v18 offset:12800\n\t" \
  "ds_read_b64_tr_b16 v[232:233], v18 offset:8200\n\t" \
  "ds_read_b64_tr_b16 v[234:235], v18 offset:8712\n\t" \
  "ds_read_b64_tr_b16 v[236:237], v18 offset:12296\n\t" \
  "ds_read_b64_tr_b16 v[238:239], v18 offset:12808\n\t" \
  "s_waitcnt lgkmcnt(0)\n\t" \
  "s_waitcnt vmcnt(8)\n\t" \
  "ds_read_b64_tr_b16 a[56:57], v17 offset:16384\n\t" \
  "ds_read_b64_tr_b16 a[58:59], v17 offset:16896\n\t" \
  "ds_read_b64_tr_b16 a[60:61], v17 offset:20480\n\t" \
  "ds_read_b64_tr_b16 a[62:63], v17 offset:20992\n\t" \
  "ds_read_b64_tr_b16 a[128:129], v17 offset:16392\n\t" \
  "ds_read_b64_tr_b16 a[130:131], v17 offset:16904\n\t" \
  "ds_read_b64_tr_b16 a[132:133], v17 offset:20488\n\t" \
  "ds_read_b64_tr_b16 a[134:135], v17 offset:21000\n\t" \
  "ds_read_b64_tr_b16 a[200:201], v18 offset:16384\n\t" \
  "ds_read_b64_tr_b16 a[202:203], v18 offset:16896\n\t" \
  "ds_read_b64_tr_b16 a[204:205], v18 offset:20480\n\t" \
  "ds_read_b64_tr_b16 a[206:207], v18 offset:20992\n\t" \
  "ds_read_b64_tr_b16 v[240:241], v18 offset:16392\n\t" \
  "ds_read_b64_tr_b16 v[242:243], v18 offset:16904\n\t" \
  "ds_read_b64_tr_b16 v[244:245], v18 offset:20488\n\t" \
  "ds_read_b64_tr_b16 v[246:247], v18 offset:21000\n\t" \
  "s_waitcnt lgkmcnt(0)\n\t" \
  "s_waitcnt vmcnt(0)\n\t" \
  "ds_read_b64_tr_b16 a[64:65], v17 offset:24576\n\t" \
  "ds_read_b64_tr_b16 a[66:67], v17 offset:25088\n\t" \
  "ds_read_b64_tr_b16 a[68:69], v17 offset:28672\n\t" \
  "ds_read_b64_tr_b16 a[70:71], v17 offset:29184\n\t" \
  "ds_read_b64_tr_b16 a[136:137], v17 offset:24584\n\t" \
  "ds_read_b64_tr_b16 a[138:139], v17 offset:25096\n\t" \
  "ds_read_b64_tr_b16 a[140:141], v17 offset:28680\n\t" \
  "ds_read_b64_tr_b16 a[142:143], v17 offset:29192\n\t" \
  "ds_read_b64_tr_b16 a[208:209], v18 offset:24576\n\t" \
  "ds_read_b64_tr_b16 a[210:211], v18 offset:25088\n\t" \
  "ds_read_b64_tr_b16 a[212:213], v18 offset:28672\n\t" \
  "ds_read_b64_tr_b16 a[214:215], v18 offset:29184\n\t" \
  "ds_read_b64_tr_b16 v[248:249], v18 offset:24584\n\t" \
  "ds_read_b64_tr_b16 v[250:251], v18 offset:25096\n\t" \
  "ds_read_b64_tr_b16 v[252:253], v18 offset:28680\n\t" \
  "ds_read_b64_tr_b16 v[254:255], v18 offset:29192\n\t" \
  "s_waitcnt lgkmcnt(0)\n\t" \
  "s_mov_b32 s84, " TK_CAP "\n\t" \
  "s_mov_b32 s48, 0\n\t" \
  "s_mov_b32 s49, 0xf0000000\n\t" \
  "s_mov_b32 s46, s41\n\t" \
  "s_mov_b32 s56, 0\n\t" \
  "v_lshrrev_b32 v7, 2, v0\n\t" \
  "s_mov_b32 s60, " TK_CTAB "\n\t" \
  "buffer_load_dword v4, v7, %[X], s60 offen offset:88 sc1\n\t" \
  "buffer_load_dword v7, v7, %[X], s60 offen offset:0 sc1\n\t" \
  "s_waitcnt vmcnt(0)\n\t" \
  "v_and_b32 v4, 1, v4\n\t" \
  "v_and_b32 v7, 1, v7\n\t" \
  "v_cmp_ne_u32 s[90:91], s67, v4\n\t" \
  "v_cmp_ne_u32 s[88:89], s67, v7\n\t" \
  "s_nop 1\n\t" \
  "s_add_u32 s83, s48, 0\n\t" \
  "s_lshr_b64 s[60:61], s[90:91], s83\n\t" \
  "s_and_b32 s60, s60, 1\n\t" \
  "s_lshl_b32 s60, s60, 13\n\t" \
  "s_add_u32 s83, s83, " TK_SG0 "\n\t" \
  "s_lshl_b32 s83, s83, 21\n\t" \
  "s_or_b32 s83, s83, s60\n\t" \
  "s_add_u32 s62, s83, s39\n\t" \
  "v_add_u32 v7, s62, v3\n\t" \
  "LCOLD_%=:\n\t" \
  "s_waitcnt vmcnt(0)\n\t" \
  "s_mov_b64 exec, 1\n\t" \
  "buffer_store_dword v6, off, %[X], s49 sc1\n\t" \
  "s_mov_b64 exec, -1\n\t" \
  "s_mov_b32 s49, 0xf0000000\n\t" \
  "s_add_u32 s55, s83, s36\n\t" \
  "s_cmp_lg_u32 s40, 0\n\t" \
  "s_cbranch_scc1 LGO_%=\n\t" \
  "s_mov_b32 s61, 0\n\t" \
  "LPH_%=:\n\t" \
  "buffer_load_dword v4, v7, %[X], 0 offen sc1\n\t" \
  "s_waitcnt vmcnt(0)\n\t" \
  "v_cmp_eq_u32 vcc, 0, v4\n\t" \
  "s_cbranch_vccz LPH_%=x\n\t" \
  "s_add_u32 s61, s61, 1\n\t" \
  "s_sleep " TK_PSH "\n\t" \
  "s_cmp_gt_u32 s61, s84\n\t" \
  "s_cbranch_scc1 LPH_%=g\n\t" \
  "s_and_b32 s60, s61, 1023\n\t" \
  "s_cmp_lg_u32 s60, 0\n\t" \
  "s_cbranch_scc1 LPH_%=\n\t" \
  "buffer_load_dword v4, off, %[X], s92 sc1\n\t" \
  "s_waitcnt vmcnt(0)\n\t" \
  "v_readfirstlane_b32 s60, v4\n\t" \
  "s_nop 0\n\t" \
  "s_cmp_eq_u32 s60, 0\n\t" \
  "s_cbranch_scc1 LPH_%=\n\t" \
  "LPH_%=g:\n\t" \
  "s_mov_b32 s84, 0\n\t" \
  "s_or_b32 s60, s48, 1610612736\n\t" \
  "v_mov_b32 v4, s60\n\t" \
  "s_mov_b64 exec, 1\n\t" \
  "buffer_store_dword v4, off, %[X], s92 sc1\n\t" \
  "s_mov_b64 exec, -1\n\t" \
  "LPH_%=x:\n\t" \
  "s_add_i32 s62, s48, s42\n\t" \
  "s_cmp_lt_i32 s62, 1\n\t" \
  "s_cbranch_scc1 LGO_%=\n\t" \
  "s_mul_i32 s63, s62, 43\n\t" \
  "s_lshr_b32 s63, s63, 7\n\t" \
  "s_mul_i32 s62, s62, 688128\n\t" \
  "s_lshl_b32 s60, s63, 15\n\t" \
  "s_add_u32 s62, s62, s60\n\t" \
  "s_lshr_b32 s60, s88, s63\n\t" \
  "s_and_b32 s60, s60, 1\n\t" \
  "s_lshl_b32 s60, s60, 13\n\t" \
  "s_add_u32 s62, s62, s60\n\t" \
  "s_add_u32 s62, s62, 660992\n\t" \
  "s_movk_i32 s63, 4000\n\t" \
  "LGC_%=:\n\t" \
  "buffer_load_dword v4, off, %[X], s62 sc1\n\t" \
  "s_memrealtime s[52:53]\n\t" \
  "s_waitcnt vmcnt(0) lgkmcnt(0)\n\t" \
  "v_readfirstlane_b32 s60, v4\n\t" \
  "s_sub_u32 s63, s63, 1\n\t" \
  "s_cbranch_scc1 LGO_%=\n\t" \
  "s_cmp_lg_u32 s60, -1\n\t" \
  "s_cbranch_scc1 LGC_%=v\n\t" \
  "s_sleep " TK_PSG "\n\t" \
  "s_branch LGC_%=\n\t" \
  "LGC_%=v:\n\t" \
  "s_not_b32 s60, s60\n\t" \
  "s_lshl_b32 s61, s52, 8\n\t" \
  "s_sub_u32 s60, s60, s61\n\t" \
  "s_ashr_i32 s60, s60, 8\n\t" \
  "s_sub_i32 s60, s60, " TK_QXC "\n\t" \
  "s_min_i32 s60, s60, 400\n\t" \
  "s_add_u32 s54, s52, s60\n\t" \
  "LGC_%=w:\n\t" \
  "s_memrealtime s[52:53]\n\t" \
  "s_waitcnt lgkmcnt(0)\n\t" \
  "s_sub_i32 s53, s52, s54\n\t" \
  "s_cmp_lt_i32 s53, 0\n\t" \
  "s_cbranch_scc1 LGC_%=w\n\t" \
  "LGO_%=:\n\t" \
  "s_barrier\n\t" \
  "s_add_u32 m0, s46, 0\n\t" \
  "s_add_u32 s47, s55, 0\n\t" \
  "buffer_load_dwordx4 v0, %[X], s47 offen sc1 lds\n\t" \
  "s_add_u32 m0, s46, 4096\n\t" \
  "s_add_u32 s47, s55, 4096\n\t" \
  "buffer_load_dwordx4 v0, %[X], s47 offen sc1 lds\n\t" \
  "s_add_u32 m0, s46, 8192\n\t" \
  "s_add_u32 s47, s55, 16384\n\t" \
  "buffer_load_dwordx4 v0, %[X], s47 offen sc1 lds\n\t" \
  "s_add_u32 m0, s46, 12288\n\t" \
  "s_add_u32 s47, s55, 20480\n\t" \
  "buffer_load_dwordx4 v0, %[X], s47 offen sc1 lds\n\t" \
  "s_add_u32 m0, s46, 16384\n\t" \
  "s_add_u32 s47, s55, 32768\n\t" \
  "buffer_load_dwordx4 v0, %[X], s47 offen sc1 lds\n\t" \
  "s_add_u32 m0, s46, 20480\n\t" \
  "s_add_u32 s47, s55, 36864\n\t" \
  "buffer_load_dwordx4 v0, %[X], s47 offen sc1 lds\n\t" \
  "s_add_u32 m0, s46, 24576\n\t" \
  "s_add_u32 s47, s55, 49152\n\t" \
  "buffer_load_dwordx4 v0, %[X], s47 offen sc1 lds\n\t" \
  "s_add_u32 m0, s46, 28672\n\t" \
  "s_add_u32 s47, s55, 53248\n\t" \
  "buffer_load_dwordx4 v0, %[X], s47 offen sc1 lds\n\t" \
  "s_add_u32 m0, s46, 32768\n\t" \
  "s_add_u32 s47, s55, 65536\n\t" \
  "buffer_load_dwordx4 v0, %[X], s47 offen sc1 lds\n\t" \
  "s_waitcnt vmcnt(5)\n\t" \
  "LTOP_%=:\n\t" \
  "s_barrier\n\t" \
  TK_PQ1 \
  "ds_read_b128 v[64:67], v1 offset:0\n\t" \
  "ds_read_b128 v[68:71], v1 offset:2048\n\t" \
  "ds_read_b128 v[72:75], v2 offset:0\n\t" \
  "ds_read_b128 v[76:79], v2 offset:2048\n\t" \
  "ds_read_b128 v[80:83], v1 offset:4096\n\t" \
  "ds_read_b128 v[84:87], v1 offset:6144\n\t" \
  "s_mov_b32 s82, s83\n\t" \
  "s_xor_b32 s46, s46, 0x10000\n\t" \
  "s_waitcnt lgkmcnt(5)\n\t" \
  "v_mfma_f32_16x16x32_bf16 v[16:19], a[0:3], v[64:67], 0\n\t" \
  "ds_read_b128 v[88:91], v2 offset:4096\n\t" \
  "v_mfma_f32_16x16x32_bf16 v[20:23], a[72:75], v[64:67], 0\n\t" \
  "s_add_u32 s83, s48, 1\n\t" \
  "s_lshr_b64 s[60:61], s[90:91], s83\n\t" \
  TK_QNP \
  "v_mfma_f32_16x16x32_bf16 v[24:27], a[144:147], v[64:67], 0\n\t" \
  "ds_read_b128 v[92:95], v2 offset:6144\n\t" \
  "v_mfma_f32_16x16x32_bf16 v[28:31], a[216:219], v[64:67], 0\n\t" \
  "s_and_b32 s60, s60, 1\n\t" \
  "s_lshl_b32 s60, s60, 13\n\t" \
  TK_QNP \
  "s_waitcnt lgkmcnt(5)\n\t" \
  "v_mfma_f32_16x16x32_bf16 v[16:19], a[4:7], v[72:75], v[16:19]\n\t" \
  "ds_read_b128 v[96:99], v1 offset:8192\n\t" \
  "v_mfma_f32_16x16x32_bf16 v[20:23], a[76:79], v[72:75], v[20:23]\n\t" \
  "s_add_u32 s83, s83, " TK_SG0 "\n\t" \
  "s_lshl_b32 s83, s83, 21\n\t" \
  TK_QNP \
  "v_mfma_f32_16x16x32_bf16 v[24:27], a[148:151], v[72:75], v[24:27]\n\t" \
  "ds_read_b128 v[100:103], v1 offset:10240\n\t" \
  "v_mfma_f32_16x16x32_bf16 v[28:31], a[220:223], v[72:75], v[28:31]\n\t" \
  "s_or_b32 s83, s83, s60\n\t" \
  "s_add_u32 s62, s83, s39\n\t" \
  TK_QNP \
  "s_waitcnt lgkmcnt(5)\n\t" \
  "v_mfma_f32_16x16x32_bf16 v[16:19], a[8:11], v[80:83], v[16:19]\n\t" \
  "ds_read_b128 v[104:107], v2 offset:8192\n\t" \
  "v_mfma_f32_16x16x32_bf16 v[20:23], a[80:83], v[80:83], v[20:23]\n\t" \
  "v_add_u32 v7, s62, v3\n\t" \
  "s_add_u32 s50, s82, s37\n\t" \
  TK_QNP \
  "v_mfma_f32_16x16x32_bf16 v[24:27], a[152:155], v[80:83], v[24:27]\n\t" \
  "ds_read_b128 v[108:111], v2 offset:10240\n\t" \
  "v_mfma_f32_16x16x32_bf16 v[28:31], a[224:227], v[80:83], v[28:31]\n\t" \
  "s_add_u32 s51, s50, 4096\n\t" \
  TK_QNP \
  "s_waitcnt lgkmcnt(5)\n\t" \
  "v_mfma_f32_16x16x32_bf16 v[16:19], a[12:15], v[88:91], v[16:19]\n\t" \
  "ds_read_b128 v[112:115], v1 offset:12288\n\t" \
  "v_mfma_f32_16x16x32_bf16 v[20:23], a[84:87], v[88:91], v[20:23]\n\t" \
  TK_QNP \
  "v_mfma_f32_16x16x32_bf16 v[24:27], a[156:159], v[88:91], v[24:27]\n\t" \
  "ds_read_b128 v[116:119], v1 offset:14336\n\t" \
  "v_mfma_f32_16x16x32_bf16 v[28:31], a[228:231], v[88:91], v[28:31]\n\t" \
  TK_QNP \
  "s_waitcnt lgkmcnt(5)\n\t" \
  "v_mfma_f32_16x16x32_bf16 v[16:19], a[16:19], v[96:99], v[16:19]\n\t" \
  "ds_read_b128 v[120:123], v2 offset:12288\n\t" \
  "v_mfma_f32_16x16x32_bf16 v[20:23], a[88:91], v[96:99], v[20:23]\n\t" \
  "s_cmp_lg_u32 s56, 0\n\t" \
  TK_QNP \
  "v_mfma_f32_16x16x32_bf16 v[24:27], a[160:163], v[96:99], v[24:27]\n\t" \
  "ds_read_b128 v[124:127], v2 offset:14336\n\t" \
  "v_mfma_f32_16x16x32_bf16 v[28:31], a[232:235], v[96:99], v[28:31]\n\t" \
  "s_cbranch_scc1 LWARM_%=\n\t" \
  "s_waitcnt vmcnt(0)\n\t" \
  "LWARM_%=:\n\t" \
  "s_barrier\n\t" \
  TK_QNP \
  "s_waitcnt lgkmcnt(5)\n\t" \
  "v_mfma_f32_16x16x32_bf16 v[16:19], a[20:23], v[104:107], v[16:19]\n\t" \
  "ds_read_b128 v[128:131], v1 offset:16384\n\t" \
  "v_mfma_f32_16x16x32_bf16 v[20:23], a[92:95], v[104:107], v[20:23]\n\t" \
  "buffer_load_dword v4, v7, %[X], 0 offen sc1\n\t" \
  TK_QNP \
  "v_mfma_f32_16x16x32_bf16 v[24:27], a[164:167], v[104:107], v[24:27]\n\t" \
  "ds_read_b128 v[132:135], v1 offset:18432\n\t" \
  "v_mfma_f32_16x16x32_bf16 v[28:31], a[236:239], v[104:107], v[28:31]\n\t" \
  TK_QNP \
  "s_waitcnt lgkmcnt(5)\n\t" \
  "v_mfma_f32_16x16x32_bf16 v[16:19], a[24:27], v[112:115], v[16:19]\n\t" \
  "ds_read_b128 v[136:139], v2 offset:16384\n\t" \
  "v_mfma_f32_16x16x32_bf16 v[20:23], a[96:99], v[112:115], v[20:23]\n\t" \
  TK_QNP \
  "v_mfma_f32_16x16x32_bf16 v[24:27], a[168:171], v[112:115], v[24:27]\n\t" \
  "ds_read_b128 v[140:143], v2 offset:18432\n\t" \
  "v_mfma_f32_16x16x32_bf16 v[28:31], a[240:243], v[112:115], v[28:31]\n\t" \
  TK_QNP \
  "s_waitcnt lgkmcnt(5)\n\t" \
  "v_mfma_f32_16x16x32_bf16 v[16:19], a[28:31], v[120:123], v[16:19]\n\t" \
  "ds_read_b128 v[144:147], v1 offset:20480\n\t" \
  "v_mfma_f32_16x16x32_bf16 v[20:23], a[100:103], v[120:123], v[20:23]\n\t" \
  TK_QNP \
  "v_mfma_f32_16x16x32_bf16 v[24:27], a[172:175], v[120:123], v[24:27]\n\t" \
  "ds_read_b128 v[148:151], v1 offset:22528\n\t" \
  "v_mfma_f32_16x16x32_bf16 v[28:31], a[244:247], v[120:123], v[28:31]\n\t" \
  TK_QNP \
  "s_waitcnt lgkmcnt(5)\n\t" \
  "v_mfma_f32_16x16x32_bf16 v[16:19], a[32:35], v[128:131], v[16:19]\n\t" \
  "ds_read_b128 v[152:155], v2 offset:20480\n\t" \
  "v_mfma_f32_16x16x32_bf16 v[20:23], a[104:107], v[128:131], v[20:23]\n\t" \
  TK_QNP \
  "v_mfma_f32_16x16x32_bf16 v[24:27], a[176:179], v[128:131], v[24:27]\n\t" \
  "ds_read_b128 v[156:159], v2 offset:22528\n\t" \
  "v_mfma_f32_16x16x32_bf16 v[28:31], a[248:251], v[128:131], v[28:31]\n\t" \
  TK_QNP \
  "s_waitcnt lgkmcnt(5)\n\t" \
  "v_mfma_f32_16x16x32_bf16 v[16:19], a[36:39], v[136:139], v[16:19]\n\t" \
  "ds_read_b128 v[160:163], v1 offset:24576\n\t" \
  "v_mfma_f32_16x16x32_bf16 v[20:23], a[108:111], v[136:139], v[20:23]\n\t" \
  TK_QNP \
  "v_mfma_f32_16x16x32_bf16 v[24:27], a[180:183], v[136:139], v[24:27]\n\t" \
  "ds_read_b128 v[164:167], v1 offset:26624\n\t" \
  "v_mfma_f32_16x16x32_bf16 v[28:31], a[252:255], v[136:139], v[28:31]\n\t" \
  TK_QNP \
  "s_waitcnt lgkmcnt(5)\n\t" \
  "v_mfma_f32_16x16x32_bf16 v[16:19], a[40:43], v[144:147], v[16:19]\n\t" \
  "ds_read_b128 v[168:171], v2 offset:24576\n\t" \
  "v_mfma_f32_16x16x32_bf16 v[20:23], a[112:115], v[144:147], v[20:23]\n\t" \
  TK_QNP \
  "v_mfma_f32_16x16x32_bf16 v[24:27], a[184:187], v[144:147], v[24:27]\n\t" \
  "ds_read_b128 v[172:175], v2 offset:26624\n\t" \
  "v_mfma_f32_16x16x32_bf16 v[28:31], v[224:227], v[144:147], v[28:31]\n\t" \
  TK_QNP \
  "s_waitcnt lgkmcnt(5)\n\t" \
  "v_mfma_f32_16x16x32_bf16 v[16:19], a[44:47], v[152:155], v[16:19]\n\t" \
  "ds_read_b128 v[176:179], v1 offset:28672\n\t" \
  "v_mfma_f32_16x16x32_bf16 v[20:23], a[116:119], v[152:155], v[20:23]\n\t" \
  TK_QNP \
  "v_mfma_f32_16x16x32_bf16 v[24:27], a[188:191], v[152:155], v[24:27]\n\t" \
  "ds_read_b128 v[180:183], v1 offset:30720\n\t" \
  "v_mfma_f32_16x16x32_bf16 v[28:31], v[228:231], v[152:155], v[28:31]\n\t" \
  "s_waitcnt vmcnt(0)\n\t" \
  "v_cmp_eq_u32 vcc, 0, v4\n\t" \
  TK_QNP \
  "s_waitcnt lgkmcnt(5)\n\t" \
  "v_mfma_f32_16x16x32_bf16 v[16:19], a[48:51], v[160:163], v[16:19]\n\t" \
  "ds_read_b128 v[184:187], v2 offset:28672\n\t" \
  "v_mfma_f32_16x16x32_bf16 v[20:23], a[120:123], v[160:163], v[20:23]\n\t" \
  "s_mov_b64 exec, 1\n\t" \
  "buffer_store_dword v6, off, %[X], s49 sc1\n\t" \
  "s_mov_b64 exec, -1\n\t" \
  "s_mov_b32 s49, 0xf0000000\n\t" \
  TK_PQ0 \
  TK_QNP \
  "v_mfma_f32_16x16x32_bf16 v[24:27], a[192:195], v[160:163], v[24:27]\n\t" \
  "ds_read_b128 v[188:191], v2 offset:30720\n\t" \
  "v_mfma_f32_16x16x32_bf16 v[28:31], v[232:235], v[160:163], v[28:31]\n\t" \
  "s_cmp_eq_u64 vcc, 0\n\t" \
  "s_cselect_b32 s60, 1, 0\n\t" \
  "v_mov_b32 v4, s60\n\t" \
  TK_QNP \
  "s_waitcnt lgkmcnt(5)\n\t" \
  "v_mfma_f32_16x16x32_bf16 v[16:19], a[52:55], v[168:171], v[16:19]\n\t" \
  "ds_read_b128 v[192:195], v1 offset:32768\n\t" \
  "v_mfma_f32_16x16x32_bf16 v[20:23], a[124:127], v[168:171], v[20:23]\n\t" \
  "ds_write_b32 v5, v4\n\t" \
  TK_QNP \
  "v_mfma_f32_16x16x32_bf16 v[24:27], a[196:199], v[168:171], v[24:27]\n\t" \
  "ds_read_b128 v[196:199], v1 offset:34816\n\t" \
  "v_mfma_f32_16x16x32_bf16 v[28:31], v[236:239], v[168:171], v[28:31]\n\t" \
  TK_QNP \
  "s_waitcnt lgkmcnt(6)\n\t" \
  "v_mfma_f32_16x16x32_bf16 v[16:19], a[56:59], v[176:179], v[16:19]\n\t" \
  "ds_read_b128 v[200:203], v2 offset:32768\n\t" \
  "v_mfma_f32_16x16x32_bf16 v[20:23], a[128:131], v[176:179], v[20:23]\n\t" \
  TK_QNP \
  "v_mfma_f32_16x16x32_bf16 v[24:27], a[200:203], v[176:179], v[24:27]\n\t" \
  "ds_read_b128 v[204:207], v2 offset:34816\n\t" \
  "v_mfma_f32_16x16x32_bf16 v[28:31], v[240:243], v[176:179], v[28:31]\n\t" \
  TK_QNP \
  "s_waitcnt lgkmcnt(6)\n\t" \
  "v_mfma_f32_16x16x32_bf16 v[16:19], a[60:63], v[184:187], v[16:19]\n\t" \
  "v_mfma_f32_16x16x32_bf16 v[20:23], a[132:135], v[184:187], v[20:23]\n\t" \
  TK_QNP \
  "v_mfma_f32_16x16x32_bf16 v[24:27], a[204:207], v[184:187], v[24:27]\n\t" \
  "v_mfma_f32_16x16x32_bf16 v[28:31], v[244:247], v[184:187], v[28:31]\n\t" \
  "s_waitcnt lgkmcnt(0)\n\t" \
  "s_barrier\n\t" \
  TK_QNP \
  "v_mfma_f32_16x16x32_bf16 v[16:19], a[64:67], v[192:195], v[16:19]\n\t" \
  "ds_read_b32 v4, v6 offset:40959\n\t" \
  "v_xor_b32 v1, 0x10000, v1\n\t" \
  "v_xor_b32 v2, 0x10000, v2\n\t" \
  "v_mfma_f32_16x16x32_bf16 v[20:23], a[136:139], v[192:195], v[20:23]\n\t" \
  TK_QNP \
  "v_mfma_f32_16x16x32_bf16 v[24:27], a[208:211], v[192:195], v[24:27]\n\t" \
  "v_mfma_f32_16x16x32_bf16 v[28:31], v[248:251], v[192:195], v[28:31]\n\t" \
  TK_QNP \
  "v_mfma_f32_16x16x32_bf16 v[16:19], a[68:71], v[200:203], v[16:19]\n\t" \
  "v_mfma_f32_16x16x32_bf16 v[20:23], a[140:143], v[200:203], v[20:23]\n\t" \
  "s_waitcnt lgkmcnt(0)\n\t" \
  "v_readfirstlane_b32 s60, v4\n\t" \
  "s_add_u32 s55, s83, s36\n\t" \
  TK_QNP \
  "v_mfma_f32_16x16x32_bf16 v[24:27], a[212:215], v[200:203], v[24:27]\n\t" \
  "s_cmp_eq_u32 s48, 63\n\t" \
  "s_cselect_b32 s56, 0, s60\n\t" \
  "v_mfma_f32_16x16x32_bf16 v[28:31], v[252:255], v[200:203], v[28:31]\n\t" \
  "s_cmp_eq_u32 s56, 0\n\t" \
  "s_cselect_b32 s55, 0xf0000000, s55\n\t" \
  TK_QNP \
  "v_mfma_f32_16x16x32_bf16 v[32:35], a[0:3], v[68:71], 0\n\t" \
  "v_cvt_pk_bf16_f32 v48, v16, v17\n\t" \
  "v_cvt_pk_bf16_f32 v49, v18, v19\n\t" \
  "v_mfma_f32_16x16x32_bf16 v[36:39], a[72:75], v[68:71], 0\n\t" \
  "v_cvt_pk_bf16_f32 v50, v20, v21\n\t" \
  "v_cvt_pk_bf16_f32 v51, v22, v23\n\t" \
  TK_QNP \
  "v_mfma_f32_16x16x32_bf16 v[32:35], a[4:7], v[76:79], v[32:35]\n\t" \
  "v_cvt_pk_bf16_f32 v52, v24, v25\n\t" \
  "v_cvt_pk_bf16_f32 v53, v26, v27\n\t" \
  "v_mfma_f32_16x16x32_bf16 v[36:39], a[76:79], v[76:79], v[36:39]\n\t" \
  "v_cvt_pk_bf16_f32 v54, v28, v29\n\t" \
  "v_cvt_pk_bf16_f32 v55, v30, v31\n\t" \
  TK_QNP \
  "v_mfma_f32_16x16x32_bf16 v[32:35], a[8:11], v[84:87], v[32:35]\n\t" \
  "buffer_store_dwordx4 v[48:51], v0, %[X], s50 offen offset:0 sc1\n\t" \
  "v_mfma_f32_16x16x32_bf16 v[36:39], a[80:83], v[84:87], v[36:39]\n\t" \
  TK_QNP \
  "v_mfma_f32_16x16x32_bf16 v[32:35], a[12:15], v[92:95], v[32:35]\n\t" \
  "s_add_u32 m0, s46, 0\n\t" \
  "s_add_u32 s47, s55, 0\n\t" \
  "buffer_load_dwordx4 v0, %[X], s47 offen sc1 lds\n\t" \
  "v_mfma_f32_16x16x32_bf16 v[36:39], a[84:87], v[92:95], v[36:39]\n\t" \
  TK_QNP \
  "v_mfma_f32_16x16x32_bf16 v[32:35], a[16:19], v[100:103], v[32:35]\n\t" \
  "v_mfma_f32_16x16x32_bf16 v[36:39], a[88:91], v[100:103], v[36:39]\n\t" \
  "s_add_u32 m0, s46, 4096\n\t" \
  "s_add_u32 s47, s55, 4096\n\t" \
  "buffer_load_dwordx4 v0, %[X], s47 offen sc1 lds\n\t" \
  TK_QNP \
  "v_mfma_f32_16x16x32_bf16 v[32:35], a[20:23], v[108:111], v[32:35]\n\t" \
  "v_mfma_f32_16x16x32_bf16 v[36:39], a[92:95], v[108:111], v[36:39]\n\t" \
  TK_QNP \
  "v_mfma_f32_16x16x32_bf16 v[32:35], a[24:27], v[116:119], v[32:35]\n\t" \
  "s_add_u32 m0, s46, 8192\n\t" \
  "s_add_u32 s47, s55, 16384\n\t" \
  "buffer_load_dwordx4 v0, %[X], s47 offen sc1 lds\n\t" \
  "v_mfma_f32_16x16x32_bf16 v[36:39], a[96:99], v[116:119], v[36:39]\n\t" \
  TK_QNP \
  "v_mfma_f32_16x16x32_bf16 v[32:35], a[28:31], v[124:127], v[32:35]\n\t" \
  "v_mfma_f32_16x16x32_bf16 v[36:39], a[100:103], v[124:127], v[36:39]\n\t" \
  "s_add_u32 m0, s46, 12288\n\t" \
  "s_add_u32 s47, s55, 20480\n\t" \
  "buffer_load_dwordx4 v0, %[X], s47 offen sc1 lds\n\t" \
  TK_QNP \
  "v_mfma_f32_16x16x32_bf16 v[32:35], a[32:35], v[132:135], v[32:35]\n\t" \
  "v_mfma_f32_16x16x32_bf16 v[36:39], a[104:107], v[132:135], v[36:39]\n\t" \
  TK_QNP \
  "v_mfma_f32_16x16x32_bf16 v[32:35], a[36:39], v[140:143], v[32:35]\n\t" \
  "s_add_u32 m0, s46, 16384\n\t" \
  "s_add_u32 s47, s55, 32768\n\t" \
  "buffer_load_dwordx4 v0, %[X], s47 offen sc1 lds\n\t" \
  "v_mfma_f32_16x16x32_bf16 v[36:39], a[108:111], v[140:143], v[36:39]\n\t" \
  TK_QNP \
  "v_mfma_f32_16x16x32_bf16 v[32:35], a[40:43], v[148:151], v[32:35]\n\t" \
  "v_mfma_f32_16x16x32_bf16 v[36:39], a[112:115], v[148:151], v[36:39]\n\t" \
  TK_QNP \
  "v_mfma_f32_16x16x32_bf16 v[32:35], a[44:47], v[156:159], v[32:35]\n\t" \
  "buffer_store_dwordx4 v[52:55], v0, %[X], s50 offen offset:1024 sc1\n\t" \
  "v_mfma_f32_16x16x32_bf16 v[36:39], a[116:119], v[156:159], v[36:39]\n\t" \
  TK_QNP \
  "v_mfma_f32_16x16x32_bf16 v[32:35], a[48:51], v[164:167], v[32:35]\n\t" \
  "v_mfma_f32_16x16x32_bf16 v[36:39], a[120:123], v[164:167], v[36:39]\n\t" \
  "s_add_u32 m0, s46, 20480\n\t" \
  "s_add_u32 s47, s55, 36864\n\t" \
  "buffer_load_dwordx4 v0, %[X], s47 offen sc1 lds\n\t" \
  TK_QNP \
  "v_mfma_f32_16x16x32_bf16 v[32:35], a[52:55], v[172:175], v[32:35]\n\t" \
  "v_mfma_f32_16x16x32_bf16 v[36:39], a[124:127], v[172:175], v[36:39]\n\t" \
  TK_QNP \
  "v_mfma_f32_16x16x32_bf16 v[32:35], a[56:59], v[180:183], v[32:35]\n\t" \
  "s_add_u32 m0, s46, 24576\n\t" \
  "s_add_u32 s47, s55, 49152\n\t" \
  "buffer_load_dwordx4 v0, %[X], s47 offen sc1 lds\n\t" \
  "v_mfma_f32_16x16x32_bf16 v[36:39], a[128:131], v[180:183], v[36:39]\n\t" \
  TK_QNP \
  "v_mfma_f32_16x16x32_bf16 v[32:35], a[60:63], v[188:191], v[32:35]\n\t" \
  "v_mfma_f32_16x16x32_bf16 v[36:39], a[132:135], v[188:191], v[36:39]\n\t" \
  "s_add_u32 m0, s46, 28672\n\t" \
  "s_add_u32 s47, s55, 53248\n\t" \
  "buffer_load_dwordx4 v0, %[X], s47 offen sc1 lds\n\t" \
  TK_QNP \
  "v_mfma_f32_16x16x32_bf16 v[32:35], a[64:67], v[196:199], v[32:35]\n\t" \
  "v_mfma_f32_16x16x32_bf16 v[36:39], a[136:139], v[196:199], v[36:39]\n\t" \
  TK_QNP \
  "v_mfma_f32_16x16x32_bf16 v[32:35], a[68:71], v[204:207], v[32:35]\n\t" \
  "s_add_u32 m0, s46, 32768\n\t" \
  "s_add_u32 s47, s55, 65536\n\t" \
  "buffer_load_dwordx4 v0, %[X], s47 offen sc1 lds\n\t" \
  "v_mfma_f32_16x16x32_bf16 v[36:39], a[140:143], v[204:207], v[36:39]\n\t" \
  TK_QNP \
  "v_mfma_f32_16x16x32_bf16 v[40:43], a[144:147], v[68:71], 0\n\t" \
  "v_mfma_f32_16x16x32_bf16 v[44:47], a[216:219], v[68:71], 0\n\t" \
  TK_QNP \
  "v_mfma_f32_16x16x32_bf16 v[40:43], a[148:151], v[76:79], v[40:43]\n\t" \
  "v_cvt_pk_bf16_f32 v56, v32, v33\n\t" \
  "v_cvt_pk_bf16_f32 v57, v34, v35\n\t" \
  "v_mfma_f32_16x16x32_bf16 v[44:47], a[220:223], v[76:79], v[44:47]\n\t" \
  "v_cvt_pk_bf16_f32 v58, v36, v37\n\t" \
  "v_cvt_pk_bf16_f32 v59, v38, v39\n\t" \
  TK_QNP \
  "v_mfma_f32_16x16x32_bf16 v[40:43], a[152:155], v[84:87], v[40:43]\n\t" \
  "v_mfma_f32_16x16x32_bf16 v[44:47], a[224:227], v[84:87], v[44:47]\n\t" \
  TK_QNP \
  "v_mfma_f32_16x16x32_bf16 v[40:43], a[156:159], v[92:95], v[40:43]\n\t" \
  "buffer_store_dwordx4 v[56:59], v0, %[X], s51 offen offset:0 sc1\n\t" \
  "v_mfma_f32_16x16x32_bf16 v[44:47], a[228:231], v[92:95], v[44:47]\n\t" \
  TK_QNP \
  "v_mfma_f32_16x16x32_bf16 v[40:43], a[160:163], v[100:103], v[40:43]\n\t" \
  "v_mfma_f32_16x16x32_bf16 v[44:47], a[232:235], v[100:103], v[44:47]\n\t" \
  TK_QNP \
  "v_mfma_f32_16x16x32_bf16 v[40:43], a[164:167], v[108:111], v[40:43]\n\t" \
  "v_mfma_f32_16x16x32_bf16 v[44:47], a[236:239], v[108:111], v[44:47]\n\t" \
  TK_QNP \
  "v_mfma_f32_16x16x32_bf16 v[40:43], a[168:171], v[116:119], v[40:43]\n\t" \
  "v_mfma_f32_16x16x32_bf16 v[44:47], a[240:243], v[116:119], v[44:47]\n\t" \
  TK_QNP \
  "v_mfma_f32_16x16x32_bf16 v[40:43], a[172:175], v[124:127], v[40:43]\n\t" \
  "v_mfma_f32_16x16x32_bf16 v[44:47], a[244:247], v[124:127], v[44:47]\n\t" \
  TK_QNP \
  "v_mfma_f32_16x16x32_bf16 v[40:43], a[176:179], v[132:135], v[40:43]\n\t" \
  "v_mfma_f32_16x16x32_bf16 v[44:47], a[248:251], v[132:135], v[44:47]\n\t" \
  TK_QNP \
  "v_mfma_f32_16x16x32_bf16 v[40:43], a[180:183], v[140:143], v[40:43]\n\t" \
  "v_mfma_f32_16x16x32_bf16 v[44:47], a[252:255], v[140:143], v[44:47]\n\t" \
  TK_QNP \
  "v_mfma_f32_16x16x32_bf16 v[40:43], a[184:187], v[148:151], v[40:43]\n\t" \
  "v_mfma_f32_16x16x32_bf16 v[44:47], v[224:227], v[148:151], v[44:47]\n\t" \
  TK_QNP \
  "v_mfma_f32_16x16x32_bf16 v[40:43], a[188:191], v[156:159], v[40:43]\n\t" \
  "v_mfma_f32_16x16x32_bf16 v[44:47], v[228:231], v[156:159], v[44:47]\n\t" \
  TK_QNP \
  "v_mfma_f32_16x16x32_bf16 v[40:43], a[192:195], v[164:167], v[40:43]\n\t" \
  "v_mfma_f32_16x16x32_bf16 v[44:47], v[232:235], v[164:167], v[44:47]\n\t" \
  TK_QNP \
  "v_mfma_f32_16x16x32_bf16 v[40:43], a[196:199], v[172:175], v[40:43]\n\t" \
  "v_mfma_f32_16x16x32_bf16 v[44:47], v[236:239], v[172:175], v[44:47]\n\t" \
  TK_QNP \
  "v_mfma_f32_16x16x32_bf16 v[40:43], a[200:203], v[180:183], v[40:43]\n\t" \
  "v_mfma_f32_16x16x32_bf16 v[44:47], v[240:243], v[180:183], v[44:47]\n\t" \
  TK_QNP \
  "v_mfma_f32_16x16x32_bf16 v[40:43], a[204:207], v[188:191], v[40:43]\n\t" \
  "v_mfma_f32_16x16x32_bf16 v[44:47], v[244:247], v[188:191], v[44:47]\n\t" \
  TK_QNP \
  "v_mfma_f32_16x16x32_bf16 v[40:43], a[208:211], v[196:199], v[40:43]\n\t" \
  "v_mfma_f32_16x16x32_bf16 v[44:47], v[248:251], v[196:199], v[44:47]\n\t" \
  TK_QNP \
  "v_mfma_f32_16x16x32_bf16 v[40:43], a[212:215], v[204:207], v[40:43]\n\t" \
  "v_mfma_f32_16x16x32_bf16 v[44:47], v[252:255], v[204:207], v[44:47]\n\t" \
  TK_QNP \
  "s_nop 4\n\t" \
  "v_cvt_pk_bf16_f32 v60, v40, v41\n\t" \
  "v_cvt_pk_bf16_f32 v61, v42, v43\n\t" \
  "s_nop 1\n\t" \
  "v_cvt_pk_bf16_f32 v62, v44, v45\n\t" \
  "v_cvt_pk_bf16_f32 v63, v46, v47\n\t" \
  "buffer_store_dwordx4 v[60:63], v0, %[X], s51 offen offset:1024 sc1\n\t" \
  TK_PQ2 \
  "s_add_u32 s49, s82, s38\n\t" \
  "s_cmp_eq_u32 s48, 63\n\t" \
  "s_cbranch_scc1 LEND_%=\n\t" \
  "s_add_u32 s48, s48, 1\n\t" \
  "s_cmp_eq_u32 s56, 0\n\t" \
  "s_cbranch_scc1 LCOLD_%=\n\t" \
  "s_waitcnt vmcnt(0)\n\t" \
  "s_mov_b64 exec, 1\n\t" \
  "buffer_store_dword v6, off, %[X], s49 sc1\n\t" \
  "s_mov_b64 exec, -1\n\t" \
  "s_mov_b32 s49, 0xf0000000\n\t" \
  "s_cmp_lg_u32 s40, 0\n\t" \
  "s_cbranch_scc1 LTOP_%=\n\t" \
  "s_add_i32 s62, s48, s42\n\t" \
  "s_cmp_lt_i32 s62, 1\n\t" \
  "s_cbranch_scc1 LTOP_%=\n\t" \
  "s_mul_i32 s63, s62, 43\n\t" \
  "s_lshr_b32 s63, s63, 7\n\t" \
  "s_mul_i32 s62, s62, 688128\n\t" \
  "s_lshl_b32 s60, s63, 15\n\t" \
  "s_add_u32 s62, s62, s60\n\t" \
  "s_lshr_b32 s60, s88, s63\n\t" \
  "s_and_b32 s60, s60, 1\n\t" \
  "s_lshl_b32 s60, s60, 13\n\t" \
  "s_add_u32 s62, s62, s60\n\t" \
  "s_add_u32 s62, s62, 660992\n\t" \
  "s_movk_i32 s63, 4000\n\t" \
  "LGW_%=:\n\t" \
  "buffer_load_dword v4, off, %[X], s62 sc1\n\t" \
  "s_memrealtime s[52:53]\n\t" \
  "s_waitcnt vmcnt(0) lgkmcnt(0)\n\t" \
  "v_readfirstlane_b32 s60, v4\n\t" \
  "s_sub_u32 s63, s63, 1\n\t" \
  "s_cbranch_scc1 LTOP_%=\n\t" \
  "s_cmp_lg_u32 s60, -1\n\t" \
  "s_cbranch_scc1 LGW_%=v\n\t" \
  "s_sleep " TK_PSG "\n\t" \
  "s_branch LGW_%=\n\t" \
  "LGW_%=v:\n\t" \
  "s_not_b32 s60, s60\n\t" \
  "s_lshl_b32 s61, s52, 8\n\t" \
  "s_sub_u32 s60, s60, s61\n\t" \
  "s_ashr_i32 s60, s60, 8\n\t" \
  "s_sub_i32 s60, s60, " TK_QXW "\n\t" \
  "s_min_i32 s60, s60, 400\n\t" \
  "s_add_u32 s54, s52, s60\n\t" \
  "LGW_%=w:\n\t" \
  "s_memrealtime s[52:53]\n\t" \
  "s_waitcnt lgkmcnt(0)\n\t" \
  "s_sub_i32 s53, s52, s54\n\t" \
  "s_cmp_lt_i32 s53, 0\n\t" \
  "s_cbranch_scc1 LGW_%=w\n\t" \
  "s_branch LTOP_%=\n\t" \
  "LEND_%=:\n\t" \
  "s_waitcnt vmcnt(0)\n\t" \
  "s_mov_b64 exec, 1\n\t" \
  "buffer_store_dword v6, off, %[X], s49 sc1\n\t" \
  "s_mov_b64 exec, -1\n\t" \
  "s_mov_b32 s49, 0xf0000000\n\t" \
  TK_PQ0 \
  ""
#define QV_ASM \
  "v_lshrrev_b32 v16, 7, v0\n\t" \
  "v_lshlrev_b32 v16, 13, v16\n\t" \
  "v_lshrrev_b32 v19, 2, v0\n\t" \
  "v_and_b32 v19, 64, v19\n\t" \
  "v_xor_b32 v19, v19, v0\n\t" \
  "v_and_b32 v19, 0x70, v19\n\t" \
  "v_add_u32 v16, v16, v19\n\t" \
  "s_mul_i32 s63, s40, 40960\n\t" \
  "v_and_b32 v19, 0x300, v0\n\t" \
  "v_lshlrev_b32 v17, 2, v19\n\t" \
  "v_and_b32 v19, 0xc0, v0\n\t" \
  "v_lshl_add_u32 v17, v19, 1, v17\n\t" \
  "v_and_b32 v19, 0x30, v0\n\t" \
  "v_lshrrev_b32 v19, 1, v19\n\t" \
  "v_add3_u32 v17, v17, v19, s63\n\t" \
  "v_lshrrev_b32 v19, 1, v0\n\t" \
  "v_and_b32 v19, 64, v19\n\t" \
  "v_add_u32 v17, v17, v19\n\t" \
  "v_xor_b32 v18, 64, v17\n\t" \
  "s_add_u32 m0, s63, 0\n\t" \
  "s_mov_b32 s60, 0\n\t" \
  "buffer_load_dwordx4 v16, %[W], s60 offen lds\n\t" \
  "s_add_u32 m0, s63, 1024\n\t" \
  "s_mov_b32 s60, 65536\n\t" \
  "buffer_load_dwordx4 v16, %[W], s60 offen lds\n\t" \
  "s_add_u32 m0, s63, 2048\n\t" \
  "s_mov_b32 s60, 131072\n\t" \
  "buffer_load_dwordx4 v16, %[W], s60 offen lds\n\t" \
  "s_add_u32 m0, s63, 3072\n\t" \
  "s_mov_b32 s60, 196608\n\t" \
  "buffer_load_dwordx4 v16, %[W], s60 offen lds\n\t" \
  "s_add_u32 m0, s63, 4096\n\t" \
  "s_mov_b32 s60, 262144\n\t" \
  "buffer_load_dwordx4 v16, %[W], s60 offen lds\n\t" \
  "s_add_u32 m0, s63, 5120\n\t" \
  "s_mov_b32 s60, 327680\n\t" \
  "buffer_load_dwordx4 v16, %[W], s60 offen lds\n\t" \
  "s_add_u32 m0, s63, 6144\n\t" \
  "s_mov_b32 s60, 393216\n\t" \
  "buffer_load_dwordx4 v16, %[W], s60 offen lds\n\t" \
  "s_add_u32 m0, s63, 7168\n\t" \
  "s_mov_b32 s60, 458752\n\t" \
  "buffer_load_dwordx4 v16, %[W], s60 offen lds\n\t" \
  "s_add_u32 m0, s63, 8192\n\t" \
  "s_mov_b32 s60, 524288\n\t" \
  "buffer_load_dwordx4 v16, %[W], s60 offen lds\n\t" \
  "s_add_u32 m0, s63, 9216\n\t" \
  "s_mov_b32 s60, 589824\n\t" \
  "buffer_load_dwordx4 v16, %[W], s60 offen lds\n\t" \
  "s_add_u32 m0, s63, 10240\n\t" \
  "s_mov_b32 s60, 655360\n\t" \
  "buffer_load_dwordx4 v16, %[W], s60 offen lds\n\t" \
  "s_add_u32 m0, s63, 11264\n\t" \
  "s_mov_b32 s60, 720896\n\t" \
  "buffer_load_dwordx4 v16, %[W], s60 offen lds\n\t" \
  "s_add_u32 m0, s63, 12288\n\t" \
  "s_mov_b32 s60, 786432\n\t" \
  "buffer_load_dwordx4 v16, %[W], s60 offen lds\n\t" \
  "s_add_u32 m0, s63, 13312\n\t" \
  "s_mov_b32 s60, 851968\n\t" \
  "buffer_load_dwordx4 v16, %[W], s60 offen lds\n\t" \
  "s_add_u32 m0, s63, 14336\n\t" \
  "s_mov_b32 s60, 917504\n\t" \
  "buffer_load_dwordx4 v16, %[W], s60 offen lds\n\t" \
  "s_add_u32 m0, s63, 15360\n\t" \
  "s_mov_b32 s60, 983040\n\t" \
  "buffer_load_dwordx4 v16, %[W], s60 offen lds\n\t" \
  "s_add_u32 m0, s63, 16384\n\t" \
  "s_mov_b32 s60, 1048576\n\t" \
  "buffer_load_dwordx4 v16, %[W], s60 offen lds\n\t" \
  "s_add_u32 m0, s63, 17408\n\t" \
  "s_mov_b32 s60, 1114112\n\t" \
  "buffer_load_dwordx4 v16, %[W], s60 offen lds\n\t" \
  "s_add_u32 m0, s63, 18432\n\t" \
  "s_mov_b32 s60, 1179648\n\t" \
  "buffer_load_dwordx4 v16, %[W], s60 offen lds\n\t" \
  "s_add_u32 m0, s63, 19456\n\t" \
  "s_mov_b32 s60, 1245184\n\t" \
  "buffer_load_dwordx4 v16, %[W], s60 offen lds\n\t" \
  "s_add_u32 m0, s63, 20480\n\t" \
  "s_mov_b32 s60, 1310720\n\t" \
  "buffer_load_dwordx4 v16, %[W], s60 offen lds\n\t" \
  "s_add_u32 m0, s63, 21504\n\t" \
  "s_mov_b32 s60, 1376256\n\t" \
  "buffer_load_dwordx4 v16, %[W], s60 offen lds\n\t" \
  "s_add_u32 m0, s63, 22528\n\t" \
  "s_mov_b32 s60, 1441792\n\t" \
  "buffer_load_dwordx4 v16, %[W], s60 offen lds\n\t" \
  "s_add_u32 m0, s63, 23552\n\t" \
  "s_mov_b32 s60, 1507328\n\t" \
  "buffer_load_dwordx4 v16, %[W], s60 offen lds\n\t" \
  "s_add_u32 m0, s63, 24576\n\t" \
  "s_mov_b32 s60, 1572864\n\t" \
  "buffer_load_dwordx4 v16, %[W], s60 offen lds\n\t" \
  "s_add_u32 m0, s63, 25600\n\t" \
  "s_mov_b32 s60, 1638400\n\t" \
  "buffer_load_dwordx4 v16, %[W], s60 offen lds\n\t" \
  "s_add_u32 m0, s63, 26624\n\t" \
  "s_mov_b32 s60, 1703936\n\t" \
  "buffer_load_dwordx4 v16, %[W], s60 offen lds\n\t" \
  "s_add_u32 m0, s63, 27648\n\t" \
  "s_mov_b32 s60, 1769472\n\t" \
  "buffer_load_dwordx4 v16, %[W], s60 offen lds\n\t" \
  "s_add_u32 m0, s63, 28672\n\t" \
  "s_mov_b32 s60, 1835008\n\t" \
  "buffer_load_dwordx4 v16, %[W], s60 offen lds\n\t" \
  "s_add_u32 m0, s63, 29696\n\t" \
  "s_mov_b32 s60, 1900544\n\t" \
  "buffer_load_dwordx4 v16, %[W], s60 offen lds\n\t" \
  "s_add_u32 m0, s63, 30720\n\t" \
  "s_mov_b32 s60, 1966080\n\t" \
  "buffer_load_dwordx4 v16, %[W], s60 offen lds\n\t" \
  "s_add_u32 m0, s63, 31744\n\t" \
  "s_mov_b32 s60, 2031616\n\t" \
  "buffer_load_dwordx4 v16, %[W], s60 offen lds\n\t" \
  "s_add_u32 m0, s63, 32768\n\t" \
  "s_mov_b32 s60, 2097152\n\t" \
  "buffer_load_dwordx4 v16, %[W], s60 offen lds\n\t" \
  "s_add_u32 m0, s63, 33792\n\t" \
  "s_mov_b32 s60, 2162688\n\t" \
  "buffer_load_dwordx4 v16, %[W], s60 offen lds\n\t" \
  "s_add_u32 m0, s63, 34816\n\t" \
  "s_mov_b32 s60, 2228224\n\t" \
  "buffer_load_dwordx4 v16, %[W], s60 offen lds\n\t" \
  "s_add_u32 m0, s63, 35840\n\t" \
  "s_mov_b32 s60, 2293760\n\t" \
  "buffer_load_dwordx4 v16, %[W], s60 offen lds\n\t" \
  "s_add_u32 m0, s63, 36864\n\t" \
  "s_mov_b32 s60, 2359296\n\t" \
  "buffer_load_dwordx4 v16, %[W], s60 offen lds\n\t" \
  "s_add_u32 m0, s63, 37888\n\t" \
  "s_mov_b32 s60, 2424832\n\t" \
  "buffer_load_dwordx4 v16, %[W], s60 offen lds\n\t" \
  "s_add_u32 m0, s63, 38912\n\t" \
  "s_mov_b32 s60, 2490368\n\t" \
  "buffer_load_dwordx4 v16, %[W], s60 offen lds\n\t" \
  "s_add_u32 m0, s63, 39936\n\t" \
  "s_mov_b32 s60, 2555904\n\t" \
  "buffer_load_dwordx4 v16, %[W], s60 offen lds\n\t" \
  "s_waitcnt vmcnt(32)\n\t" \
  "ds_read_b64_tr_b16 a[0:1], v17 offset:0\n\t" \
  "ds_read_b64_tr_b16 a[2:3], v17 offset:512\n\t" \
  "ds_read_b64_tr_b16 a[4:5], v17 offset:4096\n\t" \
  "ds_read_b64_tr_b16 a[6:7], v17 offset:4608\n\t" \
  "ds_read_b64_tr_b16 a[72:73], v17 offset:32\n\t" \
  "ds_read_b64_tr_b16 a[74:75], v17 offset:544\n\t" \
  "ds_read_b64_tr_b16 a[76:77], v17 offset:4128\n\t" \
  "ds_read_b64_tr_b16 a[78:79], v17 offset:4640\n\t" \
  "ds_read_b64_tr_b16 a[144:145], v18 offset:0\n\t" \
  "ds_read_b64_tr_b16 a[146:147], v18 offset:512\n\t" \
  "ds_read_b64_tr_b16 a[148:149], v18 offset:4096\n\t" \
  "ds_read_b64_tr_b16 a[150:151], v18 offset:4608\n\t" \
  "ds_read_b64_tr_b16 a[216:217], v18 offset:32\n\t" \
  "ds_read_b64_tr_b16 a[218:219], v18 offset:544\n\t" \
  "ds_read_b64_tr_b16 a[220:221], v18 offset:4128\n\t" \
  "ds_read_b64_tr_b16 a[222:223], v18 offset:4640\n\t" \
  "s_waitcnt lgkmcnt(0)\n\t" \
  "s_add_u32 m0, s63, 0\n\t" \
  "s_mov_b32 s60, 2621440\n\t" \
  "buffer_load_dwordx4 v16, %[W], s60 offen lds\n\t" \
  "s_add_u32 m0, s63, 1024\n\t" \
  "s_mov_b32 s60, 2686976\n\t" \
  "buffer_load_dwordx4 v16, %[W], s60 offen lds\n\t" \
  "s_add_u32 m0, s63, 2048\n\t" \
  "s_mov_b32 s60, 2752512\n\t" \
  "buffer_load_dwordx4 v16, %[W], s60 offen lds\n\t" \
  "s_add_u32 m0, s63, 3072\n\t" \
  "s_mov_b32 s60, 2818048\n\t" \
  "buffer_load_dwordx4 v16, %[W], s60 offen lds\n\t" \
  "s_add_u32 m0, s63, 4096\n\t" \
  "s_mov_b32 s60, 2883584\n\t" \
  "buffer_load_dwordx4 v16, %[W], s60 offen lds\n\t" \
  "s_add_u32 m0, s63, 5120\n\t" \
  "s_mov_b32 s60, 2949120\n\t" \
  "buffer_load_dwordx4 v16, %[W], s60 offen lds\n\t" \
  "s_add_u32 m0, s63, 6144\n\t" \
  "s_mov_b32 s60, 3014656\n\t" \
  "buffer_load_dwordx4 v16, %[W], s60 offen lds\n\t" \
  "s_add_u32 m0, s63, 7168\n\t" \
  "s_mov_b32 s60, 3080192\n\t" \
  "buffer_load_dwordx4 v16, %[W], s60 offen lds\n\t" \
  "s_waitcnt vmcnt(32)\n\t" \
  "ds_read_b64_tr_b16 a[8:9], v17 offset:8192\n\t" \
  "ds_read_b64_tr_b16 a[10:11], v17 offset:8704\n\t" \
  "ds_read_b64_tr_b16 a[12:13], v17 offset:12288\n\t" \
  "ds_read_b64_tr_b16 a[14:15], v17 offset:12800\n\t" \
  "ds_read_b64_tr_b16 a[80:81], v17 offset:8224\n\t" \
  "ds_read_b64_tr_b16 a[82:83], v17 offset:8736\n\t" \
  "ds_read_b64_tr_b16 a[84:85], v17 offset:12320\n\t" \
  "ds_read_b64_tr_b16 a[86:87], v17 offset:12832\n\t" \
  "ds_read_b64_tr_b16 a[152:153], v18 offset:8192\n\t" \
  "ds_read_b64_tr_b16 a[154:155], v18 offset:8704\n\t" \
  "ds_read_b64_tr_b16 a[156:157], v18 offset:12288\n\t" \
  "ds_read_b64_tr_b16 a[158:159], v18 offset:12800\n\t" \
  "ds_read_b64_tr_b16 a[224:225], v18 offset:8224\n\t" \
  "ds_read_b64_tr_b16 a[226:227], v18 offset:8736\n\t" \
  "ds_read_b64_tr_b16 a[228:229], v18 offset:12320\n\t" \
  "ds_read_b64_tr_b16 a[230:231], v18 offset:12832\n\t" \
  "s_waitcnt lgkmcnt(0)\n\t" \
  "s_add_u32 m0, s63, 8192\n\t" \
  "s_mov_b32 s60, 3145728\n\t" \
  "buffer_load_dwordx4 v16, %[W], s60 offen lds\n\t" \
  "s_add_u32 m0, s63, 9216\n\t" \
  "s_mov_b32 s60, 3211264\n\t" \
  "buffer_load_dwordx4 v16, %[W], s60 offen lds\n\t" \
  "s_add_u32 m0, s63, 10240\n\t" \
  "s_mov_b32 s60, 3276800\n\t" \
  "buffer_load_dwordx4 v16, %[W], s60 offen lds\n\t" \
  "s_add_u32 m0, s63, 11264\n\t" \
  "s_mov_b32 s60, 3342336\n\t" \
  "buffer_load_dwordx4 v16, %[W], s60 offen lds\n\t" \
  "s_add_u32 m0, s63, 12288\n\t" \
  "s_mov_b32 s60, 3407872\n\t" \
  "buffer_load_dwordx4 v16, %[W], s60 offen lds\n\t" \
  "s_add_u32 m0, s63, 13312\n\t" \
  "s_mov_b32 s60, 3473408\n\t" \
  "buffer_load_dwordx4 v16, %[W], s60 offen lds\n\t" \
  "s_add_u32 m0, s63, 14336\n\t" \
  "s_mov_b32 s60, 3538944\n\t" \
  "buffer_load_dwordx4 v16, %[W], s60 offen lds\n\t" \
  "s_add_u32 m0, s63, 15360\n\t" \
  "s_mov_b32 s60, 3604480\n\t" \
  "buffer_load_dwordx4 v16, %[W], s60 offen lds\n\t" \
  "s_waitcnt vmcnt(32)\n\t" \
  "ds_read_b64_tr_b16 a[16:17], v17 offset:16384\n\t" \
  "ds_read_b64_tr_b16 a[18:19], v17 offset:16896\n\t" \
  "ds_read_b64_tr_b16 a[20:21], v17 offset:20480\n\t" \
  "ds_read_b64_tr_b16 a[22:23], v17 offset:20992\n\t" \
  "ds_read_b64_tr_b16 a[88:89], v17 offset:16416\n\t" \
  "ds_read_b64_tr_b16 a[90:91], v17 offset:16928\n\t" \
  "ds_read_b64_tr_b16 a[92:93], v17 offset:20512\n\t" \
  "ds_read_b64_tr_b16 a[94:95], v17 offset:21024\n\t" \
  "ds_read_b64_tr_b16 a[160:161], v18 offset:16384\n\t" \
  "ds_read_b64_tr_b16 a[162:163], v18 offset:16896\n\t" \
  "ds_read_b64_tr_b16 a[164:165], v18 offset:20480\n\t" \
  "ds_read_b64_tr_b16 a[166:167], v18 offset:20992\n\t" \
  "ds_read_b64_tr_b16 a[232:233], v18 offset:16416\n\t" \
  "ds_read_b64_tr_b16 a[234:235], v18 offset:16928\n\t" \
  "ds_read_b64_tr_b16 a[236:237], v18 offset:20512\n\t" \
  "ds_read_b64_tr_b16 a[238:239], v18 offset:21024\n\t" \
  "s_waitcnt lgkmcnt(0)\n\t" \
  "s_add_u32 m0, s63, 16384\n\t" \
  "s_mov_b32 s60, 3670016\n\t" \
  "buffer_load_dwordx4 v16, %[W], s60 offen lds\n\t" \
  "s_add_u32 m0, s63, 17408\n\t" \
  "s_mov_b32 s60, 3735552\n\t" \
  "buffer_load_dwordx4 v16, %[W], s60 offen lds\n\t" \
  "s_add_u32 m0, s63, 18432\n\t" \
  "s_mov_b32 s60, 3801088\n\t" \
  "buffer_load_dwordx4 v16, %[W], s60 offen lds\n\t" \
  "s_add_u32 m0, s63, 19456\n\t" \
  "s_mov_b32 s60, 3866624\n\t" \
  "buffer_load_dwordx4 v16, %[W], s60 offen lds\n\t" \
  "s_add_u32 m0, s63, 20480\n\t" \
  "s_mov_b32 s60, 3932160\n\t" \
  "buffer_load_dwordx4 v16, %[W], s60 offen lds\n\t" \
  "s_add_u32 m0, s63, 21504\n\t" \
  "s_mov_b32 s60, 3997696\n\t" \
  "buffer_load_dwordx4 v16, %[W], s60 offen lds\n\t" \
  "s_add_u32 m0, s63, 22528\n\t" \
  "s_mov_b32 s60, 4063232\n\t" \
  "buffer_load_dwordx4 v16, %[W], s60 offen lds\n\t" \
  "s_add_u32 m0, s63, 23552\n\t" \
  "s_mov_b32 s60, 4128768\n\t" \
  "buffer_load_dwordx4 v16, %[W], s60 offen lds\n\t" \
  "s_waitcnt vmcnt(32)\n\t" \
  "ds_read_b64_tr_b16 a[24:25], v17 offset:24576\n\t" \
  "ds_read_b64_tr_b16 a[26:27], v17 offset:25088\n\t" \
  "ds_read_b64_tr_b16 a[28:29], v17 offset:28672\n\t" \
  "ds_read_b64_tr_b16 a[30:31], v17 offset:29184\n\t" \
  "ds_read_b64_tr_b16 a[96:97], v17 offset:24608\n\t" \
  "ds_read_b64_tr_b16 a[98:99], v17 offset:25120\n\t" \
  "ds_read_b64_tr_b16 a[100:101], v17 offset:28704\n\t" \
  "ds_read_b64_tr_b16 a[102:103], v17 offset:29216\n\t" \
  "ds_read_b64_tr_b16 a[168:169], v18 offset:24576\n\t" \
  "ds_read_b64_tr_b16 a[170:171], v18 offset:25088\n\t" \
  "ds_read_b64_tr_b16 a[172:173], v18 offset:28672\n\t" \
  "ds_read_b64_tr_b16 a[174:175], v18 offset:29184\n\t" \
  "ds_read_b64_tr_b16 a[240:241], v18 offset:24608\n\t" \
  "ds_read_b64_tr_b16 a[242:243], v18 offset:25120\n\t" \
  "ds_read_b64_tr_b16 a[244:245], v18 offset:28704\n\t" \
  "ds_read_b64_tr_b16 a[246:247], v18 offset:29216\n\t" \
  "s_waitcnt lgkmcnt(0)\n\t" \
  "s_add_u32 m0, s63, 24576\n\t" \
  "s_mov_b32 s60, 4194304\n\t" \
  "buffer_load_dwordx4 v16, %[W], s60 offen lds\n\t" \
  "s_add_u32 m0, s63, 25600\n\t" \
  "s_mov_b32 s60, 4259840\n\t" \
  "buffer_load_dwordx4 v16, %[W], s60 offen lds\n\t" \
  "s_add_u32 m0, s63, 26624\n\t" \
  "s_mov_b32 s60, 4325376\n\t" \
  "buffer_load_dwordx4 v16, %[W], s60 offen lds\n\t" \
  "s_add_u32 m0, s63, 27648\n\t" \
  "s_mov_b32 s60, 4390912\n\t" \
  "buffer_load_dwordx4 v16, %[W], s60 offen lds\n\t" \
  "s_add_u32 m0, s63, 28672\n\t" \
  "s_mov_b32 s60, 4456448\n\t" \
  "buffer_load_dwordx4 v16, %[W], s60 offen lds\n\t" \
  "s_add_u32 m0, s63, 29696\n\t" \
  "s_mov_b32 s60, 4521984\n\t" \
  "buffer_load_dwordx4 v16, %[W], s60 offen lds\n\t" \
  "s_add_u32 m0, s63, 30720\n\t" \
  "s_mov_b32 s60, 4587520\n\t" \
  "buffer_load_dwordx4 v16, %[W], s60 offen lds\n\t" \
  "s_add_u32 m0, s63, 31744\n\t" \
  "s_mov_b32 s60, 4653056\n\t" \
  "buffer_load_dwordx4 v16, %[W], s60 offen lds\n\t" \
  "s_waitcnt vmcnt(32)\n\t" \
  "ds_read_b64_tr_b16 a[32:33], v17 offset:32768\n\t" \
  "ds_read_b64_tr_b16 a[34:35], v17 offset:33280\n\t" \
  "ds_read_b64_tr_b16 a[36:37], v17 offset:36864\n\t" \
  "ds_read_b64_tr_b16 a[38:39], v17 offset:37376\n\t" \
  "ds_read_b64_tr_b16 a[104:105], v17 offset:32800\n\t" \
  "ds_read_b64_tr_b16 a[106:107], v17 offset:33312\n\t" \
  "ds_read_b64_tr_b16 a[108:109], v17 offset:36896\n\t" \
  "ds_read_b64_tr_b16 a[110:111], v17 offset:37408\n\t" \
  "ds_read_b64_tr_b16 a[176:177], v18 offset:32768\n\t" \
  "ds_read_b64_tr_b16 a[178:179], v18 offset:33280\n\t" \
  "ds_read_b64_tr_b16 a[180:181], v18 offset:36864\n\t" \
  "ds_read_b64_tr_b16 a[182:183], v18 offset:37376\n\t" \
  "ds_read_b64_tr_b16 a[248:249], v18 offset:32800\n\t" \
  "ds_read_b64_tr_b16 a[250:251], v18 offset:33312\n\t" \
  "ds_read_b64_tr_b16 a[252:253], v18 offset:36896\n\t" \
  "ds_read_b64_tr_b16 a[254:255], v18 offset:37408\n\t" \
  "s_waitcnt lgkmcnt(0)\n\t" \
  "s_waitcnt vmcnt(24)\n\t" \
  "ds_read_b64_tr_b16 a[40:41], v17 offset:0\n\t" \
  "ds_read_b64_tr_b16 a[42:43], v17 offset:512\n\t" \
  "ds_read_b64_tr_b16 a[44:45], v17 offset:4096\n\t" \
  "ds_read_b64_tr_b16 a[46:47], v17 offset:4608\n\t" \
  "ds_read_b64_tr_b16 a[112:113], v17 offset:32\n\t" \
  "ds_read_b64_tr_b16 a[114:115], v17 offset:544\n\t" \
  "ds_read_b64_tr_b16 a[116:117], v17 offset:4128\n\t" \
  "ds_read_b64_tr_b16 a[118:119], v17 offset:4640\n\t" \
  "ds_read_b64_tr_b16 a[184:185], v18 offset:0\n\t" \
  "ds_read_b64_tr_b16 a[186:187], v18 offset:512\n\t" \
  "ds_read_b64_tr_b16 a[188:189], v18 offset:4096\n\t" \
  "ds_read_b64_tr_b16 a[190:191], v18 offset:4608\n\t" \
  "ds_read_b64_tr_b16 v[224:225], v18 offset:32\n\t" \
  "ds_read_b64_tr_b16 v[226:227], v18 offset:544\n\t" \
  "ds_read_b64_tr_b16 v[228:229], v18 offset:4128\n\t" \
  "ds_read_b64_tr_b16 v[230:231], v18 offset:4640\n\t" \
  "s_waitcnt lgkmcnt(0)\n\t" \
  "s_waitcnt vmcnt(16)\n\t" \
  "ds_read_b64_tr_b16 a[48:49], v17 offset:8192\n\t" \
  "ds_read_b64_tr_b16 a[50:51], v17 offset:8704\n\t" \
  "ds_read_b64_tr_b16 a[52:53], v17 offset:12288\n\t" \
  "ds_read_b64_tr_b16 a[54:55], v17 offset:12800\n\t" \
  "ds_read_b64_tr_b16 a[120:121], v17 offset:8224\n\t" \
  "ds_read_b64_tr_b16 a[122:123], v17 offset:8736\n\t" \
  "ds_read_b64_tr_b16 a[124:125], v17 offset:12320\n\t" \
  "ds_read_b64_tr_b16 a[126:127], v17 offset:12832\n\t" \
  "ds_read_b64_tr_b16 a[192:193], v18 offset:8192\n\t" \
  "ds_read_b64_tr_b16 a[194:195], v18 offset:8704\n\t" \
  "ds_read_b64_tr_b16 a[196:197], v18 offset:12288\n\t" \
  "ds_read_b64_tr_b16 a[198:199], v18 offset:12800\n\t" \
  "ds_read_b64_tr_b16 v[232:233], v18 offset:8224\n\t" \
  "ds_read_b64_tr_b16 v[234:235], v18 offset:8736\n\t" \
  "ds_read_b64_tr_b16 v[236:237], v18 offset:12320\n\t" \
  "ds_read_b64_tr_b16 v[238:239], v18 offset:12832\n\t" \
  "s_waitcnt lgkmcnt(0)\n\t" \
  "s_waitcnt vmcnt(8)\n\t" \
  "ds_read_b64_tr_b16 a[56:57], v17 offset:16384\n\t" \
  "ds_read_b64_tr_b16 a[58:59], v17 offset:16896\n\t" \
  "ds_read_b64_tr_b16 a[60:61], v17 offset:20480\n\t" \
  "ds_read_b64_tr_b16 a[62:63], v17 offset:20992\n\t" \
  "ds_read_b64_tr_b16 a[128:129], v17 offset:16416\n\t" \
  "ds_read_b64_tr_b16 a[130:131], v17 offset:16928\n\t" \
  "ds_read_b64_tr_b16 a[132:133], v17 offset:20512\n\t" \
  "ds_read_b64_tr_b16 a[134:135], v17 offset:21024\n\t" \
  "ds_read_b64_tr_b16 a[200:201], v18 offset:16384\n\t" \
  "ds_read_b64_tr_b16 a[202:203], v18 offset:16896\n\t" \
  "ds_read_b64_tr_b16 a[204:205], v18 offset:20480\n\t" \
  "ds_read_b64_tr_b16 a[206:207], v18 offset:20992\n\t" \
  "ds_read_b64_tr_b16 v[240:241], v18 offset:16416\n\t" \
  "ds_read_b64_tr_b16 v[242:243], v18 offset:16928\n\t" \
  "ds_read_b64_tr_b16 v[244:245], v18 offset:20512\n\t" \
  "ds_read_b64_tr_b16 v[246:247], v18 offset:21024\n\t" \
  "s_waitcnt lgkmcnt(0)\n\t" \
  "s_waitcnt vmcnt(0)\n\t" \
  "ds_read_b64_tr_b16 a[64:65], v17 offset:24576\n\t" \
  "ds_read_b64_tr_b16 a[66:67], v17 offset:25088\n\t" \
  "ds_read_b64_tr_b16 a[68:69], v17 offset:28672\n\t" \
  "ds_read_b64_tr_b16 a[70:71], v17 offset:29184\n\t" \
  "ds_read_b64_tr_b16 a[136:137], v17 offset:24608\n\t" \
  "ds_read_b64_tr_b16 a[138:139], v17 offset:25120\n\t" \
  "ds_read_b64_tr_b16 a[140:141], v17 offset:28704\n\t" \
  "ds_read_b64_tr_b16 a[142:143], v17 offset:29216\n\t" \
  "ds_read_b64_tr_b16 a[208:209], v18 offset:24576\n\t" \
  "ds_read_b64_tr_b16 a[210:211], v18 offset:25088\n\t" \
  "ds_read_b64_tr_b16 a[212:213], v18 offset:28672\n\t" \
  "ds_read_b64_tr_b16 a[214:215], v18 offset:29184\n\t" \
  "ds_read_b64_tr_b16 v[248:249], v18 offset:24608\n\t" \
  "ds_read_b64_tr_b16 v[250:251], v18 offset:25120\n\t" \
  "ds_read_b64_tr_b16 v[252:253], v18 offset:28704\n\t" \
  "ds_read_b64_tr_b16 v[254:255], v18 offset:29216\n\t" \
  "s_waitcnt lgkmcnt(0)\n\t" \
  "s_mov_b32 s84, " TK_CAP "\n\t" \
  "s_mov_b32 s48, 0\n\t" \
  "s_mov_b32 s49, 0xf0000000\n\t" \
  "s_mov_b32 s46, s41\n\t" \
  "s_mov_b32 s56, 0\n\t" \
  "v_lshrrev_b32 v7, 2, v0\n\t" \
  "s_mov_b32 s60, " TK_CTAB "\n\t" \
  "buffer_load_dword v4, v7, %[X], s60 offen offset:88 sc1\n\t" \
  "buffer_load_dword v7, v7, %[X], s60 offen offset:0 sc1\n\t" \
  "s_waitcnt vmcnt(0)\n\t" \
  "v_and_b32 v4, 1, v4\n\t" \
  "v_and_b32 v7, 1, v7\n\t" \
  "v_cmp_ne_u32 s[90:91], s67, v4\n\t" \
  "v_cmp_ne_u32 s[88:89], s67, v7\n\t" \
  "s_nop 1\n\t" \
  "s_add_u32 s83, s48, 0\n\t" \
  "s_lshr_b64 s[60:61], s[90:91], s83\n\t" \
  "s_and_b32 s60, s60, 1\n\t" \
  "s_lshl_b32 s60, s60, 13\n\t" \
  "s_add_u32 s83, s83, " TK_SG0 "\n\t" \
  "s_lshl_b32 s83, s83, 21\n\t" \
  "s_or_b32 s83, s83, s60\n\t" \
  "s_add_u32 s62, s83, s39\n\t" \
  "v_add_u32 v7, s62, v3\n\t" \
  "LCOLD_%=:\n\t" \
  "s_waitcnt vmcnt(0)\n\t" \
  "s_mov_b64 exec, 1\n\t" \
  "buffer_store_dword v6, off, %[X], s49 sc1\n\t" \
  "s_mov_b64 exec, -1\n\t" \
  "s_mov_b32 s49, 0xf0000000\n\t" \
  "s_add_u32 s55, s83, s36\n\t" \
  "s_cmp_lg_u32 s40, 0\n\t" \
  "s_cbranch_scc1 LGO_%=\n\t" \
  "s_mov_b32 s61, 0\n\t" \
  "LPH_%=:\n\t" \
  "buffer_load_dword v4, v7, %[X], 0 offen sc1\n\t" \
  "s_waitcnt vmcnt(0)\n\t" \
  "v_cmp_eq_u32 vcc, 0, v4\n\t" \
  "s_cbranch_vccz LPH_%=x\n\t" \
  "s_add_u32 s61, s61, 1\n\t" \
  "s_sleep " TK_PSH "\n\t" \
  "s_cmp_gt_u32 s61, s84\n\t" \
  "s_cbranch_scc1 LPH_%=g\n\t" \
  "s_and_b32 s60, s61, 1023\n\t" \
  "s_cmp_lg_u32 s60, 0\n\t" \
  "s_cbranch_scc1 LPH_%=\n\t" \
  "buffer_load_dword v4, off, %[X], s92 sc1\n\t" \
  "s_waitcnt vmcnt(0)\n\t" \
  "v_readfirstlane_b32 s60, v4\n\t" \
  "s_nop 0\n\t" \
  "s_cmp_eq_u32 s60, 0\n\t" \
  "s_cbranch_scc1 LPH_%=\n\t" \
  "LPH_%=g:\n\t" \
  "s_mov_b32 s84, 0\n\t" \
  "s_or_b32 s60, s48, 1610612736\n\t" \
  "v_mov_b32 v4, s60\n\t" \
  "s_mov_b64 exec, 1\n\t" \
  "buffer_store_dword v4, off, %[X], s92 sc1\n\t" \
  "s_mov_b64 exec, -1\n\t" \
  "LPH_%=x:\n\t" \
  "s_add_i32 s62, s48, s42\n\t" \
  "s_cmp_lt_i32 s62, 1\n\t" \
  "s_cbranch_scc1 LGO_%=\n\t" \
  "s_mul_i32 s63, s62, 43\n\t" \
  "s_lshr_b32 s63, s63, 7\n\t" \
  "s_mul_i32 s62, s62, 688128\n\t" \
  "s_lshl_b32 s60, s63, 15\n\t" \
  "s_add_u32 s62, s62, s60\n\t" \
  "s_lshr_b32 s60, s88, s63\n\t" \
  "s_and_b32 s60, s60, 1\n\t" \
  "s_lshl_b32 s60, s60, 13\n\t" \
  "s_add_u32 s62, s62, s60\n\t" \
  "s_add_u32 s62, s62, 660992\n\t" \
  "s_movk_i32 s63, 4000\n\t" \
  "LGC_%=:\n\t" \
  "buffer_load_dword v4, off, %[X], s62 sc1\n\t" \
  "s_memrealtime s[52:53]\n\t" \
  "s_waitcnt vmcnt(0) lgkmcnt(0)\n\t" \
  "v_readfirstlane_b32 s60, v4\n\t" \
  "s_sub_u32 s63, s63, 1\n\t" \
  "s_cbranch_scc1 LGO_%=\n\t" \
  "s_cmp_lg_u32 s60, -1\n\t" \
  "s_cbranch_scc1 LGC_%=v\n\t" \
  "s_sleep " TK_PSG "\n\t" \
  "s_branch LGC_%=\n\t" \
  "LGC_%=v:\n\t" \
  "s_not_b32 s60, s60\n\t" \
  "s_lshl_b32 s61, s52, 8\n\t" \
  "s_sub_u32 s60, s60, s61\n\t" \
  "s_ashr_i32 s60, s60, 8\n\t" \
  "s_sub_i32 s60, s60, " TK_QXC "\n\t" \
  "s_min_i32 s60, s60, 400\n\t" \
  "s_add_u32 s54, s52, s60\n\t" \
  "LGC_%=w:\n\t" \
  "s_memrealtime s[52:53]\n\t" \
  "s_waitcnt lgkmcnt(0)\n\t" \
  "s_sub_i32 s53, s52, s54\n\t" \
  "s_cmp_lt_i32 s53, 0\n\t" \
  "s_cbranch_scc1 LGC_%=w\n\t" \
  "LGO_%=:\n\t" \
  "s_barrier\n\t" \
  "s_add_u32 m0, s46, 0\n\t" \
  "s_add_u32 s47, s55, 0\n\t" \
  "buffer_load_dwordx4 v0, %[X], s47 offen sc1 lds\n\t" \
  "s_add_u32 m0, s46, 4096\n\t" \
  "s_add_u32 s47, s55, 4096\n\t" \
  "buffer_load_dwordx4 v0, %[X], s47 offen sc1 lds\n\t" \
  "s_add_u32 m0, s46, 8192\n\t" \
  "s_add_u32 s47, s55, 16384\n\t" \
  "buffer_load_dwordx4 v0, %[X], s47 offen sc1 lds\n\t" \
  "s_add_u32 m0, s46, 12288\n\t" \
  "s_add_u32 s47, s55, 20480\n\t" \
  "buffer_load_dwordx4 v0, %[X], s47 offen sc1 lds\n\t" \
  "s_add_u32 m0, s46, 16384\n\t" \
  "s_add_u32 s47, s55, 32768\n\t" \
  "buffer_load_dwordx4 v0, %[X], s47 offen sc1 lds\n\t" \
  "s_add_u32 m0, s46, 20480\n\t" \
  "s_add_u32 s47, s55, 36864\n\t" \
  "buffer_load_dwordx4 v0, %[X], s47 offen sc1 lds\n\t" \
  "s_add_u32 m0, s46, 24576\n\t" \
  "s_add_u32 s47, s55, 49152\n\t" \
  "buffer_load_dwordx4 v0, %[X], s47 offen sc1 lds\n\t" \
  "s_add_u32 m0, s46, 28672\n\t" \
  "s_add_u32 s47, s55, 53248\n\t" \
  "buffer_load_dwordx4 v0, %[X], s47 offen sc1 lds\n\t" \
  "s_add_u32 m0, s46, 32768\n\t" \
  "s_add_u32 s47, s55, 65536\n\t" \
  "buffer_load_dwordx4 v0, %[X], s47 offen sc1 lds\n\t" \
  "s_waitcnt vmcnt(5)\n\t" \
  "LTOP_%=:\n\t" \
  "s_barrier\n\t" \
  TK_PQ1 \
  "ds_read_b128 v[64:67], v1 offset:0\n\t" \
  "ds_read_b128 v[68:71], v1 offset:2048\n\t" \
  "ds_read_b128 v[72:75], v2 offset:0\n\t" \
  "ds_read_b128 v[76:79], v2 offset:2048\n\t" \
  "ds_read_b128 v[80:83], v1 offset:4096\n\t" \
  "ds_read_b128 v[84:87], v1 offset:6144\n\t" \
  "s_mov_b32 s82, s83\n\t" \
  "s_xor_b32 s46, s46, 0x10000\n\t" \
  "s_waitcnt lgkmcnt(5)\n\t" \
  "v_mfma_f32_16x16x32_bf16 v[16:19], v[64:67], a[0:3], 0\n\t" \
  "ds_read_b128 v[88:91], v2 offset:4096\n\t" \
  "s_waitcnt lgkmcnt(5)\n\t" \
  "v_mfma_f32_16x16x32_bf16 v[20:23], v[68:71], a[0:3], 0\n\t" \
  "s_add_u32 s83, s48, 1\n\t" \
  "s_lshr_b64 s[60:61], s[90:91], s83\n\t" \
  TK_QNP \
  "v_mfma_f32_16x16x32_bf16 v[24:27], v[64:67], a[72:75], 0\n\t" \
  "ds_read_b128 v[92:95], v2 offset:6144\n\t" \
  "v_mfma_f32_16x16x32_bf16 v[28:31], v[68:71], a[72:75], 0\n\t" \
  "s_and_b32 s60, s60, 1\n\t" \
  "s_lshl_b32 s60, s60, 13\n\t" \
  TK_QNP \
  "s_waitcnt lgkmcnt(5)\n\t" \
  "v_mfma_f32_16x16x32_bf16 v[16:19], v[72:75], a[4:7], v[16:19]\n\t" \
  "ds_read_b128 v[96:99], v1 offset:8192\n\t" \
  "s_waitcnt lgkmcnt(5)\n\t" \
  "v_mfma_f32_16x16x32_bf16 v[20:23], v[76:79], a[4:7], v[20:23]\n\t" \
  "s_add_u32 s83, s83, " TK_SG0 "\n\t" \
  "s_lshl_b32 s83, s83, 21\n\t" \
  TK_QNP \
  "v_mfma_f32_16x16x32_bf16 v[24:27], v[72:75], a[76:79], v[24:27]\n\t" \
  "ds_read_b128 v[100:103], v1 offset:10240\n\t" \
  "v_mfma_f32_16x16x32_bf16 v[28:31], v[76:79], a[76:79], v[28:31]\n\t" \
  "s_or_b32 s83, s83, s60\n\t" \
  "s_add_u32 s62, s83, s39\n\t" \
  TK_QNP \
  "s_waitcnt lgkmcnt(5)\n\t" \
  "v_mfma_f32_16x16x32_bf16 v[16:19], v[80:83], a[8:11], v[16:19]\n\t" \
  "ds_read_b128 v[104:107], v2 offset:8192\n\t" \
  "s_waitcnt lgkmcnt(5)\n\t" \
  "v_mfma_f32_16x16x32_bf16 v[20:23], v[84:87], a[8:11], v[20:23]\n\t" \
  "v_add_u32 v7, s62, v3\n\t" \
  "s_add_u32 s50, s82, s37\n\t" \
  TK_QNP \
  "v_mfma_f32_16x16x32_bf16 v[24:27], v[80:83], a[80:83], v[24:27]\n\t" \
  "ds_read_b128 v[108:111], v2 offset:10240\n\t" \
  "v_mfma_f32_16x16x32_bf16 v[28:31], v[84:87], a[80:83], v[28:31]\n\t" \
  "s_add_u32 s51, s50, 4096\n\t" \
  TK_QNP \
  "s_waitcnt lgkmcnt(5)\n\t" \
  "v_mfma_f32_16x16x32_bf16 v[16:19], v[88:91], a[12:15], v[16:19]\n\t" \
  "ds_read_b128 v[112:115], v1 offset:12288\n\t" \
  "s_waitcnt lgkmcnt(5)\n\t" \
  "v_mfma_f32_16x16x32_bf16 v[20:23], v[92:95], a[12:15], v[20:23]\n\t" \
  TK_QNP \
  "v_mfma_f32_16x16x32_bf16 v[24:27], v[88:91], a[84:87], v[24:27]\n\t" \
  "ds_read_b128 v[116:119], v1 offset:14336\n\t" \
  "v_mfma_f32_16x16x32_bf16 v[28:31], v[92:95], a[84:87], v[28:31]\n\t" \
  TK_QNP \
  "s_waitcnt lgkmcnt(5)\n\t" \
  "v_mfma_f32_16x16x32_bf16 v[16:19], v[96:99], a[16:19], v[16:19]\n\t" \
  "ds_read_b128 v[120:123], v2 offset:12288\n\t" \
  "s_waitcnt lgkmcnt(5)\n\t" \
  "v_mfma_f32_16x16x32_bf16 v[20:23], v[100:103], a[16:19], v[20:23]\n\t" \
  "s_cmp_lg_u32 s56, 0\n\t" \
  TK_QNP \
  "v_mfma_f32_16x16x32_bf16 v[24:27], v[96:99], a[88:91], v[24:27]\n\t" \
  "ds_read_b128 v[124:127], v2 offset:14336\n\t" \
  "v_mfma_f32_16x16x32_bf16 v[28:31], v[100:103], a[88:91], v[28:31]\n\t" \
  "s_cbranch_scc1 LWARM_%=\n\t" \
  "s_waitcnt vmcnt(0)\n\t" \
  "LWARM_%=:\n\t" \
  "s_barrier\n\t" \
  TK_QNP \
  "s_waitcnt lgkmcnt(5)\n\t" \
  "v_mfma_f32_16x16x32_bf16 v[16:19], v[104:107], a[20:23], v[16:19]\n\t" \
  "ds_read_b128 v[128:131], v1 offset:16384\n\t" \
  "s_waitcnt lgkmcnt(5)\n\t" \
  "v_mfma_f32_16x16x32_bf16 v[20:23], v[108:111], a[20:23], v[20:23]\n\t" \
  "buffer_load_dword v4, v7, %[X], 0 offen sc1\n\t" \
  TK_QNP \
  "v_mfma_f32_16x16x32_bf16 v[24:27], v[104:107], a[92:95], v[24:27]\n\t" \
  "ds_read_b128 v[132:135], v1 offset:18432\n\t" \
  "v_mfma_f32_16x16x32_bf16 v[28:31], v[108:111], a[92:95], v[28:31]\n\t" \
  TK_QNP \
  "s_waitcnt lgkmcnt(5)\n\t" \
  "v_mfma_f32_16x16x32_bf16 v[16:19], v[112:115], a[24:27], v[16:19]\n\t" \
  "ds_read_b128 v[136:139], v2 offset:16384\n\t" \
  "s_waitcnt lgkmcnt(5)\n\t" \
  "v_mfma_f32_16x16x32_bf16 v[20:23], v[116:119], a[24:27], v[20:23]\n\t" \
  TK_QNP \
  "v_mfma_f32_16x16x32_bf16 v[24:27], v[112:115], a[96:99], v[24:27]\n\t" \
  "ds_read_b128 v[140:143], v2 offset:18432\n\t" \
  "v_mfma_f32_16x16x32_bf16 v[28:31], v[116:119], a[96:99], v[28:31]\n\t" \
  TK_QNP \
  "s_waitcnt lgkmcnt(5)\n\t" \
  "v_mfma_f32_16x16x32_bf16 v[16:19], v[120:123], a[28:31], v[16:19]\n\t" \
  "ds_read_b128 v[144:147], v1 offset:20480\n\t" \
  "s_waitcnt lgkmcnt(5)\n\t" \
  "v_mfma_f32_16x16x32_bf16 v[20:23], v[124:127], a[28:31], v[20:23]\n\t" \
  TK_QNP \
  "v_mfma_f32_16x16x32_bf16 v[24:27], v[120:123], a[100:103], v[24:27]\n\t" \
  "ds_read_b128 v[148:151], v1 offset:22528\n\t" \
  "v_mfma_f32_16x16x32_bf16 v[28:31], v[124:127], a[100:103], v[28:31]\n\t" \
  TK_QNP \
  "s_waitcnt lgkmcnt(5)\n\t" \
  "v_mfma_f32_16x16x32_bf16 v[16:19], v[128:131], a[32:35], v[16:19]\n\t" \
  "ds_read_b128 v[152:155], v2 offset:20480\n\t" \
  "s_waitcnt lgkmcnt(5)\n\t" \
  "v_mfma_f32_16x16x32_bf16 v[20:23], v[132:135], a[32:35], v[20:23]\n\t" \
  TK_QNP \
  "v_mfma_f32_16x16x32_bf16 v[24:27], v[128:131], a[104:107], v[24:27]\n\t" \
  "ds_read_b128 v[156:159], v2 offset:22528\n\t" \
  "v_mfma_f32_16x16x32_bf16 v[28:31], v[132:135], a[104:107], v[28:31]\n\t" \
  TK_QNP \
  "s_waitcnt lgkmcnt(5)\n\t" \
  "v_mfma_f32_16x16x32_bf16 v[16:19], v[136:139], a[36:39], v[16:19]\n\t" \
  "ds_read_b128 v[160:163], v1 offset:24576\n\t" \
  "s_waitcnt lgkmcnt(5)\n\t" \
  "v_mfma_f32_16x16x32_bf16 v[20:23], v[140:143], a[36:39], v[20:23]\n\t" \
  TK_QNP \
  "v_mfma_f32_16x16x32_bf16 v[24:27], v[136:139], a[108:111], v[24:27]\n\t" \
  "ds_read_b128 v[164:167], v1 offset:26624\n\t" \
  "v_mfma_f32_16x16x32_bf16 v[28:31], v[140:143], a[108:111], v[28:31]\n\t" \
  TK_QNP \
  "s_waitcnt lgkmcnt(5)\n\t" \
  "v_mfma_f32_16x16x32_bf16 v[16:19], v[144:147], a[40:43], v[16:19]\n\t" \
  "ds_read_b128 v[168:171], v2 offset:24576\n\t" \
  "s_waitcnt lgkmcnt(5)\n\t" \
  "v_mfma_f32_16x16x32_bf16 v[20:23], v[148:151], a[40:43], v[20:23]\n\t" \
  TK_QNP \
  "v_mfma_f32_16x16x32_bf16 v[24:27], v[144:147], a[112:115], v[24:27]\n\t" \
  "ds_read_b128 v[172:175], v2 offset:26624\n\t" \
  "v_mfma_f32_16x16x32_bf16 v[28:31], v[148:151], a[112:115], v[28:31]\n\t" \
  TK_QNP \
  "s_waitcnt lgkmcnt(5)\n\t" \
  "v_mfma_f32_16x16x32_bf16 v[16:19], v[152:155], a[44:47], v[16:19]\n\t" \
  "ds_read_b128 v[176:179], v1 offset:28672\n\t" \
  "s_waitcnt lgkmcnt(5)\n\t" \
  "v_mfma_f32_16x16x32_bf16 v[20:23], v[156:159], a[44:47], v[20:23]\n\t" \
  TK_QNP \
  "v_mfma_f32_16x16x32_bf16 v[24:27], v[152:155], a[116:119], v[24:27]\n\t" \
  "ds_read_b128 v[180:183], v1 offset:30720\n\t" \
  "v_mfma_f32_16x16x32_bf16 v[28:31], v[156:159], a[116:119], v[28:31]\n\t" \
  "s_waitcnt vmcnt(0)\n\t" \
  "v_cmp_eq_u32 vcc, 0, v4\n\t" \
  TK_QNP \
  "s_waitcnt lgkmcnt(5)\n\t" \
  "v_mfma_f32_16x16x32_bf16 v[16:19], v[160:163], a[48:51], v[16:19]\n\t" \
  "ds_read_b128 v[184:187], v2 offset:28672\n\t" \
  "s_waitcnt lgkmcnt(5)\n\t" \
  "v_mfma_f32_16x16x32_bf16 v[20:23], v[164:167], a[48:51], v[20:23]\n\t" \
  "s_mov_b64 exec, 1\n\t" \
  "buffer_store_dword v6, off, %[X], s49 sc1\n\t" \
  "s_mov_b64 exec, -1\n\t" \
  "s_mov_b32 s49, 0xf0000000\n\t" \
  TK_PQ0 \
  TK_QNP \
  "v_mfma_f32_16x16x32_bf16 v[24:27], v[160:163], a[120:123], v[24:27]\n\t" \
  "ds_read_b128 v[188:191], v2 offset:30720\n\t" \
  "v_mfma_f32_16x16x32_bf16 v[28:31], v[164:167], a[120:123], v[28:31]\n\t" \
  "s_cmp_eq_u64 vcc, 0\n\t" \
  "s_cselect_b32 s60, 1, 0\n\t" \
  "v_mov_b32 v4, s60\n\t" \
  TK_QNP \
  "s_waitcnt lgkmcnt(5)\n\t" \
  "v_mfma_f32_16x16x32_bf16 v[16:19], v[168:171], a[52:55], v[16:19]\n\t" \
  "ds_read_b128 v[192:195], v1 offset:32768\n\t" \
  "s_waitcnt lgkmcnt(5)\n\t" \
  "v_mfma_f32_16x16x32_bf16 v[20:23], v[172:175], a[52:55], v[20:23]\n\t" \
  "ds_write_b32 v5, v4\n\t" \
  TK_QNP \
  "v_mfma_f32_16x16x32_bf16 v[24:27], v[168:171], a[124:127], v[24:27]\n\t" \
  "ds_read_b128 v[196:199], v1 offset:34816\n\t" \
  "v_mfma_f32_16x16x32_bf16 v[28:31], v[172:175], a[124:127], v[28:31]\n\t" \
  TK_QNP \
  "s_waitcnt lgkmcnt(6)\n\t" \
  "v_mfma_f32_16x16x32_bf16 v[16:19], v[176:179], a[56:59], v[16:19]\n\t" \
  "ds_read_b128 v[200:203], v2 offset:32768\n\t" \
  "s_waitcnt lgkmcnt(6)\n\t" \
  "v_mfma_f32_16x16x32_bf16 v[20:23], v[180:183], a[56:59], v[20:23]\n\t" \
  TK_QNP \
  "v_mfma_f32_16x16x32_bf16 v[24:27], v[176:179], a[128:131], v[24:27]\n\t" \
  "ds_read_b128 v[204:207], v2 offset:34816\n\t" \
  "v_mfma_f32_16x16x32_bf16 v[28:31], v[180:183], a[128:131], v[28:31]\n\t" \
  TK_QNP \
  "s_waitcnt lgkmcnt(6)\n\t" \
  "v_mfma_f32_16x16x32_bf16 v[16:19], v[184:187], a[60:63], v[16:19]\n\t" \
  "s_waitcnt lgkmcnt(5)\n\t" \
  "v_mfma_f32_16x16x32_bf16 v[20:23], v[188:191], a[60:63], v[20:23]\n\t" \
  TK_QNP \
  "v_mfma_f32_16x16x32_bf16 v[24:27], v[184:187], a[132:135], v[24:27]\n\t" \
  "v_mfma_f32_16x16x32_bf16 v[28:31], v[188:191], a[132:135], v[28:31]\n\t" \
  "s_waitcnt lgkmcnt(0)\n\t" \
  "s_barrier\n\t" \
  TK_QNP \
  "v_mfma_f32_16x16x32_bf16 v[16:19], v[192:195], a[64:67], v[16:19]\n\t" \
  "ds_read_b32 v4, v6 offset:40959\n\t" \
  "v_xor_b32 v1, 0x10000, v1\n\t" \
  "v_xor_b32 v2, 0x10000, v2\n\t" \
  "v_mfma_f32_16x16x32_bf16 v[20:23], v[196:199], a[64:67], v[20:23]\n\t" \
  TK_QNP \
  "v_mfma_f32_16x16x32_bf16 v[24:27], v[192:195], a[136:139], v[24:27]\n\t" \
  "v_mfma_f32_16x16x32_bf16 v[28:31], v[196:199], a[136:139], v[28:31]\n\t" \
  TK_QNP \
  "v_mfma_f32_16x16x32_bf16 v[16:19], v[200:203], a[68:71], v[16:19]\n\t" \
  "v_mfma_f32_16x16x32_bf16 v[20:23], v[204:207], a[68:71], v[20:23]\n\t" \
  "s_waitcnt lgkmcnt(0)\n\t" \
  "v_readfirstlane_b32 s60, v4\n\t" \
  "s_add_u32 s55, s83, s36\n\t" \
  TK_QNP \
  "v_mfma_f32_16x16x32_bf16 v[24:27], v[200:203], a[140:143], v[24:27]\n\t" \
  "s_cmp_eq_u32 s48, 63\n\t" \
  "s_cselect_b32 s56, 0, s60\n\t" \
  "v_mfma_f32_16x16x32_bf16 v[28:31], v[204:207], a[140:143], v[28:31]\n\t" \
  "s_cmp_eq_u32 s56, 0\n\t" \
  "s_cselect_b32 s55, 0xf0000000, s55\n\t" \
  TK_QNP \
  "v_mfma_f32_16x16x32_bf16 v[32:35], v[64:67], a[144:147], 0\n\t" \
  "v_cvt_pk_bf16_f32 v48, v16, v17\n\t" \
  "v_cvt_pk_bf16_f32 v49, v18, v19\n\t" \
  "v_mfma_f32_16x16x32_bf16 v[36:39], v[68:71], a[144:147], 0\n\t" \
  "v_cvt_pk_bf16_f32 v50, v20, v21\n\t" \
  "v_cvt_pk_bf16_f32 v51, v22, v23\n\t" \
  TK_QNP \
  "v_mfma_f32_16x16x32_bf16 v[32:35], v[72:75], a[148:151], v[32:35]\n\t" \
  "v_cvt_pk_bf16_f32 v52, v24, v25\n\t" \
  "v_cvt_pk_bf16_f32 v53, v26, v27\n\t" \
  "v_mfma_f32_16x16x32_bf16 v[36:39], v[76:79], a[148:151], v[36:39]\n\t" \
  "v_cvt_pk_bf16_f32 v54, v28, v29\n\t" \
  "v_cvt_pk_bf16_f32 v55, v30, v31\n\t" \
  TK_QNP \
  "v_mfma_f32_16x16x32_bf16 v[32:35], v[80:83], a[152:155], v[32:35]\n\t" \
  "buffer_store_dwordx4 v[48:51], v0, %[X], s50 offen offset:0 sc1\n\t" \
  "v_mfma_f32_16x16x32_bf16 v[36:39], v[84:87], a[152:155], v[36:39]\n\t" \
  TK_QNP \
  "v_mfma_f32_16x16x32_bf16 v[32:35], v[88:91], a[156:159], v[32:35]\n\t" \
  "s_add_u32 m0, s46, 0\n\t" \
  "s_add_u32 s47, s55, 0\n\t" \
  "buffer_load_dwordx4 v0, %[X], s47 offen sc1 lds\n\t" \
  "v_mfma_f32_16x16x32_bf16 v[36:39], v[92:95], a[156:159], v[36:39]\n\t" \
  TK_QNP \
  "v_mfma_f32_16x16x32_bf16 v[32:35], v[96:99], a[160:163], v[32:35]\n\t" \
  "v_mfma_f32_16x16x32_bf16 v[36:39], v[100:103], a[160:163], v[36:39]\n\t" \
  "s_add_u32 m0, s46, 4096\n\t" \
  "s_add_u32 s47, s55, 4096\n\t" \
  "buffer_load_dwordx4 v0, %[X], s47 offen sc1 lds\n\t" \
  TK_QNP \
  "v_mfma_f32_16x16x32_bf16 v[32:35], v[104:107], a[164:167], v[32:35]\n\t" \
  "v_mfma_f32_16x16x32_bf16 v[36:39], v[108:111], a[164:167], v[36:39]\n\t" \
  TK_QNP \
  "v_mfma_f32_16x16x32_bf16 v[32:35], v[112:115], a[168:171], v[32:35]\n\t" \
  "s_add_u32 m0, s46, 8192\n\t" \
  "s_add_u32 s47, s55, 16384\n\t" \
  "buffer_load_dwordx4 v0, %[X], s47 offen sc1 lds\n\t" \
  "v_mfma_f32_16x16x32_bf16 v[36:39], v[116:119], a[168:171], v[36:39]\n\t" \
  TK_QNP \
  "v_mfma_f32_16x16x32_bf16 v[32:35], v[120:123], a[172:175], v[32:35]\n\t" \
  "v_mfma_f32_16x16x32_bf16 v[36:39], v[124:127], a[172:175], v[36:39]\n\t" \
  "s_add_u32 m0, s46, 12288\n\t" \
  "s_add_u32 s47, s55, 20480\n\t" \
  "buffer_load_dwordx4 v0, %[X], s47 offen sc1 lds\n\t" \
  TK_QNP \
  "v_mfma_f32_16x16x32_bf16 v[32:35], v[128:131], a[176:179], v[32:35]\n\t" \
  "v_mfma_f32_16x16x32_bf16 v[36:39], v[132:135], a[176:179], v[36:39]\n\t" \
  TK_QNP \
  "v_mfma_f32_16x16x32_bf16 v[32:35], v[136:139], a[180:183], v[32:35]\n\t" \
  "s_add_u32 m0, s46, 16384\n\t" \
  "s_add_u32 s47, s55, 32768\n\t" \
  "buffer_load_dwordx4 v0, %[X], s47 offen sc1 lds\n\t" \
  "v_mfma_f32_16x16x32_bf16 v[36:39], v[140:143], a[180:183], v[36:39]\n\t" \
  TK_QNP \
  "v_mfma_f32_16x16x32_bf16 v[32:35], v[144:147], a[184:187], v[32:35]\n\t" \
  "v_mfma_f32_16x16x32_bf16 v[36:39], v[148:151], a[184:187], v[36:39]\n\t" \
  TK_QNP \
  "v_mfma_f32_16x16x32_bf16 v[32:35], v[152:155], a[188:191], v[32:35]\n\t" \
  "buffer_store_dwordx4 v[52:55], v0, %[X], s50 offen offset:1024 sc1\n\t" \
  "v_mfma_f32_16x16x32_bf16 v[36:39], v[156:159], a[188:191], v[36:39]\n\t" \
  TK_QNP \
  "v_mfma_f32_16x16x32_bf16 v[32:35], v[160:163], a[192:195], v[32:35]\n\t" \
  "v_mfma_f32_16x16x32_bf16 v[36:39], v[164:167], a[192:195], v[36:39]\n\t" \
  "s_add_u32 m0, s46, 20480\n\t" \
  "s_add_u32 s47, s55, 36864\n\t" \
  "buffer_load_dwordx4 v0, %[X], s47 offen sc1 lds\n\t" \
  TK_QNP \
  "v_mfma_f32_16x16x32_bf16 v[32:35], v[168:171], a[196:199], v[32:35]\n\t" \
  "v_mfma_f32_16x16x32_bf16 v[36:39], v[172:175], a[196:199], v[36:39]\n\t" \
  TK_QNP \
  "v_mfma_f32_16x16x32_bf16 v[32:35], v[176:179], a[200:203], v[32:35]\n\t" \
  "s_add_u32 m0, s46, 24576\n\t" \
  "s_add_u32 s47, s55, 49152\n\t" \
  "buffer_load_dwordx4 v0, %[X], s47 offen sc1 lds\n\t" \
  "v_mfma_f32_16x16x32_bf16 v[36:39], v[180:183], a[200:203], v[36:39]\n\t" \
  TK_QNP \
  "v_mfma_f32_16x16x32_bf16 v[32:35], v[184:187], a[204:207], v[32:35]\n\t" \
  "v_mfma_f32_16x16x32_bf16 v[36:39], v[188:191], a[204:207], v[36:39]\n\t" \
  "s_add_u32 m0, s46, 28672\n\t" \
  "s_add_u32 s47, s55, 53248\n\t" \
  "buffer_load_dwordx4 v0, %[X], s47 offen sc1 lds\n\t" \
  TK_QNP \
  "v_mfma_f32_16x16x32_bf16 v[32:35], v[192:195], a[208:211], v[32:35]\n\t" \
  "v_mfma_f32_16x16x32_bf16 v[36:39], v[196:199], a[208:211], v[36:39]\n\t" \
  TK_QNP \
  "v_mfma_f32_16x16x32_bf16 v[32:35], v[200:203], a[212:215], v[32:35]\n\t" \
  "s_add_u32 m0, s46, 32768\n\t" \
  "s_add_u32 s47, s55, 65536\n\t" \
  "buffer_load_dwordx4 v0, %[X], s47 offen sc1 lds\n\t" \
  "v_mfma_f32_16x16x32_bf16 v[36:39], v[204:207], a[212:215], v[36:39]\n\t" \
  TK_QNP \
  "v_mfma_f32_16x16x32_bf16 v[40:43], v[64:67], a[216:219], 0\n\t" \
  "v_mfma_f32_16x16x32_bf16 v[44:47], v[68:71], a[216:219], 0\n\t" \
  TK_QNP \
  "v_mfma_f32_16x16x32_bf16 v[40:43], v[72:75], a[220:223], v[40:43]\n\t" \
  "v_cvt_pk_bf16_f32 v56, v32, v33\n\t" \
  "v_cvt_pk_bf16_f32 v57, v34, v35\n\t" \
  "v_mfma_f32_16x16x32_bf16 v[44:47], v[76:79], a[220:223], v[44:47]\n\t" \
  "v_cvt_pk_bf16_f32 v58, v36, v37\n\t" \
  "v_cvt_pk_bf16_f32 v59, v38, v39\n\t" \
  TK_QNP \
  "v_mfma_f32_16x16x32_bf16 v[40:43], v[80:83], a[224:227], v[40:43]\n\t" \
  "v_mfma_f32_16x16x32_bf16 v[44:47], v[84:87], a[224:227], v[44:47]\n\t" \
  TK_QNP \
  "v_mfma_f32_16x16x32_bf16 v[40:43], v[88:91], a[228:231], v[40:43]\n\t" \
  "buffer_store_dwordx4 v[56:59], v0, %[X], s50 offen offset:2048 sc1\n\t" \
  "v_mfma_f32_16x16x32_bf16 v[44:47], v[92:95], a[228:231], v[44:47]\n\t" \
  TK_QNP \
  "v_mfma_f32_16x16x32_bf16 v[40:43], v[96:99], a[232:235], v[40:43]\n\t" \
  "v_mfma_f32_16x16x32_bf16 v[44:47], v[100:103], a[232:235], v[44:47]\n\t" \
  TK_QNP \
  "v_mfma_f32_16x16x32_bf16 v[40:43], v[104:107], a[236:239], v[40:43]\n\t" \
  "v_mfma_f32_16x16x32_bf16 v[44:47], v[108:111], a[236:239], v[44:47]\n\t" \
  TK_QNP \
  "v_mfma_f32_16x16x32_bf16 v[40:43], v[112:115], a[240:243], v[40:43]\n\t" \
  "v_mfma_f32_16x16x32_bf16 v[44:47], v[116:119], a[240:243], v[44:47]\n\t" \
  TK_QNP \
  "v_mfma_f32_16x16x32_bf16 v[40:43], v[120:123], a[244:247], v[40:43]\n\t" \
  "v_mfma_f32_16x16x32_bf16 v[44:47], v[124:127], a[244:247], v[44:47]\n\t" \
  TK_QNP \
  "v_mfma_f32_16x16x32_bf16 v[40:43], v[128:131], a[248:251], v[40:43]\n\t" \
  "v_mfma_f32_16x16x32_bf16 v[44:47], v[132:135], a[248:251], v[44:47]\n\t" \
  TK_QNP \
  "v_mfma_f32_16x16x32_bf16 v[40:43], v[136:139], a[252:255], v[40:43]\n\t" \
  "v_mfma_f32_16x16x32_bf16 v[44:47], v[140:143], a[252:255], v[44:47]\n\t" \
  TK_QNP \
  "v_mfma_f32_16x16x32_bf16 v[40:43], v[144:147], v[224:227], v[40:43]\n\t" \
  "v_mfma_f32_16x16x32_bf16 v[44:47], v[148:151], v[224:227], v[44:47]\n\t" \
  TK_QNP \
  "v_mfma_f32_16x16x32_bf16 v[40:43], v[152:155], v[228:231], v[40:43]\n\t" \
  "v_mfma_f32_16x16x32_bf16 v[44:47], v[156:159], v[228:231], v[44:47]\n\t" \
  TK_QNP \
  "v_mfma_f32_16x16x32_bf16 v[40:43], v[160:163], v[232:235], v[40:43]\n\t" \
  "v_mfma_f32_16x16x32_bf16 v[44:47], v[164:167], v[232:235], v[44:47]\n\t" \
  TK_QNP \
  "v_mfma_f32_16x16x32_bf16 v[40:43], v[168:171], v[236:239], v[40:43]\n\t" \
  "v_mfma_f32_16x16x32_bf16 v[44:47], v[172:175], v[236:239], v[44:47]\n\t" \
  TK_QNP \
  "v_mfma_f32_16x16x32_bf16 v[40:43], v[176:179], v[240:243], v[40:43]\n\t" \
  "v_mfma_f32_16x16x32_bf16 v[44:47], v[180:183], v[240:243], v[44:47]\n\t" \
  TK_QNP \
  "v_mfma_f32_16x16x32_bf16 v[40:43], v[184:187], v[244:247], v[40:43]\n\t" \
  "v_mfma_f32_16x16x32_bf16 v[44:47], v[188:191], v[244:247], v[44:47]\n\t" \
  TK_QNP \
  "v_mfma_f32_16x16x32_bf16 v[40:43], v[192:195], v[248:251], v[40:43]\n\t" \
  "v_mfma_f32_16x16x32_bf16 v[44:47], v[196:199], v[248:251], v[44:47]\n\t" \
  TK_QNP \
  "v_mfma_f32_16x16x32_bf16 v[40:43], v[200:203], v[252:255], v[40:43]\n\t" \
  "v_mfma_f32_16x16x32_bf16 v[44:47], v[204:207], v[252:255], v[44:47]\n\t" \
  TK_QNP \
  "s_nop 4\n\t" \
  "v_cvt_pk_bf16_f32 v60, v40, v41\n\t" \
  "v_cvt_pk_bf16_f32 v61, v42, v43\n\t" \
  "s_nop 1\n\t" \
  "v_cvt_pk_bf16_f32 v62, v44, v45\n\t" \
  "v_cvt_pk_bf16_f32 v63, v46, v47\n\t" \
  "buffer_store_dwordx4 v[60:63], v0, %[X], s50 offen offset:3072 sc1\n\t" \
  TK_PQ2 \
  "s_add_u32 s49, s82, s38\n\t" \
  "s_cmp_eq_u32 s48, 63\n\t" \
  "s_cbranch_scc1 LEND_%=\n\t" \
  "s_add_u32 s48, s48, 1\n\t" \
  "s_cmp_eq_u32 s56, 0\n\t" \
  "s_cbranch_scc1 LCOLD_%=\n\t" \
  "s_waitcnt vmcnt(0)\n\t" \
  "s_mov_b64 exec, 1\n\t" \
  "buffer_store_dword v6, off, %[X], s49 sc1\n\t" \
  "s_mov_b64 exec, -1\n\t" \
  "s_mov_b32 s49, 0xf0000000\n\t" \
  "s_cmp_lg_u32 s40, 0\n\t" \
  "s_cbranch_scc1 LTOP_%=\n\t" \
  "s_add_i32 s62, s48, s42\n\t" \
  "s_cmp_lt_i32 s62, 1\n\t" \
  "s_cbranch_scc1 LTOP_%=\n\t" \
  "s_mul_i32 s63, s62, 43\n\t" \
  "s_lshr_b32 s63, s63, 7\n\t" \
  "s_mul_i32 s62, s62, 688128\n\t" \
  "s_lshl_b32 s60, s63, 15\n\t" \
  "s_add_u32 s62, s62, s60\n\t" \
  "s_lshr_b32 s60, s88, s63\n\t" \
  "s_and_b32 s60, s60, 1\n\t" \
  "s_lshl_b32 s60, s60, 13\n\t" \
  "s_add_u32 s62, s62, s60\n\t" \
  "s_add_u32 s62, s62, 660992\n\t" \
  "s_movk_i32 s63, 4000\n\t" \
  "LGW_%=:\n\t" \
  "buffer_load_dword v4, off, %[X], s62 sc1\n\t" \
  "s_memrealtime s[52:53]\n\t" \
  "s_waitcnt vmcnt(0) lgkmcnt(0)\n\t" \
  "v_readfirstlane_b32 s60, v4\n\t" \
  "s_sub_u32 s63, s63, 1\n\t" \
  "s_cbranch_scc1 LTOP_%=\n\t" \
  "s_cmp_lg_u32 s60, -1\n\t" \
  "s_cbranch_scc1 LGW_%=v\n\t" \
  "s_sleep " TK_PSG "\n\t" \
  "s_branch LGW_%=\n\t" \
  "LGW_%=v:\n\t" \
  "s_not_b32 s60, s60\n\t" \
  "s_lshl_b32 s61, s52, 8\n\t" \
  "s_sub_u32 s60, s60, s61\n\t" \
  "s_ashr_i32 s60, s60, 8\n\t" \
  "s_sub_i32 s60, s60, " TK_QXW "\n\t" \
  "s_min_i32 s60, s60, 400\n\t" \
  "s_add_u32 s54, s52, s60\n\t" \
  "LGW_%=w:\n\t" \
  "s_memrealtime s[52:53]\n\t" \
  "s_waitcnt lgkmcnt(0)\n\t" \
  "s_sub_i32 s53, s52, s54\n\t" \
  "s_cmp_lt_i32 s53, 0\n\t" \
  "s_cbranch_scc1 LGW_%=w\n\t" \
  "s_branch LTOP_%=\n\t" \
  "LEND_%=:\n\t" \
  "s_waitcnt vmcnt(0)\n\t" \
  "s_mov_b64 exec, 1\n\t" \
  "buffer_store_dword v6, off, %[X], s49 sc1\n\t" \
  "s_mov_b64 exec, -1\n\t" \
  "s_mov_b32 s49, 0xf0000000\n\t" \
  TK_PQ0 \
  ""
__device__ __forceinline__ unsigned hoff(int r, int piece) {
  const int m = 4 * (r >> 3) + (r & 3), kp = piece >> 3;
  return (P_H + (unsigned)(kp >> 1)) * PAIR + (unsigned)((kp & 1) * 4096 + (16 * ((r >> 2) & 1) + m) * 128 + (((piece ^ m) & 7) << 4));
}
template <int SLEEP>
__device__ __forceinline__ void wait_all(rsrc_t X, unsigned vo, unsigned so) {
  unsigned spins = 0;
  while (true) {
    asm volatile("" ::: "memory");
    if (__all(ld4(X, vo, so) != 0u)) break;
    if (spin_fail(spins, X)) break;
    __builtin_amdgcn_s_sleep(SLEEP);
  }
  asm volatile("" ::: "memory");
}
#define VQ_CLOB "v16", "v17", "v18", "v19", "v20", "v21", "v22", "v23", "v24", "v25", "v26", "v27", "v28", "v29", "v30", "v31", "v32", "v33", "v34", "v35", "v36", "v37", "v38", "v39", "v40", "v41", "v42", "v43", "v44", "v45", "v46", "v47", "v48", "v49", "v50", "v51", "v52", "v53", "v54", "v55", "v56", "v57", "v58", "v59", "v60", "v61", "v62", "v63", "v64", "v65", "v66", "v67", "v68", "v69", "v70", "v71", "v72", "v73", "v74", "v75", "v76", "v77", "v78", "v79", "v80", "v81", "v82", "v83", "v84", "v85", "v86", "v87", "v88", "v89", "v90", "v91", "v92", "v93", "v94", "v95", "v96", "v97", "v98", "v99", "v100", "v101", "v102", "v103", "v104", "v105", "v106", "v107", "v108", "v109", "v110", "v111", "v112", "v113", "v114", "v115", "v116", "v117", "v118", "v119", "v120", "v121", "v122", "v123", "v124", "v125", "v126", "v127", "v128", "v129", "v130", "v131", "v132", "v133", "v134", "v135", "v136", "v137", "v138", "v139", "v140", "v141", "v142", "v143", "v144", "v145", "v146", "v147", "v148", "v149", "v150", "v151", "v152", "v153", "v154", "v155", "v156", "v157", "v158", "v159", "v160", "v161", "v162", "v163", "v164", "v165", "v166", "v167", "v168", "v169", "v170", "v171", "v172", "v173", "v174", "v175", "v176", "v177", "v178", "v179", "v180", "v181", "v182", "v183", "v184", "v185", "v186", "v187", "v188", "v189", "v190", "v191", "v192", "v193", "v194", "v195", "v196", "v197", "v198", "v199", "v200", "v201", "v202", "v203", "v204", "v205", "v206", "v207", "v220", "v221"
#define VA_CLOB "v40", "v41", "v42", "v43", "v44", "v45", "v46", "v47", "v48", "v49", "v50", "v51", "v52", "v53", "v54", "v55", "v56", "v57", "v58", "v59", "v60", "v61", "v62", "v63", "v64", "v65", "v66", "v67", "v68", "v69", "v70", "v71", "v72", "v73", "v74", "v75", "v76", "v77", "v78", "v79", "v80", "v81", "v82", "v83", "v84", "v85", "v86", "v87", "v88", "v89", "v90", "v91", "v92", "v93", "v94", "v95", "v96", "v97", "v98", "v99", "v100", "v101", "v102", "v103", "v104", "v105", "v106", "v107", "v108", "v109", "v110", "v111", "v112", "v113", "v114", "v115", "v116", "v117", "v118", "v119", "v120", "v121", "v122", "v123", "v124", "v125", "v126", "v127", "v128", "v129", "v130", "v131", "v132", "v133", "v134", "v135", "v136", "v137", "v138", "v139", "v140", "v141", "v142", "v143", "v144", "v145", "v146", "v147", "v148", "v149", "v150", "v151", "v152", "v153", "v154", "v155", "v156", "v157", "v158", "v159", "v160", "v161", "v162", "v163", "v164", "v165", "v166", "v167", "v168", "v169", "v170", "v171", "v172", "v173", "v174", "v175", "v176", "v177", "v178", "v179", "v180", "v181", "v182", "v183", "v184", "v185", "v186", "v187", "v188", "v189", "v190", "v191", "v192", "v193", "v194", "v195", "v196", "v197", "v198", "v199", "v200", "v201", "v202", "v203", "v204", "v205", "v206", "v207", "v208", "v209", "v210", "v211", "v220", "v221"
#define TOK_COMMON() \
  const MP p = getp(); \
  const int c = blockIdx.x - NCHW, xq = c & 3, L = (c >> 2) & 1, i = c >> 3; \
  const int tid = threadIdx.x, lane = tid & 63, w = __builtin_amdgcn_readfirstlane(tid >> 6); \
  const rsrc_t X = mkr(p.xb, XB_BYTES)
#define W_IN [W] "s"(WR)
#define W_CLOB "a0", "a1", "a2", "a3", "a4", "a5", "a6", "a7", "a8", "a9", "a10", "a11", "a12", "a13", "a14", "a15", "a16", "a17", "a18", "a19", "a20", "a21", "a22", "a23", "a24", "a25", "a26", "a27", "a28", "a29", "a30", "a31", "a32", "a33", "a34", "a35", "a36", "a37", "a38", "a39", "a40", "a41", "a42", "a43", "a44", "a45", "a46", "a47", "a48", "a49", "a50", "a51", "a52", "a53", "a54", "a55", "a56", "a57", "a58", "a59", "a60", "a61", "a62", "a63", "a64", "a65", "a66", "a67", "a68", "a69", "a70", "a71", "a72", "a73", "a74", "a75", "a76", "a77", "a78", "a79", "a80", "a81", "a82", "a83", "a84", "a85", "a86", "a87", "a88", "a89", "a90", "a91", "a92", "a93", "a94", "a95", "a96", "a97", "a98", "a99", "a100", "a101", "a102", "a103", "a104", "a105", "a106", "a107", "a108", "a109", "a110", "a111", "a112", "a113", "a114", "a115", "a116", "a117", "a118", "a119", "a120", "a121", "a122", "a123", "a124", "a125", "a126", "a127", "a128", "a129", "a130", "a131", "a132", "a133", "a134", "a135", "a136", "a137", "a138", "a139", "a140", "a141", "a142", "a143", "a144", "a145", "a146", "a147", "a148", "a149", "a150", "a151", "a152", "a153", "a154", "a155", "a156", "a157", "a158", "a159", "a160", "a161", "a162", "a163", "a164", "a165", "a166", "a167", "a168", "a169", "a170", "a171", "a172", "a173", "a174", "a175", "a176", "a177", "a178", "a179", "a180", "a181", "a182", "a183", "a184", "a185", "a186", "a187", "a188", "a189", "a190", "a191", "a192", "a193", "a194", "a195", "a196", "a197", "a198", "a199", "a200", "a201", "a202", "a203", "a204", "a205", "a206", "a207", "a208", "a209", "a210", "a211", "a212", "a213", "a214", "a215", "a216", "a217", "a218", "a219", "a220", "a221", "a222", "a223", "a224", "a225", "a226", "a227", "a228", "a229", "a230", "a231", "a232", "a233", "a234", "a235", "a236", "a237", "a238", "a239", "a240", "a241", "a242", "a243", "a244", "a245", "a246", "a247", "a248", "a249", "a250", "a251", "a252", "a253", "a254", "a255", "v224", "v225", "v226", "v227", "v228", "v229", "v230", "v231", "v232", "v233", "v234", "v235", "v236", "v237", "v238", "v239", "v240", "v241", "v242", "v243", "v244", "v245", "v246", "v247", "v248", "v249", "v250", "v251", "v252", "v253", "v254", "v255"
#define BOOT_IN "{s67}"(L), [ctab] "i"(CTAB_OFF)
#define S_CLOB "s88", "s89", "s90", "s91", "s46", "s47", "s48", "s49", "s50", "s51", "s52", "s53", "s54", "s55", "s56", "s57", "s58", "s59", "s60", "s61", "s62", "s63", "s68", "s69", "s70", "s71", "s72", "s73", "s82", "s83", "s84", "m0", "vcc", "scc", "memory"
typedef unsigned v8w __attribute__((ext_vector_type(8)));

__device__ __forceinline__ void qkv_role() {
  TOK_COMMON();
  const int role = i * 4 + w, hh = 8 * xq + role / 6, part = role % 6, which = part >> 1, half = part & 1;
  const rsrc_t WR = mkr((which == 0 ? p.Wq : (which == 1 ? p.Wk : p.Wv)) + hh * DH + 64 * half, DM * HD * 2u - (unsigned)(hh * DH + 64 * half) * 2u);
  const int m = lane & 15, g = lane >> 4;
  const unsigned a0 = (unsigned)(m * 128 + ((g ^ (m & 7)) << 4));
  v8w lv = {(unsigned)lane * 16u, a0, a0 ^ 64u, (unsigned)(lane & (L ? 15 : 3)) * 128u, 0u, 40960u + 4u * (unsigned)w, 1u, 0u};
  const unsigned c36 = XQL + P_H * PAIR + (unsigned)w * 1024u, c37 = XQ + (unsigned)(3 * hh + which) * PAIR + (unsigned)half * (which == 2 ? 4096u : 2048u);
  const unsigned c38 = P_FA * PAIR + (unsigned)(hh * 128 + part * 16), c39 = w == 0 ? P_FB * PAIR + 4096u : 0xF0000000u, c41 = (unsigned)w * 1024u;
  const int c42 = L ? 1 - QLEAD1 : 1 - QLEAD;
  unsigned d74 = TRC_OFF + 262144u + (unsigned)((c * 4 + w) * 66) * 8u;
  TT(64, 0);
#define Q_CALL(A) asm volatile("s_waitcnt vmcnt(0) lgkmcnt(0)\n\t" A : "+{v[0:7]}"(lv), "+{s74}"(d74) \
               : [X] "s"(X), "{s36}"(c36), "{s37}"(c37), "{s38}"(c38), "{s39}"(c39), "{s40}"(w), "{s41}"(c41), "{s42}"(c42), "{s45}"(i), "{s92}"(TMO_OFF), BOOT_IN, [cap] "i"(SPIN_CAP), [qxw] "i"(QXW), [qxc] "i"(QXC), W_IN : VQ_CLOB, W_CLOB, S_CLOB)
  if (which == 2) { Q_CALL(QV_ASM "LQVEND:"); __builtin_amdgcn_endpgm(); }
  Q_CALL(QK_ASM "LQKEND:");
  __builtin_amdgcn_endpgm();
}

__device__ __forceinline__ void ap_role() {
  TOK_COMMON();
  const int hp = 4 * xq + ((i - 12) >> 1), ch = i & 1, a = w >> 1, rt = w & 1, h = 2 * hp + a;
  const int n = lane & 15, g = lane >> 4;
  const rsrc_t WR = mkr(p.Wo + (long)hp * 2 * DH * DM, 2u * DH * DM * 2u);
  const unsigned l16 = (unsigned)lane * 16u, ab = (unsigned)a * 24576u + l16;
  v16u lv = {l16, ab, ab + (unsigned)rt * 4096u, 49152u + (unsigned)w * 4096u + l16, 49152u + (unsigned)(w ^ 2) * 4096u + l16, l16 & ~32u, l16 >> 1,
             (unsigned)(g * 262144 + n * 16), (unsigned)(n * 32 + g * 8), (unsigned)((lane & 7) % 6) * 16u, 0u, 0u, 0u, 0u, 0u, 0u};
  v8w mk;
  const int qi = 8 * (n >> 2) + (n & 3) + 4 * rt;
#pragma unroll
  for (int k = 0; k < 8; ++k) mk[k] = 8 * g + k > qi ? 0xFF800000u : 0u;
  v16u zz = {0u, 0u, 0u, 0u, 0u, 0u, 0u, 0u, 0u, 0u, 0u, 0u, 0u, 0u, 0u, 0u};
  const unsigned tb = XAL + (unsigned)(3 * h) * PAIR;
  const unsigned c36 = tb + (rt ? PAIR + 4096u : 0u), c37 = tb + (rt ? 2u * PAIR : 4096u), c38 = tb + (rt ? 2u * PAIR + 4096u : PAIR);
  const unsigned c39 = (P_KV + (unsigned)(h >> 1)) * PAIR + (unsigned)(h & 1) * 4096u, c41 = P_FA * PAIR + (unsigned)h * 128u;
  const unsigned c42 = w == 0 ? P_FB * PAIR + (unsigned)(2 * hp + ch) * 128u : 0xF0000000u;
  const unsigned c43 = XA + TP0 * XBLK + (unsigned)(8 * rt + (hp >> 3)) * PAIR + (unsigned)((hp & 7) * 1024 + ch * 512 + a * 256);
  unsigned d44 = (TP0 + 8u) * XBLK + (unsigned)(hp >> 1) * PAIR + (unsigned)((((hp & 1) * 4 + 2 * ch + a) * 2 + rt) * 512);
  const unsigned c45 = (unsigned)(a * 24576 + rt * 12288);
  unsigned d74 = TRC_OFF + 262144u + (unsigned)((c * 4 + w) * 66) * 8u;
  asm volatile("s_waitcnt vmcnt(0) lgkmcnt(0)\n\t" AP_ASM "LAPEND:" : "+{v[0:15]}"(lv), "+{v[24:39]}"(zz), "+{s74}"(d74), "+{s44}"(d44)
               : [X] "s"(X), "{s36}"(c36), "{s37}"(c37), "{s38}"(c38), "{s39}"(c39), "{s41}"(c41), "{s42}"(c42), "{s43}"(c43), "{s45}"(c45), "{s64}"(512 * ch + 256 * a), "{s65}"(1024 + 64 * ch + 32 * a), "{s66}"(147456 * a), BOOT_IN,
                 "{s92}"(TMO_OFF), [cap] "i"(SPIN_CAP), "{v[16:23]}"(mk), W_IN : VA_CLOB, W_CLOB, S_CLOB);
  __builtin_amdgcn_endpgm();
}

template <int N> __device__ __forceinline__ float rorN(float v) { return __uint_as_float((unsigned)__builtin_amdgcn_update_dpp(0, (int)__float_as_uint(v), 0x120 + N, 0xf, 0xf, false)); }
__device__ __forceinline__ void up8(v4u v, float* x) {
  x[0] = lo2f(v.x); x[1] = hi2f(v.x); x[2] = lo2f(v.y); x[3] = hi2f(v.y); x[4] = lo2f(v.z); x[5] = hi2f(v.z); x[6] = lo2f(v.w); x[7] = hi2f(v.w);
}
__device__ __forceinline__ v4u pk8(const float* x) { return (v4u){pk2(x[0], x[1]), pk2(x[2], x[3]), pk2(x[4], x[5]), pk2(x[6], x[7])}; }

__device__ __forceinline__ void rb9(float (&x)[9]) {
#pragma unroll
  for (int i = 0; i < 8; i += 2) { const unsigned u = pk2(x[i], x[i + 1]); x[i] = lo2f(u); x[i + 1] = hi2f(u); }
  x[8] = lo2f(pk2(x[8], x[8]));
}
template <int R>
__device__ __forceinline__ void lnR(const float (&x)[R][9], float (&o)[R][9]) {
  float s[2 * R], pv[R];
#pragma unroll
  for (int r = 0; r < R; ++r) {
    pv[r] = __uint_as_float(__builtin_amdgcn_readfirstlane(__float_as_uint(x[r][0])));
    const f32x2 p2 = {pv[r], pv[r]};
    f32x2 d[4];
#pragma unroll
    for (int i = 0; i < 4; ++i) d[i] = (f32x2){x[r][2 * i], x[r][2 * i + 1]} - p2;
    const float d8 = x[r][8] - pv[r];
    const f32x2 a = (d[0] + d[1]) + (d[2] + d[3]);
    const f32x2 q = __builtin_elementwise_fma(d[3], d[3], __builtin_elementwise_fma(d[2], d[2], __builtin_elementwise_fma(d[1], d[1], d[0] * d[0])));
    s[2 * r] = (a[0] + d8) + a[1];
    s[2 * r + 1] = __builtin_fmaf(d8, d8, q[0]) + q[1];
  }
#pragma unroll
  for (int k = 0; k < 2 * R; ++k) s[k] += rorN<8>(s[k]);
#pragma unroll
  for (int k = 0; k < 2 * R; ++k) s[k] += rorN<4>(s[k]);
#pragma unroll
  for (int k = 0; k < 2 * R; ++k) s[k] += rorN<2>(s[k]);
#pragma unroll
  for (int k = 0; k < 2 * R; ++k) s[k] += rorN<1>(s[k]);
#pragma unroll
  for (int r = 0; r < R; ++r) {
    auto u = __builtin_amdgcn_permlane16_swap(__float_as_uint(s[2 * r]), __float_as_uint(s[2 * r + 1]), false, false);
    s[2 * r] = __uint_as_float(u[0]) + __uint_as_float(u[1]);
  }
#pragma unroll
  for (int r = 0; r < R; ++r) {
    const unsigned u = __float_as_uint(s[2 * r]);
    const float md = (__uint_as_float(__builtin_amdgcn_readlane(u, 0)) + __uint_as_float(__builtin_amdgcn_readlane(u, 32))) * (1.0f / DM);
    const float sq = (__uint_as_float(__builtin_amdgcn_readlane(u, 16)) + __uint_as_float(__builtin_amdgcn_readlane(u, 48))) * (1.0f / DM);
    const unsigned mv = pk2(pv[r] + md, __builtin_fmaxf(__builtin_fmaf(-md, md, sq), 0.f));
    const float m = lo2f(mv), var = hi2f(mv);
    const float ve = lo2f(pk2(var + 1e-5f, 0.f));
    const float den = lo2f(pk2(__builtin_amdgcn_sqrtf(ve), 0.f));
    const float inv = __builtin_amdgcn_rcpf(den);
#pragma unroll
    for (int i = 0; i < 9; ++i) o[r][i] = (x[r][i] - m) * inv;
  }
}
__device__ __forceinline__ void ln9(const float (&x)[9], float (&o)[9]) { lnR<1>((const float (&)[1][9])x, (float (&)[1][9])o); }
__device__ __forceinline__ v4u gat8(float v) {
  const unsigned d = pk2(v, __uint_as_float((unsigned)__builtin_amdgcn_mov_dpp((int)__float_as_uint(v), 0xB1, 0xf, 0xf, true)));
  return (v4u){d, (unsigned)__builtin_amdgcn_mov_dpp((int)d, 0x102, 0xf, 0xf, true), (unsigned)__builtin_amdgcn_mov_dpp((int)d, 0x104, 0xf, 0xf, true),
               (unsigned)__builtin_amdgcn_mov_dpp((int)d, 0x106, 0xf, 0xf, true)};
}
__device__ __forceinline__ void tok_row(const MP& p, const rsrc_t X, unsigned sb1, int L, int s, int r, int lane, const v4u (&v)[18], v4u px, v4u py, unsigned px9, unsigned py9) {
  const long tok = (long)s * SEG + r;
  const int m = lane & 15, g = lane >> 4;
  const int xp = 4 * (g & 1) + 2 * (g >> 1) + (m >> 3);
  const unsigned zr = opq(0u);
  const unsigned one = (g == (m >> 2)) ? (opq(0x3F80u) << (16 * (m & 1))) : zr;
  const unsigned ax = (m & 2) ? zr : one, ay = (m & 2) ? one : zr;
  const v4u A1 = {ax, ay, zr, zr}, A2 = {zr, zr, ax, ay};
  const unsigned on2 = opq(0x3F80u) << (16 * (m & 1));
  const int q2 = (m >> 1) & 3;
  const v4u sel = {q2 == 0 ? on2 : zr, q2 == 1 ? on2 : zr, q2 == 2 ? on2 : zr, q2 == 3 ? on2 : zr};
  f32x4 d1, d2, e1;
  asm volatile("s_nop 1\n\t" MF "%0, %1, %3, 0\n\t" MF "%0, %2, %3, %0" : "=&v"(e1) : "v"(v[0]), "v"(v[1]), "v"(sel));
#pragma unroll
  for (int i = 0; i < 16; ++i) {
    if (i == 0) asm volatile("s_nop 1\n\t" MF "%0, %2, %4, 0\n\t" MF "%1, %3, %4, 0" : "=&v"(d1), "=&v"(d2) : "v"(A1), "v"(A2), "v"(v[2]));
    else asm volatile(MF "%0, %2, %4, %0\n\t" MF "%1, %3, %4, %1" : "+v"(d1), "+v"(d2) : "v"(A1), "v"(A2), "v"(v[2 + i]));
  }
  asm volatile("s_nop 7" : "+v"(d1), "+v"(d2), "+v"(e1));
  float o[9];
#pragma unroll
  for (int i = 0; i < 4; ++i) { o[i] = d1[i]; o[4 + i] = d2[i]; }
  {
    auto a02 = __builtin_amdgcn_permlane32_swap(__float_as_uint(e1[0]), __float_as_uint(e1[2]), false, false);
    auto a13 = __builtin_amdgcn_permlane32_swap(__float_as_uint(e1[1]), __float_as_uint(e1[3]), false, false);
    o[8] = m >= 8 ? __uint_as_float(a13[0]) + __uint_as_float(a13[1]) : __uint_as_float(a02[0]) + __uint_as_float(a02[1]);
  }
  float x[9];
  up8(px, x); x[8] = lo2f(px9);
#pragma unroll
  for (int i = 0; i < 9; ++i) x[i] += o[i];
  if (L == 0) {
    float y[9], ln[9];
    up8(py, y); y[8] = lo2f(py9);
    rb9(x);
    ln9(x, ln);
#pragma unroll
    for (int i = 0; i < 9; ++i) x[i] = __builtin_fmaf(ln[i], y[i], x[i]);
    rb9(x);
    st16(X, (unsigned)lane * 16u, sb1 + (P_X3 + (unsigned)(r >> 3)) * PAIR + (unsigned)(r & 7) * 1024u, pk8(x));
    const v4u x9 = gat8(x[8]);
    if ((lane & 7) == 0) st16(X, (unsigned)xp * 16u, sb1 + (P_H + 4u) * PAIR + 4096u + (unsigned)r * 128u, x9);
    ln9(x, ln);
    st16(X, hoff(r, lane), sb1, pk8(ln));
    const v4u h9 = gat8(ln[8]);
    if ((lane & 7) == 0) st16(X, hoff(r, 64 + xp), sb1, h9);
  } else {
    *(v4u*)&p.out[tok * DM + 8 * lane] = pk8(x);
    const v4u o9 = gat8(x[8]);
    if ((lane & 7) == 0) *(v4u*)&p.out[tok * DM + 512 + 8 * xp] = o9;
  }
}

__device__ __forceinline__ void row_role(char* smem) {
  TOK_COMMON();
  const int rw = 4 * xq + (i - 20), r = 2 * rw + (w >> 1);
  const unsigned long long sm = die_mask(X, SG0, L, lane);
  const unsigned tm = (unsigned)die_mask(X, TP0, L, lane), dm = (unsigned)die_mask(X, 0u, L, lane);
  __attribute__((address_space(3))) unsigned* cnt = (__attribute__((address_space(3))) unsigned*)smem;
  BAR();
  if (tid < 64) cnt[tid] = 0u;
  BAR();
  for (int s = w & 1; s < NSEG; s += 2) {
    int ln_ = lane; asm volatile("" : "+v"(ln_));
    const int m = ln_ & 15, g = ln_ >> 4;
    const unsigned l16 = (unsigned)ln_ * 16u, xo = (unsigned)(8 * (4 * (g & 1) + 2 * (g >> 1) + (m >> 3)) + (m & 7)) * 2u;
    const unsigned sb = SEGB(sm, s), sb1 = sb ^ (L ? 0u : 8192u);
    if (s + ARMK < NSEG) {
      const unsigned sv = opq(SENT), sl = SLOT_AT(s + ARMK) + ((dm >> ((s + ARMK) / 3)) & 1u) * 8192u;
      for (int j = r; j < 82; j += 32) {
#pragma unroll
        for (int k = 0; k < 4; ++k) st16(X, l16 + (unsigned)k * 1024u, sl + (unsigned)(j >> 1) * PAIR + (unsigned)(j & 1) * 4096u, (v4u){sv, sv, sv, sv});
      }
    }
    wait_all<PSL>(X, 0u, sb + P_FB * PAIR + 4096u + (unsigned)(L ? rw : r >> 3) * 128u);
    const v4u px = ld16(X, l16, sb + (P_X3 + (unsigned)(r >> 3)) * PAIR + (unsigned)(r & 7) * 1024u);
    const unsigned px9 = __builtin_amdgcn_raw_buffer_load_b16(X, xo, sb + (P_H + 4u) * PAIR + 4096u + (unsigned)r * 128u, 16);
    const rsrc_t YX = mkr(p.yT + (2 * s + (r >> 4)) * 9216, L ? 0u : 18432u);
    const v2u ya = ld8(YX, (unsigned)(ln_ * 256 + ((r ^ m) & 15) * 8), 0u), yb = ld8(YX, (unsigned)(ln_ * 256 + 128 + ((r ^ m) & 15) * 8), 0u);
    const v4u py = {ya.x, ya.y, yb.x, yb.y};
    const unsigned py9 = __builtin_amdgcn_raw_buffer_load_b16(YX, 16384u + (xo >> 3) * 128u + (((unsigned)r ^ (xo >> 4)) & 15u) * 8u + (xo & 6u), 0u, 16);
    if (!__all(ld4(X, (unsigned)(ln_ & 31) * 128u, sb + P_FB * PAIR) != 0u)) {
      if (__builtin_amdgcn_readfirstlane(ld4(X, 0u, sb + P_FA * PAIR + 4096u + (unsigned)r * 128u)) == 0u) {
        wait_all<PSR>(X, 0u, sb + P_FA * PAIR + 4096u + (unsigned)r * 128u);
        __builtin_amdgcn_s_sleep(RDLY);
      }
      wait_all<PSL>(X, (unsigned)(ln_ & 31) * 128u, sb + P_FB * PAIR);
    }
    asm volatile("" ::: "memory");
    TT(s, 0);
    const unsigned j = (unsigned)s & 15u, to = XRL + TP0 * XBLK + (j << 20) + ((tm >> (j >> 1)) & 1u) * 8192u + (unsigned)r * 2u * PAIR;
    const unsigned t9 = (TP0 + 8u) * XBLK + ((tm >> 8) & 1u) * 8192u + (j << 17) + (unsigned)(((r >> 2) & 1) * 512 + (4 * (r >> 3) + (r & 3)) * 32);
    const unsigned v9 = (unsigned)(g * (int)PAIR + (m >> 1) * 1024 + (m & 1) * 16);
    v4u v[18];
#pragma unroll
    for (int k = 0; k < 2; ++k) v[k] = ld16(X, v9, t9 + (unsigned)k * 4u * PAIR);
#pragma unroll
    for (int k = 0; k < 16; ++k) v[2 + k] = ld16(X, l16 + (unsigned)(k & 3) * 1024u, to + (unsigned)(k >> 3) * PAIR + (unsigned)(k & 4) * 1024u);
    SB();
    tok_row(p, X, sb1, L, s, r, ln_, v, px, py, px9, py9);
    VM0();
    unsigned o1 = 0u;
    if (ln_ == 0) o1 = __hip_atomic_fetch_add(cnt + s, 1u, __ATOMIC_RELAXED, __HIP_MEMORY_SCOPE_WORKGROUP);
    if (__builtin_amdgcn_readfirstlane(o1) == 1u && ln_ < 2 - L) st4(X, ln_ ? sb1 : sb + 2048u, P_FB * PAIR + 4096u + (unsigned)rw * 128u, opq(1u));
    TT(s, 1);
  }
}

__device__ __forceinline__ void rows_task(const MP& p, const rsrc_t X, unsigned long long sm, u16* ys, int grp, int ng) {
  const int tid = threadIdx.x, lane = tid & 63, w = __builtin_amdgcn_readfirstlane(tid >> 6);
  const int m = lane & 15, g = lane >> 4;
  const int xp = 4 * (g & 1) + 2 * (g >> 1) + (m >> 3), c9 = 512 + 8 * xp + (m & 7);
  const unsigned sb = SEGB(sm, grp >> 2);
  v4u yv[5], xa[4];
  unsigned x9[4];
#pragma unroll
  for (int i = 0; i < 5; ++i) yv[i] = ld16(mkr(p.yT + (grp >> 1) * 9216, 18432u), (unsigned)(tid + 256 * i) * 16u, 0u);
#pragma unroll
  for (int rr = 0; rr < 4; ++rr) {
    const long t = grp * 8 + w + 4 * (rr & (2 * ng - 1));
    xa[rr] = *(const v4u*)&p.xin[t * DM + 8 * lane]; x9[rr] = p.xin[t * DM + c9];
  }
#pragma unroll
  for (int i = 0; i < 5; ++i) ((v4u*)ys)[tid + 256 * i] = yv[i];
  BAR();
  SP(13 - 3 * ng);
  const unsigned o9 = (lane & 7) == 0 ? 0u : 0xF0000000u;
#pragma unroll 1
  for (int k = 0; k < ng; ++k) {
    float x[2][9], y[2][9], ln[2][9];
    const int t0 = (grp + k) * 8 + w;
#pragma unroll
    for (int rr = 0; rr < 2; ++rr) {
      const int j = (t0 + 4 * rr) & 15;
      const v2u ya = *(const v2u*)&ys[lane * 128 + (j ^ m) * 4], yb = *(const v2u*)&ys[lane * 128 + 64 + (j ^ m) * 4];
      up8(xa[rr], x[rr]); x[rr][8] = lo2f(x9[rr]); up8((v4u){ya.x, ya.y, yb.x, yb.y}, y[rr]); y[rr][8] = bf2f(ys[(c9 >> 2) * 64 + (j ^ xp) * 4 + (m & 3)]);
      xa[rr] = xa[2 + rr]; x9[rr] = x9[2 + rr];
    }
    lnR<2>(x, ln);
#pragma unroll
    for (int rr = 0; rr < 2; ++rr) {
#pragma unroll
      for (int i = 0; i < 9; ++i) x[rr][i] = __builtin_fmaf(ln[rr][i], y[rr][i], x[rr][i]);
      rb9(x[rr]);
      const int r = (t0 + 4 * rr) & 31;
      st16(X, (unsigned)lane * 16u, sb + (P_X3 + (unsigned)(r >> 3)) * PAIR + (unsigned)(r & 7) * 1024u, pk8(x[rr]));
      st16(X, (unsigned)xp * 16u + o9, sb + (P_H + 4u) * PAIR + 4096u + (unsigned)r * 128u, gat8(x[rr][8]));
    }
    lnR<2>(x, ln);
#pragma unroll
    for (int rr = 0; rr < 2; ++rr) {
      const int r = (t0 + 4 * rr) & 31;
      st16(X, hoff(r, lane), sb, pk8(ln[rr]));
      st16(X, hoff(r, 64 + xp) + o9, sb, gat8(ln[rr][8]));
    }
  }
  SP(14 - 3 * ng);
  VM0();
  BAR();
  SP(15 - 3 * ng);
  if (tid < ng) st4(X, (unsigned)tid * 128u, sb + P_FB * PAIR + 4096u + (unsigned)(grp & 3) * 128u, opq(1u));
}

__device__ __forceinline__ void arrive(const MP& p, int k, int grp) {
  VM0();
  BAR();
  if (threadIdx.x < 64) {
    unsigned old = 0u;
    if (threadIdx.x == 0) old = __hip_atomic_fetch_add(p.xb + MF_OFF / 4u + (k * 16 + grp) * 32, 1u, __ATOMIC_RELAXED, __HIP_MEMORY_SCOPE_AGENT);
    if (__builtin_amdgcn_readfirstlane(old) == 15u && threadIdx.x < 8) st4(mkr(p.xb, XB_BYTES), threadIdx.x * 128u, RDY_OFF(k) + (unsigned)grp * 1024u, opq(1u));
  }
}
__device__ __forceinline__ void wait_ready(const MP& p, int k) {
  if (threadIdx.x < 64) wait_all<PSL>(mkr(p.xb, XB_BYTES), (threadIdx.x % 9u) * 1024u, RDY_OFF(k) + (blockIdx.x & 7u) * 128u);
  BAR();
}
__device__ __forceinline__ void start_work(char* smem, int wk) {
  const MP p = getp();
  const int w = __builtin_amdgcn_readfirstlane(threadIdx.x >> 6);
#define NARM (ARMK - 3u)
  conv_task(p.kvT, p.ssm_k, p.ssm_D, p.yT, (u16*)(smem + w * 32768), 4 * wk + w, [&]() {
    const unsigned sv = opq(SENT);
    constexpr unsigned S16 = (SLOT - PAIR) / 16u;
#pragma unroll
    for (unsigned k = 0; k < (NARM * S16 + 36863u) / 36864u; ++k) {
      const unsigned i = k * 36864u + wk * 256 + threadIdx.x;
      st16(mkr(p.xb, XB_BYTES), i < NARM * S16 ? SLOT_AT(3u + i / S16) + (i % S16) * 16u : 0xF0000000u, 0u, (v4u){sv, sv, sv, sv});
    }
  });
  SP(3);
  arrive(p, 0, wk >> 4);
  SP(4);
}
__device__ __forceinline__ void token_start(char* smem) {
  TOK_COMMON();
  (void)w;
  const int k = 4 * i + xq;
  if (L || (i >= 12 && i < 16)) start_work(smem, L ? 32 + k : 80 + k);
  if (i >= (L ? 12 : 20)) {
    const unsigned long long sm = die_mask(X, SG0, 0, lane);
    int g = L ? 111 - k : k - 96;
    while (g < 120) {
      if (g >= 0 && g < 64) {
        SP(5);
        wait_ready(p, 0);
        SP(6);
      }
      rows_task(p, X, sm, (u16*)smem, g < 0 ? g + 16 : 16 + 2 * g, g < 0 ? 1 : 2);
      g += g < 0 ? 16 : (L ? 48 : 120);
    }
  }
}
__device__ __forceinline__ void token_wg(char* smem) {
  if ((unsigned)(size_t)(__attribute__((address_space(3))) char*)smem != 0u) return;
  token_start(smem);
  const int i = ((int)blockIdx.x - NCHW) >> 3;
  if (i < 12) qkv_role();
  else if (i < 20) ap_role();
  else row_role(smem);
}

__global__ __launch_bounds__(256, 1) void k_main(MP) {
  extern __shared__ __attribute__((aligned(16))) char smem[];
  asm volatile("LKBEG:");
  SP(13);
  if (blockIdx.x < NCHW) chain_wg(smem);
  else token_wg(smem);
}

extern "C" void kernel_launch(void* const* d_in, const int* in_sizes, int n_in, void* d_out, int out_size, void* d_ws,
                              size_t ws_size, hipStream_t stream) {
  const u16* A_q = (const u16*)d_in[0];
  const u16* A_k = (const u16*)d_in[1];
  const u16* A_v = (const u16*)d_in[2];
  const u16* A_shift = (const u16*)d_in[3];
  const u16* A_ssmk = (const u16*)d_in[4];
  const u16* A_ssmD = (const u16*)d_in[5];
  const u16* B_mem0 = (const u16*)d_in[7];
  const u16* B_Wq = (const u16*)d_in[8];
  const u16* B_Wk = (const u16*)d_in[9];
  const u16* B_Wv = (const u16*)d_in[10];
  const u16* B_Wo = (const u16*)d_in[11];

  char* ws = (char*)d_ws;
  size_t off = (size_t)(-(uintptr_t)ws) & (XBLK - 1u);
  auto alloc = [&](size_t bytes) { char* q = ws + off; off += (bytes + 4095) & ~(size_t)4095; return q; };
  unsigned* xb = (unsigned*)alloc((size_t)XB_BYTES);
  u16* kvT = (u16*)alloc((size_t)DM * S_LEN * 2);
  u16* yT = (u16*)alloc((size_t)DM * S_LEN * 2);

  PreP pp;
  pp.kk = A_k; pp.vv = A_v; pp.shift_k = A_shift; pp.ssm_k = A_ssmk; pp.ssm_D = A_ssmD; pp.yT = yT; pp.kvT = kvT;
  pp.Wq = B_Wq; pp.Wk = B_Wk; pp.Wv = B_Wv; pp.Wo = B_Wo; pp.xb = xb; pp.mem0 = B_mem0;
  k_pre<<<240 + PRE_FILL + 256 + 288, 256, 0, stream>>>(pp);
  MP mp;
  mp.out = (u16*)d_out; mp.xb = xb;
  mp.xin = A_q; mp.yT = yT; mp.kvT = kvT; mp.ssm_k = A_ssmk; mp.ssm_D = A_ssmD;
  mp.Wq = B_Wq; mp.Wk = B_Wk; mp.Wv = B_Wv; mp.Wo = B_Wo;
  const int smem_bytes = 160 * 1024;
  (void)hipFuncSetAttribute((const void*)k_main, hipFuncAttributeMaxDynamicSharedMemorySize, smem_bytes);
  k_main<<<NCHW + NTOKW, 256, smem_bytes, stream>>>(mp);
}
```

```cpp
#include <hip/hip_runtime.h>
#include <hip/hip_bf16.h>
#include <stdint.h>

typedef unsigned short u16;
using bf16x8 = __attribute__((ext_vector_type(8))) short;
using bf16x4 = __attribute__((ext_vector_type(4))) short;
using f32x4 = __attribute__((ext_vector_type(4))) float;
using f32x2 = __attribute__((ext_vector_type(2))) float;
using f32x16 = __attribute__((ext_vector_type(16))) float;
using bfx8 = __attribute__((ext_vector_type(8))) __bf16;
using bfx2 = __attribute__((ext_vector_type(2))) __bf16;
typedef unsigned v4u __attribute__((ext_vector_type(4)));
typedef unsigned v2u __attribute__((ext_vector_type(2)));
typedef unsigned v8u __attribute__((ext_vector_type(8)));
typedef short v4s __attribute__((ext_vector_type(4)));

#define S_LEN 2048
#define DM 576
#define NH 32
#define DH 128
#define HD (NH * DH)
#define MEMR 8
#define SEG 32
#define NSEG 64

__device__ __forceinline__ float bf2f(u16 x) { return __uint_as_float(((unsigned)x) << 16); }
__device__ __forceinline__ u16 f2bf(float f) {
  unsigned u = __float_as_uint(f);
  u += 0x7FFFu + ((u >> 16) & 1u);
  return (u16)(u >> 16);
}
__device__ __forceinline__ float rbf(float f) { return bf2f(f2bf(f)); }
__device__ __forceinline__ unsigned pk2(float a, float b) { f32x2 v = {a, b}; bfx2 r = __builtin_convertvector(v, bfx2); return __builtin_bit_cast(unsigned, r); }
__device__ __forceinline__ float lo2f(unsigned u) { return __uint_as_float(u << 16); }
__device__ __forceinline__ float hi2f(unsigned u) { return __uint_as_float(u & 0xFFFF0000u); }

__device__ __forceinline__ float wave_sum(float v) {
#pragma unroll
  for (int o = 32; o > 0; o >>= 1) v += __shfl_xor(v, o, 64);
  return v;
}

#define NFC 128
#define XBLK 2097152u
#define PAIR 16384u
#define SLOT 688128u
#define AGO 589824u
#define AGT 655360u
#define SG0 22
#define TP0 86
#define NXB 95
#define XB_BYTES (NXB * XBLK)
#define SLOT_AT(i) ((unsigned)((i) / 3) * XBLK + (unsigned)((i) % 3) * SLOT)
#define HT_OFF SLOT_AT(65)
#define CTAB_OFF (HT_OFF + 16384u)
#define MF_OFF (HT_OFF + 20480u)
#define TMO_OFF (MF_OFF + 15u * 128u)
#define RDY_OFF(k) (MF_OFF + 4096u + (k) * 10240u)
#define MF_BYTES 16384u
#define TRC_OFF SLOT_AT(64)
#define CAL_OFF (XBLK - 16384u)
#define P_KV 96u
#define P_H 112u
#define P_X3 117u
#define P_FA 121u
#define P_FB 122u
#define ARMK 8
#define NCAL 8
#define NCA2 0
#define SRING 66
#define KPM 0
__device__ __forceinline__ unsigned kv2(const unsigned (&u)[4], unsigned v, v2u w0, v2u w1) {
  const float a = __builtin_fmaf(lo2f(u[3]), lo2f(w0.x), __builtin_fmaf(lo2f(u[2]), hi2f(w0.x), __builtin_fmaf(lo2f(u[1]), lo2f(w0.y), lo2f(u[0]) * hi2f(w0.y))));
  const float b = __builtin_fmaf(hi2f(u[3]), lo2f(w1.x), __builtin_fmaf(hi2f(u[2]), hi2f(w1.x), __builtin_fmaf(hi2f(u[1]), lo2f(w1.y), hi2f(u[0]) * hi2f(w1.y))));
  return pk2(a * lo2f(v), b * hi2f(v));
}
struct PreP {
  const u16* kk; const u16* vv; const u16* shift_k; const u16* ssm_k; const u16* ssm_D; u16* yT; u16* kvT;
  const u16* Wq; const u16* Wk; const u16* Wv; const u16* Wo; unsigned* xb; const u16* mem0;
};
#define PRE_FILL 112
#define PFN 4
__global__ __launch_bounds__(256, 4) void k_pre(PreP p) {
  __shared__ __attribute__((aligned(16))) float sh[64 * 36];
  const int tid = threadIdx.x, b0 = blockIdx.x, b = b0 < 240 ? b0 : (b0 < 528 ? b0 + PRE_FILL + 256 : b0 - 288);
  const __amdgpu_buffer_rsrc_t rx = __builtin_amdgcn_make_buffer_rsrc((void*)p.xb, 0, XB_BYTES, 0x00020000);
  const v4u ones = {0xFFFFFFFFu, 0xFFFFFFFFu, 0xFFFFFFFFu, 0xFFFFFFFFu}, zero = {0, 0, 0, 0};
  if (b < 96) {
    if (tid >= 64 || (KPM & 16)) return;
    const unsigned cb = (unsigned)b * XBLK + CAL_OFF;
    __builtin_amdgcn_raw_buffer_store_b128(zero, rx, (unsigned)(tid & 1) * 8192u + (unsigned)(tid >> 1) * 256u, cb, 16);
    if (b < SG0) __builtin_amdgcn_raw_buffer_store_b128(zero, rx, (unsigned)tid * 512u + 128u, cb - PAIR, 16);
    asm volatile("s_waitcnt vmcnt(0)" ::: "memory");
    unsigned mn[2] = {~0u, ~0u};
    for (int i = 0; i < NCAL; ++i) {
      const unsigned long long c0 = __builtin_amdgcn_s_memtime();
      asm volatile("s_waitcnt lgkmcnt(0)" ::: "memory");
      const unsigned v = __builtin_amdgcn_raw_buffer_load_b32(rx, 0, cb + (unsigned)(i & 1) * 8192u + (unsigned)(i >> 1) * 256u, 16);
      asm volatile("s_waitcnt vmcnt(0)" :: "v"(v) : "memory");
      const unsigned d = (unsigned)(__builtin_amdgcn_s_memtime() - c0);
      if (i & 1) mn[1] = d < mn[1] ? d : mn[1]; else mn[0] = d < mn[0] ? d : mn[0];
    }
    unsigned xcc; asm volatile("s_getreg_b32 %0, hwreg(HW_REG_XCC_ID)" : "=s"(xcc));
    unsigned res = mn[1] < mn[0] ? 1u : 0u;
    if (b < SG0) {
      unsigned m4[4] = {~0u, ~0u, ~0u, ~0u};
      for (int i = 0; i < NCA2; ++i) {
#pragma unroll
        for (int k = 0; k < 4; ++k) {
          const unsigned long long c0 = __builtin_amdgcn_s_memtime();
          asm volatile("s_waitcnt lgkmcnt(0)" ::: "memory");
          const unsigned v = __builtin_amdgcn_raw_buffer_load_b32(rx, 0, cb - PAIR + (unsigned)(k & 1) * PAIR + res * 8192u + (unsigned)(k >> 1) * 4096u + (unsigned)i * 1024u + 128u, 16);
          asm volatile("s_waitcnt vmcnt(0)" :: "v"(v) : "memory");
          const unsigned d = (unsigned)(__builtin_amdgcn_s_memtime() - c0);
          m4[k] = d < m4[k] ? d : m4[k];
        }
      }
      res |= ((m4[1] + m4[3] < m4[0] + m4[2] ? 0u : 2u) | (m4[2] + m4[3] < m4[0] + m4[1] ? 0u : 4u)) ^ ((xcc & 3u) << 1);
    }
    if (tid == 0) __builtin_amdgcn_raw_buffer_store_b32(res ^ ((xcc >> 2) & 1u), rx, 0, CTAB_OFF + (unsigned)b * 4u, 16);
  } else if (b < 240) {
    if (KPM & 1) return;
    const __amdgpu_buffer_rsrc_t rk = __builtin_amdgcn_make_buffer_rsrc((void*)p.kk, 0, S_LEN * DM * 2, 0x00020000);
    float (*kx)[132] = (float (*)[132])sh;
    float (*cf)[140] = (float (*)[140])(sh + 4 * 132);
    const int d0 = (b - 96) * 4, q = tid & 3, dd = (tid >> 2) & 3, t0 = (tid >> 4) * 8;
    const float Dv = bf2f(p.ssm_D[d0 + dd]);
    {
      const int s = tid >> 1, d = d0 + (tid & 1) * 2;
      unsigned u[4];
#pragma unroll
      for (int a = 0; a < 4; ++a) u[a] = __builtin_amdgcn_raw_buffer_load_b32(rk, (unsigned)((s - 3 + a) * DM + d) * 2u, 0, 0);
      const unsigned vr = *(const unsigned*)&p.vv[s * DM + d], c0 = *(const unsigned*)&p.ssm_k[(long)(d0 + (tid >> 6)) * S_LEN + (tid & 63) * 2];
      const v4u w = *(const v4u*)&p.shift_k[d * 4];
      const unsigned r = kv2(u, vr, (v2u){w.x, w.y}, (v2u){w.z, w.w});
      kx[(tid & 1) * 2][s] = lo2f(r); kx[(tid & 1) * 2 + 1][s] = hi2f(r);
      *(f32x2*)&cf[tid >> 6][8 + (tid & 63) * 2] = (f32x2){lo2f(c0), hi2f(c0)};
      if (tid < 32) cf[tid >> 3][tid & 7] = 0.f;
    }
    __syncthreads();
    float y[8];
#pragma unroll
    for (int i = 0; i < 8; ++i) y[i] = 0.f;
#pragma unroll 1
    for (int j = 8 * q; j <= t0; j += 32) {
      f32x4 kk[2], cw[4];
#pragma unroll
      for (int x = 0; x < 2; ++x) kk[x] = *(const f32x4*)&kx[dd][j + 4 * x];
#pragma unroll
      for (int x = 0; x < 4; ++x) cw[x] = *(const f32x4*)&cf[dd][t0 - j + 4 * x];
#pragma unroll
      for (int jj = 0; jj < 8; ++jj)
#pragma unroll
        for (int i = 0; i < 8; ++i) y[i] = __builtin_fmaf(cw[(8 + i - jj) >> 2][(8 + i - jj) & 3], kk[jj >> 2][jj & 3], y[i]);
    }
#pragma unroll
    for (int i = 0; i < 8; ++i) {
      y[i] += __uint_as_float((unsigned)__builtin_amdgcn_mov_dpp((int)__float_as_uint(y[i]), 0xB1, 0xf, 0xf, true));
      y[i] += __uint_as_float((unsigned)__builtin_amdgcn_mov_dpp((int)__float_as_uint(y[i]), 0x4E, 0xf, 0xf, true));
    }
    const float ya = q == 0 ? y[0] : (q == 1 ? y[2] : (q == 2 ? y[4] : y[6])), yb = q == 0 ? y[1] : (q == 1 ? y[3] : (q == 2 ? y[5] : y[7]));
    const int t = t0 + 2 * q, f = (d0 >> 3) & 15;
    const unsigned r0 = pk2(ya, yb), r1 = pk2(kx[dd][t] * Dv, kx[dd][t + 1] * Dv), o = pk2(lo2f(r0) + lo2f(r1), hi2f(r0) + hi2f(r1));
    u16* dst = p.yT + (t >> 4) * 9216 + (d0 >> 2) * 64 + dd;
    dst[((t & 15) ^ f) << 2] = (u16)(o & 0xFFFFu); dst[(((t + 1) & 15) ^ f) << 2] = (u16)(o >> 16);
  } else if (b < 240 + PRE_FILL) {
    if (KPM & 32) return;
    const unsigned i0 = (unsigned)(b - 240) * 256u + tid;
    if (i0 < (SLOT - AGO) / 16u) {
      const unsigned o = AGO + i0 * 16u, s = o < AGT ? (i0 >> 11) * 4u + ((o >> 10) & 3u) : 8u, r = o & 1023u;
      const unsigned gm = s < 8u ? r >> 8 : (r >> 6) & 3u, m3 = (r >> 4) & 3u, ga = s < 8u ? (r >> 6) & 3u : r >> 8, row = 4u * (gm & 1u) + 2u * (gm >> 1) + (m3 & 1u), half = m3 >> 1;
      const unsigned* src = (const unsigned*)(p.mem0 + row * DM + 64u * s + 16u * ga + 2u * half);
      __builtin_amdgcn_raw_buffer_store_b128((v4u){src[0], src[2], src[4], src[6]}, rx, o, 0, 16);
    }
    for (unsigned i = i0; i < 131072u + MF_BYTES / 16u; i += PRE_FILL * 256)
      __builtin_amdgcn_raw_buffer_store_b128(zero, rx, i < 131072u ? (SG0 + (i >> 11)) * XBLK + P_FA * PAIR + (i & 2047u) * 16u : MF_OFF + (i & 2047u) * 16u, 0, 16);
    for (unsigned i = i0; i < 2u * SLOT / 16u; i += PRE_FILL * 256) __builtin_amdgcn_raw_buffer_store_b128(ones, rx, SLOT + i * 16u, 0, 16);
  } else if (b >= 240 + PRE_FILL + 256) {
    if (KPM & 2) return;
    const __amdgpu_buffer_rsrc_t rk = __builtin_amdgcn_make_buffer_rsrc((void*)p.kk, 0, S_LEN * DM * 2, 0x00020000);
    u16 (*tile)[72] = (u16 (*)[72])sh;
    const int bid = b - 240 - PRE_FILL - 256, d0 = (bid % 9) * 64, s0 = (bid / 9) * 64;
    const int sr = (tid >> 3) * 2, d = d0 + (tid & 7) * 8;
    v4u kr[5], vr[2], w[4];
#pragma unroll
    for (int a = 0; a < 5; ++a) kr[a] = __builtin_amdgcn_raw_buffer_load_b128(rk, (unsigned)((s0 + sr - 3 + a) * DM + d) * 2u, 0, 0);
#pragma unroll
    for (int a = 0; a < 2; ++a) vr[a] = *(const v4u*)&p.vv[(long)(s0 + sr + a) * DM + d];
#pragma unroll
    for (int a = 0; a < 4; ++a) w[a] = *(const v4u*)&p.shift_k[(d + 2 * a) * 4];
    __builtin_amdgcn_sched_barrier(0);
#pragma unroll
    for (int it = 0; it < 2; ++it) {
      v4u o;
#pragma unroll
      for (int j = 0; j < 4; ++j) { const unsigned u[4] = {kr[it][j], kr[it + 1][j], kr[it + 2][j], kr[it + 3][j]}; o[j] = kv2(u, vr[it][j], (v2u){w[j].x, w[j].y}, (v2u){w[j].z, w[j].w}); }
      *(v4u*)&tile[sr + it][(tid & 7) * 8] = o;
    }
    __syncthreads();
    const int lane = tid & 63, wv = tid >> 6, g = lane >> 4, r = (lane >> 2) & 3, c = lane & 3;
    v2u o[4];
#pragma unroll
    for (int i = 0; i < 4; ++i) o[i] = __builtin_bit_cast(v2u, __builtin_amdgcn_ds_read_tr16_b64_v4i16((__attribute__((address_space(3))) v4s*)&tile[16 * g + 4 * i + r][16 * wv + 4 * c]));
    u16* dst = p.kvT + (long)(d0 + 16 * wv + (lane & 15)) * S_LEN + s0 + 16 * g;
    *(v4u*)dst = (v4u){o[0].x, o[0].y, o[1].x, o[1].y};
    *(v4u*)(dst + 8) = (v4u){o[2].x, o[2].y, o[3].x, o[3].y};
  } else {
    if (KPM & 4) return;
    const int x = b & 7, k = (b - 240 - PRE_FILL) >> 3;
    unsigned long long pc; unsigned l0, l1, l2, l3; int rel;
    asm volatile("s_getpc_b64 %0\nLPRE:\n\ts_mov_b32 %1, LKBEG-LPRE\n\ts_mov_b32 %2, LCHEND-LKBEG\n\ts_mov_b32 %3, LQVEND-LKBEG\n\ts_mov_b32 %4, LQKEND-LKBEG\n\ts_mov_b32 %5, LAPEND-LKBEG"
                 : "=s"(pc), "=s"(rel), "=s"(l0), "=s"(l1), "=s"(l2), "=s"(l3));
    l0 = l0 > l1 ? l0 : l1; l2 = l2 > l3 ? l2 : l3; l0 = l0 > l2 ? l0 : l2;
    const unsigned o = (unsigned)(k * 64 + (tid & 63)) * 64u;
    unsigned a[PFN + 2];
    a[PFN] = *(const __attribute__((address_space(1))) unsigned*)(pc + rel + (o < l0 ? o : 0u));
    a[PFN + 1] = __builtin_amdgcn_raw_buffer_load_b32(rx, k == 31 ? tid * XBLK + CAL_OFF + 8064u : 0xF0000000u, 0, 16);
#pragma unroll
    for (int i = 0; i < PFN; ++i) {
      const int q = (i * 32 + k) * 256 + tid, r2 = q >> 5, gr = r2 >> 5, row = (gr / 3) * 64 + (x >> 2) * 32 + (r2 & 31), off = (q & 31) * 32, ty = gr % 3;
      a[i] = *(const unsigned*)(gr < 27 ? (ty == 0 ? p.Wq : (ty == 1 ? p.Wk : p.Wv)) + (long)row * HD + (x & 3) * 1024 + off
                                        : p.Wo + ((long)(x & 3) * DM + (gr < 36 ? gr - 27 : 0) * 64 + (x >> 2) * 32 + (r2 & 31)) * 1024 + off);
    }
#pragma unroll
    for (int i = 0; i < PFN + 2; ++i) asm volatile("" :: "v"(a[i]));
  }
}

#define CREV_STRIDE 2208
#define SP(k)
#define LWAIT() asm volatile("s_waitcnt lgkmcnt(0)" ::: "memory")
template <class F>
__device__ __forceinline__ void conv_task(const u16* __restrict__ kvT, const u16* __restrict__ ssm_k, const u16* __restrict__ ssm_D, u16* yT, u16* shb, int d, F fill) {
  const int lane = threadIdx.x & 63;
  u16* crev = shb + 64;
  u16* kvs = crev + 4 * CREV_STRIDE;
  const int X0 = 2048 + 63;
  SP(0);
  v4u cv[4], kq[4];
#pragma unroll
  for (int j = 0; j < 4; ++j) { cv[j] = *(const v4u*)&ssm_k[(long)d * S_LEN + lane * 32 + 8 * j]; kq[j] = *(const v4u*)&kvT[(long)d * S_LEN + (j * 64 + lane) * 8]; }
  const float Dv = bf2f(ssm_D[d]);
  const v4u z4 = {0u, 0u, 0u, 0u};
  *(v4u*)&kvs[(lane >> 2) * 40 + (lane & 3) * 8] = z4;
  if (lane < 32) *(v4u*)&crev[(lane >> 3) * CREV_STRIDE + (lane & 7) * 8] = z4;
  if (lane < 36) *(v4u*)&crev[(lane / 9) * CREV_STRIDE + 2112 + (lane % 9) * 8] = z4;
  {
    unsigned r[18];
#pragma unroll
    for (int k = 0; k < 16; ++k) { const unsigned u = cv[3 - (k >> 2)][3 - (k & 3)]; r[k] = __builtin_amdgcn_alignbyte(u, u, 2); }
#pragma unroll
    for (int k = 0; k < 2; ++k) r[16 + k] = (unsigned)__builtin_amdgcn_update_dpp(0, (int)r[k], 0x138, 0xf, 0xf, true);
    LWAIT();
#pragma unroll
    for (int rho = 0; rho < 4; ++rho)
#pragma unroll
      for (int j = 0; j < 4; ++j) {
        v4u o;
#pragma unroll
        for (int k = 0; k < 4; ++k) { const int q = 4 * j + k + (rho >> 1); o[k] = (rho & 1) ? __builtin_amdgcn_alignbyte(r[q + 1], r[q], 2) : r[q]; }
        *(v4u*)&crev[rho * CREV_STRIDE + 2080 - 32 * lane + 8 * j] = o;
      }
    if (lane == 63) {
      *(v2u*)&crev[1 * CREV_STRIDE + 60] = (v2u){0u, r[0] << 16};
      *(v2u*)&crev[2 * CREV_STRIDE + 60] = (v2u){0u, r[0]};
      *(v2u*)&crev[3 * CREV_STRIDE + 60] = (v2u){r[0] << 16, __builtin_amdgcn_alignbyte(r[1], r[0], 2)};
    }
#pragma unroll
    for (int j = 0; j < 4; ++j) *(v4u*)&kvs[(16 + j * 16 + (lane >> 2)) * 40 + (lane & 3) * 8] = kq[j];
  }
  fill();
  LWAIT();
  SP(1);
  const int n = lane & 15, g = lane >> 4;
  const int rho = (X0 - n) & 3;
  f32x4 acc[4][2];
#pragma unroll
  for (int q = 0; q < 8; ++q) acc[q >> 1][q & 1] = (f32x4){0.f, 0.f, 0.f, 0.f};
  {
    typedef __attribute__((address_space(3))) const v2u* l2p;
    typedef __attribute__((address_space(3))) const bf16x8* l4p;
    unsigned a0 = (unsigned)(size_t)(__attribute__((address_space(3))) u16*)&crev[rho * CREV_STRIDE + (X0 - (n - 8 * g) - rho)] - 3232u, a1 = a0 + 8u;
    unsigned b0 = (unsigned)(size_t)(__attribute__((address_space(3))) u16*)&kvs[(14 + n) * 40 + 8 * g];
    asm volatile("" : "+v"(a1));
#define AFR(m, f) ({ const unsigned o_ = 3232u - 64u * (m) - 1024u * ((f) >> 1) - 32u * ((f) & 1); const v2u lo_ = *(l2p)(size_t)(a0 + o_), hi_ = *(l2p)(size_t)(a1 + o_); \
                     __builtin_bit_cast(bf16x8, (v4u){lo_.x, lo_.y, hi_.x, hi_.y}); })
#define BFR(m, j) (*(l4p)(size_t)(b0 + 80u * (2 - (m)) + 1280u * (j)))
    bf16x8 af[2][8], bf[2][4];
#pragma unroll
    for (int f = 0; f < 8; ++f) { af[0][f] = AFR(0, f); af[1][f] = af[0][f]; __builtin_amdgcn_sched_barrier(0); }
#pragma unroll
    for (int j = 0; j < 4; ++j) { bf[0][j] = BFR(0, j); bf[1][j] = bf[0][j]; }
#pragma unroll 1
    for (int mo = 0; mo < 8; ++mo) {
#pragma unroll
      for (int m = 0; m < 2; ++m) {
        int k = 0;
#pragma unroll
        for (int a = 0; a < 4; ++a)
#pragma unroll
          for (int ct = a; ct < 4; ++ct)
#pragma unroll
            for (int ti = 0; ti < 2; ++ti) {
              acc[ct][ti] = __builtin_amdgcn_mfma_f32_16x16x32_bf16(__builtin_bit_cast(bfx8, af[m][2 * a + ti]), __builtin_bit_cast(bfx8, bf[m][ct - a]), acc[ct][ti], 0, 0, 0);
              if (k < 2) af[m ^ 1][k] = AFR(m + 1, k);
              else if (k < 6) bf[m ^ 1][k - 2] = BFR(m + 1, k - 2);
              else if (k < 12) af[m ^ 1][k - 4] = AFR(m + 1, k - 4);
              __builtin_amdgcn_sched_barrier(0);
              ++k;
            }
      }
      a0 -= 128u; a1 -= 128u; b0 -= 160u;
    }
  }
  SP(2);
  u16* yo = crev;
  LWAIT();
#pragma unroll
  for (int q = 0; q < 8; ++q) {
    const int o = (16 * (q >> 1) + n) * 40 + 16 * (q & 1) + 4 * g;
    const v2u kx = *(const v2u*)&kvs[16 * 40 + o];
    const f32x4 y = acc[q >> 1][q & 1];
    const unsigned y0 = pk2(y[0], y[1]), y1 = pk2(y[2], y[3]), r0 = pk2(lo2f(kx.x) * Dv, hi2f(kx.x) * Dv), r1 = pk2(lo2f(kx.y) * Dv, hi2f(kx.y) * Dv);
    *(v2u*)&yo[o] = (v2u){pk2(lo2f(y0) + lo2f(r0), hi2f(y0) + hi2f(r0)), pk2(lo2f(y1) + lo2f(r1), hi2f(y1) + hi2f(r1))};
  }
  LWAIT();
  __builtin_amdgcn_s_barrier();
  asm volatile("" ::: "memory");
  {
    const int tid = threadIdx.x, f = (d >> 3) & 15;
    const u16* y0 = shb - (tid >> 6) * 16384 + 64;
#pragma unroll
    for (int k = 0; k < 4; ++k) {
      const int tile = 32 * k + (tid >> 3), t = 16 * tile + 2 * ((tid & 7) ^ (f >> 1));
      unsigned u[4];
#pragma unroll
      for (int c = 0; c < 4; ++c) u[c] = *(const unsigned*)&y0[c * 16384 + (t >> 5) * 40 + (t & 31)];
      const v2u e = {__builtin_amdgcn_perm(u[1], u[0], 0x05040100u), __builtin_amdgcn_perm(u[3], u[2], 0x05040100u)}, o = {__builtin_amdgcn_perm(u[1], u[0], 0x07060302u), __builtin_amdgcn_perm(u[3], u[2], 0x07060302u)};
      if (tile >= NFC / 16) __builtin_amdgcn_raw_buffer_store_b128((f & 1) ? (v4u){o.x, o.y, e.x, e.y} : (v4u){e.x, e.y, o.x, o.y}, __builtin_amdgcn_make_buffer_rsrc((void*)yT, 0, DM * S_LEN * 2, 0x00020000),
                                                                   tile * 18432 + (tid & 7) * 16, (d >> 2) * 128, 16);
    }
  }
}

#define NCHW 64
#define NTOKW 192
#define PSL2 10
#define PSL 4
#define SENT 0xFFFFFFFFu
#define SPIN_CAP 300000u
#define MAP 0
__device__ __forceinline__ int vblock() {
  const int b = blockIdx.x;
  if (MAP == 0) return b;
  const int L4 = b & 4, x = b & 3, j = b >> 3;
  if (MAP == 1) {
    if (x == 0) return (j >> 3) + L4 + 8 * (j & 7);
    const int k = j * 3 + x - 1;
    return 64 + (k & 3) + L4 + 8 * (k >> 2);
  }
  if (x < 2) {
    if (j < 16) return 2 * x + (j >> 3) + L4 + 8 * (j & 7);
    const int q = 2 * x + (j & 1), i = 16 + ((j - 16) >> 1);
    return 64 + q + L4 + 8 * i;
  }
  return 64 + 2 * (x - 2) + (j & 1) + L4 + 8 * (j >> 1);
}
struct MP {
  const u16* Wq; const u16* Wk; const u16* Wv; const u16* Wo; u16* out; unsigned* xb;
  const u16* xin; u16* yT; u16* kvT; const u16* ssm_k; const u16* ssm_D;
};

__device__ __forceinline__ MP getp() {
  MP r;
#if defined(__HIP_DEVICE_COMPILE__)
  const __attribute__((address_space(4))) unsigned long long* k = (const __attribute__((address_space(4))) unsigned long long*)__builtin_amdgcn_kernarg_segment_ptr();
  asm volatile("" : "+s"(k));
#define GP(T, i) ((T*)(__attribute__((address_space(1))) T*)k[i])
  r.Wq = GP(const u16, 0);
  r.Wk = GP(const u16, 1);
  r.Wv = GP(const u16, 2);
  r.Wo = GP(const u16, 3);
  r.out = GP(u16, 4);
  r.xb = GP(unsigned, 5);
  r.xin = GP(const u16, 6);
  r.yT = GP(u16, 7);
  r.kvT = GP(u16, 8);
  r.ssm_k = GP(const u16, 9);
  r.ssm_D = GP(const u16, 10);
#endif
  return r;
}
typedef __amdgpu_buffer_rsrc_t rsrc_t;
__device__ __forceinline__ rsrc_t mkr(const void* p, unsigned bytes) { return __builtin_amdgcn_make_buffer_rsrc((void*)p, 0, bytes, 0x00020000); }
__device__ __forceinline__ void st16(rsrc_t r, unsigned vo, unsigned so, v4u v) { __builtin_amdgcn_raw_buffer_store_b128(v, r, vo, so, 16); }
__device__ __forceinline__ void st8(rsrc_t r, unsigned vo, unsigned so, v2u v) { __builtin_amdgcn_raw_buffer_store_b64(v, r, vo, so, 16); }
__device__ __forceinline__ void st4(rsrc_t r, unsigned vo, unsigned so, unsigned v) { __builtin_amdgcn_raw_buffer_store_b32(v, r, vo, so, 16); }
__device__ __forceinline__ v4u ld16(rsrc_t r, unsigned vo, unsigned so) { return __builtin_amdgcn_raw_buffer_load_b128(r, vo, so, 16); }
__device__ __forceinline__ v2u ld8(rsrc_t r, unsigned vo, unsigned so) { return __builtin_amdgcn_raw_buffer_load_b64(r, vo, so, 16); }
__device__ __forceinline__ unsigned ld4(rsrc_t r, unsigned vo, unsigned so) { return __builtin_amdgcn_raw_buffer_load_b32(r, vo, so, 16); }
__device__ __forceinline__ float sum4g(float v) {
  auto r = __builtin_amdgcn_permlane16_swap(__float_as_uint(v), __float_as_uint(v), false, false);
  v = __uint_as_float(r[0]) + __uint_as_float(r[1]);
  auto q = __builtin_amdgcn_permlane32_swap(__float_as_uint(v), __float_as_uint(v), false, false);
  return __uint_as_float(q[0]) + __uint_as_float(q[1]);
}
__device__ __forceinline__ unsigned opq(unsigned x) { asm volatile("" : "+v"(x)); return x; }

__device__ __forceinline__ unsigned long long die_mask(rsrc_t X, unsigned first, int L, int lane) { return __ballot((ld4(X, (unsigned)lane * 4u, CTAB_OFF + first * 4u) & 1u) != (unsigned)L); }
#define SEGB(m, s) ((unsigned)(SG0 + (s)) * XBLK + (unsigned)(((m) >> (s)) & 1ull) * 8192u)
__device__ __forceinline__ bool spin_fail(unsigned& spins, rsrc_t F) {
  ++spins;
  if (spins > SPIN_CAP) { unsigned one = 0x40000000u | (blockIdx.x << 8) | threadIdx.x; asm volatile("" : "+v"(one)); if (__builtin_amdgcn_readfirstlane(ld4(F, 0, TMO_OFF)) == 0u) st4(F, 0, TMO_OFF, one); return true; }
  if ((spins & 1023u) == 0u) return __builtin_amdgcn_readfirstlane(ld4(F, 0, TMO_OFF)) != 0;
  return false;
}
__device__ __forceinline__ unsigned rtc() { return (unsigned)__builtin_amdgcn_s_memrealtime(); }
__device__ __forceinline__ unsigned wait_until(unsigned T) { unsigned n = rtc(); for (int i = 0; i < 8192 && (int)(n - T) < 0; ++i) n = rtc(); return n; }
__device__ __forceinline__ unsigned umax3(unsigned a, unsigned b, unsigned c) { unsigned m = a > b ? a : b; return m > c ? m : c; }
__device__ __forceinline__ float fmx(float a, float b) { float d; asm("v_max_f32 %0, %1, %2\n\ts_nop 1" : "=v"(d) : "v"(a), "v"(b)); return d; }
__device__ __forceinline__ float fmx3(float a, float b, float c) { float d; asm("v_max3_f32 %0, %1, %2, %3" : "=v"(d) : "v"(a), "v"(b), "v"(c)); return d; }
__device__ __forceinline__ float max4gx(float v) {
  auto r = __builtin_amdgcn_permlane16_swap(__float_as_uint(v), __float_as_uint(v), false, false);
  v = fmx(__uint_as_float(r[0]), __uint_as_float(r[1]));
  auto q = __builtin_amdgcn_permlane32_swap(__float_as_uint(v), __float_as_uint(v), false, false);
  return fmx(__uint_as_float(q[0]), __uint_as_float(q[1]));
}
__device__ __forceinline__ float ror8(float v) { return __uint_as_float((unsigned)__builtin_amdgcn_update_dpp(0, (int)__float_as_uint(v), 0x128, 0xf, 0xf, false)); }
__device__ __forceinline__ unsigned ror8u(unsigned v) { return (unsigned)__builtin_amdgcn_update_dpp(0, (int)v, 0x128, 0xf, 0xf, false); }
__device__ __forceinline__ unsigned wave_umax(unsigned v, int lane) {
#pragma unroll
  for (int o = 32; o > 0; o >>= 1) { unsigned x = (unsigned)__builtin_amdgcn_ds_bpermute((lane ^ o) << 2, (int)v); v = x > v ? x : v; }
  return v;
}
#define BAR() do { asm volatile("s_waitcnt lgkmcnt(0)" ::: "memory"); __builtin_amdgcn_s_barrier(); asm volatile("" ::: "memory"); } while (0)
#define SB() __builtin_amdgcn_sched_barrier(0)
#define MF "v_mfma_f32_16x16x32_bf16 "
#define C2X4(d0, d1, a0, a1, b) asm volatile( \
    MF "%0, %2, %10, 0\n\t" MF "%1, %6, %10, 0\n\t" MF "%0, %3, %11, %0\n\t" MF "%1, %7, %11, %1\n\t" \
    MF "%0, %4, %12, %0\n\t" MF "%1, %8, %12, %1\n\t" MF "%0, %5, %13, %0\n\t" MF "%1, %9, %13, %1" \
    : "=&v"(d0), "=&v"(d1) : "v"((a0)[0]), "v"((a0)[1]), "v"((a0)[2]), "v"((a0)[3]), "v"((a1)[0]), "v"((a1)[1]), "v"((a1)[2]), "v"((a1)[3]), \
      "v"((b)[0]), "v"((b)[1]), "v"((b)[2]), "v"((b)[3]))
#define C1X4(d0, a0, b) asm volatile( \
    MF "%0, %1, %5, 0\n\t" MF "%0, %2, %6, %0\n\t" MF "%0, %3, %7, %0\n\t" MF "%0, %4, %8, %0" \
    : "=&v"(d0) : "v"((a0)[0]), "v"((a0)[1]), "v"((a0)[2]), "v"((a0)[3]), "v"((b)[0]), "v"((b)[1]), "v"((b)[2]), "v"((b)[3]))
#define VM0() __builtin_amdgcn_s_waitcnt(0x0F70)
#define DL1 12
#define SCH_B0 52
#define DLL1 6
#define DLL2 4
#define DLF 24
#define SCH_PEN 0
#define DLX 4
#define LATE 2
#define TKL 28
#define UP 1
#define DN 100
#define DL3 16
#define RO1 0
#define CH_RO1 CH_S(RO1)
#define FIN 0
#define CH_FIN CH_S(FIN)
#define RSA 0
#define AFF 0
#define AFG 0
#define CH_AFF CH_S(AFF)
#define CH_AFG CH_S(AFG)
#define RSL 2
#define CH_S2(x) #x
#define CH_S(x) CH_S2(x)
#define CH_ST "s_memtime s[62:63]\n\ts_waitcnt lgkmcnt(0)\n\ts_sub_u32 s67, s62, s67\n\t"
#define CH_BASE "s_memtime s[94:95]\n\ts_waitcnt lgkmcnt(0)\n\ts_mov_b32 s67, s94\n\t"
#define CH_DPC
#define CH_TOKCHK "s_cbranch_vccz LG3_%=\n\t"
#define CH_SEGC
#define CH_SG0 CH_S(SG0)
#define CH_ARMD "%[armd]"
#define CH_ARMS "%[arms]"
#define CH_SLOT "688128"
#define CH_SKIP "32768"
#define CH_DL1 CH_S(DL1)
#define CH_DLL1 CH_S(DLL1)
#define CH_DLL2 CH_S(DLL2)
#define CH_DLF CH_S(DLF)
#define CH_B0 CH_S(SCH_B0)
#define CH_PEN CH_S(SCH_PEN)
#define CH_RSL CH_S(RSL)
#define CH_DL3 CH_S(DL3)
#define CH_UP CH_S(UP)
#define CH_DN CH_S(DN)
#define CH_DLX CH_S(DLX)
#define CH_LATE CH_S(LATE)
#define CH_DL1L "%[dl1l]"
#define CH_TKL CH_S(TKL)
#define CH_SCHW "656384"
#define CH_CAP "%[cap]"
#define CH_CTAB "%[ctab]"
#define CH_RING "%[ring]"
#define CH_COW
#define CH_INW "s_sub_u32 s93, s48, 10\n\ts_cmp_lt_u32 s93, 50\n\t"
#define CH_ST1
#define CH_ST2
#define TOUCH 2
#define FDL 6
#define CH_FDL CH_S(FDL)
#define CH_TC0
#define CH_TCH
#define CH_ST3
#define CH_ST4
#define CH_TRC
#define CH_TG2
#define CH_TG5
#define CH_STV
#define CH_STW
#define CH_DRY "s_branch LLOAD_%=\n\t"
#define RFL(x) ((unsigned)__builtin_amdgcn_readfirstlane((int)(x)))
#define CH_DP0
#define CH_DP1
#define CH_DP2
#define CH_DP3
#define CH_DP4
#define CH_DP5
#define CH_DP6
#define CH_DP7
#define CH_DP8
#define CH_DP9
#define CH_DP10
#define CH_DP11
#define CH_DP12
#define CH_DP13
#define CH_DP14
#define CH_DP15
#define CH_DP16
#define CH_DP17
#define CH_OPS {1, 2, 3, 4, 5, 6, 7, 8, 17, 18, 20, 22, 23, 24, 64, 65, 67, 68, 69, 70, 71, 80, 81, 82, 83, 84, 85, 86, 87, 88, 128, 129, 130, 131, 132, 133, 134, 135, 136, 144, 145, 146, 147, 149, 150, 151, 195, 209, 210, 211, 227, 241, 242, 256, 0, 16, 19, 21, 66, 72, 148, 152, 208, 240, 192, 224, 193, 225, 194, 226, 243, 257}
#define CH_ASM \
  "s_mov_b32 s84, " CH_CAP "\n\t" \
  "s_mov_b32 s83, " CH_CAP "\n\t" \
  "s_mov_b32 s68, 0\n\t" \
  "s_mov_b32 s74, 0\n\t" \
  "s_mov_b32 s76, 0\n\t" \
  "s_mov_b32 s77, 0\n\t" \
  "s_mov_b32 s78, 0\n\t" \
  "s_mov_b32 s79, 0\n\t" \
  "s_mov_b32 s56, 0xcccccccc\n\t" \
  "s_mov_b32 s57, 0xcccccccc\n\t" \
  "s_mov_b32 s58, 0x33333333\n\t" \
  "s_mov_b32 s59, 0x33333333\n\t" \
  "s_mov_b32 s24, 0x01010101\n\t" \
  "s_cmp_eq_u32 s40, 0\n\t" \
  "s_cselect_b32 s25, 1, 0\n\t" \
  "s_mov_b32 s88, 0x3f803f80\n\t" \
  "s_mov_b32 s89, 0x3f803f80\n\t" \
  "s_mov_b32 s66, s65\n\t" \
  "s_mov_b32 s47, s65\n\t" \
  "s_mov_b32 s72, 0\n\t" \
  "s_mov_b32 s73, 0\n\t" \
  "s_mov_b32 s67, 0\n\t" \
  "s_mul_i32 s60, s40, 36864\n\t" \
  "s_add_u32 s60, s60, 16384\n\t" \
  "v_add_u32 v70, s60, v69\n\t" \
  "v_bfe_i32 v68, v69, 5, 1\n\t" \
  "v_and_b32 v68, 0xaaaaaaaa, v68\n\t" \
  "v_or_b32 v68, 0x44444444, v68\n\t" \
  "v_and_b32 v73, 32, v69\n\t" \
  "v_lshl_add_u32 v73, v73, 5, v69\n\t" \
  "s_mov_b32 s35, s30\n\t" \
  "s_mov_b32 s30, 0\n\t" \
  "s_mov_b32 s34, 0\n\t" \
  CH_DRY \
  "s_cmp_eq_u32 s40, 0\n\t" \
  "s_cbranch_scc1 LW0_%=\n\t" \
  "s_barrier\n\t" \
  "s_cmp_eq_u32 s40, 1\n\t" \
  "s_cbranch_scc1 LG1_%=\n\t" \
  "s_cmp_eq_u32 s40, 2\n\t" \
  "s_cbranch_scc1 LG2_%=\n\t" \
  "s_branch LG3_%=\n\t" \
  "LLOAD_%=:\n\t" \
  "s_waitcnt vmcnt(0) lgkmcnt(0)\n\t" \
  "s_mov_b32 s30, s35\n\t" \
  "s_movk_i32 s34, 1000\n\t" \
  "v_lshrrev_b32 v74, 2, v69\n\t" \
  "s_mov_b32 s60, " CH_CTAB "\n\t" \
  "buffer_load_dword v72, v74, s[28:31], s60 offen offset:88 sc1\n\t" \
  "buffer_load_dword v74, v74, s[28:31], s60 offen sc1\n\t" \
  "v_bfe_u32 v0, v69, 8, 1\n\t" \
  "v_bfe_u32 v1, v69, 9, 1\n\t" \
  "v_lshlrev_b32 v0, 2, v0\n\t" \
  "v_add_u32 v0, v0, v1\n\t" \
  "v_mul_u32_u24 v0, 4608, v0\n\t" \
  "v_lshrrev_b32 v1, 1, v69\n\t" \
  "v_and_b32 v1, 0x60, v1\n\t" \
  "v_and_b32 v2, 16, v69\n\t" \
  "v_bfe_u32 v3, v69, 5, 1\n\t" \
  "v_add3_u32 v64, v0, v1, v2\n\t" \
  "v_lshl_add_u32 v64, v3, 2, v64\n\t" \
  "v_lshl_add_u32 v67, v3, 3, v0\n\t" \
  "v_bfe_u32 v1, v69, 7, 1\n\t" \
  "v_lshl_add_u32 v67, v1, 2, v67\n\t" \
  "v_bfe_u32 v1, v69, 6, 1\n\t" \
  "v_lshl_add_u32 v67, v1, 4, v67\n\t" \
  "v_bfe_u32 v1, v69, 4, 1\n\t" \
  "v_lshl_add_u32 v67, v1, 1, v67\n\t" \
  "v_lshrrev_b32 v71, 8, v69\n\t" \
  "v_lshlrev_b32 v1, 6, v71\n\t" \
  "v_and_b32 v2, 0xf0, v69\n\t" \
  "v_xor_b32 v1, v1, v2\n\t" \
  "v_lshl_add_u32 v71, v71, 13, v1\n\t" \
  "s_lshl_b32 s60, s40, 17\n\t" \
  "v_add_u32 v71, s60, v71\n\t" \
  "v_and_b32 v1, 0x300, v69\n\t" \
  "v_lshlrev_b32 v65, 3, v1\n\t" \
  "v_and_b32 v1, 0xc0, v69\n\t" \
  "v_lshl_add_u32 v65, v1, 2, v65\n\t" \
  "s_lshl_b32 s60, s40, 6\n\t" \
  "v_xor_b32 v1, s60, v1\n\t" \
  "v_and_b32 v2, 48, v69\n\t" \
  "v_add3_u32 v65, v65, v1, v2\n\t" \
  "v_lshrrev_b32 v2, 1, v2\n\t" \
  "v_sub_u32 v66, v65, v2\n\t" \
  "s_lshl_b32 s54, s40, 12\n\t" \
  "s_lshl_b32 s55, s40, 7\n\t" \
  "s_lshl_b32 s75, s40, 5\n\t" \
  "s_add_u32 s75, s75, 1024\n\t" \
  "s_mov_b32 s62, 0x05040100\n\t" \
  "s_mov_b32 s63, 0x07060302\n\t" \
  "s_barrier\n\t" \
  "s_add_u32 m0, s54, 0\n\t" \
  "s_mov_b32 s60, 0\n\t" \
  "buffer_load_dwordx4 v71, %[Wq], s60 offen lds\n\t" \
  "s_add_u32 m0, s54, 1024\n\t" \
  "s_mov_b32 s60, 32768\n\t" \
  "buffer_load_dwordx4 v71, %[Wq], s60 offen lds\n\t" \
  "s_add_u32 m0, s54, 2048\n\t" \
  "s_mov_b32 s60, 65536\n\t" \
  "buffer_load_dwordx4 v71, %[Wq], s60 offen lds\n\t" \
  "s_add_u32 m0, s54, 3072\n\t" \
  "s_mov_b32 s60, 98304\n\t" \
  "buffer_load_dwordx4 v71, %[Wq], s60 offen lds\n\t" \
  "s_add_u32 m0, s54, 16384\n\t" \
  "s_mov_b32 s60, 0\n\t" \
  "buffer_load_dwordx4 v71, %[Wk], s60 offen lds\n\t" \
  "s_add_u32 m0, s54, 17408\n\t" \
  "s_mov_b32 s60, 32768\n\t" \
  "buffer_load_dwordx4 v71, %[Wk], s60 offen lds\n\t" \
  "s_add_u32 m0, s54, 18432\n\t" \
  "s_mov_b32 s60, 65536\n\t" \
  "buffer_load_dwordx4 v71, %[Wk], s60 offen lds\n\t" \
  "s_add_u32 m0, s54, 19456\n\t" \
  "s_mov_b32 s60, 98304\n\t" \
  "buffer_load_dwordx4 v71, %[Wk], s60 offen lds\n\t" \
  "s_add_u32 m0, s54, 53248\n\t" \
  "s_mov_b32 s60, 524288\n\t" \
  "buffer_load_dwordx4 v71, %[Wq], s60 offen lds\n\t" \
  "s_add_u32 m0, s54, 54272\n\t" \
  "s_mov_b32 s60, 557056\n\t" \
  "buffer_load_dwordx4 v71, %[Wq], s60 offen lds\n\t" \
  "s_add_u32 m0, s54, 55296\n\t" \
  "s_mov_b32 s60, 589824\n\t" \
  "buffer_load_dwordx4 v71, %[Wq], s60 offen lds\n\t" \
  "s_add_u32 m0, s54, 56320\n\t" \
  "s_mov_b32 s60, 622592\n\t" \
  "buffer_load_dwordx4 v71, %[Wq], s60 offen lds\n\t" \
  "s_add_u32 m0, s54, 90112\n\t" \
  "s_mov_b32 s60, 524288\n\t" \
  "buffer_load_dwordx4 v71, %[Wk], s60 offen lds\n\t" \
  "s_add_u32 m0, s54, 91136\n\t" \
  "s_mov_b32 s60, 557056\n\t" \
  "buffer_load_dwordx4 v71, %[Wk], s60 offen lds\n\t" \
  "s_add_u32 m0, s54, 92160\n\t" \
  "s_mov_b32 s60, 589824\n\t" \
  "buffer_load_dwordx4 v71, %[Wk], s60 offen lds\n\t" \
  "s_add_u32 m0, s54, 93184\n\t" \
  "s_mov_b32 s60, 622592\n\t" \
  "buffer_load_dwordx4 v71, %[Wk], s60 offen lds\n\t" \
  "s_add_u32 m0, s54, 126976\n\t" \
  "s_mov_b32 s60, 1048576\n\t" \
  "buffer_load_dwordx4 v71, %[Wq], s60 offen lds\n\t" \
  "s_add_u32 m0, s54, 128000\n\t" \
  "s_mov_b32 s60, 1081344\n\t" \
  "buffer_load_dwordx4 v71, %[Wq], s60 offen lds\n\t" \
  "s_add_u32 m0, s54, 129024\n\t" \
  "s_mov_b32 s60, 1114112\n\t" \
  "buffer_load_dwordx4 v71, %[Wq], s60 offen lds\n\t" \
  "s_add_u32 m0, s54, 130048\n\t" \
  "s_mov_b32 s60, 1146880\n\t" \
  "buffer_load_dwordx4 v71, %[Wq], s60 offen lds\n\t" \
  "s_xor_b32 s60, s40, 0\n\t" \
  "s_mul_i32 s60, s60, 36864\n\t" \
  "s_add_u32 s60, s60, s55\n\t" \
  "s_add_u32 s60, s60, 0\n\t" \
  "s_add_u32 s61, s60, 9216\n\t" \
  "buffer_load_dwordx3 v[80:82], v64, %[Wo], s60 offen offset:0\n\t" \
  "buffer_load_dwordx3 v[84:86], v64, %[Wo], s60 offen offset:1152\n\t" \
  "buffer_load_dwordx3 v[88:90], v64, %[Wo], s60 offen offset:2304\n\t" \
  "buffer_load_dwordx3 v[92:94], v64, %[Wo], s60 offen offset:3456\n\t" \
  "buffer_load_dwordx3 v[96:98], v64, %[Wo], s61 offen offset:0\n\t" \
  "buffer_load_dwordx3 v[100:102], v64, %[Wo], s61 offen offset:1152\n\t" \
  "buffer_load_dwordx3 v[104:106], v64, %[Wo], s61 offen offset:2304\n\t" \
  "buffer_load_dwordx3 v[108:110], v64, %[Wo], s61 offen offset:3456\n\t" \
  "s_xor_b32 s60, s40, 0\n\t" \
  "s_mul_i32 s60, s60, 36864\n\t" \
  "s_add_u32 s60, s60, s55\n\t" \
  "s_add_u32 s60, s60, 512\n\t" \
  "s_add_u32 s61, s60, 9216\n\t" \
  "buffer_load_dwordx3 v[112:114], v64, %[Wo], s60 offen offset:0\n\t" \
  "buffer_load_dwordx3 v[116:118], v64, %[Wo], s60 offen offset:1152\n\t" \
  "buffer_load_dwordx3 v[120:122], v64, %[Wo], s60 offen offset:2304\n\t" \
  "buffer_load_dwordx3 v[124:126], v64, %[Wo], s60 offen offset:3456\n\t" \
  "buffer_load_dwordx3 v[128:130], v64, %[Wo], s61 offen offset:0\n\t" \
  "buffer_load_dwordx3 v[132:134], v64, %[Wo], s61 offen offset:1152\n\t" \
  "buffer_load_dwordx3 v[136:138], v64, %[Wo], s61 offen offset:2304\n\t" \
  "buffer_load_dwordx3 v[140:142], v64, %[Wo], s61 offen offset:3456\n\t" \
  "s_xor_b32 s60, s40, 1\n\t" \
  "s_mul_i32 s60, s60, 36864\n\t" \
  "s_add_u32 s60, s60, s55\n\t" \
  "s_add_u32 s60, s60, 0\n\t" \
  "s_add_u32 s61, s60, 9216\n\t" \
  "buffer_load_dwordx3 v[144:146], v64, %[Wo], s60 offen offset:0\n\t" \
  "buffer_load_dwordx3 v[148:150], v64, %[Wo], s60 offen offset:1152\n\t" \
  "buffer_load_dwordx3 v[152:154], v64, %[Wo], s60 offen offset:2304\n\t" \
  "buffer_load_dwordx3 v[156:158], v64, %[Wo], s60 offen offset:3456\n\t" \
  "buffer_load_dwordx3 v[160:162], v64, %[Wo], s61 offen offset:0\n\t" \
  "buffer_load_dwordx3 v[164:166], v64, %[Wo], s61 offen offset:1152\n\t" \
  "buffer_load_dwordx3 v[168:170], v64, %[Wo], s61 offen offset:2304\n\t" \
  "buffer_load_dwordx3 v[172:174], v64, %[Wo], s61 offen offset:3456\n\t" \
  "s_waitcnt vmcnt(40) lgkmcnt(0)\n\t" \
  "s_barrier\n\t" \
  "ds_read_b64_tr_b16 v[0:1], v65 offset:0\n\t" \
  "ds_read_b64_tr_b16 v[2:3], v65 offset:1024\n\t" \
  "ds_read_b64_tr_b16 v[4:5], v65 offset:8192\n\t" \
  "ds_read_b64_tr_b16 v[6:7], v65 offset:9216\n\t" \
  "ds_read_b64_tr_b16 v[8:9], v65 offset:8\n\t" \
  "ds_read_b64_tr_b16 v[10:11], v65 offset:1032\n\t" \
  "ds_read_b64_tr_b16 v[12:13], v65 offset:8200\n\t" \
  "ds_read_b64_tr_b16 v[14:15], v65 offset:9224\n\t" \
  "s_waitcnt vmcnt(36) lgkmcnt(0)\n\t" \
  "s_barrier\n\t" \
  "ds_read_b64_tr_b16 a[112:113], v65 offset:16384\n\t" \
  "ds_read_b64_tr_b16 a[114:115], v65 offset:17408\n\t" \
  "ds_read_b64_tr_b16 a[116:117], v65 offset:24576\n\t" \
  "ds_read_b64_tr_b16 a[118:119], v65 offset:25600\n\t" \
  "ds_read_b64_tr_b16 a[168:169], v65 offset:16392\n\t" \
  "ds_read_b64_tr_b16 a[170:171], v65 offset:17416\n\t" \
  "ds_read_b64_tr_b16 a[172:173], v65 offset:24584\n\t" \
  "ds_read_b64_tr_b16 a[174:175], v65 offset:25608\n\t" \
  "s_add_u32 m0, s54, 0\n\t" \
  "s_mov_b32 s60, 1048576\n\t" \
  "buffer_load_dwordx4 v71, %[Wk], s60 offen lds\n\t" \
  "s_add_u32 m0, s54, 1024\n\t" \
  "s_mov_b32 s60, 1081344\n\t" \
  "buffer_load_dwordx4 v71, %[Wk], s60 offen lds\n\t" \
  "s_add_u32 m0, s54, 2048\n\t" \
  "s_mov_b32 s60, 1114112\n\t" \
  "buffer_load_dwordx4 v71, %[Wk], s60 offen lds\n\t" \
  "s_add_u32 m0, s54, 3072\n\t" \
  "s_mov_b32 s60, 1146880\n\t" \
  "buffer_load_dwordx4 v71, %[Wk], s60 offen lds\n\t" \
  "s_add_u32 s60, s75, 0\n\t" \
  "buffer_load_ushort v248, v67, %[Wo], s60 offen\n\t" \
  "buffer_load_ushort v83, v67, %[Wo], s60 offen offset:1152\n\t" \
  "s_add_u32 s60, s75, 36864\n\t" \
  "buffer_load_ushort v249, v67, %[Wo], s60 offen\n\t" \
  "buffer_load_ushort v87, v67, %[Wo], s60 offen offset:1152\n\t" \
  "s_add_u32 s60, s75, 2304\n\t" \
  "buffer_load_ushort v250, v67, %[Wo], s60 offen\n\t" \
  "buffer_load_ushort v91, v67, %[Wo], s60 offen offset:1152\n\t" \
  "s_add_u32 s60, s75, 39168\n\t" \
  "buffer_load_ushort v251, v67, %[Wo], s60 offen\n\t" \
  "buffer_load_ushort v95, v67, %[Wo], s60 offen offset:1152\n\t" \
  "s_add_u32 s60, s75, 9216\n\t" \
  "buffer_load_ushort v252, v67, %[Wo], s60 offen\n\t" \
  "buffer_load_ushort v99, v67, %[Wo], s60 offen offset:1152\n\t" \
  "s_add_u32 s60, s75, 46080\n\t" \
  "buffer_load_ushort v253, v67, %[Wo], s60 offen\n\t" \
  "buffer_load_ushort v103, v67, %[Wo], s60 offen offset:1152\n\t" \
  "s_add_u32 s60, s75, 11520\n\t" \
  "buffer_load_ushort v254, v67, %[Wo], s60 offen\n\t" \
  "buffer_load_ushort v107, v67, %[Wo], s60 offen offset:1152\n\t" \
  "s_add_u32 s60, s75, 48384\n\t" \
  "buffer_load_ushort v255, v67, %[Wo], s60 offen\n\t" \
  "buffer_load_ushort v111, v67, %[Wo], s60 offen offset:1152\n\t" \
  "v_add_u32 v79, 53248, v65\n\t" \
  "s_waitcnt vmcnt(52) lgkmcnt(0)\n\t" \
  "s_barrier\n\t" \
  "ds_read_b64_tr_b16 a[0:1], v79 offset:0\n\t" \
  "ds_read_b64_tr_b16 a[2:3], v79 offset:1024\n\t" \
  "ds_read_b64_tr_b16 a[4:5], v79 offset:8192\n\t" \
  "ds_read_b64_tr_b16 a[6:7], v79 offset:9216\n\t" \
  "ds_read_b64_tr_b16 a[64:65], v79 offset:8\n\t" \
  "ds_read_b64_tr_b16 a[66:67], v79 offset:1032\n\t" \
  "ds_read_b64_tr_b16 a[68:69], v79 offset:8200\n\t" \
  "ds_read_b64_tr_b16 a[70:71], v79 offset:9224\n\t" \
  "s_add_u32 m0, s54, 16384\n\t" \
  "s_mov_b32 s60, 1572864\n\t" \
  "buffer_load_dwordx4 v71, %[Wq], s60 offen lds\n\t" \
  "s_add_u32 m0, s54, 17408\n\t" \
  "s_mov_b32 s60, 1605632\n\t" \
  "buffer_load_dwordx4 v71, %[Wq], s60 offen lds\n\t" \
  "s_add_u32 m0, s54, 18432\n\t" \
  "s_mov_b32 s60, 1638400\n\t" \
  "buffer_load_dwordx4 v71, %[Wq], s60 offen lds\n\t" \
  "s_add_u32 m0, s54, 19456\n\t" \
  "s_mov_b32 s60, 1671168\n\t" \
  "buffer_load_dwordx4 v71, %[Wq], s60 offen lds\n\t" \
  "s_waitcnt lgkmcnt(0)\n\t" \
  "s_waitcnt vmcnt(46)\n\t" \
  "v_perm_b32 v176, v84, v80, s62\n\t" \
  "v_perm_b32 v177, v84, v80, s63\n\t" \
  "s_waitcnt vmcnt(44)\n\t" \
  "v_perm_b32 v178, v92, v88, s62\n\t" \
  "v_perm_b32 v179, v92, v88, s63\n\t" \
  "s_waitcnt vmcnt(42)\n\t" \
  "v_perm_b32 v180, v100, v96, s62\n\t" \
  "v_perm_b32 v181, v100, v96, s63\n\t" \
  "s_waitcnt vmcnt(40)\n\t" \
  "v_perm_b32 v182, v108, v104, s62\n\t" \
  "v_perm_b32 v183, v108, v104, s63\n\t" \
  "ds_write_b128 v70, v[176:179] offset:20480\n\t" \
  "ds_write_b128 v70, v[180:183] offset:21504\n\t" \
  "s_waitcnt lgkmcnt(0)\n\t" \
  "v_perm_b32 v176, v86, v82, s62\n\t" \
  "v_perm_b32 v177, v86, v82, s63\n\t" \
  "v_perm_b32 v178, v94, v90, s62\n\t" \
  "v_perm_b32 v179, v94, v90, s63\n\t" \
  "v_perm_b32 v180, v102, v98, s62\n\t" \
  "v_perm_b32 v181, v102, v98, s63\n\t" \
  "v_perm_b32 v182, v110, v106, s62\n\t" \
  "v_perm_b32 v183, v110, v106, s63\n\t" \
  "ds_write_b128 v70, v[176:179] offset:16384\n\t" \
  "ds_write_b128 v70, v[180:183] offset:17408\n\t" \
  "s_xor_b32 s60, s40, 1\n\t" \
  "s_mul_i32 s60, s60, 36864\n\t" \
  "s_add_u32 s60, s60, s55\n\t" \
  "s_add_u32 s60, s60, 512\n\t" \
  "s_add_u32 s61, s60, 9216\n\t" \
  "buffer_load_dwordx3 v[80:82], v64, %[Wo], s60 offen offset:0\n\t" \
  "buffer_load_dwordx3 v[84:86], v64, %[Wo], s60 offen offset:1152\n\t" \
  "buffer_load_dwordx3 v[88:90], v64, %[Wo], s60 offen offset:2304\n\t" \
  "buffer_load_dwordx3 v[92:94], v64, %[Wo], s60 offen offset:3456\n\t" \
  "buffer_load_dwordx3 v[96:98], v64, %[Wo], s61 offen offset:0\n\t" \
  "buffer_load_dwordx3 v[100:102], v64, %[Wo], s61 offen offset:1152\n\t" \
  "buffer_load_dwordx3 v[104:106], v64, %[Wo], s61 offen offset:2304\n\t" \
  "buffer_load_dwordx3 v[108:110], v64, %[Wo], s61 offen offset:3456\n\t" \
  "v_add_u32 v79, 90112, v65\n\t" \
  "s_waitcnt vmcnt(60) lgkmcnt(0)\n\t" \
  "s_barrier\n\t" \
  "ds_read_b64_tr_b16 a[120:121], v79 offset:0\n\t" \
  "ds_read_b64_tr_b16 a[122:123], v79 offset:1024\n\t" \
  "ds_read_b64_tr_b16 a[124:125], v79 offset:8192\n\t" \
  "ds_read_b64_tr_b16 a[126:127], v79 offset:9216\n\t" \
  "ds_read_b64_tr_b16 a[176:177], v79 offset:8\n\t" \
  "ds_read_b64_tr_b16 a[178:179], v79 offset:1032\n\t" \
  "ds_read_b64_tr_b16 a[180:181], v79 offset:8200\n\t" \
  "ds_read_b64_tr_b16 a[182:183], v79 offset:9224\n\t" \
  "s_add_u32 m0, s54, 53248\n\t" \
  "s_mov_b32 s60, 1572864\n\t" \
  "buffer_load_dwordx4 v71, %[Wk], s60 offen lds\n\t" \
  "s_add_u32 m0, s54, 54272\n\t" \
  "s_mov_b32 s60, 1605632\n\t" \
  "buffer_load_dwordx4 v71, %[Wk], s60 offen lds\n\t" \
  "s_add_u32 m0, s54, 55296\n\t" \
  "s_mov_b32 s60, 1638400\n\t" \
  "buffer_load_dwordx4 v71, %[Wk], s60 offen lds\n\t" \
  "s_add_u32 m0, s54, 56320\n\t" \
  "s_mov_b32 s60, 1671168\n\t" \
  "buffer_load_dwordx4 v71, %[Wk], s60 offen lds\n\t" \
  "v_add_u32 v79, 126976, v65\n\t" \
  "s_waitcnt vmcnt(60) lgkmcnt(0)\n\t" \
  "s_barrier\n\t" \
  "ds_read_b64_tr_b16 a[8:9], v79 offset:0\n\t" \
  "ds_read_b64_tr_b16 a[10:11], v79 offset:1024\n\t" \
  "ds_read_b64_tr_b16 a[12:13], v79 offset:8192\n\t" \
  "ds_read_b64_tr_b16 a[14:15], v79 offset:9216\n\t" \
  "ds_read_b64_tr_b16 a[72:73], v79 offset:8\n\t" \
  "ds_read_b64_tr_b16 a[74:75], v79 offset:1032\n\t" \
  "ds_read_b64_tr_b16 a[76:77], v79 offset:8200\n\t" \
  "ds_read_b64_tr_b16 a[78:79], v79 offset:9224\n\t" \
  "s_add_u32 m0, s54, 90112\n\t" \
  "s_mov_b32 s60, 2097152\n\t" \
  "buffer_load_dwordx4 v71, %[Wq], s60 offen lds\n\t" \
  "s_add_u32 m0, s54, 91136\n\t" \
  "s_mov_b32 s60, 2129920\n\t" \
  "buffer_load_dwordx4 v71, %[Wq], s60 offen lds\n\t" \
  "s_add_u32 m0, s54, 92160\n\t" \
  "s_mov_b32 s60, 2162688\n\t" \
  "buffer_load_dwordx4 v71, %[Wq], s60 offen lds\n\t" \
  "s_add_u32 m0, s54, 93184\n\t" \
  "s_mov_b32 s60, 2195456\n\t" \
  "buffer_load_dwordx4 v71, %[Wq], s60 offen lds\n\t" \
  "s_waitcnt lgkmcnt(0)\n\t" \
  "s_waitcnt vmcnt(54)\n\t" \
  "v_perm_b32 v176, v116, v112, s62\n\t" \
  "v_perm_b32 v177, v116, v112, s63\n\t" \
  "s_waitcnt vmcnt(52)\n\t" \
  "v_perm_b32 v178, v124, v120, s62\n\t" \
  "v_perm_b32 v179, v124, v120, s63\n\t" \
  "s_waitcnt vmcnt(50)\n\t" \
  "v_perm_b32 v180, v132, v128, s62\n\t" \
  "v_perm_b32 v181, v132, v128, s63\n\t" \
  "s_waitcnt vmcnt(48)\n\t" \
  "v_perm_b32 v182, v140, v136, s62\n\t" \
  "v_perm_b32 v183, v140, v136, s63\n\t" \
  "ds_write_b128 v70, v[176:179] offset:22528\n\t" \
  "ds_write_b128 v70, v[180:183] offset:23552\n\t" \
  "s_waitcnt lgkmcnt(0)\n\t" \
  "v_perm_b32 v176, v118, v114, s62\n\t" \
  "v_perm_b32 v177, v118, v114, s63\n\t" \
  "v_perm_b32 v178, v126, v122, s62\n\t" \
  "v_perm_b32 v179, v126, v122, s63\n\t" \
  "v_perm_b32 v180, v134, v130, s62\n\t" \
  "v_perm_b32 v181, v134, v130, s63\n\t" \
  "v_perm_b32 v182, v142, v138, s62\n\t" \
  "v_perm_b32 v183, v142, v138, s63\n\t" \
  "ds_write_b128 v70, v[176:179] offset:18432\n\t" \
  "ds_write_b128 v70, v[180:183] offset:19456\n\t" \
  "s_xor_b32 s60, s40, 2\n\t" \
  "s_mul_i32 s60, s60, 36864\n\t" \
  "s_add_u32 s60, s60, s55\n\t" \
  "s_add_u32 s60, s60, 0\n\t" \
  "s_add_u32 s61, s60, 9216\n\t" \
  "buffer_load_dwordx3 v[112:114], v64, %[Wo], s60 offen offset:0\n\t" \
  "buffer_load_dwordx3 v[116:118], v64, %[Wo], s60 offen offset:1152\n\t" \
  "buffer_load_dwordx3 v[120:122], v64, %[Wo], s60 offen offset:2304\n\t" \
  "buffer_load_dwordx3 v[124:126], v64, %[Wo], s60 offen offset:3456\n\t" \
  "buffer_load_dwordx3 v[128:130], v64, %[Wo], s61 offen offset:0\n\t" \
  "buffer_load_dwordx3 v[132:134], v64, %[Wo], s61 offen offset:1152\n\t" \
  "buffer_load_dwordx3 v[136:138], v64, %[Wo], s61 offen offset:2304\n\t" \
  "buffer_load_dwordx3 v[140:142], v64, %[Wo], s61 offen offset:3456\n\t" \
  "s_waitcnt vmcnt(44) lgkmcnt(0)\n\t" \
  "s_barrier\n\t" \
  "ds_read_b64_tr_b16 v[32:33], v65 offset:0\n\t" \
  "ds_read_b64_tr_b16 v[34:35], v65 offset:1024\n\t" \
  "ds_read_b64_tr_b16 v[36:37], v65 offset:8192\n\t" \
  "ds_read_b64_tr_b16 v[38:39], v65 offset:9216\n\t" \
  "ds_read_b64_tr_b16 a[184:185], v65 offset:8\n\t" \
  "ds_read_b64_tr_b16 a[186:187], v65 offset:1032\n\t" \
  "ds_read_b64_tr_b16 a[188:189], v65 offset:8200\n\t" \
  "ds_read_b64_tr_b16 a[190:191], v65 offset:9224\n\t" \
  "s_add_u32 m0, s54, 126976\n\t" \
  "s_mov_b32 s60, 2097152\n\t" \
  "buffer_load_dwordx4 v71, %[Wk], s60 offen lds\n\t" \
  "s_add_u32 m0, s54, 128000\n\t" \
  "s_mov_b32 s60, 2129920\n\t" \
  "buffer_load_dwordx4 v71, %[Wk], s60 offen lds\n\t" \
  "s_add_u32 m0, s54, 129024\n\t" \
  "s_mov_b32 s60, 2162688\n\t" \
  "buffer_load_dwordx4 v71, %[Wk], s60 offen lds\n\t" \
  "s_add_u32 m0, s54, 130048\n\t" \
  "s_mov_b32 s60, 2195456\n\t" \
  "buffer_load_dwordx4 v71, %[Wk], s60 offen lds\n\t" \
  "s_waitcnt vmcnt(28) lgkmcnt(0)\n\t" \
  "s_barrier\n\t" \
  "ds_read_b64_tr_b16 a[16:17], v65 offset:16384\n\t" \
  "ds_read_b64_tr_b16 a[18:19], v65 offset:17408\n\t" \
  "ds_read_b64_tr_b16 a[20:21], v65 offset:24576\n\t" \
  "ds_read_b64_tr_b16 a[22:23], v65 offset:25600\n\t" \
  "ds_read_b64_tr_b16 v[16:17], v65 offset:16392\n\t" \
  "ds_read_b64_tr_b16 v[18:19], v65 offset:17416\n\t" \
  "ds_read_b64_tr_b16 v[20:21], v65 offset:24584\n\t" \
  "ds_read_b64_tr_b16 v[22:23], v65 offset:25608\n\t" \
  "s_add_u32 m0, s54, 0\n\t" \
  "s_mov_b32 s60, 2621440\n\t" \
  "buffer_load_dwordx4 v71, %[Wq], s60 offen lds\n\t" \
  "s_add_u32 m0, s54, 1024\n\t" \
  "s_mov_b32 s60, 2654208\n\t" \
  "buffer_load_dwordx4 v71, %[Wq], s60 offen lds\n\t" \
  "s_add_u32 m0, s54, 2048\n\t" \
  "s_mov_b32 s60, 2686976\n\t" \
  "buffer_load_dwordx4 v71, %[Wq], s60 offen lds\n\t" \
  "s_add_u32 m0, s54, 3072\n\t" \
  "s_mov_b32 s60, 2719744\n\t" \
  "buffer_load_dwordx4 v71, %[Wq], s60 offen lds\n\t" \
  "s_waitcnt lgkmcnt(0)\n\t" \
  "v_perm_b32 v176, v148, v144, s62\n\t" \
  "v_perm_b32 v177, v148, v144, s63\n\t" \
  "v_perm_b32 v178, v156, v152, s62\n\t" \
  "v_perm_b32 v179, v156, v152, s63\n\t" \
  "v_perm_b32 v180, v164, v160, s62\n\t" \
  "v_perm_b32 v181, v164, v160, s63\n\t" \
  "v_perm_b32 v182, v172, v168, s62\n\t" \
  "v_perm_b32 v183, v172, v168, s63\n\t" \
  "ds_write_b128 v70, v[176:179] offset:24576\n\t" \
  "ds_write_b128 v70, v[180:183] offset:25600\n\t" \
  "v_perm_b32 v200, v150, v146, s62\n\t" \
  "v_perm_b32 v201, v150, v146, s63\n\t" \
  "v_perm_b32 v202, v158, v154, s62\n\t" \
  "v_perm_b32 v203, v158, v154, s63\n\t" \
  "v_perm_b32 v204, v166, v162, s62\n\t" \
  "v_perm_b32 v205, v166, v162, s63\n\t" \
  "v_perm_b32 v206, v174, v170, s62\n\t" \
  "v_perm_b32 v207, v174, v170, s63\n\t" \
  "s_xor_b32 s60, s40, 2\n\t" \
  "s_mul_i32 s60, s60, 36864\n\t" \
  "s_add_u32 s60, s60, s55\n\t" \
  "s_add_u32 s60, s60, 512\n\t" \
  "s_add_u32 s61, s60, 9216\n\t" \
  "buffer_load_dwordx3 v[144:146], v64, %[Wo], s60 offen offset:0\n\t" \
  "buffer_load_dwordx3 v[148:150], v64, %[Wo], s60 offen offset:1152\n\t" \
  "buffer_load_dwordx3 v[152:154], v64, %[Wo], s60 offen offset:2304\n\t" \
  "buffer_load_dwordx3 v[156:158], v64, %[Wo], s60 offen offset:3456\n\t" \
  "buffer_load_dwordx3 v[160:162], v64, %[Wo], s61 offen offset:0\n\t" \
  "buffer_load_dwordx3 v[164:166], v64, %[Wo], s61 offen offset:1152\n\t" \
  "buffer_load_dwordx3 v[168:170], v64, %[Wo], s61 offen offset:2304\n\t" \
  "buffer_load_dwordx3 v[172:174], v64, %[Wo], s61 offen offset:3456\n\t" \
  "v_add_u32 v79, 53248, v65\n\t" \
  "s_waitcnt vmcnt(28) lgkmcnt(0)\n\t" \
  "s_barrier\n\t" \
  "ds_read_b64_tr_b16 a[128:129], v79 offset:0\n\t" \
  "ds_read_b64_tr_b16 a[130:131], v79 offset:1024\n\t" \
  "ds_read_b64_tr_b16 a[132:133], v79 offset:8192\n\t" \
  "ds_read_b64_tr_b16 a[134:135], v79 offset:9216\n\t" \
  "ds_read_b64_tr_b16 a[192:193], v79 offset:8\n\t" \
  "ds_read_b64_tr_b16 a[194:195], v79 offset:1032\n\t" \
  "ds_read_b64_tr_b16 a[196:197], v79 offset:8200\n\t" \
  "ds_read_b64_tr_b16 a[198:199], v79 offset:9224\n\t" \
  "s_add_u32 m0, s54, 16384\n\t" \
  "s_mov_b32 s60, 2621440\n\t" \
  "buffer_load_dwordx4 v71, %[Wk], s60 offen lds\n\t" \
  "s_add_u32 m0, s54, 17408\n\t" \
  "s_mov_b32 s60, 2654208\n\t" \
  "buffer_load_dwordx4 v71, %[Wk], s60 offen lds\n\t" \
  "s_add_u32 m0, s54, 18432\n\t" \
  "s_mov_b32 s60, 2686976\n\t" \
  "buffer_load_dwordx4 v71, %[Wk], s60 offen lds\n\t" \
  "s_add_u32 m0, s54, 19456\n\t" \
  "s_mov_b32 s60, 2719744\n\t" \
  "buffer_load_dwordx4 v71, %[Wk], s60 offen lds\n\t" \
  "v_lshl_or_b32 v248, v83, 16, v248\n\t" \
  "v_lshl_or_b32 v249, v87, 16, v249\n\t" \
  "v_lshl_or_b32 v250, v91, 16, v250\n\t" \
  "v_lshl_or_b32 v251, v95, 16, v251\n\t" \
  "v_lshl_or_b32 v252, v99, 16, v252\n\t" \
  "v_lshl_or_b32 v253, v103, 16, v253\n\t" \
  "v_lshl_or_b32 v254, v107, 16, v254\n\t" \
  "v_lshl_or_b32 v255, v111, 16, v255\n\t" \
  "v_add_u32 v79, 90112, v65\n\t" \
  "s_waitcnt vmcnt(28) lgkmcnt(0)\n\t" \
  "s_barrier\n\t" \
  "ds_read_b64_tr_b16 a[24:25], v79 offset:0\n\t" \
  "ds_read_b64_tr_b16 a[26:27], v79 offset:1024\n\t" \
  "ds_read_b64_tr_b16 a[28:29], v79 offset:8192\n\t" \
  "ds_read_b64_tr_b16 a[30:31], v79 offset:9216\n\t" \
  "ds_read_b64_tr_b16 a[80:81], v79 offset:8\n\t" \
  "ds_read_b64_tr_b16 a[82:83], v79 offset:1032\n\t" \
  "ds_read_b64_tr_b16 a[84:85], v79 offset:8200\n\t" \
  "ds_read_b64_tr_b16 a[86:87], v79 offset:9224\n\t" \
  "s_add_u32 m0, s54, 53248\n\t" \
  "s_mov_b32 s60, 3145728\n\t" \
  "buffer_load_dwordx4 v71, %[Wq], s60 offen lds\n\t" \
  "s_add_u32 m0, s54, 54272\n\t" \
  "s_mov_b32 s60, 3178496\n\t" \
  "buffer_load_dwordx4 v71, %[Wq], s60 offen lds\n\t" \
  "s_add_u32 m0, s54, 55296\n\t" \
  "s_mov_b32 s60, 3211264\n\t" \
  "buffer_load_dwordx4 v71, %[Wq], s60 offen lds\n\t" \
  "s_add_u32 m0, s54, 56320\n\t" \
  "s_mov_b32 s60, 3244032\n\t" \
  "buffer_load_dwordx4 v71, %[Wq], s60 offen lds\n\t" \
  "s_waitcnt lgkmcnt(0)\n\t" \
  "v_perm_b32 v176, v84, v80, s62\n\t" \
  "v_perm_b32 v177, v84, v80, s63\n\t" \
  "v_perm_b32 v178, v92, v88, s62\n\t" \
  "v_perm_b32 v179, v92, v88, s63\n\t" \
  "v_perm_b32 v180, v100, v96, s62\n\t" \
  "v_perm_b32 v181, v100, v96, s63\n\t" \
  "v_perm_b32 v182, v108, v104, s62\n\t" \
  "v_perm_b32 v183, v108, v104, s63\n\t" \
  "ds_write_b128 v70, v[176:179] offset:26624\n\t" \
  "ds_write_b128 v70, v[180:183] offset:27648\n\t" \
  "v_perm_b32 v232, v86, v82, s62\n\t" \
  "v_perm_b32 v233, v86, v82, s63\n\t" \
  "v_perm_b32 v234, v94, v90, s62\n\t" \
  "v_perm_b32 v235, v94, v90, s63\n\t" \
  "v_perm_b32 v236, v102, v98, s62\n\t" \
  "v_perm_b32 v237, v102, v98, s63\n\t" \
  "v_perm_b32 v238, v110, v106, s62\n\t" \
  "v_perm_b32 v239, v110, v106, s63\n\t" \
  "s_xor_b32 s60, s40, 3\n\t" \
  "s_mul_i32 s60, s60, 36864\n\t" \
  "s_add_u32 s60, s60, s55\n\t" \
  "s_add_u32 s60, s60, 0\n\t" \
  "s_add_u32 s61, s60, 9216\n\t" \
  "buffer_load_dwordx3 v[80:82], v64, %[Wo], s60 offen offset:0\n\t" \
  "buffer_load_dwordx3 v[84:86], v64, %[Wo], s60 offen offset:1152\n\t" \
  "buffer_load_dwordx3 v[88:90], v64, %[Wo], s60 offen offset:2304\n\t" \
  "buffer_load_dwordx3 v[92:94], v64, %[Wo], s60 offen offset:3456\n\t" \
  "buffer_load_dwordx3 v[96:98], v64, %[Wo], s61 offen offset:0\n\t" \
  "buffer_load_dwordx3 v[100:102], v64, %[Wo], s61 offen offset:1152\n\t" \
  "buffer_load_dwordx3 v[104:106], v64, %[Wo], s61 offen offset:2304\n\t" \
  "buffer_load_dwordx3 v[108:110], v64, %[Wo], s61 offen offset:3456\n\t" \
  "v_add_u32 v79, 126976, v65\n\t" \
  "s_waitcnt vmcnt(28) lgkmcnt(0)\n\t" \
  "s_barrier\n\t" \
  "ds_read_b64_tr_b16 a[136:137], v79 offset:0\n\t" \
  "ds_read_b64_tr_b16 a[138:139], v79 offset:1024\n\t" \
  "ds_read_b64_tr_b16 a[140:141], v79 offset:8192\n\t" \
  "ds_read_b64_tr_b16 a[142:143], v79 offset:9216\n\t" \
  "ds_read_b64_tr_b16 a[200:201], v79 offset:8\n\t" \
  "ds_read_b64_tr_b16 a[202:203], v79 offset:1032\n\t" \
  "ds_read_b64_tr_b16 a[204:205], v79 offset:8200\n\t" \
  "ds_read_b64_tr_b16 a[206:207], v79 offset:9224\n\t" \
  "s_add_u32 m0, s54, 90112\n\t" \
  "s_mov_b32 s60, 3145728\n\t" \
  "buffer_load_dwordx4 v71, %[Wk], s60 offen lds\n\t" \
  "s_add_u32 m0, s54, 91136\n\t" \
  "s_mov_b32 s60, 3178496\n\t" \
  "buffer_load_dwordx4 v71, %[Wk], s60 offen lds\n\t" \
  "s_add_u32 m0, s54, 92160\n\t" \
  "s_mov_b32 s60, 3211264\n\t" \
  "buffer_load_dwordx4 v71, %[Wk], s60 offen lds\n\t" \
  "s_add_u32 m0, s54, 93184\n\t" \
  "s_mov_b32 s60, 3244032\n\t" \
  "buffer_load_dwordx4 v71, %[Wk], s60 offen lds\n\t" \
  "s_add_u32 s60, s75, 73728\n\t" \
  "buffer_load_ushort v184, v67, %[Wo], s60 offen\n\t" \
  "buffer_load_ushort v83, v67, %[Wo], s60 offen offset:1152\n\t" \
  "s_add_u32 s60, s75, 110592\n\t" \
  "buffer_load_ushort v185, v67, %[Wo], s60 offen\n\t" \
  "buffer_load_ushort v87, v67, %[Wo], s60 offen offset:1152\n\t" \
  "s_add_u32 s60, s75, 76032\n\t" \
  "buffer_load_ushort v186, v67, %[Wo], s60 offen\n\t" \
  "buffer_load_ushort v91, v67, %[Wo], s60 offen offset:1152\n\t" \
  "s_add_u32 s60, s75, 112896\n\t" \
  "buffer_load_ushort v187, v67, %[Wo], s60 offen\n\t" \
  "buffer_load_ushort v95, v67, %[Wo], s60 offen offset:1152\n\t" \
  "s_add_u32 s60, s75, 82944\n\t" \
  "buffer_load_ushort v188, v67, %[Wo], s60 offen\n\t" \
  "buffer_load_ushort v99, v67, %[Wo], s60 offen offset:1152\n\t" \
  "s_add_u32 s60, s75, 119808\n\t" \
  "buffer_load_ushort v189, v67, %[Wo], s60 offen\n\t" \
  "buffer_load_ushort v103, v67, %[Wo], s60 offen offset:1152\n\t" \
  "s_add_u32 s60, s75, 85248\n\t" \
  "buffer_load_ushort v190, v67, %[Wo], s60 offen\n\t" \
  "buffer_load_ushort v107, v67, %[Wo], s60 offen offset:1152\n\t" \
  "s_add_u32 s60, s75, 122112\n\t" \
  "buffer_load_ushort v191, v67, %[Wo], s60 offen\n\t" \
  "buffer_load_ushort v111, v67, %[Wo], s60 offen offset:1152\n\t" \
  "s_waitcnt vmcnt(44) lgkmcnt(0)\n\t" \
  "s_barrier\n\t" \
  "ds_read_b64_tr_b16 a[32:33], v65 offset:0\n\t" \
  "ds_read_b64_tr_b16 a[34:35], v65 offset:1024\n\t" \
  "ds_read_b64_tr_b16 a[36:37], v65 offset:8192\n\t" \
  "ds_read_b64_tr_b16 a[38:39], v65 offset:9216\n\t" \
  "ds_read_b64_tr_b16 v[24:25], v65 offset:8\n\t" \
  "ds_read_b64_tr_b16 v[26:27], v65 offset:1032\n\t" \
  "ds_read_b64_tr_b16 v[28:29], v65 offset:8200\n\t" \
  "ds_read_b64_tr_b16 v[30:31], v65 offset:9224\n\t" \
  "s_add_u32 m0, s54, 126976\n\t" \
  "s_mov_b32 s60, 3670016\n\t" \
  "buffer_load_dwordx4 v71, %[Wq], s60 offen lds\n\t" \
  "s_add_u32 m0, s54, 128000\n\t" \
  "s_mov_b32 s60, 3702784\n\t" \
  "buffer_load_dwordx4 v71, %[Wq], s60 offen lds\n\t" \
  "s_add_u32 m0, s54, 129024\n\t" \
  "s_mov_b32 s60, 3735552\n\t" \
  "buffer_load_dwordx4 v71, %[Wq], s60 offen lds\n\t" \
  "s_add_u32 m0, s54, 130048\n\t" \
  "s_mov_b32 s60, 3768320\n\t" \
  "buffer_load_dwordx4 v71, %[Wq], s60 offen lds\n\t" \
  "s_waitcnt lgkmcnt(0)\n\t" \
  "v_perm_b32 v176, v116, v112, s62\n\t" \
  "v_perm_b32 v177, v116, v112, s63\n\t" \
  "v_perm_b32 v178, v124, v120, s62\n\t" \
  "v_perm_b32 v179, v124, v120, s63\n\t" \
  "v_perm_b32 v180, v132, v128, s62\n\t" \
  "v_perm_b32 v181, v132, v128, s63\n\t" \
  "v_perm_b32 v182, v140, v136, s62\n\t" \
  "v_perm_b32 v183, v140, v136, s63\n\t" \
  "ds_write_b128 v70, v[176:179] offset:28672\n\t" \
  "ds_write_b128 v70, v[180:183] offset:29696\n\t" \
  "v_perm_b32 v208, v118, v114, s62\n\t" \
  "v_perm_b32 v209, v118, v114, s63\n\t" \
  "v_perm_b32 v210, v126, v122, s62\n\t" \
  "v_perm_b32 v211, v126, v122, s63\n\t" \
  "v_perm_b32 v212, v134, v130, s62\n\t" \
  "v_perm_b32 v213, v134, v130, s63\n\t" \
  "v_perm_b32 v214, v142, v138, s62\n\t" \
  "v_perm_b32 v215, v142, v138, s63\n\t" \
  "s_xor_b32 s60, s40, 3\n\t" \
  "s_mul_i32 s60, s60, 36864\n\t" \
  "s_add_u32 s60, s60, s55\n\t" \
  "s_add_u32 s60, s60, 512\n\t" \
  "s_add_u32 s61, s60, 9216\n\t" \
  "buffer_load_dwordx3 v[112:114], v64, %[Wo], s60 offen offset:0\n\t" \
  "buffer_load_dwordx3 v[116:118], v64, %[Wo], s60 offen offset:1152\n\t" \
  "buffer_load_dwordx3 v[120:122], v64, %[Wo], s60 offen offset:2304\n\t" \
  "buffer_load_dwordx3 v[124:126], v64, %[Wo], s60 offen offset:3456\n\t" \
  "buffer_load_dwordx3 v[128:130], v64, %[Wo], s61 offen offset:0\n\t" \
  "buffer_load_dwordx3 v[132:134], v64, %[Wo], s61 offen offset:1152\n\t" \
  "buffer_load_dwordx3 v[136:138], v64, %[Wo], s61 offen offset:2304\n\t" \
  "buffer_load_dwordx3 v[140:142], v64, %[Wo], s61 offen offset:3456\n\t" \
  "s_waitcnt vmcnt(44) lgkmcnt(0)\n\t" \
  "s_barrier\n\t" \
  "ds_read_b64_tr_b16 a[144:145], v65 offset:16384\n\t" \
  "ds_read_b64_tr_b16 a[146:147], v65 offset:17408\n\t" \
  "ds_read_b64_tr_b16 a[148:149], v65 offset:24576\n\t" \
  "ds_read_b64_tr_b16 a[150:151], v65 offset:25600\n\t" \
  "ds_read_b64_tr_b16 a[208:209], v65 offset:16392\n\t" \
  "ds_read_b64_tr_b16 a[210:211], v65 offset:17416\n\t" \
  "ds_read_b64_tr_b16 a[212:213], v65 offset:24584\n\t" \
  "ds_read_b64_tr_b16 a[214:215], v65 offset:25608\n\t" \
  "s_add_u32 m0, s54, 0\n\t" \
  "s_mov_b32 s60, 3670016\n\t" \
  "buffer_load_dwordx4 v71, %[Wk], s60 offen lds\n\t" \
  "s_add_u32 m0, s54, 1024\n\t" \
  "s_mov_b32 s60, 3702784\n\t" \
  "buffer_load_dwordx4 v71, %[Wk], s60 offen lds\n\t" \
  "s_add_u32 m0, s54, 2048\n\t" \
  "s_mov_b32 s60, 3735552\n\t" \
  "buffer_load_dwordx4 v71, %[Wk], s60 offen lds\n\t" \
  "s_add_u32 m0, s54, 3072\n\t" \
  "s_mov_b32 s60, 3768320\n\t" \
  "buffer_load_dwordx4 v71, %[Wk], s60 offen lds\n\t" \
  "v_add_u32 v79, 53248, v65\n\t" \
  "s_waitcnt vmcnt(44) lgkmcnt(0)\n\t" \
  "s_barrier\n\t" \
  "ds_read_b64_tr_b16 a[40:41], v79 offset:0\n\t" \
  "ds_read_b64_tr_b16 a[42:43], v79 offset:1024\n\t" \
  "ds_read_b64_tr_b16 a[44:45], v79 offset:8192\n\t" \
  "ds_read_b64_tr_b16 a[46:47], v79 offset:9216\n\t" \
  "ds_read_b64_tr_b16 a[88:89], v79 offset:8\n\t" \
  "ds_read_b64_tr_b16 a[90:91], v79 offset:1032\n\t" \
  "ds_read_b64_tr_b16 a[92:93], v79 offset:8200\n\t" \
  "ds_read_b64_tr_b16 a[94:95], v79 offset:9224\n\t" \
  "s_add_u32 m0, s54, 16384\n\t" \
  "s_mov_b32 s60, 4194304\n\t" \
  "buffer_load_dwordx4 v71, %[Wq], s60 offen lds\n\t" \
  "s_add_u32 m0, s54, 17408\n\t" \
  "s_mov_b32 s60, 4227072\n\t" \
  "buffer_load_dwordx4 v71, %[Wq], s60 offen lds\n\t" \
  "s_add_u32 m0, s54, 18432\n\t" \
  "s_mov_b32 s60, 4259840\n\t" \
  "buffer_load_dwordx4 v71, %[Wq], s60 offen lds\n\t" \
  "s_add_u32 m0, s54, 19456\n\t" \
  "s_mov_b32 s60, 4292608\n\t" \
  "buffer_load_dwordx4 v71, %[Wq], s60 offen lds\n\t" \
  "s_waitcnt lgkmcnt(0)\n\t" \
  "v_perm_b32 v176, v148, v144, s62\n\t" \
  "v_perm_b32 v177, v148, v144, s63\n\t" \
  "v_perm_b32 v178, v156, v152, s62\n\t" \
  "v_perm_b32 v179, v156, v152, s63\n\t" \
  "v_perm_b32 v180, v164, v160, s62\n\t" \
  "v_perm_b32 v181, v164, v160, s63\n\t" \
  "v_perm_b32 v182, v172, v168, s62\n\t" \
  "v_perm_b32 v183, v172, v168, s63\n\t" \
  "ds_write_b128 v70, v[176:179] offset:30720\n\t" \
  "ds_write_b128 v70, v[180:183] offset:31744\n\t" \
  "v_perm_b32 v240, v150, v146, s62\n\t" \
  "v_perm_b32 v241, v150, v146, s63\n\t" \
  "v_perm_b32 v242, v158, v154, s62\n\t" \
  "v_perm_b32 v243, v158, v154, s63\n\t" \
  "v_perm_b32 v244, v166, v162, s62\n\t" \
  "v_perm_b32 v245, v166, v162, s63\n\t" \
  "v_perm_b32 v246, v174, v170, s62\n\t" \
  "v_perm_b32 v247, v174, v170, s63\n\t" \
  "v_add_u32 v79, 90112, v65\n\t" \
  "s_waitcnt vmcnt(36) lgkmcnt(0)\n\t" \
  "s_barrier\n\t" \
  "ds_read_b64_tr_b16 a[152:153], v79 offset:0\n\t" \
  "ds_read_b64_tr_b16 a[154:155], v79 offset:1024\n\t" \
  "ds_read_b64_tr_b16 a[156:157], v79 offset:8192\n\t" \
  "ds_read_b64_tr_b16 a[158:159], v79 offset:9216\n\t" \
  "ds_read_b64_tr_b16 a[216:217], v79 offset:8\n\t" \
  "ds_read_b64_tr_b16 a[218:219], v79 offset:1032\n\t" \
  "ds_read_b64_tr_b16 a[220:221], v79 offset:8200\n\t" \
  "ds_read_b64_tr_b16 a[222:223], v79 offset:9224\n\t" \
  "s_add_u32 m0, s54, 53248\n\t" \
  "s_mov_b32 s60, 4194304\n\t" \
  "buffer_load_dwordx4 v71, %[Wk], s60 offen lds\n\t" \
  "s_add_u32 m0, s54, 54272\n\t" \
  "s_mov_b32 s60, 4227072\n\t" \
  "buffer_load_dwordx4 v71, %[Wk], s60 offen lds\n\t" \
  "s_add_u32 m0, s54, 55296\n\t" \
  "s_mov_b32 s60, 4259840\n\t" \
  "buffer_load_dwordx4 v71, %[Wk], s60 offen lds\n\t" \
  "s_add_u32 m0, s54, 56320\n\t" \
  "s_mov_b32 s60, 4292608\n\t" \
  "buffer_load_dwordx4 v71, %[Wk], s60 offen lds\n\t" \
  "v_add_u32 v79, 126976, v65\n\t" \
  "s_waitcnt vmcnt(20) lgkmcnt(0)\n\t" \
  "s_barrier\n\t" \
  "ds_read_b64_tr_b16 a[48:49], v79 offset:0\n\t" \
  "ds_read_b64_tr_b16 a[50:51], v79 offset:1024\n\t" \
  "ds_read_b64_tr_b16 a[52:53], v79 offset:8192\n\t" \
  "ds_read_b64_tr_b16 a[54:55], v79 offset:9216\n\t" \
  "ds_read_b64_tr_b16 a[96:97], v79 offset:8\n\t" \
  "ds_read_b64_tr_b16 a[98:99], v79 offset:1032\n\t" \
  "ds_read_b64_tr_b16 a[100:101], v79 offset:8200\n\t" \
  "ds_read_b64_tr_b16 a[102:103], v79 offset:9224\n\t" \
  "s_add_u32 m0, s54, 90112\n\t" \
  "s_mov_b32 s60, 0\n\t" \
  "buffer_load_dwordx4 v71, %[Wv], s60 offen lds\n\t" \
  "s_add_u32 m0, s54, 91136\n\t" \
  "s_mov_b32 s60, 32768\n\t" \
  "buffer_load_dwordx4 v71, %[Wv], s60 offen lds\n\t" \
  "s_add_u32 m0, s54, 92160\n\t" \
  "s_mov_b32 s60, 65536\n\t" \
  "buffer_load_dwordx4 v71, %[Wv], s60 offen lds\n\t" \
  "s_add_u32 m0, s54, 93184\n\t" \
  "s_mov_b32 s60, 98304\n\t" \
  "buffer_load_dwordx4 v71, %[Wv], s60 offen lds\n\t" \
  "v_perm_b32 v192, v84, v80, s62\n\t" \
  "v_perm_b32 v193, v84, v80, s63\n\t" \
  "v_perm_b32 v194, v92, v88, s62\n\t" \
  "v_perm_b32 v195, v92, v88, s63\n\t" \
  "v_perm_b32 v196, v100, v96, s62\n\t" \
  "v_perm_b32 v197, v100, v96, s63\n\t" \
  "v_perm_b32 v198, v108, v104, s62\n\t" \
  "v_perm_b32 v199, v108, v104, s63\n\t" \
  "v_perm_b32 v216, v86, v82, s62\n\t" \
  "v_perm_b32 v217, v86, v82, s63\n\t" \
  "v_perm_b32 v218, v94, v90, s62\n\t" \
  "v_perm_b32 v219, v94, v90, s63\n\t" \
  "v_perm_b32 v220, v102, v98, s62\n\t" \
  "v_perm_b32 v221, v102, v98, s63\n\t" \
  "v_perm_b32 v222, v110, v106, s62\n\t" \
  "v_perm_b32 v223, v110, v106, s63\n\t" \
  "s_waitcnt vmcnt(12) lgkmcnt(0)\n\t" \
  "s_barrier\n\t" \
  "ds_read_b64_tr_b16 a[160:161], v65 offset:0\n\t" \
  "ds_read_b64_tr_b16 a[162:163], v65 offset:1024\n\t" \
  "ds_read_b64_tr_b16 a[164:165], v65 offset:8192\n\t" \
  "ds_read_b64_tr_b16 a[166:167], v65 offset:9216\n\t" \
  "ds_read_b64_tr_b16 a[224:225], v65 offset:8\n\t" \
  "ds_read_b64_tr_b16 a[226:227], v65 offset:1032\n\t" \
  "ds_read_b64_tr_b16 a[228:229], v65 offset:8200\n\t" \
  "ds_read_b64_tr_b16 a[230:231], v65 offset:9224\n\t" \
  "s_add_u32 m0, s54, 126976\n\t" \
  "s_mov_b32 s60, 524288\n\t" \
  "buffer_load_dwordx4 v71, %[Wv], s60 offen lds\n\t" \
  "s_add_u32 m0, s54, 128000\n\t" \
  "s_mov_b32 s60, 557056\n\t" \
  "buffer_load_dwordx4 v71, %[Wv], s60 offen lds\n\t" \
  "s_add_u32 m0, s54, 129024\n\t" \
  "s_mov_b32 s60, 589824\n\t" \
  "buffer_load_dwordx4 v71, %[Wv], s60 offen lds\n\t" \
  "s_add_u32 m0, s54, 130048\n\t" \
  "s_mov_b32 s60, 622592\n\t" \
  "buffer_load_dwordx4 v71, %[Wv], s60 offen lds\n\t" \
  "v_lshl_or_b32 v184, v83, 16, v184\n\t" \
  "v_lshl_or_b32 v185, v87, 16, v185\n\t" \
  "v_lshl_or_b32 v186, v91, 16, v186\n\t" \
  "v_lshl_or_b32 v187, v95, 16, v187\n\t" \
  "v_lshl_or_b32 v188, v99, 16, v188\n\t" \
  "v_lshl_or_b32 v189, v103, 16, v189\n\t" \
  "v_lshl_or_b32 v190, v107, 16, v190\n\t" \
  "v_lshl_or_b32 v191, v111, 16, v191\n\t" \
  "ds_write_b128 v70, v[184:187] offset:34816\n\t" \
  "ds_write_b128 v70, v[188:191] offset:35840\n\t" \
  "s_waitcnt vmcnt(12) lgkmcnt(0)\n\t" \
  "s_barrier\n\t" \
  "ds_read_b64_tr_b16 a[56:57], v65 offset:16384\n\t" \
  "ds_read_b64_tr_b16 a[58:59], v65 offset:17408\n\t" \
  "ds_read_b64_tr_b16 a[60:61], v65 offset:24576\n\t" \
  "ds_read_b64_tr_b16 a[62:63], v65 offset:25600\n\t" \
  "ds_read_b64_tr_b16 a[104:105], v65 offset:16392\n\t" \
  "ds_read_b64_tr_b16 a[106:107], v65 offset:17416\n\t" \
  "ds_read_b64_tr_b16 a[108:109], v65 offset:24584\n\t" \
  "ds_read_b64_tr_b16 a[110:111], v65 offset:25608\n\t" \
  "s_add_u32 m0, s54, 0\n\t" \
  "s_mov_b32 s60, 1048576\n\t" \
  "buffer_load_dwordx4 v71, %[Wv], s60 offen lds\n\t" \
  "s_add_u32 m0, s54, 1024\n\t" \
  "s_mov_b32 s60, 1081344\n\t" \
  "buffer_load_dwordx4 v71, %[Wv], s60 offen lds\n\t" \
  "s_add_u32 m0, s54, 2048\n\t" \
  "s_mov_b32 s60, 1114112\n\t" \
  "buffer_load_dwordx4 v71, %[Wv], s60 offen lds\n\t" \
  "s_add_u32 m0, s54, 3072\n\t" \
  "s_mov_b32 s60, 1146880\n\t" \
  "buffer_load_dwordx4 v71, %[Wv], s60 offen lds\n\t" \
  "v_perm_b32 v224, v116, v112, s62\n\t" \
  "v_perm_b32 v225, v116, v112, s63\n\t" \
  "v_perm_b32 v226, v124, v120, s62\n\t" \
  "v_perm_b32 v227, v124, v120, s63\n\t" \
  "v_perm_b32 v228, v132, v128, s62\n\t" \
  "v_perm_b32 v229, v132, v128, s63\n\t" \
  "v_perm_b32 v230, v140, v136, s62\n\t" \
  "v_perm_b32 v231, v140, v136, s63\n\t" \
  "s_waitcnt lgkmcnt(0)\n\t" \
  "v_perm_b32 v176, v118, v114, s62\n\t" \
  "v_perm_b32 v177, v118, v114, s63\n\t" \
  "v_perm_b32 v178, v126, v122, s62\n\t" \
  "v_perm_b32 v179, v126, v122, s63\n\t" \
  "v_perm_b32 v180, v134, v130, s62\n\t" \
  "v_perm_b32 v181, v134, v130, s63\n\t" \
  "v_perm_b32 v182, v142, v138, s62\n\t" \
  "v_perm_b32 v183, v142, v138, s63\n\t" \
  "ds_write_b128 v70, v[176:179] offset:32768\n\t" \
  "ds_write_b128 v70, v[180:183] offset:33792\n\t" \
  "v_add_u32 v79, 53248, v65\n\t" \
  "s_waitcnt vmcnt(12) lgkmcnt(0)\n\t" \
  "s_barrier\n\t" \
  "ds_read_b64_tr_b16 v[40:41], v79 offset:0\n\t" \
  "ds_read_b64_tr_b16 v[42:43], v79 offset:1024\n\t" \
  "ds_read_b64_tr_b16 v[44:45], v79 offset:8192\n\t" \
  "ds_read_b64_tr_b16 v[46:47], v79 offset:9216\n\t" \
  "ds_read_b64_tr_b16 a[232:233], v79 offset:8\n\t" \
  "ds_read_b64_tr_b16 a[234:235], v79 offset:1032\n\t" \
  "ds_read_b64_tr_b16 a[236:237], v79 offset:8200\n\t" \
  "ds_read_b64_tr_b16 a[238:239], v79 offset:9224\n\t" \
  "s_add_u32 m0, s54, 16384\n\t" \
  "s_mov_b32 s60, 1572864\n\t" \
  "buffer_load_dwordx4 v71, %[Wv], s60 offen lds\n\t" \
  "s_add_u32 m0, s54, 17408\n\t" \
  "s_mov_b32 s60, 1605632\n\t" \
  "buffer_load_dwordx4 v71, %[Wv], s60 offen lds\n\t" \
  "s_add_u32 m0, s54, 18432\n\t" \
  "s_mov_b32 s60, 1638400\n\t" \
  "buffer_load_dwordx4 v71, %[Wv], s60 offen lds\n\t" \
  "s_add_u32 m0, s54, 19456\n\t" \
  "s_mov_b32 s60, 1671168\n\t" \
  "buffer_load_dwordx4 v71, %[Wv], s60 offen lds\n\t" \
  "v_add_u32 v79, 90112, v66\n\t" \
  "s_waitcnt vmcnt(12) lgkmcnt(0)\n\t" \
  "s_barrier\n\t" \
  "ds_read_b64_tr_b16 a[240:241], v79 offset:0\n\t" \
  "ds_read_b64_tr_b16 a[242:243], v79 offset:1024\n\t" \
  "ds_read_b64_tr_b16 a[244:245], v79 offset:8192\n\t" \
  "ds_read_b64_tr_b16 a[246:247], v79 offset:9216\n\t" \
  "ds_read_b64_tr_b16 v[136:137], v79 offset:32\n\t" \
  "ds_read_b64_tr_b16 v[138:139], v79 offset:1056\n\t" \
  "ds_read_b64_tr_b16 v[140:141], v79 offset:8224\n\t" \
  "ds_read_b64_tr_b16 v[142:143], v79 offset:9248\n\t" \
  "s_add_u32 m0, s54, 53248\n\t" \
  "s_mov_b32 s60, 2097152\n\t" \
  "buffer_load_dwordx4 v71, %[Wv], s60 offen lds\n\t" \
  "s_add_u32 m0, s54, 54272\n\t" \
  "s_mov_b32 s60, 2129920\n\t" \
  "buffer_load_dwordx4 v71, %[Wv], s60 offen lds\n\t" \
  "s_add_u32 m0, s54, 55296\n\t" \
  "s_mov_b32 s60, 2162688\n\t" \
  "buffer_load_dwordx4 v71, %[Wv], s60 offen lds\n\t" \
  "s_add_u32 m0, s54, 56320\n\t" \
  "s_mov_b32 s60, 2195456\n\t" \
  "buffer_load_dwordx4 v71, %[Wv], s60 offen lds\n\t" \
  "v_add_u32 v79, 126976, v66\n\t" \
  "s_waitcnt vmcnt(12) lgkmcnt(0)\n\t" \
  "s_barrier\n\t" \
  "ds_read_b64_tr_b16 a[248:249], v79 offset:0\n\t" \
  "ds_read_b64_tr_b16 a[250:251], v79 offset:1024\n\t" \
  "ds_read_b64_tr_b16 a[252:253], v79 offset:8192\n\t" \
  "ds_read_b64_tr_b16 a[254:255], v79 offset:9216\n\t" \
  "ds_read_b64_tr_b16 v[144:145], v79 offset:32\n\t" \
  "ds_read_b64_tr_b16 v[146:147], v79 offset:1056\n\t" \
  "ds_read_b64_tr_b16 v[148:149], v79 offset:8224\n\t" \
  "ds_read_b64_tr_b16 v[150:151], v79 offset:9248\n\t" \
  "s_add_u32 m0, s54, 90112\n\t" \
  "s_mov_b32 s60, 2621440\n\t" \
  "buffer_load_dwordx4 v71, %[Wv], s60 offen lds\n\t" \
  "s_add_u32 m0, s54, 91136\n\t" \
  "s_mov_b32 s60, 2654208\n\t" \
  "buffer_load_dwordx4 v71, %[Wv], s60 offen lds\n\t" \
  "s_add_u32 m0, s54, 92160\n\t" \
  "s_mov_b32 s60, 2686976\n\t" \
  "buffer_load_dwordx4 v71, %[Wv], s60 offen lds\n\t" \
  "s_add_u32 m0, s54, 93184\n\t" \
  "s_mov_b32 s60, 2719744\n\t" \
  "buffer_load_dwordx4 v71, %[Wv], s60 offen lds\n\t" \
  "s_waitcnt vmcnt(12) lgkmcnt(0)\n\t" \
  "s_barrier\n\t" \
  "ds_read_b64_tr_b16 v[80:81], v66 offset:0\n\t" \
  "ds_read_b64_tr_b16 v[82:83], v66 offset:1024\n\t" \
  "ds_read_b64_tr_b16 v[84:85], v66 offset:8192\n\t" \
  "ds_read_b64_tr_b16 v[86:87], v66 offset:9216\n\t" \
  "ds_read_b64_tr_b16 v[152:153], v66 offset:32\n\t" \
  "ds_read_b64_tr_b16 v[154:155], v66 offset:1056\n\t" \
  "ds_read_b64_tr_b16 v[156:157], v66 offset:8224\n\t" \
  "ds_read_b64_tr_b16 v[158:159], v66 offset:9248\n\t" \
  "s_add_u32 m0, s54, 126976\n\t" \
  "s_mov_b32 s60, 3145728\n\t" \
  "buffer_load_dwordx4 v71, %[Wv], s60 offen lds\n\t" \
  "s_add_u32 m0, s54, 128000\n\t" \
  "s_mov_b32 s60, 3178496\n\t" \
  "buffer_load_dwordx4 v71, %[Wv], s60 offen lds\n\t" \
  "s_add_u32 m0, s54, 129024\n\t" \
  "s_mov_b32 s60, 3211264\n\t" \
  "buffer_load_dwordx4 v71, %[Wv], s60 offen lds\n\t" \
  "s_add_u32 m0, s54, 130048\n\t" \
  "s_mov_b32 s60, 3244032\n\t" \
  "buffer_load_dwordx4 v71, %[Wv], s60 offen lds\n\t" \
  "s_waitcnt vmcnt(12) lgkmcnt(0)\n\t" \
  "s_barrier\n\t" \
  "ds_read_b64_tr_b16 v[88:89], v66 offset:16384\n\t" \
  "ds_read_b64_tr_b16 v[90:91], v66 offset:17408\n\t" \
  "ds_read_b64_tr_b16 v[92:93], v66 offset:24576\n\t" \
  "ds_read_b64_tr_b16 v[94:95], v66 offset:25600\n\t" \
  "ds_read_b64_tr_b16 v[160:161], v66 offset:16416\n\t" \
  "ds_read_b64_tr_b16 v[162:163], v66 offset:17440\n\t" \
  "ds_read_b64_tr_b16 v[164:165], v66 offset:24608\n\t" \
  "ds_read_b64_tr_b16 v[166:167], v66 offset:25632\n\t" \
  "s_add_u32 m0, s54, 0\n\t" \
  "s_mov_b32 s60, 3670016\n\t" \
  "buffer_load_dwordx4 v71, %[Wv], s60 offen lds\n\t" \
  "s_add_u32 m0, s54, 1024\n\t" \
  "s_mov_b32 s60, 3702784\n\t" \
  "buffer_load_dwordx4 v71, %[Wv], s60 offen lds\n\t" \
  "s_add_u32 m0, s54, 2048\n\t" \
  "s_mov_b32 s60, 3735552\n\t" \
  "buffer_load_dwordx4 v71, %[Wv], s60 offen lds\n\t" \
  "s_add_u32 m0, s54, 3072\n\t" \
  "s_mov_b32 s60, 3768320\n\t" \
  "buffer_load_dwordx4 v71, %[Wv], s60 offen lds\n\t" \
  "v_add_u32 v79, 53248, v66\n\t" \
  "s_waitcnt vmcnt(12) lgkmcnt(0)\n\t" \
  "s_barrier\n\t" \
  "ds_read_b64_tr_b16 v[96:97], v79 offset:0\n\t" \
  "ds_read_b64_tr_b16 v[98:99], v79 offset:1024\n\t" \
  "ds_read_b64_tr_b16 v[100:101], v79 offset:8192\n\t" \
  "ds_read_b64_tr_b16 v[102:103], v79 offset:9216\n\t" \
  "ds_read_b64_tr_b16 v[48:49], v79 offset:32\n\t" \
  "ds_read_b64_tr_b16 v[50:51], v79 offset:1056\n\t" \
  "ds_read_b64_tr_b16 v[52:53], v79 offset:8224\n\t" \
  "ds_read_b64_tr_b16 v[54:55], v79 offset:9248\n\t" \
  "s_add_u32 m0, s54, 16384\n\t" \
  "s_mov_b32 s60, 4194304\n\t" \
  "buffer_load_dwordx4 v71, %[Wv], s60 offen lds\n\t" \
  "s_add_u32 m0, s54, 17408\n\t" \
  "s_mov_b32 s60, 4227072\n\t" \
  "buffer_load_dwordx4 v71, %[Wv], s60 offen lds\n\t" \
  "s_add_u32 m0, s54, 18432\n\t" \
  "s_mov_b32 s60, 4259840\n\t" \
  "buffer_load_dwordx4 v71, %[Wv], s60 offen lds\n\t" \
  "s_add_u32 m0, s54, 19456\n\t" \
  "s_mov_b32 s60, 4292608\n\t" \
  "buffer_load_dwordx4 v71, %[Wv], s60 offen lds\n\t" \
  "v_add_u32 v79, 90112, v66\n\t" \
  "s_waitcnt vmcnt(12) lgkmcnt(0)\n\t" \
  "s_barrier\n\t" \
  "ds_read_b64_tr_b16 v[104:105], v79 offset:0\n\t" \
  "ds_read_b64_tr_b16 v[106:107], v79 offset:1024\n\t" \
  "ds_read_b64_tr_b16 v[108:109], v79 offset:8192\n\t" \
  "ds_read_b64_tr_b16 v[110:111], v79 offset:9216\n\t" \
  "ds_read_b64_tr_b16 v[168:169], v79 offset:32\n\t" \
  "ds_read_b64_tr_b16 v[170:171], v79 offset:1056\n\t" \
  "ds_read_b64_tr_b16 v[172:173], v79 offset:8224\n\t" \
  "ds_read_b64_tr_b16 v[174:175], v79 offset:9248\n\t" \
  "v_add_u32 v79, 126976, v66\n\t" \
  "s_waitcnt vmcnt(8) lgkmcnt(0)\n\t" \
  "s_barrier\n\t" \
  "ds_read_b64_tr_b16 v[112:113], v79 offset:0\n\t" \
  "ds_read_b64_tr_b16 v[114:115], v79 offset:1024\n\t" \
  "ds_read_b64_tr_b16 v[116:117], v79 offset:8192\n\t" \
  "ds_read_b64_tr_b16 v[118:119], v79 offset:9216\n\t" \
  "ds_read_b64_tr_b16 v[176:177], v79 offset:32\n\t" \
  "ds_read_b64_tr_b16 v[178:179], v79 offset:1056\n\t" \
  "ds_read_b64_tr_b16 v[180:181], v79 offset:8224\n\t" \
  "ds_read_b64_tr_b16 v[182:183], v79 offset:9248\n\t" \
  "s_waitcnt vmcnt(4) lgkmcnt(0)\n\t" \
  "s_barrier\n\t" \
  "ds_read_b64_tr_b16 v[120:121], v66 offset:0\n\t" \
  "ds_read_b64_tr_b16 v[122:123], v66 offset:1024\n\t" \
  "ds_read_b64_tr_b16 v[124:125], v66 offset:8192\n\t" \
  "ds_read_b64_tr_b16 v[126:127], v66 offset:9216\n\t" \
  "ds_read_b64_tr_b16 v[184:185], v66 offset:32\n\t" \
  "ds_read_b64_tr_b16 v[186:187], v66 offset:1056\n\t" \
  "ds_read_b64_tr_b16 v[188:189], v66 offset:8224\n\t" \
  "ds_read_b64_tr_b16 v[190:191], v66 offset:9248\n\t" \
  "s_waitcnt vmcnt(0) lgkmcnt(0)\n\t" \
  "s_barrier\n\t" \
  "ds_read_b64_tr_b16 v[128:129], v66 offset:16384\n\t" \
  "ds_read_b64_tr_b16 v[130:131], v66 offset:17408\n\t" \
  "ds_read_b64_tr_b16 v[132:133], v66 offset:24576\n\t" \
  "ds_read_b64_tr_b16 v[134:135], v66 offset:25600\n\t" \
  "ds_read_b64_tr_b16 v[56:57], v66 offset:16416\n\t" \
  "ds_read_b64_tr_b16 v[58:59], v66 offset:17440\n\t" \
  "ds_read_b64_tr_b16 v[60:61], v66 offset:24608\n\t" \
  "ds_read_b64_tr_b16 v[62:63], v66 offset:25632\n\t" \
  CH_STV \
  "s_waitcnt lgkmcnt(0)\n\t" \
  "s_barrier\n\t" \
  "ds_write_b128 v70, v[0:3] offset:0\n\t" \
  "ds_write_b128 v70, v[4:7] offset:1024\n\t" \
  "ds_write_b128 v70, v[8:11] offset:2048\n\t" \
  "ds_write_b128 v70, v[12:15] offset:3072\n\t" \
  "ds_write_b128 v70, v[16:19] offset:4096\n\t" \
  "ds_write_b128 v70, v[20:23] offset:5120\n\t" \
  "ds_write_b128 v70, v[24:27] offset:6144\n\t" \
  "ds_write_b128 v70, v[28:31] offset:7168\n\t" \
  "ds_write_b128 v70, v[32:35] offset:8192\n\t" \
  "ds_write_b128 v70, v[36:39] offset:9216\n\t" \
  "ds_write_b128 v70, v[40:43] offset:10240\n\t" \
  "ds_write_b128 v70, v[44:47] offset:11264\n\t" \
  "ds_write_b128 v70, v[48:51] offset:12288\n\t" \
  "ds_write_b128 v70, v[52:55] offset:13312\n\t" \
  "ds_write_b128 v70, v[56:59] offset:14336\n\t" \
  "ds_write_b128 v70, v[60:63] offset:15360\n\t" \
  "s_waitcnt lgkmcnt(0)\n\t" \
  CH_STW \
  "v_and_b32 v0, 2, v74\n\t" \
  "v_cmp_ne_u32 vcc, 0, v0\n\t" \
  "s_nop 1\n\t" \
  "s_lshl_b32 s60, vcc_lo, 1\n\t" \
  "s_bfe_u32 s62, vcc_lo, 0x10002\n\t" \
  "s_or_b32 s60, s60, s62\n\t" \
  "s_lshr_b32 s61, vcc_lo, 1\n\t" \
  "s_bitset0_b32 s61, 21\n\t" \
  "s_bfe_u32 s62, vcc_lo, 0x10013\n\t" \
  "s_lshl_b32 s62, s62, 21\n\t" \
  "s_or_b32 s61, s61, s62\n\t" \
  "s_and_b32 s62, s60, s61\n\t" \
  "s_or_b32 s60, s60, s61\n\t" \
  "s_and_b32 s60, s60, vcc_lo\n\t" \
  "s_or_b32 s22, s60, s62\n\t" \
  "v_and_b32 v0, 4, v74\n\t" \
  "v_cmp_ne_u32 vcc, 0, v0\n\t" \
  "s_nop 1\n\t" \
  "s_lshl_b32 s60, vcc_lo, 1\n\t" \
  "s_bfe_u32 s62, vcc_lo, 0x10002\n\t" \
  "s_or_b32 s60, s60, s62\n\t" \
  "s_lshr_b32 s61, vcc_lo, 1\n\t" \
  "s_bitset0_b32 s61, 21\n\t" \
  "s_bfe_u32 s62, vcc_lo, 0x10013\n\t" \
  "s_lshl_b32 s62, s62, 21\n\t" \
  "s_or_b32 s61, s61, s62\n\t" \
  "s_and_b32 s62, s60, s61\n\t" \
  "s_or_b32 s60, s60, s61\n\t" \
  "s_and_b32 s60, s60, vcc_lo\n\t" \
  "s_or_b32 s23, s60, s62\n\t" \
  CH_COW \
  "v_and_b32 v72, 1, v72\n\t" \
  "v_and_b32 v74, 1, v74\n\t" \
  "v_cmp_ne_u32 s[90:91], s32, v72\n\t" \
  "v_cmp_ne_u32 vcc, s32, v74\n\t" \
  "s_nop 1\n\t" \
  "s_mov_b32 s96, vcc_lo\n\t" \
  "s_mov_b32 s21, vcc_lo\n\t" \
  "s_mov_b32 s48, -1\n\t" \
  "v_and_b32 v73, 32, v69\n\t" \
  "v_lshl_add_u32 v73, v73, 5, v69\n\t" \
  "s_add_u32 s97, s41, s53\n\t" \
  "s_lshr_b32 s62, s97, 21\n\t" \
  "s_lshr_b32 s60, s22, s62\n\t" \
  "s_lshr_b32 s63, s97, 19\n\t" \
  "s_xor_b32 s60, s60, s63\n\t" \
  "s_and_b32 s60, s60, " CH_AFF "\n\t" \
  "s_lshr_b32 s61, s23, s62\n\t" \
  "s_lshr_b32 s63, s97, 20\n\t" \
  "s_xor_b32 s61, s61, s63\n\t" \
  "s_and_b32 s61, s61, " CH_AFG "\n\t" \
  "s_lshl_b32 s62, s60, 14\n\t" \
  "s_xor_b32 s97, s97, s62\n\t" \
  "s_lshl_b32 s62, s61, 12\n\t" \
  "s_xor_b32 s97, s97, s62\n\t" \
  "s_lshl_b32 s60, s60, 8\n\t" \
  "s_lshl_b32 s61, s61, 7\n\t" \
  "s_xor_b32 s98, s37, s60\n\t" \
  "s_xor_b32 s98, s98, s61\n\t" \
  "s_add_u32 s98, s98, s53\n\t" \
  "s_add_u32 s82, s48, 0\n\t" \
  CH_SEGC \
  "s_lshr_b64 s[60:61], s[90:91], s82\n\t" \
  "s_and_b32 s60, s60, 1\n\t" \
  "s_lshl_b32 s60, s60, 13\n\t" \
  "s_add_u32 s82, s82, " CH_SG0 "\n\t" \
  "s_lshl_b32 s82, s82, 21\n\t" \
  "s_or_b32 s82, s82, s60\n\t" \
  "s_add_u32 s50, s82, s38\n\t" \
  "s_add_u32 s69, s82, s39\n\t" \
  "s_add_u32 s82, s48, 1\n\t" \
  CH_SEGC \
  "s_lshr_b64 s[60:61], s[90:91], s82\n\t" \
  "s_and_b32 s60, s60, 1\n\t" \
  "s_lshl_b32 s60, s60, 13\n\t" \
  "s_add_u32 s82, s82, " CH_SG0 "\n\t" \
  "s_lshl_b32 s82, s82, 21\n\t" \
  "s_or_b32 s82, s82, s60\n\t" \
  "s_add_u32 s52, s82, s43\n\t" \
  "s_add_u32 s82, s48, " CH_ARMD "\n\t" \
  CH_SEGC \
  "s_lshr_b64 s[60:61], s[90:91], s82\n\t" \
  "s_and_b32 s60, s60, 1\n\t" \
  "s_lshl_b32 s60, s60, 13\n\t" \
  "s_add_u32 s82, s82, " CH_SG0 "\n\t" \
  "s_lshl_b32 s82, s82, 21\n\t" \
  "s_or_b32 s82, s82, s60\n\t" \
  "s_add_u32 s85, s82, s45\n\t" \
  "s_cmp_lt_i32 s48, " CH_ARMS "\n\t" \
  "s_cselect_b32 s85, s27, s85\n\t" \
  "v_and_b32 v37, 0x30, v69\n\t" \
  "v_add_u32 v37, s52, v37\n\t" \
  "s_mov_b32 s48, 0\n\t" \
  "s_mov_b32 s86, 1\n\t" \
  "s_and_b32 s49, s96, 1\n\t" \
  "s_lshl_b32 s49, s49, 13\n\t" \
  "s_add_u32 s53, s49, " CH_SLOT "\n\t" \
  "s_movk_i32 s60, 124\n\t" \
  "s_cmp_eq_u32 s40, 3\n\t" \
  "s_cselect_b32 s81, 76, s60\n\t" \
  "s_add_u32 s82, s48, 0\n\t" \
  CH_SEGC \
  "s_lshr_b64 s[60:61], s[90:91], s82\n\t" \
  "s_and_b32 s60, s60, 1\n\t" \
  "s_lshl_b32 s60, s60, 13\n\t" \
  "s_add_u32 s82, s82, " CH_SG0 "\n\t" \
  "s_lshl_b32 s82, s82, 21\n\t" \
  "s_or_b32 s82, s82, s60\n\t" \
  "s_add_u32 s50, s82, s38\n\t" \
  "s_add_u32 s69, s82, s39\n\t" \
  "s_add_u32 s51, s82, s43\n\t" \
  "s_add_u32 s97, s41, s53\n\t" \
  "s_lshr_b32 s62, s97, 21\n\t" \
  "s_lshr_b32 s60, s22, s62\n\t" \
  "s_lshr_b32 s63, s97, 19\n\t" \
  "s_xor_b32 s60, s60, s63\n\t" \
  "s_and_b32 s60, s60, " CH_AFF "\n\t" \
  "s_lshr_b32 s61, s23, s62\n\t" \
  "s_lshr_b32 s63, s97, 20\n\t" \
  "s_xor_b32 s61, s61, s63\n\t" \
  "s_and_b32 s61, s61, " CH_AFG "\n\t" \
  "s_lshl_b32 s62, s60, 14\n\t" \
  "s_xor_b32 s97, s97, s62\n\t" \
  "s_lshl_b32 s62, s61, 12\n\t" \
  "s_xor_b32 s97, s97, s62\n\t" \
  "s_lshl_b32 s60, s60, 8\n\t" \
  "s_lshl_b32 s61, s61, 7\n\t" \
  "s_xor_b32 s98, s37, s60\n\t" \
  "s_xor_b32 s98, s98, s61\n\t" \
  "s_add_u32 s98, s98, s53\n\t" \
  "s_add_u32 s82, s48, 0\n\t" \
  CH_SEGC \
  "s_lshr_b64 s[60:61], s[90:91], s82\n\t" \
  "s_and_b32 s60, s60, 1\n\t" \
  "s_lshl_b32 s60, s60, 13\n\t" \
  "s_add_u32 s82, s82, " CH_SG0 "\n\t" \
  "s_lshl_b32 s82, s82, 21\n\t" \
  "s_or_b32 s82, s82, s60\n\t" \
  "s_add_u32 s50, s82, s38\n\t" \
  "s_add_u32 s69, s82, s39\n\t" \
  "s_add_u32 s82, s48, 1\n\t" \
  CH_SEGC \
  "s_lshr_b64 s[60:61], s[90:91], s82\n\t" \
  "s_and_b32 s60, s60, 1\n\t" \
  "s_lshl_b32 s60, s60, 13\n\t" \
  "s_add_u32 s82, s82, " CH_SG0 "\n\t" \
  "s_lshl_b32 s82, s82, 21\n\t" \
  "s_or_b32 s82, s82, s60\n\t" \
  "s_add_u32 s52, s82, s43\n\t" \
  "s_add_u32 s82, s48, " CH_ARMD "\n\t" \
  CH_SEGC \
  "s_lshr_b64 s[60:61], s[90:91], s82\n\t" \
  "s_and_b32 s60, s60, 1\n\t" \
  "s_lshl_b32 s60, s60, 13\n\t" \
  "s_add_u32 s82, s82, " CH_SG0 "\n\t" \
  "s_lshl_b32 s82, s82, 21\n\t" \
  "s_or_b32 s82, s82, s60\n\t" \
  "s_add_u32 s85, s82, s45\n\t" \
  "s_cmp_lt_i32 s48, " CH_ARMS "\n\t" \
  "s_cselect_b32 s85, s27, s85\n\t" \
  "s_cmp_eq_u32 s40, 3\n\t" \
  "s_cbranch_scc1 LI3_%=\n\t" \
  "s_add_u32 s97, s41, s53\n\t" \
  "s_lshr_b32 s62, s97, 21\n\t" \
  "s_lshr_b32 s60, s22, s62\n\t" \
  "s_lshr_b32 s63, s97, 19\n\t" \
  "s_xor_b32 s60, s60, s63\n\t" \
  "s_and_b32 s60, s60, " CH_AFF "\n\t" \
  "s_lshr_b32 s61, s23, s62\n\t" \
  "s_lshr_b32 s63, s97, 20\n\t" \
  "s_xor_b32 s61, s61, s63\n\t" \
  "s_and_b32 s61, s61, " CH_AFG "\n\t" \
  "s_lshl_b32 s62, s60, 14\n\t" \
  "s_xor_b32 s97, s97, s62\n\t" \
  "s_lshl_b32 s62, s61, 12\n\t" \
  "s_xor_b32 s97, s97, s62\n\t" \
  "s_lshl_b32 s60, s60, 7\n\t" \
  "s_lshl_b32 s61, s61, 10\n\t" \
  "s_xor_b32 s98, s37, s60\n\t" \
  "s_xor_b32 s98, s98, s61\n\t" \
  "s_add_u32 s98, s98, s53\n\t" \
  "s_add_u32 s82, s48, 0\n\t" \
  CH_SEGC \
  "s_lshr_b64 s[60:61], s[90:91], s82\n\t" \
  "s_and_b32 s60, s60, 1\n\t" \
  "s_lshl_b32 s60, s60, 13\n\t" \
  "s_add_u32 s82, s82, " CH_SG0 "\n\t" \
  "s_lshl_b32 s82, s82, 21\n\t" \
  "s_or_b32 s82, s82, s60\n\t" \
  "s_add_u32 s50, s82, s38\n\t" \
  "s_add_u32 s69, s82, s39\n\t" \
  "LI3_%=:\n\t" \
  "s_memrealtime s[62:63]\n\t" \
  "s_waitcnt lgkmcnt(0)\n\t" \
  "s_mov_b32 s65, s62\n\t" \
  "s_sub_u32 s47, s62, 1000\n\t" \
  "s_mov_b64 vcc, -1\n\t" \
  "s_cmp_eq_u32 s40, 0\n\t" \
  "s_cbranch_scc1 LW0_%=\n\t" \
  "s_barrier\n\t" \
  "s_cmp_eq_u32 s40, 1\n\t" \
  "s_cbranch_scc1 LG1_%=\n\t" \
  "s_cmp_eq_u32 s40, 2\n\t" \
  "s_cbranch_scc1 LG2_%=\n\t" \
  "s_branch LTK_%=\n\t" \
  ".p2align 6\n\t" \
  "LW0_%=:\n\t" \
  "v_lshlrev_b32 v78, 2, v69\n\t" \
  "v_and_b32 v78, 0x300, v78\n\t" \
  "v_lshrrev_b32 v79, 8, v69\n\t" \
  "v_lshl_add_u32 v78, v79, 6, v78\n\t" \
  "v_and_b32 v79, 48, v69\n\t" \
  "v_add_u32 v78, v78, v79\n\t" \
  "s_add_u32 s98, s49, s42\n\t" \
  "s_lshr_b32 s62, s98, 21\n\t" \
  "s_lshr_b32 s60, s22, s62\n\t" \
  "s_lshr_b32 s63, s98, 19\n\t" \
  "s_xor_b32 s60, s60, s63\n\t" \
  "s_and_b32 s60, s60, " CH_AFF "\n\t" \
  "s_lshr_b32 s61, s23, s62\n\t" \
  "s_lshr_b32 s63, s98, 20\n\t" \
  "s_xor_b32 s61, s61, s63\n\t" \
  "s_and_b32 s61, s61, " CH_AFG "\n\t" \
  "s_lshl_b32 s62, s60, 14\n\t" \
  "s_xor_b32 s98, s98, s62\n\t" \
  "s_lshl_b32 s62, s61, 12\n\t" \
  "s_xor_b32 s98, s98, s62\n\t" \
  "s_add_u32 s97, s98, 32768\n\t" \
  "s_add_u32 s99, s98, 65536\n\t" \
  "s_barrier\n\t" \
  "s_memrealtime s[62:63]\n\t" \
  "s_waitcnt lgkmcnt(0)\n\t" \
  "s_add_u32 s62, s62, " CH_DLL2 "\n\t" \
  "s_sub_i32 s63, s62, s65\n\t" \
  "s_add_u32 s62, s62, " CH_DLX "\n\t" \
  "s_cmp_gt_i32 s63, 0\n\t" \
  "s_cselect_b32 s65, s62, s65\n\t" \
  "s_mov_b32 m0, 0\n\t" \
  "s_movk_i32 s60, 8192\n\t" \
  "LWT_%=:\n\t" \
  "s_memrealtime s[62:63]\n\t" \
  "s_sub_u32 s60, s60, 1\n\t" \
  "s_cselect_b32 s61, 0, -1\n\t" \
  "s_waitcnt lgkmcnt(0)\n\t" \
  "s_sub_i32 s63, s62, s65\n\t" \
  "s_and_b32 s63, s63, s61\n\t" \
  "s_cmp_lt_i32 s63, " CH_FIN "\n\t" \
  "s_cbranch_scc1 LWT_%=\n\t" \
  "s_cmp_lt_i32 s63, 0\n\t" \
  "s_cbranch_scc1 LWT_%=f\n\t" \
  "LWT_%=g:\n\t" \
  "LDMA_%=:\n\t" \
  CH_DPC \
  "buffer_load_dwordx4 v78, s[28:31], s98 offen offset:0 sc1 lds\n\t" \
  "buffer_load_dwordx4 v78, s[28:31], s98 offen offset:1024 sc1 lds\n\t" \
  "buffer_load_dwordx4 v78, s[28:31], s98 offen offset:2048 sc1 lds\n\t" \
  "buffer_load_dwordx4 v78, s[28:31], s98 offen offset:3072 sc1 lds\n\t" \
  "s_movk_i32 m0, 4096\n\t" \
  "s_mov_b32 s66, s62\n\t" \
  "buffer_load_dwordx4 v78, s[28:31], s97 offen offset:0 sc1 lds\n\t" \
  "buffer_load_dwordx4 v78, s[28:31], s97 offen offset:1024 sc1 lds\n\t" \
  "buffer_load_dwordx4 v78, s[28:31], s97 offen offset:2048 sc1 lds\n\t" \
  "buffer_load_dwordx4 v78, s[28:31], s97 offen offset:3072 sc1 lds\n\t" \
  "s_movk_i32 m0, 8192\n\t" \
  "s_nop 0\n\t" \
  "buffer_load_dwordx4 v69, s[28:31], s99 offen sc1 lds\n\t" \
  CH_ST1 \
  ".p2align 6\n\t" \
  "LG0_%=:\n\t" \
  "s_barrier\n\t" \
  "buffer_load_dwordx4 v[52:55], v69, s[28:31], s50 offen sc1\n\t" \
  "buffer_load_dwordx4 v[56:59], v69, s[28:31], s50 offen offset:1024 sc1\n\t" \
  "s_add_u32 s64, s50, 16384\n\t" \
  "s_movk_i32 m0, 11264\n\t" \
  "v_lshlrev_b32 v75, 1, v69\n\t" \
  "buffer_load_dword v75, s[28:31], s64 offen sc1 lds\n\t" \
  "s_memrealtime s[62:63]\n\t" \
  "s_waitcnt lgkmcnt(0)\n\t" \
  "s_sub_u32 s62, s62, " CH_TKL "\n\t" \
  "s_sub_i32 s63, s62, s65\n\t" \
  "s_cmp_gt_i32 s63, 0\n\t" \
  "s_cselect_b32 s62, s62, s65\n\t" \
  "s_sub_i32 s63, s62, s47\n\t" \
  "s_cmp_gt_i32 s63, " CH_LATE "\n\t" \
  "s_cselect_b32 s71, " CH_SCHW ", s46\n\t" \
  "s_add_u32 s61, s47, " CH_LATE "\n\t" \
  "s_cmp_gt_i32 s63, " CH_LATE "\n\t" \
  "s_cselect_b32 s62, s62, s61\n\t" \
  "s_add_u32 s71, s71, s53\n\t" \
  "s_add_u32 s60, s62, s81\n\t" \
  "s_add_u32 s60, s60, " CH_DL1L "\n\t" \
  "s_lshl_b32 s60, s60, 8\n\t" \
  "s_or_b32 s60, s60, " CH_B0 "\n\t" \
  "s_not_b32 s60, s60\n\t" \
  "v_mov_b32 v76, s60\n\t" \
  "s_mov_b64 exec, s[24:25]\n\t" \
  "buffer_atomic_umin v76, v69, s[28:31], s71 offen sc1\n\t" \
  "s_mov_b64 exec, -1\n\t" \
  "ds_read_b128 v[44:47], v70 offset:0\n\t" \
  "ds_read_b128 v[48:51], v70 offset:1024\n\t" \
  "ds_read_b128 v[60:63], v70 offset:2048\n\t" \
  "ds_read_b128 v[64:67], v70 offset:3072\n\t" \
  "v_mov_b64 v[36:37], 0\n\t" \
  "v_mov_b64 v[38:39], 0\n\t" \
  "v_mov_b64 v[40:41], 0\n\t" \
  "v_mov_b64 v[42:43], 0\n\t" \
  "v_lshrrev_b32 v71, 1, v69\n\t" \
  "v_lshrrev_b32 v72, 3, v69\n\t" \
  "v_and_b32 v72, 0x78, v72\n\t" \
  "v_and_b32 v74, 0x30, v69\n\t" \
  "v_lshl_add_u32 v72, v74, 3, v72\n\t" \
  "s_add_u32 s54, s53, s36\n\t" \
  "s_add_u32 s55, s54, 524288\n\t" \
  "s_add_u32 s54, s54, 0\n\t" \
  "s_add_u32 s75, s54, 262144\n\t" \
  "s_add_u32 s80, s53, s26\n\t" \
  "s_or_b32 s87, s53, 0x1fff80\n\t" \
  "s_add_u32 s87, s87, 2097152\n\t" \
  CH_TC0 \
  "s_waitcnt vmcnt(9)\n\t" \
  "s_barrier\n\t" \
  CH_DP0 \
  "ds_read_b128 v[0:3], v69 offset:0\n\t" \
  "ds_read_b128 v[4:7], v69 offset:1024\n\t" \
  "ds_read_b128 v[8:11], v69 offset:2048\n\t" \
  "ds_read_b128 v[12:15], v69 offset:3072\n\t" \
  "s_waitcnt lgkmcnt(3)\n\t" \
  "v_smfmac_f32_16x16x64_bf16 v[36:39], v[0:3], v[44:51], v68\n\t" \
  "ds_read_b128 v[44:47], v70 offset:4096\n\t" \
  "v_smfmac_f32_16x16x64_bf16 v[40:43], v[0:3], v[60:67], v68\n\t" \
  "ds_read_b128 v[48:51], v70 offset:5120\n\t" \
  "s_waitcnt lgkmcnt(4)\n\t" \
  "v_smfmac_f32_16x16x64_bf16 v[36:39], v[4:7], a[0:7], v68\n\t" \
  "s_waitcnt vmcnt(4)\n\t" \
  "s_barrier\n\t" \
  "ds_read_b128 v[16:19], v69 offset:4096\n\t" \
  CH_DP1 \
  CH_TG2 \
  "v_smfmac_f32_16x16x64_bf16 v[40:43], v[4:7], a[64:71], v68\n\t" \
  "ds_read_b128 v[20:23], v69 offset:5120\n\t" \
  "s_waitcnt lgkmcnt(5)\n\t" \
  "v_smfmac_f32_16x16x64_bf16 v[36:39], v[8:11], a[8:15], v68\n\t" \
  "ds_read_b128 v[24:27], v69 offset:6144\n\t" \
  "v_smfmac_f32_16x16x64_bf16 v[40:43], v[8:11], a[72:79], v68\n\t" \
  "ds_read_b128 v[28:31], v69 offset:7168\n\t" \
  "s_waitcnt lgkmcnt(6)\n\t" \
  "v_smfmac_f32_16x16x64_bf16 v[36:39], v[12:15], a[16:23], v68\n\t" \
  "ds_read_b128 v[32:35], v69 offset:8192\n\t" \
  "s_waitcnt lgkmcnt(5)\n\t" \
  "v_smfmac_f32_16x16x64_bf16 v[40:43], v[12:15], v[44:51], v68\n\t" \
  "ds_read_b128 v[60:63], v70 offset:6144\n\t" \
  "s_waitcnt lgkmcnt(5)\n\t" \
  "v_smfmac_f32_16x16x64_bf16 v[36:39], v[16:19], a[24:31], v68\n\t" \
  "ds_read_b128 v[64:67], v70 offset:7168\n\t" \
  "v_smfmac_f32_16x16x64_bf16 v[40:43], v[16:19], a[80:87], v68\n\t" \
  "s_waitcnt lgkmcnt(5)\n\t" \
  "v_mov_b64 v[44:45], 0\n\t" \
  "v_smfmac_f32_16x16x64_bf16 v[36:39], v[20:23], a[32:39], v68\n\t" \
  "s_waitcnt lgkmcnt(4)\n\t" \
  "v_mov_b64 v[46:47], 0\n\t" \
  "v_smfmac_f32_16x16x64_bf16 v[36:39], v[24:27], a[40:47], v68\n\t" \
  "s_waitcnt lgkmcnt(2)\n\t" \
  "v_mov_b64 v[48:49], 0\n\t" \
  "v_smfmac_f32_16x16x64_bf16 v[36:39], v[28:31], a[48:55], v68\n\t" \
  "v_mov_b64 v[50:51], 0\n\t" \
  "v_smfmac_f32_16x16x64_bf16 v[36:39], v[32:35], a[56:63], v68\n\t" \
  "s_waitcnt lgkmcnt(0)\n\t" \
  "v_smfmac_f32_16x16x64_bf16 v[40:43], v[20:23], v[60:67], v68\n\t" \
  "ds_read_b128 v[60:63], v70 offset:8192\n\t" \
  "v_smfmac_f32_16x16x64_bf16 v[40:43], v[24:27], a[88:95], v68\n\t" \
  "ds_read_b128 v[64:67], v70 offset:9216\n\t" \
  "v_add_f32 v74, v36, v38\n\t" \
  "v_smfmac_f32_16x16x64_bf16 v[40:43], v[28:31], a[96:103], v68\n\t" \
  "v_add_f32 v37, v37, v39\n\t" \
  "v_smfmac_f32_16x16x64_bf16 v[40:43], v[32:35], a[104:111], v68\n\t" \
  "v_cmp_u_f32 vcc, v74, v37\n\t" \
  CH_DP2 \
  "v_smfmac_f32_16x16x64_bf16 v[44:47], v[0:3], a[112:119], v68\n\t" \
  "s_cbranch_vccnz LFAIL0_%=\n\t" \
  "LGO0_%=:\n\t" \
  "v_cvt_pk_bf16_f32 v74, v74, v37\n\t" \
  "v_smfmac_f32_16x16x64_bf16 v[44:47], v[4:7], a[120:127], v68\n\t" \
  "s_waitcnt lgkmcnt(0)\n\t" \
  "v_add_f32 v75, v40, v42\n\t" \
  "v_smfmac_f32_16x16x64_bf16 v[44:47], v[8:11], v[60:67], v68\n\t" \
  "ds_read_b128 v[60:63], v70 offset:10240\n\t" \
  "v_add_f32 v41, v41, v43\n\t" \
  "v_cvt_pk_bf16_f32 v75, v75, v41\n\t" \
  "v_smfmac_f32_16x16x64_bf16 v[44:47], v[12:15], a[128:135], v68\n\t" \
  "ds_write2st64_b32 v71, v74, v75 offset0:36 offset1:38\n\t" \
  "v_smfmac_f32_16x16x64_bf16 v[44:47], v[16:19], a[136:143], v68\n\t" \
  "ds_read_b128 v[64:67], v70 offset:11264\n\t" \
  "v_smfmac_f32_16x16x64_bf16 v[44:47], v[20:23], a[144:151], v68\n\t" \
  "v_smfmac_f32_16x16x64_bf16 v[44:47], v[24:27], a[152:159], v68\n\t" \
  "v_smfmac_f32_16x16x64_bf16 v[44:47], v[28:31], a[160:167], v68\n\t" \
  "s_waitcnt lgkmcnt(0)\n\t" \
  "v_smfmac_f32_16x16x64_bf16 v[44:47], v[32:35], v[60:67], v68\n\t" \
  "buffer_load_dwordx4 v[60:63], v69, s[28:31], s64 offen sc1\n\t" \
  "s_barrier\n\t" \
  "ds_read_b64_tr_b16 v[76:77], v72 offset:9216\n\t" \
  CH_DP3 \
  "v_smfmac_f32_16x16x64_bf16 v[48:51], v[0:3], a[168:175], v68\n\t" \
  "ds_read_b64_tr_b16 v[78:79], v72 offset:9728\n\t" \
  "v_smfmac_f32_16x16x64_bf16 v[48:51], v[4:7], a[176:183], v68\n\t" \
  "v_add_f32 v74, v44, v46\n\t" \
  "v_smfmac_f32_16x16x64_bf16 v[48:51], v[8:11], a[184:191], v68\n\t" \
  "v_add_f32 v45, v45, v47\n\t" \
  "v_smfmac_f32_16x16x64_bf16 v[48:51], v[12:15], a[192:199], v68\n\t" \
  "s_waitcnt vmcnt(4)\n\t" \
  "s_waitcnt lgkmcnt(0)\n\t" \
  "v_cvt_pk_bf16_f32 v74, v74, v45\n\t" \
  "v_mfma_f32_16x16x32_bf16 v[36:39], v[52:55], v[76:79], 0\n\t" \
  "buffer_load_dwordx4 v[64:67], v69, s[28:31], s64 offen offset:1024 sc1\n\t" \
  "s_waitcnt vmcnt(4)\n\t" \
  "v_mfma_f32_16x16x32_bf16 v[40:43], v[56:59], v[76:79], 0\n\t" \
  "v_mov_b64 v[52:53], 0\n\t" \
  "v_mov_b64 v[54:55], 0\n\t" \
  "v_smfmac_f32_16x16x64_bf16 v[48:51], v[16:19], a[200:207], v68\n\t" \
  "v_mov_b64 v[56:57], 0\n\t" \
  "v_mov_b64 v[58:59], 0\n\t" \
  "v_smfmac_f32_16x16x64_bf16 v[48:51], v[20:23], a[208:215], v68\n\t" \
  "ds_write_b32 v71, v74 offset:0\n\t" \
  "v_add_f32_dpp v36, v40, v36 quad_perm:[2,3,0,1] row_mask:0xf bank_mask:0xf\n\t" \
  "v_smfmac_f32_16x16x64_bf16 v[48:51], v[24:27], a[216:223], v68\n\t" \
  "v_add_f32_dpp v37, v41, v37 quad_perm:[2,3,0,1] row_mask:0xf bank_mask:0xf\n\t" \
  "v_smfmac_f32_16x16x64_bf16 v[48:51], v[28:31], a[224:231], v68\n\t" \
  "v_add_f32_dpp v38, v42, v38 quad_perm:[2,3,0,1] row_mask:0xf bank_mask:0xf\n\t" \
  "v_smfmac_f32_16x16x64_bf16 v[48:51], v[32:35], a[232:239], v68\n\t" \
  "v_add_f32_dpp v39, v43, v39 quad_perm:[2,3,0,1] row_mask:0xf bank_mask:0xf\n\t" \
  CH_DP4 \
  "v_smfmac_f32_16x16x64_bf16 v[52:55], v[0:3], a[240:247], v68\n\t" \
  "s_mov_b64 exec, s[58:59]\n\t" \
  "ds_write_b128 v69, v[36:39] offset:2048\n\t" \
  "s_mov_b64 exec, -1\n\t" \
  "v_smfmac_f32_16x16x64_bf16 v[52:55], v[4:7], a[248:255], v68\n\t" \
  "buffer_load_dword off, s[28:31], s87 sc1 lds\n\t" \
  "v_add_f32 v75, v48, v50\n\t" \
  "v_add_f32 v49, v49, v51\n\t" \
  "v_cvt_pk_bf16_f32 v75, v75, v49\n\t" \
  "v_smfmac_f32_16x16x64_bf16 v[56:59], v[0:3], v[136:143], v68\n\t" \
  "ds_write_b32 v71, v75 offset:512\n\t" \
  "v_smfmac_f32_16x16x64_bf16 v[56:59], v[4:7], v[144:151], v68\n\t" \
  "ds_read_b64_tr_b16 v[0:1], v72 offset:10240\n\t" \
  "ds_read_b64_tr_b16 v[2:3], v72 offset:10752\n\t" \
  "v_smfmac_f32_16x16x64_bf16 v[52:55], v[8:11], v[80:87], v68\n\t" \
  "ds_read_b128 v[36:39], v70 offset:12288\n\t" \
  "v_smfmac_f32_16x16x64_bf16 v[56:59], v[8:11], v[152:159], v68\n\t" \
  "ds_read_b128 v[40:43], v70 offset:13312\n\t" \
  "v_smfmac_f32_16x16x64_bf16 v[52:55], v[12:15], v[88:95], v68\n\t" \
  "s_waitcnt lgkmcnt(4)\n\t" \
  "s_barrier\n\t" \
  CH_DP5 \
  CH_TG5 \
  "ds_read_b64_tr_b16 v[44:45], v72 offset:0\n\t" \
  "ds_read_b64_tr_b16 v[46:47], v72 offset:512\n\t" \
  "v_smfmac_f32_16x16x64_bf16 v[56:59], v[12:15], v[160:167], v68\n\t" \
  "ds_read_b64_tr_b16 v[48:49], v72 offset:1024\n\t" \
  "ds_read_b64_tr_b16 v[50:51], v72 offset:1536\n\t" \
  "v_smfmac_f32_16x16x64_bf16 v[52:55], v[16:19], v[96:103], v68\n\t" \
  "ds_read_b128 v[4:7], v69 offset:2048\n\t" \
  "s_waitcnt lgkmcnt(5)\n\t" \
  "v_smfmac_f32_16x16x64_bf16 v[56:59], v[16:19], v[36:43], v68\n\t" \
  "ds_read_b128 v[8:11], v69 offset:3072\n\t" \
  "s_waitcnt lgkmcnt(4)\n\t" \
  "v_mfma_f32_16x16x32_bf16 v[12:15], v[44:47], v[76:79], 0\n\t" \
  "ds_read_b128 v[36:39], v70 offset:14336\n\t" \
  "s_waitcnt lgkmcnt(3)\n\t" \
  "v_mfma_f32_16x16x32_bf16 v[12:15], v[48:51], v[0:3], v[12:15]\n\t" \
  "ds_read_b128 v[40:43], v70 offset:15360\n\t" \
  "v_smfmac_f32_16x16x64_bf16 v[52:55], v[20:23], v[104:111], v68\n\t" \
  "s_waitcnt lgkmcnt(2)\n\t" \
  "v_add_f32 v4, v4, v8\n\t" \
  "v_add_f32 v5, v5, v9\n\t" \
  "v_add_f32 v6, v6, v10\n\t" \
  CH_DP6 \
  "v_cndmask_b32 v78, v13, v14, s[56:57]\n\t" \
  "v_cndmask_b32 v76, v12, v15, s[56:57]\n\t" \
  "v_add_f32 v7, v7, v11\n\t" \
  "v_max3_f32 v77, v4, v5, v6\n\t" \
  "v_add_f32_dpp v76, v78, v76 quad_perm:[2,3,0,1] row_mask:0xf bank_mask:0xf\n\t" \
  "v_max3_f32 v77, v77, v7, v76\n\t" \
  "v_smfmac_f32_16x16x64_bf16 v[56:59], v[20:23], v[168:175], v68\n\t" \
  "ds_read_b128 v[44:47], v70 offset:16384\n\t" \
  "v_max_f32_dpp v77, v77, v77 quad_perm:[2,3,0,1] row_mask:0xf bank_mask:0xf\n\t" \
  "v_mov_b32 v78, v77\n\t" \
  "v_smfmac_f32_16x16x64_bf16 v[52:55], v[24:27], v[112:119], v68\n\t" \
  "ds_read_b128 v[48:51], v70 offset:17408\n\t" \
  "v_permlane16_swap_b32 v77, v78\n\t" \
  "v_max_f32 v77, v77, v78\n\t" \
  "v_mov_b32 v78, v77\n\t" \
  "v_smfmac_f32_16x16x64_bf16 v[52:55], v[28:31], v[120:127], v68\n\t" \
  "ds_read_b128 v[16:19], v70 offset:18432\n\t" \
  "v_permlane32_swap_b32 v77, v78\n\t" \
  "v_max_f32 v77, v77, v78\n\t" \
  "v_smfmac_f32_16x16x64_bf16 v[52:55], v[32:35], v[128:135], v68\n\t" \
  "ds_read_b128 v[20:23], v70 offset:19456\n\t" \
  "v_mul_f32 v77, 0xbe0293ee, v77\n\t" \
  CH_DP7 \
  "v_smfmac_f32_16x16x64_bf16 v[56:59], v[24:27], v[176:183], v68\n\t" \
  "v_fmamk_f32 v4, v4, 0x3e0293ee, v77\n\t" \
  "v_smfmac_f32_16x16x64_bf16 v[56:59], v[28:31], v[184:191], v68\n\t" \
  "ds_read_b128 v[24:27], v70 offset:22528\n\t" \
  "s_waitcnt lgkmcnt(4)\n\t" \
  "v_fmamk_f32 v5, v5, 0x3e0293ee, v77\n\t" \
  "v_smfmac_f32_16x16x64_bf16 v[56:59], v[32:35], v[36:43], v68\n\t" \
  "ds_read_b128 v[28:31], v70 offset:23552\n\t" \
  "v_fmamk_f32 v6, v6, 0x3e0293ee, v77\n\t" \
  "v_fmamk_f32 v7, v7, 0x3e0293ee, v77\n\t" \
  "v_fmamk_f32 v76, v76, 0x3e0293ee, v77\n\t" \
  "v_exp_f32 v4, v4\n\t" \
  "v_exp_f32 v5, v5\n\t" \
  "v_exp_f32 v6, v6\n\t" \
  "v_exp_f32 v7, v7\n\t" \
  "v_cvt_pk_bf16_f32 v4, v4, v5\n\t" \
  "v_exp_f32 v76, v76\n\t" \
  "v_cvt_pk_bf16_f32 v5, v6, v7\n\t" \
  "v_mov_b32_dpp v12, v4 quad_perm:[0,1,0,1] row_mask:0xf bank_mask:0xf\n\t" \
  "v_mov_b32_dpp v14, v4 quad_perm:[2,3,2,3] row_mask:0xf bank_mask:0xf\n\t" \
  "v_mov_b32_dpp v13, v5 quad_perm:[0,1,0,1] row_mask:0xf bank_mask:0xf\n\t" \
  "v_mov_b32_dpp v15, v5 quad_perm:[2,3,2,3] row_mask:0xf bank_mask:0xf\n\t" \
  "v_add_f32 v52, v52, v54\n\t" \
  "v_add_f32 v53, v53, v55\n\t" \
  "s_waitcnt vmcnt(2)\n\t" \
  CH_DP8 \
  "v_mfma_f32_16x16x32_bf16 v[0:3], v[60:63], v[12:15], 0\n\t" \
  "ds_read_b128 v[36:39], v70 offset:20480\n\t" \
  "v_add_f32 v56, v56, v58\n\t" \
  "v_add_f32 v57, v57, v59\n\t" \
  "v_cvt_pk_bf16_f32 v52, v52, v53\n\t" \
  "v_cvt_pk_bf16_f32 v53, v56, v57\n\t" \
  "s_waitcnt vmcnt(1)\n\t" \
  "v_mfma_f32_16x16x32_bf16 v[4:7], v[64:67], v[12:15], 0\n\t" \
  "buffer_store_dwordx2 v[52:53], v71, s[28:31], s69 offen offset:2048 sc1\n\t" \
  "ds_read_b128 v[40:43], v70 offset:21504\n\t" \
  "v_mov_b64 v[56:57], s[88:89]\n\t" \
  "v_mov_b64 v[58:59], s[88:89]\n\t" \
  "v_mov_b32_dpp v78, v76 quad_perm:[0,1,0,1] row_mask:0xf bank_mask:0xf\n\t" \
  "v_mov_b32_dpp v79, v76 quad_perm:[2,3,2,3] row_mask:0xf bank_mask:0xf\n\t" \
  "v_mfma_f32_16x16x32_bf16 v[32:35], v[56:59], v[12:15], 0\n\t" \
  "ds_read_b128 v[60:63], v70 offset:24576\n\t" \
  "v_cvt_pk_bf16_f32 v11, v78, v79\n\t" \
  "v_cvt_pk_bf16_f32 v8, v78, v79\n\t" \
  "v_mov_b64 v[54:55], 0\n\t" \
  "v_mov_b32 v9, 0\n\t" \
  "v_mov_b32 v10, 0\n\t" \
  "v_mov_b64 v[14:15], 0\n\t" \
  "s_nop 0\n\t" \
  "v_mfma_f32_16x16x32_bf16 v[32:35], v[54:57], v[8:11], v[32:35]\n\t" \
  "s_cmp_eq_u32 s48, 63\n\t" \
  "v_mfma_f32_16x16x32_bf16 v[0:3], v[52:55], v[8:11], v[0:3]\n\t" \
  "ds_read_b128 v[64:67], v70 offset:25600\n\t" \
  "v_mov_b64 v[56:57], 0\n\t" \
  "v_mov_b64 v[58:59], 0\n\t" \
  "v_mfma_f32_16x16x32_bf16 v[4:7], v[52:55], v[10:13], v[4:7]\n\t" \
  "s_cbranch_scc1 LLAST0_%=\n\t" \
  "v_rcp_f32 v74, v32\n\t" \
  "v_mov_b64 v[12:13], 0\n\t" \
  "v_mov_b32 v8, 0\n\t" \
  "v_mul_f32 v0, v0, v74\n\t" \
  "v_mul_f32 v1, v1, v74\n\t" \
  "v_mul_f32 v2, v2, v74\n\t" \
  "v_mul_f32 v3, v3, v74\n\t" \
  "v_cvt_pk_bf16_f32 v76, v0, v1\n\t" \
  "v_cvt_pk_bf16_f32 v77, v2, v3\n\t" \
  "v_pk_mul_f32 v[4:5], v[4:5], v[74:75] op_sel_hi:[1,0]\n\t" \
  "v_pk_mul_f32 v[6:7], v[6:7], v[74:75] op_sel_hi:[1,0]\n\t" \
  "v_cvt_pk_bf16_f32 v78, v4, v5\n\t" \
  "v_cvt_pk_bf16_f32 v79, v6, v7\n\t" \
  "ds_write_b128 v69, v[76:79] offset:4096\n\t" \
  CH_DP9 \
  "s_waitcnt lgkmcnt(2)\n\t" \
  "v_smfmac_f32_16x16x64_bf16 v[12:15], v[76:79], v[44:51], v68\n\t" \
  "ds_read_b128 v[44:47], v70 offset:26624\n\t" \
  "v_mov_b32 v11, 0\n\t" \
  "v_smfmac_f32_16x16x64_bf16 v[56:59], v[76:79], v[16:23], v68\n\t" \
  "ds_read_b128 v[48:51], v70 offset:27648\n\t" \
  "v_mov_b64 v[52:53], 0\n\t" \
  "v_smfmac_f32_16x16x64_bf16 v[8:11], v[76:79], v[36:43], v68\n\t" \
  "ds_read_b128 v[16:19], v70 offset:28672\n\t" \
  "v_smfmac_f32_16x16x64_bf16 v[52:55], v[76:79], v[24:31], v68\n\t" \
  "s_waitcnt lgkmcnt(3)\n\t" \
  "s_barrier\n\t" \
  CH_DP10 \
  "ds_read_b128 v[0:3], v69 offset:5120\n\t" \
  "ds_read_b128 v[4:7], v69 offset:6144\n\t" \
  "v_lshrrev_b32 v71, 7, v69\n\t" \
  "v_and_b32 v74, 0x70, v69\n\t" \
  "v_lshl_add_u32 v71, v71, 14, v74\n\t" \
  "v_bfe_u32 v72, v69, 2, 7\n\t" \
  "v_and_b32 v74, 0x200, v69\n\t" \
  "s_waitcnt lgkmcnt(1)\n\t" \
  "v_smfmac_f32_16x16x64_bf16 v[8:11], v[0:3], v[60:67], v68\n\t" \
  "ds_read_b128 v[20:23], v70 offset:29696\n\t" \
  "v_mov_b64 v[76:77], 0\n\t" \
  "v_smfmac_f32_16x16x64_bf16 v[12:15], v[0:3], v[200:207], v68\n\t" \
  "ds_read_b128 v[32:35], v69 offset:7168\n\t" \
  "v_mov_b64 v[78:79], 0\n\t" \
  "v_smfmac_f32_16x16x64_bf16 v[52:55], v[0:3], v[44:51], v68\n\t" \
  "ds_read_b128 v[24:27], v70 offset:30720\n\t" \
  "v_lshl_add_u32 v72, v74, 3, v72\n\t" \
  "v_smfmac_f32_16x16x64_bf16 v[56:59], v[0:3], v[232:239], v68\n\t" \
  "buffer_load_dword v75, off, s[28:31], s80 sc1\n\t" \
  "ds_read_b128 v[28:31], v70 offset:31744\n\t" \
  "s_waitcnt lgkmcnt(4)\n\t" \
  "v_smfmac_f32_16x16x64_bf16 v[12:15], v[4:7], v[208:215], v68\n\t" \
  "ds_read_b128 v[60:63], v70 offset:32768\n\t" \
  "s_waitcnt lgkmcnt(4)\n\t" \
  "v_smfmac_f32_16x16x64_bf16 v[8:11], v[4:7], v[16:23], v68\n\t" \
  "ds_read_b128 v[64:67], v70 offset:33792\n\t" \
  "s_waitcnt lgkmcnt(4)\n\t" \
  "v_smfmac_f32_16x16x64_bf16 v[8:11], v[32:35], v[192:199], v68\n\t" \
  "ds_read_b128 v[36:39], v73 offset:4096\n\t" \
  "v_smfmac_f32_16x16x64_bf16 v[12:15], v[32:35], v[216:223], v68\n\t" \
  "ds_read_b128 v[40:43], v73 offset:6144\n\t" \
  "s_waitcnt lgkmcnt(4)\n\t" \
  "v_smfmac_f32_16x16x64_bf16 v[52:55], v[4:7], v[24:31], v68\n\t" \
  "ds_read_b128 v[44:47], v70 offset:34816\n\t" \
  "v_cvt_pk_bf16_f32 v8, v8, v9\n\t" \
  "v_smfmac_f32_16x16x64_bf16 v[52:55], v[32:35], v[224:231], v68\n\t" \
  "ds_read_b128 v[48:51], v70 offset:35840\n\t" \
  "v_cvt_pk_bf16_f32 v9, v10, v11\n\t" \
  "v_smfmac_f32_16x16x64_bf16 v[56:59], v[4:7], v[240:247], v68\n\t" \
  "s_waitcnt lgkmcnt(4)\n\t" \
  "v_cvt_pk_bf16_f32 v10, v12, v13\n\t" \
  "v_smfmac_f32_16x16x64_bf16 v[56:59], v[32:35], v[60:67], v68\n\t" \
  "s_waitcnt lgkmcnt(3)\n\t" \
  "v_cvt_pk_bf16_f32 v11, v14, v15\n\t" \
  "v_smfmac_f32_16x16x64_bf16 v[76:79], v[36:39], v[248:255], v68\n\t" \
  "buffer_store_dwordx4 v[8:11], v71, s[28:31], s54 offen sc1\n\t" \
  "s_waitcnt lgkmcnt(0)\n\t" \
  CH_DP11 \
  "v_cvt_pk_bf16_f32 v52, v52, v53\n\t" \
  "v_smfmac_f32_16x16x64_bf16 v[76:79], v[40:43], v[44:51], v68\n\t" \
  "v_cvt_pk_bf16_f32 v53, v54, v55\n\t" \
  "v_cvt_pk_bf16_f32 v54, v56, v57\n\t" \
  "v_cvt_pk_bf16_f32 v55, v58, v59\n\t" \
  "buffer_store_dwordx4 v[52:55], v71, s[28:31], s75 offen sc1\n\t" \
  "s_nop 2\n\t" \
  "v_add_f32 v76, v76, v78\n\t" \
  "v_add_f32 v77, v77, v79\n\t" \
  "v_cvt_pk_bf16_f32 v76, v76, v77\n\t" \
  "buffer_store_dword v76, v72, s[28:31], s55 offen sc1\n\t" \
  CH_DP12 \
  "s_memrealtime s[70:71]\n\t" \
  "s_waitcnt vmcnt(3)\n\t" \
  "v_readfirstlane_b32 s65, v75\n\t" \
  "s_barrier\n\t" \
  "s_waitcnt lgkmcnt(0)\n\t" \
  "s_not_b32 s65, s65\n\t" \
  "s_lshl_b32 s61, s70, 8\n\t" \
  "s_andn2_b32 s64, s65, 0xff\n\t" \
  "s_sub_u32 s64, s64, s61\n\t" \
  "s_ashr_i32 s64, s64, 8\n\t" \
  "s_cmp_eq_u32 s65, 0\n\t" \
  "s_cselect_b32 s64, " CH_DLF ", s64\n\t" \
  "s_cselect_b32 s65, " CH_B0 ", s65\n\t" \
  "s_add_u32 s47, s64, s70\n\t" \
  "s_min_i32 s64, s64, s34\n\t" \
  "s_max_i32 s64, s64, " CH_DLL1 "\n\t" \
  "s_add_u32 s64, s64, s70\n\t" \
  CH_ST2 \
  "s_add_u32 s55, s64, " CH_FDL "\n\t" \
  "s_movk_i32 s60, 8192\n\t" \
  "FWT_%=:\n\t" \
  "s_memrealtime s[62:63]\n\t" \
  "s_sub_u32 s60, s60, 1\n\t" \
  "s_cselect_b32 s61, 0, -1\n\t" \
  "s_waitcnt lgkmcnt(0)\n\t" \
  "s_sub_i32 s63, s62, s55\n\t" \
  "s_and_b32 s63, s63, s61\n\t" \
  "s_cmp_lt_i32 s63, 0\n\t" \
  "s_cbranch_scc1 FWT_%=\n\t" \
  "s_mov_b64 exec, 1\n\t" \
  "buffer_store_dword v68, off, s[28:31], s51 offset:4064 sc1\n\t" \
  "s_mov_b64 exec, -1\n\t" \
  CH_TCH \
  "s_and_b32 s65, s65, 0xff\n\t" \
  "s_add_u32 s47, s47, s65\n\t" \
  "s_add_u32 s65, s65, s64\n\t" \
  "s_sub_u32 s60, s70, s66\n\t" \
  "s_add_u32 s61, s81, " CH_UP "\n\t" \
  "s_min_u32 s60, s60, s61\n\t" \
  "s_sub_u32 s61, s81, " CH_DN "\n\t" \
  "s_max_u32 s60, s60, s61\n\t" \
  "s_cmp_eq_u32 s68, 0\n\t" \
  "s_cselect_b32 s81, s60, s81\n\t" \
  CH_TRC \
  "s_add_u32 s48, s48, 1\n\t" \
  "s_mov_b32 s49, s53\n\t" \
  "s_add_u32 s53, s53, " CH_SLOT "\n\t" \
  "s_add_u32 s60, s53, " CH_SKIP "\n\t" \
  "s_lshr_b32 s61, s96, 1\n\t" \
  "s_add_u32 s86, s86, 1\n\t" \
  "s_cmp_eq_u32 s86, 3\n\t" \
  "s_cselect_b32 s53, s60, s53\n\t" \
  "s_cselect_b32 s96, s61, s96\n\t" \
  "s_cselect_b32 s86, 0, s86\n\t" \
  "s_cmp_ge_u32 s53, " CH_RING "\n\t" \
  "s_cselect_b32 s53, 0, s53\n\t" \
  "s_cselect_b32 s96, s21, s96\n\t" \
  "s_andn2_b32 s53, s53, 0x2000\n\t" \
  "s_and_b32 s60, s96, 1\n\t" \
  "s_lshl_b32 s60, s60, 13\n\t" \
  "s_or_b32 s53, s53, s60\n\t" \
  "s_mov_b32 s68, 0\n\t" \
  "s_add_u32 s82, s48, 0\n\t" \
  CH_SEGC \
  "s_lshr_b64 s[60:61], s[90:91], s82\n\t" \
  "s_and_b32 s60, s60, 1\n\t" \
  "s_lshl_b32 s60, s60, 13\n\t" \
  "s_add_u32 s82, s82, " CH_SG0 "\n\t" \
  "s_lshl_b32 s82, s82, 21\n\t" \
  "s_or_b32 s82, s82, s60\n\t" \
  "s_add_u32 s50, s82, s38\n\t" \
  "s_add_u32 s69, s82, s39\n\t" \
  "s_add_u32 s51, s82, s43\n\t" \
  "s_cmp_lg_u32 s34, 0\n\t" \
  "s_cbranch_scc1 LW0_%=\n\t" \
  "s_barrier\n\t" \
  "s_branch LLOAD_%=\n\t" \
  "LFAIL0_%=:\n\t" \
  "v_max_u32 v75, v0, v1\n\t" \
  "v_max3_u32 v75, v75, v2, v3\n\t" \
  "v_max3_u32 v75, v75, v4, v5\n\t" \
  "v_max3_u32 v75, v75, v6, v7\n\t" \
  "v_max3_u32 v75, v75, v8, v9\n\t" \
  "v_max3_u32 v75, v75, v10, v11\n\t" \
  "v_max3_u32 v75, v75, v12, v13\n\t" \
  "v_max3_u32 v75, v75, v14, v15\n\t" \
  "v_max3_u32 v75, v75, v16, v17\n\t" \
  "v_max3_u32 v75, v75, v18, v19\n\t" \
  "v_max3_u32 v75, v75, v20, v21\n\t" \
  "v_max3_u32 v75, v75, v22, v23\n\t" \
  "v_max3_u32 v75, v75, v24, v25\n\t" \
  "v_max3_u32 v75, v75, v26, v27\n\t" \
  "v_max3_u32 v75, v75, v28, v29\n\t" \
  "v_max3_u32 v75, v75, v30, v31\n\t" \
  "v_max3_u32 v75, v75, v32, v33\n\t" \
  "v_max3_u32 v75, v75, v34, v35\n\t" \
  "v_cmp_eq_u32 vcc, -1, v75\n\t" \
  "s_cbranch_vccz LGO0_%=\n\t" \
  "s_add_u32 s68, s68, 1\n\t" \
  "s_waitcnt lgkmcnt(0)\n\t" \
  "s_cmp_gt_u32 s68, s83\n\t" \
  "s_cbranch_scc1 LGV0_%=\n\t" \
  "s_barrier\n\t" \
  "s_memrealtime s[62:63]\n\t" \
  "s_mov_b32 m0, 0\n\t" \
  "s_waitcnt lgkmcnt(0)\n\t" \
  "s_add_u32 s65, s62, " CH_PEN "\n\t" \
  "s_branch LDMA_%=\n\t" \
  "LWT_%=f:\n\t" \
  "s_cmp_lt_i32 s63, -2\n\t" \
  "s_cbranch_scc0 LWT_%=g\n\t" \
  "s_nop 4\n\t" \
  "s_branch LWT_%=g\n\t" \
  "LGV0_%=:\n\t" \
  "s_mov_b32 s83, 0\n\t" \
  "s_lshl_b32 s60, s36, 4\n\t" \
  "s_or_b32 s60, s60, s48\n\t" \
  "s_or_b32 s60, s60, 805306368\n\t" \
  "v_mov_b32 v76, s60\n\t" \
  "v_mov_b32 v77, 0\n\t" \
  "s_mov_b64 exec, 1\n\t" \
  "buffer_atomic_cmpswap v[76:77], off, s[28:31], s92 sc1\n\t" \
  "s_mov_b64 exec, -1\n\t" \
  "s_branch LGO0_%=\n\t" \
  "LLAST0_%=:\n\t" \
  "s_waitcnt vmcnt(0) lgkmcnt(0)\n\t" \
  "s_barrier\n\t" \
  "s_mov_b64 exec, 1\n\t" \
  "buffer_store_dword v68, off, s[28:31], s51 offset:4064 sc1\n\t" \
  "s_mov_b64 exec, -1\n\t" \
  "s_branch LEND_%=\n\t" \
  ".p2align 6\n\t" \
  "LG1_%=:\n\t" \
  "s_barrier\n\t" \
  "buffer_load_dwordx4 v[52:55], v69, s[28:31], s50 offen sc1\n\t" \
  "buffer_load_dwordx4 v[56:59], v69, s[28:31], s50 offen offset:1024 sc1\n\t" \
  "s_add_u32 s64, s50, 14336\n\t" \
  "s_movk_i32 m0, 11264\n\t" \
  "v_lshlrev_b32 v75, 1, v69\n\t" \
  "buffer_load_dword v75, s[28:31], s64 offen sc1 lds\n\t" \
  "ds_read_b128 v[44:47], v70 offset:0\n\t" \
  "ds_read_b128 v[48:51], v70 offset:1024\n\t" \
  "ds_read_b128 v[60:63], v70 offset:2048\n\t" \
  "ds_read_b128 v[64:67], v70 offset:3072\n\t" \
  "v_mov_b64 v[36:37], 0\n\t" \
  "v_mov_b64 v[38:39], 0\n\t" \
  "v_mov_b64 v[40:41], 0\n\t" \
  "v_mov_b64 v[42:43], 0\n\t" \
  "v_lshrrev_b32 v71, 1, v69\n\t" \
  "v_add_u32 v71, 4, v71\n\t" \
  "v_lshrrev_b32 v72, 3, v69\n\t" \
  "v_and_b32 v72, 0x78, v72\n\t" \
  "v_and_b32 v74, 0x30, v69\n\t" \
  "v_lshl_add_u32 v72, v74, 3, v72\n\t" \
  "s_add_u32 s54, s53, s36\n\t" \
  "s_add_u32 s55, s54, 540672\n\t" \
  "s_add_u32 s54, s54, 4096\n\t" \
  "s_add_u32 s75, s54, 262144\n\t" \
  "s_add_u32 s80, s53, s26\n\t" \
  "s_barrier\n\t" \
  CH_DP0 \
  "ds_read_b128 v[0:3], v69 offset:0\n\t" \
  "ds_read_b128 v[4:7], v69 offset:1024\n\t" \
  "ds_read_b128 v[8:11], v69 offset:2048\n\t" \
  "ds_read_b128 v[12:15], v69 offset:3072\n\t" \
  "s_waitcnt lgkmcnt(3)\n\t" \
  "v_smfmac_f32_16x16x64_bf16 v[36:39], v[0:3], v[44:51], v68\n\t" \
  "ds_read_b128 v[44:47], v70 offset:4096\n\t" \
  "v_smfmac_f32_16x16x64_bf16 v[40:43], v[0:3], v[60:67], v68\n\t" \
  "ds_read_b128 v[48:51], v70 offset:5120\n\t" \
  "s_waitcnt lgkmcnt(4)\n\t" \
  "v_smfmac_f32_16x16x64_bf16 v[36:39], v[4:7], a[0:7], v68\n\t" \
  "s_barrier\n\t" \
  "ds_read_b128 v[16:19], v69 offset:4096\n\t" \
  CH_DP1 \
  "v_smfmac_f32_16x16x64_bf16 v[40:43], v[4:7], a[64:71], v68\n\t" \
  "ds_read_b128 v[20:23], v69 offset:5120\n\t" \
  "s_waitcnt lgkmcnt(5)\n\t" \
  "v_smfmac_f32_16x16x64_bf16 v[36:39], v[8:11], a[8:15], v68\n\t" \
  "ds_read_b128 v[24:27], v69 offset:6144\n\t" \
  "v_smfmac_f32_16x16x64_bf16 v[40:43], v[8:11], a[72:79], v68\n\t" \
  "ds_read_b128 v[28:31], v69 offset:7168\n\t" \
  "s_waitcnt lgkmcnt(6)\n\t" \
  "v_smfmac_f32_16x16x64_bf16 v[36:39], v[12:15], a[16:23], v68\n\t" \
  "ds_read_b128 v[32:35], v69 offset:8192\n\t" \
  "s_waitcnt lgkmcnt(5)\n\t" \
  "v_smfmac_f32_16x16x64_bf16 v[40:43], v[12:15], v[44:51], v68\n\t" \
  "ds_read_b128 v[60:63], v70 offset:6144\n\t" \
  "s_waitcnt lgkmcnt(5)\n\t" \
  "v_smfmac_f32_16x16x64_bf16 v[36:39], v[16:19], a[24:31], v68\n\t" \
  "ds_read_b128 v[64:67], v70 offset:7168\n\t" \
  "v_smfmac_f32_16x16x64_bf16 v[40:43], v[16:19], a[80:87], v68\n\t" \
  "s_waitcnt lgkmcnt(5)\n\t" \
  "v_mov_b64 v[44:45], 0\n\t" \
  "v_smfmac_f32_16x16x64_bf16 v[36:39], v[20:23], a[32:39], v68\n\t" \
  "s_waitcnt lgkmcnt(4)\n\t" \
  "v_mov_b64 v[46:47], 0\n\t" \
  "v_smfmac_f32_16x16x64_bf16 v[36:39], v[24:27], a[40:47], v68\n\t" \
  "s_waitcnt lgkmcnt(2)\n\t" \
  "v_mov_b64 v[48:49], 0\n\t" \
  "v_smfmac_f32_16x16x64_bf16 v[36:39], v[28:31], a[48:55], v68\n\t" \
  "v_mov_b64 v[50:51], 0\n\t" \
  "v_smfmac_f32_16x16x64_bf16 v[36:39], v[32:35], a[56:63], v68\n\t" \
  "s_waitcnt lgkmcnt(0)\n\t" \
  "v_smfmac_f32_16x16x64_bf16 v[40:43], v[20:23], v[60:67], v68\n\t" \
  "ds_read_b128 v[60:63], v70 offset:8192\n\t" \
  "v_smfmac_f32_16x16x64_bf16 v[40:43], v[24:27], a[88:95], v68\n\t" \
  "ds_read_b128 v[64:67], v70 offset:9216\n\t" \
  "v_add_f32 v74, v36, v38\n\t" \
  "v_smfmac_f32_16x16x64_bf16 v[40:43], v[28:31], a[96:103], v68\n\t" \
  "v_add_f32 v37, v37, v39\n\t" \
  "v_smfmac_f32_16x16x64_bf16 v[40:43], v[32:35], a[104:111], v68\n\t" \
  "v_cmp_u_f32 vcc, v74, v37\n\t" \
  CH_DP2 \
  "v_smfmac_f32_16x16x64_bf16 v[44:47], v[0:3], a[112:119], v68\n\t" \
  "s_cbranch_vccnz LFAIL1_%=\n\t" \
  "LGO1_%=:\n\t" \
  "v_cvt_pk_bf16_f32 v74, v74, v37\n\t" \
  "v_smfmac_f32_16x16x64_bf16 v[44:47], v[4:7], a[120:127], v68\n\t" \
  "s_waitcnt lgkmcnt(0)\n\t" \
  "v_add_f32 v75, v40, v42\n\t" \
  "v_smfmac_f32_16x16x64_bf16 v[44:47], v[8:11], v[60:67], v68\n\t" \
  "ds_read_b128 v[60:63], v70 offset:10240\n\t" \
  "v_add_f32 v41, v41, v43\n\t" \
  "v_cvt_pk_bf16_f32 v75, v75, v41\n\t" \
  "v_smfmac_f32_16x16x64_bf16 v[44:47], v[12:15], a[128:135], v68\n\t" \
  "ds_write2st64_b32 v71, v74, v75 offset0:36 offset1:38\n\t" \
  "v_smfmac_f32_16x16x64_bf16 v[44:47], v[16:19], a[136:143], v68\n\t" \
  "ds_read_b128 v[64:67], v70 offset:11264\n\t" \
  "v_smfmac_f32_16x16x64_bf16 v[44:47], v[20:23], a[144:151], v68\n\t" \
  "v_smfmac_f32_16x16x64_bf16 v[44:47], v[24:27], a[152:159], v68\n\t" \
  "v_smfmac_f32_16x16x64_bf16 v[44:47], v[28:31], a[160:167], v68\n\t" \
  "s_waitcnt lgkmcnt(0)\n\t" \
  "v_smfmac_f32_16x16x64_bf16 v[44:47], v[32:35], v[60:67], v68\n\t" \
  "s_barrier\n\t" \
  "ds_read_b64_tr_b16 v[76:77], v72 offset:9216\n\t" \
  CH_DP3 \
  "v_smfmac_f32_16x16x64_bf16 v[48:51], v[0:3], a[168:175], v68\n\t" \
  "buffer_load_dwordx4 v[60:63], v69, s[28:31], s64 offen sc1\n\t" \
  "ds_read_b64_tr_b16 v[78:79], v72 offset:9728\n\t" \
  "v_smfmac_f32_16x16x64_bf16 v[48:51], v[4:7], a[176:183], v68\n\t" \
  "v_add_f32 v74, v44, v46\n\t" \
  "v_smfmac_f32_16x16x64_bf16 v[48:51], v[8:11], a[184:191], v68\n\t" \
  "v_add_f32 v45, v45, v47\n\t" \
  "v_smfmac_f32_16x16x64_bf16 v[48:51], v[12:15], a[192:199], v68\n\t" \
  "s_waitcnt vmcnt(3)\n\t" \
  "s_waitcnt lgkmcnt(0)\n\t" \
  "v_cvt_pk_bf16_f32 v74, v74, v45\n\t" \
  "v_mfma_f32_16x16x32_bf16 v[36:39], v[52:55], v[76:79], 0\n\t" \
  "s_waitcnt vmcnt(2)\n\t" \
  "v_mfma_f32_16x16x32_bf16 v[40:43], v[56:59], v[76:79], 0\n\t" \
  "buffer_load_dwordx4 v[64:67], v69, s[28:31], s64 offen offset:1024 sc1\n\t" \
  "v_mov_b64 v[52:53], 0\n\t" \
  "v_mov_b64 v[54:55], 0\n\t" \
  "v_smfmac_f32_16x16x64_bf16 v[48:51], v[16:19], a[200:207], v68\n\t" \
  "v_mov_b64 v[56:57], 0\n\t" \
  "v_mov_b64 v[58:59], 0\n\t" \
  "v_smfmac_f32_16x16x64_bf16 v[48:51], v[20:23], a[208:215], v68\n\t" \
  "ds_write_b32 v71, v74 offset:0\n\t" \
  "v_add_f32_dpp v36, v40, v36 quad_perm:[2,3,0,1] row_mask:0xf bank_mask:0xf\n\t" \
  "v_smfmac_f32_16x16x64_bf16 v[48:51], v[24:27], a[216:223], v68\n\t" \
  "v_add_f32_dpp v37, v41, v37 quad_perm:[2,3,0,1] row_mask:0xf bank_mask:0xf\n\t" \
  "v_smfmac_f32_16x16x64_bf16 v[48:51], v[28:31], a[224:231], v68\n\t" \
  "v_add_f32_dpp v38, v42, v38 quad_perm:[2,3,0,1] row_mask:0xf bank_mask:0xf\n\t" \
  "v_smfmac_f32_16x16x64_bf16 v[48:51], v[32:35], a[232:239], v68\n\t" \
  "v_add_f32_dpp v39, v43, v39 quad_perm:[2,3,0,1] row_mask:0xf bank_mask:0xf\n\t" \
  CH_DP4 \
  "v_smfmac_f32_16x16x64_bf16 v[52:55], v[0:3], a[240:247], v68\n\t" \
  "s_mov_b64 exec, s[58:59]\n\t" \
  "ds_write_b128 v69, v[36:39] offset:2080\n\t" \
  "s_mov_b64 exec, -1\n\t" \
  "v_smfmac_f32_16x16x64_bf16 v[52:55], v[4:7], a[248:255], v68\n\t" \
  "v_add_f32 v75, v48, v50\n\t" \
  "v_add_f32 v49, v49, v51\n\t" \
  "v_cvt_pk_bf16_f32 v75, v75, v49\n\t" \
  "v_smfmac_f32_16x16x64_bf16 v[56:59], v[0:3], v[136:143], v68\n\t" \
  "ds_write_b32 v71, v75 offset:512\n\t" \
  "v_smfmac_f32_16x16x64_bf16 v[56:59], v[4:7], v[144:151], v68\n\t" \
  "ds_read_b64_tr_b16 v[0:1], v72 offset:10240\n\t" \
  "ds_read_b64_tr_b16 v[2:3], v72 offset:10752\n\t" \
  "v_smfmac_f32_16x16x64_bf16 v[52:55], v[8:11], v[80:87], v68\n\t" \
  "ds_read_b128 v[36:39], v70 offset:12288\n\t" \
  "v_smfmac_f32_16x16x64_bf16 v[56:59], v[8:11], v[152:159], v68\n\t" \
  "ds_read_b128 v[40:43], v70 offset:13312\n\t" \
  "v_smfmac_f32_16x16x64_bf16 v[52:55], v[12:15], v[88:95], v68\n\t" \
  "s_waitcnt lgkmcnt(4)\n\t" \
  "s_barrier\n\t" \
  CH_DP5 \
  "ds_read_b64_tr_b16 v[44:45], v72 offset:0\n\t" \
  "ds_read_b64_tr_b16 v[46:47], v72 offset:512\n\t" \
  "v_smfmac_f32_16x16x64_bf16 v[56:59], v[12:15], v[160:167], v68\n\t" \
  "ds_read_b64_tr_b16 v[48:49], v72 offset:1024\n\t" \
  "ds_read_b64_tr_b16 v[50:51], v72 offset:1536\n\t" \
  "v_smfmac_f32_16x16x64_bf16 v[52:55], v[16:19], v[96:103], v68\n\t" \
  "ds_read_b128 v[4:7], v69 offset:2048\n\t" \
  "s_waitcnt lgkmcnt(5)\n\t" \
  "v_smfmac_f32_16x16x64_bf16 v[56:59], v[16:19], v[36:43], v68\n\t" \
  "ds_read_b128 v[8:11], v69 offset:3072\n\t" \
  "s_waitcnt lgkmcnt(4)\n\t" \
  "v_mfma_f32_16x16x32_bf16 v[12:15], v[44:47], v[76:79], 0\n\t" \
  "ds_read_b128 v[36:39], v70 offset:14336\n\t" \
  "s_waitcnt lgkmcnt(3)\n\t" \
  "v_mfma_f32_16x16x32_bf16 v[12:15], v[48:51], v[0:3], v[12:15]\n\t" \
  "buffer_store_dwordx4 v[44:47], v69, s[28:31], s69 offen offset:0 sc1\n\t" \
  "ds_read_b128 v[40:43], v70 offset:15360\n\t" \
  "v_smfmac_f32_16x16x64_bf16 v[52:55], v[20:23], v[104:111], v68\n\t" \
  "s_waitcnt lgkmcnt(2)\n\t" \
  "v_add_f32 v4, v4, v8\n\t" \
  "v_add_f32 v5, v5, v9\n\t" \
  "v_add_f32 v6, v6, v10\n\t" \
  CH_DP6 \
  "v_cndmask_b32 v78, v13, v14, s[56:57]\n\t" \
  "v_cndmask_b32 v76, v12, v15, s[56:57]\n\t" \
  "v_add_f32 v7, v7, v11\n\t" \
  "v_max3_f32 v77, v4, v5, v6\n\t" \
  "v_add_f32_dpp v76, v78, v76 quad_perm:[2,3,0,1] row_mask:0xf bank_mask:0xf\n\t" \
  "v_max3_f32 v77, v77, v7, v76\n\t" \
  "v_smfmac_f32_16x16x64_bf16 v[56:59], v[20:23], v[168:175], v68\n\t" \
  "ds_read_b128 v[44:47], v70 offset:16384\n\t" \
  "v_max_f32_dpp v77, v77, v77 quad_perm:[2,3,0,1] row_mask:0xf bank_mask:0xf\n\t" \
  "v_mov_b32 v78, v77\n\t" \
  "v_smfmac_f32_16x16x64_bf16 v[52:55], v[24:27], v[112:119], v68\n\t" \
  "ds_read_b128 v[48:51], v70 offset:17408\n\t" \
  "v_permlane16_swap_b32 v77, v78\n\t" \
  "v_max_f32 v77, v77, v78\n\t" \
  "v_mov_b32 v78, v77\n\t" \
  "v_smfmac_f32_16x16x64_bf16 v[52:55], v[28:31], v[120:127], v68\n\t" \
  "ds_read_b128 v[16:19], v70 offset:18432\n\t" \
  "v_permlane32_swap_b32 v77, v78\n\t" \
  "v_max_f32 v77, v77, v78\n\t" \
  "v_smfmac_f32_16x16x64_bf16 v[52:55], v[32:35], v[128:135], v68\n\t" \
  "ds_read_b128 v[20:23], v70 offset:19456\n\t" \
  "v_mul_f32 v77, 0xbe0293ee, v77\n\t" \
  CH_DP7 \
  "v_smfmac_f32_16x16x64_bf16 v[56:59], v[24:27], v[176:183], v68\n\t" \
  "v_fmamk_f32 v4, v4, 0x3e0293ee, v77\n\t" \
  "v_smfmac_f32_16x16x64_bf16 v[56:59], v[28:31], v[184:191], v68\n\t" \
  "ds_read_b128 v[24:27], v70 offset:22528\n\t" \
  "s_waitcnt lgkmcnt(4)\n\t" \
  "v_fmamk_f32 v5, v5, 0x3e0293ee, v77\n\t" \
  "v_smfmac_f32_16x16x64_bf16 v[56:59], v[32:35], v[36:43], v68\n\t" \
  "ds_read_b128 v[28:31], v70 offset:23552\n\t" \
  "v_fmamk_f32 v6, v6, 0x3e0293ee, v77\n\t" \
  "v_fmamk_f32 v7, v7, 0x3e0293ee, v77\n\t" \
  "v_fmamk_f32 v76, v76, 0x3e0293ee, v77\n\t" \
  "v_exp_f32 v4, v4\n\t" \
  "v_exp_f32 v5, v5\n\t" \
  "v_exp_f32 v6, v6\n\t" \
  "v_exp_f32 v7, v7\n\t" \
  "v_cvt_pk_bf16_f32 v4, v4, v5\n\t" \
  "v_exp_f32 v76, v76\n\t" \
  "v_cvt_pk_bf16_f32 v5, v6, v7\n\t" \
  "v_mov_b32_dpp v12, v4 quad_perm:[0,1,0,1] row_mask:0xf bank_mask:0xf\n\t" \
  "v_mov_b32_dpp v14, v4 quad_perm:[2,3,2,3] row_mask:0xf bank_mask:0xf\n\t" \
  "v_mov_b32_dpp v13, v5 quad_perm:[0,1,0,1] row_mask:0xf bank_mask:0xf\n\t" \
  "v_mov_b32_dpp v15, v5 quad_perm:[2,3,2,3] row_mask:0xf bank_mask:0xf\n\t" \
  "v_add_f32 v52, v52, v54\n\t" \
  "v_add_f32 v53, v53, v55\n\t" \
  "s_waitcnt vmcnt(2)\n\t" \
  CH_DP8 \
  "v_mfma_f32_16x16x32_bf16 v[0:3], v[60:63], v[12:15], 0\n\t" \
  "ds_read_b128 v[36:39], v70 offset:20480\n\t" \
  "v_add_f32 v56, v56, v58\n\t" \
  "v_add_f32 v57, v57, v59\n\t" \
  "v_cvt_pk_bf16_f32 v52, v52, v53\n\t" \
  "v_cvt_pk_bf16_f32 v53, v56, v57\n\t" \
  "s_waitcnt vmcnt(1)\n\t" \
  "v_mfma_f32_16x16x32_bf16 v[4:7], v[64:67], v[12:15], 0\n\t" \
  "ds_read_b128 v[40:43], v70 offset:21504\n\t" \
  "v_mov_b64 v[56:57], s[88:89]\n\t" \
  "v_mov_b64 v[58:59], s[88:89]\n\t" \
  "v_mov_b32_dpp v78, v76 quad_perm:[0,1,0,1] row_mask:0xf bank_mask:0xf\n\t" \
  "v_mov_b32_dpp v79, v76 quad_perm:[2,3,2,3] row_mask:0xf bank_mask:0xf\n\t" \
  "v_mfma_f32_16x16x32_bf16 v[32:35], v[56:59], v[12:15], 0\n\t" \
  "buffer_store_dwordx2 v[52:53], v71, s[28:31], s69 offen offset:2556 sc1\n\t" \
  "ds_read_b128 v[60:63], v70 offset:24576\n\t" \
  "v_cvt_pk_bf16_f32 v11, v78, v79\n\t" \
  "v_cvt_pk_bf16_f32 v8, v78, v79\n\t" \
  "v_mov_b64 v[54:55], 0\n\t" \
  "v_mov_b32 v9, 0\n\t" \
  "v_mov_b32 v10, 0\n\t" \
  "v_mov_b64 v[14:15], 0\n\t" \
  "s_nop 0\n\t" \
  "v_mfma_f32_16x16x32_bf16 v[32:35], v[54:57], v[8:11], v[32:35]\n\t" \
  "s_cmp_eq_u32 s48, 63\n\t" \
  "v_mfma_f32_16x16x32_bf16 v[0:3], v[52:55], v[8:11], v[0:3]\n\t" \
  "ds_read_b128 v[64:67], v70 offset:25600\n\t" \
  "v_mov_b64 v[56:57], 0\n\t" \
  "v_mov_b64 v[58:59], 0\n\t" \
  "v_mfma_f32_16x16x32_bf16 v[4:7], v[52:55], v[10:13], v[4:7]\n\t" \
  "s_cbranch_scc1 LLAST1_%=\n\t" \
  "v_rcp_f32 v74, v32\n\t" \
  "v_mov_b64 v[12:13], 0\n\t" \
  "v_mov_b32 v8, 0\n\t" \
  "v_mul_f32 v0, v0, v74\n\t" \
  "v_mul_f32 v1, v1, v74\n\t" \
  "v_mul_f32 v2, v2, v74\n\t" \
  "v_mul_f32 v3, v3, v74\n\t" \
  "v_cvt_pk_bf16_f32 v76, v0, v1\n\t" \
  "v_cvt_pk_bf16_f32 v77, v2, v3\n\t" \
  "v_pk_mul_f32 v[4:5], v[4:5], v[74:75] op_sel_hi:[1,0]\n\t" \
  "v_pk_mul_f32 v[6:7], v[6:7], v[74:75] op_sel_hi:[1,0]\n\t" \
  "v_cvt_pk_bf16_f32 v78, v4, v5\n\t" \
  "v_cvt_pk_bf16_f32 v79, v6, v7\n\t" \
  "ds_write_b128 v69, v[76:79] offset:5120\n\t" \
  CH_DP9 \
  "s_waitcnt lgkmcnt(2)\n\t" \
  "v_smfmac_f32_16x16x64_bf16 v[12:15], v[76:79], v[44:51], v68\n\t" \
  "ds_read_b128 v[44:47], v70 offset:26624\n\t" \
  "v_mov_b32 v11, 0\n\t" \
  "v_smfmac_f32_16x16x64_bf16 v[56:59], v[76:79], v[16:23], v68\n\t" \
  "ds_read_b128 v[48:51], v70 offset:27648\n\t" \
  "v_mov_b64 v[52:53], 0\n\t" \
  "v_smfmac_f32_16x16x64_bf16 v[8:11], v[76:79], v[36:43], v68\n\t" \
  "ds_read_b128 v[16:19], v70 offset:28672\n\t" \
  "v_smfmac_f32_16x16x64_bf16 v[52:55], v[76:79], v[24:31], v68\n\t" \
  "s_waitcnt lgkmcnt(3)\n\t" \
  "s_barrier\n\t" \
  CH_DP10 \
  "ds_read_b128 v[0:3], v69 offset:4096\n\t" \
  "ds_read_b128 v[4:7], v69 offset:7168\n\t" \
  "v_lshrrev_b32 v71, 7, v69\n\t" \
  "v_and_b32 v74, 0x70, v69\n\t" \
  "v_lshl_add_u32 v71, v71, 14, v74\n\t" \
  "v_bfe_u32 v72, v69, 2, 7\n\t" \
  "v_and_b32 v74, 0x200, v69\n\t" \
  "s_waitcnt lgkmcnt(1)\n\t" \
  "v_smfmac_f32_16x16x64_bf16 v[8:11], v[0:3], v[60:67], v68\n\t" \
  "buffer_load_dword v75, off, s[28:31], s80 sc1\n\t" \
  "ds_read_b128 v[20:23], v70 offset:29696\n\t" \
  "v_mov_b64 v[76:77], 0\n\t" \
  "v_smfmac_f32_16x16x64_bf16 v[12:15], v[0:3], v[200:207], v68\n\t" \
  "ds_read_b128 v[32:35], v69 offset:6144\n\t" \
  "v_mov_b64 v[78:79], 0\n\t" \
  "v_smfmac_f32_16x16x64_bf16 v[52:55], v[0:3], v[44:51], v68\n\t" \
  "ds_read_b128 v[24:27], v70 offset:30720\n\t" \
  "v_lshl_add_u32 v72, v74, 3, v72\n\t" \
  "v_smfmac_f32_16x16x64_bf16 v[56:59], v[0:3], v[232:239], v68\n\t" \
  "ds_read_b128 v[28:31], v70 offset:31744\n\t" \
  "s_waitcnt lgkmcnt(4)\n\t" \
  "v_smfmac_f32_16x16x64_bf16 v[12:15], v[4:7], v[208:215], v68\n\t" \
  "ds_read_b128 v[60:63], v70 offset:32768\n\t" \
  "s_waitcnt lgkmcnt(4)\n\t" \
  "v_smfmac_f32_16x16x64_bf16 v[8:11], v[4:7], v[16:23], v68\n\t" \
  "ds_read_b128 v[64:67], v70 offset:33792\n\t" \
  "s_waitcnt lgkmcnt(4)\n\t" \
  "v_smfmac_f32_16x16x64_bf16 v[8:11], v[32:35], v[192:199], v68\n\t" \
  "ds_read_b128 v[36:39], v73 offset:4096\n\t" \
  "v_smfmac_f32_16x16x64_bf16 v[12:15], v[32:35], v[216:223], v68\n\t" \
  "ds_read_b128 v[40:43], v73 offset:6144\n\t" \
  "s_waitcnt lgkmcnt(4)\n\t" \
  "v_smfmac_f32_16x16x64_bf16 v[52:55], v[4:7], v[24:31], v68\n\t" \
  "ds_read_b128 v[44:47], v70 offset:34816\n\t" \
  "v_cvt_pk_bf16_f32 v8, v8, v9\n\t" \
  "v_smfmac_f32_16x16x64_bf16 v[52:55], v[32:35], v[224:231], v68\n\t" \
  "ds_read_b128 v[48:51], v70 offset:35840\n\t" \
  "v_cvt_pk_bf16_f32 v9, v10, v11\n\t" \
  "v_smfmac_f32_16x16x64_bf16 v[56:59], v[4:7], v[240:247], v68\n\t" \
  "s_waitcnt lgkmcnt(4)\n\t" \
  "v_cvt_pk_bf16_f32 v10, v12, v13\n\t" \
  "v_smfmac_f32_16x16x64_bf16 v[56:59], v[32:35], v[60:67], v68\n\t" \
  "s_waitcnt lgkmcnt(3)\n\t" \
  "v_cvt_pk_bf16_f32 v11, v14, v15\n\t" \
  "v_smfmac_f32_16x16x64_bf16 v[76:79], v[36:39], v[248:255], v68\n\t" \
  "buffer_store_dwordx4 v[8:11], v71, s[28:31], s54 offen sc1\n\t" \
  "s_waitcnt lgkmcnt(0)\n\t" \
  CH_DP11 \
  "v_cvt_pk_bf16_f32 v52, v52, v53\n\t" \
  "v_smfmac_f32_16x16x64_bf16 v[76:79], v[40:43], v[44:51], v68\n\t" \
  "v_cvt_pk_bf16_f32 v53, v54, v55\n\t" \
  "v_cvt_pk_bf16_f32 v54, v56, v57\n\t" \
  "v_cvt_pk_bf16_f32 v55, v58, v59\n\t" \
  "buffer_store_dwordx4 v[52:55], v71, s[28:31], s75 offen sc1\n\t" \
  "s_nop 2\n\t" \
  "v_add_f32 v76, v76, v78\n\t" \
  "v_add_f32 v77, v77, v79\n\t" \
  "v_cvt_pk_bf16_f32 v76, v76, v77\n\t" \
  "buffer_store_dword v76, v72, s[28:31], s55 offen sc1\n\t" \
  CH_DP12 \
  "s_memrealtime s[70:71]\n\t" \
  "s_waitcnt vmcnt(3)\n\t" \
  "v_readfirstlane_b32 s65, v75\n\t" \
  "s_barrier\n\t" \
  "s_waitcnt lgkmcnt(0)\n\t" \
  "s_not_b32 s65, s65\n\t" \
  "s_lshl_b32 s61, s70, 8\n\t" \
  "s_andn2_b32 s64, s65, 0xff\n\t" \
  "s_sub_u32 s64, s64, s61\n\t" \
  "s_ashr_i32 s64, s64, 8\n\t" \
  "s_cmp_eq_u32 s65, 0\n\t" \
  "s_cselect_b32 s64, " CH_DLF ", s64\n\t" \
  "s_sub_i32 s64, s64, " CH_RO1 "\n\t" \
  "s_min_i32 s64, s64, s34\n\t" \
  "s_max_i32 s64, s64, " CH_DLL1 "\n\t" \
  "s_add_u32 s64, s64, s70\n\t" \
  CH_ST2 \
  "s_mov_b32 s74, 0\n\t" \
  "s_movk_i32 s60, 8192\n\t" \
  "RWT1_%=:\n\t" \
  "s_memrealtime s[72:73]\n\t" \
  "s_sub_u32 s60, s60, 1\n\t" \
  "s_cselect_b32 s61, 0, -1\n\t" \
  "s_waitcnt lgkmcnt(0)\n\t" \
  "s_sub_i32 s73, s72, s64\n\t" \
  "s_and_b32 s73, s73, s61\n\t" \
  "s_cmp_lt_i32 s73, " CH_FIN "\n\t" \
  "s_cbranch_scc1 RWT1_%=\n\t" \
  "s_cmp_lt_i32 s73, 0\n\t" \
  "s_cbranch_scc1 RWT1_%=f\n\t" \
  "RWT1_%=g:\n\t" \
  "RRETRY1_%=:\n\t" \
  CH_DP13 \
  "buffer_load_dwordx4 v[0:3], v69, s[28:31], s97 offen offset:0 sc1\n\t" \
  "buffer_load_dwordx4 v[4:7], v69, s[28:31], s97 offen offset:1024 sc1\n\t" \
  "buffer_load_dwordx4 v[8:11], v69, s[28:31], s97 offen offset:2048 sc1\n\t" \
  "buffer_load_dwordx4 v[12:15], v69, s[28:31], s97 offen offset:3072 sc1\n\t" \
  "v_lshrrev_b32 v20, 2, v69\n\t" \
  "v_and_b32 v20, 16, v20\n\t" \
  "v_mov_b32 v21, 0x3f80\n\t" \
  "v_lshlrev_b32 v21, v20, v21\n\t" \
  "v_bfe_u32 v20, v69, 4, 2\n\t" \
  "v_cmp_eq_u32 vcc, 0, v20\n\t" \
  "s_nop 1\n\t" \
  "v_cndmask_b32 v32, 0, v21, vcc\n\t" \
  "v_cmp_eq_u32 vcc, 1, v20\n\t" \
  "s_nop 1\n\t" \
  "v_cndmask_b32 v33, 0, v21, vcc\n\t" \
  "v_cmp_eq_u32 vcc, 2, v20\n\t" \
  "s_nop 1\n\t" \
  "v_cndmask_b32 v34, 0, v21, vcc\n\t" \
  "v_cmp_eq_u32 vcc, 3, v20\n\t" \
  "s_nop 1\n\t" \
  "v_cndmask_b32 v35, 0, v21, vcc\n\t" \
  "v_bfe_u32 v38, v69, 7, 1\n\t" \
  "v_bfe_u32 v20, v69, 9, 1\n\t" \
  "v_lshl_add_u32 v38, v20, 2, v38\n\t" \
  "v_lshlrev_b32 v38, 12, v38\n\t" \
  "v_and_b32 v20, 0x60, v69\n\t" \
  "v_add_u32 v38, v38, v20\n\t" \
  "v_bfe_u32 v20, v69, 8, 1\n\t" \
  "v_lshl_add_u32 v38, v20, 4, v38\n\t" \
  "v_bfe_u32 v20, v69, 4, 1\n\t" \
  "v_lshl_add_u32 v38, v20, 3, v38\n\t" \
  "v_mov_b32 v44, 0\n\t" \
  CH_DP17 \
  "s_waitcnt vmcnt(3)\n\t" \
  CH_DP14 \
  "v_mfma_f32_16x16x32_bf16 v[40:43], v[32:35], v[0:3], 0\n\t" \
  "v_max3_u32 v44, v44, v0, v1\n\t" \
  "v_max3_u32 v44, v44, v2, v3\n\t" \
  "s_waitcnt vmcnt(2)\n\t" \
  "v_mfma_f32_16x16x32_bf16 v[40:43], v[32:35], v[4:7], v[40:43]\n\t" \
  "v_max3_u32 v44, v44, v4, v5\n\t" \
  "v_max3_u32 v44, v44, v6, v7\n\t" \
  "s_waitcnt vmcnt(1)\n\t" \
  "v_mfma_f32_16x16x32_bf16 v[40:43], v[32:35], v[8:11], v[40:43]\n\t" \
  "v_max3_u32 v44, v44, v8, v9\n\t" \
  "v_max3_u32 v44, v44, v10, v11\n\t" \
  "s_waitcnt vmcnt(0)\n\t" \
  CH_DP15 \
  "v_mfma_f32_16x16x32_bf16 v[40:43], v[32:35], v[12:15], v[40:43]\n\t" \
  "v_max3_u32 v44, v44, v12, v13\n\t" \
  "v_max3_u32 v44, v44, v14, v15\n\t" \
  "v_cmp_eq_u32 vcc, -1, v44\n\t" \
  "s_cbranch_vccnz RFAIL1_%=\n\t" \
  "s_nop 3\n\t" \
  "v_add_f32_dpp v40, v40, v40 row_ror:8 row_mask:0xf bank_mask:0xf\n\t" \
  "v_add_f32_dpp v41, v41, v41 row_ror:8 row_mask:0xf bank_mask:0xf\n\t" \
  "v_add_f32_dpp v42, v42, v42 row_ror:8 row_mask:0xf bank_mask:0xf\n\t" \
  "v_add_f32_dpp v43, v43, v43 row_ror:8 row_mask:0xf bank_mask:0xf\n\t" \
  "v_cvt_pk_bf16_f32 v40, v40, v41\n\t" \
  "v_cvt_pk_bf16_f32 v41, v42, v43\n\t" \
  "buffer_store_dwordx2 v[40:41], v38, s[28:31], s98 offen sc1\n\t" \
  "RDONE1_%=:\n\t" \
  CH_DP16 \
  CH_ST4 \
  "s_barrier\n\t" \
  CH_ST3 \
  CH_TRC \
  "s_add_u32 s48, s48, 1\n\t" \
  "s_mov_b32 s49, s53\n\t" \
  "s_add_u32 s53, s53, " CH_SLOT "\n\t" \
  "s_add_u32 s60, s53, " CH_SKIP "\n\t" \
  "s_lshr_b32 s61, s96, 1\n\t" \
  "s_add_u32 s86, s86, 1\n\t" \
  "s_cmp_eq_u32 s86, 3\n\t" \
  "s_cselect_b32 s53, s60, s53\n\t" \
  "s_cselect_b32 s96, s61, s96\n\t" \
  "s_cselect_b32 s86, 0, s86\n\t" \
  "s_cmp_ge_u32 s53, " CH_RING "\n\t" \
  "s_cselect_b32 s53, 0, s53\n\t" \
  "s_cselect_b32 s96, s21, s96\n\t" \
  "s_andn2_b32 s53, s53, 0x2000\n\t" \
  "s_and_b32 s60, s96, 1\n\t" \
  "s_lshl_b32 s60, s60, 13\n\t" \
  "s_or_b32 s53, s53, s60\n\t" \
  "s_mov_b32 s68, 0\n\t" \
  "s_add_u32 s97, s41, s53\n\t" \
  "s_lshr_b32 s62, s97, 21\n\t" \
  "s_lshr_b32 s60, s22, s62\n\t" \
  "s_lshr_b32 s63, s97, 19\n\t" \
  "s_xor_b32 s60, s60, s63\n\t" \
  "s_and_b32 s60, s60, " CH_AFF "\n\t" \
  "s_lshr_b32 s61, s23, s62\n\t" \
  "s_lshr_b32 s63, s97, 20\n\t" \
  "s_xor_b32 s61, s61, s63\n\t" \
  "s_and_b32 s61, s61, " CH_AFG "\n\t" \
  "s_lshl_b32 s62, s60, 14\n\t" \
  "s_xor_b32 s97, s97, s62\n\t" \
  "s_lshl_b32 s62, s61, 12\n\t" \
  "s_xor_b32 s97, s97, s62\n\t" \
  "s_lshl_b32 s60, s60, 7\n\t" \
  "s_lshl_b32 s61, s61, 10\n\t" \
  "s_xor_b32 s98, s37, s60\n\t" \
  "s_xor_b32 s98, s98, s61\n\t" \
  "s_add_u32 s98, s98, s53\n\t" \
  "s_add_u32 s82, s48, 0\n\t" \
  CH_SEGC \
  "s_lshr_b64 s[60:61], s[90:91], s82\n\t" \
  "s_and_b32 s60, s60, 1\n\t" \
  "s_lshl_b32 s60, s60, 13\n\t" \
  "s_add_u32 s82, s82, " CH_SG0 "\n\t" \
  "s_lshl_b32 s82, s82, 21\n\t" \
  "s_or_b32 s82, s82, s60\n\t" \
  "s_add_u32 s50, s82, s38\n\t" \
  "s_add_u32 s69, s82, s39\n\t" \
  "s_cmp_eq_u32 s34, 0\n\t" \
  "s_cbranch_scc1 LLOAD_%=\n\t" \
  "s_branch LG1_%=\n\t" \
  "RFAIL1_%=:\n\t" \
  "s_add_u32 s74, s74, 1\n\t" \
  "s_sleep " CH_RSL "\n\t" \
  "s_waitcnt vmcnt(0)\n\t" \
  "s_cmp_gt_u32 s74, s84\n\t" \
  "s_cbranch_scc1 RG1_%=\n\t" \
  "s_and_b32 s60, s74, 1023\n\t" \
  "s_cmp_lg_u32 s60, 0\n\t" \
  "s_cbranch_scc1 RRETRY1_%=\n\t" \
  "buffer_load_dword v46, off, s[28:31], s92 sc1\n\t" \
  "s_waitcnt vmcnt(0)\n\t" \
  "v_readfirstlane_b32 s60, v46\n\t" \
  "s_nop 0\n\t" \
  "s_cmp_eq_u32 s60, 0\n\t" \
  "s_cbranch_scc1 RRETRY1_%=\n\t" \
  "RG1_%=:\n\t" \
  "s_mov_b32 s84, 0\n\t" \
  "s_lshl_b32 s60, s36, 4\n\t" \
  "s_or_b32 s60, s60, s48\n\t" \
  "s_or_b32 s60, s60, 536870912\n\t" \
  "v_mov_b32 v76, s60\n\t" \
  "v_mov_b32 v77, 0\n\t" \
  "s_mov_b64 exec, 1\n\t" \
  "buffer_atomic_cmpswap v[76:77], off, s[28:31], s92 sc1\n\t" \
  "s_mov_b64 exec, -1\n\t" \
  "s_branch RDONE1_%=\n\t" \
  "LFAIL1_%=:\n\t" \
  "v_max_u32 v75, v0, v1\n\t" \
  "v_max3_u32 v75, v75, v2, v3\n\t" \
  "v_max3_u32 v75, v75, v4, v5\n\t" \
  "v_max3_u32 v75, v75, v6, v7\n\t" \
  "v_max3_u32 v75, v75, v8, v9\n\t" \
  "v_max3_u32 v75, v75, v10, v11\n\t" \
  "v_max3_u32 v75, v75, v12, v13\n\t" \
  "v_max3_u32 v75, v75, v14, v15\n\t" \
  "v_max3_u32 v75, v75, v16, v17\n\t" \
  "v_max3_u32 v75, v75, v18, v19\n\t" \
  "v_max3_u32 v75, v75, v20, v21\n\t" \
  "v_max3_u32 v75, v75, v22, v23\n\t" \
  "v_max3_u32 v75, v75, v24, v25\n\t" \
  "v_max3_u32 v75, v75, v26, v27\n\t" \
  "v_max3_u32 v75, v75, v28, v29\n\t" \
  "v_max3_u32 v75, v75, v30, v31\n\t" \
  "v_max3_u32 v75, v75, v32, v33\n\t" \
  "v_max3_u32 v75, v75, v34, v35\n\t" \
  "v_cmp_eq_u32 vcc, -1, v75\n\t" \
  "s_cbranch_vccz LGO1_%=\n\t" \
  "s_add_u32 s68, s68, 1\n\t" \
  "s_waitcnt lgkmcnt(0)\n\t" \
  "s_cmp_gt_u32 s68, s83\n\t" \
  "s_cbranch_scc1 LGV1_%=\n\t" \
  "s_barrier\n\t" \
  "s_waitcnt vmcnt(8)\n\t" \
  "s_branch LG1_%=\n\t" \
  "RWT1_%=f:\n\t" \
  "s_cmp_lt_i32 s73, -2\n\t" \
  "s_cbranch_scc0 RWT1_%=g\n\t" \
  "s_nop 4\n\t" \
  "s_branch RWT1_%=g\n\t" \
  "LGV1_%=:\n\t" \
  "s_mov_b32 s83, 0\n\t" \
  "s_lshl_b32 s60, s36, 4\n\t" \
  "s_or_b32 s60, s60, s48\n\t" \
  "s_or_b32 s60, s60, 805306368\n\t" \
  "v_mov_b32 v76, s60\n\t" \
  "v_mov_b32 v77, 0\n\t" \
  "s_mov_b64 exec, 1\n\t" \
  "buffer_atomic_cmpswap v[76:77], off, s[28:31], s92 sc1\n\t" \
  "s_mov_b64 exec, -1\n\t" \
  "s_branch LGO1_%=\n\t" \
  "LLAST1_%=:\n\t" \
  "s_waitcnt vmcnt(0) lgkmcnt(0)\n\t" \
  "s_barrier\n\t" \
  "s_branch LEND_%=\n\t" \
  ".p2align 6\n\t" \
  "LG2_%=:\n\t" \
  "s_barrier\n\t" \
  "buffer_load_dwordx4 v[52:55], v69, s[28:31], s50 offen sc1\n\t" \
  "buffer_load_dwordx4 v[56:59], v69, s[28:31], s50 offen offset:1024 sc1\n\t" \
  "s_add_u32 s64, s50, 18432\n\t" \
  "s_movk_i32 m0, 11264\n\t" \
  "v_lshlrev_b32 v75, 1, v69\n\t" \
  "buffer_load_dword v75, s[28:31], s64 offen sc1 lds\n\t" \
  "ds_read_b128 v[44:47], v70 offset:0\n\t" \
  "ds_read_b128 v[48:51], v70 offset:1024\n\t" \
  "ds_read_b128 v[60:63], v70 offset:2048\n\t" \
  "ds_read_b128 v[64:67], v70 offset:3072\n\t" \
  "v_mov_b64 v[36:37], 0\n\t" \
  "v_mov_b64 v[38:39], 0\n\t" \
  "v_mov_b64 v[40:41], 0\n\t" \
  "v_mov_b64 v[42:43], 0\n\t" \
  "v_lshrrev_b32 v71, 1, v69\n\t" \
  "v_lshrrev_b32 v72, 3, v69\n\t" \
  "v_and_b32 v72, 0x78, v72\n\t" \
  "v_and_b32 v74, 0x30, v69\n\t" \
  "v_lshl_add_u32 v72, v74, 3, v72\n\t" \
  "s_add_u32 s54, s53, s36\n\t" \
  "s_add_u32 s55, s54, 557056\n\t" \
  "s_add_u32 s54, s54, 131072\n\t" \
  "s_add_u32 s75, s54, 262144\n\t" \
  "s_add_u32 s80, s53, s26\n\t" \
  "s_barrier\n\t" \
  CH_DP0 \
  "ds_read_b128 v[0:3], v69 offset:0\n\t" \
  "ds_read_b128 v[4:7], v69 offset:1024\n\t" \
  "ds_read_b128 v[8:11], v69 offset:2048\n\t" \
  "ds_read_b128 v[12:15], v69 offset:3072\n\t" \
  "s_waitcnt lgkmcnt(3)\n\t" \
  "v_smfmac_f32_16x16x64_bf16 v[36:39], v[0:3], v[44:51], v68\n\t" \
  "ds_read_b128 v[44:47], v70 offset:4096\n\t" \
  "v_smfmac_f32_16x16x64_bf16 v[40:43], v[0:3], v[60:67], v68\n\t" \
  "ds_read_b128 v[48:51], v70 offset:5120\n\t" \
  "s_waitcnt lgkmcnt(4)\n\t" \
  "v_smfmac_f32_16x16x64_bf16 v[36:39], v[4:7], a[0:7], v68\n\t" \
  "s_barrier\n\t" \
  "ds_read_b128 v[16:19], v69 offset:4096\n\t" \
  CH_DP1 \
  "v_smfmac_f32_16x16x64_bf16 v[40:43], v[4:7], a[64:71], v68\n\t" \
  "ds_read_b128 v[20:23], v69 offset:5120\n\t" \
  "s_waitcnt lgkmcnt(5)\n\t" \
  "v_smfmac_f32_16x16x64_bf16 v[36:39], v[8:11], a[8:15], v68\n\t" \
  "ds_read_b128 v[24:27], v69 offset:6144\n\t" \
  "v_smfmac_f32_16x16x64_bf16 v[40:43], v[8:11], a[72:79], v68\n\t" \
  "ds_read_b128 v[28:31], v69 offset:7168\n\t" \
  "s_waitcnt lgkmcnt(6)\n\t" \
  "v_smfmac_f32_16x16x64_bf16 v[36:39], v[12:15], a[16:23], v68\n\t" \
  "ds_read_b128 v[32:35], v69 offset:8192\n\t" \
  "s_waitcnt lgkmcnt(5)\n\t" \
  "v_smfmac_f32_16x16x64_bf16 v[40:43], v[12:15], v[44:51], v68\n\t" \
  "ds_read_b128 v[60:63], v70 offset:6144\n\t" \
  "s_waitcnt lgkmcnt(5)\n\t" \
  "v_smfmac_f32_16x16x64_bf16 v[36:39], v[16:19], a[24:31], v68\n\t" \
  "ds_read_b128 v[64:67], v70 offset:7168\n\t" \
  "v_smfmac_f32_16x16x64_bf16 v[40:43], v[16:19], a[80:87], v68\n\t" \
  "s_waitcnt lgkmcnt(5)\n\t" \
  "v_mov_b64 v[44:45], 0\n\t" \
  "v_smfmac_f32_16x16x64_bf16 v[36:39], v[20:23], a[32:39], v68\n\t" \
  "s_waitcnt lgkmcnt(4)\n\t" \
  "v_mov_b64 v[46:47], 0\n\t" \
  "v_smfmac_f32_16x16x64_bf16 v[36:39], v[24:27], a[40:47], v68\n\t" \
  "s_waitcnt lgkmcnt(2)\n\t" \
  "v_mov_b64 v[48:49], 0\n\t" \
  "v_smfmac_f32_16x16x64_bf16 v[36:39], v[28:31], a[48:55], v68\n\t" \
  "v_mov_b64 v[50:51], 0\n\t" \
  "v_smfmac_f32_16x16x64_bf16 v[36:39], v[32:35], a[56:63], v68\n\t" \
  "s_waitcnt lgkmcnt(0)\n\t" \
  "v_smfmac_f32_16x16x64_bf16 v[40:43], v[20:23], v[60:67], v68\n\t" \
  "ds_read_b128 v[60:63], v70 offset:8192\n\t" \
  "v_smfmac_f32_16x16x64_bf16 v[40:43], v[24:27], a[88:95], v68\n\t" \
  "ds_read_b128 v[64:67], v70 offset:9216\n\t" \
  "v_add_f32 v74, v36, v38\n\t" \
  "v_smfmac_f32_16x16x64_bf16 v[40:43], v[28:31], a[96:103], v68\n\t" \
  "v_add_f32 v37, v37, v39\n\t" \
  "v_smfmac_f32_16x16x64_bf16 v[40:43], v[32:35], a[104:111], v68\n\t" \
  "v_cmp_u_f32 vcc, v74, v37\n\t" \
  CH_DP2 \
  "v_smfmac_f32_16x16x64_bf16 v[44:47], v[0:3], a[112:119], v68\n\t" \
  "s_cbranch_vccnz LFAIL2_%=\n\t" \
  "LGO2_%=:\n\t" \
  "v_cvt_pk_bf16_f32 v74, v74, v37\n\t" \
  "v_smfmac_f32_16x16x64_bf16 v[44:47], v[4:7], a[120:127], v68\n\t" \
  "s_waitcnt lgkmcnt(0)\n\t" \
  "v_add_f32 v75, v40, v42\n\t" \
  "v_smfmac_f32_16x16x64_bf16 v[44:47], v[8:11], v[60:67], v68\n\t" \
  "ds_read_b128 v[60:63], v70 offset:10240\n\t" \
  "v_add_f32 v41, v41, v43\n\t" \
  "v_cvt_pk_bf16_f32 v75, v75, v41\n\t" \
  "v_smfmac_f32_16x16x64_bf16 v[44:47], v[12:15], a[128:135], v68\n\t" \
  "ds_write2st64_b32 v71, v74, v75 offset0:40 offset1:42\n\t" \
  "v_smfmac_f32_16x16x64_bf16 v[44:47], v[16:19], a[136:143], v68\n\t" \
  "ds_read_b128 v[64:67], v70 offset:11264\n\t" \
  "v_smfmac_f32_16x16x64_bf16 v[44:47], v[20:23], a[144:151], v68\n\t" \
  "v_smfmac_f32_16x16x64_bf16 v[44:47], v[24:27], a[152:159], v68\n\t" \
  "v_smfmac_f32_16x16x64_bf16 v[44:47], v[28:31], a[160:167], v68\n\t" \
  "s_waitcnt lgkmcnt(0)\n\t" \
  "v_smfmac_f32_16x16x64_bf16 v[44:47], v[32:35], v[60:67], v68\n\t" \
  "s_barrier\n\t" \
  "ds_read_b64_tr_b16 v[76:77], v72 offset:10240\n\t" \
  CH_DP3 \
  "v_smfmac_f32_16x16x64_bf16 v[48:51], v[0:3], a[168:175], v68\n\t" \
  "ds_read_b64_tr_b16 v[78:79], v72 offset:10752\n\t" \
  "v_smfmac_f32_16x16x64_bf16 v[48:51], v[4:7], a[176:183], v68\n\t" \
  "buffer_load_dwordx4 v[60:63], v69, s[28:31], s64 offen sc1\n\t" \
  "v_add_f32 v74, v44, v46\n\t" \
  "v_smfmac_f32_16x16x64_bf16 v[48:51], v[8:11], a[184:191], v68\n\t" \
  "v_add_f32 v45, v45, v47\n\t" \
  "v_smfmac_f32_16x16x64_bf16 v[48:51], v[12:15], a[192:199], v68\n\t" \
  "buffer_load_dwordx4 v[64:67], v69, s[28:31], s64 offen offset:1024 sc1\n\t" \
  "s_waitcnt vmcnt(4)\n\t" \
  "s_waitcnt lgkmcnt(0)\n\t" \
  "v_cvt_pk_bf16_f32 v74, v74, v45\n\t" \
  "v_mfma_f32_16x16x32_bf16 v[36:39], v[52:55], v[76:79], 0\n\t" \
  "s_waitcnt vmcnt(3)\n\t" \
  "v_mfma_f32_16x16x32_bf16 v[40:43], v[56:59], v[76:79], 0\n\t" \
  "v_mov_b64 v[52:53], 0\n\t" \
  "v_mov_b64 v[54:55], 0\n\t" \
  "v_smfmac_f32_16x16x64_bf16 v[48:51], v[16:19], a[200:207], v68\n\t" \
  "v_mov_b64 v[56:57], 0\n\t" \
  "v_mov_b64 v[58:59], 0\n\t" \
  "v_smfmac_f32_16x16x64_bf16 v[48:51], v[20:23], a[208:215], v68\n\t" \
  "ds_write_b32 v71, v74 offset:1024\n\t" \
  "v_add_f32_dpp v36, v40, v36 quad_perm:[2,3,0,1] row_mask:0xf bank_mask:0xf\n\t" \
  "v_smfmac_f32_16x16x64_bf16 v[48:51], v[24:27], a[216:223], v68\n\t" \
  "v_add_f32_dpp v37, v41, v37 quad_perm:[2,3,0,1] row_mask:0xf bank_mask:0xf\n\t" \
  "v_smfmac_f32_16x16x64_bf16 v[48:51], v[28:31], a[224:231], v68\n\t" \
  "v_add_f32_dpp v38, v42, v38 quad_perm:[2,3,0,1] row_mask:0xf bank_mask:0xf\n\t" \
  "v_smfmac_f32_16x16x64_bf16 v[48:51], v[32:35], a[232:239], v68\n\t" \
  "v_add_f32_dpp v39, v43, v39 quad_perm:[2,3,0,1] row_mask:0xf bank_mask:0xf\n\t" \
  CH_DP4 \
  "v_smfmac_f32_16x16x64_bf16 v[52:55], v[0:3], a[240:247], v68\n\t" \
  "s_mov_b64 exec, s[58:59]\n\t" \
  "ds_write_b128 v69, v[36:39] offset:3072\n\t" \
  "s_mov_b64 exec, -1\n\t" \
  "v_smfmac_f32_16x16x64_bf16 v[52:55], v[4:7], a[248:255], v68\n\t" \
  "v_add_f32 v75, v48, v50\n\t" \
  "v_add_f32 v49, v49, v51\n\t" \
  "v_cvt_pk_bf16_f32 v75, v75, v49\n\t" \
  "v_smfmac_f32_16x16x64_bf16 v[56:59], v[0:3], v[136:143], v68\n\t" \
  "ds_write_b32 v71, v75 offset:1536\n\t" \
  "v_smfmac_f32_16x16x64_bf16 v[56:59], v[4:7], v[144:151], v68\n\t" \
  "ds_read_b64_tr_b16 v[0:1], v72 offset:9216\n\t" \
  "ds_read_b64_tr_b16 v[2:3], v72 offset:9728\n\t" \
  "v_smfmac_f32_16x16x64_bf16 v[52:55], v[8:11], v[80:87], v68\n\t" \
  "ds_read_b128 v[36:39], v70 offset:12288\n\t" \
  "v_smfmac_f32_16x16x64_bf16 v[56:59], v[8:11], v[152:159], v68\n\t" \
  "ds_read_b128 v[40:43], v70 offset:13312\n\t" \
  "v_smfmac_f32_16x16x64_bf16 v[52:55], v[12:15], v[88:95], v68\n\t" \
  "s_waitcnt lgkmcnt(4)\n\t" \
  "s_barrier\n\t" \
  CH_DP5 \
  "ds_read_b64_tr_b16 v[44:45], v72 offset:1024\n\t" \
  "ds_read_b64_tr_b16 v[46:47], v72 offset:1536\n\t" \
  "v_smfmac_f32_16x16x64_bf16 v[56:59], v[12:15], v[160:167], v68\n\t" \
  "ds_read_b64_tr_b16 v[48:49], v72 offset:0\n\t" \
  "ds_read_b64_tr_b16 v[50:51], v72 offset:512\n\t" \
  "v_smfmac_f32_16x16x64_bf16 v[52:55], v[16:19], v[96:103], v68\n\t" \
  "ds_read_b128 v[4:7], v69 offset:2048\n\t" \
  "s_waitcnt lgkmcnt(5)\n\t" \
  "v_smfmac_f32_16x16x64_bf16 v[56:59], v[16:19], v[36:43], v68\n\t" \
  "ds_read_b128 v[8:11], v69 offset:3072\n\t" \
  "s_waitcnt lgkmcnt(4)\n\t" \
  "v_mfma_f32_16x16x32_bf16 v[12:15], v[44:47], v[76:79], 0\n\t" \
  "ds_read_b128 v[36:39], v70 offset:14336\n\t" \
  "s_waitcnt lgkmcnt(3)\n\t" \
  "v_mfma_f32_16x16x32_bf16 v[12:15], v[48:51], v[0:3], v[12:15]\n\t" \
  "ds_read_b128 v[40:43], v70 offset:15360\n\t" \
  "v_smfmac_f32_16x16x64_bf16 v[52:55], v[20:23], v[104:111], v68\n\t" \
  "buffer_store_dwordx4 v[44:47], v69, s[28:31], s69 offen offset:1024 sc1\n\t" \
  "s_waitcnt lgkmcnt(2)\n\t" \
  "v_add_f32 v4, v4, v8\n\t" \
  "v_add_f32 v5, v5, v9\n\t" \
  "v_add_f32 v6, v6, v10\n\t" \
  CH_DP6 \
  "v_cndmask_b32 v78, v13, v14, s[56:57]\n\t" \
  "v_cndmask_b32 v76, v12, v15, s[56:57]\n\t" \
  "v_add_f32 v7, v7, v11\n\t" \
  "v_max3_f32 v77, v4, v5, v6\n\t" \
  "v_add_f32_dpp v76, v78, v76 quad_perm:[2,3,0,1] row_mask:0xf bank_mask:0xf\n\t" \
  "v_max3_f32 v77, v77, v7, v76\n\t" \
  "v_smfmac_f32_16x16x64_bf16 v[56:59], v[20:23], v[168:175], v68\n\t" \
  "ds_read_b128 v[44:47], v70 offset:16384\n\t" \
  "v_max_f32_dpp v77, v77, v77 quad_perm:[2,3,0,1] row_mask:0xf bank_mask:0xf\n\t" \
  "v_mov_b32 v78, v77\n\t" \
  "v_smfmac_f32_16x16x64_bf16 v[52:55], v[24:27], v[112:119], v68\n\t" \
  "ds_read_b128 v[48:51], v70 offset:17408\n\t" \
  "v_permlane16_swap_b32 v77, v78\n\t" \
  "v_max_f32 v77, v77, v78\n\t" \
  "v_mov_b32 v78, v77\n\t" \
  "v_smfmac_f32_16x16x64_bf16 v[52:55], v[28:31], v[120:127], v68\n\t" \
  "ds_read_b128 v[16:19], v70 offset:18432\n\t" \
  "v_permlane32_swap_b32 v77, v78\n\t" \
  "v_max_f32 v77, v77, v78\n\t" \
  "v_smfmac_f32_16x16x64_bf16 v[52:55], v[32:35], v[128:135], v68\n\t" \
  "ds_read_b128 v[20:23], v70 offset:19456\n\t" \
  "v_mul_f32 v77, 0xbe0293ee, v77\n\t" \
  CH_DP7 \
  "v_smfmac_f32_16x16x64_bf16 v[56:59], v[24:27], v[176:183], v68\n\t" \
  "v_fmamk_f32 v4, v4, 0x3e0293ee, v77\n\t" \
  "v_smfmac_f32_16x16x64_bf16 v[56:59], v[28:31], v[184:191], v68\n\t" \
  "ds_read_b128 v[24:27], v70 offset:22528\n\t" \
  "s_waitcnt lgkmcnt(4)\n\t" \
  "v_fmamk_f32 v5, v5, 0x3e0293ee, v77\n\t" \
  "v_smfmac_f32_16x16x64_bf16 v[56:59], v[32:35], v[36:43], v68\n\t" \
  "ds_read_b128 v[28:31], v70 offset:23552\n\t" \
  "v_fmamk_f32 v6, v6, 0x3e0293ee, v77\n\t" \
  "v_fmamk_f32 v7, v7, 0x3e0293ee, v77\n\t" \
  "v_fmamk_f32 v76, v76, 0x3e0293ee, v77\n\t" \
  "v_exp_f32 v4, v4\n\t" \
  "v_exp_f32 v5, v5\n\t" \
  "v_exp_f32 v6, v6\n\t" \
  "v_exp_f32 v7, v7\n\t" \
  "v_cvt_pk_bf16_f32 v4, v4, v5\n\t" \
  "v_exp_f32 v76, v76\n\t" \
  "v_cvt_pk_bf16_f32 v5, v6, v7\n\t" \
  "v_mov_b32_dpp v12, v4 quad_perm:[0,1,0,1] row_mask:0xf bank_mask:0xf\n\t" \
  "v_mov_b32_dpp v14, v4 quad_perm:[2,3,2,3] row_mask:0xf bank_mask:0xf\n\t" \
  "v_mov_b32_dpp v13, v5 quad_perm:[0,1,0,1] row_mask:0xf bank_mask:0xf\n\t" \
  "v_mov_b32_dpp v15, v5 quad_perm:[2,3,2,3] row_mask:0xf bank_mask:0xf\n\t" \
  "v_add_f32 v52, v52, v54\n\t" \
  "v_add_f32 v53, v53, v55\n\t" \
  "s_waitcnt vmcnt(2)\n\t" \
  CH_DP8 \
  "v_mfma_f32_16x16x32_bf16 v[0:3], v[60:63], v[12:15], 0\n\t" \
  "ds_read_b128 v[36:39], v70 offset:20480\n\t" \
  "v_add_f32 v56, v56, v58\n\t" \
  "v_add_f32 v57, v57, v59\n\t" \
  "v_cvt_pk_bf16_f32 v52, v52, v53\n\t" \
  "v_cvt_pk_bf16_f32 v53, v56, v57\n\t" \
  "s_waitcnt vmcnt(1)\n\t" \
  "v_mfma_f32_16x16x32_bf16 v[4:7], v[64:67], v[12:15], 0\n\t" \
  "ds_read_b128 v[40:43], v70 offset:21504\n\t" \
  "v_mov_b64 v[56:57], s[88:89]\n\t" \
  "v_mov_b64 v[58:59], s[88:89]\n\t" \
  "v_mov_b32_dpp v78, v76 quad_perm:[0,1,0,1] row_mask:0xf bank_mask:0xf\n\t" \
  "v_mov_b32_dpp v79, v76 quad_perm:[2,3,2,3] row_mask:0xf bank_mask:0xf\n\t" \
  "v_mfma_f32_16x16x32_bf16 v[32:35], v[56:59], v[12:15], 0\n\t" \
  "ds_read_b128 v[60:63], v70 offset:24576\n\t" \
  "v_cvt_pk_bf16_f32 v11, v78, v79\n\t" \
  "v_cvt_pk_bf16_f32 v8, v78, v79\n\t" \
  "v_mov_b64 v[54:55], 0\n\t" \
  "v_mov_b32 v9, 0\n\t" \
  "v_mov_b32 v10, 0\n\t" \
  "v_mov_b64 v[14:15], 0\n\t" \
  "s_nop 0\n\t" \
  "v_mfma_f32_16x16x32_bf16 v[32:35], v[54:57], v[8:11], v[32:35]\n\t" \
  "buffer_store_dwordx2 v[52:53], v71, s[28:31], s69 offen offset:3072 sc1\n\t" \
  "s_cmp_eq_u32 s48, 63\n\t" \
  "v_mfma_f32_16x16x32_bf16 v[0:3], v[52:55], v[8:11], v[0:3]\n\t" \
  "ds_read_b128 v[64:67], v70 offset:25600\n\t" \
  "v_mov_b64 v[56:57], 0\n\t" \
  "v_mov_b64 v[58:59], 0\n\t" \
  "v_mfma_f32_16x16x32_bf16 v[4:7], v[52:55], v[10:13], v[4:7]\n\t" \
  "s_cbranch_scc1 LLAST2_%=\n\t" \
  "v_rcp_f32 v74, v32\n\t" \
  "v_mov_b64 v[12:13], 0\n\t" \
  "v_mov_b32 v8, 0\n\t" \
  "v_mul_f32 v0, v0, v74\n\t" \
  "v_mul_f32 v1, v1, v74\n\t" \
  "v_mul_f32 v2, v2, v74\n\t" \
  "v_mul_f32 v3, v3, v74\n\t" \
  "v_cvt_pk_bf16_f32 v76, v0, v1\n\t" \
  "v_cvt_pk_bf16_f32 v77, v2, v3\n\t" \
  "v_pk_mul_f32 v[4:5], v[4:5], v[74:75] op_sel_hi:[1,0]\n\t" \
  "v_pk_mul_f32 v[6:7], v[6:7], v[74:75] op_sel_hi:[1,0]\n\t" \
  "v_cvt_pk_bf16_f32 v78, v4, v5\n\t" \
  "v_cvt_pk_bf16_f32 v79, v6, v7\n\t" \
  "ds_write_b128 v69, v[76:79] offset:6144\n\t" \
  CH_DP9 \
  "s_waitcnt lgkmcnt(2)\n\t" \
  "v_smfmac_f32_16x16x64_bf16 v[12:15], v[76:79], v[44:51], v68\n\t" \
  "ds_read_b128 v[44:47], v70 offset:26624\n\t" \
  "v_mov_b32 v11, 0\n\t" \
  "v_smfmac_f32_16x16x64_bf16 v[56:59], v[76:79], v[16:23], v68\n\t" \
  "ds_read_b128 v[48:51], v70 offset:27648\n\t" \
  "v_mov_b64 v[52:53], 0\n\t" \
  "v_smfmac_f32_16x16x64_bf16 v[8:11], v[76:79], v[36:43], v68\n\t" \
  "ds_read_b128 v[16:19], v70 offset:28672\n\t" \
  "v_smfmac_f32_16x16x64_bf16 v[52:55], v[76:79], v[24:31], v68\n\t" \
  "s_waitcnt lgkmcnt(3)\n\t" \
  "s_barrier\n\t" \
  CH_DP10 \
  "ds_read_b128 v[0:3], v69 offset:7168\n\t" \
  "ds_read_b128 v[4:7], v69 offset:4096\n\t" \
  "v_lshrrev_b32 v71, 7, v69\n\t" \
  "v_and_b32 v74, 0x70, v69\n\t" \
  "v_lshl_add_u32 v71, v71, 14, v74\n\t" \
  "v_bfe_u32 v72, v69, 2, 7\n\t" \
  "v_and_b32 v74, 0x200, v69\n\t" \
  "s_waitcnt lgkmcnt(1)\n\t" \
  "v_smfmac_f32_16x16x64_bf16 v[8:11], v[0:3], v[60:67], v68\n\t" \
  "ds_read_b128 v[20:23], v70 offset:29696\n\t" \
  "v_mov_b64 v[76:77], 0\n\t" \
  "v_smfmac_f32_16x16x64_bf16 v[12:15], v[0:3], v[200:207], v68\n\t" \
  "buffer_load_dword v75, off, s[28:31], s80 sc1\n\t" \
  "ds_read_b128 v[32:35], v69 offset:5120\n\t" \
  "v_mov_b64 v[78:79], 0\n\t" \
  "v_smfmac_f32_16x16x64_bf16 v[52:55], v[0:3], v[44:51], v68\n\t" \
  "ds_read_b128 v[24:27], v70 offset:30720\n\t" \
  "v_lshl_add_u32 v72, v74, 3, v72\n\t" \
  "v_smfmac_f32_16x16x64_bf16 v[56:59], v[0:3], v[232:239], v68\n\t" \
  "ds_read_b128 v[28:31], v70 offset:31744\n\t" \
  "s_waitcnt lgkmcnt(4)\n\t" \
  "v_smfmac_f32_16x16x64_bf16 v[12:15], v[4:7], v[208:215], v68\n\t" \
  "ds_read_b128 v[60:63], v70 offset:32768\n\t" \
  "s_waitcnt lgkmcnt(4)\n\t" \
  "v_smfmac_f32_16x16x64_bf16 v[8:11], v[4:7], v[16:23], v68\n\t" \
  "ds_read_b128 v[64:67], v70 offset:33792\n\t" \
  "s_waitcnt lgkmcnt(4)\n\t" \
  "v_smfmac_f32_16x16x64_bf16 v[8:11], v[32:35], v[192:199], v68\n\t" \
  "ds_read_b128 v[36:39], v73 offset:4096\n\t" \
  "v_smfmac_f32_16x16x64_bf16 v[12:15], v[32:35], v[216:223], v68\n\t" \
  "ds_read_b128 v[40:43], v73 offset:6144\n\t" \
  "s_waitcnt lgkmcnt(4)\n\t" \
  "v_smfmac_f32_16x16x64_bf16 v[52:55], v[4:7], v[24:31], v68\n\t" \
  "ds_read_b128 v[44:47], v70 offset:34816\n\t" \
  "v_cvt_pk_bf16_f32 v8, v8, v9\n\t" \
  "v_smfmac_f32_16x16x64_bf16 v[52:55], v[32:35], v[224:231], v68\n\t" \
  "ds_read_b128 v[48:51], v70 offset:35840\n\t" \
  "v_cvt_pk_bf16_f32 v9, v10, v11\n\t" \
  "v_smfmac_f32_16x16x64_bf16 v[56:59], v[4:7], v[240:247], v68\n\t" \
  "s_waitcnt lgkmcnt(4)\n\t" \
  "v_cvt_pk_bf16_f32 v10, v12, v13\n\t" \
  "v_smfmac_f32_16x16x64_bf16 v[56:59], v[32:35], v[60:67], v68\n\t" \
  "s_waitcnt lgkmcnt(3)\n\t" \
  "v_cvt_pk_bf16_f32 v11, v14, v15\n\t" \
  "v_smfmac_f32_16x16x64_bf16 v[76:79], v[36:39], v[248:255], v68\n\t" \
  "buffer_store_dwordx4 v[8:11], v71, s[28:31], s54 offen sc1\n\t" \
  "s_waitcnt lgkmcnt(0)\n\t" \
  CH_DP11 \
  "v_cvt_pk_bf16_f32 v52, v52, v53\n\t" \
  "v_smfmac_f32_16x16x64_bf16 v[76:79], v[40:43], v[44:51], v68\n\t" \
  "v_cvt_pk_bf16_f32 v53, v54, v55\n\t" \
  "v_cvt_pk_bf16_f32 v54, v56, v57\n\t" \
  "v_cvt_pk_bf16_f32 v55, v58, v59\n\t" \
  "buffer_store_dwordx4 v[52:55], v71, s[28:31], s75 offen sc1\n\t" \
  "s_nop 2\n\t" \
  "v_add_f32 v76, v76, v78\n\t" \
  "v_add_f32 v77, v77, v79\n\t" \
  "v_cvt_pk_bf16_f32 v76, v76, v77\n\t" \
  "buffer_store_dword v76, v72, s[28:31], s55 offen sc1\n\t" \
  CH_DP12 \
  "s_memrealtime s[70:71]\n\t" \
  "s_waitcnt vmcnt(3)\n\t" \
  "v_readfirstlane_b32 s65, v75\n\t" \
  "s_barrier\n\t" \
  "s_waitcnt lgkmcnt(0)\n\t" \
  "s_not_b32 s65, s65\n\t" \
  "s_lshl_b32 s61, s70, 8\n\t" \
  "s_andn2_b32 s64, s65, 0xff\n\t" \
  "s_sub_u32 s64, s64, s61\n\t" \
  "s_ashr_i32 s64, s64, 8\n\t" \
  "s_cmp_eq_u32 s65, 0\n\t" \
  "s_cselect_b32 s64, " CH_DLF ", s64\n\t" \
  "s_min_i32 s64, s64, s34\n\t" \
  "s_max_i32 s64, s64, " CH_DLL1 "\n\t" \
  "s_add_u32 s64, s64, s70\n\t" \
  CH_ST2 \
  "s_mov_b32 s74, 0\n\t" \
  "s_movk_i32 s60, 8192\n\t" \
  "RWT2_%=:\n\t" \
  "s_memrealtime s[72:73]\n\t" \
  "s_sub_u32 s60, s60, 1\n\t" \
  "s_cselect_b32 s61, 0, -1\n\t" \
  "s_waitcnt lgkmcnt(0)\n\t" \
  "s_sub_i32 s73, s72, s64\n\t" \
  "s_and_b32 s73, s73, s61\n\t" \
  "s_cmp_lt_i32 s73, " CH_FIN "\n\t" \
  "s_cbranch_scc1 RWT2_%=\n\t" \
  "s_cmp_lt_i32 s73, 0\n\t" \
  "s_cbranch_scc1 RWT2_%=f\n\t" \
  "RWT2_%=g:\n\t" \
  "RRETRY2_%=:\n\t" \
  CH_DP13 \
  "buffer_load_dwordx4 v[0:3], v69, s[28:31], s97 offen offset:0 sc1\n\t" \
  "buffer_load_dwordx4 v[4:7], v69, s[28:31], s97 offen offset:1024 sc1\n\t" \
  "buffer_load_dwordx4 v[8:11], v69, s[28:31], s97 offen offset:2048 sc1\n\t" \
  "buffer_load_dwordx4 v[12:15], v69, s[28:31], s97 offen offset:3072 sc1\n\t" \
  "v_lshrrev_b32 v20, 2, v69\n\t" \
  "v_and_b32 v20, 16, v20\n\t" \
  "v_mov_b32 v21, 0x3f80\n\t" \
  "v_lshlrev_b32 v21, v20, v21\n\t" \
  "v_bfe_u32 v20, v69, 4, 2\n\t" \
  "v_cmp_eq_u32 vcc, 0, v20\n\t" \
  "s_nop 1\n\t" \
  "v_cndmask_b32 v32, 0, v21, vcc\n\t" \
  "v_cmp_eq_u32 vcc, 1, v20\n\t" \
  "s_nop 1\n\t" \
  "v_cndmask_b32 v33, 0, v21, vcc\n\t" \
  "v_cmp_eq_u32 vcc, 2, v20\n\t" \
  "s_nop 1\n\t" \
  "v_cndmask_b32 v34, 0, v21, vcc\n\t" \
  "v_cmp_eq_u32 vcc, 3, v20\n\t" \
  "s_nop 1\n\t" \
  "v_cndmask_b32 v35, 0, v21, vcc\n\t" \
  "v_bfe_u32 v38, v69, 7, 1\n\t" \
  "v_bfe_u32 v20, v69, 9, 1\n\t" \
  "v_lshl_add_u32 v38, v20, 2, v38\n\t" \
  "v_lshlrev_b32 v38, 12, v38\n\t" \
  "v_and_b32 v20, 0x60, v69\n\t" \
  "v_add_u32 v38, v38, v20\n\t" \
  "v_bfe_u32 v20, v69, 8, 1\n\t" \
  "v_lshl_add_u32 v38, v20, 4, v38\n\t" \
  "v_bfe_u32 v20, v69, 4, 1\n\t" \
  "v_lshl_add_u32 v38, v20, 3, v38\n\t" \
  "v_mov_b32 v44, 0\n\t" \
  CH_DP17 \
  "s_waitcnt vmcnt(3)\n\t" \
  CH_DP14 \
  "v_mfma_f32_16x16x32_bf16 v[40:43], v[32:35], v[0:3], 0\n\t" \
  "v_max3_u32 v44, v44, v0, v1\n\t" \
  "v_max3_u32 v44, v44, v2, v3\n\t" \
  "s_waitcnt vmcnt(2)\n\t" \
  "v_mfma_f32_16x16x32_bf16 v[40:43], v[32:35], v[4:7], v[40:43]\n\t" \
  "v_max3_u32 v44, v44, v4, v5\n\t" \
  "v_max3_u32 v44, v44, v6, v7\n\t" \
  "s_waitcnt vmcnt(1)\n\t" \
  "v_mfma_f32_16x16x32_bf16 v[40:43], v[32:35], v[8:11], v[40:43]\n\t" \
  "v_max3_u32 v44, v44, v8, v9\n\t" \
  "v_max3_u32 v44, v44, v10, v11\n\t" \
  "s_waitcnt vmcnt(0)\n\t" \
  CH_DP15 \
  "v_mfma_f32_16x16x32_bf16 v[40:43], v[32:35], v[12:15], v[40:43]\n\t" \
  "v_max3_u32 v44, v44, v12, v13\n\t" \
  "v_max3_u32 v44, v44, v14, v15\n\t" \
  "v_cmp_eq_u32 vcc, -1, v44\n\t" \
  "s_cbranch_vccnz RFAIL2_%=\n\t" \
  "s_nop 3\n\t" \
  "v_add_f32_dpp v40, v40, v40 row_ror:8 row_mask:0xf bank_mask:0xf\n\t" \
  "v_add_f32_dpp v41, v41, v41 row_ror:8 row_mask:0xf bank_mask:0xf\n\t" \
  "v_add_f32_dpp v42, v42, v42 row_ror:8 row_mask:0xf bank_mask:0xf\n\t" \
  "v_add_f32_dpp v43, v43, v43 row_ror:8 row_mask:0xf bank_mask:0xf\n\t" \
  "v_cvt_pk_bf16_f32 v40, v40, v41\n\t" \
  "v_cvt_pk_bf16_f32 v41, v42, v43\n\t" \
  "buffer_store_dwordx2 v[40:41], v38, s[28:31], s98 offen sc1\n\t" \
  "RDONE2_%=:\n\t" \
  CH_DP16 \
  CH_ST4 \
  "s_barrier\n\t" \
  CH_ST3 \
  CH_TRC \
  "s_add_u32 s48, s48, 1\n\t" \
  "s_mov_b32 s49, s53\n\t" \
  "s_add_u32 s53, s53, " CH_SLOT "\n\t" \
  "s_add_u32 s60, s53, " CH_SKIP "\n\t" \
  "s_lshr_b32 s61, s96, 1\n\t" \
  "s_add_u32 s86, s86, 1\n\t" \
  "s_cmp_eq_u32 s86, 3\n\t" \
  "s_cselect_b32 s53, s60, s53\n\t" \
  "s_cselect_b32 s96, s61, s96\n\t" \
  "s_cselect_b32 s86, 0, s86\n\t" \
  "s_cmp_ge_u32 s53, " CH_RING "\n\t" \
  "s_cselect_b32 s53, 0, s53\n\t" \
  "s_cselect_b32 s96, s21, s96\n\t" \
  "s_andn2_b32 s53, s53, 0x2000\n\t" \
  "s_and_b32 s60, s96, 1\n\t" \
  "s_lshl_b32 s60, s60, 13\n\t" \
  "s_or_b32 s53, s53, s60\n\t" \
  "s_mov_b32 s68, 0\n\t" \
  "s_add_u32 s97, s41, s53\n\t" \
  "s_lshr_b32 s62, s97, 21\n\t" \
  "s_lshr_b32 s60, s22, s62\n\t" \
  "s_lshr_b32 s63, s97, 19\n\t" \
  "s_xor_b32 s60, s60, s63\n\t" \
  "s_and_b32 s60, s60, " CH_AFF "\n\t" \
  "s_lshr_b32 s61, s23, s62\n\t" \
  "s_lshr_b32 s63, s97, 20\n\t" \
  "s_xor_b32 s61, s61, s63\n\t" \
  "s_and_b32 s61, s61, " CH_AFG "\n\t" \
  "s_lshl_b32 s62, s60, 14\n\t" \
  "s_xor_b32 s97, s97, s62\n\t" \
  "s_lshl_b32 s62, s61, 12\n\t" \
  "s_xor_b32 s97, s97, s62\n\t" \
  "s_lshl_b32 s60, s60, 7\n\t" \
  "s_lshl_b32 s61, s61, 10\n\t" \
  "s_xor_b32 s98, s37, s60\n\t" \
  "s_xor_b32 s98, s98, s61\n\t" \
  "s_add_u32 s98, s98, s53\n\t" \
  "s_add_u32 s82, s48, 0\n\t" \
  CH_SEGC \
  "s_lshr_b64 s[60:61], s[90:91], s82\n\t" \
  "s_and_b32 s60, s60, 1\n\t" \
  "s_lshl_b32 s60, s60, 13\n\t" \
  "s_add_u32 s82, s82, " CH_SG0 "\n\t" \
  "s_lshl_b32 s82, s82, 21\n\t" \
  "s_or_b32 s82, s82, s60\n\t" \
  "s_add_u32 s50, s82, s38\n\t" \
  "s_add_u32 s69, s82, s39\n\t" \
  "s_cmp_eq_u32 s34, 0\n\t" \
  "s_cbranch_scc1 LLOAD_%=\n\t" \
  "s_branch LG2_%=\n\t" \
  "RFAIL2_%=:\n\t" \
  "s_add_u32 s74, s74, 1\n\t" \
  "s_sleep " CH_RSL "\n\t" \
  "s_waitcnt vmcnt(0)\n\t" \
  "s_cmp_gt_u32 s74, s84\n\t" \
  "s_cbranch_scc1 RG2_%=\n\t" \
  "s_and_b32 s60, s74, 1023\n\t" \
  "s_cmp_lg_u32 s60, 0\n\t" \
  "s_cbranch_scc1 RRETRY2_%=\n\t" \
  "buffer_load_dword v46, off, s[28:31], s92 sc1\n\t" \
  "s_waitcnt vmcnt(0)\n\t" \
  "v_readfirstlane_b32 s60, v46\n\t" \
  "s_nop 0\n\t" \
  "s_cmp_eq_u32 s60, 0\n\t" \
  "s_cbranch_scc1 RRETRY2_%=\n\t" \
  "RG2_%=:\n\t" \
  "s_mov_b32 s84, 0\n\t" \
  "s_lshl_b32 s60, s36, 4\n\t" \
  "s_or_b32 s60, s60, s48\n\t" \
  "s_or_b32 s60, s60, 536870912\n\t" \
  "v_mov_b32 v76, s60\n\t" \
  "v_mov_b32 v77, 0\n\t" \
  "s_mov_b64 exec, 1\n\t" \
  "buffer_atomic_cmpswap v[76:77], off, s[28:31], s92 sc1\n\t" \
  "s_mov_b64 exec, -1\n\t" \
  "s_branch RDONE2_%=\n\t" \
  "LFAIL2_%=:\n\t" \
  "v_max_u32 v75, v0, v1\n\t" \
  "v_max3_u32 v75, v75, v2, v3\n\t" \
  "v_max3_u32 v75, v75, v4, v5\n\t" \
  "v_max3_u32 v75, v75, v6, v7\n\t" \
  "v_max3_u32 v75, v75, v8, v9\n\t" \
  "v_max3_u32 v75, v75, v10, v11\n\t" \
  "v_max3_u32 v75, v75, v12, v13\n\t" \
  "v_max3_u32 v75, v75, v14, v15\n\t" \
  "v_max3_u32 v75, v75, v16, v17\n\t" \
  "v_max3_u32 v75, v75, v18, v19\n\t" \
  "v_max3_u32 v75, v75, v20, v21\n\t" \
  "v_max3_u32 v75, v75, v22, v23\n\t" \
  "v_max3_u32 v75, v75, v24, v25\n\t" \
  "v_max3_u32 v75, v75, v26, v27\n\t" \
  "v_max3_u32 v75, v75, v28, v29\n\t" \
  "v_max3_u32 v75, v75, v30, v31\n\t" \
  "v_max3_u32 v75, v75, v32, v33\n\t" \
  "v_max3_u32 v75, v75, v34, v35\n\t" \
  "v_cmp_eq_u32 vcc, -1, v75\n\t" \
  "s_cbranch_vccz LGO2_%=\n\t" \
  "s_add_u32 s68, s68, 1\n\t" \
  "s_waitcnt lgkmcnt(0)\n\t" \
  "s_cmp_gt_u32 s68, s83\n\t" \
  "s_cbranch_scc1 LGV2_%=\n\t" \
  "s_barrier\n\t" \
  "s_waitcnt vmcnt(8)\n\t" \
  "s_branch LG2_%=\n\t" \
  "RWT2_%=f:\n\t" \
  "s_cmp_lt_i32 s73, -2\n\t" \
  "s_cbranch_scc0 RWT2_%=g\n\t" \
  "s_nop 4\n\t" \
  "s_branch RWT2_%=g\n\t" \
  "LGV2_%=:\n\t" \
  "s_mov_b32 s83, 0\n\t" \
  "s_lshl_b32 s60, s36, 4\n\t" \
  "s_or_b32 s60, s60, s48\n\t" \
  "s_or_b32 s60, s60, 805306368\n\t" \
  "v_mov_b32 v76, s60\n\t" \
  "v_mov_b32 v77, 0\n\t" \
  "s_mov_b64 exec, 1\n\t" \
  "buffer_atomic_cmpswap v[76:77], off, s[28:31], s92 sc1\n\t" \
  "s_mov_b64 exec, -1\n\t" \
  "s_branch LGO2_%=\n\t" \
  "LLAST2_%=:\n\t" \
  "s_waitcnt vmcnt(0) lgkmcnt(0)\n\t" \
  "s_barrier\n\t" \
  "s_branch LEND_%=\n\t" \
  ".p2align 6\n\t" \
  "LG3_%=:\n\t" \
  "s_barrier\n\t" \
  "buffer_load_dwordx4 v[52:55], v69, s[28:31], s50 offen sc1\n\t" \
  "buffer_load_dwordx4 v[56:59], v69, s[28:31], s50 offen offset:1024 sc1\n\t" \
  "s_add_u32 s64, s50, 16384\n\t" \
  "s_movk_i32 m0, 11264\n\t" \
  "v_lshlrev_b32 v75, 1, v69\n\t" \
  "buffer_load_dword v75, s[28:31], s64 offen sc1 lds\n\t" \
  "s_add_u32 s47, s81, " CH_DL3 "\n\t" \
  "s_lshl_b32 s47, s47, 8\n\t" \
  "s_or_b32 s47, s47, " CH_B0 "\n\t" \
  "s_not_b32 s47, s47\n\t" \
  "s_add_u32 s71, s53, s46\n\t" \
  "ds_read_b128 v[44:47], v70 offset:0\n\t" \
  "ds_read_b128 v[48:51], v70 offset:1024\n\t" \
  "ds_read_b128 v[60:63], v70 offset:2048\n\t" \
  "ds_read_b128 v[64:67], v70 offset:3072\n\t" \
  "v_mov_b64 v[36:37], 0\n\t" \
  "v_mov_b64 v[38:39], 0\n\t" \
  "v_mov_b64 v[40:41], 0\n\t" \
  "v_mov_b64 v[42:43], 0\n\t" \
  "v_lshrrev_b32 v71, 1, v69\n\t" \
  "v_add_u32 v71, 4, v71\n\t" \
  "v_lshrrev_b32 v72, 3, v69\n\t" \
  "v_and_b32 v72, 0x78, v72\n\t" \
  "v_and_b32 v74, 0x30, v69\n\t" \
  "v_lshl_add_u32 v72, v74, 3, v72\n\t" \
  "s_add_u32 s54, s53, s36\n\t" \
  "s_add_u32 s55, s54, 573440\n\t" \
  "s_add_u32 s54, s54, 135168\n\t" \
  "s_add_u32 s75, s54, 262144\n\t" \
  "s_add_u32 s80, s53, s26\n\t" \
  "s_or_b32 s87, s50, 0x1fff80\n\t" \
  "s_add_u32 s87, s87, 4194304\n\t" \
  CH_TC0 \
  "s_barrier\n\t" \
  CH_DP0 \
  "ds_read_b128 v[0:3], v69 offset:0\n\t" \
  "ds_read_b128 v[4:7], v69 offset:1024\n\t" \
  "ds_read_b128 v[8:11], v69 offset:2048\n\t" \
  "ds_read_b128 v[12:15], v69 offset:3072\n\t" \
  "s_waitcnt lgkmcnt(3)\n\t" \
  "v_smfmac_f32_16x16x64_bf16 v[36:39], v[0:3], v[44:51], v68\n\t" \
  "ds_read_b128 v[44:47], v70 offset:4096\n\t" \
  "v_smfmac_f32_16x16x64_bf16 v[40:43], v[0:3], v[60:67], v68\n\t" \
  "ds_read_b128 v[48:51], v70 offset:5120\n\t" \
  "s_waitcnt lgkmcnt(4)\n\t" \
  "v_smfmac_f32_16x16x64_bf16 v[36:39], v[4:7], a[0:7], v68\n\t" \
  "s_barrier\n\t" \
  "ds_read_b128 v[16:19], v69 offset:4096\n\t" \
  CH_DP1 \
  "v_smfmac_f32_16x16x64_bf16 v[40:43], v[4:7], a[64:71], v68\n\t" \
  "ds_read_b128 v[20:23], v69 offset:5120\n\t" \
  "s_waitcnt lgkmcnt(5)\n\t" \
  "v_smfmac_f32_16x16x64_bf16 v[36:39], v[8:11], a[8:15], v68\n\t" \
  "ds_read_b128 v[24:27], v69 offset:6144\n\t" \
  "v_smfmac_f32_16x16x64_bf16 v[40:43], v[8:11], a[72:79], v68\n\t" \
  "ds_read_b128 v[28:31], v69 offset:7168\n\t" \
  "s_waitcnt lgkmcnt(6)\n\t" \
  "v_smfmac_f32_16x16x64_bf16 v[36:39], v[12:15], a[16:23], v68\n\t" \
  "ds_read_b128 v[32:35], v69 offset:8192\n\t" \
  "s_waitcnt lgkmcnt(5)\n\t" \
  "v_smfmac_f32_16x16x64_bf16 v[40:43], v[12:15], v[44:51], v68\n\t" \
  "ds_read_b128 v[60:63], v70 offset:6144\n\t" \
  "s_waitcnt lgkmcnt(5)\n\t" \
  "v_smfmac_f32_16x16x64_bf16 v[36:39], v[16:19], a[24:31], v68\n\t" \
  "ds_read_b128 v[64:67], v70 offset:7168\n\t" \
  "v_smfmac_f32_16x16x64_bf16 v[40:43], v[16:19], a[80:87], v68\n\t" \
  "s_waitcnt lgkmcnt(5)\n\t" \
  "v_mov_b64 v[44:45], 0\n\t" \
  "v_smfmac_f32_16x16x64_bf16 v[36:39], v[20:23], a[32:39], v68\n\t" \
  "s_waitcnt lgkmcnt(4)\n\t" \
  "v_mov_b64 v[46:47], 0\n\t" \
  "v_smfmac_f32_16x16x64_bf16 v[36:39], v[24:27], a[40:47], v68\n\t" \
  "s_waitcnt lgkmcnt(2)\n\t" \
  "v_mov_b64 v[48:49], 0\n\t" \
  "v_smfmac_f32_16x16x64_bf16 v[36:39], v[28:31], a[48:55], v68\n\t" \
  "v_mov_b64 v[50:51], 0\n\t" \
  "v_smfmac_f32_16x16x64_bf16 v[36:39], v[32:35], a[56:63], v68\n\t" \
  "s_waitcnt lgkmcnt(0)\n\t" \
  "v_smfmac_f32_16x16x64_bf16 v[40:43], v[20:23], v[60:67], v68\n\t" \
  "ds_read_b128 v[60:63], v70 offset:8192\n\t" \
  "v_smfmac_f32_16x16x64_bf16 v[40:43], v[24:27], a[88:95], v68\n\t" \
  "ds_read_b128 v[64:67], v70 offset:9216\n\t" \
  "v_add_f32 v74, v36, v38\n\t" \
  "v_smfmac_f32_16x16x64_bf16 v[40:43], v[28:31], a[96:103], v68\n\t" \
  "v_add_f32 v37, v37, v39\n\t" \
  "v_smfmac_f32_16x16x64_bf16 v[40:43], v[32:35], a[104:111], v68\n\t" \
  "v_cmp_u_f32 vcc, v74, v37\n\t" \
  CH_DP2 \
  "v_smfmac_f32_16x16x64_bf16 v[44:47], v[0:3], a[112:119], v68\n\t" \
  "s_cbranch_vccnz LFAIL3_%=\n\t" \
  "LGO3_%=:\n\t" \
  "v_cvt_pk_bf16_f32 v74, v74, v37\n\t" \
  "v_smfmac_f32_16x16x64_bf16 v[44:47], v[4:7], a[120:127], v68\n\t" \
  "s_waitcnt lgkmcnt(0)\n\t" \
  "v_add_f32 v75, v40, v42\n\t" \
  "v_smfmac_f32_16x16x64_bf16 v[44:47], v[8:11], v[60:67], v68\n\t" \
  "ds_read_b128 v[60:63], v70 offset:10240\n\t" \
  "v_add_f32 v41, v41, v43\n\t" \
  "v_cvt_pk_bf16_f32 v75, v75, v41\n\t" \
  "v_smfmac_f32_16x16x64_bf16 v[44:47], v[12:15], a[128:135], v68\n\t" \
  "ds_write2st64_b32 v71, v74, v75 offset0:40 offset1:42\n\t" \
  "v_smfmac_f32_16x16x64_bf16 v[44:47], v[16:19], a[136:143], v68\n\t" \
  "ds_read_b128 v[64:67], v70 offset:11264\n\t" \
  "s_memrealtime s[66:67]\n\t" \
  "v_smfmac_f32_16x16x64_bf16 v[44:47], v[20:23], a[144:151], v68\n\t" \
  "v_smfmac_f32_16x16x64_bf16 v[44:47], v[24:27], a[152:159], v68\n\t" \
  "v_smfmac_f32_16x16x64_bf16 v[44:47], v[28:31], a[160:167], v68\n\t" \
  "s_waitcnt lgkmcnt(0)\n\t" \
  "v_smfmac_f32_16x16x64_bf16 v[44:47], v[32:35], v[60:67], v68\n\t" \
  "s_barrier\n\t" \
  "ds_read_b64_tr_b16 v[76:77], v72 offset:10240\n\t" \
  CH_DP3 \
  "v_smfmac_f32_16x16x64_bf16 v[48:51], v[0:3], a[168:175], v68\n\t" \
  "s_lshl_b32 s60, s66, 8\n\t" \
  "s_sub_u32 s60, s47, s60\n\t" \
  "ds_read_b64_tr_b16 v[78:79], v72 offset:10752\n\t" \
  "v_mov_b32 v75, s60\n\t" \
  "v_smfmac_f32_16x16x64_bf16 v[48:51], v[4:7], a[176:183], v68\n\t" \
  "s_mov_b64 exec, s[24:25]\n\t" \
  "buffer_atomic_umin v75, v69, s[28:31], s71 offen sc1\n\t" \
  "s_mov_b64 exec, -1\n\t" \
  "v_add_f32 v74, v44, v46\n\t" \
  "v_smfmac_f32_16x16x64_bf16 v[48:51], v[8:11], a[184:191], v68\n\t" \
  "buffer_load_dwordx4 v[60:63], v69, s[28:31], s64 offen sc1\n\t" \
  "v_add_f32 v45, v45, v47\n\t" \
  "v_smfmac_f32_16x16x64_bf16 v[48:51], v[12:15], a[192:199], v68\n\t" \
  "s_waitcnt vmcnt(4)\n\t" \
  "s_waitcnt lgkmcnt(0)\n\t" \
  "v_cvt_pk_bf16_f32 v74, v74, v45\n\t" \
  "v_mfma_f32_16x16x32_bf16 v[36:39], v[52:55], v[76:79], 0\n\t" \
  "s_waitcnt vmcnt(3)\n\t" \
  "v_mfma_f32_16x16x32_bf16 v[40:43], v[56:59], v[76:79], 0\n\t" \
  "v_mov_b64 v[52:53], 0\n\t" \
  "v_mov_b64 v[54:55], 0\n\t" \
  "v_smfmac_f32_16x16x64_bf16 v[48:51], v[16:19], a[200:207], v68\n\t" \
  "buffer_load_dwordx4 v[64:67], v69, s[28:31], s64 offen offset:1024 sc1\n\t" \
  "v_mov_b64 v[56:57], 0\n\t" \
  "v_mov_b64 v[58:59], 0\n\t" \
  "v_smfmac_f32_16x16x64_bf16 v[48:51], v[20:23], a[208:215], v68\n\t" \
  "ds_write_b32 v71, v74 offset:1024\n\t" \
  "v_add_f32_dpp v36, v40, v36 quad_perm:[2,3,0,1] row_mask:0xf bank_mask:0xf\n\t" \
  "v_smfmac_f32_16x16x64_bf16 v[48:51], v[24:27], a[216:223], v68\n\t" \
  "v_add_f32_dpp v37, v41, v37 quad_perm:[2,3,0,1] row_mask:0xf bank_mask:0xf\n\t" \
  "v_smfmac_f32_16x16x64_bf16 v[48:51], v[28:31], a[224:231], v68\n\t" \
  "v_add_f32_dpp v38, v42, v38 quad_perm:[2,3,0,1] row_mask:0xf bank_mask:0xf\n\t" \
  "v_smfmac_f32_16x16x64_bf16 v[48:51], v[32:35], a[232:239], v68\n\t" \
  "v_add_f32_dpp v39, v43, v39 quad_perm:[2,3,0,1] row_mask:0xf bank_mask:0xf\n\t" \
  CH_DP4 \
  "v_smfmac_f32_16x16x64_bf16 v[52:55], v[0:3], a[240:247], v68\n\t" \
  "s_mov_b64 exec, s[58:59]\n\t" \
  "ds_write_b128 v69, v[36:39] offset:3104\n\t" \
  "s_mov_b64 exec, -1\n\t" \
  "v_smfmac_f32_16x16x64_bf16 v[52:55], v[4:7], a[248:255], v68\n\t" \
  "buffer_load_dword off, s[28:31], s87 sc1 lds\n\t" \
  "v_add_f32 v75, v48, v50\n\t" \
  "v_add_f32 v49, v49, v51\n\t" \
  "v_cvt_pk_bf16_f32 v75, v75, v49\n\t" \
  "v_smfmac_f32_16x16x64_bf16 v[56:59], v[0:3], v[136:143], v68\n\t" \
  "ds_write_b32 v71, v75 offset:1536\n\t" \
  "v_smfmac_f32_16x16x64_bf16 v[56:59], v[4:7], v[144:151], v68\n\t" \
  "ds_read_b64_tr_b16 v[0:1], v72 offset:9216\n\t" \
  "ds_read_b64_tr_b16 v[2:3], v72 offset:9728\n\t" \
  "v_smfmac_f32_16x16x64_bf16 v[52:55], v[8:11], v[80:87], v68\n\t" \
  "ds_read_b128 v[36:39], v70 offset:12288\n\t" \
  "v_smfmac_f32_16x16x64_bf16 v[56:59], v[8:11], v[152:159], v68\n\t" \
  "ds_read_b128 v[40:43], v70 offset:13312\n\t" \
  "v_smfmac_f32_16x16x64_bf16 v[52:55], v[12:15], v[88:95], v68\n\t" \
  "s_waitcnt lgkmcnt(4)\n\t" \
  "s_barrier\n\t" \
  CH_DP5 \
  "ds_read_b64_tr_b16 v[44:45], v72 offset:1024\n\t" \
  "ds_read_b64_tr_b16 v[46:47], v72 offset:1536\n\t" \
  "v_smfmac_f32_16x16x64_bf16 v[56:59], v[12:15], v[160:167], v68\n\t" \
  "ds_read_b64_tr_b16 v[48:49], v72 offset:0\n\t" \
  "ds_read_b64_tr_b16 v[50:51], v72 offset:512\n\t" \
  "v_smfmac_f32_16x16x64_bf16 v[52:55], v[16:19], v[96:103], v68\n\t" \
  "ds_read_b128 v[4:7], v69 offset:2048\n\t" \
  "s_waitcnt lgkmcnt(5)\n\t" \
  "v_smfmac_f32_16x16x64_bf16 v[56:59], v[16:19], v[36:43], v68\n\t" \
  "ds_read_b128 v[8:11], v69 offset:3072\n\t" \
  "s_waitcnt lgkmcnt(4)\n\t" \
  "v_mfma_f32_16x16x32_bf16 v[12:15], v[44:47], v[76:79], 0\n\t" \
  "ds_read_b128 v[36:39], v70 offset:14336\n\t" \
  "s_waitcnt lgkmcnt(3)\n\t" \
  "v_mfma_f32_16x16x32_bf16 v[12:15], v[48:51], v[0:3], v[12:15]\n\t" \
  "ds_read_b128 v[40:43], v70 offset:15360\n\t" \
  "v_smfmac_f32_16x16x64_bf16 v[52:55], v[20:23], v[104:111], v68\n\t" \
  "s_waitcnt lgkmcnt(2)\n\t" \
  "v_add_f32 v4, v4, v8\n\t" \
  "v_add_f32 v5, v5, v9\n\t" \
  "v_add_f32 v6, v6, v10\n\t" \
  CH_DP6 \
  "v_cndmask_b32 v78, v13, v14, s[56:57]\n\t" \
  "v_cndmask_b32 v76, v12, v15, s[56:57]\n\t" \
  "v_add_f32 v7, v7, v11\n\t" \
  "v_max3_f32 v77, v4, v5, v6\n\t" \
  "v_add_f32_dpp v76, v78, v76 quad_perm:[2,3,0,1] row_mask:0xf bank_mask:0xf\n\t" \
  "v_max3_f32 v77, v77, v7, v76\n\t" \
  "v_smfmac_f32_16x16x64_bf16 v[56:59], v[20:23], v[168:175], v68\n\t" \
  "ds_read_b128 v[44:47], v70 offset:16384\n\t" \
  "v_max_f32_dpp v77, v77, v77 quad_perm:[2,3,0,1] row_mask:0xf bank_mask:0xf\n\t" \
  "v_mov_b32 v78, v77\n\t" \
  "v_smfmac_f32_16x16x64_bf16 v[52:55], v[24:27], v[112:119], v68\n\t" \
  "ds_read_b128 v[48:51], v70 offset:17408\n\t" \
  "v_permlane16_swap_b32 v77, v78\n\t" \
  "v_max_f32 v77, v77, v78\n\t" \
  "v_mov_b32 v78, v77\n\t" \
  "v_smfmac_f32_16x16x64_bf16 v[52:55], v[28:31], v[120:127], v68\n\t" \
  "ds_read_b128 v[16:19], v70 offset:18432\n\t" \
  "v_permlane32_swap_b32 v77, v78\n\t" \
  "v_max_f32 v77, v77, v78\n\t" \
  "v_smfmac_f32_16x16x64_bf16 v[52:55], v[32:35], v[128:135], v68\n\t" \
  "ds_read_b128 v[20:23], v70 offset:19456\n\t" \
  "v_mul_f32 v77, 0xbe0293ee, v77\n\t" \
  CH_DP7 \
  "v_smfmac_f32_16x16x64_bf16 v[56:59], v[24:27], v[176:183], v68\n\t" \
  "v_fmamk_f32 v4, v4, 0x3e0293ee, v77\n\t" \
  "v_smfmac_f32_16x16x64_bf16 v[56:59], v[28:31], v[184:191], v68\n\t" \
  "ds_read_b128 v[24:27], v70 offset:22528\n\t" \
  "s_waitcnt lgkmcnt(4)\n\t" \
  "v_fmamk_f32 v5, v5, 0x3e0293ee, v77\n\t" \
  "v_smfmac_f32_16x16x64_bf16 v[56:59], v[32:35], v[36:43], v68\n\t" \
  "ds_read_b128 v[28:31], v70 offset:23552\n\t" \
  "v_fmamk_f32 v6, v6, 0x3e0293ee, v77\n\t" \
  "v_fmamk_f32 v7, v7, 0x3e0293ee, v77\n\t" \
  "v_fmamk_f32 v76, v76, 0x3e0293ee, v77\n\t" \
  "v_exp_f32 v4, v4\n\t" \
  "v_exp_f32 v5, v5\n\t" \
  "v_exp_f32 v6, v6\n\t" \
  "v_exp_f32 v7, v7\n\t" \
  "v_cvt_pk_bf16_f32 v4, v4, v5\n\t" \
  "v_exp_f32 v76, v76\n\t" \
  "v_cvt_pk_bf16_f32 v5, v6, v7\n\t" \
  "v_mov_b32_dpp v12, v4 quad_perm:[0,1,0,1] row_mask:0xf bank_mask:0xf\n\t" \
  "v_mov_b32_dpp v14, v4 quad_perm:[2,3,2,3] row_mask:0xf bank_mask:0xf\n\t" \
  "v_mov_b32_dpp v13, v5 quad_perm:[0,1,0,1] row_mask:0xf bank_mask:0xf\n\t" \
  "v_mov_b32_dpp v15, v5 quad_perm:[2,3,2,3] row_mask:0xf bank_mask:0xf\n\t" \
  "v_add_f32 v52, v52, v54\n\t" \
  "v_add_f32 v53, v53, v55\n\t" \
  "s_waitcnt vmcnt(2)\n\t" \
  CH_DP8 \
  "v_mfma_f32_16x16x32_bf16 v[0:3], v[60:63], v[12:15], 0\n\t" \
  "ds_read_b128 v[36:39], v70 offset:20480\n\t" \
  "v_add_f32 v56, v56, v58\n\t" \
  "v_add_f32 v57, v57, v59\n\t" \
  "v_cvt_pk_bf16_f32 v52, v52, v53\n\t" \
  "v_cvt_pk_bf16_f32 v53, v56, v57\n\t" \
  "s_waitcnt vmcnt(1)\n\t" \
  "v_mfma_f32_16x16x32_bf16 v[4:7], v[64:67], v[12:15], 0\n\t" \
  "ds_read_b128 v[40:43], v70 offset:21504\n\t" \
  "v_mov_b64 v[56:57], s[88:89]\n\t" \
  "v_mov_b64 v[58:59], s[88:89]\n\t" \
  "v_mov_b32_dpp v78, v76 quad_perm:[0,1,0,1] row_mask:0xf bank_mask:0xf\n\t" \
  "v_mov_b32_dpp v79, v76 quad_perm:[2,3,2,3] row_mask:0xf bank_mask:0xf\n\t" \
  "v_mfma_f32_16x16x32_bf16 v[32:35], v[56:59], v[12:15], 0\n\t" \
  "ds_read_b128 v[60:63], v70 offset:24576\n\t" \
  "v_cvt_pk_bf16_f32 v11, v78, v79\n\t" \
  "v_cvt_pk_bf16_f32 v8, v78, v79\n\t" \
  "v_mov_b64 v[54:55], 0\n\t" \
  "v_mov_b32 v9, 0\n\t" \
  "v_mov_b32 v10, 0\n\t" \
  "v_mov_b64 v[14:15], 0\n\t" \
  "s_nop 0\n\t" \
  "v_mfma_f32_16x16x32_bf16 v[32:35], v[54:57], v[8:11], v[32:35]\n\t" \
  "s_cmp_eq_u32 s48, 63\n\t" \
  "v_mfma_f32_16x16x32_bf16 v[0:3], v[52:55], v[8:11], v[0:3]\n\t" \
  "buffer_store_dwordx2 v[52:53], v71, s[28:31], s69 offen offset:3580 sc1\n\t" \
  "ds_read_b128 v[64:67], v70 offset:25600\n\t" \
  "v_mov_b64 v[56:57], 0\n\t" \
  "v_mov_b64 v[58:59], 0\n\t" \
  "v_mfma_f32_16x16x32_bf16 v[4:7], v[52:55], v[10:13], v[4:7]\n\t" \
  "s_cbranch_scc1 LLAST3_%=\n\t" \
  "v_rcp_f32 v74, v32\n\t" \
  "v_mov_b64 v[12:13], 0\n\t" \
  "v_mov_b32 v8, 0\n\t" \
  "v_mul_f32 v0, v0, v74\n\t" \
  "v_mul_f32 v1, v1, v74\n\t" \
  "v_mul_f32 v2, v2, v74\n\t" \
  "v_mul_f32 v3, v3, v74\n\t" \
  "v_cvt_pk_bf16_f32 v76, v0, v1\n\t" \
  "v_cvt_pk_bf16_f32 v77, v2, v3\n\t" \
  "v_pk_mul_f32 v[4:5], v[4:5], v[74:75] op_sel_hi:[1,0]\n\t" \
  "v_pk_mul_f32 v[6:7], v[6:7], v[74:75] op_sel_hi:[1,0]\n\t" \
  "v_cvt_pk_bf16_f32 v78, v4, v5\n\t" \
  "v_cvt_pk_bf16_f32 v79, v6, v7\n\t" \
  "ds_write_b128 v69, v[76:79] offset:7168\n\t" \
  CH_DP9 \
  "s_waitcnt lgkmcnt(2)\n\t" \
  "v_smfmac_f32_16x16x64_bf16 v[12:15], v[76:79], v[44:51], v68\n\t" \
  "ds_read_b128 v[44:47], v70 offset:26624\n\t" \
  "v_mov_b32 v11, 0\n\t" \
  "v_smfmac_f32_16x16x64_bf16 v[56:59], v[76:79], v[16:23], v68\n\t" \
  "ds_read_b128 v[48:51], v70 offset:27648\n\t" \
  "v_mov_b64 v[52:53], 0\n\t" \
  "v_smfmac_f32_16x16x64_bf16 v[8:11], v[76:79], v[36:43], v68\n\t" \
  "ds_read_b128 v[16:19], v70 offset:28672\n\t" \
  "v_smfmac_f32_16x16x64_bf16 v[52:55], v[76:79], v[24:31], v68\n\t" \
  "s_waitcnt lgkmcnt(3)\n\t" \
  "s_barrier\n\t" \
  CH_DP10 \
  "ds_read_b128 v[0:3], v69 offset:6144\n\t" \
  "ds_read_b128 v[4:7], v69 offset:5120\n\t" \
  "v_lshrrev_b32 v71, 7, v69\n\t" \
  "v_and_b32 v74, 0x70, v69\n\t" \
  "v_lshl_add_u32 v71, v71, 14, v74\n\t" \
  "v_bfe_u32 v72, v69, 2, 7\n\t" \
  "v_and_b32 v74, 0x200, v69\n\t" \
  "s_waitcnt lgkmcnt(1)\n\t" \
  "v_smfmac_f32_16x16x64_bf16 v[8:11], v[0:3], v[60:67], v68\n\t" \
  "ds_read_b128 v[20:23], v70 offset:29696\n\t" \
  "v_mov_b64 v[76:77], 0\n\t" \
  "v_smfmac_f32_16x16x64_bf16 v[12:15], v[0:3], v[200:207], v68\n\t" \
  "ds_read_b128 v[32:35], v69 offset:4096\n\t" \
  "v_mov_b64 v[78:79], 0\n\t" \
  "v_smfmac_f32_16x16x64_bf16 v[52:55], v[0:3], v[44:51], v68\n\t" \
  "buffer_load_dword v75, off, s[28:31], s80 sc1\n\t" \
  "ds_read_b128 v[24:27], v70 offset:30720\n\t" \
  "v_lshl_add_u32 v72, v74, 3, v72\n\t" \
  "v_smfmac_f32_16x16x64_bf16 v[56:59], v[0:3], v[232:239], v68\n\t" \
  "ds_read_b128 v[28:31], v70 offset:31744\n\t" \
  "s_waitcnt lgkmcnt(4)\n\t" \
  "v_smfmac_f32_16x16x64_bf16 v[12:15], v[4:7], v[208:215], v68\n\t" \
  "ds_read_b128 v[60:63], v70 offset:32768\n\t" \
  "s_waitcnt lgkmcnt(4)\n\t" \
  "v_smfmac_f32_16x16x64_bf16 v[8:11], v[4:7], v[16:23], v68\n\t" \
  "ds_read_b128 v[64:67], v70 offset:33792\n\t" \
  "s_waitcnt lgkmcnt(4)\n\t" \
  "v_smfmac_f32_16x16x64_bf16 v[8:11], v[32:35], v[192:199], v68\n\t" \
  "ds_read_b128 v[36:39], v73 offset:4096\n\t" \
  "v_smfmac_f32_16x16x64_bf16 v[12:15], v[32:35], v[216:223], v68\n\t" \
  "ds_read_b128 v[40:43], v73 offset:6144\n\t" \
  "s_waitcnt lgkmcnt(4)\n\t" \
  "v_smfmac_f32_16x16x64_bf16 v[52:55], v[4:7], v[24:31], v68\n\t" \
  "ds_read_b128 v[44:47], v70 offset:34816\n\t" \
  "v_cvt_pk_bf16_f32 v8, v8, v9\n\t" \
  "v_smfmac_f32_16x16x64_bf16 v[52:55], v[32:35], v[224:231], v68\n\t" \
  "ds_read_b128 v[48:51], v70 offset:35840\n\t" \
  "v_cvt_pk_bf16_f32 v9, v10, v11\n\t" \
  "v_smfmac_f32_16x16x64_bf16 v[56:59], v[4:7], v[240:247], v68\n\t" \
  "s_waitcnt lgkmcnt(4)\n\t" \
  "v_cvt_pk_bf16_f32 v10, v12, v13\n\t" \
  "v_smfmac_f32_16x16x64_bf16 v[56:59], v[32:35], v[60:67], v68\n\t" \
  "s_waitcnt lgkmcnt(3)\n\t" \
  "v_cvt_pk_bf16_f32 v11, v14, v15\n\t" \
  "v_smfmac_f32_16x16x64_bf16 v[76:79], v[36:39], v[248:255], v68\n\t" \
  "buffer_store_dwordx4 v[8:11], v71, s[28:31], s54 offen sc1\n\t" \
  "s_waitcnt lgkmcnt(0)\n\t" \
  CH_DP11 \
  "v_cvt_pk_bf16_f32 v52, v52, v53\n\t" \
  "v_smfmac_f32_16x16x64_bf16 v[76:79], v[40:43], v[44:51], v68\n\t" \
  "v_cvt_pk_bf16_f32 v53, v54, v55\n\t" \
  "v_cvt_pk_bf16_f32 v54, v56, v57\n\t" \
  "v_cvt_pk_bf16_f32 v55, v58, v59\n\t" \
  "buffer_store_dwordx4 v[52:55], v71, s[28:31], s75 offen sc1\n\t" \
  "s_nop 2\n\t" \
  "v_add_f32 v76, v76, v78\n\t" \
  "v_add_f32 v77, v77, v79\n\t" \
  "v_cvt_pk_bf16_f32 v76, v76, v77\n\t" \
  "buffer_store_dword v76, v72, s[28:31], s55 offen sc1\n\t" \
  CH_DP12 \
  "s_memrealtime s[70:71]\n\t" \
  "s_waitcnt vmcnt(3)\n\t" \
  "v_readfirstlane_b32 s65, v75\n\t" \
  "s_barrier\n\t" \
  "s_waitcnt lgkmcnt(0)\n\t" \
  "s_not_b32 s65, s65\n\t" \
  "s_lshl_b32 s61, s70, 8\n\t" \
  "s_andn2_b32 s64, s65, 0xff\n\t" \
  "s_sub_u32 s64, s64, s61\n\t" \
  "s_ashr_i32 s64, s64, 8\n\t" \
  "s_cmp_eq_u32 s65, 0\n\t" \
  "s_cselect_b32 s64, " CH_DLF ", s64\n\t" \
  "s_min_i32 s64, s64, s34\n\t" \
  "s_max_i32 s64, s64, " CH_DLL1 "\n\t" \
  "s_add_u32 s64, s64, s70\n\t" \
  CH_ST2 \
  "s_mov_b32 s74, 0\n\t" \
  "v_and_b32 v37, 0x30, v69\n\t" \
  "v_add_u32 v37, s52, v37\n\t" \
  "s_mov_b32 exec_lo, 0\n\t" \
  "v_mov_b32 v37, s85\n\t" \
  "s_mov_b32 exec_lo, -1\n\t" \
  "s_cmp_lt_i32 s41, 0\n\t" \
  "s_cbranch_scc1 LIDLE_%=\n\t" \
  "s_movk_i32 s60, 8192\n\t" \
  "RWT3_%=:\n\t" \
  "s_memrealtime s[72:73]\n\t" \
  "s_sub_u32 s60, s60, 1\n\t" \
  "s_cselect_b32 s61, 0, -1\n\t" \
  "s_waitcnt lgkmcnt(0)\n\t" \
  "s_sub_i32 s73, s72, s64\n\t" \
  "s_and_b32 s73, s73, s61\n\t" \
  "s_cmp_lt_i32 s73, " CH_FIN "\n\t" \
  "s_cbranch_scc1 RWT3_%=\n\t" \
  "s_cmp_lt_i32 s73, 0\n\t" \
  "s_cbranch_scc1 RWT3_%=f\n\t" \
  "RWT3_%=g:\n\t" \
  "RRETRY3_%=:\n\t" \
  CH_DP13 \
  "buffer_load_dwordx4 v[0:3], v69, s[28:31], s97 offen offset:0 sc1\n\t" \
  "buffer_load_dwordx4 v[4:7], v69, s[28:31], s97 offen offset:1024 sc1\n\t" \
  "buffer_load_dwordx4 v[8:11], v69, s[28:31], s97 offen offset:2048 sc1\n\t" \
  "buffer_load_dwordx4 v[12:15], v69, s[28:31], s97 offen offset:3072 sc1\n\t" \
  "v_lshrrev_b32 v20, 2, v69\n\t" \
  "v_and_b32 v20, 16, v20\n\t" \
  "v_mov_b32 v21, 0x3f80\n\t" \
  "v_lshlrev_b32 v21, v20, v21\n\t" \
  "v_bfe_u32 v20, v69, 4, 2\n\t" \
  "v_cmp_eq_u32 vcc, 0, v20\n\t" \
  "s_nop 1\n\t" \
  "v_cndmask_b32 v32, 0, v21, vcc\n\t" \
  "v_cmp_eq_u32 vcc, 1, v20\n\t" \
  "s_nop 1\n\t" \
  "v_cndmask_b32 v33, 0, v21, vcc\n\t" \
  "v_cmp_eq_u32 vcc, 2, v20\n\t" \
  "s_nop 1\n\t" \
  "v_cndmask_b32 v34, 0, v21, vcc\n\t" \
  "v_cmp_eq_u32 vcc, 3, v20\n\t" \
  "s_nop 1\n\t" \
  "v_cndmask_b32 v35, 0, v21, vcc\n\t" \
  "v_bfe_u32 v38, v69, 7, 1\n\t" \
  "v_bfe_u32 v20, v69, 9, 1\n\t" \
  "v_lshl_add_u32 v38, v20, 2, v38\n\t" \
  "v_lshlrev_b32 v38, 12, v38\n\t" \
  "v_and_b32 v20, 0x60, v69\n\t" \
  "v_add_u32 v38, v38, v20\n\t" \
  "v_bfe_u32 v20, v69, 8, 1\n\t" \
  "v_lshl_add_u32 v38, v20, 4, v38\n\t" \
  "v_bfe_u32 v20, v69, 4, 1\n\t" \
  "v_lshl_add_u32 v38, v20, 3, v38\n\t" \
  "v_mov_b32 v44, 0\n\t" \
  "buffer_load_dword v39, v37, s[28:31], 0 offen sc1\n\t" \
  CH_DP17 \
  "s_waitcnt vmcnt(4)\n\t" \
  CH_DP14 \
  "v_mfma_f32_16x16x32_bf16 v[40:43], v[32:35], v[0:3], 0\n\t" \
  "v_max3_u32 v44, v44, v0, v1\n\t" \
  "v_max3_u32 v44, v44, v2, v3\n\t" \
  "s_waitcnt vmcnt(3)\n\t" \
  "v_mfma_f32_16x16x32_bf16 v[40:43], v[32:35], v[4:7], v[40:43]\n\t" \
  "v_max3_u32 v44, v44, v4, v5\n\t" \
  "v_max3_u32 v44, v44, v6, v7\n\t" \
  "s_waitcnt vmcnt(2)\n\t" \
  "v_mfma_f32_16x16x32_bf16 v[40:43], v[32:35], v[8:11], v[40:43]\n\t" \
  "v_max3_u32 v44, v44, v8, v9\n\t" \
  "v_max3_u32 v44, v44, v10, v11\n\t" \
  "s_waitcnt vmcnt(1)\n\t" \
  CH_DP15 \
  "v_mfma_f32_16x16x32_bf16 v[40:43], v[32:35], v[12:15], v[40:43]\n\t" \
  "v_max3_u32 v44, v44, v12, v13\n\t" \
  "v_max3_u32 v44, v44, v14, v15\n\t" \
  "v_cmp_eq_u32 vcc, -1, v44\n\t" \
  "s_cbranch_vccnz RFAIL3_%=\n\t" \
  "s_nop 3\n\t" \
  "v_add_f32_dpp v40, v40, v40 row_ror:8 row_mask:0xf bank_mask:0xf\n\t" \
  "v_add_f32_dpp v41, v41, v41 row_ror:8 row_mask:0xf bank_mask:0xf\n\t" \
  "v_add_f32_dpp v42, v42, v42 row_ror:8 row_mask:0xf bank_mask:0xf\n\t" \
  "v_add_f32_dpp v43, v43, v43 row_ror:8 row_mask:0xf bank_mask:0xf\n\t" \
  "v_cvt_pk_bf16_f32 v40, v40, v41\n\t" \
  "v_cvt_pk_bf16_f32 v41, v42, v43\n\t" \
  "buffer_store_dwordx2 v[40:41], v38, s[28:31], s98 offen sc1\n\t" \
  "RDONE3_%=:\n\t" \
  CH_DP16 \
  CH_ST4 \
  "s_barrier\n\t" \
  CH_ST3 \
  "s_waitcnt vmcnt(1)\n\t" \
  "v_cmp_eq_u32 vcc, 0, v39\n\t" \
  "s_sub_u32 s60, s70, s66\n\t" \
  "s_add_u32 s81, s81, 1\n\t" \
  "s_min_u32 s81, s81, s60\n\t" \
  CH_TRC \
  "s_add_u32 s48, s48, 1\n\t" \
  "s_mov_b32 s49, s53\n\t" \
  "s_add_u32 s53, s53, " CH_SLOT "\n\t" \
  "s_add_u32 s60, s53, " CH_SKIP "\n\t" \
  "s_lshr_b32 s61, s96, 1\n\t" \
  "s_add_u32 s86, s86, 1\n\t" \
  "s_cmp_eq_u32 s86, 3\n\t" \
  "s_cselect_b32 s53, s60, s53\n\t" \
  "s_cselect_b32 s96, s61, s96\n\t" \
  "s_cselect_b32 s86, 0, s86\n\t" \
  "s_cmp_ge_u32 s53, " CH_RING "\n\t" \
  "s_cselect_b32 s53, 0, s53\n\t" \
  "s_cselect_b32 s96, s21, s96\n\t" \
  "s_andn2_b32 s53, s53, 0x2000\n\t" \
  "s_and_b32 s60, s96, 1\n\t" \
  "s_lshl_b32 s60, s60, 13\n\t" \
  "s_or_b32 s53, s53, s60\n\t" \
  "s_mov_b32 s68, 0\n\t" \
  "s_add_u32 s97, s41, s53\n\t" \
  "s_lshr_b32 s62, s97, 21\n\t" \
  "s_lshr_b32 s60, s22, s62\n\t" \
  "s_lshr_b32 s63, s97, 19\n\t" \
  "s_xor_b32 s60, s60, s63\n\t" \
  "s_and_b32 s60, s60, " CH_AFF "\n\t" \
  "s_lshr_b32 s61, s23, s62\n\t" \
  "s_lshr_b32 s63, s97, 20\n\t" \
  "s_xor_b32 s61, s61, s63\n\t" \
  "s_and_b32 s61, s61, " CH_AFG "\n\t" \
  "s_lshl_b32 s62, s60, 14\n\t" \
  "s_xor_b32 s97, s97, s62\n\t" \
  "s_lshl_b32 s62, s61, 12\n\t" \
  "s_xor_b32 s97, s97, s62\n\t" \
  "s_lshl_b32 s60, s60, 8\n\t" \
  "s_lshl_b32 s61, s61, 7\n\t" \
  "s_xor_b32 s98, s37, s60\n\t" \
  "s_xor_b32 s98, s98, s61\n\t" \
  "s_add_u32 s98, s98, s53\n\t" \
  "s_add_u32 s82, s48, 0\n\t" \
  CH_SEGC \
  "s_lshr_b64 s[60:61], s[90:91], s82\n\t" \
  "s_and_b32 s60, s60, 1\n\t" \
  "s_lshl_b32 s60, s60, 13\n\t" \
  "s_add_u32 s82, s82, " CH_SG0 "\n\t" \
  "s_lshl_b32 s82, s82, 21\n\t" \
  "s_or_b32 s82, s82, s60\n\t" \
  "s_add_u32 s50, s82, s38\n\t" \
  "s_add_u32 s69, s82, s39\n\t" \
  "s_add_u32 s82, s48, 1\n\t" \
  CH_SEGC \
  "s_lshr_b64 s[60:61], s[90:91], s82\n\t" \
  "s_and_b32 s60, s60, 1\n\t" \
  "s_lshl_b32 s60, s60, 13\n\t" \
  "s_add_u32 s82, s82, " CH_SG0 "\n\t" \
  "s_lshl_b32 s82, s82, 21\n\t" \
  "s_or_b32 s82, s82, s60\n\t" \
  "s_add_u32 s52, s82, s43\n\t" \
  "s_add_u32 s82, s48, " CH_ARMD "\n\t" \
  CH_SEGC \
  "s_lshr_b64 s[60:61], s[90:91], s82\n\t" \
  "s_and_b32 s60, s60, 1\n\t" \
  "s_lshl_b32 s60, s60, 13\n\t" \
  "s_add_u32 s82, s82, " CH_SG0 "\n\t" \
  "s_lshl_b32 s82, s82, 21\n\t" \
  "s_or_b32 s82, s82, s60\n\t" \
  "s_add_u32 s85, s82, s45\n\t" \
  "s_cmp_lt_i32 s48, " CH_ARMS "\n\t" \
  "s_cselect_b32 s85, s27, s85\n\t" \
  "s_cmp_eq_u32 s34, 0\n\t" \
  "s_cbranch_scc1 LLOAD_%=\n\t" \
  "LTK_%=:\n\t" \
  CH_TOKCHK \
  "s_mov_b32 s61, 0\n\t" \
  "LTW_%=:\n\t" \
  "buffer_load_dword v1, v37, s[28:31], 0 offen sc1\n\t" \
  "s_waitcnt vmcnt(0)\n\t" \
  "v_cmp_eq_u32 vcc, 0, v1\n\t" \
  "s_cbranch_vccz LG3_%=\n\t" \
  "s_add_u32 s61, s61, 1\n\t" \
  "s_sleep 2\n\t" \
  "s_cmp_gt_u32 s61, s84\n\t" \
  "s_cbranch_scc1 LTG_%=\n\t" \
  "s_and_b32 s60, s61, 1023\n\t" \
  "s_cmp_lg_u32 s60, 0\n\t" \
  "s_cbranch_scc1 LTW_%=\n\t" \
  "buffer_load_dword v46, off, s[28:31], s92 sc1\n\t" \
  "s_waitcnt vmcnt(0)\n\t" \
  "v_readfirstlane_b32 s60, v46\n\t" \
  "s_nop 0\n\t" \
  "s_cmp_eq_u32 s60, 0\n\t" \
  "s_cbranch_scc1 LTW_%=\n\t" \
  "LTG_%=:\n\t" \
  "s_mov_b32 s84, 0\n\t" \
  "s_lshl_b32 s60, s36, 4\n\t" \
  "s_or_b32 s60, s60, s48\n\t" \
  "s_or_b32 s60, s60, 268435456\n\t" \
  "v_mov_b32 v76, s60\n\t" \
  "v_mov_b32 v77, 0\n\t" \
  "s_mov_b64 exec, 1\n\t" \
  "buffer_atomic_cmpswap v[76:77], off, s[28:31], s92 sc1\n\t" \
  "s_mov_b64 exec, -1\n\t" \
  "s_branch LG3_%=\n\t" \
  "LIDLE_%=:\n\t" \
  "s_add_u32 s64, s64, 8\n\t" \
  "s_movk_i32 s60, 8192\n\t" \
  "IWT_%=:\n\t" \
  "s_memrealtime s[72:73]\n\t" \
  "s_sub_u32 s60, s60, 1\n\t" \
  "s_cselect_b32 s61, 0, -1\n\t" \
  "s_waitcnt lgkmcnt(0)\n\t" \
  "s_sub_i32 s73, s72, s64\n\t" \
  "s_and_b32 s73, s73, s61\n\t" \
  "s_cmp_lt_i32 s73, 0\n\t" \
  "s_cbranch_scc1 IWT_%=\n\t" \
  "buffer_load_dword v39, v37, s[28:31], 0 offen sc1\n\t" \
  "buffer_store_dword v37, off, s[28:31], s41\n\t" \
  "s_branch RDONE3_%=\n\t" \
  "RFAIL3_%=:\n\t" \
  "s_add_u32 s74, s74, 1\n\t" \
  "s_sleep " CH_RSL "\n\t" \
  "s_waitcnt vmcnt(0)\n\t" \
  "s_cmp_gt_u32 s74, s84\n\t" \
  "s_cbranch_scc1 RG3_%=\n\t" \
  "s_and_b32 s60, s74, 1023\n\t" \
  "s_cmp_lg_u32 s60, 0\n\t" \
  "s_cbranch_scc1 RRETRY3_%=\n\t" \
  "buffer_load_dword v46, off, s[28:31], s92 sc1\n\t" \
  "s_waitcnt vmcnt(0)\n\t" \
  "v_readfirstlane_b32 s60, v46\n\t" \
  "s_nop 0\n\t" \
  "s_cmp_eq_u32 s60, 0\n\t" \
  "s_cbranch_scc1 RRETRY3_%=\n\t" \
  "RG3_%=:\n\t" \
  "s_mov_b32 s84, 0\n\t" \
  "s_lshl_b32 s60, s36, 4\n\t" \
  "s_or_b32 s60, s60, s48\n\t" \
  "s_or_b32 s60, s60, 536870912\n\t" \
  "v_mov_b32 v76, s60\n\t" \
  "v_mov_b32 v77, 0\n\t" \
  "s_mov_b64 exec, 1\n\t" \
  "buffer_atomic_cmpswap v[76:77], off, s[28:31], s92 sc1\n\t" \
  "s_mov_b64 exec, -1\n\t" \
  "s_branch RDONE3_%=\n\t" \
  "LFAIL3_%=:\n\t" \
  "v_max_u32 v75, v0, v1\n\t" \
  "v_max3_u32 v75, v75, v2, v3\n\t" \
  "v_max3_u32 v75, v75, v4, v5\n\t" \
  "v_max3_u32 v75, v75, v6, v7\n\t" \
  "v_max3_u32 v75, v75, v8, v9\n\t" \
  "v_max3_u32 v75, v75, v10, v11\n\t" \
  "v_max3_u32 v75, v75, v12, v13\n\t" \
  "v_max3_u32 v75, v75, v14, v15\n\t" \
  "v_max3_u32 v75, v75, v16, v17\n\t" \
  "v_max3_u32 v75, v75, v18, v19\n\t" \
  "v_max3_u32 v75, v75, v20, v21\n\t" \
  "v_max3_u32 v75, v75, v22, v23\n\t" \
  "v_max3_u32 v75, v75, v24, v25\n\t" \
  "v_max3_u32 v75, v75, v26, v27\n\t" \
  "v_max3_u32 v75, v75, v28, v29\n\t" \
  "v_max3_u32 v75, v75, v30, v31\n\t" \
  "v_max3_u32 v75, v75, v32, v33\n\t" \
  "v_max3_u32 v75, v75, v34, v35\n\t" \
  "v_cmp_eq_u32 vcc, -1, v75\n\t" \
  "s_cbranch_vccz LGO3_%=\n\t" \
  "s_add_u32 s68, s68, 1\n\t" \
  "s_waitcnt lgkmcnt(0)\n\t" \
  "s_cmp_gt_u32 s68, s83\n\t" \
  "s_cbranch_scc1 LGV3_%=\n\t" \
  "s_barrier\n\t" \
  "s_waitcnt vmcnt(8)\n\t" \
  "s_branch LG3_%=\n\t" \
  "RWT3_%=f:\n\t" \
  "s_cmp_lt_i32 s73, -2\n\t" \
  "s_cbranch_scc0 RWT3_%=g\n\t" \
  "s_nop 4\n\t" \
  "s_branch RWT3_%=g\n\t" \
  "LGV3_%=:\n\t" \
  "s_mov_b32 s83, 0\n\t" \
  "s_lshl_b32 s60, s36, 4\n\t" \
  "s_or_b32 s60, s60, s48\n\t" \
  "s_or_b32 s60, s60, 805306368\n\t" \
  "v_mov_b32 v76, s60\n\t" \
  "v_mov_b32 v77, 0\n\t" \
  "s_mov_b64 exec, 1\n\t" \
  "buffer_atomic_cmpswap v[76:77], off, s[28:31], s92 sc1\n\t" \
  "s_mov_b64 exec, -1\n\t" \
  "s_branch LGO3_%=\n\t" \
  "LLAST3_%=:\n\t" \
  "s_waitcnt vmcnt(0) lgkmcnt(0)\n\t" \
  "s_barrier\n\t" \
  "LEND_%=:\n\t" \
  ""
typedef unsigned v32u __attribute__((ext_vector_type(32)));
typedef unsigned v16u __attribute__((ext_vector_type(16)));
#define CH_WCLOB "a0", "a1", "a2", "a3", "a4", "a5", "a6", "a7", "a8", "a9", "a10", "a11", "a12", "a13", "a14", "a15", "a16", "a17", "a18", "a19", "a20", "a21", "a22", "a23", "a24", "a25", "a26", "a27", "a28", "a29", "a30", "a31", "a32", "a33", "a34", "a35", "a36", "a37", "a38", "a39", "a40", "a41", "a42", "a43", "a44", "a45", "a46", "a47", "a48", "a49", "a50", "a51", "a52", "a53", "a54", "a55", "a56", "a57", "a58", "a59", "a60", "a61", "a62", "a63", "a64", "a65", "a66", "a67", "a68", "a69", "a70", "a71", "a72", "a73", "a74", "a75", "a76", "a77", "a78", "a79", "a80", "a81", "a82", "a83", "a84", "a85", "a86", "a87", "a88", "a89", "a90", "a91", "a92", "a93", "a94", "a95", "a96", "a97", "a98", "a99", "a100", "a101", "a102", "a103", "a104", "a105", "a106", "a107", "a108", "a109", "a110", "a111", "a112", "a113", "a114", "a115", "a116", "a117", "a118", "a119", "a120", "a121", "a122", "a123", "a124", "a125", "a126", "a127", "a128", "a129", "a130", "a131", "a132", "a133", "a134", "a135", "a136", "a137", "a138", "a139", "a140", "a141", "a142", "a143", "a144", "a145", "a146", "a147", "a148", "a149", "a150", "a151", "a152", "a153", "a154", "a155", "a156", "a157", "a158", "a159", "a160", "a161", "a162", "a163", "a164", "a165", "a166", "a167", "a168", "a169", "a170", "a171", "a172", "a173", "a174", "a175", "a176", "a177", "a178", "a179", "a180", "a181", "a182", "a183", "a184", "a185", "a186", "a187", "a188", "a189", "a190", "a191", "a192", "a193", "a194", "a195", "a196", "a197", "a198", "a199", "a200", "a201", "a202", "a203", "a204", "a205", "a206", "a207", "a208", "a209", "a210", "a211", "a212", "a213", "a214", "a215", "a216", "a217", "a218", "a219", "a220", "a221", "a222", "a223", "a224", "a225", "a226", "a227", "a228", "a229", "a230", "a231", "a232", "a233", "a234", "a235", "a236", "a237", "a238", "a239", "a240", "a241", "a242", "a243", "a244", "a245", "a246", "a247", "a248", "a249", "a250", "a251", "a252", "a253", "a254", "a255", "v80", "v81", "v82", "v83", "v84", "v85", "v86", "v87", "v88", "v89", "v90", "v91", "v92", "v93", "v94", "v95", "v96", "v97", "v98", "v99", "v100", "v101", "v102", "v103", "v104", "v105", "v106", "v107", "v108", "v109", "v110", "v111", "v112", "v113", "v114", "v115", "v116", "v117", "v118", "v119", "v120", "v121", "v122", "v123", "v124", "v125", "v126", "v127", "v128", "v129", "v130", "v131", "v132", "v133", "v134", "v135", "v136", "v137", "v138", "v139", "v140", "v141", "v142", "v143", "v144", "v145", "v146", "v147", "v148", "v149", "v150", "v151", "v152", "v153", "v154", "v155", "v156", "v157", "v158", "v159", "v160", "v161", "v162", "v163", "v164", "v165", "v166", "v167", "v168", "v169", "v170", "v171", "v172", "v173", "v174", "v175", "v176", "v177", "v178", "v179", "v180", "v181", "v182", "v183", "v184", "v185", "v186", "v187", "v188", "v189", "v190", "v191", "v192", "v193", "v194", "v195", "v196", "v197", "v198", "v199", "v200", "v201", "v202", "v203", "v204", "v205", "v206", "v207", "v208", "v209", "v210", "v211", "v212", "v213", "v214", "v215", "v216", "v217", "v218", "v219", "v220", "v221", "v222", "v223", "v224", "v225", "v226", "v227", "v228", "v229", "v230", "v231", "v232", "v233", "v234", "v235", "v236", "v237", "v238", "v239", "v240", "v241", "v242", "v243", "v244", "v245", "v246", "v247", "v248", "v249", "v250", "v251", "v252", "v253", "v254", "v255"
#define CH_H(b) (8 * ((b) & 3) + ((b) >> 3))
#define CH_L(b) (((b) >> 2) & 1)
__device__ __forceinline__ void start_work(char* smem, int wk);
__device__ __forceinline__ void chain_wg(char* smem) {
  if (CH_L(vblock())) start_work(smem, CH_H(vblock()));
  const MP p = getp();
  const int b = vblock(), h = CH_H(b), L = CH_L(b);
  const int w = __builtin_amdgcn_readfirstlane(threadIdx.x >> 6);
  unsigned lp16 = (threadIdx.x & 63u) * 16u;
  rsrc_t X = mkr(p.xb, XB_BYTES);
  if ((unsigned)(size_t)(__attribute__((address_space(3))) char*)smem != 0u) return;
  const unsigned wsz = DM * HD * 2u - (unsigned)(h * DH) * 2u;
  const rsrc_t RQ = mkr(p.Wq + h * DH, wsz), RK = mkr(p.Wk + h * DH, wsz), RV = mkr(p.Wv + h * DH, wsz), RO = mkr(p.Wo + (long)h * DH * DM, DH * DM * 2u);
  unsigned xcc; asm volatile("s_getreg_b32 %0, hwreg(HW_REG_XCC_ID)" : "=s"(xcc)); xcc &= 3u;
  unsigned d96 = 0u;
  const unsigned c43 = P_FA * PAIR + (unsigned)h * 128u + 32u;
  const unsigned h128 = (unsigned)h * 128u, uw = (unsigned)w, t = uw >> 1, T = uw & 1u, NONE = 0xF0000000u;
  const unsigned hx = (unsigned)h >> 3, hl = (unsigned)h & 7u;
  const unsigned u0 = w == 3 ? (unsigned)h >> 2 : (hl & 1u) + hx * 2u + (hl >> 1) * 8u + (w == 2 ? 32u : 0u);
  const bool idle = w == 3 && (h & 3);
  const unsigned c37 = idle ? NONE : (w == 3 ? AGT + u0 * 128u : AGO + (u0 >> 5) * 32768u + (u0 & 31u) * 128u);
  const unsigned c41 = idle ? NONE : (w == 3 ? 524288u + (u0 >> 1) * 16384u + (u0 & 1u) * 4096u : (u0 & 7u) * 16384u + ((u0 >> 3) & 1u) * 4096u + (u0 >> 4) * 131072u);
  const unsigned c42 = AGO + (xcc & 1u) * 16384u + (xcc >> 1) * 4096u;
  const unsigned c46 = (w == 3 ? (h & 7) == 5 && DL3 > -50 : h == 30) ? AGT + 1024u : NONE;
  const unsigned c38 = (unsigned)(3 * h + 1) * PAIR + T * 4096u + t * 2048u, c39 = (P_KV + (unsigned)(h >> 1)) * PAIR + (unsigned)(h & 1) * 4096u;
  const unsigned c45 = P_FB * PAIR + 4096u + (unsigned)(16 + (h & 15)) * 128u;
  unsigned d48 = 0u, d49 = 0u, d53 = SLOT, d86 = 1u, d65 = rtc();
  unsigned mn = 120u;
  v4u stt;
  asm volatile("s_waitcnt vmcnt(0) lgkmcnt(0)\n\t" CH_ASM "LCHEND:"
               : "+{s48}"(d48), "+{s49}"(d49), "+{s53}"(d53), "+{s65}"(d65), "+{s81}"(mn), "+{s86}"(d86), "+{s96}"(d96), "=&{s[76:79]}"(stt), "+{v69}"(lp16), "+{s[28:31]}"(X)
               : [Wq] "s"(RQ), [Wk] "s"(RK), [Wv] "s"(RV), [Wo] "s"(RO), "{s33}"(HT_OFF + 8192u + 256u + (unsigned)b * 4u), "{s36}"(h128), "{s37}"(c37), "{s40}"(w), "{s41}"(c41), "{s42}"(c42), "{s46}"(c46), "{s92}"(TMO_OFF), "{s38}"(c38), "{s39}"(c39), "{s43}"(c43), "{s45}"(c45), "{s32}"(L), "{s26}"(AGT + 1024u + xcc * 128u), "{s27}"(RDY_OFF(0) + (unsigned)((h % 9) * 1024 + (b & 7) * 128)), [ctab] "i"(CTAB_OFF), [ring] "i"(SRING / 3 * XBLK), [dl1l] "i"(DL1 - LATE), [armd] "i"(3 - ARMK), [arms] "i"(ARMK - 3),
                 [cap] "i"(SPIN_CAP)
               : "v0", "v1", "v2", "v3", "v4", "v5", "v6", "v7", "v8", "v9", "v10", "v11", "v12", "v13", "v14", "v15", "v16", "v17", "v18", "v19",
                 "v20", "v21", "v22", "v23", "v24", "v25", "v26", "v27", "v28", "v29", "v30", "v31", "v32", "v33", "v34", "v35", "v36", "v37", "v38", "v39",
                 "v40", "v41", "v42", "v43", "v44", "v45", "v46", "v47", "v48", "v49", "v50", "v51", "v52", "v53", "v54", "v55", "v56", "v57", "v58", "v59",
                 "v60", "v61", "v62", "v63", "v64", "v65", "v66", "v67", "v68", "v70", "v71", "v72", "v73", "v74", "v75", "v76", "v77", "v78", "v79",
                 CH_WCLOB, "s20", "s21", "s22", "s23", "s24", "s25", "s90", "s91", "s34", "s35", "s47", "s87", "s50", "s51", "s52", "s69", "s82", "s85", "s54", "s75", "s80", "s55", "s56", "s57", "s58", "s59", "s60", "s61", "s62", "s63", "s64", "s66", "s67", "s68", "s70", "s71", "s72", "s73", "s74", "s88", "s89",
                 "s83", "s84", "s97", "s98", "s99", "s93", "s94", "s95", "m0", "vcc", "scc", "memory");
}
#define XQ 0u
#define XQL 0u
#define XA 0u
#define XAL 0u
#define XRL 0u
#define QLEAD 4
#define TK_QDL
#define TK_ADL
#define ARMD 0
#define ARMD1 0
#define ARMS 0
#define ARMT 0
#define RONE 2
#define ARMP 0
#define TK_RING "%[ring]"
#define PSP 4
#define PSR 8
#define RDLY 40
#define TK_ASL
#define TK_QSL
#define ROT 1
#define PACE 1
#define TK_HAVE "s_cmp_eq_u32 vcc_lo, 0\n\t"
#define TK_PACE "s_cmp_gt_i32 s42, -60\n\ts_cbranch_scc1 LPACE_%=\n\t"
#define TK_QDL2
#define TK_QNP
#define QLEAD1 2
#define QXW -10
#define QXC QXW
#define TK_QXW "%[qxw]"
#define TK_QXC "%[qxc]"
#define PSG 16
#define TK_PSG CH_S(PSG)
#define PSK 2
#define PSH 2
#define TK_CAP "%[cap]"
#define TK_CTAB "%[ctab]"
#define TK_SG0 CH_S(SG0)
#define TK_PSK CH_S(PSK)
#define TK_PSH CH_S(PSH)
#define TK_PA0
#define TK_PA1
#define TK_PA2
#define TK_PQ0
#define TK_PQ1
#define TK_PQ2
#define TT(s, i)
#define TK_AC(n)
#define TK_ACE
#define TK_AC0 TK_AC(212)
#define TK_AC1 TK_AC(213)
#define TK_AC2 TK_AC(214)
#define TK_AC3 TK_AC(215)
#define TK_AC4 TK_AC(216)
#define TK_AC5 TK_AC(217)
#define TK_AC6 TK_AC(218)
#define TK_AC7 TK_AC(219)
#define TK_AC8 TK_AC(220)
#define AP_ASM \
  "v_lshrrev_b32 v40, 8, v0\n\t" \
  "v_mul_u32_u24 v40, 4608, v40\n\t" \
  "v_and_b32 v41, 0xf0, v0\n\t" \
  "v_lshrrev_b32 v42, 3, v41\n\t" \
  "v_add3_u32 v41, v41, v40, s64\n\t" \
  "v_add3_u32 v42, v42, v40, s65\n\t" \
  "s_mov_b32 s62, 0x05040100\n\t" \
  "s_mov_b32 s63, 0x07060302\n\t" \
  "s_xor_b32 s60, s66, 0\n\t" \
  "s_add_u32 s60, s60, 0\n\t" \
  "s_add_u32 s61, s60, 18432\n\t" \
  "buffer_load_dwordx4 v[48:51], v41, %[W], s60 offen offset:0\n\t" \
  "buffer_load_dwordx4 v[52:55], v41, %[W], s60 offen offset:1152\n\t" \
  "buffer_load_dwordx4 v[56:59], v41, %[W], s60 offen offset:2304\n\t" \
  "buffer_load_dwordx4 v[60:63], v41, %[W], s60 offen offset:3456\n\t" \
  "buffer_load_dwordx4 v[64:67], v41, %[W], s61 offen offset:0\n\t" \
  "buffer_load_dwordx4 v[68:71], v41, %[W], s61 offen offset:1152\n\t" \
  "buffer_load_dwordx4 v[72:75], v41, %[W], s61 offen offset:2304\n\t" \
  "buffer_load_dwordx4 v[76:79], v41, %[W], s61 offen offset:3456\n\t" \
  "s_xor_b32 s60, s66, 0\n\t" \
  "s_add_u32 s60, s60, 36864\n\t" \
  "s_add_u32 s61, s60, 18432\n\t" \
  "buffer_load_dwordx4 v[80:83], v41, %[W], s60 offen offset:0\n\t" \
  "buffer_load_dwordx4 v[84:87], v41, %[W], s60 offen offset:1152\n\t" \
  "buffer_load_dwordx4 v[88:91], v41, %[W], s60 offen offset:2304\n\t" \
  "buffer_load_dwordx4 v[92:95], v41, %[W], s60 offen offset:3456\n\t" \
  "buffer_load_dwordx4 v[96:99], v41, %[W], s61 offen offset:0\n\t" \
  "buffer_load_dwordx4 v[100:103], v41, %[W], s61 offen offset:1152\n\t" \
  "buffer_load_dwordx4 v[104:107], v41, %[W], s61 offen offset:2304\n\t" \
  "buffer_load_dwordx4 v[108:111], v41, %[W], s61 offen offset:3456\n\t" \
  "s_xor_b32 s60, s66, 0\n\t" \
  "s_add_u32 s60, s60, 73728\n\t" \
  "s_add_u32 s61, s60, 18432\n\t" \
  "buffer_load_dwordx4 v[112:115], v41, %[W], s60 offen offset:0\n\t" \
  "buffer_load_dwordx4 v[116:119], v41, %[W], s60 offen offset:1152\n\t" \
  "buffer_load_dwordx4 v[120:123], v41, %[W], s60 offen offset:2304\n\t" \
  "buffer_load_dwordx4 v[124:127], v41, %[W], s60 offen offset:3456\n\t" \
  "buffer_load_dwordx4 v[128:131], v41, %[W], s61 offen offset:0\n\t" \
  "buffer_load_dwordx4 v[132:135], v41, %[W], s61 offen offset:1152\n\t" \
  "buffer_load_dwordx4 v[136:139], v41, %[W], s61 offen offset:2304\n\t" \
  "buffer_load_dwordx4 v[140:143], v41, %[W], s61 offen offset:3456\n\t" \
  "s_xor_b32 s60, s66, 0\n\t" \
  "s_add_u32 s60, s60, 110592\n\t" \
  "s_add_u32 s61, s60, 18432\n\t" \
  "buffer_load_dwordx4 v[144:147], v41, %[W], s60 offen offset:0\n\t" \
  "buffer_load_dwordx4 v[148:151], v41, %[W], s60 offen offset:1152\n\t" \
  "buffer_load_dwordx4 v[152:155], v41, %[W], s60 offen offset:2304\n\t" \
  "buffer_load_dwordx4 v[156:159], v41, %[W], s60 offen offset:3456\n\t" \
  "buffer_load_dwordx4 v[160:163], v41, %[W], s61 offen offset:0\n\t" \
  "buffer_load_dwordx4 v[164:167], v41, %[W], s61 offen offset:1152\n\t" \
  "buffer_load_dwordx4 v[168:171], v41, %[W], s61 offen offset:2304\n\t" \
  "buffer_load_dwordx4 v[172:175], v41, %[W], s61 offen offset:3456\n\t" \
  "s_xor_b32 s60, s66, 147456\n\t" \
  "s_add_u32 s60, s60, 0\n\t" \
  "s_add_u32 s61, s60, 18432\n\t" \
  "buffer_load_dwordx4 v[176:179], v41, %[W], s60 offen offset:0\n\t" \
  "buffer_load_dwordx4 v[180:183], v41, %[W], s60 offen offset:1152\n\t" \
  "buffer_load_dwordx4 v[184:187], v41, %[W], s60 offen offset:2304\n\t" \
  "buffer_load_dwordx4 v[188:191], v41, %[W], s60 offen offset:3456\n\t" \
  "buffer_load_dwordx4 v[192:195], v41, %[W], s61 offen offset:0\n\t" \
  "buffer_load_dwordx4 v[196:199], v41, %[W], s61 offen offset:1152\n\t" \
  "buffer_load_dwordx4 v[200:203], v41, %[W], s61 offen offset:2304\n\t" \
  "buffer_load_dwordx4 v[204:207], v41, %[W], s61 offen offset:3456\n\t" \
  "s_waitcnt vmcnt(38)\n\t" \
  "v_perm_b32 v44, v52, v48, s62\n\t" \
  "s_waitcnt vmcnt(36)\n\t" \
  "v_perm_b32 v45, v60, v56, s62\n\t" \
  "s_waitcnt vmcnt(34)\n\t" \
  "v_perm_b32 v46, v68, v64, s62\n\t" \
  "s_waitcnt vmcnt(32)\n\t" \
  "v_perm_b32 v47, v76, v72, s62\n\t" \
  "v_accvgpr_write_b32 a0, v44\n\t" \
  "v_accvgpr_write_b32 a1, v45\n\t" \
  "v_accvgpr_write_b32 a2, v46\n\t" \
  "v_accvgpr_write_b32 a3, v47\n\t" \
  "v_perm_b32 v44, v52, v48, s63\n\t" \
  "v_perm_b32 v45, v60, v56, s63\n\t" \
  "v_perm_b32 v46, v68, v64, s63\n\t" \
  "v_perm_b32 v47, v76, v72, s63\n\t" \
  "v_accvgpr_write_b32 a4, v44\n\t" \
  "v_accvgpr_write_b32 a5, v45\n\t" \
  "v_accvgpr_write_b32 a6, v46\n\t" \
  "v_accvgpr_write_b32 a7, v47\n\t" \
  "v_perm_b32 v44, v53, v49, s62\n\t" \
  "v_perm_b32 v45, v61, v57, s62\n\t" \
  "v_perm_b32 v46, v69, v65, s62\n\t" \
  "v_perm_b32 v47, v77, v73, s62\n\t" \
  "v_accvgpr_write_b32 a8, v44\n\t" \
  "v_accvgpr_write_b32 a9, v45\n\t" \
  "v_accvgpr_write_b32 a10, v46\n\t" \
  "v_accvgpr_write_b32 a11, v47\n\t" \
  "v_perm_b32 v44, v53, v49, s63\n\t" \
  "v_perm_b32 v45, v61, v57, s63\n\t" \
  "v_perm_b32 v46, v69, v65, s63\n\t" \
  "v_perm_b32 v47, v77, v73, s63\n\t" \
  "v_accvgpr_write_b32 a12, v44\n\t" \
  "v_accvgpr_write_b32 a13, v45\n\t" \
  "v_accvgpr_write_b32 a14, v46\n\t" \
  "v_accvgpr_write_b32 a15, v47\n\t" \
  "v_perm_b32 v44, v54, v50, s62\n\t" \
  "v_perm_b32 v45, v62, v58, s62\n\t" \
  "v_perm_b32 v46, v70, v66, s62\n\t" \
  "v_perm_b32 v47, v78, v74, s62\n\t" \
  "v_accvgpr_write_b32 a16, v44\n\t" \
  "v_accvgpr_write_b32 a17, v45\n\t" \
  "v_accvgpr_write_b32 a18, v46\n\t" \
  "v_accvgpr_write_b32 a19, v47\n\t" \
  "v_perm_b32 v44, v54, v50, s63\n\t" \
  "v_perm_b32 v45, v62, v58, s63\n\t" \
  "v_perm_b32 v46, v70, v66, s63\n\t" \
  "v_perm_b32 v47, v78, v74, s63\n\t" \
  "v_accvgpr_write_b32 a20, v44\n\t" \
  "v_accvgpr_write_b32 a21, v45\n\t" \
  "v_accvgpr_write_b32 a22, v46\n\t" \
  "v_accvgpr_write_b32 a23, v47\n\t" \
  "v_perm_b32 v44, v55, v51, s62\n\t" \
  "v_perm_b32 v45, v63, v59, s62\n\t" \
  "v_perm_b32 v46, v71, v67, s62\n\t" \
  "v_perm_b32 v47, v79, v75, s62\n\t" \
  "v_accvgpr_write_b32 a24, v44\n\t" \
  "v_accvgpr_write_b32 a25, v45\n\t" \
  "v_accvgpr_write_b32 a26, v46\n\t" \
  "v_accvgpr_write_b32 a27, v47\n\t" \
  "v_perm_b32 v44, v55, v51, s63\n\t" \
  "v_perm_b32 v45, v63, v59, s63\n\t" \
  "v_perm_b32 v46, v71, v67, s63\n\t" \
  "v_perm_b32 v47, v79, v75, s63\n\t" \
  "v_accvgpr_write_b32 a28, v44\n\t" \
  "v_accvgpr_write_b32 a29, v45\n\t" \
  "v_accvgpr_write_b32 a30, v46\n\t" \
  "v_accvgpr_write_b32 a31, v47\n\t" \
  "s_xor_b32 s60, s66, 147456\n\t" \
  "s_add_u32 s60, s60, 36864\n\t" \
  "s_add_u32 s61, s60, 18432\n\t" \
  "buffer_load_dwordx4 v[48:51], v41, %[W], s60 offen offset:0\n\t" \
  "buffer_load_dwordx4 v[52:55], v41, %[W], s60 offen offset:1152\n\t" \
  "buffer_load_dwordx4 v[56:59], v41, %[W], s60 offen offset:2304\n\t" \
  "buffer_load_dwordx4 v[60:63], v41, %[W], s60 offen offset:3456\n\t" \
  "buffer_load_dwordx4 v[64:67], v41, %[W], s61 offen offset:0\n\t" \
  "buffer_load_dwordx4 v[68:71], v41, %[W], s61 offen offset:1152\n\t" \
  "buffer_load_dwordx4 v[72:75], v41, %[W], s61 offen offset:2304\n\t" \
  "buffer_load_dwordx4 v[76:79], v41, %[W], s61 offen offset:3456\n\t" \
  "s_waitcnt vmcnt(38)\n\t" \
  "v_perm_b32 v44, v84, v80, s62\n\t" \
  "s_waitcnt vmcnt(36)\n\t" \
  "v_perm_b32 v45, v92, v88, s62\n\t" \
  "s_waitcnt vmcnt(34)\n\t" \
  "v_perm_b32 v46, v100, v96, s62\n\t" \
  "s_waitcnt vmcnt(32)\n\t" \
  "v_perm_b32 v47, v108, v104, s62\n\t" \
  "v_accvgpr_write_b32 a32, v44\n\t" \
  "v_accvgpr_write_b32 a33, v45\n\t" \
  "v_accvgpr_write_b32 a34, v46\n\t" \
  "v_accvgpr_write_b32 a35, v47\n\t" \
  "v_perm_b32 v44, v84, v80, s63\n\t" \
  "v_perm_b32 v45, v92, v88, s63\n\t" \
  "v_perm_b32 v46, v100, v96, s63\n\t" \
  "v_perm_b32 v47, v108, v104, s63\n\t" \
  "v_accvgpr_write_b32 a36, v44\n\t" \
  "v_accvgpr_write_b32 a37, v45\n\t" \
  "v_accvgpr_write_b32 a38, v46\n\t" \
  "v_accvgpr_write_b32 a39, v47\n\t" \
  "v_perm_b32 v44, v85, v81, s62\n\t" \
  "v_perm_b32 v45, v93, v89, s62\n\t" \
  "v_perm_b32 v46, v101, v97, s62\n\t" \
  "v_perm_b32 v47, v109, v105, s62\n\t" \
  "v_accvgpr_write_b32 a40, v44\n\t" \
  "v_accvgpr_write_b32 a41, v45\n\t" \
  "v_accvgpr_write_b32 a42, v46\n\t" \
  "v_accvgpr_write_b32 a43, v47\n\t" \
  "v_perm_b32 v44, v85, v81, s63\n\t" \
  "v_perm_b32 v45, v93, v89, s63\n\t" \
  "v_perm_b32 v46, v101, v97, s63\n\t" \
  "v_perm_b32 v47, v109, v105, s63\n\t" \
  "v_accvgpr_write_b32 a44, v44\n\t" \
  "v_accvgpr_write_b32 a45, v45\n\t" \
  "v_accvgpr_write_b32 a46, v46\n\t" \
  "v_accvgpr_write_b32 a47, v47\n\t" \
  "v_perm_b32 v44, v86, v82, s62\n\t" \
  "v_perm_b32 v45, v94, v90, s62\n\t" \
  "v_perm_b32 v46, v102, v98, s62\n\t" \
  "v_perm_b32 v47, v110, v106, s62\n\t" \
  "v_accvgpr_write_b32 a48, v44\n\t" \
  "v_accvgpr_write_b32 a49, v45\n\t" \
  "v_accvgpr_write_b32 a50, v46\n\t" \
  "v_accvgpr_write_b32 a51, v47\n\t" \
  "v_perm_b32 v44, v86, v82, s63\n\t" \
  "v_perm_b32 v45, v94, v90, s63\n\t" \
  "v_perm_b32 v46, v102, v98, s63\n\t" \
  "v_perm_b32 v47, v110, v106, s63\n\t" \
  "v_accvgpr_write_b32 a52, v44\n\t" \
  "v_accvgpr_write_b32 a53, v45\n\t" \
  "v_accvgpr_write_b32 a54, v46\n\t" \
  "v_accvgpr_write_b32 a55, v47\n\t" \
  "v_perm_b32 v44, v87, v83, s62\n\t" \
  "v_perm_b32 v45, v95, v91, s62\n\t" \
  "v_perm_b32 v46, v103, v99, s62\n\t" \
  "v_perm_b32 v47, v111, v107, s62\n\t" \
  "v_accvgpr_write_b32 a56, v44\n\t" \
  "v_accvgpr_write_b32 a57, v45\n\t" \
  "v_accvgpr_write_b32 a58, v46\n\t" \
  "v_accvgpr_write_b32 a59, v47\n\t" \
  "v_perm_b32 v44, v87, v83, s63\n\t" \
  "v_perm_b32 v45, v95, v91, s63\n\t" \
  "v_perm_b32 v46, v103, v99, s63\n\t" \
  "v_perm_b32 v47, v111, v107, s63\n\t" \
  "v_accvgpr_write_b32 a60, v44\n\t" \
  "v_accvgpr_write_b32 a61, v45\n\t" \
  "v_accvgpr_write_b32 a62, v46\n\t" \
  "v_accvgpr_write_b32 a63, v47\n\t" \
  "s_xor_b32 s60, s66, 147456\n\t" \
  "s_add_u32 s60, s60, 73728\n\t" \
  "s_add_u32 s61, s60, 18432\n\t" \
  "buffer_load_dwordx4 v[80:83], v41, %[W], s60 offen offset:0\n\t" \
  "buffer_load_dwordx4 v[84:87], v41, %[W], s60 offen offset:1152\n\t" \
  "buffer_load_dwordx4 v[88:91], v41, %[W], s60 offen offset:2304\n\t" \
  "buffer_load_dwordx4 v[92:95], v41, %[W], s60 offen offset:3456\n\t" \
  "buffer_load_dwordx4 v[96:99], v41, %[W], s61 offen offset:0\n\t" \
  "buffer_load_dwordx4 v[100:103], v41, %[W], s61 offen offset:1152\n\t" \
  "buffer_load_dwordx4 v[104:107], v41, %[W], s61 offen offset:2304\n\t" \
  "buffer_load_dwordx4 v[108:111], v41, %[W], s61 offen offset:3456\n\t" \
  "s_waitcnt vmcnt(38)\n\t" \
  "v_perm_b32 v44, v116, v112, s62\n\t" \
  "s_waitcnt vmcnt(36)\n\t" \
  "v_perm_b32 v45, v124, v120, s62\n\t" \
  "s_waitcnt vmcnt(34)\n\t" \
  "v_perm_b32 v46, v132, v128, s62\n\t" \
  "s_waitcnt vmcnt(32)\n\t" \
  "v_perm_b32 v47, v140, v136, s62\n\t" \
  "v_accvgpr_write_b32 a64, v44\n\t" \
  "v_accvgpr_write_b32 a65, v45\n\t" \
  "v_accvgpr_write_b32 a66, v46\n\t" \
  "v_accvgpr_write_b32 a67, v47\n\t" \
  "v_perm_b32 v44, v116, v112, s63\n\t" \
  "v_perm_b32 v45, v124, v120, s63\n\t" \
  "v_perm_b32 v46, v132, v128, s63\n\t" \
  "v_perm_b32 v47, v140, v136, s63\n\t" \
  "v_accvgpr_write_b32 a68, v44\n\t" \
  "v_accvgpr_write_b32 a69, v45\n\t" \
  "v_accvgpr_write_b32 a70, v46\n\t" \
  "v_accvgpr_write_b32 a71, v47\n\t" \
  "v_perm_b32 v44, v117, v113, s62\n\t" \
  "v_perm_b32 v45, v125, v121, s62\n\t" \
  "v_perm_b32 v46, v133, v129, s62\n\t" \
  "v_perm_b32 v47, v141, v137, s62\n\t" \
  "v_accvgpr_write_b32 a72, v44\n\t" \
  "v_accvgpr_write_b32 a73, v45\n\t" \
  "v_accvgpr_write_b32 a74, v46\n\t" \
  "v_accvgpr_write_b32 a75, v47\n\t" \
  "v_perm_b32 v44, v117, v113, s63\n\t" \
  "v_perm_b32 v45, v125, v121, s63\n\t" \
  "v_perm_b32 v46, v133, v129, s63\n\t" \
  "v_perm_b32 v47, v141, v137, s63\n\t" \
  "v_accvgpr_write_b32 a76, v44\n\t" \
  "v_accvgpr_write_b32 a77, v45\n\t" \
  "v_accvgpr_write_b32 a78, v46\n\t" \
  "v_accvgpr_write_b32 a79, v47\n\t" \
  "v_perm_b32 v44, v118, v114, s62\n\t" \
  "v_perm_b32 v45, v126, v122, s62\n\t" \
  "v_perm_b32 v46, v134, v130, s62\n\t" \
  "v_perm_b32 v47, v142, v138, s62\n\t" \
  "v_accvgpr_write_b32 a80, v44\n\t" \
  "v_accvgpr_write_b32 a81, v45\n\t" \
  "v_accvgpr_write_b32 a82, v46\n\t" \
  "v_accvgpr_write_b32 a83, v47\n\t" \
  "v_perm_b32 v44, v118, v114, s63\n\t" \
  "v_perm_b32 v45, v126, v122, s63\n\t" \
  "v_perm_b32 v46, v134, v130, s63\n\t" \
  "v_perm_b32 v47, v142, v138, s63\n\t" \
  "v_accvgpr_write_b32 a84, v44\n\t" \
  "v_accvgpr_write_b32 a85, v45\n\t" \
  "v_accvgpr_write_b32 a86, v46\n\t" \
  "v_accvgpr_write_b32 a87, v47\n\t" \
  "v_perm_b32 v44, v119, v115, s62\n\t" \
  "v_perm_b32 v45, v127, v123, s62\n\t" \
  "v_perm_b32 v46, v135, v131, s62\n\t" \
  "v_perm_b32 v47, v143, v139, s62\n\t" \
  "v_accvgpr_write_b32 a88, v44\n\t" \
  "v_accvgpr_write_b32 a89, v45\n\t" \
  "v_accvgpr_write_b32 a90, v46\n\t" \
  "v_accvgpr_write_b32 a91, v47\n\t" \
  "v_perm_b32 v44, v119, v115, s63\n\t" \
  "v_perm_b32 v45, v127, v123, s63\n\t" \
  "v_perm_b32 v46, v135, v131, s63\n\t" \
  "v_perm_b32 v47, v143, v139, s63\n\t" \
  "v_accvgpr_write_b32 a92, v44\n\t" \
  "v_accvgpr_write_b32 a93, v45\n\t" \
  "v_accvgpr_write_b32 a94, v46\n\t" \
  "v_accvgpr_write_b32 a95, v47\n\t" \
  "s_xor_b32 s60, s66, 147456\n\t" \
  "s_add_u32 s60, s60, 110592\n\t" \
  "s_add_u32 s61, s60, 18432\n\t" \
  "buffer_load_dwordx4 v[112:115], v41, %[W], s60 offen offset:0\n\t" \
  "buffer_load_dwordx4 v[116:119], v41, %[W], s60 offen offset:1152\n\t" \
  "buffer_load_dwordx4 v[120:123], v41, %[W], s60 offen offset:2304\n\t" \
  "buffer_load_dwordx4 v[124:127], v41, %[W], s60 offen offset:3456\n\t" \
  "buffer_load_dwordx4 v[128:131], v41, %[W], s61 offen offset:0\n\t" \
  "buffer_load_dwordx4 v[132:135], v41, %[W], s61 offen offset:1152\n\t" \
  "buffer_load_dwordx4 v[136:139], v41, %[W], s61 offen offset:2304\n\t" \
  "buffer_load_dwordx4 v[140:143], v41, %[W], s61 offen offset:3456\n\t" \
  "s_waitcnt vmcnt(38)\n\t" \
  "v_perm_b32 v44, v148, v144, s62\n\t" \
  "s_waitcnt vmcnt(36)\n\t" \
  "v_perm_b32 v45, v156, v152, s62\n\t" \
  "s_waitcnt vmcnt(34)\n\t" \
  "v_perm_b32 v46, v164, v160, s62\n\t" \
  "s_waitcnt vmcnt(32)\n\t" \
  "v_perm_b32 v47, v172, v168, s62\n\t" \
  "v_accvgpr_write_b32 a96, v44\n\t" \
  "v_accvgpr_write_b32 a97, v45\n\t" \
  "v_accvgpr_write_b32 a98, v46\n\t" \
  "v_accvgpr_write_b32 a99, v47\n\t" \
  "v_perm_b32 v44, v148, v144, s63\n\t" \
  "v_perm_b32 v45, v156, v152, s63\n\t" \
  "v_perm_b32 v46, v164, v160, s63\n\t" \
  "v_perm_b32 v47, v172, v168, s63\n\t" \
  "v_accvgpr_write_b32 a100, v44\n\t" \
  "v_accvgpr_write_b32 a101, v45\n\t" \
  "v_accvgpr_write_b32 a102, v46\n\t" \
  "v_accvgpr_write_b32 a103, v47\n\t" \
  "v_perm_b32 v44, v149, v145, s62\n\t" \
  "v_perm_b32 v45, v157, v153, s62\n\t" \
  "v_perm_b32 v46, v165, v161, s62\n\t" \
  "v_perm_b32 v47, v173, v169, s62\n\t" \
  "v_accvgpr_write_b32 a104, v44\n\t" \
  "v_accvgpr_write_b32 a105, v45\n\t" \
  "v_accvgpr_write_b32 a106, v46\n\t" \
  "v_accvgpr_write_b32 a107, v47\n\t" \
  "v_perm_b32 v44, v149, v145, s63\n\t" \
  "v_perm_b32 v45, v157, v153, s63\n\t" \
  "v_perm_b32 v46, v165, v161, s63\n\t" \
  "v_perm_b32 v47, v173, v169, s63\n\t" \
  "v_accvgpr_write_b32 a108, v44\n\t" \
  "v_accvgpr_write_b32 a109, v45\n\t" \
  "v_accvgpr_write_b32 a110, v46\n\t" \
  "v_accvgpr_write_b32 a111, v47\n\t" \
  "v_perm_b32 v44, v150, v146, s62\n\t" \
  "v_perm_b32 v45, v158, v154, s62\n\t" \
  "v_perm_b32 v46, v166, v162, s62\n\t" \
  "v_perm_b32 v47, v174, v170, s62\n\t" \
  "v_accvgpr_write_b32 a112, v44\n\t" \
  "v_accvgpr_write_b32 a113, v45\n\t" \
  "v_accvgpr_write_b32 a114, v46\n\t" \
  "v_accvgpr_write_b32 a115, v47\n\t" \
  "v_perm_b32 v44, v150, v146, s63\n\t" \
  "v_perm_b32 v45, v158, v154, s63\n\t" \
  "v_perm_b32 v46, v166, v162, s63\n\t" \
  "v_perm_b32 v47, v174, v170, s63\n\t" \
  "v_accvgpr_write_b32 a116, v44\n\t" \
  "v_accvgpr_write_b32 a117, v45\n\t" \
  "v_accvgpr_write_b32 a118, v46\n\t" \
  "v_accvgpr_write_b32 a119, v47\n\t" \
  "v_perm_b32 v44, v151, v147, s62\n\t" \
  "v_perm_b32 v45, v159, v155, s62\n\t" \
  "v_perm_b32 v46, v167, v163, s62\n\t" \
  "v_perm_b32 v47, v175, v171, s62\n\t" \
  "v_accvgpr_write_b32 a120, v44\n\t" \
  "v_accvgpr_write_b32 a121, v45\n\t" \
  "v_accvgpr_write_b32 a122, v46\n\t" \
  "v_accvgpr_write_b32 a123, v47\n\t" \
  "v_perm_b32 v44, v151, v147, s63\n\t" \
  "v_perm_b32 v45, v159, v155, s63\n\t" \
  "v_perm_b32 v46, v167, v163, s63\n\t" \
  "v_perm_b32 v47, v175, v171, s63\n\t" \
  "v_accvgpr_write_b32 a124, v44\n\t" \
  "v_accvgpr_write_b32 a125, v45\n\t" \
  "v_accvgpr_write_b32 a126, v46\n\t" \
  "v_accvgpr_write_b32 a127, v47\n\t" \
  "s_xor_b32 s60, s66, 0\n\t" \
  "s_add_u32 s60, s60, 0\n\t" \
  "s_add_u32 s61, s60, 18432\n\t" \
  "buffer_load_ushort v224, v42, %[W], s60 offen offset:0\n\t" \
  "buffer_load_ushort v144, v42, %[W], s60 offen offset:1152\n\t" \
  "buffer_load_ushort v225, v42, %[W], s60 offen offset:2304\n\t" \
  "buffer_load_ushort v145, v42, %[W], s60 offen offset:3456\n\t" \
  "buffer_load_ushort v226, v42, %[W], s61 offen offset:0\n\t" \
  "buffer_load_ushort v146, v42, %[W], s61 offen offset:1152\n\t" \
  "buffer_load_ushort v227, v42, %[W], s61 offen offset:2304\n\t" \
  "buffer_load_ushort v147, v42, %[W], s61 offen offset:3456\n\t" \
  "s_xor_b32 s60, s66, 0\n\t" \
  "s_add_u32 s60, s60, 36864\n\t" \
  "s_add_u32 s61, s60, 18432\n\t" \
  "buffer_load_ushort v228, v42, %[W], s60 offen offset:0\n\t" \
  "buffer_load_ushort v148, v42, %[W], s60 offen offset:1152\n\t" \
  "buffer_load_ushort v229, v42, %[W], s60 offen offset:2304\n\t" \
  "buffer_load_ushort v149, v42, %[W], s60 offen offset:3456\n\t" \
  "buffer_load_ushort v230, v42, %[W], s61 offen offset:0\n\t" \
  "buffer_load_ushort v150, v42, %[W], s61 offen offset:1152\n\t" \
  "buffer_load_ushort v231, v42, %[W], s61 offen offset:2304\n\t" \
  "buffer_load_ushort v151, v42, %[W], s61 offen offset:3456\n\t" \
  "s_waitcnt vmcnt(46)\n\t" \
  "v_perm_b32 v44, v180, v176, s62\n\t" \
  "s_waitcnt vmcnt(44)\n\t" \
  "v_perm_b32 v45, v188, v184, s62\n\t" \
  "s_waitcnt vmcnt(42)\n\t" \
  "v_perm_b32 v46, v196, v192, s62\n\t" \
  "s_waitcnt vmcnt(40)\n\t" \
  "v_perm_b32 v47, v204, v200, s62\n\t" \
  "v_accvgpr_write_b32 a128, v44\n\t" \
  "v_accvgpr_write_b32 a129, v45\n\t" \
  "v_accvgpr_write_b32 a130, v46\n\t" \
  "v_accvgpr_write_b32 a131, v47\n\t" \
  "v_perm_b32 v44, v180, v176, s63\n\t" \
  "v_perm_b32 v45, v188, v184, s63\n\t" \
  "v_perm_b32 v46, v196, v192, s63\n\t" \
  "v_perm_b32 v47, v204, v200, s63\n\t" \
  "v_accvgpr_write_b32 a132, v44\n\t" \
  "v_accvgpr_write_b32 a133, v45\n\t" \
  "v_accvgpr_write_b32 a134, v46\n\t" \
  "v_accvgpr_write_b32 a135, v47\n\t" \
  "v_perm_b32 v44, v181, v177, s62\n\t" \
  "v_perm_b32 v45, v189, v185, s62\n\t" \
  "v_perm_b32 v46, v197, v193, s62\n\t" \
  "v_perm_b32 v47, v205, v201, s62\n\t" \
  "v_accvgpr_write_b32 a136, v44\n\t" \
  "v_accvgpr_write_b32 a137, v45\n\t" \
  "v_accvgpr_write_b32 a138, v46\n\t" \
  "v_accvgpr_write_b32 a139, v47\n\t" \
  "v_perm_b32 v44, v181, v177, s63\n\t" \
  "v_perm_b32 v45, v189, v185, s63\n\t" \
  "v_perm_b32 v46, v197, v193, s63\n\t" \
  "v_perm_b32 v47, v205, v201, s63\n\t" \
  "v_accvgpr_write_b32 a140, v44\n\t" \
  "v_accvgpr_write_b32 a141, v45\n\t" \
  "v_accvgpr_write_b32 a142, v46\n\t" \
  "v_accvgpr_write_b32 a143, v47\n\t" \
  "v_perm_b32 v44, v182, v178, s62\n\t" \
  "v_perm_b32 v45, v190, v186, s62\n\t" \
  "v_perm_b32 v46, v198, v194, s62\n\t" \
  "v_perm_b32 v47, v206, v202, s62\n\t" \
  "v_accvgpr_write_b32 a144, v44\n\t" \
  "v_accvgpr_write_b32 a145, v45\n\t" \
  "v_accvgpr_write_b32 a146, v46\n\t" \
  "v_accvgpr_write_b32 a147, v47\n\t" \
  "v_perm_b32 v44, v182, v178, s63\n\t" \
  "v_perm_b32 v45, v190, v186, s63\n\t" \
  "v_perm_b32 v46, v198, v194, s63\n\t" \
  "v_perm_b32 v47, v206, v202, s63\n\t" \
  "v_accvgpr_write_b32 a148, v44\n\t" \
  "v_accvgpr_write_b32 a149, v45\n\t" \
  "v_accvgpr_write_b32 a150, v46\n\t" \
  "v_accvgpr_write_b32 a151, v47\n\t" \
  "v_perm_b32 v44, v183, v179, s62\n\t" \
  "v_perm_b32 v45, v191, v187, s62\n\t" \
  "v_perm_b32 v46, v199, v195, s62\n\t" \
  "v_perm_b32 v47, v207, v203, s62\n\t" \
  "v_accvgpr_write_b32 a152, v44\n\t" \
  "v_accvgpr_write_b32 a153, v45\n\t" \
  "v_accvgpr_write_b32 a154, v46\n\t" \
  "v_accvgpr_write_b32 a155, v47\n\t" \
  "v_perm_b32 v44, v183, v179, s63\n\t" \
  "v_perm_b32 v45, v191, v187, s63\n\t" \
  "v_perm_b32 v46, v199, v195, s63\n\t" \
  "v_perm_b32 v47, v207, v203, s63\n\t" \
  "v_accvgpr_write_b32 a156, v44\n\t" \
  "v_accvgpr_write_b32 a157, v45\n\t" \
  "v_accvgpr_write_b32 a158, v46\n\t" \
  "v_accvgpr_write_b32 a159, v47\n\t" \
  "s_xor_b32 s60, s66, 0\n\t" \
  "s_add_u32 s60, s60, 73728\n\t" \
  "s_add_u32 s61, s60, 18432\n\t" \
  "buffer_load_ushort v232, v42, %[W], s60 offen offset:0\n\t" \
  "buffer_load_ushort v152, v42, %[W], s60 offen offset:1152\n\t" \
  "buffer_load_ushort v233, v42, %[W], s60 offen offset:2304\n\t" \
  "buffer_load_ushort v153, v42, %[W], s60 offen offset:3456\n\t" \
  "buffer_load_ushort v234, v42, %[W], s61 offen offset:0\n\t" \
  "buffer_load_ushort v154, v42, %[W], s61 offen offset:1152\n\t" \
  "buffer_load_ushort v235, v42, %[W], s61 offen offset:2304\n\t" \
  "buffer_load_ushort v155, v42, %[W], s61 offen offset:3456\n\t" \
  "s_xor_b32 s60, s66, 0\n\t" \
  "s_add_u32 s60, s60, 110592\n\t" \
  "s_add_u32 s61, s60, 18432\n\t" \
  "buffer_load_ushort v236, v42, %[W], s60 offen offset:0\n\t" \
  "buffer_load_ushort v156, v42, %[W], s60 offen offset:1152\n\t" \
  "buffer_load_ushort v237, v42, %[W], s60 offen offset:2304\n\t" \
  "buffer_load_ushort v157, v42, %[W], s60 offen offset:3456\n\t" \
  "buffer_load_ushort v238, v42, %[W], s61 offen offset:0\n\t" \
  "buffer_load_ushort v158, v42, %[W], s61 offen offset:1152\n\t" \
  "buffer_load_ushort v239, v42, %[W], s61 offen offset:2304\n\t" \
  "buffer_load_ushort v159, v42, %[W], s61 offen offset:3456\n\t" \
  "s_waitcnt vmcnt(54)\n\t" \
  "v_perm_b32 v44, v52, v48, s62\n\t" \
  "s_waitcnt vmcnt(52)\n\t" \
  "v_perm_b32 v45, v60, v56, s62\n\t" \
  "s_waitcnt vmcnt(50)\n\t" \
  "v_perm_b32 v46, v68, v64, s62\n\t" \
  "s_waitcnt vmcnt(48)\n\t" \
  "v_perm_b32 v47, v76, v72, s62\n\t" \
  "v_accvgpr_write_b32 a160, v44\n\t" \
  "v_accvgpr_write_b32 a161, v45\n\t" \
  "v_accvgpr_write_b32 a162, v46\n\t" \
  "v_accvgpr_write_b32 a163, v47\n\t" \
  "v_perm_b32 v44, v52, v48, s63\n\t" \
  "v_perm_b32 v45, v60, v56, s63\n\t" \
  "v_perm_b32 v46, v68, v64, s63\n\t" \
  "v_perm_b32 v47, v76, v72, s63\n\t" \
  "v_accvgpr_write_b32 a164, v44\n\t" \
  "v_accvgpr_write_b32 a165, v45\n\t" \
  "v_accvgpr_write_b32 a166, v46\n\t" \
  "v_accvgpr_write_b32 a167, v47\n\t" \
  "v_perm_b32 v44, v53, v49, s62\n\t" \
  "v_perm_b32 v45, v61, v57, s62\n\t" \
  "v_perm_b32 v46, v69, v65, s62\n\t" \
  "v_perm_b32 v47, v77, v73, s62\n\t" \
  "v_accvgpr_write_b32 a168, v44\n\t" \
  "v_accvgpr_write_b32 a169, v45\n\t" \
  "v_accvgpr_write_b32 a170, v46\n\t" \
  "v_accvgpr_write_b32 a171, v47\n\t" \
  "v_perm_b32 v44, v53, v49, s63\n\t" \
  "v_perm_b32 v45, v61, v57, s63\n\t" \
  "v_perm_b32 v46, v69, v65, s63\n\t" \
  "v_perm_b32 v47, v77, v73, s63\n\t" \
  "v_accvgpr_write_b32 a172, v44\n\t" \
  "v_accvgpr_write_b32 a173, v45\n\t" \
  "v_accvgpr_write_b32 a174, v46\n\t" \
  "v_accvgpr_write_b32 a175, v47\n\t" \
  "v_perm_b32 v44, v54, v50, s62\n\t" \
  "v_perm_b32 v45, v62, v58, s62\n\t" \
  "v_perm_b32 v46, v70, v66, s62\n\t" \
  "v_perm_b32 v47, v78, v74, s62\n\t" \
  "v_accvgpr_write_b32 a176, v44\n\t" \
  "v_accvgpr_write_b32 a177, v45\n\t" \
  "v_accvgpr_write_b32 a178, v46\n\t" \
  "v_accvgpr_write_b32 a179, v47\n\t" \
  "v_perm_b32 v44, v54, v50, s63\n\t" \
  "v_perm_b32 v45, v62, v58, s63\n\t" \
  "v_perm_b32 v46, v70, v66, s63\n\t" \
  "v_perm_b32 v47, v78, v74, s63\n\t" \
  "v_accvgpr_write_b32 a180, v44\n\t" \
  "v_accvgpr_write_b32 a181, v45\n\t" \
  "v_accvgpr_write_b32 a182, v46\n\t" \
  "v_accvgpr_write_b32 a183, v47\n\t" \
  "v_perm_b32 v44, v55, v51, s62\n\t" \
  "v_perm_b32 v45, v63, v59, s62\n\t" \
  "v_perm_b32 v46, v71, v67, s62\n\t" \
  "v_perm_b32 v47, v79, v75, s62\n\t" \
  "v_accvgpr_write_b32 a184, v44\n\t" \
  "v_accvgpr_write_b32 a185, v45\n\t" \
  "v_accvgpr_write_b32 a186, v46\n\t" \
  "v_accvgpr_write_b32 a187, v47\n\t" \
  "v_perm_b32 v44, v55, v51, s63\n\t" \
  "v_perm_b32 v45, v63, v59, s63\n\t" \
  "v_perm_b32 v46, v71, v67, s63\n\t" \
  "v_perm_b32 v47, v79, v75, s63\n\t" \
  "v_accvgpr_write_b32 a188, v44\n\t" \
  "v_accvgpr_write_b32 a189, v45\n\t" \
  "v_accvgpr_write_b32 a190, v46\n\t" \
  "v_accvgpr_write_b32 a191, v47\n\t" \
  "s_xor_b32 s60, s66, 147456\n\t" \
  "s_add_u32 s60, s60, 0\n\t" \
  "s_add_u32 s61, s60, 18432\n\t" \
  "buffer_load_ushort v240, v42, %[W], s60 offen offset:0\n\t" \
  "buffer_load_ushort v160, v42, %[W], s60 offen offset:1152\n\t" \
  "buffer_load_ushort v241, v42, %[W], s60 offen offset:2304\n\t" \
  "buffer_load_ushort v161, v42, %[W], s60 offen offset:3456\n\t" \
  "buffer_load_ushort v242, v42, %[W], s61 offen offset:0\n\t" \
  "buffer_load_ushort v162, v42, %[W], s61 offen offset:1152\n\t" \
  "buffer_load_ushort v243, v42, %[W], s61 offen offset:2304\n\t" \
  "buffer_load_ushort v163, v42, %[W], s61 offen offset:3456\n\t" \
  "s_waitcnt vmcnt(54)\n\t" \
  "v_perm_b32 v44, v84, v80, s62\n\t" \
  "s_waitcnt vmcnt(52)\n\t" \
  "v_perm_b32 v45, v92, v88, s62\n\t" \
  "s_waitcnt vmcnt(50)\n\t" \
  "v_perm_b32 v46, v100, v96, s62\n\t" \
  "s_waitcnt vmcnt(48)\n\t" \
  "v_perm_b32 v47, v108, v104, s62\n\t" \
  "v_accvgpr_write_b32 a192, v44\n\t" \
  "v_accvgpr_write_b32 a193, v45\n\t" \
  "v_accvgpr_write_b32 a194, v46\n\t" \
  "v_accvgpr_write_b32 a195, v47\n\t" \
  "v_perm_b32 v44, v84, v80, s63\n\t" \
  "v_perm_b32 v45, v92, v88, s63\n\t" \
  "v_perm_b32 v46, v100, v96, s63\n\t" \
  "v_perm_b32 v47, v108, v104, s63\n\t" \
  "v_accvgpr_write_b32 a196, v44\n\t" \
  "v_accvgpr_write_b32 a197, v45\n\t" \
  "v_accvgpr_write_b32 a198, v46\n\t" \
  "v_accvgpr_write_b32 a199, v47\n\t" \
  "v_perm_b32 v44, v85, v81, s62\n\t" \
  "v_perm_b32 v45, v93, v89, s62\n\t" \
  "v_perm_b32 v46, v101, v97, s62\n\t" \
  "v_perm_b32 v47, v109, v105, s62\n\t" \
  "v_accvgpr_write_b32 a200, v44\n\t" \
  "v_accvgpr_write_b32 a201, v45\n\t" \
  "v_accvgpr_write_b32 a202, v46\n\t" \
  "v_accvgpr_write_b32 a203, v47\n\t" \
  "v_perm_b32 v44, v85, v81, s63\n\t" \
  "v_perm_b32 v45, v93, v89, s63\n\t" \
  "v_perm_b32 v46, v101, v97, s63\n\t" \
  "v_perm_b32 v47, v109, v105, s63\n\t" \
  "v_accvgpr_write_b32 a204, v44\n\t" \
  "v_accvgpr_write_b32 a205, v45\n\t" \
  "v_accvgpr_write_b32 a206, v46\n\t" \
  "v_accvgpr_write_b32 a207, v47\n\t" \
  "v_perm_b32 v44, v86, v82, s62\n\t" \
  "v_perm_b32 v45, v94, v90, s62\n\t" \
  "v_perm_b32 v46, v102, v98, s62\n\t" \
  "v_perm_b32 v47, v110, v106, s62\n\t" \
  "v_accvgpr_write_b32 a208, v44\n\t" \
  "v_accvgpr_write_b32 a209, v45\n\t" \
  "v_accvgpr_write_b32 a210, v46\n\t" \
  "v_accvgpr_write_b32 a211, v47\n\t" \
  "v_perm_b32 v44, v86, v82, s63\n\t" \
  "v_perm_b32 v45, v94, v90, s63\n\t" \
  "v_perm_b32 v46, v102, v98, s63\n\t" \
  "v_perm_b32 v47, v110, v106, s63\n\t" \
  "v_accvgpr_write_b32 a212, v44\n\t" \
  "v_accvgpr_write_b32 a213, v45\n\t" \
  "v_accvgpr_write_b32 a214, v46\n\t" \
  "v_accvgpr_write_b32 a215, v47\n\t" \
  "v_perm_b32 v44, v87, v83, s62\n\t" \
  "v_perm_b32 v45, v95, v91, s62\n\t" \
  "v_perm_b32 v46, v103, v99, s62\n\t" \
  "v_perm_b32 v47, v111, v107, s62\n\t" \
  "v_accvgpr_write_b32 a216, v44\n\t" \
  "v_accvgpr_write_b32 a217, v45\n\t" \
  "v_accvgpr_write_b32 a218, v46\n\t" \
  "v_accvgpr_write_b32 a219, v47\n\t" \
  "v_perm_b32 v44, v87, v83, s63\n\t" \
  "v_perm_b32 v45, v95, v91, s63\n\t" \
  "v_perm_b32 v46, v103, v99, s63\n\t" \
  "v_perm_b32 v47, v111, v107, s63\n\t" \
  "v_accvgpr_write_b32 a220, v44\n\t" \
  "v_accvgpr_write_b32 a221, v45\n\t" \
  "v_accvgpr_write_b32 a222, v46\n\t" \
  "v_accvgpr_write_b32 a223, v47\n\t" \
  "s_xor_b32 s60, s66, 147456\n\t" \
  "s_add_u32 s60, s60, 36864\n\t" \
  "s_add_u32 s61, s60, 18432\n\t" \
  "buffer_load_ushort v244, v42, %[W], s60 offen offset:0\n\t" \
  "buffer_load_ushort v164, v42, %[W], s60 offen offset:1152\n\t" \
  "buffer_load_ushort v245, v42, %[W], s60 offen offset:2304\n\t" \
  "buffer_load_ushort v165, v42, %[W], s60 offen offset:3456\n\t" \
  "buffer_load_ushort v246, v42, %[W], s61 offen offset:0\n\t" \
  "buffer_load_ushort v166, v42, %[W], s61 offen offset:1152\n\t" \
  "buffer_load_ushort v247, v42, %[W], s61 offen offset:2304\n\t" \
  "buffer_load_ushort v167, v42, %[W], s61 offen offset:3456\n\t" \
  "s_waitcnt vmcnt(54)\n\t" \
  "v_perm_b32 v44, v116, v112, s62\n\t" \
  "s_waitcnt vmcnt(52)\n\t" \
  "v_perm_b32 v45, v124, v120, s62\n\t" \
  "s_waitcnt vmcnt(50)\n\t" \
  "v_perm_b32 v46, v132, v128, s62\n\t" \
  "s_waitcnt vmcnt(48)\n\t" \
  "v_perm_b32 v47, v140, v136, s62\n\t" \
  "v_accvgpr_write_b32 a224, v44\n\t" \
  "v_accvgpr_write_b32 a225, v45\n\t" \
  "v_accvgpr_write_b32 a226, v46\n\t" \
  "v_accvgpr_write_b32 a227, v47\n\t" \
  "v_perm_b32 v44, v116, v112, s63\n\t" \
  "v_perm_b32 v45, v124, v120, s63\n\t" \
  "v_perm_b32 v46, v132, v128, s63\n\t" \
  "v_perm_b32 v47, v140, v136, s63\n\t" \
  "v_accvgpr_write_b32 a228, v44\n\t" \
  "v_accvgpr_write_b32 a229, v45\n\t" \
  "v_accvgpr_write_b32 a230, v46\n\t" \
  "v_accvgpr_write_b32 a231, v47\n\t" \
  "v_perm_b32 v44, v117, v113, s62\n\t" \
  "v_perm_b32 v45, v125, v121, s62\n\t" \
  "v_perm_b32 v46, v133, v129, s62\n\t" \
  "v_perm_b32 v47, v141, v137, s62\n\t" \
  "v_accvgpr_write_b32 a232, v44\n\t" \
  "v_accvgpr_write_b32 a233, v45\n\t" \
  "v_accvgpr_write_b32 a234, v46\n\t" \
  "v_accvgpr_write_b32 a235, v47\n\t" \
  "v_perm_b32 v44, v117, v113, s63\n\t" \
  "v_perm_b32 v45, v125, v121, s63\n\t" \
  "v_perm_b32 v46, v133, v129, s63\n\t" \
  "v_perm_b32 v47, v141, v137, s63\n\t" \
  "v_accvgpr_write_b32 a236, v44\n\t" \
  "v_accvgpr_write_b32 a237, v45\n\t" \
  "v_accvgpr_write_b32 a238, v46\n\t" \
  "v_accvgpr_write_b32 a239, v47\n\t" \
  "v_perm_b32 v44, v118, v114, s62\n\t" \
  "v_perm_b32 v45, v126, v122, s62\n\t" \
  "v_perm_b32 v46, v134, v130, s62\n\t" \
  "v_perm_b32 v47, v142, v138, s62\n\t" \
  "v_accvgpr_write_b32 a240, v44\n\t" \
  "v_accvgpr_write_b32 a241, v45\n\t" \
  "v_accvgpr_write_b32 a242, v46\n\t" \
  "v_accvgpr_write_b32 a243, v47\n\t" \
  "v_perm_b32 v44, v118, v114, s63\n\t" \
  "v_perm_b32 v45, v126, v122, s63\n\t" \
  "v_perm_b32 v46, v134, v130, s63\n\t" \
  "v_perm_b32 v47, v142, v138, s63\n\t" \
  "v_accvgpr_write_b32 a244, v44\n\t" \
  "v_accvgpr_write_b32 a245, v45\n\t" \
  "v_accvgpr_write_b32 a246, v46\n\t" \
  "v_accvgpr_write_b32 a247, v47\n\t" \
  "v_perm_b32 v44, v119, v115, s62\n\t" \
  "v_perm_b32 v45, v127, v123, s62\n\t" \
  "v_perm_b32 v46, v135, v131, s62\n\t" \
  "v_perm_b32 v47, v143, v139, s62\n\t" \
  "v_accvgpr_write_b32 a248, v44\n\t" \
  "v_accvgpr_write_b32 a249, v45\n\t" \
  "v_accvgpr_write_b32 a250, v46\n\t" \
  "v_accvgpr_write_b32 a251, v47\n\t" \
  "v_perm_b32 v44, v119, v115, s63\n\t" \
  "v_perm_b32 v45, v127, v123, s63\n\t" \
  "v_perm_b32 v46, v135, v131, s63\n\t" \
  "v_perm_b32 v47, v143, v139, s63\n\t" \
  "v_accvgpr_write_b32 a252, v44\n\t" \
  "v_accvgpr_write_b32 a253, v45\n\t" \
  "v_accvgpr_write_b32 a254, v46\n\t" \
  "v_accvgpr_write_b32 a255, v47\n\t" \
  "s_xor_b32 s60, s66, 147456\n\t" \
  "s_add_u32 s60, s60, 73728\n\t" \
  "s_add_u32 s61, s60, 18432\n\t" \
  "buffer_load_ushort v248, v42, %[W], s60 offen offset:0\n\t" \
  "buffer_load_ushort v168, v42, %[W], s60 offen offset:1152\n\t" \
  "buffer_load_ushort v249, v42, %[W], s60 offen offset:2304\n\t" \
  "buffer_load_ushort v169, v42, %[W], s60 offen offset:3456\n\t" \
  "buffer_load_ushort v250, v42, %[W], s61 offen offset:0\n\t" \
  "buffer_load_ushort v170, v42, %[W], s61 offen offset:1152\n\t" \
  "buffer_load_ushort v251, v42, %[W], s61 offen offset:2304\n\t" \
  "buffer_load_ushort v171, v42, %[W], s61 offen offset:3456\n\t" \
  "s_xor_b32 s60, s66, 147456\n\t" \
  "s_add_u32 s60, s60, 110592\n\t" \
  "s_add_u32 s61, s60, 18432\n\t" \
  "s_waitcnt vmcnt(40)\n\t" \
  "buffer_load_ushort v252, v42, %[W], s60 offen offset:0\n\t" \
  "buffer_load_ushort v172, v42, %[W], s60 offen offset:1152\n\t" \
  "buffer_load_ushort v253, v42, %[W], s60 offen offset:2304\n\t" \
  "buffer_load_ushort v173, v42, %[W], s60 offen offset:3456\n\t" \
  "buffer_load_ushort v254, v42, %[W], s61 offen offset:0\n\t" \
  "buffer_load_ushort v174, v42, %[W], s61 offen offset:1152\n\t" \
  "buffer_load_ushort v255, v42, %[W], s61 offen offset:2304\n\t" \
  "buffer_load_ushort v175, v42, %[W], s61 offen offset:3456\n\t" \
  "v_lshl_or_b32 v224, v144, 16, v224\n\t" \
  "v_lshl_or_b32 v225, v145, 16, v225\n\t" \
  "v_lshl_or_b32 v226, v146, 16, v226\n\t" \
  "v_lshl_or_b32 v227, v147, 16, v227\n\t" \
  "v_lshl_or_b32 v228, v148, 16, v228\n\t" \
  "v_lshl_or_b32 v229, v149, 16, v229\n\t" \
  "v_lshl_or_b32 v230, v150, 16, v230\n\t" \
  "v_lshl_or_b32 v231, v151, 16, v231\n\t" \
  "s_waitcnt vmcnt(46)\n\t" \
  "v_lshl_or_b32 v232, v152, 16, v232\n\t" \
  "s_waitcnt vmcnt(44)\n\t" \
  "v_lshl_or_b32 v233, v153, 16, v233\n\t" \
  "s_waitcnt vmcnt(42)\n\t" \
  "v_lshl_or_b32 v234, v154, 16, v234\n\t" \
  "s_waitcnt vmcnt(40)\n\t" \
  "v_lshl_or_b32 v235, v155, 16, v235\n\t" \
  "s_waitcnt vmcnt(38)\n\t" \
  "v_lshl_or_b32 v236, v156, 16, v236\n\t" \
  "s_waitcnt vmcnt(36)\n\t" \
  "v_lshl_or_b32 v237, v157, 16, v237\n\t" \
  "s_waitcnt vmcnt(34)\n\t" \
  "v_lshl_or_b32 v238, v158, 16, v238\n\t" \
  "s_waitcnt vmcnt(32)\n\t" \
  "v_lshl_or_b32 v239, v159, 16, v239\n\t" \
  "s_waitcnt vmcnt(30)\n\t" \
  "v_lshl_or_b32 v240, v160, 16, v240\n\t" \
  "s_waitcnt vmcnt(28)\n\t" \
  "v_lshl_or_b32 v241, v161, 16, v241\n\t" \
  "s_waitcnt vmcnt(26)\n\t" \
  "v_lshl_or_b32 v242, v162, 16, v242\n\t" \
  "s_waitcnt vmcnt(24)\n\t" \
  "v_lshl_or_b32 v243, v163, 16, v243\n\t" \
  "s_waitcnt vmcnt(22)\n\t" \
  "v_lshl_or_b32 v244, v164, 16, v244\n\t" \
  "s_waitcnt vmcnt(20)\n\t" \
  "v_lshl_or_b32 v245, v165, 16, v245\n\t" \
  "s_waitcnt vmcnt(18)\n\t" \
  "v_lshl_or_b32 v246, v166, 16, v246\n\t" \
  "s_waitcnt vmcnt(16)\n\t" \
  "v_lshl_or_b32 v247, v167, 16, v247\n\t" \
  "s_waitcnt vmcnt(14)\n\t" \
  "v_lshl_or_b32 v248, v168, 16, v248\n\t" \
  "s_waitcnt vmcnt(12)\n\t" \
  "v_lshl_or_b32 v249, v169, 16, v249\n\t" \
  "s_waitcnt vmcnt(10)\n\t" \
  "v_lshl_or_b32 v250, v170, 16, v250\n\t" \
  "s_waitcnt vmcnt(8)\n\t" \
  "v_lshl_or_b32 v251, v171, 16, v251\n\t" \
  "s_waitcnt vmcnt(6)\n\t" \
  "v_lshl_or_b32 v252, v172, 16, v252\n\t" \
  "s_waitcnt vmcnt(4)\n\t" \
  "v_lshl_or_b32 v253, v173, 16, v253\n\t" \
  "s_waitcnt vmcnt(2)\n\t" \
  "v_lshl_or_b32 v254, v174, 16, v254\n\t" \
  "s_waitcnt vmcnt(0)\n\t" \
  "v_lshl_or_b32 v255, v175, 16, v255\n\t" \
  "s_mov_b32 s84, " TK_CAP "\n\t" \
  "s_mov_b32 s48, 0\n\t" \
  "s_mov_b32 s49, 0xf0000000\n\t" \
  "s_mov_b32 s46, s45\n\t" \
  "v_mov_b32 v5, 0\n\t" \
  "v_lshrrev_b32 v13, 2, v0\n\t" \
  "s_mov_b32 s60, " TK_CTAB "\n\t" \
  "buffer_load_dword v10, v13, %[X], s60 offen offset:88 sc1\n\t" \
  "buffer_load_dword v13, v13, %[X], s60 offen offset:344 sc1\n\t" \
  "s_waitcnt vmcnt(0)\n\t" \
  "v_and_b32 v10, 1, v10\n\t" \
  "v_and_b32 v13, 1, v13\n\t" \
  "v_cmp_ne_u32 s[90:91], s67, v10\n\t" \
  "v_cmp_ne_u32 s[88:89], s67, v13\n\t" \
  "s_nop 1\n\t" \
  "s_and_b32 s60, s88, 256\n\t" \
  "s_lshl_b32 s60, s60, 5\n\t" \
  "s_add_u32 s44, s44, s60\n\t" \
  "s_add_u32 s83, s48, 0\n\t" \
  "s_lshr_b64 s[60:61], s[90:91], s83\n\t" \
  "s_and_b32 s60, s60, 1\n\t" \
  "s_lshl_b32 s60, s60, 13\n\t" \
  "s_add_u32 s83, s83, " TK_SG0 "\n\t" \
  "s_lshl_b32 s83, s83, 21\n\t" \
  "s_or_b32 s83, s83, s60\n\t" \
  "LCOLD_%=:\n\t" \
  "s_add_u32 s62, s83, s41\n\t" \
  "s_mov_b32 s61, 0\n\t" \
  "LPQ_%=:\n\t" \
  "buffer_load_dword v10, v9, %[X], s62 offen sc1\n\t" \
  "s_waitcnt vmcnt(0)\n\t" \
  "v_cmp_eq_u32 vcc, 0, v10\n\t" \
  "s_cbranch_vccz LPQ_%=x\n\t" \
  "s_add_u32 s61, s61, 1\n\t" \
  "s_sleep 4\n\t" \
  "s_cmp_gt_u32 s61, s84\n\t" \
  "s_cbranch_scc1 LPQ_%=g\n\t" \
  "s_and_b32 s60, s61, 1023\n\t" \
  "s_cmp_lg_u32 s60, 0\n\t" \
  "s_cbranch_scc1 LPQ_%=\n\t" \
  "buffer_load_dword v10, off, %[X], s92 sc1\n\t" \
  "s_waitcnt vmcnt(0)\n\t" \
  "v_readfirstlane_b32 s60, v10\n\t" \
  "s_nop 0\n\t" \
  "s_cmp_eq_u32 s60, 0\n\t" \
  "s_cbranch_scc1 LPQ_%=\n\t" \
  "LPQ_%=g:\n\t" \
  "s_mov_b32 s84, 0\n\t" \
  "s_or_b32 s60, s48, 1073741824\n\t" \
  "v_mov_b32 v10, s60\n\t" \
  "s_mov_b64 exec, 1\n\t" \
  "buffer_store_dword v10, off, %[X], s92 sc1\n\t" \
  "s_mov_b64 exec, -1\n\t" \
  "LPQ_%=x:\n\t" \
  "s_add_u32 m0, s46, 0\n\t" \
  "s_add_u32 s47, s83, s36\n\t" \
  "buffer_load_dwordx4 v0, %[X], s47 offen offset:0 sc1 lds\n\t" \
  "buffer_load_dwordx4 v0, %[X], s47 offen offset:1024 sc1 lds\n\t" \
  "buffer_load_dwordx4 v0, %[X], s47 offen offset:2048 sc1 lds\n\t" \
  "buffer_load_dwordx4 v0, %[X], s47 offen offset:3072 sc1 lds\n\t" \
  "s_add_u32 m0, s46, 4096\n\t" \
  "s_add_u32 s47, s83, s37\n\t" \
  "buffer_load_dwordx4 v0, %[X], s47 offen offset:0 sc1 lds\n\t" \
  "buffer_load_dwordx4 v0, %[X], s47 offen offset:1024 sc1 lds\n\t" \
  "buffer_load_dwordx4 v0, %[X], s47 offen offset:2048 sc1 lds\n\t" \
  "buffer_load_dwordx4 v0, %[X], s47 offen offset:3072 sc1 lds\n\t" \
  "s_add_u32 m0, s46, 8192\n\t" \
  "s_add_u32 s47, s83, s38\n\t" \
  "buffer_load_dwordx4 v0, %[X], s47 offen offset:0 sc1 lds\n\t" \
  "buffer_load_dwordx4 v0, %[X], s47 offen offset:1024 sc1 lds\n\t" \
  "buffer_load_dwordx4 v0, %[X], s47 offen offset:2048 sc1 lds\n\t" \
  "buffer_load_dwordx4 v0, %[X], s47 offen offset:3072 sc1 lds\n\t" \
  "s_waitcnt vmcnt(0)\n\t" \
  "LTOP_%=:\n\t" \
  TK_AC0 \
  "s_barrier\n\t" \
  TK_AC1 \
  "ds_read_b128 v[48:51], v2 offset:0\n\t" \
  "ds_read_b128 v[64:67], v1 offset:8192\n\t" \
  "ds_read_b128 v[80:83], v1 offset:12288\n\t" \
  "ds_read_b128 v[52:55], v2 offset:1024\n\t" \
  "ds_read_b128 v[68:71], v1 offset:9216\n\t" \
  "ds_read_b128 v[84:87], v1 offset:13312\n\t" \
  "ds_read_b128 v[56:59], v2 offset:2048\n\t" \
  "ds_read_b128 v[72:75], v1 offset:10240\n\t" \
  "ds_read_b128 v[88:91], v1 offset:14336\n\t" \
  "ds_read_b128 v[60:63], v2 offset:3072\n\t" \
  "ds_read_b128 v[76:79], v1 offset:11264\n\t" \
  "ds_read_b128 v[92:95], v1 offset:15360\n\t" \
  "v_readfirstlane_b32 s57, v5\n\t" \
  "s_mov_b32 s82, s83\n\t" \
  "s_xor_b32 s46, s46, 0x10000\n\t" \
  "s_add_u32 s58, s82, s41\n\t" \
  "s_add_u32 s58, s58, 4096\n\t" \
  "s_add_u32 s59, s82, s39\n\t" \
  "s_cmp_eq_u32 s57, 0\n\t" \
  "s_cbranch_scc1 LNG_%=\n\t" \
  "buffer_load_dwordx4 v[196:199], v0, %[X], s59 offen offset:0 sc1\n\t" \
  "buffer_load_dwordx4 v[200:203], v0, %[X], s59 offen offset:1024 sc1\n\t" \
  "buffer_load_dwordx2 v[204:205], v6, %[X], s59 offen offset:2048 sc1\n\t" \
  "buffer_load_dwordx2 v[206:207], v6, %[X], s59 offen offset:2560 sc1\n\t" \
  "buffer_load_dwordx2 v[208:209], v6, %[X], s59 offen offset:3072 sc1\n\t" \
  "buffer_load_dwordx2 v[210:211], v6, %[X], s59 offen offset:3584 sc1\n\t" \
  "LNG_%=:\n\t" \
  "s_waitcnt lgkmcnt(10)\n\t" \
  "v_mfma_f32_16x16x32_bf16 v[36:39], v[64:67], v[48:51], v[16:19]\n\t" \
  "ds_read_b128 v[96:99], v1 offset:16384\n\t" \
  "s_waitcnt lgkmcnt(10)\n\t" \
  "v_mfma_f32_16x16x32_bf16 v[40:43], v[80:83], v[48:51], v[20:23]\n\t" \
  "ds_read_b128 v[100:103], v1 offset:17408\n\t" \
  "s_waitcnt lgkmcnt(9)\n\t" \
  "v_mfma_f32_16x16x32_bf16 v[36:39], v[68:71], v[52:55], v[36:39]\n\t" \
  "ds_read_b128 v[104:107], v1 offset:18432\n\t" \
  "s_waitcnt lgkmcnt(9)\n\t" \
  "v_mfma_f32_16x16x32_bf16 v[40:43], v[84:87], v[52:55], v[40:43]\n\t" \
  "ds_read_b128 v[108:111], v1 offset:19456\n\t" \
  "s_waitcnt lgkmcnt(8)\n\t" \
  "v_mfma_f32_16x16x32_bf16 v[36:39], v[72:75], v[56:59], v[36:39]\n\t" \
  "ds_read_b128 v[112:115], v1 offset:20480\n\t" \
  "s_waitcnt lgkmcnt(8)\n\t" \
  "v_mfma_f32_16x16x32_bf16 v[40:43], v[88:91], v[56:59], v[40:43]\n\t" \
  "ds_read_b128 v[116:119], v1 offset:21504\n\t" \
  "s_waitcnt lgkmcnt(7)\n\t" \
  "v_mfma_f32_16x16x32_bf16 v[36:39], v[76:79], v[60:63], v[36:39]\n\t" \
  "ds_read_b128 v[120:123], v1 offset:22528\n\t" \
  "s_waitcnt lgkmcnt(7)\n\t" \
  "v_mfma_f32_16x16x32_bf16 v[40:43], v[92:95], v[60:63], v[40:43]\n\t" \
  "ds_read_b128 v[124:127], v1 offset:23552\n\t" \
  "s_add_u32 s83, s48, 1\n\t" \
  "s_lshr_b64 s[60:61], s[90:91], s83\n\t" \
  "s_and_b32 s60, s60, 1\n\t" \
  "s_lshl_b32 s60, s60, 13\n\t" \
  "s_add_u32 s83, s83, " TK_SG0 "\n\t" \
  "s_lshl_b32 s83, s83, 21\n\t" \
  "s_or_b32 s83, s83, s60\n\t" \
  "v_xor_b32 v1, 0x10000, v1\n\t" \
  "v_xor_b32 v2, 0x10000, v2\n\t" \
  "v_max3_f32 v11, v36, v37, v38\n\t" \
  "v_max3_f32 v11, v11, v39, v40\n\t" \
  "v_max3_f32 v13, v41, v42, v43\n\t" \
  "v_max_f32 v11, v11, v13\n\t" \
  "v_mov_b32 v13, v11\n\t" \
  "s_nop 1\n\t" \
  "v_permlane16_swap_b32 v11, v13\n\t" \
  "v_max_f32 v11, v11, v13\n\t" \
  "v_mov_b32 v13, v11\n\t" \
  "s_nop 1\n\t" \
  "v_permlane32_swap_b32 v11, v13\n\t" \
  "v_max_f32 v11, v11, v13\n\t" \
  "v_mul_f32 v13, 0xbe0293ee, v11\n\t" \
  "v_fmamk_f32 v36, v36, 0x3e0293ee, v13\n\t" \
  "v_fmamk_f32 v37, v37, 0x3e0293ee, v13\n\t" \
  "v_fmamk_f32 v38, v38, 0x3e0293ee, v13\n\t" \
  "v_fmamk_f32 v39, v39, 0x3e0293ee, v13\n\t" \
  "v_fmamk_f32 v40, v40, 0x3e0293ee, v13\n\t" \
  "v_fmamk_f32 v41, v41, 0x3e0293ee, v13\n\t" \
  "v_fmamk_f32 v42, v42, 0x3e0293ee, v13\n\t" \
  "v_fmamk_f32 v43, v43, 0x3e0293ee, v13\n\t" \
  "s_waitcnt vmcnt(0)\n\t" \
  "s_barrier\n\t" \
  "buffer_load_dword v5, off, %[X], s58 sc1\n\t" \
  "s_mov_b64 exec, 1\n\t" \
  "buffer_store_dword v3, off, %[X], s49 sc1\n\t" \
  "s_mov_b64 exec, -1\n\t" \
  "s_mov_b32 s49, 0xf0000000\n\t" \
  "v_exp_f32 v36, v36\n\t" \
  "v_exp_f32 v37, v37\n\t" \
  "v_exp_f32 v38, v38\n\t" \
  "v_exp_f32 v39, v39\n\t" \
  "v_exp_f32 v40, v40\n\t" \
  "v_exp_f32 v41, v41\n\t" \
  "v_exp_f32 v42, v42\n\t" \
  "v_exp_f32 v43, v43\n\t" \
  "v_cvt_pk_bf16_f32 v44, v36, v37\n\t" \
  "v_cvt_pk_bf16_f32 v45, v38, v39\n\t" \
  "v_cvt_pk_bf16_f32 v46, v40, v41\n\t" \
  "v_cvt_pk_bf16_f32 v47, v42, v43\n\t" \
  "v_add_f32 v36, v36, v37\n\t" \
  "v_add_f32 v38, v38, v39\n\t" \
  "s_waitcnt lgkmcnt(7)\n\t" \
  "v_mfma_f32_16x16x32_bf16 v[128:131], v[96:99], v[44:47], 0\n\t" \
  "v_add_f32 v40, v40, v41\n\t" \
  "s_and_b32 s62, s48, 15\n\t" \
  "s_lshr_b32 s63, s62, 1\n\t" \
  "s_waitcnt lgkmcnt(6)\n\t" \
  "v_mfma_f32_16x16x32_bf16 v[132:135], v[100:103], v[44:47], 0\n\t" \
  "v_add_f32 v42, v42, v43\n\t" \
  "s_lshr_b32 s60, s88, s63\n\t" \
  "s_and_b32 s60, s60, 1\n\t" \
  "s_waitcnt lgkmcnt(5)\n\t" \
  "v_mfma_f32_16x16x32_bf16 v[136:139], v[104:107], v[44:47], 0\n\t" \
  "v_add_f32 v36, v36, v38\n\t" \
  "s_lshl_b32 s60, s60, 13\n\t" \
  "s_lshl_b32 s61, s62, 20\n\t" \
  "s_waitcnt lgkmcnt(4)\n\t" \
  "v_mfma_f32_16x16x32_bf16 v[140:143], v[108:111], v[44:47], 0\n\t" \
  "v_add_f32 v40, v40, v42\n\t" \
  "s_add_u32 s50, s61, s60\n\t" \
  "s_add_u32 s50, s50, s43\n\t" \
  "s_waitcnt lgkmcnt(3)\n\t" \
  "v_mfma_f32_16x16x32_bf16 v[144:147], v[112:115], v[44:47], 0\n\t" \
  "v_add_f32 v12, v36, v40\n\t" \
  "s_add_u32 s51, s50, 32768\n\t" \
  "s_add_u32 s52, s51, 32768\n\t" \
  "s_waitcnt lgkmcnt(2)\n\t" \
  "v_mfma_f32_16x16x32_bf16 v[148:151], v[116:119], v[44:47], 0\n\t" \
  "s_add_u32 s53, s52, 32768\n\t" \
  "s_lshl_b32 s61, s62, 17\n\t" \
  "s_waitcnt lgkmcnt(1)\n\t" \
  "v_mfma_f32_16x16x32_bf16 v[152:155], v[120:123], v[44:47], 0\n\t" \
  "s_add_u32 s54, s61, s44\n\t" \
  "s_add_u32 s63, s83, s41\n\t" \
  "s_waitcnt lgkmcnt(0)\n\t" \
  "v_mfma_f32_16x16x32_bf16 v[156:159], v[124:127], v[44:47], 0\n\t" \
  TK_AC2 \
  "s_waitcnt vmcnt(1)\n\t" \
  "v_readfirstlane_b32 s60, v5\n\t" \
  TK_AC3 \
  TK_PA0 \
  "s_cmp_lg_u32 s57, 0\n\t" \
  "s_cbranch_scc1 LGOT_%=\n\t" \
  "s_cmp_lg_u32 s60, 0\n\t" \
  "s_cbranch_scc1 LPK_%=x\n\t" \
  "s_mov_b32 s61, 0\n\t" \
  "LPK_%=:\n\t" \
  "buffer_load_dword v5, off, %[X], s58 sc1\n\t" \
  "s_waitcnt vmcnt(0)\n\t" \
  "v_cmp_eq_u32 vcc, 0, v5\n\t" \
  "s_cbranch_vccz LPK_%=x\n\t" \
  "s_add_u32 s61, s61, 1\n\t" \
  "s_sleep " TK_PSK "\n\t" \
  "s_cmp_gt_u32 s61, s84\n\t" \
  "s_cbranch_scc1 LPK_%=g\n\t" \
  "s_and_b32 s60, s61, 1023\n\t" \
  "s_cmp_lg_u32 s60, 0\n\t" \
  "s_cbranch_scc1 LPK_%=\n\t" \
  "buffer_load_dword v5, off, %[X], s92 sc1\n\t" \
  "s_waitcnt vmcnt(0)\n\t" \
  "v_readfirstlane_b32 s60, v5\n\t" \
  "s_nop 0\n\t" \
  "s_cmp_eq_u32 s60, 0\n\t" \
  "s_cbranch_scc1 LPK_%=\n\t" \
  "LPK_%=g:\n\t" \
  "s_mov_b32 s84, 0\n\t" \
  "s_or_b32 s60, s48, 1342177280\n\t" \
  "v_mov_b32 v5, s60\n\t" \
  "s_mov_b64 exec, 1\n\t" \
  "buffer_store_dword v5, off, %[X], s92 sc1\n\t" \
  "s_mov_b64 exec, -1\n\t" \
  "LPK_%=x:\n\t" \
  TK_ADL \
  "buffer_load_dwordx4 v[196:199], v0, %[X], s59 offen offset:0 sc1\n\t" \
  "buffer_load_dwordx4 v[200:203], v0, %[X], s59 offen offset:1024 sc1\n\t" \
  "buffer_load_dwordx2 v[204:205], v6, %[X], s59 offen offset:2048 sc1\n\t" \
  "buffer_load_dwordx2 v[206:207], v6, %[X], s59 offen offset:2560 sc1\n\t" \
  "buffer_load_dwordx2 v[208:209], v6, %[X], s59 offen offset:3072 sc1\n\t" \
  "buffer_load_dwordx2 v[210:211], v6, %[X], s59 offen offset:3584 sc1\n\t" \
  "LGOT_%=:\n\t" \
  TK_PA1 \
  TK_AC4 \
  "buffer_load_dword v10, v9, %[X], s63 offen sc1\n\t" \
  "s_add_u32 s49, s82, s42\n\t" \
  "s_waitcnt vmcnt(6)\n\t" \
  "v_mfma_f32_16x16x32_bf16 v[36:39], v[196:199], v[48:51], 0\n\t" \
  "v_mfma_f32_16x16x32_bf16 v[40:43], v[196:199], v[52:55], 0\n\t" \
  "s_waitcnt vmcnt(5)\n\t" \
  "v_mfma_f32_16x16x32_bf16 v[36:39], v[200:203], v[56:59], v[36:39]\n\t" \
  "v_mfma_f32_16x16x32_bf16 v[40:43], v[200:203], v[60:63], v[40:43]\n\t" \
  TK_AC5 \
  "s_nop 8\n\t" \
  "v_add_f32 v36, v36, v42\n\t" \
  "v_add_f32 v37, v37, v43\n\t" \
  "v_max3_f32 v14, v36, v37, v11\n\t" \
  "v_mov_b32 v13, v14\n\t" \
  "s_nop 1\n\t" \
  "v_permlane16_swap_b32 v14, v13\n\t" \
  "v_max_f32 v14, v14, v13\n\t" \
  "v_mov_b32 v13, v14\n\t" \
  "s_nop 1\n\t" \
  "v_permlane32_swap_b32 v14, v13\n\t" \
  "v_max_f32 v14, v14, v13\n\t" \
  "v_mul_f32 v14, 0xbe0293ee, v14\n\t" \
  "v_fmamk_f32 v11, v11, 0x3e0293ee, v14\n\t" \
  "v_fmamk_f32 v36, v36, 0x3e0293ee, v14\n\t" \
  "v_fmamk_f32 v37, v37, 0x3e0293ee, v14\n\t" \
  "v_exp_f32 v11, v11\n\t" \
  "v_exp_f32 v36, v36\n\t" \
  "v_exp_f32 v37, v37\n\t" \
  "v_fma_f32 v12, v12, v11, v36\n\t" \
  "v_add_f32 v12, v12, v37\n\t" \
  "v_mov_b32 v13, v12\n\t" \
  "s_nop 1\n\t" \
  "v_permlane16_swap_b32 v12, v13\n\t" \
  "v_add_f32 v12, v12, v13\n\t" \
  "v_mov_b32 v13, v12\n\t" \
  "s_nop 1\n\t" \
  "v_permlane32_swap_b32 v12, v13\n\t" \
  "v_add_f32 v12, v12, v13\n\t" \
  "v_rcp_f32 v12, v12\n\t" \
  "s_nop 0\n\t" \
  "v_mul_f32 v14, v11, v12\n\t" \
  "v_mul_f32 v36, v36, v12\n\t" \
  "v_mul_f32 v37, v37, v12\n\t" \
  "v_cvt_pk_bf16_f32 v26, v36, v37\n\t" \
  "v_cvt_pk_bf16_f32 v33, v36, v37\n\t" \
  "v_pk_mul_f32 v[128:129], v[128:129], v[14:15] op_sel_hi:[1,0]\n\t" \
  "v_pk_mul_f32 v[130:131], v[130:131], v[14:15] op_sel_hi:[1,0]\n\t" \
  "v_pk_mul_f32 v[132:133], v[132:133], v[14:15] op_sel_hi:[1,0]\n\t" \
  "v_pk_mul_f32 v[134:135], v[134:135], v[14:15] op_sel_hi:[1,0]\n\t" \
  "v_pk_mul_f32 v[136:137], v[136:137], v[14:15] op_sel_hi:[1,0]\n\t" \
  "v_pk_mul_f32 v[138:139], v[138:139], v[14:15] op_sel_hi:[1,0]\n\t" \
  "v_pk_mul_f32 v[140:141], v[140:141], v[14:15] op_sel_hi:[1,0]\n\t" \
  "v_pk_mul_f32 v[142:143], v[142:143], v[14:15] op_sel_hi:[1,0]\n\t" \
  "s_waitcnt vmcnt(3)\n\t" \
  "v_mfma_f32_16x16x32_bf16 v[128:131], v[204:207], v[26:29], v[128:131]\n\t" \
  "v_mul_f32 v144, v144, v14\n\t" \
  "v_mul_f32 v145, v145, v14\n\t" \
  "v_mfma_f32_16x16x32_bf16 v[132:135], v[204:207], v[32:35], v[132:135]\n\t" \
  "v_mul_f32 v146, v146, v14\n\t" \
  "v_mul_f32 v147, v147, v14\n\t" \
  "v_mfma_f32_16x16x32_bf16 v[136:139], v[204:207], v[24:27], v[136:139]\n\t" \
  "v_mul_f32 v148, v148, v14\n\t" \
  "v_mul_f32 v149, v149, v14\n\t" \
  "v_mfma_f32_16x16x32_bf16 v[140:143], v[204:207], v[30:33], v[140:143]\n\t" \
  "v_cvt_pk_bf16_f32 v64, v128, v129\n\t" \
  "v_cvt_pk_bf16_f32 v65, v130, v131\n\t" \
  "v_cvt_pk_bf16_f32 v66, v132, v133\n\t" \
  "v_cvt_pk_bf16_f32 v67, v134, v135\n\t" \
  "v_mul_f32 v150, v150, v14\n\t" \
  "v_mul_f32 v151, v151, v14\n\t" \
  TK_AC6 \
  "v_mfma_f32_16x16x32_bf16 v[192:195], v[224:227], v[64:67], 0\n\t" \
  "ds_write_b128 v3, v[64:67] offset:0\n\t" \
  "v_cvt_pk_bf16_f32 v68, v136, v137\n\t" \
  "v_cvt_pk_bf16_f32 v69, v138, v139\n\t" \
  "v_mfma_f32_16x16x32_bf16 v[160:163], v[64:67], a[0:3], 0\n\t" \
  "v_cvt_pk_bf16_f32 v70, v140, v141\n\t" \
  "v_cvt_pk_bf16_f32 v71, v142, v143\n\t" \
  "s_waitcnt vmcnt(1)\n\t" \
  "v_mfma_f32_16x16x32_bf16 v[144:147], v[208:211], v[26:29], v[144:147]\n\t" \
  "v_mul_f32 v152, v152, v14\n\t" \
  "v_mul_f32 v153, v153, v14\n\t" \
  "v_mfma_f32_16x16x32_bf16 v[148:151], v[208:211], v[32:35], v[148:151]\n\t" \
  "v_mul_f32 v154, v154, v14\n\t" \
  "v_mul_f32 v155, v155, v14\n\t" \
  "v_mfma_f32_16x16x32_bf16 v[164:167], v[64:67], a[4:7], 0\n\t" \
  "ds_write_b128 v3, v[68:71] offset:1024\n\t" \
  "v_mul_f32 v156, v156, v14\n\t" \
  "v_mul_f32 v157, v157, v14\n\t" \
  "v_mfma_f32_16x16x32_bf16 v[168:171], v[64:67], a[8:11], 0\n\t" \
  "v_mul_f32 v158, v158, v14\n\t" \
  "v_mul_f32 v159, v159, v14\n\t" \
  "v_mfma_f32_16x16x32_bf16 v[172:175], v[64:67], a[12:15], 0\n\t" \
  "v_cvt_pk_bf16_f32 v72, v144, v145\n\t" \
  "v_cvt_pk_bf16_f32 v73, v146, v147\n\t" \
  "v_mfma_f32_16x16x32_bf16 v[152:155], v[208:211], v[24:27], v[152:155]\n\t" \
  "v_cvt_pk_bf16_f32 v74, v148, v149\n\t" \
  "v_cvt_pk_bf16_f32 v75, v150, v151\n\t" \
  "v_mfma_f32_16x16x32_bf16 v[156:159], v[208:211], v[30:33], v[156:159]\n\t" \
  "v_mfma_f32_16x16x32_bf16 v[176:179], v[64:67], a[16:19], 0\n\t" \
  "ds_write_b128 v3, v[72:75] offset:2048\n\t" \
  "v_mfma_f32_16x16x32_bf16 v[180:183], v[64:67], a[20:23], 0\n\t" \
  "v_mfma_f32_16x16x32_bf16 v[184:187], v[64:67], a[24:27], 0\n\t" \
  "v_cvt_pk_bf16_f32 v76, v152, v153\n\t" \
  "v_cvt_pk_bf16_f32 v77, v154, v155\n\t" \
  "v_mfma_f32_16x16x32_bf16 v[188:191], v[64:67], a[28:31], 0\n\t" \
  "v_cvt_pk_bf16_f32 v78, v156, v157\n\t" \
  "v_cvt_pk_bf16_f32 v79, v158, v159\n\t" \
  "v_mfma_f32_16x16x32_bf16 v[192:195], v[228:231], v[68:71], v[192:195]\n\t" \
  "ds_write_b128 v3, v[76:79] offset:3072\n\t" \
  "v_mfma_f32_16x16x32_bf16 v[160:163], v[68:71], a[32:35], v[160:163]\n\t" \
  "s_waitcnt vmcnt(0)\n\t" \
  "v_cmp_eq_u32 vcc, 0, v10\n\t" \
  "v_mfma_f32_16x16x32_bf16 v[164:167], v[68:71], a[36:39], v[164:167]\n\t" \
  "s_cmp_eq_u64 vcc, 0\n\t" \
  "s_cselect_b32 s55, s83, 0xf0000000\n\t" \
  "s_cmp_eq_u32 s48, 63\n\t" \
  "v_mfma_f32_16x16x32_bf16 v[168:171], v[68:71], a[40:43], v[168:171]\n\t" \
  "s_cselect_b32 s55, 0xf0000000, s55\n\t" \
  "s_mov_b32 s56, s83\n\t" \
  "s_mov_b32 s83, s55\n\t" \
  "v_mfma_f32_16x16x32_bf16 v[172:175], v[68:71], a[44:47], v[172:175]\n\t" \
  "v_mfma_f32_16x16x32_bf16 v[176:179], v[68:71], a[48:51], v[176:179]\n\t" \
  "s_add_u32 m0, s46, 0\n\t" \
  "s_add_u32 s47, s83, s36\n\t" \
  "buffer_load_dwordx4 v0, %[X], s47 offen offset:0 sc1 lds\n\t" \
  "v_mfma_f32_16x16x32_bf16 v[180:183], v[68:71], a[52:55], v[180:183]\n\t" \
  "v_mfma_f32_16x16x32_bf16 v[184:187], v[68:71], a[56:59], v[184:187]\n\t" \
  "s_waitcnt lgkmcnt(0)\n\t" \
  "s_barrier\n\t" \
  "v_mfma_f32_16x16x32_bf16 v[188:191], v[68:71], a[60:63], v[188:191]\n\t" \
  "ds_read_b128 v[80:83], v4 offset:0\n\t" \
  "v_mfma_f32_16x16x32_bf16 v[192:195], v[232:235], v[72:75], v[192:195]\n\t" \
  "buffer_load_dwordx4 v0, %[X], s47 offen offset:1024 sc1 lds\n\t" \
  "v_mfma_f32_16x16x32_bf16 v[160:163], v[72:75], a[64:67], v[160:163]\n\t" \
  "ds_read_b128 v[84:87], v4 offset:1024\n\t" \
  "v_mfma_f32_16x16x32_bf16 v[164:167], v[72:75], a[68:71], v[164:167]\n\t" \
  "v_mfma_f32_16x16x32_bf16 v[168:171], v[72:75], a[72:75], v[168:171]\n\t" \
  "ds_read_b128 v[88:91], v4 offset:2048\n\t" \
  "v_mfma_f32_16x16x32_bf16 v[172:175], v[72:75], a[76:79], v[172:175]\n\t" \
  "buffer_load_dwordx4 v0, %[X], s47 offen offset:2048 sc1 lds\n\t" \
  "v_mfma_f32_16x16x32_bf16 v[176:179], v[72:75], a[80:83], v[176:179]\n\t" \
  "ds_read_b128 v[92:95], v4 offset:3072\n\t" \
  "v_mfma_f32_16x16x32_bf16 v[180:183], v[72:75], a[84:87], v[180:183]\n\t" \
  "v_mfma_f32_16x16x32_bf16 v[184:187], v[72:75], a[88:91], v[184:187]\n\t" \
  "v_mfma_f32_16x16x32_bf16 v[188:191], v[72:75], a[92:95], v[188:191]\n\t" \
  "buffer_load_dwordx4 v0, %[X], s47 offen offset:3072 sc1 lds\n\t" \
  "v_mfma_f32_16x16x32_bf16 v[192:195], v[236:239], v[76:79], v[192:195]\n\t" \
  "v_mfma_f32_16x16x32_bf16 v[160:163], v[76:79], a[96:99], v[160:163]\n\t" \
  "v_mfma_f32_16x16x32_bf16 v[164:167], v[76:79], a[100:103], v[164:167]\n\t" \
  "v_mfma_f32_16x16x32_bf16 v[168:171], v[76:79], a[104:107], v[168:171]\n\t" \
  "s_add_u32 m0, s46, 4096\n\t" \
  "s_add_u32 s47, s83, s37\n\t" \
  "buffer_load_dwordx4 v0, %[X], s47 offen offset:0 sc1 lds\n\t" \
  "v_mfma_f32_16x16x32_bf16 v[172:175], v[76:79], a[108:111], v[172:175]\n\t" \
  "v_mfma_f32_16x16x32_bf16 v[176:179], v[76:79], a[112:115], v[176:179]\n\t" \
  "v_mfma_f32_16x16x32_bf16 v[180:183], v[76:79], a[116:119], v[180:183]\n\t" \
  "v_mfma_f32_16x16x32_bf16 v[184:187], v[76:79], a[120:123], v[184:187]\n\t" \
  "buffer_load_dwordx4 v0, %[X], s47 offen offset:1024 sc1 lds\n\t" \
  "v_mfma_f32_16x16x32_bf16 v[188:191], v[76:79], a[124:127], v[188:191]\n\t" \
  "s_waitcnt lgkmcnt(3)\n\t" \
  "v_mfma_f32_16x16x32_bf16 v[192:195], v[240:243], v[80:83], v[192:195]\n\t" \
  "v_mfma_f32_16x16x32_bf16 v[160:163], v[80:83], a[128:131], v[160:163]\n\t" \
  "v_mfma_f32_16x16x32_bf16 v[164:167], v[80:83], a[132:135], v[164:167]\n\t" \
  "buffer_load_dwordx4 v0, %[X], s47 offen offset:2048 sc1 lds\n\t" \
  "v_mfma_f32_16x16x32_bf16 v[168:171], v[80:83], a[136:139], v[168:171]\n\t" \
  "v_mfma_f32_16x16x32_bf16 v[172:175], v[80:83], a[140:143], v[172:175]\n\t" \
  "v_mfma_f32_16x16x32_bf16 v[176:179], v[80:83], a[144:147], v[176:179]\n\t" \
  "v_mfma_f32_16x16x32_bf16 v[180:183], v[80:83], a[148:151], v[180:183]\n\t" \
  "buffer_load_dwordx4 v0, %[X], s47 offen offset:3072 sc1 lds\n\t" \
  "v_mfma_f32_16x16x32_bf16 v[184:187], v[80:83], a[152:155], v[184:187]\n\t" \
  "v_mfma_f32_16x16x32_bf16 v[188:191], v[80:83], a[156:159], v[188:191]\n\t" \
  "s_waitcnt lgkmcnt(2)\n\t" \
  "v_mfma_f32_16x16x32_bf16 v[192:195], v[244:247], v[84:87], v[192:195]\n\t" \
  "v_mfma_f32_16x16x32_bf16 v[160:163], v[84:87], a[160:163], v[160:163]\n\t" \
  "s_add_u32 m0, s46, 8192\n\t" \
  "s_add_u32 s47, s83, s38\n\t" \
  "buffer_load_dwordx4 v0, %[X], s47 offen offset:0 sc1 lds\n\t" \
  "v_mfma_f32_16x16x32_bf16 v[164:167], v[84:87], a[164:167], v[164:167]\n\t" \
  "v_mfma_f32_16x16x32_bf16 v[168:171], v[84:87], a[168:171], v[168:171]\n\t" \
  "v_mfma_f32_16x16x32_bf16 v[172:175], v[84:87], a[172:175], v[172:175]\n\t" \
  "v_mfma_f32_16x16x32_bf16 v[176:179], v[84:87], a[176:179], v[176:179]\n\t" \
  "buffer_load_dwordx4 v0, %[X], s47 offen offset:1024 sc1 lds\n\t" \
  "v_mfma_f32_16x16x32_bf16 v[180:183], v[84:87], a[180:183], v[180:183]\n\t" \
  "v_mfma_f32_16x16x32_bf16 v[184:187], v[84:87], a[184:187], v[184:187]\n\t" \
  "v_mfma_f32_16x16x32_bf16 v[188:191], v[84:87], a[188:191], v[188:191]\n\t" \
  "s_waitcnt lgkmcnt(1)\n\t" \
  "v_mfma_f32_16x16x32_bf16 v[192:195], v[248:251], v[88:91], v[192:195]\n\t" \
  "buffer_load_dwordx4 v0, %[X], s47 offen offset:2048 sc1 lds\n\t" \
  "v_mfma_f32_16x16x32_bf16 v[160:163], v[88:91], a[192:195], v[160:163]\n\t" \
  "v_mfma_f32_16x16x32_bf16 v[164:167], v[88:91], a[196:199], v[164:167]\n\t" \
  "v_mfma_f32_16x16x32_bf16 v[168:171], v[88:91], a[200:203], v[168:171]\n\t" \
  "v_mfma_f32_16x16x32_bf16 v[172:175], v[88:91], a[204:207], v[172:175]\n\t" \
  "buffer_load_dwordx4 v0, %[X], s47 offen offset:3072 sc1 lds\n\t" \
  "v_mfma_f32_16x16x32_bf16 v[176:179], v[88:91], a[208:211], v[176:179]\n\t" \
  "v_mfma_f32_16x16x32_bf16 v[180:183], v[88:91], a[212:215], v[180:183]\n\t" \
  "v_mfma_f32_16x16x32_bf16 v[184:187], v[88:91], a[216:219], v[184:187]\n\t" \
  "s_add_u32 s63, s63, 4096\n\t" \
  "v_mfma_f32_16x16x32_bf16 v[188:191], v[88:91], a[220:223], v[188:191]\n\t" \
  "buffer_load_dword v5, off, %[X], s63 sc1\n\t" \
  "s_waitcnt lgkmcnt(0)\n\t" \
  "v_mfma_f32_16x16x32_bf16 v[192:195], v[252:255], v[92:95], v[192:195]\n\t" \
  "v_mfma_f32_16x16x32_bf16 v[160:163], v[92:95], a[224:227], v[160:163]\n\t" \
  "v_mfma_f32_16x16x32_bf16 v[164:167], v[92:95], a[228:231], v[164:167]\n\t" \
  "v_mfma_f32_16x16x32_bf16 v[168:171], v[92:95], a[232:235], v[168:171]\n\t" \
  "v_cvt_pk_bf16_f32 v112, v192, v193\n\t" \
  "v_cvt_pk_bf16_f32 v113, v194, v195\n\t" \
  "v_mfma_f32_16x16x32_bf16 v[172:175], v[92:95], a[236:239], v[172:175]\n\t" \
  "buffer_store_dwordx2 v[112:113], v8, %[X], s54 offen sc1\n\t" \
  "v_mfma_f32_16x16x32_bf16 v[176:179], v[92:95], a[240:243], v[176:179]\n\t" \
  "v_cvt_pk_bf16_f32 v96, v160, v164\n\t" \
  "v_cvt_pk_bf16_f32 v100, v161, v165\n\t" \
  "v_mfma_f32_16x16x32_bf16 v[180:183], v[92:95], a[244:247], v[180:183]\n\t" \
  "v_cvt_pk_bf16_f32 v104, v162, v166\n\t" \
  "v_cvt_pk_bf16_f32 v108, v163, v167\n\t" \
  "v_mfma_f32_16x16x32_bf16 v[184:187], v[92:95], a[248:251], v[184:187]\n\t" \
  "v_cvt_pk_bf16_f32 v97, v168, v172\n\t" \
  "v_cvt_pk_bf16_f32 v101, v169, v173\n\t" \
  "v_mfma_f32_16x16x32_bf16 v[188:191], v[92:95], a[252:255], v[188:191]\n\t" \
  "v_cvt_pk_bf16_f32 v105, v170, v174\n\t" \
  "v_cvt_pk_bf16_f32 v109, v171, v175\n\t" \
  "v_cvt_pk_bf16_f32 v98, v176, v180\n\t" \
  "v_cvt_pk_bf16_f32 v102, v177, v181\n\t" \
  "v_cvt_pk_bf16_f32 v106, v178, v182\n\t" \
  "v_cvt_pk_bf16_f32 v110, v179, v183\n\t" \
  TK_AC7 \
  "s_nop 1\n\t" \
  "v_cvt_pk_bf16_f32 v99, v184, v188\n\t" \
  "buffer_store_dwordx4 v[96:99], v7, %[X], s50 offen sc1\n\t" \
  TK_ASL \
  "v_cvt_pk_bf16_f32 v103, v185, v189\n\t" \
  "buffer_store_dwordx4 v[100:103], v7, %[X], s51 offen sc1\n\t" \
  TK_ASL \
  "v_cvt_pk_bf16_f32 v107, v186, v190\n\t" \
  "buffer_store_dwordx4 v[104:107], v7, %[X], s52 offen sc1\n\t" \
  TK_ASL \
  "v_cvt_pk_bf16_f32 v111, v187, v191\n\t" \
  "buffer_store_dwordx4 v[108:111], v7, %[X], s53 offen sc1\n\t" \
  TK_PA2 \
  TK_AC8 \
  "s_cmp_eq_u32 s48, 63\n\t" \
  "s_cbranch_scc1 LEND_%=\n\t" \
  "s_add_u32 s48, s48, 1\n\t" \
  "s_cmp_eq_u32 s83, s56\n\t" \
  "s_mov_b32 s83, s56\n\t" \
  "s_cbranch_scc0 LCOLD_%=\n\t" \
  "s_waitcnt vmcnt(5)\n\t" \
  "s_branch LTOP_%=\n\t" \
  "LEND_%=:\n\t" \
  "s_waitcnt vmcnt(0)\n\t" \
  "s_barrier\n\t" \
  "s_mov_b64 exec, 1\n\t" \
  "buffer_store_dword v3, off, %[X], s49 sc1\n\t" \
  "s_mov_b64 exec, -1\n\t" \
  "s_mov_b32 s49, 0xf0000000\n\t" \
  TK_PA0 \
  TK_ACE \
  ""
#define QK_ASM \
  "v_lshrrev_b32 v16, 7, v0\n\t" \
  "v_lshlrev_b32 v16, 13, v16\n\t" \
  "v_lshrrev_b32 v19, 2, v0\n\t" \
  "v_and_b32 v19, 64, v19\n\t" \
  "v_xor_b32 v19, v19, v0\n\t" \
  "v_and_b32 v19, 0x70, v19\n\t" \
  "v_add_u32 v16, v16, v19\n\t" \
  "s_mul_i32 s63, s40, 40960\n\t" \
  "v_and_b32 v19, 0x300, v0\n\t" \
  "v_lshlrev_b32 v17, 2, v19\n\t" \
  "v_and_b32 v19, 0xc0, v0\n\t" \
  "v_lshl_add_u32 v17, v19, 1, v17\n\t" \
  "v_and_b32 v19, 0x30, v0\n\t" \
  "v_add3_u32 v17, v17, v19, s63\n\t" \
  "v_lshrrev_b32 v19, 1, v0\n\t" \
  "v_and_b32 v19, 64, v19\n\t" \
  "v_add_u32 v17, v17, v19\n\t" \
  "v_xor_b32 v18, 64, v17\n\t" \
  "s_add_u32 m0, s63, 0\n\t" \
  "s_mov_b32 s60, 0\n\t" \
  "buffer_load_dwordx4 v16, %[W], s60 offen lds\n\t" \
  "s_add_u32 m0, s63, 1024\n\t" \
  "s_mov_b32 s60, 65536\n\t" \
  "buffer_load_dwordx4 v16, %[W], s60 offen lds\n\t" \
  "s_add_u32 m0, s63, 2048\n\t" \
  "s_mov_b32 s60, 131072\n\t" \
  "buffer_load_dwordx4 v16, %[W], s60 offen lds\n\t" \
  "s_add_u32 m0, s63, 3072\n\t" \
  "s_mov_b32 s60, 196608\n\t" \
  "buffer_load_dwordx4 v16, %[W], s60 offen lds\n\t" \
  "s_add_u32 m0, s63, 4096\n\t" \
  "s_mov_b32 s60, 262144\n\t" \
  "buffer_load_dwordx4 v16, %[W], s60 offen lds\n\t" \
  "s_add_u32 m0, s63, 5120\n\t" \
  "s_mov_b32 s60, 327680\n\t" \
  "buffer_load_dwordx4 v16, %[W], s60 offen lds\n\t" \
  "s_add_u32 m0, s63, 6144\n\t" \
  "s_mov_b32 s60, 393216\n\t" \
  "buffer_load_dwordx4 v16, %[W], s60 offen lds\n\t" \
  "s_add_u32 m0, s63, 7168\n\t" \
  "s_mov_b32 s60, 458752\n\t" \
  "buffer_load_dwordx4 v16, %[W], s60 offen lds\n\t" \
  "s_add_u32 m0, s63, 8192\n\t" \
  "s_mov_b32 s60, 524288\n\t" \
  "buffer_load_dwordx4 v16, %[W], s60 offen lds\n\t" \
  "s_add_u32 m0, s63, 9216\n\t" \
  "s_mov_b32 s60, 589824\n\t" \
  "buffer_load_dwordx4 v16, %[W], s60 offen lds\n\t" \
  "s_add_u32 m0, s63, 10240\n\t" \
  "s_mov_b32 s60, 655360\n\t" \
  "buffer_load_dwordx4 v16, %[W], s60 offen lds\n\t" \
  "s_add_u32 m0, s63, 11264\n\t" \
  "s_mov_b32 s60, 720896\n\t" \
  "buffer_load_dwordx4 v16, %[W], s60 offen lds\n\t" \
  "s_add_u32 m0, s63, 12288\n\t" \
  "s_mov_b32 s60, 786432\n\t" \
  "buffer_load_dwordx4 v16, %[W], s60 offen lds\n\t" \
  "s_add_u32 m0, s63, 13312\n\t" \
  "s_mov_b32 s60, 851968\n\t" \
  "buffer_load_dwordx4 v16, %[W], s60 offen lds\n\t" \
  "s_add_u32 m0, s63, 14336\n\t" \
  "s_mov_b32 s60, 917504\n\t" \
  "buffer_load_dwordx4 v16, %[W], s60 offen lds\n\t" \
  "s_add_u32 m0, s63, 15360\n\t" \
  "s_mov_b32 s60, 983040\n\t" \
  "buffer_load_dwordx4 v16, %[W], s60 offen lds\n\t" \
  "s_add_u32 m0, s63, 16384\n\t" \
  "s_mov_b32 s60, 1048576\n\t" \
  "buffer_load_dwordx4 v16, %[W], s60 offen lds\n\t" \
  "s_add_u32 m0, s63, 17408\n\t" \
  "s_mov_b32 s60, 1114112\n\t" \
  "buffer_load_dwordx4 v16, %[W], s60 offen lds\n\t" \
  "s_add_u32 m0, s63, 18432\n\t" \
  "s_mov_b32 s60, 1179648\n\t" \
  "buffer_load_dwordx4 v16, %[W], s60 offen lds\n\t" \
  "s_add_u32 m0, s63, 19456\n\t" \
  "s_mov_b32 s60, 1245184\n\t" \
  "buffer_load_dwordx4 v16, %[W], s60 offen lds\n\t" \
  "s_add_u32 m0, s63, 20480\n\t" \
  "s_mov_b32 s60, 1310720\n\t" \
  "buffer_load_dwordx4 v16, %[W], s60 offen lds\n\t" \
  "s_add_u32 m0, s63, 21504\n\t" \
  "s_mov_b32 s60, 1376256\n\t" \
  "buffer_load_dwordx4 v16, %[W], s60 offen lds\n\t" \
  "s_add_u32 m0, s63, 22528\n\t" \
  "s_mov_b32 s60, 1441792\n\t" \
  "buffer_load_dwordx4 v16, %[W], s60 offen lds\n\t" \
  "s_add_u32 m0, s63, 23552\n\t" \
  "s_mov_b32 s60, 1507328\n\t" \
  "buffer_load_dwordx4 v16, %[W], s60 offen lds\n\t" \
  "s_add_u32 m0, s63, 24576\n\t" \
  "s_mov_b32 s60, 1572864\n\t" \
  "buffer_load_dwordx4 v16, %[W], s60 offen lds\n\t" \
  "s_add_u32 m0, s63, 25600\n\t" \
  "s_mov_b32 s60, 1638400\n\t" \
  "buffer_load_dwordx4 v16, %[W], s60 offen lds\n\t" \
  "s_add_u32 m0, s63, 26624\n\t" \
  "s_mov_b32 s60, 1703936\n\t" \
  "buffer_load_dwordx4 v16, %[W], s60 offen lds\n\t" \
  "s_add_u32 m0, s63, 27648\n\t" \
  "s_mov_b32 s60, 1769472\n\t" \
  "buffer_load_dwordx4 v16, %[W], s60 offen lds\n\t" \
  "s_add_u32 m0, s63, 28672\n\t" \
  "s_mov_b32 s60, 1835008\n\t" \
  "buffer_load_dwordx4 v16, %[W], s60 offen lds\n\t" \
  "s_add_u32 m0, s63, 29696\n\t" \
  "s_mov_b32 s60, 1900544\n\t" \
  "buffer_load_dwordx4 v16, %[W], s60 offen lds\n\t" \
  "s_add_u32 m0, s63, 30720\n\t" \
  "s_mov_b32 s60, 1966080\n\t" \
  "buffer_load_dwordx4 v16, %[W], s60 offen lds\n\t" \
  "s_add_u32 m0, s63, 31744\n\t" \
  "s_mov_b32 s60, 2031616\n\t" \
  "buffer_load_dwordx4 v16, %[W], s60 offen lds\n\t" \
  "s_add_u32 m0, s63, 32768\n\t" \
  "s_mov_b32 s60, 2097152\n\t" \
  "buffer_load_dwordx4 v16, %[W], s60 offen lds\n\t" \
  "s_add_u32 m0, s63, 33792\n\t" \
  "s_mov_b32 s60, 2162688\n\t" \
  "buffer_load_dwordx4 v16, %[W], s60 offen lds\n\t" \
  "s_add_u32 m0, s63, 34816\n\t" \
  "s_mov_b32 s60, 2228224\n\t" \
  "buffer_load_dwordx4 v16, %[W], s60 offen lds\n\t" \
  "s_add_u32 m0, s63, 35840\n\t" \
  "s_mov_b32 s60, 2293760\n\t" \
  "buffer_load_dwordx4 v16, %[W], s60 offen lds\n\t" \
  "s_add_u32 m0, s63, 36864\n\t" \
  "s_mov_b32 s60, 2359296\n\t" \
  "buffer_load_dwordx4 v16, %[W], s60 offen lds\n\t" \
  "s_add_u32 m0, s63, 37888\n\t" \
  "s_mov_b32 s60, 2424832\n\t" \
  "buffer_load_dwordx4 v16, %[W], s60 offen lds\n\t" \
  "s_add_u32 m0, s63, 38912\n\t" \
  "s_mov_b32 s60, 2490368\n\t" \
  "buffer_load_dwordx4 v16, %[W], s60 offen lds\n\t" \
  "s_add_u32 m0, s63, 39936\n\t" \
  "s_mov_b32 s60, 2555904\n\t" \
  "buffer_load_dwordx4 v16, %[W], s60 offen lds\n\t" \
  "s_waitcnt vmcnt(32)\n\t" \
  "ds_read_b64_tr_b16 a[0:1], v17 offset:0\n\t" \
  "ds_read_b64_tr_b16 a[2:3], v17 offset:512\n\t" \
  "ds_read_b64_tr_b16 a[4:5], v17 offset:4096\n\t" \
  "ds_read_b64_tr_b16 a[6:7], v17 offset:4608\n\t" \
  "ds_read_b64_tr_b16 a[72:73], v17 offset:8\n\t" \
  "ds_read_b64_tr_b16 a[74:75], v17 offset:520\n\t" \
  "ds_read_b64_tr_b16 a[76:77], v17 offset:4104\n\t" \
  "ds_read_b64_tr_b16 a[78:79], v17 offset:4616\n\t" \
  "ds_read_b64_tr_b16 a[144:145], v18 offset:0\n\t" \
  "ds_read_b64_tr_b16 a[146:147], v18 offset:512\n\t" \
  "ds_read_b64_tr_b16 a[148:149], v18 offset:4096\n\t" \
  "ds_read_b64_tr_b16 a[150:151], v18 offset:4608\n\t" \
  "ds_read_b64_tr_b16 a[216:217], v18 offset:8\n\t" \
  "ds_read_b64_tr_b16 a[218:219], v18 offset:520\n\t" \
  "ds_read_b64_tr_b16 a[220:221], v18 offset:4104\n\t" \
  "ds_read_b64_tr_b16 a[222:223], v18 offset:4616\n\t" \
  "s_waitcnt lgkmcnt(0)\n\t" \
  "s_add_u32 m0, s63, 0\n\t" \
  "s_mov_b32 s60, 2621440\n\t" \
  "buffer_load_dwordx4 v16, %[W], s60 offen lds\n\t" \
  "s_add_u32 m0, s63, 1024\n\t" \
  "s_mov_b32 s60, 2686976\n\t" \
  "buffer_load_dwordx4 v16, %[W], s60 offen lds\n\t" \
  "s_add_u32 m0, s63, 2048\n\t" \
  "s_mov_b32 s60, 2752512\n\t" \
  "buffer_load_dwordx4 v16, %[W], s60 offen lds\n\t" \
  "s_add_u32 m0, s63, 3072\n\t" \
  "s_mov_b32 s60, 2818048\n\t" \
  "buffer_load_dwordx4 v16, %[W], s60 offen lds\n\t" \
  "s_add_u32 m0, s63, 4096\n\t" \
  "s_mov_b32 s60, 2883584\n\t" \
  "buffer_load_dwordx4 v16, %[W], s60 offen lds\n\t" \
  "s_add_u32 m0, s63, 5120\n\t" \
  "s_mov_b32 s60, 2949120\n\t" \
  "buffer_load_dwordx4 v16, %[W], s60 offen lds\n\t" \
  "s_add_u32 m0, s63, 6144\n\t" \
  "s_mov_b32 s60, 3014656\n\t" \
  "buffer_load_dwordx4 v16, %[W], s60 offen lds\n\t" \
  "s_add_u32 m0, s63, 7168\n\t" \
  "s_mov_b32 s60, 3080192\n\t" \
  "buffer_load_dwordx4 v16, %[W], s60 offen lds\n\t" \
  "s_waitcnt vmcnt(32)\n\t" \
  "ds_read_b64_tr_b16 a[8:9], v17 offset:8192\n\t" \
  "ds_read_b64_tr_b16 a[10:11], v17 offset:8704\n\t" \
  "ds_read_b64_tr_b16 a[12:13], v17 offset:12288\n\t" \
  "ds_read_b64_tr_b16 a[14:15], v17 offset:12800\n\t" \
  "ds_read_b64_tr_b16 a[80:81], v17 offset:8200\n\t" \
  "ds_read_b64_tr_b16 a[82:83], v17 offset:8712\n\t" \
  "ds_read_b64_tr_b16 a[84:85], v17 offset:12296\n\t" \
  "ds_read_b64_tr_b16 a[86:87], v17 offset:12808\n\t" \
  "ds_read_b64_tr_b16 a[152:153], v18 offset:8192\n\t" \
  "ds_read_b64_tr_b16 a[154:155], v18 offset:8704\n\t" \
  "ds_read_b64_tr_b16 a[156:157], v18 offset:12288\n\t" \
  "ds_read_b64_tr_b16 a[158:159], v18 offset:12800\n\t" \
  "ds_read_b64_tr_b16 a[224:225], v18 offset:8200\n\t" \
  "ds_read_b64_tr_b16 a[226:227], v18 offset:8712\n\t" \
  "ds_read_b64_tr_b16 a[228:229], v18 offset:12296\n\t" \
  "ds_read_b64_tr_b16 a[230:231], v18 offset:12808\n\t" \
  "s_waitcnt lgkmcnt(0)\n\t" \
  "s_add_u32 m0, s63, 8192\n\t" \
  "s_mov_b32 s60, 3145728\n\t" \
  "buffer_load_dwordx4 v16, %[W], s60 offen lds\n\t" \
  "s_add_u32 m0, s63, 9216\n\t" \
  "s_mov_b32 s60, 3211264\n\t" \
  "buffer_load_dwordx4 v16, %[W], s60 offen lds\n\t" \
  "s_add_u32 m0, s63, 10240\n\t" \
  "s_mov_b32 s60, 3276800\n\t" \
  "buffer_load_dwordx4 v16, %[W], s60 offen lds\n\t" \
  "s_add_u32 m0, s63, 11264\n\t" \
  "s_mov_b32 s60, 3342336\n\t" \
  "buffer_load_dwordx4 v16, %[W], s60 offen lds\n\t" \
  "s_add_u32 m0, s63, 12288\n\t" \
  "s_mov_b32 s60, 3407872\n\t" \
  "buffer_load_dwordx4 v16, %[W], s60 offen lds\n\t" \
  "s_add_u32 m0, s63, 13312\n\t" \
  "s_mov_b32 s60, 3473408\n\t" \
  "buffer_load_dwordx4 v16, %[W], s60 offen lds\n\t" \
  "s_add_u32 m0, s63, 14336\n\t" \
  "s_mov_b32 s60, 3538944\n\t" \
  "buffer_load_dwordx4 v16, %[W], s60 offen lds\n\t" \
  "s_add_u32 m0, s63, 15360\n\t" \
  "s_mov_b32 s60, 3604480\n\t" \
  "buffer_load_dwordx4 v16, %[W], s60 offen lds\n\t" \
  "s_waitcnt vmcnt(32)\n\t" \
  "ds_read_b64_tr_b16 a[16:17], v17 offset:16384\n\t" \
  "ds_read_b64_tr_b16 a[18:19], v17 offset:16896\n\t" \
  "ds_read_b64_tr_b16 a[20:21], v17 offset:20480\n\t" \
  "ds_read_b64_tr_b16 a[22:23], v17 offset:20992\n\t" \
  "ds_read_b64_tr_b16 a[88:89], v17 offset:16392\n\t" \
  "ds_read_b64_tr_b16 a[90:91], v17 offset:16904\n\t" \
  "ds_read_b64_tr_b16 a[92:93], v17 offset:20488\n\t" \
  "ds_read_b64_tr_b16 a[94:95], v17 offset:21000\n\t" \
  "ds_read_b64_tr_b16 a[160:161], v18 offset:16384\n\t" \
  "ds_read_b64_tr_b16 a[162:163], v18 offset:16896\n\t" \
  "ds_read_b64_tr_b16 a[164:165], v18 offset:20480\n\t" \
  "ds_read_b64_tr_b16 a[166:167], v18 offset:20992\n\t" \
  "ds_read_b64_tr_b16 a[232:233], v18 offset:16392\n\t" \
  "ds_read_b64_tr_b16 a[234:235], v18 offset:16904\n\t" \
  "ds_read_b64_tr_b16 a[236:237], v18 offset:20488\n\t" \
  "ds_read_b64_tr_b16 a[238:239], v18 offset:21000\n\t" \
  "s_waitcnt lgkmcnt(0)\n\t" \
  "s_add_u32 m0, s63, 16384\n\t" \
  "s_mov_b32 s60, 3670016\n\t" \
  "buffer_load_dwordx4 v16, %[W], s60 offen lds\n\t" \
  "s_add_u32 m0, s63, 17408\n\t" \
  "s_mov_b32 s60, 3735552\n\t" \
  "buffer_load_dwordx4 v16, %[W], s60 offen lds\n\t" \
  "s_add_u32 m0, s63, 18432\n\t" \
  "s_mov_b32 s60, 3801088\n\t" \
  "buffer_load_dwordx4 v16, %[W], s60 offen lds\n\t" \
  "s_add_u32 m0, s63, 19456\n\t" \
  "s_mov_b32 s60, 3866624\n\t" \
  "buffer_load_dwordx4 v16, %[W], s60 offen lds\n\t" \
  "s_add_u32 m0, s63, 20480\n\t" \
  "s_mov_b32 s60, 3932160\n\t" \
  "buffer_load_dwordx4 v16, %[W], s60 offen lds\n\t" \
  "s_add_u32 m0, s63, 21504\n\t" \
  "s_mov_b32 s60, 3997696\n\t" \
  "buffer_load_dwordx4 v16, %[W], s60 offen lds\n\t" \
  "s_add_u32 m0, s63, 22528\n\t" \
  "s_mov_b32 s60, 4063232\n\t" \
  "buffer_load_dwordx4 v16, %[W], s60 offen lds\n\t" \
  "s_add_u32 m0, s63, 23552\n\t" \
  "s_mov_b32 s60, 4128768\n\t" \
  "buffer_load_dwordx4 v16, %[W], s60 offen lds\n\t" \
  "s_waitcnt vmcnt(32)\n\t" \
  "ds_read_b64_tr_b16 a[24:25], v17 offset:24576\n\t" \
  "ds_read_b64_tr_b16 a[26:27], v17 offset:25088\n\t" \
  "ds_read_b64_tr_b16 a[28:29], v17 offset:28672\n\t" \
  "ds_read_b64_tr_b16 a[30:31], v17 offset:29184\n\t" \
  "ds_read_b64_tr_b16 a[96:97], v17 offset:24584\n\t" \
  "ds_read_b64_tr_b16 a[98:99], v17 offset:25096\n\t" \
  "ds_read_b64_tr_b16 a[100:101], v17 offset:28680\n\t" \
  "ds_read_b64_tr_b16 a[102:103], v17 offset:29192\n\t" \
  "ds_read_b64_tr_b16 a[168:169], v18 offset:24576\n\t" \
  "ds_read_b64_tr_b16 a[170:171], v18 offset:25088\n\t" \
  "ds_read_b64_tr_b16 a[172:173], v18 offset:28672\n\t" \
  "ds_read_b64_tr_b16 a[174:175], v18 offset:29184\n\t" \
  "ds_read_b64_tr_b16 a[240:241], v18 offset:24584\n\t" \
  "ds_read_b64_tr_b16 a[242:243], v18 offset:25096\n\t" \
  "ds_read_b64_tr_b16 a[244:245], v18 offset:28680\n\t" \
  "ds_read_b64_tr_b16 a[246:247], v18 offset:29192\n\t" \
  "s_waitcnt lgkmcnt(0)\n\t" \
  "s_add_u32 m0, s63, 24576\n\t" \
  "s_mov_b32 s60, 4194304\n\t" \
  "buffer_load_dwordx4 v16, %[W], s60 offen lds\n\t" \
  "s_add_u32 m0, s63, 25600\n\t" \
  "s_mov_b32 s60, 4259840\n\t" \
  "buffer_load_dwordx4 v16, %[W], s60 offen lds\n\t" \
  "s_add_u32 m0, s63, 26624\n\t" \
  "s_mov_b32 s60, 4325376\n\t" \
  "buffer_load_dwordx4 v16, %[W], s60 offen lds\n\t" \
  "s_add_u32 m0, s63, 27648\n\t" \
  "s_mov_b32 s60, 4390912\n\t" \
  "buffer_load_dwordx4 v16, %[W], s60 offen lds\n\t" \
  "s_add_u32 m0, s63, 28672\n\t" \
  "s_mov_b32 s60, 4456448\n\t" \
  "buffer_load_dwordx4 v16, %[W], s60 offen lds\n\t" \
  "s_add_u32 m0, s63, 29696\n\t" \
  "s_mov_b32 s60, 4521984\n\t" \
  "buffer_load_dwordx4 v16, %[W], s60 offen lds\n\t" \
  "s_add_u32 m0, s63, 30720\n\t" \
  "s_mov_b32 s60, 4587520\n\t" \
  "buffer_load_dwordx4 v16, %[W], s60 offen lds\n\t" \
  "s_add_u32 m0, s63, 31744\n\t" \
  "s_mov_b32 s60, 4653056\n\t" \
  "buffer_load_dwordx4 v16, %[W], s60 offen lds\n\t" \
  "s_waitcnt vmcnt(32)\n\t" \
  "ds_read_b64_tr_b16 a[32:33], v17 offset:32768\n\t" \
  "ds_read_b64_tr_b16 a[34:35], v17 offset:33280\n\t" \
  "ds_read_b64_tr_b16 a[36:37], v17 offset:36864\n\t" \
  "ds_read_b64_tr_b16 a[38:39], v17 offset:37376\n\t" \
  "ds_read_b64_tr_b16 a[104:105], v17 offset:32776\n\t" \
  "ds_read_b64_tr_b16 a[106:107], v17 offset:33288\n\t" \
  "ds_read_b64_tr_b16 a[108:109], v17 offset:36872\n\t" \
  "ds_read_b64_tr_b16 a[110:111], v17 offset:37384\n\t" \
  "ds_read_b64_tr_b16 a[176:177], v18 offset:32768\n\t" \
  "ds_read_b64_tr_b16 a[178:179], v18 offset:33280\n\t" \
  "ds_read_b64_tr_b16 a[180:181], v18 offset:36864\n\t" \
  "ds_read_b64_tr_b16 a[182:183], v18 offset:37376\n\t" \
  "ds_read_b64_tr_b16 a[248:249], v18 offset:32776\n\t" \
  "ds_read_b64_tr_b16 a[250:251], v18 offset:33288\n\t" \
  "ds_read_b64_tr_b16 a[252:253], v18 offset:36872\n\t" \
  "ds_read_b64_tr_b16 a[254:255], v18 offset:37384\n\t" \
  "s_waitcnt lgkmcnt(0)\n\t" \
  "s_waitcnt vmcnt(24)\n\t" \
  "ds_read_b64_tr_b16 a[40:41], v17 offset:0\n\t" \
  "ds_read_b64_tr_b16 a[42:43], v17 offset:512\n\t" \
  "ds_read_b64_tr_b16 a[44:45], v17 offset:4096\n\t" \
  "ds_read_b64_tr_b16 a[46:47], v17 offset:4608\n\t" \
  "ds_read_b64_tr_b16 a[112:113], v17 offset:8\n\t" \
  "ds_read_b64_tr_b16 a[114:115], v17 offset:520\n\t" \
  "ds_read_b64_tr_b16 a[116:117], v17 offset:4104\n\t" \
  "ds_read_b64_tr_b16 a[118:119], v17 offset:4616\n\t" \
  "ds_read_b64_tr_b16 a[184:185], v18 offset:0\n\t" \
  "ds_read_b64_tr_b16 a[186:187], v18 offset:512\n\t" \
  "ds_read_b64_tr_b16 a[188:189], v18 offset:4096\n\t" \
  "ds_read_b64_tr_b16 a[190:191], v18 offset:4608\n\t" \
  "ds_read_b64_tr_b16 v[224:225], v18 offset:8\n\t" \
  "ds_read_b64_tr_b16 v[226:227], v18 offset:520\n\t" \
  "ds_read_b64_tr_b16 v[228:229], v18 offset:4104\n\t" \
  "ds_read_b64_tr_b16 v[230:231], v18 offset:4616\n\t" \
  "s_waitcnt lgkmcnt(0)\n\t" \
  "s_waitcnt vmcnt(16)\n\t" \
  "ds_read_b64_tr_b16 a[48:49], v17 offset:8192\n\t" \
  "ds_read_b64_tr_b16 a[50:51], v17 offset:8704\n\t" \
  "ds_read_b64_tr_b16 a[52:53], v17 offset:12288\n\t" \
  "ds_read_b64_tr_b16 a[54:55], v17 offset:12800\n\t" \
  "ds_read_b64_tr_b16 a[120:121], v17 offset:8200\n\t" \
  "ds_read_b64_tr_b16 a[122:123], v17 offset:8712\n\t" \
  "ds_read_b64_tr_b16 a[124:125], v17 offset:12296\n\t" \
  "ds_read_b64_tr_b16 a[126:127], v17 offset:12808\n\t" \
  "ds_read_b64_tr_b16 a[192:193], v18 offset:8192\n\t" \
  "ds_read_b64_tr_b16 a[194:195], v18 offset:8704\n\t" \
  "ds_read_b64_tr_b16 a[196:197], v18 offset:12288\n\t" \
  "ds_read_b64_tr_b16 a[198:199], v18 offset:12800\n\t" \
  "ds_read_b64_tr_b16 v[232:233], v18 offset:8200\n\t" \
  "ds_read_b64_tr_b16 v[234:235], v18 offset:8712\n\t" \
  "ds_read_b64_tr_b16 v[236:237], v18 offset:12296\n\t" \
  "ds_read_b64_tr_b16 v[238:239], v18 offset:12808\n\t" \
  "s_waitcnt lgkmcnt(0)\n\t" \
  "s_waitcnt vmcnt(8)\n\t" \
  "ds_read_b64_tr_b16 a[56:57], v17 offset:16384\n\t" \
  "ds_read_b64_tr_b16 a[58:59], v17 offset:16896\n\t" \
  "ds_read_b64_tr_b16 a[60:61], v17 offset:20480\n\t" \
  "ds_read_b64_tr_b16 a[62:63], v17 offset:20992\n\t" \
  "ds_read_b64_tr_b16 a[128:129], v17 offset:16392\n\t" \
  "ds_read_b64_tr_b16 a[130:131], v17 offset:16904\n\t" \
  "ds_read_b64_tr_b16 a[132:133], v17 offset:20488\n\t" \
  "ds_read_b64_tr_b16 a[134:135], v17 offset:21000\n\t" \
  "ds_read_b64_tr_b16 a[200:201], v18 offset:16384\n\t" \
  "ds_read_b64_tr_b16 a[202:203], v18 offset:16896\n\t" \
  "ds_read_b64_tr_b16 a[204:205], v18 offset:20480\n\t" \
  "ds_read_b64_tr_b16 a[206:207], v18 offset:20992\n\t" \
  "ds_read_b64_tr_b16 v[240:241], v18 offset:16392\n\t" \
  "ds_read_b64_tr_b16 v[242:243], v18 offset:16904\n\t" \
  "ds_read_b64_tr_b16 v[244:245], v18 offset:20488\n\t" \
  "ds_read_b64_tr_b16 v[246:247], v18 offset:21000\n\t" \
  "s_waitcnt lgkmcnt(0)\n\t" \
  "s_waitcnt vmcnt(0)\n\t" \
  "ds_read_b64_tr_b16 a[64:65], v17 offset:24576\n\t" \
  "ds_read_b64_tr_b16 a[66:67], v17 offset:25088\n\t" \
  "ds_read_b64_tr_b16 a[68:69], v17 offset:28672\n\t" \
  "ds_read_b64_tr_b16 a[70:71], v17 offset:29184\n\t" \
  "ds_read_b64_tr_b16 a[136:137], v17 offset:24584\n\t" \
  "ds_read_b64_tr_b16 a[138:139], v17 offset:25096\n\t" \
  "ds_read_b64_tr_b16 a[140:141], v17 offset:28680\n\t" \
  "ds_read_b64_tr_b16 a[142:143], v17 offset:29192\n\t" \
  "ds_read_b64_tr_b16 a[208:209], v18 offset:24576\n\t" \
  "ds_read_b64_tr_b16 a[210:211], v18 offset:25088\n\t" \
  "ds_read_b64_tr_b16 a[212:213], v18 offset:28672\n\t" \
  "ds_read_b64_tr_b16 a[214:215], v18 offset:29184\n\t" \
  "ds_read_b64_tr_b16 v[248:249], v18 offset:24584\n\t" \
  "ds_read_b64_tr_b16 v[250:251], v18 offset:25096\n\t" \
  "ds_read_b64_tr_b16 v[252:253], v18 offset:28680\n\t" \
  "ds_read_b64_tr_b16 v[254:255], v18 offset:29192\n\t" \
  "s_waitcnt lgkmcnt(0)\n\t" \
  "s_mov_b32 s84, " TK_CAP "\n\t" \
  "s_mov_b32 s48, 0\n\t" \
  "s_mov_b32 s49, 0xf0000000\n\t" \
  "s_mov_b32 s46, s41\n\t" \
  "s_mov_b32 s56, 0\n\t" \
  "v_lshrrev_b32 v7, 2, v0\n\t" \
  "s_mov_b32 s60, " TK_CTAB "\n\t" \
  "buffer_load_dword v4, v7, %[X], s60 offen offset:88 sc1\n\t" \
  "buffer_load_dword v7, v7, %[X], s60 offen offset:0 sc1\n\t" \
  "s_waitcnt vmcnt(0)\n\t" \
  "v_and_b32 v4, 1, v4\n\t" \
  "v_and_b32 v7, 1, v7\n\t" \
  "v_cmp_ne_u32 s[90:91], s67, v4\n\t" \
  "v_cmp_ne_u32 s[88:89], s67, v7\n\t" \
  "s_nop 1\n\t" \
  "s_add_u32 s83, s48, 0\n\t" \
  "s_lshr_b64 s[60:61], s[90:91], s83\n\t" \
  "s_and_b32 s60, s60, 1\n\t" \
  "s_lshl_b32 s60, s60, 13\n\t" \
  "s_add_u32 s83, s83, " TK_SG0 "\n\t" \
  "s_lshl_b32 s83, s83, 21\n\t" \
  "s_or_b32 s83, s83, s60\n\t" \
  "s_add_u32 s62, s83, s39\n\t" \
  "v_add_u32 v7, s62, v3\n\t" \
  "LCOLD_%=:\n\t" \
  "s_waitcnt vmcnt(0)\n\t" \
  "s_mov_b64 exec, 1\n\t" \
  "buffer_store_dword v6, off, %[X], s49 sc1\n\t" \
  "s_mov_b64 exec, -1\n\t" \
  "s_mov_b32 s49, 0xf0000000\n\t" \
  "s_add_u32 s55, s83, s36\n\t" \
  "s_cmp_lg_u32 s40, 0\n\t" \
  "s_cbranch_scc1 LGO_%=\n\t" \
  "s_mov_b32 s61, 0\n\t" \
  "LPH_%=:\n\t" \
  "buffer_load_dword v4, v7, %[X], 0 offen sc1\n\t" \
  "s_waitcnt vmcnt(0)\n\t" \
  "v_cmp_eq_u32 vcc, 0, v4\n\t" \
  "s_cbranch_vccz LPH_%=x\n\t" \
  "s_add_u32 s61, s61, 1\n\t" \
  "s_sleep " TK_PSH "\n\t" \
  "s_cmp_gt_u32 s61, s84\n\t" \
  "s_cbranch_scc1 LPH_%=g\n\t" \
  "s_and_b32 s60, s61, 1023\n\t" \
  "s_cmp_lg_u32 s60, 0\n\t" \
  "s_cbranch_scc1 LPH_%=\n\t" \
  "buffer_load_dword v4, off, %[X], s92 sc1\n\t" \
  "s_waitcnt vmcnt(0)\n\t" \
  "v_readfirstlane_b32 s60, v4\n\t" \
  "s_nop 0\n\t" \
  "s_cmp_eq_u32 s60, 0\n\t" \
  "s_cbranch_scc1 LPH_%=\n\t" \
  "LPH_%=g:\n\t" \
  "s_mov_b32 s84, 0\n\t" \
  "s_or_b32 s60, s48, 1610612736\n\t" \
  "v_mov_b32 v4, s60\n\t" \
  "s_mov_b64 exec, 1\n\t" \
  "buffer_store_dword v4, off, %[X], s92 sc1\n\t" \
  "s_mov_b64 exec, -1\n\t" \
  "LPH_%=x:\n\t" \
  "s_add_i32 s62, s48, s42\n\t" \
  "s_cmp_lt_i32 s62, 1\n\t" \
  "s_cbranch_scc1 LGO_%=\n\t" \
  "LGC_%=m:\n\t" \
  "s_sub_u32 s62, s62, " TK_RING "\n\t" \
  "s_cbranch_scc0 LGC_%=m\n\t" \
  "s_add_u32 s62, s62, " TK_RING "\n\t" \
  "s_mul_i32 s63, s62, 43\n\t" \
  "s_lshr_b32 s63, s63, 7\n\t" \
  "s_mul_i32 s62, s62, 688128\n\t" \
  "s_lshl_b32 s60, s63, 15\n\t" \
  "s_add_u32 s62, s62, s60\n\t" \
  "s_lshr_b32 s60, s88, s63\n\t" \
  "s_and_b32 s60, s60, 1\n\t" \
  "s_lshl_b32 s60, s60, 13\n\t" \
  "s_add_u32 s62, s62, s60\n\t" \
  "s_add_u32 s62, s62, 656896\n\t" \
  "s_movk_i32 s63, 4000\n\t" \
  "LGC_%=:\n\t" \
  "buffer_load_dword v4, off, %[X], s62 sc1\n\t" \
  "s_memrealtime s[52:53]\n\t" \
  "s_waitcnt vmcnt(0) lgkmcnt(0)\n\t" \
  "v_readfirstlane_b32 s60, v4\n\t" \
  "s_sub_u32 s63, s63, 1\n\t" \
  "s_cbranch_scc1 LGO_%=\n\t" \
  "s_cmp_lg_u32 s60, -1\n\t" \
  "s_cbranch_scc1 LGC_%=v\n\t" \
  "s_sleep " TK_PSG "\n\t" \
  "s_branch LGC_%=\n\t" \
  "LGC_%=v:\n\t" \
  "s_not_b32 s60, s60\n\t" \
  "s_lshl_b32 s61, s52, 8\n\t" \
  "s_sub_u32 s60, s60, s61\n\t" \
  "s_ashr_i32 s60, s60, 8\n\t" \
  "s_sub_i32 s60, s60, " TK_QXC "\n\t" \
  "s_min_i32 s60, s60, 400\n\t" \
  "s_add_u32 s54, s52, s60\n\t" \
  "LGC_%=w:\n\t" \
  "s_memrealtime s[52:53]\n\t" \
  "s_waitcnt lgkmcnt(0)\n\t" \
  "s_sub_i32 s53, s52, s54\n\t" \
  "s_cmp_lt_i32 s53, 0\n\t" \
  "s_cbranch_scc1 LGC_%=w\n\t" \
  "LGO_%=:\n\t" \
  "s_barrier\n\t" \
  "s_add_u32 m0, s46, 0\n\t" \
  "s_add_u32 s47, s55, 0\n\t" \
  "buffer_load_dwordx4 v0, %[X], s47 offen sc1 lds\n\t" \
  "s_add_u32 m0, s46, 4096\n\t" \
  "s_add_u32 s47, s55, 4096\n\t" \
  "buffer_load_dwordx4 v0, %[X], s47 offen sc1 lds\n\t" \
  "s_add_u32 m0, s46, 8192\n\t" \
  "s_add_u32 s47, s55, 16384\n\t" \
  "buffer_load_dwordx4 v0, %[X], s47 offen sc1 lds\n\t" \
  "s_add_u32 m0, s46, 12288\n\t" \
  "s_add_u32 s47, s55, 20480\n\t" \
  "buffer_load_dwordx4 v0, %[X], s47 offen sc1 lds\n\t" \
  "s_add_u32 m0, s46, 16384\n\t" \
  "s_add_u32 s47, s55, 32768\n\t" \
  "buffer_load_dwordx4 v0, %[X], s47 offen sc1 lds\n\t" \
  "s_add_u32 m0, s46, 20480\n\t" \
  "s_add_u32 s47, s55, 36864\n\t" \
  "buffer_load_dwordx4 v0, %[X], s47 offen sc1 lds\n\t" \
  "s_add_u32 m0, s46, 24576\n\t" \
  "s_add_u32 s47, s55, 49152\n\t" \
  "buffer_load_dwordx4 v0, %[X], s47 offen sc1 lds\n\t" \
  "s_add_u32 m0, s46, 28672\n\t" \
  "s_add_u32 s47, s55, 53248\n\t" \
  "buffer_load_dwordx4 v0, %[X], s47 offen sc1 lds\n\t" \
  "s_add_u32 m0, s46, 32768\n\t" \
  "s_add_u32 s47, s55, 65536\n\t" \
  "buffer_load_dwordx4 v0, %[X], s47 offen sc1 lds\n\t" \
  "s_waitcnt vmcnt(5)\n\t" \
  "LTOP_%=:\n\t" \
  "s_barrier\n\t" \
  TK_PQ1 \
  "ds_read_b128 v[64:67], v1 offset:0\n\t" \
  "ds_read_b128 v[68:71], v1 offset:2048\n\t" \
  "ds_read_b128 v[72:75], v2 offset:0\n\t" \
  "ds_read_b128 v[76:79], v2 offset:2048\n\t" \
  "ds_read_b128 v[80:83], v1 offset:4096\n\t" \
  "ds_read_b128 v[84:87], v1 offset:6144\n\t" \
  "s_mov_b32 s82, s83\n\t" \
  "s_xor_b32 s46, s46, 0x10000\n\t" \
  "s_waitcnt lgkmcnt(5)\n\t" \
  "v_mfma_f32_16x16x32_bf16 v[16:19], a[0:3], v[64:67], 0\n\t" \
  "ds_read_b128 v[88:91], v2 offset:4096\n\t" \
  "v_mfma_f32_16x16x32_bf16 v[20:23], a[72:75], v[64:67], 0\n\t" \
  "s_add_u32 s83, s48, 1\n\t" \
  "s_lshr_b64 s[60:61], s[90:91], s83\n\t" \
  TK_QNP \
  "v_mfma_f32_16x16x32_bf16 v[24:27], a[144:147], v[64:67], 0\n\t" \
  "ds_read_b128 v[92:95], v2 offset:6144\n\t" \
  "v_mfma_f32_16x16x32_bf16 v[28:31], a[216:219], v[64:67], 0\n\t" \
  "s_and_b32 s60, s60, 1\n\t" \
  "s_lshl_b32 s60, s60, 13\n\t" \
  TK_QNP \
  "s_waitcnt lgkmcnt(5)\n\t" \
  "v_mfma_f32_16x16x32_bf16 v[16:19], a[4:7], v[72:75], v[16:19]\n\t" \
  "ds_read_b128 v[96:99], v1 offset:8192\n\t" \
  "v_mfma_f32_16x16x32_bf16 v[20:23], a[76:79], v[72:75], v[20:23]\n\t" \
  "s_add_u32 s83, s83, " TK_SG0 "\n\t" \
  "s_lshl_b32 s83, s83, 21\n\t" \
  TK_QNP \
  "v_mfma_f32_16x16x32_bf16 v[24:27], a[148:151], v[72:75], v[24:27]\n\t" \
  "ds_read_b128 v[100:103], v1 offset:10240\n\t" \
  "v_mfma_f32_16x16x32_bf16 v[28:31], a[220:223], v[72:75], v[28:31]\n\t" \
  "s_or_b32 s83, s83, s60\n\t" \
  "s_add_u32 s62, s83, s39\n\t" \
  TK_QNP \
  "s_waitcnt lgkmcnt(5)\n\t" \
  "v_mfma_f32_16x16x32_bf16 v[16:19], a[8:11], v[80:83], v[16:19]\n\t" \
  "ds_read_b128 v[104:107], v2 offset:8192\n\t" \
  "v_mfma_f32_16x16x32_bf16 v[20:23], a[80:83], v[80:83], v[20:23]\n\t" \
  "v_add_u32 v7, s62, v3\n\t" \
  "s_add_u32 s50, s82, s37\n\t" \
  TK_QNP \
  "v_mfma_f32_16x16x32_bf16 v[24:27], a[152:155], v[80:83], v[24:27]\n\t" \
  "ds_read_b128 v[108:111], v2 offset:10240\n\t" \
  "v_mfma_f32_16x16x32_bf16 v[28:31], a[224:227], v[80:83], v[28:31]\n\t" \
  "s_add_u32 s51, s50, 4096\n\t" \
  TK_QNP \
  "s_waitcnt lgkmcnt(5)\n\t" \
  "v_mfma_f32_16x16x32_bf16 v[16:19], a[12:15], v[88:91], v[16:19]\n\t" \
  "ds_read_b128 v[112:115], v1 offset:12288\n\t" \
  "v_mfma_f32_16x16x32_bf16 v[20:23], a[84:87], v[88:91], v[20:23]\n\t" \
  TK_QNP \
  "v_mfma_f32_16x16x32_bf16 v[24:27], a[156:159], v[88:91], v[24:27]\n\t" \
  "ds_read_b128 v[116:119], v1 offset:14336\n\t" \
  "v_mfma_f32_16x16x32_bf16 v[28:31], a[228:231], v[88:91], v[28:31]\n\t" \
  TK_QNP \
  "s_waitcnt lgkmcnt(5)\n\t" \
  "v_mfma_f32_16x16x32_bf16 v[16:19], a[16:19], v[96:99], v[16:19]\n\t" \
  "ds_read_b128 v[120:123], v2 offset:12288\n\t" \
  "v_mfma_f32_16x16x32_bf16 v[20:23], a[88:91], v[96:99], v[20:23]\n\t" \
  "s_cmp_lg_u32 s56, 0\n\t" \
  TK_QNP \
  "v_mfma_f32_16x16x32_bf16 v[24:27], a[160:163], v[96:99], v[24:27]\n\t" \
  "ds_read_b128 v[124:127], v2 offset:14336\n\t" \
  "v_mfma_f32_16x16x32_bf16 v[28:31], a[232:235], v[96:99], v[28:31]\n\t" \
  "s_cbranch_scc1 LWARM_%=\n\t" \
  "s_waitcnt vmcnt(0)\n\t" \
  "LWARM_%=:\n\t" \
  "s_barrier\n\t" \
  TK_QNP \
  "s_waitcnt lgkmcnt(5)\n\t" \
  "v_mfma_f32_16x16x32_bf16 v[16:19], a[20:23], v[104:107], v[16:19]\n\t" \
  "ds_read_b128 v[128:131], v1 offset:16384\n\t" \
  "v_mfma_f32_16x16x32_bf16 v[20:23], a[92:95], v[104:107], v[20:23]\n\t" \
  TK_QNP \
  "v_mfma_f32_16x16x32_bf16 v[24:27], a[164:167], v[104:107], v[24:27]\n\t" \
  "ds_read_b128 v[132:135], v1 offset:18432\n\t" \
  "v_mfma_f32_16x16x32_bf16 v[28:31], a[236:239], v[104:107], v[28:31]\n\t" \
  TK_QNP \
  "s_waitcnt lgkmcnt(5)\n\t" \
  "v_mfma_f32_16x16x32_bf16 v[16:19], a[24:27], v[112:115], v[16:19]\n\t" \
  "ds_read_b128 v[136:139], v2 offset:16384\n\t" \
  "v_mfma_f32_16x16x32_bf16 v[20:23], a[96:99], v[112:115], v[20:23]\n\t" \
  TK_QNP \
  "v_mfma_f32_16x16x32_bf16 v[24:27], a[168:171], v[112:115], v[24:27]\n\t" \
  "ds_read_b128 v[140:143], v2 offset:18432\n\t" \
  "v_mfma_f32_16x16x32_bf16 v[28:31], a[240:243], v[112:115], v[28:31]\n\t" \
  TK_QNP \
  "s_waitcnt lgkmcnt(5)\n\t" \
  "v_mfma_f32_16x16x32_bf16 v[16:19], a[28:31], v[120:123], v[16:19]\n\t" \
  "ds_read_b128 v[144:147], v1 offset:20480\n\t" \
  "v_mfma_f32_16x16x32_bf16 v[20:23], a[100:103], v[120:123], v[20:23]\n\t" \
  TK_QNP \
  "v_mfma_f32_16x16x32_bf16 v[24:27], a[172:175], v[120:123], v[24:27]\n\t" \
  "ds_read_b128 v[148:151], v1 offset:22528\n\t" \
  "v_mfma_f32_16x16x32_bf16 v[28:31], a[244:247], v[120:123], v[28:31]\n\t" \
  TK_QNP \
  "s_waitcnt lgkmcnt(5)\n\t" \
  "v_mfma_f32_16x16x32_bf16 v[16:19], a[32:35], v[128:131], v[16:19]\n\t" \
  "ds_read_b128 v[152:155], v2 offset:20480\n\t" \
  "v_mfma_f32_16x16x32_bf16 v[20:23], a[104:107], v[128:131], v[20:23]\n\t" \
  TK_QNP \
  "v_mfma_f32_16x16x32_bf16 v[24:27], a[176:179], v[128:131], v[24:27]\n\t" \
  "ds_read_b128 v[156:159], v2 offset:22528\n\t" \
  "v_mfma_f32_16x16x32_bf16 v[28:31], a[248:251], v[128:131], v[28:31]\n\t" \
  TK_QNP \
  "s_waitcnt lgkmcnt(5)\n\t" \
  "v_mfma_f32_16x16x32_bf16 v[16:19], a[36:39], v[136:139], v[16:19]\n\t" \
  "ds_read_b128 v[160:163], v1 offset:24576\n\t" \
  "v_mfma_f32_16x16x32_bf16 v[20:23], a[108:111], v[136:139], v[20:23]\n\t" \
  TK_QNP \
  "v_mfma_f32_16x16x32_bf16 v[24:27], a[180:183], v[136:139], v[24:27]\n\t" \
  "ds_read_b128 v[164:167], v1 offset:26624\n\t" \
  "v_mfma_f32_16x16x32_bf16 v[28:31], a[252:255], v[136:139], v[28:31]\n\t" \
  TK_QNP \
  "s_waitcnt lgkmcnt(5)\n\t" \
  "v_mfma_f32_16x16x32_bf16 v[16:19], a[40:43], v[144:147], v[16:19]\n\t" \
  "ds_read_b128 v[168:171], v2 offset:24576\n\t" \
  "v_mfma_f32_16x16x32_bf16 v[20:23], a[112:115], v[144:147], v[20:23]\n\t" \
  TK_QNP \
  "v_mfma_f32_16x16x32_bf16 v[24:27], a[184:187], v[144:147], v[24:27]\n\t" \
  "ds_read_b128 v[172:175], v2 offset:26624\n\t" \
  "v_mfma_f32_16x16x32_bf16 v[28:31], v[224:227], v[144:147], v[28:31]\n\t" \
  TK_QNP \
  "s_waitcnt lgkmcnt(5)\n\t" \
  "v_mfma_f32_16x16x32_bf16 v[16:19], a[44:47], v[152:155], v[16:19]\n\t" \
  "ds_read_b128 v[176:179], v1 offset:28672\n\t" \
  "v_mfma_f32_16x16x32_bf16 v[20:23], a[116:119], v[152:155], v[20:23]\n\t" \
  TK_QNP \
  "v_mfma_f32_16x16x32_bf16 v[24:27], a[188:191], v[152:155], v[24:27]\n\t" \
  "ds_read_b128 v[180:183], v1 offset:30720\n\t" \
  "v_mfma_f32_16x16x32_bf16 v[28:31], v[228:231], v[152:155], v[28:31]\n\t" \
  TK_QNP \
  "s_waitcnt lgkmcnt(5)\n\t" \
  "v_mfma_f32_16x16x32_bf16 v[16:19], a[48:51], v[160:163], v[16:19]\n\t" \
  "ds_read_b128 v[184:187], v2 offset:28672\n\t" \
  "v_mfma_f32_16x16x32_bf16 v[20:23], a[120:123], v[160:163], v[20:23]\n\t" \
  TK_PQ0 \
  TK_QNP \
  "v_mfma_f32_16x16x32_bf16 v[24:27], a[192:195], v[160:163], v[24:27]\n\t" \
  "ds_read_b128 v[188:191], v2 offset:30720\n\t" \
  "v_mfma_f32_16x16x32_bf16 v[28:31], v[232:235], v[160:163], v[28:31]\n\t" \
  TK_QNP \
  "s_waitcnt lgkmcnt(5)\n\t" \
  "v_mfma_f32_16x16x32_bf16 v[16:19], a[52:55], v[168:171], v[16:19]\n\t" \
  "ds_read_b128 v[192:195], v1 offset:32768\n\t" \
  "v_mfma_f32_16x16x32_bf16 v[20:23], a[124:127], v[168:171], v[20:23]\n\t" \
  TK_QNP \
  "v_mfma_f32_16x16x32_bf16 v[24:27], a[196:199], v[168:171], v[24:27]\n\t" \
  "ds_read_b128 v[196:199], v1 offset:34816\n\t" \
  "v_mfma_f32_16x16x32_bf16 v[28:31], v[236:239], v[168:171], v[28:31]\n\t" \
  TK_QNP \
  "s_waitcnt lgkmcnt(5)\n\t" \
  "v_mfma_f32_16x16x32_bf16 v[16:19], a[56:59], v[176:179], v[16:19]\n\t" \
  "ds_read_b128 v[200:203], v2 offset:32768\n\t" \
  "v_mfma_f32_16x16x32_bf16 v[20:23], a[128:131], v[176:179], v[20:23]\n\t" \
  TK_QNP \
  "v_mfma_f32_16x16x32_bf16 v[24:27], a[200:203], v[176:179], v[24:27]\n\t" \
  "ds_read_b128 v[204:207], v2 offset:34816\n\t" \
  "v_mfma_f32_16x16x32_bf16 v[28:31], v[240:243], v[176:179], v[28:31]\n\t" \
  TK_QNP \
  "s_waitcnt lgkmcnt(5)\n\t" \
  "v_mfma_f32_16x16x32_bf16 v[16:19], a[60:63], v[184:187], v[16:19]\n\t" \
  "v_mfma_f32_16x16x32_bf16 v[20:23], a[132:135], v[184:187], v[20:23]\n\t" \
  "buffer_load_dword v4, v7, %[X], 0 offen sc1\n\t" \
  TK_QNP \
  "v_mfma_f32_16x16x32_bf16 v[24:27], a[204:207], v[184:187], v[24:27]\n\t" \
  "v_mfma_f32_16x16x32_bf16 v[28:31], v[244:247], v[184:187], v[28:31]\n\t" \
  TK_QNP \
  "s_waitcnt lgkmcnt(3)\n\t" \
  "v_mfma_f32_16x16x32_bf16 v[16:19], a[64:67], v[192:195], v[16:19]\n\t" \
  "v_xor_b32 v1, 0x10000, v1\n\t" \
  "v_xor_b32 v2, 0x10000, v2\n\t" \
  "v_mfma_f32_16x16x32_bf16 v[20:23], a[136:139], v[192:195], v[20:23]\n\t" \
  TK_QNP \
  "v_mfma_f32_16x16x32_bf16 v[24:27], a[208:211], v[192:195], v[24:27]\n\t" \
  "v_mfma_f32_16x16x32_bf16 v[28:31], v[248:251], v[192:195], v[28:31]\n\t" \
  TK_QNP \
  "s_waitcnt lgkmcnt(1)\n\t" \
  "v_mfma_f32_16x16x32_bf16 v[16:19], a[68:71], v[200:203], v[16:19]\n\t" \
  "v_mfma_f32_16x16x32_bf16 v[20:23], a[140:143], v[200:203], v[20:23]\n\t" \
  TK_QNP \
  "v_mfma_f32_16x16x32_bf16 v[24:27], a[212:215], v[200:203], v[24:27]\n\t" \
  "v_mfma_f32_16x16x32_bf16 v[28:31], v[252:255], v[200:203], v[28:31]\n\t" \
  TK_QNP \
  "v_mfma_f32_16x16x32_bf16 v[32:35], a[0:3], v[68:71], 0\n\t" \
  "v_cvt_pk_bf16_f32 v48, v16, v17\n\t" \
  "v_cvt_pk_bf16_f32 v49, v18, v19\n\t" \
  "v_mfma_f32_16x16x32_bf16 v[36:39], a[72:75], v[68:71], 0\n\t" \
  "v_cvt_pk_bf16_f32 v50, v20, v21\n\t" \
  "v_cvt_pk_bf16_f32 v51, v22, v23\n\t" \
  TK_QNP \
  "v_mfma_f32_16x16x32_bf16 v[32:35], a[4:7], v[76:79], v[32:35]\n\t" \
  "v_cvt_pk_bf16_f32 v52, v24, v25\n\t" \
  "v_cvt_pk_bf16_f32 v53, v26, v27\n\t" \
  "v_mfma_f32_16x16x32_bf16 v[36:39], a[76:79], v[76:79], v[36:39]\n\t" \
  "v_cvt_pk_bf16_f32 v54, v28, v29\n\t" \
  "v_cvt_pk_bf16_f32 v55, v30, v31\n\t" \
  TK_QNP \
  "v_mfma_f32_16x16x32_bf16 v[32:35], a[8:11], v[84:87], v[32:35]\n\t" \
  "buffer_store_dwordx4 v[48:51], v0, %[X], s50 offen offset:0 sc1\n\t" \
  "v_mfma_f32_16x16x32_bf16 v[36:39], a[80:83], v[84:87], v[36:39]\n\t" \
  TK_QNP \
  "v_mfma_f32_16x16x32_bf16 v[32:35], a[12:15], v[92:95], v[32:35]\n\t" \
  "v_mfma_f32_16x16x32_bf16 v[36:39], a[84:87], v[92:95], v[36:39]\n\t" \
  TK_QNP \
  "v_mfma_f32_16x16x32_bf16 v[32:35], a[16:19], v[100:103], v[32:35]\n\t" \
  "v_mfma_f32_16x16x32_bf16 v[36:39], a[88:91], v[100:103], v[36:39]\n\t" \
  TK_QNP \
  "v_mfma_f32_16x16x32_bf16 v[32:35], a[20:23], v[108:111], v[32:35]\n\t" \
  "v_mfma_f32_16x16x32_bf16 v[36:39], a[92:95], v[108:111], v[36:39]\n\t" \
  TK_QNP \
  "v_mfma_f32_16x16x32_bf16 v[32:35], a[24:27], v[116:119], v[32:35]\n\t" \
  "v_mfma_f32_16x16x32_bf16 v[36:39], a[96:99], v[116:119], v[36:39]\n\t" \
  TK_QNP \
  "v_mfma_f32_16x16x32_bf16 v[32:35], a[28:31], v[124:127], v[32:35]\n\t" \
  "v_mfma_f32_16x16x32_bf16 v[36:39], a[100:103], v[124:127], v[36:39]\n\t" \
  "s_waitcnt vmcnt(1)\n\t" \
  "v_cmp_eq_u32 vcc, 0, v4\n\t" \
  TK_QNP \
  "v_mfma_f32_16x16x32_bf16 v[32:35], a[32:35], v[132:135], v[32:35]\n\t" \
  "v_mfma_f32_16x16x32_bf16 v[36:39], a[104:107], v[132:135], v[36:39]\n\t" \
  "s_cmp_eq_u64 vcc, 0\n\t" \
  "s_cselect_b32 s60, 1, 0\n\t" \
  "v_mov_b32 v4, s60\n\t" \
  TK_QNP \
  "v_mfma_f32_16x16x32_bf16 v[32:35], a[36:39], v[140:143], v[32:35]\n\t" \
  "ds_write_b32 v5, v4\n\t" \
  "v_mfma_f32_16x16x32_bf16 v[36:39], a[108:111], v[140:143], v[36:39]\n\t" \
  TK_QNP \
  "v_mfma_f32_16x16x32_bf16 v[32:35], a[40:43], v[148:151], v[32:35]\n\t" \
  "v_mfma_f32_16x16x32_bf16 v[36:39], a[112:115], v[148:151], v[36:39]\n\t" \
  "buffer_store_dwordx4 v[52:55], v0, %[X], s50 offen offset:1024 sc1\n\t" \
  TK_QNP \
  "v_mfma_f32_16x16x32_bf16 v[32:35], a[44:47], v[156:159], v[32:35]\n\t" \
  "v_mfma_f32_16x16x32_bf16 v[36:39], a[116:119], v[156:159], v[36:39]\n\t" \
  "s_waitcnt lgkmcnt(0)\n\t" \
  "s_barrier\n\t" \
  TK_QNP \
  "v_mfma_f32_16x16x32_bf16 v[32:35], a[48:51], v[164:167], v[32:35]\n\t" \
  "ds_read_b32 v4, v6 offset:40959\n\t" \
  "v_mfma_f32_16x16x32_bf16 v[36:39], a[120:123], v[164:167], v[36:39]\n\t" \
  TK_QNP \
  "v_mfma_f32_16x16x32_bf16 v[32:35], a[52:55], v[172:175], v[32:35]\n\t" \
  "v_mfma_f32_16x16x32_bf16 v[36:39], a[124:127], v[172:175], v[36:39]\n\t" \
  TK_QNP \
  "v_mfma_f32_16x16x32_bf16 v[32:35], a[56:59], v[180:183], v[32:35]\n\t" \
  "v_mfma_f32_16x16x32_bf16 v[36:39], a[128:131], v[180:183], v[36:39]\n\t" \
  "s_waitcnt lgkmcnt(0)\n\t" \
  "v_readfirstlane_b32 s60, v4\n\t" \
  "s_add_u32 s55, s83, s36\n\t" \
  TK_QNP \
  "v_mfma_f32_16x16x32_bf16 v[32:35], a[60:63], v[188:191], v[32:35]\n\t" \
  "s_cmp_eq_u32 s48, 63\n\t" \
  "s_cselect_b32 s56, 0, s60\n\t" \
  "v_mfma_f32_16x16x32_bf16 v[36:39], a[132:135], v[188:191], v[36:39]\n\t" \
  "s_cmp_eq_u32 s56, 0\n\t" \
  "s_cselect_b32 s55, 0xf0000000, s55\n\t" \
  TK_QNP \
  "v_mfma_f32_16x16x32_bf16 v[32:35], a[64:67], v[196:199], v[32:35]\n\t" \
  "v_mfma_f32_16x16x32_bf16 v[36:39], a[136:139], v[196:199], v[36:39]\n\t" \
  TK_QNP \
  "v_mfma_f32_16x16x32_bf16 v[32:35], a[68:71], v[204:207], v[32:35]\n\t" \
  "s_add_u32 m0, s46, 0\n\t" \
  "s_add_u32 s47, s55, 0\n\t" \
  "buffer_load_dwordx4 v0, %[X], s47 offen sc1 lds\n\t" \
  "v_mfma_f32_16x16x32_bf16 v[36:39], a[140:143], v[204:207], v[36:39]\n\t" \
  TK_QNP \
  "v_mfma_f32_16x16x32_bf16 v[40:43], a[144:147], v[68:71], 0\n\t" \
  "v_mfma_f32_16x16x32_bf16 v[44:47], a[216:219], v[68:71], 0\n\t" \
  TK_QNP \
  "v_mfma_f32_16x16x32_bf16 v[40:43], a[148:151], v[76:79], v[40:43]\n\t" \
  "v_cvt_pk_bf16_f32 v56, v32, v33\n\t" \
  "v_cvt_pk_bf16_f32 v57, v34, v35\n\t" \
  "s_add_u32 m0, s46, 4096\n\t" \
  "s_add_u32 s47, s55, 4096\n\t" \
  "buffer_load_dwordx4 v0, %[X], s47 offen sc1 lds\n\t" \
  "v_mfma_f32_16x16x32_bf16 v[44:47], a[220:223], v[76:79], v[44:47]\n\t" \
  "v_cvt_pk_bf16_f32 v58, v36, v37\n\t" \
  "v_cvt_pk_bf16_f32 v59, v38, v39\n\t" \
  TK_QNP \
  "v_mfma_f32_16x16x32_bf16 v[40:43], a[152:155], v[84:87], v[40:43]\n\t" \
  "v_mfma_f32_16x16x32_bf16 v[44:47], a[224:227], v[84:87], v[44:47]\n\t" \
  "buffer_store_dwordx4 v[56:59], v0, %[X], s51 offen offset:0 sc1\n\t" \
  TK_QNP \
  "v_mfma_f32_16x16x32_bf16 v[40:43], a[156:159], v[92:95], v[40:43]\n\t" \
  "s_add_u32 m0, s46, 8192\n\t" \
  "s_add_u32 s47, s55, 16384\n\t" \
  "buffer_load_dwordx4 v0, %[X], s47 offen sc1 lds\n\t" \
  "v_mfma_f32_16x16x32_bf16 v[44:47], a[228:231], v[92:95], v[44:47]\n\t" \
  TK_QNP \
  "v_mfma_f32_16x16x32_bf16 v[40:43], a[160:163], v[100:103], v[40:43]\n\t" \
  "v_mfma_f32_16x16x32_bf16 v[44:47], a[232:235], v[100:103], v[44:47]\n\t" \
  TK_QNP \
  "v_mfma_f32_16x16x32_bf16 v[40:43], a[164:167], v[108:111], v[40:43]\n\t" \
  "s_add_u32 m0, s46, 12288\n\t" \
  "s_add_u32 s47, s55, 20480\n\t" \
  "buffer_load_dwordx4 v0, %[X], s47 offen sc1 lds\n\t" \
  "v_mfma_f32_16x16x32_bf16 v[44:47], a[236:239], v[108:111], v[44:47]\n\t" \
  TK_QNP \
  "v_mfma_f32_16x16x32_bf16 v[40:43], a[168:171], v[116:119], v[40:43]\n\t" \
  "v_mfma_f32_16x16x32_bf16 v[44:47], a[240:243], v[116:119], v[44:47]\n\t" \
  TK_QNP \
  "v_mfma_f32_16x16x32_bf16 v[40:43], a[172:175], v[124:127], v[40:43]\n\t" \
  "s_add_u32 m0, s46, 16384\n\t" \
  "s_add_u32 s47, s55, 32768\n\t" \
  "buffer_load_dwordx4 v0, %[X], s47 offen sc1 lds\n\t" \
  "v_mfma_f32_16x16x32_bf16 v[44:47], a[244:247], v[124:127], v[44:47]\n\t" \
  TK_QNP \
  "v_mfma_f32_16x16x32_bf16 v[40:43], a[176:179], v[132:135], v[40:43]\n\t" \
  "v_mfma_f32_16x16x32_bf16 v[44:47], a[248:251], v[132:135], v[44:47]\n\t" \
  TK_QNP \
  "v_mfma_f32_16x16x32_bf16 v[40:43], a[180:183], v[140:143], v[40:43]\n\t" \
  "s_add_u32 m0, s46, 20480\n\t" \
  "s_add_u32 s47, s55, 36864\n\t" \
  "buffer_load_dwordx4 v0, %[X], s47 offen sc1 lds\n\t" \
  "v_mfma_f32_16x16x32_bf16 v[44:47], a[252:255], v[140:143], v[44:47]\n\t" \
  TK_QNP \
  "v_mfma_f32_16x16x32_bf16 v[40:43], a[184:187], v[148:151], v[40:43]\n\t" \
  "v_mfma_f32_16x16x32_bf16 v[44:47], v[224:227], v[148:151], v[44:47]\n\t" \
  TK_QNP \
  "v_mfma_f32_16x16x32_bf16 v[40:43], a[188:191], v[156:159], v[40:43]\n\t" \
  "s_add_u32 m0, s46, 24576\n\t" \
  "s_add_u32 s47, s55, 49152\n\t" \
  "buffer_load_dwordx4 v0, %[X], s47 offen sc1 lds\n\t" \
  "v_mfma_f32_16x16x32_bf16 v[44:47], v[228:231], v[156:159], v[44:47]\n\t" \
  TK_QNP \
  "v_mfma_f32_16x16x32_bf16 v[40:43], a[192:195], v[164:167], v[40:43]\n\t" \
  "v_mfma_f32_16x16x32_bf16 v[44:47], v[232:235], v[164:167], v[44:47]\n\t" \
  TK_QNP \
  "v_mfma_f32_16x16x32_bf16 v[40:43], a[196:199], v[172:175], v[40:43]\n\t" \
  "s_add_u32 m0, s46, 28672\n\t" \
  "s_add_u32 s47, s55, 53248\n\t" \
  "buffer_load_dwordx4 v0, %[X], s47 offen sc1 lds\n\t" \
  "v_mfma_f32_16x16x32_bf16 v[44:47], v[236:239], v[172:175], v[44:47]\n\t" \
  TK_QNP \
  "v_mfma_f32_16x16x32_bf16 v[40:43], a[200:203], v[180:183], v[40:43]\n\t" \
  "v_mfma_f32_16x16x32_bf16 v[44:47], v[240:243], v[180:183], v[44:47]\n\t" \
  TK_QNP \
  "v_mfma_f32_16x16x32_bf16 v[40:43], a[204:207], v[188:191], v[40:43]\n\t" \
  "s_add_u32 m0, s46, 32768\n\t" \
  "s_add_u32 s47, s55, 65536\n\t" \
  "buffer_load_dwordx4 v0, %[X], s47 offen sc1 lds\n\t" \
  "v_mfma_f32_16x16x32_bf16 v[44:47], v[244:247], v[188:191], v[44:47]\n\t" \
  TK_QNP \
  "v_mfma_f32_16x16x32_bf16 v[40:43], a[208:211], v[196:199], v[40:43]\n\t" \
  "v_mfma_f32_16x16x32_bf16 v[44:47], v[248:251], v[196:199], v[44:47]\n\t" \
  TK_QNP \
  "v_mfma_f32_16x16x32_bf16 v[40:43], a[212:215], v[204:207], v[40:43]\n\t" \
  "v_mfma_f32_16x16x32_bf16 v[44:47], v[252:255], v[204:207], v[44:47]\n\t" \
  TK_QNP \
  "s_nop 4\n\t" \
  "v_cvt_pk_bf16_f32 v60, v40, v41\n\t" \
  "v_cvt_pk_bf16_f32 v61, v42, v43\n\t" \
  "s_nop 1\n\t" \
  "v_cvt_pk_bf16_f32 v62, v44, v45\n\t" \
  "v_cvt_pk_bf16_f32 v63, v46, v47\n\t" \
  "buffer_store_dwordx4 v[60:63], v0, %[X], s51 offen offset:1024 sc1\n\t" \
  TK_PQ2 \
  "s_add_u32 s49, s82, s38\n\t" \
  "s_cmp_eq_u32 s48, 63\n\t" \
  "s_cbranch_scc1 LEND_%=\n\t" \
  "s_add_u32 s48, s48, 1\n\t" \
  "s_cmp_eq_u32 s56, 0\n\t" \
  "s_cbranch_scc1 LCOLD_%=\n\t" \
  "s_waitcnt vmcnt(0)\n\t" \
  "s_mov_b64 exec, 1\n\t" \
  "buffer_store_dword v6, off, %[X], s49 sc1\n\t" \
  "s_mov_b64 exec, -1\n\t" \
  "s_mov_b32 s49, 0xf0000000\n\t" \
  "s_cmp_lg_u32 s40, 0\n\t" \
  "s_cbranch_scc1 LTOP_%=\n\t" \
  "s_add_i32 s62, s48, s42\n\t" \
  "s_cmp_lt_i32 s62, 1\n\t" \
  "s_cbranch_scc1 LTOP_%=\n\t" \
  "LGW_%=m:\n\t" \
  "s_sub_u32 s62, s62, " TK_RING "\n\t" \
  "s_cbranch_scc0 LGW_%=m\n\t" \
  "s_add_u32 s62, s62, " TK_RING "\n\t" \
  "s_mul_i32 s63, s62, 43\n\t" \
  "s_lshr_b32 s63, s63, 7\n\t" \
  "s_mul_i32 s62, s62, 688128\n\t" \
  "s_lshl_b32 s60, s63, 15\n\t" \
  "s_add_u32 s62, s62, s60\n\t" \
  "s_lshr_b32 s60, s88, s63\n\t" \
  "s_and_b32 s60, s60, 1\n\t" \
  "s_lshl_b32 s60, s60, 13\n\t" \
  "s_add_u32 s62, s62, s60\n\t" \
  "s_add_u32 s62, s62, 656896\n\t" \
  "s_movk_i32 s63, 4000\n\t" \
  "LGW_%=:\n\t" \
  "buffer_load_dword v4, off, %[X], s62 sc1\n\t" \
  "s_memrealtime s[52:53]\n\t" \
  "s_waitcnt vmcnt(0) lgkmcnt(0)\n\t" \
  "v_readfirstlane_b32 s60, v4\n\t" \
  "s_sub_u32 s63, s63, 1\n\t" \
  "s_cbranch_scc1 LTOP_%=\n\t" \
  "s_cmp_lg_u32 s60, -1\n\t" \
  "s_cbranch_scc1 LGW_%=v\n\t" \
  "s_sleep " TK_PSG "\n\t" \
  "s_branch LGW_%=\n\t" \
  "LGW_%=v:\n\t" \
  "s_not_b32 s60, s60\n\t" \
  "s_lshl_b32 s61, s52, 8\n\t" \
  "s_sub_u32 s60, s60, s61\n\t" \
  "s_ashr_i32 s60, s60, 8\n\t" \
  "s_sub_i32 s60, s60, " TK_QXW "\n\t" \
  "s_min_i32 s60, s60, 400\n\t" \
  "s_add_u32 s54, s52, s60\n\t" \
  "LGW_%=w:\n\t" \
  "s_memrealtime s[52:53]\n\t" \
  "s_waitcnt lgkmcnt(0)\n\t" \
  "s_sub_i32 s53, s52, s54\n\t" \
  "s_cmp_lt_i32 s53, 0\n\t" \
  "s_cbranch_scc1 LGW_%=w\n\t" \
  "s_branch LTOP_%=\n\t" \
  "LEND_%=:\n\t" \
  "s_waitcnt vmcnt(0)\n\t" \
  "s_mov_b64 exec, 1\n\t" \
  "buffer_store_dword v6, off, %[X], s49 sc1\n\t" \
  "s_mov_b64 exec, -1\n\t" \
  "s_mov_b32 s49, 0xf0000000\n\t" \
  TK_PQ0 \
  ""
#define QV_ASM \
  "v_lshrrev_b32 v16, 7, v0\n\t" \
  "v_lshlrev_b32 v16, 13, v16\n\t" \
  "v_lshrrev_b32 v19, 2, v0\n\t" \
  "v_and_b32 v19, 64, v19\n\t" \
  "v_xor_b32 v19, v19, v0\n\t" \
  "v_and_b32 v19, 0x70, v19\n\t" \
  "v_add_u32 v16, v16, v19\n\t" \
  "s_mul_i32 s63, s40, 40960\n\t" \
  "v_and_b32 v19, 0x300, v0\n\t" \
  "v_lshlrev_b32 v17, 2, v19\n\t" \
  "v_and_b32 v19, 0xc0, v0\n\t" \
  "v_lshl_add_u32 v17, v19, 1, v17\n\t" \
  "v_and_b32 v19, 0x30, v0\n\t" \
  "v_lshrrev_b32 v19, 1, v19\n\t" \
  "v_add3_u32 v17, v17, v19, s63\n\t" \
  "v_lshrrev_b32 v19, 1, v0\n\t" \
  "v_and_b32 v19, 64, v19\n\t" \
  "v_add_u32 v17, v17, v19\n\t" \
  "v_xor_b32 v18, 64, v17\n\t" \
  "s_add_u32 m0, s63, 0\n\t" \
  "s_mov_b32 s60, 0\n\t" \
  "buffer_load_dwordx4 v16, %[W], s60 offen lds\n\t" \
  "s_add_u32 m0, s63, 1024\n\t" \
  "s_mov_b32 s60, 65536\n\t" \
  "buffer_load_dwordx4 v16, %[W], s60 offen lds\n\t" \
  "s_add_u32 m0, s63, 2048\n\t" \
  "s_mov_b32 s60, 131072\n\t" \
  "buffer_load_dwordx4 v16, %[W], s60 offen lds\n\t" \
  "s_add_u32 m0, s63, 3072\n\t" \
  "s_mov_b32 s60, 196608\n\t" \
  "buffer_load_dwordx4 v16, %[W], s60 offen lds\n\t" \
  "s_add_u32 m0, s63, 4096\n\t" \
  "s_mov_b32 s60, 262144\n\t" \
  "buffer_load_dwordx4 v16, %[W], s60 offen lds\n\t" \
  "s_add_u32 m0, s63, 5120\n\t" \
  "s_mov_b32 s60, 327680\n\t" \
  "buffer_load_dwordx4 v16, %[W], s60 offen lds\n\t" \
  "s_add_u32 m0, s63, 6144\n\t" \
  "s_mov_b32 s60, 393216\n\t" \
  "buffer_load_dwordx4 v16, %[W], s60 offen lds\n\t" \
  "s_add_u32 m0, s63, 7168\n\t" \
  "s_mov_b32 s60, 458752\n\t" \
  "buffer_load_dwordx4 v16, %[W], s60 offen lds\n\t" \
  "s_add_u32 m0, s63, 8192\n\t" \
  "s_mov_b32 s60, 524288\n\t" \
  "buffer_load_dwordx4 v16, %[W], s60 offen lds\n\t" \
  "s_add_u32 m0, s63, 9216\n\t" \
  "s_mov_b32 s60, 589824\n\t" \
  "buffer_load_dwordx4 v16, %[W], s60 offen lds\n\t" \
  "s_add_u32 m0, s63, 10240\n\t" \
  "s_mov_b32 s60, 655360\n\t" \
  "buffer_load_dwordx4 v16, %[W], s60 offen lds\n\t" \
  "s_add_u32 m0, s63, 11264\n\t" \
  "s_mov_b32 s60, 720896\n\t" \
  "buffer_load_dwordx4 v16, %[W], s60 offen lds\n\t" \
  "s_add_u32 m0, s63, 12288\n\t" \
  "s_mov_b32 s60, 786432\n\t" \
  "buffer_load_dwordx4 v16, %[W], s60 offen lds\n\t" \
  "s_add_u32 m0, s63, 13312\n\t" \
  "s_mov_b32 s60, 851968\n\t" \
  "buffer_load_dwordx4 v16, %[W], s60 offen lds\n\t" \
  "s_add_u32 m0, s63, 14336\n\t" \
  "s_mov_b32 s60, 917504\n\t" \
  "buffer_load_dwordx4 v16, %[W], s60 offen lds\n\t" \
  "s_add_u32 m0, s63, 15360\n\t" \
  "s_mov_b32 s60, 983040\n\t" \
  "buffer_load_dwordx4 v16, %[W], s60 offen lds\n\t" \
  "s_add_u32 m0, s63, 16384\n\t" \
  "s_mov_b32 s60, 1048576\n\t" \
  "buffer_load_dwordx4 v16, %[W], s60 offen lds\n\t" \
  "s_add_u32 m0, s63, 17408\n\t" \
  "s_mov_b32 s60, 1114112\n\t" \
  "buffer_load_dwordx4 v16, %[W], s60 offen lds\n\t" \
  "s_add_u32 m0, s63, 18432\n\t" \
  "s_mov_b32 s60, 1179648\n\t" \
  "buffer_load_dwordx4 v16, %[W], s60 offen lds\n\t" \
  "s_add_u32 m0, s63, 19456\n\t" \
  "s_mov_b32 s60, 1245184\n\t" \
  "buffer_load_dwordx4 v16, %[W], s60 offen lds\n\t" \
  "s_add_u32 m0, s63, 20480\n\t" \
  "s_mov_b32 s60, 1310720\n\t" \
  "buffer_load_dwordx4 v16, %[W], s60 offen lds\n\t" \
  "s_add_u32 m0, s63, 21504\n\t" \
  "s_mov_b32 s60, 1376256\n\t" \
  "buffer_load_dwordx4 v16, %[W], s60 offen lds\n\t" \
  "s_add_u32 m0, s63, 22528\n\t" \
  "s_mov_b32 s60, 1441792\n\t" \
  "buffer_load_dwordx4 v16, %[W], s60 offen lds\n\t" \
  "s_add_u32 m0, s63, 23552\n\t" \
  "s_mov_b32 s60, 1507328\n\t" \
  "buffer_load_dwordx4 v16, %[W], s60 offen lds\n\t" \
  "s_add_u32 m0, s63, 24576\n\t" \
  "s_mov_b32 s60, 1572864\n\t" \
  "buffer_load_dwordx4 v16, %[W], s60 offen lds\n\t" \
  "s_add_u32 m0, s63, 25600\n\t" \
  "s_mov_b32 s60, 1638400\n\t" \
  "buffer_load_dwordx4 v16, %[W], s60 offen lds\n\t" \
  "s_add_u32 m0, s63, 26624\n\t" \
  "s_mov_b32 s60, 1703936\n\t" \
  "buffer_load_dwordx4 v16, %[W], s60 offen lds\n\t" \
  "s_add_u32 m0, s63, 27648\n\t" \
  "s_mov_b32 s60, 1769472\n\t" \
  "buffer_load_dwordx4 v16, %[W], s60 offen lds\n\t" \
  "s_add_u32 m0, s63, 28672\n\t" \
  "s_mov_b32 s60, 1835008\n\t" \
  "buffer_load_dwordx4 v16, %[W], s60 offen lds\n\t" \
  "s_add_u32 m0, s63, 29696\n\t" \
  "s_mov_b32 s60, 1900544\n\t" \
  "buffer_load_dwordx4 v16, %[W], s60 offen lds\n\t" \
  "s_add_u32 m0, s63, 30720\n\t" \
  "s_mov_b32 s60, 1966080\n\t" \
  "buffer_load_dwordx4 v16, %[W], s60 offen lds\n\t" \
  "s_add_u32 m0, s63, 31744\n\t" \
  "s_mov_b32 s60, 2031616\n\t" \
  "buffer_load_dwordx4 v16, %[W], s60 offen lds\n\t" \
  "s_add_u32 m0, s63, 32768\n\t" \
  "s_mov_b32 s60, 2097152\n\t" \
  "buffer_load_dwordx4 v16, %[W], s60 offen lds\n\t" \
  "s_add_u32 m0, s63, 33792\n\t" \
  "s_mov_b32 s60, 2162688\n\t" \
  "buffer_load_dwordx4 v16, %[W], s60 offen lds\n\t" \
  "s_add_u32 m0, s63, 34816\n\t" \
  "s_mov_b32 s60, 2228224\n\t" \
  "buffer_load_dwordx4 v16, %[W], s60 offen lds\n\t" \
  "s_add_u32 m0, s63, 35840\n\t" \
  "s_mov_b32 s60, 2293760\n\t" \
  "buffer_load_dwordx4 v16, %[W], s60 offen lds\n\t" \
  "s_add_u32 m0, s63, 36864\n\t" \
  "s_mov_b32 s60, 2359296\n\t" \
  "buffer_load_dwordx4 v16, %[W], s60 offen lds\n\t" \
  "s_add_u32 m0, s63, 37888\n\t" \
  "s_mov_b32 s60, 2424832\n\t" \
  "buffer_load_dwordx4 v16, %[W], s60 offen lds\n\t" \
  "s_add_u32 m0, s63, 38912\n\t" \
  "s_mov_b32 s60, 2490368\n\t" \
  "buffer_load_dwordx4 v16, %[W], s60 offen lds\n\t" \
  "s_add_u32 m0, s63, 39936\n\t" \
  "s_mov_b32 s60, 2555904\n\t" \
  "buffer_load_dwordx4 v16, %[W], s60 offen lds\n\t" \
  "s_waitcnt vmcnt(32)\n\t" \
  "ds_read_b64_tr_b16 a[0:1], v17 offset:0\n\t" \
  "ds_read_b64_tr_b16 a[2:3], v17 offset:512\n\t" \
  "ds_read_b64_tr_b16 a[4:5], v17 offset:4096\n\t" \
  "ds_read_b64_tr_b16 a[6:7], v17 offset:4608\n\t" \
  "ds_read_b64_tr_b16 a[72:73], v17 offset:32\n\t" \
  "ds_read_b64_tr_b16 a[74:75], v17 offset:544\n\t" \
  "ds_read_b64_tr_b16 a[76:77], v17 offset:4128\n\t" \
  "ds_read_b64_tr_b16 a[78:79], v17 offset:4640\n\t" \
  "ds_read_b64_tr_b16 a[144:145], v18 offset:0\n\t" \
  "ds_read_b64_tr_b16 a[146:147], v18 offset:512\n\t" \
  "ds_read_b64_tr_b16 a[148:149], v18 offset:4096\n\t" \
  "ds_read_b64_tr_b16 a[150:151], v18 offset:4608\n\t" \
  "ds_read_b64_tr_b16 a[216:217], v18 offset:32\n\t" \
  "ds_read_b64_tr_b16 a[218:219], v18 offset:544\n\t" \
  "ds_read_b64_tr_b16 a[220:221], v18 offset:4128\n\t" \
  "ds_read_b64_tr_b16 a[222:223], v18 offset:4640\n\t" \
  "s_waitcnt lgkmcnt(0)\n\t" \
  "s_add_u32 m0, s63, 0\n\t" \
  "s_mov_b32 s60, 2621440\n\t" \
  "buffer_load_dwordx4 v16, %[W], s60 offen lds\n\t" \
  "s_add_u32 m0, s63, 1024\n\t" \
  "s_mov_b32 s60, 2686976\n\t" \
  "buffer_load_dwordx4 v16, %[W], s60 offen lds\n\t" \
  "s_add_u32 m0, s63, 2048\n\t" \
  "s_mov_b32 s60, 2752512\n\t" \
  "buffer_load_dwordx4 v16, %[W], s60 offen lds\n\t" \
  "s_add_u32 m0, s63, 3072\n\t" \
  "s_mov_b32 s60, 2818048\n\t" \
  "buffer_load_dwordx4 v16, %[W], s60 offen lds\n\t" \
  "s_add_u32 m0, s63, 4096\n\t" \
  "s_mov_b32 s60, 2883584\n\t" \
  "buffer_load_dwordx4 v16, %[W], s60 offen lds\n\t" \
  "s_add_u32 m0, s63, 5120\n\t" \
  "s_mov_b32 s60, 2949120\n\t" \
  "buffer_load_dwordx4 v16, %[W], s60 offen lds\n\t" \
  "s_add_u32 m0, s63, 6144\n\t" \
  "s_mov_b32 s60, 3014656\n\t" \
  "buffer_load_dwordx4 v16, %[W], s60 offen lds\n\t" \
  "s_add_u32 m0, s63, 7168\n\t" \
  "s_mov_b32 s60, 3080192\n\t" \
  "buffer_load_dwordx4 v16, %[W], s60 offen lds\n\t" \
  "s_waitcnt vmcnt(32)\n\t" \
  "ds_read_b64_tr_b16 a[8:9], v17 offset:8192\n\t" \
  "ds_read_b64_tr_b16 a[10:11], v17 offset:8704\n\t" \
  "ds_read_b64_tr_b16 a[12:13], v17 offset:12288\n\t" \
  "ds_read_b64_tr_b16 a[14:15], v17 offset:12800\n\t" \
  "ds_read_b64_tr_b16 a[80:81], v17 offset:8224\n\t" \
  "ds_read_b64_tr_b16 a[82:83], v17 offset:8736\n\t" \
  "ds_read_b64_tr_b16 a[84:85], v17 offset:12320\n\t" \
  "ds_read_b64_tr_b16 a[86:87], v17 offset:12832\n\t" \
  "ds_read_b64_tr_b16 a[152:153], v18 offset:8192\n\t" \
  "ds_read_b64_tr_b16 a[154:155], v18 offset:8704\n\t" \
  "ds_read_b64_tr_b16 a[156:157], v18 offset:12288\n\t" \
  "ds_read_b64_tr_b16 a[158:159], v18 offset:12800\n\t" \
  "ds_read_b64_tr_b16 a[224:225], v18 offset:8224\n\t" \
  "ds_read_b64_tr_b16 a[226:227], v18 offset:8736\n\t" \
  "ds_read_b64_tr_b16 a[228:229], v18 offset:12320\n\t" \
  "ds_read_b64_tr_b16 a[230:231], v18 offset:12832\n\t" \
  "s_waitcnt lgkmcnt(0)\n\t" \
  "s_add_u32 m0, s63, 8192\n\t" \
  "s_mov_b32 s60, 3145728\n\t" \
  "buffer_load_dwordx4 v16, %[W], s60 offen lds\n\t" \
  "s_add_u32 m0, s63, 9216\n\t" \
  "s_mov_b32 s60, 3211264\n\t" \
  "buffer_load_dwordx4 v16, %[W], s60 offen lds\n\t" \
  "s_add_u32 m0, s63, 10240\n\t" \
  "s_mov_b32 s60, 3276800\n\t" \
  "buffer_load_dwordx4 v16, %[W], s60 offen lds\n\t" \
  "s_add_u32 m0, s63, 11264\n\t" \
  "s_mov_b32 s60, 3342336\n\t" \
  "buffer_load_dwordx4 v16, %[W], s60 offen lds\n\t" \
  "s_add_u32 m0, s63, 12288\n\t" \
  "s_mov_b32 s60, 3407872\n\t" \
  "buffer_load_dwordx4 v16, %[W], s60 offen lds\n\t" \
  "s_add_u32 m0, s63, 13312\n\t" \
  "s_mov_b32 s60, 3473408\n\t" \
  "buffer_load_dwordx4 v16, %[W], s60 offen lds\n\t" \
  "s_add_u32 m0, s63, 14336\n\t" \
  "s_mov_b32 s60, 3538944\n\t" \
  "buffer_load_dwordx4 v16, %[W], s60 offen lds\n\t" \
  "s_add_u32 m0, s63, 15360\n\t" \
  "s_mov_b32 s60, 3604480\n\t" \
  "buffer_load_dwordx4 v16, %[W], s60 offen lds\n\t" \
  "s_waitcnt vmcnt(32)\n\t" \
  "ds_read_b64_tr_b16 a[16:17], v17 offset:16384\n\t" \
  "ds_read_b64_tr_b16 a[18:19], v17 offset:16896\n\t" \
  "ds_read_b64_tr_b16 a[20:21], v17 offset:20480\n\t" \
  "ds_read_b64_tr_b16 a[22:23], v17 offset:20992\n\t" \
  "ds_read_b64_tr_b16 a[88:89], v17 offset:16416\n\t" \
  "ds_read_b64_tr_b16 a[90:91], v17 offset:16928\n\t" \
  "ds_read_b64_tr_b16 a[92:93], v17 offset:20512\n\t" \
  "ds_read_b64_tr_b16 a[94:95], v17 offset:21024\n\t" \
  "ds_read_b64_tr_b16 a[160:161], v18 offset:16384\n\t" \
  "ds_read_b64_tr_b16 a[162:163], v18 offset:16896\n\t" \
  "ds_read_b64_tr_b16 a[164:165], v18 offset:20480\n\t" \
  "ds_read_b64_tr_b16 a[166:167], v18 offset:20992\n\t" \
  "ds_read_b64_tr_b16 a[232:233], v18 offset:16416\n\t" \
  "ds_read_b64_tr_b16 a[234:235], v18 offset:16928\n\t" \
  "ds_read_b64_tr_b16 a[236:237], v18 offset:20512\n\t" \
  "ds_read_b64_tr_b16 a[238:239], v18 offset:21024\n\t" \
  "s_waitcnt lgkmcnt(0)\n\t" \
  "s_add_u32 m0, s63, 16384\n\t" \
  "s_mov_b32 s60, 3670016\n\t" \
  "buffer_load_dwordx4 v16, %[W], s60 offen lds\n\t" \
  "s_add_u32 m0, s63, 17408\n\t" \
  "s_mov_b32 s60, 3735552\n\t" \
  "buffer_load_dwordx4 v16, %[W], s60 offen lds\n\t" \
  "s_add_u32 m0, s63, 18432\n\t" \
  "s_mov_b32 s60, 3801088\n\t" \
  "buffer_load_dwordx4 v16, %[W], s60 offen lds\n\t" \
  "s_add_u32 m0, s63, 19456\n\t" \
  "s_mov_b32 s60, 3866624\n\t" \
  "buffer_load_dwordx4 v16, %[W], s60 offen lds\n\t" \
  "s_add_u32 m0, s63, 20480\n\t" \
  "s_mov_b32 s60, 3932160\n\t" \
  "buffer_load_dwordx4 v16, %[W], s60 offen lds\n\t" \
  "s_add_u32 m0, s63, 21504\n\t" \
  "s_mov_b32 s60, 3997696\n\t" \
  "buffer_load_dwordx4 v16, %[W], s60 offen lds\n\t" \
  "s_add_u32 m0, s63, 22528\n\t" \
  "s_mov_b32 s60, 4063232\n\t" \
  "buffer_load_dwordx4 v16, %[W], s60 offen lds\n\t" \
  "s_add_u32 m0, s63, 23552\n\t" \
  "s_mov_b32 s60, 4128768\n\t" \
  "buffer_load_dwordx4 v16, %[W], s60 offen lds\n\t" \
  "s_waitcnt vmcnt(32)\n\t" \
  "ds_read_b64_tr_b16 a[24:25], v17 offset:24576\n\t" \
  "ds_read_b64_tr_b16 a[26:27], v17 offset:25088\n\t" \
  "ds_read_b64_tr_b16 a[28:29], v17 offset:28672\n\t" \
  "ds_read_b64_tr_b16 a[30:31], v17 offset:29184\n\t" \
  "ds_read_b64_tr_b16 a[96:97], v17 offset:24608\n\t" \
  "ds_read_b64_tr_b16 a[98:99], v17 offset:25120\n\t" \
  "ds_read_b64_tr_b16 a[100:101], v17 offset:28704\n\t" \
  "ds_read_b64_tr_b16 a[102:103], v17 offset:29216\n\t" \
  "ds_read_b64_tr_b16 a[168:169], v18 offset:24576\n\t" \
  "ds_read_b64_tr_b16 a[170:171], v18 offset:25088\n\t" \
  "ds_read_b64_tr_b16 a[172:173], v18 offset:28672\n\t" \
  "ds_read_b64_tr_b16 a[174:175], v18 offset:29184\n\t" \
  "ds_read_b64_tr_b16 a[240:241], v18 offset:24608\n\t" \
  "ds_read_b64_tr_b16 a[242:243], v18 offset:25120\n\t" \
  "ds_read_b64_tr_b16 a[244:245], v18 offset:28704\n\t" \
  "ds_read_b64_tr_b16 a[246:247], v18 offset:29216\n\t" \
  "s_waitcnt lgkmcnt(0)\n\t" \
  "s_add_u32 m0, s63, 24576\n\t" \
  "s_mov_b32 s60, 4194304\n\t" \
  "buffer_load_dwordx4 v16, %[W], s60 offen lds\n\t" \
  "s_add_u32 m0, s63, 25600\n\t" \
  "s_mov_b32 s60, 4259840\n\t" \
  "buffer_load_dwordx4 v16, %[W], s60 offen lds\n\t" \
  "s_add_u32 m0, s63, 26624\n\t" \
  "s_mov_b32 s60, 4325376\n\t" \
  "buffer_load_dwordx4 v16, %[W], s60 offen lds\n\t" \
  "s_add_u32 m0, s63, 27648\n\t" \
  "s_mov_b32 s60, 4390912\n\t" \
  "buffer_load_dwordx4 v16, %[W], s60 offen lds\n\t" \
  "s_add_u32 m0, s63, 28672\n\t" \
  "s_mov_b32 s60, 4456448\n\t" \
  "buffer_load_dwordx4 v16, %[W], s60 offen lds\n\t" \
  "s_add_u32 m0, s63, 29696\n\t" \
  "s_mov_b32 s60, 4521984\n\t" \
  "buffer_load_dwordx4 v16, %[W], s60 offen lds\n\t" \
  "s_add_u32 m0, s63, 30720\n\t" \
  "s_mov_b32 s60, 4587520\n\t" \
  "buffer_load_dwordx4 v16, %[W], s60 offen lds\n\t" \
  "s_add_u32 m0, s63, 31744\n\t" \
  "s_mov_b32 s60, 4653056\n\t" \
  "buffer_load_dwordx4 v16, %[W], s60 offen lds\n\t" \
  "s_waitcnt vmcnt(32)\n\t" \
  "ds_read_b64_tr_b16 a[32:33], v17 offset:32768\n\t" \
  "ds_read_b64_tr_b16 a[34:35], v17 offset:33280\n\t" \
  "ds_read_b64_tr_b16 a[36:37], v17 offset:36864\n\t" \
  "ds_read_b64_tr_b16 a[38:39], v17 offset:37376\n\t" \
  "ds_read_b64_tr_b16 a[104:105], v17 offset:32800\n\t" \
  "ds_read_b64_tr_b16 a[106:107], v17 offset:33312\n\t" \
  "ds_read_b64_tr_b16 a[108:109], v17 offset:36896\n\t" \
  "ds_read_b64_tr_b16 a[110:111], v17 offset:37408\n\t" \
  "ds_read_b64_tr_b16 a[176:177], v18 offset:32768\n\t" \
  "ds_read_b64_tr_b16 a[178:179], v18 offset:33280\n\t" \
  "ds_read_b64_tr_b16 a[180:181], v18 offset:36864\n\t" \
  "ds_read_b64_tr_b16 a[182:183], v18 offset:37376\n\t" \
  "ds_read_b64_tr_b16 a[248:249], v18 offset:32800\n\t" \
  "ds_read_b64_tr_b16 a[250:251], v18 offset:33312\n\t" \
  "ds_read_b64_tr_b16 a[252:253], v18 offset:36896\n\t" \
  "ds_read_b64_tr_b16 a[254:255], v18 offset:37408\n\t" \
  "s_waitcnt lgkmcnt(0)\n\t" \
  "s_waitcnt vmcnt(24)\n\t" \
  "ds_read_b64_tr_b16 a[40:41], v17 offset:0\n\t" \
  "ds_read_b64_tr_b16 a[42:43], v17 offset:512\n\t" \
  "ds_read_b64_tr_b16 a[44:45], v17 offset:4096\n\t" \
  "ds_read_b64_tr_b16 a[46:47], v17 offset:4608\n\t" \
  "ds_read_b64_tr_b16 a[112:113], v17 offset:32\n\t" \
  "ds_read_b64_tr_b16 a[114:115], v17 offset:544\n\t" \
  "ds_read_b64_tr_b16 a[116:117], v17 offset:4128\n\t" \
  "ds_read_b64_tr_b16 a[118:119], v17 offset:4640\n\t" \
  "ds_read_b64_tr_b16 a[184:185], v18 offset:0\n\t" \
  "ds_read_b64_tr_b16 a[186:187], v18 offset:512\n\t" \
  "ds_read_b64_tr_b16 a[188:189], v18 offset:4096\n\t" \
  "ds_read_b64_tr_b16 a[190:191], v18 offset:4608\n\t" \
  "ds_read_b64_tr_b16 v[224:225], v18 offset:32\n\t" \
  "ds_read_b64_tr_b16 v[226:227], v18 offset:544\n\t" \
  "ds_read_b64_tr_b16 v[228:229], v18 offset:4128\n\t" \
  "ds_read_b64_tr_b16 v[230:231], v18 offset:4640\n\t" \
  "s_waitcnt lgkmcnt(0)\n\t" \
  "s_waitcnt vmcnt(16)\n\t" \
  "ds_read_b64_tr_b16 a[48:49], v17 offset:8192\n\t" \
  "ds_read_b64_tr_b16 a[50:51], v17 offset:8704\n\t" \
  "ds_read_b64_tr_b16 a[52:53], v17 offset:12288\n\t" \
  "ds_read_b64_tr_b16 a[54:55], v17 offset:12800\n\t" \
  "ds_read_b64_tr_b16 a[120:121], v17 offset:8224\n\t" \
  "ds_read_b64_tr_b16 a[122:123], v17 offset:8736\n\t" \
  "ds_read_b64_tr_b16 a[124:125], v17 offset:12320\n\t" \
  "ds_read_b64_tr_b16 a[126:127], v17 offset:12832\n\t" \
  "ds_read_b64_tr_b16 a[192:193], v18 offset:8192\n\t" \
  "ds_read_b64_tr_b16 a[194:195], v18 offset:8704\n\t" \
  "ds_read_b64_tr_b16 a[196:197], v18 offset:12288\n\t" \
  "ds_read_b64_tr_b16 a[198:199], v18 offset:12800\n\t" \
  "ds_read_b64_tr_b16 v[232:233], v18 offset:8224\n\t" \
  "ds_read_b64_tr_b16 v[234:235], v18 offset:8736\n\t" \
  "ds_read_b64_tr_b16 v[236:237], v18 offset:12320\n\t" \
  "ds_read_b64_tr_b16 v[238:239], v18 offset:12832\n\t" \
  "s_waitcnt lgkmcnt(0)\n\t" \
  "s_waitcnt vmcnt(8)\n\t" \
  "ds_read_b64_tr_b16 a[56:57], v17 offset:16384\n\t" \
  "ds_read_b64_tr_b16 a[58:59], v17 offset:16896\n\t" \
  "ds_read_b64_tr_b16 a[60:61], v17 offset:20480\n\t" \
  "ds_read_b64_tr_b16 a[62:63], v17 offset:20992\n\t" \
  "ds_read_b64_tr_b16 a[128:129], v17 offset:16416\n\t" \
  "ds_read_b64_tr_b16 a[130:131], v17 offset:16928\n\t" \
  "ds_read_b64_tr_b16 a[132:133], v17 offset:20512\n\t" \
  "ds_read_b64_tr_b16 a[134:135], v17 offset:21024\n\t" \
  "ds_read_b64_tr_b16 a[200:201], v18 offset:16384\n\t" \
  "ds_read_b64_tr_b16 a[202:203], v18 offset:16896\n\t" \
  "ds_read_b64_tr_b16 a[204:205], v18 offset:20480\n\t" \
  "ds_read_b64_tr_b16 a[206:207], v18 offset:20992\n\t" \
  "ds_read_b64_tr_b16 v[240:241], v18 offset:16416\n\t" \
  "ds_read_b64_tr_b16 v[242:243], v18 offset:16928\n\t" \
  "ds_read_b64_tr_b16 v[244:245], v18 offset:20512\n\t" \
  "ds_read_b64_tr_b16 v[246:247], v18 offset:21024\n\t" \
  "s_waitcnt lgkmcnt(0)\n\t" \
  "s_waitcnt vmcnt(0)\n\t" \
  "ds_read_b64_tr_b16 a[64:65], v17 offset:24576\n\t" \
  "ds_read_b64_tr_b16 a[66:67], v17 offset:25088\n\t" \
  "ds_read_b64_tr_b16 a[68:69], v17 offset:28672\n\t" \
  "ds_read_b64_tr_b16 a[70:71], v17 offset:29184\n\t" \
  "ds_read_b64_tr_b16 a[136:137], v17 offset:24608\n\t" \
  "ds_read_b64_tr_b16 a[138:139], v17 offset:25120\n\t" \
  "ds_read_b64_tr_b16 a[140:141], v17 offset:28704\n\t" \
  "ds_read_b64_tr_b16 a[142:143], v17 offset:29216\n\t" \
  "ds_read_b64_tr_b16 a[208:209], v18 offset:24576\n\t" \
  "ds_read_b64_tr_b16 a[210:211], v18 offset:25088\n\t" \
  "ds_read_b64_tr_b16 a[212:213], v18 offset:28672\n\t" \
  "ds_read_b64_tr_b16 a[214:215], v18 offset:29184\n\t" \
  "ds_read_b64_tr_b16 v[248:249], v18 offset:24608\n\t" \
  "ds_read_b64_tr_b16 v[250:251], v18 offset:25120\n\t" \
  "ds_read_b64_tr_b16 v[252:253], v18 offset:28704\n\t" \
  "ds_read_b64_tr_b16 v[254:255], v18 offset:29216\n\t" \
  "s_waitcnt lgkmcnt(0)\n\t" \
  "s_mov_b32 s84, " TK_CAP "\n\t" \
  "s_mov_b32 s48, 0\n\t" \
  "s_mov_b32 s49, 0xf0000000\n\t" \
  "s_mov_b32 s46, s41\n\t" \
  "s_mov_b32 s56, 0\n\t" \
  "v_lshrrev_b32 v7, 2, v0\n\t" \
  "s_mov_b32 s60, " TK_CTAB "\n\t" \
  "buffer_load_dword v4, v7, %[X], s60 offen offset:88 sc1\n\t" \
  "buffer_load_dword v7, v7, %[X], s60 offen offset:0 sc1\n\t" \
  "s_waitcnt vmcnt(0)\n\t" \
  "v_and_b32 v4, 1, v4\n\t" \
  "v_and_b32 v7, 1, v7\n\t" \
  "v_cmp_ne_u32 s[90:91], s67, v4\n\t" \
  "v_cmp_ne_u32 s[88:89], s67, v7\n\t" \
  "s_nop 1\n\t" \
  "s_add_u32 s83, s48, 0\n\t" \
  "s_lshr_b64 s[60:61], s[90:91], s83\n\t" \
  "s_and_b32 s60, s60, 1\n\t" \
  "s_lshl_b32 s60, s60, 13\n\t" \
  "s_add_u32 s83, s83, " TK_SG0 "\n\t" \
  "s_lshl_b32 s83, s83, 21\n\t" \
  "s_or_b32 s83, s83, s60\n\t" \
  "s_add_u32 s62, s83, s39\n\t" \
  "v_add_u32 v7, s62, v3\n\t" \
  "LCOLD_%=:\n\t" \
  "s_waitcnt vmcnt(0)\n\t" \
  "s_mov_b64 exec, 1\n\t" \
  "buffer_store_dword v6, off, %[X], s49 sc1\n\t" \
  "s_mov_b64 exec, -1\n\t" \
  "s_mov_b32 s49, 0xf0000000\n\t" \
  "s_add_u32 s55, s83, s36\n\t" \
  "s_cmp_lg_u32 s40, 0\n\t" \
  "s_cbranch_scc1 LGO_%=\n\t" \
  "s_mov_b32 s61, 0\n\t" \
  "LPH_%=:\n\t" \
  "buffer_load_dword v4, v7, %[X], 0 offen sc1\n\t" \
  "s_waitcnt vmcnt(0)\n\t" \
  "v_cmp_eq_u32 vcc, 0, v4\n\t" \
  "s_cbranch_vccz LPH_%=x\n\t" \
  "s_add_u32 s61, s61, 1\n\t" \
  "s_sleep " TK_PSH "\n\t" \
  "s_cmp_gt_u32 s61, s84\n\t" \
  "s_cbranch_scc1 LPH_%=g\n\t" \
  "s_and_b32 s60, s61, 1023\n\t" \
  "s_cmp_lg_u32 s60, 0\n\t" \
  "s_cbranch_scc1 LPH_%=\n\t" \
  "buffer_load_dword v4, off, %[X], s92 sc1\n\t" \
  "s_waitcnt vmcnt(0)\n\t" \
  "v_readfirstlane_b32 s60, v4\n\t" \
  "s_nop 0\n\t" \
  "s_cmp_eq_u32 s60, 0\n\t" \
  "s_cbranch_scc1 LPH_%=\n\t" \
  "LPH_%=g:\n\t" \
  "s_mov_b32 s84, 0\n\t" \
  "s_or_b32 s60, s48, 1610612736\n\t" \
  "v_mov_b32 v4, s60\n\t" \
  "s_mov_b64 exec, 1\n\t" \
  "buffer_store_dword v4, off, %[X], s92 sc1\n\t" \
  "s_mov_b64 exec, -1\n\t" \
  "LPH_%=x:\n\t" \
  "s_add_i32 s62, s48, s42\n\t" \
  "s_cmp_lt_i32 s62, 1\n\t" \
  "s_cbranch_scc1 LGO_%=\n\t" \
  "LGC_%=m:\n\t" \
  "s_sub_u32 s62, s62, " TK_RING "\n\t" \
  "s_cbranch_scc0 LGC_%=m\n\t" \
  "s_add_u32 s62, s62, " TK_RING "\n\t" \
  "s_mul_i32 s63, s62, 43\n\t" \
  "s_lshr_b32 s63, s63, 7\n\t" \
  "s_mul_i32 s62, s62, 688128\n\t" \
  "s_lshl_b32 s60, s63, 15\n\t" \
  "s_add_u32 s62, s62, s60\n\t" \
  "s_lshr_b32 s60, s88, s63\n\t" \
  "s_and_b32 s60, s60, 1\n\t" \
  "s_lshl_b32 s60, s60, 13\n\t" \
  "s_add_u32 s62, s62, s60\n\t" \
  "s_add_u32 s62, s62, 656896\n\t" \
  "s_movk_i32 s63, 4000\n\t" \
  "LGC_%=:\n\t" \
  "buffer_load_dword v4, off, %[X], s62 sc1\n\t" \
  "s_memrealtime s[52:53]\n\t" \
  "s_waitcnt vmcnt(0) lgkmcnt(0)\n\t" \
  "v_readfirstlane_b32 s60, v4\n\t" \
  "s_sub_u32 s63, s63, 1\n\t" \
  "s_cbranch_scc1 LGO_%=\n\t" \
  "s_cmp_lg_u32 s60, -1\n\t" \
  "s_cbranch_scc1 LGC_%=v\n\t" \
  "s_sleep " TK_PSG "\n\t" \
  "s_branch LGC_%=\n\t" \
  "LGC_%=v:\n\t" \
  "s_not_b32 s60, s60\n\t" \
  "s_lshl_b32 s61, s52, 8\n\t" \
  "s_sub_u32 s60, s60, s61\n\t" \
  "s_ashr_i32 s60, s60, 8\n\t" \
  "s_sub_i32 s60, s60, " TK_QXC "\n\t" \
  "s_min_i32 s60, s60, 400\n\t" \
  "s_add_u32 s54, s52, s60\n\t" \
  "LGC_%=w:\n\t" \
  "s_memrealtime s[52:53]\n\t" \
  "s_waitcnt lgkmcnt(0)\n\t" \
  "s_sub_i32 s53, s52, s54\n\t" \
  "s_cmp_lt_i32 s53, 0\n\t" \
  "s_cbranch_scc1 LGC_%=w\n\t" \
  "LGO_%=:\n\t" \
  "s_barrier\n\t" \
  "s_add_u32 m0, s46, 0\n\t" \
  "s_add_u32 s47, s55, 0\n\t" \
  "buffer_load_dwordx4 v0, %[X], s47 offen sc1 lds\n\t" \
  "s_add_u32 m0, s46, 4096\n\t" \
  "s_add_u32 s47, s55, 4096\n\t" \
  "buffer_load_dwordx4 v0, %[X], s47 offen sc1 lds\n\t" \
  "s_add_u32 m0, s46, 8192\n\t" \
  "s_add_u32 s47, s55, 16384\n\t" \
  "buffer_load_dwordx4 v0, %[X], s47 offen sc1 lds\n\t" \
  "s_add_u32 m0, s46, 12288\n\t" \
  "s_add_u32 s47, s55, 20480\n\t" \
  "buffer_load_dwordx4 v0, %[X], s47 offen sc1 lds\n\t" \
  "s_add_u32 m0, s46, 16384\n\t" \
  "s_add_u32 s47, s55, 32768\n\t" \
  "buffer_load_dwordx4 v0, %[X], s47 offen sc1 lds\n\t" \
  "s_add_u32 m0, s46, 20480\n\t" \
  "s_add_u32 s47, s55, 36864\n\t" \
  "buffer_load_dwordx4 v0, %[X], s47 offen sc1 lds\n\t" \
  "s_add_u32 m0, s46, 24576\n\t" \
  "s_add_u32 s47, s55, 49152\n\t" \
  "buffer_load_dwordx4 v0, %[X], s47 offen sc1 lds\n\t" \
  "s_add_u32 m0, s46, 28672\n\t" \
  "s_add_u32 s47, s55, 53248\n\t" \
  "buffer_load_dwordx4 v0, %[X], s47 offen sc1 lds\n\t" \
  "s_add_u32 m0, s46, 32768\n\t" \
  "s_add_u32 s47, s55, 65536\n\t" \
  "buffer_load_dwordx4 v0, %[X], s47 offen sc1 lds\n\t" \
  "s_waitcnt vmcnt(5)\n\t" \
  "LTOP_%=:\n\t" \
  "s_barrier\n\t" \
  TK_PQ1 \
  "ds_read_b128 v[64:67], v1 offset:0\n\t" \
  "ds_read_b128 v[68:71], v1 offset:2048\n\t" \
  "ds_read_b128 v[72:75], v2 offset:0\n\t" \
  "ds_read_b128 v[76:79], v2 offset:2048\n\t" \
  "ds_read_b128 v[80:83], v1 offset:4096\n\t" \
  "ds_read_b128 v[84:87], v1 offset:6144\n\t" \
  "s_mov_b32 s82, s83\n\t" \
  "s_xor_b32 s46, s46, 0x10000\n\t" \
  "s_waitcnt lgkmcnt(5)\n\t" \
  "v_mfma_f32_16x16x32_bf16 v[16:19], v[64:67], a[0:3], 0\n\t" \
  "ds_read_b128 v[88:91], v2 offset:4096\n\t" \
  "s_waitcnt lgkmcnt(5)\n\t" \
  "v_mfma_f32_16x16x32_bf16 v[20:23], v[68:71], a[0:3], 0\n\t" \
  "s_add_u32 s83, s48, 1\n\t" \
  "s_lshr_b64 s[60:61], s[90:91], s83\n\t" \
  TK_QNP \
  "v_mfma_f32_16x16x32_bf16 v[24:27], v[64:67], a[72:75], 0\n\t" \
  "ds_read_b128 v[92:95], v2 offset:6144\n\t" \
  "v_mfma_f32_16x16x32_bf16 v[28:31], v[68:71], a[72:75], 0\n\t" \
  "s_and_b32 s60, s60, 1\n\t" \
  "s_lshl_b32 s60, s60, 13\n\t" \
  TK_QNP \
  "s_waitcnt lgkmcnt(5)\n\t" \
  "v_mfma_f32_16x16x32_bf16 v[16:19], v[72:75], a[4:7], v[16:19]\n\t" \
  "ds_read_b128 v[96:99], v1 offset:8192\n\t" \
  "s_waitcnt lgkmcnt(5)\n\t" \
  "v_mfma_f32_16x16x32_bf16 v[20:23], v[76:79], a[4:7], v[20:23]\n\t" \
  "s_add_u32 s83, s83, " TK_SG0 "\n\t" \
  "s_lshl_b32 s83, s83, 21\n\t" \
  TK_QNP \
  "v_mfma_f32_16x16x32_bf16 v[24:27], v[72:75], a[76:79], v[24:27]\n\t" \
  "ds_read_b128 v[100:103], v1 offset:10240\n\t" \
  "v_mfma_f32_16x16x32_bf16 v[28:31], v[76:79], a[76:79], v[28:31]\n\t" \
  "s_or_b32 s83, s83, s60\n\t" \
  "s_add_u32 s62, s83, s39\n\t" \
  TK_QNP \
  "s_waitcnt lgkmcnt(5)\n\t" \
  "v_mfma_f32_16x16x32_bf16 v[16:19], v[80:83], a[8:11], v[16:19]\n\t" \
  "ds_read_b128 v[104:107], v2 offset:8192\n\t" \
  "s_waitcnt lgkmcnt(5)\n\t" \
  "v_mfma_f32_16x16x32_bf16 v[20:23], v[84:87], a[8:11], v[20:23]\n\t" \
  "v_add_u32 v7, s62, v3\n\t" \
  "s_add_u32 s50, s82, s37\n\t" \
  TK_QNP \
  "v_mfma_f32_16x16x32_bf16 v[24:27], v[80:83], a[80:83], v[24:27]\n\t" \
  "ds_read_b128 v[108:111], v2 offset:10240\n\t" \
  "v_mfma_f32_16x16x32_bf16 v[28:31], v[84:87], a[80:83], v[28:31]\n\t" \
  "s_add_u32 s51, s50, 4096\n\t" \
  TK_QNP \
  "s_waitcnt lgkmcnt(5)\n\t" \
  "v_mfma_f32_16x16x32_bf16 v[16:19], v[88:91], a[12:15], v[16:19]\n\t" \
  "ds_read_b128 v[112:115], v1 offset:12288\n\t" \
  "s_waitcnt lgkmcnt(5)\n\t" \
  "v_mfma_f32_16x16x32_bf16 v[20:23], v[92:95], a[12:15], v[20:23]\n\t" \
  TK_QNP \
  "v_mfma_f32_16x16x32_bf16 v[24:27], v[88:91], a[84:87], v[24:27]\n\t" \
  "ds_read_b128 v[116:119], v1 offset:14336\n\t" \
  "v_mfma_f32_16x16x32_bf16 v[28:31], v[92:95], a[84:87], v[28:31]\n\t" \
  TK_QNP \
  "s_waitcnt lgkmcnt(5)\n\t" \
  "v_mfma_f32_16x16x32_bf16 v[16:19], v[96:99], a[16:19], v[16:19]\n\t" \
  "ds_read_b128 v[120:123], v2 offset:12288\n\t" \
  "s_waitcnt lgkmcnt(5)\n\t" \
  "v_mfma_f32_16x16x32_bf16 v[20:23], v[100:103], a[16:19], v[20:23]\n\t" \
  "s_cmp_lg_u32 s56, 0\n\t" \
  TK_QNP \
  "v_mfma_f32_16x16x32_bf16 v[24:27], v[96:99], a[88:91], v[24:27]\n\t" \
  "ds_read_b128 v[124:127], v2 offset:14336\n\t" \
  "v_mfma_f32_16x16x32_bf16 v[28:31], v[100:103], a[88:91], v[28:31]\n\t" \
  "s_cbranch_scc1 LWARM_%=\n\t" \
  "s_waitcnt vmcnt(0)\n\t" \
  "LWARM_%=:\n\t" \
  "s_barrier\n\t" \
  TK_QNP \
  "s_waitcnt lgkmcnt(5)\n\t" \
  "v_mfma_f32_16x16x32_bf16 v[16:19], v[104:107], a[20:23], v[16:19]\n\t" \
  "ds_read_b128 v[128:131], v1 offset:16384\n\t" \
  "s_waitcnt lgkmcnt(5)\n\t" \
  "v_mfma_f32_16x16x32_bf16 v[20:23], v[108:111], a[20:23], v[20:23]\n\t" \
  TK_QNP \
  "v_mfma_f32_16x16x32_bf16 v[24:27], v[104:107], a[92:95], v[24:27]\n\t" \
  "ds_read_b128 v[132:135], v1 offset:18432\n\t" \
  "v_mfma_f32_16x16x32_bf16 v[28:31], v[108:111], a[92:95], v[28:31]\n\t" \
  TK_QNP \
  "s_waitcnt lgkmcnt(5)\n\t" \
  "v_mfma_f32_16x16x32_bf16 v[16:19], v[112:115], a[24:27], v[16:19]\n\t" \
  "ds_read_b128 v[136:139], v2 offset:16384\n\t" \
  "s_waitcnt lgkmcnt(5)\n\t" \
  "v_mfma_f32_16x16x32_bf16 v[20:23], v[116:119], a[24:27], v[20:23]\n\t" \
  TK_QNP \
  "v_mfma_f32_16x16x32_bf16 v[24:27], v[112:115], a[96:99], v[24:27]\n\t" \
  "ds_read_b128 v[140:143], v2 offset:18432\n\t" \
  "v_mfma_f32_16x16x32_bf16 v[28:31], v[116:119], a[96:99], v[28:31]\n\t" \
  TK_QNP \
  "s_waitcnt lgkmcnt(5)\n\t" \
  "v_mfma_f32_16x16x32_bf16 v[16:19], v[120:123], a[28:31], v[16:19]\n\t" \
  "ds_read_b128 v[144:147], v1 offset:20480\n\t" \
  "s_waitcnt lgkmcnt(5)\n\t" \
  "v_mfma_f32_16x16x32_bf16 v[20:23], v[124:127], a[28:31], v[20:23]\n\t" \
  TK_QNP \
  "v_mfma_f32_16x16x32_bf16 v[24:27], v[120:123], a[100:103], v[24:27]\n\t" \
  "ds_read_b128 v[148:151], v1 offset:22528\n\t" \
  "v_mfma_f32_16x16x32_bf16 v[28:31], v[124:127], a[100:103], v[28:31]\n\t" \
  TK_QNP \
  "s_waitcnt lgkmcnt(5)\n\t" \
  "v_mfma_f32_16x16x32_bf16 v[16:19], v[128:131], a[32:35], v[16:19]\n\t" \
  "ds_read_b128 v[152:155], v2 offset:20480\n\t" \
  "s_waitcnt lgkmcnt(5)\n\t" \
  "v_mfma_f32_16x16x32_bf16 v[20:23], v[132:135], a[32:35], v[20:23]\n\t" \
  TK_QNP \
  "v_mfma_f32_16x16x32_bf16 v[24:27], v[128:131], a[104:107], v[24:27]\n\t" \
  "ds_read_b128 v[156:159], v2 offset:22528\n\t" \
  "v_mfma_f32_16x16x32_bf16 v[28:31], v[132:135], a[104:107], v[28:31]\n\t" \
  TK_QNP \
  "s_waitcnt lgkmcnt(5)\n\t" \
  "v_mfma_f32_16x16x32_bf16 v[16:19], v[136:139], a[36:39], v[16:19]\n\t" \
  "ds_read_b128 v[160:163], v1 offset:24576\n\t" \
  "s_waitcnt lgkmcnt(5)\n\t" \
  "v_mfma_f32_16x16x32_bf16 v[20:23], v[140:143], a[36:39], v[20:23]\n\t" \
  TK_QNP \
  "v_mfma_f32_16x16x32_bf16 v[24:27], v[136:139], a[108:111], v[24:27]\n\t" \
  "ds_read_b128 v[164:167], v1 offset:26624\n\t" \
  "v_mfma_f32_16x16x32_bf16 v[28:31], v[140:143], a[108:111], v[28:31]\n\t" \
  TK_QNP \
  "s_waitcnt lgkmcnt(5)\n\t" \
  "v_mfma_f32_16x16x32_bf16 v[16:19], v[144:147], a[40:43], v[16:19]\n\t" \
  "ds_read_b128 v[168:171], v2 offset:24576\n\t" \
  "s_waitcnt lgkmcnt(5)\n\t" \
  "v_mfma_f32_16x16x32_bf16 v[20:23], v[148:151], a[40:43], v[20:23]\n\t" \
  TK_QNP \
  "v_mfma_f32_16x16x32_bf16 v[24:27], v[144:147], a[112:115], v[24:27]\n\t" \
  "ds_read_b128 v[172:175], v2 offset:26624\n\t" \
  "v_mfma_f32_16x16x32_bf16 v[28:31], v[148:151], a[112:115], v[28:31]\n\t" \
  TK_QNP \
  "s_waitcnt lgkmcnt(5)\n\t" \
  "v_mfma_f32_16x16x32_bf16 v[16:19], v[152:155], a[44:47], v[16:19]\n\t" \
  "ds_read_b128 v[176:179], v1 offset:28672\n\t" \
  "s_waitcnt lgkmcnt(5)\n\t" \
  "v_mfma_f32_16x16x32_bf16 v[20:23], v[156:159], a[44:47], v[20:23]\n\t" \
  TK_QNP \
  "v_mfma_f32_16x16x32_bf16 v[24:27], v[152:155], a[116:119], v[24:27]\n\t" \
  "ds_read_b128 v[180:183], v1 offset:30720\n\t" \
  "v_mfma_f32_16x16x32_bf16 v[28:31], v[156:159], a[116:119], v[28:31]\n\t" \
  TK_QNP \
  "s_waitcnt lgkmcnt(5)\n\t" \
  "v_mfma_f32_16x16x32_bf16 v[16:19], v[160:163], a[48:51], v[16:19]\n\t" \
  "ds_read_b128 v[184:187], v2 offset:28672\n\t" \
  "s_waitcnt lgkmcnt(5)\n\t" \
  "v_mfma_f32_16x16x32_bf16 v[20:23], v[164:167], a[48:51], v[20:23]\n\t" \
  TK_PQ0 \
  TK_QNP \
  "v_mfma_f32_16x16x32_bf16 v[24:27], v[160:163], a[120:123], v[24:27]\n\t" \
  "ds_read_b128 v[188:191], v2 offset:30720\n\t" \
  "v_mfma_f32_16x16x32_bf16 v[28:31], v[164:167], a[120:123], v[28:31]\n\t" \
  TK_QNP \
  "s_waitcnt lgkmcnt(5)\n\t" \
  "v_mfma_f32_16x16x32_bf16 v[16:19], v[168:171], a[52:55], v[16:19]\n\t" \
  "ds_read_b128 v[192:195], v1 offset:32768\n\t" \
  "s_waitcnt lgkmcnt(5)\n\t" \
  "v_mfma_f32_16x16x32_bf16 v[20:23], v[172:175], a[52:55], v[20:23]\n\t" \
  TK_QNP \
  "v_mfma_f32_16x16x32_bf16 v[24:27], v[168:171], a[124:127], v[24:27]\n\t" \
  "ds_read_b128 v[196:199], v1 offset:34816\n\t" \
  "v_mfma_f32_16x16x32_bf16 v[28:31], v[172:175], a[124:127], v[28:31]\n\t" \
  TK_QNP \
  "s_waitcnt lgkmcnt(5)\n\t" \
  "v_mfma_f32_16x16x32_bf16 v[16:19], v[176:179], a[56:59], v[16:19]\n\t" \
  "ds_read_b128 v[200:203], v2 offset:32768\n\t" \
  "s_waitcnt lgkmcnt(5)\n\t" \
  "v_mfma_f32_16x16x32_bf16 v[20:23], v[180:183], a[56:59], v[20:23]\n\t" \
  TK_QNP \
  "v_mfma_f32_16x16x32_bf16 v[24:27], v[176:179], a[128:131], v[24:27]\n\t" \
  "ds_read_b128 v[204:207], v2 offset:34816\n\t" \
  "v_mfma_f32_16x16x32_bf16 v[28:31], v[180:183], a[128:131], v[28:31]\n\t" \
  TK_QNP \
  "s_waitcnt lgkmcnt(5)\n\t" \
  "v_mfma_f32_16x16x32_bf16 v[16:19], v[184:187], a[60:63], v[16:19]\n\t" \
  "s_waitcnt lgkmcnt(4)\n\t" \
  "v_mfma_f32_16x16x32_bf16 v[20:23], v[188:191], a[60:63], v[20:23]\n\t" \
  "buffer_load_dword v4, v7, %[X], 0 offen sc1\n\t" \
  TK_QNP \
  "v_mfma_f32_16x16x32_bf16 v[24:27], v[184:187], a[132:135], v[24:27]\n\t" \
  "v_mfma_f32_16x16x32_bf16 v[28:31], v[188:191], a[132:135], v[28:31]\n\t" \
  TK_QNP \
  "s_waitcnt lgkmcnt(3)\n\t" \
  "v_mfma_f32_16x16x32_bf16 v[16:19], v[192:195], a[64:67], v[16:19]\n\t" \
  "v_xor_b32 v1, 0x10000, v1\n\t" \
  "v_xor_b32 v2, 0x10000, v2\n\t" \
  "s_waitcnt lgkmcnt(2)\n\t" \
  "v_mfma_f32_16x16x32_bf16 v[20:23], v[196:199], a[64:67], v[20:23]\n\t" \
  TK_QNP \
  "v_mfma_f32_16x16x32_bf16 v[24:27], v[192:195], a[136:139], v[24:27]\n\t" \
  "v_mfma_f32_16x16x32_bf16 v[28:31], v[196:199], a[136:139], v[28:31]\n\t" \
  TK_QNP \
  "s_waitcnt lgkmcnt(1)\n\t" \
  "v_mfma_f32_16x16x32_bf16 v[16:19], v[200:203], a[68:71], v[16:19]\n\t" \
  "s_waitcnt lgkmcnt(0)\n\t" \
  "v_mfma_f32_16x16x32_bf16 v[20:23], v[204:207], a[68:71], v[20:23]\n\t" \
  TK_QNP \
  "v_mfma_f32_16x16x32_bf16 v[24:27], v[200:203], a[140:143], v[24:27]\n\t" \
  "v_mfma_f32_16x16x32_bf16 v[28:31], v[204:207], a[140:143], v[28:31]\n\t" \
  TK_QNP \
  "v_mfma_f32_16x16x32_bf16 v[32:35], v[64:67], a[144:147], 0\n\t" \
  "v_cvt_pk_bf16_f32 v48, v16, v17\n\t" \
  "v_cvt_pk_bf16_f32 v49, v18, v19\n\t" \
  "v_mfma_f32_16x16x32_bf16 v[36:39], v[68:71], a[144:147], 0\n\t" \
  "v_cvt_pk_bf16_f32 v50, v20, v21\n\t" \
  "v_cvt_pk_bf16_f32 v51, v22, v23\n\t" \
  TK_QNP \
  "v_mfma_f32_16x16x32_bf16 v[32:35], v[72:75], a[148:151], v[32:35]\n\t" \
  "v_cvt_pk_bf16_f32 v52, v24, v25\n\t" \
  "v_cvt_pk_bf16_f32 v53, v26, v27\n\t" \
  "v_mfma_f32_16x16x32_bf16 v[36:39], v[76:79], a[148:151], v[36:39]\n\t" \
  "v_cvt_pk_bf16_f32 v54, v28, v29\n\t" \
  "v_cvt_pk_bf16_f32 v55, v30, v31\n\t" \
  TK_QNP \
  "v_mfma_f32_16x16x32_bf16 v[32:35], v[80:83], a[152:155], v[32:35]\n\t" \
  "buffer_store_dwordx4 v[48:51], v0, %[X], s50 offen offset:0 sc1\n\t" \
  "v_mfma_f32_16x16x32_bf16 v[36:39], v[84:87], a[152:155], v[36:39]\n\t" \
  TK_QNP \
  "v_mfma_f32_16x16x32_bf16 v[32:35], v[88:91], a[156:159], v[32:35]\n\t" \
  "v_mfma_f32_16x16x32_bf16 v[36:39], v[92:95], a[156:159], v[36:39]\n\t" \
  TK_QNP \
  "v_mfma_f32_16x16x32_bf16 v[32:35], v[96:99], a[160:163], v[32:35]\n\t" \
  "v_mfma_f32_16x16x32_bf16 v[36:39], v[100:103], a[160:163], v[36:39]\n\t" \
  TK_QNP \
  "v_mfma_f32_16x16x32_bf16 v[32:35], v[104:107], a[164:167], v[32:35]\n\t" \
  "v_mfma_f32_16x16x32_bf16 v[36:39], v[108:111], a[164:167], v[36:39]\n\t" \
  TK_QNP \
  "v_mfma_f32_16x16x32_bf16 v[32:35], v[112:115], a[168:171], v[32:35]\n\t" \
  "v_mfma_f32_16x16x32_bf16 v[36:39], v[116:119], a[168:171], v[36:39]\n\t" \
  TK_QNP \
  "v_mfma_f32_16x16x32_bf16 v[32:35], v[120:123], a[172:175], v[32:35]\n\t" \
  "v_mfma_f32_16x16x32_bf16 v[36:39], v[124:127], a[172:175], v[36:39]\n\t" \
  "s_waitcnt vmcnt(1)\n\t" \
  "v_cmp_eq_u32 vcc, 0, v4\n\t" \
  TK_QNP \
  "v_mfma_f32_16x16x32_bf16 v[32:35], v[128:131], a[176:179], v[32:35]\n\t" \
  "v_mfma_f32_16x16x32_bf16 v[36:39], v[132:135], a[176:179], v[36:39]\n\t" \
  "s_cmp_eq_u64 vcc, 0\n\t" \
  "s_cselect_b32 s60, 1, 0\n\t" \
  "v_mov_b32 v4, s60\n\t" \
  TK_QNP \
  "v_mfma_f32_16x16x32_bf16 v[32:35], v[136:139], a[180:183], v[32:35]\n\t" \
  "ds_write_b32 v5, v4\n\t" \
  "v_mfma_f32_16x16x32_bf16 v[36:39], v[140:143], a[180:183], v[36:39]\n\t" \
  TK_QNP \
  "v_mfma_f32_16x16x32_bf16 v[32:35], v[144:147], a[184:187], v[32:35]\n\t" \
  "v_mfma_f32_16x16x32_bf16 v[36:39], v[148:151], a[184:187], v[36:39]\n\t" \
  "buffer_store_dwordx4 v[52:55], v0, %[X], s50 offen offset:1024 sc1\n\t" \
  TK_QNP \
  "v_mfma_f32_16x16x32_bf16 v[32:35], v[152:155], a[188:191], v[32:35]\n\t" \
  "v_mfma_f32_16x16x32_bf16 v[36:39], v[156:159], a[188:191], v[36:39]\n\t" \
  "s_waitcnt lgkmcnt(0)\n\t" \
  "s_barrier\n\t" \
  TK_QNP \
  "v_mfma_f32_16x16x32_bf16 v[32:35], v[160:163], a[192:195], v[32:35]\n\t" \
  "ds_read_b32 v4, v6 offset:40959\n\t" \
  "v_mfma_f32_16x16x32_bf16 v[36:39], v[164:167], a[192:195], v[36:39]\n\t" \
  TK_QNP \
  "v_mfma_f32_16x16x32_bf16 v[32:35], v[168:171], a[196:199], v[32:35]\n\t" \
  "v_mfma_f32_16x16x32_bf16 v[36:39], v[172:175], a[196:199], v[36:39]\n\t" \
  TK_QNP \
  "v_mfma_f32_16x16x32_bf16 v[32:35], v[176:179], a[200:203], v[32:35]\n\t" \
  "v_mfma_f32_16x16x32_bf16 v[36:39], v[180:183], a[200:203], v[36:39]\n\t" \
  "s_waitcnt lgkmcnt(0)\n\t" \
  "v_readfirstlane_b32 s60, v4\n\t" \
  "s_add_u32 s55, s83, s36\n\t" \
  TK_QNP \
  "v_mfma_f32_16x16x32_bf16 v[32:35], v[184:187], a[204:207], v[32:35]\n\t" \
  "s_cmp_eq_u32 s48, 63\n\t" \
  "s_cselect_b32 s56, 0, s60\n\t" \
  "v_mfma_f32_16x16x32_bf16 v[36:39], v[188:191], a[204:207], v[36:39]\n\t" \
  "s_cmp_eq_u32 s56, 0\n\t" \
  "s_cselect_b32 s55, 0xf0000000, s55\n\t" \
  TK_QNP \
  "v_mfma_f32_16x16x32_bf16 v[32:35], v[192:195], a[208:211], v[32:35]\n\t" \
  "v_mfma_f32_16x16x32_bf16 v[36:39], v[196:199], a[208:211], v[36:39]\n\t" \
  TK_QNP \
  "v_mfma_f32_16x16x32_bf16 v[32:35], v[200:203], a[212:215], v[32:35]\n\t" \
  "s_add_u32 m0, s46, 0\n\t" \
  "s_add_u32 s47, s55, 0\n\t" \
  "buffer_load_dwordx4 v0, %[X], s47 offen sc1 lds\n\t" \
  "v_mfma_f32_16x16x32_bf16 v[36:39], v[204:207], a[212:215], v[36:39]\n\t" \
  TK_QNP \
  "v_mfma_f32_16x16x32_bf16 v[40:43], v[64:67], a[216:219], 0\n\t" \
  "v_mfma_f32_16x16x32_bf16 v[44:47], v[68:71], a[216:219], 0\n\t" \
  TK_QNP \
  "v_mfma_f32_16x16x32_bf16 v[40:43], v[72:75], a[220:223], v[40:43]\n\t" \
  "v_cvt_pk_bf16_f32 v56, v32, v33\n\t" \
  "v_cvt_pk_bf16_f32 v57, v34, v35\n\t" \
  "s_add_u32 m0, s46, 4096\n\t" \
  "s_add_u32 s47, s55, 4096\n\t" \
  "buffer_load_dwordx4 v0, %[X], s47 offen sc1 lds\n\t" \
  "v_mfma_f32_16x16x32_bf16 v[44:47], v[76:79], a[220:223], v[44:47]\n\t" \
  "v_cvt_pk_bf16_f32 v58, v36, v37\n\t" \
  "v_cvt_pk_bf16_f32 v59, v38, v39\n\t" \
  TK_QNP \
  "v_mfma_f32_16x16x32_bf16 v[40:43], v[80:83], a[224:227], v[40:43]\n\t" \
  "v_mfma_f32_16x16x32_bf16 v[44:47], v[84:87], a[224:227], v[44:47]\n\t" \
  "buffer_store_dwordx4 v[56:59], v0, %[X], s50 offen offset:2048 sc1\n\t" \
  TK_QNP \
  "v_mfma_f32_16x16x32_bf16 v[40:43], v[88:91], a[228:231], v[40:43]\n\t" \
  "s_add_u32 m0, s46, 8192\n\t" \
  "s_add_u32 s47, s55, 16384\n\t" \
  "buffer_load_dwordx4 v0, %[X], s47 offen sc1 lds\n\t" \
  "v_mfma_f32_16x16x32_bf16 v[44:47], v[92:95], a[228:231], v[44:47]\n\t" \
  TK_QNP \
  "v_mfma_f32_16x16x32_bf16 v[40:43], v[96:99], a[232:235], v[40:43]\n\t" \
  "v_mfma_f32_16x16x32_bf16 v[44:47], v[100:103], a[232:235], v[44:47]\n\t" \
  TK_QNP \
  "v_mfma_f32_16x16x32_bf16 v[40:43], v[104:107], a[236:239], v[40:43]\n\t" \
  "s_add_u32 m0, s46, 12288\n\t" \
  "s_add_u32 s47, s55, 20480\n\t" \
  "buffer_load_dwordx4 v0, %[X], s47 offen sc1 lds\n\t" \
  "v_mfma_f32_16x16x32_bf16 v[44:47], v[108:111], a[236:239], v[44:47]\n\t" \
  TK_QNP \
  "v_mfma_f32_16x16x32_bf16 v[40:43], v[112:115], a[240:243], v[40:43]\n\t" \
  "v_mfma_f32_16x16x32_bf16 v[44:47], v[116:119], a[240:243], v[44:47]\n\t" \
  TK_QNP \
  "v_mfma_f32_16x16x32_bf16 v[40:43], v[120:123], a[244:247], v[40:43]\n\t" \
  "s_add_u32 m0, s46, 16384\n\t" \
  "s_add_u32 s47, s55, 32768\n\t" \
  "buffer_load_dwordx4 v0, %[X], s47 offen sc1 lds\n\t" \
  "v_mfma_f32_16x16x32_bf16 v[44:47], v[124:127], a[244:247], v[44:47]\n\t" \
  TK_QNP \
  "v_mfma_f32_16x16x32_bf16 v[40:43], v[128:131], a[248:251], v[40:43]\n\t" \
  "v_mfma_f32_16x16x32_bf16 v[44:47], v[132:135], a[248:251], v[44:47]\n\t" \
  TK_QNP \
  "v_mfma_f32_16x16x32_bf16 v[40:43], v[136:139], a[252:255], v[40:43]\n\t" \
  "s_add_u32 m0, s46, 20480\n\t" \
  "s_add_u32 s47, s55, 36864\n\t" \
  "buffer_load_dwordx4 v0, %[X], s47 offen sc1 lds\n\t" \
  "v_mfma_f32_16x16x32_bf16 v[44:47], v[140:143], a[252:255], v[44:47]\n\t" \
  TK_QNP \
  "v_mfma_f32_16x16x32_bf16 v[40:43], v[144:147], v[224:227], v[40:43]\n\t" \
  "v_mfma_f32_16x16x32_bf16 v[44:47], v[148:151], v[224:227], v[44:47]\n\t" \
  TK_QNP \
  "v_mfma_f32_16x16x32_bf16 v[40:43], v[152:155], v[228:231], v[40:43]\n\t" \
  "s_add_u32 m0, s46, 24576\n\t" \
  "s_add_u32 s47, s55, 49152\n\t" \
  "buffer_load_dwordx4 v0, %[X], s47 offen sc1 lds\n\t" \
  "v_mfma_f32_16x16x32_bf16 v[44:47], v[156:159], v[228:231], v[44:47]\n\t" \
  TK_QNP \
  "v_mfma_f32_16x16x32_bf16 v[40:43], v[160:163], v[232:235], v[40:43]\n\t" \
  "v_mfma_f32_16x16x32_bf16 v[44:47], v[164:167], v[232:235], v[44:47]\n\t" \
  TK_QNP \
  "v_mfma_f32_16x16x32_bf16 v[40:43], v[168:171], v[236:239], v[40:43]\n\t" \
  "s_add_u32 m0, s46, 28672\n\t" \
  "s_add_u32 s47, s55, 53248\n\t" \
  "buffer_load_dwordx4 v0, %[X], s47 offen sc1 lds\n\t" \
  "v_mfma_f32_16x16x32_bf16 v[44:47], v[172:175], v[236:239], v[44:47]\n\t" \
  TK_QNP \
  "v_mfma_f32_16x16x32_bf16 v[40:43], v[176:179], v[240:243], v[40:43]\n\t" \
  "v_mfma_f32_16x16x32_bf16 v[44:47], v[180:183], v[240:243], v[44:47]\n\t" \
  TK_QNP \
  "v_mfma_f32_16x16x32_bf16 v[40:43], v[184:187], v[244:247], v[40:43]\n\t" \
  "s_add_u32 m0, s46, 32768\n\t" \
  "s_add_u32 s47, s55, 65536\n\t" \
  "buffer_load_dwordx4 v0, %[X], s47 offen sc1 lds\n\t" \
  "v_mfma_f32_16x16x32_bf16 v[44:47], v[188:191], v[244:247], v[44:47]\n\t" \
  TK_QNP \
  "v_mfma_f32_16x16x32_bf16 v[40:43], v[192:195], v[248:251], v[40:43]\n\t" \
  "v_mfma_f32_16x16x32_bf16 v[44:47], v[196:199], v[248:251], v[44:47]\n\t" \
  TK_QNP \
  "v_mfma_f32_16x16x32_bf16 v[40:43], v[200:203], v[252:255], v[40:43]\n\t" \
  "v_mfma_f32_16x16x32_bf16 v[44:47], v[204:207], v[252:255], v[44:47]\n\t" \
  TK_QNP \
  "s_nop 4\n\t" \
  "v_cvt_pk_bf16_f32 v60, v40, v41\n\t" \
  "v_cvt_pk_bf16_f32 v61, v42, v43\n\t" \
  "s_nop 1\n\t" \
  "v_cvt_pk_bf16_f32 v62, v44, v45\n\t" \
  "v_cvt_pk_bf16_f32 v63, v46, v47\n\t" \
  "buffer_store_dwordx4 v[60:63], v0, %[X], s50 offen offset:3072 sc1\n\t" \
  TK_PQ2 \
  "s_add_u32 s49, s82, s38\n\t" \
  "s_cmp_eq_u32 s48, 63\n\t" \
  "s_cbranch_scc1 LEND_%=\n\t" \
  "s_add_u32 s48, s48, 1\n\t" \
  "s_cmp_eq_u32 s56, 0\n\t" \
  "s_cbranch_scc1 LCOLD_%=\n\t" \
  "s_waitcnt vmcnt(0)\n\t" \
  "s_mov_b64 exec, 1\n\t" \
  "buffer_store_dword v6, off, %[X], s49 sc1\n\t" \
  "s_mov_b64 exec, -1\n\t" \
  "s_mov_b32 s49, 0xf0000000\n\t" \
  "s_cmp_lg_u32 s40, 0\n\t" \
  "s_cbranch_scc1 LTOP_%=\n\t" \
  "s_add_i32 s62, s48, s42\n\t" \
  "s_cmp_lt_i32 s62, 1\n\t" \
  "s_cbranch_scc1 LTOP_%=\n\t" \
  "LGW_%=m:\n\t" \
  "s_sub_u32 s62, s62, " TK_RING "\n\t" \
  "s_cbranch_scc0 LGW_%=m\n\t" \
  "s_add_u32 s62, s62, " TK_RING "\n\t" \
  "s_mul_i32 s63, s62, 43\n\t" \
  "s_lshr_b32 s63, s63, 7\n\t" \
  "s_mul_i32 s62, s62, 688128\n\t" \
  "s_lshl_b32 s60, s63, 15\n\t" \
  "s_add_u32 s62, s62, s60\n\t" \
  "s_lshr_b32 s60, s88, s63\n\t" \
  "s_and_b32 s60, s60, 1\n\t" \
  "s_lshl_b32 s60, s60, 13\n\t" \
  "s_add_u32 s62, s62, s60\n\t" \
  "s_add_u32 s62, s62, 656896\n\t" \
  "s_movk_i32 s63, 4000\n\t" \
  "LGW_%=:\n\t" \
  "buffer_load_dword v4, off, %[X], s62 sc1\n\t" \
  "s_memrealtime s[52:53]\n\t" \
  "s_waitcnt vmcnt(0) lgkmcnt(0)\n\t" \
  "v_readfirstlane_b32 s60, v4\n\t" \
  "s_sub_u32 s63, s63, 1\n\t" \
  "s_cbranch_scc1 LTOP_%=\n\t" \
  "s_cmp_lg_u32 s60, -1\n\t" \
  "s_cbranch_scc1 LGW_%=v\n\t" \
  "s_sleep " TK_PSG "\n\t" \
  "s_branch LGW_%=\n\t" \
  "LGW_%=v:\n\t" \
  "s_not_b32 s60, s60\n\t" \
  "s_lshl_b32 s61, s52, 8\n\t" \
  "s_sub_u32 s60, s60, s61\n\t" \
  "s_ashr_i32 s60, s60, 8\n\t" \
  "s_sub_i32 s60, s60, " TK_QXW "\n\t" \
  "s_min_i32 s60, s60, 400\n\t" \
  "s_add_u32 s54, s52, s60\n\t" \
  "LGW_%=w:\n\t" \
  "s_memrealtime s[52:53]\n\t" \
  "s_waitcnt lgkmcnt(0)\n\t" \
  "s_sub_i32 s53, s52, s54\n\t" \
  "s_cmp_lt_i32 s53, 0\n\t" \
  "s_cbranch_scc1 LGW_%=w\n\t" \
  "s_branch LTOP_%=\n\t" \
  "LEND_%=:\n\t" \
  "s_waitcnt vmcnt(0)\n\t" \
  "s_mov_b64 exec, 1\n\t" \
  "buffer_store_dword v6, off, %[X], s49 sc1\n\t" \
  "s_mov_b64 exec, -1\n\t" \
  "s_mov_b32 s49, 0xf0000000\n\t" \
  TK_PQ0 \
  ""
__device__ __forceinline__ unsigned hoff(int r, int piece) {
  const int m = 4 * (r >> 3) + (r & 3), kp = piece >> 3;
  return (P_H + (unsigned)(kp >> 1)) * PAIR + (unsigned)((kp & 1) * 4096 + (16 * ((r >> 2) & 1) + m) * 128 + (((piece ^ m) & 7) << 4));
}
template <int SLEEP>
__device__ __forceinline__ void wait_all(rsrc_t X, unsigned vo, unsigned so) {
  unsigned spins = 0;
  while (true) {
    asm volatile("" ::: "memory");
    if (__all(ld4(X, vo, so) != 0u)) break;
    if (spin_fail(spins, X)) break;
    __builtin_amdgcn_s_sleep(SLEEP);
  }
  asm volatile("" ::: "memory");
}
#define VQ_CLOB "v16", "v17", "v18", "v19", "v20", "v21", "v22", "v23", "v24", "v25", "v26", "v27", "v28", "v29", "v30", "v31", "v32", "v33", "v34", "v35", "v36", "v37", "v38", "v39", "v40", "v41", "v42", "v43", "v44", "v45", "v46", "v47", "v48", "v49", "v50", "v51", "v52", "v53", "v54", "v55", "v56", "v57", "v58", "v59", "v60", "v61", "v62", "v63", "v64", "v65", "v66", "v67", "v68", "v69", "v70", "v71", "v72", "v73", "v74", "v75", "v76", "v77", "v78", "v79", "v80", "v81", "v82", "v83", "v84", "v85", "v86", "v87", "v88", "v89", "v90", "v91", "v92", "v93", "v94", "v95", "v96", "v97", "v98", "v99", "v100", "v101", "v102", "v103", "v104", "v105", "v106", "v107", "v108", "v109", "v110", "v111", "v112", "v113", "v114", "v115", "v116", "v117", "v118", "v119", "v120", "v121", "v122", "v123", "v124", "v125", "v126", "v127", "v128", "v129", "v130", "v131", "v132", "v133", "v134", "v135", "v136", "v137", "v138", "v139", "v140", "v141", "v142", "v143", "v144", "v145", "v146", "v147", "v148", "v149", "v150", "v151", "v152", "v153", "v154", "v155", "v156", "v157", "v158", "v159", "v160", "v161", "v162", "v163", "v164", "v165", "v166", "v167", "v168", "v169", "v170", "v171", "v172", "v173", "v174", "v175", "v176", "v177", "v178", "v179", "v180", "v181", "v182", "v183", "v184", "v185", "v186", "v187", "v188", "v189", "v190", "v191", "v192", "v193", "v194", "v195", "v196", "v197", "v198", "v199", "v200", "v201", "v202", "v203", "v204", "v205", "v206", "v207", "v220", "v221"
#define VA_CLOB "v40", "v41", "v42", "v43", "v44", "v45", "v46", "v47", "v48", "v49", "v50", "v51", "v52", "v53", "v54", "v55", "v56", "v57", "v58", "v59", "v60", "v61", "v62", "v63", "v64", "v65", "v66", "v67", "v68", "v69", "v70", "v71", "v72", "v73", "v74", "v75", "v76", "v77", "v78", "v79", "v80", "v81", "v82", "v83", "v84", "v85", "v86", "v87", "v88", "v89", "v90", "v91", "v92", "v93", "v94", "v95", "v96", "v97", "v98", "v99", "v100", "v101", "v102", "v103", "v104", "v105", "v106", "v107", "v108", "v109", "v110", "v111", "v112", "v113", "v114", "v115", "v116", "v117", "v118", "v119", "v120", "v121", "v122", "v123", "v124", "v125", "v126", "v127", "v128", "v129", "v130", "v131", "v132", "v133", "v134", "v135", "v136", "v137", "v138", "v139", "v140", "v141", "v142", "v143", "v144", "v145", "v146", "v147", "v148", "v149", "v150", "v151", "v152", "v153", "v154", "v155", "v156", "v157", "v158", "v159", "v160", "v161", "v162", "v163", "v164", "v165", "v166", "v167", "v168", "v169", "v170", "v171", "v172", "v173", "v174", "v175", "v176", "v177", "v178", "v179", "v180", "v181", "v182", "v183", "v184", "v185", "v186", "v187", "v188", "v189", "v190", "v191", "v192", "v193", "v194", "v195", "v196", "v197", "v198", "v199", "v200", "v201", "v202", "v203", "v204", "v205", "v206", "v207", "v208", "v209", "v210", "v211", "v220", "v221"
#define TOK_COMMON() \
  const MP p = getp(); \
  const int c = vblock() - NCHW, xq = c & 3, L = (c >> 2) & 1, i = c >> 3; \
  const int tid = threadIdx.x, lane = tid & 63, w = __builtin_amdgcn_readfirstlane(tid >> 6); \
  const rsrc_t X = mkr(p.xb, XB_BYTES)
#define W_IN [W] "s"(WR)
#define W_CLOB "a0", "a1", "a2", "a3", "a4", "a5", "a6", "a7", "a8", "a9", "a10", "a11", "a12", "a13", "a14", "a15", "a16", "a17", "a18", "a19", "a20", "a21", "a22", "a23", "a24", "a25", "a26", "a27", "a28", "a29", "a30", "a31", "a32", "a33", "a34", "a35", "a36", "a37", "a38", "a39", "a40", "a41", "a42", "a43", "a44", "a45", "a46", "a47", "a48", "a49", "a50", "a51", "a52", "a53", "a54", "a55", "a56", "a57", "a58", "a59", "a60", "a61", "a62", "a63", "a64", "a65", "a66", "a67", "a68", "a69", "a70", "a71", "a72", "a73", "a74", "a75", "a76", "a77", "a78", "a79", "a80", "a81", "a82", "a83", "a84", "a85", "a86", "a87", "a88", "a89", "a90", "a91", "a92", "a93", "a94", "a95", "a96", "a97", "a98", "a99", "a100", "a101", "a102", "a103", "a104", "a105", "a106", "a107", "a108", "a109", "a110", "a111", "a112", "a113", "a114", "a115", "a116", "a117", "a118", "a119", "a120", "a121", "a122", "a123", "a124", "a125", "a126", "a127", "a128", "a129", "a130", "a131", "a132", "a133", "a134", "a135", "a136", "a137", "a138", "a139", "a140", "a141", "a142", "a143", "a144", "a145", "a146", "a147", "a148", "a149", "a150", "a151", "a152", "a153", "a154", "a155", "a156", "a157", "a158", "a159", "a160", "a161", "a162", "a163", "a164", "a165", "a166", "a167", "a168", "a169", "a170", "a171", "a172", "a173", "a174", "a175", "a176", "a177", "a178", "a179", "a180", "a181", "a182", "a183", "a184", "a185", "a186", "a187", "a188", "a189", "a190", "a191", "a192", "a193", "a194", "a195", "a196", "a197", "a198", "a199", "a200", "a201", "a202", "a203", "a204", "a205", "a206", "a207", "a208", "a209", "a210", "a211", "a212", "a213", "a214", "a215", "a216", "a217", "a218", "a219", "a220", "a221", "a222", "a223", "a224", "a225", "a226", "a227", "a228", "a229", "a230", "a231", "a232", "a233", "a234", "a235", "a236", "a237", "a238", "a239", "a240", "a241", "a242", "a243", "a244", "a245", "a246", "a247", "a248", "a249", "a250", "a251", "a252", "a253", "a254", "a255", "v224", "v225", "v226", "v227", "v228", "v229", "v230", "v231", "v232", "v233", "v234", "v235", "v236", "v237", "v238", "v239", "v240", "v241", "v242", "v243", "v244", "v245", "v246", "v247", "v248", "v249", "v250", "v251", "v252", "v253", "v254", "v255"
#define BOOT_IN "{s67}"(L), [ctab] "i"(CTAB_OFF)
#define S_CLOB "s88", "s89", "s90", "s91", "s46", "s47", "s48", "s49", "s50", "s51", "s52", "s53", "s54", "s55", "s56", "s57", "s58", "s59", "s60", "s61", "s62", "s63", "s68", "s69", "s70", "s71", "s72", "s73", "s82", "s83", "s84", "m0", "vcc", "scc", "memory"
typedef unsigned v8w __attribute__((ext_vector_type(8)));

__device__ __forceinline__ void qkv_role() {
  TOK_COMMON();
  const int role = i * 4 + w, hh = 8 * xq + role / 6, part = role % 6, which = part >> 1, half = part & 1;
  const rsrc_t WR = mkr((which == 0 ? p.Wq : (which == 1 ? p.Wk : p.Wv)) + hh * DH + 64 * half, DM * HD * 2u - (unsigned)(hh * DH + 64 * half) * 2u);
  const int m = lane & 15, g = lane >> 4;
  const unsigned a0 = (unsigned)(m * 128 + ((g ^ (m & 7)) << 4));
  v8w lv = {(unsigned)lane * 16u, a0, a0 ^ 64u, (unsigned)(lane & (L ? 15 : 3)) * 128u, 0u, 40960u + 4u * (unsigned)w, 1u, 0u};
  const unsigned c36 = XQL + P_H * PAIR + (unsigned)w * 1024u, c37 = XQ + (unsigned)(3 * hh + which) * PAIR + (unsigned)half * (which == 2 ? 4096u : 2048u);
  const unsigned c38 = P_FA * PAIR + (unsigned)(hh * 128 + part * 16), c39 = w == 0 ? P_FB * PAIR + 4096u : 0xF0000000u, c41 = (unsigned)w * 1024u;
  const int c42 = L ? 1 - QLEAD1 : 1 - QLEAD;
  unsigned d74 = TRC_OFF + 262144u + (unsigned)((c * 4 + w) * 66) * 8u;
  TT(64, 0);
#define Q_CALL(A) asm volatile("s_waitcnt vmcnt(0) lgkmcnt(0)\n\t" A : "+{v[0:7]}"(lv), "+{s74}"(d74) \
               : [X] "s"(X), "{s36}"(c36), "{s37}"(c37), "{s38}"(c38), "{s39}"(c39), "{s40}"(w), "{s41}"(c41), "{s42}"(c42), "{s45}"(i), "{s92}"(TMO_OFF), BOOT_IN, [cap] "i"(SPIN_CAP), [qxw] "i"(QXW), [ring] "i"(SRING), [qxc] "i"(QXC), W_IN : VQ_CLOB, W_CLOB, S_CLOB)
  if (which == 2) { Q_CALL(QV_ASM "LQVEND:"); __builtin_amdgcn_endpgm(); }
  Q_CALL(QK_ASM "LQKEND:");
  __builtin_amdgcn_endpgm();
}

__device__ __forceinline__ void ap_role() {
  TOK_COMMON();
  const int hp = 4 * xq + ((i - 12) >> 1), ch = i & 1, a = w >> 1, rt = w & 1, h = 2 * hp + a;
  const int n = lane & 15, g = lane >> 4;
  const rsrc_t WR = mkr(p.Wo + (long)hp * 2 * DH * DM, 2u * DH * DM * 2u);
  const unsigned l16 = (unsigned)lane * 16u, ab = (unsigned)a * 24576u + l16;
  v16u lv = {l16, ab, ab + (unsigned)rt * 4096u, 49152u + (unsigned)w * 4096u + l16, 49152u + (unsigned)(w ^ 2) * 4096u + l16, l16 & ~32u, l16 >> 1,
             (unsigned)(g * 262144 + n * 16), (unsigned)(n * 32 + g * 8), (unsigned)((lane & 7) % 6) * 16u, 0u, 0u, 0u, 0u, 0u, 0u};
  v8w mk;
  const int qi = 8 * (n >> 2) + (n & 3) + 4 * rt;
#pragma unroll
  for (int k = 0; k < 8; ++k) mk[k] = 8 * g + k > qi ? 0xFF800000u : 0u;
  v16u zz = {0u, 0u, 0u, 0u, 0u, 0u, 0u, 0u, 0u, 0u, 0u, 0u, 0u, 0u, 0u, 0u};
  const unsigned tb = XAL + (unsigned)(3 * h) * PAIR;
  const unsigned c36 = tb + (rt ? PAIR + 4096u : 0u), c37 = tb + (rt ? 2u * PAIR : 4096u), c38 = tb + (rt ? 2u * PAIR + 4096u : PAIR);
  const unsigned c39 = (P_KV + (unsigned)(h >> 1)) * PAIR + (unsigned)(h & 1) * 4096u, c41 = P_FA * PAIR + (unsigned)h * 128u;
  const unsigned c42 = w == 0 ? P_FB * PAIR + (unsigned)(2 * hp + ch) * 128u : 0xF0000000u;
  const unsigned c43 = XA + TP0 * XBLK + (unsigned)(8 * rt + (hp >> 3)) * PAIR + (unsigned)((hp & 7) * 1024 + ch * 512 + a * 256);
  unsigned d44 = (TP0 + 8u) * XBLK + (unsigned)(hp >> 1) * PAIR + (unsigned)((((hp & 1) * 4 + 2 * ch + a) * 2 + rt) * 512);
  const unsigned c45 = (unsigned)(a * 24576 + rt * 12288);
  unsigned d74 = TRC_OFF + 262144u + (unsigned)((c * 4 + w) * 66) * 8u;
  asm volatile("s_waitcnt vmcnt(0) lgkmcnt(0)\n\t" AP_ASM "LAPEND:" : "+{v[0:15]}"(lv), "+{v[24:39]}"(zz), "+{s74}"(d74), "+{s44}"(d44)
               : [X] "s"(X), "{s36}"(c36), "{s37}"(c37), "{s38}"(c38), "{s39}"(c39), "{s41}"(c41), "{s42}"(c42), "{s43}"(c43), "{s45}"(c45), "{s64}"(512 * ch + 256 * a), "{s65}"(1024 + 64 * ch + 32 * a), "{s66}"(147456 * a), BOOT_IN,
                 "{s92}"(TMO_OFF), [cap] "i"(SPIN_CAP), "{v[16:23]}"(mk), W_IN : VA_CLOB, W_CLOB, S_CLOB);
  __builtin_amdgcn_endpgm();
}

template <int N> __device__ __forceinline__ float rorN(float v) { return __uint_as_float((unsigned)__builtin_amdgcn_update_dpp(0, (int)__float_as_uint(v), 0x120 + N, 0xf, 0xf, false)); }
__device__ __forceinline__ void up8(v4u v, float* x) {
  x[0] = lo2f(v.x); x[1] = hi2f(v.x); x[2] = lo2f(v.y); x[3] = hi2f(v.y); x[4] = lo2f(v.z); x[5] = hi2f(v.z); x[6] = lo2f(v.w); x[7] = hi2f(v.w);
}
__device__ __forceinline__ v4u pk8(const float* x) { return (v4u){pk2(x[0], x[1]), pk2(x[2], x[3]), pk2(x[4], x[5]), pk2(x[6], x[7])}; }

__device__ __forceinline__ void rb9(float (&x)[9]) {
#pragma unroll
  for (int i = 0; i < 8; i += 2) { const unsigned u = pk2(x[i], x[i + 1]); x[i] = lo2f(u); x[i + 1] = hi2f(u); }
  x[8] = lo2f(pk2(x[8], x[8]));
}
template <int R>
__device__ __forceinline__ void lnR(const float (&x)[R][9], float (&o)[R][9]) {
  float s[2 * R], pv[R];
#pragma unroll
  for (int r = 0; r < R; ++r) {
    pv[r] = __uint_as_float(__builtin_amdgcn_readfirstlane(__float_as_uint(x[r][0])));
    const f32x2 p2 = {pv[r], pv[r]};
    f32x2 d[4];
#pragma unroll
    for (int i = 0; i < 4; ++i) d[i] = (f32x2){x[r][2 * i], x[r][2 * i + 1]} - p2;
    const float d8 = x[r][8] - pv[r];
    const f32x2 a = (d[0] + d[1]) + (d[2] + d[3]);
    const f32x2 q = __builtin_elementwise_fma(d[3], d[3], __builtin_elementwise_fma(d[2], d[2], __builtin_elementwise_fma(d[1], d[1], d[0] * d[0])));
    s[2 * r] = (a[0] + d8) + a[1];
    s[2 * r + 1] = __builtin_fmaf(d8, d8, q[0]) + q[1];
  }
#pragma unroll
  for (int k = 0; k < 2 * R; ++k) s[k] += rorN<8>(s[k]);
#pragma unroll
  for (int k = 0; k < 2 * R; ++k) s[k] += rorN<4>(s[k]);
#pragma unroll
  for (int k = 0; k < 2 * R; ++k) s[k] += rorN<2>(s[k]);
#pragma unroll
  for (int k = 0; k < 2 * R; ++k) s[k] += rorN<1>(s[k]);
#pragma unroll
  for (int r = 0; r < R; ++r) {
    auto u = __builtin_amdgcn_permlane16_swap(__float_as_uint(s[2 * r]), __float_as_uint(s[2 * r + 1]), false, false);
    s[2 * r] = __uint_as_float(u[0]) + __uint_as_float(u[1]);
  }
#pragma unroll
  for (int r = 0; r < R; ++r) {
    const unsigned u = __float_as_uint(s[2 * r]);
    const float md = (__uint_as_float(__builtin_amdgcn_readlane(u, 0)) + __uint_as_float(__builtin_amdgcn_readlane(u, 32))) * (1.0f / DM);
    const float sq = (__uint_as_float(__builtin_amdgcn_readlane(u, 16)) + __uint_as_float(__builtin_amdgcn_readlane(u, 48))) * (1.0f / DM);
    const unsigned mv = pk2(pv[r] + md, __builtin_fmaxf(__builtin_fmaf(-md, md, sq), 0.f));
    const float m = lo2f(mv), var = hi2f(mv);
    const float ve = lo2f(pk2(var + 1e-5f, 0.f));
    const float den = lo2f(pk2(__builtin_amdgcn_sqrtf(ve), 0.f));
    const float inv = __builtin_amdgcn_rcpf(den);
#pragma unroll
    for (int i = 0; i < 9; ++i) o[r][i] = (x[r][i] - m) * inv;
  }
}
__device__ __forceinline__ void ln9(const float (&x)[9], float (&o)[9]) { lnR<1>((const float (&)[1][9])x, (float (&)[1][9])o); }
__device__ __forceinline__ v4u gat8(float v) {
  const unsigned d = pk2(v, __uint_as_float((unsigned)__builtin_amdgcn_mov_dpp((int)__float_as_uint(v), 0xB1, 0xf, 0xf, true)));
  return (v4u){d, (unsigned)__builtin_amdgcn_mov_dpp((int)d, 0x102, 0xf, 0xf, true), (unsigned)__builtin_amdgcn_mov_dpp((int)d, 0x104, 0xf, 0xf, true),
               (unsigned)__builtin_amdgcn_mov_dpp((int)d, 0x106, 0xf, 0xf, true)};
}
__device__ __forceinline__ void tok_row(const MP& p, const rsrc_t X, unsigned sb1, int L, int s, int r, int lane, const v4u (&v)[18], v4u px, v4u py, unsigned px9, unsigned py9) {
  const long tok = (long)s * SEG + r;
  const int m = lane & 15, g = lane >> 4;
  const int xp = 4 * (g & 1) + 2 * (g >> 1) + (m >> 3);
  const unsigned zr = opq(0u);
  const unsigned one = (g == (m >> 2)) ? (opq(0x3F80u) << (16 * (m & 1))) : zr;
  const unsigned ax = (m & 2) ? zr : one, ay = (m & 2) ? one : zr;
  const v4u A1 = {ax, ay, zr, zr}, A2 = {zr, zr, ax, ay};
  const unsigned on2 = opq(0x3F80u) << (16 * (m & 1));
  const int q2 = (m >> 1) & 3;
  const v4u sel = {q2 == 0 ? on2 : zr, q2 == 1 ? on2 : zr, q2 == 2 ? on2 : zr, q2 == 3 ? on2 : zr};
  f32x4 d1, d2, e1;
  asm volatile("s_nop 1\n\t" MF "%0, %1, %3, 0\n\t" MF "%0, %2, %3, %0" : "=&v"(e1) : "v"(v[0]), "v"(v[1]), "v"(sel));
#pragma unroll
  for (int i = 0; i < 16; ++i) {
    if (i == 0) asm volatile("s_nop 1\n\t" MF "%0, %2, %4, 0\n\t" MF "%1, %3, %4, 0" : "=&v"(d1), "=&v"(d2) : "v"(A1), "v"(A2), "v"(v[2]));
    else asm volatile(MF "%0, %2, %4, %0\n\t" MF "%1, %3, %4, %1" : "+v"(d1), "+v"(d2) : "v"(A1), "v"(A2), "v"(v[2 + i]));
  }
  asm volatile("s_nop 7" : "+v"(d1), "+v"(d2), "+v"(e1));
  float o[9];
#pragma unroll
  for (int i = 0; i < 4; ++i) { o[i] = d1[i]; o[4 + i] = d2[i]; }
  {
    auto a02 = __builtin_amdgcn_permlane32_swap(__float_as_uint(e1[0]), __float_as_uint(e1[2]), false, false);
    auto a13 = __builtin_amdgcn_permlane32_swap(__float_as_uint(e1[1]), __float_as_uint(e1[3]), false, false);
    o[8] = m >= 8 ? __uint_as_float(a13[0]) + __uint_as_float(a13[1]) : __uint_as_float(a02[0]) + __uint_as_float(a02[1]);
  }
  float x[9];
  up8(px, x); x[8] = lo2f(px9);
#pragma unroll
  for (int i = 0; i < 9; ++i) x[i] += o[i];
  if (L == 0) {
    float y[9], ln[9];
    up8(py, y); y[8] = lo2f(py9);
    rb9(x);
    ln9(x, ln);
#pragma unroll
    for (int i = 0; i < 9; ++i) x[i] = __builtin_fmaf(ln[i], y[i], x[i]);
    rb9(x);
    st16(X, (unsigned)lane * 16u, sb1 + (P_X3 + (unsigned)(r >> 3)) * PAIR + (unsigned)(r & 7) * 1024u, pk8(x));
    const v4u x9 = gat8(x[8]);
    if ((lane & 7) == 0) st16(X, (unsigned)xp * 16u, sb1 + (P_H + 4u) * PAIR + 4096u + (unsigned)r * 128u, x9);
    ln9(x, ln);
    st16(X, hoff(r, lane), sb1, pk8(ln));
    const v4u h9 = gat8(ln[8]);
    if ((lane & 7) == 0) st16(X, hoff(r, 64 + xp), sb1, h9);
  } else {
    *(v4u*)&p.out[tok * DM + 8 * lane] = pk8(x);
    const v4u o9 = gat8(x[8]);
    if ((lane & 7) == 0) *(v4u*)&p.out[tok * DM + 512 + 8 * xp] = o9;
  }
}

__device__ __forceinline__ void row_role(char* smem) {
  TOK_COMMON();
  const int rw = 4 * xq + (i - 20), r = 2 * rw + (w >> 1);
  const unsigned long long sm = die_mask(X, SG0, L, lane);
  const unsigned tm = (unsigned)die_mask(X, TP0, L, lane), dm = (unsigned)die_mask(X, 0u, L, lane);
  __attribute__((address_space(3))) unsigned* cnt = (__attribute__((address_space(3))) unsigned*)smem;
  BAR();
  if (tid < 64) cnt[tid] = 0u;
  BAR();
  for (int s = w & 1; s < NSEG; s += 2) {
    int ln_ = lane; asm volatile("" : "+v"(ln_));
    const int m = ln_ & 15, g = ln_ >> 4;
    const unsigned l16 = (unsigned)ln_ * 16u, xo = (unsigned)(8 * (4 * (g & 1) + 2 * (g >> 1) + (m >> 3)) + (m & 7)) * 2u;
    const unsigned sb = SEGB(sm, s), sb1 = sb ^ (L ? 0u : 8192u);
    if (L) __builtin_amdgcn_s_sleep(ARMD1); else __builtin_amdgcn_s_sleep(ARMD);
    if (s + ARMK < NSEG) {
      const unsigned sv = opq(SENT), sl = SLOT_AT((s + ARMK) % SRING) + ((dm >> ((s + ARMK) % SRING / 3)) & 1u) * 8192u;
      for (int j = r; j < 84; j += 32) {
#pragma unroll
        for (int k = 0; k < 4; ++k) { st16(X, l16 + (unsigned)k * 1024u, sl + (unsigned)(j >> 1) * PAIR + (unsigned)(j & 1) * 4096u, (v4u){sv, sv, sv, sv}); if (ARMP) __builtin_amdgcn_s_sleep(ARMP); }
      }
    }
    if (ARMS) __builtin_amdgcn_s_sleep(ARMS);
    wait_all<PSL>(X, 0u, sb + P_FB * PAIR + 4096u + (unsigned)(L ? rw : r >> 3) * 128u);
    const v4u px = ld16(X, l16, sb + (P_X3 + (unsigned)(r >> 3)) * PAIR + (unsigned)(r & 7) * 1024u);
    const unsigned px9 = __builtin_amdgcn_raw_buffer_load_b16(X, xo, sb + (P_H + 4u) * PAIR + 4096u + (unsigned)r * 128u, 16);
    const rsrc_t YX = mkr(p.yT + (2 * s + (r >> 4)) * 9216, L ? 0u : 18432u);
    const v2u ya = ld8(YX, (unsigned)(ln_ * 256 + ((r ^ m) & 15) * 8), 0u), yb = ld8(YX, (unsigned)(ln_ * 256 + 128 + ((r ^ m) & 15) * 8), 0u);
    const v4u py = {ya.x, ya.y, yb.x, yb.y};
    const unsigned py9 = __builtin_amdgcn_raw_buffer_load_b16(YX, 16384u + (xo >> 3) * 128u + (((unsigned)r ^ (xo >> 4)) & 15u) * 8u + (xo & 6u), 0u, 16);
    if (ARMT) { if (L) __builtin_amdgcn_s_sleep(ARMT + 22); else __builtin_amdgcn_s_sleep(ARMT); }
    if (__builtin_amdgcn_readfirstlane(ld4(X, 0u, sb + P_FA * PAIR + 4096u + (unsigned)r * 128u)) == 0u) {
      wait_all<PSR>(X, 0u, sb + P_FA * PAIR + 4096u + (unsigned)r * 128u);
      __builtin_amdgcn_s_sleep(RDLY);
    }
    if (RONE) { wait_all<PSP>(X, 0u, sb + P_FB * PAIR + (unsigned)r * 128u); __builtin_amdgcn_s_sleep(RONE); }
    wait_all<PSP>(X, (unsigned)(ln_ & 31) * 128u, sb + P_FB * PAIR);
    TT(s, 0);
    const unsigned j = (unsigned)s & 15u, to = XRL + TP0 * XBLK + (j << 20) + ((tm >> (j >> 1)) & 1u) * 8192u + (unsigned)r * 2u * PAIR;
    const unsigned t9 = (TP0 + 8u) * XBLK + ((tm >> 8) & 1u) * 8192u + (j << 17) + (unsigned)(((r >> 2) & 1) * 512 + (4 * (r >> 3) + (r & 3)) * 32);
    const unsigned v9 = (unsigned)(g * (int)PAIR + (m >> 1) * 1024 + (m & 1) * 16);
    v4u v[18];
#pragma unroll
    for (int k = 0; k < 2; ++k) v[k] = ld16(X, v9, t9 + (unsigned)k * 4u * PAIR);
#pragma unroll
    for (int k = 0; k < 16; ++k) v[2 + k] = ld16(X, l16 + (unsigned)(k & 3) * 1024u, to + (unsigned)(k >> 3) * PAIR + (unsigned)(k & 4) * 1024u);
    SB();
    tok_row(p, X, sb1, L, s, r, ln_, v, px, py, px9, py9);
    VM0();
    unsigned o1 = 0u;
    if (ln_ == 0) o1 = __hip_atomic_fetch_add(cnt + s, 1u, __ATOMIC_RELAXED, __HIP_MEMORY_SCOPE_WORKGROUP);
    if (__builtin_amdgcn_readfirstlane(o1) == 1u && ln_ < 2 - L) st4(X, ln_ ? sb1 : sb + 2048u, P_FB * PAIR + 4096u + (unsigned)rw * 128u, opq(1u));
    TT(s, 1);
  }
}

__device__ __forceinline__ void rows_task(const MP& p, const rsrc_t X, unsigned long long sm, u16* ys, int grp, int ng) {
  const int tid = threadIdx.x, lane = tid & 63, w = __builtin_amdgcn_readfirstlane(tid >> 6);
  const int m = lane & 15, g = lane >> 4;
  const int xp = 4 * (g & 1) + 2 * (g >> 1) + (m >> 3), c9 = 512 + 8 * xp + (m & 7);
  const unsigned sb = SEGB(sm, grp >> 2);
  v4u yv[5], xa[4];
  unsigned x9[4];
#pragma unroll
  for (int i = 0; i < 5; ++i) yv[i] = ld16(mkr(p.yT + (grp >> 1) * 9216, 18432u), (unsigned)(tid + 256 * i) * 16u, 0u);
#pragma unroll
  for (int rr = 0; rr < 4; ++rr) {
    const long t = grp * 8 + w + 4 * (rr & (2 * ng - 1));
    xa[rr] = *(const v4u*)&p.xin[t * DM + 8 * lane]; x9[rr] = p.xin[t * DM + c9];
  }
#pragma unroll
  for (int i = 0; i < 5; ++i) ((v4u*)ys)[tid + 256 * i] = yv[i];
  BAR();
  SP(13 - 3 * ng);
  const unsigned o9 = (lane & 7) == 0 ? 0u : 0xF0000000u;
#pragma unroll 1
  for (int k = 0; k < ng; ++k) {
    float x[2][9], y[2][9], ln[2][9];
    const int t0 = (grp + k) * 8 + w;
#pragma unroll
    for (int rr = 0; rr < 2; ++rr) {
      const int j = (t0 + 4 * rr) & 15;
      const v2u ya = *(const v2u*)&ys[lane * 128 + (j ^ m) * 4], yb = *(const v2u*)&ys[lane * 128 + 64 + (j ^ m) * 4];
      up8(xa[rr], x[rr]); x[rr][8] = lo2f(x9[rr]); up8((v4u){ya.x, ya.y, yb.x, yb.y}, y[rr]); y[rr][8] = bf2f(ys[(c9 >> 2) * 64 + (j ^ xp) * 4 + (m & 3)]);
      xa[rr] = xa[2 + rr]; x9[rr] = x9[2 + rr];
    }
    lnR<2>(x, ln);
#pragma unroll
    for (int rr = 0; rr < 2; ++rr) {
#pragma unroll
      for (int i = 0; i < 9; ++i) x[rr][i] = __builtin_fmaf(ln[rr][i], y[rr][i], x[rr][i]);
      rb9(x[rr]);
      const int r = (t0 + 4 * rr) & 31;
      st16(X, (unsigned)lane * 16u, sb + (P_X3 + (unsigned)(r >> 3)) * PAIR + (unsigned)(r & 7) * 1024u, pk8(x[rr]));
      st16(X, (unsigned)xp * 16u + o9, sb + (P_H + 4u) * PAIR + 4096u + (unsigned)r * 128u, gat8(x[rr][8]));
    }
    lnR<2>(x, ln);
#pragma unroll
    for (int rr = 0; rr < 2; ++rr) {
      const int r = (t0 + 4 * rr) & 31;
      st16(X, hoff(r, lane), sb, pk8(ln[rr]));
      st16(X, hoff(r, 64 + xp) + o9, sb, gat8(ln[rr][8]));
    }
  }
  SP(14 - 3 * ng);
  VM0();
  BAR();
  SP(15 - 3 * ng);
  if (tid < ng) st4(X, (unsigned)tid * 128u, sb + P_FB * PAIR + 4096u + (unsigned)(grp & 3) * 128u, opq(1u));
}

__device__ __forceinline__ void arrive(const MP& p, int k, int grp) {
  VM0();
  BAR();
  if (threadIdx.x < 64) {
    unsigned old = 0u;
    if (threadIdx.x == 0) old = __hip_atomic_fetch_add(p.xb + MF_OFF / 4u + (k * 16 + grp) * 32, 1u, __ATOMIC_RELAXED, __HIP_MEMORY_SCOPE_AGENT);
    if (__builtin_amdgcn_readfirstlane(old) == 15u && threadIdx.x < 8) st4(mkr(p.xb, XB_BYTES), threadIdx.x * 128u, RDY_OFF(k) + (unsigned)grp * 1024u, opq(1u));
  }
}
__device__ __forceinline__ void wait_ready(const MP& p, int k) {
  if (threadIdx.x < 64) wait_all<PSL>(mkr(p.xb, XB_BYTES), (threadIdx.x % 9u) * 1024u, RDY_OFF(k) + (blockIdx.x & 7u) * 128u);
  BAR();
}
__device__ __forceinline__ void start_work(char* smem, int wk) {
  const MP p = getp();
  const int w = __builtin_amdgcn_readfirstlane(threadIdx.x >> 6);
#define NARM (ARMK - 3u)
  conv_task(p.kvT, p.ssm_k, p.ssm_D, p.yT, (u16*)(smem + w * 32768), 4 * wk + w, [&]() {
    const unsigned sv = opq(SENT);
    constexpr unsigned S16 = SLOT / 16u;
#pragma unroll
    for (unsigned k = 0; k < (NARM * S16 + 36863u) / 36864u; ++k) {
      const unsigned i = k * 36864u + wk * 256 + threadIdx.x;
      st16(mkr(p.xb, XB_BYTES), i < NARM * S16 ? SLOT_AT(3u + i / S16) + (i % S16) * 16u : 0xF0000000u, 0u, (v4u){sv, sv, sv, sv});
    }
  });
  SP(3);
  arrive(p, 0, wk >> 4);
  SP(4);
}
__device__ __forceinline__ void token_start(char* smem) {
  TOK_COMMON();
  (void)w;
  const int k = 4 * i + xq;
  if (L || (i >= 12 && i < 16)) start_work(smem, L ? 32 + k : 80 + k);
  if (i >= (L ? 12 : 20)) {
    const unsigned long long sm = die_mask(X, SG0, 0, lane);
    int g = L ? 111 - k : k - 96;
    while (g < 120) {
      if (g >= 0 && g < 64) {
        SP(5);
        wait_ready(p, 0);
        SP(6);
      }
      rows_task(p, X, sm, (u16*)smem, g < 0 ? g + 16 : 16 + 2 * g, g < 0 ? 1 : 2);
      g += g < 0 ? 16 : (L ? 48 : 120);
    }
  }
}
__device__ __forceinline__ void token_wg(char* smem) {
  if ((unsigned)(size_t)(__attribute__((address_space(3))) char*)smem != 0u) return;
  token_start(smem);
  const int i = (vblock() - NCHW) >> 3;
  if (i < 12) qkv_role();
  else if (i < 20) ap_role();
  else row_role(smem);
}

__global__ __launch_bounds__(256, 1) void k_main(MP) {
  extern __shared__ __attribute__((aligned(16))) char smem[];
  asm volatile("LKBEG:");
  SP(13);
  if (vblock() < NCHW) chain_wg(smem);
  else token_wg(smem);
}

extern "C" void kernel_launch(void* const* d_in, const int* in_sizes, int n_in, void* d_out, int out_size, void* d_ws,
                              size_t ws_size, hipStream_t stream) {
  const u16* A_q = (const u16*)d_in[0];
  const u16* A_k = (const u16*)d_in[1];
  const u16* A_v = (const u16*)d_in[2];
  const u16* A_shift = (const u16*)d_in[3];
  const u16* A_ssmk = (const u16*)d_in[4];
  const u16* A_ssmD = (const u16*)d_in[5];
  const u16* B_mem0 = (const u16*)d_in[7];
  const u16* B_Wq = (const u16*)d_in[8];
  const u16* B_Wk = (const u16*)d_in[9];
  const u16* B_Wv = (const u16*)d_in[10];
  const u16* B_Wo = (const u16*)d_in[11];

  char* ws = (char*)d_ws;
  size_t off = (size_t)(-(uintptr_t)ws) & (XBLK - 1u);
  auto alloc = [&](size_t bytes) { char* q = ws + off; off += (bytes + 4095) & ~(size_t)4095; return q; };
  unsigned* xb = (unsigned*)alloc((size_t)XB_BYTES);
  u16* kvT = (u16*)alloc((size_t)DM * S_LEN * 2);
  u16* yT = (u16*)alloc((size_t)DM * S_LEN * 2);

  PreP pp;
  pp.kk = A_k; pp.vv = A_v; pp.shift_k = A_shift; pp.ssm_k = A_ssmk; pp.ssm_D = A_ssmD; pp.yT = yT; pp.kvT = kvT;
  pp.Wq = B_Wq; pp.Wk = B_Wk; pp.Wv = B_Wv; pp.Wo = B_Wo; pp.xb = xb; pp.mem0 = B_mem0;
  k_pre<<<240 + PRE_FILL + 256 + 288, 256, 0, stream>>>(pp);
  MP mp;
  mp.out = (u16*)d_out; mp.xb = xb;
  mp.xin = A_q; mp.yT = yT; mp.kvT = kvT; mp.ssm_k = A_ssmk; mp.ssm_D = A_ssmD;
  mp.Wq = B_Wq; mp.Wk = B_Wk; mp.Wv = B_Wv; mp.Wo = B_Wo;
  const int smem_bytes = 160 * 1024;
  (void)hipFuncSetAttribute((const void*)k_main, hipFuncAttributeMaxDynamicSharedMemorySize, smem_bytes);
  k_main<<<NCHW + NTOKW, 256, smem_bytes, stream>>>(mp);
}
```

```cpp
#include <hip/hip_runtime.h>
#include <stdint.h>

typedef unsigned short u16;
using bf16x8 = __attribute__((ext_vector_type(8))) short;
using f32x4 = __attribute__((ext_vector_type(4))) float;
using f32x2 = __attribute__((ext_vector_type(2))) float;
using bfx8 = __attribute__((ext_vector_type(8))) __bf16;
using bfx2 = __attribute__((ext_vector_type(2))) __bf16;
typedef unsigned v4u __attribute__((ext_vector_type(4)));
typedef unsigned v2u __attribute__((ext_vector_type(2)));
typedef short v4s __attribute__((ext_vector_type(4)));

#define S_LEN 2048
#define DM 576
#define NH 32
#define DH 128
#define HD (NH * DH)
#define SEG 32
#define NSEG 64

__device__ __forceinline__ float bf2f(u16 x) { return __uint_as_float(((unsigned)x) << 16); }
__device__ __forceinline__ unsigned pk2(float a, float b) { f32x2 v = {a, b}; bfx2 r = __builtin_convertvector(v, bfx2); return __builtin_bit_cast(unsigned, r); }
__device__ __forceinline__ float lo2f(unsigned u) { return __uint_as_float(u << 16); }
__device__ __forceinline__ float hi2f(unsigned u) { return __uint_as_float(u & 0xFFFF0000u); }

#define NFC 128
#define XBLK 2097152u
#define PAIR 16384u
#define SLOT 688128u
#define AGO 589824u
#define AGT 655360u
#define SG0 22
#define TP0 86
#define NXB 95
#define XB_BYTES (NXB * XBLK)
#define SLOT_AT(i) ((unsigned)((i) / 3) * XBLK + (unsigned)((i) % 3) * SLOT)
#define HT_OFF SLOT_AT(65)
#define CTAB_OFF (HT_OFF + 16384u)
#define MF_OFF (HT_OFF + 20480u)
#define TMO_OFF (MF_OFF + 15u * 128u)
#define RDY_OFF(k) (MF_OFF + 4096u + (k) * 10240u)
#define MF_BYTES 16384u
#define TRC_OFF SLOT_AT(64)
#define CAL_OFF (XBLK - 16384u)
#define P_KV 96u
#define P_H 112u
#define P_X3 117u
#define P_FA 121u
#define P_FB 122u
#define ARMK 8
#define NCAL 8
__device__ __forceinline__ unsigned kv2(const unsigned (&u)[4], unsigned v, v2u w0, v2u w1) {
  const float a = __builtin_fmaf(lo2f(u[3]), lo2f(w0.x), __builtin_fmaf(lo2f(u[2]), hi2f(w0.x), __builtin_fmaf(lo2f(u[1]), lo2f(w0.y), lo2f(u[0]) * hi2f(w0.y))));
  const float b = __builtin_fmaf(hi2f(u[3]), lo2f(w1.x), __builtin_fmaf(hi2f(u[2]), hi2f(w1.x), __builtin_fmaf(hi2f(u[1]), lo2f(w1.y), hi2f(u[0]) * hi2f(w1.y))));
  return pk2(a * lo2f(v), b * hi2f(v));
}
struct PreP {
  const u16* kk; const u16* vv; const u16* shift_k; const u16* ssm_k; const u16* ssm_D; u16* yT; u16* kvT;
  const u16* Wq; const u16* Wk; const u16* Wv; const u16* Wo; unsigned* xb; const u16* mem0;
};
#define PRE_FILL 112
#define PFN 5
__global__ __launch_bounds__(256, 4) void k_pre(PreP p) {
  __shared__ __attribute__((aligned(16))) float sh[64 * 36];
  const int tid = threadIdx.x, b0 = blockIdx.x, b = b0 < 240 ? b0 : (b0 < 528 ? b0 + PRE_FILL + 256 : b0 - 288);
  const __amdgpu_buffer_rsrc_t rx = __builtin_amdgcn_make_buffer_rsrc((void*)p.xb, 0, XB_BYTES, 0x00020000);
  const v4u ones = {0xFFFFFFFFu, 0xFFFFFFFFu, 0xFFFFFFFFu, 0xFFFFFFFFu}, zero = {0, 0, 0, 0};
  if (b < 96) {
    if (tid >= 64) return;
    const unsigned cb = (unsigned)b * XBLK + CAL_OFF;
    __builtin_amdgcn_raw_buffer_store_b128(zero, rx, (unsigned)(tid & 1) * 8192u + (unsigned)(tid >> 1) * 256u, cb, 16);
    asm volatile("s_waitcnt vmcnt(0)" ::: "memory");
    unsigned mn[2] = {~0u, ~0u};
    for (int i = 0; i < NCAL; ++i) {
      const unsigned long long c0 = __builtin_amdgcn_s_memtime();
      asm volatile("s_waitcnt lgkmcnt(0)" ::: "memory");
      const unsigned v = __builtin_amdgcn_raw_buffer_load_b32(rx, 0, cb + (unsigned)(i & 1) * 8192u + (unsigned)(i >> 1) * 256u, 16);
      asm volatile("s_waitcnt vmcnt(0)" :: "v"(v) : "memory");
      const unsigned d = (unsigned)(__builtin_amdgcn_s_memtime() - c0);
      if (i & 1) mn[1] = d < mn[1] ? d : mn[1]; else mn[0] = d < mn[0] ? d : mn[0];
    }
    unsigned xcc; asm volatile("s_getreg_b32 %0, hwreg(HW_REG_XCC_ID)" : "=s"(xcc));
    const unsigned res = mn[1] < mn[0] ? 1u : 0u;
    if (tid == 0) __builtin_amdgcn_raw_buffer_store_b32(res ^ ((xcc >> 2) & 1u), rx, 0, CTAB_OFF + (unsigned)b * 4u, 16);
  } else if (b < 240) {
    const __amdgpu_buffer_rsrc_t rk = __builtin_amdgcn_make_buffer_rsrc((void*)p.kk, 0, S_LEN * DM * 2, 0x00020000);
    float (*kx)[132] = (float (*)[132])sh;
    float (*cf)[140] = (float (*)[140])(sh + 4 * 132);
    const int d0 = (b - 96) * 4, q = tid & 3, dd = (tid >> 2) & 3, t0 = (tid >> 4) * 8;
    const float Dv = bf2f(p.ssm_D[d0 + dd]);
    {
      const int s = tid >> 1, d = d0 + (tid & 1) * 2;
      unsigned u[4];
#pragma unroll
      for (int a = 0; a < 4; ++a) u[a] = __builtin_amdgcn_raw_buffer_load_b32(rk, (unsigned)((s - 3 + a) * DM + d) * 2u, 0, 0);
      const unsigned vr = *(const unsigned*)&p.vv[s * DM + d], c0 = *(const unsigned*)&p.ssm_k[(long)(d0 + (tid >> 6)) * S_LEN + (tid & 63) * 2];
      const v4u w = *(const v4u*)&p.shift_k[d * 4];
      const unsigned r = kv2(u, vr, (v2u){w.x, w.y}, (v2u){w.z, w.w});
      kx[(tid & 1) * 2][s] = lo2f(r); kx[(tid & 1) * 2 + 1][s] = hi2f(r);
      *(f32x2*)&cf[tid >> 6][8 + (tid & 63) * 2] = (f32x2){lo2f(c0), hi2f(c0)};
      if (tid < 32) cf[tid >> 3][tid & 7] = 0.f;
    }
    __syncthreads();
    float y[8];
#pragma unroll
    for (int i = 0; i < 8; ++i) y[i] = 0.f;
#pragma unroll 1
    for (int j = 8 * q; j <= t0; j += 32) {
      f32x4 kk[2], cw[4];
#pragma unroll
      for (int x = 0; x < 2; ++x) kk[x] = *(const f32x4*)&kx[dd][j + 4 * x];
#pragma unroll
      for (int x = 0; x < 4; ++x) cw[x] = *(const f32x4*)&cf[dd][t0 - j + 4 * x];
#pragma unroll
      for (int jj = 0; jj < 8; ++jj)
#pragma unroll
        for (int i = 0; i < 8; ++i) y[i] = __builtin_fmaf(cw[(8 + i - jj) >> 2][(8 + i - jj) & 3], kk[jj >> 2][jj & 3], y[i]);
    }
#pragma unroll
    for (int i = 0; i < 8; ++i) {
      y[i] += __uint_as_float((unsigned)__builtin_amdgcn_mov_dpp((int)__float_as_uint(y[i]), 0xB1, 0xf, 0xf, true));
      y[i] += __uint_as_float((unsigned)__builtin_amdgcn_mov_dpp((int)__float_as_uint(y[i]), 0x4E, 0xf, 0xf, true));
    }
    const float ya = q == 0 ? y[0] : (q == 1 ? y[2] : (q == 2 ? y[4] : y[6])), yb = q == 0 ? y[1] : (q == 1 ? y[3] : (q == 2 ? y[5] : y[7]));
    const int t = t0 + 2 * q, f = (d0 >> 3) & 15;
    const unsigned r0 = pk2(ya, yb), r1 = pk2(kx[dd][t] * Dv, kx[dd][t + 1] * Dv), o = pk2(lo2f(r0) + lo2f(r1), hi2f(r0) + hi2f(r1));
    u16* dst = p.yT + (t >> 4) * 9216 + (d0 >> 2) * 64 + dd;
    dst[((t & 15) ^ f) << 2] = (u16)(o & 0xFFFFu); dst[(((t + 1) & 15) ^ f) << 2] = (u16)(o >> 16);
  } else if (b < 240 + PRE_FILL) {
    const unsigned i0 = (unsigned)(b - 240) * 256u + tid;
    if (i0 < (SLOT - AGO) / 16u) {
      const unsigned o = AGO + i0 * 16u, s = o < AGT ? (i0 >> 11) * 4u + ((o >> 10) & 3u) : 8u, r = o & 1023u;
      const unsigned gm = s < 8u ? r >> 8 : (r >> 6) & 3u, m3 = (r >> 4) & 3u, ga = s < 8u ? (r >> 6) & 3u : r >> 8, row = 4u * (gm & 1u) + 2u * (gm >> 1) + (m3 & 1u), half = m3 >> 1;
      const unsigned* src = (const unsigned*)(p.mem0 + row * DM + 64u * s + 16u * ga + 2u * half);
      __builtin_amdgcn_raw_buffer_store_b128((v4u){src[0], src[2], src[4], src[6]}, rx, o, 0, 16);
    }
    for (unsigned i = i0; i < 131072u + MF_BYTES / 16u; i += PRE_FILL * 256)
      __builtin_amdgcn_raw_buffer_store_b128(zero, rx, i < 131072u ? (SG0 + (i >> 11)) * XBLK + P_FA * PAIR + (i & 2047u) * 16u : MF_OFF + (i & 2047u) * 16u, 0, 16);
    for (unsigned i = i0; i < 2u * SLOT / 16u; i += PRE_FILL * 256) __builtin_amdgcn_raw_buffer_store_b128(ones, rx, SLOT + i * 16u, 0, 16);
  } else if (b >= 240 + PRE_FILL + 256) {
    const __amdgpu_buffer_rsrc_t rk = __builtin_amdgcn_make_buffer_rsrc((void*)p.kk, 0, S_LEN * DM * 2, 0x00020000);
    u16 (*tile)[72] = (u16 (*)[72])sh;
    const int bid = b - 240 - PRE_FILL - 256, d0 = (bid % 9) * 64, s0 = (bid / 9) * 64;
    const int sr = (tid >> 3) * 2, d = d0 + (tid & 7) * 8;
    v4u kr[5], vr[2], w[4];
#pragma unroll
    for (int a = 0; a < 5; ++a) kr[a] = __builtin_amdgcn_raw_buffer_load_b128(rk, (unsigned)((s0 + sr - 3 + a) * DM + d) * 2u, 0, 0);
#pragma unroll
    for (int a = 0; a < 2; ++a) vr[a] = *(const v4u*)&p.vv[(long)(s0 + sr + a) * DM + d];
#pragma unroll
    for (int a = 0; a < 4; ++a) w[a] = *(const v4u*)&p.shift_k[(d + 2 * a) * 4];
    __builtin_amdgcn_sched_barrier(0);
#pragma unroll
    for (int it = 0; it < 2; ++it) {
      v4u o;
#pragma unroll
      for (int j = 0; j < 4; ++j) { const unsigned u[4] = {kr[it][j], kr[it + 1][j], kr[it + 2][j], kr[it + 3][j]}; o[j] = kv2(u, vr[it][j], (v2u){w[j].x, w[j].y}, (v2u){w[j].z, w[j].w}); }
      *(v4u*)&tile[sr + it][(tid & 7) * 8] = o;
    }
    __syncthreads();
    const int lane = tid & 63, wv = tid >> 6, g = lane >> 4, r = (lane >> 2) & 3, c = lane & 3;
    v2u o[4];
#pragma unroll
    for (int i = 0; i < 4; ++i) o[i] = __builtin_bit_cast(v2u, __builtin_amdgcn_ds_read_tr16_b64_v4i16((__attribute__((address_space(3))) v4s*)&tile[16 * g + 4 * i + r][16 * wv + 4 * c]));
    u16* dst = p.kvT + (long)(d0 + 16 * wv + (lane & 15)) * S_LEN + s0 + 16 * g;
    *(v4u*)dst = (v4u){o[0].x, o[0].y, o[1].x, o[1].y};
    *(v4u*)(dst + 8) = (v4u){o[2].x, o[2].y, o[3].x, o[3].y};
  } else {
    const int x = b & 7, k = (b - 240 - PRE_FILL) >> 3;
    unsigned long long pc; unsigned l0, l1, l2, l3; int rel;
    asm volatile("s_getpc_b64 %0\nLPRE:\n\ts_mov_b32 %1, LKBEG-LPRE\n\ts_mov_b32 %2, LCHEND-LKBEG\n\ts_mov_b32 %3, LQVEND-LKBEG\n\ts_mov_b32 %4, LQKEND-LKBEG\n\ts_mov_b32 %5, LAPEND-LKBEG"
                 : "=s"(pc), "=s"(rel), "=s"(l0), "=s"(l1), "=s"(l2), "=s"(l3));
    l0 = l0 > l1 ? l0 : l1; l2 = l2 > l3 ? l2 : l3; l0 = l0 > l2 ? l0 : l2;
    const unsigned o = (unsigned)(k * 64 + (tid & 63)) * 64u;
    unsigned a[PFN + 2];
    a[PFN] = *(const __attribute__((address_space(1))) unsigned*)(pc + rel + (o < l0 ? o : 0u));
    a[PFN + 1] = __builtin_amdgcn_raw_buffer_load_b32(rx, k == 31 ? tid * XBLK + CAL_OFF + 8064u : 0xF0000000u, 0, 16);
#pragma unroll
    for (int i = 0; i < PFN; ++i) {
      const int q = (i * 32 + k) * 256 + tid, r2 = q >> 5, gr = r2 >> 5, row = (gr / 3) * 64 + (x >> 2) * 32 + (r2 & 31), off = (q & 31) * 32, ty = gr % 3;
      a[i] = *(const unsigned*)(gr < 27 ? (ty == 0 ? p.Wq : (ty == 1 ? p.Wk : p.Wv)) + (long)row * HD + (x & 3) * 1024 + off
                                        : p.Wo + ((long)(x & 3) * DM + (gr < 36 ? gr - 27 : 0) * 64 + (x >> 2) * 32 + (r2 & 31)) * 1024 + off);
    }
#pragma unroll
    for (int i = 0; i < PFN + 2; ++i) asm volatile("" :: "v"(a[i]));
  }
}

#define CREV_STRIDE 2208
#define LWAIT() asm volatile("s_waitcnt lgkmcnt(0)" ::: "memory")
template <class F>
__device__ __forceinline__ void conv_task(const u16* __restrict__ kvT, const u16* __restrict__ ssm_k, const u16* __restrict__ ssm_D, u16* yT, u16* shb, int d, F fill) {
  const int lane = threadIdx.x & 63;
  u16* crev = shb + 64;
  u16* kvs = crev + 4 * CREV_STRIDE;
  const int X0 = 2048 + 63;
  v4u cv[4], kq[4];
#pragma unroll
  for (int j = 0; j < 4; ++j) { cv[j] = *(const v4u*)&ssm_k[(long)d * S_LEN + lane * 32 + 8 * j]; kq[j] = *(const v4u*)&kvT[(long)d * S_LEN + (j * 64 + lane) * 8]; }
  const float Dv = bf2f(ssm_D[d]);
  const v4u z4 = {0u, 0u, 0u, 0u};
  *(v4u*)&kvs[(lane >> 2) * 40 + (lane & 3) * 8] = z4;
  if (lane < 32) *(v4u*)&crev[(lane >> 3) * CREV_STRIDE + (lane & 7) * 8] = z4;
  if (lane < 36) *(v4u*)&crev[(lane / 9) * CREV_STRIDE + 2112 + (lane % 9) * 8] = z4;
  {
    unsigned r[18];
#pragma unroll
    for (int k = 0; k < 16; ++k) { const unsigned u = cv[3 - (k >> 2)][3 - (k & 3)]; r[k] = __builtin_amdgcn_alignbyte(u, u, 2); }
#pragma unroll
    for (int k = 0; k < 2; ++k) r[16 + k] = (unsigned)__builtin_amdgcn_update_dpp(0, (int)r[k], 0x138, 0xf, 0xf, true);
    LWAIT();
#pragma unroll
    for (int rho = 0; rho < 4; ++rho)
#pragma unroll
      for (int j = 0; j < 4; ++j) {
        v4u o;
#pragma unroll
        for (int k = 0; k < 4; ++k) { const int q = 4 * j + k + (rho >> 1); o[k] = (rho & 1) ? __builtin_amdgcn_alignbyte(r[q + 1], r[q], 2) : r[q]; }
        *(v4u*)&crev[rho * CREV_STRIDE + 2080 - 32 * lane + 8 * j] = o;
      }
    if (lane == 63) {
      *(v2u*)&crev[1 * CREV_STRIDE + 60] = (v2u){0u, r[0] << 16};
      *(v2u*)&crev[2 * CREV_STRIDE + 60] = (v2u){0u, r[0]};
      *(v2u*)&crev[3 * CREV_STRIDE + 60] = (v2u){r[0] << 16, __builtin_amdgcn_alignbyte(r[1], r[0], 2)};
    }
#pragma unroll
    for (int j = 0; j < 4; ++j) *(v4u*)&kvs[(16 + j * 16 + (lane >> 2)) * 40 + (lane & 3) * 8] = kq[j];
  }
  fill();
  LWAIT();
  const int n = lane & 15, g = lane >> 4;
  const int rho = (X0 - n) & 3;
  f32x4 acc[4][2];
#pragma unroll
  for (int q = 0; q < 8; ++q) acc[q >> 1][q & 1] = (f32x4){0.f, 0.f, 0.f, 0.f};
  {
    typedef __attribute__((address_space(3))) const v2u* l2p;
    typedef __attribute__((address_space(3))) const bf16x8* l4p;
    unsigned a0 = (unsigned)(size_t)(__attribute__((address_space(3))) u16*)&crev[rho * CREV_STRIDE + (X0 - (n - 8 * g) - rho)] - 3232u, a1 = a0 + 8u;
    unsigned b0 = (unsigned)(size_t)(__attribute__((address_space(3))) u16*)&kvs[(14 + n) * 40 + 8 * g];
    asm volatile("" : "+v"(a1));
#define AFR(m, f) ({ const unsigned o_ = 3232u - 64u * (m) - 1024u * ((f) >> 1) - 32u * ((f) & 1); const v2u lo_ = *(l2p)(size_t)(a0 + o_), hi_ = *(l2p)(size_t)(a1 + o_); \
                     __builtin_bit_cast(bf16x8, (v4u){lo_.x, lo_.y, hi_.x, hi_.y}); })
#define BFR(m, j) (*(l4p)(size_t)(b0 + 80u * (2 - (m)) + 1280u * (j)))
    bf16x8 af[2][8], bf[2][4];
#pragma unroll
    for (int f = 0; f < 8; ++f) { af[0][f] = AFR(0, f); af[1][f] = af[0][f]; __builtin_amdgcn_sched_barrier(0); }
#pragma unroll
    for (int j = 0; j < 4; ++j) { bf[0][j] = BFR(0, j); bf[1][j] = bf[0][j]; }
#pragma unroll 1
    for (int mo = 0; mo < 8; ++mo) {
#pragma unroll
      for (int m = 0; m < 2; ++m) {
        int k = 0;
#pragma unroll
        for (int a = 0; a < 4; ++a)
#pragma unroll
          for (int ct = a; ct < 4; ++ct)
#pragma unroll
            for (int ti = 0; ti < 2; ++ti) {
              acc[ct][ti] = __builtin_amdgcn_mfma_f32_16x16x32_bf16(__builtin_bit_cast(bfx8, af[m][2 * a + ti]), __builtin_bit_cast(bfx8, bf[m][ct - a]), acc[ct][ti], 0, 0, 0);
              if (k < 2) af[m ^ 1][k] = AFR(m + 1, k);
              else if (k < 6) bf[m ^ 1][k - 2] = BFR(m + 1, k - 2);
              else if (k < 12) af[m ^ 1][k - 4] = AFR(m + 1, k - 4);
              __builtin_amdgcn_sched_barrier(0);
              ++k;
            }
      }
      a0 -= 128u; a1 -= 128u; b0 -= 160u;
    }
  }
  u16* yo = crev;
  LWAIT();
#pragma unroll
  for (int q = 0; q < 8; ++q) {
    const int o = (16 * (q >> 1) + n) * 40 + 16 * (q & 1) + 4 * g;
    const v2u kx = *(const v2u*)&kvs[16 * 40 + o];
    const f32x4 y = acc[q >> 1][q & 1];
    const unsigned y0 = pk2(y[0], y[1]), y1 = pk2(y[2], y[3]), r0 = pk2(lo2f(kx.x) * Dv, hi2f(kx.x) * Dv), r1 = pk2(lo2f(kx.y) * Dv, hi2f(kx.y) * Dv);
    *(v2u*)&yo[o] = (v2u){pk2(lo2f(y0) + lo2f(r0), hi2f(y0) + hi2f(r0)), pk2(lo2f(y1) + lo2f(r1), hi2f(y1) + hi2f(r1))};
  }
  LWAIT();
  __builtin_amdgcn_s_barrier();
  asm volatile("" ::: "memory");
  {
    const int tid = threadIdx.x, f = (d >> 3) & 15;
    const u16* y0 = shb - (tid >> 6) * 16384 + 64;
#pragma unroll
    for (int k = 0; k < 4; ++k) {
      const int tile = 32 * k + (tid >> 3), t = 16 * tile + 2 * ((tid & 7) ^ (f >> 1));
      unsigned u[4];
#pragma unroll
      for (int c = 0; c < 4; ++c) u[c] = *(const unsigned*)&y0[c * 16384 + (t >> 5) * 40 + (t & 31)];
      const v2u e = {__builtin_amdgcn_perm(u[1], u[0], 0x05040100u), __builtin_amdgcn_perm(u[3], u[2], 0x05040100u)}, o = {__builtin_amdgcn_perm(u[1], u[0], 0x07060302u), __builtin_amdgcn_perm(u[3], u[2], 0x07060302u)};
      if (tile >= NFC / 16) __builtin_amdgcn_raw_buffer_store_b128((f & 1) ? (v4u){o.x, o.y, e.x, e.y} : (v4u){e.x, e.y, o.x, o.y}, __builtin_amdgcn_make_buffer_rsrc((void*)yT, 0, DM * S_LEN * 2, 0x00020000),
                                                                   tile * 18432 + (tid & 7) * 16, (d >> 2) * 128, 16);
    }
  }
}

#define NCHW 64
#define NTOKW 192
#define PSL 4
#define SENT 0xFFFFFFFFu
#define SPIN_CAP 300000u
struct MP {
  const u16* Wq; const u16* Wk; const u16* Wv; const u16* Wo; u16* out; unsigned* xb;
  const u16* xin; u16* yT; u16* kvT; const u16* ssm_k; const u16* ssm_D;
};

__device__ __forceinline__ MP getp() {
  MP r;
#if defined(__HIP_DEVICE_COMPILE__)
  const __attribute__((address_space(4))) unsigned long long* k = (const __attribute__((address_space(4))) unsigned long long*)__builtin_amdgcn_kernarg_segment_ptr();
  asm volatile("" : "+s"(k));
#define GP(T, i) ((T*)(__attribute__((address_space(1))) T*)k[i])
  r.Wq = GP(const u16, 0);
  r.Wk = GP(const u16, 1);
  r.Wv = GP(const u16, 2);
  r.Wo = GP(const u16, 3);
  r.out = GP(u16, 4);
  r.xb = GP(unsigned, 5);
  r.xin = GP(const u16, 6);
  r.yT = GP(u16, 7);
  r.kvT = GP(u16, 8);
  r.ssm_k = GP(const u16, 9);
  r.ssm_D = GP(const u16, 10);
#endif
  return r;
}
typedef __amdgpu_buffer_rsrc_t rsrc_t;
__device__ __forceinline__ rsrc_t mkr(const void* p, unsigned bytes) { return __builtin_amdgcn_make_buffer_rsrc((void*)p, 0, bytes, 0x00020000); }
__device__ __forceinline__ void st16(rsrc_t r, unsigned vo, unsigned so, v4u v) { __builtin_amdgcn_raw_buffer_store_b128(v, r, vo, so, 16); }
__device__ __forceinline__ void st4(rsrc_t r, unsigned vo, unsigned so, unsigned v) { __builtin_amdgcn_raw_buffer_store_b32(v, r, vo, so, 16); }
__device__ __forceinline__ v4u ld16(rsrc_t r, unsigned vo, unsigned so) { return __builtin_amdgcn_raw_buffer_load_b128(r, vo, so, 16); }
__device__ __forceinline__ v2u ld8(rsrc_t r, unsigned vo, unsigned so) { return __builtin_amdgcn_raw_buffer_load_b64(r, vo, so, 16); }
__device__ __forceinline__ unsigned ld4(rsrc_t r, unsigned vo, unsigned so) { return __builtin_amdgcn_raw_buffer_load_b32(r, vo, so, 16); }
__device__ __forceinline__ unsigned opq(unsigned x) { asm volatile("" : "+v"(x)); return x; }

__device__ __forceinline__ unsigned long long die_mask(rsrc_t X, unsigned first, int L, int lane) { return __ballot((ld4(X, (unsigned)lane * 4u, CTAB_OFF + first * 4u) & 1u) != (unsigned)L); }
#define SEGB(m, s) ((unsigned)(SG0 + (s)) * XBLK + (unsigned)(((m) >> (s)) & 1ull) * 8192u)
__device__ __forceinline__ bool spin_fail(unsigned& spins, rsrc_t F) {
  ++spins;
  if (spins > SPIN_CAP) { unsigned one = 0x40000000u | (blockIdx.x << 8) | threadIdx.x; asm volatile("" : "+v"(one)); if (__builtin_amdgcn_readfirstlane(ld4(F, 0, TMO_OFF)) == 0u) st4(F, 0, TMO_OFF, one); return true; }
  if ((spins & 1023u) == 0u) return __builtin_amdgcn_readfirstlane(ld4(F, 0, TMO_OFF)) != 0;
  return false;
}
__device__ __forceinline__ unsigned rtc() { return (unsigned)__builtin_amdgcn_s_memrealtime(); }
#define BAR() do { asm volatile("s_waitcnt lgkmcnt(0)" ::: "memory"); __builtin_amdgcn_s_barrier(); asm volatile("" ::: "memory"); } while (0)
#define SB() __builtin_amdgcn_sched_barrier(0)
#define MF "v_mfma_f32_16x16x32_bf16 "
#define VM0() __builtin_amdgcn_s_waitcnt(0x0F70)
#define DL1 12
#define SCH_B0 52
#define DLL1 6
#define DLL2 4
#define DLF 24
#define SCH_PEN 0
#define DLX 4
#define LATE 2
#define TKL 28
#define UP 1
#define DN 100
#define DL3 16
#define FIN -3
#define CH_FIN CH_S(FIN)
#define DLT 16
#define DLY 20
#define CH_DLT CH_S(DLT)
#define CH_DLY CH_S(DLY)
#define RSL 2
#define CH_S2(x) #x
#define CH_S(x) CH_S2(x)
#define CH_TOKCHK "s_cbranch_vccz LG3_%=\n\t"
#define CH_SG0 CH_S(SG0)
#define CH_ARMD "%[armd]"
#define CH_ARMS "%[arms]"
#define CH_SLOT "688128"
#define CH_SKIP "32768"
#define CH_DLL1 CH_S(DLL1)
#define CH_DLL2 CH_S(DLL2)
#define CH_DLF CH_S(DLF)
#define CH_B0 CH_S(SCH_B0)
#define CH_PEN CH_S(SCH_PEN)
#define CH_RSL CH_S(RSL)
#define CH_DL3 CH_S(DL3)
#define CH_UP CH_S(UP)
#define CH_DN CH_S(DN)
#define CH_DLX CH_S(DLX)
#define CH_LATE CH_S(LATE)
#define CH_DL1L "%[dl1l]"
#define CH_TKL CH_S(TKL)
#define CH_SCHW "656384"
#define CH_CAP "%[cap]"
#define CH_CTAB "%[ctab]"
#define FDL 6
#define CH_FDL CH_S(FDL)
#define CH_ASM \
  "s_mov_b32 s84, " CH_CAP "\n\t" \
  "s_mov_b32 s83, " CH_CAP "\n\t" \
  "s_mov_b32 s68, 0\n\t" \
  "s_mov_b32 s74, 0\n\t" \
  "s_mov_b32 s76, 0\n\t" \
  "s_mov_b32 s77, 0\n\t" \
  "s_mov_b32 s78, 0\n\t" \
  "s_mov_b32 s79, 0\n\t" \
  "s_mov_b32 s56, 0xcccccccc\n\t" \
  "s_mov_b32 s57, 0xcccccccc\n\t" \
  "s_mov_b32 s58, 0x33333333\n\t" \
  "s_mov_b32 s59, 0x33333333\n\t" \
  "s_mov_b32 s24, 0x01010101\n\t" \
  "s_mov_b32 s25, 0\n\t" \
  "s_mov_b32 s88, 0x3f803f80\n\t" \
  "s_mov_b32 s89, 0x3f803f80\n\t" \
  "s_mov_b32 s66, s65\n\t" \
  "s_mov_b32 s47, s65\n\t" \
  "s_mov_b32 s72, 0\n\t" \
  "s_mov_b32 s73, 0\n\t" \
  "s_mov_b32 s67, 0\n\t" \
  "s_mul_i32 s60, s40, 36864\n\t" \
  "s_add_u32 s60, s60, 16384\n\t" \
  "v_add_u32 v70, s60, v69\n\t" \
  "v_bfe_i32 v68, v69, 5, 1\n\t" \
  "v_and_b32 v68, 0xaaaaaaaa, v68\n\t" \
  "v_or_b32 v68, 0x44444444, v68\n\t" \
  "v_and_b32 v73, 32, v69\n\t" \
  "v_lshl_add_u32 v73, v73, 5, v69\n\t" \
  "LLOAD_%=:\n\t" \
  "v_lshrrev_b32 v74, 2, v69\n\t" \
  "s_mov_b32 s60, " CH_CTAB "\n\t" \
  "buffer_load_dword v72, v74, s[28:31], s60 offen offset:88 sc1\n\t" \
  "buffer_load_dword v74, v74, s[28:31], s60 offen sc1\n\t" \
  "v_bfe_u32 v0, v69, 8, 1\n\t" \
  "v_bfe_u32 v1, v69, 9, 1\n\t" \
  "v_lshlrev_b32 v0, 2, v0\n\t" \
  "v_add_u32 v0, v0, v1\n\t" \
  "v_mul_u32_u24 v0, 4608, v0\n\t" \
  "v_lshrrev_b32 v1, 1, v69\n\t" \
  "v_and_b32 v1, 0x60, v1\n\t" \
  "v_and_b32 v2, 16, v69\n\t" \
  "v_bfe_u32 v3, v69, 5, 1\n\t" \
  "v_add3_u32 v64, v0, v1, v2\n\t" \
  "v_lshl_add_u32 v64, v3, 2, v64\n\t" \
  "v_lshl_add_u32 v67, v3, 3, v0\n\t" \
  "v_bfe_u32 v1, v69, 7, 1\n\t" \
  "v_lshl_add_u32 v67, v1, 2, v67\n\t" \
  "v_bfe_u32 v1, v69, 6, 1\n\t" \
  "v_lshl_add_u32 v67, v1, 4, v67\n\t" \
  "v_bfe_u32 v1, v69, 4, 1\n\t" \
  "v_lshl_add_u32 v67, v1, 1, v67\n\t" \
  "v_lshrrev_b32 v71, 8, v69\n\t" \
  "v_lshlrev_b32 v1, 6, v71\n\t" \
  "v_and_b32 v2, 0xf0, v69\n\t" \
  "v_xor_b32 v1, v1, v2\n\t" \
  "v_lshl_add_u32 v71, v71, 13, v1\n\t" \
  "s_lshl_b32 s60, s40, 17\n\t" \
  "v_add_u32 v71, s60, v71\n\t" \
  "v_and_b32 v1, 0x300, v69\n\t" \
  "v_lshlrev_b32 v65, 3, v1\n\t" \
  "v_and_b32 v1, 0xc0, v69\n\t" \
  "v_lshl_add_u32 v65, v1, 2, v65\n\t" \
  "s_lshl_b32 s60, s40, 6\n\t" \
  "v_xor_b32 v1, s60, v1\n\t" \
  "v_and_b32 v2, 48, v69\n\t" \
  "v_add3_u32 v65, v65, v1, v2\n\t" \
  "v_lshrrev_b32 v2, 1, v2\n\t" \
  "v_sub_u32 v66, v65, v2\n\t" \
  "s_lshl_b32 s54, s40, 12\n\t" \
  "s_lshl_b32 s55, s40, 7\n\t" \
  "s_lshl_b32 s75, s40, 5\n\t" \
  "s_add_u32 s75, s75, 1024\n\t" \
  "s_mov_b32 s62, 0x05040100\n\t" \
  "s_mov_b32 s63, 0x07060302\n\t" \
  "s_barrier\n\t" \
  "s_add_u32 m0, s54, 0\n\t" \
  "s_mov_b32 s60, 0\n\t" \
  "buffer_load_dwordx4 v71, %[Wq], s60 offen lds\n\t" \
  "s_add_u32 m0, s54, 1024\n\t" \
  "s_mov_b32 s60, 32768\n\t" \
  "buffer_load_dwordx4 v71, %[Wq], s60 offen lds\n\t" \
  "s_add_u32 m0, s54, 2048\n\t" \
  "s_mov_b32 s60, 65536\n\t" \
  "buffer_load_dwordx4 v71, %[Wq], s60 offen lds\n\t" \
  "s_add_u32 m0, s54, 3072\n\t" \
  "s_mov_b32 s60, 98304\n\t" \
  "buffer_load_dwordx4 v71, %[Wq], s60 offen lds\n\t" \
  "s_add_u32 m0, s54, 16384\n\t" \
  "s_mov_b32 s60, 0\n\t" \
  "buffer_load_dwordx4 v71, %[Wk], s60 offen lds\n\t" \
  "s_add_u32 m0, s54, 17408\n\t" \
  "s_mov_b32 s60, 32768\n\t" \
  "buffer_load_dwordx4 v71, %[Wk], s60 offen lds\n\t" \
  "s_add_u32 m0, s54, 18432\n\t" \
  "s_mov_b32 s60, 65536\n\t" \
  "buffer_load_dwordx4 v71, %[Wk], s60 offen lds\n\t" \
  "s_add_u32 m0, s54, 19456\n\t" \
  "s_mov_b32 s60, 98304\n\t" \
  "buffer_load_dwordx4 v71, %[Wk], s60 offen lds\n\t" \
  "s_add_u32 m0, s54, 53248\n\t" \
  "s_mov_b32 s60, 0\n\t" \
  "buffer_load_dwordx4 v71, %[Wv], s60 offen lds\n\t" \
  "s_add_u32 m0, s54, 54272\n\t" \
  "s_mov_b32 s60, 32768\n\t" \
  "buffer_load_dwordx4 v71, %[Wv], s60 offen lds\n\t" \
  "s_add_u32 m0, s54, 55296\n\t" \
  "s_mov_b32 s60, 65536\n\t" \
  "buffer_load_dwordx4 v71, %[Wv], s60 offen lds\n\t" \
  "s_add_u32 m0, s54, 56320\n\t" \
  "s_mov_b32 s60, 98304\n\t" \
  "buffer_load_dwordx4 v71, %[Wv], s60 offen lds\n\t" \
  "s_add_u32 m0, s54, 90112\n\t" \
  "s_mov_b32 s60, 524288\n\t" \
  "buffer_load_dwordx4 v71, %[Wq], s60 offen lds\n\t" \
  "s_add_u32 m0, s54, 91136\n\t" \
  "s_mov_b32 s60, 557056\n\t" \
  "buffer_load_dwordx4 v71, %[Wq], s60 offen lds\n\t" \
  "s_add_u32 m0, s54, 92160\n\t" \
  "s_mov_b32 s60, 589824\n\t" \
  "buffer_load_dwordx4 v71, %[Wq], s60 offen lds\n\t" \
  "s_add_u32 m0, s54, 93184\n\t" \
  "s_mov_b32 s60, 622592\n\t" \
  "buffer_load_dwordx4 v71, %[Wq], s60 offen lds\n\t" \
  "s_add_u32 m0, s54, 126976\n\t" \
  "s_mov_b32 s60, 524288\n\t" \
  "buffer_load_dwordx4 v71, %[Wk], s60 offen lds\n\t" \
  "s_add_u32 m0, s54, 128000\n\t" \
  "s_mov_b32 s60, 557056\n\t" \
  "buffer_load_dwordx4 v71, %[Wk], s60 offen lds\n\t" \
  "s_add_u32 m0, s54, 129024\n\t" \
  "s_mov_b32 s60, 589824\n\t" \
  "buffer_load_dwordx4 v71, %[Wk], s60 offen lds\n\t" \
  "s_add_u32 m0, s54, 130048\n\t" \
  "s_mov_b32 s60, 622592\n\t" \
  "buffer_load_dwordx4 v71, %[Wk], s60 offen lds\n\t" \
  "s_xor_b32 s60, s40, 0\n\t" \
  "s_mul_i32 s60, s60, 36864\n\t" \
  "s_add_u32 s60, s60, s55\n\t" \
  "s_add_u32 s60, s60, 0\n\t" \
  "s_add_u32 s61, s60, 9216\n\t" \
  "buffer_load_dwordx3 v[80:82], v64, %[Wo], s60 offen offset:0\n\t" \
  "buffer_load_dwordx3 v[84:86], v64, %[Wo], s60 offen offset:1152\n\t" \
  "buffer_load_dwordx3 v[88:90], v64, %[Wo], s60 offen offset:2304\n\t" \
  "buffer_load_dwordx3 v[92:94], v64, %[Wo], s60 offen offset:3456\n\t" \
  "buffer_load_dwordx3 v[96:98], v64, %[Wo], s61 offen offset:0\n\t" \
  "buffer_load_dwordx3 v[100:102], v64, %[Wo], s61 offen offset:1152\n\t" \
  "buffer_load_dwordx3 v[104:106], v64, %[Wo], s61 offen offset:2304\n\t" \
  "buffer_load_dwordx3 v[108:110], v64, %[Wo], s61 offen offset:3456\n\t" \
  "s_xor_b32 s60, s40, 0\n\t" \
  "s_mul_i32 s60, s60, 36864\n\t" \
  "s_add_u32 s60, s60, s55\n\t" \
  "s_add_u32 s60, s60, 512\n\t" \
  "s_add_u32 s61, s60, 9216\n\t" \
  "buffer_load_dwordx3 v[112:114], v64, %[Wo], s60 offen offset:0\n\t" \
  "buffer_load_dwordx3 v[116:118], v64, %[Wo], s60 offen offset:1152\n\t" \
  "buffer_load_dwordx3 v[120:122], v64, %[Wo], s60 offen offset:2304\n\t" \
  "buffer_load_dwordx3 v[124:126], v64, %[Wo], s60 offen offset:3456\n\t" \
  "buffer_load_dwordx3 v[128:130], v64, %[Wo], s61 offen offset:0\n\t" \
  "buffer_load_dwordx3 v[132:134], v64, %[Wo], s61 offen offset:1152\n\t" \
  "buffer_load_dwordx3 v[136:138], v64, %[Wo], s61 offen offset:2304\n\t" \
  "buffer_load_dwordx3 v[140:142], v64, %[Wo], s61 offen offset:3456\n\t" \
  "s_xor_b32 s60, s40, 1\n\t" \
  "s_mul_i32 s60, s60, 36864\n\t" \
  "s_add_u32 s60, s60, s55\n\t" \
  "s_add_u32 s60, s60, 0\n\t" \
  "s_add_u32 s61, s60, 9216\n\t" \
  "buffer_load_dwordx3 v[144:146], v64, %[Wo], s60 offen offset:0\n\t" \
  "buffer_load_dwordx3 v[148:150], v64, %[Wo], s60 offen offset:1152\n\t" \
  "buffer_load_dwordx3 v[152:154], v64, %[Wo], s60 offen offset:2304\n\t" \
  "buffer_load_dwordx3 v[156:158], v64, %[Wo], s60 offen offset:3456\n\t" \
  "buffer_load_dwordx3 v[160:162], v64, %[Wo], s61 offen offset:0\n\t" \
  "buffer_load_dwordx3 v[164:166], v64, %[Wo], s61 offen offset:1152\n\t" \
  "buffer_load_dwordx3 v[168:170], v64, %[Wo], s61 offen offset:2304\n\t" \
  "buffer_load_dwordx3 v[172:174], v64, %[Wo], s61 offen offset:3456\n\t" \
  "s_waitcnt vmcnt(40) lgkmcnt(0)\n\t" \
  "s_barrier\n\t" \
  "ds_read_b64_tr_b16 v[0:1], v65 offset:0\n\t" \
  "ds_read_b64_tr_b16 v[2:3], v65 offset:1024\n\t" \
  "ds_read_b64_tr_b16 v[4:5], v65 offset:8192\n\t" \
  "ds_read_b64_tr_b16 v[6:7], v65 offset:9216\n\t" \
  "ds_read_b64_tr_b16 v[8:9], v65 offset:8\n\t" \
  "ds_read_b64_tr_b16 v[10:11], v65 offset:1032\n\t" \
  "ds_read_b64_tr_b16 v[12:13], v65 offset:8200\n\t" \
  "ds_read_b64_tr_b16 v[14:15], v65 offset:9224\n\t" \
  "s_waitcnt vmcnt(36) lgkmcnt(0)\n\t" \
  "s_barrier\n\t" \
  "ds_read_b64_tr_b16 a[0:1], v65 offset:16384\n\t" \
  "ds_read_b64_tr_b16 a[2:3], v65 offset:17408\n\t" \
  "ds_read_b64_tr_b16 a[4:5], v65 offset:24576\n\t" \
  "ds_read_b64_tr_b16 a[6:7], v65 offset:25600\n\t" \
  "ds_read_b64_tr_b16 a[8:9], v65 offset:16392\n\t" \
  "ds_read_b64_tr_b16 a[10:11], v65 offset:17416\n\t" \
  "ds_read_b64_tr_b16 a[12:13], v65 offset:24584\n\t" \
  "ds_read_b64_tr_b16 a[14:15], v65 offset:25608\n\t" \
  "s_add_u32 m0, s54, 0\n\t" \
  "s_mov_b32 s60, 524288\n\t" \
  "buffer_load_dwordx4 v71, %[Wv], s60 offen lds\n\t" \
  "s_add_u32 m0, s54, 1024\n\t" \
  "s_mov_b32 s60, 557056\n\t" \
  "buffer_load_dwordx4 v71, %[Wv], s60 offen lds\n\t" \
  "s_add_u32 m0, s54, 2048\n\t" \
  "s_mov_b32 s60, 589824\n\t" \
  "buffer_load_dwordx4 v71, %[Wv], s60 offen lds\n\t" \
  "s_add_u32 m0, s54, 3072\n\t" \
  "s_mov_b32 s60, 622592\n\t" \
  "buffer_load_dwordx4 v71, %[Wv], s60 offen lds\n\t" \
  "s_add_u32 s60, s75, 0\n\t" \
  "buffer_load_ushort v248, v67, %[Wo], s60 offen\n\t" \
  "buffer_load_ushort v83, v67, %[Wo], s60 offen offset:1152\n\t" \
  "s_add_u32 s60, s75, 36864\n\t" \
  "buffer_load_ushort v249, v67, %[Wo], s60 offen\n\t" \
  "buffer_load_ushort v87, v67, %[Wo], s60 offen offset:1152\n\t" \
  "s_add_u32 s60, s75, 2304\n\t" \
  "buffer_load_ushort v250, v67, %[Wo], s60 offen\n\t" \
  "buffer_load_ushort v91, v67, %[Wo], s60 offen offset:1152\n\t" \
  "s_add_u32 s60, s75, 39168\n\t" \
  "buffer_load_ushort v251, v67, %[Wo], s60 offen\n\t" \
  "buffer_load_ushort v95, v67, %[Wo], s60 offen offset:1152\n\t" \
  "s_add_u32 s60, s75, 9216\n\t" \
  "buffer_load_ushort v252, v67, %[Wo], s60 offen\n\t" \
  "buffer_load_ushort v99, v67, %[Wo], s60 offen offset:1152\n\t" \
  "s_add_u32 s60, s75, 46080\n\t" \
  "buffer_load_ushort v253, v67, %[Wo], s60 offen\n\t" \
  "buffer_load_ushort v103, v67, %[Wo], s60 offen offset:1152\n\t" \
  "s_add_u32 s60, s75, 11520\n\t" \
  "buffer_load_ushort v254, v67, %[Wo], s60 offen\n\t" \
  "buffer_load_ushort v107, v67, %[Wo], s60 offen offset:1152\n\t" \
  "s_add_u32 s60, s75, 48384\n\t" \
  "buffer_load_ushort v255, v67, %[Wo], s60 offen\n\t" \
  "buffer_load_ushort v111, v67, %[Wo], s60 offen offset:1152\n\t" \
  "v_add_u32 v79, 53248, v66\n\t" \
  "s_waitcnt vmcnt(52) lgkmcnt(0)\n\t" \
  "s_barrier\n\t" \
  "ds_read_b64_tr_b16 a[16:17], v79 offset:0\n\t" \
  "ds_read_b64_tr_b16 a[18:19], v79 offset:1024\n\t" \
  "ds_read_b64_tr_b16 a[20:21], v79 offset:8192\n\t" \
  "ds_read_b64_tr_b16 a[22:23], v79 offset:9216\n\t" \
  "ds_read_b64_tr_b16 a[24:25], v79 offset:32\n\t" \
  "ds_read_b64_tr_b16 a[26:27], v79 offset:1056\n\t" \
  "ds_read_b64_tr_b16 a[28:29], v79 offset:8224\n\t" \
  "ds_read_b64_tr_b16 a[30:31], v79 offset:9248\n\t" \
  "s_add_u32 m0, s54, 16384\n\t" \
  "s_mov_b32 s60, 1048576\n\t" \
  "buffer_load_dwordx4 v71, %[Wq], s60 offen lds\n\t" \
  "s_add_u32 m0, s54, 17408\n\t" \
  "s_mov_b32 s60, 1081344\n\t" \
  "buffer_load_dwordx4 v71, %[Wq], s60 offen lds\n\t" \
  "s_add_u32 m0, s54, 18432\n\t" \
  "s_mov_b32 s60, 1114112\n\t" \
  "buffer_load_dwordx4 v71, %[Wq], s60 offen lds\n\t" \
  "s_add_u32 m0, s54, 19456\n\t" \
  "s_mov_b32 s60, 1146880\n\t" \
  "buffer_load_dwordx4 v71, %[Wq], s60 offen lds\n\t" \
  "s_waitcnt lgkmcnt(0)\n\t" \
  "s_waitcnt vmcnt(46)\n\t" \
  "v_perm_b32 v176, v84, v80, s62\n\t" \
  "v_perm_b32 v177, v84, v80, s63\n\t" \
  "s_waitcnt vmcnt(44)\n\t" \
  "v_perm_b32 v178, v92, v88, s62\n\t" \
  "v_perm_b32 v179, v92, v88, s63\n\t" \
  "s_waitcnt vmcnt(42)\n\t" \
  "v_perm_b32 v180, v100, v96, s62\n\t" \
  "v_perm_b32 v181, v100, v96, s63\n\t" \
  "s_waitcnt vmcnt(40)\n\t" \
  "v_perm_b32 v182, v108, v104, s62\n\t" \
  "v_perm_b32 v183, v108, v104, s63\n\t" \
  "ds_write_b128 v70, v[176:179] offset:20480\n\t" \
  "ds_write_b128 v70, v[180:183] offset:21504\n\t" \
  "s_waitcnt lgkmcnt(0)\n\t" \
  "v_perm_b32 v176, v86, v82, s62\n\t" \
  "v_perm_b32 v177, v86, v82, s63\n\t" \
  "v_perm_b32 v178, v94, v90, s62\n\t" \
  "v_perm_b32 v179, v94, v90, s63\n\t" \
  "v_perm_b32 v180, v102, v98, s62\n\t" \
  "v_perm_b32 v181, v102, v98, s63\n\t" \
  "v_perm_b32 v182, v110, v106, s62\n\t" \
  "v_perm_b32 v183, v110, v106, s63\n\t" \
  "ds_write_b128 v70, v[176:179] offset:16384\n\t" \
  "ds_write_b128 v70, v[180:183] offset:17408\n\t" \
  "s_xor_b32 s60, s40, 1\n\t" \
  "s_mul_i32 s60, s60, 36864\n\t" \
  "s_add_u32 s60, s60, s55\n\t" \
  "s_add_u32 s60, s60, 512\n\t" \
  "s_add_u32 s61, s60, 9216\n\t" \
  "buffer_load_dwordx3 v[80:82], v64, %[Wo], s60 offen offset:0\n\t" \
  "buffer_load_dwordx3 v[84:86], v64, %[Wo], s60 offen offset:1152\n\t" \
  "buffer_load_dwordx3 v[88:90], v64, %[Wo], s60 offen offset:2304\n\t" \
  "buffer_load_dwordx3 v[92:94], v64, %[Wo], s60 offen offset:3456\n\t" \
  "buffer_load_dwordx3 v[96:98], v64, %[Wo], s61 offen offset:0\n\t" \
  "buffer_load_dwordx3 v[100:102], v64, %[Wo], s61 offen offset:1152\n\t" \
  "buffer_load_dwordx3 v[104:106], v64, %[Wo], s61 offen offset:2304\n\t" \
  "buffer_load_dwordx3 v[108:110], v64, %[Wo], s61 offen offset:3456\n\t" \
  "v_add_u32 v79, 90112, v65\n\t" \
  "s_waitcnt vmcnt(60) lgkmcnt(0)\n\t" \
  "s_barrier\n\t" \
  "ds_read_b64_tr_b16 a[32:33], v79 offset:0\n\t" \
  "ds_read_b64_tr_b16 a[34:35], v79 offset:1024\n\t" \
  "ds_read_b64_tr_b16 a[36:37], v79 offset:8192\n\t" \
  "ds_read_b64_tr_b16 a[38:39], v79 offset:9216\n\t" \
  "ds_read_b64_tr_b16 a[40:41], v79 offset:8\n\t" \
  "ds_read_b64_tr_b16 a[42:43], v79 offset:1032\n\t" \
  "ds_read_b64_tr_b16 a[44:45], v79 offset:8200\n\t" \
  "ds_read_b64_tr_b16 a[46:47], v79 offset:9224\n\t" \
  "s_add_u32 m0, s54, 53248\n\t" \
  "s_mov_b32 s60, 1048576\n\t" \
  "buffer_load_dwordx4 v71, %[Wk], s60 offen lds\n\t" \
  "s_add_u32 m0, s54, 54272\n\t" \
  "s_mov_b32 s60, 1081344\n\t" \
  "buffer_load_dwordx4 v71, %[Wk], s60 offen lds\n\t" \
  "s_add_u32 m0, s54, 55296\n\t" \
  "s_mov_b32 s60, 1114112\n\t" \
  "buffer_load_dwordx4 v71, %[Wk], s60 offen lds\n\t" \
  "s_add_u32 m0, s54, 56320\n\t" \
  "s_mov_b32 s60, 1146880\n\t" \
  "buffer_load_dwordx4 v71, %[Wk], s60 offen lds\n\t" \
  "v_add_u32 v79, 126976, v65\n\t" \
  "s_waitcnt vmcnt(60) lgkmcnt(0)\n\t" \
  "s_barrier\n\t" \
  "ds_read_b64_tr_b16 a[48:49], v79 offset:0\n\t" \
  "ds_read_b64_tr_b16 a[50:51], v79 offset:1024\n\t" \
  "ds_read_b64_tr_b16 a[52:53], v79 offset:8192\n\t" \
  "ds_read_b64_tr_b16 a[54:55], v79 offset:9216\n\t" \
  "ds_read_b64_tr_b16 a[56:57], v79 offset:8\n\t" \
  "ds_read_b64_tr_b16 a[58:59], v79 offset:1032\n\t" \
  "ds_read_b64_tr_b16 a[60:61], v79 offset:8200\n\t" \
  "ds_read_b64_tr_b16 a[62:63], v79 offset:9224\n\t" \
  "s_add_u32 m0, s54, 90112\n\t" \
  "s_mov_b32 s60, 1048576\n\t" \
  "buffer_load_dwordx4 v71, %[Wv], s60 offen lds\n\t" \
  "s_add_u32 m0, s54, 91136\n\t" \
  "s_mov_b32 s60, 1081344\n\t" \
  "buffer_load_dwordx4 v71, %[Wv], s60 offen lds\n\t" \
  "s_add_u32 m0, s54, 92160\n\t" \
  "s_mov_b32 s60, 1114112\n\t" \
  "buffer_load_dwordx4 v71, %[Wv], s60 offen lds\n\t" \
  "s_add_u32 m0, s54, 93184\n\t" \
  "s_mov_b32 s60, 1146880\n\t" \
  "buffer_load_dwordx4 v71, %[Wv], s60 offen lds\n\t" \
  "s_waitcnt lgkmcnt(0)\n\t" \
  "s_waitcnt vmcnt(54)\n\t" \
  "v_perm_b32 v176, v116, v112, s62\n\t" \
  "v_perm_b32 v177, v116, v112, s63\n\t" \
  "s_waitcnt vmcnt(52)\n\t" \
  "v_perm_b32 v178, v124, v120, s62\n\t" \
  "v_perm_b32 v179, v124, v120, s63\n\t" \
  "s_waitcnt vmcnt(50)\n\t" \
  "v_perm_b32 v180, v132, v128, s62\n\t" \
  "v_perm_b32 v181, v132, v128, s63\n\t" \
  "s_waitcnt vmcnt(48)\n\t" \
  "v_perm_b32 v182, v140, v136, s62\n\t" \
  "v_perm_b32 v183, v140, v136, s63\n\t" \
  "ds_write_b128 v70, v[176:179] offset:22528\n\t" \
  "ds_write_b128 v70, v[180:183] offset:23552\n\t" \
  "s_waitcnt lgkmcnt(0)\n\t" \
  "v_perm_b32 v176, v118, v114, s62\n\t" \
  "v_perm_b32 v177, v118, v114, s63\n\t" \
  "v_perm_b32 v178, v126, v122, s62\n\t" \
  "v_perm_b32 v179, v126, v122, s63\n\t" \
  "v_perm_b32 v180, v134, v130, s62\n\t" \
  "v_perm_b32 v181, v134, v130, s63\n\t" \
  "v_perm_b32 v182, v142, v138, s62\n\t" \
  "v_perm_b32 v183, v142, v138, s63\n\t" \
  "ds_write_b128 v70, v[176:179] offset:18432\n\t" \
  "ds_write_b128 v70, v[180:183] offset:19456\n\t" \
  "s_xor_b32 s60, s40, 2\n\t" \
  "s_mul_i32 s60, s60, 36864\n\t" \
  "s_add_u32 s60, s60, s55\n\t" \
  "s_add_u32 s60, s60, 0\n\t" \
  "s_add_u32 s61, s60, 9216\n\t" \
  "buffer_load_dwordx3 v[112:114], v64, %[Wo], s60 offen offset:0\n\t" \
  "buffer_load_dwordx3 v[116:118], v64, %[Wo], s60 offen offset:1152\n\t" \
  "buffer_load_dwordx3 v[120:122], v64, %[Wo], s60 offen offset:2304\n\t" \
  "buffer_load_dwordx3 v[124:126], v64, %[Wo], s60 offen offset:3456\n\t" \
  "buffer_load_dwordx3 v[128:130], v64, %[Wo], s61 offen offset:0\n\t" \
  "buffer_load_dwordx3 v[132:134], v64, %[Wo], s61 offen offset:1152\n\t" \
  "buffer_load_dwordx3 v[136:138], v64, %[Wo], s61 offen offset:2304\n\t" \
  "buffer_load_dwordx3 v[140:142], v64, %[Wo], s61 offen offset:3456\n\t" \
  "s_waitcnt vmcnt(44) lgkmcnt(0)\n\t" \
  "s_barrier\n\t" \
  "ds_read_b64_tr_b16 a[64:65], v66 offset:0\n\t" \
  "ds_read_b64_tr_b16 a[66:67], v66 offset:1024\n\t" \
  "ds_read_b64_tr_b16 a[68:69], v66 offset:8192\n\t" \
  "ds_read_b64_tr_b16 a[70:71], v66 offset:9216\n\t" \
  "ds_read_b64_tr_b16 a[72:73], v66 offset:32\n\t" \
  "ds_read_b64_tr_b16 a[74:75], v66 offset:1056\n\t" \
  "ds_read_b64_tr_b16 a[76:77], v66 offset:8224\n\t" \
  "ds_read_b64_tr_b16 a[78:79], v66 offset:9248\n\t" \
  "s_add_u32 m0, s54, 126976\n\t" \
  "s_mov_b32 s60, 1572864\n\t" \
  "buffer_load_dwordx4 v71, %[Wq], s60 offen lds\n\t" \
  "s_add_u32 m0, s54, 128000\n\t" \
  "s_mov_b32 s60, 1605632\n\t" \
  "buffer_load_dwordx4 v71, %[Wq], s60 offen lds\n\t" \
  "s_add_u32 m0, s54, 129024\n\t" \
  "s_mov_b32 s60, 1638400\n\t" \
  "buffer_load_dwordx4 v71, %[Wq], s60 offen lds\n\t" \
  "s_add_u32 m0, s54, 130048\n\t" \
  "s_mov_b32 s60, 1671168\n\t" \
  "buffer_load_dwordx4 v71, %[Wq], s60 offen lds\n\t" \
  "s_waitcnt vmcnt(28) lgkmcnt(0)\n\t" \
  "s_barrier\n\t" \
  "ds_read_b64_tr_b16 a[80:81], v65 offset:16384\n\t" \
  "ds_read_b64_tr_b16 a[82:83], v65 offset:17408\n\t" \
  "ds_read_b64_tr_b16 a[84:85], v65 offset:24576\n\t" \
  "ds_read_b64_tr_b16 a[86:87], v65 offset:25600\n\t" \
  "ds_read_b64_tr_b16 a[88:89], v65 offset:16392\n\t" \
  "ds_read_b64_tr_b16 a[90:91], v65 offset:17416\n\t" \
  "ds_read_b64_tr_b16 a[92:93], v65 offset:24584\n\t" \
  "ds_read_b64_tr_b16 a[94:95], v65 offset:25608\n\t" \
  "s_add_u32 m0, s54, 0\n\t" \
  "s_mov_b32 s60, 1572864\n\t" \
  "buffer_load_dwordx4 v71, %[Wk], s60 offen lds\n\t" \
  "s_add_u32 m0, s54, 1024\n\t" \
  "s_mov_b32 s60, 1605632\n\t" \
  "buffer_load_dwordx4 v71, %[Wk], s60 offen lds\n\t" \
  "s_add_u32 m0, s54, 2048\n\t" \
  "s_mov_b32 s60, 1638400\n\t" \
  "buffer_load_dwordx4 v71, %[Wk], s60 offen lds\n\t" \
  "s_add_u32 m0, s54, 3072\n\t" \
  "s_mov_b32 s60, 1671168\n\t" \
  "buffer_load_dwordx4 v71, %[Wk], s60 offen lds\n\t" \
  "s_waitcnt lgkmcnt(0)\n\t" \
  "v_perm_b32 v176, v148, v144, s62\n\t" \
  "v_perm_b32 v177, v148, v144, s63\n\t" \
  "v_perm_b32 v178, v156, v152, s62\n\t" \
  "v_perm_b32 v179, v156, v152, s63\n\t" \
  "v_perm_b32 v180, v164, v160, s62\n\t" \
  "v_perm_b32 v181, v164, v160, s63\n\t" \
  "v_perm_b32 v182, v172, v168, s62\n\t" \
  "v_perm_b32 v183, v172, v168, s63\n\t" \
  "ds_write_b128 v70, v[176:179] offset:24576\n\t" \
  "ds_write_b128 v70, v[180:183] offset:25600\n\t" \
  "v_perm_b32 v200, v150, v146, s62\n\t" \
  "v_perm_b32 v201, v150, v146, s63\n\t" \
  "v_perm_b32 v202, v158, v154, s62\n\t" \
  "v_perm_b32 v203, v158, v154, s63\n\t" \
  "v_perm_b32 v204, v166, v162, s62\n\t" \
  "v_perm_b32 v205, v166, v162, s63\n\t" \
  "v_perm_b32 v206, v174, v170, s62\n\t" \
  "v_perm_b32 v207, v174, v170, s63\n\t" \
  "s_xor_b32 s60, s40, 2\n\t" \
  "s_mul_i32 s60, s60, 36864\n\t" \
  "s_add_u32 s60, s60, s55\n\t" \
  "s_add_u32 s60, s60, 512\n\t" \
  "s_add_u32 s61, s60, 9216\n\t" \
  "buffer_load_dwordx3 v[144:146], v64, %[Wo], s60 offen offset:0\n\t" \
  "buffer_load_dwordx3 v[148:150], v64, %[Wo], s60 offen offset:1152\n\t" \
  "buffer_load_dwordx3 v[152:154], v64, %[Wo], s60 offen offset:2304\n\t" \
  "buffer_load_dwordx3 v[156:158], v64, %[Wo], s60 offen offset:3456\n\t" \
  "buffer_load_dwordx3 v[160:162], v64, %[Wo], s61 offen offset:0\n\t" \
  "buffer_load_dwordx3 v[164:166], v64, %[Wo], s61 offen offset:1152\n\t" \
  "buffer_load_dwordx3 v[168:170], v64, %[Wo], s61 offen offset:2304\n\t" \
  "buffer_load_dwordx3 v[172:174], v64, %[Wo], s61 offen offset:3456\n\t" \
  "v_add_u32 v79, 53248, v65\n\t" \
  "s_waitcnt vmcnt(28) lgkmcnt(0)\n\t" \
  "s_barrier\n\t" \
  "ds_read_b64_tr_b16 v[32:33], v79 offset:0\n\t" \
  "ds_read_b64_tr_b16 v[34:35], v79 offset:1024\n\t" \
  "ds_read_b64_tr_b16 v[36:37], v79 offset:8192\n\t" \
  "ds_read_b64_tr_b16 v[38:39], v79 offset:9216\n\t" \
  "ds_read_b64_tr_b16 a[96:97], v79 offset:8\n\t" \
  "ds_read_b64_tr_b16 a[98:99], v79 offset:1032\n\t" \
  "ds_read_b64_tr_b16 a[100:101], v79 offset:8200\n\t" \
  "ds_read_b64_tr_b16 a[102:103], v79 offset:9224\n\t" \
  "s_add_u32 m0, s54, 16384\n\t" \
  "s_mov_b32 s60, 1572864\n\t" \
  "buffer_load_dwordx4 v71, %[Wv], s60 offen lds\n\t" \
  "s_add_u32 m0, s54, 17408\n\t" \
  "s_mov_b32 s60, 1605632\n\t" \
  "buffer_load_dwordx4 v71, %[Wv], s60 offen lds\n\t" \
  "s_add_u32 m0, s54, 18432\n\t" \
  "s_mov_b32 s60, 1638400\n\t" \
  "buffer_load_dwordx4 v71, %[Wv], s60 offen lds\n\t" \
  "s_add_u32 m0, s54, 19456\n\t" \
  "s_mov_b32 s60, 1671168\n\t" \
  "buffer_load_dwordx4 v71, %[Wv], s60 offen lds\n\t" \
  "v_lshl_or_b32 v248, v83, 16, v248\n\t" \
  "v_lshl_or_b32 v249, v87, 16, v249\n\t" \
  "v_lshl_or_b32 v250, v91, 16, v250\n\t" \
  "v_lshl_or_b32 v251, v95, 16, v251\n\t" \
  "v_lshl_or_b32 v252, v99, 16, v252\n\t" \
  "v_lshl_or_b32 v253, v103, 16, v253\n\t" \
  "v_lshl_or_b32 v254, v107, 16, v254\n\t" \
  "v_lshl_or_b32 v255, v111, 16, v255\n\t" \
  "v_add_u32 v79, 90112, v66\n\t" \
  "s_waitcnt vmcnt(28) lgkmcnt(0)\n\t" \
  "s_barrier\n\t" \
  "ds_read_b64_tr_b16 a[104:105], v79 offset:0\n\t" \
  "ds_read_b64_tr_b16 a[106:107], v79 offset:1024\n\t" \
  "ds_read_b64_tr_b16 a[108:109], v79 offset:8192\n\t" \
  "ds_read_b64_tr_b16 a[110:111], v79 offset:9216\n\t" \
  "ds_read_b64_tr_b16 a[112:113], v79 offset:32\n\t" \
  "ds_read_b64_tr_b16 a[114:115], v79 offset:1056\n\t" \
  "ds_read_b64_tr_b16 a[116:117], v79 offset:8224\n\t" \
  "ds_read_b64_tr_b16 a[118:119], v79 offset:9248\n\t" \
  "s_add_u32 m0, s54, 53248\n\t" \
  "s_mov_b32 s60, 2097152\n\t" \
  "buffer_load_dwordx4 v71, %[Wq], s60 offen lds\n\t" \
  "s_add_u32 m0, s54, 54272\n\t" \
  "s_mov_b32 s60, 2129920\n\t" \
  "buffer_load_dwordx4 v71, %[Wq], s60 offen lds\n\t" \
  "s_add_u32 m0, s54, 55296\n\t" \
  "s_mov_b32 s60, 2162688\n\t" \
  "buffer_load_dwordx4 v71, %[Wq], s60 offen lds\n\t" \
  "s_add_u32 m0, s54, 56320\n\t" \
  "s_mov_b32 s60, 2195456\n\t" \
  "buffer_load_dwordx4 v71, %[Wq], s60 offen lds\n\t" \
  "s_waitcnt lgkmcnt(0)\n\t" \
  "v_perm_b32 v176, v84, v80, s62\n\t" \
  "v_perm_b32 v177, v84, v80, s63\n\t" \
  "v_perm_b32 v178, v92, v88, s62\n\t" \
  "v_perm_b32 v179, v92, v88, s63\n\t" \
  "v_perm_b32 v180, v100, v96, s62\n\t" \
  "v_perm_b32 v181, v100, v96, s63\n\t" \
  "v_perm_b32 v182, v108, v104, s62\n\t" \
  "v_perm_b32 v183, v108, v104, s63\n\t" \
  "ds_write_b128 v70, v[176:179] offset:26624\n\t" \
  "ds_write_b128 v70, v[180:183] offset:27648\n\t" \
  "v_perm_b32 v232, v86, v82, s62\n\t" \
  "v_perm_b32 v233, v86, v82, s63\n\t" \
  "v_perm_b32 v234, v94, v90, s62\n\t" \
  "v_perm_b32 v235, v94, v90, s63\n\t" \
  "v_perm_b32 v236, v102, v98, s62\n\t" \
  "v_perm_b32 v237, v102, v98, s63\n\t" \
  "v_perm_b32 v238, v110, v106, s62\n\t" \
  "v_perm_b32 v239, v110, v106, s63\n\t" \
  "s_xor_b32 s60, s40, 3\n\t" \
  "s_mul_i32 s60, s60, 36864\n\t" \
  "s_add_u32 s60, s60, s55\n\t" \
  "s_add_u32 s60, s60, 0\n\t" \
  "s_add_u32 s61, s60, 9216\n\t" \
  "buffer_load_dwordx3 v[80:82], v64, %[Wo], s60 offen offset:0\n\t" \
  "buffer_load_dwordx3 v[84:86], v64, %[Wo], s60 offen offset:1152\n\t" \
  "buffer_load_dwordx3 v[88:90], v64, %[Wo], s60 offen offset:2304\n\t" \
  "buffer_load_dwordx3 v[92:94], v64, %[Wo], s60 offen offset:3456\n\t" \
  "buffer_load_dwordx3 v[96:98], v64, %[Wo], s61 offen offset:0\n\t" \
  "buffer_load_dwordx3 v[100:102], v64, %[Wo], s61 offen offset:1152\n\t" \
  "buffer_load_dwordx3 v[104:106], v64, %[Wo], s61 offen offset:2304\n\t" \
  "buffer_load_dwordx3 v[108:110], v64, %[Wo], s61 offen offset:3456\n\t" \
  "v_add_u32 v79, 126976, v65\n\t" \
  "s_waitcnt vmcnt(28) lgkmcnt(0)\n\t" \
  "s_barrier\n\t" \
  "ds_read_b64_tr_b16 a[120:121], v79 offset:0\n\t" \
  "ds_read_b64_tr_b16 a[122:123], v79 offset:1024\n\t" \
  "ds_read_b64_tr_b16 a[124:125], v79 offset:8192\n\t" \
  "ds_read_b64_tr_b16 a[126:127], v79 offset:9216\n\t" \
  "ds_read_b64_tr_b16 v[16:17], v79 offset:8\n\t" \
  "ds_read_b64_tr_b16 v[18:19], v79 offset:1032\n\t" \
  "ds_read_b64_tr_b16 v[20:21], v79 offset:8200\n\t" \
  "ds_read_b64_tr_b16 v[22:23], v79 offset:9224\n\t" \
  "s_add_u32 m0, s54, 90112\n\t" \
  "s_mov_b32 s60, 2097152\n\t" \
  "buffer_load_dwordx4 v71, %[Wk], s60 offen lds\n\t" \
  "s_add_u32 m0, s54, 91136\n\t" \
  "s_mov_b32 s60, 2129920\n\t" \
  "buffer_load_dwordx4 v71, %[Wk], s60 offen lds\n\t" \
  "s_add_u32 m0, s54, 92160\n\t" \
  "s_mov_b32 s60, 2162688\n\t" \
  "buffer_load_dwordx4 v71, %[Wk], s60 offen lds\n\t" \
  "s_add_u32 m0, s54, 93184\n\t" \
  "s_mov_b32 s60, 2195456\n\t" \
  "buffer_load_dwordx4 v71, %[Wk], s60 offen lds\n\t" \
  "s_add_u32 s60, s75, 73728\n\t" \
  "buffer_load_ushort v184, v67, %[Wo], s60 offen\n\t" \
  "buffer_load_ushort v83, v67, %[Wo], s60 offen offset:1152\n\t" \
  "s_add_u32 s60, s75, 110592\n\t" \
  "buffer_load_ushort v185, v67, %[Wo], s60 offen\n\t" \
  "buffer_load_ushort v87, v67, %[Wo], s60 offen offset:1152\n\t" \
  "s_add_u32 s60, s75, 76032\n\t" \
  "buffer_load_ushort v186, v67, %[Wo], s60 offen\n\t" \
  "buffer_load_ushort v91, v67, %[Wo], s60 offen offset:1152\n\t" \
  "s_add_u32 s60, s75, 112896\n\t" \
  "buffer_load_ushort v187, v67, %[Wo], s60 offen\n\t" \
  "buffer_load_ushort v95, v67, %[Wo], s60 offen offset:1152\n\t" \
  "s_add_u32 s60, s75, 82944\n\t" \
  "buffer_load_ushort v188, v67, %[Wo], s60 offen\n\t" \
  "buffer_load_ushort v99, v67, %[Wo], s60 offen offset:1152\n\t" \
  "s_add_u32 s60, s75, 119808\n\t" \
  "buffer_load_ushort v189, v67, %[Wo], s60 offen\n\t" \
  "buffer_load_ushort v103, v67, %[Wo], s60 offen offset:1152\n\t" \
  "s_add_u32 s60, s75, 85248\n\t" \
  "buffer_load_ushort v190, v67, %[Wo], s60 offen\n\t" \
  "buffer_load_ushort v107, v67, %[Wo], s60 offen offset:1152\n\t" \
  "s_add_u32 s60, s75, 122112\n\t" \
  "buffer_load_ushort v191, v67, %[Wo], s60 offen\n\t" \
  "buffer_load_ushort v111, v67, %[Wo], s60 offen offset:1152\n\t" \
  "s_waitcnt vmcnt(44) lgkmcnt(0)\n\t" \
  "s_barrier\n\t" \
  "ds_read_b64_tr_b16 a[128:129], v65 offset:0\n\t" \
  "ds_read_b64_tr_b16 a[130:131], v65 offset:1024\n\t" \
  "ds_read_b64_tr_b16 a[132:133], v65 offset:8192\n\t" \
  "ds_read_b64_tr_b16 a[134:135], v65 offset:9216\n\t" \
  "ds_read_b64_tr_b16 a[136:137], v65 offset:8\n\t" \
  "ds_read_b64_tr_b16 a[138:139], v65 offset:1032\n\t" \
  "ds_read_b64_tr_b16 a[140:141], v65 offset:8200\n\t" \
  "ds_read_b64_tr_b16 a[142:143], v65 offset:9224\n\t" \
  "s_add_u32 m0, s54, 126976\n\t" \
  "s_mov_b32 s60, 2097152\n\t" \
  "buffer_load_dwordx4 v71, %[Wv], s60 offen lds\n\t" \
  "s_add_u32 m0, s54, 128000\n\t" \
  "s_mov_b32 s60, 2129920\n\t" \
  "buffer_load_dwordx4 v71, %[Wv], s60 offen lds\n\t" \
  "s_add_u32 m0, s54, 129024\n\t" \
  "s_mov_b32 s60, 2162688\n\t" \
  "buffer_load_dwordx4 v71, %[Wv], s60 offen lds\n\t" \
  "s_add_u32 m0, s54, 130048\n\t" \
  "s_mov_b32 s60, 2195456\n\t" \
  "buffer_load_dwordx4 v71, %[Wv], s60 offen lds\n\t" \
  "s_waitcnt lgkmcnt(0)\n\t" \
  "v_perm_b32 v176, v116, v112, s62\n\t" \
  "v_perm_b32 v177, v116, v112, s63\n\t" \
  "v_perm_b32 v178, v124, v120, s62\n\t" \
  "v_perm_b32 v179, v124, v120, s63\n\t" \
  "v_perm_b32 v180, v132, v128, s62\n\t" \
  "v_perm_b32 v181, v132, v128, s63\n\t" \
  "v_perm_b32 v182, v140, v136, s62\n\t" \
  "v_perm_b32 v183, v140, v136, s63\n\t" \
  "ds_write_b128 v70, v[176:179] offset:28672\n\t" \
  "ds_write_b128 v70, v[180:183] offset:29696\n\t" \
  "v_perm_b32 v208, v118, v114, s62\n\t" \
  "v_perm_b32 v209, v118, v114, s63\n\t" \
  "v_perm_b32 v210, v126, v122, s62\n\t" \
  "v_perm_b32 v211, v126, v122, s63\n\t" \
  "v_perm_b32 v212, v134, v130, s62\n\t" \
  "v_perm_b32 v213, v134, v130, s63\n\t" \
  "v_perm_b32 v214, v142, v138, s62\n\t" \
  "v_perm_b32 v215, v142, v138, s63\n\t" \
  "s_xor_b32 s60, s40, 3\n\t" \
  "s_mul_i32 s60, s60, 36864\n\t" \
  "s_add_u32 s60, s60, s55\n\t" \
  "s_add_u32 s60, s60, 512\n\t" \
  "s_add_u32 s61, s60, 9216\n\t" \
  "buffer_load_dwordx3 v[112:114], v64, %[Wo], s60 offen offset:0\n\t" \
  "buffer_load_dwordx3 v[116:118], v64, %[Wo], s60 offen offset:1152\n\t" \
  "buffer_load_dwordx3 v[120:122], v64, %[Wo], s60 offen offset:2304\n\t" \
  "buffer_load_dwordx3 v[124:126], v64, %[Wo], s60 offen offset:3456\n\t" \
  "buffer_load_dwordx3 v[128:130], v64, %[Wo], s61 offen offset:0\n\t" \
  "buffer_load_dwordx3 v[132:134], v64, %[Wo], s61 offen offset:1152\n\t" \
  "buffer_load_dwordx3 v[136:138], v64, %[Wo], s61 offen offset:2304\n\t" \
  "buffer_load_dwordx3 v[140:142], v64, %[Wo], s61 offen offset:3456\n\t" \
  "s_waitcnt vmcnt(44) lgkmcnt(0)\n\t" \
  "s_barrier\n\t" \
  "ds_read_b64_tr_b16 a[144:145], v66 offset:16384\n\t" \
  "ds_read_b64_tr_b16 a[146:147], v66 offset:17408\n\t" \
  "ds_read_b64_tr_b16 a[148:149], v66 offset:24576\n\t" \
  "ds_read_b64_tr_b16 a[150:151], v66 offset:25600\n\t" \
  "ds_read_b64_tr_b16 a[152:153], v66 offset:16416\n\t" \
  "ds_read_b64_tr_b16 a[154:155], v66 offset:17440\n\t" \
  "ds_read_b64_tr_b16 a[156:157], v66 offset:24608\n\t" \
  "ds_read_b64_tr_b16 a[158:159], v66 offset:25632\n\t" \
  "s_add_u32 m0, s54, 0\n\t" \
  "s_mov_b32 s60, 2621440\n\t" \
  "buffer_load_dwordx4 v71, %[Wq], s60 offen lds\n\t" \
  "s_add_u32 m0, s54, 1024\n\t" \
  "s_mov_b32 s60, 2654208\n\t" \
  "buffer_load_dwordx4 v71, %[Wq], s60 offen lds\n\t" \
  "s_add_u32 m0, s54, 2048\n\t" \
  "s_mov_b32 s60, 2686976\n\t" \
  "buffer_load_dwordx4 v71, %[Wq], s60 offen lds\n\t" \
  "s_add_u32 m0, s54, 3072\n\t" \
  "s_mov_b32 s60, 2719744\n\t" \
  "buffer_load_dwordx4 v71, %[Wq], s60 offen lds\n\t" \
  "v_add_u32 v79, 53248, v65\n\t" \
  "s_waitcnt vmcnt(44) lgkmcnt(0)\n\t" \
  "s_barrier\n\t" \
  "ds_read_b64_tr_b16 a[160:161], v79 offset:0\n\t" \
  "ds_read_b64_tr_b16 a[162:163], v79 offset:1024\n\t" \
  "ds_read_b64_tr_b16 a[164:165], v79 offset:8192\n\t" \
  "ds_read_b64_tr_b16 a[166:167], v79 offset:9216\n\t" \
  "ds_read_b64_tr_b16 a[168:169], v79 offset:8\n\t" \
  "ds_read_b64_tr_b16 a[170:171], v79 offset:1032\n\t" \
  "ds_read_b64_tr_b16 a[172:173], v79 offset:8200\n\t" \
  "ds_read_b64_tr_b16 a[174:175], v79 offset:9224\n\t" \
  "s_add_u32 m0, s54, 16384\n\t" \
  "s_mov_b32 s60, 2621440\n\t" \
  "buffer_load_dwordx4 v71, %[Wk], s60 offen lds\n\t" \
  "s_add_u32 m0, s54, 17408\n\t" \
  "s_mov_b32 s60, 2654208\n\t" \
  "buffer_load_dwordx4 v71, %[Wk], s60 offen lds\n\t" \
  "s_add_u32 m0, s54, 18432\n\t" \
  "s_mov_b32 s60, 2686976\n\t" \
  "buffer_load_dwordx4 v71, %[Wk], s60 offen lds\n\t" \
  "s_add_u32 m0, s54, 19456\n\t" \
  "s_mov_b32 s60, 2719744\n\t" \
  "buffer_load_dwordx4 v71, %[Wk], s60 offen lds\n\t" \
  "s_waitcnt lgkmcnt(0)\n\t" \
  "v_perm_b32 v176, v148, v144, s62\n\t" \
  "v_perm_b32 v177, v148, v144, s63\n\t" \
  "v_perm_b32 v178, v156, v152, s62\n\t" \
  "v_perm_b32 v179, v156, v152, s63\n\t" \
  "v_perm_b32 v180, v164, v160, s62\n\t" \
  "v_perm_b32 v181, v164, v160, s63\n\t" \
  "v_perm_b32 v182, v172, v168, s62\n\t" \
  "v_perm_b32 v183, v172, v168, s63\n\t" \
  "ds_write_b128 v70, v[176:179] offset:30720\n\t" \
  "ds_write_b128 v70, v[180:183] offset:31744\n\t" \
  "v_perm_b32 v240, v150, v146, s62\n\t" \
  "v_perm_b32 v241, v150, v146, s63\n\t" \
  "v_perm_b32 v242, v158, v154, s62\n\t" \
  "v_perm_b32 v243, v158, v154, s63\n\t" \
  "v_perm_b32 v244, v166, v162, s62\n\t" \
  "v_perm_b32 v245, v166, v162, s63\n\t" \
  "v_perm_b32 v246, v174, v170, s62\n\t" \
  "v_perm_b32 v247, v174, v170, s63\n\t" \
  "v_add_u32 v79, 90112, v65\n\t" \
  "s_waitcnt vmcnt(36) lgkmcnt(0)\n\t" \
  "s_barrier\n\t" \
  "ds_read_b64_tr_b16 a[176:177], v79 offset:0\n\t" \
  "ds_read_b64_tr_b16 a[178:179], v79 offset:1024\n\t" \
  "ds_read_b64_tr_b16 a[180:181], v79 offset:8192\n\t" \
  "ds_read_b64_tr_b16 a[182:183], v79 offset:9216\n\t" \
  "ds_read_b64_tr_b16 a[184:185], v79 offset:8\n\t" \
  "ds_read_b64_tr_b16 a[186:187], v79 offset:1032\n\t" \
  "ds_read_b64_tr_b16 a[188:189], v79 offset:8200\n\t" \
  "ds_read_b64_tr_b16 a[190:191], v79 offset:9224\n\t" \
  "s_add_u32 m0, s54, 53248\n\t" \
  "s_mov_b32 s60, 2621440\n\t" \
  "buffer_load_dwordx4 v71, %[Wv], s60 offen lds\n\t" \
  "s_add_u32 m0, s54, 54272\n\t" \
  "s_mov_b32 s60, 2654208\n\t" \
  "buffer_load_dwordx4 v71, %[Wv], s60 offen lds\n\t" \
  "s_add_u32 m0, s54, 55296\n\t" \
  "s_mov_b32 s60, 2686976\n\t" \
  "buffer_load_dwordx4 v71, %[Wv], s60 offen lds\n\t" \
  "s_add_u32 m0, s54, 56320\n\t" \
  "s_mov_b32 s60, 2719744\n\t" \
  "buffer_load_dwordx4 v71, %[Wv], s60 offen lds\n\t" \
  "v_add_u32 v79, 126976, v66\n\t" \
  "s_waitcnt vmcnt(20) lgkmcnt(0)\n\t" \
  "s_barrier\n\t" \
  "ds_read_b64_tr_b16 a[192:193], v79 offset:0\n\t" \
  "ds_read_b64_tr_b16 a[194:195], v79 offset:1024\n\t" \
  "ds_read_b64_tr_b16 a[196:197], v79 offset:8192\n\t" \
  "ds_read_b64_tr_b16 a[198:199], v79 offset:9216\n\t" \
  "ds_read_b64_tr_b16 v[48:49], v79 offset:32\n\t" \
  "ds_read_b64_tr_b16 v[50:51], v79 offset:1056\n\t" \
  "ds_read_b64_tr_b16 v[52:53], v79 offset:8224\n\t" \
  "ds_read_b64_tr_b16 v[54:55], v79 offset:9248\n\t" \
  "s_add_u32 m0, s54, 90112\n\t" \
  "s_mov_b32 s60, 3145728\n\t" \
  "buffer_load_dwordx4 v71, %[Wq], s60 offen lds\n\t" \
  "s_add_u32 m0, s54, 91136\n\t" \
  "s_mov_b32 s60, 3178496\n\t" \
  "buffer_load_dwordx4 v71, %[Wq], s60 offen lds\n\t" \
  "s_add_u32 m0, s54, 92160\n\t" \
  "s_mov_b32 s60, 3211264\n\t" \
  "buffer_load_dwordx4 v71, %[Wq], s60 offen lds\n\t" \
  "s_add_u32 m0, s54, 93184\n\t" \
  "s_mov_b32 s60, 3244032\n\t" \
  "buffer_load_dwordx4 v71, %[Wq], s60 offen lds\n\t" \
  "v_perm_b32 v192, v84, v80, s62\n\t" \
  "v_perm_b32 v193, v84, v80, s63\n\t" \
  "v_perm_b32 v194, v92, v88, s62\n\t" \
  "v_perm_b32 v195, v92, v88, s63\n\t" \
  "v_perm_b32 v196, v100, v96, s62\n\t" \
  "v_perm_b32 v197, v100, v96, s63\n\t" \
  "v_perm_b32 v198, v108, v104, s62\n\t" \
  "v_perm_b32 v199, v108, v104, s63\n\t" \
  "v_perm_b32 v216, v86, v82, s62\n\t" \
  "v_perm_b32 v217, v86, v82, s63\n\t" \
  "v_perm_b32 v218, v94, v90, s62\n\t" \
  "v_perm_b32 v219, v94, v90, s63\n\t" \
  "v_perm_b32 v220, v102, v98, s62\n\t" \
  "v_perm_b32 v221, v102, v98, s63\n\t" \
  "v_perm_b32 v222, v110, v106, s62\n\t" \
  "v_perm_b32 v223, v110, v106, s63\n\t" \
  "s_waitcnt vmcnt(12) lgkmcnt(0)\n\t" \
  "s_barrier\n\t" \
  "ds_read_b64_tr_b16 a[200:201], v65 offset:0\n\t" \
  "ds_read_b64_tr_b16 a[202:203], v65 offset:1024\n\t" \
  "ds_read_b64_tr_b16 a[204:205], v65 offset:8192\n\t" \
  "ds_read_b64_tr_b16 a[206:207], v65 offset:9216\n\t" \
  "ds_read_b64_tr_b16 v[24:25], v65 offset:8\n\t" \
  "ds_read_b64_tr_b16 v[26:27], v65 offset:1032\n\t" \
  "ds_read_b64_tr_b16 v[28:29], v65 offset:8200\n\t" \
  "ds_read_b64_tr_b16 v[30:31], v65 offset:9224\n\t" \
  "s_add_u32 m0, s54, 126976\n\t" \
  "s_mov_b32 s60, 3145728\n\t" \
  "buffer_load_dwordx4 v71, %[Wk], s60 offen lds\n\t" \
  "s_add_u32 m0, s54, 128000\n\t" \
  "s_mov_b32 s60, 3178496\n\t" \
  "buffer_load_dwordx4 v71, %[Wk], s60 offen lds\n\t" \
  "s_add_u32 m0, s54, 129024\n\t" \
  "s_mov_b32 s60, 3211264\n\t" \
  "buffer_load_dwordx4 v71, %[Wk], s60 offen lds\n\t" \
  "s_add_u32 m0, s54, 130048\n\t" \
  "s_mov_b32 s60, 3244032\n\t" \
  "buffer_load_dwordx4 v71, %[Wk], s60 offen lds\n\t" \
  "v_lshl_or_b32 v184, v83, 16, v184\n\t" \
  "v_lshl_or_b32 v185, v87, 16, v185\n\t" \
  "v_lshl_or_b32 v186, v91, 16, v186\n\t" \
  "v_lshl_or_b32 v187, v95, 16, v187\n\t" \
  "v_lshl_or_b32 v188, v99, 16, v188\n\t" \
  "v_lshl_or_b32 v189, v103, 16, v189\n\t" \
  "v_lshl_or_b32 v190, v107, 16, v190\n\t" \
  "v_lshl_or_b32 v191, v111, 16, v191\n\t" \
  "ds_write_b128 v70, v[184:187] offset:34816\n\t" \
  "ds_write_b128 v70, v[188:191] offset:35840\n\t" \
  "s_waitcnt vmcnt(12) lgkmcnt(0)\n\t" \
  "s_barrier\n\t" \
  "ds_read_b64_tr_b16 a[208:209], v65 offset:16384\n\t" \
  "ds_read_b64_tr_b16 a[210:211], v65 offset:17408\n\t" \
  "ds_read_b64_tr_b16 a[212:213], v65 offset:24576\n\t" \
  "ds_read_b64_tr_b16 a[214:215], v65 offset:25600\n\t" \
  "ds_read_b64_tr_b16 a[216:217], v65 offset:16392\n\t" \
  "ds_read_b64_tr_b16 a[218:219], v65 offset:17416\n\t" \
  "ds_read_b64_tr_b16 a[220:221], v65 offset:24584\n\t" \
  "ds_read_b64_tr_b16 a[222:223], v65 offset:25608\n\t" \
  "s_add_u32 m0, s54, 0\n\t" \
  "s_mov_b32 s60, 3145728\n\t" \
  "buffer_load_dwordx4 v71, %[Wv], s60 offen lds\n\t" \
  "s_add_u32 m0, s54, 1024\n\t" \
  "s_mov_b32 s60, 3178496\n\t" \
  "buffer_load_dwordx4 v71, %[Wv], s60 offen lds\n\t" \
  "s_add_u32 m0, s54, 2048\n\t" \
  "s_mov_b32 s60, 3211264\n\t" \
  "buffer_load_dwordx4 v71, %[Wv], s60 offen lds\n\t" \
  "s_add_u32 m0, s54, 3072\n\t" \
  "s_mov_b32 s60, 3244032\n\t" \
  "buffer_load_dwordx4 v71, %[Wv], s60 offen lds\n\t" \
  "v_perm_b32 v224, v116, v112, s62\n\t" \
  "v_perm_b32 v225, v116, v112, s63\n\t" \
  "v_perm_b32 v226, v124, v120, s62\n\t" \
  "v_perm_b32 v227, v124, v120, s63\n\t" \
  "v_perm_b32 v228, v132, v128, s62\n\t" \
  "v_perm_b32 v229, v132, v128, s63\n\t" \
  "v_perm_b32 v230, v140, v136, s62\n\t" \
  "v_perm_b32 v231, v140, v136, s63\n\t" \
  "s_waitcnt lgkmcnt(0)\n\t" \
  "v_perm_b32 v176, v118, v114, s62\n\t" \
  "v_perm_b32 v177, v118, v114, s63\n\t" \
  "v_perm_b32 v178, v126, v122, s62\n\t" \
  "v_perm_b32 v179, v126, v122, s63\n\t" \
  "v_perm_b32 v180, v134, v130, s62\n\t" \
  "v_perm_b32 v181, v134, v130, s63\n\t" \
  "v_perm_b32 v182, v142, v138, s62\n\t" \
  "v_perm_b32 v183, v142, v138, s63\n\t" \
  "ds_write_b128 v70, v[176:179] offset:32768\n\t" \
  "ds_write_b128 v70, v[180:183] offset:33792\n\t" \
  "v_add_u32 v79, 53248, v66\n\t" \
  "s_waitcnt vmcnt(12) lgkmcnt(0)\n\t" \
  "s_barrier\n\t" \
  "ds_read_b64_tr_b16 a[224:225], v79 offset:0\n\t" \
  "ds_read_b64_tr_b16 a[226:227], v79 offset:1024\n\t" \
  "ds_read_b64_tr_b16 a[228:229], v79 offset:8192\n\t" \
  "ds_read_b64_tr_b16 a[230:231], v79 offset:9216\n\t" \
  "ds_read_b64_tr_b16 a[232:233], v79 offset:32\n\t" \
  "ds_read_b64_tr_b16 a[234:235], v79 offset:1056\n\t" \
  "ds_read_b64_tr_b16 a[236:237], v79 offset:8224\n\t" \
  "ds_read_b64_tr_b16 a[238:239], v79 offset:9248\n\t" \
  "s_add_u32 m0, s54, 16384\n\t" \
  "s_mov_b32 s60, 3670016\n\t" \
  "buffer_load_dwordx4 v71, %[Wq], s60 offen lds\n\t" \
  "s_add_u32 m0, s54, 17408\n\t" \
  "s_mov_b32 s60, 3702784\n\t" \
  "buffer_load_dwordx4 v71, %[Wq], s60 offen lds\n\t" \
  "s_add_u32 m0, s54, 18432\n\t" \
  "s_mov_b32 s60, 3735552\n\t" \
  "buffer_load_dwordx4 v71, %[Wq], s60 offen lds\n\t" \
  "s_add_u32 m0, s54, 19456\n\t" \
  "s_mov_b32 s60, 3768320\n\t" \
  "buffer_load_dwordx4 v71, %[Wq], s60 offen lds\n\t" \
  "v_add_u32 v79, 90112, v65\n\t" \
  "s_waitcnt vmcnt(12) lgkmcnt(0)\n\t" \
  "s_barrier\n\t" \
  "ds_read_b64_tr_b16 a[240:241], v79 offset:0\n\t" \
  "ds_read_b64_tr_b16 a[242:243], v79 offset:1024\n\t" \
  "ds_read_b64_tr_b16 a[244:245], v79 offset:8192\n\t" \
  "ds_read_b64_tr_b16 a[246:247], v79 offset:9216\n\t" \
  "ds_read_b64_tr_b16 a[248:249], v79 offset:8\n\t" \
  "ds_read_b64_tr_b16 a[250:251], v79 offset:1032\n\t" \
  "ds_read_b64_tr_b16 a[252:253], v79 offset:8200\n\t" \
  "ds_read_b64_tr_b16 a[254:255], v79 offset:9224\n\t" \
  "s_add_u32 m0, s54, 53248\n\t" \
  "s_mov_b32 s60, 3670016\n\t" \
  "buffer_load_dwordx4 v71, %[Wk], s60 offen lds\n\t" \
  "s_add_u32 m0, s54, 54272\n\t" \
  "s_mov_b32 s60, 3702784\n\t" \
  "buffer_load_dwordx4 v71, %[Wk], s60 offen lds\n\t" \
  "s_add_u32 m0, s54, 55296\n\t" \
  "s_mov_b32 s60, 3735552\n\t" \
  "buffer_load_dwordx4 v71, %[Wk], s60 offen lds\n\t" \
  "s_add_u32 m0, s54, 56320\n\t" \
  "s_mov_b32 s60, 3768320\n\t" \
  "buffer_load_dwordx4 v71, %[Wk], s60 offen lds\n\t" \
  "v_add_u32 v79, 126976, v65\n\t" \
  "s_waitcnt vmcnt(12) lgkmcnt(0)\n\t" \
  "s_barrier\n\t" \
  "ds_read_b64_tr_b16 v[80:81], v79 offset:0\n\t" \
  "ds_read_b64_tr_b16 v[82:83], v79 offset:1024\n\t" \
  "ds_read_b64_tr_b16 v[84:85], v79 offset:8192\n\t" \
  "ds_read_b64_tr_b16 v[86:87], v79 offset:9216\n\t" \
  "ds_read_b64_tr_b16 v[88:89], v79 offset:8\n\t" \
  "ds_read_b64_tr_b16 v[90:91], v79 offset:1032\n\t" \
  "ds_read_b64_tr_b16 v[92:93], v79 offset:8200\n\t" \
  "ds_read_b64_tr_b16 v[94:95], v79 offset:9224\n\t" \
  "s_add_u32 m0, s54, 90112\n\t" \
  "s_mov_b32 s60, 3670016\n\t" \
  "buffer_load_dwordx4 v71, %[Wv], s60 offen lds\n\t" \
  "s_add_u32 m0, s54, 91136\n\t" \
  "s_mov_b32 s60, 3702784\n\t" \
  "buffer_load_dwordx4 v71, %[Wv], s60 offen lds\n\t" \
  "s_add_u32 m0, s54, 92160\n\t" \
  "s_mov_b32 s60, 3735552\n\t" \
  "buffer_load_dwordx4 v71, %[Wv], s60 offen lds\n\t" \
  "s_add_u32 m0, s54, 93184\n\t" \
  "s_mov_b32 s60, 3768320\n\t" \
  "buffer_load_dwordx4 v71, %[Wv], s60 offen lds\n\t" \
  "s_waitcnt vmcnt(12) lgkmcnt(0)\n\t" \
  "s_barrier\n\t" \
  "ds_read_b64_tr_b16 v[96:97], v66 offset:0\n\t" \
  "ds_read_b64_tr_b16 v[98:99], v66 offset:1024\n\t" \
  "ds_read_b64_tr_b16 v[100:101], v66 offset:8192\n\t" \
  "ds_read_b64_tr_b16 v[102:103], v66 offset:9216\n\t" \
  "ds_read_b64_tr_b16 v[104:105], v66 offset:32\n\t" \
  "ds_read_b64_tr_b16 v[106:107], v66 offset:1056\n\t" \
  "ds_read_b64_tr_b16 v[108:109], v66 offset:8224\n\t" \
  "ds_read_b64_tr_b16 v[110:111], v66 offset:9248\n\t" \
  "s_add_u32 m0, s54, 126976\n\t" \
  "s_mov_b32 s60, 4194304\n\t" \
  "buffer_load_dwordx4 v71, %[Wq], s60 offen lds\n\t" \
  "s_add_u32 m0, s54, 128000\n\t" \
  "s_mov_b32 s60, 4227072\n\t" \
  "buffer_load_dwordx4 v71, %[Wq], s60 offen lds\n\t" \
  "s_add_u32 m0, s54, 129024\n\t" \
  "s_mov_b32 s60, 4259840\n\t" \
  "buffer_load_dwordx4 v71, %[Wq], s60 offen lds\n\t" \
  "s_add_u32 m0, s54, 130048\n\t" \
  "s_mov_b32 s60, 4292608\n\t" \
  "buffer_load_dwordx4 v71, %[Wq], s60 offen lds\n\t" \
  "s_waitcnt vmcnt(12) lgkmcnt(0)\n\t" \
  "s_barrier\n\t" \
  "ds_read_b64_tr_b16 v[112:113], v65 offset:16384\n\t" \
  "ds_read_b64_tr_b16 v[114:115], v65 offset:17408\n\t" \
  "ds_read_b64_tr_b16 v[116:117], v65 offset:24576\n\t" \
  "ds_read_b64_tr_b16 v[118:119], v65 offset:25600\n\t" \
  "ds_read_b64_tr_b16 v[120:121], v65 offset:16392\n\t" \
  "ds_read_b64_tr_b16 v[122:123], v65 offset:17416\n\t" \
  "ds_read_b64_tr_b16 v[124:125], v65 offset:24584\n\t" \
  "ds_read_b64_tr_b16 v[126:127], v65 offset:25608\n\t" \
  "s_add_u32 m0, s54, 0\n\t" \
  "s_mov_b32 s60, 4194304\n\t" \
  "buffer_load_dwordx4 v71, %[Wk], s60 offen lds\n\t" \
  "s_add_u32 m0, s54, 1024\n\t" \
  "s_mov_b32 s60, 4227072\n\t" \
  "buffer_load_dwordx4 v71, %[Wk], s60 offen lds\n\t" \
  "s_add_u32 m0, s54, 2048\n\t" \
  "s_mov_b32 s60, 4259840\n\t" \
  "buffer_load_dwordx4 v71, %[Wk], s60 offen lds\n\t" \
  "s_add_u32 m0, s54, 3072\n\t" \
  "s_mov_b32 s60, 4292608\n\t" \
  "buffer_load_dwordx4 v71, %[Wk], s60 offen lds\n\t" \
  "v_add_u32 v79, 53248, v65\n\t" \
  "s_waitcnt vmcnt(12) lgkmcnt(0)\n\t" \
  "s_barrier\n\t" \
  "ds_read_b64_tr_b16 v[128:129], v79 offset:0\n\t" \
  "ds_read_b64_tr_b16 v[130:131], v79 offset:1024\n\t" \
  "ds_read_b64_tr_b16 v[132:133], v79 offset:8192\n\t" \
  "ds_read_b64_tr_b16 v[134:135], v79 offset:9216\n\t" \
  "ds_read_b64_tr_b16 v[136:137], v79 offset:8\n\t" \
  "ds_read_b64_tr_b16 v[138:139], v79 offset:1032\n\t" \
  "ds_read_b64_tr_b16 v[140:141], v79 offset:8200\n\t" \
  "ds_read_b64_tr_b16 v[142:143], v79 offset:9224\n\t" \
  "s_add_u32 m0, s54, 16384\n\t" \
  "s_mov_b32 s60, 4194304\n\t" \
  "buffer_load_dwordx4 v71, %[Wv], s60 offen lds\n\t" \
  "s_add_u32 m0, s54, 17408\n\t" \
  "s_mov_b32 s60, 4227072\n\t" \
  "buffer_load_dwordx4 v71, %[Wv], s60 offen lds\n\t" \
  "s_add_u32 m0, s54, 18432\n\t" \
  "s_mov_b32 s60, 4259840\n\t" \
  "buffer_load_dwordx4 v71, %[Wv], s60 offen lds\n\t" \
  "s_add_u32 m0, s54, 19456\n\t" \
  "s_mov_b32 s60, 4292608\n\t" \
  "buffer_load_dwordx4 v71, %[Wv], s60 offen lds\n\t" \
  "v_add_u32 v79, 90112, v66\n\t" \
  "s_waitcnt vmcnt(12) lgkmcnt(0)\n\t" \
  "s_barrier\n\t" \
  "ds_read_b64_tr_b16 v[144:145], v79 offset:0\n\t" \
  "ds_read_b64_tr_b16 v[146:147], v79 offset:1024\n\t" \
  "ds_read_b64_tr_b16 v[148:149], v79 offset:8192\n\t" \
  "ds_read_b64_tr_b16 v[150:151], v79 offset:9216\n\t" \
  "ds_read_b64_tr_b16 v[152:153], v79 offset:32\n\t" \
  "ds_read_b64_tr_b16 v[154:155], v79 offset:1056\n\t" \
  "ds_read_b64_tr_b16 v[156:157], v79 offset:8224\n\t" \
  "ds_read_b64_tr_b16 v[158:159], v79 offset:9248\n\t" \
  "v_add_u32 v79, 126976, v65\n\t" \
  "s_waitcnt vmcnt(8) lgkmcnt(0)\n\t" \
  "s_barrier\n\t" \
  "ds_read_b64_tr_b16 v[160:161], v79 offset:0\n\t" \
  "ds_read_b64_tr_b16 v[162:163], v79 offset:1024\n\t" \
  "ds_read_b64_tr_b16 v[164:165], v79 offset:8192\n\t" \
  "ds_read_b64_tr_b16 v[166:167], v79 offset:9216\n\t" \
  "ds_read_b64_tr_b16 v[168:169], v79 offset:8\n\t" \
  "ds_read_b64_tr_b16 v[170:171], v79 offset:1032\n\t" \
  "ds_read_b64_tr_b16 v[172:173], v79 offset:8200\n\t" \
  "ds_read_b64_tr_b16 v[174:175], v79 offset:9224\n\t" \
  "s_waitcnt vmcnt(4) lgkmcnt(0)\n\t" \
  "s_barrier\n\t" \
  "ds_read_b64_tr_b16 v[40:41], v65 offset:0\n\t" \
  "ds_read_b64_tr_b16 v[42:43], v65 offset:1024\n\t" \
  "ds_read_b64_tr_b16 v[44:45], v65 offset:8192\n\t" \
  "ds_read_b64_tr_b16 v[46:47], v65 offset:9216\n\t" \
  "ds_read_b64_tr_b16 v[176:177], v65 offset:8\n\t" \
  "ds_read_b64_tr_b16 v[178:179], v65 offset:1032\n\t" \
  "ds_read_b64_tr_b16 v[180:181], v65 offset:8200\n\t" \
  "ds_read_b64_tr_b16 v[182:183], v65 offset:9224\n\t" \
  "s_waitcnt vmcnt(0) lgkmcnt(0)\n\t" \
  "s_barrier\n\t" \
  "ds_read_b64_tr_b16 v[184:185], v66 offset:16384\n\t" \
  "ds_read_b64_tr_b16 v[186:187], v66 offset:17408\n\t" \
  "ds_read_b64_tr_b16 v[188:189], v66 offset:24576\n\t" \
  "ds_read_b64_tr_b16 v[190:191], v66 offset:25600\n\t" \
  "ds_read_b64_tr_b16 v[56:57], v66 offset:16416\n\t" \
  "ds_read_b64_tr_b16 v[58:59], v66 offset:17440\n\t" \
  "ds_read_b64_tr_b16 v[60:61], v66 offset:24608\n\t" \
  "ds_read_b64_tr_b16 v[62:63], v66 offset:25632\n\t" \
  "s_waitcnt lgkmcnt(0)\n\t" \
  "s_barrier\n\t" \
  "ds_write_b128 v70, v[0:3] offset:0\n\t" \
  "ds_write_b128 v70, v[4:7] offset:1024\n\t" \
  "ds_write_b128 v70, v[8:11] offset:2048\n\t" \
  "ds_write_b128 v70, v[12:15] offset:3072\n\t" \
  "ds_write_b128 v70, v[16:19] offset:4096\n\t" \
  "ds_write_b128 v70, v[20:23] offset:5120\n\t" \
  "ds_write_b128 v70, v[24:27] offset:6144\n\t" \
  "ds_write_b128 v70, v[28:31] offset:7168\n\t" \
  "ds_write_b128 v70, v[32:35] offset:8192\n\t" \
  "ds_write_b128 v70, v[36:39] offset:9216\n\t" \
  "ds_write_b128 v70, v[40:43] offset:10240\n\t" \
  "ds_write_b128 v70, v[44:47] offset:11264\n\t" \
  "ds_write_b128 v70, v[48:51] offset:12288\n\t" \
  "ds_write_b128 v70, v[52:55] offset:13312\n\t" \
  "ds_write_b128 v70, v[56:59] offset:14336\n\t" \
  "ds_write_b128 v70, v[60:63] offset:15360\n\t" \
  "s_waitcnt lgkmcnt(0)\n\t" \
  "v_and_b32 v72, 1, v72\n\t" \
  "v_and_b32 v74, 1, v74\n\t" \
  "v_cmp_ne_u32 s[90:91], s32, v72\n\t" \
  "v_cmp_ne_u32 vcc, s32, v74\n\t" \
  "s_nop 1\n\t" \
  "s_mov_b32 s96, vcc_lo\n\t" \
  "s_mov_b32 s48, -1\n\t" \
  "v_and_b32 v73, 32, v69\n\t" \
  "v_lshl_add_u32 v73, v73, 5, v69\n\t" \
  "s_add_u32 s97, s41, s53\n\t" \
  "s_add_u32 s98, s37, s53\n\t" \
  "s_add_u32 s82, s48, 0\n\t" \
  "s_lshr_b64 s[60:61], s[90:91], s82\n\t" \
  "s_and_b32 s60, s60, 1\n\t" \
  "s_lshl_b32 s60, s60, 13\n\t" \
  "s_add_u32 s82, s82, " CH_SG0 "\n\t" \
  "s_lshl_b32 s82, s82, 21\n\t" \
  "s_or_b32 s82, s82, s60\n\t" \
  "s_add_u32 s50, s82, s38\n\t" \
  "s_add_u32 s69, s82, s39\n\t" \
  "s_add_u32 s82, s48, 1\n\t" \
  "s_lshr_b64 s[60:61], s[90:91], s82\n\t" \
  "s_and_b32 s60, s60, 1\n\t" \
  "s_lshl_b32 s60, s60, 13\n\t" \
  "s_add_u32 s82, s82, " CH_SG0 "\n\t" \
  "s_lshl_b32 s82, s82, 21\n\t" \
  "s_or_b32 s82, s82, s60\n\t" \
  "s_add_u32 s52, s82, s43\n\t" \
  "s_add_u32 s82, s48, " CH_ARMD "\n\t" \
  "s_lshr_b64 s[60:61], s[90:91], s82\n\t" \
  "s_and_b32 s60, s60, 1\n\t" \
  "s_lshl_b32 s60, s60, 13\n\t" \
  "s_add_u32 s82, s82, " CH_SG0 "\n\t" \
  "s_lshl_b32 s82, s82, 21\n\t" \
  "s_or_b32 s82, s82, s60\n\t" \
  "s_add_u32 s85, s82, s45\n\t" \
  "s_cmp_lt_i32 s48, " CH_ARMS "\n\t" \
  "s_cselect_b32 s85, s27, s85\n\t" \
  "v_and_b32 v37, 0x30, v69\n\t" \
  "v_add_u32 v37, s52, v37\n\t" \
  "s_mov_b32 s48, 0\n\t" \
  "s_mov_b32 s86, 1\n\t" \
  "s_and_b32 s49, s96, 1\n\t" \
  "s_lshl_b32 s49, s49, 13\n\t" \
  "s_add_u32 s53, s49, " CH_SLOT "\n\t" \
  "s_movk_i32 s60, 124\n\t" \
  "s_cmp_eq_u32 s40, 3\n\t" \
  "s_cselect_b32 s81, 76, s60\n\t" \
  "s_add_u32 s82, s48, 0\n\t" \
  "s_lshr_b64 s[60:61], s[90:91], s82\n\t" \
  "s_and_b32 s60, s60, 1\n\t" \
  "s_lshl_b32 s60, s60, 13\n\t" \
  "s_add_u32 s82, s82, " CH_SG0 "\n\t" \
  "s_lshl_b32 s82, s82, 21\n\t" \
  "s_or_b32 s82, s82, s60\n\t" \
  "s_add_u32 s50, s82, s38\n\t" \
  "s_add_u32 s69, s82, s39\n\t" \
  "s_add_u32 s51, s82, s43\n\t" \
  "s_add_u32 s97, s41, s53\n\t" \
  "s_add_u32 s98, s37, s53\n\t" \
  "s_add_u32 s82, s48, 0\n\t" \
  "s_lshr_b64 s[60:61], s[90:91], s82\n\t" \
  "s_and_b32 s60, s60, 1\n\t" \
  "s_lshl_b32 s60, s60, 13\n\t" \
  "s_add_u32 s82, s82, " CH_SG0 "\n\t" \
  "s_lshl_b32 s82, s82, 21\n\t" \
  "s_or_b32 s82, s82, s60\n\t" \
  "s_add_u32 s50, s82, s38\n\t" \
  "s_add_u32 s69, s82, s39\n\t" \
  "s_add_u32 s82, s48, 1\n\t" \
  "s_lshr_b64 s[60:61], s[90:91], s82\n\t" \
  "s_and_b32 s60, s60, 1\n\t" \
  "s_lshl_b32 s60, s60, 13\n\t" \
  "s_add_u32 s82, s82, " CH_SG0 "\n\t" \
  "s_lshl_b32 s82, s82, 21\n\t" \
  "s_or_b32 s82, s82, s60\n\t" \
  "s_add_u32 s52, s82, s43\n\t" \
  "s_add_u32 s82, s48, " CH_ARMD "\n\t" \
  "s_lshr_b64 s[60:61], s[90:91], s82\n\t" \
  "s_and_b32 s60, s60, 1\n\t" \
  "s_lshl_b32 s60, s60, 13\n\t" \
  "s_add_u32 s82, s82, " CH_SG0 "\n\t" \
  "s_lshl_b32 s82, s82, 21\n\t" \
  "s_or_b32 s82, s82, s60\n\t" \
  "s_add_u32 s85, s82, s45\n\t" \
  "s_cmp_lt_i32 s48, " CH_ARMS "\n\t" \
  "s_cselect_b32 s85, s27, s85\n\t" \
  "s_memrealtime s[62:63]\n\t" \
  "s_waitcnt lgkmcnt(0)\n\t" \
  "s_mov_b32 s65, s62\n\t" \
  "s_sub_u32 s47, s62, 1000\n\t" \
  "s_mov_b64 vcc, -1\n\t" \
  "s_cmp_eq_u32 s40, 0\n\t" \
  "s_cbranch_scc1 LW0_%=\n\t" \
  "s_barrier\n\t" \
  "s_cmp_eq_u32 s40, 1\n\t" \
  "s_cbranch_scc1 LG1_%=\n\t" \
  "s_cmp_eq_u32 s40, 2\n\t" \
  "s_cbranch_scc1 LG2_%=\n\t" \
  "s_branch LTK_%=\n\t" \
  ".p2align 6\n\t" \
  "LW0_%=:\n\t" \
  "v_lshlrev_b32 v78, 2, v69\n\t" \
  "v_and_b32 v78, 0x300, v78\n\t" \
  "v_lshrrev_b32 v79, 8, v69\n\t" \
  "v_lshl_add_u32 v78, v79, 6, v78\n\t" \
  "v_and_b32 v79, 48, v69\n\t" \
  "v_add_u32 v78, v78, v79\n\t" \
  "s_add_u32 s98, s49, s42\n\t" \
  "s_add_u32 s97, s98, 32768\n\t" \
  "s_add_u32 s99, s98, 65536\n\t" \
  "s_barrier\n\t" \
  "s_memrealtime s[62:63]\n\t" \
  "s_waitcnt lgkmcnt(0)\n\t" \
  "s_add_u32 s62, s62, " CH_DLL2 "\n\t" \
  "s_sub_i32 s63, s62, s65\n\t" \
  "s_cmp_gt_i32 s63, " CH_DLT "\n\t" \
  "s_cselect_b32 s60, " CH_DLY ", " CH_DLX "\n\t" \
  "s_add_u32 s62, s62, s60\n\t" \
  "s_cmp_gt_i32 s63, 0\n\t" \
  "s_cselect_b32 s65, s62, s65\n\t" \
  "s_mov_b32 m0, 0\n\t" \
  "s_movk_i32 s60, 8192\n\t" \
  "LWT_%=:\n\t" \
  "s_memrealtime s[62:63]\n\t" \
  "s_sub_u32 s60, s60, 1\n\t" \
  "s_cselect_b32 s61, 0, -1\n\t" \
  "s_waitcnt lgkmcnt(0)\n\t" \
  "s_sub_i32 s63, s62, s65\n\t" \
  "s_and_b32 s63, s63, s61\n\t" \
  "s_cmp_lt_i32 s63, " CH_FIN "\n\t" \
  "s_cbranch_scc1 LWT_%=\n\t" \
  "s_cmp_lt_i32 s63, 0\n\t" \
  "s_cbranch_scc1 LWT_%=f\n\t" \
  "LWT_%=g:\n\t" \
  "LDMA_%=:\n\t" \
  "buffer_load_dwordx4 v78, s[28:31], s98 offen offset:0 sc1 lds\n\t" \
  "buffer_load_dwordx4 v78, s[28:31], s98 offen offset:1024 sc1 lds\n\t" \
  "buffer_load_dwordx4 v78, s[28:31], s98 offen offset:2048 sc1 lds\n\t" \
  "buffer_load_dwordx4 v78, s[28:31], s98 offen offset:3072 sc1 lds\n\t" \
  "s_movk_i32 m0, 4096\n\t" \
  "s_mov_b32 s66, s62\n\t" \
  "buffer_load_dwordx4 v78, s[28:31], s97 offen offset:0 sc1 lds\n\t" \
  "buffer_load_dwordx4 v78, s[28:31], s97 offen offset:1024 sc1 lds\n\t" \
  "buffer_load_dwordx4 v78, s[28:31], s97 offen offset:2048 sc1 lds\n\t" \
  "buffer_load_dwordx4 v78, s[28:31], s97 offen offset:3072 sc1 lds\n\t" \
  "s_movk_i32 m0, 8192\n\t" \
  "s_nop 0\n\t" \
  "buffer_load_dwordx4 v69, s[28:31], s99 offen sc1 lds\n\t" \
  ".p2align 6\n\t" \
  "LG0_%=:\n\t" \
  "s_barrier\n\t" \
  "buffer_load_dwordx4 v[52:55], v69, s[28:31], s50 offen sc1\n\t" \
  "buffer_load_dwordx4 v[56:59], v69, s[28:31], s50 offen offset:1024 sc1\n\t" \
  "s_add_u32 s64, s50, 16384\n\t" \
  "s_movk_i32 m0, 11264\n\t" \
  "v_lshlrev_b32 v75, 1, v69\n\t" \
  "buffer_load_dword v75, s[28:31], s64 offen sc1 lds\n\t" \
  "s_memrealtime s[62:63]\n\t" \
  "s_waitcnt lgkmcnt(0)\n\t" \
  "s_sub_u32 s62, s62, " CH_TKL "\n\t" \
  "s_sub_i32 s63, s62, s65\n\t" \
  "s_cmp_gt_i32 s63, 0\n\t" \
  "s_cselect_b32 s62, s62, s65\n\t" \
  "s_sub_i32 s63, s62, s47\n\t" \
  "s_cmp_gt_i32 s63, " CH_LATE "\n\t" \
  "s_cselect_b32 s71, " CH_SCHW ", s46\n\t" \
  "s_add_u32 s61, s47, " CH_LATE "\n\t" \
  "s_cmp_gt_i32 s63, " CH_LATE "\n\t" \
  "s_cselect_b32 s62, s62, s61\n\t" \
  "s_add_u32 s71, s71, s53\n\t" \
  "s_add_u32 s60, s62, s81\n\t" \
  "s_add_u32 s60, s60, " CH_DL1L "\n\t" \
  "s_lshl_b32 s60, s60, 8\n\t" \
  "s_or_b32 s60, s60, " CH_B0 "\n\t" \
  "s_not_b32 s60, s60\n\t" \
  "v_mov_b32 v76, s60\n\t" \
  "s_lshr_b32 s60, s46, 31\n\t" \
  "s_or_b32 s60, s60, s68\n\t" \
  "s_cselect_b32 s25, 0, 1\n\t" \
  "s_mov_b64 exec, s[24:25]\n\t" \
  "buffer_atomic_umin v76, v69, s[28:31], s71 offen sc1\n\t" \
  "s_mov_b64 exec, -1\n\t" \
  "ds_read_b128 v[44:47], v70 offset:0\n\t" \
  "ds_read_b128 v[48:51], v70 offset:1024\n\t" \
  "ds_read_b128 v[60:63], v70 offset:2048\n\t" \
  "ds_read_b128 v[64:67], v70 offset:3072\n\t" \
  "v_mov_b64 v[36:37], 0\n\t" \
  "v_mov_b64 v[38:39], 0\n\t" \
  "v_mov_b64 v[40:41], 0\n\t" \
  "v_mov_b64 v[42:43], 0\n\t" \
  "v_lshrrev_b32 v71, 1, v69\n\t" \
  "v_lshrrev_b32 v72, 3, v69\n\t" \
  "v_and_b32 v72, 0x78, v72\n\t" \
  "v_and_b32 v74, 0x30, v69\n\t" \
  "v_lshl_add_u32 v72, v74, 3, v72\n\t" \
  "s_add_u32 s54, s53, s36\n\t" \
  "s_add_u32 s55, s54, 524288\n\t" \
  "s_add_u32 s54, s54, 0\n\t" \
  "s_add_u32 s75, s54, 262144\n\t" \
  "s_add_u32 s80, s53, s26\n\t" \
  "s_or_b32 s87, s53, 0x1fff80\n\t" \
  "s_add_u32 s87, s87, 2097152\n\t" \
  "s_waitcnt vmcnt(9)\n\t" \
  "s_barrier\n\t" \
  "ds_read_b128 v[0:3], v69 offset:0\n\t" \
  "ds_read_b128 v[4:7], v69 offset:1024\n\t" \
  "ds_read_b128 v[8:11], v69 offset:2048\n\t" \
  "ds_read_b128 v[12:15], v69 offset:3072\n\t" \
  "s_waitcnt lgkmcnt(3)\n\t" \
  "v_smfmac_f32_16x16x64_bf16 v[36:39], v[0:3], v[44:51], v68\n\t" \
  "ds_read_b128 v[44:47], v70 offset:4096\n\t" \
  "v_smfmac_f32_16x16x64_bf16 v[40:43], v[0:3], v[60:67], v68\n\t" \
  "ds_read_b128 v[48:51], v70 offset:5120\n\t" \
  "s_waitcnt lgkmcnt(4)\n\t" \
  "v_smfmac_f32_16x16x64_bf16 v[36:39], v[4:7], a[32:39], v68\n\t" \
  "s_waitcnt vmcnt(4)\n\t" \
  "s_barrier\n\t" \
  "ds_read_b128 v[16:19], v69 offset:4096\n\t" \
  "v_smfmac_f32_16x16x64_bf16 v[40:43], v[4:7], a[40:47], v68\n\t" \
  "ds_read_b128 v[20:23], v69 offset:5120\n\t" \
  "s_waitcnt lgkmcnt(5)\n\t" \
  "v_smfmac_f32_16x16x64_bf16 v[36:39], v[8:11], a[80:87], v68\n\t" \
  "ds_read_b128 v[24:27], v69 offset:6144\n\t" \
  "v_smfmac_f32_16x16x64_bf16 v[40:43], v[8:11], a[88:95], v68\n\t" \
  "ds_read_b128 v[28:31], v69 offset:7168\n\t" \
  "s_waitcnt lgkmcnt(6)\n\t" \
  "v_smfmac_f32_16x16x64_bf16 v[36:39], v[12:15], a[120:127], v68\n\t" \
  "ds_read_b128 v[32:35], v69 offset:8192\n\t" \
  "s_waitcnt lgkmcnt(5)\n\t" \
  "v_smfmac_f32_16x16x64_bf16 v[40:43], v[12:15], v[44:51], v68\n\t" \
  "ds_read_b128 v[60:63], v70 offset:6144\n\t" \
  "s_waitcnt lgkmcnt(5)\n\t" \
  "v_smfmac_f32_16x16x64_bf16 v[36:39], v[16:19], a[160:167], v68\n\t" \
  "ds_read_b128 v[64:67], v70 offset:7168\n\t" \
  "v_smfmac_f32_16x16x64_bf16 v[40:43], v[16:19], a[168:175], v68\n\t" \
  "s_waitcnt lgkmcnt(5)\n\t" \
  "v_mov_b64 v[44:45], 0\n\t" \
  "v_smfmac_f32_16x16x64_bf16 v[36:39], v[20:23], a[200:207], v68\n\t" \
  "s_waitcnt lgkmcnt(4)\n\t" \
  "v_mov_b64 v[46:47], 0\n\t" \
  "v_smfmac_f32_16x16x64_bf16 v[36:39], v[24:27], a[240:247], v68\n\t" \
  "s_waitcnt lgkmcnt(2)\n\t" \
  "v_mov_b64 v[48:49], 0\n\t" \
  "v_smfmac_f32_16x16x64_bf16 v[36:39], v[28:31], v[112:119], v68\n\t" \
  "v_mov_b64 v[50:51], 0\n\t" \
  "v_smfmac_f32_16x16x64_bf16 v[36:39], v[32:35], v[160:167], v68\n\t" \
  "s_waitcnt lgkmcnt(0)\n\t" \
  "v_smfmac_f32_16x16x64_bf16 v[40:43], v[20:23], v[60:67], v68\n\t" \
  "ds_read_b128 v[60:63], v70 offset:8192\n\t" \
  "v_smfmac_f32_16x16x64_bf16 v[40:43], v[24:27], a[248:255], v68\n\t" \
  "ds_read_b128 v[64:67], v70 offset:9216\n\t" \
  "v_add_f32 v74, v36, v38\n\t" \
  "v_smfmac_f32_16x16x64_bf16 v[40:43], v[28:31], v[120:127], v68\n\t" \
  "v_add_f32 v37, v37, v39\n\t" \
  "v_smfmac_f32_16x16x64_bf16 v[40:43], v[32:35], v[168:175], v68\n\t" \
  "v_cmp_u_f32 vcc, v74, v37\n\t" \
  "v_smfmac_f32_16x16x64_bf16 v[44:47], v[0:3], a[0:7], v68\n\t" \
  "s_cbranch_vccnz LFAIL0_%=\n\t" \
  "LGO0_%=:\n\t" \
  "v_cvt_pk_bf16_f32 v74, v74, v37\n\t" \
  "v_smfmac_f32_16x16x64_bf16 v[44:47], v[4:7], a[48:55], v68\n\t" \
  "s_waitcnt lgkmcnt(0)\n\t" \
  "v_add_f32 v75, v40, v42\n\t" \
  "v_smfmac_f32_16x16x64_bf16 v[44:47], v[8:11], v[60:67], v68\n\t" \
  "ds_read_b128 v[60:63], v70 offset:10240\n\t" \
  "v_add_f32 v41, v41, v43\n\t" \
  "v_cvt_pk_bf16_f32 v75, v75, v41\n\t" \
  "v_smfmac_f32_16x16x64_bf16 v[44:47], v[12:15], a[128:135], v68\n\t" \
  "ds_write2st64_b32 v71, v74, v75 offset0:36 offset1:38\n\t" \
  "v_smfmac_f32_16x16x64_bf16 v[44:47], v[16:19], a[176:183], v68\n\t" \
  "ds_read_b128 v[64:67], v70 offset:11264\n\t" \
  "v_smfmac_f32_16x16x64_bf16 v[44:47], v[20:23], a[208:215], v68\n\t" \
  "v_smfmac_f32_16x16x64_bf16 v[44:47], v[24:27], v[80:87], v68\n\t" \
  "v_smfmac_f32_16x16x64_bf16 v[44:47], v[28:31], v[128:135], v68\n\t" \
  "s_waitcnt lgkmcnt(0)\n\t" \
  "v_smfmac_f32_16x16x64_bf16 v[44:47], v[32:35], v[60:67], v68\n\t" \
  "buffer_load_dwordx4 v[60:63], v69, s[28:31], s64 offen sc1\n\t" \
  "s_barrier\n\t" \
  "ds_read_b64_tr_b16 v[76:77], v72 offset:9216\n\t" \
  "v_smfmac_f32_16x16x64_bf16 v[48:51], v[0:3], a[8:15], v68\n\t" \
  "ds_read_b64_tr_b16 v[78:79], v72 offset:9728\n\t" \
  "v_smfmac_f32_16x16x64_bf16 v[48:51], v[4:7], a[56:63], v68\n\t" \
  "v_add_f32 v74, v44, v46\n\t" \
  "v_smfmac_f32_16x16x64_bf16 v[48:51], v[8:11], a[96:103], v68\n\t" \
  "v_add_f32 v45, v45, v47\n\t" \
  "v_smfmac_f32_16x16x64_bf16 v[48:51], v[12:15], a[136:143], v68\n\t" \
  "s_waitcnt vmcnt(4)\n\t" \
  "s_waitcnt lgkmcnt(0)\n\t" \
  "v_cvt_pk_bf16_f32 v74, v74, v45\n\t" \
  "v_mfma_f32_16x16x32_bf16 v[36:39], v[52:55], v[76:79], 0\n\t" \
  "buffer_load_dwordx4 v[64:67], v69, s[28:31], s64 offen offset:1024 sc1\n\t" \
  "s_waitcnt vmcnt(4)\n\t" \
  "v_mfma_f32_16x16x32_bf16 v[40:43], v[56:59], v[76:79], 0\n\t" \
  "v_mov_b64 v[52:53], 0\n\t" \
  "v_mov_b64 v[54:55], 0\n\t" \
  "v_smfmac_f32_16x16x64_bf16 v[48:51], v[16:19], a[184:191], v68\n\t" \
  "v_mov_b64 v[56:57], 0\n\t" \
  "v_mov_b64 v[58:59], 0\n\t" \
  "v_smfmac_f32_16x16x64_bf16 v[48:51], v[20:23], a[216:223], v68\n\t" \
  "ds_write_b32 v71, v74 offset:0\n\t" \
  "v_add_f32_dpp v36, v40, v36 quad_perm:[2,3,0,1] row_mask:0xf bank_mask:0xf\n\t" \
  "v_smfmac_f32_16x16x64_bf16 v[48:51], v[24:27], v[88:95], v68\n\t" \
  "v_add_f32_dpp v37, v41, v37 quad_perm:[2,3,0,1] row_mask:0xf bank_mask:0xf\n\t" \
  "v_smfmac_f32_16x16x64_bf16 v[48:51], v[28:31], v[136:143], v68\n\t" \
  "v_add_f32_dpp v38, v42, v38 quad_perm:[2,3,0,1] row_mask:0xf bank_mask:0xf\n\t" \
  "v_smfmac_f32_16x16x64_bf16 v[48:51], v[32:35], v[176:183], v68\n\t" \
  "v_add_f32_dpp v39, v43, v39 quad_perm:[2,3,0,1] row_mask:0xf bank_mask:0xf\n\t" \
  "v_smfmac_f32_16x16x64_bf16 v[52:55], v[0:3], a[16:23], v68\n\t" \
  "s_mov_b64 exec, s[58:59]\n\t" \
  "ds_write_b128 v69, v[36:39] offset:2048\n\t" \
  "s_mov_b64 exec, -1\n\t" \
  "v_smfmac_f32_16x16x64_bf16 v[52:55], v[4:7], a[64:71], v68\n\t" \
  "buffer_load_dword off, s[28:31], s87 sc1 lds\n\t" \
  "v_add_f32 v75, v48, v50\n\t" \
  "v_add_f32 v49, v49, v51\n\t" \
  "v_cvt_pk_bf16_f32 v75, v75, v49\n\t" \
  "v_smfmac_f32_16x16x64_bf16 v[56:59], v[0:3], a[24:31], v68\n\t" \
  "ds_write_b32 v71, v75 offset:512\n\t" \
  "v_smfmac_f32_16x16x64_bf16 v[56:59], v[4:7], a[72:79], v68\n\t" \
  "ds_read_b64_tr_b16 v[0:1], v72 offset:10240\n\t" \
  "ds_read_b64_tr_b16 v[2:3], v72 offset:10752\n\t" \
  "v_smfmac_f32_16x16x64_bf16 v[52:55], v[8:11], a[104:111], v68\n\t" \
  "ds_read_b128 v[36:39], v70 offset:12288\n\t" \
  "v_smfmac_f32_16x16x64_bf16 v[56:59], v[8:11], a[112:119], v68\n\t" \
  "ds_read_b128 v[40:43], v70 offset:13312\n\t" \
  "v_smfmac_f32_16x16x64_bf16 v[52:55], v[12:15], a[144:151], v68\n\t" \
  "s_waitcnt lgkmcnt(4)\n\t" \
  "s_barrier\n\t" \
  "ds_read_b64_tr_b16 v[44:45], v72 offset:0\n\t" \
  "ds_read_b64_tr_b16 v[46:47], v72 offset:512\n\t" \
  "v_smfmac_f32_16x16x64_bf16 v[56:59], v[12:15], a[152:159], v68\n\t" \
  "ds_read_b64_tr_b16 v[48:49], v72 offset:1024\n\t" \
  "ds_read_b64_tr_b16 v[50:51], v72 offset:1536\n\t" \
  "v_smfmac_f32_16x16x64_bf16 v[52:55], v[16:19], a[192:199], v68\n\t" \
  "ds_read_b128 v[4:7], v69 offset:2048\n\t" \
  "s_waitcnt lgkmcnt(5)\n\t" \
  "v_smfmac_f32_16x16x64_bf16 v[56:59], v[16:19], v[36:43], v68\n\t" \
  "ds_read_b128 v[8:11], v69 offset:3072\n\t" \
  "s_waitcnt lgkmcnt(4)\n\t" \
  "v_mfma_f32_16x16x32_bf16 v[12:15], v[44:47], v[76:79], 0\n\t" \
  "ds_read_b128 v[36:39], v70 offset:14336\n\t" \
  "s_waitcnt lgkmcnt(3)\n\t" \
  "v_mfma_f32_16x16x32_bf16 v[12:15], v[48:51], v[0:3], v[12:15]\n\t" \
  "ds_read_b128 v[40:43], v70 offset:15360\n\t" \
  "v_smfmac_f32_16x16x64_bf16 v[52:55], v[20:23], a[224:231], v68\n\t" \
  "s_waitcnt lgkmcnt(2)\n\t" \
  "v_add_f32 v4, v4, v8\n\t" \
  "v_add_f32 v5, v5, v9\n\t" \
  "v_add_f32 v6, v6, v10\n\t" \
  "v_cndmask_b32 v78, v13, v14, s[56:57]\n\t" \
  "v_cndmask_b32 v76, v12, v15, s[56:57]\n\t" \
  "v_add_f32 v7, v7, v11\n\t" \
  "v_max3_f32 v77, v4, v5, v6\n\t" \
  "v_add_f32_dpp v76, v78, v76 quad_perm:[2,3,0,1] row_mask:0xf bank_mask:0xf\n\t" \
  "v_max3_f32 v77, v77, v7, v76\n\t" \
  "v_smfmac_f32_16x16x64_bf16 v[56:59], v[20:23], a[232:239], v68\n\t" \
  "ds_read_b128 v[44:47], v70 offset:16384\n\t" \
  "v_max_f32_dpp v77, v77, v77 quad_perm:[2,3,0,1] row_mask:0xf bank_mask:0xf\n\t" \
  "v_mov_b32 v78, v77\n\t" \
  "v_smfmac_f32_16x16x64_bf16 v[52:55], v[24:27], v[96:103], v68\n\t" \
  "ds_read_b128 v[48:51], v70 offset:17408\n\t" \
  "v_permlane16_swap_b32 v77, v78\n\t" \
  "v_max_f32 v77, v77, v78\n\t" \
  "v_mov_b32 v78, v77\n\t" \
  "v_smfmac_f32_16x16x64_bf16 v[52:55], v[28:31], v[144:151], v68\n\t" \
  "ds_read_b128 v[16:19], v70 offset:18432\n\t" \
  "v_permlane32_swap_b32 v77, v78\n\t" \
  "v_max_f32 v77, v77, v78\n\t" \
  "v_smfmac_f32_16x16x64_bf16 v[52:55], v[32:35], v[184:191], v68\n\t" \
  "ds_read_b128 v[20:23], v70 offset:19456\n\t" \
  "v_mul_f32 v77, 0xbe0293ee, v77\n\t" \
  "v_smfmac_f32_16x16x64_bf16 v[56:59], v[24:27], v[104:111], v68\n\t" \
  "v_fmamk_f32 v4, v4, 0x3e0293ee, v77\n\t" \
  "v_smfmac_f32_16x16x64_bf16 v[56:59], v[28:31], v[152:159], v68\n\t" \
  "ds_read_b128 v[24:27], v70 offset:22528\n\t" \
  "s_waitcnt lgkmcnt(4)\n\t" \
  "v_fmamk_f32 v5, v5, 0x3e0293ee, v77\n\t" \
  "v_smfmac_f32_16x16x64_bf16 v[56:59], v[32:35], v[36:43], v68\n\t" \
  "ds_read_b128 v[28:31], v70 offset:23552\n\t" \
  "v_fmamk_f32 v6, v6, 0x3e0293ee, v77\n\t" \
  "v_fmamk_f32 v7, v7, 0x3e0293ee, v77\n\t" \
  "v_fmamk_f32 v76, v76, 0x3e0293ee, v77\n\t" \
  "v_exp_f32 v4, v4\n\t" \
  "v_exp_f32 v5, v5\n\t" \
  "v_exp_f32 v6, v6\n\t" \
  "v_exp_f32 v7, v7\n\t" \
  "v_cvt_pk_bf16_f32 v4, v4, v5\n\t" \
  "v_exp_f32 v76, v76\n\t" \
  "v_cvt_pk_bf16_f32 v5, v6, v7\n\t" \
  "v_mov_b32_dpp v12, v4 quad_perm:[0,1,0,1] row_mask:0xf bank_mask:0xf\n\t" \
  "v_mov_b32_dpp v14, v4 quad_perm:[2,3,2,3] row_mask:0xf bank_mask:0xf\n\t" \
  "v_mov_b32_dpp v13, v5 quad_perm:[0,1,0,1] row_mask:0xf bank_mask:0xf\n\t" \
  "v_mov_b32_dpp v15, v5 quad_perm:[2,3,2,3] row_mask:0xf bank_mask:0xf\n\t" \
  "v_add_f32 v52, v52, v54\n\t" \
  "v_add_f32 v53, v53, v55\n\t" \
  "s_waitcnt vmcnt(2)\n\t" \
  "v_mfma_f32_16x16x32_bf16 v[0:3], v[60:63], v[12:15], 0\n\t" \
  "ds_read_b128 v[36:39], v70 offset:20480\n\t" \
  "v_add_f32 v56, v56, v58\n\t" \
  "v_add_f32 v57, v57, v59\n\t" \
  "v_cvt_pk_bf16_f32 v52, v52, v53\n\t" \
  "v_cvt_pk_bf16_f32 v53, v56, v57\n\t" \
  "s_waitcnt vmcnt(1)\n\t" \
  "v_mfma_f32_16x16x32_bf16 v[4:7], v[64:67], v[12:15], 0\n\t" \
  "buffer_store_dwordx2 v[52:53], v71, s[28:31], s69 offen offset:2048 sc1\n\t" \
  "ds_read_b128 v[40:43], v70 offset:21504\n\t" \
  "v_mov_b64 v[56:57], s[88:89]\n\t" \
  "v_mov_b64 v[58:59], s[88:89]\n\t" \
  "v_mov_b32_dpp v78, v76 quad_perm:[0,1,0,1] row_mask:0xf bank_mask:0xf\n\t" \
  "v_mov_b32_dpp v79, v76 quad_perm:[2,3,2,3] row_mask:0xf bank_mask:0xf\n\t" \
  "v_mfma_f32_16x16x32_bf16 v[32:35], v[56:59], v[12:15], 0\n\t" \
  "ds_read_b128 v[60:63], v70 offset:24576\n\t" \
  "v_cvt_pk_bf16_f32 v11, v78, v79\n\t" \
  "v_cvt_pk_bf16_f32 v8, v78, v79\n\t" \
  "v_mov_b64 v[54:55], 0\n\t" \
  "v_mov_b32 v9, 0\n\t" \
  "v_mov_b32 v10, 0\n\t" \
  "v_mov_b64 v[14:15], 0\n\t" \
  "s_nop 0\n\t" \
  "v_mfma_f32_16x16x32_bf16 v[32:35], v[54:57], v[8:11], v[32:35]\n\t" \
  "s_cmp_eq_u32 s48, 63\n\t" \
  "v_mfma_f32_16x16x32_bf16 v[0:3], v[52:55], v[8:11], v[0:3]\n\t" \
  "ds_read_b128 v[64:67], v70 offset:25600\n\t" \
  "v_mov_b64 v[56:57], 0\n\t" \
  "v_mov_b64 v[58:59], 0\n\t" \
  "v_mfma_f32_16x16x32_bf16 v[4:7], v[52:55], v[10:13], v[4:7]\n\t" \
  "s_cbranch_scc1 LLAST0_%=\n\t" \
  "v_rcp_f32 v74, v32\n\t" \
  "v_mov_b64 v[12:13], 0\n\t" \
  "v_mov_b32 v8, 0\n\t" \
  "v_mul_f32 v0, v0, v74\n\t" \
  "v_mul_f32 v1, v1, v74\n\t" \
  "v_mul_f32 v2, v2, v74\n\t" \
  "v_mul_f32 v3, v3, v74\n\t" \
  "v_cvt_pk_bf16_f32 v76, v0, v1\n\t" \
  "v_cvt_pk_bf16_f32 v77, v2, v3\n\t" \
  "v_pk_mul_f32 v[4:5], v[4:5], v[74:75] op_sel_hi:[1,0]\n\t" \
  "v_pk_mul_f32 v[6:7], v[6:7], v[74:75] op_sel_hi:[1,0]\n\t" \
  "v_cvt_pk_bf16_f32 v78, v4, v5\n\t" \
  "v_cvt_pk_bf16_f32 v79, v6, v7\n\t" \
  "ds_write_b128 v69, v[76:79] offset:4096\n\t" \
  "s_waitcnt lgkmcnt(2)\n\t" \
  "v_smfmac_f32_16x16x64_bf16 v[12:15], v[76:79], v[44:51], v68\n\t" \
  "ds_read_b128 v[44:47], v70 offset:26624\n\t" \
  "v_mov_b32 v11, 0\n\t" \
  "v_smfmac_f32_16x16x64_bf16 v[56:59], v[76:79], v[16:23], v68\n\t" \
  "ds_read_b128 v[48:51], v70 offset:27648\n\t" \
  "v_mov_b64 v[52:53], 0\n\t" \
  "v_smfmac_f32_16x16x64_bf16 v[8:11], v[76:79], v[36:43], v68\n\t" \
  "ds_read_b128 v[16:19], v70 offset:28672\n\t" \
  "v_smfmac_f32_16x16x64_bf16 v[52:55], v[76:79], v[24:31], v68\n\t" \
  "s_waitcnt lgkmcnt(3)\n\t" \
  "s_barrier\n\t" \
  "ds_read_b128 v[0:3], v69 offset:5120\n\t" \
  "ds_read_b128 v[4:7], v69 offset:6144\n\t" \
  "v_lshrrev_b32 v71, 7, v69\n\t" \
  "v_and_b32 v74, 0x70, v69\n\t" \
  "v_lshl_add_u32 v71, v71, 14, v74\n\t" \
  "v_bfe_u32 v72, v69, 2, 7\n\t" \
  "v_and_b32 v74, 0x200, v69\n\t" \
  "s_waitcnt lgkmcnt(1)\n\t" \
  "v_smfmac_f32_16x16x64_bf16 v[8:11], v[0:3], v[60:67], v68\n\t" \
  "ds_read_b128 v[20:23], v70 offset:29696\n\t" \
  "v_mov_b64 v[76:77], 0\n\t" \
  "v_smfmac_f32_16x16x64_bf16 v[12:15], v[0:3], v[200:207], v68\n\t" \
  "ds_read_b128 v[32:35], v69 offset:7168\n\t" \
  "v_mov_b64 v[78:79], 0\n\t" \
  "v_smfmac_f32_16x16x64_bf16 v[52:55], v[0:3], v[44:51], v68\n\t" \
  "ds_read_b128 v[24:27], v70 offset:30720\n\t" \
  "v_lshl_add_u32 v72, v74, 3, v72\n\t" \
  "v_smfmac_f32_16x16x64_bf16 v[56:59], v[0:3], v[232:239], v68\n\t" \
  "buffer_load_dword v75, off, s[28:31], s80 sc1\n\t" \
  "ds_read_b128 v[28:31], v70 offset:31744\n\t" \
  "s_waitcnt lgkmcnt(4)\n\t" \
  "v_smfmac_f32_16x16x64_bf16 v[12:15], v[4:7], v[208:215], v68\n\t" \
  "ds_read_b128 v[60:63], v70 offset:32768\n\t" \
  "s_waitcnt lgkmcnt(4)\n\t" \
  "v_smfmac_f32_16x16x64_bf16 v[8:11], v[4:7], v[16:23], v68\n\t" \
  "ds_read_b128 v[64:67], v70 offset:33792\n\t" \
  "s_waitcnt lgkmcnt(4)\n\t" \
  "v_smfmac_f32_16x16x64_bf16 v[8:11], v[32:35], v[192:199], v68\n\t" \
  "ds_read_b128 v[36:39], v73 offset:4096\n\t" \
  "v_smfmac_f32_16x16x64_bf16 v[12:15], v[32:35], v[216:223], v68\n\t" \
  "ds_read_b128 v[40:43], v73 offset:6144\n\t" \
  "s_waitcnt lgkmcnt(4)\n\t" \
  "v_smfmac_f32_16x16x64_bf16 v[52:55], v[4:7], v[24:31], v68\n\t" \
  "ds_read_b128 v[44:47], v70 offset:34816\n\t" \
  "v_cvt_pk_bf16_f32 v8, v8, v9\n\t" \
  "v_smfmac_f32_16x16x64_bf16 v[52:55], v[32:35], v[224:231], v68\n\t" \
  "ds_read_b128 v[48:51], v70 offset:35840\n\t" \
  "v_cvt_pk_bf16_f32 v9, v10, v11\n\t" \
  "v_smfmac_f32_16x16x64_bf16 v[56:59], v[4:7], v[240:247], v68\n\t" \
  "s_waitcnt lgkmcnt(4)\n\t" \
  "v_cvt_pk_bf16_f32 v10, v12, v13\n\t" \
  "v_smfmac_f32_16x16x64_bf16 v[56:59], v[32:35], v[60:67], v68\n\t" \
  "s_waitcnt lgkmcnt(3)\n\t" \
  "v_cvt_pk_bf16_f32 v11, v14, v15\n\t" \
  "v_smfmac_f32_16x16x64_bf16 v[76:79], v[36:39], v[248:255], v68\n\t" \
  "buffer_store_dwordx4 v[8:11], v71, s[28:31], s54 offen sc1\n\t" \
  "s_waitcnt lgkmcnt(0)\n\t" \
  "v_cvt_pk_bf16_f32 v52, v52, v53\n\t" \
  "v_smfmac_f32_16x16x64_bf16 v[76:79], v[40:43], v[44:51], v68\n\t" \
  "v_cvt_pk_bf16_f32 v53, v54, v55\n\t" \
  "v_cvt_pk_bf16_f32 v54, v56, v57\n\t" \
  "v_cvt_pk_bf16_f32 v55, v58, v59\n\t" \
  "buffer_store_dwordx4 v[52:55], v71, s[28:31], s75 offen sc1\n\t" \
  "s_nop 2\n\t" \
  "v_add_f32 v76, v76, v78\n\t" \
  "v_add_f32 v77, v77, v79\n\t" \
  "v_cvt_pk_bf16_f32 v76, v76, v77\n\t" \
  "buffer_store_dword v76, v72, s[28:31], s55 offen sc1\n\t" \
  "s_memrealtime s[70:71]\n\t" \
  "s_waitcnt vmcnt(3)\n\t" \
  "v_readfirstlane_b32 s65, v75\n\t" \
  "s_barrier\n\t" \
  "s_waitcnt lgkmcnt(0)\n\t" \
  "s_not_b32 s65, s65\n\t" \
  "s_lshl_b32 s61, s70, 8\n\t" \
  "s_andn2_b32 s64, s65, 0xff\n\t" \
  "s_sub_u32 s64, s64, s61\n\t" \
  "s_ashr_i32 s64, s64, 8\n\t" \
  "s_cmp_eq_u32 s65, 0\n\t" \
  "s_cselect_b32 s64, " CH_DLF ", s64\n\t" \
  "s_cselect_b32 s65, " CH_B0 ", s65\n\t" \
  "s_add_u32 s47, s64, s70\n\t" \
  "s_min_i32 s64, s64, 1000\n\t" \
  "s_max_i32 s64, s64, " CH_DLL1 "\n\t" \
  "s_add_u32 s64, s64, s70\n\t" \
  "s_add_u32 s55, s64, " CH_FDL "\n\t" \
  "s_movk_i32 s60, 8192\n\t" \
  "FWT_%=:\n\t" \
  "s_memrealtime s[62:63]\n\t" \
  "s_sub_u32 s60, s60, 1\n\t" \
  "s_cselect_b32 s61, 0, -1\n\t" \
  "s_waitcnt lgkmcnt(0)\n\t" \
  "s_sub_i32 s63, s62, s55\n\t" \
  "s_and_b32 s63, s63, s61\n\t" \
  "s_cmp_lt_i32 s63, 0\n\t" \
  "s_cbranch_scc1 FWT_%=\n\t" \
  "s_mov_b64 exec, 1\n\t" \
  "buffer_store_dword v68, off, s[28:31], s51 offset:4064 sc1\n\t" \
  "s_mov_b64 exec, -1\n\t" \
  "s_and_b32 s65, s65, 0xff\n\t" \
  "s_add_u32 s47, s47, s65\n\t" \
  "s_add_u32 s65, s65, s64\n\t" \
  "s_sub_u32 s60, s70, s66\n\t" \
  "s_add_u32 s61, s81, " CH_UP "\n\t" \
  "s_min_u32 s60, s60, s61\n\t" \
  "s_sub_u32 s61, s81, " CH_DN "\n\t" \
  "s_max_u32 s60, s60, s61\n\t" \
  "s_cmp_eq_u32 s68, 0\n\t" \
  "s_cselect_b32 s81, s60, s81\n\t" \
  "s_add_u32 s48, s48, 1\n\t" \
  "s_mov_b32 s49, s53\n\t" \
  "s_add_u32 s53, s53, " CH_SLOT "\n\t" \
  "s_add_u32 s60, s53, " CH_SKIP "\n\t" \
  "s_lshr_b32 s61, s96, 1\n\t" \
  "s_add_u32 s86, s86, 1\n\t" \
  "s_cmp_eq_u32 s86, 3\n\t" \
  "s_cselect_b32 s53, s60, s53\n\t" \
  "s_cselect_b32 s96, s61, s96\n\t" \
  "s_cselect_b32 s86, 0, s86\n\t" \
  "s_andn2_b32 s53, s53, 0x2000\n\t" \
  "s_and_b32 s60, s96, 1\n\t" \
  "s_lshl_b32 s60, s60, 13\n\t" \
  "s_or_b32 s53, s53, s60\n\t" \
  "s_mov_b32 s68, 0\n\t" \
  "s_add_u32 s82, s48, 0\n\t" \
  "s_lshr_b64 s[60:61], s[90:91], s82\n\t" \
  "s_and_b32 s60, s60, 1\n\t" \
  "s_lshl_b32 s60, s60, 13\n\t" \
  "s_add_u32 s82, s82, " CH_SG0 "\n\t" \
  "s_lshl_b32 s82, s82, 21\n\t" \
  "s_or_b32 s82, s82, s60\n\t" \
  "s_add_u32 s50, s82, s38\n\t" \
  "s_add_u32 s69, s82, s39\n\t" \
  "s_add_u32 s51, s82, s43\n\t" \
  "s_branch LW0_%=\n\t" \
  "LFAIL0_%=:\n\t" \
  "v_max_u32 v75, v0, v1\n\t" \
  "v_max3_u32 v75, v75, v2, v3\n\t" \
  "v_max3_u32 v75, v75, v4, v5\n\t" \
  "v_max3_u32 v75, v75, v6, v7\n\t" \
  "v_max3_u32 v75, v75, v8, v9\n\t" \
  "v_max3_u32 v75, v75, v10, v11\n\t" \
  "v_max3_u32 v75, v75, v12, v13\n\t" \
  "v_max3_u32 v75, v75, v14, v15\n\t" \
  "v_max3_u32 v75, v75, v16, v17\n\t" \
  "v_max3_u32 v75, v75, v18, v19\n\t" \
  "v_max3_u32 v75, v75, v20, v21\n\t" \
  "v_max3_u32 v75, v75, v22, v23\n\t" \
  "v_max3_u32 v75, v75, v24, v25\n\t" \
  "v_max3_u32 v75, v75, v26, v27\n\t" \
  "v_max3_u32 v75, v75, v28, v29\n\t" \
  "v_max3_u32 v75, v75, v30, v31\n\t" \
  "v_max3_u32 v75, v75, v32, v33\n\t" \
  "v_max3_u32 v75, v75, v34, v35\n\t" \
  "v_cmp_eq_u32 vcc, -1, v75\n\t" \
  "s_cbranch_vccz LGO0_%=\n\t" \
  "s_add_u32 s68, s68, 1\n\t" \
  "s_waitcnt lgkmcnt(0)\n\t" \
  "s_cmp_gt_u32 s68, s83\n\t" \
  "s_cbranch_scc1 LGV0_%=\n\t" \
  "s_barrier\n\t" \
  "s_memrealtime s[62:63]\n\t" \
  "s_mov_b32 m0, 0\n\t" \
  "s_waitcnt lgkmcnt(0)\n\t" \
  "s_add_u32 s65, s62, " CH_PEN "\n\t" \
  "s_branch LDMA_%=\n\t" \
  "LWT_%=f:\n\t" \
  "s_cmp_lt_i32 s63, -2\n\t" \
  "s_cbranch_scc0 LWT_%=g\n\t" \
  "s_nop 4\n\t" \
  "s_branch LWT_%=g\n\t" \
  "LGV0_%=:\n\t" \
  "s_mov_b32 s83, 0\n\t" \
  "s_lshl_b32 s60, s36, 4\n\t" \
  "s_or_b32 s60, s60, s48\n\t" \
  "s_or_b32 s60, s60, 805306368\n\t" \
  "v_mov_b32 v76, s60\n\t" \
  "v_mov_b32 v77, 0\n\t" \
  "s_mov_b64 exec, 1\n\t" \
  "buffer_atomic_cmpswap v[76:77], off, s[28:31], s92 sc1\n\t" \
  "s_mov_b64 exec, -1\n\t" \
  "s_branch LGO0_%=\n\t" \
  "LLAST0_%=:\n\t" \
  "s_waitcnt vmcnt(0) lgkmcnt(0)\n\t" \
  "s_barrier\n\t" \
  "s_mov_b64 exec, 1\n\t" \
  "buffer_store_dword v68, off, s[28:31], s51 offset:4064 sc1\n\t" \
  "s_mov_b64 exec, -1\n\t" \
  "s_branch LEND_%=\n\t" \
  ".p2align 6\n\t" \
  "LG1_%=:\n\t" \
  "s_barrier\n\t" \
  "buffer_load_dwordx4 v[52:55], v69, s[28:31], s50 offen sc1\n\t" \
  "buffer_load_dwordx4 v[56:59], v69, s[28:31], s50 offen offset:1024 sc1\n\t" \
  "s_add_u32 s64, s50, 14336\n\t" \
  "s_movk_i32 m0, 11264\n\t" \
  "v_lshlrev_b32 v75, 1, v69\n\t" \
  "buffer_load_dword v75, s[28:31], s64 offen sc1 lds\n\t" \
  "ds_read_b128 v[44:47], v70 offset:0\n\t" \
  "ds_read_b128 v[48:51], v70 offset:1024\n\t" \
  "ds_read_b128 v[60:63], v70 offset:2048\n\t" \
  "ds_read_b128 v[64:67], v70 offset:3072\n\t" \
  "v_mov_b64 v[36:37], 0\n\t" \
  "v_mov_b64 v[38:39], 0\n\t" \
  "v_mov_b64 v[40:41], 0\n\t" \
  "v_mov_b64 v[42:43], 0\n\t" \
  "v_lshrrev_b32 v71, 1, v69\n\t" \
  "v_add_u32 v71, 4, v71\n\t" \
  "v_lshrrev_b32 v72, 3, v69\n\t" \
  "v_and_b32 v72, 0x78, v72\n\t" \
  "v_and_b32 v74, 0x30, v69\n\t" \
  "v_lshl_add_u32 v72, v74, 3, v72\n\t" \
  "s_add_u32 s54, s53, s36\n\t" \
  "s_add_u32 s55, s54, 540672\n\t" \
  "s_add_u32 s54, s54, 4096\n\t" \
  "s_add_u32 s75, s54, 262144\n\t" \
  "s_add_u32 s80, s53, s26\n\t" \
  "s_barrier\n\t" \
  "ds_read_b128 v[0:3], v69 offset:0\n\t" \
  "ds_read_b128 v[4:7], v69 offset:1024\n\t" \
  "ds_read_b128 v[8:11], v69 offset:2048\n\t" \
  "ds_read_b128 v[12:15], v69 offset:3072\n\t" \
  "s_waitcnt lgkmcnt(3)\n\t" \
  "v_smfmac_f32_16x16x64_bf16 v[36:39], v[0:3], v[44:51], v68\n\t" \
  "ds_read_b128 v[44:47], v70 offset:4096\n\t" \
  "v_smfmac_f32_16x16x64_bf16 v[40:43], v[0:3], v[60:67], v68\n\t" \
  "ds_read_b128 v[48:51], v70 offset:5120\n\t" \
  "s_waitcnt lgkmcnt(4)\n\t" \
  "v_smfmac_f32_16x16x64_bf16 v[36:39], v[4:7], a[32:39], v68\n\t" \
  "s_barrier\n\t" \
  "ds_read_b128 v[16:19], v69 offset:4096\n\t" \
  "v_smfmac_f32_16x16x64_bf16 v[40:43], v[4:7], a[40:47], v68\n\t" \
  "ds_read_b128 v[20:23], v69 offset:5120\n\t" \
  "s_waitcnt lgkmcnt(5)\n\t" \
  "v_smfmac_f32_16x16x64_bf16 v[36:39], v[8:11], a[80:87], v68\n\t" \
  "ds_read_b128 v[24:27], v69 offset:6144\n\t" \
  "v_smfmac_f32_16x16x64_bf16 v[40:43], v[8:11], a[88:95], v68\n\t" \
  "ds_read_b128 v[28:31], v69 offset:7168\n\t" \
  "s_waitcnt lgkmcnt(6)\n\t" \
  "v_smfmac_f32_16x16x64_bf16 v[36:39], v[12:15], a[120:127], v68\n\t" \
  "ds_read_b128 v[32:35], v69 offset:8192\n\t" \
  "s_waitcnt lgkmcnt(5)\n\t" \
  "v_smfmac_f32_16x16x64_bf16 v[40:43], v[12:15], v[44:51], v68\n\t" \
  "ds_read_b128 v[60:63], v70 offset:6144\n\t" \
  "s_waitcnt lgkmcnt(5)\n\t" \
  "v_smfmac_f32_16x16x64_bf16 v[36:39], v[16:19], a[160:167], v68\n\t" \
  "ds_read_b128 v[64:67], v70 offset:7168\n\t" \
  "v_smfmac_f32_16x16x64_bf16 v[40:43], v[16:19], a[168:175], v68\n\t" \
  "s_waitcnt lgkmcnt(5)\n\t" \
  "v_mov_b64 v[44:45], 0\n\t" \
  "v_smfmac_f32_16x16x64_bf16 v[36:39], v[20:23], a[200:207], v68\n\t" \
  "s_waitcnt lgkmcnt(4)\n\t" \
  "v_mov_b64 v[46:47], 0\n\t" \
  "v_smfmac_f32_16x16x64_bf16 v[36:39], v[24:27], a[240:247], v68\n\t" \
  "s_waitcnt lgkmcnt(2)\n\t" \
  "v_mov_b64 v[48:49], 0\n\t" \
  "v_smfmac_f32_16x16x64_bf16 v[36:39], v[28:31], v[112:119], v68\n\t" \
  "v_mov_b64 v[50:51], 0\n\t" \
  "v_smfmac_f32_16x16x64_bf16 v[36:39], v[32:35], v[160:167], v68\n\t" \
  "s_waitcnt lgkmcnt(0)\n\t" \
  "v_smfmac_f32_16x16x64_bf16 v[40:43], v[20:23], v[60:67], v68\n\t" \
  "ds_read_b128 v[60:63], v70 offset:8192\n\t" \
  "v_smfmac_f32_16x16x64_bf16 v[40:43], v[24:27], a[248:255], v68\n\t" \
  "ds_read_b128 v[64:67], v70 offset:9216\n\t" \
  "v_add_f32 v74, v36, v38\n\t" \
  "v_smfmac_f32_16x16x64_bf16 v[40:43], v[28:31], v[120:127], v68\n\t" \
  "v_add_f32 v37, v37, v39\n\t" \
  "v_smfmac_f32_16x16x64_bf16 v[40:43], v[32:35], v[168:175], v68\n\t" \
  "v_cmp_u_f32 vcc, v74, v37\n\t" \
  "v_smfmac_f32_16x16x64_bf16 v[44:47], v[0:3], a[0:7], v68\n\t" \
  "s_cbranch_vccnz LFAIL1_%=\n\t" \
  "LGO1_%=:\n\t" \
  "v_cvt_pk_bf16_f32 v74, v74, v37\n\t" \
  "v_smfmac_f32_16x16x64_bf16 v[44:47], v[4:7], a[48:55], v68\n\t" \
  "s_waitcnt lgkmcnt(0)\n\t" \
  "v_add_f32 v75, v40, v42\n\t" \
  "v_smfmac_f32_16x16x64_bf16 v[44:47], v[8:11], v[60:67], v68\n\t" \
  "ds_read_b128 v[60:63], v70 offset:10240\n\t" \
  "v_add_f32 v41, v41, v43\n\t" \
  "v_cvt_pk_bf16_f32 v75, v75, v41\n\t" \
  "v_smfmac_f32_16x16x64_bf16 v[44:47], v[12:15], a[128:135], v68\n\t" \
  "ds_write2st64_b32 v71, v74, v75 offset0:36 offset1:38\n\t" \
  "v_smfmac_f32_16x16x64_bf16 v[44:47], v[16:19], a[176:183], v68\n\t" \
  "ds_read_b128 v[64:67], v70 offset:11264\n\t" \
  "v_smfmac_f32_16x16x64_bf16 v[44:47], v[20:23], a[208:215], v68\n\t" \
  "v_smfmac_f32_16x16x64_bf16 v[44:47], v[24:27], v[80:87], v68\n\t" \
  "v_smfmac_f32_16x16x64_bf16 v[44:47], v[28:31], v[128:135], v68\n\t" \
  "s_waitcnt lgkmcnt(0)\n\t" \
  "v_smfmac_f32_16x16x64_bf16 v[44:47], v[32:35], v[60:67], v68\n\t" \
  "s_barrier\n\t" \
  "ds_read_b64_tr_b16 v[76:77], v72 offset:9216\n\t" \
  "v_smfmac_f32_16x16x64_bf16 v[48:51], v[0:3], a[8:15], v68\n\t" \
  "buffer_load_dwordx4 v[60:63], v69, s[28:31], s64 offen sc1\n\t" \
  "ds_read_b64_tr_b16 v[78:79], v72 offset:9728\n\t" \
  "v_smfmac_f32_16x16x64_bf16 v[48:51], v[4:7], a[56:63], v68\n\t" \
  "v_add_f32 v74, v44, v46\n\t" \
  "v_smfmac_f32_16x16x64_bf16 v[48:51], v[8:11], a[96:103], v68\n\t" \
  "v_add_f32 v45, v45, v47\n\t" \
  "v_smfmac_f32_16x16x64_bf16 v[48:51], v[12:15], a[136:143], v68\n\t" \
  "s_waitcnt vmcnt(3)\n\t" \
  "s_waitcnt lgkmcnt(0)\n\t" \
  "v_cvt_pk_bf16_f32 v74, v74, v45\n\t" \
  "v_mfma_f32_16x16x32_bf16 v[36:39], v[52:55], v[76:79], 0\n\t" \
  "s_waitcnt vmcnt(2)\n\t" \
  "v_mfma_f32_16x16x32_bf16 v[40:43], v[56:59], v[76:79], 0\n\t" \
  "buffer_load_dwordx4 v[64:67], v69, s[28:31], s64 offen offset:1024 sc1\n\t" \
  "v_mov_b64 v[52:53], 0\n\t" \
  "v_mov_b64 v[54:55], 0\n\t" \
  "v_smfmac_f32_16x16x64_bf16 v[48:51], v[16:19], a[184:191], v68\n\t" \
  "v_mov_b64 v[56:57], 0\n\t" \
  "v_mov_b64 v[58:59], 0\n\t" \
  "v_smfmac_f32_16x16x64_bf16 v[48:51], v[20:23], a[216:223], v68\n\t" \
  "ds_write_b32 v71, v74 offset:0\n\t" \
  "v_add_f32_dpp v36, v40, v36 quad_perm:[2,3,0,1] row_mask:0xf bank_mask:0xf\n\t" \
  "v_smfmac_f32_16x16x64_bf16 v[48:51], v[24:27], v[88:95], v68\n\t" \
  "v_add_f32_dpp v37, v41, v37 quad_perm:[2,3,0,1] row_mask:0xf bank_mask:0xf\n\t" \
  "v_smfmac_f32_16x16x64_bf16 v[48:51], v[28:31], v[136:143], v68\n\t" \
  "v_add_f32_dpp v38, v42, v38 quad_perm:[2,3,0,1] row_mask:0xf bank_mask:0xf\n\t" \
  "v_smfmac_f32_16x16x64_bf16 v[48:51], v[32:35], v[176:183], v68\n\t" \
  "v_add_f32_dpp v39, v43, v39 quad_perm:[2,3,0,1] row_mask:0xf bank_mask:0xf\n\t" \
  "v_smfmac_f32_16x16x64_bf16 v[52:55], v[0:3], a[16:23], v68\n\t" \
  "s_mov_b64 exec, s[58:59]\n\t" \
  "ds_write_b128 v69, v[36:39] offset:2080\n\t" \
  "s_mov_b64 exec, -1\n\t" \
  "v_smfmac_f32_16x16x64_bf16 v[52:55], v[4:7], a[64:71], v68\n\t" \
  "v_add_f32 v75, v48, v50\n\t" \
  "v_add_f32 v49, v49, v51\n\t" \
  "v_cvt_pk_bf16_f32 v75, v75, v49\n\t" \
  "v_smfmac_f32_16x16x64_bf16 v[56:59], v[0:3], a[24:31], v68\n\t" \
  "ds_write_b32 v71, v75 offset:512\n\t" \
  "v_smfmac_f32_16x16x64_bf16 v[56:59], v[4:7], a[72:79], v68\n\t" \
  "ds_read_b64_tr_b16 v[0:1], v72 offset:10240\n\t" \
  "ds_read_b64_tr_b16 v[2:3], v72 offset:10752\n\t" \
  "v_smfmac_f32_16x16x64_bf16 v[52:55], v[8:11], a[104:111], v68\n\t" \
  "ds_read_b128 v[36:39], v70 offset:12288\n\t" \
  "v_smfmac_f32_16x16x64_bf16 v[56:59], v[8:11], a[112:119], v68\n\t" \
  "ds_read_b128 v[40:43], v70 offset:13312\n\t" \
  "v_smfmac_f32_16x16x64_bf16 v[52:55], v[12:15], a[144:151], v68\n\t" \
  "s_waitcnt lgkmcnt(4)\n\t" \
  "s_barrier\n\t" \
  "ds_read_b64_tr_b16 v[44:45], v72 offset:0\n\t" \
  "ds_read_b64_tr_b16 v[46:47], v72 offset:512\n\t" \
  "v_smfmac_f32_16x16x64_bf16 v[56:59], v[12:15], a[152:159], v68\n\t" \
  "ds_read_b64_tr_b16 v[48:49], v72 offset:1024\n\t" \
  "ds_read_b64_tr_b16 v[50:51], v72 offset:1536\n\t" \
  "v_smfmac_f32_16x16x64_bf16 v[52:55], v[16:19], a[192:199], v68\n\t" \
  "ds_read_b128 v[4:7], v69 offset:2048\n\t" \
  "s_waitcnt lgkmcnt(5)\n\t" \
  "v_smfmac_f32_16x16x64_bf16 v[56:59], v[16:19], v[36:43], v68\n\t" \
  "ds_read_b128 v[8:11], v69 offset:3072\n\t" \
  "s_waitcnt lgkmcnt(4)\n\t" \
  "v_mfma_f32_16x16x32_bf16 v[12:15], v[44:47], v[76:79], 0\n\t" \
  "ds_read_b128 v[36:39], v70 offset:14336\n\t" \
  "s_waitcnt lgkmcnt(3)\n\t" \
  "v_mfma_f32_16x16x32_bf16 v[12:15], v[48:51], v[0:3], v[12:15]\n\t" \
  "buffer_store_dwordx4 v[44:47], v69, s[28:31], s69 offen offset:0 sc1\n\t" \
  "ds_read_b128 v[40:43], v70 offset:15360\n\t" \
  "v_smfmac_f32_16x16x64_bf16 v[52:55], v[20:23], a[224:231], v68\n\t" \
  "s_waitcnt lgkmcnt(2)\n\t" \
  "v_add_f32 v4, v4, v8\n\t" \
  "v_add_f32 v5, v5, v9\n\t" \
  "v_add_f32 v6, v6, v10\n\t" \
  "v_cndmask_b32 v78, v13, v14, s[56:57]\n\t" \
  "v_cndmask_b32 v76, v12, v15, s[56:57]\n\t" \
  "v_add_f32 v7, v7, v11\n\t" \
  "v_max3_f32 v77, v4, v5, v6\n\t" \
  "v_add_f32_dpp v76, v78, v76 quad_perm:[2,3,0,1] row_mask:0xf bank_mask:0xf\n\t" \
  "v_max3_f32 v77, v77, v7, v76\n\t" \
  "v_smfmac_f32_16x16x64_bf16 v[56:59], v[20:23], a[232:239], v68\n\t" \
  "ds_read_b128 v[44:47], v70 offset:16384\n\t" \
  "v_max_f32_dpp v77, v77, v77 quad_perm:[2,3,0,1] row_mask:0xf bank_mask:0xf\n\t" \
  "v_mov_b32 v78, v77\n\t" \
  "v_smfmac_f32_16x16x64_bf16 v[52:55], v[24:27], v[96:103], v68\n\t" \
  "ds_read_b128 v[48:51], v70 offset:17408\n\t" \
  "v_permlane16_swap_b32 v77, v78\n\t" \
  "v_max_f32 v77, v77, v78\n\t" \
  "v_mov_b32 v78, v77\n\t" \
  "v_smfmac_f32_16x16x64_bf16 v[52:55], v[28:31], v[144:151], v68\n\t" \
  "ds_read_b128 v[16:19], v70 offset:18432\n\t" \
  "v_permlane32_swap_b32 v77, v78\n\t" \
  "v_max_f32 v77, v77, v78\n\t" \
  "v_smfmac_f32_16x16x64_bf16 v[52:55], v[32:35], v[184:191], v68\n\t" \
  "ds_read_b128 v[20:23], v70 offset:19456\n\t" \
  "v_mul_f32 v77, 0xbe0293ee, v77\n\t" \
  "v_smfmac_f32_16x16x64_bf16 v[56:59], v[24:27], v[104:111], v68\n\t" \
  "v_fmamk_f32 v4, v4, 0x3e0293ee, v77\n\t" \
  "v_smfmac_f32_16x16x64_bf16 v[56:59], v[28:31], v[152:159], v68\n\t" \
  "ds_read_b128 v[24:27], v70 offset:22528\n\t" \
  "s_waitcnt lgkmcnt(4)\n\t" \
  "v_fmamk_f32 v5, v5, 0x3e0293ee, v77\n\t" \
  "v_smfmac_f32_16x16x64_bf16 v[56:59], v[32:35], v[36:43], v68\n\t" \
  "ds_read_b128 v[28:31], v70 offset:23552\n\t" \
  "v_fmamk_f32 v6, v6, 0x3e0293ee, v77\n\t" \
  "v_fmamk_f32 v7, v7, 0x3e0293ee, v77\n\t" \
  "v_fmamk_f32 v76, v76, 0x3e0293ee, v77\n\t" \
  "v_exp_f32 v4, v4\n\t" \
  "v_exp_f32 v5, v5\n\t" \
  "v_exp_f32 v6, v6\n\t" \
  "v_exp_f32 v7, v7\n\t" \
  "v_cvt_pk_bf16_f32 v4, v4, v5\n\t" \
  "v_exp_f32 v76, v76\n\t" \
  "v_cvt_pk_bf16_f32 v5, v6, v7\n\t" \
  "v_mov_b32_dpp v12, v4 quad_perm:[0,1,0,1] row_mask:0xf bank_mask:0xf\n\t" \
  "v_mov_b32_dpp v14, v4 quad_perm:[2,3,2,3] row_mask:0xf bank_mask:0xf\n\t" \
  "v_mov_b32_dpp v13, v5 quad_perm:[0,1,0,1] row_mask:0xf bank_mask:0xf\n\t" \
  "v_mov_b32_dpp v15, v5 quad_perm:[2,3,2,3] row_mask:0xf bank_mask:0xf\n\t" \
  "v_add_f32 v52, v52, v54\n\t" \
  "v_add_f32 v53, v53, v55\n\t" \
  "s_waitcnt vmcnt(2)\n\t" \
  "v_mfma_f32_16x16x32_bf16 v[0:3], v[60:63], v[12:15], 0\n\t" \
  "ds_read_b128 v[36:39], v70 offset:20480\n\t" \
  "v_add_f32 v56, v56, v58\n\t" \
  "v_add_f32 v57, v57, v59\n\t" \
  "v_cvt_pk_bf16_f32 v52, v52, v53\n\t" \
  "v_cvt_pk_bf16_f32 v53, v56, v57\n\t" \
  "s_waitcnt vmcnt(1)\n\t" \
  "v_mfma_f32_16x16x32_bf16 v[4:7], v[64:67], v[12:15], 0\n\t" \
  "ds_read_b128 v[40:43], v70 offset:21504\n\t" \
  "v_mov_b64 v[56:57], s[88:89]\n\t" \
  "v_mov_b64 v[58:59], s[88:89]\n\t" \
  "v_mov_b32_dpp v78, v76 quad_perm:[0,1,0,1] row_mask:0xf bank_mask:0xf\n\t" \
  "v_mov_b32_dpp v79, v76 quad_perm:[2,3,2,3] row_mask:0xf bank_mask:0xf\n\t" \
  "v_mfma_f32_16x16x32_bf16 v[32:35], v[56:59], v[12:15], 0\n\t" \
  "buffer_store_dwordx2 v[52:53], v71, s[28:31], s69 offen offset:2556 sc1\n\t" \
  "ds_read_b128 v[60:63], v70 offset:24576\n\t" \
  "v_cvt_pk_bf16_f32 v11, v78, v79\n\t" \
  "v_cvt_pk_bf16_f32 v8, v78, v79\n\t" \
  "v_mov_b64 v[54:55], 0\n\t" \
  "v_mov_b32 v9, 0\n\t" \
  "v_mov_b32 v10, 0\n\t" \
  "v_mov_b64 v[14:15], 0\n\t" \
  "s_nop 0\n\t" \
  "v_mfma_f32_16x16x32_bf16 v[32:35], v[54:57], v[8:11], v[32:35]\n\t" \
  "s_cmp_eq_u32 s48, 63\n\t" \
  "v_mfma_f32_16x16x32_bf16 v[0:3], v[52:55], v[8:11], v[0:3]\n\t" \
  "ds_read_b128 v[64:67], v70 offset:25600\n\t" \
  "v_mov_b64 v[56:57], 0\n\t" \
  "v_mov_b64 v[58:59], 0\n\t" \
  "v_mfma_f32_16x16x32_bf16 v[4:7], v[52:55], v[10:13], v[4:7]\n\t" \
  "s_cbranch_scc1 LLAST1_%=\n\t" \
  "v_rcp_f32 v74, v32\n\t" \
  "v_mov_b64 v[12:13], 0\n\t" \
  "v_mov_b32 v8, 0\n\t" \
  "v_mul_f32 v0, v0, v74\n\t" \
  "v_mul_f32 v1, v1, v74\n\t" \
  "v_mul_f32 v2, v2, v74\n\t" \
  "v_mul_f32 v3, v3, v74\n\t" \
  "v_cvt_pk_bf16_f32 v76, v0, v1\n\t" \
  "v_cvt_pk_bf16_f32 v77, v2, v3\n\t" \
  "v_pk_mul_f32 v[4:5], v[4:5], v[74:75] op_sel_hi:[1,0]\n\t" \
  "v_pk_mul_f32 v[6:7], v[6:7], v[74:75] op_sel_hi:[1,0]\n\t" \
  "v_cvt_pk_bf16_f32 v78, v4, v5\n\t" \
  "v_cvt_pk_bf16_f32 v79, v6, v7\n\t" \
  "ds_write_b128 v69, v[76:79] offset:5120\n\t" \
  "s_waitcnt lgkmcnt(2)\n\t" \
  "v_smfmac_f32_16x16x64_bf16 v[12:15], v[76:79], v[44:51], v68\n\t" \
  "ds_read_b128 v[44:47], v70 offset:26624\n\t" \
  "v_mov_b32 v11, 0\n\t" \
  "v_smfmac_f32_16x16x64_bf16 v[56:59], v[76:79], v[16:23], v68\n\t" \
  "ds_read_b128 v[48:51], v70 offset:27648\n\t" \
  "v_mov_b64 v[52:53], 0\n\t" \
  "v_smfmac_f32_16x16x64_bf16 v[8:11], v[76:79], v[36:43], v68\n\t" \
  "ds_read_b128 v[16:19], v70 offset:28672\n\t" \
  "v_smfmac_f32_16x16x64_bf16 v[52:55], v[76:79], v[24:31], v68\n\t" \
  "s_waitcnt lgkmcnt(3)\n\t" \
  "s_barrier\n\t" \
  "ds_read_b128 v[0:3], v69 offset:4096\n\t" \
  "ds_read_b128 v[4:7], v69 offset:7168\n\t" \
  "v_lshrrev_b32 v71, 7, v69\n\t" \
  "v_and_b32 v74, 0x70, v69\n\t" \
  "v_lshl_add_u32 v71, v71, 14, v74\n\t" \
  "v_bfe_u32 v72, v69, 2, 7\n\t" \
  "v_and_b32 v74, 0x200, v69\n\t" \
  "s_waitcnt lgkmcnt(1)\n\t" \
  "v_smfmac_f32_16x16x64_bf16 v[8:11], v[0:3], v[60:67], v68\n\t" \
  "buffer_load_dword v75, off, s[28:31], s80 sc1\n\t" \
  "ds_read_b128 v[20:23], v70 offset:29696\n\t" \
  "v_mov_b64 v[76:77], 0\n\t" \
  "v_smfmac_f32_16x16x64_bf16 v[12:15], v[0:3], v[200:207], v68\n\t" \
  "ds_read_b128 v[32:35], v69 offset:6144\n\t" \
  "v_mov_b64 v[78:79], 0\n\t" \
  "v_smfmac_f32_16x16x64_bf16 v[52:55], v[0:3], v[44:51], v68\n\t" \
  "ds_read_b128 v[24:27], v70 offset:30720\n\t" \
  "v_lshl_add_u32 v72, v74, 3, v72\n\t" \
  "v_smfmac_f32_16x16x64_bf16 v[56:59], v[0:3], v[232:239], v68\n\t" \
  "ds_read_b128 v[28:31], v70 offset:31744\n\t" \
  "s_waitcnt lgkmcnt(4)\n\t" \
  "v_smfmac_f32_16x16x64_bf16 v[12:15], v[4:7], v[208:215], v68\n\t" \
  "ds_read_b128 v[60:63], v70 offset:32768\n\t" \
  "s_waitcnt lgkmcnt(4)\n\t" \
  "v_smfmac_f32_16x16x64_bf16 v[8:11], v[4:7], v[16:23], v68\n\t" \
  "ds_read_b128 v[64:67], v70 offset:33792\n\t" \
  "s_waitcnt lgkmcnt(4)\n\t" \
  "v_smfmac_f32_16x16x64_bf16 v[8:11], v[32:35], v[192:199], v68\n\t" \
  "ds_read_b128 v[36:39], v73 offset:4096\n\t" \
  "v_smfmac_f32_16x16x64_bf16 v[12:15], v[32:35], v[216:223], v68\n\t" \
  "ds_read_b128 v[40:43], v73 offset:6144\n\t" \
  "s_waitcnt lgkmcnt(4)\n\t" \
  "v_smfmac_f32_16x16x64_bf16 v[52:55], v[4:7], v[24:31], v68\n\t" \
  "ds_read_b128 v[44:47], v70 offset:34816\n\t" \
  "v_cvt_pk_bf16_f32 v8, v8, v9\n\t" \
  "v_smfmac_f32_16x16x64_bf16 v[52:55], v[32:35], v[224:231], v68\n\t" \
  "ds_read_b128 v[48:51], v70 offset:35840\n\t" \
  "v_cvt_pk_bf16_f32 v9, v10, v11\n\t" \
  "v_smfmac_f32_16x16x64_bf16 v[56:59], v[4:7], v[240:247], v68\n\t" \
  "s_waitcnt lgkmcnt(4)\n\t" \
  "v_cvt_pk_bf16_f32 v10, v12, v13\n\t" \
  "v_smfmac_f32_16x16x64_bf16 v[56:59], v[32:35], v[60:67], v68\n\t" \
  "s_waitcnt lgkmcnt(3)\n\t" \
  "v_cvt_pk_bf16_f32 v11, v14, v15\n\t" \
  "v_smfmac_f32_16x16x64_bf16 v[76:79], v[36:39], v[248:255], v68\n\t" \
  "buffer_store_dwordx4 v[8:11], v71, s[28:31], s54 offen sc1\n\t" \
  "s_waitcnt lgkmcnt(0)\n\t" \
  "v_cvt_pk_bf16_f32 v52, v52, v53\n\t" \
  "v_smfmac_f32_16x16x64_bf16 v[76:79], v[40:43], v[44:51], v68\n\t" \
  "v_cvt_pk_bf16_f32 v53, v54, v55\n\t" \
  "v_cvt_pk_bf16_f32 v54, v56, v57\n\t" \
  "v_cvt_pk_bf16_f32 v55, v58, v59\n\t" \
  "buffer_store_dwordx4 v[52:55], v71, s[28:31], s75 offen sc1\n\t" \
  "s_nop 2\n\t" \
  "v_add_f32 v76, v76, v78\n\t" \
  "v_add_f32 v77, v77, v79\n\t" \
  "v_cvt_pk_bf16_f32 v76, v76, v77\n\t" \
  "buffer_store_dword v76, v72, s[28:31], s55 offen sc1\n\t" \
  "s_memrealtime s[70:71]\n\t" \
  "s_waitcnt vmcnt(3)\n\t" \
  "v_readfirstlane_b32 s65, v75\n\t" \
  "s_barrier\n\t" \
  "s_waitcnt lgkmcnt(0)\n\t" \
  "s_not_b32 s65, s65\n\t" \
  "s_lshl_b32 s61, s70, 8\n\t" \
  "s_andn2_b32 s64, s65, 0xff\n\t" \
  "s_sub_u32 s64, s64, s61\n\t" \
  "s_ashr_i32 s64, s64, 8\n\t" \
  "s_cmp_eq_u32 s65, 0\n\t" \
  "s_cselect_b32 s64, " CH_DLF ", s64\n\t" \
  "s_min_i32 s64, s64, 1000\n\t" \
  "s_max_i32 s64, s64, " CH_DLL1 "\n\t" \
  "s_add_u32 s64, s64, s70\n\t" \
  "s_mov_b32 s74, 0\n\t" \
  "s_movk_i32 s60, 8192\n\t" \
  "RWT1_%=:\n\t" \
  "s_memrealtime s[72:73]\n\t" \
  "s_sub_u32 s60, s60, 1\n\t" \
  "s_cselect_b32 s61, 0, -1\n\t" \
  "s_waitcnt lgkmcnt(0)\n\t" \
  "s_sub_i32 s73, s72, s64\n\t" \
  "s_and_b32 s73, s73, s61\n\t" \
  "s_cmp_lt_i32 s73, " CH_FIN "\n\t" \
  "s_cbranch_scc1 RWT1_%=\n\t" \
  "s_cmp_lt_i32 s73, 0\n\t" \
  "s_cbranch_scc1 RWT1_%=f\n\t" \
  "RWT1_%=g:\n\t" \
  "RRETRY1_%=:\n\t" \
  "buffer_load_dwordx4 v[0:3], v69, s[28:31], s97 offen offset:0 sc1\n\t" \
  "buffer_load_dwordx4 v[4:7], v69, s[28:31], s97 offen offset:1024 sc1\n\t" \
  "buffer_load_dwordx4 v[8:11], v69, s[28:31], s97 offen offset:2048 sc1\n\t" \
  "buffer_load_dwordx4 v[12:15], v69, s[28:31], s97 offen offset:3072 sc1\n\t" \
  "v_lshrrev_b32 v20, 2, v69\n\t" \
  "v_and_b32 v20, 16, v20\n\t" \
  "v_mov_b32 v21, 0x3f80\n\t" \
  "v_lshlrev_b32 v21, v20, v21\n\t" \
  "v_bfe_u32 v20, v69, 4, 2\n\t" \
  "v_cmp_eq_u32 vcc, 0, v20\n\t" \
  "s_nop 1\n\t" \
  "v_cndmask_b32 v32, 0, v21, vcc\n\t" \
  "v_cmp_eq_u32 vcc, 1, v20\n\t" \
  "s_nop 1\n\t" \
  "v_cndmask_b32 v33, 0, v21, vcc\n\t" \
  "v_cmp_eq_u32 vcc, 2, v20\n\t" \
  "s_nop 1\n\t" \
  "v_cndmask_b32 v34, 0, v21, vcc\n\t" \
  "v_cmp_eq_u32 vcc, 3, v20\n\t" \
  "s_nop 1\n\t" \
  "v_cndmask_b32 v35, 0, v21, vcc\n\t" \
  "v_bfe_u32 v38, v69, 7, 1\n\t" \
  "v_bfe_u32 v20, v69, 9, 1\n\t" \
  "v_lshl_add_u32 v38, v20, 2, v38\n\t" \
  "v_lshlrev_b32 v38, 12, v38\n\t" \
  "v_and_b32 v20, 0x60, v69\n\t" \
  "v_add_u32 v38, v38, v20\n\t" \
  "v_bfe_u32 v20, v69, 8, 1\n\t" \
  "v_lshl_add_u32 v38, v20, 4, v38\n\t" \
  "v_bfe_u32 v20, v69, 4, 1\n\t" \
  "v_lshl_add_u32 v38, v20, 3, v38\n\t" \
  "v_mov_b32 v44, 0\n\t" \
  "s_waitcnt vmcnt(3)\n\t" \
  "v_mfma_f32_16x16x32_bf16 v[40:43], v[32:35], v[0:3], 0\n\t" \
  "v_max3_u32 v44, v44, v0, v1\n\t" \
  "v_max3_u32 v44, v44, v2, v3\n\t" \
  "s_waitcnt vmcnt(2)\n\t" \
  "v_mfma_f32_16x16x32_bf16 v[40:43], v[32:35], v[4:7], v[40:43]\n\t" \
  "v_max3_u32 v44, v44, v4, v5\n\t" \
  "v_max3_u32 v44, v44, v6, v7\n\t" \
  "s_waitcnt vmcnt(1)\n\t" \
  "v_mfma_f32_16x16x32_bf16 v[40:43], v[32:35], v[8:11], v[40:43]\n\t" \
  "v_max3_u32 v44, v44, v8, v9\n\t" \
  "v_max3_u32 v44, v44, v10, v11\n\t" \
  "s_waitcnt vmcnt(0)\n\t" \
  "v_mfma_f32_16x16x32_bf16 v[40:43], v[32:35], v[12:15], v[40:43]\n\t" \
  "v_max3_u32 v44, v44, v12, v13\n\t" \
  "v_max3_u32 v44, v44, v14, v15\n\t" \
  "v_cmp_eq_u32 vcc, -1, v44\n\t" \
  "s_cbranch_vccnz RFAIL1_%=\n\t" \
  "s_nop 3\n\t" \
  "v_add_f32_dpp v40, v40, v40 row_ror:8 row_mask:0xf bank_mask:0xf\n\t" \
  "v_add_f32_dpp v41, v41, v41 row_ror:8 row_mask:0xf bank_mask:0xf\n\t" \
  "v_add_f32_dpp v42, v42, v42 row_ror:8 row_mask:0xf bank_mask:0xf\n\t" \
  "v_add_f32_dpp v43, v43, v43 row_ror:8 row_mask:0xf bank_mask:0xf\n\t" \
  "v_cvt_pk_bf16_f32 v40, v40, v41\n\t" \
  "v_cvt_pk_bf16_f32 v41, v42, v43\n\t" \
  "buffer_store_dwordx2 v[40:41], v38, s[28:31], s98 offen sc1\n\t" \
  "RDONE1_%=:\n\t" \
  "s_barrier\n\t" \
  "s_add_u32 s48, s48, 1\n\t" \
  "s_mov_b32 s49, s53\n\t" \
  "s_add_u32 s53, s53, " CH_SLOT "\n\t" \
  "s_add_u32 s60, s53, " CH_SKIP "\n\t" \
  "s_lshr_b32 s61, s96, 1\n\t" \
  "s_add_u32 s86, s86, 1\n\t" \
  "s_cmp_eq_u32 s86, 3\n\t" \
  "s_cselect_b32 s53, s60, s53\n\t" \
  "s_cselect_b32 s96, s61, s96\n\t" \
  "s_cselect_b32 s86, 0, s86\n\t" \
  "s_andn2_b32 s53, s53, 0x2000\n\t" \
  "s_and_b32 s60, s96, 1\n\t" \
  "s_lshl_b32 s60, s60, 13\n\t" \
  "s_or_b32 s53, s53, s60\n\t" \
  "s_mov_b32 s68, 0\n\t" \
  "s_add_u32 s97, s41, s53\n\t" \
  "s_add_u32 s98, s37, s53\n\t" \
  "s_add_u32 s82, s48, 0\n\t" \
  "s_lshr_b64 s[60:61], s[90:91], s82\n\t" \
  "s_and_b32 s60, s60, 1\n\t" \
  "s_lshl_b32 s60, s60, 13\n\t" \
  "s_add_u32 s82, s82, " CH_SG0 "\n\t" \
  "s_lshl_b32 s82, s82, 21\n\t" \
  "s_or_b32 s82, s82, s60\n\t" \
  "s_add_u32 s50, s82, s38\n\t" \
  "s_add_u32 s69, s82, s39\n\t" \
  "s_branch LG1_%=\n\t" \
  "RFAIL1_%=:\n\t" \
  "s_add_u32 s74, s74, 1\n\t" \
  "s_sleep " CH_RSL "\n\t" \
  "s_waitcnt vmcnt(0)\n\t" \
  "s_cmp_gt_u32 s74, s84\n\t" \
  "s_cbranch_scc1 RG1_%=\n\t" \
  "s_and_b32 s60, s74, 1023\n\t" \
  "s_cmp_lg_u32 s60, 0\n\t" \
  "s_cbranch_scc1 RRETRY1_%=\n\t" \
  "buffer_load_dword v46, off, s[28:31], s92 sc1\n\t" \
  "s_waitcnt vmcnt(0)\n\t" \
  "v_readfirstlane_b32 s60, v46\n\t" \
  "s_nop 0\n\t" \
  "s_cmp_eq_u32 s60, 0\n\t" \
  "s_cbranch_scc1 RRETRY1_%=\n\t" \
  "RG1_%=:\n\t" \
  "s_mov_b32 s84, 0\n\t" \
  "s_lshl_b32 s60, s36, 4\n\t" \
  "s_or_b32 s60, s60, s48\n\t" \
  "s_or_b32 s60, s60, 536870912\n\t" \
  "v_mov_b32 v76, s60\n\t" \
  "v_mov_b32 v77, 0\n\t" \
  "s_mov_b64 exec, 1\n\t" \
  "buffer_atomic_cmpswap v[76:77], off, s[28:31], s92 sc1\n\t" \
  "s_mov_b64 exec, -1\n\t" \
  "s_branch RDONE1_%=\n\t" \
  "LFAIL1_%=:\n\t" \
  "v_max_u32 v75, v0, v1\n\t" \
  "v_max3_u32 v75, v75, v2, v3\n\t" \
  "v_max3_u32 v75, v75, v4, v5\n\t" \
  "v_max3_u32 v75, v75, v6, v7\n\t" \
  "v_max3_u32 v75, v75, v8, v9\n\t" \
  "v_max3_u32 v75, v75, v10, v11\n\t" \
  "v_max3_u32 v75, v75, v12, v13\n\t" \
  "v_max3_u32 v75, v75, v14, v15\n\t" \
  "v_max3_u32 v75, v75, v16, v17\n\t" \
  "v_max3_u32 v75, v75, v18, v19\n\t" \
  "v_max3_u32 v75, v75, v20, v21\n\t" \
  "v_max3_u32 v75, v75, v22, v23\n\t" \
  "v_max3_u32 v75, v75, v24, v25\n\t" \
  "v_max3_u32 v75, v75, v26, v27\n\t" \
  "v_max3_u32 v75, v75, v28, v29\n\t" \
  "v_max3_u32 v75, v75, v30, v31\n\t" \
  "v_max3_u32 v75, v75, v32, v33\n\t" \
  "v_max3_u32 v75, v75, v34, v35\n\t" \
  "v_cmp_eq_u32 vcc, -1, v75\n\t" \
  "s_cbranch_vccz LGO1_%=\n\t" \
  "s_add_u32 s68, s68, 1\n\t" \
  "s_waitcnt lgkmcnt(0)\n\t" \
  "s_cmp_gt_u32 s68, s83\n\t" \
  "s_cbranch_scc1 LGV1_%=\n\t" \
  "s_barrier\n\t" \
  "s_waitcnt vmcnt(8)\n\t" \
  "s_branch LG1_%=\n\t" \
  "RWT1_%=f:\n\t" \
  "s_cmp_lt_i32 s73, -2\n\t" \
  "s_cbranch_scc0 RWT1_%=g\n\t" \
  "s_nop 4\n\t" \
  "s_branch RWT1_%=g\n\t" \
  "LGV1_%=:\n\t" \
  "s_mov_b32 s83, 0\n\t" \
  "s_lshl_b32 s60, s36, 4\n\t" \
  "s_or_b32 s60, s60, s48\n\t" \
  "s_or_b32 s60, s60, 805306368\n\t" \
  "v_mov_b32 v76, s60\n\t" \
  "v_mov_b32 v77, 0\n\t" \
  "s_mov_b64 exec, 1\n\t" \
  "buffer_atomic_cmpswap v[76:77], off, s[28:31], s92 sc1\n\t" \
  "s_mov_b64 exec, -1\n\t" \
  "s_branch LGO1_%=\n\t" \
  "LLAST1_%=:\n\t" \
  "s_waitcnt vmcnt(0) lgkmcnt(0)\n\t" \
  "s_barrier\n\t" \
  "s_branch LEND_%=\n\t" \
  ".p2align 6\n\t" \
  "LG2_%=:\n\t" \
  "s_barrier\n\t" \
  "buffer_load_dwordx4 v[52:55], v69, s[28:31], s50 offen sc1\n\t" \
  "buffer_load_dwordx4 v[56:59], v69, s[28:31], s50 offen offset:1024 sc1\n\t" \
  "s_add_u32 s64, s50, 18432\n\t" \
  "s_movk_i32 m0, 11264\n\t" \
  "v_lshlrev_b32 v75, 1, v69\n\t" \
  "buffer_load_dword v75, s[28:31], s64 offen sc1 lds\n\t" \
  "ds_read_b128 v[44:47], v70 offset:0\n\t" \
  "ds_read_b128 v[48:51], v70 offset:1024\n\t" \
  "ds_read_b128 v[60:63], v70 offset:2048\n\t" \
  "ds_read_b128 v[64:67], v70 offset:3072\n\t" \
  "v_mov_b64 v[36:37], 0\n\t" \
  "v_mov_b64 v[38:39], 0\n\t" \
  "v_mov_b64 v[40:41], 0\n\t" \
  "v_mov_b64 v[42:43], 0\n\t" \
  "v_lshrrev_b32 v71, 1, v69\n\t" \
  "v_lshrrev_b32 v72, 3, v69\n\t" \
  "v_and_b32 v72, 0x78, v72\n\t" \
  "v_and_b32 v74, 0x30, v69\n\t" \
  "v_lshl_add_u32 v72, v74, 3, v72\n\t" \
  "s_add_u32 s54, s53, s36\n\t" \
  "s_add_u32 s55, s54, 557056\n\t" \
  "s_add_u32 s54, s54, 131072\n\t" \
  "s_add_u32 s75, s54, 262144\n\t" \
  "s_add_u32 s80, s53, s26\n\t" \
  "s_barrier\n\t" \
  "ds_read_b128 v[0:3], v69 offset:0\n\t" \
  "ds_read_b128 v[4:7], v69 offset:1024\n\t" \
  "ds_read_b128 v[8:11], v69 offset:2048\n\t" \
  "ds_read_b128 v[12:15], v69 offset:3072\n\t" \
  "s_waitcnt lgkmcnt(3)\n\t" \
  "v_smfmac_f32_16x16x64_bf16 v[36:39], v[0:3], v[44:51], v68\n\t" \
  "ds_read_b128 v[44:47], v70 offset:4096\n\t" \
  "v_smfmac_f32_16x16x64_bf16 v[40:43], v[0:3], v[60:67], v68\n\t" \
  "ds_read_b128 v[48:51], v70 offset:5120\n\t" \
  "s_waitcnt lgkmcnt(4)\n\t" \
  "v_smfmac_f32_16x16x64_bf16 v[36:39], v[4:7], a[32:39], v68\n\t" \
  "s_barrier\n\t" \
  "ds_read_b128 v[16:19], v69 offset:4096\n\t" \
  "v_smfmac_f32_16x16x64_bf16 v[40:43], v[4:7], a[40:47], v68\n\t" \
  "ds_read_b128 v[20:23], v69 offset:5120\n\t" \
  "s_waitcnt lgkmcnt(5)\n\t" \
  "v_smfmac_f32_16x16x64_bf16 v[36:39], v[8:11], a[80:87], v68\n\t" \
  "ds_read_b128 v[24:27], v69 offset:6144\n\t" \
  "v_smfmac_f32_16x16x64_bf16 v[40:43], v[8:11], a[88:95], v68\n\t" \
  "ds_read_b128 v[28:31], v69 offset:7168\n\t" \
  "s_waitcnt lgkmcnt(6)\n\t" \
  "v_smfmac_f32_16x16x64_bf16 v[36:39], v[12:15], a[120:127], v68\n\t" \
  "ds_read_b128 v[32:35], v69 offset:8192\n\t" \
  "s_waitcnt lgkmcnt(5)\n\t" \
  "v_smfmac_f32_16x16x64_bf16 v[40:43], v[12:15], v[44:51], v68\n\t" \
  "ds_read_b128 v[60:63], v70 offset:6144\n\t" \
  "s_waitcnt lgkmcnt(5)\n\t" \
  "v_smfmac_f32_16x16x64_bf16 v[36:39], v[16:19], a[160:167], v68\n\t" \
  "ds_read_b128 v[64:67], v70 offset:7168\n\t" \
  "v_smfmac_f32_16x16x64_bf16 v[40:43], v[16:19], a[168:175], v68\n\t" \
  "s_waitcnt lgkmcnt(5)\n\t" \
  "v_mov_b64 v[44:45], 0\n\t" \
  "v_smfmac_f32_16x16x64_bf16 v[36:39], v[20:23], a[200:207], v68\n\t" \
  "s_waitcnt lgkmcnt(4)\n\t" \
  "v_mov_b64 v[46:47], 0\n\t" \
  "v_smfmac_f32_16x16x64_bf16 v[36:39], v[24:27], a[240:247], v68\n\t" \
  "s_waitcnt lgkmcnt(2)\n\t" \
  "v_mov_b64 v[48:49], 0\n\t" \
  "v_smfmac_f32_16x16x64_bf16 v[36:39], v[28:31], v[112:119], v68\n\t" \
  "v_mov_b64 v[50:51], 0\n\t" \
  "v_smfmac_f32_16x16x64_bf16 v[36:39], v[32:35], v[160:167], v68\n\t" \
  "s_waitcnt lgkmcnt(0)\n\t" \
  "v_smfmac_f32_16x16x64_bf16 v[40:43], v[20:23], v[60:67], v68\n\t" \
  "ds_read_b128 v[60:63], v70 offset:8192\n\t" \
  "v_smfmac_f32_16x16x64_bf16 v[40:43], v[24:27], a[248:255], v68\n\t" \
  "ds_read_b128 v[64:67], v70 offset:9216\n\t" \
  "v_add_f32 v74, v36, v38\n\t" \
  "v_smfmac_f32_16x16x64_bf16 v[40:43], v[28:31], v[120:127], v68\n\t" \
  "v_add_f32 v37, v37, v39\n\t" \
  "v_smfmac_f32_16x16x64_bf16 v[40:43], v[32:35], v[168:175], v68\n\t" \
  "v_cmp_u_f32 vcc, v74, v37\n\t" \
  "v_smfmac_f32_16x16x64_bf16 v[44:47], v[0:3], a[0:7], v68\n\t" \
  "s_cbranch_vccnz LFAIL2_%=\n\t" \
  "LGO2_%=:\n\t" \
  "v_cvt_pk_bf16_f32 v74, v74, v37\n\t" \
  "v_smfmac_f32_16x16x64_bf16 v[44:47], v[4:7], a[48:55], v68\n\t" \
  "s_waitcnt lgkmcnt(0)\n\t" \
  "v_add_f32 v75, v40, v42\n\t" \
  "v_smfmac_f32_16x16x64_bf16 v[44:47], v[8:11], v[60:67], v68\n\t" \
  "ds_read_b128 v[60:63], v70 offset:10240\n\t" \
  "v_add_f32 v41, v41, v43\n\t" \
  "v_cvt_pk_bf16_f32 v75, v75, v41\n\t" \
  "v_smfmac_f32_16x16x64_bf16 v[44:47], v[12:15], a[128:135], v68\n\t" \
  "ds_write2st64_b32 v71, v74, v75 offset0:40 offset1:42\n\t" \
  "v_smfmac_f32_16x16x64_bf16 v[44:47], v[16:19], a[176:183], v68\n\t" \
  "ds_read_b128 v[64:67], v70 offset:11264\n\t" \
  "v_smfmac_f32_16x16x64_bf16 v[44:47], v[20:23], a[208:215], v68\n\t" \
  "v_smfmac_f32_16x16x64_bf16 v[44:47], v[24:27], v[80:87], v68\n\t" \
  "v_smfmac_f32_16x16x64_bf16 v[44:47], v[28:31], v[128:135], v68\n\t" \
  "s_waitcnt lgkmcnt(0)\n\t" \
  "v_smfmac_f32_16x16x64_bf16 v[44:47], v[32:35], v[60:67], v68\n\t" \
  "s_barrier\n\t" \
  "ds_read_b64_tr_b16 v[76:77], v72 offset:10240\n\t" \
  "v_smfmac_f32_16x16x64_bf16 v[48:51], v[0:3], a[8:15], v68\n\t" \
  "ds_read_b64_tr_b16 v[78:79], v72 offset:10752\n\t" \
  "v_smfmac_f32_16x16x64_bf16 v[48:51], v[4:7], a[56:63], v68\n\t" \
  "buffer_load_dwordx4 v[60:63], v69, s[28:31], s64 offen sc1\n\t" \
  "v_add_f32 v74, v44, v46\n\t" \
  "v_smfmac_f32_16x16x64_bf16 v[48:51], v[8:11], a[96:103], v68\n\t" \
  "v_add_f32 v45, v45, v47\n\t" \
  "v_smfmac_f32_16x16x64_bf16 v[48:51], v[12:15], a[136:143], v68\n\t" \
  "buffer_load_dwordx4 v[64:67], v69, s[28:31], s64 offen offset:1024 sc1\n\t" \
  "s_waitcnt vmcnt(4)\n\t" \
  "s_waitcnt lgkmcnt(0)\n\t" \
  "v_cvt_pk_bf16_f32 v74, v74, v45\n\t" \
  "v_mfma_f32_16x16x32_bf16 v[36:39], v[52:55], v[76:79], 0\n\t" \
  "s_waitcnt vmcnt(3)\n\t" \
  "v_mfma_f32_16x16x32_bf16 v[40:43], v[56:59], v[76:79], 0\n\t" \
  "v_mov_b64 v[52:53], 0\n\t" \
  "v_mov_b64 v[54:55], 0\n\t" \
  "v_smfmac_f32_16x16x64_bf16 v[48:51], v[16:19], a[184:191], v68\n\t" \
  "v_mov_b64 v[56:57], 0\n\t" \
  "v_mov_b64 v[58:59], 0\n\t" \
  "v_smfmac_f32_16x16x64_bf16 v[48:51], v[20:23], a[216:223], v68\n\t" \
  "ds_write_b32 v71, v74 offset:1024\n\t" \
  "v_add_f32_dpp v36, v40, v36 quad_perm:[2,3,0,1] row_mask:0xf bank_mask:0xf\n\t" \
  "v_smfmac_f32_16x16x64_bf16 v[48:51], v[24:27], v[88:95], v68\n\t" \
  "v_add_f32_dpp v37, v41, v37 quad_perm:[2,3,0,1] row_mask:0xf bank_mask:0xf\n\t" \
  "v_smfmac_f32_16x16x64_bf16 v[48:51], v[28:31], v[136:143], v68\n\t" \
  "v_add_f32_dpp v38, v42, v38 quad_perm:[2,3,0,1] row_mask:0xf bank_mask:0xf\n\t" \
  "v_smfmac_f32_16x16x64_bf16 v[48:51], v[32:35], v[176:183], v68\n\t" \
  "v_add_f32_dpp v39, v43, v39 quad_perm:[2,3,0,1] row_mask:0xf bank_mask:0xf\n\t" \
  "v_smfmac_f32_16x16x64_bf16 v[52:55], v[0:3], a[16:23], v68\n\t" \
  "s_mov_b64 exec, s[58:59]\n\t" \
  "ds_write_b128 v69, v[36:39] offset:3072\n\t" \
  "s_mov_b64 exec, -1\n\t" \
  "v_smfmac_f32_16x16x64_bf16 v[52:55], v[4:7], a[64:71], v68\n\t" \
  "v_add_f32 v75, v48, v50\n\t" \
  "v_add_f32 v49, v49, v51\n\t" \
  "v_cvt_pk_bf16_f32 v75, v75, v49\n\t" \
  "v_smfmac_f32_16x16x64_bf16 v[56:59], v[0:3], a[24:31], v68\n\t" \
  "ds_write_b32 v71, v75 offset:1536\n\t" \
  "v_smfmac_f32_16x16x64_bf16 v[56:59], v[4:7], a[72:79], v68\n\t" \
  "ds_read_b64_tr_b16 v[0:1], v72 offset:9216\n\t" \
  "ds_read_b64_tr_b16 v[2:3], v72 offset:9728\n\t" \
  "v_smfmac_f32_16x16x64_bf16 v[52:55], v[8:11], a[104:111], v68\n\t" \
  "ds_read_b128 v[36:39], v70 offset:12288\n\t" \
  "v_smfmac_f32_16x16x64_bf16 v[56:59], v[8:11], a[112:119], v68\n\t" \
  "ds_read_b128 v[40:43], v70 offset:13312\n\t" \
  "v_smfmac_f32_16x16x64_bf16 v[52:55], v[12:15], a[144:151], v68\n\t" \
  "s_waitcnt lgkmcnt(4)\n\t" \
  "s_barrier\n\t" \
  "ds_read_b64_tr_b16 v[44:45], v72 offset:1024\n\t" \
  "ds_read_b64_tr_b16 v[46:47], v72 offset:1536\n\t" \
  "v_smfmac_f32_16x16x64_bf16 v[56:59], v[12:15], a[152:159], v68\n\t" \
  "ds_read_b64_tr_b16 v[48:49], v72 offset:0\n\t" \
  "ds_read_b64_tr_b16 v[50:51], v72 offset:512\n\t" \
  "v_smfmac_f32_16x16x64_bf16 v[52:55], v[16:19], a[192:199], v68\n\t" \
  "ds_read_b128 v[4:7], v69 offset:2048\n\t" \
  "s_waitcnt lgkmcnt(5)\n\t" \
  "v_smfmac_f32_16x16x64_bf16 v[56:59], v[16:19], v[36:43], v68\n\t" \
  "ds_read_b128 v[8:11], v69 offset:3072\n\t" \
  "s_waitcnt lgkmcnt(4)\n\t" \
  "v_mfma_f32_16x16x32_bf16 v[12:15], v[44:47], v[76:79], 0\n\t" \
  "ds_read_b128 v[36:39], v70 offset:14336\n\t" \
  "s_waitcnt lgkmcnt(3)\n\t" \
  "v_mfma_f32_16x16x32_bf16 v[12:15], v[48:51], v[0:3], v[12:15]\n\t" \
  "ds_read_b128 v[40:43], v70 offset:15360\n\t" \
  "v_smfmac_f32_16x16x64_bf16 v[52:55], v[20:23], a[224:231], v68\n\t" \
  "buffer_store_dwordx4 v[44:47], v69, s[28:31], s69 offen offset:1024 sc1\n\t" \
  "s_waitcnt lgkmcnt(2)\n\t" \
  "v_add_f32 v4, v4, v8\n\t" \
  "v_add_f32 v5, v5, v9\n\t" \
  "v_add_f32 v6, v6, v10\n\t" \
  "v_cndmask_b32 v78, v13, v14, s[56:57]\n\t" \
  "v_cndmask_b32 v76, v12, v15, s[56:57]\n\t" \
  "v_add_f32 v7, v7, v11\n\t" \
  "v_max3_f32 v77, v4, v5, v6\n\t" \
  "v_add_f32_dpp v76, v78, v76 quad_perm:[2,3,0,1] row_mask:0xf bank_mask:0xf\n\t" \
  "v_max3_f32 v77, v77, v7, v76\n\t" \
  "v_smfmac_f32_16x16x64_bf16 v[56:59], v[20:23], a[232:239], v68\n\t" \
  "ds_read_b128 v[44:47], v70 offset:16384\n\t" \
  "v_max_f32_dpp v77, v77, v77 quad_perm:[2,3,0,1] row_mask:0xf bank_mask:0xf\n\t" \
  "v_mov_b32 v78, v77\n\t" \
  "v_smfmac_f32_16x16x64_bf16 v[52:55], v[24:27], v[96:103], v68\n\t" \
  "ds_read_b128 v[48:51], v70 offset:17408\n\t" \
  "v_permlane16_swap_b32 v77, v78\n\t" \
  "v_max_f32 v77, v77, v78\n\t" \
  "v_mov_b32 v78, v77\n\t" \
  "v_smfmac_f32_16x16x64_bf16 v[52:55], v[28:31], v[144:151], v68\n\t" \
  "ds_read_b128 v[16:19], v70 offset:18432\n\t" \
  "v_permlane32_swap_b32 v77, v78\n\t" \
  "v_max_f32 v77, v77, v78\n\t" \
  "v_smfmac_f32_16x16x64_bf16 v[52:55], v[32:35], v[184:191], v68\n\t" \
  "ds_read_b128 v[20:23], v70 offset:19456\n\t" \
  "v_mul_f32 v77, 0xbe0293ee, v77\n\t" \
  "v_smfmac_f32_16x16x64_bf16 v[56:59], v[24:27], v[104:111], v68\n\t" \
  "v_fmamk_f32 v4, v4, 0x3e0293ee, v77\n\t" \
  "v_smfmac_f32_16x16x64_bf16 v[56:59], v[28:31], v[152:159], v68\n\t" \
  "ds_read_b128 v[24:27], v70 offset:22528\n\t" \
  "s_waitcnt lgkmcnt(4)\n\t" \
  "v_fmamk_f32 v5, v5, 0x3e0293ee, v77\n\t" \
  "v_smfmac_f32_16x16x64_bf16 v[56:59], v[32:35], v[36:43], v68\n\t" \
  "ds_read_b128 v[28:31], v70 offset:23552\n\t" \
  "v_fmamk_f32 v6, v6, 0x3e0293ee, v77\n\t" \
  "v_fmamk_f32 v7, v7, 0x3e0293ee, v77\n\t" \
  "v_fmamk_f32 v76, v76, 0x3e0293ee, v77\n\t" \
  "v_exp_f32 v4, v4\n\t" \
  "v_exp_f32 v5, v5\n\t" \
  "v_exp_f32 v6, v6\n\t" \
  "v_exp_f32 v7, v7\n\t" \
  "v_cvt_pk_bf16_f32 v4, v4, v5\n\t" \
  "v_exp_f32 v76, v76\n\t" \
  "v_cvt_pk_bf16_f32 v5, v6, v7\n\t" \
  "v_mov_b32_dpp v12, v4 quad_perm:[0,1,0,1] row_mask:0xf bank_mask:0xf\n\t" \
  "v_mov_b32_dpp v14, v4 quad_perm:[2,3,2,3] row_mask:0xf bank_mask:0xf\n\t" \
  "v_mov_b32_dpp v13, v5 quad_perm:[0,1,0,1] row_mask:0xf bank_mask:0xf\n\t" \
  "v_mov_b32_dpp v15, v5 quad_perm:[2,3,2,3] row_mask:0xf bank_mask:0xf\n\t" \
  "v_add_f32 v52, v52, v54\n\t" \
  "v_add_f32 v53, v53, v55\n\t" \
  "s_waitcnt vmcnt(2)\n\t" \
  "v_mfma_f32_16x16x32_bf16 v[0:3], v[60:63], v[12:15], 0\n\t" \
  "ds_read_b128 v[36:39], v70 offset:20480\n\t" \
  "v_add_f32 v56, v56, v58\n\t" \
  "v_add_f32 v57, v57, v59\n\t" \
  "v_cvt_pk_bf16_f32 v52, v52, v53\n\t" \
  "v_cvt_pk_bf16_f32 v53, v56, v57\n\t" \
  "s_waitcnt vmcnt(1)\n\t" \
  "v_mfma_f32_16x16x32_bf16 v[4:7], v[64:67], v[12:15], 0\n\t" \
  "ds_read_b128 v[40:43], v70 offset:21504\n\t" \
  "v_mov_b64 v[56:57], s[88:89]\n\t" \
  "v_mov_b64 v[58:59], s[88:89]\n\t" \
  "v_mov_b32_dpp v78, v76 quad_perm:[0,1,0,1] row_mask:0xf bank_mask:0xf\n\t" \
  "v_mov_b32_dpp v79, v76 quad_perm:[2,3,2,3] row_mask:0xf bank_mask:0xf\n\t" \
  "v_mfma_f32_16x16x32_bf16 v[32:35], v[56:59], v[12:15], 0\n\t" \
  "ds_read_b128 v[60:63], v70 offset:24576\n\t" \
  "v_cvt_pk_bf16_f32 v11, v78, v79\n\t" \
  "v_cvt_pk_bf16_f32 v8, v78, v79\n\t" \
  "v_mov_b64 v[54:55], 0\n\t" \
  "v_mov_b32 v9, 0\n\t" \
  "v_mov_b32 v10, 0\n\t" \
  "v_mov_b64 v[14:15], 0\n\t" \
  "s_nop 0\n\t" \
  "v_mfma_f32_16x16x32_bf16 v[32:35], v[54:57], v[8:11], v[32:35]\n\t" \
  "buffer_store_dwordx2 v[52:53], v71, s[28:31], s69 offen offset:3072 sc1\n\t" \
  "s_cmp_eq_u32 s48, 63\n\t" \
  "v_mfma_f32_16x16x32_bf16 v[0:3], v[52:55], v[8:11], v[0:3]\n\t" \
  "ds_read_b128 v[64:67], v70 offset:25600\n\t" \
  "v_mov_b64 v[56:57], 0\n\t" \
  "v_mov_b64 v[58:59], 0\n\t" \
  "v_mfma_f32_16x16x32_bf16 v[4:7], v[52:55], v[10:13], v[4:7]\n\t" \
  "s_cbranch_scc1 LLAST2_%=\n\t" \
  "v_rcp_f32 v74, v32\n\t" \
  "v_mov_b64 v[12:13], 0\n\t" \
  "v_mov_b32 v8, 0\n\t" \
  "v_mul_f32 v0, v0, v74\n\t" \
  "v_mul_f32 v1, v1, v74\n\t" \
  "v_mul_f32 v2, v2, v74\n\t" \
  "v_mul_f32 v3, v3, v74\n\t" \
  "v_cvt_pk_bf16_f32 v76, v0, v1\n\t" \
  "v_cvt_pk_bf16_f32 v77, v2, v3\n\t" \
  "v_pk_mul_f32 v[4:5], v[4:5], v[74:75] op_sel_hi:[1,0]\n\t" \
  "v_pk_mul_f32 v[6:7], v[6:7], v[74:75] op_sel_hi:[1,0]\n\t" \
  "v_cvt_pk_bf16_f32 v78, v4, v5\n\t" \
  "v_cvt_pk_bf16_f32 v79, v6, v7\n\t" \
  "ds_write_b128 v69, v[76:79] offset:6144\n\t" \
  "s_waitcnt lgkmcnt(2)\n\t" \
  "v_smfmac_f32_16x16x64_bf16 v[12:15], v[76:79], v[44:51], v68\n\t" \
  "ds_read_b128 v[44:47], v70 offset:26624\n\t" \
  "v_mov_b32 v11, 0\n\t" \
  "v_smfmac_f32_16x16x64_bf16 v[56:59], v[76:79], v[16:23], v68\n\t" \
  "ds_read_b128 v[48:51], v70 offset:27648\n\t" \
  "v_mov_b64 v[52:53], 0\n\t" \
  "v_smfmac_f32_16x16x64_bf16 v[8:11], v[76:79], v[36:43], v68\n\t" \
  "ds_read_b128 v[16:19], v70 offset:28672\n\t" \
  "v_smfmac_f32_16x16x64_bf16 v[52:55], v[76:79], v[24:31], v68\n\t" \
  "s_waitcnt lgkmcnt(3)\n\t" \
  "s_barrier\n\t" \
  "ds_read_b128 v[0:3], v69 offset:7168\n\t" \
  "ds_read_b128 v[4:7], v69 offset:4096\n\t" \
  "v_lshrrev_b32 v71, 7, v69\n\t" \
  "v_and_b32 v74, 0x70, v69\n\t" \
  "v_lshl_add_u32 v71, v71, 14, v74\n\t" \
  "v_bfe_u32 v72, v69, 2, 7\n\t" \
  "v_and_b32 v74, 0x200, v69\n\t" \
  "s_waitcnt lgkmcnt(1)\n\t" \
  "v_smfmac_f32_16x16x64_bf16 v[8:11], v[0:3], v[60:67], v68\n\t" \
  "ds_read_b128 v[20:23], v70 offset:29696\n\t" \
  "v_mov_b64 v[76:77], 0\n\t" \
  "v_smfmac_f32_16x16x64_bf16 v[12:15], v[0:3], v[200:207], v68\n\t" \
  "buffer_load_dword v75, off, s[28:31], s80 sc1\n\t" \
  "ds_read_b128 v[32:35], v69 offset:5120\n\t" \
  "v_mov_b64 v[78:79], 0\n\t" \
  "v_smfmac_f32_16x16x64_bf16 v[52:55], v[0:3], v[44:51], v68\n\t" \
  "ds_read_b128 v[24:27], v70 offset:30720\n\t" \
  "v_lshl_add_u32 v72, v74, 3, v72\n\t" \
  "v_smfmac_f32_16x16x64_bf16 v[56:59], v[0:3], v[232:239], v68\n\t" \
  "ds_read_b128 v[28:31], v70 offset:31744\n\t" \
  "s_waitcnt lgkmcnt(4)\n\t" \
  "v_smfmac_f32_16x16x64_bf16 v[12:15], v[4:7], v[208:215], v68\n\t" \
  "ds_read_b128 v[60:63], v70 offset:32768\n\t" \
  "s_waitcnt lgkmcnt(4)\n\t" \
  "v_smfmac_f32_16x16x64_bf16 v[8:11], v[4:7], v[16:23], v68\n\t" \
  "ds_read_b128 v[64:67], v70 offset:33792\n\t" \
  "s_waitcnt lgkmcnt(4)\n\t" \
  "v_smfmac_f32_16x16x64_bf16 v[8:11], v[32:35], v[192:199], v68\n\t" \
  "ds_read_b128 v[36:39], v73 offset:4096\n\t" \
  "v_smfmac_f32_16x16x64_bf16 v[12:15], v[32:35], v[216:223], v68\n\t" \
  "ds_read_b128 v[40:43], v73 offset:6144\n\t" \
  "s_waitcnt lgkmcnt(4)\n\t" \
  "v_smfmac_f32_16x16x64_bf16 v[52:55], v[4:7], v[24:31], v68\n\t" \
  "ds_read_b128 v[44:47], v70 offset:34816\n\t" \
  "v_cvt_pk_bf16_f32 v8, v8, v9\n\t" \
  "v_smfmac_f32_16x16x64_bf16 v[52:55], v[32:35], v[224:231], v68\n\t" \
  "ds_read_b128 v[48:51], v70 offset:35840\n\t" \
  "v_cvt_pk_bf16_f32 v9, v10, v11\n\t" \
  "v_smfmac_f32_16x16x64_bf16 v[56:59], v[4:7], v[240:247], v68\n\t" \
  "s_waitcnt lgkmcnt(4)\n\t" \
  "v_cvt_pk_bf16_f32 v10, v12, v13\n\t" \
  "v_smfmac_f32_16x16x64_bf16 v[56:59], v[32:35], v[60:67], v68\n\t" \
  "s_waitcnt lgkmcnt(3)\n\t" \
  "v_cvt_pk_bf16_f32 v11, v14, v15\n\t" \
  "v_smfmac_f32_16x16x64_bf16 v[76:79], v[36:39], v[248:255], v68\n\t" \
  "buffer_store_dwordx4 v[8:11], v71, s[28:31], s54 offen sc1\n\t" \
  "s_waitcnt lgkmcnt(0)\n\t" \
  "v_cvt_pk_bf16_f32 v52, v52, v53\n\t" \
  "v_smfmac_f32_16x16x64_bf16 v[76:79], v[40:43], v[44:51], v68\n\t" \
  "v_cvt_pk_bf16_f32 v53, v54, v55\n\t" \
  "v_cvt_pk_bf16_f32 v54, v56, v57\n\t" \
  "v_cvt_pk_bf16_f32 v55, v58, v59\n\t" \
  "buffer_store_dwordx4 v[52:55], v71, s[28:31], s75 offen sc1\n\t" \
  "s_nop 2\n\t" \
  "v_add_f32 v76, v76, v78\n\t" \
  "v_add_f32 v77, v77, v79\n\t" \
  "v_cvt_pk_bf16_f32 v76, v76, v77\n\t" \
  "buffer_store_dword v76, v72, s[28:31], s55 offen sc1\n\t" \
  "s_memrealtime s[70:71]\n\t" \
  "s_waitcnt vmcnt(3)\n\t" \
  "v_readfirstlane_b32 s65, v75\n\t" \
  "s_barrier\n\t" \
  "s_waitcnt lgkmcnt(0)\n\t" \
  "s_not_b32 s65, s65\n\t" \
  "s_lshl_b32 s61, s70, 8\n\t" \
  "s_andn2_b32 s64, s65, 0xff\n\t" \
  "s_sub_u32 s64, s64, s61\n\t" \
  "s_ashr_i32 s64, s64, 8\n\t" \
  "s_cmp_eq_u32 s65, 0\n\t" \
  "s_cselect_b32 s64, " CH_DLF ", s64\n\t" \
  "s_min_i32 s64, s64, 1000\n\t" \
  "s_max_i32 s64, s64, " CH_DLL1 "\n\t" \
  "s_add_u32 s64, s64, s70\n\t" \
  "s_mov_b32 s74, 0\n\t" \
  "s_movk_i32 s60, 8192\n\t" \
  "RWT2_%=:\n\t" \
  "s_memrealtime s[72:73]\n\t" \
  "s_sub_u32 s60, s60, 1\n\t" \
  "s_cselect_b32 s61, 0, -1\n\t" \
  "s_waitcnt lgkmcnt(0)\n\t" \
  "s_sub_i32 s73, s72, s64\n\t" \
  "s_and_b32 s73, s73, s61\n\t" \
  "s_cmp_lt_i32 s73, " CH_FIN "\n\t" \
  "s_cbranch_scc1 RWT2_%=\n\t" \
  "s_cmp_lt_i32 s73, 0\n\t" \
  "s_cbranch_scc1 RWT2_%=f\n\t" \
  "RWT2_%=g:\n\t" \
  "RRETRY2_%=:\n\t" \
  "buffer_load_dwordx4 v[0:3], v69, s[28:31], s97 offen offset:0 sc1\n\t" \
  "buffer_load_dwordx4 v[4:7], v69, s[28:31], s97 offen offset:1024 sc1\n\t" \
  "buffer_load_dwordx4 v[8:11], v69, s[28:31], s97 offen offset:2048 sc1\n\t" \
  "buffer_load_dwordx4 v[12:15], v69, s[28:31], s97 offen offset:3072 sc1\n\t" \
  "v_lshrrev_b32 v20, 2, v69\n\t" \
  "v_and_b32 v20, 16, v20\n\t" \
  "v_mov_b32 v21, 0x3f80\n\t" \
  "v_lshlrev_b32 v21, v20, v21\n\t" \
  "v_bfe_u32 v20, v69, 4, 2\n\t" \
  "v_cmp_eq_u32 vcc, 0, v20\n\t" \
  "s_nop 1\n\t" \
  "v_cndmask_b32 v32, 0, v21, vcc\n\t" \
  "v_cmp_eq_u32 vcc, 1, v20\n\t" \
  "s_nop 1\n\t" \
  "v_cndmask_b32 v33, 0, v21, vcc\n\t" \
  "v_cmp_eq_u32 vcc, 2, v20\n\t" \
  "s_nop 1\n\t" \
  "v_cndmask_b32 v34, 0, v21, vcc\n\t" \
  "v_cmp_eq_u32 vcc, 3, v20\n\t" \
  "s_nop 1\n\t" \
  "v_cndmask_b32 v35, 0, v21, vcc\n\t" \
  "v_bfe_u32 v38, v69, 7, 1\n\t" \
  "v_bfe_u32 v20, v69, 9, 1\n\t" \
  "v_lshl_add_u32 v38, v20, 2, v38\n\t" \
  "v_lshlrev_b32 v38, 12, v38\n\t" \
  "v_and_b32 v20, 0x60, v69\n\t" \
  "v_add_u32 v38, v38, v20\n\t" \
  "v_bfe_u32 v20, v69, 8, 1\n\t" \
  "v_lshl_add_u32 v38, v20, 4, v38\n\t" \
  "v_bfe_u32 v20, v69, 4, 1\n\t" \
  "v_lshl_add_u32 v38, v20, 3, v38\n\t" \
  "v_mov_b32 v44, 0\n\t" \
  "s_waitcnt vmcnt(3)\n\t" \
  "v_mfma_f32_16x16x32_bf16 v[40:43], v[32:35], v[0:3], 0\n\t" \
  "v_max3_u32 v44, v44, v0, v1\n\t" \
  "v_max3_u32 v44, v44, v2, v3\n\t" \
  "s_waitcnt vmcnt(2)\n\t" \
  "v_mfma_f32_16x16x32_bf16 v[40:43], v[32:35], v[4:7], v[40:43]\n\t" \
  "v_max3_u32 v44, v44, v4, v5\n\t" \
  "v_max3_u32 v44, v44, v6, v7\n\t" \
  "s_waitcnt vmcnt(1)\n\t" \
  "v_mfma_f32_16x16x32_bf16 v[40:43], v[32:35], v[8:11], v[40:43]\n\t" \
  "v_max3_u32 v44, v44, v8, v9\n\t" \
  "v_max3_u32 v44, v44, v10, v11\n\t" \
  "s_waitcnt vmcnt(0)\n\t" \
  "v_mfma_f32_16x16x32_bf16 v[40:43], v[32:35], v[12:15], v[40:43]\n\t" \
  "v_max3_u32 v44, v44, v12, v13\n\t" \
  "v_max3_u32 v44, v44, v14, v15\n\t" \
  "v_cmp_eq_u32 vcc, -1, v44\n\t" \
  "s_cbranch_vccnz RFAIL2_%=\n\t" \
  "s_nop 3\n\t" \
  "v_add_f32_dpp v40, v40, v40 row_ror:8 row_mask:0xf bank_mask:0xf\n\t" \
  "v_add_f32_dpp v41, v41, v41 row_ror:8 row_mask:0xf bank_mask:0xf\n\t" \
  "v_add_f32_dpp v42, v42, v42 row_ror:8 row_mask:0xf bank_mask:0xf\n\t" \
  "v_add_f32_dpp v43, v43, v43 row_ror:8 row_mask:0xf bank_mask:0xf\n\t" \
  "v_cvt_pk_bf16_f32 v40, v40, v41\n\t" \
  "v_cvt_pk_bf16_f32 v41, v42, v43\n\t" \
  "buffer_store_dwordx2 v[40:41], v38, s[28:31], s98 offen sc1\n\t" \
  "RDONE2_%=:\n\t" \
  "s_barrier\n\t" \
  "s_add_u32 s48, s48, 1\n\t" \
  "s_mov_b32 s49, s53\n\t" \
  "s_add_u32 s53, s53, " CH_SLOT "\n\t" \
  "s_add_u32 s60, s53, " CH_SKIP "\n\t" \
  "s_lshr_b32 s61, s96, 1\n\t" \
  "s_add_u32 s86, s86, 1\n\t" \
  "s_cmp_eq_u32 s86, 3\n\t" \
  "s_cselect_b32 s53, s60, s53\n\t" \
  "s_cselect_b32 s96, s61, s96\n\t" \
  "s_cselect_b32 s86, 0, s86\n\t" \
  "s_andn2_b32 s53, s53, 0x2000\n\t" \
  "s_and_b32 s60, s96, 1\n\t" \
  "s_lshl_b32 s60, s60, 13\n\t" \
  "s_or_b32 s53, s53, s60\n\t" \
  "s_mov_b32 s68, 0\n\t" \
  "s_add_u32 s97, s41, s53\n\t" \
  "s_add_u32 s98, s37, s53\n\t" \
  "s_add_u32 s82, s48, 0\n\t" \
  "s_lshr_b64 s[60:61], s[90:91], s82\n\t" \
  "s_and_b32 s60, s60, 1\n\t" \
  "s_lshl_b32 s60, s60, 13\n\t" \
  "s_add_u32 s82, s82, " CH_SG0 "\n\t" \
  "s_lshl_b32 s82, s82, 21\n\t" \
  "s_or_b32 s82, s82, s60\n\t" \
  "s_add_u32 s50, s82, s38\n\t" \
  "s_add_u32 s69, s82, s39\n\t" \
  "s_branch LG2_%=\n\t" \
  "RFAIL2_%=:\n\t" \
  "s_add_u32 s74, s74, 1\n\t" \
  "s_sleep " CH_RSL "\n\t" \
  "s_waitcnt vmcnt(0)\n\t" \
  "s_cmp_gt_u32 s74, s84\n\t" \
  "s_cbranch_scc1 RG2_%=\n\t" \
  "s_and_b32 s60, s74, 1023\n\t" \
  "s_cmp_lg_u32 s60, 0\n\t" \
  "s_cbranch_scc1 RRETRY2_%=\n\t" \
  "buffer_load_dword v46, off, s[28:31], s92 sc1\n\t" \
  "s_waitcnt vmcnt(0)\n\t" \
  "v_readfirstlane_b32 s60, v46\n\t" \
  "s_nop 0\n\t" \
  "s_cmp_eq_u32 s60, 0\n\t" \
  "s_cbranch_scc1 RRETRY2_%=\n\t" \
  "RG2_%=:\n\t" \
  "s_mov_b32 s84, 0\n\t" \
  "s_lshl_b32 s60, s36, 4\n\t" \
  "s_or_b32 s60, s60, s48\n\t" \
  "s_or_b32 s60, s60, 536870912\n\t" \
  "v_mov_b32 v76, s60\n\t" \
  "v_mov_b32 v77, 0\n\t" \
  "s_mov_b64 exec, 1\n\t" \
  "buffer_atomic_cmpswap v[76:77], off, s[28:31], s92 sc1\n\t" \
  "s_mov_b64 exec, -1\n\t" \
  "s_branch RDONE2_%=\n\t" \
  "LFAIL2_%=:\n\t" \
  "v_max_u32 v75, v0, v1\n\t" \
  "v_max3_u32 v75, v75, v2, v3\n\t" \
  "v_max3_u32 v75, v75, v4, v5\n\t" \
  "v_max3_u32 v75, v75, v6, v7\n\t" \
  "v_max3_u32 v75, v75, v8, v9\n\t" \
  "v_max3_u32 v75, v75, v10, v11\n\t" \
  "v_max3_u32 v75, v75, v12, v13\n\t" \
  "v_max3_u32 v75, v75, v14, v15\n\t" \
  "v_max3_u32 v75, v75, v16, v17\n\t" \
  "v_max3_u32 v75, v75, v18, v19\n\t" \
  "v_max3_u32 v75, v75, v20, v21\n\t" \
  "v_max3_u32 v75, v75, v22, v23\n\t" \
  "v_max3_u32 v75, v75, v24, v25\n\t" \
  "v_max3_u32 v75, v75, v26, v27\n\t" \
  "v_max3_u32 v75, v75, v28, v29\n\t" \
  "v_max3_u32 v75, v75, v30, v31\n\t" \
  "v_max3_u32 v75, v75, v32, v33\n\t" \
  "v_max3_u32 v75, v75, v34, v35\n\t" \
  "v_cmp_eq_u32 vcc, -1, v75\n\t" \
  "s_cbranch_vccz LGO2_%=\n\t" \
  "s_add_u32 s68, s68, 1\n\t" \
  "s_waitcnt lgkmcnt(0)\n\t" \
  "s_cmp_gt_u32 s68, s83\n\t" \
  "s_cbranch_scc1 LGV2_%=\n\t" \
  "s_barrier\n\t" \
  "s_waitcnt vmcnt(8)\n\t" \
  "s_branch LG2_%=\n\t" \
  "RWT2_%=f:\n\t" \
  "s_cmp_lt_i32 s73, -2\n\t" \
  "s_cbranch_scc0 RWT2_%=g\n\t" \
  "s_nop 4\n\t" \
  "s_branch RWT2_%=g\n\t" \
  "LGV2_%=:\n\t" \
  "s_mov_b32 s83, 0\n\t" \
  "s_lshl_b32 s60, s36, 4\n\t" \
  "s_or_b32 s60, s60, s48\n\t" \
  "s_or_b32 s60, s60, 805306368\n\t" \
  "v_mov_b32 v76, s60\n\t" \
  "v_mov_b32 v77, 0\n\t" \
  "s_mov_b64 exec, 1\n\t" \
  "buffer_atomic_cmpswap v[76:77], off, s[28:31], s92 sc1\n\t" \
  "s_mov_b64 exec, -1\n\t" \
  "s_branch LGO2_%=\n\t" \
  "LLAST2_%=:\n\t" \
  "s_waitcnt vmcnt(0) lgkmcnt(0)\n\t" \
  "s_barrier\n\t" \
  "s_branch LEND_%=\n\t" \
  ".p2align 6\n\t" \
  "LG3_%=:\n\t" \
  "s_barrier\n\t" \
  "buffer_load_dwordx4 v[52:55], v69, s[28:31], s50 offen sc1\n\t" \
  "buffer_load_dwordx4 v[56:59], v69, s[28:31], s50 offen offset:1024 sc1\n\t" \
  "s_add_u32 s64, s50, 16384\n\t" \
  "s_movk_i32 m0, 11264\n\t" \
  "v_lshlrev_b32 v75, 1, v69\n\t" \
  "buffer_load_dword v75, s[28:31], s64 offen sc1 lds\n\t" \
  "s_add_u32 s47, s81, " CH_DL3 "\n\t" \
  "s_lshl_b32 s47, s47, 8\n\t" \
  "s_or_b32 s47, s47, " CH_B0 "\n\t" \
  "s_not_b32 s47, s47\n\t" \
  "s_add_u32 s71, s53, s46\n\t" \
  "ds_read_b128 v[44:47], v70 offset:0\n\t" \
  "ds_read_b128 v[48:51], v70 offset:1024\n\t" \
  "ds_read_b128 v[60:63], v70 offset:2048\n\t" \
  "ds_read_b128 v[64:67], v70 offset:3072\n\t" \
  "v_mov_b64 v[36:37], 0\n\t" \
  "v_mov_b64 v[38:39], 0\n\t" \
  "v_mov_b64 v[40:41], 0\n\t" \
  "v_mov_b64 v[42:43], 0\n\t" \
  "v_lshrrev_b32 v71, 1, v69\n\t" \
  "v_add_u32 v71, 4, v71\n\t" \
  "v_lshrrev_b32 v72, 3, v69\n\t" \
  "v_and_b32 v72, 0x78, v72\n\t" \
  "v_and_b32 v74, 0x30, v69\n\t" \
  "v_lshl_add_u32 v72, v74, 3, v72\n\t" \
  "s_add_u32 s54, s53, s36\n\t" \
  "s_add_u32 s55, s54, 573440\n\t" \
  "s_add_u32 s54, s54, 135168\n\t" \
  "s_add_u32 s75, s54, 262144\n\t" \
  "s_add_u32 s80, s53, s26\n\t" \
  "s_or_b32 s87, s50, 0x1fff80\n\t" \
  "s_add_u32 s87, s87, 4194304\n\t" \
  "s_barrier\n\t" \
  "ds_read_b128 v[0:3], v69 offset:0\n\t" \
  "ds_read_b128 v[4:7], v69 offset:1024\n\t" \
  "ds_read_b128 v[8:11], v69 offset:2048\n\t" \
  "ds_read_b128 v[12:15], v69 offset:3072\n\t" \
  "s_waitcnt lgkmcnt(3)\n\t" \
  "v_smfmac_f32_16x16x64_bf16 v[36:39], v[0:3], v[44:51], v68\n\t" \
  "ds_read_b128 v[44:47], v70 offset:4096\n\t" \
  "v_smfmac_f32_16x16x64_bf16 v[40:43], v[0:3], v[60:67], v68\n\t" \
  "ds_read_b128 v[48:51], v70 offset:5120\n\t" \
  "s_waitcnt lgkmcnt(4)\n\t" \
  "v_smfmac_f32_16x16x64_bf16 v[36:39], v[4:7], a[32:39], v68\n\t" \
  "s_barrier\n\t" \
  "ds_read_b128 v[16:19], v69 offset:4096\n\t" \
  "v_smfmac_f32_16x16x64_bf16 v[40:43], v[4:7], a[40:47], v68\n\t" \
  "ds_read_b128 v[20:23], v69 offset:5120\n\t" \
  "s_waitcnt lgkmcnt(5)\n\t" \
  "v_smfmac_f32_16x16x64_bf16 v[36:39], v[8:11], a[80:87], v68\n\t" \
  "ds_read_b128 v[24:27], v69 offset:6144\n\t" \
  "v_smfmac_f32_16x16x64_bf16 v[40:43], v[8:11], a[88:95], v68\n\t" \
  "ds_read_b128 v[28:31], v69 offset:7168\n\t" \
  "s_waitcnt lgkmcnt(6)\n\t" \
  "v_smfmac_f32_16x16x64_bf16 v[36:39], v[12:15], a[120:127], v68\n\t" \
  "ds_read_b128 v[32:35], v69 offset:8192\n\t" \
  "s_waitcnt lgkmcnt(5)\n\t" \
  "v_smfmac_f32_16x16x64_bf16 v[40:43], v[12:15], v[44:51], v68\n\t" \
  "ds_read_b128 v[60:63], v70 offset:6144\n\t" \
  "s_waitcnt lgkmcnt(5)\n\t" \
  "v_smfmac_f32_16x16x64_bf16 v[36:39], v[16:19], a[160:167], v68\n\t" \
  "ds_read_b128 v[64:67], v70 offset:7168\n\t" \
  "v_smfmac_f32_16x16x64_bf16 v[40:43], v[16:19], a[168:175], v68\n\t" \
  "s_waitcnt lgkmcnt(5)\n\t" \
  "v_mov_b64 v[44:45], 0\n\t" \
  "v_smfmac_f32_16x16x64_bf16 v[36:39], v[20:23], a[200:207], v68\n\t" \
  "s_waitcnt lgkmcnt(4)\n\t" \
  "v_mov_b64 v[46:47], 0\n\t" \
  "v_smfmac_f32_16x16x64_bf16 v[36:39], v[24:27], a[240:247], v68\n\t" \
  "s_waitcnt lgkmcnt(2)\n\t" \
  "v_mov_b64 v[48:49], 0\n\t" \
  "v_smfmac_f32_16x16x64_bf16 v[36:39], v[28:31], v[112:119], v68\n\t" \
  "v_mov_b64 v[50:51], 0\n\t" \
  "v_smfmac_f32_16x16x64_bf16 v[36:39], v[32:35], v[160:167], v68\n\t" \
  "s_waitcnt lgkmcnt(0)\n\t" \
  "v_smfmac_f32_16x16x64_bf16 v[40:43], v[20:23], v[60:67], v68\n\t" \
  "ds_read_b128 v[60:63], v70 offset:8192\n\t" \
  "v_smfmac_f32_16x16x64_bf16 v[40:43], v[24:27], a[248:255], v68\n\t" \
  "ds_read_b128 v[64:67], v70 offset:9216\n\t" \
  "v_add_f32 v74, v36, v38\n\t" \
  "v_smfmac_f32_16x16x64_bf16 v[40:43], v[28:31], v[120:127], v68\n\t" \
  "v_add_f32 v37, v37, v39\n\t" \
  "v_smfmac_f32_16x16x64_bf16 v[40:43], v[32:35], v[168:175], v68\n\t" \
  "v_cmp_u_f32 vcc, v74, v37\n\t" \
  "v_smfmac_f32_16x16x64_bf16 v[44:47], v[0:3], a[0:7], v68\n\t" \
  "s_cbranch_vccnz LFAIL3_%=\n\t" \
  "LGO3_%=:\n\t" \
  "v_cvt_pk_bf16_f32 v74, v74, v37\n\t" \
  "v_smfmac_f32_16x16x64_bf16 v[44:47], v[4:7], a[48:55], v68\n\t" \
  "s_waitcnt lgkmcnt(0)\n\t" \
  "v_add_f32 v75, v40, v42\n\t" \
  "v_smfmac_f32_16x16x64_bf16 v[44:47], v[8:11], v[60:67], v68\n\t" \
  "ds_read_b128 v[60:63], v70 offset:10240\n\t" \
  "v_add_f32 v41, v41, v43\n\t" \
  "v_cvt_pk_bf16_f32 v75, v75, v41\n\t" \
  "v_smfmac_f32_16x16x64_bf16 v[44:47], v[12:15], a[128:135], v68\n\t" \
  "ds_write2st64_b32 v71, v74, v75 offset0:40 offset1:42\n\t" \
  "v_smfmac_f32_16x16x64_bf16 v[44:47], v[16:19], a[176:183], v68\n\t" \
  "ds_read_b128 v[64:67], v70 offset:11264\n\t" \
  "s_memrealtime s[66:67]\n\t" \
  "v_smfmac_f32_16x16x64_bf16 v[44:47], v[20:23], a[208:215], v68\n\t" \
  "v_smfmac_f32_16x16x64_bf16 v[44:47], v[24:27], v[80:87], v68\n\t" \
  "v_smfmac_f32_16x16x64_bf16 v[44:47], v[28:31], v[128:135], v68\n\t" \
  "s_waitcnt lgkmcnt(0)\n\t" \
  "v_smfmac_f32_16x16x64_bf16 v[44:47], v[32:35], v[60:67], v68\n\t" \
  "s_barrier\n\t" \
  "ds_read_b64_tr_b16 v[76:77], v72 offset:10240\n\t" \
  "v_smfmac_f32_16x16x64_bf16 v[48:51], v[0:3], a[8:15], v68\n\t" \
  "s_lshl_b32 s60, s66, 8\n\t" \
  "s_sub_u32 s60, s47, s60\n\t" \
  "ds_read_b64_tr_b16 v[78:79], v72 offset:10752\n\t" \
  "v_mov_b32 v75, s60\n\t" \
  "v_smfmac_f32_16x16x64_bf16 v[48:51], v[4:7], a[56:63], v68\n\t" \
  "s_mov_b64 exec, s[24:25]\n\t" \
  "buffer_atomic_umin v75, v69, s[28:31], s71 offen sc1\n\t" \
  "s_mov_b64 exec, -1\n\t" \
  "v_add_f32 v74, v44, v46\n\t" \
  "v_smfmac_f32_16x16x64_bf16 v[48:51], v[8:11], a[96:103], v68\n\t" \
  "buffer_load_dwordx4 v[60:63], v69, s[28:31], s64 offen sc1\n\t" \
  "v_add_f32 v45, v45, v47\n\t" \
  "v_smfmac_f32_16x16x64_bf16 v[48:51], v[12:15], a[136:143], v68\n\t" \
  "s_waitcnt vmcnt(4)\n\t" \
  "s_waitcnt lgkmcnt(0)\n\t" \
  "v_cvt_pk_bf16_f32 v74, v74, v45\n\t" \
  "v_mfma_f32_16x16x32_bf16 v[36:39], v[52:55], v[76:79], 0\n\t" \
  "s_waitcnt vmcnt(3)\n\t" \
  "v_mfma_f32_16x16x32_bf16 v[40:43], v[56:59], v[76:79], 0\n\t" \
  "v_mov_b64 v[52:53], 0\n\t" \
  "v_mov_b64 v[54:55], 0\n\t" \
  "v_smfmac_f32_16x16x64_bf16 v[48:51], v[16:19], a[184:191], v68\n\t" \
  "buffer_load_dwordx4 v[64:67], v69, s[28:31], s64 offen offset:1024 sc1\n\t" \
  "v_mov_b64 v[56:57], 0\n\t" \
  "v_mov_b64 v[58:59], 0\n\t" \
  "v_smfmac_f32_16x16x64_bf16 v[48:51], v[20:23], a[216:223], v68\n\t" \
  "ds_write_b32 v71, v74 offset:1024\n\t" \
  "v_add_f32_dpp v36, v40, v36 quad_perm:[2,3,0,1] row_mask:0xf bank_mask:0xf\n\t" \
  "v_smfmac_f32_16x16x64_bf16 v[48:51], v[24:27], v[88:95], v68\n\t" \
  "v_add_f32_dpp v37, v41, v37 quad_perm:[2,3,0,1] row_mask:0xf bank_mask:0xf\n\t" \
  "v_smfmac_f32_16x16x64_bf16 v[48:51], v[28:31], v[136:143], v68\n\t" \
  "v_add_f32_dpp v38, v42, v38 quad_perm:[2,3,0,1] row_mask:0xf bank_mask:0xf\n\t" \
  "v_smfmac_f32_16x16x64_bf16 v[48:51], v[32:35], v[176:183], v68\n\t" \
  "v_add_f32_dpp v39, v43, v39 quad_perm:[2,3,0,1] row_mask:0xf bank_mask:0xf\n\t" \
  "v_smfmac_f32_16x16x64_bf16 v[52:55], v[0:3], a[16:23], v68\n\t" \
  "s_mov_b64 exec, s[58:59]\n\t" \
  "ds_write_b128 v69, v[36:39] offset:3104\n\t" \
  "s_mov_b64 exec, -1\n\t" \
  "v_smfmac_f32_16x16x64_bf16 v[52:55], v[4:7], a[64:71], v68\n\t" \
  "buffer_load_dword off, s[28:31], s87 sc1 lds\n\t" \
  "v_add_f32 v75, v48, v50\n\t" \
  "v_add_f32 v49, v49, v51\n\t" \
  "v_cvt_pk_bf16_f32 v75, v75, v49\n\t" \
  "v_smfmac_f32_16x16x64_bf16 v[56:59], v[0:3], a[24:31], v68\n\t" \
  "ds_write_b32 v71, v75 offset:1536\n\t" \
  "v_smfmac_f32_16x16x64_bf16 v[56:59], v[4:7], a[72:79], v68\n\t" \
  "ds_read_b64_tr_b16 v[0:1], v72 offset:9216\n\t" \
  "ds_read_b64_tr_b16 v[2:3], v72 offset:9728\n\t" \
  "v_smfmac_f32_16x16x64_bf16 v[52:55], v[8:11], a[104:111], v68\n\t" \
  "ds_read_b128 v[36:39], v70 offset:12288\n\t" \
  "v_smfmac_f32_16x16x64_bf16 v[56:59], v[8:11], a[112:119], v68\n\t" \
  "ds_read_b128 v[40:43], v70 offset:13312\n\t" \
  "v_smfmac_f32_16x16x64_bf16 v[52:55], v[12:15], a[144:151], v68\n\t" \
  "s_waitcnt lgkmcnt(4)\n\t" \
  "s_barrier\n\t" \
  "ds_read_b64_tr_b16 v[44:45], v72 offset:1024\n\t" \
  "ds_read_b64_tr_b16 v[46:47], v72 offset:1536\n\t" \
  "v_smfmac_f32_16x16x64_bf16 v[56:59], v[12:15], a[152:159], v68\n\t" \
  "ds_read_b64_tr_b16 v[48:49], v72 offset:0\n\t" \
  "ds_read_b64_tr_b16 v[50:51], v72 offset:512\n\t" \
  "v_smfmac_f32_16x16x64_bf16 v[52:55], v[16:19], a[192:199], v68\n\t" \
  "ds_read_b128 v[4:7], v69 offset:2048\n\t" \
  "s_waitcnt lgkmcnt(5)\n\t" \
  "v_smfmac_f32_16x16x64_bf16 v[56:59], v[16:19], v[36:43], v68\n\t" \
  "ds_read_b128 v[8:11], v69 offset:3072\n\t" \
  "s_waitcnt lgkmcnt(4)\n\t" \
  "v_mfma_f32_16x16x32_bf16 v[12:15], v[44:47], v[76:79], 0\n\t" \
  "ds_read_b128 v[36:39], v70 offset:14336\n\t" \
  "s_waitcnt lgkmcnt(3)\n\t" \
  "v_mfma_f32_16x16x32_bf16 v[12:15], v[48:51], v[0:3], v[12:15]\n\t" \
  "ds_read_b128 v[40:43], v70 offset:15360\n\t" \
  "v_smfmac_f32_16x16x64_bf16 v[52:55], v[20:23], a[224:231], v68\n\t" \
  "s_waitcnt lgkmcnt(2)\n\t" \
  "v_add_f32 v4, v4, v8\n\t" \
  "v_add_f32 v5, v5, v9\n\t" \
  "v_add_f32 v6, v6, v10\n\t" \
  "v_cndmask_b32 v78, v13, v14, s[56:57]\n\t" \
  "v_cndmask_b32 v76, v12, v15, s[56:57]\n\t" \
  "v_add_f32 v7, v7, v11\n\t" \
  "v_max3_f32 v77, v4, v5, v6\n\t" \
  "v_add_f32_dpp v76, v78, v76 quad_perm:[2,3,0,1] row_mask:0xf bank_mask:0xf\n\t" \
  "v_max3_f32 v77, v77, v7, v76\n\t" \
  "v_smfmac_f32_16x16x64_bf16 v[56:59], v[20:23], a[232:239], v68\n\t" \
  "ds_read_b128 v[44:47], v70 offset:16384\n\t" \
  "v_max_f32_dpp v77, v77, v77 quad_perm:[2,3,0,1] row_mask:0xf bank_mask:0xf\n\t" \
  "v_mov_b32 v78, v77\n\t" \
  "v_smfmac_f32_16x16x64_bf16 v[52:55], v[24:27], v[96:103], v68\n\t" \
  "ds_read_b128 v[48:51], v70 offset:17408\n\t" \
  "v_permlane16_swap_b32 v77, v78\n\t" \
  "v_max_f32 v77, v77, v78\n\t" \
  "v_mov_b32 v78, v77\n\t" \
  "v_smfmac_f32_16x16x64_bf16 v[52:55], v[28:31], v[144:151], v68\n\t" \
  "ds_read_b128 v[16:19], v70 offset:18432\n\t" \
  "v_permlane32_swap_b32 v77, v78\n\t" \
  "v_max_f32 v77, v77, v78\n\t" \
  "v_smfmac_f32_16x16x64_bf16 v[52:55], v[32:35], v[184:191], v68\n\t" \
  "ds_read_b128 v[20:23], v70 offset:19456\n\t" \
  "v_mul_f32 v77, 0xbe0293ee, v77\n\t" \
  "v_smfmac_f32_16x16x64_bf16 v[56:59], v[24:27], v[104:111], v68\n\t" \
  "v_fmamk_f32 v4, v4, 0x3e0293ee, v77\n\t" \
  "v_smfmac_f32_16x16x64_bf16 v[56:59], v[28:31], v[152:159], v68\n\t" \
  "ds_read_b128 v[24:27], v70 offset:22528\n\t" \
  "s_waitcnt lgkmcnt(4)\n\t" \
  "v_fmamk_f32 v5, v5, 0x3e0293ee, v77\n\t" \
  "v_smfmac_f32_16x16x64_bf16 v[56:59], v[32:35], v[36:43], v68\n\t" \
  "ds_read_b128 v[28:31], v70 offset:23552\n\t" \
  "v_fmamk_f32 v6, v6, 0x3e0293ee, v77\n\t" \
  "v_fmamk_f32 v7, v7, 0x3e0293ee, v77\n\t" \
  "v_fmamk_f32 v76, v76, 0x3e0293ee, v77\n\t" \
  "v_exp_f32 v4, v4\n\t" \
  "v_exp_f32 v5, v5\n\t" \
  "v_exp_f32 v6, v6\n\t" \
  "v_exp_f32 v7, v7\n\t" \
  "v_cvt_pk_bf16_f32 v4, v4, v5\n\t" \
  "v_exp_f32 v76, v76\n\t" \
  "v_cvt_pk_bf16_f32 v5, v6, v7\n\t" \
  "v_mov_b32_dpp v12, v4 quad_perm:[0,1,0,1] row_mask:0xf bank_mask:0xf\n\t" \
  "v_mov_b32_dpp v14, v4 quad_perm:[2,3,2,3] row_mask:0xf bank_mask:0xf\n\t" \
  "v_mov_b32_dpp v13, v5 quad_perm:[0,1,0,1] row_mask:0xf bank_mask:0xf\n\t" \
  "v_mov_b32_dpp v15, v5 quad_perm:[2,3,2,3] row_mask:0xf bank_mask:0xf\n\t" \
  "v_add_f32 v52, v52, v54\n\t" \
  "v_add_f32 v53, v53, v55\n\t" \
  "s_waitcnt vmcnt(2)\n\t" \
  "v_mfma_f32_16x16x32_bf16 v[0:3], v[60:63], v[12:15], 0\n\t" \
  "ds_read_b128 v[36:39], v70 offset:20480\n\t" \
  "v_add_f32 v56, v56, v58\n\t" \
  "v_add_f32 v57, v57, v59\n\t" \
  "v_cvt_pk_bf16_f32 v52, v52, v53\n\t" \
  "v_cvt_pk_bf16_f32 v53, v56, v57\n\t" \
  "s_waitcnt vmcnt(1)\n\t" \
  "v_mfma_f32_16x16x32_bf16 v[4:7], v[64:67], v[12:15], 0\n\t" \
  "ds_read_b128 v[40:43], v70 offset:21504\n\t" \
  "v_mov_b64 v[56:57], s[88:89]\n\t" \
  "v_mov_b64 v[58:59], s[88:89]\n\t" \
  "v_mov_b32_dpp v78, v76 quad_perm:[0,1,0,1] row_mask:0xf bank_mask:0xf\n\t" \
  "v_mov_b32_dpp v79, v76 quad_perm:[2,3,2,3] row_mask:0xf bank_mask:0xf\n\t" \
  "v_mfma_f32_16x16x32_bf16 v[32:35], v[56:59], v[12:15], 0\n\t" \
  "ds_read_b128 v[60:63], v70 offset:24576\n\t" \
  "v_cvt_pk_bf16_f32 v11, v78, v79\n\t" \
  "v_cvt_pk_bf16_f32 v8, v78, v79\n\t" \
  "v_mov_b64 v[54:55], 0\n\t" \
  "v_mov_b32 v9, 0\n\t" \
  "v_mov_b32 v10, 0\n\t" \
  "v_mov_b64 v[14:15], 0\n\t" \
  "s_nop 0\n\t" \
  "v_mfma_f32_16x16x32_bf16 v[32:35], v[54:57], v[8:11], v[32:35]\n\t" \
  "s_cmp_eq_u32 s48, 63\n\t" \
  "v_mfma_f32_16x16x32_bf16 v[0:3], v[52:55], v[8:11], v[0:3]\n\t" \
  "buffer_store_dwordx2 v[52:53], v71, s[28:31], s69 offen offset:3580 sc1\n\t" \
  "ds_read_b128 v[64:67], v70 offset:25600\n\t" \
  "v_mov_b64 v[56:57], 0\n\t" \
  "v_mov_b64 v[58:59], 0\n\t" \
  "v_mfma_f32_16x16x32_bf16 v[4:7], v[52:55], v[10:13], v[4:7]\n\t" \
  "s_cbranch_scc1 LLAST3_%=\n\t" \
  "v_rcp_f32 v74, v32\n\t" \
  "v_mov_b64 v[12:13], 0\n\t" \
  "v_mov_b32 v8, 0\n\t" \
  "v_mul_f32 v0, v0, v74\n\t" \
  "v_mul_f32 v1, v1, v74\n\t" \
  "v_mul_f32 v2, v2, v74\n\t" \
  "v_mul_f32 v3, v3, v74\n\t" \
  "v_cvt_pk_bf16_f32 v76, v0, v1\n\t" \
  "v_cvt_pk_bf16_f32 v77, v2, v3\n\t" \
  "v_pk_mul_f32 v[4:5], v[4:5], v[74:75] op_sel_hi:[1,0]\n\t" \
  "v_pk_mul_f32 v[6:7], v[6:7], v[74:75] op_sel_hi:[1,0]\n\t" \
  "v_cvt_pk_bf16_f32 v78, v4, v5\n\t" \
  "v_cvt_pk_bf16_f32 v79, v6, v7\n\t" \
  "ds_write_b128 v69, v[76:79] offset:7168\n\t" \
  "s_waitcnt lgkmcnt(2)\n\t" \
  "v_smfmac_f32_16x16x64_bf16 v[12:15], v[76:79], v[44:51], v68\n\t" \
  "ds_read_b128 v[44:47], v70 offset:26624\n\t" \
  "v_mov_b32 v11, 0\n\t" \
  "v_smfmac_f32_16x16x64_bf16 v[56:59], v[76:79], v[16:23], v68\n\t" \
  "ds_read_b128 v[48:51], v70 offset:27648\n\t" \
  "v_mov_b64 v[52:53], 0\n\t" \
  "v_smfmac_f32_16x16x64_bf16 v[8:11], v[76:79], v[36:43], v68\n\t" \
  "ds_read_b128 v[16:19], v70 offset:28672\n\t" \
  "v_smfmac_f32_16x16x64_bf16 v[52:55], v[76:79], v[24:31], v68\n\t" \
  "s_waitcnt lgkmcnt(3)\n\t" \
  "s_barrier\n\t" \
  "ds_read_b128 v[0:3], v69 offset:6144\n\t" \
  "ds_read_b128 v[4:7], v69 offset:5120\n\t" \
  "v_lshrrev_b32 v71, 7, v69\n\t" \
  "v_and_b32 v74, 0x70, v69\n\t" \
  "v_lshl_add_u32 v71, v71, 14, v74\n\t" \
  "v_bfe_u32 v72, v69, 2, 7\n\t" \
  "v_and_b32 v74, 0x200, v69\n\t" \
  "s_waitcnt lgkmcnt(1)\n\t" \
  "v_smfmac_f32_16x16x64_bf16 v[8:11], v[0:3], v[60:67], v68\n\t" \
  "ds_read_b128 v[20:23], v70 offset:29696\n\t" \
  "v_mov_b64 v[76:77], 0\n\t" \
  "v_smfmac_f32_16x16x64_bf16 v[12:15], v[0:3], v[200:207], v68\n\t" \
  "ds_read_b128 v[32:35], v69 offset:4096\n\t" \
  "v_mov_b64 v[78:79], 0\n\t" \
  "v_smfmac_f32_16x16x64_bf16 v[52:55], v[0:3], v[44:51], v68\n\t" \
  "buffer_load_dword v75, off, s[28:31], s80 sc1\n\t" \
  "ds_read_b128 v[24:27], v70 offset:30720\n\t" \
  "v_lshl_add_u32 v72, v74, 3, v72\n\t" \
  "v_smfmac_f32_16x16x64_bf16 v[56:59], v[0:3], v[232:239], v68\n\t" \
  "ds_read_b128 v[28:31], v70 offset:31744\n\t" \
  "s_waitcnt lgkmcnt(4)\n\t" \
  "v_smfmac_f32_16x16x64_bf16 v[12:15], v[4:7], v[208:215], v68\n\t" \
  "ds_read_b128 v[60:63], v70 offset:32768\n\t" \
  "s_waitcnt lgkmcnt(4)\n\t" \
  "v_smfmac_f32_16x16x64_bf16 v[8:11], v[4:7], v[16:23], v68\n\t" \
  "ds_read_b128 v[64:67], v70 offset:33792\n\t" \
  "s_waitcnt lgkmcnt(4)\n\t" \
  "v_smfmac_f32_16x16x64_bf16 v[8:11], v[32:35], v[192:199], v68\n\t" \
  "ds_read_b128 v[36:39], v73 offset:4096\n\t" \
  "v_smfmac_f32_16x16x64_bf16 v[12:15], v[32:35], v[216:223], v68\n\t" \
  "ds_read_b128 v[40:43], v73 offset:6144\n\t" \
  "s_waitcnt lgkmcnt(4)\n\t" \
  "v_smfmac_f32_16x16x64_bf16 v[52:55], v[4:7], v[24:31], v68\n\t" \
  "ds_read_b128 v[44:47], v70 offset:34816\n\t" \
  "v_cvt_pk_bf16_f32 v8, v8, v9\n\t" \
  "v_smfmac_f32_16x16x64_bf16 v[52:55], v[32:35], v[224:231], v68\n\t" \
  "ds_read_b128 v[48:51], v70 offset:35840\n\t" \
  "v_cvt_pk_bf16_f32 v9, v10, v11\n\t" \
  "v_smfmac_f32_16x16x64_bf16 v[56:59], v[4:7], v[240:247], v68\n\t" \
  "s_waitcnt lgkmcnt(4)\n\t" \
  "v_cvt_pk_bf16_f32 v10, v12, v13\n\t" \
  "v_smfmac_f32_16x16x64_bf16 v[56:59], v[32:35], v[60:67], v68\n\t" \
  "s_waitcnt lgkmcnt(3)\n\t" \
  "v_cvt_pk_bf16_f32 v11, v14, v15\n\t" \
  "v_smfmac_f32_16x16x64_bf16 v[76:79], v[36:39], v[248:255], v68\n\t" \
  "buffer_store_dwordx4 v[8:11], v71, s[28:31], s54 offen sc1\n\t" \
  "s_waitcnt lgkmcnt(0)\n\t" \
  "v_cvt_pk_bf16_f32 v52, v52, v53\n\t" \
  "v_smfmac_f32_16x16x64_bf16 v[76:79], v[40:43], v[44:51], v68\n\t" \
  "v_cvt_pk_bf16_f32 v53, v54, v55\n\t" \
  "v_cvt_pk_bf16_f32 v54, v56, v57\n\t" \
  "v_cvt_pk_bf16_f32 v55, v58, v59\n\t" \
  "buffer_store_dwordx4 v[52:55], v71, s[28:31], s75 offen sc1\n\t" \
  "s_nop 2\n\t" \
  "v_add_f32 v76, v76, v78\n\t" \
  "v_add_f32 v77, v77, v79\n\t" \
  "v_cvt_pk_bf16_f32 v76, v76, v77\n\t" \
  "buffer_store_dword v76, v72, s[28:31], s55 offen sc1\n\t" \
  "s_memrealtime s[70:71]\n\t" \
  "s_waitcnt vmcnt(3)\n\t" \
  "v_readfirstlane_b32 s65, v75\n\t" \
  "s_barrier\n\t" \
  "s_waitcnt lgkmcnt(0)\n\t" \
  "s_not_b32 s65, s65\n\t" \
  "s_lshl_b32 s61, s70, 8\n\t" \
  "s_andn2_b32 s64, s65, 0xff\n\t" \
  "s_sub_u32 s64, s64, s61\n\t" \
  "s_ashr_i32 s64, s64, 8\n\t" \
  "s_cmp_eq_u32 s65, 0\n\t" \
  "s_cselect_b32 s64, " CH_DLF ", s64\n\t" \
  "s_min_i32 s64, s64, 1000\n\t" \
  "s_max_i32 s64, s64, " CH_DLL1 "\n\t" \
  "s_add_u32 s64, s64, s70\n\t" \
  "s_mov_b32 s74, 0\n\t" \
  "v_and_b32 v37, 0x30, v69\n\t" \
  "v_add_u32 v37, s52, v37\n\t" \
  "s_mov_b32 exec_lo, 0\n\t" \
  "v_mov_b32 v37, s85\n\t" \
  "s_mov_b32 exec_lo, -1\n\t" \
  "s_cmp_lt_i32 s41, 0\n\t" \
  "s_cbranch_scc1 LIDLE_%=\n\t" \
  "s_movk_i32 s60, 8192\n\t" \
  "RWT3_%=:\n\t" \
  "s_memrealtime s[72:73]\n\t" \
  "s_sub_u32 s60, s60, 1\n\t" \
  "s_cselect_b32 s61, 0, -1\n\t" \
  "s_waitcnt lgkmcnt(0)\n\t" \
  "s_sub_i32 s73, s72, s64\n\t" \
  "s_and_b32 s73, s73, s61\n\t" \
  "s_cmp_lt_i32 s73, " CH_FIN "\n\t" \
  "s_cbranch_scc1 RWT3_%=\n\t" \
  "s_cmp_lt_i32 s73, 0\n\t" \
  "s_cbranch_scc1 RWT3_%=f\n\t" \
  "RWT3_%=g:\n\t" \
  "RRETRY3_%=:\n\t" \
  "buffer_load_dwordx4 v[0:3], v69, s[28:31], s97 offen offset:0 sc1\n\t" \
  "buffer_load_dwordx4 v[4:7], v69, s[28:31], s97 offen offset:1024 sc1\n\t" \
  "buffer_load_dwordx4 v[8:11], v69, s[28:31], s97 offen offset:2048 sc1\n\t" \
  "buffer_load_dwordx4 v[12:15], v69, s[28:31], s97 offen offset:3072 sc1\n\t" \
  "v_lshrrev_b32 v20, 2, v69\n\t" \
  "v_and_b32 v20, 16, v20\n\t" \
  "v_mov_b32 v21, 0x3f80\n\t" \
  "v_lshlrev_b32 v21, v20, v21\n\t" \
  "v_bfe_u32 v20, v69, 4, 2\n\t" \
  "v_cmp_eq_u32 vcc, 0, v20\n\t" \
  "s_nop 1\n\t" \
  "v_cndmask_b32 v32, 0, v21, vcc\n\t" \
  "v_cmp_eq_u32 vcc, 1, v20\n\t" \
  "s_nop 1\n\t" \
  "v_cndmask_b32 v33, 0, v21, vcc\n\t" \
  "v_cmp_eq_u32 vcc, 2, v20\n\t" \
  "s_nop 1\n\t" \
  "v_cndmask_b32 v34, 0, v21, vcc\n\t" \
  "v_cmp_eq_u32 vcc, 3, v20\n\t" \
  "s_nop 1\n\t" \
  "v_cndmask_b32 v35, 0, v21, vcc\n\t" \
  "v_bfe_u32 v38, v69, 7, 1\n\t" \
  "v_bfe_u32 v20, v69, 9, 1\n\t" \
  "v_lshl_add_u32 v38, v20, 2, v38\n\t" \
  "v_lshlrev_b32 v38, 12, v38\n\t" \
  "v_and_b32 v20, 0x60, v69\n\t" \
  "v_add_u32 v38, v38, v20\n\t" \
  "v_bfe_u32 v20, v69, 8, 1\n\t" \
  "v_lshl_add_u32 v38, v20, 4, v38\n\t" \
  "v_bfe_u32 v20, v69, 4, 1\n\t" \
  "v_lshl_add_u32 v38, v20, 3, v38\n\t" \
  "v_mov_b32 v44, 0\n\t" \
  "buffer_load_dword v39, v37, s[28:31], 0 offen sc1\n\t" \
  "s_waitcnt vmcnt(4)\n\t" \
  "v_mfma_f32_16x16x32_bf16 v[40:43], v[32:35], v[0:3], 0\n\t" \
  "v_max3_u32 v44, v44, v0, v1\n\t" \
  "v_max3_u32 v44, v44, v2, v3\n\t" \
  "s_waitcnt vmcnt(3)\n\t" \
  "v_mfma_f32_16x16x32_bf16 v[40:43], v[32:35], v[4:7], v[40:43]\n\t" \
  "v_max3_u32 v44, v44, v4, v5\n\t" \
  "v_max3_u32 v44, v44, v6, v7\n\t" \
  "s_waitcnt vmcnt(2)\n\t" \
  "v_mfma_f32_16x16x32_bf16 v[40:43], v[32:35], v[8:11], v[40:43]\n\t" \
  "v_max3_u32 v44, v44, v8, v9\n\t" \
  "v_max3_u32 v44, v44, v10, v11\n\t" \
  "s_waitcnt vmcnt(1)\n\t" \
  "v_mfma_f32_16x16x32_bf16 v[40:43], v[32:35], v[12:15], v[40:43]\n\t" \
  "v_max3_u32 v44, v44, v12, v13\n\t" \
  "v_max3_u32 v44, v44, v14, v15\n\t" \
  "v_cmp_eq_u32 vcc, -1, v44\n\t" \
  "s_cbranch_vccnz RFAIL3_%=\n\t" \
  "s_nop 3\n\t" \
  "v_add_f32_dpp v40, v40, v40 row_ror:8 row_mask:0xf bank_mask:0xf\n\t" \
  "v_add_f32_dpp v41, v41, v41 row_ror:8 row_mask:0xf bank_mask:0xf\n\t" \
  "v_add_f32_dpp v42, v42, v42 row_ror:8 row_mask:0xf bank_mask:0xf\n\t" \
  "v_add_f32_dpp v43, v43, v43 row_ror:8 row_mask:0xf bank_mask:0xf\n\t" \
  "v_cvt_pk_bf16_f32 v40, v40, v41\n\t" \
  "v_cvt_pk_bf16_f32 v41, v42, v43\n\t" \
  "buffer_store_dwordx2 v[40:41], v38, s[28:31], s98 offen sc1\n\t" \
  "RDONE3_%=:\n\t" \
  "s_barrier\n\t" \
  "s_waitcnt vmcnt(1)\n\t" \
  "v_cmp_eq_u32 vcc, 0, v39\n\t" \
  "s_sub_u32 s60, s70, s66\n\t" \
  "s_add_u32 s81, s81, 1\n\t" \
  "s_min_u32 s81, s81, s60\n\t" \
  "s_add_u32 s48, s48, 1\n\t" \
  "s_mov_b32 s49, s53\n\t" \
  "s_add_u32 s53, s53, " CH_SLOT "\n\t" \
  "s_add_u32 s60, s53, " CH_SKIP "\n\t" \
  "s_lshr_b32 s61, s96, 1\n\t" \
  "s_add_u32 s86, s86, 1\n\t" \
  "s_cmp_eq_u32 s86, 3\n\t" \
  "s_cselect_b32 s53, s60, s53\n\t" \
  "s_cselect_b32 s96, s61, s96\n\t" \
  "s_cselect_b32 s86, 0, s86\n\t" \
  "s_andn2_b32 s53, s53, 0x2000\n\t" \
  "s_and_b32 s60, s96, 1\n\t" \
  "s_lshl_b32 s60, s60, 13\n\t" \
  "s_or_b32 s53, s53, s60\n\t" \
  "s_mov_b32 s68, 0\n\t" \
  "s_add_u32 s97, s41, s53\n\t" \
  "s_add_u32 s98, s37, s53\n\t" \
  "s_add_u32 s82, s48, 0\n\t" \
  "s_lshr_b64 s[60:61], s[90:91], s82\n\t" \
  "s_and_b32 s60, s60, 1\n\t" \
  "s_lshl_b32 s60, s60, 13\n\t" \
  "s_add_u32 s82, s82, " CH_SG0 "\n\t" \
  "s_lshl_b32 s82, s82, 21\n\t" \
  "s_or_b32 s82, s82, s60\n\t" \
  "s_add_u32 s50, s82, s38\n\t" \
  "s_add_u32 s69, s82, s39\n\t" \
  "s_add_u32 s82, s48, 1\n\t" \
  "s_lshr_b64 s[60:61], s[90:91], s82\n\t" \
  "s_and_b32 s60, s60, 1\n\t" \
  "s_lshl_b32 s60, s60, 13\n\t" \
  "s_add_u32 s82, s82, " CH_SG0 "\n\t" \
  "s_lshl_b32 s82, s82, 21\n\t" \
  "s_or_b32 s82, s82, s60\n\t" \
  "s_add_u32 s52, s82, s43\n\t" \
  "s_add_u32 s82, s48, " CH_ARMD "\n\t" \
  "s_lshr_b64 s[60:61], s[90:91], s82\n\t" \
  "s_and_b32 s60, s60, 1\n\t" \
  "s_lshl_b32 s60, s60, 13\n\t" \
  "s_add_u32 s82, s82, " CH_SG0 "\n\t" \
  "s_lshl_b32 s82, s82, 21\n\t" \
  "s_or_b32 s82, s82, s60\n\t" \
  "s_add_u32 s85, s82, s45\n\t" \
  "s_cmp_lt_i32 s48, " CH_ARMS "\n\t" \
  "s_cselect_b32 s85, s27, s85\n\t" \
  "LTK_%=:\n\t" \
  CH_TOKCHK \
  "s_mov_b32 s61, 0\n\t" \
  "LTW_%=:\n\t" \
  "buffer_load_dword v1, v37, s[28:31], 0 offen sc1\n\t" \
  "s_waitcnt vmcnt(0)\n\t" \
  "v_cmp_eq_u32 vcc, 0, v1\n\t" \
  "s_cbranch_vccz LG3_%=\n\t" \
  "s_add_u32 s61, s61, 1\n\t" \
  "s_sleep 2\n\t" \
  "s_cmp_gt_u32 s61, s84\n\t" \
  "s_cbranch_scc1 LTG_%=\n\t" \
  "s_and_b32 s60, s61, 1023\n\t" \
  "s_cmp_lg_u32 s60, 0\n\t" \
  "s_cbranch_scc1 LTW_%=\n\t" \
  "buffer_load_dword v46, off, s[28:31], s92 sc1\n\t" \
  "s_waitcnt vmcnt(0)\n\t" \
  "v_readfirstlane_b32 s60, v46\n\t" \
  "s_nop 0\n\t" \
  "s_cmp_eq_u32 s60, 0\n\t" \
  "s_cbranch_scc1 LTW_%=\n\t" \
  "LTG_%=:\n\t" \
  "s_mov_b32 s84, 0\n\t" \
  "s_lshl_b32 s60, s36, 4\n\t" \
  "s_or_b32 s60, s60, s48\n\t" \
  "s_or_b32 s60, s60, 268435456\n\t" \
  "v_mov_b32 v76, s60\n\t" \
  "v_mov_b32 v77, 0\n\t" \
  "s_mov_b64 exec, 1\n\t" \
  "buffer_atomic_cmpswap v[76:77], off, s[28:31], s92 sc1\n\t" \
  "s_mov_b64 exec, -1\n\t" \
  "s_branch LG3_%=\n\t" \
  "LIDLE_%=:\n\t" \
  "s_add_u32 s64, s64, 8\n\t" \
  "s_movk_i32 s60, 8192\n\t" \
  "IWT_%=:\n\t" \
  "s_memrealtime s[72:73]\n\t" \
  "s_sub_u32 s60, s60, 1\n\t" \
  "s_cselect_b32 s61, 0, -1\n\t" \
  "s_waitcnt lgkmcnt(0)\n\t" \
  "s_sub_i32 s73, s72, s64\n\t" \
  "s_and_b32 s73, s73, s61\n\t" \
  "s_cmp_lt_i32 s73, 0\n\t" \
  "s_cbranch_scc1 IWT_%=\n\t" \
  "buffer_load_dword v39, v37, s[28:31], 0 offen sc1\n\t" \
  "buffer_store_dword v37, off, s[28:31], s41\n\t" \
  "s_branch RDONE3_%=\n\t" \
  "RFAIL3_%=:\n\t" \
  "s_add_u32 s74, s74, 1\n\t" \
  "s_sleep " CH_RSL "\n\t" \
  "s_waitcnt vmcnt(0)\n\t" \
  "s_cmp_gt_u32 s74, s84\n\t" \
  "s_cbranch_scc1 RG3_%=\n\t" \
  "s_and_b32 s60, s74, 1023\n\t" \
  "s_cmp_lg_u32 s60, 0\n\t" \
  "s_cbranch_scc1 RRETRY3_%=\n\t" \
  "buffer_load_dword v46, off, s[28:31], s92 sc1\n\t" \
  "s_waitcnt vmcnt(0)\n\t" \
  "v_readfirstlane_b32 s60, v46\n\t" \
  "s_nop 0\n\t" \
  "s_cmp_eq_u32 s60, 0\n\t" \
  "s_cbranch_scc1 RRETRY3_%=\n\t" \
  "RG3_%=:\n\t" \
  "s_mov_b32 s84, 0\n\t" \
  "s_lshl_b32 s60, s36, 4\n\t" \
  "s_or_b32 s60, s60, s48\n\t" \
  "s_or_b32 s60, s60, 536870912\n\t" \
  "v_mov_b32 v76, s60\n\t" \
  "v_mov_b32 v77, 0\n\t" \
  "s_mov_b64 exec, 1\n\t" \
  "buffer_atomic_cmpswap v[76:77], off, s[28:31], s92 sc1\n\t" \
  "s_mov_b64 exec, -1\n\t" \
  "s_branch RDONE3_%=\n\t" \
  "LFAIL3_%=:\n\t" \
  "v_max_u32 v75, v0, v1\n\t" \
  "v_max3_u32 v75, v75, v2, v3\n\t" \
  "v_max3_u32 v75, v75, v4, v5\n\t" \
  "v_max3_u32 v75, v75, v6, v7\n\t" \
  "v_max3_u32 v75, v75, v8, v9\n\t" \
  "v_max3_u32 v75, v75, v10, v11\n\t" \
  "v_max3_u32 v75, v75, v12, v13\n\t" \
  "v_max3_u32 v75, v75, v14, v15\n\t" \
  "v_max3_u32 v75, v75, v16, v17\n\t" \
  "v_max3_u32 v75, v75, v18, v19\n\t" \
  "v_max3_u32 v75, v75, v20, v21\n\t" \
  "v_max3_u32 v75, v75, v22, v23\n\t" \
  "v_max3_u32 v75, v75, v24, v25\n\t" \
  "v_max3_u32 v75, v75, v26, v27\n\t" \
  "v_max3_u32 v75, v75, v28, v29\n\t" \
  "v_max3_u32 v75, v75, v30, v31\n\t" \
  "v_max3_u32 v75, v75, v32, v33\n\t" \
  "v_max3_u32 v75, v75, v34, v35\n\t" \
  "v_cmp_eq_u32 vcc, -1, v75\n\t" \
  "s_cbranch_vccz LGO3_%=\n\t" \
  "s_add_u32 s68, s68, 1\n\t" \
  "s_waitcnt lgkmcnt(0)\n\t" \
  "s_cmp_gt_u32 s68, s83\n\t" \
  "s_cbranch_scc1 LGV3_%=\n\t" \
  "s_barrier\n\t" \
  "s_waitcnt vmcnt(8)\n\t" \
  "s_branch LG3_%=\n\t" \
  "RWT3_%=f:\n\t" \
  "s_cmp_lt_i32 s73, -2\n\t" \
  "s_cbranch_scc0 RWT3_%=g\n\t" \
  "s_nop 4\n\t" \
  "s_branch RWT3_%=g\n\t" \
  "LGV3_%=:\n\t" \
  "s_mov_b32 s83, 0\n\t" \
  "s_lshl_b32 s60, s36, 4\n\t" \
  "s_or_b32 s60, s60, s48\n\t" \
  "s_or_b32 s60, s60, 805306368\n\t" \
  "v_mov_b32 v76, s60\n\t" \
  "v_mov_b32 v77, 0\n\t" \
  "s_mov_b64 exec, 1\n\t" \
  "buffer_atomic_cmpswap v[76:77], off, s[28:31], s92 sc1\n\t" \
  "s_mov_b64 exec, -1\n\t" \
  "s_branch LGO3_%=\n\t" \
  "LLAST3_%=:\n\t" \
  "s_waitcnt vmcnt(0) lgkmcnt(0)\n\t" \
  "s_barrier\n\t" \
  "LEND_%=:\n\t" \
  ""
typedef unsigned v16u __attribute__((ext_vector_type(16)));
#define CH_WCLOB "a0", "a1", "a2", "a3", "a4", "a5", "a6", "a7", "a8", "a9", "a10", "a11", "a12", "a13", "a14", "a15", "a16", "a17", "a18", "a19", "a20", "a21", "a22", "a23", "a24", "a25", "a26", "a27", "a28", "a29", "a30", "a31", "a32", "a33", "a34", "a35", "a36", "a37", "a38", "a39", "a40", "a41", "a42", "a43", "a44", "a45", "a46", "a47", "a48", "a49", "a50", "a51", "a52", "a53", "a54", "a55", "a56", "a57", "a58", "a59", "a60", "a61", "a62", "a63", "a64", "a65", "a66", "a67", "a68", "a69", "a70", "a71", "a72", "a73", "a74", "a75", "a76", "a77", "a78", "a79", "a80", "a81", "a82", "a83", "a84", "a85", "a86", "a87", "a88", "a89", "a90", "a91", "a92", "a93", "a94", "a95", "a96", "a97", "a98", "a99", "a100", "a101", "a102", "a103", "a104", "a105", "a106", "a107", "a108", "a109", "a110", "a111", "a112", "a113", "a114", "a115", "a116", "a117", "a118", "a119", "a120", "a121", "a122", "a123", "a124", "a125", "a126", "a127", "a128", "a129", "a130", "a131", "a132", "a133", "a134", "a135", "a136", "a137", "a138", "a139", "a140", "a141", "a142", "a143", "a144", "a145", "a146", "a147", "a148", "a149", "a150", "a151", "a152", "a153", "a154", "a155", "a156", "a157", "a158", "a159", "a160", "a161", "a162", "a163", "a164", "a165", "a166", "a167", "a168", "a169", "a170", "a171", "a172", "a173", "a174", "a175", "a176", "a177", "a178", "a179", "a180", "a181", "a182", "a183", "a184", "a185", "a186", "a187", "a188", "a189", "a190", "a191", "a192", "a193", "a194", "a195", "a196", "a197", "a198", "a199", "a200", "a201", "a202", "a203", "a204", "a205", "a206", "a207", "a208", "a209", "a210", "a211", "a212", "a213", "a214", "a215", "a216", "a217", "a218", "a219", "a220", "a221", "a222", "a223", "a224", "a225", "a226", "a227", "a228", "a229", "a230", "a231", "a232", "a233", "a234", "a235", "a236", "a237", "a238", "a239", "a240", "a241", "a242", "a243", "a244", "a245", "a246", "a247", "a248", "a249", "a250", "a251", "a252", "a253", "a254", "a255", "v80", "v81", "v82", "v83", "v84", "v85", "v86", "v87", "v88", "v89", "v90", "v91", "v92", "v93", "v94", "v95", "v96", "v97", "v98", "v99", "v100", "v101", "v102", "v103", "v104", "v105", "v106", "v107", "v108", "v109", "v110", "v111", "v112", "v113", "v114", "v115", "v116", "v117", "v118", "v119", "v120", "v121", "v122", "v123", "v124", "v125", "v126", "v127", "v128", "v129", "v130", "v131", "v132", "v133", "v134", "v135", "v136", "v137", "v138", "v139", "v140", "v141", "v142", "v143", "v144", "v145", "v146", "v147", "v148", "v149", "v150", "v151", "v152", "v153", "v154", "v155", "v156", "v157", "v158", "v159", "v160", "v161", "v162", "v163", "v164", "v165", "v166", "v167", "v168", "v169", "v170", "v171", "v172", "v173", "v174", "v175", "v176", "v177", "v178", "v179", "v180", "v181", "v182", "v183", "v184", "v185", "v186", "v187", "v188", "v189", "v190", "v191", "v192", "v193", "v194", "v195", "v196", "v197", "v198", "v199", "v200", "v201", "v202", "v203", "v204", "v205", "v206", "v207", "v208", "v209", "v210", "v211", "v212", "v213", "v214", "v215", "v216", "v217", "v218", "v219", "v220", "v221", "v222", "v223", "v224", "v225", "v226", "v227", "v228", "v229", "v230", "v231", "v232", "v233", "v234", "v235", "v236", "v237", "v238", "v239", "v240", "v241", "v242", "v243", "v244", "v245", "v246", "v247", "v248", "v249", "v250", "v251", "v252", "v253", "v254", "v255"
#define CH_H(b) (8 * ((b) & 3) + ((b) >> 3))
#define CH_L(b) (((b) >> 2) & 1)
__device__ __forceinline__ void start_work(char* smem, int wk);
__device__ __forceinline__ void chain_wg(char* smem) {
  if (CH_L((int)blockIdx.x)) start_work(smem, CH_H((int)blockIdx.x));
  const MP p = getp();
  const int b = (int)blockIdx.x, h = CH_H(b), L = CH_L(b);
  const int w = __builtin_amdgcn_readfirstlane(threadIdx.x >> 6);
  unsigned lp16 = (threadIdx.x & 63u) * 16u;
  rsrc_t X = mkr(p.xb, XB_BYTES);
  if ((unsigned)(size_t)(__attribute__((address_space(3))) char*)smem != 0u) return;
  const unsigned wsz = DM * HD * 2u - (unsigned)(h * DH) * 2u;
  const rsrc_t RQ = mkr(p.Wq + h * DH, wsz), RK = mkr(p.Wk + h * DH, wsz), RV = mkr(p.Wv + h * DH, wsz), RO = mkr(p.Wo + (long)h * DH * DM, DH * DM * 2u);
  unsigned xcc; asm volatile("s_getreg_b32 %0, hwreg(HW_REG_XCC_ID)" : "=s"(xcc)); xcc &= 3u;
  unsigned d96 = 0u;
  const unsigned c43 = P_FA * PAIR + (unsigned)h * 128u + 32u;
  const unsigned h128 = (unsigned)h * 128u, uw = (unsigned)w, t = uw >> 1, T = uw & 1u, NONE = 0xF0000000u;
  const unsigned u0 = w == 3 ? (unsigned)h >> 2 : (unsigned)((h & 1) + (h >> 3) * 2 + ((h >> 1) & 3) * 8 + (w == 2 ? 32 : 0));
  const bool idle = w == 3 && (h & 3);
  const unsigned c37 = idle ? NONE : (w == 3 ? AGT + u0 * 128u : AGO + (u0 >> 5) * 32768u + (u0 & 31u) * 128u);
  const unsigned c41 = idle ? NONE : (w == 3 ? 524288u + (u0 >> 1) * 16384u + (u0 & 1u) * 4096u : (u0 & 7u) * 16384u + ((u0 >> 3) & 1u) * 4096u + (u0 >> 4) * 131072u);
  const unsigned c42 = AGO + (xcc & 1u) * 16384u + (xcc >> 1) * 4096u;
  const unsigned c46 = (w == 3 ? (h & 7) == 5 && DL3 > -50 : h == 30) ? AGT + 1024u : NONE;
  const unsigned c38 = (unsigned)(3 * h + 1) * PAIR + T * 4096u + t * 2048u, c39 = (P_KV + (unsigned)(h >> 1)) * PAIR + (unsigned)(h & 1) * 4096u;
  const unsigned c45 = P_FB * PAIR + 4096u + (unsigned)(16 + (h & 15)) * 128u;
  unsigned d48 = 0u, d49 = 0u, d53 = SLOT, d86 = 1u, d65 = rtc();
  unsigned mn = 120u;
  v4u stt;
  asm volatile("s_waitcnt vmcnt(0) lgkmcnt(0)\n\t" CH_ASM "LCHEND:"
               : "+{s48}"(d48), "+{s49}"(d49), "+{s53}"(d53), "+{s65}"(d65), "+{s81}"(mn), "+{s86}"(d86), "+{s96}"(d96), "=&{s[76:79]}"(stt), "+{v69}"(lp16), "+{s[28:31]}"(X)
               : [Wq] "s"(RQ), [Wk] "s"(RK), [Wv] "s"(RV), [Wo] "s"(RO), "{s33}"(HT_OFF + 8192u + 256u + (unsigned)b * 4u), "{s36}"(h128), "{s37}"(c37), "{s40}"(w), "{s41}"(c41), "{s42}"(c42), "{s46}"(c46), "{s92}"(TMO_OFF), "{s38}"(c38), "{s39}"(c39), "{s43}"(c43), "{s45}"(c45), "{s32}"(L), "{s26}"(AGT + 1024u + xcc * 128u), "{s27}"(RDY_OFF(0) + (unsigned)((h % 9) * 1024 + (b & 7) * 128)), [ctab] "i"(CTAB_OFF), [dl1l] "i"(DL1 - LATE), [armd] "i"(3 - ARMK), [arms] "i"(ARMK - 3),
                 [cap] "i"(SPIN_CAP)
               : "v0", "v1", "v2", "v3", "v4", "v5", "v6", "v7", "v8", "v9", "v10", "v11", "v12", "v13", "v14", "v15", "v16", "v17", "v18", "v19",
                 "v20", "v21", "v22", "v23", "v24", "v25", "v26", "v27", "v28", "v29", "v30", "v31", "v32", "v33", "v34", "v35", "v36", "v37", "v38", "v39",
                 "v40", "v41", "v42", "v43", "v44", "v45", "v46", "v47", "v48", "v49", "v50", "v51", "v52", "v53", "v54", "v55", "v56", "v57", "v58", "v59",
                 "v60", "v61", "v62", "v63", "v64", "v65", "v66", "v67", "v68", "v70", "v71", "v72", "v73", "v74", "v75", "v76", "v77", "v78", "v79",
                 CH_WCLOB, "s24", "s25", "s90", "s91", "s47", "s87", "s50", "s51", "s52", "s69", "s82", "s85", "s54", "s75", "s80", "s55", "s56", "s57", "s58", "s59", "s60", "s61", "s62", "s63", "s64", "s66", "s67", "s68", "s70", "s71", "s72", "s73", "s74", "s88", "s89",
                 "s83", "s84", "s97", "s98", "s99", "s93", "s94", "s95", "m0", "vcc", "scc", "memory");
}
#define QLEAD 4
#define RONE 2
#define PSP 4
#define PSR 8
#define RDLY 40
#define QLEAD1 2
#define QXW -10
#define QXC QXW
#define TK_QXW "%[qxw]"
#define TK_QXC "%[qxc]"
#define PSG 16
#define TK_PSG CH_S(PSG)
#define PSK 2
#define PSH 2
#define TK_CAP "%[cap]"
#define TK_CTAB "%[ctab]"
#define TK_SG0 CH_S(SG0)
#define TK_PSK CH_S(PSK)
#define TK_PSH CH_S(PSH)
#define AP_ASM \
  "v_lshrrev_b32 v40, 8, v0\n\t" \
  "v_mul_u32_u24 v40, 4608, v40\n\t" \
  "v_and_b32 v41, 0xf0, v0\n\t" \
  "v_lshrrev_b32 v42, 3, v41\n\t" \
  "v_add3_u32 v41, v41, v40, s64\n\t" \
  "v_add3_u32 v42, v42, v40, s65\n\t" \
  "s_mov_b32 s62, 0x05040100\n\t" \
  "s_mov_b32 s63, 0x07060302\n\t" \
  "s_xor_b32 s60, s66, 0\n\t" \
  "s_add_u32 s60, s60, 0\n\t" \
  "s_add_u32 s61, s60, 18432\n\t" \
  "buffer_load_dwordx4 v[48:51], v41, %[W], s60 offen offset:0\n\t" \
  "buffer_load_dwordx4 v[52:55], v41, %[W], s60 offen offset:1152\n\t" \
  "buffer_load_dwordx4 v[56:59], v41, %[W], s60 offen offset:2304\n\t" \
  "buffer_load_dwordx4 v[60:63], v41, %[W], s60 offen offset:3456\n\t" \
  "buffer_load_dwordx4 v[64:67], v41, %[W], s61 offen offset:0\n\t" \
  "buffer_load_dwordx4 v[68:71], v41, %[W], s61 offen offset:1152\n\t" \
  "buffer_load_dwordx4 v[72:75], v41, %[W], s61 offen offset:2304\n\t" \
  "buffer_load_dwordx4 v[76:79], v41, %[W], s61 offen offset:3456\n\t" \
  "s_xor_b32 s60, s66, 0\n\t" \
  "s_add_u32 s60, s60, 36864\n\t" \
  "s_add_u32 s61, s60, 18432\n\t" \
  "buffer_load_dwordx4 v[80:83], v41, %[W], s60 offen offset:0\n\t" \
  "buffer_load_dwordx4 v[84:87], v41, %[W], s60 offen offset:1152\n\t" \
  "buffer_load_dwordx4 v[88:91], v41, %[W], s60 offen offset:2304\n\t" \
  "buffer_load_dwordx4 v[92:95], v41, %[W], s60 offen offset:3456\n\t" \
  "buffer_load_dwordx4 v[96:99], v41, %[W], s61 offen offset:0\n\t" \
  "buffer_load_dwordx4 v[100:103], v41, %[W], s61 offen offset:1152\n\t" \
  "buffer_load_dwordx4 v[104:107], v41, %[W], s61 offen offset:2304\n\t" \
  "buffer_load_dwordx4 v[108:111], v41, %[W], s61 offen offset:3456\n\t" \
  "s_xor_b32 s60, s66, 0\n\t" \
  "s_add_u32 s60, s60, 73728\n\t" \
  "s_add_u32 s61, s60, 18432\n\t" \
  "buffer_load_dwordx4 v[112:115], v41, %[W], s60 offen offset:0\n\t" \
  "buffer_load_dwordx4 v[116:119], v41, %[W], s60 offen offset:1152\n\t" \
  "buffer_load_dwordx4 v[120:123], v41, %[W], s60 offen offset:2304\n\t" \
  "buffer_load_dwordx4 v[124:127], v41, %[W], s60 offen offset:3456\n\t" \
  "buffer_load_dwordx4 v[128:131], v41, %[W], s61 offen offset:0\n\t" \
  "buffer_load_dwordx4 v[132:135], v41, %[W], s61 offen offset:1152\n\t" \
  "buffer_load_dwordx4 v[136:139], v41, %[W], s61 offen offset:2304\n\t" \
  "buffer_load_dwordx4 v[140:143], v41, %[W], s61 offen offset:3456\n\t" \
  "s_xor_b32 s60, s66, 0\n\t" \
  "s_add_u32 s60, s60, 110592\n\t" \
  "s_add_u32 s61, s60, 18432\n\t" \
  "buffer_load_dwordx4 v[144:147], v41, %[W], s60 offen offset:0\n\t" \
  "buffer_load_dwordx4 v[148:151], v41, %[W], s60 offen offset:1152\n\t" \
  "buffer_load_dwordx4 v[152:155], v41, %[W], s60 offen offset:2304\n\t" \
  "buffer_load_dwordx4 v[156:159], v41, %[W], s60 offen offset:3456\n\t" \
  "buffer_load_dwordx4 v[160:163], v41, %[W], s61 offen offset:0\n\t" \
  "buffer_load_dwordx4 v[164:167], v41, %[W], s61 offen offset:1152\n\t" \
  "buffer_load_dwordx4 v[168:171], v41, %[W], s61 offen offset:2304\n\t" \
  "buffer_load_dwordx4 v[172:175], v41, %[W], s61 offen offset:3456\n\t" \
  "s_xor_b32 s60, s66, 147456\n\t" \
  "s_add_u32 s60, s60, 0\n\t" \
  "s_add_u32 s61, s60, 18432\n\t" \
  "buffer_load_dwordx4 v[176:179], v41, %[W], s60 offen offset:0\n\t" \
  "buffer_load_dwordx4 v[180:183], v41, %[W], s60 offen offset:1152\n\t" \
  "buffer_load_dwordx4 v[184:187], v41, %[W], s60 offen offset:2304\n\t" \
  "buffer_load_dwordx4 v[188:191], v41, %[W], s60 offen offset:3456\n\t" \
  "buffer_load_dwordx4 v[192:195], v41, %[W], s61 offen offset:0\n\t" \
  "buffer_load_dwordx4 v[196:199], v41, %[W], s61 offen offset:1152\n\t" \
  "buffer_load_dwordx4 v[200:203], v41, %[W], s61 offen offset:2304\n\t" \
  "buffer_load_dwordx4 v[204:207], v41, %[W], s61 offen offset:3456\n\t" \
  "s_waitcnt vmcnt(38)\n\t" \
  "v_perm_b32 v44, v52, v48, s62\n\t" \
  "s_waitcnt vmcnt(36)\n\t" \
  "v_perm_b32 v45, v60, v56, s62\n\t" \
  "s_waitcnt vmcnt(34)\n\t" \
  "v_perm_b32 v46, v68, v64, s62\n\t" \
  "s_waitcnt vmcnt(32)\n\t" \
  "v_perm_b32 v47, v76, v72, s62\n\t" \
  "v_accvgpr_write_b32 a0, v44\n\t" \
  "v_accvgpr_write_b32 a1, v45\n\t" \
  "v_accvgpr_write_b32 a2, v46\n\t" \
  "v_accvgpr_write_b32 a3, v47\n\t" \
  "v_perm_b32 v44, v52, v48, s63\n\t" \
  "v_perm_b32 v45, v60, v56, s63\n\t" \
  "v_perm_b32 v46, v68, v64, s63\n\t" \
  "v_perm_b32 v47, v76, v72, s63\n\t" \
  "v_accvgpr_write_b32 a4, v44\n\t" \
  "v_accvgpr_write_b32 a5, v45\n\t" \
  "v_accvgpr_write_b32 a6, v46\n\t" \
  "v_accvgpr_write_b32 a7, v47\n\t" \
  "v_perm_b32 v44, v53, v49, s62\n\t" \
  "v_perm_b32 v45, v61, v57, s62\n\t" \
  "v_perm_b32 v46, v69, v65, s62\n\t" \
  "v_perm_b32 v47, v77, v73, s62\n\t" \
  "v_accvgpr_write_b32 a8, v44\n\t" \
  "v_accvgpr_write_b32 a9, v45\n\t" \
  "v_accvgpr_write_b32 a10, v46\n\t" \
  "v_accvgpr_write_b32 a11, v47\n\t" \
  "v_perm_b32 v44, v53, v49, s63\n\t" \
  "v_perm_b32 v45, v61, v57, s63\n\t" \
  "v_perm_b32 v46, v69, v65, s63\n\t" \
  "v_perm_b32 v47, v77, v73, s63\n\t" \
  "v_accvgpr_write_b32 a12, v44\n\t" \
  "v_accvgpr_write_b32 a13, v45\n\t" \
  "v_accvgpr_write_b32 a14, v46\n\t" \
  "v_accvgpr_write_b32 a15, v47\n\t" \
  "v_perm_b32 v44, v54, v50, s62\n\t" \
  "v_perm_b32 v45, v62, v58, s62\n\t" \
  "v_perm_b32 v46, v70, v66, s62\n\t" \
  "v_perm_b32 v47, v78, v74, s62\n\t" \
  "v_accvgpr_write_b32 a16, v44\n\t" \
  "v_accvgpr_write_b32 a17, v45\n\t" \
  "v_accvgpr_write_b32 a18, v46\n\t" \
  "v_accvgpr_write_b32 a19, v47\n\t" \
  "v_perm_b32 v44, v54, v50, s63\n\t" \
  "v_perm_b32 v45, v62, v58, s63\n\t" \
  "v_perm_b32 v46, v70, v66, s63\n\t" \
  "v_perm_b32 v47, v78, v74, s63\n\t" \
  "v_accvgpr_write_b32 a20, v44\n\t" \
  "v_accvgpr_write_b32 a21, v45\n\t" \
  "v_accvgpr_write_b32 a22, v46\n\t" \
  "v_accvgpr_write_b32 a23, v47\n\t" \
  "v_perm_b32 v44, v55, v51, s62\n\t" \
  "v_perm_b32 v45, v63, v59, s62\n\t" \
  "v_perm_b32 v46, v71, v67, s62\n\t" \
  "v_perm_b32 v47, v79, v75, s62\n\t" \
  "v_accvgpr_write_b32 a24, v44\n\t" \
  "v_accvgpr_write_b32 a25, v45\n\t" \
  "v_accvgpr_write_b32 a26, v46\n\t" \
  "v_accvgpr_write_b32 a27, v47\n\t" \
  "v_perm_b32 v44, v55, v51, s63\n\t" \
  "v_perm_b32 v45, v63, v59, s63\n\t" \
  "v_perm_b32 v46, v71, v67, s63\n\t" \
  "v_perm_b32 v47, v79, v75, s63\n\t" \
  "v_accvgpr_write_b32 a28, v44\n\t" \
  "v_accvgpr_write_b32 a29, v45\n\t" \
  "v_accvgpr_write_b32 a30, v46\n\t" \
  "v_accvgpr_write_b32 a31, v47\n\t" \
  "s_xor_b32 s60, s66, 147456\n\t" \
  "s_add_u32 s60, s60, 36864\n\t" \
  "s_add_u32 s61, s60, 18432\n\t" \
  "buffer_load_dwordx4 v[48:51], v41, %[W], s60 offen offset:0\n\t" \
  "buffer_load_dwordx4 v[52:55], v41, %[W], s60 offen offset:1152\n\t" \
  "buffer_load_dwordx4 v[56:59], v41, %[W], s60 offen offset:2304\n\t" \
  "buffer_load_dwordx4 v[60:63], v41, %[W], s60 offen offset:3456\n\t" \
  "buffer_load_dwordx4 v[64:67], v41, %[W], s61 offen offset:0\n\t" \
  "buffer_load_dwordx4 v[68:71], v41, %[W], s61 offen offset:1152\n\t" \
  "buffer_load_dwordx4 v[72:75], v41, %[W], s61 offen offset:2304\n\t" \
  "buffer_load_dwordx4 v[76:79], v41, %[W], s61 offen offset:3456\n\t" \
  "s_waitcnt vmcnt(38)\n\t" \
  "v_perm_b32 v44, v84, v80, s62\n\t" \
  "s_waitcnt vmcnt(36)\n\t" \
  "v_perm_b32 v45, v92, v88, s62\n\t" \
  "s_waitcnt vmcnt(34)\n\t" \
  "v_perm_b32 v46, v100, v96, s62\n\t" \
  "s_waitcnt vmcnt(32)\n\t" \
  "v_perm_b32 v47, v108, v104, s62\n\t" \
  "v_accvgpr_write_b32 a32, v44\n\t" \
  "v_accvgpr_write_b32 a33, v45\n\t" \
  "v_accvgpr_write_b32 a34, v46\n\t" \
  "v_accvgpr_write_b32 a35, v47\n\t" \
  "v_perm_b32 v44, v84, v80, s63\n\t" \
  "v_perm_b32 v45, v92, v88, s63\n\t" \
  "v_perm_b32 v46, v100, v96, s63\n\t" \
  "v_perm_b32 v47, v108, v104, s63\n\t" \
  "v_accvgpr_write_b32 a36, v44\n\t" \
  "v_accvgpr_write_b32 a37, v45\n\t" \
  "v_accvgpr_write_b32 a38, v46\n\t" \
  "v_accvgpr_write_b32 a39, v47\n\t" \
  "v_perm_b32 v44, v85, v81, s62\n\t" \
  "v_perm_b32 v45, v93, v89, s62\n\t" \
  "v_perm_b32 v46, v101, v97, s62\n\t" \
  "v_perm_b32 v47, v109, v105, s62\n\t" \
  "v_accvgpr_write_b32 a40, v44\n\t" \
  "v_accvgpr_write_b32 a41, v45\n\t" \
  "v_accvgpr_write_b32 a42, v46\n\t" \
  "v_accvgpr_write_b32 a43, v47\n\t" \
  "v_perm_b32 v44, v85, v81, s63\n\t" \
  "v_perm_b32 v45, v93, v89, s63\n\t" \
  "v_perm_b32 v46, v101, v97, s63\n\t" \
  "v_perm_b32 v47, v109, v105, s63\n\t" \
  "v_accvgpr_write_b32 a44, v44\n\t" \
  "v_accvgpr_write_b32 a45, v45\n\t" \
  "v_accvgpr_write_b32 a46, v46\n\t" \
  "v_accvgpr_write_b32 a47, v47\n\t" \
  "v_perm_b32 v44, v86, v82, s62\n\t" \
  "v_perm_b32 v45, v94, v90, s62\n\t" \
  "v_perm_b32 v46, v102, v98, s62\n\t" \
  "v_perm_b32 v47, v110, v106, s62\n\t" \
  "v_accvgpr_write_b32 a48, v44\n\t" \
  "v_accvgpr_write_b32 a49, v45\n\t" \
  "v_accvgpr_write_b32 a50, v46\n\t" \
  "v_accvgpr_write_b32 a51, v47\n\t" \
  "v_perm_b32 v44, v86, v82, s63\n\t" \
  "v_perm_b32 v45, v94, v90, s63\n\t" \
  "v_perm_b32 v46, v102, v98, s63\n\t" \
  "v_perm_b32 v47, v110, v106, s63\n\t" \
  "v_accvgpr_write_b32 a52, v44\n\t" \
  "v_accvgpr_write_b32 a53, v45\n\t" \
  "v_accvgpr_write_b32 a54, v46\n\t" \
  "v_accvgpr_write_b32 a55, v47\n\t" \
  "v_perm_b32 v44, v87, v83, s62\n\t" \
  "v_perm_b32 v45, v95, v91, s62\n\t" \
  "v_perm_b32 v46, v103, v99, s62\n\t" \
  "v_perm_b32 v47, v111, v107, s62\n\t" \
  "v_accvgpr_write_b32 a56, v44\n\t" \
  "v_accvgpr_write_b32 a57, v45\n\t" \
  "v_accvgpr_write_b32 a58, v46\n\t" \
  "v_accvgpr_write_b32 a59, v47\n\t" \
  "v_perm_b32 v44, v87, v83, s63\n\t" \
  "v_perm_b32 v45, v95, v91, s63\n\t" \
  "v_perm_b32 v46, v103, v99, s63\n\t" \
  "v_perm_b32 v47, v111, v107, s63\n\t" \
  "v_accvgpr_write_b32 a60, v44\n\t" \
  "v_accvgpr_write_b32 a61, v45\n\t" \
  "v_accvgpr_write_b32 a62, v46\n\t" \
  "v_accvgpr_write_b32 a63, v47\n\t" \
  "s_xor_b32 s60, s66, 147456\n\t" \
  "s_add_u32 s60, s60, 73728\n\t" \
  "s_add_u32 s61, s60, 18432\n\t" \
  "buffer_load_dwordx4 v[80:83], v41, %[W], s60 offen offset:0\n\t" \
  "buffer_load_dwordx4 v[84:87], v41, %[W], s60 offen offset:1152\n\t" \
  "buffer_load_dwordx4 v[88:91], v41, %[W], s60 offen offset:2304\n\t" \
  "buffer_load_dwordx4 v[92:95], v41, %[W], s60 offen offset:3456\n\t" \
  "buffer_load_dwordx4 v[96:99], v41, %[W], s61 offen offset:0\n\t" \
  "buffer_load_dwordx4 v[100:103], v41, %[W], s61 offen offset:1152\n\t" \
  "buffer_load_dwordx4 v[104:107], v41, %[W], s61 offen offset:2304\n\t" \
  "buffer_load_dwordx4 v[108:111], v41, %[W], s61 offen offset:3456\n\t" \
  "s_waitcnt vmcnt(38)\n\t" \
  "v_perm_b32 v44, v116, v112, s62\n\t" \
  "s_waitcnt vmcnt(36)\n\t" \
  "v_perm_b32 v45, v124, v120, s62\n\t" \
  "s_waitcnt vmcnt(34)\n\t" \
  "v_perm_b32 v46, v132, v128, s62\n\t" \
  "s_waitcnt vmcnt(32)\n\t" \
  "v_perm_b32 v47, v140, v136, s62\n\t" \
  "v_accvgpr_write_b32 a64, v44\n\t" \
  "v_accvgpr_write_b32 a65, v45\n\t" \
  "v_accvgpr_write_b32 a66, v46\n\t" \
  "v_accvgpr_write_b32 a67, v47\n\t" \
  "v_perm_b32 v44, v116, v112, s63\n\t" \
  "v_perm_b32 v45, v124, v120, s63\n\t" \
  "v_perm_b32 v46, v132, v128, s63\n\t" \
  "v_perm_b32 v47, v140, v136, s63\n\t" \
  "v_accvgpr_write_b32 a68, v44\n\t" \
  "v_accvgpr_write_b32 a69, v45\n\t" \
  "v_accvgpr_write_b32 a70, v46\n\t" \
  "v_accvgpr_write_b32 a71, v47\n\t" \
  "v_perm_b32 v44, v117, v113, s62\n\t" \
  "v_perm_b32 v45, v125, v121, s62\n\t" \
  "v_perm_b32 v46, v133, v129, s62\n\t" \
  "v_perm_b32 v47, v141, v137, s62\n\t" \
  "v_accvgpr_write_b32 a72, v44\n\t" \
  "v_accvgpr_write_b32 a73, v45\n\t" \
  "v_accvgpr_write_b32 a74, v46\n\t" \
  "v_accvgpr_write_b32 a75, v47\n\t" \
  "v_perm_b32 v44, v117, v113, s63\n\t" \
  "v_perm_b32 v45, v125, v121, s63\n\t" \
  "v_perm_b32 v46, v133, v129, s63\n\t" \
  "v_perm_b32 v47, v141, v137, s63\n\t" \
  "v_accvgpr_write_b32 a76, v44\n\t" \
  "v_accvgpr_write_b32 a77, v45\n\t" \
  "v_accvgpr_write_b32 a78, v46\n\t" \
  "v_accvgpr_write_b32 a79, v47\n\t" \
  "v_perm_b32 v44, v118, v114, s62\n\t" \
  "v_perm_b32 v45, v126, v122, s62\n\t" \
  "v_perm_b32 v46, v134, v130, s62\n\t" \
  "v_perm_b32 v47, v142, v138, s62\n\t" \
  "v_accvgpr_write_b32 a80, v44\n\t" \
  "v_accvgpr_write_b32 a81, v45\n\t" \
  "v_accvgpr_write_b32 a82, v46\n\t" \
  "v_accvgpr_write_b32 a83, v47\n\t" \
  "v_perm_b32 v44, v118, v114, s63\n\t" \
  "v_perm_b32 v45, v126, v122, s63\n\t" \
  "v_perm_b32 v46, v134, v130, s63\n\t" \
  "v_perm_b32 v47, v142, v138, s63\n\t" \
  "v_accvgpr_write_b32 a84, v44\n\t" \
  "v_accvgpr_write_b32 a85, v45\n\t" \
  "v_accvgpr_write_b32 a86, v46\n\t" \
  "v_accvgpr_write_b32 a87, v47\n\t" \
  "v_perm_b32 v44, v119, v115, s62\n\t" \
  "v_perm_b32 v45, v127, v123, s62\n\t" \
  "v_perm_b32 v46, v135, v131, s62\n\t" \
  "v_perm_b32 v47, v143, v139, s62\n\t" \
  "v_accvgpr_write_b32 a88, v44\n\t" \
  "v_accvgpr_write_b32 a89, v45\n\t" \
  "v_accvgpr_write_b32 a90, v46\n\t" \
  "v_accvgpr_write_b32 a91, v47\n\t" \
  "v_perm_b32 v44, v119, v115, s63\n\t" \
  "v_perm_b32 v45, v127, v123, s63\n\t" \
  "v_perm_b32 v46, v135, v131, s63\n\t" \
  "v_perm_b32 v47, v143, v139, s63\n\t" \
  "v_accvgpr_write_b32 a92, v44\n\t" \
  "v_accvgpr_write_b32 a93, v45\n\t" \
  "v_accvgpr_write_b32 a94, v46\n\t" \
  "v_accvgpr_write_b32 a95, v47\n\t" \
  "s_xor_b32 s60, s66, 147456\n\t" \
  "s_add_u32 s60, s60, 110592\n\t" \
  "s_add_u32 s61, s60, 18432\n\t" \
  "buffer_load_dwordx4 v[112:115], v41, %[W], s60 offen offset:0\n\t" \
  "buffer_load_dwordx4 v[116:119], v41, %[W], s60 offen offset:1152\n\t" \
  "buffer_load_dwordx4 v[120:123], v41, %[W], s60 offen offset:2304\n\t" \
  "buffer_load_dwordx4 v[124:127], v41, %[W], s60 offen offset:3456\n\t" \
  "buffer_load_dwordx4 v[128:131], v41, %[W], s61 offen offset:0\n\t" \
  "buffer_load_dwordx4 v[132:135], v41, %[W], s61 offen offset:1152\n\t" \
  "buffer_load_dwordx4 v[136:139], v41, %[W], s61 offen offset:2304\n\t" \
  "buffer_load_dwordx4 v[140:143], v41, %[W], s61 offen offset:3456\n\t" \
  "s_waitcnt vmcnt(38)\n\t" \
  "v_perm_b32 v44, v148, v144, s62\n\t" \
  "s_waitcnt vmcnt(36)\n\t" \
  "v_perm_b32 v45, v156, v152, s62\n\t" \
  "s_waitcnt vmcnt(34)\n\t" \
  "v_perm_b32 v46, v164, v160, s62\n\t" \
  "s_waitcnt vmcnt(32)\n\t" \
  "v_perm_b32 v47, v172, v168, s62\n\t" \
  "v_accvgpr_write_b32 a96, v44\n\t" \
  "v_accvgpr_write_b32 a97, v45\n\t" \
  "v_accvgpr_write_b32 a98, v46\n\t" \
  "v_accvgpr_write_b32 a99, v47\n\t" \
  "v_perm_b32 v44, v148, v144, s63\n\t" \
  "v_perm_b32 v45, v156, v152, s63\n\t" \
  "v_perm_b32 v46, v164, v160, s63\n\t" \
  "v_perm_b32 v47, v172, v168, s63\n\t" \
  "v_accvgpr_write_b32 a100, v44\n\t" \
  "v_accvgpr_write_b32 a101, v45\n\t" \
  "v_accvgpr_write_b32 a102, v46\n\t" \
  "v_accvgpr_write_b32 a103, v47\n\t" \
  "v_perm_b32 v44, v149, v145, s62\n\t" \
  "v_perm_b32 v45, v157, v153, s62\n\t" \
  "v_perm_b32 v46, v165, v161, s62\n\t" \
  "v_perm_b32 v47, v173, v169, s62\n\t" \
  "v_accvgpr_write_b32 a104, v44\n\t" \
  "v_accvgpr_write_b32 a105, v45\n\t" \
  "v_accvgpr_write_b32 a106, v46\n\t" \
  "v_accvgpr_write_b32 a107, v47\n\t" \
  "v_perm_b32 v44, v149, v145, s63\n\t" \
  "v_perm_b32 v45, v157, v153, s63\n\t" \
  "v_perm_b32 v46, v165, v161, s63\n\t" \
  "v_perm_b32 v47, v173, v169, s63\n\t" \
  "v_accvgpr_write_b32 a108, v44\n\t" \
  "v_accvgpr_write_b32 a109, v45\n\t" \
  "v_accvgpr_write_b32 a110, v46\n\t" \
  "v_accvgpr_write_b32 a111, v47\n\t" \
  "v_perm_b32 v44, v150, v146, s62\n\t" \
  "v_perm_b32 v45, v158, v154, s62\n\t" \
  "v_perm_b32 v46, v166, v162, s62\n\t" \
  "v_perm_b32 v47, v174, v170, s62\n\t" \
  "v_accvgpr_write_b32 a112, v44\n\t" \
  "v_accvgpr_write_b32 a113, v45\n\t" \
  "v_accvgpr_write_b32 a114, v46\n\t" \
  "v_accvgpr_write_b32 a115, v47\n\t" \
  "v_perm_b32 v44, v150, v146, s63\n\t" \
  "v_perm_b32 v45, v158, v154, s63\n\t" \
  "v_perm_b32 v46, v166, v162, s63\n\t" \
  "v_perm_b32 v47, v174, v170, s63\n\t" \
  "v_accvgpr_write_b32 a116, v44\n\t" \
  "v_accvgpr_write_b32 a117, v45\n\t" \
  "v_accvgpr_write_b32 a118, v46\n\t" \
  "v_accvgpr_write_b32 a119, v47\n\t" \
  "v_perm_b32 v44, v151, v147, s62\n\t" \
  "v_perm_b32 v45, v159, v155, s62\n\t" \
  "v_perm_b32 v46, v167, v163, s62\n\t" \
  "v_perm_b32 v47, v175, v171, s62\n\t" \
  "v_accvgpr_write_b32 a120, v44\n\t" \
  "v_accvgpr_write_b32 a121, v45\n\t" \
  "v_accvgpr_write_b32 a122, v46\n\t" \
  "v_accvgpr_write_b32 a123, v47\n\t" \
  "v_perm_b32 v44, v151, v147, s63\n\t" \
  "v_perm_b32 v45, v159, v155, s63\n\t" \
  "v_perm_b32 v46, v167, v163, s63\n\t" \
  "v_perm_b32 v47, v175, v171, s63\n\t" \
  "v_accvgpr_write_b32 a124, v44\n\t" \
  "v_accvgpr_write_b32 a125, v45\n\t" \
  "v_accvgpr_write_b32 a126, v46\n\t" \
  "v_accvgpr_write_b32 a127, v47\n\t" \
  "s_xor_b32 s60, s66, 0\n\t" \
  "s_add_u32 s60, s60, 0\n\t" \
  "s_add_u32 s61, s60, 18432\n\t" \
  "buffer_load_ushort v224, v42, %[W], s60 offen offset:0\n\t" \
  "buffer_load_ushort v144, v42, %[W], s60 offen offset:1152\n\t" \
  "buffer_load_ushort v225, v42, %[W], s60 offen offset:2304\n\t" \
  "buffer_load_ushort v145, v42, %[W], s60 offen offset:3456\n\t" \
  "buffer_load_ushort v226, v42, %[W], s61 offen offset:0\n\t" \
  "buffer_load_ushort v146, v42, %[W], s61 offen offset:1152\n\t" \
  "buffer_load_ushort v227, v42, %[W], s61 offen offset:2304\n\t" \
  "buffer_load_ushort v147, v42, %[W], s61 offen offset:3456\n\t" \
  "s_xor_b32 s60, s66, 0\n\t" \
  "s_add_u32 s60, s60, 36864\n\t" \
  "s_add_u32 s61, s60, 18432\n\t" \
  "buffer_load_ushort v228, v42, %[W], s60 offen offset:0\n\t" \
  "buffer_load_ushort v148, v42, %[W], s60 offen offset:1152\n\t" \
  "buffer_load_ushort v229, v42, %[W], s60 offen offset:2304\n\t" \
  "buffer_load_ushort v149, v42, %[W], s60 offen offset:3456\n\t" \
  "buffer_load_ushort v230, v42, %[W], s61 offen offset:0\n\t" \
  "buffer_load_ushort v150, v42, %[W], s61 offen offset:1152\n\t" \
  "buffer_load_ushort v231, v42, %[W], s61 offen offset:2304\n\t" \
  "buffer_load_ushort v151, v42, %[W], s61 offen offset:3456\n\t" \
  "s_waitcnt vmcnt(46)\n\t" \
  "v_perm_b32 v44, v180, v176, s62\n\t" \
  "s_waitcnt vmcnt(44)\n\t" \
  "v_perm_b32 v45, v188, v184, s62\n\t" \
  "s_waitcnt vmcnt(42)\n\t" \
  "v_perm_b32 v46, v196, v192, s62\n\t" \
  "s_waitcnt vmcnt(40)\n\t" \
  "v_perm_b32 v47, v204, v200, s62\n\t" \
  "v_accvgpr_write_b32 a128, v44\n\t" \
  "v_accvgpr_write_b32 a129, v45\n\t" \
  "v_accvgpr_write_b32 a130, v46\n\t" \
  "v_accvgpr_write_b32 a131, v47\n\t" \
  "v_perm_b32 v44, v180, v176, s63\n\t" \
  "v_perm_b32 v45, v188, v184, s63\n\t" \
  "v_perm_b32 v46, v196, v192, s63\n\t" \
  "v_perm_b32 v47, v204, v200, s63\n\t" \
  "v_accvgpr_write_b32 a132, v44\n\t" \
  "v_accvgpr_write_b32 a133, v45\n\t" \
  "v_accvgpr_write_b32 a134, v46\n\t" \
  "v_accvgpr_write_b32 a135, v47\n\t" \
  "v_perm_b32 v44, v181, v177, s62\n\t" \
  "v_perm_b32 v45, v189, v185, s62\n\t" \
  "v_perm_b32 v46, v197, v193, s62\n\t" \
  "v_perm_b32 v47, v205, v201, s62\n\t" \
  "v_accvgpr_write_b32 a136, v44\n\t" \
  "v_accvgpr_write_b32 a137, v45\n\t" \
  "v_accvgpr_write_b32 a138, v46\n\t" \
  "v_accvgpr_write_b32 a139, v47\n\t" \
  "v_perm_b32 v44, v181, v177, s63\n\t" \
  "v_perm_b32 v45, v189, v185, s63\n\t" \
  "v_perm_b32 v46, v197, v193, s63\n\t" \
  "v_perm_b32 v47, v205, v201, s63\n\t" \
  "v_accvgpr_write_b32 a140, v44\n\t" \
  "v_accvgpr_write_b32 a141, v45\n\t" \
  "v_accvgpr_write_b32 a142, v46\n\t" \
  "v_accvgpr_write_b32 a143, v47\n\t" \
  "v_perm_b32 v44, v182, v178, s62\n\t" \
  "v_perm_b32 v45, v190, v186, s62\n\t" \
  "v_perm_b32 v46, v198, v194, s62\n\t" \
  "v_perm_b32 v47, v206, v202, s62\n\t" \
  "v_accvgpr_write_b32 a144, v44\n\t" \
  "v_accvgpr_write_b32 a145, v45\n\t" \
  "v_accvgpr_write_b32 a146, v46\n\t" \
  "v_accvgpr_write_b32 a147, v47\n\t" \
  "v_perm_b32 v44, v182, v178, s63\n\t" \
  "v_perm_b32 v45, v190, v186, s63\n\t" \
  "v_perm_b32 v46, v198, v194, s63\n\t" \
  "v_perm_b32 v47, v206, v202, s63\n\t" \
  "v_accvgpr_write_b32 a148, v44\n\t" \
  "v_accvgpr_write_b32 a149, v45\n\t" \
  "v_accvgpr_write_b32 a150, v46\n\t" \
  "v_accvgpr_write_b32 a151, v47\n\t" \
  "v_perm_b32 v44, v183, v179, s62\n\t" \
  "v_perm_b32 v45, v191, v187, s62\n\t" \
  "v_perm_b32 v46, v199, v195, s62\n\t" \
  "v_perm_b32 v47, v207, v203, s62\n\t" \
  "v_accvgpr_write_b32 a152, v44\n\t" \
  "v_accvgpr_write_b32 a153, v45\n\t" \
  "v_accvgpr_write_b32 a154, v46\n\t" \
  "v_accvgpr_write_b32 a155, v47\n\t" \
  "v_perm_b32 v44, v183, v179, s63\n\t" \
  "v_perm_b32 v45, v191, v187, s63\n\t" \
  "v_perm_b32 v46, v199, v195, s63\n\t" \
  "v_perm_b32 v47, v207, v203, s63\n\t" \
  "v_accvgpr_write_b32 a156, v44\n\t" \
  "v_accvgpr_write_b32 a157, v45\n\t" \
  "v_accvgpr_write_b32 a158, v46\n\t" \
  "v_accvgpr_write_b32 a159, v47\n\t" \
  "s_xor_b32 s60, s66, 0\n\t" \
  "s_add_u32 s60, s60, 73728\n\t" \
  "s_add_u32 s61, s60, 18432\n\t" \
  "buffer_load_ushort v232, v42, %[W], s60 offen offset:0\n\t" \
  "buffer_load_ushort v152, v42, %[W], s60 offen offset:1152\n\t" \
  "buffer_load_ushort v233, v42, %[W], s60 offen offset:2304\n\t" \
  "buffer_load_ushort v153, v42, %[W], s60 offen offset:3456\n\t" \
  "buffer_load_ushort v234, v42, %[W], s61 offen offset:0\n\t" \
  "buffer_load_ushort v154, v42, %[W], s61 offen offset:1152\n\t" \
  "buffer_load_ushort v235, v42, %[W], s61 offen offset:2304\n\t" \
  "buffer_load_ushort v155, v42, %[W], s61 offen offset:3456\n\t" \
  "s_xor_b32 s60, s66, 0\n\t" \
  "s_add_u32 s60, s60, 110592\n\t" \
  "s_add_u32 s61, s60, 18432\n\t" \
  "buffer_load_ushort v236, v42, %[W], s60 offen offset:0\n\t" \
  "buffer_load_ushort v156, v42, %[W], s60 offen offset:1152\n\t" \
  "buffer_load_ushort v237, v42, %[W], s60 offen offset:2304\n\t" \
  "buffer_load_ushort v157, v42, %[W], s60 offen offset:3456\n\t" \
  "buffer_load_ushort v238, v42, %[W], s61 offen offset:0\n\t" \
  "buffer_load_ushort v158, v42, %[W], s61 offen offset:1152\n\t" \
  "buffer_load_ushort v239, v42, %[W], s61 offen offset:2304\n\t" \
  "buffer_load_ushort v159, v42, %[W], s61 offen offset:3456\n\t" \
  "s_waitcnt vmcnt(54)\n\t" \
  "v_perm_b32 v44, v52, v48, s62\n\t" \
  "s_waitcnt vmcnt(52)\n\t" \
  "v_perm_b32 v45, v60, v56, s62\n\t" \
  "s_waitcnt vmcnt(50)\n\t" \
  "v_perm_b32 v46, v68, v64, s62\n\t" \
  "s_waitcnt vmcnt(48)\n\t" \
  "v_perm_b32 v47, v76, v72, s62\n\t" \
  "v_accvgpr_write_b32 a160, v44\n\t" \
  "v_accvgpr_write_b32 a161, v45\n\t" \
  "v_accvgpr_write_b32 a162, v46\n\t" \
  "v_accvgpr_write_b32 a163, v47\n\t" \
  "v_perm_b32 v44, v52, v48, s63\n\t" \
  "v_perm_b32 v45, v60, v56, s63\n\t" \
  "v_perm_b32 v46, v68, v64, s63\n\t" \
  "v_perm_b32 v47, v76, v72, s63\n\t" \
  "v_accvgpr_write_b32 a164, v44\n\t" \
  "v_accvgpr_write_b32 a165, v45\n\t" \
  "v_accvgpr_write_b32 a166, v46\n\t" \
  "v_accvgpr_write_b32 a167, v47\n\t" \
  "v_perm_b32 v44, v53, v49, s62\n\t" \
  "v_perm_b32 v45, v61, v57, s62\n\t" \
  "v_perm_b32 v46, v69, v65, s62\n\t" \
  "v_perm_b32 v47, v77, v73, s62\n\t" \
  "v_accvgpr_write_b32 a168, v44\n\t" \
  "v_accvgpr_write_b32 a169, v45\n\t" \
  "v_accvgpr_write_b32 a170, v46\n\t" \
  "v_accvgpr_write_b32 a171, v47\n\t" \
  "v_perm_b32 v44, v53, v49, s63\n\t" \
  "v_perm_b32 v45, v61, v57, s63\n\t" \
  "v_perm_b32 v46, v69, v65, s63\n\t" \
  "v_perm_b32 v47, v77, v73, s63\n\t" \
  "v_accvgpr_write_b32 a172, v44\n\t" \
  "v_accvgpr_write_b32 a173, v45\n\t" \
  "v_accvgpr_write_b32 a174, v46\n\t" \
  "v_accvgpr_write_b32 a175, v47\n\t" \
  "v_perm_b32 v44, v54, v50, s62\n\t" \
  "v_perm_b32 v45, v62, v58, s62\n\t" \
  "v_perm_b32 v46, v70, v66, s62\n\t" \
  "v_perm_b32 v47, v78, v74, s62\n\t" \
  "v_accvgpr_write_b32 a176, v44\n\t" \
  "v_accvgpr_write_b32 a177, v45\n\t" \
  "v_accvgpr_write_b32 a178, v46\n\t" \
  "v_accvgpr_write_b32 a179, v47\n\t" \
  "v_perm_b32 v44, v54, v50, s63\n\t" \
  "v_perm_b32 v45, v62, v58, s63\n\t" \
  "v_perm_b32 v46, v70, v66, s63\n\t" \
  "v_perm_b32 v47, v78, v74, s63\n\t" \
  "v_accvgpr_write_b32 a180, v44\n\t" \
  "v_accvgpr_write_b32 a181, v45\n\t" \
  "v_accvgpr_write_b32 a182, v46\n\t" \
  "v_accvgpr_write_b32 a183, v47\n\t" \
  "v_perm_b32 v44, v55, v51, s62\n\t" \
  "v_perm_b32 v45, v63, v59, s62\n\t" \
  "v_perm_b32 v46, v71, v67, s62\n\t" \
  "v_perm_b32 v47, v79, v75, s62\n\t" \
  "v_accvgpr_write_b32 a184, v44\n\t" \
  "v_accvgpr_write_b32 a185, v45\n\t" \
  "v_accvgpr_write_b32 a186, v46\n\t" \
  "v_accvgpr_write_b32 a187, v47\n\t" \
  "v_perm_b32 v44, v55, v51, s63\n\t" \
  "v_perm_b32 v45, v63, v59, s63\n\t" \
  "v_perm_b32 v46, v71, v67, s63\n\t" \
  "v_perm_b32 v47, v79, v75, s63\n\t" \
  "v_accvgpr_write_b32 a188, v44\n\t" \
  "v_accvgpr_write_b32 a189, v45\n\t" \
  "v_accvgpr_write_b32 a190, v46\n\t" \
  "v_accvgpr_write_b32 a191, v47\n\t" \
  "s_xor_b32 s60, s66, 147456\n\t" \
  "s_add_u32 s60, s60, 0\n\t" \
  "s_add_u32 s61, s60, 18432\n\t" \
  "buffer_load_ushort v240, v42, %[W], s60 offen offset:0\n\t" \
  "buffer_load_ushort v160, v42, %[W], s60 offen offset:1152\n\t" \
  "buffer_load_ushort v241, v42, %[W], s60 offen offset:2304\n\t" \
  "buffer_load_ushort v161, v42, %[W], s60 offen offset:3456\n\t" \
  "buffer_load_ushort v242, v42, %[W], s61 offen offset:0\n\t" \
  "buffer_load_ushort v162, v42, %[W], s61 offen offset:1152\n\t" \
  "buffer_load_ushort v243, v42, %[W], s61 offen offset:2304\n\t" \
  "buffer_load_ushort v163, v42, %[W], s61 offen offset:3456\n\t" \
  "s_waitcnt vmcnt(54)\n\t" \
  "v_perm_b32 v44, v84, v80, s62\n\t" \
  "s_waitcnt vmcnt(52)\n\t" \
  "v_perm_b32 v45, v92, v88, s62\n\t" \
  "s_waitcnt vmcnt(50)\n\t" \
  "v_perm_b32 v46, v100, v96, s62\n\t" \
  "s_waitcnt vmcnt(48)\n\t" \
  "v_perm_b32 v47, v108, v104, s62\n\t" \
  "v_accvgpr_write_b32 a192, v44\n\t" \
  "v_accvgpr_write_b32 a193, v45\n\t" \
  "v_accvgpr_write_b32 a194, v46\n\t" \
  "v_accvgpr_write_b32 a195, v47\n\t" \
  "v_perm_b32 v44, v84, v80, s63\n\t" \
  "v_perm_b32 v45, v92, v88, s63\n\t" \
  "v_perm_b32 v46, v100, v96, s63\n\t" \
  "v_perm_b32 v47, v108, v104, s63\n\t" \
  "v_accvgpr_write_b32 a196, v44\n\t" \
  "v_accvgpr_write_b32 a197, v45\n\t" \
  "v_accvgpr_write_b32 a198, v46\n\t" \
  "v_accvgpr_write_b32 a199, v47\n\t" \
  "v_perm_b32 v44, v85, v81, s62\n\t" \
  "v_perm_b32 v45, v93, v89, s62\n\t" \
  "v_perm_b32 v46, v101, v97, s62\n\t" \
  "v_perm_b32 v47, v109, v105, s62\n\t" \
  "v_accvgpr_write_b32 a200, v44\n\t" \
  "v_accvgpr_write_b32 a201, v45\n\t" \
  "v_accvgpr_write_b32 a202, v46\n\t" \
  "v_accvgpr_write_b32 a203, v47\n\t" \
  "v_perm_b32 v44, v85, v81, s63\n\t" \
  "v_perm_b32 v45, v93, v89, s63\n\t" \
  "v_perm_b32 v46, v101, v97, s63\n\t" \
  "v_perm_b32 v47, v109, v105, s63\n\t" \
  "v_accvgpr_write_b32 a204, v44\n\t" \
  "v_accvgpr_write_b32 a205, v45\n\t" \
  "v_accvgpr_write_b32 a206, v46\n\t" \
  "v_accvgpr_write_b32 a207, v47\n\t" \
  "v_perm_b32 v44, v86, v82, s62\n\t" \
  "v_perm_b32 v45, v94, v90, s62\n\t" \
  "v_perm_b32 v46, v102, v98, s62\n\t" \
  "v_perm_b32 v47, v110, v106, s62\n\t" \
  "v_accvgpr_write_b32 a208, v44\n\t" \
  "v_accvgpr_write_b32 a209, v45\n\t" \
  "v_accvgpr_write_b32 a210, v46\n\t" \
  "v_accvgpr_write_b32 a211, v47\n\t" \
  "v_perm_b32 v44, v86, v82, s63\n\t" \
  "v_perm_b32 v45, v94, v90, s63\n\t" \
  "v_perm_b32 v46, v102, v98, s63\n\t" \
  "v_perm_b32 v47, v110, v106, s63\n\t" \
  "v_accvgpr_write_b32 a212, v44\n\t" \
  "v_accvgpr_write_b32 a213, v45\n\t" \
  "v_accvgpr_write_b32 a214, v46\n\t" \
  "v_accvgpr_write_b32 a215, v47\n\t" \
  "v_perm_b32 v44, v87, v83, s62\n\t" \
  "v_perm_b32 v45, v95, v91, s62\n\t" \
  "v_perm_b32 v46, v103, v99, s62\n\t" \
  "v_perm_b32 v47, v111, v107, s62\n\t" \
  "v_accvgpr_write_b32 a216, v44\n\t" \
  "v_accvgpr_write_b32 a217, v45\n\t" \
  "v_accvgpr_write_b32 a218, v46\n\t" \
  "v_accvgpr_write_b32 a219, v47\n\t" \
  "v_perm_b32 v44, v87, v83, s63\n\t" \
  "v_perm_b32 v45, v95, v91, s63\n\t" \
  "v_perm_b32 v46, v103, v99, s63\n\t" \
  "v_perm_b32 v47, v111, v107, s63\n\t" \
  "v_accvgpr_write_b32 a220, v44\n\t" \
  "v_accvgpr_write_b32 a221, v45\n\t" \
  "v_accvgpr_write_b32 a222, v46\n\t" \
  "v_accvgpr_write_b32 a223, v47\n\t" \
  "s_xor_b32 s60, s66, 147456\n\t" \
  "s_add_u32 s60, s60, 36864\n\t" \
  "s_add_u32 s61, s60, 18432\n\t" \
  "buffer_load_ushort v244, v42, %[W], s60 offen offset:0\n\t" \
  "buffer_load_ushort v164, v42, %[W], s60 offen offset:1152\n\t" \
  "buffer_load_ushort v245, v42, %[W], s60 offen offset:2304\n\t" \
  "buffer_load_ushort v165, v42, %[W], s60 offen offset:3456\n\t" \
  "buffer_load_ushort v246, v42, %[W], s61 offen offset:0\n\t" \
  "buffer_load_ushort v166, v42, %[W], s61 offen offset:1152\n\t" \
  "buffer_load_ushort v247, v42, %[W], s61 offen offset:2304\n\t" \
  "buffer_load_ushort v167, v42, %[W], s61 offen offset:3456\n\t" \
  "s_waitcnt vmcnt(54)\n\t" \
  "v_perm_b32 v44, v116, v112, s62\n\t" \
  "s_waitcnt vmcnt(52)\n\t" \
  "v_perm_b32 v45, v124, v120, s62\n\t" \
  "s_waitcnt vmcnt(50)\n\t" \
  "v_perm_b32 v46, v132, v128, s62\n\t" \
  "s_waitcnt vmcnt(48)\n\t" \
  "v_perm_b32 v47, v140, v136, s62\n\t" \
  "v_accvgpr_write_b32 a224, v44\n\t" \
  "v_accvgpr_write_b32 a225, v45\n\t" \
  "v_accvgpr_write_b32 a226, v46\n\t" \
  "v_accvgpr_write_b32 a227, v47\n\t" \
  "v_perm_b32 v44, v116, v112, s63\n\t" \
  "v_perm_b32 v45, v124, v120, s63\n\t" \
  "v_perm_b32 v46, v132, v128, s63\n\t" \
  "v_perm_b32 v47, v140, v136, s63\n\t" \
  "v_accvgpr_write_b32 a228, v44\n\t" \
  "v_accvgpr_write_b32 a229, v45\n\t" \
  "v_accvgpr_write_b32 a230, v46\n\t" \
  "v_accvgpr_write_b32 a231, v47\n\t" \
  "v_perm_b32 v44, v117, v113, s62\n\t" \
  "v_perm_b32 v45, v125, v121, s62\n\t" \
  "v_perm_b32 v46, v133, v129, s62\n\t" \
  "v_perm_b32 v47, v141, v137, s62\n\t" \
  "v_accvgpr_write_b32 a232, v44\n\t" \
  "v_accvgpr_write_b32 a233, v45\n\t" \
  "v_accvgpr_write_b32 a234, v46\n\t" \
  "v_accvgpr_write_b32 a235, v47\n\t" \
  "v_perm_b32 v44, v117, v113, s63\n\t" \
  "v_perm_b32 v45, v125, v121, s63\n\t" \
  "v_perm_b32 v46, v133, v129, s63\n\t" \
  "v_perm_b32 v47, v141, v137, s63\n\t" \
  "v_accvgpr_write_b32 a236, v44\n\t" \
  "v_accvgpr_write_b32 a237, v45\n\t" \
  "v_accvgpr_write_b32 a238, v46\n\t" \
  "v_accvgpr_write_b32 a239, v47\n\t" \
  "v_perm_b32 v44, v118, v114, s62\n\t" \
  "v_perm_b32 v45, v126, v122, s62\n\t" \
  "v_perm_b32 v46, v134, v130, s62\n\t" \
  "v_perm_b32 v47, v142, v138, s62\n\t" \
  "v_accvgpr_write_b32 a240, v44\n\t" \
  "v_accvgpr_write_b32 a241, v45\n\t" \
  "v_accvgpr_write_b32 a242, v46\n\t" \
  "v_accvgpr_write_b32 a243, v47\n\t" \
  "v_perm_b32 v44, v118, v114, s63\n\t" \
  "v_perm_b32 v45, v126, v122, s63\n\t" \
  "v_perm_b32 v46, v134, v130, s63\n\t" \
  "v_perm_b32 v47, v142, v138, s63\n\t" \
  "v_accvgpr_write_b32 a244, v44\n\t" \
  "v_accvgpr_write_b32 a245, v45\n\t" \
  "v_accvgpr_write_b32 a246, v46\n\t" \
  "v_accvgpr_write_b32 a247, v47\n\t" \
  "v_perm_b32 v44, v119, v115, s62\n\t" \
  "v_perm_b32 v45, v127, v123, s62\n\t" \
  "v_perm_b32 v46, v135, v131, s62\n\t" \
  "v_perm_b32 v47, v143, v139, s62\n\t" \
  "v_accvgpr_write_b32 a248, v44\n\t" \
  "v_accvgpr_write_b32 a249, v45\n\t" \
  "v_accvgpr_write_b32 a250, v46\n\t" \
  "v_accvgpr_write_b32 a251, v47\n\t" \
  "v_perm_b32 v44, v119, v115, s63\n\t" \
  "v_perm_b32 v45, v127, v123, s63\n\t" \
  "v_perm_b32 v46, v135, v131, s63\n\t" \
  "v_perm_b32 v47, v143, v139, s63\n\t" \
  "v_accvgpr_write_b32 a252, v44\n\t" \
  "v_accvgpr_write_b32 a253, v45\n\t" \
  "v_accvgpr_write_b32 a254, v46\n\t" \
  "v_accvgpr_write_b32 a255, v47\n\t" \
  "s_xor_b32 s60, s66, 147456\n\t" \
  "s_add_u32 s60, s60, 73728\n\t" \
  "s_add_u32 s61, s60, 18432\n\t" \
  "buffer_load_ushort v248, v42, %[W], s60 offen offset:0\n\t" \
  "buffer_load_ushort v168, v42, %[W], s60 offen offset:1152\n\t" \
  "buffer_load_ushort v249, v42, %[W], s60 offen offset:2304\n\t" \
  "buffer_load_ushort v169, v42, %[W], s60 offen offset:3456\n\t" \
  "buffer_load_ushort v250, v42, %[W], s61 offen offset:0\n\t" \
  "buffer_load_ushort v170, v42, %[W], s61 offen offset:1152\n\t" \
  "buffer_load_ushort v251, v42, %[W], s61 offen offset:2304\n\t" \
  "buffer_load_ushort v171, v42, %[W], s61 offen offset:3456\n\t" \
  "s_xor_b32 s60, s66, 147456\n\t" \
  "s_add_u32 s60, s60, 110592\n\t" \
  "s_add_u32 s61, s60, 18432\n\t" \
  "s_waitcnt vmcnt(40)\n\t" \
  "buffer_load_ushort v252, v42, %[W], s60 offen offset:0\n\t" \
  "buffer_load_ushort v172, v42, %[W], s60 offen offset:1152\n\t" \
  "buffer_load_ushort v253, v42, %[W], s60 offen offset:2304\n\t" \
  "buffer_load_ushort v173, v42, %[W], s60 offen offset:3456\n\t" \
  "buffer_load_ushort v254, v42, %[W], s61 offen offset:0\n\t" \
  "buffer_load_ushort v174, v42, %[W], s61 offen offset:1152\n\t" \
  "buffer_load_ushort v255, v42, %[W], s61 offen offset:2304\n\t" \
  "buffer_load_ushort v175, v42, %[W], s61 offen offset:3456\n\t" \
  "v_lshl_or_b32 v224, v144, 16, v224\n\t" \
  "v_lshl_or_b32 v225, v145, 16, v225\n\t" \
  "v_lshl_or_b32 v226, v146, 16, v226\n\t" \
  "v_lshl_or_b32 v227, v147, 16, v227\n\t" \
  "v_lshl_or_b32 v228, v148, 16, v228\n\t" \
  "v_lshl_or_b32 v229, v149, 16, v229\n\t" \
  "v_lshl_or_b32 v230, v150, 16, v230\n\t" \
  "v_lshl_or_b32 v231, v151, 16, v231\n\t" \
  "s_waitcnt vmcnt(46)\n\t" \
  "v_lshl_or_b32 v232, v152, 16, v232\n\t" \
  "s_waitcnt vmcnt(44)\n\t" \
  "v_lshl_or_b32 v233, v153, 16, v233\n\t" \
  "s_waitcnt vmcnt(42)\n\t" \
  "v_lshl_or_b32 v234, v154, 16, v234\n\t" \
  "s_waitcnt vmcnt(40)\n\t" \
  "v_lshl_or_b32 v235, v155, 16, v235\n\t" \
  "s_waitcnt vmcnt(38)\n\t" \
  "v_lshl_or_b32 v236, v156, 16, v236\n\t" \
  "s_waitcnt vmcnt(36)\n\t" \
  "v_lshl_or_b32 v237, v157, 16, v237\n\t" \
  "s_waitcnt vmcnt(34)\n\t" \
  "v_lshl_or_b32 v238, v158, 16, v238\n\t" \
  "s_waitcnt vmcnt(32)\n\t" \
  "v_lshl_or_b32 v239, v159, 16, v239\n\t" \
  "s_waitcnt vmcnt(30)\n\t" \
  "v_lshl_or_b32 v240, v160, 16, v240\n\t" \
  "s_waitcnt vmcnt(28)\n\t" \
  "v_lshl_or_b32 v241, v161, 16, v241\n\t" \
  "s_waitcnt vmcnt(26)\n\t" \
  "v_lshl_or_b32 v242, v162, 16, v242\n\t" \
  "s_waitcnt vmcnt(24)\n\t" \
  "v_lshl_or_b32 v243, v163, 16, v243\n\t" \
  "s_waitcnt vmcnt(22)\n\t" \
  "v_lshl_or_b32 v244, v164, 16, v244\n\t" \
  "s_waitcnt vmcnt(20)\n\t" \
  "v_lshl_or_b32 v245, v165, 16, v245\n\t" \
  "s_waitcnt vmcnt(18)\n\t" \
  "v_lshl_or_b32 v246, v166, 16, v246\n\t" \
  "s_waitcnt vmcnt(16)\n\t" \
  "v_lshl_or_b32 v247, v167, 16, v247\n\t" \
  "s_waitcnt vmcnt(14)\n\t" \
  "v_lshl_or_b32 v248, v168, 16, v248\n\t" \
  "s_waitcnt vmcnt(12)\n\t" \
  "v_lshl_or_b32 v249, v169, 16, v249\n\t" \
  "s_waitcnt vmcnt(10)\n\t" \
  "v_lshl_or_b32 v250, v170, 16, v250\n\t" \
  "s_waitcnt vmcnt(8)\n\t" \
  "v_lshl_or_b32 v251, v171, 16, v251\n\t" \
  "s_waitcnt vmcnt(6)\n\t" \
  "v_lshl_or_b32 v252, v172, 16, v252\n\t" \
  "s_waitcnt vmcnt(4)\n\t" \
  "v_lshl_or_b32 v253, v173, 16, v253\n\t" \
  "s_waitcnt vmcnt(2)\n\t" \
  "v_lshl_or_b32 v254, v174, 16, v254\n\t" \
  "s_waitcnt vmcnt(0)\n\t" \
  "v_lshl_or_b32 v255, v175, 16, v255\n\t" \
  "s_mov_b32 s84, " TK_CAP "\n\t" \
  "s_mov_b32 s48, 0\n\t" \
  "s_mov_b32 s49, 0xf0000000\n\t" \
  "s_mov_b32 s46, s45\n\t" \
  "v_mov_b32 v5, 0\n\t" \
  "v_lshrrev_b32 v13, 2, v0\n\t" \
  "s_mov_b32 s60, " TK_CTAB "\n\t" \
  "buffer_load_dword v10, v13, %[X], s60 offen offset:88 sc1\n\t" \
  "buffer_load_dword v13, v13, %[X], s60 offen offset:344 sc1\n\t" \
  "s_waitcnt vmcnt(0)\n\t" \
  "v_and_b32 v10, 1, v10\n\t" \
  "v_and_b32 v13, 1, v13\n\t" \
  "v_cmp_ne_u32 s[90:91], s67, v10\n\t" \
  "v_cmp_ne_u32 s[88:89], s67, v13\n\t" \
  "s_nop 1\n\t" \
  "s_and_b32 s60, s88, 256\n\t" \
  "s_lshl_b32 s60, s60, 5\n\t" \
  "s_add_u32 s44, s44, s60\n\t" \
  "s_add_u32 s83, s48, 0\n\t" \
  "s_lshr_b64 s[60:61], s[90:91], s83\n\t" \
  "s_and_b32 s60, s60, 1\n\t" \
  "s_lshl_b32 s60, s60, 13\n\t" \
  "s_add_u32 s83, s83, " TK_SG0 "\n\t" \
  "s_lshl_b32 s83, s83, 21\n\t" \
  "s_or_b32 s83, s83, s60\n\t" \
  "LCOLD_%=:\n\t" \
  "s_add_u32 s62, s83, s41\n\t" \
  "s_mov_b32 s61, 0\n\t" \
  "LPQ_%=:\n\t" \
  "buffer_load_dword v10, v9, %[X], s62 offen sc1\n\t" \
  "s_waitcnt vmcnt(0)\n\t" \
  "v_cmp_eq_u32 vcc, 0, v10\n\t" \
  "s_cbranch_vccz LPQ_%=x\n\t" \
  "s_add_u32 s61, s61, 1\n\t" \
  "s_sleep 4\n\t" \
  "s_cmp_gt_u32 s61, s84\n\t" \
  "s_cbranch_scc1 LPQ_%=g\n\t" \
  "s_and_b32 s60, s61, 1023\n\t" \
  "s_cmp_lg_u32 s60, 0\n\t" \
  "s_cbranch_scc1 LPQ_%=\n\t" \
  "buffer_load_dword v10, off, %[X], s92 sc1\n\t" \
  "s_waitcnt vmcnt(0)\n\t" \
  "v_readfirstlane_b32 s60, v10\n\t" \
  "s_nop 0\n\t" \
  "s_cmp_eq_u32 s60, 0\n\t" \
  "s_cbranch_scc1 LPQ_%=\n\t" \
  "LPQ_%=g:\n\t" \
  "s_mov_b32 s84, 0\n\t" \
  "s_or_b32 s60, s48, 1073741824\n\t" \
  "v_mov_b32 v10, s60\n\t" \
  "s_mov_b64 exec, 1\n\t" \
  "buffer_store_dword v10, off, %[X], s92 sc1\n\t" \
  "s_mov_b64 exec, -1\n\t" \
  "LPQ_%=x:\n\t" \
  "s_add_u32 m0, s46, 0\n\t" \
  "s_add_u32 s47, s83, s36\n\t" \
  "buffer_load_dwordx4 v0, %[X], s47 offen offset:0 sc1 lds\n\t" \
  "buffer_load_dwordx4 v0, %[X], s47 offen offset:1024 sc1 lds\n\t" \
  "buffer_load_dwordx4 v0, %[X], s47 offen offset:2048 sc1 lds\n\t" \
  "buffer_load_dwordx4 v0, %[X], s47 offen offset:3072 sc1 lds\n\t" \
  "s_add_u32 m0, s46, 4096\n\t" \
  "s_add_u32 s47, s83, s37\n\t" \
  "buffer_load_dwordx4 v0, %[X], s47 offen offset:0 sc1 lds\n\t" \
  "buffer_load_dwordx4 v0, %[X], s47 offen offset:1024 sc1 lds\n\t" \
  "buffer_load_dwordx4 v0, %[X], s47 offen offset:2048 sc1 lds\n\t" \
  "buffer_load_dwordx4 v0, %[X], s47 offen offset:3072 sc1 lds\n\t" \
  "s_add_u32 m0, s46, 8192\n\t" \
  "s_add_u32 s47, s83, s38\n\t" \
  "buffer_load_dwordx4 v0, %[X], s47 offen offset:0 sc1 lds\n\t" \
  "buffer_load_dwordx4 v0, %[X], s47 offen offset:1024 sc1 lds\n\t" \
  "buffer_load_dwordx4 v0, %[X], s47 offen offset:2048 sc1 lds\n\t" \
  "buffer_load_dwordx4 v0, %[X], s47 offen offset:3072 sc1 lds\n\t" \
  "s_waitcnt vmcnt(0)\n\t" \
  "LTOP_%=:\n\t" \
  "s_barrier\n\t" \
  "ds_read_b128 v[48:51], v2 offset:0\n\t" \
  "ds_read_b128 v[64:67], v1 offset:8192\n\t" \
  "ds_read_b128 v[80:83], v1 offset:12288\n\t" \
  "ds_read_b128 v[52:55], v2 offset:1024\n\t" \
  "ds_read_b128 v[68:71], v1 offset:9216\n\t" \
  "ds_read_b128 v[84:87], v1 offset:13312\n\t" \
  "ds_read_b128 v[56:59], v2 offset:2048\n\t" \
  "ds_read_b128 v[72:75], v1 offset:10240\n\t" \
  "ds_read_b128 v[88:91], v1 offset:14336\n\t" \
  "ds_read_b128 v[60:63], v2 offset:3072\n\t" \
  "ds_read_b128 v[76:79], v1 offset:11264\n\t" \
  "ds_read_b128 v[92:95], v1 offset:15360\n\t" \
  "v_readfirstlane_b32 s57, v5\n\t" \
  "s_mov_b32 s82, s83\n\t" \
  "s_xor_b32 s46, s46, 0x10000\n\t" \
  "s_add_u32 s58, s82, s41\n\t" \
  "s_add_u32 s58, s58, 4096\n\t" \
  "s_add_u32 s59, s82, s39\n\t" \
  "s_cmp_eq_u32 s57, 0\n\t" \
  "s_cbranch_scc1 LNG_%=\n\t" \
  "buffer_load_dwordx4 v[196:199], v0, %[X], s59 offen offset:0 sc1\n\t" \
  "buffer_load_dwordx4 v[200:203], v0, %[X], s59 offen offset:1024 sc1\n\t" \
  "buffer_load_dwordx2 v[204:205], v6, %[X], s59 offen offset:2048 sc1\n\t" \
  "buffer_load_dwordx2 v[206:207], v6, %[X], s59 offen offset:2560 sc1\n\t" \
  "buffer_load_dwordx2 v[208:209], v6, %[X], s59 offen offset:3072 sc1\n\t" \
  "buffer_load_dwordx2 v[210:211], v6, %[X], s59 offen offset:3584 sc1\n\t" \
  "LNG_%=:\n\t" \
  "s_waitcnt lgkmcnt(10)\n\t" \
  "v_mfma_f32_16x16x32_bf16 v[36:39], v[64:67], v[48:51], v[16:19]\n\t" \
  "ds_read_b128 v[96:99], v1 offset:16384\n\t" \
  "s_waitcnt lgkmcnt(10)\n\t" \
  "v_mfma_f32_16x16x32_bf16 v[40:43], v[80:83], v[48:51], v[20:23]\n\t" \
  "ds_read_b128 v[100:103], v1 offset:17408\n\t" \
  "s_waitcnt lgkmcnt(9)\n\t" \
  "v_mfma_f32_16x16x32_bf16 v[36:39], v[68:71], v[52:55], v[36:39]\n\t" \
  "ds_read_b128 v[104:107], v1 offset:18432\n\t" \
  "s_waitcnt lgkmcnt(9)\n\t" \
  "v_mfma_f32_16x16x32_bf16 v[40:43], v[84:87], v[52:55], v[40:43]\n\t" \
  "ds_read_b128 v[108:111], v1 offset:19456\n\t" \
  "s_waitcnt lgkmcnt(8)\n\t" \
  "v_mfma_f32_16x16x32_bf16 v[36:39], v[72:75], v[56:59], v[36:39]\n\t" \
  "ds_read_b128 v[112:115], v1 offset:20480\n\t" \
  "s_waitcnt lgkmcnt(8)\n\t" \
  "v_mfma_f32_16x16x32_bf16 v[40:43], v[88:91], v[56:59], v[40:43]\n\t" \
  "ds_read_b128 v[116:119], v1 offset:21504\n\t" \
  "s_waitcnt lgkmcnt(7)\n\t" \
  "v_mfma_f32_16x16x32_bf16 v[36:39], v[76:79], v[60:63], v[36:39]\n\t" \
  "ds_read_b128 v[120:123], v1 offset:22528\n\t" \
  "s_waitcnt lgkmcnt(7)\n\t" \
  "v_mfma_f32_16x16x32_bf16 v[40:43], v[92:95], v[60:63], v[40:43]\n\t" \
  "ds_read_b128 v[124:127], v1 offset:23552\n\t" \
  "s_add_u32 s83, s48, 1\n\t" \
  "s_lshr_b64 s[60:61], s[90:91], s83\n\t" \
  "s_and_b32 s60, s60, 1\n\t" \
  "s_lshl_b32 s60, s60, 13\n\t" \
  "s_add_u32 s83, s83, " TK_SG0 "\n\t" \
  "s_lshl_b32 s83, s83, 21\n\t" \
  "s_or_b32 s83, s83, s60\n\t" \
  "v_xor_b32 v1, 0x10000, v1\n\t" \
  "v_xor_b32 v2, 0x10000, v2\n\t" \
  "v_max3_f32 v11, v36, v37, v38\n\t" \
  "v_max3_f32 v11, v11, v39, v40\n\t" \
  "v_max3_f32 v13, v41, v42, v43\n\t" \
  "v_max_f32 v11, v11, v13\n\t" \
  "v_mov_b32 v13, v11\n\t" \
  "s_nop 1\n\t" \
  "v_permlane16_swap_b32 v11, v13\n\t" \
  "v_max_f32 v11, v11, v13\n\t" \
  "v_mov_b32 v13, v11\n\t" \
  "s_nop 1\n\t" \
  "v_permlane32_swap_b32 v11, v13\n\t" \
  "v_max_f32 v11, v11, v13\n\t" \
  "v_mul_f32 v13, 0xbe0293ee, v11\n\t" \
  "v_fmamk_f32 v36, v36, 0x3e0293ee, v13\n\t" \
  "v_fmamk_f32 v37, v37, 0x3e0293ee, v13\n\t" \
  "v_fmamk_f32 v38, v38, 0x3e0293ee, v13\n\t" \
  "v_fmamk_f32 v39, v39, 0x3e0293ee, v13\n\t" \
  "v_fmamk_f32 v40, v40, 0x3e0293ee, v13\n\t" \
  "v_fmamk_f32 v41, v41, 0x3e0293ee, v13\n\t" \
  "v_fmamk_f32 v42, v42, 0x3e0293ee, v13\n\t" \
  "v_fmamk_f32 v43, v43, 0x3e0293ee, v13\n\t" \
  "s_waitcnt vmcnt(0)\n\t" \
  "s_barrier\n\t" \
  "buffer_load_dword v5, off, %[X], s58 sc1\n\t" \
  "s_mov_b64 exec, 1\n\t" \
  "buffer_store_dword v3, off, %[X], s49 sc1\n\t" \
  "s_mov_b64 exec, -1\n\t" \
  "s_mov_b32 s49, 0xf0000000\n\t" \
  "v_exp_f32 v36, v36\n\t" \
  "v_exp_f32 v37, v37\n\t" \
  "v_exp_f32 v38, v38\n\t" \
  "v_exp_f32 v39, v39\n\t" \
  "v_exp_f32 v40, v40\n\t" \
  "v_exp_f32 v41, v41\n\t" \
  "v_exp_f32 v42, v42\n\t" \
  "v_exp_f32 v43, v43\n\t" \
  "v_cvt_pk_bf16_f32 v44, v36, v37\n\t" \
  "v_cvt_pk_bf16_f32 v45, v38, v39\n\t" \
  "v_cvt_pk_bf16_f32 v46, v40, v41\n\t" \
  "v_cvt_pk_bf16_f32 v47, v42, v43\n\t" \
  "v_add_f32 v36, v36, v37\n\t" \
  "v_add_f32 v38, v38, v39\n\t" \
  "s_waitcnt lgkmcnt(7)\n\t" \
  "v_mfma_f32_16x16x32_bf16 v[128:131], v[96:99], v[44:47], 0\n\t" \
  "v_add_f32 v40, v40, v41\n\t" \
  "s_and_b32 s62, s48, 15\n\t" \
  "s_lshr_b32 s63, s62, 1\n\t" \
  "s_waitcnt lgkmcnt(6)\n\t" \
  "v_mfma_f32_16x16x32_bf16 v[132:135], v[100:103], v[44:47], 0\n\t" \
  "v_add_f32 v42, v42, v43\n\t" \
  "s_lshr_b32 s60, s88, s63\n\t" \
  "s_and_b32 s60, s60, 1\n\t" \
  "s_waitcnt lgkmcnt(5)\n\t" \
  "v_mfma_f32_16x16x32_bf16 v[136:139], v[104:107], v[44:47], 0\n\t" \
  "v_add_f32 v36, v36, v38\n\t" \
  "s_lshl_b32 s60, s60, 13\n\t" \
  "s_lshl_b32 s61, s62, 20\n\t" \
  "s_waitcnt lgkmcnt(4)\n\t" \
  "v_mfma_f32_16x16x32_bf16 v[140:143], v[108:111], v[44:47], 0\n\t" \
  "v_add_f32 v40, v40, v42\n\t" \
  "s_add_u32 s50, s61, s60\n\t" \
  "s_add_u32 s50, s50, s43\n\t" \
  "s_waitcnt lgkmcnt(3)\n\t" \
  "v_mfma_f32_16x16x32_bf16 v[144:147], v[112:115], v[44:47], 0\n\t" \
  "v_add_f32 v12, v36, v40\n\t" \
  "s_add_u32 s51, s50, 32768\n\t" \
  "s_add_u32 s52, s51, 32768\n\t" \
  "s_waitcnt lgkmcnt(2)\n\t" \
  "v_mfma_f32_16x16x32_bf16 v[148:151], v[116:119], v[44:47], 0\n\t" \
  "s_add_u32 s53, s52, 32768\n\t" \
  "s_lshl_b32 s61, s62, 17\n\t" \
  "s_waitcnt lgkmcnt(1)\n\t" \
  "v_mfma_f32_16x16x32_bf16 v[152:155], v[120:123], v[44:47], 0\n\t" \
  "s_add_u32 s54, s61, s44\n\t" \
  "s_add_u32 s63, s83, s41\n\t" \
  "s_waitcnt lgkmcnt(0)\n\t" \
  "v_mfma_f32_16x16x32_bf16 v[156:159], v[124:127], v[44:47], 0\n\t" \
  "s_waitcnt vmcnt(1)\n\t" \
  "v_readfirstlane_b32 s60, v5\n\t" \
  "s_cmp_lg_u32 s57, 0\n\t" \
  "s_cbranch_scc1 LGOT_%=\n\t" \
  "s_cmp_lg_u32 s60, 0\n\t" \
  "s_cbranch_scc1 LPK_%=x\n\t" \
  "s_mov_b32 s61, 0\n\t" \
  "LPK_%=:\n\t" \
  "buffer_load_dword v5, off, %[X], s58 sc1\n\t" \
  "s_waitcnt vmcnt(0)\n\t" \
  "v_cmp_eq_u32 vcc, 0, v5\n\t" \
  "s_cbranch_vccz LPK_%=x\n\t" \
  "s_add_u32 s61, s61, 1\n\t" \
  "s_sleep " TK_PSK "\n\t" \
  "s_cmp_gt_u32 s61, s84\n\t" \
  "s_cbranch_scc1 LPK_%=g\n\t" \
  "s_and_b32 s60, s61, 1023\n\t" \
  "s_cmp_lg_u32 s60, 0\n\t" \
  "s_cbranch_scc1 LPK_%=\n\t" \
  "buffer_load_dword v5, off, %[X], s92 sc1\n\t" \
  "s_waitcnt vmcnt(0)\n\t" \
  "v_readfirstlane_b32 s60, v5\n\t" \
  "s_nop 0\n\t" \
  "s_cmp_eq_u32 s60, 0\n\t" \
  "s_cbranch_scc1 LPK_%=\n\t" \
  "LPK_%=g:\n\t" \
  "s_mov_b32 s84, 0\n\t" \
  "s_or_b32 s60, s48, 1342177280\n\t" \
  "v_mov_b32 v5, s60\n\t" \
  "s_mov_b64 exec, 1\n\t" \
  "buffer_store_dword v5, off, %[X], s92 sc1\n\t" \
  "s_mov_b64 exec, -1\n\t" \
  "LPK_%=x:\n\t" \
  "buffer_load_dwordx4 v[196:199], v0, %[X], s59 offen offset:0 sc1\n\t" \
  "buffer_load_dwordx4 v[200:203], v0, %[X], s59 offen offset:1024 sc1\n\t" \
  "buffer_load_dwordx2 v[204:205], v6, %[X], s59 offen offset:2048 sc1\n\t" \
  "buffer_load_dwordx2 v[206:207], v6, %[X], s59 offen offset:2560 sc1\n\t" \
  "buffer_load_dwordx2 v[208:209], v6, %[X], s59 offen offset:3072 sc1\n\t" \
  "buffer_load_dwordx2 v[210:211], v6, %[X], s59 offen offset:3584 sc1\n\t" \
  "LGOT_%=:\n\t" \
  "buffer_load_dword v10, v9, %[X], s63 offen sc1\n\t" \
  "s_add_u32 s49, s82, s42\n\t" \
  "s_waitcnt vmcnt(6)\n\t" \
  "v_mfma_f32_16x16x32_bf16 v[36:39], v[196:199], v[48:51], 0\n\t" \
  "v_mfma_f32_16x16x32_bf16 v[40:43], v[196:199], v[52:55], 0\n\t" \
  "s_waitcnt vmcnt(5)\n\t" \
  "v_mfma_f32_16x16x32_bf16 v[36:39], v[200:203], v[56:59], v[36:39]\n\t" \
  "v_mfma_f32_16x16x32_bf16 v[40:43], v[200:203], v[60:63], v[40:43]\n\t" \
  "s_nop 8\n\t" \
  "v_add_f32 v36, v36, v42\n\t" \
  "v_add_f32 v37, v37, v43\n\t" \
  "v_max3_f32 v14, v36, v37, v11\n\t" \
  "v_mov_b32 v13, v14\n\t" \
  "s_nop 1\n\t" \
  "v_permlane16_swap_b32 v14, v13\n\t" \
  "v_max_f32 v14, v14, v13\n\t" \
  "v_mov_b32 v13, v14\n\t" \
  "s_nop 1\n\t" \
  "v_permlane32_swap_b32 v14, v13\n\t" \
  "v_max_f32 v14, v14, v13\n\t" \
  "v_mul_f32 v14, 0xbe0293ee, v14\n\t" \
  "v_fmamk_f32 v11, v11, 0x3e0293ee, v14\n\t" \
  "v_fmamk_f32 v36, v36, 0x3e0293ee, v14\n\t" \
  "v_fmamk_f32 v37, v37, 0x3e0293ee, v14\n\t" \
  "v_exp_f32 v11, v11\n\t" \
  "v_exp_f32 v36, v36\n\t" \
  "v_exp_f32 v37, v37\n\t" \
  "v_fma_f32 v12, v12, v11, v36\n\t" \
  "v_add_f32 v12, v12, v37\n\t" \
  "v_mov_b32 v13, v12\n\t" \
  "s_nop 1\n\t" \
  "v_permlane16_swap_b32 v12, v13\n\t" \
  "v_add_f32 v12, v12, v13\n\t" \
  "v_mov_b32 v13, v12\n\t" \
  "s_nop 1\n\t" \
  "v_permlane32_swap_b32 v12, v13\n\t" \
  "v_add_f32 v12, v12, v13\n\t" \
  "v_rcp_f32 v12, v12\n\t" \
  "s_nop 0\n\t" \
  "v_mul_f32 v14, v11, v12\n\t" \
  "v_mul_f32 v36, v36, v12\n\t" \
  "v_mul_f32 v37, v37, v12\n\t" \
  "v_cvt_pk_bf16_f32 v26, v36, v37\n\t" \
  "v_cvt_pk_bf16_f32 v33, v36, v37\n\t" \
  "v_pk_mul_f32 v[128:129], v[128:129], v[14:15] op_sel_hi:[1,0]\n\t" \
  "v_pk_mul_f32 v[130:131], v[130:131], v[14:15] op_sel_hi:[1,0]\n\t" \
  "v_pk_mul_f32 v[132:133], v[132:133], v[14:15] op_sel_hi:[1,0]\n\t" \
  "v_pk_mul_f32 v[134:135], v[134:135], v[14:15] op_sel_hi:[1,0]\n\t" \
  "v_pk_mul_f32 v[136:137], v[136:137], v[14:15] op_sel_hi:[1,0]\n\t" \
  "v_pk_mul_f32 v[138:139], v[138:139], v[14:15] op_sel_hi:[1,0]\n\t" \
  "v_pk_mul_f32 v[140:141], v[140:141], v[14:15] op_sel_hi:[1,0]\n\t" \
  "v_pk_mul_f32 v[142:143], v[142:143], v[14:15] op_sel_hi:[1,0]\n\t" \
  "s_waitcnt vmcnt(3)\n\t" \
  "v_mfma_f32_16x16x32_bf16 v[128:131], v[204:207], v[26:29], v[128:131]\n\t" \
  "v_mul_f32 v144, v144, v14\n\t" \
  "v_mul_f32 v145, v145, v14\n\t" \
  "v_mfma_f32_16x16x32_bf16 v[132:135], v[204:207], v[32:35], v[132:135]\n\t" \
  "v_mul_f32 v146, v146, v14\n\t" \
  "v_mul_f32 v147, v147, v14\n\t" \
  "v_mfma_f32_16x16x32_bf16 v[136:139], v[204:207], v[24:27], v[136:139]\n\t" \
  "v_mul_f32 v148, v148, v14\n\t" \
  "v_mul_f32 v149, v149, v14\n\t" \
  "v_mfma_f32_16x16x32_bf16 v[140:143], v[204:207], v[30:33], v[140:143]\n\t" \
  "v_cvt_pk_bf16_f32 v64, v128, v129\n\t" \
  "v_cvt_pk_bf16_f32 v65, v130, v131\n\t" \
  "v_cvt_pk_bf16_f32 v66, v132, v133\n\t" \
  "v_cvt_pk_bf16_f32 v67, v134, v135\n\t" \
  "v_mul_f32 v150, v150, v14\n\t" \
  "v_mul_f32 v151, v151, v14\n\t" \
  "v_mfma_f32_16x16x32_bf16 v[192:195], v[224:227], v[64:67], 0\n\t" \
  "ds_write_b128 v3, v[64:67] offset:0\n\t" \
  "v_cvt_pk_bf16_f32 v68, v136, v137\n\t" \
  "v_cvt_pk_bf16_f32 v69, v138, v139\n\t" \
  "v_mfma_f32_16x16x32_bf16 v[160:163], v[64:67], a[0:3], 0\n\t" \
  "v_cvt_pk_bf16_f32 v70, v140, v141\n\t" \
  "v_cvt_pk_bf16_f32 v71, v142, v143\n\t" \
  "s_waitcnt vmcnt(1)\n\t" \
  "v_mfma_f32_16x16x32_bf16 v[144:147], v[208:211], v[26:29], v[144:147]\n\t" \
  "v_mul_f32 v152, v152, v14\n\t" \
  "v_mul_f32 v153, v153, v14\n\t" \
  "v_mfma_f32_16x16x32_bf16 v[148:151], v[208:211], v[32:35], v[148:151]\n\t" \
  "v_mul_f32 v154, v154, v14\n\t" \
  "v_mul_f32 v155, v155, v14\n\t" \
  "v_mfma_f32_16x16x32_bf16 v[164:167], v[64:67], a[4:7], 0\n\t" \
  "ds_write_b128 v3, v[68:71] offset:1024\n\t" \
  "v_mul_f32 v156, v156, v14\n\t" \
  "v_mul_f32 v157, v157, v14\n\t" \
  "v_mfma_f32_16x16x32_bf16 v[168:171], v[64:67], a[8:11], 0\n\t" \
  "v_mul_f32 v158, v158, v14\n\t" \
  "v_mul_f32 v159, v159, v14\n\t" \
  "v_mfma_f32_16x16x32_bf16 v[172:175], v[64:67], a[12:15], 0\n\t" \
  "v_cvt_pk_bf16_f32 v72, v144, v145\n\t" \
  "v_cvt_pk_bf16_f32 v73, v146, v147\n\t" \
  "v_mfma_f32_16x16x32_bf16 v[152:155], v[208:211], v[24:27], v[152:155]\n\t" \
  "v_cvt_pk_bf16_f32 v74, v148, v149\n\t" \
  "v_cvt_pk_bf16_f32 v75, v150, v151\n\t" \
  "v_mfma_f32_16x16x32_bf16 v[156:159], v[208:211], v[30:33], v[156:159]\n\t" \
  "v_mfma_f32_16x16x32_bf16 v[176:179], v[64:67], a[16:19], 0\n\t" \
  "ds_write_b128 v3, v[72:75] offset:2048\n\t" \
  "v_mfma_f32_16x16x32_bf16 v[180:183], v[64:67], a[20:23], 0\n\t" \
  "v_mfma_f32_16x16x32_bf16 v[184:187], v[64:67], a[24:27], 0\n\t" \
  "v_cvt_pk_bf16_f32 v76, v152, v153\n\t" \
  "v_cvt_pk_bf16_f32 v77, v154, v155\n\t" \
  "v_mfma_f32_16x16x32_bf16 v[188:191], v[64:67], a[28:31], 0\n\t" \
  "v_cvt_pk_bf16_f32 v78, v156, v157\n\t" \
  "v_cvt_pk_bf16_f32 v79, v158, v159\n\t" \
  "v_mfma_f32_16x16x32_bf16 v[192:195], v[228:231], v[68:71], v[192:195]\n\t" \
  "ds_write_b128 v3, v[76:79] offset:3072\n\t" \
  "v_mfma_f32_16x16x32_bf16 v[160:163], v[68:71], a[32:35], v[160:163]\n\t" \
  "s_waitcnt vmcnt(0)\n\t" \
  "v_cmp_eq_u32 vcc, 0, v10\n\t" \
  "v_mfma_f32_16x16x32_bf16 v[164:167], v[68:71], a[36:39], v[164:167]\n\t" \
  "s_cmp_eq_u64 vcc, 0\n\t" \
  "s_cselect_b32 s55, s83, 0xf0000000\n\t" \
  "s_cmp_eq_u32 s48, 63\n\t" \
  "v_mfma_f32_16x16x32_bf16 v[168:171], v[68:71], a[40:43], v[168:171]\n\t" \
  "s_cselect_b32 s55, 0xf0000000, s55\n\t" \
  "s_mov_b32 s56, s83\n\t" \
  "s_mov_b32 s83, s55\n\t" \
  "v_mfma_f32_16x16x32_bf16 v[172:175], v[68:71], a[44:47], v[172:175]\n\t" \
  "v_mfma_f32_16x16x32_bf16 v[176:179], v[68:71], a[48:51], v[176:179]\n\t" \
  "s_add_u32 m0, s46, 0\n\t" \
  "s_add_u32 s47, s83, s36\n\t" \
  "buffer_load_dwordx4 v0, %[X], s47 offen offset:0 sc1 lds\n\t" \
  "v_mfma_f32_16x16x32_bf16 v[180:183], v[68:71], a[52:55], v[180:183]\n\t" \
  "v_mfma_f32_16x16x32_bf16 v[184:187], v[68:71], a[56:59], v[184:187]\n\t" \
  "s_waitcnt lgkmcnt(0)\n\t" \
  "s_barrier\n\t" \
  "v_mfma_f32_16x16x32_bf16 v[188:191], v[68:71], a[60:63], v[188:191]\n\t" \
  "ds_read_b128 v[80:83], v4 offset:0\n\t" \
  "v_mfma_f32_16x16x32_bf16 v[192:195], v[232:235], v[72:75], v[192:195]\n\t" \
  "buffer_load_dwordx4 v0, %[X], s47 offen offset:1024 sc1 lds\n\t" \
  "v_mfma_f32_16x16x32_bf16 v[160:163], v[72:75], a[64:67], v[160:163]\n\t" \
  "ds_read_b128 v[84:87], v4 offset:1024\n\t" \
  "v_mfma_f32_16x16x32_bf16 v[164:167], v[72:75], a[68:71], v[164:167]\n\t" \
  "v_mfma_f32_16x16x32_bf16 v[168:171], v[72:75], a[72:75], v[168:171]\n\t" \
  "ds_read_b128 v[88:91], v4 offset:2048\n\t" \
  "v_mfma_f32_16x16x32_bf16 v[172:175], v[72:75], a[76:79], v[172:175]\n\t" \
  "buffer_load_dwordx4 v0, %[X], s47 offen offset:2048 sc1 lds\n\t" \
  "v_mfma_f32_16x16x32_bf16 v[176:179], v[72:75], a[80:83], v[176:179]\n\t" \
  "ds_read_b128 v[92:95], v4 offset:3072\n\t" \
  "v_mfma_f32_16x16x32_bf16 v[180:183], v[72:75], a[84:87], v[180:183]\n\t" \
  "v_mfma_f32_16x16x32_bf16 v[184:187], v[72:75], a[88:91], v[184:187]\n\t" \
  "v_mfma_f32_16x16x32_bf16 v[188:191], v[72:75], a[92:95], v[188:191]\n\t" \
  "buffer_load_dwordx4 v0, %[X], s47 offen offset:3072 sc1 lds\n\t" \
  "v_mfma_f32_16x16x32_bf16 v[192:195], v[236:239], v[76:79], v[192:195]\n\t" \
  "v_mfma_f32_16x16x32_bf16 v[160:163], v[76:79], a[96:99], v[160:163]\n\t" \
  "v_mfma_f32_16x16x32_bf16 v[164:167], v[76:79], a[100:103], v[164:167]\n\t" \
  "v_mfma_f32_16x16x32_bf16 v[168:171], v[76:79], a[104:107], v[168:171]\n\t" \
  "s_add_u32 m0, s46, 4096\n\t" \
  "s_add_u32 s47, s83, s37\n\t" \
  "buffer_load_dwordx4 v0, %[X], s47 offen offset:0 sc1 lds\n\t" \
  "v_mfma_f32_16x16x32_bf16 v[172:175], v[76:79], a[108:111], v[172:175]\n\t" \
  "v_mfma_f32_16x16x32_bf16 v[176:179], v[76:79], a[112:115], v[176:179]\n\t" \
  "v_mfma_f32_16x16x32_bf16 v[180:183], v[76:79], a[116:119], v[180:183]\n\t" \
  "v_mfma_f32_16x16x32_bf16 v[184:187], v[76:79], a[120:123], v[184:187]\n\t" \
  "buffer_load_dwordx4 v0, %[X], s47 offen offset:1024 sc1 lds\n\t" \
  "v_mfma_f32_16x16x32_bf16 v[188:191], v[76:79], a[124:127], v[188:191]\n\t" \
  "s_waitcnt lgkmcnt(3)\n\t" \
  "v_mfma_f32_16x16x32_bf16 v[192:195], v[240:243], v[80:83], v[192:195]\n\t" \
  "v_mfma_f32_16x16x32_bf16 v[160:163], v[80:83], a[128:131], v[160:163]\n\t" \
  "v_mfma_f32_16x16x32_bf16 v[164:167], v[80:83], a[132:135], v[164:167]\n\t" \
  "buffer_load_dwordx4 v0, %[X], s47 offen offset:2048 sc1 lds\n\t" \
  "v_mfma_f32_16x16x32_bf16 v[168:171], v[80:83], a[136:139], v[168:171]\n\t" \
  "v_mfma_f32_16x16x32_bf16 v[172:175], v[80:83], a[140:143], v[172:175]\n\t" \
  "v_mfma_f32_16x16x32_bf16 v[176:179], v[80:83], a[144:147], v[176:179]\n\t" \
  "v_mfma_f32_16x16x32_bf16 v[180:183], v[80:83], a[148:151], v[180:183]\n\t" \
  "buffer_load_dwordx4 v0, %[X], s47 offen offset:3072 sc1 lds\n\t" \
  "v_mfma_f32_16x16x32_bf16 v[184:187], v[80:83], a[152:155], v[184:187]\n\t" \
  "v_mfma_f32_16x16x32_bf16 v[188:191], v[80:83], a[156:159], v[188:191]\n\t" \
  "s_waitcnt lgkmcnt(2)\n\t" \
  "v_mfma_f32_16x16x32_bf16 v[192:195], v[244:247], v[84:87], v[192:195]\n\t" \
  "v_mfma_f32_16x16x32_bf16 v[160:163], v[84:87], a[160:163], v[160:163]\n\t" \
  "s_add_u32 m0, s46, 8192\n\t" \
  "s_add_u32 s47, s83, s38\n\t" \
  "buffer_load_dwordx4 v0, %[X], s47 offen offset:0 sc1 lds\n\t" \
  "v_mfma_f32_16x16x32_bf16 v[164:167], v[84:87], a[164:167], v[164:167]\n\t" \
  "v_mfma_f32_16x16x32_bf16 v[168:171], v[84:87], a[168:171], v[168:171]\n\t" \
  "v_mfma_f32_16x16x32_bf16 v[172:175], v[84:87], a[172:175], v[172:175]\n\t" \
  "v_mfma_f32_16x16x32_bf16 v[176:179], v[84:87], a[176:179], v[176:179]\n\t" \
  "buffer_load_dwordx4 v0, %[X], s47 offen offset:1024 sc1 lds\n\t" \
  "v_mfma_f32_16x16x32_bf16 v[180:183], v[84:87], a[180:183], v[180:183]\n\t" \
  "v_mfma_f32_16x16x32_bf16 v[184:187], v[84:87], a[184:187], v[184:187]\n\t" \
  "v_mfma_f32_16x16x32_bf16 v[188:191], v[84:87], a[188:191], v[188:191]\n\t" \
  "s_waitcnt lgkmcnt(1)\n\t" \
  "v_mfma_f32_16x16x32_bf16 v[192:195], v[248:251], v[88:91], v[192:195]\n\t" \
  "buffer_load_dwordx4 v0, %[X], s47 offen offset:2048 sc1 lds\n\t" \
  "v_mfma_f32_16x16x32_bf16 v[160:163], v[88:91], a[192:195], v[160:163]\n\t" \
  "v_mfma_f32_16x16x32_bf16 v[164:167], v[88:91], a[196:199], v[164:167]\n\t" \
  "v_mfma_f32_16x16x32_bf16 v[168:171], v[88:91], a[200:203], v[168:171]\n\t" \
  "v_mfma_f32_16x16x32_bf16 v[172:175], v[88:91], a[204:207], v[172:175]\n\t" \
  "buffer_load_dwordx4 v0, %[X], s47 offen offset:3072 sc1 lds\n\t" \
  "v_mfma_f32_16x16x32_bf16 v[176:179], v[88:91], a[208:211], v[176:179]\n\t" \
  "v_mfma_f32_16x16x32_bf16 v[180:183], v[88:91], a[212:215], v[180:183]\n\t" \
  "v_mfma_f32_16x16x32_bf16 v[184:187], v[88:91], a[216:219], v[184:187]\n\t" \
  "s_add_u32 s63, s63, 4096\n\t" \
  "v_mfma_f32_16x16x32_bf16 v[188:191], v[88:91], a[220:223], v[188:191]\n\t" \
  "buffer_load_dword v5, off, %[X], s63 sc1\n\t" \
  "s_waitcnt lgkmcnt(0)\n\t" \
  "v_mfma_f32_16x16x32_bf16 v[192:195], v[252:255], v[92:95], v[192:195]\n\t" \
  "v_mfma_f32_16x16x32_bf16 v[160:163], v[92:95], a[224:227], v[160:163]\n\t" \
  "v_mfma_f32_16x16x32_bf16 v[164:167], v[92:95], a[228:231], v[164:167]\n\t" \
  "v_mfma_f32_16x16x32_bf16 v[168:171], v[92:95], a[232:235], v[168:171]\n\t" \
  "v_cvt_pk_bf16_f32 v112, v192, v193\n\t" \
  "v_cvt_pk_bf16_f32 v113, v194, v195\n\t" \
  "v_mfma_f32_16x16x32_bf16 v[172:175], v[92:95], a[236:239], v[172:175]\n\t" \
  "buffer_store_dwordx2 v[112:113], v8, %[X], s54 offen sc1\n\t" \
  "v_mfma_f32_16x16x32_bf16 v[176:179], v[92:95], a[240:243], v[176:179]\n\t" \
  "v_cvt_pk_bf16_f32 v96, v160, v164\n\t" \
  "v_cvt_pk_bf16_f32 v100, v161, v165\n\t" \
  "v_mfma_f32_16x16x32_bf16 v[180:183], v[92:95], a[244:247], v[180:183]\n\t" \
  "v_cvt_pk_bf16_f32 v104, v162, v166\n\t" \
  "v_cvt_pk_bf16_f32 v108, v163, v167\n\t" \
  "v_mfma_f32_16x16x32_bf16 v[184:187], v[92:95], a[248:251], v[184:187]\n\t" \
  "v_cvt_pk_bf16_f32 v97, v168, v172\n\t" \
  "v_cvt_pk_bf16_f32 v101, v169, v173\n\t" \
  "v_mfma_f32_16x16x32_bf16 v[188:191], v[92:95], a[252:255], v[188:191]\n\t" \
  "v_cvt_pk_bf16_f32 v105, v170, v174\n\t" \
  "v_cvt_pk_bf16_f32 v109, v171, v175\n\t" \
  "v_cvt_pk_bf16_f32 v98, v176, v180\n\t" \
  "v_cvt_pk_bf16_f32 v102, v177, v181\n\t" \
  "v_cvt_pk_bf16_f32 v106, v178, v182\n\t" \
  "v_cvt_pk_bf16_f32 v110, v179, v183\n\t" \
  "s_nop 1\n\t" \
  "v_cvt_pk_bf16_f32 v99, v184, v188\n\t" \
  "buffer_store_dwordx4 v[96:99], v7, %[X], s50 offen sc1\n\t" \
  "v_cvt_pk_bf16_f32 v103, v185, v189\n\t" \
  "buffer_store_dwordx4 v[100:103], v7, %[X], s51 offen sc1\n\t" \
  "v_cvt_pk_bf16_f32 v107, v186, v190\n\t" \
  "buffer_store_dwordx4 v[104:107], v7, %[X], s52 offen sc1\n\t" \
  "v_cvt_pk_bf16_f32 v111, v187, v191\n\t" \
  "buffer_store_dwordx4 v[108:111], v7, %[X], s53 offen sc1\n\t" \
  "s_cmp_eq_u32 s48, 63\n\t" \
  "s_cbranch_scc1 LEND_%=\n\t" \
  "s_add_u32 s48, s48, 1\n\t" \
  "s_cmp_eq_u32 s83, s56\n\t" \
  "s_mov_b32 s83, s56\n\t" \
  "s_cbranch_scc0 LCOLD_%=\n\t" \
  "s_waitcnt vmcnt(5)\n\t" \
  "s_branch LTOP_%=\n\t" \
  "LEND_%=:\n\t" \
  "s_waitcnt vmcnt(0)\n\t" \
  "s_barrier\n\t" \
  "s_mov_b64 exec, 1\n\t" \
  "buffer_store_dword v3, off, %[X], s49 sc1\n\t" \
  "s_mov_b64 exec, -1\n\t" \
  "s_mov_b32 s49, 0xf0000000\n\t" \
  ""
#define QK_ASM \
  "v_lshrrev_b32 v16, 7, v0\n\t" \
  "v_lshlrev_b32 v16, 13, v16\n\t" \
  "v_lshrrev_b32 v19, 2, v0\n\t" \
  "v_and_b32 v19, 64, v19\n\t" \
  "v_xor_b32 v19, v19, v0\n\t" \
  "v_and_b32 v19, 0x70, v19\n\t" \
  "v_add_u32 v16, v16, v19\n\t" \
  "s_mul_i32 s63, s40, 40960\n\t" \
  "v_and_b32 v19, 0x300, v0\n\t" \
  "v_lshlrev_b32 v17, 2, v19\n\t" \
  "v_and_b32 v19, 0xc0, v0\n\t" \
  "v_lshl_add_u32 v17, v19, 1, v17\n\t" \
  "v_and_b32 v19, 0x30, v0\n\t" \
  "v_add3_u32 v17, v17, v19, s63\n\t" \
  "v_lshrrev_b32 v19, 1, v0\n\t" \
  "v_and_b32 v19, 64, v19\n\t" \
  "v_add_u32 v17, v17, v19\n\t" \
  "v_xor_b32 v18, 64, v17\n\t" \
  "s_add_u32 m0, s63, 0\n\t" \
  "s_mov_b32 s60, 0\n\t" \
  "buffer_load_dwordx4 v16, %[W], s60 offen lds\n\t" \
  "s_add_u32 m0, s63, 1024\n\t" \
  "s_mov_b32 s60, 65536\n\t" \
  "buffer_load_dwordx4 v16, %[W], s60 offen lds\n\t" \
  "s_add_u32 m0, s63, 2048\n\t" \
  "s_mov_b32 s60, 131072\n\t" \
  "buffer_load_dwordx4 v16, %[W], s60 offen lds\n\t" \
  "s_add_u32 m0, s63, 3072\n\t" \
  "s_mov_b32 s60, 196608\n\t" \
  "buffer_load_dwordx4 v16, %[W], s60 offen lds\n\t" \
  "s_add_u32 m0, s63, 4096\n\t" \
  "s_mov_b32 s60, 262144\n\t" \
  "buffer_load_dwordx4 v16, %[W], s60 offen lds\n\t" \
  "s_add_u32 m0, s63, 5120\n\t" \
  "s_mov_b32 s60, 327680\n\t" \
  "buffer_load_dwordx4 v16, %[W], s60 offen lds\n\t" \
  "s_add_u32 m0, s63, 6144\n\t" \
  "s_mov_b32 s60, 393216\n\t" \
  "buffer_load_dwordx4 v16, %[W], s60 offen lds\n\t" \
  "s_add_u32 m0, s63, 7168\n\t" \
  "s_mov_b32 s60, 458752\n\t" \
  "buffer_load_dwordx4 v16, %[W], s60 offen lds\n\t" \
  "s_add_u32 m0, s63, 8192\n\t" \
  "s_mov_b32 s60, 524288\n\t" \
  "buffer_load_dwordx4 v16, %[W], s60 offen lds\n\t" \
  "s_add_u32 m0, s63, 9216\n\t" \
  "s_mov_b32 s60, 589824\n\t" \
  "buffer_load_dwordx4 v16, %[W], s60 offen lds\n\t" \
  "s_add_u32 m0, s63, 10240\n\t" \
  "s_mov_b32 s60, 655360\n\t" \
  "buffer_load_dwordx4 v16, %[W], s60 offen lds\n\t" \
  "s_add_u32 m0, s63, 11264\n\t" \
  "s_mov_b32 s60, 720896\n\t" \
  "buffer_load_dwordx4 v16, %[W], s60 offen lds\n\t" \
  "s_add_u32 m0, s63, 12288\n\t" \
  "s_mov_b32 s60, 786432\n\t" \
  "buffer_load_dwordx4 v16, %[W], s60 offen lds\n\t" \
  "s_add_u32 m0, s63, 13312\n\t" \
  "s_mov_b32 s60, 851968\n\t" \
  "buffer_load_dwordx4 v16, %[W], s60 offen lds\n\t" \
  "s_add_u32 m0, s63, 14336\n\t" \
  "s_mov_b32 s60, 917504\n\t" \
  "buffer_load_dwordx4 v16, %[W], s60 offen lds\n\t" \
  "s_add_u32 m0, s63, 15360\n\t" \
  "s_mov_b32 s60, 983040\n\t" \
  "buffer_load_dwordx4 v16, %[W], s60 offen lds\n\t" \
  "s_add_u32 m0, s63, 16384\n\t" \
  "s_mov_b32 s60, 1048576\n\t" \
  "buffer_load_dwordx4 v16, %[W], s60 offen lds\n\t" \
  "s_add_u32 m0, s63, 17408\n\t" \
  "s_mov_b32 s60, 1114112\n\t" \
  "buffer_load_dwordx4 v16, %[W], s60 offen lds\n\t" \
  "s_add_u32 m0, s63, 18432\n\t" \
  "s_mov_b32 s60, 1179648\n\t" \
  "buffer_load_dwordx4 v16, %[W], s60 offen lds\n\t" \
  "s_add_u32 m0, s63, 19456\n\t" \
  "s_mov_b32 s60, 1245184\n\t" \
  "buffer_load_dwordx4 v16, %[W], s60 offen lds\n\t" \
  "s_add_u32 m0, s63, 20480\n\t" \
  "s_mov_b32 s60, 1310720\n\t" \
  "buffer_load_dwordx4 v16, %[W], s60 offen lds\n\t" \
  "s_add_u32 m0, s63, 21504\n\t" \
  "s_mov_b32 s60, 1376256\n\t" \
  "buffer_load_dwordx4 v16, %[W], s60 offen lds\n\t" \
  "s_add_u32 m0, s63, 22528\n\t" \
  "s_mov_b32 s60, 1441792\n\t" \
  "buffer_load_dwordx4 v16, %[W], s60 offen lds\n\t" \
  "s_add_u32 m0, s63, 23552\n\t" \
  "s_mov_b32 s60, 1507328\n\t" \
  "buffer_load_dwordx4 v16, %[W], s60 offen lds\n\t" \
  "s_add_u32 m0, s63, 24576\n\t" \
  "s_mov_b32 s60, 1572864\n\t" \
  "buffer_load_dwordx4 v16, %[W], s60 offen lds\n\t" \
  "s_add_u32 m0, s63, 25600\n\t" \
  "s_mov_b32 s60, 1638400\n\t" \
  "buffer_load_dwordx4 v16, %[W], s60 offen lds\n\t" \
  "s_add_u32 m0, s63, 26624\n\t" \
  "s_mov_b32 s60, 1703936\n\t" \
  "buffer_load_dwordx4 v16, %[W], s60 offen lds\n\t" \
  "s_add_u32 m0, s63, 27648\n\t" \
  "s_mov_b32 s60, 1769472\n\t" \
  "buffer_load_dwordx4 v16, %[W], s60 offen lds\n\t" \
  "s_add_u32 m0, s63, 28672\n\t" \
  "s_mov_b32 s60, 1835008\n\t" \
  "buffer_load_dwordx4 v16, %[W], s60 offen lds\n\t" \
  "s_add_u32 m0, s63, 29696\n\t" \
  "s_mov_b32 s60, 1900544\n\t" \
  "buffer_load_dwordx4 v16, %[W], s60 offen lds\n\t" \
  "s_add_u32 m0, s63, 30720\n\t" \
  "s_mov_b32 s60, 1966080\n\t" \
  "buffer_load_dwordx4 v16, %[W], s60 offen lds\n\t" \
  "s_add_u32 m0, s63, 31744\n\t" \
  "s_mov_b32 s60, 2031616\n\t" \
  "buffer_load_dwordx4 v16, %[W], s60 offen lds\n\t" \
  "s_add_u32 m0, s63, 32768\n\t" \
  "s_mov_b32 s60, 2097152\n\t" \
  "buffer_load_dwordx4 v16, %[W], s60 offen lds\n\t" \
  "s_add_u32 m0, s63, 33792\n\t" \
  "s_mov_b32 s60, 2162688\n\t" \
  "buffer_load_dwordx4 v16, %[W], s60 offen lds\n\t" \
  "s_add_u32 m0, s63, 34816\n\t" \
  "s_mov_b32 s60, 2228224\n\t" \
  "buffer_load_dwordx4 v16, %[W], s60 offen lds\n\t" \
  "s_add_u32 m0, s63, 35840\n\t" \
  "s_mov_b32 s60, 2293760\n\t" \
  "buffer_load_dwordx4 v16, %[W], s60 offen lds\n\t" \
  "s_add_u32 m0, s63, 36864\n\t" \
  "s_mov_b32 s60, 2359296\n\t" \
  "buffer_load_dwordx4 v16, %[W], s60 offen lds\n\t" \
  "s_add_u32 m0, s63, 37888\n\t" \
  "s_mov_b32 s60, 2424832\n\t" \
  "buffer_load_dwordx4 v16, %[W], s60 offen lds\n\t" \
  "s_add_u32 m0, s63, 38912\n\t" \
  "s_mov_b32 s60, 2490368\n\t" \
  "buffer_load_dwordx4 v16, %[W], s60 offen lds\n\t" \
  "s_add_u32 m0, s63, 39936\n\t" \
  "s_mov_b32 s60, 2555904\n\t" \
  "buffer_load_dwordx4 v16, %[W], s60 offen lds\n\t" \
  "s_waitcnt vmcnt(32)\n\t" \
  "ds_read_b64_tr_b16 a[0:1], v17 offset:0\n\t" \
  "ds_read_b64_tr_b16 a[2:3], v17 offset:512\n\t" \
  "ds_read_b64_tr_b16 a[4:5], v17 offset:4096\n\t" \
  "ds_read_b64_tr_b16 a[6:7], v17 offset:4608\n\t" \
  "ds_read_b64_tr_b16 a[72:73], v17 offset:8\n\t" \
  "ds_read_b64_tr_b16 a[74:75], v17 offset:520\n\t" \
  "ds_read_b64_tr_b16 a[76:77], v17 offset:4104\n\t" \
  "ds_read_b64_tr_b16 a[78:79], v17 offset:4616\n\t" \
  "ds_read_b64_tr_b16 a[144:145], v18 offset:0\n\t" \
  "ds_read_b64_tr_b16 a[146:147], v18 offset:512\n\t" \
  "ds_read_b64_tr_b16 a[148:149], v18 offset:4096\n\t" \
  "ds_read_b64_tr_b16 a[150:151], v18 offset:4608\n\t" \
  "ds_read_b64_tr_b16 a[216:217], v18 offset:8\n\t" \
  "ds_read_b64_tr_b16 a[218:219], v18 offset:520\n\t" \
  "ds_read_b64_tr_b16 a[220:221], v18 offset:4104\n\t" \
  "ds_read_b64_tr_b16 a[222:223], v18 offset:4616\n\t" \
  "s_waitcnt lgkmcnt(0)\n\t" \
  "s_add_u32 m0, s63, 0\n\t" \
  "s_mov_b32 s60, 2621440\n\t" \
  "buffer_load_dwordx4 v16, %[W], s60 offen lds\n\t" \
  "s_add_u32 m0, s63, 1024\n\t" \
  "s_mov_b32 s60, 2686976\n\t" \
  "buffer_load_dwordx4 v16, %[W], s60 offen lds\n\t" \
  "s_add_u32 m0, s63, 2048\n\t" \
  "s_mov_b32 s60, 2752512\n\t" \
  "buffer_load_dwordx4 v16, %[W], s60 offen lds\n\t" \
  "s_add_u32 m0, s63, 3072\n\t" \
  "s_mov_b32 s60, 2818048\n\t" \
  "buffer_load_dwordx4 v16, %[W], s60 offen lds\n\t" \
  "s_add_u32 m0, s63, 4096\n\t" \
  "s_mov_b32 s60, 2883584\n\t" \
  "buffer_load_dwordx4 v16, %[W], s60 offen lds\n\t" \
  "s_add_u32 m0, s63, 5120\n\t" \
  "s_mov_b32 s60, 2949120\n\t" \
  "buffer_load_dwordx4 v16, %[W], s60 offen lds\n\t" \
  "s_add_u32 m0, s63, 6144\n\t" \
  "s_mov_b32 s60, 3014656\n\t" \
  "buffer_load_dwordx4 v16, %[W], s60 offen lds\n\t" \
  "s_add_u32 m0, s63, 7168\n\t" \
  "s_mov_b32 s60, 3080192\n\t" \
  "buffer_load_dwordx4 v16, %[W], s60 offen lds\n\t" \
  "s_waitcnt vmcnt(32)\n\t" \
  "ds_read_b64_tr_b16 a[8:9], v17 offset:8192\n\t" \
  "ds_read_b64_tr_b16 a[10:11], v17 offset:8704\n\t" \
  "ds_read_b64_tr_b16 a[12:13], v17 offset:12288\n\t" \
  "ds_read_b64_tr_b16 a[14:15], v17 offset:12800\n\t" \
  "ds_read_b64_tr_b16 a[80:81], v17 offset:8200\n\t" \
  "ds_read_b64_tr_b16 a[82:83], v17 offset:8712\n\t" \
  "ds_read_b64_tr_b16 a[84:85], v17 offset:12296\n\t" \
  "ds_read_b64_tr_b16 a[86:87], v17 offset:12808\n\t" \
  "ds_read_b64_tr_b16 a[152:153], v18 offset:8192\n\t" \
  "ds_read_b64_tr_b16 a[154:155], v18 offset:8704\n\t" \
  "ds_read_b64_tr_b16 a[156:157], v18 offset:12288\n\t" \
  "ds_read_b64_tr_b16 a[158:159], v18 offset:12800\n\t" \
  "ds_read_b64_tr_b16 a[224:225], v18 offset:8200\n\t" \
  "ds_read_b64_tr_b16 a[226:227], v18 offset:8712\n\t" \
  "ds_read_b64_tr_b16 a[228:229], v18 offset:12296\n\t" \
  "ds_read_b64_tr_b16 a[230:231], v18 offset:12808\n\t" \
  "s_waitcnt lgkmcnt(0)\n\t" \
  "s_add_u32 m0, s63, 8192\n\t" \
  "s_mov_b32 s60, 3145728\n\t" \
  "buffer_load_dwordx4 v16, %[W], s60 offen lds\n\t" \
  "s_add_u32 m0, s63, 9216\n\t" \
  "s_mov_b32 s60, 3211264\n\t" \
  "buffer_load_dwordx4 v16, %[W], s60 offen lds\n\t" \
  "s_add_u32 m0, s63, 10240\n\t" \
  "s_mov_b32 s60, 3276800\n\t" \
  "buffer_load_dwordx4 v16, %[W], s60 offen lds\n\t" \
  "s_add_u32 m0, s63, 11264\n\t" \
  "s_mov_b32 s60, 3342336\n\t" \
  "buffer_load_dwordx4 v16, %[W], s60 offen lds\n\t" \
  "s_add_u32 m0, s63, 12288\n\t" \
  "s_mov_b32 s60, 3407872\n\t" \
  "buffer_load_dwordx4 v16, %[W], s60 offen lds\n\t" \
  "s_add_u32 m0, s63, 13312\n\t" \
  "s_mov_b32 s60, 3473408\n\t" \
  "buffer_load_dwordx4 v16, %[W], s60 offen lds\n\t" \
  "s_add_u32 m0, s63, 14336\n\t" \
  "s_mov_b32 s60, 3538944\n\t" \
  "buffer_load_dwordx4 v16, %[W], s60 offen lds\n\t" \
  "s_add_u32 m0, s63, 15360\n\t" \
  "s_mov_b32 s60, 3604480\n\t" \
  "buffer_load_dwordx4 v16, %[W], s60 offen lds\n\t" \
  "s_waitcnt vmcnt(32)\n\t" \
  "ds_read_b64_tr_b16 a[16:17], v17 offset:16384\n\t" \
  "ds_read_b64_tr_b16 a[18:19], v17 offset:16896\n\t" \
  "ds_read_b64_tr_b16 a[20:21], v17 offset:20480\n\t" \
  "ds_read_b64_tr_b16 a[22:23], v17 offset:20992\n\t" \
  "ds_read_b64_tr_b16 a[88:89], v17 offset:16392\n\t" \
  "ds_read_b64_tr_b16 a[90:91], v17 offset:16904\n\t" \
  "ds_read_b64_tr_b16 a[92:93], v17 offset:20488\n\t" \
  "ds_read_b64_tr_b16 a[94:95], v17 offset:21000\n\t" \
  "ds_read_b64_tr_b16 a[160:161], v18 offset:16384\n\t" \
  "ds_read_b64_tr_b16 a[162:163], v18 offset:16896\n\t" \
  "ds_read_b64_tr_b16 a[164:165], v18 offset:20480\n\t" \
  "ds_read_b64_tr_b16 a[166:167], v18 offset:20992\n\t" \
  "ds_read_b64_tr_b16 a[232:233], v18 offset:16392\n\t" \
  "ds_read_b64_tr_b16 a[234:235], v18 offset:16904\n\t" \
  "ds_read_b64_tr_b16 a[236:237], v18 offset:20488\n\t" \
  "ds_read_b64_tr_b16 a[238:239], v18 offset:21000\n\t" \
  "s_waitcnt lgkmcnt(0)\n\t" \
  "s_add_u32 m0, s63, 16384\n\t" \
  "s_mov_b32 s60, 3670016\n\t" \
  "buffer_load_dwordx4 v16, %[W], s60 offen lds\n\t" \
  "s_add_u32 m0, s63, 17408\n\t" \
  "s_mov_b32 s60, 3735552\n\t" \
  "buffer_load_dwordx4 v16, %[W], s60 offen lds\n\t" \
  "s_add_u32 m0, s63, 18432\n\t" \
  "s_mov_b32 s60, 3801088\n\t" \
  "buffer_load_dwordx4 v16, %[W], s60 offen lds\n\t" \
  "s_add_u32 m0, s63, 19456\n\t" \
  "s_mov_b32 s60, 3866624\n\t" \
  "buffer_load_dwordx4 v16, %[W], s60 offen lds\n\t" \
  "s_add_u32 m0, s63, 20480\n\t" \
  "s_mov_b32 s60, 3932160\n\t" \
  "buffer_load_dwordx4 v16, %[W], s60 offen lds\n\t" \
  "s_add_u32 m0, s63, 21504\n\t" \
  "s_mov_b32 s60, 3997696\n\t" \
  "buffer_load_dwordx4 v16, %[W], s60 offen lds\n\t" \
  "s_add_u32 m0, s63, 22528\n\t" \
  "s_mov_b32 s60, 4063232\n\t" \
  "buffer_load_dwordx4 v16, %[W], s60 offen lds\n\t" \
  "s_add_u32 m0, s63, 23552\n\t" \
  "s_mov_b32 s60, 4128768\n\t" \
  "buffer_load_dwordx4 v16, %[W], s60 offen lds\n\t" \
  "s_waitcnt vmcnt(32)\n\t" \
  "ds_read_b64_tr_b16 a[24:25], v17 offset:24576\n\t" \
  "ds_read_b64_tr_b16 a[26:27], v17 offset:25088\n\t" \
  "ds_read_b64_tr_b16 a[28:29], v17 offset:28672\n\t" \
  "ds_read_b64_tr_b16 a[30:31], v17 offset:29184\n\t" \
  "ds_read_b64_tr_b16 a[96:97], v17 offset:24584\n\t" \
  "ds_read_b64_tr_b16 a[98:99], v17 offset:25096\n\t" \
  "ds_read_b64_tr_b16 a[100:101], v17 offset:28680\n\t" \
  "ds_read_b64_tr_b16 a[102:103], v17 offset:29192\n\t" \
  "ds_read_b64_tr_b16 a[168:169], v18 offset:24576\n\t" \
  "ds_read_b64_tr_b16 a[170:171], v18 offset:25088\n\t" \
  "ds_read_b64_tr_b16 a[172:173], v18 offset:28672\n\t" \
  "ds_read_b64_tr_b16 a[174:175], v18 offset:29184\n\t" \
  "ds_read_b64_tr_b16 a[240:241], v18 offset:24584\n\t" \
  "ds_read_b64_tr_b16 a[242:243], v18 offset:25096\n\t" \
  "ds_read_b64_tr_b16 a[244:245], v18 offset:28680\n\t" \
  "ds_read_b64_tr_b16 a[246:247], v18 offset:29192\n\t" \
  "s_waitcnt lgkmcnt(0)\n\t" \
  "s_add_u32 m0, s63, 24576\n\t" \
  "s_mov_b32 s60, 4194304\n\t" \
  "buffer_load_dwordx4 v16, %[W], s60 offen lds\n\t" \
  "s_add_u32 m0, s63, 25600\n\t" \
  "s_mov_b32 s60, 4259840\n\t" \
  "buffer_load_dwordx4 v16, %[W], s60 offen lds\n\t" \
  "s_add_u32 m0, s63, 26624\n\t" \
  "s_mov_b32 s60, 4325376\n\t" \
  "buffer_load_dwordx4 v16, %[W], s60 offen lds\n\t" \
  "s_add_u32 m0, s63, 27648\n\t" \
  "s_mov_b32 s60, 4390912\n\t" \
  "buffer_load_dwordx4 v16, %[W], s60 offen lds\n\t" \
  "s_add_u32 m0, s63, 28672\n\t" \
  "s_mov_b32 s60, 4456448\n\t" \
  "buffer_load_dwordx4 v16, %[W], s60 offen lds\n\t" \
  "s_add_u32 m0, s63, 29696\n\t" \
  "s_mov_b32 s60, 4521984\n\t" \
  "buffer_load_dwordx4 v16, %[W], s60 offen lds\n\t" \
  "s_add_u32 m0, s63, 30720\n\t" \
  "s_mov_b32 s60, 4587520\n\t" \
  "buffer_load_dwordx4 v16, %[W], s60 offen lds\n\t" \
  "s_add_u32 m0, s63, 31744\n\t" \
  "s_mov_b32 s60, 4653056\n\t" \
  "buffer_load_dwordx4 v16, %[W], s60 offen lds\n\t" \
  "s_waitcnt vmcnt(32)\n\t" \
  "ds_read_b64_tr_b16 a[32:33], v17 offset:32768\n\t" \
  "ds_read_b64_tr_b16 a[34:35], v17 offset:33280\n\t" \
  "ds_read_b64_tr_b16 a[36:37], v17 offset:36864\n\t" \
  "ds_read_b64_tr_b16 a[38:39], v17 offset:37376\n\t" \
  "ds_read_b64_tr_b16 a[104:105], v17 offset:32776\n\t" \
  "ds_read_b64_tr_b16 a[106:107], v17 offset:33288\n\t" \
  "ds_read_b64_tr_b16 a[108:109], v17 offset:36872\n\t" \
  "ds_read_b64_tr_b16 a[110:111], v17 offset:37384\n\t" \
  "ds_read_b64_tr_b16 a[176:177], v18 offset:32768\n\t" \
  "ds_read_b64_tr_b16 a[178:179], v18 offset:33280\n\t" \
  "ds_read_b64_tr_b16 a[180:181], v18 offset:36864\n\t" \
  "ds_read_b64_tr_b16 a[182:183], v18 offset:37376\n\t" \
  "ds_read_b64_tr_b16 a[248:249], v18 offset:32776\n\t" \
  "ds_read_b64_tr_b16 a[250:251], v18 offset:33288\n\t" \
  "ds_read_b64_tr_b16 a[252:253], v18 offset:36872\n\t" \
  "ds_read_b64_tr_b16 a[254:255], v18 offset:37384\n\t" \
  "s_waitcnt lgkmcnt(0)\n\t" \
  "s_waitcnt vmcnt(24)\n\t" \
  "ds_read_b64_tr_b16 a[40:41], v17 offset:0\n\t" \
  "ds_read_b64_tr_b16 a[42:43], v17 offset:512\n\t" \
  "ds_read_b64_tr_b16 a[44:45], v17 offset:4096\n\t" \
  "ds_read_b64_tr_b16 a[46:47], v17 offset:4608\n\t" \
  "ds_read_b64_tr_b16 a[112:113], v17 offset:8\n\t" \
  "ds_read_b64_tr_b16 a[114:115], v17 offset:520\n\t" \
  "ds_read_b64_tr_b16 a[116:117], v17 offset:4104\n\t" \
  "ds_read_b64_tr_b16 a[118:119], v17 offset:4616\n\t" \
  "ds_read_b64_tr_b16 a[184:185], v18 offset:0\n\t" \
  "ds_read_b64_tr_b16 a[186:187], v18 offset:512\n\t" \
  "ds_read_b64_tr_b16 a[188:189], v18 offset:4096\n\t" \
  "ds_read_b64_tr_b16 a[190:191], v18 offset:4608\n\t" \
  "ds_read_b64_tr_b16 v[224:225], v18 offset:8\n\t" \
  "ds_read_b64_tr_b16 v[226:227], v18 offset:520\n\t" \
  "ds_read_b64_tr_b16 v[228:229], v18 offset:4104\n\t" \
  "ds_read_b64_tr_b16 v[230:231], v18 offset:4616\n\t" \
  "s_waitcnt lgkmcnt(0)\n\t" \
  "s_waitcnt vmcnt(16)\n\t" \
  "ds_read_b64_tr_b16 a[48:49], v17 offset:8192\n\t" \
  "ds_read_b64_tr_b16 a[50:51], v17 offset:8704\n\t" \
  "ds_read_b64_tr_b16 a[52:53], v17 offset:12288\n\t" \
  "ds_read_b64_tr_b16 a[54:55], v17 offset:12800\n\t" \
  "ds_read_b64_tr_b16 a[120:121], v17 offset:8200\n\t" \
  "ds_read_b64_tr_b16 a[122:123], v17 offset:8712\n\t" \
  "ds_read_b64_tr_b16 a[124:125], v17 offset:12296\n\t" \
  "ds_read_b64_tr_b16 a[126:127], v17 offset:12808\n\t" \
  "ds_read_b64_tr_b16 a[192:193], v18 offset:8192\n\t" \
  "ds_read_b64_tr_b16 a[194:195], v18 offset:8704\n\t" \
  "ds_read_b64_tr_b16 a[196:197], v18 offset:12288\n\t" \
  "ds_read_b64_tr_b16 a[198:199], v18 offset:12800\n\t" \
  "ds_read_b64_tr_b16 v[232:233], v18 offset:8200\n\t" \
  "ds_read_b64_tr_b16 v[234:235], v18 offset:8712\n\t" \
  "ds_read_b64_tr_b16 v[236:237], v18 offset:12296\n\t" \
  "ds_read_b64_tr_b16 v[238:239], v18 offset:12808\n\t" \
  "s_waitcnt lgkmcnt(0)\n\t" \
  "s_waitcnt vmcnt(8)\n\t" \
  "ds_read_b64_tr_b16 a[56:57], v17 offset:16384\n\t" \
  "ds_read_b64_tr_b16 a[58:59], v17 offset:16896\n\t" \
  "ds_read_b64_tr_b16 a[60:61], v17 offset:20480\n\t" \
  "ds_read_b64_tr_b16 a[62:63], v17 offset:20992\n\t" \
  "ds_read_b64_tr_b16 a[128:129], v17 offset:16392\n\t" \
  "ds_read_b64_tr_b16 a[130:131], v17 offset:16904\n\t" \
  "ds_read_b64_tr_b16 a[132:133], v17 offset:20488\n\t" \
  "ds_read_b64_tr_b16 a[134:135], v17 offset:21000\n\t" \
  "ds_read_b64_tr_b16 a[200:201], v18 offset:16384\n\t" \
  "ds_read_b64_tr_b16 a[202:203], v18 offset:16896\n\t" \
  "ds_read_b64_tr_b16 a[204:205], v18 offset:20480\n\t" \
  "ds_read_b64_tr_b16 a[206:207], v18 offset:20992\n\t" \
  "ds_read_b64_tr_b16 v[240:241], v18 offset:16392\n\t" \
  "ds_read_b64_tr_b16 v[242:243], v18 offset:16904\n\t" \
  "ds_read_b64_tr_b16 v[244:245], v18 offset:20488\n\t" \
  "ds_read_b64_tr_b16 v[246:247], v18 offset:21000\n\t" \
  "s_waitcnt lgkmcnt(0)\n\t" \
  "s_waitcnt vmcnt(0)\n\t" \
  "ds_read_b64_tr_b16 a[64:65], v17 offset:24576\n\t" \
  "ds_read_b64_tr_b16 a[66:67], v17 offset:25088\n\t" \
  "ds_read_b64_tr_b16 a[68:69], v17 offset:28672\n\t" \
  "ds_read_b64_tr_b16 a[70:71], v17 offset:29184\n\t" \
  "ds_read_b64_tr_b16 a[136:137], v17 offset:24584\n\t" \
  "ds_read_b64_tr_b16 a[138:139], v17 offset:25096\n\t" \
  "ds_read_b64_tr_b16 a[140:141], v17 offset:28680\n\t" \
  "ds_read_b64_tr_b16 a[142:143], v17 offset:29192\n\t" \
  "ds_read_b64_tr_b16 a[208:209], v18 offset:24576\n\t" \
  "ds_read_b64_tr_b16 a[210:211], v18 offset:25088\n\t" \
  "ds_read_b64_tr_b16 a[212:213], v18 offset:28672\n\t" \
  "ds_read_b64_tr_b16 a[214:215], v18 offset:29184\n\t" \
  "ds_read_b64_tr_b16 v[248:249], v18 offset:24584\n\t" \
  "ds_read_b64_tr_b16 v[250:251], v18 offset:25096\n\t" \
  "ds_read_b64_tr_b16 v[252:253], v18 offset:28680\n\t" \
  "ds_read_b64_tr_b16 v[254:255], v18 offset:29192\n\t" \
  "s_waitcnt lgkmcnt(0)\n\t" \
  "s_mov_b32 s84, " TK_CAP "\n\t" \
  "s_mov_b32 s48, 0\n\t" \
  "s_mov_b32 s49, 0xf0000000\n\t" \
  "s_mov_b32 s46, s41\n\t" \
  "s_mov_b32 s56, 0\n\t" \
  "v_lshrrev_b32 v7, 2, v0\n\t" \
  "s_mov_b32 s60, " TK_CTAB "\n\t" \
  "buffer_load_dword v4, v7, %[X], s60 offen offset:88 sc1\n\t" \
  "buffer_load_dword v7, v7, %[X], s60 offen offset:0 sc1\n\t" \
  "s_waitcnt vmcnt(0)\n\t" \
  "v_and_b32 v4, 1, v4\n\t" \
  "v_and_b32 v7, 1, v7\n\t" \
  "v_cmp_ne_u32 s[90:91], s67, v4\n\t" \
  "v_cmp_ne_u32 s[88:89], s67, v7\n\t" \
  "s_nop 1\n\t" \
  "s_mov_b32 s57, 0xf0000000\n\t" \
  "s_add_u32 s83, s48, 0\n\t" \
  "s_lshr_b64 s[60:61], s[90:91], s83\n\t" \
  "s_and_b32 s60, s60, 1\n\t" \
  "s_lshl_b32 s60, s60, 13\n\t" \
  "s_add_u32 s83, s83, " TK_SG0 "\n\t" \
  "s_lshl_b32 s83, s83, 21\n\t" \
  "s_or_b32 s83, s83, s60\n\t" \
  "s_add_u32 s62, s83, s39\n\t" \
  "v_add_u32 v7, s62, v3\n\t" \
  "LCOLD_%=:\n\t" \
  "s_waitcnt vmcnt(0)\n\t" \
  "s_mov_b64 exec, 1\n\t" \
  "buffer_store_dword v6, off, %[X], s49 sc1\n\t" \
  "s_mov_b64 exec, -1\n\t" \
  "s_mov_b32 s49, 0xf0000000\n\t" \
  "s_add_u32 s55, s83, s36\n\t" \
  "s_cmp_lg_u32 s40, 0\n\t" \
  "s_cbranch_scc1 LGO_%=\n\t" \
  "s_and_b32 s62, s45, 3\n\t" \
  "s_lshl_b32 s62, s62, 7\n\t" \
  "s_add_u32 s62, s62, s83\n\t" \
  "s_add_u32 s62, s62, s39\n\t" \
  "s_mov_b32 s61, 0\n\t" \
  "LPO_%=:\n\t" \
  "buffer_load_dword v4, off, %[X], s62 sc1\n\t" \
  "s_waitcnt vmcnt(0)\n\t" \
  "v_cmp_eq_u32 vcc, 0, v4\n\t" \
  "s_cbranch_vccz LPO_%=x\n\t" \
  "s_add_u32 s61, s61, 1\n\t" \
  "s_sleep " TK_PSH "\n\t" \
  "s_cmp_gt_u32 s61, s84\n\t" \
  "s_cbranch_scc1 LPO_%=g\n\t" \
  "s_and_b32 s60, s61, 1023\n\t" \
  "s_cmp_lg_u32 s60, 0\n\t" \
  "s_cbranch_scc1 LPO_%=\n\t" \
  "buffer_load_dword v4, off, %[X], s92 sc1\n\t" \
  "s_waitcnt vmcnt(0)\n\t" \
  "v_readfirstlane_b32 s60, v4\n\t" \
  "s_nop 0\n\t" \
  "s_cmp_eq_u32 s60, 0\n\t" \
  "s_cbranch_scc1 LPO_%=\n\t" \
  "LPO_%=g:\n\t" \
  "s_mov_b32 s84, 0\n\t" \
  "s_or_b32 s60, s48, 1610612736\n\t" \
  "v_mov_b32 v4, s60\n\t" \
  "s_mov_b64 exec, 1\n\t" \
  "buffer_store_dword v4, off, %[X], s92 sc1\n\t" \
  "s_mov_b64 exec, -1\n\t" \
  "LPO_%=x:\n\t" \
  "s_mov_b32 s61, 0\n\t" \
  "LPH_%=:\n\t" \
  "buffer_load_dword v4, v7, %[X], 0 offen sc1\n\t" \
  "s_waitcnt vmcnt(0)\n\t" \
  "v_cmp_eq_u32 vcc, 0, v4\n\t" \
  "s_cbranch_vccz LPH_%=x\n\t" \
  "s_add_u32 s61, s61, 1\n\t" \
  "s_sleep " TK_PSH "\n\t" \
  "s_cmp_gt_u32 s61, s84\n\t" \
  "s_cbranch_scc1 LPH_%=g\n\t" \
  "s_and_b32 s60, s61, 1023\n\t" \
  "s_cmp_lg_u32 s60, 0\n\t" \
  "s_cbranch_scc1 LPH_%=\n\t" \
  "buffer_load_dword v4, off, %[X], s92 sc1\n\t" \
  "s_waitcnt vmcnt(0)\n\t" \
  "v_readfirstlane_b32 s60, v4\n\t" \
  "s_nop 0\n\t" \
  "s_cmp_eq_u32 s60, 0\n\t" \
  "s_cbranch_scc1 LPH_%=\n\t" \
  "LPH_%=g:\n\t" \
  "s_mov_b32 s84, 0\n\t" \
  "s_or_b32 s60, s48, 1610612736\n\t" \
  "v_mov_b32 v4, s60\n\t" \
  "s_mov_b64 exec, 1\n\t" \
  "buffer_store_dword v4, off, %[X], s92 sc1\n\t" \
  "s_mov_b64 exec, -1\n\t" \
  "LPH_%=x:\n\t" \
  "LGO_%=:\n\t" \
  "s_barrier\n\t" \
  "s_add_u32 m0, s46, 0\n\t" \
  "s_add_u32 s47, s55, 0\n\t" \
  "buffer_load_dwordx4 v0, %[X], s47 offen sc1 lds\n\t" \
  "s_add_u32 m0, s46, 4096\n\t" \
  "s_add_u32 s47, s55, 4096\n\t" \
  "buffer_load_dwordx4 v0, %[X], s47 offen sc1 lds\n\t" \
  "s_add_u32 m0, s46, 8192\n\t" \
  "s_add_u32 s47, s55, 16384\n\t" \
  "buffer_load_dwordx4 v0, %[X], s47 offen sc1 lds\n\t" \
  "s_add_u32 m0, s46, 12288\n\t" \
  "s_add_u32 s47, s55, 20480\n\t" \
  "buffer_load_dwordx4 v0, %[X], s47 offen sc1 lds\n\t" \
  "s_add_u32 m0, s46, 16384\n\t" \
  "s_add_u32 s47, s55, 32768\n\t" \
  "buffer_load_dwordx4 v0, %[X], s47 offen sc1 lds\n\t" \
  "s_add_u32 m0, s46, 20480\n\t" \
  "s_add_u32 s47, s55, 36864\n\t" \
  "buffer_load_dwordx4 v0, %[X], s47 offen sc1 lds\n\t" \
  "s_add_u32 m0, s46, 24576\n\t" \
  "s_add_u32 s47, s55, 49152\n\t" \
  "buffer_load_dwordx4 v0, %[X], s47 offen sc1 lds\n\t" \
  "s_add_u32 m0, s46, 28672\n\t" \
  "s_add_u32 s47, s55, 53248\n\t" \
  "buffer_load_dwordx4 v0, %[X], s47 offen sc1 lds\n\t" \
  "s_add_u32 m0, s46, 32768\n\t" \
  "s_add_u32 s47, s55, 65536\n\t" \
  "buffer_load_dwordx4 v0, %[X], s47 offen sc1 lds\n\t" \
  "s_waitcnt vmcnt(5)\n\t" \
  "s_cmp_lt_i32 s57, 0\n\t" \
  "s_cbranch_scc1 LTOP_%=\n\t" \
  "s_movk_i32 s63, 4000\n\t" \
  "s_branch LGC_%=u\n\t" \
  "LGC_%=:\n\t" \
  "s_sleep " TK_PSG "\n\t" \
  "buffer_load_dword v8, off, %[X], s57 sc1\n\t" \
  "s_waitcnt vmcnt(0)\n\t" \
  "LGC_%=u:\n\t" \
  "s_memrealtime s[52:53]\n\t" \
  "v_readfirstlane_b32 s60, v8\n\t" \
  "s_waitcnt lgkmcnt(0)\n\t" \
  "s_sub_u32 s63, s63, 1\n\t" \
  "s_cbranch_scc1 LTOP_%=\n\t" \
  "s_cmp_eq_u32 s60, -1\n\t" \
  "s_cbranch_scc1 LGC_%=\n\t" \
  "s_not_b32 s60, s60\n\t" \
  "s_lshl_b32 s61, s52, 8\n\t" \
  "s_sub_u32 s60, s60, s61\n\t" \
  "s_ashr_i32 s60, s60, 8\n\t" \
  "s_sub_i32 s60, s60, " TK_QXC "\n\t" \
  "s_min_i32 s60, s60, 400\n\t" \
  "s_add_u32 s54, s52, s60\n\t" \
  "LGC_%=w:\n\t" \
  "s_sleep 1\n\t" \
  "s_memrealtime s[52:53]\n\t" \
  "s_waitcnt lgkmcnt(0)\n\t" \
  "s_sub_i32 s53, s52, s54\n\t" \
  "s_cmp_lt_i32 s53, 0\n\t" \
  "s_cbranch_scc1 LGC_%=w\n\t" \
  "LTOP_%=:\n\t" \
  "s_barrier\n\t" \
  "ds_read_b128 v[64:67], v1 offset:0\n\t" \
  "ds_read_b128 v[68:71], v1 offset:2048\n\t" \
  "ds_read_b128 v[72:75], v2 offset:0\n\t" \
  "ds_read_b128 v[76:79], v2 offset:2048\n\t" \
  "ds_read_b128 v[80:83], v1 offset:4096\n\t" \
  "ds_read_b128 v[84:87], v1 offset:6144\n\t" \
  "s_mov_b32 s82, s83\n\t" \
  "s_xor_b32 s46, s46, 0x10000\n\t" \
  "s_waitcnt lgkmcnt(5)\n\t" \
  "v_mfma_f32_16x16x32_bf16 v[16:19], a[0:3], v[64:67], 0\n\t" \
  "ds_read_b128 v[88:91], v2 offset:4096\n\t" \
  "v_mfma_f32_16x16x32_bf16 v[20:23], a[72:75], v[64:67], 0\n\t" \
  "s_add_u32 s83, s48, 1\n\t" \
  "s_lshr_b64 s[60:61], s[90:91], s83\n\t" \
  "v_mfma_f32_16x16x32_bf16 v[24:27], a[144:147], v[64:67], 0\n\t" \
  "ds_read_b128 v[92:95], v2 offset:6144\n\t" \
  "v_mfma_f32_16x16x32_bf16 v[28:31], a[216:219], v[64:67], 0\n\t" \
  "s_and_b32 s60, s60, 1\n\t" \
  "s_lshl_b32 s60, s60, 13\n\t" \
  "s_waitcnt lgkmcnt(5)\n\t" \
  "v_mfma_f32_16x16x32_bf16 v[16:19], a[4:7], v[72:75], v[16:19]\n\t" \
  "ds_read_b128 v[96:99], v1 offset:8192\n\t" \
  "v_mfma_f32_16x16x32_bf16 v[20:23], a[76:79], v[72:75], v[20:23]\n\t" \
  "s_add_u32 s83, s83, " TK_SG0 "\n\t" \
  "s_lshl_b32 s83, s83, 21\n\t" \
  "v_mfma_f32_16x16x32_bf16 v[24:27], a[148:151], v[72:75], v[24:27]\n\t" \
  "ds_read_b128 v[100:103], v1 offset:10240\n\t" \
  "v_mfma_f32_16x16x32_bf16 v[28:31], a[220:223], v[72:75], v[28:31]\n\t" \
  "s_or_b32 s83, s83, s60\n\t" \
  "s_add_u32 s62, s83, s39\n\t" \
  "s_waitcnt lgkmcnt(5)\n\t" \
  "v_mfma_f32_16x16x32_bf16 v[16:19], a[8:11], v[80:83], v[16:19]\n\t" \
  "ds_read_b128 v[104:107], v2 offset:8192\n\t" \
  "v_mfma_f32_16x16x32_bf16 v[20:23], a[80:83], v[80:83], v[20:23]\n\t" \
  "v_add_u32 v7, s62, v3\n\t" \
  "s_add_u32 s50, s82, s37\n\t" \
  "v_mfma_f32_16x16x32_bf16 v[24:27], a[152:155], v[80:83], v[24:27]\n\t" \
  "ds_read_b128 v[108:111], v2 offset:10240\n\t" \
  "v_mfma_f32_16x16x32_bf16 v[28:31], a[224:227], v[80:83], v[28:31]\n\t" \
  "s_add_u32 s51, s50, 4096\n\t" \
  "s_waitcnt lgkmcnt(5)\n\t" \
  "v_mfma_f32_16x16x32_bf16 v[16:19], a[12:15], v[88:91], v[16:19]\n\t" \
  "ds_read_b128 v[112:115], v1 offset:12288\n\t" \
  "v_mfma_f32_16x16x32_bf16 v[20:23], a[84:87], v[88:91], v[20:23]\n\t" \
  "v_mfma_f32_16x16x32_bf16 v[24:27], a[156:159], v[88:91], v[24:27]\n\t" \
  "ds_read_b128 v[116:119], v1 offset:14336\n\t" \
  "v_mfma_f32_16x16x32_bf16 v[28:31], a[228:231], v[88:91], v[28:31]\n\t" \
  "s_waitcnt lgkmcnt(5)\n\t" \
  "v_mfma_f32_16x16x32_bf16 v[16:19], a[16:19], v[96:99], v[16:19]\n\t" \
  "ds_read_b128 v[120:123], v2 offset:12288\n\t" \
  "v_mfma_f32_16x16x32_bf16 v[20:23], a[88:91], v[96:99], v[20:23]\n\t" \
  "s_cmp_lg_u32 s56, 0\n\t" \
  "v_mfma_f32_16x16x32_bf16 v[24:27], a[160:163], v[96:99], v[24:27]\n\t" \
  "ds_read_b128 v[124:127], v2 offset:14336\n\t" \
  "v_mfma_f32_16x16x32_bf16 v[28:31], a[232:235], v[96:99], v[28:31]\n\t" \
  "s_cbranch_scc1 LWARM_%=\n\t" \
  "s_waitcnt vmcnt(0)\n\t" \
  "LWARM_%=:\n\t" \
  "s_barrier\n\t" \
  "s_waitcnt lgkmcnt(5)\n\t" \
  "v_mfma_f32_16x16x32_bf16 v[16:19], a[20:23], v[104:107], v[16:19]\n\t" \
  "ds_read_b128 v[128:131], v1 offset:16384\n\t" \
  "v_mfma_f32_16x16x32_bf16 v[20:23], a[92:95], v[104:107], v[20:23]\n\t" \
  "v_mfma_f32_16x16x32_bf16 v[24:27], a[164:167], v[104:107], v[24:27]\n\t" \
  "ds_read_b128 v[132:135], v1 offset:18432\n\t" \
  "v_mfma_f32_16x16x32_bf16 v[28:31], a[236:239], v[104:107], v[28:31]\n\t" \
  "s_add_i32 s62, s48, s42\n\t" \
  "s_add_i32 s62, s62, 1\n\t" \
  "s_waitcnt lgkmcnt(5)\n\t" \
  "v_mfma_f32_16x16x32_bf16 v[16:19], a[24:27], v[112:115], v[16:19]\n\t" \
  "ds_read_b128 v[136:139], v2 offset:16384\n\t" \
  "v_mfma_f32_16x16x32_bf16 v[20:23], a[96:99], v[112:115], v[20:23]\n\t" \
  "s_mul_i32 s63, s62, 43\n\t" \
  "s_lshr_b32 s63, s63, 7\n\t" \
  "v_mfma_f32_16x16x32_bf16 v[24:27], a[168:171], v[112:115], v[24:27]\n\t" \
  "ds_read_b128 v[140:143], v2 offset:18432\n\t" \
  "v_mfma_f32_16x16x32_bf16 v[28:31], a[240:243], v[112:115], v[28:31]\n\t" \
  "s_mul_i32 s57, s62, 688128\n\t" \
  "s_lshl_b32 s60, s63, 15\n\t" \
  "s_waitcnt lgkmcnt(5)\n\t" \
  "v_mfma_f32_16x16x32_bf16 v[16:19], a[28:31], v[120:123], v[16:19]\n\t" \
  "ds_read_b128 v[144:147], v1 offset:20480\n\t" \
  "v_mfma_f32_16x16x32_bf16 v[20:23], a[100:103], v[120:123], v[20:23]\n\t" \
  "s_add_u32 s57, s57, s60\n\t" \
  "s_lshr_b32 s60, s88, s63\n\t" \
  "v_mfma_f32_16x16x32_bf16 v[24:27], a[172:175], v[120:123], v[24:27]\n\t" \
  "ds_read_b128 v[148:151], v1 offset:22528\n\t" \
  "v_mfma_f32_16x16x32_bf16 v[28:31], a[244:247], v[120:123], v[28:31]\n\t" \
  "s_and_b32 s60, s60, 1\n\t" \
  "s_lshl_b32 s60, s60, 13\n\t" \
  "s_waitcnt lgkmcnt(5)\n\t" \
  "v_mfma_f32_16x16x32_bf16 v[16:19], a[32:35], v[128:131], v[16:19]\n\t" \
  "ds_read_b128 v[152:155], v2 offset:20480\n\t" \
  "v_mfma_f32_16x16x32_bf16 v[20:23], a[104:107], v[128:131], v[20:23]\n\t" \
  "s_add_u32 s57, s57, s60\n\t" \
  "s_add_u32 s57, s57, 656896\n\t" \
  "v_mfma_f32_16x16x32_bf16 v[24:27], a[176:179], v[128:131], v[24:27]\n\t" \
  "ds_read_b128 v[156:159], v2 offset:22528\n\t" \
  "v_mfma_f32_16x16x32_bf16 v[28:31], a[248:251], v[128:131], v[28:31]\n\t" \
  "s_cmp_lt_i32 s62, 1\n\t" \
  "s_cselect_b32 s57, 0xf0000000, s57\n\t" \
  "s_waitcnt lgkmcnt(5)\n\t" \
  "v_mfma_f32_16x16x32_bf16 v[16:19], a[36:39], v[136:139], v[16:19]\n\t" \
  "ds_read_b128 v[160:163], v1 offset:24576\n\t" \
  "v_mfma_f32_16x16x32_bf16 v[20:23], a[108:111], v[136:139], v[20:23]\n\t" \
  "s_cmp_lg_u32 s40, 0\n\t" \
  "s_cselect_b32 s57, 0xf0000000, s57\n\t" \
  "v_mfma_f32_16x16x32_bf16 v[24:27], a[180:183], v[136:139], v[24:27]\n\t" \
  "ds_read_b128 v[164:167], v1 offset:26624\n\t" \
  "v_mfma_f32_16x16x32_bf16 v[28:31], a[252:255], v[136:139], v[28:31]\n\t" \
  "s_waitcnt lgkmcnt(5)\n\t" \
  "v_mfma_f32_16x16x32_bf16 v[16:19], a[40:43], v[144:147], v[16:19]\n\t" \
  "ds_read_b128 v[168:171], v2 offset:24576\n\t" \
  "v_mfma_f32_16x16x32_bf16 v[20:23], a[112:115], v[144:147], v[20:23]\n\t" \
  "v_mfma_f32_16x16x32_bf16 v[24:27], a[184:187], v[144:147], v[24:27]\n\t" \
  "ds_read_b128 v[172:175], v2 offset:26624\n\t" \
  "v_mfma_f32_16x16x32_bf16 v[28:31], v[224:227], v[144:147], v[28:31]\n\t" \
  "s_waitcnt lgkmcnt(5)\n\t" \
  "v_mfma_f32_16x16x32_bf16 v[16:19], a[44:47], v[152:155], v[16:19]\n\t" \
  "ds_read_b128 v[176:179], v1 offset:28672\n\t" \
  "v_mfma_f32_16x16x32_bf16 v[20:23], a[116:119], v[152:155], v[20:23]\n\t" \
  "v_mfma_f32_16x16x32_bf16 v[24:27], a[188:191], v[152:155], v[24:27]\n\t" \
  "ds_read_b128 v[180:183], v1 offset:30720\n\t" \
  "v_mfma_f32_16x16x32_bf16 v[28:31], v[228:231], v[152:155], v[28:31]\n\t" \
  "s_waitcnt lgkmcnt(5)\n\t" \
  "v_mfma_f32_16x16x32_bf16 v[16:19], a[48:51], v[160:163], v[16:19]\n\t" \
  "ds_read_b128 v[184:187], v2 offset:28672\n\t" \
  "v_mfma_f32_16x16x32_bf16 v[20:23], a[120:123], v[160:163], v[20:23]\n\t" \
  "v_mfma_f32_16x16x32_bf16 v[24:27], a[192:195], v[160:163], v[24:27]\n\t" \
  "ds_read_b128 v[188:191], v2 offset:30720\n\t" \
  "v_mfma_f32_16x16x32_bf16 v[28:31], v[232:235], v[160:163], v[28:31]\n\t" \
  "s_waitcnt lgkmcnt(5)\n\t" \
  "v_mfma_f32_16x16x32_bf16 v[16:19], a[52:55], v[168:171], v[16:19]\n\t" \
  "ds_read_b128 v[192:195], v1 offset:32768\n\t" \
  "v_mfma_f32_16x16x32_bf16 v[20:23], a[124:127], v[168:171], v[20:23]\n\t" \
  "v_mfma_f32_16x16x32_bf16 v[24:27], a[196:199], v[168:171], v[24:27]\n\t" \
  "ds_read_b128 v[196:199], v1 offset:34816\n\t" \
  "v_mfma_f32_16x16x32_bf16 v[28:31], v[236:239], v[168:171], v[28:31]\n\t" \
  "s_waitcnt lgkmcnt(5)\n\t" \
  "v_mfma_f32_16x16x32_bf16 v[16:19], a[56:59], v[176:179], v[16:19]\n\t" \
  "ds_read_b128 v[200:203], v2 offset:32768\n\t" \
  "v_mfma_f32_16x16x32_bf16 v[20:23], a[128:131], v[176:179], v[20:23]\n\t" \
  "v_mfma_f32_16x16x32_bf16 v[24:27], a[200:203], v[176:179], v[24:27]\n\t" \
  "ds_read_b128 v[204:207], v2 offset:34816\n\t" \
  "v_mfma_f32_16x16x32_bf16 v[28:31], v[240:243], v[176:179], v[28:31]\n\t" \
  "s_waitcnt lgkmcnt(5)\n\t" \
  "v_mfma_f32_16x16x32_bf16 v[16:19], a[60:63], v[184:187], v[16:19]\n\t" \
  "v_mfma_f32_16x16x32_bf16 v[20:23], a[132:135], v[184:187], v[20:23]\n\t" \
  "buffer_load_dword v4, v7, %[X], 0 offen sc1\n\t" \
  "v_mfma_f32_16x16x32_bf16 v[24:27], a[204:207], v[184:187], v[24:27]\n\t" \
  "v_mfma_f32_16x16x32_bf16 v[28:31], v[244:247], v[184:187], v[28:31]\n\t" \
  "s_waitcnt lgkmcnt(3)\n\t" \
  "v_mfma_f32_16x16x32_bf16 v[16:19], a[64:67], v[192:195], v[16:19]\n\t" \
  "v_xor_b32 v1, 0x10000, v1\n\t" \
  "v_xor_b32 v2, 0x10000, v2\n\t" \
  "v_mfma_f32_16x16x32_bf16 v[20:23], a[136:139], v[192:195], v[20:23]\n\t" \
  "v_mfma_f32_16x16x32_bf16 v[24:27], a[208:211], v[192:195], v[24:27]\n\t" \
  "v_mfma_f32_16x16x32_bf16 v[28:31], v[248:251], v[192:195], v[28:31]\n\t" \
  "s_waitcnt lgkmcnt(1)\n\t" \
  "v_mfma_f32_16x16x32_bf16 v[16:19], a[68:71], v[200:203], v[16:19]\n\t" \
  "v_mfma_f32_16x16x32_bf16 v[20:23], a[140:143], v[200:203], v[20:23]\n\t" \
  "v_mfma_f32_16x16x32_bf16 v[24:27], a[212:215], v[200:203], v[24:27]\n\t" \
  "v_mfma_f32_16x16x32_bf16 v[28:31], v[252:255], v[200:203], v[28:31]\n\t" \
  "v_mfma_f32_16x16x32_bf16 v[32:35], a[0:3], v[68:71], 0\n\t" \
  "v_cvt_pk_bf16_f32 v48, v16, v17\n\t" \
  "v_cvt_pk_bf16_f32 v49, v18, v19\n\t" \
  "v_mfma_f32_16x16x32_bf16 v[36:39], a[72:75], v[68:71], 0\n\t" \
  "v_cvt_pk_bf16_f32 v50, v20, v21\n\t" \
  "v_cvt_pk_bf16_f32 v51, v22, v23\n\t" \
  "v_mfma_f32_16x16x32_bf16 v[32:35], a[4:7], v[76:79], v[32:35]\n\t" \
  "v_cvt_pk_bf16_f32 v52, v24, v25\n\t" \
  "v_cvt_pk_bf16_f32 v53, v26, v27\n\t" \
  "v_mfma_f32_16x16x32_bf16 v[36:39], a[76:79], v[76:79], v[36:39]\n\t" \
  "v_cvt_pk_bf16_f32 v54, v28, v29\n\t" \
  "v_cvt_pk_bf16_f32 v55, v30, v31\n\t" \
  "v_mfma_f32_16x16x32_bf16 v[32:35], a[8:11], v[84:87], v[32:35]\n\t" \
  "buffer_store_dwordx4 v[48:51], v0, %[X], s50 offen offset:0 sc1\n\t" \
  "v_mfma_f32_16x16x32_bf16 v[36:39], a[80:83], v[84:87], v[36:39]\n\t" \
  "v_mfma_f32_16x16x32_bf16 v[32:35], a[12:15], v[92:95], v[32:35]\n\t" \
  "v_mfma_f32_16x16x32_bf16 v[36:39], a[84:87], v[92:95], v[36:39]\n\t" \
  "v_mfma_f32_16x16x32_bf16 v[32:35], a[16:19], v[100:103], v[32:35]\n\t" \
  "v_mfma_f32_16x16x32_bf16 v[36:39], a[88:91], v[100:103], v[36:39]\n\t" \
  "v_mfma_f32_16x16x32_bf16 v[32:35], a[20:23], v[108:111], v[32:35]\n\t" \
  "v_mfma_f32_16x16x32_bf16 v[36:39], a[92:95], v[108:111], v[36:39]\n\t" \
  "v_mfma_f32_16x16x32_bf16 v[32:35], a[24:27], v[116:119], v[32:35]\n\t" \
  "v_mfma_f32_16x16x32_bf16 v[36:39], a[96:99], v[116:119], v[36:39]\n\t" \
  "v_mfma_f32_16x16x32_bf16 v[32:35], a[28:31], v[124:127], v[32:35]\n\t" \
  "v_mfma_f32_16x16x32_bf16 v[36:39], a[100:103], v[124:127], v[36:39]\n\t" \
  "s_waitcnt vmcnt(1)\n\t" \
  "v_cmp_eq_u32 vcc, 0, v4\n\t" \
  "v_mfma_f32_16x16x32_bf16 v[32:35], a[32:35], v[132:135], v[32:35]\n\t" \
  "v_mfma_f32_16x16x32_bf16 v[36:39], a[104:107], v[132:135], v[36:39]\n\t" \
  "s_cmp_eq_u64 vcc, 0\n\t" \
  "s_cselect_b32 s60, 1, 0\n\t" \
  "v_mov_b32 v4, s60\n\t" \
  "v_mfma_f32_16x16x32_bf16 v[32:35], a[36:39], v[140:143], v[32:35]\n\t" \
  "ds_write_b32 v5, v4\n\t" \
  "v_mfma_f32_16x16x32_bf16 v[36:39], a[108:111], v[140:143], v[36:39]\n\t" \
  "v_mfma_f32_16x16x32_bf16 v[32:35], a[40:43], v[148:151], v[32:35]\n\t" \
  "v_mfma_f32_16x16x32_bf16 v[36:39], a[112:115], v[148:151], v[36:39]\n\t" \
  "buffer_store_dwordx4 v[52:55], v0, %[X], s50 offen offset:1024 sc1\n\t" \
  "v_mfma_f32_16x16x32_bf16 v[32:35], a[44:47], v[156:159], v[32:35]\n\t" \
  "v_mfma_f32_16x16x32_bf16 v[36:39], a[116:119], v[156:159], v[36:39]\n\t" \
  "s_waitcnt lgkmcnt(0)\n\t" \
  "s_barrier\n\t" \
  "v_mfma_f32_16x16x32_bf16 v[32:35], a[48:51], v[164:167], v[32:35]\n\t" \
  "ds_read_b32 v4, v6 offset:40959\n\t" \
  "v_mfma_f32_16x16x32_bf16 v[36:39], a[120:123], v[164:167], v[36:39]\n\t" \
  "v_mfma_f32_16x16x32_bf16 v[32:35], a[52:55], v[172:175], v[32:35]\n\t" \
  "v_mfma_f32_16x16x32_bf16 v[36:39], a[124:127], v[172:175], v[36:39]\n\t" \
  "v_mfma_f32_16x16x32_bf16 v[32:35], a[56:59], v[180:183], v[32:35]\n\t" \
  "v_mfma_f32_16x16x32_bf16 v[36:39], a[128:131], v[180:183], v[36:39]\n\t" \
  "s_waitcnt lgkmcnt(0)\n\t" \
  "v_readfirstlane_b32 s60, v4\n\t" \
  "s_add_u32 s55, s83, s36\n\t" \
  "v_mfma_f32_16x16x32_bf16 v[32:35], a[60:63], v[188:191], v[32:35]\n\t" \
  "s_cmp_eq_u32 s48, 63\n\t" \
  "s_cselect_b32 s56, 0, s60\n\t" \
  "v_mfma_f32_16x16x32_bf16 v[36:39], a[132:135], v[188:191], v[36:39]\n\t" \
  "s_cmp_eq_u32 s56, 0\n\t" \
  "s_cselect_b32 s55, 0xf0000000, s55\n\t" \
  "v_mfma_f32_16x16x32_bf16 v[32:35], a[64:67], v[196:199], v[32:35]\n\t" \
  "v_mfma_f32_16x16x32_bf16 v[36:39], a[136:139], v[196:199], v[36:39]\n\t" \
  "v_mfma_f32_16x16x32_bf16 v[32:35], a[68:71], v[204:207], v[32:35]\n\t" \
  "s_add_u32 m0, s46, 0\n\t" \
  "s_add_u32 s47, s55, 0\n\t" \
  "buffer_load_dwordx4 v0, %[X], s47 offen sc1 lds\n\t" \
  "v_mfma_f32_16x16x32_bf16 v[36:39], a[140:143], v[204:207], v[36:39]\n\t" \
  "v_mfma_f32_16x16x32_bf16 v[40:43], a[144:147], v[68:71], 0\n\t" \
  "v_mfma_f32_16x16x32_bf16 v[44:47], a[216:219], v[68:71], 0\n\t" \
  "v_mfma_f32_16x16x32_bf16 v[40:43], a[148:151], v[76:79], v[40:43]\n\t" \
  "v_cvt_pk_bf16_f32 v56, v32, v33\n\t" \
  "v_cvt_pk_bf16_f32 v57, v34, v35\n\t" \
  "s_add_u32 m0, s46, 4096\n\t" \
  "s_add_u32 s47, s55, 4096\n\t" \
  "buffer_load_dwordx4 v0, %[X], s47 offen sc1 lds\n\t" \
  "v_mfma_f32_16x16x32_bf16 v[44:47], a[220:223], v[76:79], v[44:47]\n\t" \
  "v_cvt_pk_bf16_f32 v58, v36, v37\n\t" \
  "v_cvt_pk_bf16_f32 v59, v38, v39\n\t" \
  "v_mfma_f32_16x16x32_bf16 v[40:43], a[152:155], v[84:87], v[40:43]\n\t" \
  "v_mfma_f32_16x16x32_bf16 v[44:47], a[224:227], v[84:87], v[44:47]\n\t" \
  "buffer_store_dwordx4 v[56:59], v0, %[X], s51 offen offset:0 sc1\n\t" \
  "v_mfma_f32_16x16x32_bf16 v[40:43], a[156:159], v[92:95], v[40:43]\n\t" \
  "s_add_u32 m0, s46, 8192\n\t" \
  "s_add_u32 s47, s55, 16384\n\t" \
  "buffer_load_dwordx4 v0, %[X], s47 offen sc1 lds\n\t" \
  "v_mfma_f32_16x16x32_bf16 v[44:47], a[228:231], v[92:95], v[44:47]\n\t" \
  "v_mfma_f32_16x16x32_bf16 v[40:43], a[160:163], v[100:103], v[40:43]\n\t" \
  "v_mfma_f32_16x16x32_bf16 v[44:47], a[232:235], v[100:103], v[44:47]\n\t" \
  "v_mfma_f32_16x16x32_bf16 v[40:43], a[164:167], v[108:111], v[40:43]\n\t" \
  "s_add_u32 m0, s46, 12288\n\t" \
  "s_add_u32 s47, s55, 20480\n\t" \
  "buffer_load_dwordx4 v0, %[X], s47 offen sc1 lds\n\t" \
  "v_mfma_f32_16x16x32_bf16 v[44:47], a[236:239], v[108:111], v[44:47]\n\t" \
  "v_mfma_f32_16x16x32_bf16 v[40:43], a[168:171], v[116:119], v[40:43]\n\t" \
  "v_mfma_f32_16x16x32_bf16 v[44:47], a[240:243], v[116:119], v[44:47]\n\t" \
  "v_mfma_f32_16x16x32_bf16 v[40:43], a[172:175], v[124:127], v[40:43]\n\t" \
  "s_add_u32 m0, s46, 16384\n\t" \
  "s_add_u32 s47, s55, 32768\n\t" \
  "buffer_load_dwordx4 v0, %[X], s47 offen sc1 lds\n\t" \
  "v_mfma_f32_16x16x32_bf16 v[44:47], a[244:247], v[124:127], v[44:47]\n\t" \
  "v_mfma_f32_16x16x32_bf16 v[40:43], a[176:179], v[132:135], v[40:43]\n\t" \
  "v_mfma_f32_16x16x32_bf16 v[44:47], a[248:251], v[132:135], v[44:47]\n\t" \
  "v_mfma_f32_16x16x32_bf16 v[40:43], a[180:183], v[140:143], v[40:43]\n\t" \
  "s_add_u32 m0, s46, 20480\n\t" \
  "s_add_u32 s47, s55, 36864\n\t" \
  "buffer_load_dwordx4 v0, %[X], s47 offen sc1 lds\n\t" \
  "v_mfma_f32_16x16x32_bf16 v[44:47], a[252:255], v[140:143], v[44:47]\n\t" \
  "v_mfma_f32_16x16x32_bf16 v[40:43], a[184:187], v[148:151], v[40:43]\n\t" \
  "v_mfma_f32_16x16x32_bf16 v[44:47], v[224:227], v[148:151], v[44:47]\n\t" \
  "v_mfma_f32_16x16x32_bf16 v[40:43], a[188:191], v[156:159], v[40:43]\n\t" \
  "s_add_u32 m0, s46, 24576\n\t" \
  "s_add_u32 s47, s55, 49152\n\t" \
  "buffer_load_dwordx4 v0, %[X], s47 offen sc1 lds\n\t" \
  "v_mfma_f32_16x16x32_bf16 v[44:47], v[228:231], v[156:159], v[44:47]\n\t" \
  "v_mfma_f32_16x16x32_bf16 v[40:43], a[192:195], v[164:167], v[40:43]\n\t" \
  "v_mfma_f32_16x16x32_bf16 v[44:47], v[232:235], v[164:167], v[44:47]\n\t" \
  "v_mfma_f32_16x16x32_bf16 v[40:43], a[196:199], v[172:175], v[40:43]\n\t" \
  "s_add_u32 m0, s46, 28672\n\t" \
  "s_add_u32 s47, s55, 53248\n\t" \
  "buffer_load_dwordx4 v0, %[X], s47 offen sc1 lds\n\t" \
  "v_mfma_f32_16x16x32_bf16 v[44:47], v[236:239], v[172:175], v[44:47]\n\t" \
  "v_mfma_f32_16x16x32_bf16 v[40:43], a[200:203], v[180:183], v[40:43]\n\t" \
  "v_mfma_f32_16x16x32_bf16 v[44:47], v[240:243], v[180:183], v[44:47]\n\t" \
  "v_mfma_f32_16x16x32_bf16 v[40:43], a[204:207], v[188:191], v[40:43]\n\t" \
  "s_add_u32 m0, s46, 32768\n\t" \
  "s_add_u32 s47, s55, 65536\n\t" \
  "buffer_load_dwordx4 v0, %[X], s47 offen sc1 lds\n\t" \
  "v_mfma_f32_16x16x32_bf16 v[44:47], v[244:247], v[188:191], v[44:47]\n\t" \
  "v_mfma_f32_16x16x32_bf16 v[40:43], a[208:211], v[196:199], v[40:43]\n\t" \
  "v_mfma_f32_16x16x32_bf16 v[44:47], v[248:251], v[196:199], v[44:47]\n\t" \
  "buffer_load_dword v8, off, %[X], s57 sc1\n\t" \
  "v_mfma_f32_16x16x32_bf16 v[40:43], a[212:215], v[204:207], v[40:43]\n\t" \
  "v_mfma_f32_16x16x32_bf16 v[44:47], v[252:255], v[204:207], v[44:47]\n\t" \
  "s_nop 4\n\t" \
  "v_cvt_pk_bf16_f32 v60, v40, v41\n\t" \
  "v_cvt_pk_bf16_f32 v61, v42, v43\n\t" \
  "s_nop 1\n\t" \
  "v_cvt_pk_bf16_f32 v62, v44, v45\n\t" \
  "v_cvt_pk_bf16_f32 v63, v46, v47\n\t" \
  "buffer_store_dwordx4 v[60:63], v0, %[X], s51 offen offset:1024 sc1\n\t" \
  "s_add_u32 s49, s82, s38\n\t" \
  "s_cmp_eq_u32 s48, 63\n\t" \
  "s_cbranch_scc1 LEND_%=\n\t" \
  "s_add_u32 s48, s48, 1\n\t" \
  "s_cmp_eq_u32 s56, 0\n\t" \
  "s_cbranch_scc1 LCOLD_%=\n\t" \
  "s_waitcnt vmcnt(0)\n\t" \
  "s_mov_b64 exec, 1\n\t" \
  "buffer_store_dword v6, off, %[X], s49 sc1\n\t" \
  "s_mov_b64 exec, -1\n\t" \
  "s_mov_b32 s49, 0xf0000000\n\t" \
  "s_cmp_lt_i32 s57, 0\n\t" \
  "s_cbranch_scc1 LTOP_%=\n\t" \
  "s_movk_i32 s63, 4000\n\t" \
  "s_branch LGW_%=u\n\t" \
  "LGW_%=:\n\t" \
  "s_sleep " TK_PSG "\n\t" \
  "buffer_load_dword v8, off, %[X], s57 sc1\n\t" \
  "s_waitcnt vmcnt(0)\n\t" \
  "LGW_%=u:\n\t" \
  "s_memrealtime s[52:53]\n\t" \
  "v_readfirstlane_b32 s60, v8\n\t" \
  "s_waitcnt lgkmcnt(0)\n\t" \
  "s_sub_u32 s63, s63, 1\n\t" \
  "s_cbranch_scc1 LTOP_%=\n\t" \
  "s_cmp_eq_u32 s60, -1\n\t" \
  "s_cbranch_scc1 LGW_%=\n\t" \
  "s_not_b32 s60, s60\n\t" \
  "s_lshl_b32 s61, s52, 8\n\t" \
  "s_sub_u32 s60, s60, s61\n\t" \
  "s_ashr_i32 s60, s60, 8\n\t" \
  "s_sub_i32 s60, s60, " TK_QXW "\n\t" \
  "s_min_i32 s60, s60, 400\n\t" \
  "s_add_u32 s54, s52, s60\n\t" \
  "LGW_%=w:\n\t" \
  "s_sleep 1\n\t" \
  "s_memrealtime s[52:53]\n\t" \
  "s_waitcnt lgkmcnt(0)\n\t" \
  "s_sub_i32 s53, s52, s54\n\t" \
  "s_cmp_lt_i32 s53, 0\n\t" \
  "s_cbranch_scc1 LGW_%=w\n\t" \
  "s_branch LTOP_%=\n\t" \
  "LEND_%=:\n\t" \
  "s_waitcnt vmcnt(0)\n\t" \
  "s_mov_b64 exec, 1\n\t" \
  "buffer_store_dword v6, off, %[X], s49 sc1\n\t" \
  "s_mov_b64 exec, -1\n\t" \
  "s_mov_b32 s49, 0xf0000000\n\t" \
  ""
#define QV_ASM \
  "v_lshrrev_b32 v16, 7, v0\n\t" \
  "v_lshlrev_b32 v16, 13, v16\n\t" \
  "v_lshrrev_b32 v19, 2, v0\n\t" \
  "v_and_b32 v19, 64, v19\n\t" \
  "v_xor_b32 v19, v19, v0\n\t" \
  "v_and_b32 v19, 0x70, v19\n\t" \
  "v_add_u32 v16, v16, v19\n\t" \
  "s_mul_i32 s63, s40, 40960\n\t" \
  "v_and_b32 v19, 0x300, v0\n\t" \
  "v_lshlrev_b32 v17, 2, v19\n\t" \
  "v_and_b32 v19, 0xc0, v0\n\t" \
  "v_lshl_add_u32 v17, v19, 1, v17\n\t" \
  "v_and_b32 v19, 0x30, v0\n\t" \
  "v_lshrrev_b32 v19, 1, v19\n\t" \
  "v_add3_u32 v17, v17, v19, s63\n\t" \
  "v_lshrrev_b32 v19, 1, v0\n\t" \
  "v_and_b32 v19, 64, v19\n\t" \
  "v_add_u32 v17, v17, v19\n\t" \
  "v_xor_b32 v18, 64, v17\n\t" \
  "s_add_u32 m0, s63, 0\n\t" \
  "s_mov_b32 s60, 0\n\t" \
  "buffer_load_dwordx4 v16, %[W], s60 offen lds\n\t" \
  "s_add_u32 m0, s63, 1024\n\t" \
  "s_mov_b32 s60, 65536\n\t" \
  "buffer_load_dwordx4 v16, %[W], s60 offen lds\n\t" \
  "s_add_u32 m0, s63, 2048\n\t" \
  "s_mov_b32 s60, 131072\n\t" \
  "buffer_load_dwordx4 v16, %[W], s60 offen lds\n\t" \
  "s_add_u32 m0, s63, 3072\n\t" \
  "s_mov_b32 s60, 196608\n\t" \
  "buffer_load_dwordx4 v16, %[W], s60 offen lds\n\t" \
  "s_add_u32 m0, s63, 4096\n\t" \
  "s_mov_b32 s60, 262144\n\t" \
  "buffer_load_dwordx4 v16, %[W], s60 offen lds\n\t" \
  "s_add_u32 m0, s63, 5120\n\t" \
  "s_mov_b32 s60, 327680\n\t" \
  "buffer_load_dwordx4 v16, %[W], s60 offen lds\n\t" \
  "s_add_u32 m0, s63, 6144\n\t" \
  "s_mov_b32 s60, 393216\n\t" \
  "buffer_load_dwordx4 v16, %[W], s60 offen lds\n\t" \
  "s_add_u32 m0, s63, 7168\n\t" \
  "s_mov_b32 s60, 458752\n\t" \
  "buffer_load_dwordx4 v16, %[W], s60 offen lds\n\t" \
  "s_add_u32 m0, s63, 8192\n\t" \
  "s_mov_b32 s60, 524288\n\t" \
  "buffer_load_dwordx4 v16, %[W], s60 offen lds\n\t" \
  "s_add_u32 m0, s63, 9216\n\t" \
  "s_mov_b32 s60, 589824\n\t" \
  "buffer_load_dwordx4 v16, %[W], s60 offen lds\n\t" \
  "s_add_u32 m0, s63, 10240\n\t" \
  "s_mov_b32 s60, 655360\n\t" \
  "buffer_load_dwordx4 v16, %[W], s60 offen lds\n\t" \
  "s_add_u32 m0, s63, 11264\n\t" \
  "s_mov_b32 s60, 720896\n\t" \
  "buffer_load_dwordx4 v16, %[W], s60 offen lds\n\t" \
  "s_add_u32 m0, s63, 12288\n\t" \
  "s_mov_b32 s60, 786432\n\t" \
  "buffer_load_dwordx4 v16, %[W], s60 offen lds\n\t" \
  "s_add_u32 m0, s63, 13312\n\t" \
  "s_mov_b32 s60, 851968\n\t" \
  "buffer_load_dwordx4 v16, %[W], s60 offen lds\n\t" \
  "s_add_u32 m0, s63, 14336\n\t" \
  "s_mov_b32 s60, 917504\n\t" \
  "buffer_load_dwordx4 v16, %[W], s60 offen lds\n\t" \
  "s_add_u32 m0, s63, 15360\n\t" \
  "s_mov_b32 s60, 983040\n\t" \
  "buffer_load_dwordx4 v16, %[W], s60 offen lds\n\t" \
  "s_add_u32 m0, s63, 16384\n\t" \
  "s_mov_b32 s60, 1048576\n\t" \
  "buffer_load_dwordx4 v16, %[W], s60 offen lds\n\t" \
  "s_add_u32 m0, s63, 17408\n\t" \
  "s_mov_b32 s60, 1114112\n\t" \
  "buffer_load_dwordx4 v16, %[W], s60 offen lds\n\t" \
  "s_add_u32 m0, s63, 18432\n\t" \
  "s_mov_b32 s60, 1179648\n\t" \
  "buffer_load_dwordx4 v16, %[W], s60 offen lds\n\t" \
  "s_add_u32 m0, s63, 19456\n\t" \
  "s_mov_b32 s60, 1245184\n\t" \
  "buffer_load_dwordx4 v16, %[W], s60 offen lds\n\t" \
  "s_add_u32 m0, s63, 20480\n\t" \
  "s_mov_b32 s60, 1310720\n\t" \
  "buffer_load_dwordx4 v16, %[W], s60 offen lds\n\t" \
  "s_add_u32 m0, s63, 21504\n\t" \
  "s_mov_b32 s60, 1376256\n\t" \
  "buffer_load_dwordx4 v16, %[W], s60 offen lds\n\t" \
  "s_add_u32 m0, s63, 22528\n\t" \
  "s_mov_b32 s60, 1441792\n\t" \
  "buffer_load_dwordx4 v16, %[W], s60 offen lds\n\t" \
  "s_add_u32 m0, s63, 23552\n\t" \
  "s_mov_b32 s60, 1507328\n\t" \
  "buffer_load_dwordx4 v16, %[W], s60 offen lds\n\t" \
  "s_add_u32 m0, s63, 24576\n\t" \
  "s_mov_b32 s60, 1572864\n\t" \
  "buffer_load_dwordx4 v16, %[W], s60 offen lds\n\t" \
  "s_add_u32 m0, s63, 25600\n\t" \
  "s_mov_b32 s60, 1638400\n\t" \
  "buffer_load_dwordx4 v16, %[W], s60 offen lds\n\t" \
  "s_add_u32 m0, s63, 26624\n\t" \
  "s_mov_b32 s60, 1703936\n\t" \
  "buffer_load_dwordx4 v16, %[W], s60 offen lds\n\t" \
  "s_add_u32 m0, s63, 27648\n\t" \
  "s_mov_b32 s60, 1769472\n\t" \
  "buffer_load_dwordx4 v16, %[W], s60 offen lds\n\t" \
  "s_add_u32 m0, s63, 28672\n\t" \
  "s_mov_b32 s60, 1835008\n\t" \
  "buffer_load_dwordx4 v16, %[W], s60 offen lds\n\t" \
  "s_add_u32 m0, s63, 29696\n\t" \
  "s_mov_b32 s60, 1900544\n\t" \
  "buffer_load_dwordx4 v16, %[W], s60 offen lds\n\t" \
  "s_add_u32 m0, s63, 30720\n\t" \
  "s_mov_b32 s60, 1966080\n\t" \
  "buffer_load_dwordx4 v16, %[W], s60 offen lds\n\t" \
  "s_add_u32 m0, s63, 31744\n\t" \
  "s_mov_b32 s60, 2031616\n\t" \
  "buffer_load_dwordx4 v16, %[W], s60 offen lds\n\t" \
  "s_add_u32 m0, s63, 32768\n\t" \
  "s_mov_b32 s60, 2097152\n\t" \
  "buffer_load_dwordx4 v16, %[W], s60 offen lds\n\t" \
  "s_add_u32 m0, s63, 33792\n\t" \
  "s_mov_b32 s60, 2162688\n\t" \
  "buffer_load_dwordx4 v16, %[W], s60 offen lds\n\t" \
  "s_add_u32 m0, s63, 34816\n\t" \
  "s_mov_b32 s60, 2228224\n\t" \
  "buffer_load_dwordx4 v16, %[W], s60 offen lds\n\t" \
  "s_add_u32 m0, s63, 35840\n\t" \
  "s_mov_b32 s60, 2293760\n\t" \
  "buffer_load_dwordx4 v16, %[W], s60 offen lds\n\t" \
  "s_add_u32 m0, s63, 36864\n\t" \
  "s_mov_b32 s60, 2359296\n\t" \
  "buffer_load_dwordx4 v16, %[W], s60 offen lds\n\t" \
  "s_add_u32 m0, s63, 37888\n\t" \
  "s_mov_b32 s60, 2424832\n\t" \
  "buffer_load_dwordx4 v16, %[W], s60 offen lds\n\t" \
  "s_add_u32 m0, s63, 38912\n\t" \
  "s_mov_b32 s60, 2490368\n\t" \
  "buffer_load_dwordx4 v16, %[W], s60 offen lds\n\t" \
  "s_add_u32 m0, s63, 39936\n\t" \
  "s_mov_b32 s60, 2555904\n\t" \
  "buffer_load_dwordx4 v16, %[W], s60 offen lds\n\t" \
  "s_waitcnt vmcnt(32)\n\t" \
  "ds_read_b64_tr_b16 a[0:1], v17 offset:0\n\t" \
  "ds_read_b64_tr_b16 a[2:3], v17 offset:512\n\t" \
  "ds_read_b64_tr_b16 a[4:5], v17 offset:4096\n\t" \
  "ds_read_b64_tr_b16 a[6:7], v17 offset:4608\n\t" \
  "ds_read_b64_tr_b16 a[72:73], v17 offset:32\n\t" \
  "ds_read_b64_tr_b16 a[74:75], v17 offset:544\n\t" \
  "ds_read_b64_tr_b16 a[76:77], v17 offset:4128\n\t" \
  "ds_read_b64_tr_b16 a[78:79], v17 offset:4640\n\t" \
  "ds_read_b64_tr_b16 a[144:145], v18 offset:0\n\t" \
  "ds_read_b64_tr_b16 a[146:147], v18 offset:512\n\t" \
  "ds_read_b64_tr_b16 a[148:149], v18 offset:4096\n\t" \
  "ds_read_b64_tr_b16 a[150:151], v18 offset:4608\n\t" \
  "ds_read_b64_tr_b16 a[216:217], v18 offset:32\n\t" \
  "ds_read_b64_tr_b16 a[218:219], v18 offset:544\n\t" \
  "ds_read_b64_tr_b16 a[220:221], v18 offset:4128\n\t" \
  "ds_read_b64_tr_b16 a[222:223], v18 offset:4640\n\t" \
  "s_waitcnt lgkmcnt(0)\n\t" \
  "s_add_u32 m0, s63, 0\n\t" \
  "s_mov_b32 s60, 2621440\n\t" \
  "buffer_load_dwordx4 v16, %[W], s60 offen lds\n\t" \
  "s_add_u32 m0, s63, 1024\n\t" \
  "s_mov_b32 s60, 2686976\n\t" \
  "buffer_load_dwordx4 v16, %[W], s60 offen lds\n\t" \
  "s_add_u32 m0, s63, 2048\n\t" \
  "s_mov_b32 s60, 2752512\n\t" \
  "buffer_load_dwordx4 v16, %[W], s60 offen lds\n\t" \
  "s_add_u32 m0, s63, 3072\n\t" \
  "s_mov_b32 s60, 2818048\n\t" \
  "buffer_load_dwordx4 v16, %[W], s60 offen lds\n\t" \
  "s_add_u32 m0, s63, 4096\n\t" \
  "s_mov_b32 s60, 2883584\n\t" \
  "buffer_load_dwordx4 v16, %[W], s60 offen lds\n\t" \
  "s_add_u32 m0, s63, 5120\n\t" \
  "s_mov_b32 s60, 2949120\n\t" \
  "buffer_load_dwordx4 v16, %[W], s60 offen lds\n\t" \
  "s_add_u32 m0, s63, 6144\n\t" \
  "s_mov_b32 s60, 3014656\n\t" \
  "buffer_load_dwordx4 v16, %[W], s60 offen lds\n\t" \
  "s_add_u32 m0, s63, 7168\n\t" \
  "s_mov_b32 s60, 3080192\n\t" \
  "buffer_load_dwordx4 v16, %[W], s60 offen lds\n\t" \
  "s_waitcnt vmcnt(32)\n\t" \
  "ds_read_b64_tr_b16 a[8:9], v17 offset:8192\n\t" \
  "ds_read_b64_tr_b16 a[10:11], v17 offset:8704\n\t" \
  "ds_read_b64_tr_b16 a[12:13], v17 offset:12288\n\t" \
  "ds_read_b64_tr_b16 a[14:15], v17 offset:12800\n\t" \
  "ds_read_b64_tr_b16 a[80:81], v17 offset:8224\n\t" \
  "ds_read_b64_tr_b16 a[82:83], v17 offset:8736\n\t" \
  "ds_read_b64_tr_b16 a[84:85], v17 offset:12320\n\t" \
  "ds_read_b64_tr_b16 a[86:87], v17 offset:12832\n\t" \
  "ds_read_b64_tr_b16 a[152:153], v18 offset:8192\n\t" \
  "ds_read_b64_tr_b16 a[154:155], v18 offset:8704\n\t" \
  "ds_read_b64_tr_b16 a[156:157], v18 offset:12288\n\t" \
  "ds_read_b64_tr_b16 a[158:159], v18 offset:12800\n\t" \
  "ds_read_b64_tr_b16 a[224:225], v18 offset:8224\n\t" \
  "ds_read_b64_tr_b16 a[226:227], v18 offset:8736\n\t" \
  "ds_read_b64_tr_b16 a[228:229], v18 offset:12320\n\t" \
  "ds_read_b64_tr_b16 a[230:231], v18 offset:12832\n\t" \
  "s_waitcnt lgkmcnt(0)\n\t" \
  "s_add_u32 m0, s63, 8192\n\t" \
  "s_mov_b32 s60, 3145728\n\t" \
  "buffer_load_dwordx4 v16, %[W], s60 offen lds\n\t" \
  "s_add_u32 m0, s63, 9216\n\t" \
  "s_mov_b32 s60, 3211264\n\t" \
  "buffer_load_dwordx4 v16, %[W], s60 offen lds\n\t" \
  "s_add_u32 m0, s63, 10240\n\t" \
  "s_mov_b32 s60, 3276800\n\t" \
  "buffer_load_dwordx4 v16, %[W], s60 offen lds\n\t" \
  "s_add_u32 m0, s63, 11264\n\t" \
  "s_mov_b32 s60, 3342336\n\t" \
  "buffer_load_dwordx4 v16, %[W], s60 offen lds\n\t" \
  "s_add_u32 m0, s63, 12288\n\t" \
  "s_mov_b32 s60, 3407872\n\t" \
  "buffer_load_dwordx4 v16, %[W], s60 offen lds\n\t" \
  "s_add_u32 m0, s63, 13312\n\t" \
  "s_mov_b32 s60, 3473408\n\t" \
  "buffer_load_dwordx4 v16, %[W], s60 offen lds\n\t" \
  "s_add_u32 m0, s63, 14336\n\t" \
  "s_mov_b32 s60, 3538944\n\t" \
  "buffer_load_dwordx4 v16, %[W], s60 offen lds\n\t" \
  "s_add_u32 m0, s63, 15360\n\t" \
  "s_mov_b32 s60, 3604480\n\t" \
  "buffer_load_dwordx4 v16, %[W], s60 offen lds\n\t" \
  "s_waitcnt vmcnt(32)\n\t" \
  "ds_read_b64_tr_b16 a[16:17], v17 offset:16384\n\t" \
  "ds_read_b64_tr_b16 a[18:19], v17 offset:16896\n\t" \
  "ds_read_b64_tr_b16 a[20:21], v17 offset:20480\n\t" \
  "ds_read_b64_tr_b16 a[22:23], v17 offset:20992\n\t" \
  "ds_read_b64_tr_b16 a[88:89], v17 offset:16416\n\t" \
  "ds_read_b64_tr_b16 a[90:91], v17 offset:16928\n\t" \
  "ds_read_b64_tr_b16 a[92:93], v17 offset:20512\n\t" \
  "ds_read_b64_tr_b16 a[94:95], v17 offset:21024\n\t" \
  "ds_read_b64_tr_b16 a[160:161], v18 offset:16384\n\t" \
  "ds_read_b64_tr_b16 a[162:163], v18 offset:16896\n\t" \
  "ds_read_b64_tr_b16 a[164:165], v18 offset:20480\n\t" \
  "ds_read_b64_tr_b16 a[166:167], v18 offset:20992\n\t" \
  "ds_read_b64_tr_b16 a[232:233], v18 offset:16416\n\t" \
  "ds_read_b64_tr_b16 a[234:235], v18 offset:16928\n\t" \
  "ds_read_b64_tr_b16 a[236:237], v18 offset:20512\n\t" \
  "ds_read_b64_tr_b16 a[238:239], v18 offset:21024\n\t" \
  "s_waitcnt lgkmcnt(0)\n\t" \
  "s_add_u32 m0, s63, 16384\n\t" \
  "s_mov_b32 s60, 3670016\n\t" \
  "buffer_load_dwordx4 v16, %[W], s60 offen lds\n\t" \
  "s_add_u32 m0, s63, 17408\n\t" \
  "s_mov_b32 s60, 3735552\n\t" \
  "buffer_load_dwordx4 v16, %[W], s60 offen lds\n\t" \
  "s_add_u32 m0, s63, 18432\n\t" \
  "s_mov_b32 s60, 3801088\n\t" \
  "buffer_load_dwordx4 v16, %[W], s60 offen lds\n\t" \
  "s_add_u32 m0, s63, 19456\n\t" \
  "s_mov_b32 s60, 3866624\n\t" \
  "buffer_load_dwordx4 v16, %[W], s60 offen lds\n\t" \
  "s_add_u32 m0, s63, 20480\n\t" \
  "s_mov_b32 s60, 3932160\n\t" \
  "buffer_load_dwordx4 v16, %[W], s60 offen lds\n\t" \
  "s_add_u32 m0, s63, 21504\n\t" \
  "s_mov_b32 s60, 3997696\n\t" \
  "buffer_load_dwordx4 v16, %[W], s60 offen lds\n\t" \
  "s_add_u32 m0, s63, 22528\n\t" \
  "s_mov_b32 s60, 4063232\n\t" \
  "buffer_load_dwordx4 v16, %[W], s60 offen lds\n\t" \
  "s_add_u32 m0, s63, 23552\n\t" \
  "s_mov_b32 s60, 4128768\n\t" \
  "buffer_load_dwordx4 v16, %[W], s60 offen lds\n\t" \
  "s_waitcnt vmcnt(32)\n\t" \
  "ds_read_b64_tr_b16 a[24:25], v17 offset:24576\n\t" \
  "ds_read_b64_tr_b16 a[26:27], v17 offset:25088\n\t" \
  "ds_read_b64_tr_b16 a[28:29], v17 offset:28672\n\t" \
  "ds_read_b64_tr_b16 a[30:31], v17 offset:29184\n\t" \
  "ds_read_b64_tr_b16 a[96:97], v17 offset:24608\n\t" \
  "ds_read_b64_tr_b16 a[98:99], v17 offset:25120\n\t" \
  "ds_read_b64_tr_b16 a[100:101], v17 offset:28704\n\t" \
  "ds_read_b64_tr_b16 a[102:103], v17 offset:29216\n\t" \
  "ds_read_b64_tr_b16 a[168:169], v18 offset:24576\n\t" \
  "ds_read_b64_tr_b16 a[170:171], v18 offset:25088\n\t" \
  "ds_read_b64_tr_b16 a[172:173], v18 offset:28672\n\t" \
  "ds_read_b64_tr_b16 a[174:175], v18 offset:29184\n\t" \
  "ds_read_b64_tr_b16 a[240:241], v18 offset:24608\n\t" \
  "ds_read_b64_tr_b16 a[242:243], v18 offset:25120\n\t" \
  "ds_read_b64_tr_b16 a[244:245], v18 offset:28704\n\t" \
  "ds_read_b64_tr_b16 a[246:247], v18 offset:29216\n\t" \
  "s_waitcnt lgkmcnt(0)\n\t" \
  "s_add_u32 m0, s63, 24576\n\t" \
  "s_mov_b32 s60, 4194304\n\t" \
  "buffer_load_dwordx4 v16, %[W], s60 offen lds\n\t" \
  "s_add_u32 m0, s63, 25600\n\t" \
  "s_mov_b32 s60, 4259840\n\t" \
  "buffer_load_dwordx4 v16, %[W], s60 offen lds\n\t" \
  "s_add_u32 m0, s63, 26624\n\t" \
  "s_mov_b32 s60, 4325376\n\t" \
  "buffer_load_dwordx4 v16, %[W], s60 offen lds\n\t" \
  "s_add_u32 m0, s63, 27648\n\t" \
  "s_mov_b32 s60, 4390912\n\t" \
  "buffer_load_dwordx4 v16, %[W], s60 offen lds\n\t" \
  "s_add_u32 m0, s63, 28672\n\t" \
  "s_mov_b32 s60, 4456448\n\t" \
  "buffer_load_dwordx4 v16, %[W], s60 offen lds\n\t" \
  "s_add_u32 m0, s63, 29696\n\t" \
  "s_mov_b32 s60, 4521984\n\t" \
  "buffer_load_dwordx4 v16, %[W], s60 offen lds\n\t" \
  "s_add_u32 m0, s63, 30720\n\t" \
  "s_mov_b32 s60, 4587520\n\t" \
  "buffer_load_dwordx4 v16, %[W], s60 offen lds\n\t" \
  "s_add_u32 m0, s63, 31744\n\t" \
  "s_mov_b32 s60, 4653056\n\t" \
  "buffer_load_dwordx4 v16, %[W], s60 offen lds\n\t" \
  "s_waitcnt vmcnt(32)\n\t" \
  "ds_read_b64_tr_b16 a[32:33], v17 offset:32768\n\t" \
  "ds_read_b64_tr_b16 a[34:35], v17 offset:33280\n\t" \
  "ds_read_b64_tr_b16 a[36:37], v17 offset:36864\n\t" \
  "ds_read_b64_tr_b16 a[38:39], v17 offset:37376\n\t" \
  "ds_read_b64_tr_b16 a[104:105], v17 offset:32800\n\t" \
  "ds_read_b64_tr_b16 a[106:107], v17 offset:33312\n\t" \
  "ds_read_b64_tr_b16 a[108:109], v17 offset:36896\n\t" \
  "ds_read_b64_tr_b16 a[110:111], v17 offset:37408\n\t" \
  "ds_read_b64_tr_b16 a[176:177], v18 offset:32768\n\t" \
  "ds_read_b64_tr_b16 a[178:179], v18 offset:33280\n\t" \
  "ds_read_b64_tr_b16 a[180:181], v18 offset:36864\n\t" \
  "ds_read_b64_tr_b16 a[182:183], v18 offset:37376\n\t" \
  "ds_read_b64_tr_b16 a[248:249], v18 offset:32800\n\t" \
  "ds_read_b64_tr_b16 a[250:251], v18 offset:33312\n\t" \
  "ds_read_b64_tr_b16 a[252:253], v18 offset:36896\n\t" \
  "ds_read_b64_tr_b16 a[254:255], v18 offset:37408\n\t" \
  "s_waitcnt lgkmcnt(0)\n\t" \
  "s_waitcnt vmcnt(24)\n\t" \
  "ds_read_b64_tr_b16 a[40:41], v17 offset:0\n\t" \
  "ds_read_b64_tr_b16 a[42:43], v17 offset:512\n\t" \
  "ds_read_b64_tr_b16 a[44:45], v17 offset:4096\n\t" \
  "ds_read_b64_tr_b16 a[46:47], v17 offset:4608\n\t" \
  "ds_read_b64_tr_b16 a[112:113], v17 offset:32\n\t" \
  "ds_read_b64_tr_b16 a[114:115], v17 offset:544\n\t" \
  "ds_read_b64_tr_b16 a[116:117], v17 offset:4128\n\t" \
  "ds_read_b64_tr_b16 a[118:119], v17 offset:4640\n\t" \
  "ds_read_b64_tr_b16 a[184:185], v18 offset:0\n\t" \
  "ds_read_b64_tr_b16 a[186:187], v18 offset:512\n\t" \
  "ds_read_b64_tr_b16 a[188:189], v18 offset:4096\n\t" \
  "ds_read_b64_tr_b16 a[190:191], v18 offset:4608\n\t" \
  "ds_read_b64_tr_b16 v[224:225], v18 offset:32\n\t" \
  "ds_read_b64_tr_b16 v[226:227], v18 offset:544\n\t" \
  "ds_read_b64_tr_b16 v[228:229], v18 offset:4128\n\t" \
  "ds_read_b64_tr_b16 v[230:231], v18 offset:4640\n\t" \
  "s_waitcnt lgkmcnt(0)\n\t" \
  "s_waitcnt vmcnt(16)\n\t" \
  "ds_read_b64_tr_b16 a[48:49], v17 offset:8192\n\t" \
  "ds_read_b64_tr_b16 a[50:51], v17 offset:8704\n\t" \
  "ds_read_b64_tr_b16 a[52:53], v17 offset:12288\n\t" \
  "ds_read_b64_tr_b16 a[54:55], v17 offset:12800\n\t" \
  "ds_read_b64_tr_b16 a[120:121], v17 offset:8224\n\t" \
  "ds_read_b64_tr_b16 a[122:123], v17 offset:8736\n\t" \
  "ds_read_b64_tr_b16 a[124:125], v17 offset:12320\n\t" \
  "ds_read_b64_tr_b16 a[126:127], v17 offset:12832\n\t" \
  "ds_read_b64_tr_b16 a[192:193], v18 offset:8192\n\t" \
  "ds_read_b64_tr_b16 a[194:195], v18 offset:8704\n\t" \
  "ds_read_b64_tr_b16 a[196:197], v18 offset:12288\n\t" \
  "ds_read_b64_tr_b16 a[198:199], v18 offset:12800\n\t" \
  "ds_read_b64_tr_b16 v[232:233], v18 offset:8224\n\t" \
  "ds_read_b64_tr_b16 v[234:235], v18 offset:8736\n\t" \
  "ds_read_b64_tr_b16 v[236:237], v18 offset:12320\n\t" \
  "ds_read_b64_tr_b16 v[238:239], v18 offset:12832\n\t" \
  "s_waitcnt lgkmcnt(0)\n\t" \
  "s_waitcnt vmcnt(8)\n\t" \
  "ds_read_b64_tr_b16 a[56:57], v17 offset:16384\n\t" \
  "ds_read_b64_tr_b16 a[58:59], v17 offset:16896\n\t" \
  "ds_read_b64_tr_b16 a[60:61], v17 offset:20480\n\t" \
  "ds_read_b64_tr_b16 a[62:63], v17 offset:20992\n\t" \
  "ds_read_b64_tr_b16 a[128:129], v17 offset:16416\n\t" \
  "ds_read_b64_tr_b16 a[130:131], v17 offset:16928\n\t" \
  "ds_read_b64_tr_b16 a[132:133], v17 offset:20512\n\t" \
  "ds_read_b64_tr_b16 a[134:135], v17 offset:21024\n\t" \
  "ds_read_b64_tr_b16 a[200:201], v18 offset:16384\n\t" \
  "ds_read_b64_tr_b16 a[202:203], v18 offset:16896\n\t" \
  "ds_read_b64_tr_b16 a[204:205], v18 offset:20480\n\t" \
  "ds_read_b64_tr_b16 a[206:207], v18 offset:20992\n\t" \
  "ds_read_b64_tr_b16 v[240:241], v18 offset:16416\n\t" \
  "ds_read_b64_tr_b16 v[242:243], v18 offset:16928\n\t" \
  "ds_read_b64_tr_b16 v[244:245], v18 offset:20512\n\t" \
  "ds_read_b64_tr_b16 v[246:247], v18 offset:21024\n\t" \
  "s_waitcnt lgkmcnt(0)\n\t" \
  "s_waitcnt vmcnt(0)\n\t" \
  "ds_read_b64_tr_b16 a[64:65], v17 offset:24576\n\t" \
  "ds_read_b64_tr_b16 a[66:67], v17 offset:25088\n\t" \
  "ds_read_b64_tr_b16 a[68:69], v17 offset:28672\n\t" \
  "ds_read_b64_tr_b16 a[70:71], v17 offset:29184\n\t" \
  "ds_read_b64_tr_b16 a[136:137], v17 offset:24608\n\t" \
  "ds_read_b64_tr_b16 a[138:139], v17 offset:25120\n\t" \
  "ds_read_b64_tr_b16 a[140:141], v17 offset:28704\n\t" \
  "ds_read_b64_tr_b16 a[142:143], v17 offset:29216\n\t" \
  "ds_read_b64_tr_b16 a[208:209], v18 offset:24576\n\t" \
  "ds_read_b64_tr_b16 a[210:211], v18 offset:25088\n\t" \
  "ds_read_b64_tr_b16 a[212:213], v18 offset:28672\n\t" \
  "ds_read_b64_tr_b16 a[214:215], v18 offset:29184\n\t" \
  "ds_read_b64_tr_b16 v[248:249], v18 offset:24608\n\t" \
  "ds_read_b64_tr_b16 v[250:251], v18 offset:25120\n\t" \
  "ds_read_b64_tr_b16 v[252:253], v18 offset:28704\n\t" \
  "ds_read_b64_tr_b16 v[254:255], v18 offset:29216\n\t" \
  "s_waitcnt lgkmcnt(0)\n\t" \
  "s_mov_b32 s84, " TK_CAP "\n\t" \
  "s_mov_b32 s48, 0\n\t" \
  "s_mov_b32 s49, 0xf0000000\n\t" \
  "s_mov_b32 s46, s41\n\t" \
  "s_mov_b32 s56, 0\n\t" \
  "v_lshrrev_b32 v7, 2, v0\n\t" \
  "s_mov_b32 s60, " TK_CTAB "\n\t" \
  "buffer_load_dword v4, v7, %[X], s60 offen offset:88 sc1\n\t" \
  "buffer_load_dword v7, v7, %[X], s60 offen offset:0 sc1\n\t" \
  "s_waitcnt vmcnt(0)\n\t" \
  "v_and_b32 v4, 1, v4\n\t" \
  "v_and_b32 v7, 1, v7\n\t" \
  "v_cmp_ne_u32 s[90:91], s67, v4\n\t" \
  "v_cmp_ne_u32 s[88:89], s67, v7\n\t" \
  "s_nop 1\n\t" \
  "s_mov_b32 s57, 0xf0000000\n\t" \
  "s_add_u32 s83, s48, 0\n\t" \
  "s_lshr_b64 s[60:61], s[90:91], s83\n\t" \
  "s_and_b32 s60, s60, 1\n\t" \
  "s_lshl_b32 s60, s60, 13\n\t" \
  "s_add_u32 s83, s83, " TK_SG0 "\n\t" \
  "s_lshl_b32 s83, s83, 21\n\t" \
  "s_or_b32 s83, s83, s60\n\t" \
  "s_add_u32 s62, s83, s39\n\t" \
  "v_add_u32 v7, s62, v3\n\t" \
  "LCOLD_%=:\n\t" \
  "s_waitcnt vmcnt(0)\n\t" \
  "s_mov_b64 exec, 1\n\t" \
  "buffer_store_dword v6, off, %[X], s49 sc1\n\t" \
  "s_mov_b64 exec, -1\n\t" \
  "s_mov_b32 s49, 0xf0000000\n\t" \
  "s_add_u32 s55, s83, s36\n\t" \
  "s_cmp_lg_u32 s40, 0\n\t" \
  "s_cbranch_scc1 LGO_%=\n\t" \
  "s_and_b32 s62, s45, 3\n\t" \
  "s_lshl_b32 s62, s62, 7\n\t" \
  "s_add_u32 s62, s62, s83\n\t" \
  "s_add_u32 s62, s62, s39\n\t" \
  "s_mov_b32 s61, 0\n\t" \
  "LPO_%=:\n\t" \
  "buffer_load_dword v4, off, %[X], s62 sc1\n\t" \
  "s_waitcnt vmcnt(0)\n\t" \
  "v_cmp_eq_u32 vcc, 0, v4\n\t" \
  "s_cbranch_vccz LPO_%=x\n\t" \
  "s_add_u32 s61, s61, 1\n\t" \
  "s_sleep " TK_PSH "\n\t" \
  "s_cmp_gt_u32 s61, s84\n\t" \
  "s_cbranch_scc1 LPO_%=g\n\t" \
  "s_and_b32 s60, s61, 1023\n\t" \
  "s_cmp_lg_u32 s60, 0\n\t" \
  "s_cbranch_scc1 LPO_%=\n\t" \
  "buffer_load_dword v4, off, %[X], s92 sc1\n\t" \
  "s_waitcnt vmcnt(0)\n\t" \
  "v_readfirstlane_b32 s60, v4\n\t" \
  "s_nop 0\n\t" \
  "s_cmp_eq_u32 s60, 0\n\t" \
  "s_cbranch_scc1 LPO_%=\n\t" \
  "LPO_%=g:\n\t" \
  "s_mov_b32 s84, 0\n\t" \
  "s_or_b32 s60, s48, 1610612736\n\t" \
  "v_mov_b32 v4, s60\n\t" \
  "s_mov_b64 exec, 1\n\t" \
  "buffer_store_dword v4, off, %[X], s92 sc1\n\t" \
  "s_mov_b64 exec, -1\n\t" \
  "LPO_%=x:\n\t" \
  "s_mov_b32 s61, 0\n\t" \
  "LPH_%=:\n\t" \
  "buffer_load_dword v4, v7, %[X], 0 offen sc1\n\t" \
  "s_waitcnt vmcnt(0)\n\t" \
  "v_cmp_eq_u32 vcc, 0, v4\n\t" \
  "s_cbranch_vccz LPH_%=x\n\t" \
  "s_add_u32 s61, s61, 1\n\t" \
  "s_sleep " TK_PSH "\n\t" \
  "s_cmp_gt_u32 s61, s84\n\t" \
  "s_cbranch_scc1 LPH_%=g\n\t" \
  "s_and_b32 s60, s61, 1023\n\t" \
  "s_cmp_lg_u32 s60, 0\n\t" \
  "s_cbranch_scc1 LPH_%=\n\t" \
  "buffer_load_dword v4, off, %[X], s92 sc1\n\t" \
  "s_waitcnt vmcnt(0)\n\t" \
  "v_readfirstlane_b32 s60, v4\n\t" \
  "s_nop 0\n\t" \
  "s_cmp_eq_u32 s60, 0\n\t" \
  "s_cbranch_scc1 LPH_%=\n\t" \
  "LPH_%=g:\n\t" \
  "s_mov_b32 s84, 0\n\t" \
  "s_or_b32 s60, s48, 1610612736\n\t" \
  "v_mov_b32 v4, s60\n\t" \
  "s_mov_b64 exec, 1\n\t" \
  "buffer_store_dword v4, off, %[X], s92 sc1\n\t" \
  "s_mov_b64 exec, -1\n\t" \
  "LPH_%=x:\n\t" \
  "LGO_%=:\n\t" \
  "s_barrier\n\t" \
  "s_add_u32 m0, s46, 0\n\t" \
  "s_add_u32 s47, s55, 0\n\t" \
  "buffer_load_dwordx4 v0, %[X], s47 offen sc1 lds\n\t" \
  "s_add_u32 m0, s46, 4096\n\t" \
  "s_add_u32 s47, s55, 4096\n\t" \
  "buffer_load_dwordx4 v0, %[X], s47 offen sc1 lds\n\t" \
  "s_add_u32 m0, s46, 8192\n\t" \
  "s_add_u32 s47, s55, 16384\n\t" \
  "buffer_load_dwordx4 v0, %[X], s47 offen sc1 lds\n\t" \
  "s_add_u32 m0, s46, 12288\n\t" \
  "s_add_u32 s47, s55, 20480\n\t" \
  "buffer_load_dwordx4 v0, %[X], s47 offen sc1 lds\n\t" \
  "s_add_u32 m0, s46, 16384\n\t" \
  "s_add_u32 s47, s55, 32768\n\t" \
  "buffer_load_dwordx4 v0, %[X], s47 offen sc1 lds\n\t" \
  "s_add_u32 m0, s46, 20480\n\t" \
  "s_add_u32 s47, s55, 36864\n\t" \
  "buffer_load_dwordx4 v0, %[X], s47 offen sc1 lds\n\t" \
  "s_add_u32 m0, s46, 24576\n\t" \
  "s_add_u32 s47, s55, 49152\n\t" \
  "buffer_load_dwordx4 v0, %[X], s47 offen sc1 lds\n\t" \
  "s_add_u32 m0, s46, 28672\n\t" \
  "s_add_u32 s47, s55, 53248\n\t" \
  "buffer_load_dwordx4 v0, %[X], s47 offen sc1 lds\n\t" \
  "s_add_u32 m0, s46, 32768\n\t" \
  "s_add_u32 s47, s55, 65536\n\t" \
  "buffer_load_dwordx4 v0, %[X], s47 offen sc1 lds\n\t" \
  "s_waitcnt vmcnt(5)\n\t" \
  "s_cmp_lt_i32 s57, 0\n\t" \
  "s_cbranch_scc1 LTOP_%=\n\t" \
  "s_movk_i32 s63, 4000\n\t" \
  "s_branch LGC_%=u\n\t" \
  "LGC_%=:\n\t" \
  "s_sleep " TK_PSG "\n\t" \
  "buffer_load_dword v8, off, %[X], s57 sc1\n\t" \
  "s_waitcnt vmcnt(0)\n\t" \
  "LGC_%=u:\n\t" \
  "s_memrealtime s[52:53]\n\t" \
  "v_readfirstlane_b32 s60, v8\n\t" \
  "s_waitcnt lgkmcnt(0)\n\t" \
  "s_sub_u32 s63, s63, 1\n\t" \
  "s_cbranch_scc1 LTOP_%=\n\t" \
  "s_cmp_eq_u32 s60, -1\n\t" \
  "s_cbranch_scc1 LGC_%=\n\t" \
  "s_not_b32 s60, s60\n\t" \
  "s_lshl_b32 s61, s52, 8\n\t" \
  "s_sub_u32 s60, s60, s61\n\t" \
  "s_ashr_i32 s60, s60, 8\n\t" \
  "s_sub_i32 s60, s60, " TK_QXC "\n\t" \
  "s_min_i32 s60, s60, 400\n\t" \
  "s_add_u32 s54, s52, s60\n\t" \
  "LGC_%=w:\n\t" \
  "s_sleep 1\n\t" \
  "s_memrealtime s[52:53]\n\t" \
  "s_waitcnt lgkmcnt(0)\n\t" \
  "s_sub_i32 s53, s52, s54\n\t" \
  "s_cmp_lt_i32 s53, 0\n\t" \
  "s_cbranch_scc1 LGC_%=w\n\t" \
  "LTOP_%=:\n\t" \
  "s_barrier\n\t" \
  "ds_read_b128 v[64:67], v1 offset:0\n\t" \
  "ds_read_b128 v[68:71], v1 offset:2048\n\t" \
  "ds_read_b128 v[72:75], v2 offset:0\n\t" \
  "ds_read_b128 v[76:79], v2 offset:2048\n\t" \
  "ds_read_b128 v[80:83], v1 offset:4096\n\t" \
  "ds_read_b128 v[84:87], v1 offset:6144\n\t" \
  "s_mov_b32 s82, s83\n\t" \
  "s_xor_b32 s46, s46, 0x10000\n\t" \
  "s_waitcnt lgkmcnt(5)\n\t" \
  "v_mfma_f32_16x16x32_bf16 v[16:19], v[64:67], a[0:3], 0\n\t" \
  "ds_read_b128 v[88:91], v2 offset:4096\n\t" \
  "s_waitcnt lgkmcnt(5)\n\t" \
  "v_mfma_f32_16x16x32_bf16 v[20:23], v[68:71], a[0:3], 0\n\t" \
  "s_add_u32 s83, s48, 1\n\t" \
  "s_lshr_b64 s[60:61], s[90:91], s83\n\t" \
  "v_mfma_f32_16x16x32_bf16 v[24:27], v[64:67], a[72:75], 0\n\t" \
  "ds_read_b128 v[92:95], v2 offset:6144\n\t" \
  "v_mfma_f32_16x16x32_bf16 v[28:31], v[68:71], a[72:75], 0\n\t" \
  "s_and_b32 s60, s60, 1\n\t" \
  "s_lshl_b32 s60, s60, 13\n\t" \
  "s_waitcnt lgkmcnt(5)\n\t" \
  "v_mfma_f32_16x16x32_bf16 v[16:19], v[72:75], a[4:7], v[16:19]\n\t" \
  "ds_read_b128 v[96:99], v1 offset:8192\n\t" \
  "s_waitcnt lgkmcnt(5)\n\t" \
  "v_mfma_f32_16x16x32_bf16 v[20:23], v[76:79], a[4:7], v[20:23]\n\t" \
  "s_add_u32 s83, s83, " TK_SG0 "\n\t" \
  "s_lshl_b32 s83, s83, 21\n\t" \
  "v_mfma_f32_16x16x32_bf16 v[24:27], v[72:75], a[76:79], v[24:27]\n\t" \
  "ds_read_b128 v[100:103], v1 offset:10240\n\t" \
  "v_mfma_f32_16x16x32_bf16 v[28:31], v[76:79], a[76:79], v[28:31]\n\t" \
  "s_or_b32 s83, s83, s60\n\t" \
  "s_add_u32 s62, s83, s39\n\t" \
  "s_waitcnt lgkmcnt(5)\n\t" \
  "v_mfma_f32_16x16x32_bf16 v[16:19], v[80:83], a[8:11], v[16:19]\n\t" \
  "ds_read_b128 v[104:107], v2 offset:8192\n\t" \
  "s_waitcnt lgkmcnt(5)\n\t" \
  "v_mfma_f32_16x16x32_bf16 v[20:23], v[84:87], a[8:11], v[20:23]\n\t" \
  "v_add_u32 v7, s62, v3\n\t" \
  "s_add_u32 s50, s82, s37\n\t" \
  "v_mfma_f32_16x16x32_bf16 v[24:27], v[80:83], a[80:83], v[24:27]\n\t" \
  "ds_read_b128 v[108:111], v2 offset:10240\n\t" \
  "v_mfma_f32_16x16x32_bf16 v[28:31], v[84:87], a[80:83], v[28:31]\n\t" \
  "s_add_u32 s51, s50, 4096\n\t" \
  "s_waitcnt lgkmcnt(5)\n\t" \
  "v_mfma_f32_16x16x32_bf16 v[16:19], v[88:91], a[12:15], v[16:19]\n\t" \
  "ds_read_b128 v[112:115], v1 offset:12288\n\t" \
  "s_waitcnt lgkmcnt(5)\n\t" \
  "v_mfma_f32_16x16x32_bf16 v[20:23], v[92:95], a[12:15], v[20:23]\n\t" \
  "v_mfma_f32_16x16x32_bf16 v[24:27], v[88:91], a[84:87], v[24:27]\n\t" \
  "ds_read_b128 v[116:119], v1 offset:14336\n\t" \
  "v_mfma_f32_16x16x32_bf16 v[28:31], v[92:95], a[84:87], v[28:31]\n\t" \
  "s_waitcnt lgkmcnt(5)\n\t" \
  "v_mfma_f32_16x16x32_bf16 v[16:19], v[96:99], a[16:19], v[16:19]\n\t" \
  "ds_read_b128 v[120:123], v2 offset:12288\n\t" \
  "s_waitcnt lgkmcnt(5)\n\t" \
  "v_mfma_f32_16x16x32_bf16 v[20:23], v[100:103], a[16:19], v[20:23]\n\t" \
  "s_cmp_lg_u32 s56, 0\n\t" \
  "v_mfma_f32_16x16x32_bf16 v[24:27], v[96:99], a[88:91], v[24:27]\n\t" \
  "ds_read_b128 v[124:127], v2 offset:14336\n\t" \
  "v_mfma_f32_16x16x32_bf16 v[28:31], v[100:103], a[88:91], v[28:31]\n\t" \
  "s_cbranch_scc1 LWARM_%=\n\t" \
  "s_waitcnt vmcnt(0)\n\t" \
  "LWARM_%=:\n\t" \
  "s_barrier\n\t" \
  "s_waitcnt lgkmcnt(5)\n\t" \
  "v_mfma_f32_16x16x32_bf16 v[16:19], v[104:107], a[20:23], v[16:19]\n\t" \
  "ds_read_b128 v[128:131], v1 offset:16384\n\t" \
  "s_waitcnt lgkmcnt(5)\n\t" \
  "v_mfma_f32_16x16x32_bf16 v[20:23], v[108:111], a[20:23], v[20:23]\n\t" \
  "v_mfma_f32_16x16x32_bf16 v[24:27], v[104:107], a[92:95], v[24:27]\n\t" \
  "ds_read_b128 v[132:135], v1 offset:18432\n\t" \
  "v_mfma_f32_16x16x32_bf16 v[28:31], v[108:111], a[92:95], v[28:31]\n\t" \
  "s_add_i32 s62, s48, s42\n\t" \
  "s_add_i32 s62, s62, 1\n\t" \
  "s_waitcnt lgkmcnt(5)\n\t" \
  "v_mfma_f32_16x16x32_bf16 v[16:19], v[112:115], a[24:27], v[16:19]\n\t" \
  "ds_read_b128 v[136:139], v2 offset:16384\n\t" \
  "s_waitcnt lgkmcnt(5)\n\t" \
  "v_mfma_f32_16x16x32_bf16 v[20:23], v[116:119], a[24:27], v[20:23]\n\t" \
  "s_mul_i32 s63, s62, 43\n\t" \
  "s_lshr_b32 s63, s63, 7\n\t" \
  "v_mfma_f32_16x16x32_bf16 v[24:27], v[112:115], a[96:99], v[24:27]\n\t" \
  "ds_read_b128 v[140:143], v2 offset:18432\n\t" \
  "v_mfma_f32_16x16x32_bf16 v[28:31], v[116:119], a[96:99], v[28:31]\n\t" \
  "s_mul_i32 s57, s62, 688128\n\t" \
  "s_lshl_b32 s60, s63, 15\n\t" \
  "s_waitcnt lgkmcnt(5)\n\t" \
  "v_mfma_f32_16x16x32_bf16 v[16:19], v[120:123], a[28:31], v[16:19]\n\t" \
  "ds_read_b128 v[144:147], v1 offset:20480\n\t" \
  "s_waitcnt lgkmcnt(5)\n\t" \
  "v_mfma_f32_16x16x32_bf16 v[20:23], v[124:127], a[28:31], v[20:23]\n\t" \
  "s_add_u32 s57, s57, s60\n\t" \
  "s_lshr_b32 s60, s88, s63\n\t" \
  "v_mfma_f32_16x16x32_bf16 v[24:27], v[120:123], a[100:103], v[24:27]\n\t" \
  "ds_read_b128 v[148:151], v1 offset:22528\n\t" \
  "v_mfma_f32_16x16x32_bf16 v[28:31], v[124:127], a[100:103], v[28:31]\n\t" \
  "s_and_b32 s60, s60, 1\n\t" \
  "s_lshl_b32 s60, s60, 13\n\t" \
  "s_waitcnt lgkmcnt(5)\n\t" \
  "v_mfma_f32_16x16x32_bf16 v[16:19], v[128:131], a[32:35], v[16:19]\n\t" \
  "ds_read_b128 v[152:155], v2 offset:20480\n\t" \
  "s_waitcnt lgkmcnt(5)\n\t" \
  "v_mfma_f32_16x16x32_bf16 v[20:23], v[132:135], a[32:35], v[20:23]\n\t" \
  "s_add_u32 s57, s57, s60\n\t" \
  "s_add_u32 s57, s57, 656896\n\t" \
  "v_mfma_f32_16x16x32_bf16 v[24:27], v[128:131], a[104:107], v[24:27]\n\t" \
  "ds_read_b128 v[156:159], v2 offset:22528\n\t" \
  "v_mfma_f32_16x16x32_bf16 v[28:31], v[132:135], a[104:107], v[28:31]\n\t" \
  "s_cmp_lt_i32 s62, 1\n\t" \
  "s_cselect_b32 s57, 0xf0000000, s57\n\t" \
  "s_waitcnt lgkmcnt(5)\n\t" \
  "v_mfma_f32_16x16x32_bf16 v[16:19], v[136:139], a[36:39], v[16:19]\n\t" \
  "ds_read_b128 v[160:163], v1 offset:24576\n\t" \
  "s_waitcnt lgkmcnt(5)\n\t" \
  "v_mfma_f32_16x16x32_bf16 v[20:23], v[140:143], a[36:39], v[20:23]\n\t" \
  "s_cmp_lg_u32 s40, 0\n\t" \
  "s_cselect_b32 s57, 0xf0000000, s57\n\t" \
  "v_mfma_f32_16x16x32_bf16 v[24:27], v[136:139], a[108:111], v[24:27]\n\t" \
  "ds_read_b128 v[164:167], v1 offset:26624\n\t" \
  "v_mfma_f32_16x16x32_bf16 v[28:31], v[140:143], a[108:111], v[28:31]\n\t" \
  "s_waitcnt lgkmcnt(5)\n\t" \
  "v_mfma_f32_16x16x32_bf16 v[16:19], v[144:147], a[40:43], v[16:19]\n\t" \
  "ds_read_b128 v[168:171], v2 offset:24576\n\t" \
  "s_waitcnt lgkmcnt(5)\n\t" \
  "v_mfma_f32_16x16x32_bf16 v[20:23], v[148:151], a[40:43], v[20:23]\n\t" \
  "v_mfma_f32_16x16x32_bf16 v[24:27], v[144:147], a[112:115], v[24:27]\n\t" \
  "ds_read_b128 v[172:175], v2 offset:26624\n\t" \
  "v_mfma_f32_16x16x32_bf16 v[28:31], v[148:151], a[112:115], v[28:31]\n\t" \
  "s_waitcnt lgkmcnt(5)\n\t" \
  "v_mfma_f32_16x16x32_bf16 v[16:19], v[152:155], a[44:47], v[16:19]\n\t" \
  "ds_read_b128 v[176:179], v1 offset:28672\n\t" \
  "s_waitcnt lgkmcnt(5)\n\t" \
  "v_mfma_f32_16x16x32_bf16 v[20:23], v[156:159], a[44:47], v[20:23]\n\t" \
  "v_mfma_f32_16x16x32_bf16 v[24:27], v[152:155], a[116:119], v[24:27]\n\t" \
  "ds_read_b128 v[180:183], v1 offset:30720\n\t" \
  "v_mfma_f32_16x16x32_bf16 v[28:31], v[156:159], a[116:119], v[28:31]\n\t" \
  "s_waitcnt lgkmcnt(5)\n\t" \
  "v_mfma_f32_16x16x32_bf16 v[16:19], v[160:163], a[48:51], v[16:19]\n\t" \
  "ds_read_b128 v[184:187], v2 offset:28672\n\t" \
  "s_waitcnt lgkmcnt(5)\n\t" \
  "v_mfma_f32_16x16x32_bf16 v[20:23], v[164:167], a[48:51], v[20:23]\n\t" \
  "v_mfma_f32_16x16x32_bf16 v[24:27], v[160:163], a[120:123], v[24:27]\n\t" \
  "ds_read_b128 v[188:191], v2 offset:30720\n\t" \
  "v_mfma_f32_16x16x32_bf16 v[28:31], v[164:167], a[120:123], v[28:31]\n\t" \
  "s_waitcnt lgkmcnt(5)\n\t" \
  "v_mfma_f32_16x16x32_bf16 v[16:19], v[168:171], a[52:55], v[16:19]\n\t" \
  "ds_read_b128 v[192:195], v1 offset:32768\n\t" \
  "s_waitcnt lgkmcnt(5)\n\t" \
  "v_mfma_f32_16x16x32_bf16 v[20:23], v[172:175], a[52:55], v[20:23]\n\t" \
  "v_mfma_f32_16x16x32_bf16 v[24:27], v[168:171], a[124:127], v[24:27]\n\t" \
  "ds_read_b128 v[196:199], v1 offset:34816\n\t" \
  "v_mfma_f32_16x16x32_bf16 v[28:31], v[172:175], a[124:127], v[28:31]\n\t" \
  "s_waitcnt lgkmcnt(5)\n\t" \
  "v_mfma_f32_16x16x32_bf16 v[16:19], v[176:179], a[56:59], v[16:19]\n\t" \
  "ds_read_b128 v[200:203], v2 offset:32768\n\t" \
  "s_waitcnt lgkmcnt(5)\n\t" \
  "v_mfma_f32_16x16x32_bf16 v[20:23], v[180:183], a[56:59], v[20:23]\n\t" \
  "v_mfma_f32_16x16x32_bf16 v[24:27], v[176:179], a[128:131], v[24:27]\n\t" \
  "ds_read_b128 v[204:207], v2 offset:34816\n\t" \
  "v_mfma_f32_16x16x32_bf16 v[28:31], v[180:183], a[128:131], v[28:31]\n\t" \
  "s_waitcnt lgkmcnt(5)\n\t" \
  "v_mfma_f32_16x16x32_bf16 v[16:19], v[184:187], a[60:63], v[16:19]\n\t" \
  "s_waitcnt lgkmcnt(4)\n\t" \
  "v_mfma_f32_16x16x32_bf16 v[20:23], v[188:191], a[60:63], v[20:23]\n\t" \
  "buffer_load_dword v4, v7, %[X], 0 offen sc1\n\t" \
  "v_mfma_f32_16x16x32_bf16 v[24:27], v[184:187], a[132:135], v[24:27]\n\t" \
  "v_mfma_f32_16x16x32_bf16 v[28:31], v[188:191], a[132:135], v[28:31]\n\t" \
  "s_waitcnt lgkmcnt(3)\n\t" \
  "v_mfma_f32_16x16x32_bf16 v[16:19], v[192:195], a[64:67], v[16:19]\n\t" \
  "v_xor_b32 v1, 0x10000, v1\n\t" \
  "v_xor_b32 v2, 0x10000, v2\n\t" \
  "s_waitcnt lgkmcnt(2)\n\t" \
  "v_mfma_f32_16x16x32_bf16 v[20:23], v[196:199], a[64:67], v[20:23]\n\t" \
  "v_mfma_f32_16x16x32_bf16 v[24:27], v[192:195], a[136:139], v[24:27]\n\t" \
  "v_mfma_f32_16x16x32_bf16 v[28:31], v[196:199], a[136:139], v[28:31]\n\t" \
  "s_waitcnt lgkmcnt(1)\n\t" \
  "v_mfma_f32_16x16x32_bf16 v[16:19], v[200:203], a[68:71], v[16:19]\n\t" \
  "s_waitcnt lgkmcnt(0)\n\t" \
  "v_mfma_f32_16x16x32_bf16 v[20:23], v[204:207], a[68:71], v[20:23]\n\t" \
  "v_mfma_f32_16x16x32_bf16 v[24:27], v[200:203], a[140:143], v[24:27]\n\t" \
  "v_mfma_f32_16x16x32_bf16 v[28:31], v[204:207], a[140:143], v[28:31]\n\t" \
  "v_mfma_f32_16x16x32_bf16 v[32:35], v[64:67], a[144:147], 0\n\t" \
  "v_cvt_pk_bf16_f32 v48, v16, v17\n\t" \
  "v_cvt_pk_bf16_f32 v49, v18, v19\n\t" \
  "v_mfma_f32_16x16x32_bf16 v[36:39], v[68:71], a[144:147], 0\n\t" \
  "v_cvt_pk_bf16_f32 v50, v20, v21\n\t" \
  "v_cvt_pk_bf16_f32 v51, v22, v23\n\t" \
  "v_mfma_f32_16x16x32_bf16 v[32:35], v[72:75], a[148:151], v[32:35]\n\t" \
  "v_cvt_pk_bf16_f32 v52, v24, v25\n\t" \
  "v_cvt_pk_bf16_f32 v53, v26, v27\n\t" \
  "v_mfma_f32_16x16x32_bf16 v[36:39], v[76:79], a[148:151], v[36:39]\n\t" \
  "v_cvt_pk_bf16_f32 v54, v28, v29\n\t" \
  "v_cvt_pk_bf16_f32 v55, v30, v31\n\t" \
  "v_mfma_f32_16x16x32_bf16 v[32:35], v[80:83], a[152:155], v[32:35]\n\t" \
  "buffer_store_dwordx4 v[48:51], v0, %[X], s50 offen offset:0 sc1\n\t" \
  "v_mfma_f32_16x16x32_bf16 v[36:39], v[84:87], a[152:155], v[36:39]\n\t" \
  "v_mfma_f32_16x16x32_bf16 v[32:35], v[88:91], a[156:159], v[32:35]\n\t" \
  "v_mfma_f32_16x16x32_bf16 v[36:39], v[92:95], a[156:159], v[36:39]\n\t" \
  "v_mfma_f32_16x16x32_bf16 v[32:35], v[96:99], a[160:163], v[32:35]\n\t" \
  "v_mfma_f32_16x16x32_bf16 v[36:39], v[100:103], a[160:163], v[36:39]\n\t" \
  "v_mfma_f32_16x16x32_bf16 v[32:35], v[104:107], a[164:167], v[32:35]\n\t" \
  "v_mfma_f32_16x16x32_bf16 v[36:39], v[108:111], a[164:167], v[36:39]\n\t" \
  "v_mfma_f32_16x16x32_bf16 v[32:35], v[112:115], a[168:171], v[32:35]\n\t" \
  "v_mfma_f32_16x16x32_bf16 v[36:39], v[116:119], a[168:171], v[36:39]\n\t" \
  "v_mfma_f32_16x16x32_bf16 v[32:35], v[120:123], a[172:175], v[32:35]\n\t" \
  "v_mfma_f32_16x16x32_bf16 v[36:39], v[124:127], a[172:175], v[36:39]\n\t" \
  "s_waitcnt vmcnt(1)\n\t" \
  "v_cmp_eq_u32 vcc, 0, v4\n\t" \
  "v_mfma_f32_16x16x32_bf16 v[32:35], v[128:131], a[176:179], v[32:35]\n\t" \
  "v_mfma_f32_16x16x32_bf16 v[36:39], v[132:135], a[176:179], v[36:39]\n\t" \
  "s_cmp_eq_u64 vcc, 0\n\t" \
  "s_cselect_b32 s60, 1, 0\n\t" \
  "v_mov_b32 v4, s60\n\t" \
  "v_mfma_f32_16x16x32_bf16 v[32:35], v[136:139], a[180:183], v[32:35]\n\t" \
  "ds_write_b32 v5, v4\n\t" \
  "v_mfma_f32_16x16x32_bf16 v[36:39], v[140:143], a[180:183], v[36:39]\n\t" \
  "v_mfma_f32_16x16x32_bf16 v[32:35], v[144:147], a[184:187], v[32:35]\n\t" \
  "v_mfma_f32_16x16x32_bf16 v[36:39], v[148:151], a[184:187], v[36:39]\n\t" \
  "buffer_store_dwordx4 v[52:55], v0, %[X], s50 offen offset:1024 sc1\n\t" \
  "v_mfma_f32_16x16x32_bf16 v[32:35], v[152:155], a[188:191], v[32:35]\n\t" \
  "v_mfma_f32_16x16x32_bf16 v[36:39], v[156:159], a[188:191], v[36:39]\n\t" \
  "s_waitcnt lgkmcnt(0)\n\t" \
  "s_barrier\n\t" \
  "v_mfma_f32_16x16x32_bf16 v[32:35], v[160:163], a[192:195], v[32:35]\n\t" \
  "ds_read_b32 v4, v6 offset:40959\n\t" \
  "v_mfma_f32_16x16x32_bf16 v[36:39], v[164:167], a[192:195], v[36:39]\n\t" \
  "v_mfma_f32_16x16x32_bf16 v[32:35], v[168:171], a[196:199], v[32:35]\n\t" \
  "v_mfma_f32_16x16x32_bf16 v[36:39], v[172:175], a[196:199], v[36:39]\n\t" \
  "v_mfma_f32_16x16x32_bf16 v[32:35], v[176:179], a[200:203], v[32:35]\n\t" \
  "v_mfma_f32_16x16x32_bf16 v[36:39], v[180:183], a[200:203], v[36:39]\n\t" \
  "s_waitcnt lgkmcnt(0)\n\t" \
  "v_readfirstlane_b32 s60, v4\n\t" \
  "s_add_u32 s55, s83, s36\n\t" \
  "v_mfma_f32_16x16x32_bf16 v[32:35], v[184:187], a[204:207], v[32:35]\n\t" \
  "s_cmp_eq_u32 s48, 63\n\t" \
  "s_cselect_b32 s56, 0, s60\n\t" \
  "v_mfma_f32_16x16x32_bf16 v[36:39], v[188:191], a[204:207], v[36:39]\n\t" \
  "s_cmp_eq_u32 s56, 0\n\t" \
  "s_cselect_b32 s55, 0xf0000000, s55\n\t" \
  "v_mfma_f32_16x16x32_bf16 v[32:35], v[192:195], a[208:211], v[32:35]\n\t" \
  "v_mfma_f32_16x16x32_bf16 v[36:39], v[196:199], a[208:211], v[36:39]\n\t" \
  "v_mfma_f32_16x16x32_bf16 v[32:35], v[200:203], a[212:215], v[32:35]\n\t" \
  "s_add_u32 m0, s46, 0\n\t" \
  "s_add_u32 s47, s55, 0\n\t" \
  "buffer_load_dwordx4 v0, %[X], s47 offen sc1 lds\n\t" \
  "v_mfma_f32_16x16x32_bf16 v[36:39], v[204:207], a[212:215], v[36:39]\n\t" \
  "v_mfma_f32_16x16x32_bf16 v[40:43], v[64:67], a[216:219], 0\n\t" \
  "v_mfma_f32_16x16x32_bf16 v[44:47], v[68:71], a[216:219], 0\n\t" \
  "v_mfma_f32_16x16x32_bf16 v[40:43], v[72:75], a[220:223], v[40:43]\n\t" \
  "v_cvt_pk_bf16_f32 v56, v32, v33\n\t" \
  "v_cvt_pk_bf16_f32 v57, v34, v35\n\t" \
  "s_add_u32 m0, s46, 4096\n\t" \
  "s_add_u32 s47, s55, 4096\n\t" \
  "buffer_load_dwordx4 v0, %[X], s47 offen sc1 lds\n\t" \
  "v_mfma_f32_16x16x32_bf16 v[44:47], v[76:79], a[220:223], v[44:47]\n\t" \
  "v_cvt_pk_bf16_f32 v58, v36, v37\n\t" \
  "v_cvt_pk_bf16_f32 v59, v38, v39\n\t" \
  "v_mfma_f32_16x16x32_bf16 v[40:43], v[80:83], a[224:227], v[40:43]\n\t" \
  "v_mfma_f32_16x16x32_bf16 v[44:47], v[84:87], a[224:227], v[44:47]\n\t" \
  "buffer_store_dwordx4 v[56:59], v0, %[X], s50 offen offset:2048 sc1\n\t" \
  "v_mfma_f32_16x16x32_bf16 v[40:43], v[88:91], a[228:231], v[40:43]\n\t" \
  "s_add_u32 m0, s46, 8192\n\t" \
  "s_add_u32 s47, s55, 16384\n\t" \
  "buffer_load_dwordx4 v0, %[X], s47 offen sc1 lds\n\t" \
  "v_mfma_f32_16x16x32_bf16 v[44:47], v[92:95], a[228:231], v[44:47]\n\t" \
  "v_mfma_f32_16x16x32_bf16 v[40:43], v[96:99], a[232:235], v[40:43]\n\t" \
  "v_mfma_f32_16x16x32_bf16 v[44:47], v[100:103], a[232:235], v[44:47]\n\t" \
  "v_mfma_f32_16x16x32_bf16 v[40:43], v[104:107], a[236:239], v[40:43]\n\t" \
  "s_add_u32 m0, s46, 12288\n\t" \
  "s_add_u32 s47, s55, 20480\n\t" \
  "buffer_load_dwordx4 v0, %[X], s47 offen sc1 lds\n\t" \
  "v_mfma_f32_16x16x32_bf16 v[44:47], v[108:111], a[236:239], v[44:47]\n\t" \
  "v_mfma_f32_16x16x32_bf16 v[40:43], v[112:115], a[240:243], v[40:43]\n\t" \
  "v_mfma_f32_16x16x32_bf16 v[44:47], v[116:119], a[240:243], v[44:47]\n\t" \
  "v_mfma_f32_16x16x32_bf16 v[40:43], v[120:123], a[244:247], v[40:43]\n\t" \
  "s_add_u32 m0, s46, 16384\n\t" \
  "s_add_u32 s47, s55, 32768\n\t" \
  "buffer_load_dwordx4 v0, %[X], s47 offen sc1 lds\n\t" \
  "v_mfma_f32_16x16x32_bf16 v[44:47], v[124:127], a[244:247], v[44:47]\n\t" \
  "v_mfma_f32_16x16x32_bf16 v[40:43], v[128:131], a[248:251], v[40:43]\n\t" \
  "v_mfma_f32_16x16x32_bf16 v[44:47], v[132:135], a[248:251], v[44:47]\n\t" \
  "v_mfma_f32_16x16x32_bf16 v[40:43], v[136:139], a[252:255], v[40:43]\n\t" \
  "s_add_u32 m0, s46, 20480\n\t" \
  "s_add_u32 s47, s55, 36864\n\t" \
  "buffer_load_dwordx4 v0, %[X], s47 offen sc1 lds\n\t" \
  "v_mfma_f32_16x16x32_bf16 v[44:47], v[140:143], a[252:255], v[44:47]\n\t" \
  "v_mfma_f32_16x16x32_bf16 v[40:43], v[144:147], v[224:227], v[40:43]\n\t" \
  "v_mfma_f32_16x16x32_bf16 v[44:47], v[148:151], v[224:227], v[44:47]\n\t" \
  "v_mfma_f32_16x16x32_bf16 v[40:43], v[152:155], v[228:231], v[40:43]\n\t" \
  "s_add_u32 m0, s46, 24576\n\t" \
  "s_add_u32 s47, s55, 49152\n\t" \
  "buffer_load_dwordx4 v0, %[X], s47 offen sc1 lds\n\t" \
  "v_mfma_f32_16x16x32_bf16 v[44:47], v[156:159], v[228:231], v[44:47]\n\t" \
  "v_mfma_f32_16x16x32_bf16 v[40:43], v[160:163], v[232:235], v[40:43]\n\t" \
  "v_mfma_f32_16x16x32_bf16 v[44:47], v[164:167], v[232:235], v[44:47]\n\t" \
  "v_mfma_f32_16x16x32_bf16 v[40:43], v[168:171], v[236:239], v[40:43]\n\t" \
  "s_add_u32 m0, s46, 28672\n\t" \
  "s_add_u32 s47, s55, 53248\n\t" \
  "buffer_load_dwordx4 v0, %[X], s47 offen sc1 lds\n\t" \
  "v_mfma_f32_16x16x32_bf16 v[44:47], v[172:175], v[236:239], v[44:47]\n\t" \
  "v_mfma_f32_16x16x32_bf16 v[40:43], v[176:179], v[240:243], v[40:43]\n\t" \
  "v_mfma_f32_16x16x32_bf16 v[44:47], v[180:183], v[240:243], v[44:47]\n\t" \
  "v_mfma_f32_16x16x32_bf16 v[40:43], v[184:187], v[244:247], v[40:43]\n\t" \
  "s_add_u32 m0, s46, 32768\n\t" \
  "s_add_u32 s47, s55, 65536\n\t" \
  "buffer_load_dwordx4 v0, %[X], s47 offen sc1 lds\n\t" \
  "v_mfma_f32_16x16x32_bf16 v[44:47], v[188:191], v[244:247], v[44:47]\n\t" \
  "v_mfma_f32_16x16x32_bf16 v[40:43], v[192:195], v[248:251], v[40:43]\n\t" \
  "v_mfma_f32_16x16x32_bf16 v[44:47], v[196:199], v[248:251], v[44:47]\n\t" \
  "buffer_load_dword v8, off, %[X], s57 sc1\n\t" \
  "v_mfma_f32_16x16x32_bf16 v[40:43], v[200:203], v[252:255], v[40:43]\n\t" \
  "v_mfma_f32_16x16x32_bf16 v[44:47], v[204:207], v[252:255], v[44:47]\n\t" \
  "s_nop 4\n\t" \
  "v_cvt_pk_bf16_f32 v60, v40, v41\n\t" \
  "v_cvt_pk_bf16_f32 v61, v42, v43\n\t" \
  "s_nop 1\n\t" \
  "v_cvt_pk_bf16_f32 v62, v44, v45\n\t" \
  "v_cvt_pk_bf16_f32 v63, v46, v47\n\t" \
  "buffer_store_dwordx4 v[60:63], v0, %[X], s50 offen offset:3072 sc1\n\t" \
  "s_add_u32 s49, s82, s38\n\t" \
  "s_cmp_eq_u32 s48, 63\n\t" \
  "s_cbranch_scc1 LEND_%=\n\t" \
  "s_add_u32 s48, s48, 1\n\t" \
  "s_cmp_eq_u32 s56, 0\n\t" \
  "s_cbranch_scc1 LCOLD_%=\n\t" \
  "s_waitcnt vmcnt(0)\n\t" \
  "s_mov_b64 exec, 1\n\t" \
  "buffer_store_dword v6, off, %[X], s49 sc1\n\t" \
  "s_mov_b64 exec, -1\n\t" \
  "s_mov_b32 s49, 0xf0000000\n\t" \
  "s_cmp_lt_i32 s57, 0\n\t" \
  "s_cbranch_scc1 LTOP_%=\n\t" \
  "s_movk_i32 s63, 4000\n\t" \
  "s_branch LGW_%=u\n\t" \
  "LGW_%=:\n\t" \
  "s_sleep " TK_PSG "\n\t" \
  "buffer_load_dword v8, off, %[X], s57 sc1\n\t" \
  "s_waitcnt vmcnt(0)\n\t" \
  "LGW_%=u:\n\t" \
  "s_memrealtime s[52:53]\n\t" \
  "v_readfirstlane_b32 s60, v8\n\t" \
  "s_waitcnt lgkmcnt(0)\n\t" \
  "s_sub_u32 s63, s63, 1\n\t" \
  "s_cbranch_scc1 LTOP_%=\n\t" \
  "s_cmp_eq_u32 s60, -1\n\t" \
  "s_cbranch_scc1 LGW_%=\n\t" \
  "s_not_b32 s60, s60\n\t" \
  "s_lshl_b32 s61, s52, 8\n\t" \
  "s_sub_u32 s60, s60, s61\n\t" \
  "s_ashr_i32 s60, s60, 8\n\t" \
  "s_sub_i32 s60, s60, " TK_QXW "\n\t" \
  "s_min_i32 s60, s60, 400\n\t" \
  "s_add_u32 s54, s52, s60\n\t" \
  "LGW_%=w:\n\t" \
  "s_sleep 1\n\t" \
  "s_memrealtime s[52:53]\n\t" \
  "s_waitcnt lgkmcnt(0)\n\t" \
  "s_sub_i32 s53, s52, s54\n\t" \
  "s_cmp_lt_i32 s53, 0\n\t" \
  "s_cbranch_scc1 LGW_%=w\n\t" \
  "s_branch LTOP_%=\n\t" \
  "LEND_%=:\n\t" \
  "s_waitcnt vmcnt(0)\n\t" \
  "s_mov_b64 exec, 1\n\t" \
  "buffer_store_dword v6, off, %[X], s49 sc1\n\t" \
  "s_mov_b64 exec, -1\n\t" \
  "s_mov_b32 s49, 0xf0000000\n\t" \
  ""
__device__ __forceinline__ unsigned hoff(int r, int piece) {
  const int m = 4 * (r >> 3) + (r & 3), kp = piece >> 3;
  return (P_H + (unsigned)(kp >> 1)) * PAIR + (unsigned)((kp & 1) * 4096 + (16 * ((r >> 2) & 1) + m) * 128 + (((piece ^ m) & 7) << 4));
}
template <int SLEEP>
__device__ __forceinline__ void wait_all(rsrc_t X, unsigned vo, unsigned so) {
  unsigned spins = 0;
  while (true) {
    asm volatile("" ::: "memory");
    if (__all(ld4(X, vo, so) != 0u)) break;
    if (spin_fail(spins, X)) break;
    __builtin_amdgcn_s_sleep(SLEEP);
  }
  asm volatile("" ::: "memory");
}
#define VQ_CLOB "v8", "v16", "v17", "v18", "v19", "v20", "v21", "v22", "v23", "v24", "v25", "v26", "v27", "v28", "v29", "v30", "v31", "v32", "v33", "v34", "v35", "v36", "v37", "v38", "v39", "v40", "v41", "v42", "v43", "v44", "v45", "v46", "v47", "v48", "v49", "v50", "v51", "v52", "v53", "v54", "v55", "v56", "v57", "v58", "v59", "v60", "v61", "v62", "v63", "v64", "v65", "v66", "v67", "v68", "v69", "v70", "v71", "v72", "v73", "v74", "v75", "v76", "v77", "v78", "v79", "v80", "v81", "v82", "v83", "v84", "v85", "v86", "v87", "v88", "v89", "v90", "v91", "v92", "v93", "v94", "v95", "v96", "v97", "v98", "v99", "v100", "v101", "v102", "v103", "v104", "v105", "v106", "v107", "v108", "v109", "v110", "v111", "v112", "v113", "v114", "v115", "v116", "v117", "v118", "v119", "v120", "v121", "v122", "v123", "v124", "v125", "v126", "v127", "v128", "v129", "v130", "v131", "v132", "v133", "v134", "v135", "v136", "v137", "v138", "v139", "v140", "v141", "v142", "v143", "v144", "v145", "v146", "v147", "v148", "v149", "v150", "v151", "v152", "v153", "v154", "v155", "v156", "v157", "v158", "v159", "v160", "v161", "v162", "v163", "v164", "v165", "v166", "v167", "v168", "v169", "v170", "v171", "v172", "v173", "v174", "v175", "v176", "v177", "v178", "v179", "v180", "v181", "v182", "v183", "v184", "v185", "v186", "v187", "v188", "v189", "v190", "v191", "v192", "v193", "v194", "v195", "v196", "v197", "v198", "v199", "v200", "v201", "v202", "v203", "v204", "v205", "v206", "v207", "v220", "v221"
#define VA_CLOB "v40", "v41", "v42", "v43", "v44", "v45", "v46", "v47", "v48", "v49", "v50", "v51", "v52", "v53", "v54", "v55", "v56", "v57", "v58", "v59", "v60", "v61", "v62", "v63", "v64", "v65", "v66", "v67", "v68", "v69", "v70", "v71", "v72", "v73", "v74", "v75", "v76", "v77", "v78", "v79", "v80", "v81", "v82", "v83", "v84", "v85", "v86", "v87", "v88", "v89", "v90", "v91", "v92", "v93", "v94", "v95", "v96", "v97", "v98", "v99", "v100", "v101", "v102", "v103", "v104", "v105", "v106", "v107", "v108", "v109", "v110", "v111", "v112", "v113", "v114", "v115", "v116", "v117", "v118", "v119", "v120", "v121", "v122", "v123", "v124", "v125", "v126", "v127", "v128", "v129", "v130", "v131", "v132", "v133", "v134", "v135", "v136", "v137", "v138", "v139", "v140", "v141", "v142", "v143", "v144", "v145", "v146", "v147", "v148", "v149", "v150", "v151", "v152", "v153", "v154", "v155", "v156", "v157", "v158", "v159", "v160", "v161", "v162", "v163", "v164", "v165", "v166", "v167", "v168", "v169", "v170", "v171", "v172", "v173", "v174", "v175", "v176", "v177", "v178", "v179", "v180", "v181", "v182", "v183", "v184", "v185", "v186", "v187", "v188", "v189", "v190", "v191", "v192", "v193", "v194", "v195", "v196", "v197", "v198", "v199", "v200", "v201", "v202", "v203", "v204", "v205", "v206", "v207", "v208", "v209", "v210", "v211", "v220", "v221"
#define TOK_COMMON() \
  const MP p = getp(); \
  const int c = (int)blockIdx.x - NCHW, xq = c & 3, L = (c >> 2) & 1, i = c >> 3; \
  const int tid = threadIdx.x, lane = tid & 63, w = __builtin_amdgcn_readfirstlane(tid >> 6); \
  const rsrc_t X = mkr(p.xb, XB_BYTES)
#define W_IN [W] "s"(WR)
#define W_CLOB "a0", "a1", "a2", "a3", "a4", "a5", "a6", "a7", "a8", "a9", "a10", "a11", "a12", "a13", "a14", "a15", "a16", "a17", "a18", "a19", "a20", "a21", "a22", "a23", "a24", "a25", "a26", "a27", "a28", "a29", "a30", "a31", "a32", "a33", "a34", "a35", "a36", "a37", "a38", "a39", "a40", "a41", "a42", "a43", "a44", "a45", "a46", "a47", "a48", "a49", "a50", "a51", "a52", "a53", "a54", "a55", "a56", "a57", "a58", "a59", "a60", "a61", "a62", "a63", "a64", "a65", "a66", "a67", "a68", "a69", "a70", "a71", "a72", "a73", "a74", "a75", "a76", "a77", "a78", "a79", "a80", "a81", "a82", "a83", "a84", "a85", "a86", "a87", "a88", "a89", "a90", "a91", "a92", "a93", "a94", "a95", "a96", "a97", "a98", "a99", "a100", "a101", "a102", "a103", "a104", "a105", "a106", "a107", "a108", "a109", "a110", "a111", "a112", "a113", "a114", "a115", "a116", "a117", "a118", "a119", "a120", "a121", "a122", "a123", "a124", "a125", "a126", "a127", "a128", "a129", "a130", "a131", "a132", "a133", "a134", "a135", "a136", "a137", "a138", "a139", "a140", "a141", "a142", "a143", "a144", "a145", "a146", "a147", "a148", "a149", "a150", "a151", "a152", "a153", "a154", "a155", "a156", "a157", "a158", "a159", "a160", "a161", "a162", "a163", "a164", "a165", "a166", "a167", "a168", "a169", "a170", "a171", "a172", "a173", "a174", "a175", "a176", "a177", "a178", "a179", "a180", "a181", "a182", "a183", "a184", "a185", "a186", "a187", "a188", "a189", "a190", "a191", "a192", "a193", "a194", "a195", "a196", "a197", "a198", "a199", "a200", "a201", "a202", "a203", "a204", "a205", "a206", "a207", "a208", "a209", "a210", "a211", "a212", "a213", "a214", "a215", "a216", "a217", "a218", "a219", "a220", "a221", "a222", "a223", "a224", "a225", "a226", "a227", "a228", "a229", "a230", "a231", "a232", "a233", "a234", "a235", "a236", "a237", "a238", "a239", "a240", "a241", "a242", "a243", "a244", "a245", "a246", "a247", "a248", "a249", "a250", "a251", "a252", "a253", "a254", "a255", "v224", "v225", "v226", "v227", "v228", "v229", "v230", "v231", "v232", "v233", "v234", "v235", "v236", "v237", "v238", "v239", "v240", "v241", "v242", "v243", "v244", "v245", "v246", "v247", "v248", "v249", "v250", "v251", "v252", "v253", "v254", "v255"
#define BOOT_IN "{s67}"(L), [ctab] "i"(CTAB_OFF)
#define S_CLOB "s88", "s89", "s90", "s91", "s46", "s47", "s48", "s49", "s50", "s51", "s52", "s53", "s54", "s55", "s56", "s57", "s58", "s59", "s60", "s61", "s62", "s63", "s68", "s69", "s70", "s71", "s72", "s73", "s82", "s83", "s84", "m0", "vcc", "scc", "memory"
typedef unsigned v8w __attribute__((ext_vector_type(8)));

__device__ __forceinline__ void qkv_role() {
  TOK_COMMON();
  const int role = i * 4 + w, hh = 8 * xq + role / 6, part = role % 6, which = part >> 1, half = part & 1;
  const rsrc_t WR = mkr((which == 0 ? p.Wq : (which == 1 ? p.Wk : p.Wv)) + hh * DH + 64 * half, DM * HD * 2u - (unsigned)(hh * DH + 64 * half) * 2u);
  const int m = lane & 15, g = lane >> 4;
  const unsigned a0 = (unsigned)(m * 128 + ((g ^ (m & 7)) << 4));
  v8w lv = {(unsigned)lane * 16u, a0, a0 ^ 64u, (unsigned)(lane & (L ? 15 : 3)) * 128u, 0u, 40960u + 4u * (unsigned)w, 1u, 0u};
  const unsigned c36 = P_H * PAIR + (unsigned)w * 1024u, c37 = (unsigned)(3 * hh + which) * PAIR + (unsigned)half * (which == 2 ? 4096u : 2048u);
  const unsigned c38 = P_FA * PAIR + (unsigned)(hh * 128 + part * 16), c39 = w == 0 ? P_FB * PAIR + 4096u : 0xF0000000u, c41 = (unsigned)w * 1024u;
  const int c42 = L ? 1 - QLEAD1 : 1 - QLEAD;
  unsigned d74 = TRC_OFF + 262144u + (unsigned)((c * 4 + w) * 66) * 8u;
#define Q_CALL(A) asm volatile("s_waitcnt vmcnt(0) lgkmcnt(0)\n\t" A : "+{v[0:7]}"(lv), "+{s74}"(d74) \
               : [X] "s"(X), "{s36}"(c36), "{s37}"(c37), "{s38}"(c38), "{s39}"(c39), "{s40}"(w), "{s41}"(c41), "{s42}"(c42), "{s45}"(i), "{s92}"(TMO_OFF), BOOT_IN, [cap] "i"(SPIN_CAP), [qxw] "i"(QXW), [qxc] "i"(QXC), W_IN : VQ_CLOB, W_CLOB, S_CLOB)
  if (which == 2) { Q_CALL(QV_ASM "LQVEND:"); __builtin_amdgcn_endpgm(); }
  Q_CALL(QK_ASM "LQKEND:");
  __builtin_amdgcn_endpgm();
}

__device__ __forceinline__ void ap_role() {
  TOK_COMMON();
  const int hp = 4 * xq + ((i - 12) >> 1), ch = i & 1, a = w >> 1, rt = w & 1, h = 2 * hp + a;
  const int n = lane & 15, g = lane >> 4;
  const rsrc_t WR = mkr(p.Wo + (long)hp * 2 * DH * DM, 2u * DH * DM * 2u);
  const unsigned l16 = (unsigned)lane * 16u, ab = (unsigned)a * 24576u + l16;
  v16u lv = {l16, ab, ab + (unsigned)rt * 4096u, 49152u + (unsigned)w * 4096u + l16, 49152u + (unsigned)(w ^ 2) * 4096u + l16, l16 & ~32u, l16 >> 1,
             (unsigned)(g * 262144 + n * 16), (unsigned)(n * 32 + g * 8), (unsigned)((lane & 7) % 6) * 16u, 0u, 0u, 0u, 0u, 0u, 0u};
  v8w mk;
  const int qi = 8 * (n >> 2) + (n & 3) + 4 * rt;
#pragma unroll
  for (int k = 0; k < 8; ++k) mk[k] = 8 * g + k > qi ? 0xFF800000u : 0u;
  v16u zz = {0u, 0u, 0u, 0u, 0u, 0u, 0u, 0u, 0u, 0u, 0u, 0u, 0u, 0u, 0u, 0u};
  const unsigned tb = (unsigned)(3 * h) * PAIR;
  const unsigned c36 = tb + (rt ? PAIR + 4096u : 0u), c37 = tb + (rt ? 2u * PAIR : 4096u), c38 = tb + (rt ? 2u * PAIR + 4096u : PAIR);
  const unsigned c39 = (P_KV + (unsigned)(h >> 1)) * PAIR + (unsigned)(h & 1) * 4096u, c41 = P_FA * PAIR + (unsigned)h * 128u;
  const unsigned c42 = w == 0 ? P_FB * PAIR + (unsigned)(2 * hp + ch) * 128u : 0xF0000000u;
  const unsigned c43 = TP0 * XBLK + (unsigned)(8 * rt + (hp >> 3)) * PAIR + (unsigned)((hp & 7) * 1024 + ch * 512 + a * 256);
  unsigned d44 = (TP0 + 8u) * XBLK + (unsigned)(hp >> 1) * PAIR + (unsigned)((((hp & 1) * 4 + 2 * ch + a) * 2 + rt) * 512);
  const unsigned c45 = (unsigned)(a * 24576 + rt * 12288);
  unsigned d74 = TRC_OFF + 262144u + (unsigned)((c * 4 + w) * 66) * 8u;
  asm volatile("s_waitcnt vmcnt(0) lgkmcnt(0)\n\t" AP_ASM "LAPEND:" : "+{v[0:15]}"(lv), "+{v[24:39]}"(zz), "+{s74}"(d74), "+{s44}"(d44)
               : [X] "s"(X), "{s36}"(c36), "{s37}"(c37), "{s38}"(c38), "{s39}"(c39), "{s41}"(c41), "{s42}"(c42), "{s43}"(c43), "{s45}"(c45), "{s64}"(512 * ch + 256 * a), "{s65}"(1024 + 64 * ch + 32 * a), "{s66}"(147456 * a), BOOT_IN,
                 "{s92}"(TMO_OFF), [cap] "i"(SPIN_CAP), "{v[16:23]}"(mk), W_IN : VA_CLOB, W_CLOB, S_CLOB);
  __builtin_amdgcn_endpgm();
}

template <int N> __device__ __forceinline__ float rorN(float v) { return __uint_as_float((unsigned)__builtin_amdgcn_update_dpp(0, (int)__float_as_uint(v), 0x120 + N, 0xf, 0xf, false)); }
__device__ __forceinline__ void up8(v4u v, float* x) {
  x[0] = lo2f(v.x); x[1] = hi2f(v.x); x[2] = lo2f(v.y); x[3] = hi2f(v.y); x[4] = lo2f(v.z); x[5] = hi2f(v.z); x[6] = lo2f(v.w); x[7] = hi2f(v.w);
}
__device__ __forceinline__ v4u pk8(const float* x) { return (v4u){pk2(x[0], x[1]), pk2(x[2], x[3]), pk2(x[4], x[5]), pk2(x[6], x[7])}; }

__device__ __forceinline__ void rb9(float (&x)[9]) {
#pragma unroll
  for (int i = 0; i < 8; i += 2) { const unsigned u = pk2(x[i], x[i + 1]); x[i] = lo2f(u); x[i + 1] = hi2f(u); }
  x[8] = lo2f(pk2(x[8], x[8]));
}
template <int R>
__device__ __forceinline__ void lnR(const float (&x)[R][9], float (&o)[R][9]) {
  float s[2 * R], pv[R];
#pragma unroll
  for (int r = 0; r < R; ++r) {
    pv[r] = __uint_as_float(__builtin_amdgcn_readfirstlane(__float_as_uint(x[r][0])));
    const f32x2 p2 = {pv[r], pv[r]};
    f32x2 d[4];
#pragma unroll
    for (int i = 0; i < 4; ++i) d[i] = (f32x2){x[r][2 * i], x[r][2 * i + 1]} - p2;
    const float d8 = x[r][8] - pv[r];
    const f32x2 a = (d[0] + d[1]) + (d[2] + d[3]);
    const f32x2 q = __builtin_elementwise_fma(d[3], d[3], __builtin_elementwise_fma(d[2], d[2], __builtin_elementwise_fma(d[1], d[1], d[0] * d[0])));
    s[2 * r] = (a[0] + d8) + a[1];
    s[2 * r + 1] = __builtin_fmaf(d8, d8, q[0]) + q[1];
  }
#pragma unroll
  for (int k = 0; k < 2 * R; ++k) s[k] += rorN<8>(s[k]);
#pragma unroll
  for (int k = 0; k < 2 * R; ++k) s[k] += rorN<4>(s[k]);
#pragma unroll
  for (int k = 0; k < 2 * R; ++k) s[k] += rorN<2>(s[k]);
#pragma unroll
  for (int k = 0; k < 2 * R; ++k) s[k] += rorN<1>(s[k]);
#pragma unroll
  for (int r = 0; r < R; ++r) {
    auto u = __builtin_amdgcn_permlane16_swap(__float_as_uint(s[2 * r]), __float_as_uint(s[2 * r + 1]), false, false);
    s[2 * r] = __uint_as_float(u[0]) + __uint_as_float(u[1]);
  }
#pragma unroll
  for (int r = 0; r < R; ++r) {
    const unsigned u = __float_as_uint(s[2 * r]);
    const float md = (__uint_as_float(__builtin_amdgcn_readlane(u, 0)) + __uint_as_float(__builtin_amdgcn_readlane(u, 32))) * (1.0f / DM);
    const float sq = (__uint_as_float(__builtin_amdgcn_readlane(u, 16)) + __uint_as_float(__builtin_amdgcn_readlane(u, 48))) * (1.0f / DM);
    const unsigned mv = pk2(pv[r] + md, __builtin_fmaxf(__builtin_fmaf(-md, md, sq), 0.f));
    const float m = lo2f(mv), var = hi2f(mv);
    const float ve = lo2f(pk2(var + 1e-5f, 0.f));
    const float den = lo2f(pk2(__builtin_amdgcn_sqrtf(ve), 0.f));
    const float inv = __builtin_amdgcn_rcpf(den);
#pragma unroll
    for (int i = 0; i < 9; ++i) o[r][i] = (x[r][i] - m) * inv;
  }
}
__device__ __forceinline__ void ln9(const float (&x)[9], float (&o)[9]) { lnR<1>((const float (&)[1][9])x, (float (&)[1][9])o); }
__device__ __forceinline__ v4u gat8(float v) {
  const unsigned d = pk2(v, __uint_as_float((unsigned)__builtin_amdgcn_mov_dpp((int)__float_as_uint(v), 0xB1, 0xf, 0xf, true)));
  return (v4u){d, (unsigned)__builtin_amdgcn_mov_dpp((int)d, 0x102, 0xf, 0xf, true), (unsigned)__builtin_amdgcn_mov_dpp((int)d, 0x104, 0xf, 0xf, true),
               (unsigned)__builtin_amdgcn_mov_dpp((int)d, 0x106, 0xf, 0xf, true)};
}
__device__ __forceinline__ void tok_row(const MP& p, const rsrc_t X, unsigned sb1, int L, int s, int r, int lane, const v4u (&v)[18], v4u px, v4u py, unsigned px9, unsigned py9) {
  const long tok = (long)s * SEG + r;
  const int m = lane & 15, g = lane >> 4;
  const int xp = 4 * (g & 1) + 2 * (g >> 1) + (m >> 3);
  const unsigned zr = opq(0u);
  const unsigned one = (g == (m >> 2)) ? (opq(0x3F80u) << (16 * (m & 1))) : zr;
  const unsigned ax = (m & 2) ? zr : one, ay = (m & 2) ? one : zr;
  const v4u A1 = {ax, ay, zr, zr}, A2 = {zr, zr, ax, ay};
  const unsigned on2 = opq(0x3F80u) << (16 * (m & 1));
  const int q2 = (m >> 1) & 3;
  const v4u sel = {q2 == 0 ? on2 : zr, q2 == 1 ? on2 : zr, q2 == 2 ? on2 : zr, q2 == 3 ? on2 : zr};
  f32x4 d1, d2, e1;
  asm volatile("s_nop 1\n\t" MF "%0, %1, %3, 0\n\t" MF "%0, %2, %3, %0" : "=&v"(e1) : "v"(v[0]), "v"(v[1]), "v"(sel));
#pragma unroll
  for (int i = 0; i < 16; ++i) {
    if (i == 0) asm volatile("s_nop 1\n\t" MF "%0, %2, %4, 0\n\t" MF "%1, %3, %4, 0" : "=&v"(d1), "=&v"(d2) : "v"(A1), "v"(A2), "v"(v[2]));
    else asm volatile(MF "%0, %2, %4, %0\n\t" MF "%1, %3, %4, %1" : "+v"(d1), "+v"(d2) : "v"(A1), "v"(A2), "v"(v[2 + i]));
  }
  asm volatile("s_nop 7" : "+v"(d1), "+v"(d2), "+v"(e1));
  float o[9];
#pragma unroll
  for (int i = 0; i < 4; ++i) { o[i] = d1[i]; o[4 + i] = d2[i]; }
  {
    auto a02 = __builtin_amdgcn_permlane32_swap(__float_as_uint(e1[0]), __float_as_uint(e1[2]), false, false);
    auto a13 = __builtin_amdgcn_permlane32_swap(__float_as_uint(e1[1]), __float_as_uint(e1[3]), false, false);
    o[8] = m >= 8 ? __uint_as_float(a13[0]) + __uint_as_float(a13[1]) : __uint_as_float(a02[0]) + __uint_as_float(a02[1]);
  }
  float x[9];
  up8(px, x); x[8] = lo2f(px9);
#pragma unroll
  for (int i = 0; i < 9; ++i) x[i] += o[i];
  if (L == 0) {
    float y[9], ln[9];
    up8(py, y); y[8] = lo2f(py9);
    rb9(x);
    ln9(x, ln);
#pragma unroll
    for (int i = 0; i < 9; ++i) x[i] = __builtin_fmaf(ln[i], y[i], x[i]);
    rb9(x);
    st16(X, (unsigned)lane * 16u, sb1 + (P_X3 + (unsigned)(r >> 3)) * PAIR + (unsigned)(r & 7) * 1024u, pk8(x));
    const v4u x9 = gat8(x[8]);
    if ((lane & 7) == 0) st16(X, (unsigned)xp * 16u, sb1 + (P_H + 4u) * PAIR + 4096u + (unsigned)r * 128u, x9);
    ln9(x, ln);
    st16(X, hoff(r, lane), sb1, pk8(ln));
    const v4u h9 = gat8(ln[8]);
    if ((lane & 7) == 0) st16(X, hoff(r, 64 + xp), sb1, h9);
  } else {
    *(v4u*)&p.out[tok * DM + 8 * lane] = pk8(x);
    const v4u o9 = gat8(x[8]);
    if ((lane & 7) == 0) *(v4u*)&p.out[tok * DM + 512 + 8 * xp] = o9;
  }
}

__device__ __forceinline__ void row_role(char* smem) {
  TOK_COMMON();
  const int rw = 4 * xq + (i - 20), r = 2 * rw + (w >> 1);
  const unsigned long long sm = die_mask(X, SG0, L, lane);
  const unsigned tm = (unsigned)die_mask(X, TP0, L, lane), dm = (unsigned)die_mask(X, 0u, L, lane);
  __attribute__((address_space(3))) unsigned* cnt = (__attribute__((address_space(3))) unsigned*)smem;
  BAR();
  if (tid < 64) cnt[tid] = 0u;
  BAR();
  for (int s = w & 1; s < NSEG; s += 2) {
    int ln_ = lane; asm volatile("" : "+v"(ln_));
    const int m = ln_ & 15, g = ln_ >> 4;
    const unsigned l16 = (unsigned)ln_ * 16u, xo = (unsigned)(8 * (4 * (g & 1) + 2 * (g >> 1) + (m >> 3)) + (m & 7)) * 2u;
    const unsigned sb = SEGB(sm, s), sb1 = sb ^ (L ? 0u : 8192u);
    if (s + ARMK < NSEG) {
      const unsigned sv = opq(SENT), sl = SLOT_AT(s + ARMK) + ((dm >> ((s + ARMK) / 3)) & 1u) * 8192u;
      for (int j = r; j < 84; j += 32) {
#pragma unroll
        for (int k = 0; k < 4; ++k) st16(X, l16 + (unsigned)k * 1024u, sl + (unsigned)(j >> 1) * PAIR + (unsigned)(j & 1) * 4096u, (v4u){sv, sv, sv, sv});
      }
    }
    wait_all<PSL>(X, 0u, sb + P_FB * PAIR + 4096u + (unsigned)(L ? rw : r >> 3) * 128u);
    const v4u px = ld16(X, l16, sb + (P_X3 + (unsigned)(r >> 3)) * PAIR + (unsigned)(r & 7) * 1024u);
    const unsigned px9 = __builtin_amdgcn_raw_buffer_load_b16(X, xo, sb + (P_H + 4u) * PAIR + 4096u + (unsigned)r * 128u, 16);
    const rsrc_t YX = mkr(p.yT + (2 * s + (r >> 4)) * 9216, L ? 0u : 18432u);
    const v2u ya = ld8(YX, (unsigned)(ln_ * 256 + ((r ^ m) & 15) * 8), 0u), yb = ld8(YX, (unsigned)(ln_ * 256 + 128 + ((r ^ m) & 15) * 8), 0u);
    const v4u py = {ya.x, ya.y, yb.x, yb.y};
    const unsigned py9 = __builtin_amdgcn_raw_buffer_load_b16(YX, 16384u + (xo >> 3) * 128u + (((unsigned)r ^ (xo >> 4)) & 15u) * 8u + (xo & 6u), 0u, 16);
    if (__builtin_amdgcn_readfirstlane(ld4(X, 0u, sb + P_FA * PAIR + 4096u + (unsigned)r * 128u)) == 0u) {
      wait_all<PSR>(X, 0u, sb + P_FA * PAIR + 4096u + (unsigned)r * 128u);
      __builtin_amdgcn_s_sleep(RDLY);
    }
    wait_all<PSP>(X, 0u, sb + P_FB * PAIR + (unsigned)r * 128u);
    __builtin_amdgcn_s_sleep(RONE);
    wait_all<PSP>(X, (unsigned)(ln_ & 31) * 128u, sb + P_FB * PAIR);
    const unsigned j = (unsigned)s & 15u, to = TP0 * XBLK + (j << 20) + ((tm >> (j >> 1)) & 1u) * 8192u + (unsigned)r * 2u * PAIR;
    const unsigned t9 = (TP0 + 8u) * XBLK + ((tm >> 8) & 1u) * 8192u + (j << 17) + (unsigned)(((r >> 2) & 1) * 512 + (4 * (r >> 3) + (r & 3)) * 32);
    const unsigned v9 = (unsigned)(g * (int)PAIR + (m >> 1) * 1024 + (m & 1) * 16);
    v4u v[18];
#pragma unroll
    for (int k = 0; k < 2; ++k) v[k] = ld16(X, v9, t9 + (unsigned)k * 4u * PAIR);
#pragma unroll
    for (int k = 0; k < 16; ++k) v[2 + k] = ld16(X, l16 + (unsigned)(k & 3) * 1024u, to + (unsigned)(k >> 3) * PAIR + (unsigned)(k & 4) * 1024u);
    SB();
    tok_row(p, X, sb1, L, s, r, ln_, v, px, py, px9, py9);
    VM0();
    unsigned o1 = 0u;
    if (ln_ == 0) o1 = __hip_atomic_fetch_add(cnt + s, 1u, __ATOMIC_RELAXED, __HIP_MEMORY_SCOPE_WORKGROUP);
    if (__builtin_amdgcn_readfirstlane(o1) == 1u && ln_ < 2 - L) st4(X, ln_ ? sb1 : sb + 2048u, P_FB * PAIR + 4096u + (unsigned)rw * 128u, opq(1u));
  }
}

__device__ __forceinline__ void rows_task(const MP& p, const rsrc_t X, unsigned long long sm, u16* ys, int grp, int ng) {
  const int tid = threadIdx.x, lane = tid & 63, w = __builtin_amdgcn_readfirstlane(tid >> 6);
  const int m = lane & 15, g = lane >> 4;
  const int xp = 4 * (g & 1) + 2 * (g >> 1) + (m >> 3), c9 = 512 + 8 * xp + (m & 7);
  const unsigned sb = SEGB(sm, grp >> 2);
  v4u yv[5], xa[4];
  unsigned x9[4];
#pragma unroll
  for (int i = 0; i < 5; ++i) yv[i] = ld16(mkr(p.yT + (grp >> 1) * 9216, 18432u), (unsigned)(tid + 256 * i) * 16u, 0u);
#pragma unroll
  for (int rr = 0; rr < 4; ++rr) {
    const long t = grp * 8 + w + 4 * (rr & (2 * ng - 1));
    xa[rr] = *(const v4u*)&p.xin[t * DM + 8 * lane]; x9[rr] = p.xin[t * DM + c9];
  }
#pragma unroll
  for (int i = 0; i < 5; ++i) ((v4u*)ys)[tid + 256 * i] = yv[i];
  BAR();
  const unsigned o9 = (lane & 7) == 0 ? 0u : 0xF0000000u;
#pragma unroll 1
  for (int k = 0; k < ng; ++k) {
    float x[2][9], y[2][9], ln[2][9];
    const int t0 = (grp + k) * 8 + w;
#pragma unroll
    for (int rr = 0; rr < 2; ++rr) {
      const int j = (t0 + 4 * rr) & 15;
      const v2u ya = *(const v2u*)&ys[lane * 128 + (j ^ m) * 4], yb = *(const v2u*)&ys[lane * 128 + 64 + (j ^ m) * 4];
      up8(xa[rr], x[rr]); x[rr][8] = lo2f(x9[rr]); up8((v4u){ya.x, ya.y, yb.x, yb.y}, y[rr]); y[rr][8] = bf2f(ys[(c9 >> 2) * 64 + (j ^ xp) * 4 + (m & 3)]);
      xa[rr] = xa[2 + rr]; x9[rr] = x9[2 + rr];
    }
    lnR<2>(x, ln);
#pragma unroll
    for (int rr = 0; rr < 2; ++rr) {
#pragma unroll
      for (int i = 0; i < 9; ++i) x[rr][i] = __builtin_fmaf(ln[rr][i], y[rr][i], x[rr][i]);
      rb9(x[rr]);
      const int r = (t0 + 4 * rr) & 31;
      st16(X, (unsigned)lane * 16u, sb + (P_X3 + (unsigned)(r >> 3)) * PAIR + (unsigned)(r & 7) * 1024u, pk8(x[rr]));
      st16(X, (unsigned)xp * 16u + o9, sb + (P_H + 4u) * PAIR + 4096u + (unsigned)r * 128u, gat8(x[rr][8]));
    }
    lnR<2>(x, ln);
#pragma unroll
    for (int rr = 0; rr < 2; ++rr) {
      const int r = (t0 + 4 * rr) & 31;
      st16(X, hoff(r, lane), sb, pk8(ln[rr]));
      st16(X, hoff(r, 64 + xp) + o9, sb, gat8(ln[rr][8]));
    }
  }
  VM0();
  BAR();
  if (tid < ng) st4(X, (unsigned)tid * 128u, sb + P_FB * PAIR + 4096u + (unsigned)(grp & 3) * 128u, opq(1u));
}

__device__ __forceinline__ void arrive(const MP& p, int k, int grp) {
  VM0();
  BAR();
  if (threadIdx.x < 64) {
    unsigned old = 0u;
    if (threadIdx.x == 0) old = __hip_atomic_fetch_add(p.xb + MF_OFF / 4u + (k * 16 + grp) * 32, 1u, __ATOMIC_RELAXED, __HIP_MEMORY_SCOPE_AGENT);
    if (__builtin_amdgcn_readfirstlane(old) == 15u && threadIdx.x < 8) st4(mkr(p.xb, XB_BYTES), threadIdx.x * 128u, RDY_OFF(k) + (unsigned)grp * 1024u, opq(1u));
  }
}
__device__ __forceinline__ void wait_ready(const MP& p, int k) {
  if (threadIdx.x < 64) wait_all<PSL>(mkr(p.xb, XB_BYTES), (threadIdx.x % 9u) * 1024u, RDY_OFF(k) + (blockIdx.x & 7u) * 128u);
  BAR();
}
__device__ __forceinline__ void start_work(char* smem, int wk) {
  const MP p = getp();
  const int w = __builtin_amdgcn_readfirstlane(threadIdx.x >> 6);
#define NARM (ARMK - 3u)
  conv_task(p.kvT, p.ssm_k, p.ssm_D, p.yT, (u16*)(smem + w * 32768), 4 * wk + w, [&]() {
    const unsigned sv = opq(SENT);
    constexpr unsigned S16 = SLOT / 16u;
#pragma unroll
    for (unsigned k = 0; k < (NARM * S16 + 36863u) / 36864u; ++k) {
      const unsigned i = k * 36864u + wk * 256 + threadIdx.x;
      st16(mkr(p.xb, XB_BYTES), i < NARM * S16 ? SLOT_AT(3u + i / S16) + (i % S16) * 16u : 0xF0000000u, 0u, (v4u){sv, sv, sv, sv});
    }
  });
  arrive(p, 0, wk >> 4);
}
__device__ __forceinline__ void token_start(char* smem) {
  TOK_COMMON();
  (void)w;
  const int k = 4 * i + xq;
  if (L || (i >= 12 && i < 16)) start_work(smem, L ? 32 + k : 80 + k);
  if (i >= (L ? 12 : 20)) {
    const unsigned long long sm = die_mask(X, SG0, 0, lane);
    int g = L ? 111 - k : k - 96;
    while (g < 120) {
      if (g >= 0 && g < 64) {
        wait_ready(p, 0);
      }
      rows_task(p, X, sm, (u16*)smem, g < 0 ? g + 16 : 16 + 2 * g, g < 0 ? 1 : 2);
      g += g < 0 ? 16 : (L ? 48 : 120);
    }
  }
}
__device__ __forceinline__ void token_wg(char* smem) {
  if ((unsigned)(size_t)(__attribute__((address_space(3))) char*)smem != 0u) return;
  token_start(smem);
  const int i = ((int)blockIdx.x - NCHW) >> 3;
  if (i < 12) qkv_role();
  else if (i < 20) ap_role();
  else row_role(smem);
}

__global__ __launch_bounds__(256, 1) void k_main(MP) {
  extern __shared__ __attribute__((aligned(16))) char smem[];
  asm volatile("LKBEG:");
  if ((int)blockIdx.x < NCHW) chain_wg(smem);
  else token_wg(smem);
}

extern "C" void kernel_launch(void* const* d_in, const int* in_sizes, int n_in, void* d_out, int out_size, void* d_ws,
                              size_t ws_size, hipStream_t stream) {
  const u16* A_q = (const u16*)d_in[0];
  const u16* A_k = (const u16*)d_in[1];
  const u16* A_v = (const u16*)d_in[2];
  const u16* A_shift = (const u16*)d_in[3];
  const u16* A_ssmk = (const u16*)d_in[4];
  const u16* A_ssmD = (const u16*)d_in[5];
  const u16* B_mem0 = (const u16*)d_in[7];
  const u16* B_Wq = (const u16*)d_in[8];
  const u16* B_Wk = (const u16*)d_in[9];
  const u16* B_Wv = (const u16*)d_in[10];
  const u16* B_Wo = (const u16*)d_in[11];

  char* ws = (char*)d_ws;
  size_t off = (size_t)(-(uintptr_t)ws) & (XBLK - 1u);
  auto alloc = [&](size_t bytes) { char* q = ws + off; off += (bytes + 4095) & ~(size_t)4095; return q; };
  unsigned* xb = (unsigned*)alloc((size_t)XB_BYTES);
  u16* kvT = (u16*)alloc((size_t)DM * S_LEN * 2);
  u16* yT = (u16*)alloc((size_t)DM * S_LEN * 2);

  PreP pp;
  pp.kk = A_k; pp.vv = A_v; pp.shift_k = A_shift; pp.ssm_k = A_ssmk; pp.ssm_D = A_ssmD; pp.yT = yT; pp.kvT = kvT;
  pp.Wq = B_Wq; pp.Wk = B_Wk; pp.Wv = B_Wv; pp.Wo = B_Wo; pp.xb = xb; pp.mem0 = B_mem0;
  k_pre<<<240 + PRE_FILL + 256 + 288, 256, 0, stream>>>(pp);
  MP mp;
  mp.out = (u16*)d_out; mp.xb = xb;
  mp.xin = A_q; mp.yT = yT; mp.kvT = kvT; mp.ssm_k = A_ssmk; mp.ssm_D = A_ssmD;
  mp.Wq = B_Wq; mp.Wk = B_Wk; mp.Wv = B_Wv; mp.Wo = B_Wo;
  const int smem_bytes = 160 * 1024;
  (void)hipFuncSetAttribute((const void*)k_main, hipFuncAttributeMaxDynamicSharedMemorySize, smem_bytes);
  k_main<<<NCHW + NTOKW, 256, smem_bytes, stream>>>(mp);
}
```

```cpp
#include <hip/hip_runtime.h>
#include <stdint.h>

typedef unsigned short u16;
using bf16x8 = __attribute__((ext_vector_type(8))) short;
using f32x4 = __attribute__((ext_vector_type(4))) float;
using f32x2 = __attribute__((ext_vector_type(2))) float;
using bfx8 = __attribute__((ext_vector_type(8))) __bf16;
using bfx2 = __attribute__((ext_vector_type(2))) __bf16;
typedef unsigned v4u __attribute__((ext_vector_type(4)));
typedef unsigned v2u __attribute__((ext_vector_type(2)));
typedef short v4s __attribute__((ext_vector_type(4)));

#define S_LEN 2048
#define DM 576
#define NH 32
#define DH 128
#define HD (NH * DH)
#define SEG 32
#define NSEG 64

__device__ __forceinline__ float bf2f(u16 x) { return __uint_as_float(((unsigned)x) << 16); }
__device__ __forceinline__ unsigned pk2(float a, float b) { f32x2 v = {a, b}; bfx2 r = __builtin_convertvector(v, bfx2); return __builtin_bit_cast(unsigned, r); }
__device__ __forceinline__ float lo2f(unsigned u) { return __uint_as_float(u << 16); }
__device__ __forceinline__ float hi2f(unsigned u) { return __uint_as_float(u & 0xFFFF0000u); }

#define NFC 128
#define XBLK 2097152u
#define PAIR 16384u
#define SLOT 688128u
#define AGO 589824u
#define AGT 655360u
#define SG0 22
#define TP0 86
#define NXB 95
#define XB_BYTES (NXB * XBLK)
#define SLOT_AT(i) ((unsigned)((i) / 3) * XBLK + (unsigned)((i) % 3) * SLOT)
#define HT_OFF SLOT_AT(65)
#define CTAB_OFF (HT_OFF + 16384u)
#define MF_OFF (HT_OFF + 20480u)
#define TMO_OFF (MF_OFF + 15u * 128u)
#define RDY_OFF(k) (MF_OFF + 4096u + (k) * 10240u)
#define MF_BYTES 16384u
#define TRC_OFF SLOT_AT(64)
#define CAL_OFF (XBLK - 16384u)
#define P_KV 96u
#define P_H 112u
#define P_X3 117u
#define P_FA 121u
#define P_FB 122u
#define ARMK 8
#define NCAL 8
__device__ __forceinline__ unsigned kv2(const unsigned (&u)[4], unsigned v, v2u w0, v2u w1) {
  const float a = __builtin_fmaf(lo2f(u[3]), lo2f(w0.x), __builtin_fmaf(lo2f(u[2]), hi2f(w0.x), __builtin_fmaf(lo2f(u[1]), lo2f(w0.y), lo2f(u[0]) * hi2f(w0.y))));
  const float b = __builtin_fmaf(hi2f(u[3]), lo2f(w1.x), __builtin_fmaf(hi2f(u[2]), hi2f(w1.x), __builtin_fmaf(hi2f(u[1]), lo2f(w1.y), hi2f(u[0]) * hi2f(w1.y))));
  return pk2(a * lo2f(v), b * hi2f(v));
}
struct PreP {
  const u16* kk; const u16* vv; const u16* shift_k; const u16* ssm_k; const u16* ssm_D; u16* yT; u16* kvT;
  const u16* Wq; const u16* Wk; const u16* Wv; const u16* Wo; unsigned* xb; const u16* mem0;
};
#define PRE_FILL 112
#define PFN 5
__global__ __launch_bounds__(256, 4) void k_pre(PreP p) {
  __shared__ __attribute__((aligned(16))) float sh[64 * 36];
  const int tid = threadIdx.x, b0 = blockIdx.x, b = b0 < 240 ? b0 : (b0 < 528 ? b0 + PRE_FILL + 256 : b0 - 288);
  const __amdgpu_buffer_rsrc_t rx = __builtin_amdgcn_make_buffer_rsrc((void*)p.xb, 0, XB_BYTES, 0x00020000);
  const v4u ones = {0xFFFFFFFFu, 0xFFFFFFFFu, 0xFFFFFFFFu, 0xFFFFFFFFu}, zero = {0, 0, 0, 0};
  if (b < 96) {
    if (tid >= 64) return;
    const unsigned cb = (unsigned)b * XBLK + CAL_OFF;
    __builtin_amdgcn_raw_buffer_store_b128(zero, rx, (unsigned)(tid & 1) * 8192u + (unsigned)(tid >> 1) * 256u, cb, 16);
    asm volatile("s_waitcnt vmcnt(0)" ::: "memory");
    unsigned mn[2] = {~0u, ~0u};
    for (int i = 0; i < NCAL; ++i) {
      const unsigned long long c0 = __builtin_amdgcn_s_memtime();
      asm volatile("s_waitcnt lgkmcnt(0)" ::: "memory");
      const unsigned v = __builtin_amdgcn_raw_buffer_load_b32(rx, 0, cb + (unsigned)(i & 1) * 8192u + (unsigned)(i >> 1) * 256u, 16);
      asm volatile("s_waitcnt vmcnt(0)" :: "v"(v) : "memory");
      const unsigned d = (unsigned)(__builtin_amdgcn_s_memtime() - c0);
      if (i & 1) mn[1] = d < mn[1] ? d : mn[1]; else mn[0] = d < mn[0] ? d : mn[0];
    }
    unsigned xcc; asm volatile("s_getreg_b32 %0, hwreg(HW_REG_XCC_ID)" : "=s"(xcc));
    const unsigned res = mn[1] < mn[0] ? 1u : 0u;
    if (tid == 0) __builtin_amdgcn_raw_buffer_store_b32(res ^ ((xcc >> 2) & 1u), rx, 0, CTAB_OFF + (unsigned)b * 4u, 16);
  } else if (b < 240) {
    const __amdgpu_buffer_rsrc_t rk = __builtin_amdgcn_make_buffer_rsrc((void*)p.kk, 0, S_LEN * DM * 2, 0x00020000);
    float (*kx)[132] = (float (*)[132])sh;
    float (*cf)[140] = (float (*)[140])(sh + 4 * 132);
    const int d0 = (b - 96) * 4, q = tid & 3, dd = (tid >> 2) & 3, t0 = (tid >> 4) * 8;
    const float Dv = bf2f(p.ssm_D[d0 + dd]);
    {
      const int s = tid >> 1, d = d0 + (tid & 1) * 2;
      unsigned u[4];
#pragma unroll
      for (int a = 0; a < 4; ++a) u[a] = __builtin_amdgcn_raw_buffer_load_b32(rk, (unsigned)((s - 3 + a) * DM + d) * 2u, 0, 0);
      const unsigned vr = *(const unsigned*)&p.vv[s * DM + d], c0 = *(const unsigned*)&p.ssm_k[(long)(d0 + (tid >> 6)) * S_LEN + (tid & 63) * 2];
      const v4u w = *(const v4u*)&p.shift_k[d * 4];
      const unsigned r = kv2(u, vr, (v2u){w.x, w.y}, (v2u){w.z, w.w});
      kx[(tid & 1) * 2][s] = lo2f(r); kx[(tid & 1) * 2 + 1][s] = hi2f(r);
      *(f32x2*)&cf[tid >> 6][8 + (tid & 63) * 2] = (f32x2){lo2f(c0), hi2f(c0)};
      if (tid < 32) cf[tid >> 3][tid & 7] = 0.f;
    }
    __syncthreads();
    float y[8];
#pragma unroll
    for (int i = 0; i < 8; ++i) y[i] = 0.f;
#pragma unroll 1
    for (int j = 8 * q; j <= t0; j += 32) {
      f32x4 kk[2], cw[4];
#pragma unroll
      for (int x = 0; x < 2; ++x) kk[x] = *(const f32x4*)&kx[dd][j + 4 * x];
#pragma unroll
      for (int x = 0; x < 4; ++x) cw[x] = *(const f32x4*)&cf[dd][t0 - j + 4 * x];
#pragma unroll
      for (int jj = 0; jj < 8; ++jj)
#pragma unroll
        for (int i = 0; i < 8; ++i) y[i] = __builtin_fmaf(cw[(8 + i - jj) >> 2][(8 + i - jj) & 3], kk[jj >> 2][jj & 3], y[i]);
    }
#pragma unroll
    for (int i = 0; i < 8; ++i) {
      y[i] += __uint_as_float((unsigned)__builtin_amdgcn_mov_dpp((int)__float_as_uint(y[i]), 0xB1, 0xf, 0xf, true));
      y[i] += __uint_as_float((unsigned)__builtin_amdgcn_mov_dpp((int)__float_as_uint(y[i]), 0x4E, 0xf, 0xf, true));
    }
    const float ya = q == 0 ? y[0] : (q == 1 ? y[2] : (q == 2 ? y[4] : y[6])), yb = q == 0 ? y[1] : (q == 1 ? y[3] : (q == 2 ? y[5] : y[7]));
    const int t = t0 + 2 * q, f = (d0 >> 3) & 15;
    const unsigned r0 = pk2(ya, yb), r1 = pk2(kx[dd][t] * Dv, kx[dd][t + 1] * Dv), o = pk2(lo2f(r0) + lo2f(r1), hi2f(r0) + hi2f(r1));
    u16* dst = p.yT + (t >> 4) * 9216 + (d0 >> 2) * 64 + dd;
    dst[((t & 15) ^ f) << 2] = (u16)(o & 0xFFFFu); dst[(((t + 1) & 15) ^ f) << 2] = (u16)(o >> 16);
  } else if (b < 240 + PRE_FILL) {
    const unsigned i0 = (unsigned)(b - 240) * 256u + tid;
    if (i0 < (SLOT - AGO) / 16u) {
      const unsigned o = AGO + i0 * 16u, s = o < AGT ? (i0 >> 11) * 4u + ((o >> 10) & 3u) : 8u, r = o & 1023u;
      const unsigned gm = s < 8u ? r >> 8 : (r >> 6) & 3u, m3 = (r >> 4) & 3u, ga = s < 8u ? (r >> 6) & 3u : r >> 8, row = 4u * (gm & 1u) + 2u * (gm >> 1) + (m3 & 1u), half = m3 >> 1;
      const unsigned* src = (const unsigned*)(p.mem0 + row * DM + 64u * s + 16u * ga + 2u * half);
      __builtin_amdgcn_raw_buffer_store_b128((v4u){src[0], src[2], src[4], src[6]}, rx, o, 0, 16);
    }
    for (unsigned i = i0; i < 131072u + MF_BYTES / 16u; i += PRE_FILL * 256)
      __builtin_amdgcn_raw_buffer_store_b128(zero, rx, i < 131072u ? (SG0 + (i >> 11)) * XBLK + P_FA * PAIR + (i & 2047u) * 16u : MF_OFF + (i & 2047u) * 16u, 0, 16);
    for (unsigned i = i0; i < 2u * SLOT / 16u; i += PRE_FILL * 256) __builtin_amdgcn_raw_buffer_store_b128(ones, rx, SLOT + i * 16u, 0, 16);
  } else if (b >= 240 + PRE_FILL + 256) {
    const __amdgpu_buffer_rsrc_t rk = __builtin_amdgcn_make_buffer_rsrc((void*)p.kk, 0, S_LEN * DM * 2, 0x00020000);
    u16 (*tile)[72] = (u16 (*)[72])sh;
    const int bid = b - 240 - PRE_FILL - 256, d0 = (bid % 9) * 64, s0 = (bid / 9) * 64;
    const int sr = (tid >> 3) * 2, d = d0 + (tid & 7) * 8;
    v4u kr[5], vr[2], w[4];
#pragma unroll
    for (int a = 0; a < 5; ++a) kr[a] = __builtin_amdgcn_raw_buffer_load_b128(rk, (unsigned)((s0 + sr - 3 + a) * DM + d) * 2u, 0, 0);
#pragma unroll
    for (int a = 0; a < 2; ++a) vr[a] = *(const v4u*)&p.vv[(long)(s0 + sr + a) * DM + d];
#pragma unroll
    for (int a = 0; a < 4; ++a) w[a] = *(const v4u*)&p.shift_k[(d + 2 * a) * 4];
    __builtin_amdgcn_sched_barrier(0);
#pragma unroll
    for (int it = 0; it < 2; ++it) {
      v4u o;
#pragma unroll
      for (int j = 0; j < 4; ++j) { const unsigned u[4] = {kr[it][j], kr[it + 1][j], kr[it + 2][j], kr[it + 3][j]}; o[j] = kv2(u, vr[it][j], (v2u){w[j].x, w[j].y}, (v2u){w[j].z, w[j].w}); }
      *(v4u*)&tile[sr + it][(tid & 7) * 8] = o;
    }
    __syncthreads();
    const int lane = tid & 63, wv = tid >> 6, g = lane >> 4, r = (lane >> 2) & 3, c = lane & 3;
    v2u o[4];
#pragma unroll
    for (int i = 0; i < 4; ++i) o[i] = __builtin_bit_cast(v2u, __builtin_amdgcn_ds_read_tr16_b64_v4i16((__attribute__((address_space(3))) v4s*)&tile[16 * g + 4 * i + r][16 * wv + 4 * c]));
    u16* dst = p.kvT + (long)(d0 + 16 * wv + (lane & 15)) * S_LEN + s0 + 16 * g;
    *(v4u*)dst = (v4u){o[0].x, o[0].y, o[1].x, o[1].y};
    *(v4u*)(dst + 8) = (v4u){o[2].x, o[2].y, o[3].x, o[3].y};
  } else {
    const int x = b & 7, k = (b - 240 - PRE_FILL) >> 3;
    unsigned long long pc; unsigned l0, l1, l2, l3; int rel;
    asm volatile("s_getpc_b64 %0\nLPRE:\n\ts_mov_b32 %1, LKBEG-LPRE\n\ts_mov_b32 %2, LCHEND-LKBEG\n\ts_mov_b32 %3, LQVEND-LKBEG\n\ts_mov_b32 %4, LQKEND-LKBEG\n\ts_mov_b32 %5, LAPEND-LKBEG"
                 : "=s"(pc), "=s"(rel), "=s"(l0), "=s"(l1), "=s"(l2), "=s"(l3));
    l0 = l0 > l1 ? l0 : l1; l2 = l2 > l3 ? l2 : l3; l0 = l0 > l2 ? l0 : l2;
    const unsigned o = (unsigned)(k * 64 + (tid & 63)) * 64u;
    unsigned a[PFN + 2];
    a[PFN] = *(const __attribute__((address_space(1))) unsigned*)(pc + rel + (o < l0 ? o : 0u));
    a[PFN + 1] = __builtin_amdgcn_raw_buffer_load_b32(rx, k == 31 ? tid * XBLK + CAL_OFF + 8064u : 0xF0000000u, 0, 16);
#pragma unroll
    for (int i = 0; i < PFN; ++i) {
      const int q = (i * 32 + k) * 256 + tid, r2 = q >> 5, gr = r2 >> 5, row = (gr / 3) * 64 + (x >> 2) * 32 + (r2 & 31), off = (q & 31) * 32, ty = gr % 3;
      a[i] = *(const unsigned*)(gr < 27 ? (ty == 0 ? p.Wq : (ty == 1 ? p.Wk : p.Wv)) + (long)row * HD + (x & 3) * 1024 + off
                                        : p.Wo + ((long)(x & 3) * DM + (gr < 36 ? gr - 27 : 0) * 64 + (x >> 2) * 32 + (r2 & 31)) * 1024 + off);
    }
#pragma unroll
    for (int i = 0; i < PFN + 2; ++i) asm volatile("" :: "v"(a[i]));
  }
}

#define CREV_STRIDE 2208
#define LWAIT() asm volatile("s_waitcnt lgkmcnt(0)" ::: "memory")
template <class F>
__device__ __forceinline__ void conv_task(const u16* __restrict__ kvT, const u16* __restrict__ ssm_k, const u16* __restrict__ ssm_D, u16* yT, u16* shb, int d, F fill) {
  const int lane = threadIdx.x & 63;
  u16* crev = shb + 64;
  u16* kvs = crev + 4 * CREV_STRIDE;
  const int X0 = 2048 + 63;
  v4u cv[4], kq[4];
#pragma unroll
  for (int j = 0; j < 4; ++j) { cv[j] = *(const v4u*)&ssm_k[(long)d * S_LEN + lane * 32 + 8 * j]; kq[j] = *(const v4u*)&kvT[(long)d * S_LEN + (j * 64 + lane) * 8]; }
  const float Dv = bf2f(ssm_D[d]);
  const v4u z4 = {0u, 0u, 0u, 0u};
  *(v4u*)&kvs[(lane >> 2) * 40 + (lane & 3) * 8] = z4;
  if (lane < 32) *(v4u*)&crev[(lane >> 3) * CREV_STRIDE + (lane & 7) * 8] = z4;
  if (lane < 36) *(v4u*)&crev[(lane / 9) * CREV_STRIDE + 2112 + (lane % 9) * 8] = z4;
  {
    unsigned r[18];
#pragma unroll
    for (int k = 0; k < 16; ++k) { const unsigned u = cv[3 - (k >> 2)][3 - (k & 3)]; r[k] = __builtin_amdgcn_alignbyte(u, u, 2); }
#pragma unroll
    for (int k = 0; k < 2; ++k) r[16 + k] = (unsigned)__builtin_amdgcn_update_dpp(0, (int)r[k], 0x138, 0xf, 0xf, true);
    LWAIT();
#pragma unroll
    for (int rho = 0; rho < 4; ++rho)
#pragma unroll
      for (int j = 0; j < 4; ++j) {
        v4u o;
#pragma unroll
        for (int k = 0; k < 4; ++k) { const int q = 4 * j + k + (rho >> 1); o[k] = (rho & 1) ? __builtin_amdgcn_alignbyte(r[q + 1], r[q], 2) : r[q]; }
        *(v4u*)&crev[rho * CREV_STRIDE + 2080 - 32 * lane + 8 * j] = o;
      }
    if (lane == 63) {
      *(v2u*)&crev[1 * CREV_STRIDE + 60] = (v2u){0u, r[0] << 16};
      *(v2u*)&crev[2 * CREV_STRIDE + 60] = (v2u){0u, r[0]};
      *(v2u*)&crev[3 * CREV_STRIDE + 60] = (v2u){r[0] << 16, __builtin_amdgcn_alignbyte(r[1], r[0], 2)};
    }
#pragma unroll
    for (int j = 0; j < 4; ++j) *(v4u*)&kvs[(16 + j * 16 + (lane >> 2)) * 40 + (lane & 3) * 8] = kq[j];
  }
  fill();
  LWAIT();
  const int n = lane & 15, g = lane >> 4;
  const int rho = (X0 - n) & 3;
  f32x4 acc[4][2];
#pragma unroll
  for (int q = 0; q < 8; ++q) acc[q >> 1][q & 1] = (f32x4){0.f, 0.f, 0.f, 0.f};
  {
    typedef __attribute__((address_space(3))) const v2u* l2p;
    typedef __attribute__((address_space(3))) const bf16x8* l4p;
    unsigned a0 = (unsigned)(size_t)(__attribute__((address_space(3))) u16*)&crev[rho * CREV_STRIDE + (X0 - (n - 8 * g) - rho)] - 3232u, a1 = a0 + 8u;
    unsigned b0 = (unsigned)(size_t)(__attribute__((address_space(3))) u16*)&kvs[(14 + n) * 40 + 8 * g];
    asm volatile("" : "+v"(a1));
#define AFR(m, f) ({ const unsigned o_ = 3232u - 64u * (m) - 1024u * ((f) >> 1) - 32u * ((f) & 1); const v2u lo_ = *(l2p)(size_t)(a0 + o_), hi_ = *(l2p)(size_t)(a1 + o_); \
                     __builtin_bit_cast(bf16x8, (v4u){lo_.x, lo_.y, hi_.x, hi_.y}); })
#define BFR(m, j) (*(l4p)(size_t)(b0 + 80u * (2 - (m)) + 1280u * (j)))
    bf16x8 af[2][8], bf[2][4];
#pragma unroll
    for (int f = 0; f < 8; ++f) { af[0][f] = AFR(0, f); af[1][f] = af[0][f]; __builtin_amdgcn_sched_barrier(0); }
#pragma unroll
    for (int j = 0; j < 4; ++j) { bf[0][j] = BFR(0, j); bf[1][j] = bf[0][j]; }
#pragma unroll 1
    for (int mo = 0; mo < 8; ++mo) {
#pragma unroll
      for (int m = 0; m < 2; ++m) {
        int k = 0;
#pragma unroll
        for (int a = 0; a < 4; ++a)
#pragma unroll
          for (int ct = a; ct < 4; ++ct)
#pragma unroll
            for (int ti = 0; ti < 2; ++ti) {
              acc[ct][ti] = __builtin_amdgcn_mfma_f32_16x16x32_bf16(__builtin_bit_cast(bfx8, af[m][2 * a + ti]), __builtin_bit_cast(bfx8, bf[m][ct - a]), acc[ct][ti], 0, 0, 0);
              if (k < 2) af[m ^ 1][k] = AFR(m + 1, k);
              else if (k < 6) bf[m ^ 1][k - 2] = BFR(m + 1, k - 2);
              else if (k < 12) af[m ^ 1][k - 4] = AFR(m + 1, k - 4);
              __builtin_amdgcn_sched_barrier(0);
              ++k;
            }
      }
      a0 -= 128u; a1 -= 128u; b0 -= 160u;
    }
  }
  u16* yo = crev;
  LWAIT();
#pragma unroll
  for (int q = 0; q < 8; ++q) {
    const int o = (16 * (q >> 1) + n) * 40 + 16 * (q & 1) + 4 * g;
    const v2u kx = *(const v2u*)&kvs[16 * 40 + o];
    const f32x4 y = acc[q >> 1][q & 1];
    const unsigned y0 = pk2(y[0], y[1]), y1 = pk2(y[2], y[3]), r0 = pk2(lo2f(kx.x) * Dv, hi2f(kx.x) * Dv), r1 = pk2(lo2f(kx.y) * Dv, hi2f(kx.y) * Dv);
    *(v2u*)&yo[o] = (v2u){pk2(lo2f(y0) + lo2f(r0), hi2f(y0) + hi2f(r0)), pk2(lo2f(y1) + lo2f(r1), hi2f(y1) + hi2f(r1))};
  }
  LWAIT();
  __builtin_amdgcn_s_barrier();
  asm volatile("" ::: "memory");
  {
    const int tid = threadIdx.x, f = (d >> 3) & 15;
    const u16* y0 = shb - (tid >> 6) * 16384 + 64;
#pragma unroll
    for (int k = 0; k < 4; ++k) {
      const int tile = 32 * k + (tid >> 3), t = 16 * tile + 2 * ((tid & 7) ^ (f >> 1));
      unsigned u[4];
#pragma unroll
      for (int c = 0; c < 4; ++c) u[c] = *(const unsigned*)&y0[c * 16384 + (t >> 5) * 40 + (t & 31)];
      const v2u e = {__builtin_amdgcn_perm(u[1], u[0], 0x05040100u), __builtin_amdgcn_perm(u[3], u[2], 0x05040100u)}, o = {__builtin_amdgcn_perm(u[1], u[0], 0x07060302u), __builtin_amdgcn_perm(u[3], u[2], 0x07060302u)};
      if (tile >= NFC / 16) __builtin_amdgcn_raw_buffer_store_b128((f & 1) ? (v4u){o.x, o.y, e.x, e.y} : (v4u){e.x, e.y, o.x, o.y}, __builtin_amdgcn_make_buffer_rsrc((void*)yT, 0, DM * S_LEN * 2, 0x00020000),
                                                                   tile * 18432 + (tid & 7) * 16, (d >> 2) * 128, 16);
    }
  }
}

#define NCHW 64
#define NTOKW 192
#define PSL 4
#define SENT 0xFFFFFFFFu
#define SPIN_CAP 300000u
struct MP {
  const u16* Wq; const u16* Wk; const u16* Wv; const u16* Wo; u16* out; unsigned* xb;
  const u16* xin; u16* yT; u16* kvT; const u16* ssm_k; const u16* ssm_D;
};

__device__ __forceinline__ MP getp() {
  MP r;
#if defined(__HIP_DEVICE_COMPILE__)
  const __attribute__((address_space(4))) unsigned long long* k = (const __attribute__((address_space(4))) unsigned long long*)__builtin_amdgcn_kernarg_segment_ptr();
  asm volatile("" : "+s"(k));
#define GP(T, i) ((T*)(__attribute__((address_space(1))) T*)k[i])
  r.Wq = GP(const u16, 0);
  r.Wk = GP(const u16, 1);
  r.Wv = GP(const u16, 2);
  r.Wo = GP(const u16, 3);
  r.out = GP(u16, 4);
  r.xb = GP(unsigned, 5);
  r.xin = GP(const u16, 6);
  r.yT = GP(u16, 7);
  r.kvT = GP(u16, 8);
  r.ssm_k = GP(const u16, 9);
  r.ssm_D = GP(const u16, 10);
#endif
  return r;
}
typedef __amdgpu_buffer_rsrc_t rsrc_t;
__device__ __forceinline__ rsrc_t mkr(const void* p, unsigned bytes) { return __builtin_amdgcn_make_buffer_rsrc((void*)p, 0, bytes, 0x00020000); }
__device__ __forceinline__ void st16(rsrc_t r, unsigned vo, unsigned so, v4u v) { __builtin_amdgcn_raw_buffer_store_b128(v, r, vo, so, 16); }
__device__ __forceinline__ void st4(rsrc_t r, unsigned vo, unsigned so, unsigned v) { __builtin_amdgcn_raw_buffer_store_b32(v, r, vo, so, 16); }
__device__ __forceinline__ v4u ld16(rsrc_t r, unsigned vo, unsigned so) { return __builtin_amdgcn_raw_buffer_load_b128(r, vo, so, 16); }
__device__ __forceinline__ v2u ld8(rsrc_t r, unsigned vo, unsigned so) { return __builtin_amdgcn_raw_buffer_load_b64(r, vo, so, 16); }
__device__ __forceinline__ unsigned ld4(rsrc_t r, unsigned vo, unsigned so) { return __builtin_amdgcn_raw_buffer_load_b32(r, vo, so, 16); }
__device__ __forceinline__ unsigned opq(unsigned x) { asm volatile("" : "+v"(x)); return x; }

__device__ __forceinline__ unsigned long long die_mask(rsrc_t X, unsigned first, int L, int lane) { return __ballot((ld4(X, (unsigned)lane * 4u, CTAB_OFF + first * 4u) & 1u) != (unsigned)L); }
#define SEGB(m, s) ((unsigned)(SG0 + (s)) * XBLK + (unsigned)(((m) >> (s)) & 1ull) * 8192u)
__device__ __forceinline__ bool spin_fail(unsigned& spins, rsrc_t F) {
  ++spins;
  if (spins > SPIN_CAP) { unsigned one = 0x40000000u | (blockIdx.x << 8) | threadIdx.x; asm volatile("" : "+v"(one)); if (__builtin_amdgcn_readfirstlane(ld4(F, 0, TMO_OFF)) == 0u) st4(F, 0, TMO_OFF, one); return true; }
  if ((spins & 1023u) == 0u) return __builtin_amdgcn_readfirstlane(ld4(F, 0, TMO_OFF)) != 0;
  return false;
}
__device__ __forceinline__ unsigned rtc() { return (unsigned)__builtin_amdgcn_s_memrealtime(); }
#define BAR() do { asm volatile("s_waitcnt lgkmcnt(0)" ::: "memory"); __builtin_amdgcn_s_barrier(); asm volatile("" ::: "memory"); } while (0)
#define SB() __builtin_amdgcn_sched_barrier(0)
#define MF "v_mfma_f32_16x16x32_bf16 "
#define VM0() __builtin_amdgcn_s_waitcnt(0x0F70)
#define DL1 12
#define SCH_B0 52
#define DLL1 6
#define DLL2 4
#define DLF 24
#define SCH_PEN 0
#define DLX 4
#define LATE 2
#define TKL 28
#define UP 1
#define DN 100
#define DL3 16
#define FIN -3
#define CH_FIN CH_S(FIN)
#define DLT 16
#define DLY 20
#define CH_DLT CH_S(DLT)
#define CH_DLY CH_S(DLY)
#define ES 0
#define ED 4
#define EB 8
#define CH_ES CH_S(ES)
#define CH_EX "%[ex]"
#define DRYL 1
#define CH_DRYL CH_S(DRYL)
#define SMAP 0x1023u
#define RSL 2
#define CH_S2(x) #x
#define CH_S(x) CH_S2(x)
#define CH_TOKCHK "s_cbranch_vccz LG3_%=\n\t"
#define CH_SG0 CH_S(SG0)
#define CH_ARMD "%[armd]"
#define CH_ARMS "%[arms]"
#define CH_SLOT "688128"
#define CH_SKIP "32768"
#define CH_DLL1 CH_S(DLL1)
#define CH_DLL2 CH_S(DLL2)
#define CH_DLF CH_S(DLF)
#define CH_B0 CH_S(SCH_B0)
#define CH_PEN CH_S(SCH_PEN)
#define CH_RSL CH_S(RSL)
#define CH_DL3 CH_S(DL3)
#define CH_UP CH_S(UP)
#define CH_DN CH_S(DN)
#define CH_DLX CH_S(DLX)
#define CH_LATE CH_S(LATE)
#define CH_DL1L "%[dl1l]"
#define CH_TKL CH_S(TKL)
#define CH_SCHW "656384"
#define CH_CAP "%[cap]"
#define CH_CTAB "%[ctab]"
#define FDL 6
#define CH_FDL CH_S(FDL)
#define CH_ASM \
  "s_mov_b32 s84, " CH_CAP "\n\t" \
  "s_mov_b32 s83, " CH_CAP "\n\t" \
  "s_mov_b32 s68, 0\n\t" \
  "s_mov_b32 s74, 0\n\t" \
  "s_mov_b32 s76, 0\n\t" \
  "s_mov_b32 s77, 0\n\t" \
  "s_mov_b32 s78, 0\n\t" \
  "s_mov_b32 s79, 0\n\t" \
  "s_mov_b32 s56, 0xcccccccc\n\t" \
  "s_mov_b32 s57, 0xcccccccc\n\t" \
  "s_mov_b32 s58, 0x33333333\n\t" \
  "s_mov_b32 s59, 0x33333333\n\t" \
  "s_mov_b32 s24, 0x01010101\n\t" \
  "s_mov_b32 s25, 0\n\t" \
  "s_mov_b32 s88, 0x3f803f80\n\t" \
  "s_mov_b32 s89, 0x3f803f80\n\t" \
  "s_mov_b32 s66, s65\n\t" \
  "s_mov_b32 s47, s65\n\t" \
  "s_mov_b32 s72, 0\n\t" \
  "s_mov_b32 s73, 0\n\t" \
  "s_mov_b32 s67, 0\n\t" \
  "s_mul_i32 s60, s40, 36864\n\t" \
  "s_add_u32 s60, s60, 16384\n\t" \
  "v_add_u32 v70, s60, v69\n\t" \
  "v_bfe_i32 v68, v69, 5, 1\n\t" \
  "v_and_b32 v68, 0xaaaaaaaa, v68\n\t" \
  "v_or_b32 v68, 0x44444444, v68\n\t" \
  "v_and_b32 v73, 32, v69\n\t" \
  "v_lshl_add_u32 v73, v73, 5, v69\n\t" \
  "LLOAD_%=:\n\t" \
  "v_lshrrev_b32 v74, 2, v69\n\t" \
  "s_mov_b32 s60, " CH_CTAB "\n\t" \
  "buffer_load_dword v72, v74, s[28:31], s60 offen offset:88 sc1\n\t" \
  "buffer_load_dword v74, v74, s[28:31], s60 offen sc1\n\t" \
  "v_lshrrev_b32 v71, 8, v69\n\t" \
  "v_lshlrev_b32 v1, 6, v71\n\t" \
  "v_and_b32 v2, 0xf0, v69\n\t" \
  "v_xor_b32 v1, v1, v2\n\t" \
  "v_lshl_add_u32 v71, v71, 13, v1\n\t" \
  "s_lshl_b32 s60, s40, 17\n\t" \
  "v_add_u32 v71, s60, v71\n\t" \
  "v_lshrrev_b32 v1, 7, v69\n\t" \
  "v_and_b32 v2, 0x70, v69\n\t" \
  "s_movk_i32 s60, 1152\n\t" \
  "v_mad_u32_u24 v75, v1, s60, v2\n\t" \
  "s_lshl_b32 s54, s40, 12\n\t" \
  "s_lshl_b32 s55, s40, 7\n\t" \
  "s_mul_i32 s75, s40, 36864\n\t" \
  "s_add_u32 s75, s75, 1024\n\t" \
  "s_mov_b32 s62, 0x05040100\n\t" \
  "s_mov_b32 s63, 0x07060302\n\t" \
  "s_barrier\n\t" \
  "s_xor_b32 s61, s40, 0\n\t" \
  "s_mul_i32 s61, s61, 36864\n\t" \
  "s_add_u32 s61, s61, s55\n\t" \
  "s_add_u32 m0, s54, 0\n\t" \
  "s_add_u32 s60, s61, 0\n\t" \
  "buffer_load_dwordx4 v75, %[Wo], s60 offen lds\n\t" \
  "s_add_u32 m0, s54, 1024\n\t" \
  "s_add_u32 s60, s61, 9216\n\t" \
  "buffer_load_dwordx4 v75, %[Wo], s60 offen lds\n\t" \
  "s_add_u32 m0, s54, 2048\n\t" \
  "s_add_u32 s60, s61, 18432\n\t" \
  "buffer_load_dwordx4 v75, %[Wo], s60 offen lds\n\t" \
  "s_add_u32 m0, s54, 3072\n\t" \
  "s_add_u32 s60, s61, 27648\n\t" \
  "buffer_load_dwordx4 v75, %[Wo], s60 offen lds\n\t" \
  "s_add_u32 m0, s54, 16384\n\t" \
  "s_mov_b32 s60, 0\n\t" \
  "buffer_load_dwordx4 v71, %[Wq], s60 offen lds\n\t" \
  "s_add_u32 m0, s54, 17408\n\t" \
  "s_mov_b32 s60, 32768\n\t" \
  "buffer_load_dwordx4 v71, %[Wq], s60 offen lds\n\t" \
  "s_add_u32 m0, s54, 18432\n\t" \
  "s_mov_b32 s60, 65536\n\t" \
  "buffer_load_dwordx4 v71, %[Wq], s60 offen lds\n\t" \
  "s_add_u32 m0, s54, 19456\n\t" \
  "s_mov_b32 s60, 98304\n\t" \
  "buffer_load_dwordx4 v71, %[Wq], s60 offen lds\n\t" \
  "s_add_u32 m0, s54, 53248\n\t" \
  "s_mov_b32 s60, 0\n\t" \
  "buffer_load_dwordx4 v71, %[Wk], s60 offen lds\n\t" \
  "s_add_u32 m0, s54, 54272\n\t" \
  "s_mov_b32 s60, 32768\n\t" \
  "buffer_load_dwordx4 v71, %[Wk], s60 offen lds\n\t" \
  "s_add_u32 m0, s54, 55296\n\t" \
  "s_mov_b32 s60, 65536\n\t" \
  "buffer_load_dwordx4 v71, %[Wk], s60 offen lds\n\t" \
  "s_add_u32 m0, s54, 56320\n\t" \
  "s_mov_b32 s60, 98304\n\t" \
  "buffer_load_dwordx4 v71, %[Wk], s60 offen lds\n\t" \
  "s_xor_b32 s61, s40, 0\n\t" \
  "s_mul_i32 s61, s61, 36864\n\t" \
  "s_add_u32 s61, s61, s55\n\t" \
  "s_add_u32 m0, s54, 90112\n\t" \
  "s_add_u32 s60, s61, 512\n\t" \
  "buffer_load_dwordx4 v75, %[Wo], s60 offen lds\n\t" \
  "s_add_u32 m0, s54, 91136\n\t" \
  "s_add_u32 s60, s61, 9728\n\t" \
  "buffer_load_dwordx4 v75, %[Wo], s60 offen lds\n\t" \
  "s_add_u32 m0, s54, 92160\n\t" \
  "s_add_u32 s60, s61, 18944\n\t" \
  "buffer_load_dwordx4 v75, %[Wo], s60 offen lds\n\t" \
  "s_add_u32 m0, s54, 93184\n\t" \
  "s_add_u32 s60, s61, 28160\n\t" \
  "buffer_load_dwordx4 v75, %[Wo], s60 offen lds\n\t" \
  "s_add_u32 m0, s54, 126976\n\t" \
  "s_mov_b32 s60, 0\n\t" \
  "buffer_load_dwordx4 v71, %[Wv], s60 offen lds\n\t" \
  "s_add_u32 m0, s54, 128000\n\t" \
  "s_mov_b32 s60, 32768\n\t" \
  "buffer_load_dwordx4 v71, %[Wv], s60 offen lds\n\t" \
  "s_add_u32 m0, s54, 129024\n\t" \
  "s_mov_b32 s60, 65536\n\t" \
  "buffer_load_dwordx4 v71, %[Wv], s60 offen lds\n\t" \
  "s_add_u32 m0, s54, 130048\n\t" \
  "s_mov_b32 s60, 98304\n\t" \
  "buffer_load_dwordx4 v71, %[Wv], s60 offen lds\n\t" \
  "v_and_b32 v1, 0x300, v69\n\t" \
  "v_lshlrev_b32 v65, 3, v1\n\t" \
  "v_and_b32 v1, 0xc0, v69\n\t" \
  "v_lshl_add_u32 v65, v1, 2, v65\n\t" \
  "s_lshl_b32 s60, s40, 6\n\t" \
  "v_xor_b32 v1, s60, v1\n\t" \
  "v_and_b32 v2, 48, v69\n\t" \
  "v_add3_u32 v65, v65, v1, v2\n\t" \
  "v_lshrrev_b32 v2, 1, v2\n\t" \
  "v_sub_u32 v66, v65, v2\n\t" \
  "v_bfe_u32 v0, v69, 8, 1\n\t" \
  "v_bfe_u32 v1, v69, 9, 1\n\t" \
  "v_lshlrev_b32 v0, 2, v0\n\t" \
  "v_add_u32 v0, v0, v1\n\t" \
  "v_lshlrev_b32 v0, 9, v0\n\t" \
  "v_lshrrev_b32 v1, 1, v69\n\t" \
  "v_and_b32 v1, 0x60, v1\n\t" \
  "v_and_b32 v2, 16, v69\n\t" \
  "v_bfe_u32 v3, v69, 5, 1\n\t" \
  "v_add3_u32 v64, v0, v1, v2\n\t" \
  "v_lshl_add_u32 v64, v3, 2, v64\n\t" \
  "v_add_u32 v64, s54, v64\n\t" \
  "v_lshl_add_u32 v67, v3, 3, v0\n\t" \
  "v_bfe_u32 v1, v69, 7, 1\n\t" \
  "v_lshl_add_u32 v67, v1, 2, v67\n\t" \
  "v_bfe_u32 v1, v69, 6, 1\n\t" \
  "v_lshl_add_u32 v67, v1, 4, v67\n\t" \
  "v_bfe_u32 v1, v69, 4, 1\n\t" \
  "v_lshl_add_u32 v67, v1, 1, v67\n\t" \
  "s_lshl_b32 s60, s40, 5\n\t" \
  "v_add_u32 v67, s60, v67\n\t" \
  "v_add_u32 v78, 0, v64\n\t" \
  "v_add_u32 v79, 1024, v64\n\t" \
  "s_waitcnt vmcnt(16) lgkmcnt(0)\n\t" \
  "s_barrier\n\t" \
  "ds_read2_b32 v[80:81], v78 offset0:0 offset1:2\n\t" \
  "ds_read2_b32 v[82:83], v78 offset0:32 offset1:34\n\t" \
  "ds_read2_b32 v[84:85], v78 offset0:64 offset1:66\n\t" \
  "ds_read2_b32 v[86:87], v78 offset0:96 offset1:98\n\t" \
  "ds_read2_b32 v[88:89], v79 offset0:0 offset1:2\n\t" \
  "ds_read2_b32 v[90:91], v79 offset0:32 offset1:34\n\t" \
  "ds_read2_b32 v[92:93], v79 offset0:64 offset1:66\n\t" \
  "ds_read2_b32 v[94:95], v79 offset0:96 offset1:98\n\t" \
  "s_waitcnt lgkmcnt(6)\n\t" \
  "v_perm_b32 v112, v82, v80, s62\n\t" \
  "v_perm_b32 v113, v82, v80, s63\n\t" \
  "s_waitcnt lgkmcnt(4)\n\t" \
  "v_perm_b32 v114, v86, v84, s62\n\t" \
  "v_perm_b32 v115, v86, v84, s63\n\t" \
  "s_waitcnt lgkmcnt(2)\n\t" \
  "v_perm_b32 v116, v90, v88, s62\n\t" \
  "v_perm_b32 v117, v90, v88, s63\n\t" \
  "s_waitcnt lgkmcnt(0)\n\t" \
  "v_perm_b32 v118, v94, v92, s62\n\t" \
  "v_perm_b32 v119, v94, v92, s63\n\t" \
  "ds_write_b128 v70, v[112:115] offset:20480\n\t" \
  "ds_write_b128 v70, v[116:119] offset:21504\n\t" \
  "v_perm_b32 v120, v83, v81, s62\n\t" \
  "v_perm_b32 v121, v83, v81, s63\n\t" \
  "v_perm_b32 v122, v87, v85, s62\n\t" \
  "v_perm_b32 v123, v87, v85, s63\n\t" \
  "v_perm_b32 v124, v91, v89, s62\n\t" \
  "v_perm_b32 v125, v91, v89, s63\n\t" \
  "v_perm_b32 v126, v95, v93, s62\n\t" \
  "v_perm_b32 v127, v95, v93, s63\n\t" \
  "ds_write_b128 v70, v[120:123] offset:16384\n\t" \
  "ds_write_b128 v70, v[124:127] offset:17408\n\t" \
  "v_add_u32 v78, 16384, v65\n\t" \
  "s_waitcnt vmcnt(12) lgkmcnt(0)\n\t" \
  "s_barrier\n\t" \
  "ds_read_b64_tr_b16 v[0:1], v78 offset:0\n\t" \
  "ds_read_b64_tr_b16 v[2:3], v78 offset:1024\n\t" \
  "ds_read_b64_tr_b16 v[4:5], v78 offset:8192\n\t" \
  "ds_read_b64_tr_b16 v[6:7], v78 offset:9216\n\t" \
  "ds_read_b64_tr_b16 v[8:9], v78 offset:8\n\t" \
  "ds_read_b64_tr_b16 v[10:11], v78 offset:1032\n\t" \
  "ds_read_b64_tr_b16 v[12:13], v78 offset:8200\n\t" \
  "ds_read_b64_tr_b16 v[14:15], v78 offset:9224\n\t" \
  "s_add_u32 m0, s54, 0\n\t" \
  "s_mov_b32 s60, 524288\n\t" \
  "buffer_load_dwordx4 v71, %[Wq], s60 offen lds\n\t" \
  "s_add_u32 m0, s54, 1024\n\t" \
  "s_mov_b32 s60, 557056\n\t" \
  "buffer_load_dwordx4 v71, %[Wq], s60 offen lds\n\t" \
  "s_add_u32 m0, s54, 2048\n\t" \
  "s_mov_b32 s60, 589824\n\t" \
  "buffer_load_dwordx4 v71, %[Wq], s60 offen lds\n\t" \
  "s_add_u32 m0, s54, 3072\n\t" \
  "s_mov_b32 s60, 622592\n\t" \
  "buffer_load_dwordx4 v71, %[Wq], s60 offen lds\n\t" \
  "v_add_u32 v78, 53248, v65\n\t" \
  "s_waitcnt vmcnt(12) lgkmcnt(0)\n\t" \
  "s_barrier\n\t" \
  "ds_read_b64_tr_b16 a[0:1], v78 offset:0\n\t" \
  "ds_read_b64_tr_b16 a[2:3], v78 offset:1024\n\t" \
  "ds_read_b64_tr_b16 a[4:5], v78 offset:8192\n\t" \
  "ds_read_b64_tr_b16 a[6:7], v78 offset:9216\n\t" \
  "ds_read_b64_tr_b16 a[8:9], v78 offset:8\n\t" \
  "ds_read_b64_tr_b16 a[10:11], v78 offset:1032\n\t" \
  "ds_read_b64_tr_b16 a[12:13], v78 offset:8200\n\t" \
  "ds_read_b64_tr_b16 a[14:15], v78 offset:9224\n\t" \
  "s_xor_b32 s61, s40, 1\n\t" \
  "s_mul_i32 s61, s61, 36864\n\t" \
  "s_add_u32 s61, s61, s55\n\t" \
  "s_add_u32 m0, s54, 16384\n\t" \
  "s_add_u32 s60, s61, 0\n\t" \
  "buffer_load_dwordx4 v75, %[Wo], s60 offen lds\n\t" \
  "s_add_u32 m0, s54, 17408\n\t" \
  "s_add_u32 s60, s61, 9216\n\t" \
  "buffer_load_dwordx4 v75, %[Wo], s60 offen lds\n\t" \
  "s_add_u32 m0, s54, 18432\n\t" \
  "s_add_u32 s60, s61, 18432\n\t" \
  "buffer_load_dwordx4 v75, %[Wo], s60 offen lds\n\t" \
  "s_add_u32 m0, s54, 19456\n\t" \
  "s_add_u32 s60, s61, 27648\n\t" \
  "buffer_load_dwordx4 v75, %[Wo], s60 offen lds\n\t" \
  "v_add_u32 v78, 90112, v64\n\t" \
  "v_add_u32 v79, 91136, v64\n\t" \
  "s_waitcnt vmcnt(12) lgkmcnt(0)\n\t" \
  "s_barrier\n\t" \
  "ds_read2_b32 v[80:81], v78 offset0:0 offset1:2\n\t" \
  "ds_read2_b32 v[82:83], v78 offset0:32 offset1:34\n\t" \
  "ds_read2_b32 v[84:85], v78 offset0:64 offset1:66\n\t" \
  "ds_read2_b32 v[86:87], v78 offset0:96 offset1:98\n\t" \
  "ds_read2_b32 v[88:89], v79 offset0:0 offset1:2\n\t" \
  "ds_read2_b32 v[90:91], v79 offset0:32 offset1:34\n\t" \
  "ds_read2_b32 v[92:93], v79 offset0:64 offset1:66\n\t" \
  "ds_read2_b32 v[94:95], v79 offset0:96 offset1:98\n\t" \
  "s_add_u32 m0, s54, 53248\n\t" \
  "s_mov_b32 s60, 524288\n\t" \
  "buffer_load_dwordx4 v71, %[Wk], s60 offen lds\n\t" \
  "s_add_u32 m0, s54, 54272\n\t" \
  "s_mov_b32 s60, 557056\n\t" \
  "buffer_load_dwordx4 v71, %[Wk], s60 offen lds\n\t" \
  "s_add_u32 m0, s54, 55296\n\t" \
  "s_mov_b32 s60, 589824\n\t" \
  "buffer_load_dwordx4 v71, %[Wk], s60 offen lds\n\t" \
  "s_add_u32 m0, s54, 56320\n\t" \
  "s_mov_b32 s60, 622592\n\t" \
  "buffer_load_dwordx4 v71, %[Wk], s60 offen lds\n\t" \
  "s_waitcnt lgkmcnt(6)\n\t" \
  "v_perm_b32 v112, v82, v80, s62\n\t" \
  "v_perm_b32 v113, v82, v80, s63\n\t" \
  "s_waitcnt lgkmcnt(4)\n\t" \
  "v_perm_b32 v114, v86, v84, s62\n\t" \
  "v_perm_b32 v115, v86, v84, s63\n\t" \
  "s_waitcnt lgkmcnt(2)\n\t" \
  "v_perm_b32 v116, v90, v88, s62\n\t" \
  "v_perm_b32 v117, v90, v88, s63\n\t" \
  "s_waitcnt lgkmcnt(0)\n\t" \
  "v_perm_b32 v118, v94, v92, s62\n\t" \
  "v_perm_b32 v119, v94, v92, s63\n\t" \
  "ds_write_b128 v70, v[112:115] offset:22528\n\t" \
  "ds_write_b128 v70, v[116:119] offset:23552\n\t" \
  "v_perm_b32 v120, v83, v81, s62\n\t" \
  "v_perm_b32 v121, v83, v81, s63\n\t" \
  "v_perm_b32 v122, v87, v85, s62\n\t" \
  "v_perm_b32 v123, v87, v85, s63\n\t" \
  "v_perm_b32 v124, v91, v89, s62\n\t" \
  "v_perm_b32 v125, v91, v89, s63\n\t" \
  "v_perm_b32 v126, v95, v93, s62\n\t" \
  "v_perm_b32 v127, v95, v93, s63\n\t" \
  "ds_write_b128 v70, v[120:123] offset:18432\n\t" \
  "ds_write_b128 v70, v[124:127] offset:19456\n\t" \
  "v_add_u32 v78, 126976, v66\n\t" \
  "s_waitcnt vmcnt(12) lgkmcnt(0)\n\t" \
  "s_barrier\n\t" \
  "ds_read_b64_tr_b16 a[16:17], v78 offset:0\n\t" \
  "ds_read_b64_tr_b16 a[18:19], v78 offset:1024\n\t" \
  "ds_read_b64_tr_b16 a[20:21], v78 offset:8192\n\t" \
  "ds_read_b64_tr_b16 a[22:23], v78 offset:9216\n\t" \
  "ds_read_b64_tr_b16 a[24:25], v78 offset:32\n\t" \
  "ds_read_b64_tr_b16 a[26:27], v78 offset:1056\n\t" \
  "ds_read_b64_tr_b16 a[28:29], v78 offset:8224\n\t" \
  "ds_read_b64_tr_b16 a[30:31], v78 offset:9248\n\t" \
  "s_add_u32 m0, s54, 90112\n\t" \
  "s_mov_b32 s60, 524288\n\t" \
  "buffer_load_dwordx4 v71, %[Wv], s60 offen lds\n\t" \
  "s_add_u32 m0, s54, 91136\n\t" \
  "s_mov_b32 s60, 557056\n\t" \
  "buffer_load_dwordx4 v71, %[Wv], s60 offen lds\n\t" \
  "s_add_u32 m0, s54, 92160\n\t" \
  "s_mov_b32 s60, 589824\n\t" \
  "buffer_load_dwordx4 v71, %[Wv], s60 offen lds\n\t" \
  "s_add_u32 m0, s54, 93184\n\t" \
  "s_mov_b32 s60, 622592\n\t" \
  "buffer_load_dwordx4 v71, %[Wv], s60 offen lds\n\t" \
  "v_add_u32 v78, 0, v65\n\t" \
  "s_waitcnt vmcnt(12) lgkmcnt(0)\n\t" \
  "s_barrier\n\t" \
  "ds_read_b64_tr_b16 a[32:33], v78 offset:0\n\t" \
  "ds_read_b64_tr_b16 a[34:35], v78 offset:1024\n\t" \
  "ds_read_b64_tr_b16 a[36:37], v78 offset:8192\n\t" \
  "ds_read_b64_tr_b16 a[38:39], v78 offset:9216\n\t" \
  "ds_read_b64_tr_b16 a[40:41], v78 offset:8\n\t" \
  "ds_read_b64_tr_b16 a[42:43], v78 offset:1032\n\t" \
  "ds_read_b64_tr_b16 a[44:45], v78 offset:8200\n\t" \
  "ds_read_b64_tr_b16 a[46:47], v78 offset:9224\n\t" \
  "s_xor_b32 s61, s40, 1\n\t" \
  "s_mul_i32 s61, s61, 36864\n\t" \
  "s_add_u32 s61, s61, s55\n\t" \
  "s_add_u32 m0, s54, 126976\n\t" \
  "s_add_u32 s60, s61, 512\n\t" \
  "buffer_load_dwordx4 v75, %[Wo], s60 offen lds\n\t" \
  "s_add_u32 m0, s54, 128000\n\t" \
  "s_add_u32 s60, s61, 9728\n\t" \
  "buffer_load_dwordx4 v75, %[Wo], s60 offen lds\n\t" \
  "s_add_u32 m0, s54, 129024\n\t" \
  "s_add_u32 s60, s61, 18944\n\t" \
  "buffer_load_dwordx4 v75, %[Wo], s60 offen lds\n\t" \
  "s_add_u32 m0, s54, 130048\n\t" \
  "s_add_u32 s60, s61, 28160\n\t" \
  "buffer_load_dwordx4 v75, %[Wo], s60 offen lds\n\t" \
  "v_add_u32 v78, 16384, v64\n\t" \
  "v_add_u32 v79, 17408, v64\n\t" \
  "s_waitcnt vmcnt(12) lgkmcnt(0)\n\t" \
  "s_barrier\n\t" \
  "ds_read2_b32 v[80:81], v78 offset0:0 offset1:2\n\t" \
  "ds_read2_b32 v[82:83], v78 offset0:32 offset1:34\n\t" \
  "ds_read2_b32 v[84:85], v78 offset0:64 offset1:66\n\t" \
  "ds_read2_b32 v[86:87], v78 offset0:96 offset1:98\n\t" \
  "ds_read2_b32 v[88:89], v79 offset0:0 offset1:2\n\t" \
  "ds_read2_b32 v[90:91], v79 offset0:32 offset1:34\n\t" \
  "ds_read2_b32 v[92:93], v79 offset0:64 offset1:66\n\t" \
  "ds_read2_b32 v[94:95], v79 offset0:96 offset1:98\n\t" \
  "s_add_u32 m0, s54, 0\n\t" \
  "s_mov_b32 s60, 1048576\n\t" \
  "buffer_load_dwordx4 v71, %[Wq], s60 offen lds\n\t" \
  "s_add_u32 m0, s54, 1024\n\t" \
  "s_mov_b32 s60, 1081344\n\t" \
  "buffer_load_dwordx4 v71, %[Wq], s60 offen lds\n\t" \
  "s_add_u32 m0, s54, 2048\n\t" \
  "s_mov_b32 s60, 1114112\n\t" \
  "buffer_load_dwordx4 v71, %[Wq], s60 offen lds\n\t" \
  "s_add_u32 m0, s54, 3072\n\t" \
  "s_mov_b32 s60, 1146880\n\t" \
  "buffer_load_dwordx4 v71, %[Wq], s60 offen lds\n\t" \
  "s_waitcnt lgkmcnt(6)\n\t" \
  "v_perm_b32 v112, v82, v80, s62\n\t" \
  "v_perm_b32 v113, v82, v80, s63\n\t" \
  "s_waitcnt lgkmcnt(4)\n\t" \
  "v_perm_b32 v114, v86, v84, s62\n\t" \
  "v_perm_b32 v115, v86, v84, s63\n\t" \
  "s_waitcnt lgkmcnt(2)\n\t" \
  "v_perm_b32 v116, v90, v88, s62\n\t" \
  "v_perm_b32 v117, v90, v88, s63\n\t" \
  "s_waitcnt lgkmcnt(0)\n\t" \
  "v_perm_b32 v118, v94, v92, s62\n\t" \
  "v_perm_b32 v119, v94, v92, s63\n\t" \
  "ds_write_b128 v70, v[112:115] offset:24576\n\t" \
  "ds_write_b128 v70, v[116:119] offset:25600\n\t" \
  "v_perm_b32 v200, v83, v81, s62\n\t" \
  "v_perm_b32 v201, v83, v81, s63\n\t" \
  "v_perm_b32 v202, v87, v85, s62\n\t" \
  "v_perm_b32 v203, v87, v85, s63\n\t" \
  "v_perm_b32 v204, v91, v89, s62\n\t" \
  "v_perm_b32 v205, v91, v89, s63\n\t" \
  "v_perm_b32 v206, v95, v93, s62\n\t" \
  "v_perm_b32 v207, v95, v93, s63\n\t" \
  "v_add_u32 v78, 53248, v65\n\t" \
  "s_waitcnt vmcnt(12) lgkmcnt(0)\n\t" \
  "s_barrier\n\t" \
  "ds_read_b64_tr_b16 a[48:49], v78 offset:0\n\t" \
  "ds_read_b64_tr_b16 a[50:51], v78 offset:1024\n\t" \
  "ds_read_b64_tr_b16 a[52:53], v78 offset:8192\n\t" \
  "ds_read_b64_tr_b16 a[54:55], v78 offset:9216\n\t" \
  "ds_read_b64_tr_b16 a[56:57], v78 offset:8\n\t" \
  "ds_read_b64_tr_b16 a[58:59], v78 offset:1032\n\t" \
  "ds_read_b64_tr_b16 a[60:61], v78 offset:8200\n\t" \
  "ds_read_b64_tr_b16 a[62:63], v78 offset:9224\n\t" \
  "s_add_u32 m0, s54, 16384\n\t" \
  "s_mov_b32 s60, 1048576\n\t" \
  "buffer_load_dwordx4 v71, %[Wk], s60 offen lds\n\t" \
  "s_add_u32 m0, s54, 17408\n\t" \
  "s_mov_b32 s60, 1081344\n\t" \
  "buffer_load_dwordx4 v71, %[Wk], s60 offen lds\n\t" \
  "s_add_u32 m0, s54, 18432\n\t" \
  "s_mov_b32 s60, 1114112\n\t" \
  "buffer_load_dwordx4 v71, %[Wk], s60 offen lds\n\t" \
  "s_add_u32 m0, s54, 19456\n\t" \
  "s_mov_b32 s60, 1146880\n\t" \
  "buffer_load_dwordx4 v71, %[Wk], s60 offen lds\n\t" \
  "v_add_u32 v78, 90112, v66\n\t" \
  "s_waitcnt vmcnt(12) lgkmcnt(0)\n\t" \
  "s_barrier\n\t" \
  "ds_read_b64_tr_b16 a[64:65], v78 offset:0\n\t" \
  "ds_read_b64_tr_b16 a[66:67], v78 offset:1024\n\t" \
  "ds_read_b64_tr_b16 a[68:69], v78 offset:8192\n\t" \
  "ds_read_b64_tr_b16 a[70:71], v78 offset:9216\n\t" \
  "ds_read_b64_tr_b16 a[72:73], v78 offset:32\n\t" \
  "ds_read_b64_tr_b16 a[74:75], v78 offset:1056\n\t" \
  "ds_read_b64_tr_b16 a[76:77], v78 offset:8224\n\t" \
  "ds_read_b64_tr_b16 a[78:79], v78 offset:9248\n\t" \
  "s_xor_b32 s61, s40, 2\n\t" \
  "s_mul_i32 s61, s61, 36864\n\t" \
  "s_add_u32 s61, s61, s55\n\t" \
  "s_add_u32 m0, s54, 53248\n\t" \
  "s_add_u32 s60, s61, 0\n\t" \
  "buffer_load_dwordx4 v75, %[Wo], s60 offen lds\n\t" \
  "s_add_u32 m0, s54, 54272\n\t" \
  "s_add_u32 s60, s61, 9216\n\t" \
  "buffer_load_dwordx4 v75, %[Wo], s60 offen lds\n\t" \
  "s_add_u32 m0, s54, 55296\n\t" \
  "s_add_u32 s60, s61, 18432\n\t" \
  "buffer_load_dwordx4 v75, %[Wo], s60 offen lds\n\t" \
  "s_add_u32 m0, s54, 56320\n\t" \
  "s_add_u32 s60, s61, 27648\n\t" \
  "buffer_load_dwordx4 v75, %[Wo], s60 offen lds\n\t" \
  "v_add_u32 v78, 126976, v64\n\t" \
  "v_add_u32 v79, 128000, v64\n\t" \
  "s_waitcnt vmcnt(12) lgkmcnt(0)\n\t" \
  "s_barrier\n\t" \
  "ds_read2_b32 v[80:81], v78 offset0:0 offset1:2\n\t" \
  "ds_read2_b32 v[82:83], v78 offset0:32 offset1:34\n\t" \
  "ds_read2_b32 v[84:85], v78 offset0:64 offset1:66\n\t" \
  "ds_read2_b32 v[86:87], v78 offset0:96 offset1:98\n\t" \
  "ds_read2_b32 v[88:89], v79 offset0:0 offset1:2\n\t" \
  "ds_read2_b32 v[90:91], v79 offset0:32 offset1:34\n\t" \
  "ds_read2_b32 v[92:93], v79 offset0:64 offset1:66\n\t" \
  "ds_read2_b32 v[94:95], v79 offset0:96 offset1:98\n\t" \
  "s_add_u32 m0, s54, 90112\n\t" \
  "s_mov_b32 s60, 1048576\n\t" \
  "buffer_load_dwordx4 v71, %[Wv], s60 offen lds\n\t" \
  "s_add_u32 m0, s54, 91136\n\t" \
  "s_mov_b32 s60, 1081344\n\t" \
  "buffer_load_dwordx4 v71, %[Wv], s60 offen lds\n\t" \
  "s_add_u32 m0, s54, 92160\n\t" \
  "s_mov_b32 s60, 1114112\n\t" \
  "buffer_load_dwordx4 v71, %[Wv], s60 offen lds\n\t" \
  "s_add_u32 m0, s54, 93184\n\t" \
  "s_mov_b32 s60, 1146880\n\t" \
  "buffer_load_dwordx4 v71, %[Wv], s60 offen lds\n\t" \
  "s_waitcnt lgkmcnt(6)\n\t" \
  "v_perm_b32 v112, v82, v80, s62\n\t" \
  "v_perm_b32 v113, v82, v80, s63\n\t" \
  "s_waitcnt lgkmcnt(4)\n\t" \
  "v_perm_b32 v114, v86, v84, s62\n\t" \
  "v_perm_b32 v115, v86, v84, s63\n\t" \
  "s_waitcnt lgkmcnt(2)\n\t" \
  "v_perm_b32 v116, v90, v88, s62\n\t" \
  "v_perm_b32 v117, v90, v88, s63\n\t" \
  "s_waitcnt lgkmcnt(0)\n\t" \
  "v_perm_b32 v118, v94, v92, s62\n\t" \
  "v_perm_b32 v119, v94, v92, s63\n\t" \
  "ds_write_b128 v70, v[112:115] offset:26624\n\t" \
  "ds_write_b128 v70, v[116:119] offset:27648\n\t" \
  "v_perm_b32 v232, v83, v81, s62\n\t" \
  "v_perm_b32 v233, v83, v81, s63\n\t" \
  "v_perm_b32 v234, v87, v85, s62\n\t" \
  "v_perm_b32 v235, v87, v85, s63\n\t" \
  "v_perm_b32 v236, v91, v89, s62\n\t" \
  "v_perm_b32 v237, v91, v89, s63\n\t" \
  "v_perm_b32 v238, v95, v93, s62\n\t" \
  "v_perm_b32 v239, v95, v93, s63\n\t" \
  "v_add_u32 v78, 0, v65\n\t" \
  "s_waitcnt vmcnt(12) lgkmcnt(0)\n\t" \
  "s_barrier\n\t" \
  "ds_read_b64_tr_b16 a[80:81], v78 offset:0\n\t" \
  "ds_read_b64_tr_b16 a[82:83], v78 offset:1024\n\t" \
  "ds_read_b64_tr_b16 a[84:85], v78 offset:8192\n\t" \
  "ds_read_b64_tr_b16 a[86:87], v78 offset:9216\n\t" \
  "ds_read_b64_tr_b16 a[88:89], v78 offset:8\n\t" \
  "ds_read_b64_tr_b16 a[90:91], v78 offset:1032\n\t" \
  "ds_read_b64_tr_b16 a[92:93], v78 offset:8200\n\t" \
  "ds_read_b64_tr_b16 a[94:95], v78 offset:9224\n\t" \
  "s_add_u32 m0, s54, 126976\n\t" \
  "s_mov_b32 s60, 1572864\n\t" \
  "buffer_load_dwordx4 v71, %[Wq], s60 offen lds\n\t" \
  "s_add_u32 m0, s54, 128000\n\t" \
  "s_mov_b32 s60, 1605632\n\t" \
  "buffer_load_dwordx4 v71, %[Wq], s60 offen lds\n\t" \
  "s_add_u32 m0, s54, 129024\n\t" \
  "s_mov_b32 s60, 1638400\n\t" \
  "buffer_load_dwordx4 v71, %[Wq], s60 offen lds\n\t" \
  "s_add_u32 m0, s54, 130048\n\t" \
  "s_mov_b32 s60, 1671168\n\t" \
  "buffer_load_dwordx4 v71, %[Wq], s60 offen lds\n\t" \
  "v_add_u32 v78, 16384, v65\n\t" \
  "s_waitcnt vmcnt(12) lgkmcnt(0)\n\t" \
  "s_barrier\n\t" \
  "ds_read_b64_tr_b16 v[32:33], v78 offset:0\n\t" \
  "ds_read_b64_tr_b16 v[34:35], v78 offset:1024\n\t" \
  "ds_read_b64_tr_b16 v[36:37], v78 offset:8192\n\t" \
  "ds_read_b64_tr_b16 v[38:39], v78 offset:9216\n\t" \
  "ds_read_b64_tr_b16 a[96:97], v78 offset:8\n\t" \
  "ds_read_b64_tr_b16 a[98:99], v78 offset:1032\n\t" \
  "ds_read_b64_tr_b16 a[100:101], v78 offset:8200\n\t" \
  "ds_read_b64_tr_b16 a[102:103], v78 offset:9224\n\t" \
  "s_xor_b32 s61, s40, 2\n\t" \
  "s_mul_i32 s61, s61, 36864\n\t" \
  "s_add_u32 s61, s61, s55\n\t" \
  "s_add_u32 m0, s54, 0\n\t" \
  "s_add_u32 s60, s61, 512\n\t" \
  "buffer_load_dwordx4 v75, %[Wo], s60 offen lds\n\t" \
  "s_add_u32 m0, s54, 1024\n\t" \
  "s_add_u32 s60, s61, 9728\n\t" \
  "buffer_load_dwordx4 v75, %[Wo], s60 offen lds\n\t" \
  "s_add_u32 m0, s54, 2048\n\t" \
  "s_add_u32 s60, s61, 18944\n\t" \
  "buffer_load_dwordx4 v75, %[Wo], s60 offen lds\n\t" \
  "s_add_u32 m0, s54, 3072\n\t" \
  "s_add_u32 s60, s61, 28160\n\t" \
  "buffer_load_dwordx4 v75, %[Wo], s60 offen lds\n\t" \
  "v_add_u32 v78, 53248, v64\n\t" \
  "v_add_u32 v79, 54272, v64\n\t" \
  "s_waitcnt vmcnt(12) lgkmcnt(0)\n\t" \
  "s_barrier\n\t" \
  "ds_read2_b32 v[80:81], v78 offset0:0 offset1:2\n\t" \
  "ds_read2_b32 v[82:83], v78 offset0:32 offset1:34\n\t" \
  "ds_read2_b32 v[84:85], v78 offset0:64 offset1:66\n\t" \
  "ds_read2_b32 v[86:87], v78 offset0:96 offset1:98\n\t" \
  "ds_read2_b32 v[88:89], v79 offset0:0 offset1:2\n\t" \
  "ds_read2_b32 v[90:91], v79 offset0:32 offset1:34\n\t" \
  "ds_read2_b32 v[92:93], v79 offset0:64 offset1:66\n\t" \
  "ds_read2_b32 v[94:95], v79 offset0:96 offset1:98\n\t" \
  "s_add_u32 m0, s54, 16384\n\t" \
  "s_mov_b32 s60, 1572864\n\t" \
  "buffer_load_dwordx4 v71, %[Wk], s60 offen lds\n\t" \
  "s_add_u32 m0, s54, 17408\n\t" \
  "s_mov_b32 s60, 1605632\n\t" \
  "buffer_load_dwordx4 v71, %[Wk], s60 offen lds\n\t" \
  "s_add_u32 m0, s54, 18432\n\t" \
  "s_mov_b32 s60, 1638400\n\t" \
  "buffer_load_dwordx4 v71, %[Wk], s60 offen lds\n\t" \
  "s_add_u32 m0, s54, 19456\n\t" \
  "s_mov_b32 s60, 1671168\n\t" \
  "buffer_load_dwordx4 v71, %[Wk], s60 offen lds\n\t" \
  "s_waitcnt lgkmcnt(6)\n\t" \
  "v_perm_b32 v112, v82, v80, s62\n\t" \
  "v_perm_b32 v113, v82, v80, s63\n\t" \
  "s_waitcnt lgkmcnt(4)\n\t" \
  "v_perm_b32 v114, v86, v84, s62\n\t" \
  "v_perm_b32 v115, v86, v84, s63\n\t" \
  "s_waitcnt lgkmcnt(2)\n\t" \
  "v_perm_b32 v116, v90, v88, s62\n\t" \
  "v_perm_b32 v117, v90, v88, s63\n\t" \
  "s_waitcnt lgkmcnt(0)\n\t" \
  "v_perm_b32 v118, v94, v92, s62\n\t" \
  "v_perm_b32 v119, v94, v92, s63\n\t" \
  "ds_write_b128 v70, v[112:115] offset:28672\n\t" \
  "ds_write_b128 v70, v[116:119] offset:29696\n\t" \
  "v_perm_b32 v208, v83, v81, s62\n\t" \
  "v_perm_b32 v209, v83, v81, s63\n\t" \
  "v_perm_b32 v210, v87, v85, s62\n\t" \
  "v_perm_b32 v211, v87, v85, s63\n\t" \
  "v_perm_b32 v212, v91, v89, s62\n\t" \
  "v_perm_b32 v213, v91, v89, s63\n\t" \
  "v_perm_b32 v214, v95, v93, s62\n\t" \
  "v_perm_b32 v215, v95, v93, s63\n\t" \
  "v_add_u32 v78, 90112, v66\n\t" \
  "s_waitcnt vmcnt(12) lgkmcnt(0)\n\t" \
  "s_barrier\n\t" \
  "ds_read_b64_tr_b16 a[104:105], v78 offset:0\n\t" \
  "ds_read_b64_tr_b16 a[106:107], v78 offset:1024\n\t" \
  "ds_read_b64_tr_b16 a[108:109], v78 offset:8192\n\t" \
  "ds_read_b64_tr_b16 a[110:111], v78 offset:9216\n\t" \
  "ds_read_b64_tr_b16 a[112:113], v78 offset:32\n\t" \
  "ds_read_b64_tr_b16 a[114:115], v78 offset:1056\n\t" \
  "ds_read_b64_tr_b16 a[116:117], v78 offset:8224\n\t" \
  "ds_read_b64_tr_b16 a[118:119], v78 offset:9248\n\t" \
  "s_add_u32 m0, s54, 53248\n\t" \
  "s_mov_b32 s60, 1572864\n\t" \
  "buffer_load_dwordx4 v71, %[Wv], s60 offen lds\n\t" \
  "s_add_u32 m0, s54, 54272\n\t" \
  "s_mov_b32 s60, 1605632\n\t" \
  "buffer_load_dwordx4 v71, %[Wv], s60 offen lds\n\t" \
  "s_add_u32 m0, s54, 55296\n\t" \
  "s_mov_b32 s60, 1638400\n\t" \
  "buffer_load_dwordx4 v71, %[Wv], s60 offen lds\n\t" \
  "s_add_u32 m0, s54, 56320\n\t" \
  "s_mov_b32 s60, 1671168\n\t" \
  "buffer_load_dwordx4 v71, %[Wv], s60 offen lds\n\t" \
  "v_add_u32 v78, 126976, v65\n\t" \
  "s_waitcnt vmcnt(12) lgkmcnt(0)\n\t" \
  "s_barrier\n\t" \
  "ds_read_b64_tr_b16 a[120:121], v78 offset:0\n\t" \
  "ds_read_b64_tr_b16 a[122:123], v78 offset:1024\n\t" \
  "ds_read_b64_tr_b16 a[124:125], v78 offset:8192\n\t" \
  "ds_read_b64_tr_b16 a[126:127], v78 offset:9216\n\t" \
  "ds_read_b64_tr_b16 v[16:17], v78 offset:8\n\t" \
  "ds_read_b64_tr_b16 v[18:19], v78 offset:1032\n\t" \
  "ds_read_b64_tr_b16 v[20:21], v78 offset:8200\n\t" \
  "ds_read_b64_tr_b16 v[22:23], v78 offset:9224\n\t" \
  "s_xor_b32 s61, s40, 3\n\t" \
  "s_mul_i32 s61, s61, 36864\n\t" \
  "s_add_u32 s61, s61, s55\n\t" \
  "s_add_u32 m0, s54, 90112\n\t" \
  "s_add_u32 s60, s61, 0\n\t" \
  "buffer_load_dwordx4 v75, %[Wo], s60 offen lds\n\t" \
  "s_add_u32 m0, s54, 91136\n\t" \
  "s_add_u32 s60, s61, 9216\n\t" \
  "buffer_load_dwordx4 v75, %[Wo], s60 offen lds\n\t" \
  "s_add_u32 m0, s54, 92160\n\t" \
  "s_add_u32 s60, s61, 18432\n\t" \
  "buffer_load_dwordx4 v75, %[Wo], s60 offen lds\n\t" \
  "s_add_u32 m0, s54, 93184\n\t" \
  "s_add_u32 s60, s61, 27648\n\t" \
  "buffer_load_dwordx4 v75, %[Wo], s60 offen lds\n\t" \
  "v_add_u32 v78, 0, v64\n\t" \
  "v_add_u32 v79, 1024, v64\n\t" \
  "s_waitcnt vmcnt(12) lgkmcnt(0)\n\t" \
  "s_barrier\n\t" \
  "ds_read2_b32 v[80:81], v78 offset0:0 offset1:2\n\t" \
  "ds_read2_b32 v[82:83], v78 offset0:32 offset1:34\n\t" \
  "ds_read2_b32 v[84:85], v78 offset0:64 offset1:66\n\t" \
  "ds_read2_b32 v[86:87], v78 offset0:96 offset1:98\n\t" \
  "ds_read2_b32 v[88:89], v79 offset0:0 offset1:2\n\t" \
  "ds_read2_b32 v[90:91], v79 offset0:32 offset1:34\n\t" \
  "ds_read2_b32 v[92:93], v79 offset0:64 offset1:66\n\t" \
  "ds_read2_b32 v[94:95], v79 offset0:96 offset1:98\n\t" \
  "s_add_u32 m0, s54, 126976\n\t" \
  "s_mov_b32 s60, 2097152\n\t" \
  "buffer_load_dwordx4 v71, %[Wq], s60 offen lds\n\t" \
  "s_add_u32 m0, s54, 128000\n\t" \
  "s_mov_b32 s60, 2129920\n\t" \
  "buffer_load_dwordx4 v71, %[Wq], s60 offen lds\n\t" \
  "s_add_u32 m0, s54, 129024\n\t" \
  "s_mov_b32 s60, 2162688\n\t" \
  "buffer_load_dwordx4 v71, %[Wq], s60 offen lds\n\t" \
  "s_add_u32 m0, s54, 130048\n\t" \
  "s_mov_b32 s60, 2195456\n\t" \
  "buffer_load_dwordx4 v71, %[Wq], s60 offen lds\n\t" \
  "s_waitcnt lgkmcnt(6)\n\t" \
  "v_perm_b32 v112, v82, v80, s62\n\t" \
  "v_perm_b32 v113, v82, v80, s63\n\t" \
  "s_waitcnt lgkmcnt(4)\n\t" \
  "v_perm_b32 v114, v86, v84, s62\n\t" \
  "v_perm_b32 v115, v86, v84, s63\n\t" \
  "s_waitcnt lgkmcnt(2)\n\t" \
  "v_perm_b32 v116, v90, v88, s62\n\t" \
  "v_perm_b32 v117, v90, v88, s63\n\t" \
  "s_waitcnt lgkmcnt(0)\n\t" \
  "v_perm_b32 v118, v94, v92, s62\n\t" \
  "v_perm_b32 v119, v94, v92, s63\n\t" \
  "ds_write_b128 v70, v[112:115] offset:30720\n\t" \
  "ds_write_b128 v70, v[116:119] offset:31744\n\t" \
  "v_perm_b32 v240, v83, v81, s62\n\t" \
  "v_perm_b32 v241, v83, v81, s63\n\t" \
  "v_perm_b32 v242, v87, v85, s62\n\t" \
  "v_perm_b32 v243, v87, v85, s63\n\t" \
  "v_perm_b32 v244, v91, v89, s62\n\t" \
  "v_perm_b32 v245, v91, v89, s63\n\t" \
  "v_perm_b32 v246, v95, v93, s62\n\t" \
  "v_perm_b32 v247, v95, v93, s63\n\t" \
  "v_add_u32 v78, 16384, v65\n\t" \
  "s_waitcnt vmcnt(12) lgkmcnt(0)\n\t" \
  "s_barrier\n\t" \
  "ds_read_b64_tr_b16 a[128:129], v78 offset:0\n\t" \
  "ds_read_b64_tr_b16 a[130:131], v78 offset:1024\n\t" \
  "ds_read_b64_tr_b16 a[132:133], v78 offset:8192\n\t" \
  "ds_read_b64_tr_b16 a[134:135], v78 offset:9216\n\t" \
  "ds_read_b64_tr_b16 a[136:137], v78 offset:8\n\t" \
  "ds_read_b64_tr_b16 a[138:139], v78 offset:1032\n\t" \
  "ds_read_b64_tr_b16 a[140:141], v78 offset:8200\n\t" \
  "ds_read_b64_tr_b16 a[142:143], v78 offset:9224\n\t" \
  "s_add_u32 m0, s54, 0\n\t" \
  "s_mov_b32 s60, 2097152\n\t" \
  "buffer_load_dwordx4 v71, %[Wk], s60 offen lds\n\t" \
  "s_add_u32 m0, s54, 1024\n\t" \
  "s_mov_b32 s60, 2129920\n\t" \
  "buffer_load_dwordx4 v71, %[Wk], s60 offen lds\n\t" \
  "s_add_u32 m0, s54, 2048\n\t" \
  "s_mov_b32 s60, 2162688\n\t" \
  "buffer_load_dwordx4 v71, %[Wk], s60 offen lds\n\t" \
  "s_add_u32 m0, s54, 3072\n\t" \
  "s_mov_b32 s60, 2195456\n\t" \
  "buffer_load_dwordx4 v71, %[Wk], s60 offen lds\n\t" \
  "v_add_u32 v78, 53248, v66\n\t" \
  "s_waitcnt vmcnt(12) lgkmcnt(0)\n\t" \
  "s_barrier\n\t" \
  "ds_read_b64_tr_b16 a[144:145], v78 offset:0\n\t" \
  "ds_read_b64_tr_b16 a[146:147], v78 offset:1024\n\t" \
  "ds_read_b64_tr_b16 a[148:149], v78 offset:8192\n\t" \
  "ds_read_b64_tr_b16 a[150:151], v78 offset:9216\n\t" \
  "ds_read_b64_tr_b16 a[152:153], v78 offset:32\n\t" \
  "ds_read_b64_tr_b16 a[154:155], v78 offset:1056\n\t" \
  "ds_read_b64_tr_b16 a[156:157], v78 offset:8224\n\t" \
  "ds_read_b64_tr_b16 a[158:159], v78 offset:9248\n\t" \
  "s_xor_b32 s61, s40, 3\n\t" \
  "s_mul_i32 s61, s61, 36864\n\t" \
  "s_add_u32 s61, s61, s55\n\t" \
  "s_add_u32 m0, s54, 16384\n\t" \
  "s_add_u32 s60, s61, 512\n\t" \
  "buffer_load_dwordx4 v75, %[Wo], s60 offen lds\n\t" \
  "s_add_u32 m0, s54, 17408\n\t" \
  "s_add_u32 s60, s61, 9728\n\t" \
  "buffer_load_dwordx4 v75, %[Wo], s60 offen lds\n\t" \
  "s_add_u32 m0, s54, 18432\n\t" \
  "s_add_u32 s60, s61, 18944\n\t" \
  "buffer_load_dwordx4 v75, %[Wo], s60 offen lds\n\t" \
  "s_add_u32 m0, s54, 19456\n\t" \
  "s_add_u32 s60, s61, 28160\n\t" \
  "buffer_load_dwordx4 v75, %[Wo], s60 offen lds\n\t" \
  "v_add_u32 v78, 90112, v64\n\t" \
  "v_add_u32 v79, 91136, v64\n\t" \
  "s_waitcnt vmcnt(12) lgkmcnt(0)\n\t" \
  "s_barrier\n\t" \
  "ds_read2_b32 v[80:81], v78 offset0:0 offset1:2\n\t" \
  "ds_read2_b32 v[82:83], v78 offset0:32 offset1:34\n\t" \
  "ds_read2_b32 v[84:85], v78 offset0:64 offset1:66\n\t" \
  "ds_read2_b32 v[86:87], v78 offset0:96 offset1:98\n\t" \
  "ds_read2_b32 v[88:89], v79 offset0:0 offset1:2\n\t" \
  "ds_read2_b32 v[90:91], v79 offset0:32 offset1:34\n\t" \
  "ds_read2_b32 v[92:93], v79 offset0:64 offset1:66\n\t" \
  "ds_read2_b32 v[94:95], v79 offset0:96 offset1:98\n\t" \
  "s_add_u32 m0, s54, 53248\n\t" \
  "s_mov_b32 s60, 2097152\n\t" \
  "buffer_load_dwordx4 v71, %[Wv], s60 offen lds\n\t" \
  "s_add_u32 m0, s54, 54272\n\t" \
  "s_mov_b32 s60, 2129920\n\t" \
  "buffer_load_dwordx4 v71, %[Wv], s60 offen lds\n\t" \
  "s_add_u32 m0, s54, 55296\n\t" \
  "s_mov_b32 s60, 2162688\n\t" \
  "buffer_load_dwordx4 v71, %[Wv], s60 offen lds\n\t" \
  "s_add_u32 m0, s54, 56320\n\t" \
  "s_mov_b32 s60, 2195456\n\t" \
  "buffer_load_dwordx4 v71, %[Wv], s60 offen lds\n\t" \
  "s_waitcnt lgkmcnt(6)\n\t" \
  "v_perm_b32 v192, v82, v80, s62\n\t" \
  "v_perm_b32 v193, v82, v80, s63\n\t" \
  "s_waitcnt lgkmcnt(4)\n\t" \
  "v_perm_b32 v194, v86, v84, s62\n\t" \
  "v_perm_b32 v195, v86, v84, s63\n\t" \
  "s_waitcnt lgkmcnt(2)\n\t" \
  "v_perm_b32 v196, v90, v88, s62\n\t" \
  "v_perm_b32 v197, v90, v88, s63\n\t" \
  "s_waitcnt lgkmcnt(0)\n\t" \
  "v_perm_b32 v198, v94, v92, s62\n\t" \
  "v_perm_b32 v199, v94, v92, s63\n\t" \
  "v_perm_b32 v216, v83, v81, s62\n\t" \
  "v_perm_b32 v217, v83, v81, s63\n\t" \
  "v_perm_b32 v218, v87, v85, s62\n\t" \
  "v_perm_b32 v219, v87, v85, s63\n\t" \
  "v_perm_b32 v220, v91, v89, s62\n\t" \
  "v_perm_b32 v221, v91, v89, s63\n\t" \
  "v_perm_b32 v222, v95, v93, s62\n\t" \
  "v_perm_b32 v223, v95, v93, s63\n\t" \
  "v_add_u32 v78, 126976, v65\n\t" \
  "s_waitcnt vmcnt(12) lgkmcnt(0)\n\t" \
  "s_barrier\n\t" \
  "ds_read_b64_tr_b16 a[160:161], v78 offset:0\n\t" \
  "ds_read_b64_tr_b16 a[162:163], v78 offset:1024\n\t" \
  "ds_read_b64_tr_b16 a[164:165], v78 offset:8192\n\t" \
  "ds_read_b64_tr_b16 a[166:167], v78 offset:9216\n\t" \
  "ds_read_b64_tr_b16 a[168:169], v78 offset:8\n\t" \
  "ds_read_b64_tr_b16 a[170:171], v78 offset:1032\n\t" \
  "ds_read_b64_tr_b16 a[172:173], v78 offset:8200\n\t" \
  "ds_read_b64_tr_b16 a[174:175], v78 offset:9224\n\t" \
  "s_add_u32 m0, s54, 90112\n\t" \
  "s_mov_b32 s60, 2621440\n\t" \
  "buffer_load_dwordx4 v71, %[Wq], s60 offen lds\n\t" \
  "s_add_u32 m0, s54, 91136\n\t" \
  "s_mov_b32 s60, 2654208\n\t" \
  "buffer_load_dwordx4 v71, %[Wq], s60 offen lds\n\t" \
  "s_add_u32 m0, s54, 92160\n\t" \
  "s_mov_b32 s60, 2686976\n\t" \
  "buffer_load_dwordx4 v71, %[Wq], s60 offen lds\n\t" \
  "s_add_u32 m0, s54, 93184\n\t" \
  "s_mov_b32 s60, 2719744\n\t" \
  "buffer_load_dwordx4 v71, %[Wq], s60 offen lds\n\t" \
  "v_add_u32 v78, 0, v65\n\t" \
  "s_waitcnt vmcnt(12) lgkmcnt(0)\n\t" \
  "s_barrier\n\t" \
  "ds_read_b64_tr_b16 a[176:177], v78 offset:0\n\t" \
  "ds_read_b64_tr_b16 a[178:179], v78 offset:1024\n\t" \
  "ds_read_b64_tr_b16 a[180:181], v78 offset:8192\n\t" \
  "ds_read_b64_tr_b16 a[182:183], v78 offset:9216\n\t" \
  "ds_read_b64_tr_b16 a[184:185], v78 offset:8\n\t" \
  "ds_read_b64_tr_b16 a[186:187], v78 offset:1032\n\t" \
  "ds_read_b64_tr_b16 a[188:189], v78 offset:8200\n\t" \
  "ds_read_b64_tr_b16 a[190:191], v78 offset:9224\n\t" \
  "s_add_u32 m0, s54, 126976\n\t" \
  "s_add_u32 s60, s75, 0\n\t" \
  "buffer_load_dwordx4 v75, %[Wo], s60 offen lds\n\t" \
  "s_add_u32 m0, s54, 128000\n\t" \
  "s_add_u32 s60, s75, 9216\n\t" \
  "buffer_load_dwordx4 v75, %[Wo], s60 offen lds\n\t" \
  "s_add_u32 m0, s54, 129024\n\t" \
  "s_add_u32 s60, s75, 18432\n\t" \
  "buffer_load_dwordx4 v75, %[Wo], s60 offen lds\n\t" \
  "s_add_u32 m0, s54, 130048\n\t" \
  "s_add_u32 s60, s75, 27648\n\t" \
  "buffer_load_dwordx4 v75, %[Wo], s60 offen lds\n\t" \
  "v_add_u32 v78, 16384, v64\n\t" \
  "v_add_u32 v79, 17408, v64\n\t" \
  "s_waitcnt vmcnt(12) lgkmcnt(0)\n\t" \
  "s_barrier\n\t" \
  "ds_read2_b32 v[80:81], v78 offset0:0 offset1:2\n\t" \
  "ds_read2_b32 v[82:83], v78 offset0:32 offset1:34\n\t" \
  "ds_read2_b32 v[84:85], v78 offset0:64 offset1:66\n\t" \
  "ds_read2_b32 v[86:87], v78 offset0:96 offset1:98\n\t" \
  "ds_read2_b32 v[88:89], v79 offset0:0 offset1:2\n\t" \
  "ds_read2_b32 v[90:91], v79 offset0:32 offset1:34\n\t" \
  "ds_read2_b32 v[92:93], v79 offset0:64 offset1:66\n\t" \
  "ds_read2_b32 v[94:95], v79 offset0:96 offset1:98\n\t" \
  "s_add_u32 m0, s54, 0\n\t" \
  "s_mov_b32 s60, 2621440\n\t" \
  "buffer_load_dwordx4 v71, %[Wk], s60 offen lds\n\t" \
  "s_add_u32 m0, s54, 1024\n\t" \
  "s_mov_b32 s60, 2654208\n\t" \
  "buffer_load_dwordx4 v71, %[Wk], s60 offen lds\n\t" \
  "s_add_u32 m0, s54, 2048\n\t" \
  "s_mov_b32 s60, 2686976\n\t" \
  "buffer_load_dwordx4 v71, %[Wk], s60 offen lds\n\t" \
  "s_add_u32 m0, s54, 3072\n\t" \
  "s_mov_b32 s60, 2719744\n\t" \
  "buffer_load_dwordx4 v71, %[Wk], s60 offen lds\n\t" \
  "s_waitcnt lgkmcnt(6)\n\t" \
  "v_perm_b32 v224, v82, v80, s62\n\t" \
  "v_perm_b32 v225, v82, v80, s63\n\t" \
  "s_waitcnt lgkmcnt(4)\n\t" \
  "v_perm_b32 v226, v86, v84, s62\n\t" \
  "v_perm_b32 v227, v86, v84, s63\n\t" \
  "s_waitcnt lgkmcnt(2)\n\t" \
  "v_perm_b32 v228, v90, v88, s62\n\t" \
  "v_perm_b32 v229, v90, v88, s63\n\t" \
  "s_waitcnt lgkmcnt(0)\n\t" \
  "v_perm_b32 v230, v94, v92, s62\n\t" \
  "v_perm_b32 v231, v94, v92, s63\n\t" \
  "v_perm_b32 v120, v83, v81, s62\n\t" \
  "v_perm_b32 v121, v83, v81, s63\n\t" \
  "v_perm_b32 v122, v87, v85, s62\n\t" \
  "v_perm_b32 v123, v87, v85, s63\n\t" \
  "v_perm_b32 v124, v91, v89, s62\n\t" \
  "v_perm_b32 v125, v91, v89, s63\n\t" \
  "v_perm_b32 v126, v95, v93, s62\n\t" \
  "v_perm_b32 v127, v95, v93, s63\n\t" \
  "ds_write_b128 v70, v[120:123] offset:32768\n\t" \
  "ds_write_b128 v70, v[124:127] offset:33792\n\t" \
  "v_add_u32 v78, 53248, v66\n\t" \
  "s_waitcnt vmcnt(12) lgkmcnt(0)\n\t" \
  "s_barrier\n\t" \
  "ds_read_b64_tr_b16 a[192:193], v78 offset:0\n\t" \
  "ds_read_b64_tr_b16 a[194:195], v78 offset:1024\n\t" \
  "ds_read_b64_tr_b16 a[196:197], v78 offset:8192\n\t" \
  "ds_read_b64_tr_b16 a[198:199], v78 offset:9216\n\t" \
  "ds_read_b64_tr_b16 v[48:49], v78 offset:32\n\t" \
  "ds_read_b64_tr_b16 v[50:51], v78 offset:1056\n\t" \
  "ds_read_b64_tr_b16 v[52:53], v78 offset:8224\n\t" \
  "ds_read_b64_tr_b16 v[54:55], v78 offset:9248\n\t" \
  "s_add_u32 m0, s54, 16384\n\t" \
  "s_mov_b32 s60, 2621440\n\t" \
  "buffer_load_dwordx4 v71, %[Wv], s60 offen lds\n\t" \
  "s_add_u32 m0, s54, 17408\n\t" \
  "s_mov_b32 s60, 2654208\n\t" \
  "buffer_load_dwordx4 v71, %[Wv], s60 offen lds\n\t" \
  "s_add_u32 m0, s54, 18432\n\t" \
  "s_mov_b32 s60, 2686976\n\t" \
  "buffer_load_dwordx4 v71, %[Wv], s60 offen lds\n\t" \
  "s_add_u32 m0, s54, 19456\n\t" \
  "s_mov_b32 s60, 2719744\n\t" \
  "buffer_load_dwordx4 v71, %[Wv], s60 offen lds\n\t" \
  "v_add_u32 v78, 90112, v65\n\t" \
  "s_waitcnt vmcnt(12) lgkmcnt(0)\n\t" \
  "s_barrier\n\t" \
  "ds_read_b64_tr_b16 a[200:201], v78 offset:0\n\t" \
  "ds_read_b64_tr_b16 a[202:203], v78 offset:1024\n\t" \
  "ds_read_b64_tr_b16 a[204:205], v78 offset:8192\n\t" \
  "ds_read_b64_tr_b16 a[206:207], v78 offset:9216\n\t" \
  "ds_read_b64_tr_b16 v[24:25], v78 offset:8\n\t" \
  "ds_read_b64_tr_b16 v[26:27], v78 offset:1032\n\t" \
  "ds_read_b64_tr_b16 v[28:29], v78 offset:8200\n\t" \
  "ds_read_b64_tr_b16 v[30:31], v78 offset:9224\n\t" \
  "s_add_u32 m0, s54, 53248\n\t" \
  "s_mov_b32 s60, 3145728\n\t" \
  "buffer_load_dwordx4 v71, %[Wq], s60 offen lds\n\t" \
  "s_add_u32 m0, s54, 54272\n\t" \
  "s_mov_b32 s60, 3178496\n\t" \
  "buffer_load_dwordx4 v71, %[Wq], s60 offen lds\n\t" \
  "s_add_u32 m0, s54, 55296\n\t" \
  "s_mov_b32 s60, 3211264\n\t" \
  "buffer_load_dwordx4 v71, %[Wq], s60 offen lds\n\t" \
  "s_add_u32 m0, s54, 56320\n\t" \
  "s_mov_b32 s60, 3244032\n\t" \
  "buffer_load_dwordx4 v71, %[Wq], s60 offen lds\n\t" \
  "v_add_u32 v78, 126976, v67\n\t" \
  "s_waitcnt vmcnt(12) lgkmcnt(0)\n\t" \
  "s_barrier\n\t" \
  "ds_read_u16 v80, v78 offset:0\n\t" \
  "ds_read_u16 v81, v78 offset:128\n\t" \
  "ds_read_u16 v82, v78 offset:4096\n\t" \
  "ds_read_u16 v83, v78 offset:4224\n\t" \
  "ds_read_u16 v84, v78 offset:256\n\t" \
  "ds_read_u16 v85, v78 offset:384\n\t" \
  "ds_read_u16 v86, v78 offset:4352\n\t" \
  "ds_read_u16 v87, v78 offset:4480\n\t" \
  "ds_read_u16 v88, v78 offset:1024\n\t" \
  "ds_read_u16 v89, v78 offset:1152\n\t" \
  "ds_read_u16 v90, v78 offset:5120\n\t" \
  "ds_read_u16 v91, v78 offset:5248\n\t" \
  "ds_read_u16 v92, v78 offset:1280\n\t" \
  "ds_read_u16 v93, v78 offset:1408\n\t" \
  "ds_read_u16 v94, v78 offset:5376\n\t" \
  "ds_read_u16 v95, v78 offset:5504\n\t" \
  "ds_read_u16 v96, v78 offset:8192\n\t" \
  "ds_read_u16 v97, v78 offset:8320\n\t" \
  "ds_read_u16 v98, v78 offset:12288\n\t" \
  "ds_read_u16 v99, v78 offset:12416\n\t" \
  "ds_read_u16 v100, v78 offset:8448\n\t" \
  "ds_read_u16 v101, v78 offset:8576\n\t" \
  "ds_read_u16 v102, v78 offset:12544\n\t" \
  "ds_read_u16 v103, v78 offset:12672\n\t" \
  "ds_read_u16 v104, v78 offset:9216\n\t" \
  "ds_read_u16 v105, v78 offset:9344\n\t" \
  "ds_read_u16 v106, v78 offset:13312\n\t" \
  "ds_read_u16 v107, v78 offset:13440\n\t" \
  "ds_read_u16 v108, v78 offset:9472\n\t" \
  "ds_read_u16 v109, v78 offset:9600\n\t" \
  "ds_read_u16 v110, v78 offset:13568\n\t" \
  "ds_read_u16 v111, v78 offset:13696\n\t" \
  "s_add_u32 m0, s54, 90112\n\t" \
  "s_mov_b32 s60, 3145728\n\t" \
  "buffer_load_dwordx4 v71, %[Wk], s60 offen lds\n\t" \
  "s_add_u32 m0, s54, 91136\n\t" \
  "s_mov_b32 s60, 3178496\n\t" \
  "buffer_load_dwordx4 v71, %[Wk], s60 offen lds\n\t" \
  "s_add_u32 m0, s54, 92160\n\t" \
  "s_mov_b32 s60, 3211264\n\t" \
  "buffer_load_dwordx4 v71, %[Wk], s60 offen lds\n\t" \
  "s_add_u32 m0, s54, 93184\n\t" \
  "s_mov_b32 s60, 3244032\n\t" \
  "buffer_load_dwordx4 v71, %[Wk], s60 offen lds\n\t" \
  "s_waitcnt lgkmcnt(0)\n\t" \
  "v_lshl_or_b32 v248, v81, 16, v80\n\t" \
  "v_lshl_or_b32 v249, v83, 16, v82\n\t" \
  "v_lshl_or_b32 v250, v85, 16, v84\n\t" \
  "v_lshl_or_b32 v251, v87, 16, v86\n\t" \
  "v_lshl_or_b32 v252, v89, 16, v88\n\t" \
  "v_lshl_or_b32 v253, v91, 16, v90\n\t" \
  "v_lshl_or_b32 v254, v93, 16, v92\n\t" \
  "v_lshl_or_b32 v255, v95, 16, v94\n\t" \
  "v_lshl_or_b32 v112, v97, 16, v96\n\t" \
  "v_lshl_or_b32 v113, v99, 16, v98\n\t" \
  "v_lshl_or_b32 v114, v101, 16, v100\n\t" \
  "v_lshl_or_b32 v115, v103, 16, v102\n\t" \
  "v_lshl_or_b32 v116, v105, 16, v104\n\t" \
  "v_lshl_or_b32 v117, v107, 16, v106\n\t" \
  "v_lshl_or_b32 v118, v109, 16, v108\n\t" \
  "v_lshl_or_b32 v119, v111, 16, v110\n\t" \
  "ds_write_b128 v70, v[112:115] offset:34816\n\t" \
  "ds_write_b128 v70, v[116:119] offset:35840\n\t" \
  "v_add_u32 v78, 0, v65\n\t" \
  "s_waitcnt vmcnt(12) lgkmcnt(0)\n\t" \
  "s_barrier\n\t" \
  "ds_read_b64_tr_b16 a[208:209], v78 offset:0\n\t" \
  "ds_read_b64_tr_b16 a[210:211], v78 offset:1024\n\t" \
  "ds_read_b64_tr_b16 a[212:213], v78 offset:8192\n\t" \
  "ds_read_b64_tr_b16 a[214:215], v78 offset:9216\n\t" \
  "ds_read_b64_tr_b16 a[216:217], v78 offset:8\n\t" \
  "ds_read_b64_tr_b16 a[218:219], v78 offset:1032\n\t" \
  "ds_read_b64_tr_b16 a[220:221], v78 offset:8200\n\t" \
  "ds_read_b64_tr_b16 a[222:223], v78 offset:9224\n\t" \
  "s_add_u32 m0, s54, 126976\n\t" \
  "s_mov_b32 s60, 3145728\n\t" \
  "buffer_load_dwordx4 v71, %[Wv], s60 offen lds\n\t" \
  "s_add_u32 m0, s54, 128000\n\t" \
  "s_mov_b32 s60, 3178496\n\t" \
  "buffer_load_dwordx4 v71, %[Wv], s60 offen lds\n\t" \
  "s_add_u32 m0, s54, 129024\n\t" \
  "s_mov_b32 s60, 3211264\n\t" \
  "buffer_load_dwordx4 v71, %[Wv], s60 offen lds\n\t" \
  "s_add_u32 m0, s54, 130048\n\t" \
  "s_mov_b32 s60, 3244032\n\t" \
  "buffer_load_dwordx4 v71, %[Wv], s60 offen lds\n\t" \
  "v_add_u32 v78, 16384, v66\n\t" \
  "s_waitcnt vmcnt(12) lgkmcnt(0)\n\t" \
  "s_barrier\n\t" \
  "ds_read_b64_tr_b16 a[224:225], v78 offset:0\n\t" \
  "ds_read_b64_tr_b16 a[226:227], v78 offset:1024\n\t" \
  "ds_read_b64_tr_b16 a[228:229], v78 offset:8192\n\t" \
  "ds_read_b64_tr_b16 a[230:231], v78 offset:9216\n\t" \
  "ds_read_b64_tr_b16 a[232:233], v78 offset:32\n\t" \
  "ds_read_b64_tr_b16 a[234:235], v78 offset:1056\n\t" \
  "ds_read_b64_tr_b16 a[236:237], v78 offset:8224\n\t" \
  "ds_read_b64_tr_b16 a[238:239], v78 offset:9248\n\t" \
  "s_add_u32 m0, s54, 0\n\t" \
  "s_mov_b32 s60, 3670016\n\t" \
  "buffer_load_dwordx4 v71, %[Wq], s60 offen lds\n\t" \
  "s_add_u32 m0, s54, 1024\n\t" \
  "s_mov_b32 s60, 3702784\n\t" \
  "buffer_load_dwordx4 v71, %[Wq], s60 offen lds\n\t" \
  "s_add_u32 m0, s54, 2048\n\t" \
  "s_mov_b32 s60, 3735552\n\t" \
  "buffer_load_dwordx4 v71, %[Wq], s60 offen lds\n\t" \
  "s_add_u32 m0, s54, 3072\n\t" \
  "s_mov_b32 s60, 3768320\n\t" \
  "buffer_load_dwordx4 v71, %[Wq], s60 offen lds\n\t" \
  "v_add_u32 v78, 53248, v65\n\t" \
  "s_waitcnt vmcnt(12) lgkmcnt(0)\n\t" \
  "s_barrier\n\t" \
  "ds_read_b64_tr_b16 a[240:241], v78 offset:0\n\t" \
  "ds_read_b64_tr_b16 a[242:243], v78 offset:1024\n\t" \
  "ds_read_b64_tr_b16 a[244:245], v78 offset:8192\n\t" \
  "ds_read_b64_tr_b16 a[246:247], v78 offset:9216\n\t" \
  "ds_read_b64_tr_b16 a[248:249], v78 offset:8\n\t" \
  "ds_read_b64_tr_b16 a[250:251], v78 offset:1032\n\t" \
  "ds_read_b64_tr_b16 a[252:253], v78 offset:8200\n\t" \
  "ds_read_b64_tr_b16 a[254:255], v78 offset:9224\n\t" \
  "s_add_u32 m0, s54, 16384\n\t" \
  "s_mov_b32 s60, 3670016\n\t" \
  "buffer_load_dwordx4 v71, %[Wk], s60 offen lds\n\t" \
  "s_add_u32 m0, s54, 17408\n\t" \
  "s_mov_b32 s60, 3702784\n\t" \
  "buffer_load_dwordx4 v71, %[Wk], s60 offen lds\n\t" \
  "s_add_u32 m0, s54, 18432\n\t" \
  "s_mov_b32 s60, 3735552\n\t" \
  "buffer_load_dwordx4 v71, %[Wk], s60 offen lds\n\t" \
  "s_add_u32 m0, s54, 19456\n\t" \
  "s_mov_b32 s60, 3768320\n\t" \
  "buffer_load_dwordx4 v71, %[Wk], s60 offen lds\n\t" \
  "v_add_u32 v78, 90112, v65\n\t" \
  "s_waitcnt vmcnt(12) lgkmcnt(0)\n\t" \
  "s_barrier\n\t" \
  "ds_read_b64_tr_b16 v[80:81], v78 offset:0\n\t" \
  "ds_read_b64_tr_b16 v[82:83], v78 offset:1024\n\t" \
  "ds_read_b64_tr_b16 v[84:85], v78 offset:8192\n\t" \
  "ds_read_b64_tr_b16 v[86:87], v78 offset:9216\n\t" \
  "ds_read_b64_tr_b16 v[88:89], v78 offset:8\n\t" \
  "ds_read_b64_tr_b16 v[90:91], v78 offset:1032\n\t" \
  "ds_read_b64_tr_b16 v[92:93], v78 offset:8200\n\t" \
  "ds_read_b64_tr_b16 v[94:95], v78 offset:9224\n\t" \
  "s_add_u32 m0, s54, 53248\n\t" \
  "s_mov_b32 s60, 3670016\n\t" \
  "buffer_load_dwordx4 v71, %[Wv], s60 offen lds\n\t" \
  "s_add_u32 m0, s54, 54272\n\t" \
  "s_mov_b32 s60, 3702784\n\t" \
  "buffer_load_dwordx4 v71, %[Wv], s60 offen lds\n\t" \
  "s_add_u32 m0, s54, 55296\n\t" \
  "s_mov_b32 s60, 3735552\n\t" \
  "buffer_load_dwordx4 v71, %[Wv], s60 offen lds\n\t" \
  "s_add_u32 m0, s54, 56320\n\t" \
  "s_mov_b32 s60, 3768320\n\t" \
  "buffer_load_dwordx4 v71, %[Wv], s60 offen lds\n\t" \
  "v_add_u32 v78, 126976, v66\n\t" \
  "s_waitcnt vmcnt(12) lgkmcnt(0)\n\t" \
  "s_barrier\n\t" \
  "ds_read_b64_tr_b16 v[96:97], v78 offset:0\n\t" \
  "ds_read_b64_tr_b16 v[98:99], v78 offset:1024\n\t" \
  "ds_read_b64_tr_b16 v[100:101], v78 offset:8192\n\t" \
  "ds_read_b64_tr_b16 v[102:103], v78 offset:9216\n\t" \
  "ds_read_b64_tr_b16 v[104:105], v78 offset:32\n\t" \
  "ds_read_b64_tr_b16 v[106:107], v78 offset:1056\n\t" \
  "ds_read_b64_tr_b16 v[108:109], v78 offset:8224\n\t" \
  "ds_read_b64_tr_b16 v[110:111], v78 offset:9248\n\t" \
  "s_add_u32 m0, s54, 90112\n\t" \
  "s_mov_b32 s60, 4194304\n\t" \
  "buffer_load_dwordx4 v71, %[Wq], s60 offen lds\n\t" \
  "s_add_u32 m0, s54, 91136\n\t" \
  "s_mov_b32 s60, 4227072\n\t" \
  "buffer_load_dwordx4 v71, %[Wq], s60 offen lds\n\t" \
  "s_add_u32 m0, s54, 92160\n\t" \
  "s_mov_b32 s60, 4259840\n\t" \
  "buffer_load_dwordx4 v71, %[Wq], s60 offen lds\n\t" \
  "s_add_u32 m0, s54, 93184\n\t" \
  "s_mov_b32 s60, 4292608\n\t" \
  "buffer_load_dwordx4 v71, %[Wq], s60 offen lds\n\t" \
  "v_add_u32 v78, 0, v65\n\t" \
  "s_waitcnt vmcnt(12) lgkmcnt(0)\n\t" \
  "s_barrier\n\t" \
  "ds_read_b64_tr_b16 v[112:113], v78 offset:0\n\t" \
  "ds_read_b64_tr_b16 v[114:115], v78 offset:1024\n\t" \
  "ds_read_b64_tr_b16 v[116:117], v78 offset:8192\n\t" \
  "ds_read_b64_tr_b16 v[118:119], v78 offset:9216\n\t" \
  "ds_read_b64_tr_b16 v[120:121], v78 offset:8\n\t" \
  "ds_read_b64_tr_b16 v[122:123], v78 offset:1032\n\t" \
  "ds_read_b64_tr_b16 v[124:125], v78 offset:8200\n\t" \
  "ds_read_b64_tr_b16 v[126:127], v78 offset:9224\n\t" \
  "s_add_u32 m0, s54, 126976\n\t" \
  "s_mov_b32 s60, 4194304\n\t" \
  "buffer_load_dwordx4 v71, %[Wk], s60 offen lds\n\t" \
  "s_add_u32 m0, s54, 128000\n\t" \
  "s_mov_b32 s60, 4227072\n\t" \
  "buffer_load_dwordx4 v71, %[Wk], s60 offen lds\n\t" \
  "s_add_u32 m0, s54, 129024\n\t" \
  "s_mov_b32 s60, 4259840\n\t" \
  "buffer_load_dwordx4 v71, %[Wk], s60 offen lds\n\t" \
  "s_add_u32 m0, s54, 130048\n\t" \
  "s_mov_b32 s60, 4292608\n\t" \
  "buffer_load_dwordx4 v71, %[Wk], s60 offen lds\n\t" \
  "v_add_u32 v78, 16384, v65\n\t" \
  "s_waitcnt vmcnt(12) lgkmcnt(0)\n\t" \
  "s_barrier\n\t" \
  "ds_read_b64_tr_b16 v[128:129], v78 offset:0\n\t" \
  "ds_read_b64_tr_b16 v[130:131], v78 offset:1024\n\t" \
  "ds_read_b64_tr_b16 v[132:133], v78 offset:8192\n\t" \
  "ds_read_b64_tr_b16 v[134:135], v78 offset:9216\n\t" \
  "ds_read_b64_tr_b16 v[136:137], v78 offset:8\n\t" \
  "ds_read_b64_tr_b16 v[138:139], v78 offset:1032\n\t" \
  "ds_read_b64_tr_b16 v[140:141], v78 offset:8200\n\t" \
  "ds_read_b64_tr_b16 v[142:143], v78 offset:9224\n\t" \
  "s_add_u32 m0, s54, 0\n\t" \
  "s_mov_b32 s60, 4194304\n\t" \
  "buffer_load_dwordx4 v71, %[Wv], s60 offen lds\n\t" \
  "s_add_u32 m0, s54, 1024\n\t" \
  "s_mov_b32 s60, 4227072\n\t" \
  "buffer_load_dwordx4 v71, %[Wv], s60 offen lds\n\t" \
  "s_add_u32 m0, s54, 2048\n\t" \
  "s_mov_b32 s60, 4259840\n\t" \
  "buffer_load_dwordx4 v71, %[Wv], s60 offen lds\n\t" \
  "s_add_u32 m0, s54, 3072\n\t" \
  "s_mov_b32 s60, 4292608\n\t" \
  "buffer_load_dwordx4 v71, %[Wv], s60 offen lds\n\t" \
  "v_add_u32 v78, 53248, v66\n\t" \
  "s_waitcnt vmcnt(12) lgkmcnt(0)\n\t" \
  "s_barrier\n\t" \
  "ds_read_b64_tr_b16 v[144:145], v78 offset:0\n\t" \
  "ds_read_b64_tr_b16 v[146:147], v78 offset:1024\n\t" \
  "ds_read_b64_tr_b16 v[148:149], v78 offset:8192\n\t" \
  "ds_read_b64_tr_b16 v[150:151], v78 offset:9216\n\t" \
  "ds_read_b64_tr_b16 v[152:153], v78 offset:32\n\t" \
  "ds_read_b64_tr_b16 v[154:155], v78 offset:1056\n\t" \
  "ds_read_b64_tr_b16 v[156:157], v78 offset:8224\n\t" \
  "ds_read_b64_tr_b16 v[158:159], v78 offset:9248\n\t" \
  "v_add_u32 v78, 90112, v65\n\t" \
  "s_waitcnt vmcnt(8) lgkmcnt(0)\n\t" \
  "s_barrier\n\t" \
  "ds_read_b64_tr_b16 v[160:161], v78 offset:0\n\t" \
  "ds_read_b64_tr_b16 v[162:163], v78 offset:1024\n\t" \
  "ds_read_b64_tr_b16 v[164:165], v78 offset:8192\n\t" \
  "ds_read_b64_tr_b16 v[166:167], v78 offset:9216\n\t" \
  "ds_read_b64_tr_b16 v[168:169], v78 offset:8\n\t" \
  "ds_read_b64_tr_b16 v[170:171], v78 offset:1032\n\t" \
  "ds_read_b64_tr_b16 v[172:173], v78 offset:8200\n\t" \
  "ds_read_b64_tr_b16 v[174:175], v78 offset:9224\n\t" \
  "v_add_u32 v78, 126976, v65\n\t" \
  "s_waitcnt vmcnt(4) lgkmcnt(0)\n\t" \
  "s_barrier\n\t" \
  "ds_read_b64_tr_b16 v[40:41], v78 offset:0\n\t" \
  "ds_read_b64_tr_b16 v[42:43], v78 offset:1024\n\t" \
  "ds_read_b64_tr_b16 v[44:45], v78 offset:8192\n\t" \
  "ds_read_b64_tr_b16 v[46:47], v78 offset:9216\n\t" \
  "ds_read_b64_tr_b16 v[176:177], v78 offset:8\n\t" \
  "ds_read_b64_tr_b16 v[178:179], v78 offset:1032\n\t" \
  "ds_read_b64_tr_b16 v[180:181], v78 offset:8200\n\t" \
  "ds_read_b64_tr_b16 v[182:183], v78 offset:9224\n\t" \
  "v_add_u32 v78, 0, v66\n\t" \
  "s_waitcnt vmcnt(0) lgkmcnt(0)\n\t" \
  "s_barrier\n\t" \
  "ds_read_b64_tr_b16 v[184:185], v78 offset:0\n\t" \
  "ds_read_b64_tr_b16 v[186:187], v78 offset:1024\n\t" \
  "ds_read_b64_tr_b16 v[188:189], v78 offset:8192\n\t" \
  "ds_read_b64_tr_b16 v[190:191], v78 offset:9216\n\t" \
  "ds_read_b64_tr_b16 v[56:57], v78 offset:32\n\t" \
  "ds_read_b64_tr_b16 v[58:59], v78 offset:1056\n\t" \
  "ds_read_b64_tr_b16 v[60:61], v78 offset:8224\n\t" \
  "ds_read_b64_tr_b16 v[62:63], v78 offset:9248\n\t" \
  "s_waitcnt lgkmcnt(0)\n\t" \
  "s_barrier\n\t" \
  "ds_write_b128 v70, v[0:3] offset:0\n\t" \
  "ds_write_b128 v70, v[4:7] offset:1024\n\t" \
  "ds_write_b128 v70, v[8:11] offset:2048\n\t" \
  "ds_write_b128 v70, v[12:15] offset:3072\n\t" \
  "ds_write_b128 v70, v[16:19] offset:4096\n\t" \
  "ds_write_b128 v70, v[20:23] offset:5120\n\t" \
  "ds_write_b128 v70, v[24:27] offset:6144\n\t" \
  "ds_write_b128 v70, v[28:31] offset:7168\n\t" \
  "ds_write_b128 v70, v[32:35] offset:8192\n\t" \
  "ds_write_b128 v70, v[36:39] offset:9216\n\t" \
  "ds_write_b128 v70, v[40:43] offset:10240\n\t" \
  "ds_write_b128 v70, v[44:47] offset:11264\n\t" \
  "ds_write_b128 v70, v[48:51] offset:12288\n\t" \
  "ds_write_b128 v70, v[52:55] offset:13312\n\t" \
  "ds_write_b128 v70, v[56:59] offset:14336\n\t" \
  "ds_write_b128 v70, v[60:63] offset:15360\n\t" \
  "s_waitcnt lgkmcnt(0)\n\t" \
  "v_and_b32 v72, 1, v72\n\t" \
  "v_and_b32 v74, 1, v74\n\t" \
  "v_cmp_ne_u32 s[90:91], s32, v72\n\t" \
  "v_cmp_ne_u32 vcc, s32, v74\n\t" \
  "s_nop 1\n\t" \
  "s_mov_b32 s96, vcc_lo\n\t" \
  "v_and_b32 v73, 32, v69\n\t" \
  "v_lshl_add_u32 v73, v73, 5, v69\n\t" \
  "s_mov_b32 s35, s30\n\t" \
  "s_cmp_lt_u32 s32, " CH_DRYL "\n\t" \
  "s_cbranch_scc1 LINI_%=\n\t" \
  "s_mov_b32 s30, 0\n\t" \
  "s_mov_b32 s34, 0\n\t" \
  "s_cmp_eq_u32 s40, 0\n\t" \
  "s_cbranch_scc1 LW0_%=\n\t" \
  "s_barrier\n\t" \
  "s_cmp_eq_u32 s40, 1\n\t" \
  "s_cbranch_scc1 LG1_%=\n\t" \
  "s_cmp_eq_u32 s40, 2\n\t" \
  "s_cbranch_scc1 LG2_%=\n\t" \
  "s_branch LG3_%=\n\t" \
  "LINI_%=:\n\t" \
  "s_waitcnt vmcnt(0) lgkmcnt(0)\n\t" \
  "s_mov_b32 s30, s35\n\t" \
  "s_movk_i32 s34, 1000\n\t" \
  "s_mov_b32 s48, -1\n\t" \
  "s_add_u32 s97, s41, s53\n\t" \
  "s_add_u32 s98, s37, s53\n\t" \
  "s_add_u32 s82, s48, 0\n\t" \
  "s_lshr_b64 s[60:61], s[90:91], s82\n\t" \
  "s_and_b32 s60, s60, 1\n\t" \
  "s_lshl_b32 s60, s60, 13\n\t" \
  "s_add_u32 s82, s82, " CH_SG0 "\n\t" \
  "s_lshl_b32 s82, s82, 21\n\t" \
  "s_or_b32 s82, s82, s60\n\t" \
  "s_add_u32 s50, s82, s38\n\t" \
  "s_add_u32 s69, s82, s39\n\t" \
  "s_add_u32 s82, s48, 1\n\t" \
  "s_lshr_b64 s[60:61], s[90:91], s82\n\t" \
  "s_and_b32 s60, s60, 1\n\t" \
  "s_lshl_b32 s60, s60, 13\n\t" \
  "s_add_u32 s82, s82, " CH_SG0 "\n\t" \
  "s_lshl_b32 s82, s82, 21\n\t" \
  "s_or_b32 s82, s82, s60\n\t" \
  "s_add_u32 s52, s82, s43\n\t" \
  "s_add_u32 s82, s48, " CH_ARMD "\n\t" \
  "s_lshr_b64 s[60:61], s[90:91], s82\n\t" \
  "s_and_b32 s60, s60, 1\n\t" \
  "s_lshl_b32 s60, s60, 13\n\t" \
  "s_add_u32 s82, s82, " CH_SG0 "\n\t" \
  "s_lshl_b32 s82, s82, 21\n\t" \
  "s_or_b32 s82, s82, s60\n\t" \
  "s_add_u32 s85, s82, s45\n\t" \
  "s_cmp_lt_i32 s48, " CH_ARMS "\n\t" \
  "s_cselect_b32 s85, s27, s85\n\t" \
  "v_and_b32 v37, 0x30, v69\n\t" \
  "v_add_u32 v37, s52, v37\n\t" \
  "s_mov_b32 s48, 0\n\t" \
  "s_mov_b32 s86, 1\n\t" \
  "s_and_b32 s49, s96, 1\n\t" \
  "s_lshl_b32 s49, s49, 13\n\t" \
  "s_add_u32 s53, s49, " CH_SLOT "\n\t" \
  "s_movk_i32 s60, 124\n\t" \
  "s_cmp_eq_u32 s40, 3\n\t" \
  "s_cselect_b32 s81, 76, s60\n\t" \
  "s_add_u32 s82, s48, 0\n\t" \
  "s_lshr_b64 s[60:61], s[90:91], s82\n\t" \
  "s_and_b32 s60, s60, 1\n\t" \
  "s_lshl_b32 s60, s60, 13\n\t" \
  "s_add_u32 s82, s82, " CH_SG0 "\n\t" \
  "s_lshl_b32 s82, s82, 21\n\t" \
  "s_or_b32 s82, s82, s60\n\t" \
  "s_add_u32 s50, s82, s38\n\t" \
  "s_add_u32 s69, s82, s39\n\t" \
  "s_add_u32 s51, s82, s43\n\t" \
  "s_add_u32 s97, s41, s53\n\t" \
  "s_add_u32 s98, s37, s53\n\t" \
  "s_add_u32 s82, s48, 0\n\t" \
  "s_lshr_b64 s[60:61], s[90:91], s82\n\t" \
  "s_and_b32 s60, s60, 1\n\t" \
  "s_lshl_b32 s60, s60, 13\n\t" \
  "s_add_u32 s82, s82, " CH_SG0 "\n\t" \
  "s_lshl_b32 s82, s82, 21\n\t" \
  "s_or_b32 s82, s82, s60\n\t" \
  "s_add_u32 s50, s82, s38\n\t" \
  "s_add_u32 s69, s82, s39\n\t" \
  "s_add_u32 s82, s48, 1\n\t" \
  "s_lshr_b64 s[60:61], s[90:91], s82\n\t" \
  "s_and_b32 s60, s60, 1\n\t" \
  "s_lshl_b32 s60, s60, 13\n\t" \
  "s_add_u32 s82, s82, " CH_SG0 "\n\t" \
  "s_lshl_b32 s82, s82, 21\n\t" \
  "s_or_b32 s82, s82, s60\n\t" \
  "s_add_u32 s52, s82, s43\n\t" \
  "s_add_u32 s82, s48, " CH_ARMD "\n\t" \
  "s_lshr_b64 s[60:61], s[90:91], s82\n\t" \
  "s_and_b32 s60, s60, 1\n\t" \
  "s_lshl_b32 s60, s60, 13\n\t" \
  "s_add_u32 s82, s82, " CH_SG0 "\n\t" \
  "s_lshl_b32 s82, s82, 21\n\t" \
  "s_or_b32 s82, s82, s60\n\t" \
  "s_add_u32 s85, s82, s45\n\t" \
  "s_cmp_lt_i32 s48, " CH_ARMS "\n\t" \
  "s_cselect_b32 s85, s27, s85\n\t" \
  "s_memrealtime s[62:63]\n\t" \
  "s_waitcnt lgkmcnt(0)\n\t" \
  "s_mov_b32 s65, s62\n\t" \
  "s_sub_u32 s47, s62, 1000\n\t" \
  "s_mov_b64 vcc, -1\n\t" \
  "s_cmp_eq_u32 s40, 0\n\t" \
  "s_cbranch_scc1 LW0_%=\n\t" \
  "s_barrier\n\t" \
  "s_cmp_eq_u32 s40, 1\n\t" \
  "s_cbranch_scc1 LG1_%=\n\t" \
  "s_cmp_eq_u32 s40, 2\n\t" \
  "s_cbranch_scc1 LG2_%=\n\t" \
  "s_branch LTK_%=\n\t" \
  ".p2align 6\n\t" \
  "LW0_%=:\n\t" \
  "v_lshlrev_b32 v78, 2, v69\n\t" \
  "v_and_b32 v78, 0x300, v78\n\t" \
  "v_lshrrev_b32 v79, 8, v69\n\t" \
  "v_lshl_add_u32 v78, v79, 6, v78\n\t" \
  "v_and_b32 v79, 48, v69\n\t" \
  "v_add_u32 v78, v78, v79\n\t" \
  "s_add_u32 s98, s49, s42\n\t" \
  "s_add_u32 s97, s98, 32768\n\t" \
  "s_add_u32 s99, s98, 65536\n\t" \
  "s_barrier\n\t" \
  "s_memrealtime s[62:63]\n\t" \
  "s_waitcnt lgkmcnt(0)\n\t" \
  "s_add_u32 s62, s62, " CH_DLL2 "\n\t" \
  "s_sub_i32 s63, s62, s65\n\t" \
  "s_cmp_gt_i32 s63, " CH_DLT "\n\t" \
  "s_cselect_b32 s60, " CH_DLY ", " CH_DLX "\n\t" \
  "s_add_u32 s62, s62, s60\n\t" \
  "s_cmp_gt_i32 s63, 0\n\t" \
  "s_cselect_b32 s65, s62, s65\n\t" \
  "s_mov_b32 m0, 0\n\t" \
  "s_movk_i32 s60, 8192\n\t" \
  "LWT_%=:\n\t" \
  "s_memrealtime s[62:63]\n\t" \
  "s_sub_u32 s60, s60, 1\n\t" \
  "s_cselect_b32 s61, 0, -1\n\t" \
  "s_waitcnt lgkmcnt(0)\n\t" \
  "s_sub_i32 s63, s62, s65\n\t" \
  "s_and_b32 s63, s63, s61\n\t" \
  "s_cmp_lt_i32 s63, " CH_FIN "\n\t" \
  "s_cbranch_scc1 LWT_%=\n\t" \
  "s_cmp_lt_i32 s63, 0\n\t" \
  "s_cbranch_scc1 LWT_%=f\n\t" \
  "LWT_%=g:\n\t" \
  "LDMA_%=:\n\t" \
  "buffer_load_dwordx4 v78, s[28:31], s98 offen offset:0 sc1 lds\n\t" \
  "buffer_load_dwordx4 v78, s[28:31], s98 offen offset:1024 sc1 lds\n\t" \
  "buffer_load_dwordx4 v78, s[28:31], s98 offen offset:2048 sc1 lds\n\t" \
  "buffer_load_dwordx4 v78, s[28:31], s98 offen offset:3072 sc1 lds\n\t" \
  "s_movk_i32 m0, 4096\n\t" \
  "s_mov_b32 s66, s62\n\t" \
  "buffer_load_dwordx4 v78, s[28:31], s97 offen offset:0 sc1 lds\n\t" \
  "buffer_load_dwordx4 v78, s[28:31], s97 offen offset:1024 sc1 lds\n\t" \
  "buffer_load_dwordx4 v78, s[28:31], s97 offen offset:2048 sc1 lds\n\t" \
  "buffer_load_dwordx4 v78, s[28:31], s97 offen offset:3072 sc1 lds\n\t" \
  "s_movk_i32 m0, 8192\n\t" \
  "s_nop 0\n\t" \
  "buffer_load_dwordx4 v69, s[28:31], s99 offen sc1 lds\n\t" \
  ".p2align 6\n\t" \
  "LG0_%=:\n\t" \
  "s_barrier\n\t" \
  "buffer_load_dwordx4 v[52:55], v69, s[28:31], s50 offen sc1\n\t" \
  "buffer_load_dwordx4 v[56:59], v69, s[28:31], s50 offen offset:1024 sc1\n\t" \
  "s_add_u32 s64, s50, 16384\n\t" \
  "s_movk_i32 m0, 11264\n\t" \
  "v_lshlrev_b32 v75, 1, v69\n\t" \
  "buffer_load_dword v75, s[28:31], s64 offen sc1 lds\n\t" \
  "s_memrealtime s[62:63]\n\t" \
  "s_waitcnt lgkmcnt(0)\n\t" \
  "s_sub_u32 s62, s62, " CH_TKL "\n\t" \
  "s_sub_i32 s63, s62, s65\n\t" \
  "s_cmp_gt_i32 s63, 0\n\t" \
  "s_cselect_b32 s62, s62, s65\n\t" \
  "s_sub_i32 s63, s62, s47\n\t" \
  "s_cmp_gt_i32 s63, " CH_LATE "\n\t" \
  "s_cselect_b32 s71, " CH_SCHW ", s46\n\t" \
  "s_add_u32 s61, s47, " CH_LATE "\n\t" \
  "s_cmp_gt_i32 s63, " CH_LATE "\n\t" \
  "s_cselect_b32 s62, s62, s61\n\t" \
  "s_add_u32 s71, s71, s53\n\t" \
  "s_add_u32 s60, s62, s81\n\t" \
  "s_add_u32 s60, s60, " CH_DL1L "\n\t" \
  "s_lshl_b32 s60, s60, 8\n\t" \
  "s_or_b32 s60, s60, " CH_B0 "\n\t" \
  "s_cmp_lt_u32 s48, " CH_ES "\n\t" \
  "s_cselect_b32 s61, " CH_EX ", 0\n\t" \
  "s_add_u32 s60, s60, s61\n\t" \
  "s_not_b32 s60, s60\n\t" \
  "v_mov_b32 v76, s60\n\t" \
  "s_lshr_b32 s60, s46, 31\n\t" \
  "s_or_b32 s60, s60, s68\n\t" \
  "s_cselect_b32 s25, 0, 1\n\t" \
  "s_mov_b64 exec, s[24:25]\n\t" \
  "buffer_atomic_umin v76, v69, s[28:31], s71 offen sc1\n\t" \
  "s_mov_b64 exec, -1\n\t" \
  "ds_read_b128 v[44:47], v70 offset:0\n\t" \
  "ds_read_b128 v[48:51], v70 offset:1024\n\t" \
  "ds_read_b128 v[60:63], v70 offset:2048\n\t" \
  "ds_read_b128 v[64:67], v70 offset:3072\n\t" \
  "v_mov_b64 v[36:37], 0\n\t" \
  "v_mov_b64 v[38:39], 0\n\t" \
  "v_mov_b64 v[40:41], 0\n\t" \
  "v_mov_b64 v[42:43], 0\n\t" \
  "v_lshrrev_b32 v71, 1, v69\n\t" \
  "v_lshrrev_b32 v72, 3, v69\n\t" \
  "v_and_b32 v72, 0x78, v72\n\t" \
  "v_and_b32 v74, 0x30, v69\n\t" \
  "v_lshl_add_u32 v72, v74, 3, v72\n\t" \
  "s_add_u32 s54, s53, s36\n\t" \
  "s_add_u32 s55, s54, 524288\n\t" \
  "s_add_u32 s54, s54, 0\n\t" \
  "s_add_u32 s75, s54, 262144\n\t" \
  "s_add_u32 s80, s53, s26\n\t" \
  "s_or_b32 s87, s53, 0x1fff80\n\t" \
  "s_add_u32 s87, s87, 2097152\n\t" \
  "s_waitcnt vmcnt(9)\n\t" \
  "s_barrier\n\t" \
  "ds_read_b128 v[0:3], v69 offset:0\n\t" \
  "ds_read_b128 v[4:7], v69 offset:1024\n\t" \
  "ds_read_b128 v[8:11], v69 offset:2048\n\t" \
  "ds_read_b128 v[12:15], v69 offset:3072\n\t" \
  "s_waitcnt lgkmcnt(3)\n\t" \
  "v_smfmac_f32_16x16x64_bf16 v[36:39], v[0:3], v[44:51], v68\n\t" \
  "ds_read_b128 v[44:47], v70 offset:4096\n\t" \
  "v_smfmac_f32_16x16x64_bf16 v[40:43], v[0:3], v[60:67], v68\n\t" \
  "ds_read_b128 v[48:51], v70 offset:5120\n\t" \
  "s_waitcnt lgkmcnt(4)\n\t" \
  "v_smfmac_f32_16x16x64_bf16 v[36:39], v[4:7], a[32:39], v68\n\t" \
  "s_waitcnt vmcnt(4)\n\t" \
  "s_barrier\n\t" \
  "ds_read_b128 v[16:19], v69 offset:4096\n\t" \
  "v_smfmac_f32_16x16x64_bf16 v[40:43], v[4:7], a[40:47], v68\n\t" \
  "ds_read_b128 v[20:23], v69 offset:5120\n\t" \
  "s_waitcnt lgkmcnt(5)\n\t" \
  "v_smfmac_f32_16x16x64_bf16 v[36:39], v[8:11], a[80:87], v68\n\t" \
  "ds_read_b128 v[24:27], v69 offset:6144\n\t" \
  "v_smfmac_f32_16x16x64_bf16 v[40:43], v[8:11], a[88:95], v68\n\t" \
  "ds_read_b128 v[28:31], v69 offset:7168\n\t" \
  "s_waitcnt lgkmcnt(6)\n\t" \
  "v_smfmac_f32_16x16x64_bf16 v[36:39], v[12:15], a[120:127], v68\n\t" \
  "ds_read_b128 v[32:35], v69 offset:8192\n\t" \
  "s_waitcnt lgkmcnt(5)\n\t" \
  "v_smfmac_f32_16x16x64_bf16 v[40:43], v[12:15], v[44:51], v68\n\t" \
  "ds_read_b128 v[60:63], v70 offset:6144\n\t" \
  "s_waitcnt lgkmcnt(5)\n\t" \
  "v_smfmac_f32_16x16x64_bf16 v[36:39], v[16:19], a[160:167], v68\n\t" \
  "ds_read_b128 v[64:67], v70 offset:7168\n\t" \
  "v_smfmac_f32_16x16x64_bf16 v[40:43], v[16:19], a[168:175], v68\n\t" \
  "s_waitcnt lgkmcnt(5)\n\t" \
  "v_mov_b64 v[44:45], 0\n\t" \
  "v_smfmac_f32_16x16x64_bf16 v[36:39], v[20:23], a[200:207], v68\n\t" \
  "s_waitcnt lgkmcnt(4)\n\t" \
  "v_mov_b64 v[46:47], 0\n\t" \
  "v_smfmac_f32_16x16x64_bf16 v[36:39], v[24:27], a[240:247], v68\n\t" \
  "s_waitcnt lgkmcnt(2)\n\t" \
  "v_mov_b64 v[48:49], 0\n\t" \
  "v_smfmac_f32_16x16x64_bf16 v[36:39], v[28:31], v[112:119], v68\n\t" \
  "v_mov_b64 v[50:51], 0\n\t" \
  "v_smfmac_f32_16x16x64_bf16 v[36:39], v[32:35], v[160:167], v68\n\t" \
  "s_waitcnt lgkmcnt(0)\n\t" \
  "v_smfmac_f32_16x16x64_bf16 v[40:43], v[20:23], v[60:67], v68\n\t" \
  "ds_read_b128 v[60:63], v70 offset:8192\n\t" \
  "v_smfmac_f32_16x16x64_bf16 v[40:43], v[24:27], a[248:255], v68\n\t" \
  "ds_read_b128 v[64:67], v70 offset:9216\n\t" \
  "v_add_f32 v74, v36, v38\n\t" \
  "v_smfmac_f32_16x16x64_bf16 v[40:43], v[28:31], v[120:127], v68\n\t" \
  "v_add_f32 v37, v37, v39\n\t" \
  "v_smfmac_f32_16x16x64_bf16 v[40:43], v[32:35], v[168:175], v68\n\t" \
  "v_cmp_u_f32 vcc, v74, v37\n\t" \
  "v_smfmac_f32_16x16x64_bf16 v[44:47], v[0:3], a[0:7], v68\n\t" \
  "s_cbranch_vccnz LFAIL0_%=\n\t" \
  "LGO0_%=:\n\t" \
  "v_cvt_pk_bf16_f32 v74, v74, v37\n\t" \
  "v_smfmac_f32_16x16x64_bf16 v[44:47], v[4:7], a[48:55], v68\n\t" \
  "s_waitcnt lgkmcnt(0)\n\t" \
  "v_add_f32 v75, v40, v42\n\t" \
  "v_smfmac_f32_16x16x64_bf16 v[44:47], v[8:11], v[60:67], v68\n\t" \
  "ds_read_b128 v[60:63], v70 offset:10240\n\t" \
  "v_add_f32 v41, v41, v43\n\t" \
  "v_cvt_pk_bf16_f32 v75, v75, v41\n\t" \
  "v_smfmac_f32_16x16x64_bf16 v[44:47], v[12:15], a[128:135], v68\n\t" \
  "ds_write2st64_b32 v71, v74, v75 offset0:36 offset1:38\n\t" \
  "v_smfmac_f32_16x16x64_bf16 v[44:47], v[16:19], a[176:183], v68\n\t" \
  "ds_read_b128 v[64:67], v70 offset:11264\n\t" \
  "v_smfmac_f32_16x16x64_bf16 v[44:47], v[20:23], a[208:215], v68\n\t" \
  "v_smfmac_f32_16x16x64_bf16 v[44:47], v[24:27], v[80:87], v68\n\t" \
  "v_smfmac_f32_16x16x64_bf16 v[44:47], v[28:31], v[128:135], v68\n\t" \
  "s_waitcnt lgkmcnt(0)\n\t" \
  "v_smfmac_f32_16x16x64_bf16 v[44:47], v[32:35], v[60:67], v68\n\t" \
  "buffer_load_dwordx4 v[60:63], v69, s[28:31], s64 offen sc1\n\t" \
  "s_barrier\n\t" \
  "ds_read_b64_tr_b16 v[76:77], v72 offset:9216\n\t" \
  "v_smfmac_f32_16x16x64_bf16 v[48:51], v[0:3], a[8:15], v68\n\t" \
  "ds_read_b64_tr_b16 v[78:79], v72 offset:9728\n\t" \
  "v_smfmac_f32_16x16x64_bf16 v[48:51], v[4:7], a[56:63], v68\n\t" \
  "v_add_f32 v74, v44, v46\n\t" \
  "v_smfmac_f32_16x16x64_bf16 v[48:51], v[8:11], a[96:103], v68\n\t" \
  "v_add_f32 v45, v45, v47\n\t" \
  "v_smfmac_f32_16x16x64_bf16 v[48:51], v[12:15], a[136:143], v68\n\t" \
  "s_waitcnt vmcnt(4)\n\t" \
  "s_waitcnt lgkmcnt(0)\n\t" \
  "v_cvt_pk_bf16_f32 v74, v74, v45\n\t" \
  "v_mfma_f32_16x16x32_bf16 v[36:39], v[52:55], v[76:79], 0\n\t" \
  "buffer_load_dwordx4 v[64:67], v69, s[28:31], s64 offen offset:1024 sc1\n\t" \
  "s_waitcnt vmcnt(4)\n\t" \
  "v_mfma_f32_16x16x32_bf16 v[40:43], v[56:59], v[76:79], 0\n\t" \
  "v_mov_b64 v[52:53], 0\n\t" \
  "v_mov_b64 v[54:55], 0\n\t" \
  "v_smfmac_f32_16x16x64_bf16 v[48:51], v[16:19], a[184:191], v68\n\t" \
  "v_mov_b64 v[56:57], 0\n\t" \
  "v_mov_b64 v[58:59], 0\n\t" \
  "v_smfmac_f32_16x16x64_bf16 v[48:51], v[20:23], a[216:223], v68\n\t" \
  "ds_write_b32 v71, v74 offset:0\n\t" \
  "v_add_f32_dpp v36, v40, v36 quad_perm:[2,3,0,1] row_mask:0xf bank_mask:0xf\n\t" \
  "v_smfmac_f32_16x16x64_bf16 v[48:51], v[24:27], v[88:95], v68\n\t" \
  "v_add_f32_dpp v37, v41, v37 quad_perm:[2,3,0,1] row_mask:0xf bank_mask:0xf\n\t" \
  "v_smfmac_f32_16x16x64_bf16 v[48:51], v[28:31], v[136:143], v68\n\t" \
  "v_add_f32_dpp v38, v42, v38 quad_perm:[2,3,0,1] row_mask:0xf bank_mask:0xf\n\t" \
  "v_smfmac_f32_16x16x64_bf16 v[48:51], v[32:35], v[176:183], v68\n\t" \
  "v_add_f32_dpp v39, v43, v39 quad_perm:[2,3,0,1] row_mask:0xf bank_mask:0xf\n\t" \
  "v_smfmac_f32_16x16x64_bf16 v[52:55], v[0:3], a[16:23], v68\n\t" \
  "s_mov_b64 exec, s[58:59]\n\t" \
  "ds_write_b128 v69, v[36:39] offset:2048\n\t" \
  "s_mov_b64 exec, -1\n\t" \
  "v_smfmac_f32_16x16x64_bf16 v[52:55], v[4:7], a[64:71], v68\n\t" \
  "buffer_load_dword off, s[28:31], s87 sc1 lds\n\t" \
  "v_add_f32 v75, v48, v50\n\t" \
  "v_add_f32 v49, v49, v51\n\t" \
  "v_cvt_pk_bf16_f32 v75, v75, v49\n\t" \
  "v_smfmac_f32_16x16x64_bf16 v[56:59], v[0:3], a[24:31], v68\n\t" \
  "ds_write_b32 v71, v75 offset:512\n\t" \
  "v_smfmac_f32_16x16x64_bf16 v[56:59], v[4:7], a[72:79], v68\n\t" \
  "ds_read_b64_tr_b16 v[0:1], v72 offset:10240\n\t" \
  "ds_read_b64_tr_b16 v[2:3], v72 offset:10752\n\t" \
  "v_smfmac_f32_16x16x64_bf16 v[52:55], v[8:11], a[104:111], v68\n\t" \
  "ds_read_b128 v[36:39], v70 offset:12288\n\t" \
  "v_smfmac_f32_16x16x64_bf16 v[56:59], v[8:11], a[112:119], v68\n\t" \
  "ds_read_b128 v[40:43], v70 offset:13312\n\t" \
  "v_smfmac_f32_16x16x64_bf16 v[52:55], v[12:15], a[144:151], v68\n\t" \
  "s_waitcnt lgkmcnt(4)\n\t" \
  "s_barrier\n\t" \
  "ds_read_b64_tr_b16 v[44:45], v72 offset:0\n\t" \
  "ds_read_b64_tr_b16 v[46:47], v72 offset:512\n\t" \
  "v_smfmac_f32_16x16x64_bf16 v[56:59], v[12:15], a[152:159], v68\n\t" \
  "ds_read_b64_tr_b16 v[48:49], v72 offset:1024\n\t" \
  "ds_read_b64_tr_b16 v[50:51], v72 offset:1536\n\t" \
  "v_smfmac_f32_16x16x64_bf16 v[52:55], v[16:19], a[192:199], v68\n\t" \
  "ds_read_b128 v[4:7], v69 offset:2048\n\t" \
  "s_waitcnt lgkmcnt(5)\n\t" \
  "v_smfmac_f32_16x16x64_bf16 v[56:59], v[16:19], v[36:43], v68\n\t" \
  "ds_read_b128 v[8:11], v69 offset:3072\n\t" \
  "s_waitcnt lgkmcnt(4)\n\t" \
  "v_mfma_f32_16x16x32_bf16 v[12:15], v[44:47], v[76:79], 0\n\t" \
  "ds_read_b128 v[36:39], v70 offset:14336\n\t" \
  "s_waitcnt lgkmcnt(3)\n\t" \
  "v_mfma_f32_16x16x32_bf16 v[12:15], v[48:51], v[0:3], v[12:15]\n\t" \
  "ds_read_b128 v[40:43], v70 offset:15360\n\t" \
  "v_smfmac_f32_16x16x64_bf16 v[52:55], v[20:23], a[224:231], v68\n\t" \
  "s_waitcnt lgkmcnt(2)\n\t" \
  "v_add_f32 v4, v4, v8\n\t" \
  "v_add_f32 v5, v5, v9\n\t" \
  "v_add_f32 v6, v6, v10\n\t" \
  "v_cndmask_b32 v78, v13, v14, s[56:57]\n\t" \
  "v_cndmask_b32 v76, v12, v15, s[56:57]\n\t" \
  "v_add_f32 v7, v7, v11\n\t" \
  "v_max3_f32 v77, v4, v5, v6\n\t" \
  "v_add_f32_dpp v76, v78, v76 quad_perm:[2,3,0,1] row_mask:0xf bank_mask:0xf\n\t" \
  "v_max3_f32 v77, v77, v7, v76\n\t" \
  "v_smfmac_f32_16x16x64_bf16 v[56:59], v[20:23], a[232:239], v68\n\t" \
  "ds_read_b128 v[44:47], v70 offset:16384\n\t" \
  "v_max_f32_dpp v77, v77, v77 quad_perm:[2,3,0,1] row_mask:0xf bank_mask:0xf\n\t" \
  "v_mov_b32 v78, v77\n\t" \
  "v_smfmac_f32_16x16x64_bf16 v[52:55], v[24:27], v[96:103], v68\n\t" \
  "ds_read_b128 v[48:51], v70 offset:17408\n\t" \
  "v_permlane16_swap_b32 v77, v78\n\t" \
  "v_max_f32 v77, v77, v78\n\t" \
  "v_mov_b32 v78, v77\n\t" \
  "v_smfmac_f32_16x16x64_bf16 v[52:55], v[28:31], v[144:151], v68\n\t" \
  "ds_read_b128 v[16:19], v70 offset:18432\n\t" \
  "v_permlane32_swap_b32 v77, v78\n\t" \
  "v_max_f32 v77, v77, v78\n\t" \
  "v_smfmac_f32_16x16x64_bf16 v[52:55], v[32:35], v[184:191], v68\n\t" \
  "ds_read_b128 v[20:23], v70 offset:19456\n\t" \
  "v_mul_f32 v77, 0xbe0293ee, v77\n\t" \
  "v_smfmac_f32_16x16x64_bf16 v[56:59], v[24:27], v[104:111], v68\n\t" \
  "v_fmamk_f32 v4, v4, 0x3e0293ee, v77\n\t" \
  "v_smfmac_f32_16x16x64_bf16 v[56:59], v[28:31], v[152:159], v68\n\t" \
  "ds_read_b128 v[24:27], v70 offset:22528\n\t" \
  "s_waitcnt lgkmcnt(4)\n\t" \
  "v_fmamk_f32 v5, v5, 0x3e0293ee, v77\n\t" \
  "v_smfmac_f32_16x16x64_bf16 v[56:59], v[32:35], v[36:43], v68\n\t" \
  "ds_read_b128 v[28:31], v70 offset:23552\n\t" \
  "v_fmamk_f32 v6, v6, 0x3e0293ee, v77\n\t" \
  "v_fmamk_f32 v7, v7, 0x3e0293ee, v77\n\t" \
  "v_fmamk_f32 v76, v76, 0x3e0293ee, v77\n\t" \
  "v_exp_f32 v4, v4\n\t" \
  "v_exp_f32 v5, v5\n\t" \
  "v_exp_f32 v6, v6\n\t" \
  "v_exp_f32 v7, v7\n\t" \
  "v_cvt_pk_bf16_f32 v4, v4, v5\n\t" \
  "v_exp_f32 v76, v76\n\t" \
  "v_cvt_pk_bf16_f32 v5, v6, v7\n\t" \
  "v_mov_b32_dpp v12, v4 quad_perm:[0,1,0,1] row_mask:0xf bank_mask:0xf\n\t" \
  "v_mov_b32_dpp v14, v4 quad_perm:[2,3,2,3] row_mask:0xf bank_mask:0xf\n\t" \
  "v_mov_b32_dpp v13, v5 quad_perm:[0,1,0,1] row_mask:0xf bank_mask:0xf\n\t" \
  "v_mov_b32_dpp v15, v5 quad_perm:[2,3,2,3] row_mask:0xf bank_mask:0xf\n\t" \
  "v_add_f32 v52, v52, v54\n\t" \
  "v_add_f32 v53, v53, v55\n\t" \
  "s_waitcnt vmcnt(2)\n\t" \
  "v_mfma_f32_16x16x32_bf16 v[0:3], v[60:63], v[12:15], 0\n\t" \
  "ds_read_b128 v[36:39], v70 offset:20480\n\t" \
  "v_add_f32 v56, v56, v58\n\t" \
  "v_add_f32 v57, v57, v59\n\t" \
  "v_cvt_pk_bf16_f32 v52, v52, v53\n\t" \
  "v_cvt_pk_bf16_f32 v53, v56, v57\n\t" \
  "s_waitcnt vmcnt(1)\n\t" \
  "v_mfma_f32_16x16x32_bf16 v[4:7], v[64:67], v[12:15], 0\n\t" \
  "buffer_store_dwordx2 v[52:53], v71, s[28:31], s69 offen offset:2048 sc1\n\t" \
  "ds_read_b128 v[40:43], v70 offset:21504\n\t" \
  "v_mov_b64 v[56:57], s[88:89]\n\t" \
  "v_mov_b64 v[58:59], s[88:89]\n\t" \
  "v_mov_b32_dpp v78, v76 quad_perm:[0,1,0,1] row_mask:0xf bank_mask:0xf\n\t" \
  "v_mov_b32_dpp v79, v76 quad_perm:[2,3,2,3] row_mask:0xf bank_mask:0xf\n\t" \
  "v_mfma_f32_16x16x32_bf16 v[32:35], v[56:59], v[12:15], 0\n\t" \
  "ds_read_b128 v[60:63], v70 offset:24576\n\t" \
  "v_cvt_pk_bf16_f32 v11, v78, v79\n\t" \
  "v_cvt_pk_bf16_f32 v8, v78, v79\n\t" \
  "v_mov_b64 v[54:55], 0\n\t" \
  "v_mov_b32 v9, 0\n\t" \
  "v_mov_b32 v10, 0\n\t" \
  "v_mov_b64 v[14:15], 0\n\t" \
  "s_nop 0\n\t" \
  "v_mfma_f32_16x16x32_bf16 v[32:35], v[54:57], v[8:11], v[32:35]\n\t" \
  "s_cmp_eq_u32 s48, 63\n\t" \
  "v_mfma_f32_16x16x32_bf16 v[0:3], v[52:55], v[8:11], v[0:3]\n\t" \
  "ds_read_b128 v[64:67], v70 offset:25600\n\t" \
  "v_mov_b64 v[56:57], 0\n\t" \
  "v_mov_b64 v[58:59], 0\n\t" \
  "v_mfma_f32_16x16x32_bf16 v[4:7], v[52:55], v[10:13], v[4:7]\n\t" \
  "s_cbranch_scc1 LLAST0_%=\n\t" \
  "v_rcp_f32 v74, v32\n\t" \
  "v_mov_b64 v[12:13], 0\n\t" \
  "v_mov_b32 v8, 0\n\t" \
  "v_mul_f32 v0, v0, v74\n\t" \
  "v_mul_f32 v1, v1, v74\n\t" \
  "v_mul_f32 v2, v2, v74\n\t" \
  "v_mul_f32 v3, v3, v74\n\t" \
  "v_cvt_pk_bf16_f32 v76, v0, v1\n\t" \
  "v_cvt_pk_bf16_f32 v77, v2, v3\n\t" \
  "v_pk_mul_f32 v[4:5], v[4:5], v[74:75] op_sel_hi:[1,0]\n\t" \
  "v_pk_mul_f32 v[6:7], v[6:7], v[74:75] op_sel_hi:[1,0]\n\t" \
  "v_cvt_pk_bf16_f32 v78, v4, v5\n\t" \
  "v_cvt_pk_bf16_f32 v79, v6, v7\n\t" \
  "ds_write_b128 v69, v[76:79] offset:4096\n\t" \
  "s_waitcnt lgkmcnt(2)\n\t" \
  "v_smfmac_f32_16x16x64_bf16 v[12:15], v[76:79], v[44:51], v68\n\t" \
  "ds_read_b128 v[44:47], v70 offset:26624\n\t" \
  "v_mov_b32 v11, 0\n\t" \
  "v_smfmac_f32_16x16x64_bf16 v[56:59], v[76:79], v[16:23], v68\n\t" \
  "ds_read_b128 v[48:51], v70 offset:27648\n\t" \
  "v_mov_b64 v[52:53], 0\n\t" \
  "v_smfmac_f32_16x16x64_bf16 v[8:11], v[76:79], v[36:43], v68\n\t" \
  "ds_read_b128 v[16:19], v70 offset:28672\n\t" \
  "v_smfmac_f32_16x16x64_bf16 v[52:55], v[76:79], v[24:31], v68\n\t" \
  "s_waitcnt lgkmcnt(3)\n\t" \
  "s_barrier\n\t" \
  "ds_read_b128 v[0:3], v69 offset:5120\n\t" \
  "ds_read_b128 v[4:7], v69 offset:6144\n\t" \
  "v_lshrrev_b32 v71, 7, v69\n\t" \
  "v_and_b32 v74, 0x70, v69\n\t" \
  "v_lshl_add_u32 v71, v71, 14, v74\n\t" \
  "v_bfe_u32 v72, v69, 2, 7\n\t" \
  "v_and_b32 v74, 0x200, v69\n\t" \
  "s_waitcnt lgkmcnt(1)\n\t" \
  "v_smfmac_f32_16x16x64_bf16 v[8:11], v[0:3], v[60:67], v68\n\t" \
  "ds_read_b128 v[20:23], v70 offset:29696\n\t" \
  "v_mov_b64 v[76:77], 0\n\t" \
  "v_smfmac_f32_16x16x64_bf16 v[12:15], v[0:3], v[200:207], v68\n\t" \
  "ds_read_b128 v[32:35], v69 offset:7168\n\t" \
  "v_mov_b64 v[78:79], 0\n\t" \
  "v_smfmac_f32_16x16x64_bf16 v[52:55], v[0:3], v[44:51], v68\n\t" \
  "ds_read_b128 v[24:27], v70 offset:30720\n\t" \
  "v_lshl_add_u32 v72, v74, 3, v72\n\t" \
  "v_smfmac_f32_16x16x64_bf16 v[56:59], v[0:3], v[232:239], v68\n\t" \
  "buffer_load_dword v75, off, s[28:31], s80 sc1\n\t" \
  "ds_read_b128 v[28:31], v70 offset:31744\n\t" \
  "s_waitcnt lgkmcnt(4)\n\t" \
  "v_smfmac_f32_16x16x64_bf16 v[12:15], v[4:7], v[208:215], v68\n\t" \
  "ds_read_b128 v[60:63], v70 offset:32768\n\t" \
  "s_waitcnt lgkmcnt(4)\n\t" \
  "v_smfmac_f32_16x16x64_bf16 v[8:11], v[4:7], v[16:23], v68\n\t" \
  "ds_read_b128 v[64:67], v70 offset:33792\n\t" \
  "s_waitcnt lgkmcnt(4)\n\t" \
  "v_smfmac_f32_16x16x64_bf16 v[8:11], v[32:35], v[192:199], v68\n\t" \
  "ds_read_b128 v[36:39], v73 offset:4096\n\t" \
  "v_smfmac_f32_16x16x64_bf16 v[12:15], v[32:35], v[216:223], v68\n\t" \
  "ds_read_b128 v[40:43], v73 offset:6144\n\t" \
  "s_waitcnt lgkmcnt(4)\n\t" \
  "v_smfmac_f32_16x16x64_bf16 v[52:55], v[4:7], v[24:31], v68\n\t" \
  "ds_read_b128 v[44:47], v70 offset:34816\n\t" \
  "v_cvt_pk_bf16_f32 v8, v8, v9\n\t" \
  "v_smfmac_f32_16x16x64_bf16 v[52:55], v[32:35], v[224:231], v68\n\t" \
  "ds_read_b128 v[48:51], v70 offset:35840\n\t" \
  "v_cvt_pk_bf16_f32 v9, v10, v11\n\t" \
  "v_smfmac_f32_16x16x64_bf16 v[56:59], v[4:7], v[240:247], v68\n\t" \
  "s_waitcnt lgkmcnt(4)\n\t" \
  "v_cvt_pk_bf16_f32 v10, v12, v13\n\t" \
  "v_smfmac_f32_16x16x64_bf16 v[56:59], v[32:35], v[60:67], v68\n\t" \
  "s_waitcnt lgkmcnt(3)\n\t" \
  "v_cvt_pk_bf16_f32 v11, v14, v15\n\t" \
  "v_smfmac_f32_16x16x64_bf16 v[76:79], v[36:39], v[248:255], v68\n\t" \
  "buffer_store_dwordx4 v[8:11], v71, s[28:31], s54 offen sc1\n\t" \
  "s_waitcnt lgkmcnt(0)\n\t" \
  "v_cvt_pk_bf16_f32 v52, v52, v53\n\t" \
  "v_smfmac_f32_16x16x64_bf16 v[76:79], v[40:43], v[44:51], v68\n\t" \
  "v_cvt_pk_bf16_f32 v53, v54, v55\n\t" \
  "v_cvt_pk_bf16_f32 v54, v56, v57\n\t" \
  "v_cvt_pk_bf16_f32 v55, v58, v59\n\t" \
  "buffer_store_dwordx4 v[52:55], v71, s[28:31], s75 offen sc1\n\t" \
  "s_nop 2\n\t" \
  "v_add_f32 v76, v76, v78\n\t" \
  "v_add_f32 v77, v77, v79\n\t" \
  "v_cvt_pk_bf16_f32 v76, v76, v77\n\t" \
  "buffer_store_dword v76, v72, s[28:31], s55 offen sc1\n\t" \
  "s_memrealtime s[70:71]\n\t" \
  "s_waitcnt vmcnt(3)\n\t" \
  "v_readfirstlane_b32 s65, v75\n\t" \
  "s_barrier\n\t" \
  "s_waitcnt lgkmcnt(0)\n\t" \
  "s_not_b32 s65, s65\n\t" \
  "s_lshl_b32 s61, s70, 8\n\t" \
  "s_andn2_b32 s64, s65, 0xff\n\t" \
  "s_sub_u32 s64, s64, s61\n\t" \
  "s_ashr_i32 s64, s64, 8\n\t" \
  "s_cmp_eq_u32 s65, 0\n\t" \
  "s_cselect_b32 s64, " CH_DLF ", s64\n\t" \
  "s_cselect_b32 s65, " CH_B0 ", s65\n\t" \
  "s_add_u32 s47, s64, s70\n\t" \
  "s_min_i32 s64, s64, s34\n\t" \
  "s_max_i32 s64, s64, " CH_DLL1 "\n\t" \
  "s_add_u32 s64, s64, s70\n\t" \
  "s_add_u32 s55, s64, " CH_FDL "\n\t" \
  "s_movk_i32 s60, 8192\n\t" \
  "FWT_%=:\n\t" \
  "s_memrealtime s[62:63]\n\t" \
  "s_sub_u32 s60, s60, 1\n\t" \
  "s_cselect_b32 s61, 0, -1\n\t" \
  "s_waitcnt lgkmcnt(0)\n\t" \
  "s_sub_i32 s63, s62, s55\n\t" \
  "s_and_b32 s63, s63, s61\n\t" \
  "s_cmp_lt_i32 s63, 0\n\t" \
  "s_cbranch_scc1 FWT_%=\n\t" \
  "s_mov_b64 exec, 1\n\t" \
  "buffer_store_dword v68, off, s[28:31], s51 offset:4064 sc1\n\t" \
  "s_mov_b64 exec, -1\n\t" \
  "s_and_b32 s65, s65, 0xff\n\t" \
  "s_add_u32 s47, s47, s65\n\t" \
  "s_add_u32 s65, s65, s64\n\t" \
  "s_sub_u32 s60, s70, s66\n\t" \
  "s_add_u32 s61, s81, " CH_UP "\n\t" \
  "s_min_u32 s60, s60, s61\n\t" \
  "s_sub_u32 s61, s81, " CH_DN "\n\t" \
  "s_max_u32 s60, s60, s61\n\t" \
  "s_cmp_eq_u32 s68, 0\n\t" \
  "s_cselect_b32 s81, s60, s81\n\t" \
  "s_add_u32 s48, s48, 1\n\t" \
  "s_mov_b32 s49, s53\n\t" \
  "s_add_u32 s53, s53, " CH_SLOT "\n\t" \
  "s_add_u32 s60, s53, " CH_SKIP "\n\t" \
  "s_lshr_b32 s61, s96, 1\n\t" \
  "s_add_u32 s86, s86, 1\n\t" \
  "s_cmp_eq_u32 s86, 3\n\t" \
  "s_cselect_b32 s53, s60, s53\n\t" \
  "s_cselect_b32 s96, s61, s96\n\t" \
  "s_cselect_b32 s86, 0, s86\n\t" \
  "s_andn2_b32 s53, s53, 0x2000\n\t" \
  "s_and_b32 s60, s96, 1\n\t" \
  "s_lshl_b32 s60, s60, 13\n\t" \
  "s_or_b32 s53, s53, s60\n\t" \
  "s_mov_b32 s68, 0\n\t" \
  "s_add_u32 s82, s48, 0\n\t" \
  "s_lshr_b64 s[60:61], s[90:91], s82\n\t" \
  "s_and_b32 s60, s60, 1\n\t" \
  "s_lshl_b32 s60, s60, 13\n\t" \
  "s_add_u32 s82, s82, " CH_SG0 "\n\t" \
  "s_lshl_b32 s82, s82, 21\n\t" \
  "s_or_b32 s82, s82, s60\n\t" \
  "s_add_u32 s50, s82, s38\n\t" \
  "s_add_u32 s69, s82, s39\n\t" \
  "s_add_u32 s51, s82, s43\n\t" \
  "s_cmp_lg_u32 s34, 0\n\t" \
  "s_cbranch_scc1 LW0_%=\n\t" \
  "s_barrier\n\t" \
  "s_branch LINI_%=\n\t" \
  "LFAIL0_%=:\n\t" \
  "v_max_u32 v75, v0, v1\n\t" \
  "v_max3_u32 v75, v75, v2, v3\n\t" \
  "v_max3_u32 v75, v75, v4, v5\n\t" \
  "v_max3_u32 v75, v75, v6, v7\n\t" \
  "v_max3_u32 v75, v75, v8, v9\n\t" \
  "v_max3_u32 v75, v75, v10, v11\n\t" \
  "v_max3_u32 v75, v75, v12, v13\n\t" \
  "v_max3_u32 v75, v75, v14, v15\n\t" \
  "v_max3_u32 v75, v75, v16, v17\n\t" \
  "v_max3_u32 v75, v75, v18, v19\n\t" \
  "v_max3_u32 v75, v75, v20, v21\n\t" \
  "v_max3_u32 v75, v75, v22, v23\n\t" \
  "v_max3_u32 v75, v75, v24, v25\n\t" \
  "v_max3_u32 v75, v75, v26, v27\n\t" \
  "v_max3_u32 v75, v75, v28, v29\n\t" \
  "v_max3_u32 v75, v75, v30, v31\n\t" \
  "v_max3_u32 v75, v75, v32, v33\n\t" \
  "v_max3_u32 v75, v75, v34, v35\n\t" \
  "v_cmp_eq_u32 vcc, -1, v75\n\t" \
  "s_cbranch_vccz LGO0_%=\n\t" \
  "s_add_u32 s68, s68, 1\n\t" \
  "s_waitcnt lgkmcnt(0)\n\t" \
  "s_cmp_gt_u32 s68, s83\n\t" \
  "s_cbranch_scc1 LGV0_%=\n\t" \
  "s_barrier\n\t" \
  "s_memrealtime s[62:63]\n\t" \
  "s_mov_b32 m0, 0\n\t" \
  "s_waitcnt lgkmcnt(0)\n\t" \
  "s_add_u32 s65, s62, " CH_PEN "\n\t" \
  "s_branch LDMA_%=\n\t" \
  "LWT_%=f:\n\t" \
  "s_cmp_lt_i32 s63, -2\n\t" \
  "s_cbranch_scc0 LWT_%=g\n\t" \
  "s_nop 4\n\t" \
  "s_branch LWT_%=g\n\t" \
  "LGV0_%=:\n\t" \
  "s_mov_b32 s83, 0\n\t" \
  "s_lshl_b32 s60, s36, 4\n\t" \
  "s_or_b32 s60, s60, s48\n\t" \
  "s_or_b32 s60, s60, 805306368\n\t" \
  "v_mov_b32 v76, s60\n\t" \
  "v_mov_b32 v77, 0\n\t" \
  "s_mov_b64 exec, 1\n\t" \
  "buffer_atomic_cmpswap v[76:77], off, s[28:31], s92 sc1\n\t" \
  "s_mov_b64 exec, -1\n\t" \
  "s_branch LGO0_%=\n\t" \
  "LLAST0_%=:\n\t" \
  "s_waitcnt vmcnt(0) lgkmcnt(0)\n\t" \
  "s_barrier\n\t" \
  "s_mov_b64 exec, 1\n\t" \
  "buffer_store_dword v68, off, s[28:31], s51 offset:4064 sc1\n\t" \
  "s_mov_b64 exec, -1\n\t" \
  "s_branch LEND_%=\n\t" \
  ".p2align 6\n\t" \
  "LG1_%=:\n\t" \
  "s_barrier\n\t" \
  "buffer_load_dwordx4 v[52:55], v69, s[28:31], s50 offen sc1\n\t" \
  "buffer_load_dwordx4 v[56:59], v69, s[28:31], s50 offen offset:1024 sc1\n\t" \
  "s_add_u32 s64, s50, 14336\n\t" \
  "s_movk_i32 m0, 11264\n\t" \
  "v_lshlrev_b32 v75, 1, v69\n\t" \
  "buffer_load_dword v75, s[28:31], s64 offen sc1 lds\n\t" \
  "ds_read_b128 v[44:47], v70 offset:0\n\t" \
  "ds_read_b128 v[48:51], v70 offset:1024\n\t" \
  "ds_read_b128 v[60:63], v70 offset:2048\n\t" \
  "ds_read_b128 v[64:67], v70 offset:3072\n\t" \
  "v_mov_b64 v[36:37], 0\n\t" \
  "v_mov_b64 v[38:39], 0\n\t" \
  "v_mov_b64 v[40:41], 0\n\t" \
  "v_mov_b64 v[42:43], 0\n\t" \
  "v_lshrrev_b32 v71, 1, v69\n\t" \
  "v_add_u32 v71, 4, v71\n\t" \
  "v_lshrrev_b32 v72, 3, v69\n\t" \
  "v_and_b32 v72, 0x78, v72\n\t" \
  "v_and_b32 v74, 0x30, v69\n\t" \
  "v_lshl_add_u32 v72, v74, 3, v72\n\t" \
  "s_add_u32 s54, s53, s36\n\t" \
  "s_add_u32 s55, s54, 540672\n\t" \
  "s_add_u32 s54, s54, 4096\n\t" \
  "s_add_u32 s75, s54, 262144\n\t" \
  "s_add_u32 s80, s53, s26\n\t" \
  "s_barrier\n\t" \
  "ds_read_b128 v[0:3], v69 offset:0\n\t" \
  "ds_read_b128 v[4:7], v69 offset:1024\n\t" \
  "ds_read_b128 v[8:11], v69 offset:2048\n\t" \
  "ds_read_b128 v[12:15], v69 offset:3072\n\t" \
  "s_waitcnt lgkmcnt(3)\n\t" \
  "v_smfmac_f32_16x16x64_bf16 v[36:39], v[0:3], v[44:51], v68\n\t" \
  "ds_read_b128 v[44:47], v70 offset:4096\n\t" \
  "v_smfmac_f32_16x16x64_bf16 v[40:43], v[0:3], v[60:67], v68\n\t" \
  "ds_read_b128 v[48:51], v70 offset:5120\n\t" \
  "s_waitcnt lgkmcnt(4)\n\t" \
  "v_smfmac_f32_16x16x64_bf16 v[36:39], v[4:7], a[32:39], v68\n\t" \
  "s_barrier\n\t" \
  "ds_read_b128 v[16:19], v69 offset:4096\n\t" \
  "v_smfmac_f32_16x16x64_bf16 v[40:43], v[4:7], a[40:47], v68\n\t" \
  "ds_read_b128 v[20:23], v69 offset:5120\n\t" \
  "s_waitcnt lgkmcnt(5)\n\t" \
  "v_smfmac_f32_16x16x64_bf16 v[36:39], v[8:11], a[80:87], v68\n\t" \
  "ds_read_b128 v[24:27], v69 offset:6144\n\t" \
  "v_smfmac_f32_16x16x64_bf16 v[40:43], v[8:11], a[88:95], v68\n\t" \
  "ds_read_b128 v[28:31], v69 offset:7168\n\t" \
  "s_waitcnt lgkmcnt(6)\n\t" \
  "v_smfmac_f32_16x16x64_bf16 v[36:39], v[12:15], a[120:127], v68\n\t" \
  "ds_read_b128 v[32:35], v69 offset:8192\n\t" \
  "s_waitcnt lgkmcnt(5)\n\t" \
  "v_smfmac_f32_16x16x64_bf16 v[40:43], v[12:15], v[44:51], v68\n\t" \
  "ds_read_b128 v[60:63], v70 offset:6144\n\t" \
  "s_waitcnt lgkmcnt(5)\n\t" \
  "v_smfmac_f32_16x16x64_bf16 v[36:39], v[16:19], a[160:167], v68\n\t" \
  "ds_read_b128 v[64:67], v70 offset:7168\n\t" \
  "v_smfmac_f32_16x16x64_bf16 v[40:43], v[16:19], a[168:175], v68\n\t" \
  "s_waitcnt lgkmcnt(5)\n\t" \
  "v_mov_b64 v[44:45], 0\n\t" \
  "v_smfmac_f32_16x16x64_bf16 v[36:39], v[20:23], a[200:207], v68\n\t" \
  "s_waitcnt lgkmcnt(4)\n\t" \
  "v_mov_b64 v[46:47], 0\n\t" \
  "v_smfmac_f32_16x16x64_bf16 v[36:39], v[24:27], a[240:247], v68\n\t" \
  "s_waitcnt lgkmcnt(2)\n\t" \
  "v_mov_b64 v[48:49], 0\n\t" \
  "v_smfmac_f32_16x16x64_bf16 v[36:39], v[28:31], v[112:119], v68\n\t" \
  "v_mov_b64 v[50:51], 0\n\t" \
  "v_smfmac_f32_16x16x64_bf16 v[36:39], v[32:35], v[160:167], v68\n\t" \
  "s_waitcnt lgkmcnt(0)\n\t" \
  "v_smfmac_f32_16x16x64_bf16 v[40:43], v[20:23], v[60:67], v68\n\t" \
  "ds_read_b128 v[60:63], v70 offset:8192\n\t" \
  "v_smfmac_f32_16x16x64_bf16 v[40:43], v[24:27], a[248:255], v68\n\t" \
  "ds_read_b128 v[64:67], v70 offset:9216\n\t" \
  "v_add_f32 v74, v36, v38\n\t" \
  "v_smfmac_f32_16x16x64_bf16 v[40:43], v[28:31], v[120:127], v68\n\t" \
  "v_add_f32 v37, v37, v39\n\t" \
  "v_smfmac_f32_16x16x64_bf16 v[40:43], v[32:35], v[168:175], v68\n\t" \
  "v_cmp_u_f32 vcc, v74, v37\n\t" \
  "v_smfmac_f32_16x16x64_bf16 v[44:47], v[0:3], a[0:7], v68\n\t" \
  "s_cbranch_vccnz LFAIL1_%=\n\t" \
  "LGO1_%=:\n\t" \
  "v_cvt_pk_bf16_f32 v74, v74, v37\n\t" \
  "v_smfmac_f32_16x16x64_bf16 v[44:47], v[4:7], a[48:55], v68\n\t" \
  "s_waitcnt lgkmcnt(0)\n\t" \
  "v_add_f32 v75, v40, v42\n\t" \
  "v_smfmac_f32_16x16x64_bf16 v[44:47], v[8:11], v[60:67], v68\n\t" \
  "ds_read_b128 v[60:63], v70 offset:10240\n\t" \
  "v_add_f32 v41, v41, v43\n\t" \
  "v_cvt_pk_bf16_f32 v75, v75, v41\n\t" \
  "v_smfmac_f32_16x16x64_bf16 v[44:47], v[12:15], a[128:135], v68\n\t" \
  "ds_write2st64_b32 v71, v74, v75 offset0:36 offset1:38\n\t" \
  "v_smfmac_f32_16x16x64_bf16 v[44:47], v[16:19], a[176:183], v68\n\t" \
  "ds_read_b128 v[64:67], v70 offset:11264\n\t" \
  "v_smfmac_f32_16x16x64_bf16 v[44:47], v[20:23], a[208:215], v68\n\t" \
  "v_smfmac_f32_16x16x64_bf16 v[44:47], v[24:27], v[80:87], v68\n\t" \
  "v_smfmac_f32_16x16x64_bf16 v[44:47], v[28:31], v[128:135], v68\n\t" \
  "s_waitcnt lgkmcnt(0)\n\t" \
  "v_smfmac_f32_16x16x64_bf16 v[44:47], v[32:35], v[60:67], v68\n\t" \
  "s_barrier\n\t" \
  "ds_read_b64_tr_b16 v[76:77], v72 offset:9216\n\t" \
  "v_smfmac_f32_16x16x64_bf16 v[48:51], v[0:3], a[8:15], v68\n\t" \
  "buffer_load_dwordx4 v[60:63], v69, s[28:31], s64 offen sc1\n\t" \
  "ds_read_b64_tr_b16 v[78:79], v72 offset:9728\n\t" \
  "v_smfmac_f32_16x16x64_bf16 v[48:51], v[4:7], a[56:63], v68\n\t" \
  "v_add_f32 v74, v44, v46\n\t" \
  "v_smfmac_f32_16x16x64_bf16 v[48:51], v[8:11], a[96:103], v68\n\t" \
  "v_add_f32 v45, v45, v47\n\t" \
  "v_smfmac_f32_16x16x64_bf16 v[48:51], v[12:15], a[136:143], v68\n\t" \
  "s_waitcnt vmcnt(3)\n\t" \
  "s_waitcnt lgkmcnt(0)\n\t" \
  "v_cvt_pk_bf16_f32 v74, v74, v45\n\t" \
  "v_mfma_f32_16x16x32_bf16 v[36:39], v[52:55], v[76:79], 0\n\t" \
  "s_waitcnt vmcnt(2)\n\t" \
  "v_mfma_f32_16x16x32_bf16 v[40:43], v[56:59], v[76:79], 0\n\t" \
  "buffer_load_dwordx4 v[64:67], v69, s[28:31], s64 offen offset:1024 sc1\n\t" \
  "v_mov_b64 v[52:53], 0\n\t" \
  "v_mov_b64 v[54:55], 0\n\t" \
  "v_smfmac_f32_16x16x64_bf16 v[48:51], v[16:19], a[184:191], v68\n\t" \
  "v_mov_b64 v[56:57], 0\n\t" \
  "v_mov_b64 v[58:59], 0\n\t" \
  "v_smfmac_f32_16x16x64_bf16 v[48:51], v[20:23], a[216:223], v68\n\t" \
  "ds_write_b32 v71, v74 offset:0\n\t" \
  "v_add_f32_dpp v36, v40, v36 quad_perm:[2,3,0,1] row_mask:0xf bank_mask:0xf\n\t" \
  "v_smfmac_f32_16x16x64_bf16 v[48:51], v[24:27], v[88:95], v68\n\t" \
  "v_add_f32_dpp v37, v41, v37 quad_perm:[2,3,0,1] row_mask:0xf bank_mask:0xf\n\t" \
  "v_smfmac_f32_16x16x64_bf16 v[48:51], v[28:31], v[136:143], v68\n\t" \
  "v_add_f32_dpp v38, v42, v38 quad_perm:[2,3,0,1] row_mask:0xf bank_mask:0xf\n\t" \
  "v_smfmac_f32_16x16x64_bf16 v[48:51], v[32:35], v[176:183], v68\n\t" \
  "v_add_f32_dpp v39, v43, v39 quad_perm:[2,3,0,1] row_mask:0xf bank_mask:0xf\n\t" \
  "v_smfmac_f32_16x16x64_bf16 v[52:55], v[0:3], a[16:23], v68\n\t" \
  "s_mov_b64 exec, s[58:59]\n\t" \
  "ds_write_b128 v69, v[36:39] offset:2080\n\t" \
  "s_mov_b64 exec, -1\n\t" \
  "v_smfmac_f32_16x16x64_bf16 v[52:55], v[4:7], a[64:71], v68\n\t" \
  "v_add_f32 v75, v48, v50\n\t" \
  "v_add_f32 v49, v49, v51\n\t" \
  "v_cvt_pk_bf16_f32 v75, v75, v49\n\t" \
  "v_smfmac_f32_16x16x64_bf16 v[56:59], v[0:3], a[24:31], v68\n\t" \
  "ds_write_b32 v71, v75 offset:512\n\t" \
  "v_smfmac_f32_16x16x64_bf16 v[56:59], v[4:7], a[72:79], v68\n\t" \
  "ds_read_b64_tr_b16 v[0:1], v72 offset:10240\n\t" \
  "ds_read_b64_tr_b16 v[2:3], v72 offset:10752\n\t" \
  "v_smfmac_f32_16x16x64_bf16 v[52:55], v[8:11], a[104:111], v68\n\t" \
  "ds_read_b128 v[36:39], v70 offset:12288\n\t" \
  "v_smfmac_f32_16x16x64_bf16 v[56:59], v[8:11], a[112:119], v68\n\t" \
  "ds_read_b128 v[40:43], v70 offset:13312\n\t" \
  "v_smfmac_f32_16x16x64_bf16 v[52:55], v[12:15], a[144:151], v68\n\t" \
  "s_waitcnt lgkmcnt(4)\n\t" \
  "s_barrier\n\t" \
  "ds_read_b64_tr_b16 v[44:45], v72 offset:0\n\t" \
  "ds_read_b64_tr_b16 v[46:47], v72 offset:512\n\t" \
  "v_smfmac_f32_16x16x64_bf16 v[56:59], v[12:15], a[152:159], v68\n\t" \
  "ds_read_b64_tr_b16 v[48:49], v72 offset:1024\n\t" \
  "ds_read_b64_tr_b16 v[50:51], v72 offset:1536\n\t" \
  "v_smfmac_f32_16x16x64_bf16 v[52:55], v[16:19], a[192:199], v68\n\t" \
  "ds_read_b128 v[4:7], v69 offset:2048\n\t" \
  "s_waitcnt lgkmcnt(5)\n\t" \
  "v_smfmac_f32_16x16x64_bf16 v[56:59], v[16:19], v[36:43], v68\n\t" \
  "ds_read_b128 v[8:11], v69 offset:3072\n\t" \
  "s_waitcnt lgkmcnt(4)\n\t" \
  "v_mfma_f32_16x16x32_bf16 v[12:15], v[44:47], v[76:79], 0\n\t" \
  "ds_read_b128 v[36:39], v70 offset:14336\n\t" \
  "s_waitcnt lgkmcnt(3)\n\t" \
  "v_mfma_f32_16x16x32_bf16 v[12:15], v[48:51], v[0:3], v[12:15]\n\t" \
  "buffer_store_dwordx4 v[44:47], v69, s[28:31], s69 offen offset:0 sc1\n\t" \
  "ds_read_b128 v[40:43], v70 offset:15360\n\t" \
  "v_smfmac_f32_16x16x64_bf16 v[52:55], v[20:23], a[224:231], v68\n\t" \
  "s_waitcnt lgkmcnt(2)\n\t" \
  "v_add_f32 v4, v4, v8\n\t" \
  "v_add_f32 v5, v5, v9\n\t" \
  "v_add_f32 v6, v6, v10\n\t" \
  "v_cndmask_b32 v78, v13, v14, s[56:57]\n\t" \
  "v_cndmask_b32 v76, v12, v15, s[56:57]\n\t" \
  "v_add_f32 v7, v7, v11\n\t" \
  "v_max3_f32 v77, v4, v5, v6\n\t" \
  "v_add_f32_dpp v76, v78, v76 quad_perm:[2,3,0,1] row_mask:0xf bank_mask:0xf\n\t" \
  "v_max3_f32 v77, v77, v7, v76\n\t" \
  "v_smfmac_f32_16x16x64_bf16 v[56:59], v[20:23], a[232:239], v68\n\t" \
  "ds_read_b128 v[44:47], v70 offset:16384\n\t" \
  "v_max_f32_dpp v77, v77, v77 quad_perm:[2,3,0,1] row_mask:0xf bank_mask:0xf\n\t" \
  "v_mov_b32 v78, v77\n\t" \
  "v_smfmac_f32_16x16x64_bf16 v[52:55], v[24:27], v[96:103], v68\n\t" \
  "ds_read_b128 v[48:51], v70 offset:17408\n\t" \
  "v_permlane16_swap_b32 v77, v78\n\t" \
  "v_max_f32 v77, v77, v78\n\t" \
  "v_mov_b32 v78, v77\n\t" \
  "v_smfmac_f32_16x16x64_bf16 v[52:55], v[28:31], v[144:151], v68\n\t" \
  "ds_read_b128 v[16:19], v70 offset:18432\n\t" \
  "v_permlane32_swap_b32 v77, v78\n\t" \
  "v_max_f32 v77, v77, v78\n\t" \
  "v_smfmac_f32_16x16x64_bf16 v[52:55], v[32:35], v[184:191], v68\n\t" \
  "ds_read_b128 v[20:23], v70 offset:19456\n\t" \
  "v_mul_f32 v77, 0xbe0293ee, v77\n\t" \
  "v_smfmac_f32_16x16x64_bf16 v[56:59], v[24:27], v[104:111], v68\n\t" \
  "v_fmamk_f32 v4, v4, 0x3e0293ee, v77\n\t" \
  "v_smfmac_f32_16x16x64_bf16 v[56:59], v[28:31], v[152:159], v68\n\t" \
  "ds_read_b128 v[24:27], v70 offset:22528\n\t" \
  "s_waitcnt lgkmcnt(4)\n\t" \
  "v_fmamk_f32 v5, v5, 0x3e0293ee, v77\n\t" \
  "v_smfmac_f32_16x16x64_bf16 v[56:59], v[32:35], v[36:43], v68\n\t" \
  "ds_read_b128 v[28:31], v70 offset:23552\n\t" \
  "v_fmamk_f32 v6, v6, 0x3e0293ee, v77\n\t" \
  "v_fmamk_f32 v7, v7, 0x3e0293ee, v77\n\t" \
  "v_fmamk_f32 v76, v76, 0x3e0293ee, v77\n\t" \
  "v_exp_f32 v4, v4\n\t" \
  "v_exp_f32 v5, v5\n\t" \
  "v_exp_f32 v6, v6\n\t" \
  "v_exp_f32 v7, v7\n\t" \
  "v_cvt_pk_bf16_f32 v4, v4, v5\n\t" \
  "v_exp_f32 v76, v76\n\t" \
  "v_cvt_pk_bf16_f32 v5, v6, v7\n\t" \
  "v_mov_b32_dpp v12, v4 quad_perm:[0,1,0,1] row_mask:0xf bank_mask:0xf\n\t" \
  "v_mov_b32_dpp v14, v4 quad_perm:[2,3,2,3] row_mask:0xf bank_mask:0xf\n\t" \
  "v_mov_b32_dpp v13, v5 quad_perm:[0,1,0,1] row_mask:0xf bank_mask:0xf\n\t" \
  "v_mov_b32_dpp v15, v5 quad_perm:[2,3,2,3] row_mask:0xf bank_mask:0xf\n\t" \
  "v_add_f32 v52, v52, v54\n\t" \
  "v_add_f32 v53, v53, v55\n\t" \
  "s_waitcnt vmcnt(2)\n\t" \
  "v_mfma_f32_16x16x32_bf16 v[0:3], v[60:63], v[12:15], 0\n\t" \
  "ds_read_b128 v[36:39], v70 offset:20480\n\t" \
  "v_add_f32 v56, v56, v58\n\t" \
  "v_add_f32 v57, v57, v59\n\t" \
  "v_cvt_pk_bf16_f32 v52, v52, v53\n\t" \
  "v_cvt_pk_bf16_f32 v53, v56, v57\n\t" \
  "s_waitcnt vmcnt(1)\n\t" \
  "v_mfma_f32_16x16x32_bf16 v[4:7], v[64:67], v[12:15], 0\n\t" \
  "ds_read_b128 v[40:43], v70 offset:21504\n\t" \
  "v_mov_b64 v[56:57], s[88:89]\n\t" \
  "v_mov_b64 v[58:59], s[88:89]\n\t" \
  "v_mov_b32_dpp v78, v76 quad_perm:[0,1,0,1] row_mask:0xf bank_mask:0xf\n\t" \
  "v_mov_b32_dpp v79, v76 quad_perm:[2,3,2,3] row_mask:0xf bank_mask:0xf\n\t" \
  "v_mfma_f32_16x16x32_bf16 v[32:35], v[56:59], v[12:15], 0\n\t" \
  "buffer_store_dwordx2 v[52:53], v71, s[28:31], s69 offen offset:2556 sc1\n\t" \
  "ds_read_b128 v[60:63], v70 offset:24576\n\t" \
  "v_cvt_pk_bf16_f32 v11, v78, v79\n\t" \
  "v_cvt_pk_bf16_f32 v8, v78, v79\n\t" \
  "v_mov_b64 v[54:55], 0\n\t" \
  "v_mov_b32 v9, 0\n\t" \
  "v_mov_b32 v10, 0\n\t" \
  "v_mov_b64 v[14:15], 0\n\t" \
  "s_nop 0\n\t" \
  "v_mfma_f32_16x16x32_bf16 v[32:35], v[54:57], v[8:11], v[32:35]\n\t" \
  "s_cmp_eq_u32 s48, 63\n\t" \
  "v_mfma_f32_16x16x32_bf16 v[0:3], v[52:55], v[8:11], v[0:3]\n\t" \
  "ds_read_b128 v[64:67], v70 offset:25600\n\t" \
  "v_mov_b64 v[56:57], 0\n\t" \
  "v_mov_b64 v[58:59], 0\n\t" \
  "v_mfma_f32_16x16x32_bf16 v[4:7], v[52:55], v[10:13], v[4:7]\n\t" \
  "s_cbranch_scc1 LLAST1_%=\n\t" \
  "v_rcp_f32 v74, v32\n\t" \
  "v_mov_b64 v[12:13], 0\n\t" \
  "v_mov_b32 v8, 0\n\t" \
  "v_mul_f32 v0, v0, v74\n\t" \
  "v_mul_f32 v1, v1, v74\n\t" \
  "v_mul_f32 v2, v2, v74\n\t" \
  "v_mul_f32 v3, v3, v74\n\t" \
  "v_cvt_pk_bf16_f32 v76, v0, v1\n\t" \
  "v_cvt_pk_bf16_f32 v77, v2, v3\n\t" \
  "v_pk_mul_f32 v[4:5], v[4:5], v[74:75] op_sel_hi:[1,0]\n\t" \
  "v_pk_mul_f32 v[6:7], v[6:7], v[74:75] op_sel_hi:[1,0]\n\t" \
  "v_cvt_pk_bf16_f32 v78, v4, v5\n\t" \
  "v_cvt_pk_bf16_f32 v79, v6, v7\n\t" \
  "ds_write_b128 v69, v[76:79] offset:5120\n\t" \
  "s_waitcnt lgkmcnt(2)\n\t" \
  "v_smfmac_f32_16x16x64_bf16 v[12:15], v[76:79], v[44:51], v68\n\t" \
  "ds_read_b128 v[44:47], v70 offset:26624\n\t" \
  "v_mov_b32 v11, 0\n\t" \
  "v_smfmac_f32_16x16x64_bf16 v[56:59], v[76:79], v[16:23], v68\n\t" \
  "ds_read_b128 v[48:51], v70 offset:27648\n\t" \
  "v_mov_b64 v[52:53], 0\n\t" \
  "v_smfmac_f32_16x16x64_bf16 v[8:11], v[76:79], v[36:43], v68\n\t" \
  "ds_read_b128 v[16:19], v70 offset:28672\n\t" \
  "v_smfmac_f32_16x16x64_bf16 v[52:55], v[76:79], v[24:31], v68\n\t" \
  "s_waitcnt lgkmcnt(3)\n\t" \
  "s_barrier\n\t" \
  "ds_read_b128 v[0:3], v69 offset:4096\n\t" \
  "ds_read_b128 v[4:7], v69 offset:7168\n\t" \
  "v_lshrrev_b32 v71, 7, v69\n\t" \
  "v_and_b32 v74, 0x70, v69\n\t" \
  "v_lshl_add_u32 v71, v71, 14, v74\n\t" \
  "v_bfe_u32 v72, v69, 2, 7\n\t" \
  "v_and_b32 v74, 0x200, v69\n\t" \
  "s_waitcnt lgkmcnt(1)\n\t" \
  "v_smfmac_f32_16x16x64_bf16 v[8:11], v[0:3], v[60:67], v68\n\t" \
  "buffer_load_dword v75, off, s[28:31], s80 sc1\n\t" \
  "ds_read_b128 v[20:23], v70 offset:29696\n\t" \
  "v_mov_b64 v[76:77], 0\n\t" \
  "v_smfmac_f32_16x16x64_bf16 v[12:15], v[0:3], v[200:207], v68\n\t" \
  "ds_read_b128 v[32:35], v69 offset:6144\n\t" \
  "v_mov_b64 v[78:79], 0\n\t" \
  "v_smfmac_f32_16x16x64_bf16 v[52:55], v[0:3], v[44:51], v68\n\t" \
  "ds_read_b128 v[24:27], v70 offset:30720\n\t" \
  "v_lshl_add_u32 v72, v74, 3, v72\n\t" \
  "v_smfmac_f32_16x16x64_bf16 v[56:59], v[0:3], v[232:239], v68\n\t" \
  "ds_read_b128 v[28:31], v70 offset:31744\n\t" \
  "s_waitcnt lgkmcnt(4)\n\t" \
  "v_smfmac_f32_16x16x64_bf16 v[12:15], v[4:7], v[208:215], v68\n\t" \
  "ds_read_b128 v[60:63], v70 offset:32768\n\t" \
  "s_waitcnt lgkmcnt(4)\n\t" \
  "v_smfmac_f32_16x16x64_bf16 v[8:11], v[4:7], v[16:23], v68\n\t" \
  "ds_read_b128 v[64:67], v70 offset:33792\n\t" \
  "s_waitcnt lgkmcnt(4)\n\t" \
  "v_smfmac_f32_16x16x64_bf16 v[8:11], v[32:35], v[192:199], v68\n\t" \
  "ds_read_b128 v[36:39], v73 offset:4096\n\t" \
  "v_smfmac_f32_16x16x64_bf16 v[12:15], v[32:35], v[216:223], v68\n\t" \
  "ds_read_b128 v[40:43], v73 offset:6144\n\t" \
  "s_waitcnt lgkmcnt(4)\n\t" \
  "v_smfmac_f32_16x16x64_bf16 v[52:55], v[4:7], v[24:31], v68\n\t" \
  "ds_read_b128 v[44:47], v70 offset:34816\n\t" \
  "v_cvt_pk_bf16_f32 v8, v8, v9\n\t" \
  "v_smfmac_f32_16x16x64_bf16 v[52:55], v[32:35], v[224:231], v68\n\t" \
  "ds_read_b128 v[48:51], v70 offset:35840\n\t" \
  "v_cvt_pk_bf16_f32 v9, v10, v11\n\t" \
  "v_smfmac_f32_16x16x64_bf16 v[56:59], v[4:7], v[240:247], v68\n\t" \
  "s_waitcnt lgkmcnt(4)\n\t" \
  "v_cvt_pk_bf16_f32 v10, v12, v13\n\t" \
  "v_smfmac_f32_16x16x64_bf16 v[56:59], v[32:35], v[60:67], v68\n\t" \
  "s_waitcnt lgkmcnt(3)\n\t" \
  "v_cvt_pk_bf16_f32 v11, v14, v15\n\t" \
  "v_smfmac_f32_16x16x64_bf16 v[76:79], v[36:39], v[248:255], v68\n\t" \
  "buffer_store_dwordx4 v[8:11], v71, s[28:31], s54 offen sc1\n\t" \
  "s_waitcnt lgkmcnt(0)\n\t" \
  "v_cvt_pk_bf16_f32 v52, v52, v53\n\t" \
  "v_smfmac_f32_16x16x64_bf16 v[76:79], v[40:43], v[44:51], v68\n\t" \
  "v_cvt_pk_bf16_f32 v53, v54, v55\n\t" \
  "v_cvt_pk_bf16_f32 v54, v56, v57\n\t" \
  "v_cvt_pk_bf16_f32 v55, v58, v59\n\t" \
  "buffer_store_dwordx4 v[52:55], v71, s[28:31], s75 offen sc1\n\t" \
  "s_nop 2\n\t" \
  "v_add_f32 v76, v76, v78\n\t" \
  "v_add_f32 v77, v77, v79\n\t" \
  "v_cvt_pk_bf16_f32 v76, v76, v77\n\t" \
  "buffer_store_dword v76, v72, s[28:31], s55 offen sc1\n\t" \
  "s_memrealtime s[70:71]\n\t" \
  "s_waitcnt vmcnt(3)\n\t" \
  "v_readfirstlane_b32 s65, v75\n\t" \
  "s_barrier\n\t" \
  "s_waitcnt lgkmcnt(0)\n\t" \
  "s_not_b32 s65, s65\n\t" \
  "s_lshl_b32 s61, s70, 8\n\t" \
  "s_andn2_b32 s64, s65, 0xff\n\t" \
  "s_sub_u32 s64, s64, s61\n\t" \
  "s_ashr_i32 s64, s64, 8\n\t" \
  "s_cmp_eq_u32 s65, 0\n\t" \
  "s_cselect_b32 s64, " CH_DLF ", s64\n\t" \
  "s_min_i32 s64, s64, s34\n\t" \
  "s_max_i32 s64, s64, " CH_DLL1 "\n\t" \
  "s_add_u32 s64, s64, s70\n\t" \
  "s_mov_b32 s74, 0\n\t" \
  "s_movk_i32 s60, 8192\n\t" \
  "RWT1_%=:\n\t" \
  "s_memrealtime s[72:73]\n\t" \
  "s_sub_u32 s60, s60, 1\n\t" \
  "s_cselect_b32 s61, 0, -1\n\t" \
  "s_waitcnt lgkmcnt(0)\n\t" \
  "s_sub_i32 s73, s72, s64\n\t" \
  "s_and_b32 s73, s73, s61\n\t" \
  "s_cmp_lt_i32 s73, " CH_FIN "\n\t" \
  "s_cbranch_scc1 RWT1_%=\n\t" \
  "s_cmp_lt_i32 s73, 0\n\t" \
  "s_cbranch_scc1 RWT1_%=f\n\t" \
  "RWT1_%=g:\n\t" \
  "RRETRY1_%=:\n\t" \
  "buffer_load_dwordx4 v[0:3], v69, s[28:31], s97 offen offset:0 sc1\n\t" \
  "buffer_load_dwordx4 v[4:7], v69, s[28:31], s97 offen offset:1024 sc1\n\t" \
  "buffer_load_dwordx4 v[8:11], v69, s[28:31], s97 offen offset:2048 sc1\n\t" \
  "buffer_load_dwordx4 v[12:15], v69, s[28:31], s97 offen offset:3072 sc1\n\t" \
  "v_lshrrev_b32 v20, 2, v69\n\t" \
  "v_and_b32 v20, 16, v20\n\t" \
  "v_mov_b32 v21, 0x3f80\n\t" \
  "v_lshlrev_b32 v21, v20, v21\n\t" \
  "v_bfe_u32 v20, v69, 4, 2\n\t" \
  "v_cmp_eq_u32 vcc, 0, v20\n\t" \
  "s_nop 1\n\t" \
  "v_cndmask_b32 v32, 0, v21, vcc\n\t" \
  "v_cmp_eq_u32 vcc, 1, v20\n\t" \
  "s_nop 1\n\t" \
  "v_cndmask_b32 v33, 0, v21, vcc\n\t" \
  "v_cmp_eq_u32 vcc, 2, v20\n\t" \
  "s_nop 1\n\t" \
  "v_cndmask_b32 v34, 0, v21, vcc\n\t" \
  "v_cmp_eq_u32 vcc, 3, v20\n\t" \
  "s_nop 1\n\t" \
  "v_cndmask_b32 v35, 0, v21, vcc\n\t" \
  "v_bfe_u32 v38, v69, 7, 1\n\t" \
  "v_bfe_u32 v20, v69, 9, 1\n\t" \
  "v_lshl_add_u32 v38, v20, 2, v38\n\t" \
  "v_lshlrev_b32 v38, 12, v38\n\t" \
  "v_and_b32 v20, 0x60, v69\n\t" \
  "v_add_u32 v38, v38, v20\n\t" \
  "v_bfe_u32 v20, v69, 8, 1\n\t" \
  "v_lshl_add_u32 v38, v20, 4, v38\n\t" \
  "v_bfe_u32 v20, v69, 4, 1\n\t" \
  "v_lshl_add_u32 v38, v20, 3, v38\n\t" \
  "v_mov_b32 v44, 0\n\t" \
  "s_waitcnt vmcnt(3)\n\t" \
  "v_mfma_f32_16x16x32_bf16 v[40:43], v[32:35], v[0:3], 0\n\t" \
  "v_max3_u32 v44, v44, v0, v1\n\t" \
  "v_max3_u32 v44, v44, v2, v3\n\t" \
  "s_waitcnt vmcnt(2)\n\t" \
  "v_mfma_f32_16x16x32_bf16 v[40:43], v[32:35], v[4:7], v[40:43]\n\t" \
  "v_max3_u32 v44, v44, v4, v5\n\t" \
  "v_max3_u32 v44, v44, v6, v7\n\t" \
  "s_waitcnt vmcnt(1)\n\t" \
  "v_mfma_f32_16x16x32_bf16 v[40:43], v[32:35], v[8:11], v[40:43]\n\t" \
  "v_max3_u32 v44, v44, v8, v9\n\t" \
  "v_max3_u32 v44, v44, v10, v11\n\t" \
  "s_waitcnt vmcnt(0)\n\t" \
  "v_mfma_f32_16x16x32_bf16 v[40:43], v[32:35], v[12:15], v[40:43]\n\t" \
  "v_max3_u32 v44, v44, v12, v13\n\t" \
  "v_max3_u32 v44, v44, v14, v15\n\t" \
  "v_cmp_eq_u32 vcc, -1, v44\n\t" \
  "s_cbranch_vccnz RFAIL1_%=\n\t" \
  "s_nop 3\n\t" \
  "v_add_f32_dpp v40, v40, v40 row_ror:8 row_mask:0xf bank_mask:0xf\n\t" \
  "v_add_f32_dpp v41, v41, v41 row_ror:8 row_mask:0xf bank_mask:0xf\n\t" \
  "v_add_f32_dpp v42, v42, v42 row_ror:8 row_mask:0xf bank_mask:0xf\n\t" \
  "v_add_f32_dpp v43, v43, v43 row_ror:8 row_mask:0xf bank_mask:0xf\n\t" \
  "v_cvt_pk_bf16_f32 v40, v40, v41\n\t" \
  "v_cvt_pk_bf16_f32 v41, v42, v43\n\t" \
  "buffer_store_dwordx2 v[40:41], v38, s[28:31], s98 offen sc1\n\t" \
  "RDONE1_%=:\n\t" \
  "s_barrier\n\t" \
  "s_add_u32 s48, s48, 1\n\t" \
  "s_mov_b32 s49, s53\n\t" \
  "s_add_u32 s53, s53, " CH_SLOT "\n\t" \
  "s_add_u32 s60, s53, " CH_SKIP "\n\t" \
  "s_lshr_b32 s61, s96, 1\n\t" \
  "s_add_u32 s86, s86, 1\n\t" \
  "s_cmp_eq_u32 s86, 3\n\t" \
  "s_cselect_b32 s53, s60, s53\n\t" \
  "s_cselect_b32 s96, s61, s96\n\t" \
  "s_cselect_b32 s86, 0, s86\n\t" \
  "s_andn2_b32 s53, s53, 0x2000\n\t" \
  "s_and_b32 s60, s96, 1\n\t" \
  "s_lshl_b32 s60, s60, 13\n\t" \
  "s_or_b32 s53, s53, s60\n\t" \
  "s_mov_b32 s68, 0\n\t" \
  "s_add_u32 s97, s41, s53\n\t" \
  "s_add_u32 s98, s37, s53\n\t" \
  "s_add_u32 s82, s48, 0\n\t" \
  "s_lshr_b64 s[60:61], s[90:91], s82\n\t" \
  "s_and_b32 s60, s60, 1\n\t" \
  "s_lshl_b32 s60, s60, 13\n\t" \
  "s_add_u32 s82, s82, " CH_SG0 "\n\t" \
  "s_lshl_b32 s82, s82, 21\n\t" \
  "s_or_b32 s82, s82, s60\n\t" \
  "s_add_u32 s50, s82, s38\n\t" \
  "s_add_u32 s69, s82, s39\n\t" \
  "s_cmp_eq_u32 s34, 0\n\t" \
  "s_cbranch_scc1 LINI_%=\n\t" \
  "s_branch LG1_%=\n\t" \
  "RFAIL1_%=:\n\t" \
  "s_add_u32 s74, s74, 1\n\t" \
  "s_sleep " CH_RSL "\n\t" \
  "s_waitcnt vmcnt(0)\n\t" \
  "s_cmp_gt_u32 s74, s84\n\t" \
  "s_cbranch_scc1 RG1_%=\n\t" \
  "s_and_b32 s60, s74, 1023\n\t" \
  "s_cmp_lg_u32 s60, 0\n\t" \
  "s_cbranch_scc1 RRETRY1_%=\n\t" \
  "buffer_load_dword v46, off, s[28:31], s92 sc1\n\t" \
  "s_waitcnt vmcnt(0)\n\t" \
  "v_readfirstlane_b32 s60, v46\n\t" \
  "s_nop 0\n\t" \
  "s_cmp_eq_u32 s60, 0\n\t" \
  "s_cbranch_scc1 RRETRY1_%=\n\t" \
  "RG1_%=:\n\t" \
  "s_mov_b32 s84, 0\n\t" \
  "s_lshl_b32 s60, s36, 4\n\t" \
  "s_or_b32 s60, s60, s48\n\t" \
  "s_or_b32 s60, s60, 536870912\n\t" \
  "v_mov_b32 v76, s60\n\t" \
  "v_mov_b32 v77, 0\n\t" \
  "s_mov_b64 exec, 1\n\t" \
  "buffer_atomic_cmpswap v[76:77], off, s[28:31], s92 sc1\n\t" \
  "s_mov_b64 exec, -1\n\t" \
  "s_branch RDONE1_%=\n\t" \
  "LFAIL1_%=:\n\t" \
  "v_max_u32 v75, v0, v1\n\t" \
  "v_max3_u32 v75, v75, v2, v3\n\t" \
  "v_max3_u32 v75, v75, v4, v5\n\t" \
  "v_max3_u32 v75, v75, v6, v7\n\t" \
  "v_max3_u32 v75, v75, v8, v9\n\t" \
  "v_max3_u32 v75, v75, v10, v11\n\t" \
  "v_max3_u32 v75, v75, v12, v13\n\t" \
  "v_max3_u32 v75, v75, v14, v15\n\t" \
  "v_max3_u32 v75, v75, v16, v17\n\t" \
  "v_max3_u32 v75, v75, v18, v19\n\t" \
  "v_max3_u32 v75, v75, v20, v21\n\t" \
  "v_max3_u32 v75, v75, v22, v23\n\t" \
  "v_max3_u32 v75, v75, v24, v25\n\t" \
  "v_max3_u32 v75, v75, v26, v27\n\t" \
  "v_max3_u32 v75, v75, v28, v29\n\t" \
  "v_max3_u32 v75, v75, v30, v31\n\t" \
  "v_max3_u32 v75, v75, v32, v33\n\t" \
  "v_max3_u32 v75, v75, v34, v35\n\t" \
  "v_cmp_eq_u32 vcc, -1, v75\n\t" \
  "s_cbranch_vccz LGO1_%=\n\t" \
  "s_add_u32 s68, s68, 1\n\t" \
  "s_waitcnt lgkmcnt(0)\n\t" \
  "s_cmp_gt_u32 s68, s83\n\t" \
  "s_cbranch_scc1 LGV1_%=\n\t" \
  "s_barrier\n\t" \
  "s_waitcnt vmcnt(8)\n\t" \
  "s_branch LG1_%=\n\t" \
  "RWT1_%=f:\n\t" \
  "s_cmp_lt_i32 s73, -2\n\t" \
  "s_cbranch_scc0 RWT1_%=g\n\t" \
  "s_nop 4\n\t" \
  "s_branch RWT1_%=g\n\t" \
  "LGV1_%=:\n\t" \
  "s_mov_b32 s83, 0\n\t" \
  "s_lshl_b32 s60, s36, 4\n\t" \
  "s_or_b32 s60, s60, s48\n\t" \
  "s_or_b32 s60, s60, 805306368\n\t" \
  "v_mov_b32 v76, s60\n\t" \
  "v_mov_b32 v77, 0\n\t" \
  "s_mov_b64 exec, 1\n\t" \
  "buffer_atomic_cmpswap v[76:77], off, s[28:31], s92 sc1\n\t" \
  "s_mov_b64 exec, -1\n\t" \
  "s_branch LGO1_%=\n\t" \
  "LLAST1_%=:\n\t" \
  "s_waitcnt vmcnt(0) lgkmcnt(0)\n\t" \
  "s_barrier\n\t" \
  "s_branch LEND_%=\n\t" \
  ".p2align 6\n\t" \
  "LG2_%=:\n\t" \
  "s_barrier\n\t" \
  "buffer_load_dwordx4 v[52:55], v69, s[28:31], s50 offen sc1\n\t" \
  "buffer_load_dwordx4 v[56:59], v69, s[28:31], s50 offen offset:1024 sc1\n\t" \
  "s_add_u32 s64, s50, 18432\n\t" \
  "s_movk_i32 m0, 11264\n\t" \
  "v_lshlrev_b32 v75, 1, v69\n\t" \
  "buffer_load_dword v75, s[28:31], s64 offen sc1 lds\n\t" \
  "ds_read_b128 v[44:47], v70 offset:0\n\t" \
  "ds_read_b128 v[48:51], v70 offset:1024\n\t" \
  "ds_read_b128 v[60:63], v70 offset:2048\n\t" \
  "ds_read_b128 v[64:67], v70 offset:3072\n\t" \
  "v_mov_b64 v[36:37], 0\n\t" \
  "v_mov_b64 v[38:39], 0\n\t" \
  "v_mov_b64 v[40:41], 0\n\t" \
  "v_mov_b64 v[42:43], 0\n\t" \
  "v_lshrrev_b32 v71, 1, v69\n\t" \
  "v_lshrrev_b32 v72, 3, v69\n\t" \
  "v_and_b32 v72, 0x78, v72\n\t" \
  "v_and_b32 v74, 0x30, v69\n\t" \
  "v_lshl_add_u32 v72, v74, 3, v72\n\t" \
  "s_add_u32 s54, s53, s36\n\t" \
  "s_add_u32 s55, s54, 557056\n\t" \
  "s_add_u32 s54, s54, 131072\n\t" \
  "s_add_u32 s75, s54, 262144\n\t" \
  "s_add_u32 s80, s53, s26\n\t" \
  "s_barrier\n\t" \
  "ds_read_b128 v[0:3], v69 offset:0\n\t" \
  "ds_read_b128 v[4:7], v69 offset:1024\n\t" \
  "ds_read_b128 v[8:11], v69 offset:2048\n\t" \
  "ds_read_b128 v[12:15], v69 offset:3072\n\t" \
  "s_waitcnt lgkmcnt(3)\n\t" \
  "v_smfmac_f32_16x16x64_bf16 v[36:39], v[0:3], v[44:51], v68\n\t" \
  "ds_read_b128 v[44:47], v70 offset:4096\n\t" \
  "v_smfmac_f32_16x16x64_bf16 v[40:43], v[0:3], v[60:67], v68\n\t" \
  "ds_read_b128 v[48:51], v70 offset:5120\n\t" \
  "s_waitcnt lgkmcnt(4)\n\t" \
  "v_smfmac_f32_16x16x64_bf16 v[36:39], v[4:7], a[32:39], v68\n\t" \
  "s_barrier\n\t" \
  "ds_read_b128 v[16:19], v69 offset:4096\n\t" \
  "v_smfmac_f32_16x16x64_bf16 v[40:43], v[4:7], a[40:47], v68\n\t" \
  "ds_read_b128 v[20:23], v69 offset:5120\n\t" \
  "s_waitcnt lgkmcnt(5)\n\t" \
  "v_smfmac_f32_16x16x64_bf16 v[36:39], v[8:11], a[80:87], v68\n\t" \
  "ds_read_b128 v[24:27], v69 offset:6144\n\t" \
  "v_smfmac_f32_16x16x64_bf16 v[40:43], v[8:11], a[88:95], v68\n\t" \
  "ds_read_b128 v[28:31], v69 offset:7168\n\t" \
  "s_waitcnt lgkmcnt(6)\n\t" \
  "v_smfmac_f32_16x16x64_bf16 v[36:39], v[12:15], a[120:127], v68\n\t" \
  "ds_read_b128 v[32:35], v69 offset:8192\n\t" \
  "s_waitcnt lgkmcnt(5)\n\t" \
  "v_smfmac_f32_16x16x64_bf16 v[40:43], v[12:15], v[44:51], v68\n\t" \
  "ds_read_b128 v[60:63], v70 offset:6144\n\t" \
  "s_waitcnt lgkmcnt(5)\n\t" \
  "v_smfmac_f32_16x16x64_bf16 v[36:39], v[16:19], a[160:167], v68\n\t" \
  "ds_read_b128 v[64:67], v70 offset:7168\n\t" \
  "v_smfmac_f32_16x16x64_bf16 v[40:43], v[16:19], a[168:175], v68\n\t" \
  "s_waitcnt lgkmcnt(5)\n\t" \
  "v_mov_b64 v[44:45], 0\n\t" \
  "v_smfmac_f32_16x16x64_bf16 v[36:39], v[20:23], a[200:207], v68\n\t" \
  "s_waitcnt lgkmcnt(4)\n\t" \
  "v_mov_b64 v[46:47], 0\n\t" \
  "v_smfmac_f32_16x16x64_bf16 v[36:39], v[24:27], a[240:247], v68\n\t" \
  "s_waitcnt lgkmcnt(2)\n\t" \
  "v_mov_b64 v[48:49], 0\n\t" \
  "v_smfmac_f32_16x16x64_bf16 v[36:39], v[28:31], v[112:119], v68\n\t" \
  "v_mov_b64 v[50:51], 0\n\t" \
  "v_smfmac_f32_16x16x64_bf16 v[36:39], v[32:35], v[160:167], v68\n\t" \
  "s_waitcnt lgkmcnt(0)\n\t" \
  "v_smfmac_f32_16x16x64_bf16 v[40:43], v[20:23], v[60:67], v68\n\t" \
  "ds_read_b128 v[60:63], v70 offset:8192\n\t" \
  "v_smfmac_f32_16x16x64_bf16 v[40:43], v[24:27], a[248:255], v68\n\t" \
  "ds_read_b128 v[64:67], v70 offset:9216\n\t" \
  "v_add_f32 v74, v36, v38\n\t" \
  "v_smfmac_f32_16x16x64_bf16 v[40:43], v[28:31], v[120:127], v68\n\t" \
  "v_add_f32 v37, v37, v39\n\t" \
  "v_smfmac_f32_16x16x64_bf16 v[40:43], v[32:35], v[168:175], v68\n\t" \
  "v_cmp_u_f32 vcc, v74, v37\n\t" \
  "v_smfmac_f32_16x16x64_bf16 v[44:47], v[0:3], a[0:7], v68\n\t" \
  "s_cbranch_vccnz LFAIL2_%=\n\t" \
  "LGO2_%=:\n\t" \
  "v_cvt_pk_bf16_f32 v74, v74, v37\n\t" \
  "v_smfmac_f32_16x16x64_bf16 v[44:47], v[4:7], a[48:55], v68\n\t" \
  "s_waitcnt lgkmcnt(0)\n\t" \
  "v_add_f32 v75, v40, v42\n\t" \
  "v_smfmac_f32_16x16x64_bf16 v[44:47], v[8:11], v[60:67], v68\n\t" \
  "ds_read_b128 v[60:63], v70 offset:10240\n\t" \
  "v_add_f32 v41, v41, v43\n\t" \
  "v_cvt_pk_bf16_f32 v75, v75, v41\n\t" \
  "v_smfmac_f32_16x16x64_bf16 v[44:47], v[12:15], a[128:135], v68\n\t" \
  "ds_write2st64_b32 v71, v74, v75 offset0:40 offset1:42\n\t" \
  "v_smfmac_f32_16x16x64_bf16 v[44:47], v[16:19], a[176:183], v68\n\t" \
  "ds_read_b128 v[64:67], v70 offset:11264\n\t" \
  "v_smfmac_f32_16x16x64_bf16 v[44:47], v[20:23], a[208:215], v68\n\t" \
  "v_smfmac_f32_16x16x64_bf16 v[44:47], v[24:27], v[80:87], v68\n\t" \
  "v_smfmac_f32_16x16x64_bf16 v[44:47], v[28:31], v[128:135], v68\n\t" \
  "s_waitcnt lgkmcnt(0)\n\t" \
  "v_smfmac_f32_16x16x64_bf16 v[44:47], v[32:35], v[60:67], v68\n\t" \
  "s_barrier\n\t" \
  "ds_read_b64_tr_b16 v[76:77], v72 offset:10240\n\t" \
  "v_smfmac_f32_16x16x64_bf16 v[48:51], v[0:3], a[8:15], v68\n\t" \
  "ds_read_b64_tr_b16 v[78:79], v72 offset:10752\n\t" \
  "v_smfmac_f32_16x16x64_bf16 v[48:51], v[4:7], a[56:63], v68\n\t" \
  "buffer_load_dwordx4 v[60:63], v69, s[28:31], s64 offen sc1\n\t" \
  "v_add_f32 v74, v44, v46\n\t" \
  "v_smfmac_f32_16x16x64_bf16 v[48:51], v[8:11], a[96:103], v68\n\t" \
  "v_add_f32 v45, v45, v47\n\t" \
  "v_smfmac_f32_16x16x64_bf16 v[48:51], v[12:15], a[136:143], v68\n\t" \
  "buffer_load_dwordx4 v[64:67], v69, s[28:31], s64 offen offset:1024 sc1\n\t" \
  "s_waitcnt vmcnt(4)\n\t" \
  "s_waitcnt lgkmcnt(0)\n\t" \
  "v_cvt_pk_bf16_f32 v74, v74, v45\n\t" \
  "v_mfma_f32_16x16x32_bf16 v[36:39], v[52:55], v[76:79], 0\n\t" \
  "s_waitcnt vmcnt(3)\n\t" \
  "v_mfma_f32_16x16x32_bf16 v[40:43], v[56:59], v[76:79], 0\n\t" \
  "v_mov_b64 v[52:53], 0\n\t" \
  "v_mov_b64 v[54:55], 0\n\t" \
  "v_smfmac_f32_16x16x64_bf16 v[48:51], v[16:19], a[184:191], v68\n\t" \
  "v_mov_b64 v[56:57], 0\n\t" \
  "v_mov_b64 v[58:59], 0\n\t" \
  "v_smfmac_f32_16x16x64_bf16 v[48:51], v[20:23], a[216:223], v68\n\t" \
  "ds_write_b32 v71, v74 offset:1024\n\t" \
  "v_add_f32_dpp v36, v40, v36 quad_perm:[2,3,0,1] row_mask:0xf bank_mask:0xf\n\t" \
  "v_smfmac_f32_16x16x64_bf16 v[48:51], v[24:27], v[88:95], v68\n\t" \
  "v_add_f32_dpp v37, v41, v37 quad_perm:[2,3,0,1] row_mask:0xf bank_mask:0xf\n\t" \
  "v_smfmac_f32_16x16x64_bf16 v[48:51], v[28:31], v[136:143], v68\n\t" \
  "v_add_f32_dpp v38, v42, v38 quad_perm:[2,3,0,1] row_mask:0xf bank_mask:0xf\n\t" \
  "v_smfmac_f32_16x16x64_bf16 v[48:51], v[32:35], v[176:183], v68\n\t" \
  "v_add_f32_dpp v39, v43, v39 quad_perm:[2,3,0,1] row_mask:0xf bank_mask:0xf\n\t" \
  "v_smfmac_f32_16x16x64_bf16 v[52:55], v[0:3], a[16:23], v68\n\t" \
  "s_mov_b64 exec, s[58:59]\n\t" \
  "ds_write_b128 v69, v[36:39] offset:3072\n\t" \
  "s_mov_b64 exec, -1\n\t" \
  "v_smfmac_f32_16x16x64_bf16 v[52:55], v[4:7], a[64:71], v68\n\t" \
  "v_add_f32 v75, v48, v50\n\t" \
  "v_add_f32 v49, v49, v51\n\t" \
  "v_cvt_pk_bf16_f32 v75, v75, v49\n\t" \
  "v_smfmac_f32_16x16x64_bf16 v[56:59], v[0:3], a[24:31], v68\n\t" \
  "ds_write_b32 v71, v75 offset:1536\n\t" \
  "v_smfmac_f32_16x16x64_bf16 v[56:59], v[4:7], a[72:79], v68\n\t" \
  "ds_read_b64_tr_b16 v[0:1], v72 offset:9216\n\t" \
  "ds_read_b64_tr_b16 v[2:3], v72 offset:9728\n\t" \
  "v_smfmac_f32_16x16x64_bf16 v[52:55], v[8:11], a[104:111], v68\n\t" \
  "ds_read_b128 v[36:39], v70 offset:12288\n\t" \
  "v_smfmac_f32_16x16x64_bf16 v[56:59], v[8:11], a[112:119], v68\n\t" \
  "ds_read_b128 v[40:43], v70 offset:13312\n\t" \
  "v_smfmac_f32_16x16x64_bf16 v[52:55], v[12:15], a[144:151], v68\n\t" \
  "s_waitcnt lgkmcnt(4)\n\t" \
  "s_barrier\n\t" \
  "ds_read_b64_tr_b16 v[44:45], v72 offset:1024\n\t" \
  "ds_read_b64_tr_b16 v[46:47], v72 offset:1536\n\t" \
  "v_smfmac_f32_16x16x64_bf16 v[56:59], v[12:15], a[152:159], v68\n\t" \
  "ds_read_b64_tr_b16 v[48:49], v72 offset:0\n\t" \
  "ds_read_b64_tr_b16 v[50:51], v72 offset:512\n\t" \
  "v_smfmac_f32_16x16x64_bf16 v[52:55], v[16:19], a[192:199], v68\n\t" \
  "ds_read_b128 v[4:7], v69 offset:2048\n\t" \
  "s_waitcnt lgkmcnt(5)\n\t" \
  "v_smfmac_f32_16x16x64_bf16 v[56:59], v[16:19], v[36:43], v68\n\t" \
  "ds_read_b128 v[8:11], v69 offset:3072\n\t" \
  "s_waitcnt lgkmcnt(4)\n\t" \
  "v_mfma_f32_16x16x32_bf16 v[12:15], v[44:47], v[76:79], 0\n\t" \
  "ds_read_b128 v[36:39], v70 offset:14336\n\t" \
  "s_waitcnt lgkmcnt(3)\n\t" \
  "v_mfma_f32_16x16x32_bf16 v[12:15], v[48:51], v[0:3], v[12:15]\n\t" \
  "ds_read_b128 v[40:43], v70 offset:15360\n\t" \
  "v_smfmac_f32_16x16x64_bf16 v[52:55], v[20:23], a[224:231], v68\n\t" \
  "buffer_store_dwordx4 v[44:47], v69, s[28:31], s69 offen offset:1024 sc1\n\t" \
  "s_waitcnt lgkmcnt(2)\n\t" \
  "v_add_f32 v4, v4, v8\n\t" \
  "v_add_f32 v5, v5, v9\n\t" \
  "v_add_f32 v6, v6, v10\n\t" \
  "v_cndmask_b32 v78, v13, v14, s[56:57]\n\t" \
  "v_cndmask_b32 v76, v12, v15, s[56:57]\n\t" \
  "v_add_f32 v7, v7, v11\n\t" \
  "v_max3_f32 v77, v4, v5, v6\n\t" \
  "v_add_f32_dpp v76, v78, v76 quad_perm:[2,3,0,1] row_mask:0xf bank_mask:0xf\n\t" \
  "v_max3_f32 v77, v77, v7, v76\n\t" \
  "v_smfmac_f32_16x16x64_bf16 v[56:59], v[20:23], a[232:239], v68\n\t" \
  "ds_read_b128 v[44:47], v70 offset:16384\n\t" \
  "v_max_f32_dpp v77, v77, v77 quad_perm:[2,3,0,1] row_mask:0xf bank_mask:0xf\n\t" \
  "v_mov_b32 v78, v77\n\t" \
  "v_smfmac_f32_16x16x64_bf16 v[52:55], v[24:27], v[96:103], v68\n\t" \
  "ds_read_b128 v[48:51], v70 offset:17408\n\t" \
  "v_permlane16_swap_b32 v77, v78\n\t" \
  "v_max_f32 v77, v77, v78\n\t" \
  "v_mov_b32 v78, v77\n\t" \
  "v_smfmac_f32_16x16x64_bf16 v[52:55], v[28:31], v[144:151], v68\n\t" \
  "ds_read_b128 v[16:19], v70 offset:18432\n\t" \
  "v_permlane32_swap_b32 v77, v78\n\t" \
  "v_max_f32 v77, v77, v78\n\t" \
  "v_smfmac_f32_16x16x64_bf16 v[52:55], v[32:35], v[184:191], v68\n\t" \
  "ds_read_b128 v[20:23], v70 offset:19456\n\t" \
  "v_mul_f32 v77, 0xbe0293ee, v77\n\t" \
  "v_smfmac_f32_16x16x64_bf16 v[56:59], v[24:27], v[104:111], v68\n\t" \
  "v_fmamk_f32 v4, v4, 0x3e0293ee, v77\n\t" \
  "v_smfmac_f32_16x16x64_bf16 v[56:59], v[28:31], v[152:159], v68\n\t" \
  "ds_read_b128 v[24:27], v70 offset:22528\n\t" \
  "s_waitcnt lgkmcnt(4)\n\t" \
  "v_fmamk_f32 v5, v5, 0x3e0293ee, v77\n\t" \
  "v_smfmac_f32_16x16x64_bf16 v[56:59], v[32:35], v[36:43], v68\n\t" \
  "ds_read_b128 v[28:31], v70 offset:23552\n\t" \
  "v_fmamk_f32 v6, v6, 0x3e0293ee, v77\n\t" \
  "v_fmamk_f32 v7, v7, 0x3e0293ee, v77\n\t" \
  "v_fmamk_f32 v76, v76, 0x3e0293ee, v77\n\t" \
  "v_exp_f32 v4, v4\n\t" \
  "v_exp_f32 v5, v5\n\t" \
  "v_exp_f32 v6, v6\n\t" \
  "v_exp_f32 v7, v7\n\t" \
  "v_cvt_pk_bf16_f32 v4, v4, v5\n\t" \
  "v_exp_f32 v76, v76\n\t" \
  "v_cvt_pk_bf16_f32 v5, v6, v7\n\t" \
  "v_mov_b32_dpp v12, v4 quad_perm:[0,1,0,1] row_mask:0xf bank_mask:0xf\n\t" \
  "v_mov_b32_dpp v14, v4 quad_perm:[2,3,2,3] row_mask:0xf bank_mask:0xf\n\t" \
  "v_mov_b32_dpp v13, v5 quad_perm:[0,1,0,1] row_mask:0xf bank_mask:0xf\n\t" \
  "v_mov_b32_dpp v15, v5 quad_perm:[2,3,2,3] row_mask:0xf bank_mask:0xf\n\t" \
  "v_add_f32 v52, v52, v54\n\t" \
  "v_add_f32 v53, v53, v55\n\t" \
  "s_waitcnt vmcnt(2)\n\t" \
  "v_mfma_f32_16x16x32_bf16 v[0:3], v[60:63], v[12:15], 0\n\t" \
  "ds_read_b128 v[36:39], v70 offset:20480\n\t" \
  "v_add_f32 v56, v56, v58\n\t" \
  "v_add_f32 v57, v57, v59\n\t" \
  "v_cvt_pk_bf16_f32 v52, v52, v53\n\t" \
  "v_cvt_pk_bf16_f32 v53, v56, v57\n\t" \
  "s_waitcnt vmcnt(1)\n\t" \
  "v_mfma_f32_16x16x32_bf16 v[4:7], v[64:67], v[12:15], 0\n\t" \
  "ds_read_b128 v[40:43], v70 offset:21504\n\t" \
  "v_mov_b64 v[56:57], s[88:89]\n\t" \
  "v_mov_b64 v[58:59], s[88:89]\n\t" \
  "v_mov_b32_dpp v78, v76 quad_perm:[0,1,0,1] row_mask:0xf bank_mask:0xf\n\t" \
  "v_mov_b32_dpp v79, v76 quad_perm:[2,3,2,3] row_mask:0xf bank_mask:0xf\n\t" \
  "v_mfma_f32_16x16x32_bf16 v[32:35], v[56:59], v[12:15], 0\n\t" \
  "ds_read_b128 v[60:63], v70 offset:24576\n\t" \
  "v_cvt_pk_bf16_f32 v11, v78, v79\n\t" \
  "v_cvt_pk_bf16_f32 v8, v78, v79\n\t" \
  "v_mov_b64 v[54:55], 0\n\t" \
  "v_mov_b32 v9, 0\n\t" \
  "v_mov_b32 v10, 0\n\t" \
  "v_mov_b64 v[14:15], 0\n\t" \
  "s_nop 0\n\t" \
  "v_mfma_f32_16x16x32_bf16 v[32:35], v[54:57], v[8:11], v[32:35]\n\t" \
  "buffer_store_dwordx2 v[52:53], v71, s[28:31], s69 offen offset:3072 sc1\n\t" \
  "s_cmp_eq_u32 s48, 63\n\t" \
  "v_mfma_f32_16x16x32_bf16 v[0:3], v[52:55], v[8:11], v[0:3]\n\t" \
  "ds_read_b128 v[64:67], v70 offset:25600\n\t" \
  "v_mov_b64 v[56:57], 0\n\t" \
  "v_mov_b64 v[58:59], 0\n\t" \
  "v_mfma_f32_16x16x32_bf16 v[4:7], v[52:55], v[10:13], v[4:7]\n\t" \
  "s_cbranch_scc1 LLAST2_%=\n\t" \
  "v_rcp_f32 v74, v32\n\t" \
  "v_mov_b64 v[12:13], 0\n\t" \
  "v_mov_b32 v8, 0\n\t" \
  "v_mul_f32 v0, v0, v74\n\t" \
  "v_mul_f32 v1, v1, v74\n\t" \
  "v_mul_f32 v2, v2, v74\n\t" \
  "v_mul_f32 v3, v3, v74\n\t" \
  "v_cvt_pk_bf16_f32 v76, v0, v1\n\t" \
  "v_cvt_pk_bf16_f32 v77, v2, v3\n\t" \
  "v_pk_mul_f32 v[4:5], v[4:5], v[74:75] op_sel_hi:[1,0]\n\t" \
  "v_pk_mul_f32 v[6:7], v[6:7], v[74:75] op_sel_hi:[1,0]\n\t" \
  "v_cvt_pk_bf16_f32 v78, v4, v5\n\t" \
  "v_cvt_pk_bf16_f32 v79, v6, v7\n\t" \
  "ds_write_b128 v69, v[76:79] offset:6144\n\t" \
  "s_waitcnt lgkmcnt(2)\n\t" \
  "v_smfmac_f32_16x16x64_bf16 v[12:15], v[76:79], v[44:51], v68\n\t" \
  "ds_read_b128 v[44:47], v70 offset:26624\n\t" \
  "v_mov_b32 v11, 0\n\t" \
  "v_smfmac_f32_16x16x64_bf16 v[56:59], v[76:79], v[16:23], v68\n\t" \
  "ds_read_b128 v[48:51], v70 offset:27648\n\t" \
  "v_mov_b64 v[52:53], 0\n\t" \
  "v_smfmac_f32_16x16x64_bf16 v[8:11], v[76:79], v[36:43], v68\n\t" \
  "ds_read_b128 v[16:19], v70 offset:28672\n\t" \
  "v_smfmac_f32_16x16x64_bf16 v[52:55], v[76:79], v[24:31], v68\n\t" \
  "s_waitcnt lgkmcnt(3)\n\t" \
  "s_barrier\n\t" \
  "ds_read_b128 v[0:3], v69 offset:7168\n\t" \
  "ds_read_b128 v[4:7], v69 offset:4096\n\t" \
  "v_lshrrev_b32 v71, 7, v69\n\t" \
  "v_and_b32 v74, 0x70, v69\n\t" \
  "v_lshl_add_u32 v71, v71, 14, v74\n\t" \
  "v_bfe_u32 v72, v69, 2, 7\n\t" \
  "v_and_b32 v74, 0x200, v69\n\t" \
  "s_waitcnt lgkmcnt(1)\n\t" \
  "v_smfmac_f32_16x16x64_bf16 v[8:11], v[0:3], v[60:67], v68\n\t" \
  "ds_read_b128 v[20:23], v70 offset:29696\n\t" \
  "v_mov_b64 v[76:77], 0\n\t" \
  "v_smfmac_f32_16x16x64_bf16 v[12:15], v[0:3], v[200:207], v68\n\t" \
  "buffer_load_dword v75, off, s[28:31], s80 sc1\n\t" \
  "ds_read_b128 v[32:35], v69 offset:5120\n\t" \
  "v_mov_b64 v[78:79], 0\n\t" \
  "v_smfmac_f32_16x16x64_bf16 v[52:55], v[0:3], v[44:51], v68\n\t" \
  "ds_read_b128 v[24:27], v70 offset:30720\n\t" \
  "v_lshl_add_u32 v72, v74, 3, v72\n\t" \
  "v_smfmac_f32_16x16x64_bf16 v[56:59], v[0:3], v[232:239], v68\n\t" \
  "ds_read_b128 v[28:31], v70 offset:31744\n\t" \
  "s_waitcnt lgkmcnt(4)\n\t" \
  "v_smfmac_f32_16x16x64_bf16 v[12:15], v[4:7], v[208:215], v68\n\t" \
  "ds_read_b128 v[60:63], v70 offset:32768\n\t" \
  "s_waitcnt lgkmcnt(4)\n\t" \
  "v_smfmac_f32_16x16x64_bf16 v[8:11], v[4:7], v[16:23], v68\n\t" \
  "ds_read_b128 v[64:67], v70 offset:33792\n\t" \
  "s_waitcnt lgkmcnt(4)\n\t" \
  "v_smfmac_f32_16x16x64_bf16 v[8:11], v[32:35], v[192:199], v68\n\t" \
  "ds_read_b128 v[36:39], v73 offset:4096\n\t" \
  "v_smfmac_f32_16x16x64_bf16 v[12:15], v[32:35], v[216:223], v68\n\t" \
  "ds_read_b128 v[40:43], v73 offset:6144\n\t" \
  "s_waitcnt lgkmcnt(4)\n\t" \
  "v_smfmac_f32_16x16x64_bf16 v[52:55], v[4:7], v[24:31], v68\n\t" \
  "ds_read_b128 v[44:47], v70 offset:34816\n\t" \
  "v_cvt_pk_bf16_f32 v8, v8, v9\n\t" \
  "v_smfmac_f32_16x16x64_bf16 v[52:55], v[32:35], v[224:231], v68\n\t" \
  "ds_read_b128 v[48:51], v70 offset:35840\n\t" \
  "v_cvt_pk_bf16_f32 v9, v10, v11\n\t" \
  "v_smfmac_f32_16x16x64_bf16 v[56:59], v[4:7], v[240:247], v68\n\t" \
  "s_waitcnt lgkmcnt(4)\n\t" \
  "v_cvt_pk_bf16_f32 v10, v12, v13\n\t" \
  "v_smfmac_f32_16x16x64_bf16 v[56:59], v[32:35], v[60:67], v68\n\t" \
  "s_waitcnt lgkmcnt(3)\n\t" \
  "v_cvt_pk_bf16_f32 v11, v14, v15\n\t" \
  "v_smfmac_f32_16x16x64_bf16 v[76:79], v[36:39], v[248:255], v68\n\t" \
  "buffer_store_dwordx4 v[8:11], v71, s[28:31], s54 offen sc1\n\t" \
  "s_waitcnt lgkmcnt(0)\n\t" \
  "v_cvt_pk_bf16_f32 v52, v52, v53\n\t" \
  "v_smfmac_f32_16x16x64_bf16 v[76:79], v[40:43], v[44:51], v68\n\t" \
  "v_cvt_pk_bf16_f32 v53, v54, v55\n\t" \
  "v_cvt_pk_bf16_f32 v54, v56, v57\n\t" \
  "v_cvt_pk_bf16_f32 v55, v58, v59\n\t" \
  "buffer_store_dwordx4 v[52:55], v71, s[28:31], s75 offen sc1\n\t" \
  "s_nop 2\n\t" \
  "v_add_f32 v76, v76, v78\n\t" \
  "v_add_f32 v77, v77, v79\n\t" \
  "v_cvt_pk_bf16_f32 v76, v76, v77\n\t" \
  "buffer_store_dword v76, v72, s[28:31], s55 offen sc1\n\t" \
  "s_memrealtime s[70:71]\n\t" \
  "s_waitcnt vmcnt(3)\n\t" \
  "v_readfirstlane_b32 s65, v75\n\t" \
  "s_barrier\n\t" \
  "s_waitcnt lgkmcnt(0)\n\t" \
  "s_not_b32 s65, s65\n\t" \
  "s_lshl_b32 s61, s70, 8\n\t" \
  "s_andn2_b32 s64, s65, 0xff\n\t" \
  "s_sub_u32 s64, s64, s61\n\t" \
  "s_ashr_i32 s64, s64, 8\n\t" \
  "s_cmp_eq_u32 s65, 0\n\t" \
  "s_cselect_b32 s64, " CH_DLF ", s64\n\t" \
  "s_min_i32 s64, s64, s34\n\t" \
  "s_max_i32 s64, s64, " CH_DLL1 "\n\t" \
  "s_add_u32 s64, s64, s70\n\t" \
  "s_mov_b32 s74, 0\n\t" \
  "s_movk_i32 s60, 8192\n\t" \
  "RWT2_%=:\n\t" \
  "s_memrealtime s[72:73]\n\t" \
  "s_sub_u32 s60, s60, 1\n\t" \
  "s_cselect_b32 s61, 0, -1\n\t" \
  "s_waitcnt lgkmcnt(0)\n\t" \
  "s_sub_i32 s73, s72, s64\n\t" \
  "s_and_b32 s73, s73, s61\n\t" \
  "s_cmp_lt_i32 s73, " CH_FIN "\n\t" \
  "s_cbranch_scc1 RWT2_%=\n\t" \
  "s_cmp_lt_i32 s73, 0\n\t" \
  "s_cbranch_scc1 RWT2_%=f\n\t" \
  "RWT2_%=g:\n\t" \
  "RRETRY2_%=:\n\t" \
  "buffer_load_dwordx4 v[0:3], v69, s[28:31], s97 offen offset:0 sc1\n\t" \
  "buffer_load_dwordx4 v[4:7], v69, s[28:31], s97 offen offset:1024 sc1\n\t" \
  "buffer_load_dwordx4 v[8:11], v69, s[28:31], s97 offen offset:2048 sc1\n\t" \
  "buffer_load_dwordx4 v[12:15], v69, s[28:31], s97 offen offset:3072 sc1\n\t" \
  "v_lshrrev_b32 v20, 2, v69\n\t" \
  "v_and_b32 v20, 16, v20\n\t" \
  "v_mov_b32 v21, 0x3f80\n\t" \
  "v_lshlrev_b32 v21, v20, v21\n\t" \
  "v_bfe_u32 v20, v69, 4, 2\n\t" \
  "v_cmp_eq_u32 vcc, 0, v20\n\t" \
  "s_nop 1\n\t" \
  "v_cndmask_b32 v32, 0, v21, vcc\n\t" \
  "v_cmp_eq_u32 vcc, 1, v20\n\t" \
  "s_nop 1\n\t" \
  "v_cndmask_b32 v33, 0, v21, vcc\n\t" \
  "v_cmp_eq_u32 vcc, 2, v20\n\t" \
  "s_nop 1\n\t" \
  "v_cndmask_b32 v34, 0, v21, vcc\n\t" \
  "v_cmp_eq_u32 vcc, 3, v20\n\t" \
  "s_nop 1\n\t" \
  "v_cndmask_b32 v35, 0, v21, vcc\n\t" \
  "v_bfe_u32 v38, v69, 7, 1\n\t" \
  "v_bfe_u32 v20, v69, 9, 1\n\t" \
  "v_lshl_add_u32 v38, v20, 2, v38\n\t" \
  "v_lshlrev_b32 v38, 12, v38\n\t" \
  "v_and_b32 v20, 0x60, v69\n\t" \
  "v_add_u32 v38, v38, v20\n\t" \
  "v_bfe_u32 v20, v69, 8, 1\n\t" \
  "v_lshl_add_u32 v38, v20, 4, v38\n\t" \
  "v_bfe_u32 v20, v69, 4, 1\n\t" \
  "v_lshl_add_u32 v38, v20, 3, v38\n\t" \
  "v_mov_b32 v44, 0\n\t" \
  "s_waitcnt vmcnt(3)\n\t" \
  "v_mfma_f32_16x16x32_bf16 v[40:43], v[32:35], v[0:3], 0\n\t" \
  "v_max3_u32 v44, v44, v0, v1\n\t" \
  "v_max3_u32 v44, v44, v2, v3\n\t" \
  "s_waitcnt vmcnt(2)\n\t" \
  "v_mfma_f32_16x16x32_bf16 v[40:43], v[32:35], v[4:7], v[40:43]\n\t" \
  "v_max3_u32 v44, v44, v4, v5\n\t" \
  "v_max3_u32 v44, v44, v6, v7\n\t" \
  "s_waitcnt vmcnt(1)\n\t" \
  "v_mfma_f32_16x16x32_bf16 v[40:43], v[32:35], v[8:11], v[40:43]\n\t" \
  "v_max3_u32 v44, v44, v8, v9\n\t" \
  "v_max3_u32 v44, v44, v10, v11\n\t" \
  "s_waitcnt vmcnt(0)\n\t" \
  "v_mfma_f32_16x16x32_bf16 v[40:43], v[32:35], v[12:15], v[40:43]\n\t" \
  "v_max3_u32 v44, v44, v12, v13\n\t" \
  "v_max3_u32 v44, v44, v14, v15\n\t" \
  "v_cmp_eq_u32 vcc, -1, v44\n\t" \
  "s_cbranch_vccnz RFAIL2_%=\n\t" \
  "s_nop 3\n\t" \
  "v_add_f32_dpp v40, v40, v40 row_ror:8 row_mask:0xf bank_mask:0xf\n\t" \
  "v_add_f32_dpp v41, v41, v41 row_ror:8 row_mask:0xf bank_mask:0xf\n\t" \
  "v_add_f32_dpp v42, v42, v42 row_ror:8 row_mask:0xf bank_mask:0xf\n\t" \
  "v_add_f32_dpp v43, v43, v43 row_ror:8 row_mask:0xf bank_mask:0xf\n\t" \
  "v_cvt_pk_bf16_f32 v40, v40, v41\n\t" \
  "v_cvt_pk_bf16_f32 v41, v42, v43\n\t" \
  "buffer_store_dwordx2 v[40:41], v38, s[28:31], s98 offen sc1\n\t" \
  "RDONE2_%=:\n\t" \
  "s_barrier\n\t" \
  "s_add_u32 s48, s48, 1\n\t" \
  "s_mov_b32 s49, s53\n\t" \
  "s_add_u32 s53, s53, " CH_SLOT "\n\t" \
  "s_add_u32 s60, s53, " CH_SKIP "\n\t" \
  "s_lshr_b32 s61, s96, 1\n\t" \
  "s_add_u32 s86, s86, 1\n\t" \
  "s_cmp_eq_u32 s86, 3\n\t" \
  "s_cselect_b32 s53, s60, s53\n\t" \
  "s_cselect_b32 s96, s61, s96\n\t" \
  "s_cselect_b32 s86, 0, s86\n\t" \
  "s_andn2_b32 s53, s53, 0x2000\n\t" \
  "s_and_b32 s60, s96, 1\n\t" \
  "s_lshl_b32 s60, s60, 13\n\t" \
  "s_or_b32 s53, s53, s60\n\t" \
  "s_mov_b32 s68, 0\n\t" \
  "s_add_u32 s97, s41, s53\n\t" \
  "s_add_u32 s98, s37, s53\n\t" \
  "s_add_u32 s82, s48, 0\n\t" \
  "s_lshr_b64 s[60:61], s[90:91], s82\n\t" \
  "s_and_b32 s60, s60, 1\n\t" \
  "s_lshl_b32 s60, s60, 13\n\t" \
  "s_add_u32 s82, s82, " CH_SG0 "\n\t" \
  "s_lshl_b32 s82, s82, 21\n\t" \
  "s_or_b32 s82, s82, s60\n\t" \
  "s_add_u32 s50, s82, s38\n\t" \
  "s_add_u32 s69, s82, s39\n\t" \
  "s_cmp_eq_u32 s34, 0\n\t" \
  "s_cbranch_scc1 LINI_%=\n\t" \
  "s_branch LG2_%=\n\t" \
  "RFAIL2_%=:\n\t" \
  "s_add_u32 s74, s74, 1\n\t" \
  "s_sleep " CH_RSL "\n\t" \
  "s_waitcnt vmcnt(0)\n\t" \
  "s_cmp_gt_u32 s74, s84\n\t" \
  "s_cbranch_scc1 RG2_%=\n\t" \
  "s_and_b32 s60, s74, 1023\n\t" \
  "s_cmp_lg_u32 s60, 0\n\t" \
  "s_cbranch_scc1 RRETRY2_%=\n\t" \
  "buffer_load_dword v46, off, s[28:31], s92 sc1\n\t" \
  "s_waitcnt vmcnt(0)\n\t" \
  "v_readfirstlane_b32 s60, v46\n\t" \
  "s_nop 0\n\t" \
  "s_cmp_eq_u32 s60, 0\n\t" \
  "s_cbranch_scc1 RRETRY2_%=\n\t" \
  "RG2_%=:\n\t" \
  "s_mov_b32 s84, 0\n\t" \
  "s_lshl_b32 s60, s36, 4\n\t" \
  "s_or_b32 s60, s60, s48\n\t" \
  "s_or_b32 s60, s60, 536870912\n\t" \
  "v_mov_b32 v76, s60\n\t" \
  "v_mov_b32 v77, 0\n\t" \
  "s_mov_b64 exec, 1\n\t" \
  "buffer_atomic_cmpswap v[76:77], off, s[28:31], s92 sc1\n\t" \
  "s_mov_b64 exec, -1\n\t" \
  "s_branch RDONE2_%=\n\t" \
  "LFAIL2_%=:\n\t" \
  "v_max_u32 v75, v0, v1\n\t" \
  "v_max3_u32 v75, v75, v2, v3\n\t" \
  "v_max3_u32 v75, v75, v4, v5\n\t" \
  "v_max3_u32 v75, v75, v6, v7\n\t" \
  "v_max3_u32 v75, v75, v8, v9\n\t" \
  "v_max3_u32 v75, v75, v10, v11\n\t" \
  "v_max3_u32 v75, v75, v12, v13\n\t" \
  "v_max3_u32 v75, v75, v14, v15\n\t" \
  "v_max3_u32 v75, v75, v16, v17\n\t" \
  "v_max3_u32 v75, v75, v18, v19\n\t" \
  "v_max3_u32 v75, v75, v20, v21\n\t" \
  "v_max3_u32 v75, v75, v22, v23\n\t" \
  "v_max3_u32 v75, v75, v24, v25\n\t" \
  "v_max3_u32 v75, v75, v26, v27\n\t" \
  "v_max3_u32 v75, v75, v28, v29\n\t" \
  "v_max3_u32 v75, v75, v30, v31\n\t" \
  "v_max3_u32 v75, v75, v32, v33\n\t" \
  "v_max3_u32 v75, v75, v34, v35\n\t" \
  "v_cmp_eq_u32 vcc, -1, v75\n\t" \
  "s_cbranch_vccz LGO2_%=\n\t" \
  "s_add_u32 s68, s68, 1\n\t" \
  "s_waitcnt lgkmcnt(0)\n\t" \
  "s_cmp_gt_u32 s68, s83\n\t" \
  "s_cbranch_scc1 LGV2_%=\n\t" \
  "s_barrier\n\t" \
  "s_waitcnt vmcnt(8)\n\t" \
  "s_branch LG2_%=\n\t" \
  "RWT2_%=f:\n\t" \
  "s_cmp_lt_i32 s73, -2\n\t" \
  "s_cbranch_scc0 RWT2_%=g\n\t" \
  "s_nop 4\n\t" \
  "s_branch RWT2_%=g\n\t" \
  "LGV2_%=:\n\t" \
  "s_mov_b32 s83, 0\n\t" \
  "s_lshl_b32 s60, s36, 4\n\t" \
  "s_or_b32 s60, s60, s48\n\t" \
  "s_or_b32 s60, s60, 805306368\n\t" \
  "v_mov_b32 v76, s60\n\t" \
  "v_mov_b32 v77, 0\n\t" \
  "s_mov_b64 exec, 1\n\t" \
  "buffer_atomic_cmpswap v[76:77], off, s[28:31], s92 sc1\n\t" \
  "s_mov_b64 exec, -1\n\t" \
  "s_branch LGO2_%=\n\t" \
  "LLAST2_%=:\n\t" \
  "s_waitcnt vmcnt(0) lgkmcnt(0)\n\t" \
  "s_barrier\n\t" \
  "s_branch LEND_%=\n\t" \
  ".p2align 6\n\t" \
  "LG3_%=:\n\t" \
  "s_barrier\n\t" \
  "buffer_load_dwordx4 v[52:55], v69, s[28:31], s50 offen sc1\n\t" \
  "buffer_load_dwordx4 v[56:59], v69, s[28:31], s50 offen offset:1024 sc1\n\t" \
  "s_add_u32 s64, s50, 16384\n\t" \
  "s_movk_i32 m0, 11264\n\t" \
  "v_lshlrev_b32 v75, 1, v69\n\t" \
  "buffer_load_dword v75, s[28:31], s64 offen sc1 lds\n\t" \
  "s_add_u32 s47, s81, " CH_DL3 "\n\t" \
  "s_lshl_b32 s47, s47, 8\n\t" \
  "s_or_b32 s47, s47, " CH_B0 "\n\t" \
  "s_cmp_lt_u32 s48, " CH_ES "\n\t" \
  "s_cselect_b32 s60, " CH_EX ", 0\n\t" \
  "s_add_u32 s47, s47, s60\n\t" \
  "s_not_b32 s47, s47\n\t" \
  "s_add_u32 s71, s53, s46\n\t" \
  "ds_read_b128 v[44:47], v70 offset:0\n\t" \
  "ds_read_b128 v[48:51], v70 offset:1024\n\t" \
  "ds_read_b128 v[60:63], v70 offset:2048\n\t" \
  "ds_read_b128 v[64:67], v70 offset:3072\n\t" \
  "v_mov_b64 v[36:37], 0\n\t" \
  "v_mov_b64 v[38:39], 0\n\t" \
  "v_mov_b64 v[40:41], 0\n\t" \
  "v_mov_b64 v[42:43], 0\n\t" \
  "v_lshrrev_b32 v71, 1, v69\n\t" \
  "v_add_u32 v71, 4, v71\n\t" \
  "v_lshrrev_b32 v72, 3, v69\n\t" \
  "v_and_b32 v72, 0x78, v72\n\t" \
  "v_and_b32 v74, 0x30, v69\n\t" \
  "v_lshl_add_u32 v72, v74, 3, v72\n\t" \
  "s_add_u32 s54, s53, s36\n\t" \
  "s_add_u32 s55, s54, 573440\n\t" \
  "s_add_u32 s54, s54, 135168\n\t" \
  "s_add_u32 s75, s54, 262144\n\t" \
  "s_add_u32 s80, s53, s26\n\t" \
  "s_or_b32 s87, s50, 0x1fff80\n\t" \
  "s_add_u32 s87, s87, 4194304\n\t" \
  "s_barrier\n\t" \
  "ds_read_b128 v[0:3], v69 offset:0\n\t" \
  "ds_read_b128 v[4:7], v69 offset:1024\n\t" \
  "ds_read_b128 v[8:11], v69 offset:2048\n\t" \
  "ds_read_b128 v[12:15], v69 offset:3072\n\t" \
  "s_waitcnt lgkmcnt(3)\n\t" \
  "v_smfmac_f32_16x16x64_bf16 v[36:39], v[0:3], v[44:51], v68\n\t" \
  "ds_read_b128 v[44:47], v70 offset:4096\n\t" \
  "v_smfmac_f32_16x16x64_bf16 v[40:43], v[0:3], v[60:67], v68\n\t" \
  "ds_read_b128 v[48:51], v70 offset:5120\n\t" \
  "s_waitcnt lgkmcnt(4)\n\t" \
  "v_smfmac_f32_16x16x64_bf16 v[36:39], v[4:7], a[32:39], v68\n\t" \
  "s_barrier\n\t" \
  "ds_read_b128 v[16:19], v69 offset:4096\n\t" \
  "v_smfmac_f32_16x16x64_bf16 v[40:43], v[4:7], a[40:47], v68\n\t" \
  "ds_read_b128 v[20:23], v69 offset:5120\n\t" \
  "s_waitcnt lgkmcnt(5)\n\t" \
  "v_smfmac_f32_16x16x64_bf16 v[36:39], v[8:11], a[80:87], v68\n\t" \
  "ds_read_b128 v[24:27], v69 offset:6144\n\t" \
  "v_smfmac_f32_16x16x64_bf16 v[40:43], v[8:11], a[88:95], v68\n\t" \
  "ds_read_b128 v[28:31], v69 offset:7168\n\t" \
  "s_waitcnt lgkmcnt(6)\n\t" \
  "v_smfmac_f32_16x16x64_bf16 v[36:39], v[12:15], a[120:127], v68\n\t" \
  "ds_read_b128 v[32:35], v69 offset:8192\n\t" \
  "s_waitcnt lgkmcnt(5)\n\t" \
  "v_smfmac_f32_16x16x64_bf16 v[40:43], v[12:15], v[44:51], v68\n\t" \
  "ds_read_b128 v[60:63], v70 offset:6144\n\t" \
  "s_waitcnt lgkmcnt(5)\n\t" \
  "v_smfmac_f32_16x16x64_bf16 v[36:39], v[16:19], a[160:167], v68\n\t" \
  "ds_read_b128 v[64:67], v70 offset:7168\n\t" \
  "v_smfmac_f32_16x16x64_bf16 v[40:43], v[16:19], a[168:175], v68\n\t" \
  "s_waitcnt lgkmcnt(5)\n\t" \
  "v_mov_b64 v[44:45], 0\n\t" \
  "v_smfmac_f32_16x16x64_bf16 v[36:39], v[20:23], a[200:207], v68\n\t" \
  "s_waitcnt lgkmcnt(4)\n\t" \
  "v_mov_b64 v[46:47], 0\n\t" \
  "v_smfmac_f32_16x16x64_bf16 v[36:39], v[24:27], a[240:247], v68\n\t" \
  "s_waitcnt lgkmcnt(2)\n\t" \
  "v_mov_b64 v[48:49], 0\n\t" \
  "v_smfmac_f32_16x16x64_bf16 v[36:39], v[28:31], v[112:119], v68\n\t" \
  "v_mov_b64 v[50:51], 0\n\t" \
  "v_smfmac_f32_16x16x64_bf16 v[36:39], v[32:35], v[160:167], v68\n\t" \
  "s_waitcnt lgkmcnt(0)\n\t" \
  "v_smfmac_f32_16x16x64_bf16 v[40:43], v[20:23], v[60:67], v68\n\t" \
  "ds_read_b128 v[60:63], v70 offset:8192\n\t" \
  "v_smfmac_f32_16x16x64_bf16 v[40:43], v[24:27], a[248:255], v68\n\t" \
  "ds_read_b128 v[64:67], v70 offset:9216\n\t" \
  "v_add_f32 v74, v36, v38\n\t" \
  "v_smfmac_f32_16x16x64_bf16 v[40:43], v[28:31], v[120:127], v68\n\t" \
  "v_add_f32 v37, v37, v39\n\t" \
  "v_smfmac_f32_16x16x64_bf16 v[40:43], v[32:35], v[168:175], v68\n\t" \
  "v_cmp_u_f32 vcc, v74, v37\n\t" \
  "v_smfmac_f32_16x16x64_bf16 v[44:47], v[0:3], a[0:7], v68\n\t" \
  "s_cbranch_vccnz LFAIL3_%=\n\t" \
  "LGO3_%=:\n\t" \
  "v_cvt_pk_bf16_f32 v74, v74, v37\n\t" \
  "v_smfmac_f32_16x16x64_bf16 v[44:47], v[4:7], a[48:55], v68\n\t" \
  "s_waitcnt lgkmcnt(0)\n\t" \
  "v_add_f32 v75, v40, v42\n\t" \
  "v_smfmac_f32_16x16x64_bf16 v[44:47], v[8:11], v[60:67], v68\n\t" \
  "ds_read_b128 v[60:63], v70 offset:10240\n\t" \
  "v_add_f32 v41, v41, v43\n\t" \
  "v_cvt_pk_bf16_f32 v75, v75, v41\n\t" \
  "v_smfmac_f32_16x16x64_bf16 v[44:47], v[12:15], a[128:135], v68\n\t" \
  "ds_write2st64_b32 v71, v74, v75 offset0:40 offset1:42\n\t" \
  "v_smfmac_f32_16x16x64_bf16 v[44:47], v[16:19], a[176:183], v68\n\t" \
  "ds_read_b128 v[64:67], v70 offset:11264\n\t" \
  "s_memrealtime s[66:67]\n\t" \
  "v_smfmac_f32_16x16x64_bf16 v[44:47], v[20:23], a[208:215], v68\n\t" \
  "v_smfmac_f32_16x16x64_bf16 v[44:47], v[24:27], v[80:87], v68\n\t" \
  "v_smfmac_f32_16x16x64_bf16 v[44:47], v[28:31], v[128:135], v68\n\t" \
  "s_waitcnt lgkmcnt(0)\n\t" \
  "v_smfmac_f32_16x16x64_bf16 v[44:47], v[32:35], v[60:67], v68\n\t" \
  "s_barrier\n\t" \
  "ds_read_b64_tr_b16 v[76:77], v72 offset:10240\n\t" \
  "v_smfmac_f32_16x16x64_bf16 v[48:51], v[0:3], a[8:15], v68\n\t" \
  "s_lshl_b32 s60, s66, 8\n\t" \
  "s_sub_u32 s60, s47, s60\n\t" \
  "ds_read_b64_tr_b16 v[78:79], v72 offset:10752\n\t" \
  "v_mov_b32 v75, s60\n\t" \
  "v_smfmac_f32_16x16x64_bf16 v[48:51], v[4:7], a[56:63], v68\n\t" \
  "s_mov_b64 exec, s[24:25]\n\t" \
  "buffer_atomic_umin v75, v69, s[28:31], s71 offen sc1\n\t" \
  "s_mov_b64 exec, -1\n\t" \
  "v_add_f32 v74, v44, v46\n\t" \
  "v_smfmac_f32_16x16x64_bf16 v[48:51], v[8:11], a[96:103], v68\n\t" \
  "buffer_load_dwordx4 v[60:63], v69, s[28:31], s64 offen sc1\n\t" \
  "v_add_f32 v45, v45, v47\n\t" \
  "v_smfmac_f32_16x16x64_bf16 v[48:51], v[12:15], a[136:143], v68\n\t" \
  "s_waitcnt vmcnt(4)\n\t" \
  "s_waitcnt lgkmcnt(0)\n\t" \
  "v_cvt_pk_bf16_f32 v74, v74, v45\n\t" \
  "v_mfma_f32_16x16x32_bf16 v[36:39], v[52:55], v[76:79], 0\n\t" \
  "s_waitcnt vmcnt(3)\n\t" \
  "v_mfma_f32_16x16x32_bf16 v[40:43], v[56:59], v[76:79], 0\n\t" \
  "v_mov_b64 v[52:53], 0\n\t" \
  "v_mov_b64 v[54:55], 0\n\t" \
  "v_smfmac_f32_16x16x64_bf16 v[48:51], v[16:19], a[184:191], v68\n\t" \
  "buffer_load_dwordx4 v[64:67], v69, s[28:31], s64 offen offset:1024 sc1\n\t" \
  "v_mov_b64 v[56:57], 0\n\t" \
  "v_mov_b64 v[58:59], 0\n\t" \
  "v_smfmac_f32_16x16x64_bf16 v[48:51], v[20:23], a[216:223], v68\n\t" \
  "ds_write_b32 v71, v74 offset:1024\n\t" \
  "v_add_f32_dpp v36, v40, v36 quad_perm:[2,3,0,1] row_mask:0xf bank_mask:0xf\n\t" \
  "v_smfmac_f32_16x16x64_bf16 v[48:51], v[24:27], v[88:95], v68\n\t" \
  "v_add_f32_dpp v37, v41, v37 quad_perm:[2,3,0,1] row_mask:0xf bank_mask:0xf\n\t" \
  "v_smfmac_f32_16x16x64_bf16 v[48:51], v[28:31], v[136:143], v68\n\t" \
  "v_add_f32_dpp v38, v42, v38 quad_perm:[2,3,0,1] row_mask:0xf bank_mask:0xf\n\t" \
  "v_smfmac_f32_16x16x64_bf16 v[48:51], v[32:35], v[176:183], v68\n\t" \
  "v_add_f32_dpp v39, v43, v39 quad_perm:[2,3,0,1] row_mask:0xf bank_mask:0xf\n\t" \
  "v_smfmac_f32_16x16x64_bf16 v[52:55], v[0:3], a[16:23], v68\n\t" \
  "s_mov_b64 exec, s[58:59]\n\t" \
  "ds_write_b128 v69, v[36:39] offset:3104\n\t" \
  "s_mov_b64 exec, -1\n\t" \
  "v_smfmac_f32_16x16x64_bf16 v[52:55], v[4:7], a[64:71], v68\n\t" \
  "buffer_load_dword off, s[28:31], s87 sc1 lds\n\t" \
  "v_add_f32 v75, v48, v50\n\t" \
  "v_add_f32 v49, v49, v51\n\t" \
  "v_cvt_pk_bf16_f32 v75, v75, v49\n\t" \
  "v_smfmac_f32_16x16x64_bf16 v[56:59], v[0:3], a[24:31], v68\n\t" \
  "ds_write_b32 v71, v75 offset:1536\n\t" \
  "v_smfmac_f32_16x16x64_bf16 v[56:59], v[4:7], a[72:79], v68\n\t" \
  "ds_read_b64_tr_b16 v[0:1], v72 offset:9216\n\t" \
  "ds_read_b64_tr_b16 v[2:3], v72 offset:9728\n\t" \
  "v_smfmac_f32_16x16x64_bf16 v[52:55], v[8:11], a[104:111], v68\n\t" \
  "ds_read_b128 v[36:39], v70 offset:12288\n\t" \
  "v_smfmac_f32_16x16x64_bf16 v[56:59], v[8:11], a[112:119], v68\n\t" \
  "ds_read_b128 v[40:43], v70 offset:13312\n\t" \
  "v_smfmac_f32_16x16x64_bf16 v[52:55], v[12:15], a[144:151], v68\n\t" \
  "s_waitcnt lgkmcnt(4)\n\t" \
  "s_barrier\n\t" \
  "ds_read_b64_tr_b16 v[44:45], v72 offset:1024\n\t" \
  "ds_read_b64_tr_b16 v[46:47], v72 offset:1536\n\t" \
  "v_smfmac_f32_16x16x64_bf16 v[56:59], v[12:15], a[152:159], v68\n\t" \
  "ds_read_b64_tr_b16 v[48:49], v72 offset:0\n\t" \
  "ds_read_b64_tr_b16 v[50:51], v72 offset:512\n\t" \
  "v_smfmac_f32_16x16x64_bf16 v[52:55], v[16:19], a[192:199], v68\n\t" \
  "ds_read_b128 v[4:7], v69 offset:2048\n\t" \
  "s_waitcnt lgkmcnt(5)\n\t" \
  "v_smfmac_f32_16x16x64_bf16 v[56:59], v[16:19], v[36:43], v68\n\t" \
  "ds_read_b128 v[8:11], v69 offset:3072\n\t" \
  "s_waitcnt lgkmcnt(4)\n\t" \
  "v_mfma_f32_16x16x32_bf16 v[12:15], v[44:47], v[76:79], 0\n\t" \
  "ds_read_b128 v[36:39], v70 offset:14336\n\t" \
  "s_waitcnt lgkmcnt(3)\n\t" \
  "v_mfma_f32_16x16x32_bf16 v[12:15], v[48:51], v[0:3], v[12:15]\n\t" \
  "ds_read_b128 v[40:43], v70 offset:15360\n\t" \
  "v_smfmac_f32_16x16x64_bf16 v[52:55], v[20:23], a[224:231], v68\n\t" \
  "s_waitcnt lgkmcnt(2)\n\t" \
  "v_add_f32 v4, v4, v8\n\t" \
  "v_add_f32 v5, v5, v9\n\t" \
  "v_add_f32 v6, v6, v10\n\t" \
  "v_cndmask_b32 v78, v13, v14, s[56:57]\n\t" \
  "v_cndmask_b32 v76, v12, v15, s[56:57]\n\t" \
  "v_add_f32 v7, v7, v11\n\t" \
  "v_max3_f32 v77, v4, v5, v6\n\t" \
  "v_add_f32_dpp v76, v78, v76 quad_perm:[2,3,0,1] row_mask:0xf bank_mask:0xf\n\t" \
  "v_max3_f32 v77, v77, v7, v76\n\t" \
  "v_smfmac_f32_16x16x64_bf16 v[56:59], v[20:23], a[232:239], v68\n\t" \
  "ds_read_b128 v[44:47], v70 offset:16384\n\t" \
  "v_max_f32_dpp v77, v77, v77 quad_perm:[2,3,0,1] row_mask:0xf bank_mask:0xf\n\t" \
  "v_mov_b32 v78, v77\n\t" \
  "v_smfmac_f32_16x16x64_bf16 v[52:55], v[24:27], v[96:103], v68\n\t" \
  "ds_read_b128 v[48:51], v70 offset:17408\n\t" \
  "v_permlane16_swap_b32 v77, v78\n\t" \
  "v_max_f32 v77, v77, v78\n\t" \
  "v_mov_b32 v78, v77\n\t" \
  "v_smfmac_f32_16x16x64_bf16 v[52:55], v[28:31], v[144:151], v68\n\t" \
  "ds_read_b128 v[16:19], v70 offset:18432\n\t" \
  "v_permlane32_swap_b32 v77, v78\n\t" \
  "v_max_f32 v77, v77, v78\n\t" \
  "v_smfmac_f32_16x16x64_bf16 v[52:55], v[32:35], v[184:191], v68\n\t" \
  "ds_read_b128 v[20:23], v70 offset:19456\n\t" \
  "v_mul_f32 v77, 0xbe0293ee, v77\n\t" \
  "v_smfmac_f32_16x16x64_bf16 v[56:59], v[24:27], v[104:111], v68\n\t" \
  "v_fmamk_f32 v4, v4, 0x3e0293ee, v77\n\t" \
  "v_smfmac_f32_16x16x64_bf16 v[56:59], v[28:31], v[152:159], v68\n\t" \
  "ds_read_b128 v[24:27], v70 offset:22528\n\t" \
  "s_waitcnt lgkmcnt(4)\n\t" \
  "v_fmamk_f32 v5, v5, 0x3e0293ee, v77\n\t" \
  "v_smfmac_f32_16x16x64_bf16 v[56:59], v[32:35], v[36:43], v68\n\t" \
  "ds_read_b128 v[28:31], v70 offset:23552\n\t" \
  "v_fmamk_f32 v6, v6, 0x3e0293ee, v77\n\t" \
  "v_fmamk_f32 v7, v7, 0x3e0293ee, v77\n\t" \
  "v_fmamk_f32 v76, v76, 0x3e0293ee, v77\n\t" \
  "v_exp_f32 v4, v4\n\t" \
  "v_exp_f32 v5, v5\n\t" \
  "v_exp_f32 v6, v6\n\t" \
  "v_exp_f32 v7, v7\n\t" \
  "v_cvt_pk_bf16_f32 v4, v4, v5\n\t" \
  "v_exp_f32 v76, v76\n\t" \
  "v_cvt_pk_bf16_f32 v5, v6, v7\n\t" \
  "v_mov_b32_dpp v12, v4 quad_perm:[0,1,0,1] row_mask:0xf bank_mask:0xf\n\t" \
  "v_mov_b32_dpp v14, v4 quad_perm:[2,3,2,3] row_mask:0xf bank_mask:0xf\n\t" \
  "v_mov_b32_dpp v13, v5 quad_perm:[0,1,0,1] row_mask:0xf bank_mask:0xf\n\t" \
  "v_mov_b32_dpp v15, v5 quad_perm:[2,3,2,3] row_mask:0xf bank_mask:0xf\n\t" \
  "v_add_f32 v52, v52, v54\n\t" \
  "v_add_f32 v53, v53, v55\n\t" \
  "s_waitcnt vmcnt(2)\n\t" \
  "v_mfma_f32_16x16x32_bf16 v[0:3], v[60:63], v[12:15], 0\n\t" \
  "ds_read_b128 v[36:39], v70 offset:20480\n\t" \
  "v_add_f32 v56, v56, v58\n\t" \
  "v_add_f32 v57, v57, v59\n\t" \
  "v_cvt_pk_bf16_f32 v52, v52, v53\n\t" \
  "v_cvt_pk_bf16_f32 v53, v56, v57\n\t" \
  "s_waitcnt vmcnt(1)\n\t" \
  "v_mfma_f32_16x16x32_bf16 v[4:7], v[64:67], v[12:15], 0\n\t" \
  "ds_read_b128 v[40:43], v70 offset:21504\n\t" \
  "v_mov_b64 v[56:57], s[88:89]\n\t" \
  "v_mov_b64 v[58:59], s[88:89]\n\t" \
  "v_mov_b32_dpp v78, v76 quad_perm:[0,1,0,1] row_mask:0xf bank_mask:0xf\n\t" \
  "v_mov_b32_dpp v79, v76 quad_perm:[2,3,2,3] row_mask:0xf bank_mask:0xf\n\t" \
  "v_mfma_f32_16x16x32_bf16 v[32:35], v[56:59], v[12:15], 0\n\t" \
  "ds_read_b128 v[60:63], v70 offset:24576\n\t" \
  "v_cvt_pk_bf16_f32 v11, v78, v79\n\t" \
  "v_cvt_pk_bf16_f32 v8, v78, v79\n\t" \
  "v_mov_b64 v[54:55], 0\n\t" \
  "v_mov_b32 v9, 0\n\t" \
  "v_mov_b32 v10, 0\n\t" \
  "v_mov_b64 v[14:15], 0\n\t" \
  "s_nop 0\n\t" \
  "v_mfma_f32_16x16x32_bf16 v[32:35], v[54:57], v[8:11], v[32:35]\n\t" \
  "s_cmp_eq_u32 s48, 63\n\t" \
  "v_mfma_f32_16x16x32_bf16 v[0:3], v[52:55], v[8:11], v[0:3]\n\t" \
  "buffer_store_dwordx2 v[52:53], v71, s[28:31], s69 offen offset:3580 sc1\n\t" \
  "ds_read_b128 v[64:67], v70 offset:25600\n\t" \
  "v_mov_b64 v[56:57], 0\n\t" \
  "v_mov_b64 v[58:59], 0\n\t" \
  "v_mfma_f32_16x16x32_bf16 v[4:7], v[52:55], v[10:13], v[4:7]\n\t" \
  "s_cbranch_scc1 LLAST3_%=\n\t" \
  "v_rcp_f32 v74, v32\n\t" \
  "v_mov_b64 v[12:13], 0\n\t" \
  "v_mov_b32 v8, 0\n\t" \
  "v_mul_f32 v0, v0, v74\n\t" \
  "v_mul_f32 v1, v1, v74\n\t" \
  "v_mul_f32 v2, v2, v74\n\t" \
  "v_mul_f32 v3, v3, v74\n\t" \
  "v_cvt_pk_bf16_f32 v76, v0, v1\n\t" \
  "v_cvt_pk_bf16_f32 v77, v2, v3\n\t" \
  "v_pk_mul_f32 v[4:5], v[4:5], v[74:75] op_sel_hi:[1,0]\n\t" \
  "v_pk_mul_f32 v[6:7], v[6:7], v[74:75] op_sel_hi:[1,0]\n\t" \
  "v_cvt_pk_bf16_f32 v78, v4, v5\n\t" \
  "v_cvt_pk_bf16_f32 v79, v6, v7\n\t" \
  "ds_write_b128 v69, v[76:79] offset:7168\n\t" \
  "s_waitcnt lgkmcnt(2)\n\t" \
  "v_smfmac_f32_16x16x64_bf16 v[12:15], v[76:79], v[44:51], v68\n\t" \
  "ds_read_b128 v[44:47], v70 offset:26624\n\t" \
  "v_mov_b32 v11, 0\n\t" \
  "v_smfmac_f32_16x16x64_bf16 v[56:59], v[76:79], v[16:23], v68\n\t" \
  "ds_read_b128 v[48:51], v70 offset:27648\n\t" \
  "v_mov_b64 v[52:53], 0\n\t" \
  "v_smfmac_f32_16x16x64_bf16 v[8:11], v[76:79], v[36:43], v68\n\t" \
  "ds_read_b128 v[16:19], v70 offset:28672\n\t" \
  "v_smfmac_f32_16x16x64_bf16 v[52:55], v[76:79], v[24:31], v68\n\t" \
  "s_waitcnt lgkmcnt(3)\n\t" \
  "s_barrier\n\t" \
  "ds_read_b128 v[0:3], v69 offset:6144\n\t" \
  "ds_read_b128 v[4:7], v69 offset:5120\n\t" \
  "v_lshrrev_b32 v71, 7, v69\n\t" \
  "v_and_b32 v74, 0x70, v69\n\t" \
  "v_lshl_add_u32 v71, v71, 14, v74\n\t" \
  "v_bfe_u32 v72, v69, 2, 7\n\t" \
  "v_and_b32 v74, 0x200, v69\n\t" \
  "s_waitcnt lgkmcnt(1)\n\t" \
  "v_smfmac_f32_16x16x64_bf16 v[8:11], v[0:3], v[60:67], v68\n\t" \
  "ds_read_b128 v[20:23], v70 offset:29696\n\t" \
  "v_mov_b64 v[76:77], 0\n\t" \
  "v_smfmac_f32_16x16x64_bf16 v[12:15], v[0:3], v[200:207], v68\n\t" \
  "ds_read_b128 v[32:35], v69 offset:4096\n\t" \
  "v_mov_b64 v[78:79], 0\n\t" \
  "v_smfmac_f32_16x16x64_bf16 v[52:55], v[0:3], v[44:51], v68\n\t" \
  "buffer_load_dword v75, off, s[28:31], s80 sc1\n\t" \
  "ds_read_b128 v[24:27], v70 offset:30720\n\t" \
  "v_lshl_add_u32 v72, v74, 3, v72\n\t" \
  "v_smfmac_f32_16x16x64_bf16 v[56:59], v[0:3], v[232:239], v68\n\t" \
  "ds_read_b128 v[28:31], v70 offset:31744\n\t" \
  "s_waitcnt lgkmcnt(4)\n\t" \
  "v_smfmac_f32_16x16x64_bf16 v[12:15], v[4:7], v[208:215], v68\n\t" \
  "ds_read_b128 v[60:63], v70 offset:32768\n\t" \
  "s_waitcnt lgkmcnt(4)\n\t" \
  "v_smfmac_f32_16x16x64_bf16 v[8:11], v[4:7], v[16:23], v68\n\t" \
  "ds_read_b128 v[64:67], v70 offset:33792\n\t" \
  "s_waitcnt lgkmcnt(4)\n\t" \
  "v_smfmac_f32_16x16x64_bf16 v[8:11], v[32:35], v[192:199], v68\n\t" \
  "ds_read_b128 v[36:39], v73 offset:4096\n\t" \
  "v_smfmac_f32_16x16x64_bf16 v[12:15], v[32:35], v[216:223], v68\n\t" \
  "ds_read_b128 v[40:43], v73 offset:6144\n\t" \
  "s_waitcnt lgkmcnt(4)\n\t" \
  "v_smfmac_f32_16x16x64_bf16 v[52:55], v[4:7], v[24:31], v68\n\t" \
  "ds_read_b128 v[44:47], v70 offset:34816\n\t" \
  "v_cvt_pk_bf16_f32 v8, v8, v9\n\t" \
  "v_smfmac_f32_16x16x64_bf16 v[52:55], v[32:35], v[224:231], v68\n\t" \
  "ds_read_b128 v[48:51], v70 offset:35840\n\t" \
  "v_cvt_pk_bf16_f32 v9, v10, v11\n\t" \
  "v_smfmac_f32_16x16x64_bf16 v[56:59], v[4:7], v[240:247], v68\n\t" \
  "s_waitcnt lgkmcnt(4)\n\t" \
  "v_cvt_pk_bf16_f32 v10, v12, v13\n\t" \
  "v_smfmac_f32_16x16x64_bf16 v[56:59], v[32:35], v[60:67], v68\n\t" \
  "s_waitcnt lgkmcnt(3)\n\t" \
  "v_cvt_pk_bf16_f32 v11, v14, v15\n\t" \
  "v_smfmac_f32_16x16x64_bf16 v[76:79], v[36:39], v[248:255], v68\n\t" \
  "buffer_store_dwordx4 v[8:11], v71, s[28:31], s54 offen sc1\n\t" \
  "s_waitcnt lgkmcnt(0)\n\t" \
  "v_cvt_pk_bf16_f32 v52, v52, v53\n\t" \
  "v_smfmac_f32_16x16x64_bf16 v[76:79], v[40:43], v[44:51], v68\n\t" \
  "v_cvt_pk_bf16_f32 v53, v54, v55\n\t" \
  "v_cvt_pk_bf16_f32 v54, v56, v57\n\t" \
  "v_cvt_pk_bf16_f32 v55, v58, v59\n\t" \
  "buffer_store_dwordx4 v[52:55], v71, s[28:31], s75 offen sc1\n\t" \
  "s_nop 2\n\t" \
  "v_add_f32 v76, v76, v78\n\t" \
  "v_add_f32 v77, v77, v79\n\t" \
  "v_cvt_pk_bf16_f32 v76, v76, v77\n\t" \
  "buffer_store_dword v76, v72, s[28:31], s55 offen sc1\n\t" \
  "s_memrealtime s[70:71]\n\t" \
  "s_waitcnt vmcnt(3)\n\t" \
  "v_readfirstlane_b32 s65, v75\n\t" \
  "s_barrier\n\t" \
  "s_waitcnt lgkmcnt(0)\n\t" \
  "s_not_b32 s65, s65\n\t" \
  "s_lshl_b32 s61, s70, 8\n\t" \
  "s_andn2_b32 s64, s65, 0xff\n\t" \
  "s_sub_u32 s64, s64, s61\n\t" \
  "s_ashr_i32 s64, s64, 8\n\t" \
  "s_cmp_eq_u32 s65, 0\n\t" \
  "s_cselect_b32 s64, " CH_DLF ", s64\n\t" \
  "s_min_i32 s64, s64, s34\n\t" \
  "s_max_i32 s64, s64, " CH_DLL1 "\n\t" \
  "s_add_u32 s64, s64, s70\n\t" \
  "s_mov_b32 s74, 0\n\t" \
  "v_and_b32 v37, 0x30, v69\n\t" \
  "v_add_u32 v37, s52, v37\n\t" \
  "s_mov_b32 exec_lo, 0\n\t" \
  "v_mov_b32 v37, s85\n\t" \
  "s_mov_b32 exec_lo, -1\n\t" \
  "s_cmp_lt_i32 s41, 0\n\t" \
  "s_cbranch_scc1 LIDLE_%=\n\t" \
  "s_movk_i32 s60, 8192\n\t" \
  "RWT3_%=:\n\t" \
  "s_memrealtime s[72:73]\n\t" \
  "s_sub_u32 s60, s60, 1\n\t" \
  "s_cselect_b32 s61, 0, -1\n\t" \
  "s_waitcnt lgkmcnt(0)\n\t" \
  "s_sub_i32 s73, s72, s64\n\t" \
  "s_and_b32 s73, s73, s61\n\t" \
  "s_cmp_lt_i32 s73, " CH_FIN "\n\t" \
  "s_cbranch_scc1 RWT3_%=\n\t" \
  "s_cmp_lt_i32 s73, 0\n\t" \
  "s_cbranch_scc1 RWT3_%=f\n\t" \
  "RWT3_%=g:\n\t" \
  "RRETRY3_%=:\n\t" \
  "buffer_load_dwordx4 v[0:3], v69, s[28:31], s97 offen offset:0 sc1\n\t" \
  "buffer_load_dwordx4 v[4:7], v69, s[28:31], s97 offen offset:1024 sc1\n\t" \
  "buffer_load_dwordx4 v[8:11], v69, s[28:31], s97 offen offset:2048 sc1\n\t" \
  "buffer_load_dwordx4 v[12:15], v69, s[28:31], s97 offen offset:3072 sc1\n\t" \
  "v_lshrrev_b32 v20, 2, v69\n\t" \
  "v_and_b32 v20, 16, v20\n\t" \
  "v_mov_b32 v21, 0x3f80\n\t" \
  "v_lshlrev_b32 v21, v20, v21\n\t" \
  "v_bfe_u32 v20, v69, 4, 2\n\t" \
  "v_cmp_eq_u32 vcc, 0, v20\n\t" \
  "s_nop 1\n\t" \
  "v_cndmask_b32 v32, 0, v21, vcc\n\t" \
  "v_cmp_eq_u32 vcc, 1, v20\n\t" \
  "s_nop 1\n\t" \
  "v_cndmask_b32 v33, 0, v21, vcc\n\t" \
  "v_cmp_eq_u32 vcc, 2, v20\n\t" \
  "s_nop 1\n\t" \
  "v_cndmask_b32 v34, 0, v21, vcc\n\t" \
  "v_cmp_eq_u32 vcc, 3, v20\n\t" \
  "s_nop 1\n\t" \
  "v_cndmask_b32 v35, 0, v21, vcc\n\t" \
  "v_bfe_u32 v38, v69, 7, 1\n\t" \
  "v_bfe_u32 v20, v69, 9, 1\n\t" \
  "v_lshl_add_u32 v38, v20, 2, v38\n\t" \
  "v_lshlrev_b32 v38, 12, v38\n\t" \
  "v_and_b32 v20, 0x60, v69\n\t" \
  "v_add_u32 v38, v38, v20\n\t" \
  "v_bfe_u32 v20, v69, 8, 1\n\t" \
  "v_lshl_add_u32 v38, v20, 4, v38\n\t" \
  "v_bfe_u32 v20, v69, 4, 1\n\t" \
  "v_lshl_add_u32 v38, v20, 3, v38\n\t" \
  "v_mov_b32 v44, 0\n\t" \
  "buffer_load_dword v39, v37, s[28:31], 0 offen sc1\n\t" \
  "s_waitcnt vmcnt(4)\n\t" \
  "v_mfma_f32_16x16x32_bf16 v[40:43], v[32:35], v[0:3], 0\n\t" \
  "v_max3_u32 v44, v44, v0, v1\n\t" \
  "v_max3_u32 v44, v44, v2, v3\n\t" \
  "s_waitcnt vmcnt(3)\n\t" \
  "v_mfma_f32_16x16x32_bf16 v[40:43], v[32:35], v[4:7], v[40:43]\n\t" \
  "v_max3_u32 v44, v44, v4, v5\n\t" \
  "v_max3_u32 v44, v44, v6, v7\n\t" \
  "s_waitcnt vmcnt(2)\n\t" \
  "v_mfma_f32_16x16x32_bf16 v[40:43], v[32:35], v[8:11], v[40:43]\n\t" \
  "v_max3_u32 v44, v44, v8, v9\n\t" \
  "v_max3_u32 v44, v44, v10, v11\n\t" \
  "s_waitcnt vmcnt(1)\n\t" \
  "v_mfma_f32_16x16x32_bf16 v[40:43], v[32:35], v[12:15], v[40:43]\n\t" \
  "v_max3_u32 v44, v44, v12, v13\n\t" \
  "v_max3_u32 v44, v44, v14, v15\n\t" \
  "v_cmp_eq_u32 vcc, -1, v44\n\t" \
  "s_cbranch_vccnz RFAIL3_%=\n\t" \
  "s_nop 3\n\t" \
  "v_add_f32_dpp v40, v40, v40 row_ror:8 row_mask:0xf bank_mask:0xf\n\t" \
  "v_add_f32_dpp v41, v41, v41 row_ror:8 row_mask:0xf bank_mask:0xf\n\t" \
  "v_add_f32_dpp v42, v42, v42 row_ror:8 row_mask:0xf bank_mask:0xf\n\t" \
  "v_add_f32_dpp v43, v43, v43 row_ror:8 row_mask:0xf bank_mask:0xf\n\t" \
  "v_cvt_pk_bf16_f32 v40, v40, v41\n\t" \
  "v_cvt_pk_bf16_f32 v41, v42, v43\n\t" \
  "buffer_store_dwordx2 v[40:41], v38, s[28:31], s98 offen sc1\n\t" \
  "RDONE3_%=:\n\t" \
  "s_barrier\n\t" \
  "s_waitcnt vmcnt(1)\n\t" \
  "v_cmp_eq_u32 vcc, 0, v39\n\t" \
  "s_sub_u32 s60, s70, s66\n\t" \
  "s_add_u32 s81, s81, 1\n\t" \
  "s_min_u32 s81, s81, s60\n\t" \
  "s_add_u32 s48, s48, 1\n\t" \
  "s_mov_b32 s49, s53\n\t" \
  "s_add_u32 s53, s53, " CH_SLOT "\n\t" \
  "s_add_u32 s60, s53, " CH_SKIP "\n\t" \
  "s_lshr_b32 s61, s96, 1\n\t" \
  "s_add_u32 s86, s86, 1\n\t" \
  "s_cmp_eq_u32 s86, 3\n\t" \
  "s_cselect_b32 s53, s60, s53\n\t" \
  "s_cselect_b32 s96, s61, s96\n\t" \
  "s_cselect_b32 s86, 0, s86\n\t" \
  "s_andn2_b32 s53, s53, 0x2000\n\t" \
  "s_and_b32 s60, s96, 1\n\t" \
  "s_lshl_b32 s60, s60, 13\n\t" \
  "s_or_b32 s53, s53, s60\n\t" \
  "s_mov_b32 s68, 0\n\t" \
  "s_add_u32 s97, s41, s53\n\t" \
  "s_add_u32 s98, s37, s53\n\t" \
  "s_add_u32 s82, s48, 0\n\t" \
  "s_lshr_b64 s[60:61], s[90:91], s82\n\t" \
  "s_and_b32 s60, s60, 1\n\t" \
  "s_lshl_b32 s60, s60, 13\n\t" \
  "s_add_u32 s82, s82, " CH_SG0 "\n\t" \
  "s_lshl_b32 s82, s82, 21\n\t" \
  "s_or_b32 s82, s82, s60\n\t" \
  "s_add_u32 s50, s82, s38\n\t" \
  "s_add_u32 s69, s82, s39\n\t" \
  "s_add_u32 s82, s48, 1\n\t" \
  "s_lshr_b64 s[60:61], s[90:91], s82\n\t" \
  "s_and_b32 s60, s60, 1\n\t" \
  "s_lshl_b32 s60, s60, 13\n\t" \
  "s_add_u32 s82, s82, " CH_SG0 "\n\t" \
  "s_lshl_b32 s82, s82, 21\n\t" \
  "s_or_b32 s82, s82, s60\n\t" \
  "s_add_u32 s52, s82, s43\n\t" \
  "s_add_u32 s82, s48, " CH_ARMD "\n\t" \
  "s_lshr_b64 s[60:61], s[90:91], s82\n\t" \
  "s_and_b32 s60, s60, 1\n\t" \
  "s_lshl_b32 s60, s60, 13\n\t" \
  "s_add_u32 s82, s82, " CH_SG0 "\n\t" \
  "s_lshl_b32 s82, s82, 21\n\t" \
  "s_or_b32 s82, s82, s60\n\t" \
  "s_add_u32 s85, s82, s45\n\t" \
  "s_cmp_lt_i32 s48, " CH_ARMS "\n\t" \
  "s_cselect_b32 s85, s27, s85\n\t" \
  "s_cmp_eq_u32 s34, 0\n\t" \
  "s_cbranch_scc1 LINI_%=\n\t" \
  "LTK_%=:\n\t" \
  CH_TOKCHK \
  "s_mov_b32 s61, 0\n\t" \
  "LTW_%=:\n\t" \
  "buffer_load_dword v1, v37, s[28:31], 0 offen sc1\n\t" \
  "s_waitcnt vmcnt(0)\n\t" \
  "v_cmp_eq_u32 vcc, 0, v1\n\t" \
  "s_cbranch_vccz LG3_%=\n\t" \
  "s_add_u32 s61, s61, 1\n\t" \
  "s_sleep 2\n\t" \
  "s_cmp_gt_u32 s61, s84\n\t" \
  "s_cbranch_scc1 LTG_%=\n\t" \
  "s_and_b32 s60, s61, 1023\n\t" \
  "s_cmp_lg_u32 s60, 0\n\t" \
  "s_cbranch_scc1 LTW_%=\n\t" \
  "buffer_load_dword v46, off, s[28:31], s92 sc1\n\t" \
  "s_waitcnt vmcnt(0)\n\t" \
  "v_readfirstlane_b32 s60, v46\n\t" \
  "s_nop 0\n\t" \
  "s_cmp_eq_u32 s60, 0\n\t" \
  "s_cbranch_scc1 LTW_%=\n\t" \
  "LTG_%=:\n\t" \
  "s_mov_b32 s84, 0\n\t" \
  "s_lshl_b32 s60, s36, 4\n\t" \
  "s_or_b32 s60, s60, s48\n\t" \
  "s_or_b32 s60, s60, 268435456\n\t" \
  "v_mov_b32 v76, s60\n\t" \
  "v_mov_b32 v77, 0\n\t" \
  "s_mov_b64 exec, 1\n\t" \
  "buffer_atomic_cmpswap v[76:77], off, s[28:31], s92 sc1\n\t" \
  "s_mov_b64 exec, -1\n\t" \
  "s_branch LG3_%=\n\t" \
  "LIDLE_%=:\n\t" \
  "s_add_u32 s64, s64, 8\n\t" \
  "s_movk_i32 s60, 8192\n\t" \
  "IWT_%=:\n\t" \
  "s_memrealtime s[72:73]\n\t" \
  "s_sub_u32 s60, s60, 1\n\t" \
  "s_cselect_b32 s61, 0, -1\n\t" \
  "s_waitcnt lgkmcnt(0)\n\t" \
  "s_sub_i32 s73, s72, s64\n\t" \
  "s_and_b32 s73, s73, s61\n\t" \
  "s_cmp_lt_i32 s73, 0\n\t" \
  "s_cbranch_scc1 IWT_%=\n\t" \
  "buffer_load_dword v39, v37, s[28:31], 0 offen sc1\n\t" \
  "buffer_store_dword v37, off, s[28:31], s41\n\t" \
  "s_branch RDONE3_%=\n\t" \
  "RFAIL3_%=:\n\t" \
  "s_add_u32 s74, s74, 1\n\t" \
  "s_sleep " CH_RSL "\n\t" \
  "s_waitcnt vmcnt(0)\n\t" \
  "s_cmp_gt_u32 s74, s84\n\t" \
  "s_cbranch_scc1 RG3_%=\n\t" \
  "s_and_b32 s60, s74, 1023\n\t" \
  "s_cmp_lg_u32 s60, 0\n\t" \
  "s_cbranch_scc1 RRETRY3_%=\n\t" \
  "buffer_load_dword v46, off, s[28:31], s92 sc1\n\t" \
  "s_waitcnt vmcnt(0)\n\t" \
  "v_readfirstlane_b32 s60, v46\n\t" \
  "s_nop 0\n\t" \
  "s_cmp_eq_u32 s60, 0\n\t" \
  "s_cbranch_scc1 RRETRY3_%=\n\t" \
  "RG3_%=:\n\t" \
  "s_mov_b32 s84, 0\n\t" \
  "s_lshl_b32 s60, s36, 4\n\t" \
  "s_or_b32 s60, s60, s48\n\t" \
  "s_or_b32 s60, s60, 536870912\n\t" \
  "v_mov_b32 v76, s60\n\t" \
  "v_mov_b32 v77, 0\n\t" \
  "s_mov_b64 exec, 1\n\t" \
  "buffer_atomic_cmpswap v[76:77], off, s[28:31], s92 sc1\n\t" \
  "s_mov_b64 exec, -1\n\t" \
  "s_branch RDONE3_%=\n\t" \
  "LFAIL3_%=:\n\t" \
  "v_max_u32 v75, v0, v1\n\t" \
  "v_max3_u32 v75, v75, v2, v3\n\t" \
  "v_max3_u32 v75, v75, v4, v5\n\t" \
  "v_max3_u32 v75, v75, v6, v7\n\t" \
  "v_max3_u32 v75, v75, v8, v9\n\t" \
  "v_max3_u32 v75, v75, v10, v11\n\t" \
  "v_max3_u32 v75, v75, v12, v13\n\t" \
  "v_max3_u32 v75, v75, v14, v15\n\t" \
  "v_max3_u32 v75, v75, v16, v17\n\t" \
  "v_max3_u32 v75, v75, v18, v19\n\t" \
  "v_max3_u32 v75, v75, v20, v21\n\t" \
  "v_max3_u32 v75, v75, v22, v23\n\t" \
  "v_max3_u32 v75, v75, v24, v25\n\t" \
  "v_max3_u32 v75, v75, v26, v27\n\t" \
  "v_max3_u32 v75, v75, v28, v29\n\t" \
  "v_max3_u32 v75, v75, v30, v31\n\t" \
  "v_max3_u32 v75, v75, v32, v33\n\t" \
  "v_max3_u32 v75, v75, v34, v35\n\t" \
  "v_cmp_eq_u32 vcc, -1, v75\n\t" \
  "s_cbranch_vccz LGO3_%=\n\t" \
  "s_add_u32 s68, s68, 1\n\t" \
  "s_waitcnt lgkmcnt(0)\n\t" \
  "s_cmp_gt_u32 s68, s83\n\t" \
  "s_cbranch_scc1 LGV3_%=\n\t" \
  "s_barrier\n\t" \
  "s_waitcnt vmcnt(8)\n\t" \
  "s_branch LG3_%=\n\t" \
  "RWT3_%=f:\n\t" \
  "s_cmp_lt_i32 s73, -2\n\t" \
  "s_cbranch_scc0 RWT3_%=g\n\t" \
  "s_nop 4\n\t" \
  "s_branch RWT3_%=g\n\t" \
  "LGV3_%=:\n\t" \
  "s_mov_b32 s83, 0\n\t" \
  "s_lshl_b32 s60, s36, 4\n\t" \
  "s_or_b32 s60, s60, s48\n\t" \
  "s_or_b32 s60, s60, 805306368\n\t" \
  "v_mov_b32 v76, s60\n\t" \
  "v_mov_b32 v77, 0\n\t" \
  "s_mov_b64 exec, 1\n\t" \
  "buffer_atomic_cmpswap v[76:77], off, s[28:31], s92 sc1\n\t" \
  "s_mov_b64 exec, -1\n\t" \
  "s_branch LGO3_%=\n\t" \
  "LLAST3_%=:\n\t" \
  "s_waitcnt vmcnt(0) lgkmcnt(0)\n\t" \
  "s_barrier\n\t" \
  "LEND_%=:\n\t" \
  ""
typedef unsigned v16u __attribute__((ext_vector_type(16)));
#define CH_WCLOB "a0", "a1", "a2", "a3", "a4", "a5", "a6", "a7", "a8", "a9", "a10", "a11", "a12", "a13", "a14", "a15", "a16", "a17", "a18", "a19", "a20", "a21", "a22", "a23", "a24", "a25", "a26", "a27", "a28", "a29", "a30", "a31", "a32", "a33", "a34", "a35", "a36", "a37", "a38", "a39", "a40", "a41", "a42", "a43", "a44", "a45", "a46", "a47", "a48", "a49", "a50", "a51", "a52", "a53", "a54", "a55", "a56", "a57", "a58", "a59", "a60", "a61", "a62", "a63", "a64", "a65", "a66", "a67", "a68", "a69", "a70", "a71", "a72", "a73", "a74", "a75", "a76", "a77", "a78", "a79", "a80", "a81", "a82", "a83", "a84", "a85", "a86", "a87", "a88", "a89", "a90", "a91", "a92", "a93", "a94", "a95", "a96", "a97", "a98", "a99", "a100", "a101", "a102", "a103", "a104", "a105", "a106", "a107", "a108", "a109", "a110", "a111", "a112", "a113", "a114", "a115", "a116", "a117", "a118", "a119", "a120", "a121", "a122", "a123", "a124", "a125", "a126", "a127", "a128", "a129", "a130", "a131", "a132", "a133", "a134", "a135", "a136", "a137", "a138", "a139", "a140", "a141", "a142", "a143", "a144", "a145", "a146", "a147", "a148", "a149", "a150", "a151", "a152", "a153", "a154", "a155", "a156", "a157", "a158", "a159", "a160", "a161", "a162", "a163", "a164", "a165", "a166", "a167", "a168", "a169", "a170", "a171", "a172", "a173", "a174", "a175", "a176", "a177", "a178", "a179", "a180", "a181", "a182", "a183", "a184", "a185", "a186", "a187", "a188", "a189", "a190", "a191", "a192", "a193", "a194", "a195", "a196", "a197", "a198", "a199", "a200", "a201", "a202", "a203", "a204", "a205", "a206", "a207", "a208", "a209", "a210", "a211", "a212", "a213", "a214", "a215", "a216", "a217", "a218", "a219", "a220", "a221", "a222", "a223", "a224", "a225", "a226", "a227", "a228", "a229", "a230", "a231", "a232", "a233", "a234", "a235", "a236", "a237", "a238", "a239", "a240", "a241", "a242", "a243", "a244", "a245", "a246", "a247", "a248", "a249", "a250", "a251", "a252", "a253", "a254", "a255", "v80", "v81", "v82", "v83", "v84", "v85", "v86", "v87", "v88", "v89", "v90", "v91", "v92", "v93", "v94", "v95", "v96", "v97", "v98", "v99", "v100", "v101", "v102", "v103", "v104", "v105", "v106", "v107", "v108", "v109", "v110", "v111", "v112", "v113", "v114", "v115", "v116", "v117", "v118", "v119", "v120", "v121", "v122", "v123", "v124", "v125", "v126", "v127", "v128", "v129", "v130", "v131", "v132", "v133", "v134", "v135", "v136", "v137", "v138", "v139", "v140", "v141", "v142", "v143", "v144", "v145", "v146", "v147", "v148", "v149", "v150", "v151", "v152", "v153", "v154", "v155", "v156", "v157", "v158", "v159", "v160", "v161", "v162", "v163", "v164", "v165", "v166", "v167", "v168", "v169", "v170", "v171", "v172", "v173", "v174", "v175", "v176", "v177", "v178", "v179", "v180", "v181", "v182", "v183", "v184", "v185", "v186", "v187", "v188", "v189", "v190", "v191", "v192", "v193", "v194", "v195", "v196", "v197", "v198", "v199", "v200", "v201", "v202", "v203", "v204", "v205", "v206", "v207", "v208", "v209", "v210", "v211", "v212", "v213", "v214", "v215", "v216", "v217", "v218", "v219", "v220", "v221", "v222", "v223", "v224", "v225", "v226", "v227", "v228", "v229", "v230", "v231", "v232", "v233", "v234", "v235", "v236", "v237", "v238", "v239", "v240", "v241", "v242", "v243", "v244", "v245", "v246", "v247", "v248", "v249", "v250", "v251", "v252", "v253", "v254", "v255"
#define CH_H(b) (8 * ((b) & 3) + ((b) >> 3))
#define CH_L(b) (((b) >> 2) & 1)
__device__ __forceinline__ void start_work(char* smem, int wk);
__device__ __forceinline__ void chain_wg(char* smem) {
  if (CH_L((int)blockIdx.x)) start_work(smem, CH_H((int)blockIdx.x));
  const MP p = getp();
  const int b = (int)blockIdx.x, h = CH_H(b), L = CH_L(b);
  int w = __builtin_amdgcn_readfirstlane(threadIdx.x >> 6);
  {
    unsigned id; asm volatile("s_getreg_b32 %0, hwreg(HW_REG_HW_ID, 4, 2)" : "=s"(id));
    volatile __attribute__((address_space(3))) unsigned* lw = (volatile __attribute__((address_space(3))) unsigned*)smem;
    lw[w] = 1u << id;
    BAR();
    if (__builtin_amdgcn_readfirstlane(lw[0] | lw[1] | lw[2] | lw[3]) == 15u) w = (int)((SMAP >> (4u * id)) & 3u);
  }
  unsigned lp16 = (threadIdx.x & 63u) * 16u;
  rsrc_t X = mkr(p.xb, XB_BYTES);
  if ((unsigned)(size_t)(__attribute__((address_space(3))) char*)smem != 0u) return;
  const unsigned wsz = DM * HD * 2u - (unsigned)(h * DH) * 2u;
  const rsrc_t RQ = mkr(p.Wq + h * DH, wsz), RK = mkr(p.Wk + h * DH, wsz), RV = mkr(p.Wv + h * DH, wsz), RO = mkr(p.Wo + (long)h * DH * DM, DH * DM * 2u);
  unsigned xcc; asm volatile("s_getreg_b32 %0, hwreg(HW_REG_XCC_ID)" : "=s"(xcc)); xcc &= 3u;
  unsigned d96 = 0u;
  const unsigned c43 = P_FA * PAIR + (unsigned)h * 128u + 32u;
  const unsigned h128 = (unsigned)h * 128u, uw = (unsigned)w, t = uw >> 1, T = uw & 1u, NONE = 0xF0000000u;
  const unsigned u0 = w == 3 ? (unsigned)h >> 2 : (unsigned)((h & 1) + (h >> 3) * 2 + ((h >> 1) & 3) * 8 + (w == 2 ? 32 : 0));
  const bool idle = w == 3 && (h & 3);
  const unsigned c37 = idle ? NONE : (w == 3 ? AGT + u0 * 128u : AGO + (u0 >> 5) * 32768u + (u0 & 31u) * 128u);
  const unsigned c41 = idle ? NONE : (w == 3 ? 524288u + (u0 >> 1) * 16384u + (u0 & 1u) * 4096u : (u0 & 7u) * 16384u + ((u0 >> 3) & 1u) * 4096u + (u0 >> 4) * 131072u);
  const unsigned c42 = AGO + (xcc & 1u) * 16384u + (xcc >> 1) * 4096u;
  const unsigned c46 = (w == 3 ? (h & 7) == 5 && DL3 > -50 : h == 30) ? AGT + 1024u : NONE;
  const unsigned c38 = (unsigned)(3 * h + 1) * PAIR + T * 4096u + t * 2048u, c39 = (P_KV + (unsigned)(h >> 1)) * PAIR + (unsigned)(h & 1) * 4096u;
  const unsigned c45 = P_FB * PAIR + 4096u + (unsigned)(16 + (h & 15)) * 128u;
  unsigned d48 = 0u, d49 = 0u, d53 = SLOT, d86 = 1u, d65 = rtc();
  unsigned mn = 120u;
  v4u stt;
  asm volatile("s_waitcnt vmcnt(0) lgkmcnt(0)\n\t" CH_ASM "LCHEND:"
               : "+{s48}"(d48), "+{s49}"(d49), "+{s53}"(d53), "+{s65}"(d65), "+{s81}"(mn), "+{s86}"(d86), "+{s96}"(d96), "=&{s[76:79]}"(stt), "+{v69}"(lp16), "+{s[28:31]}"(X)
               : [Wq] "s"(RQ), [Wk] "s"(RK), [Wv] "s"(RV), [Wo] "s"(RO), "{s33}"(HT_OFF + 8192u + 256u + (unsigned)b * 4u), "{s36}"(h128), "{s37}"(c37), "{s40}"(w), "{s41}"(c41), "{s42}"(c42), "{s46}"(c46), "{s92}"(TMO_OFF), "{s38}"(c38), "{s39}"(c39), "{s43}"(c43), "{s45}"(c45), "{s32}"(L), "{s26}"(AGT + 1024u + xcc * 128u), "{s27}"(RDY_OFF(0) + (unsigned)((h % 9) * 1024 + (b & 7) * 128)), [ctab] "i"(CTAB_OFF), [ex] "i"(ED * 256 + EB), [dl1l] "i"(DL1 - LATE), [armd] "i"(3 - ARMK), [arms] "i"(ARMK - 3),
                 [cap] "i"(SPIN_CAP)
               : "v0", "v1", "v2", "v3", "v4", "v5", "v6", "v7", "v8", "v9", "v10", "v11", "v12", "v13", "v14", "v15", "v16", "v17", "v18", "v19",
                 "v20", "v21", "v22", "v23", "v24", "v25", "v26", "v27", "v28", "v29", "v30", "v31", "v32", "v33", "v34", "v35", "v36", "v37", "v38", "v39",
                 "v40", "v41", "v42", "v43", "v44", "v45", "v46", "v47", "v48", "v49", "v50", "v51", "v52", "v53", "v54", "v55", "v56", "v57", "v58", "v59",
                 "v60", "v61", "v62", "v63", "v64", "v65", "v66", "v67", "v68", "v70", "v71", "v72", "v73", "v74", "v75", "v76", "v77", "v78", "v79",
                 CH_WCLOB, "s34", "s35", "s24", "s25", "s90", "s91", "s47", "s87", "s50", "s51", "s52", "s69", "s82", "s85", "s54", "s75", "s80", "s55", "s56", "s57", "s58", "s59", "s60", "s61", "s62", "s63", "s64", "s66", "s67", "s68", "s70", "s71", "s72", "s73", "s74", "s88", "s89",
                 "s83", "s84", "s97", "s98", "s99", "s93", "s94", "s95", "m0", "vcc", "scc", "memory");
}
#define QLEAD 4
#define RONE 2
#define OAUX 0
#define PSP 4
#define QLEAD1 2
#define QXW -18
#define QXC QXW
#define TK_QXW "%[qxw]"
#define TK_QXC "%[qxc]"
#define PSG 16
#define TK_PSG CH_S(PSG)
#define AX 30
#define TK_AX CH_S(AX)
#define PSK 2
#define PSH 2
#define TK_CAP "%[cap]"
#define TK_CTAB "%[ctab]"
#define TK_SG0 CH_S(SG0)
#define TK_PSK CH_S(PSK)
#define TK_PSH CH_S(PSH)
#define AP_ASM \
  "v_lshrrev_b32 v40, 8, v0\n\t" \
  "v_mul_u32_u24 v40, 4608, v40\n\t" \
  "v_and_b32 v41, 0xf0, v0\n\t" \
  "v_lshrrev_b32 v42, 3, v41\n\t" \
  "v_add3_u32 v41, v41, v40, s64\n\t" \
  "v_add3_u32 v42, v42, v40, s65\n\t" \
  "s_mov_b32 s62, 0x05040100\n\t" \
  "s_mov_b32 s63, 0x07060302\n\t" \
  "s_xor_b32 s60, s66, 0\n\t" \
  "s_add_u32 s60, s60, 0\n\t" \
  "s_add_u32 s61, s60, 18432\n\t" \
  "buffer_load_dwordx4 v[48:51], v41, %[W], s60 offen offset:0\n\t" \
  "buffer_load_dwordx4 v[52:55], v41, %[W], s60 offen offset:1152\n\t" \
  "buffer_load_dwordx4 v[56:59], v41, %[W], s60 offen offset:2304\n\t" \
  "buffer_load_dwordx4 v[60:63], v41, %[W], s60 offen offset:3456\n\t" \
  "buffer_load_dwordx4 v[64:67], v41, %[W], s61 offen offset:0\n\t" \
  "buffer_load_dwordx4 v[68:71], v41, %[W], s61 offen offset:1152\n\t" \
  "buffer_load_dwordx4 v[72:75], v41, %[W], s61 offen offset:2304\n\t" \
  "buffer_load_dwordx4 v[76:79], v41, %[W], s61 offen offset:3456\n\t" \
  "s_xor_b32 s60, s66, 0\n\t" \
  "s_add_u32 s60, s60, 36864\n\t" \
  "s_add_u32 s61, s60, 18432\n\t" \
  "buffer_load_dwordx4 v[80:83], v41, %[W], s60 offen offset:0\n\t" \
  "buffer_load_dwordx4 v[84:87], v41, %[W], s60 offen offset:1152\n\t" \
  "buffer_load_dwordx4 v[88:91], v41, %[W], s60 offen offset:2304\n\t" \
  "buffer_load_dwordx4 v[92:95], v41, %[W], s60 offen offset:3456\n\t" \
  "buffer_load_dwordx4 v[96:99], v41, %[W], s61 offen offset:0\n\t" \
  "buffer_load_dwordx4 v[100:103], v41, %[W], s61 offen offset:1152\n\t" \
  "buffer_load_dwordx4 v[104:107], v41, %[W], s61 offen offset:2304\n\t" \
  "buffer_load_dwordx4 v[108:111], v41, %[W], s61 offen offset:3456\n\t" \
  "s_xor_b32 s60, s66, 0\n\t" \
  "s_add_u32 s60, s60, 73728\n\t" \
  "s_add_u32 s61, s60, 18432\n\t" \
  "buffer_load_dwordx4 v[112:115], v41, %[W], s60 offen offset:0\n\t" \
  "buffer_load_dwordx4 v[116:119], v41, %[W], s60 offen offset:1152\n\t" \
  "buffer_load_dwordx4 v[120:123], v41, %[W], s60 offen offset:2304\n\t" \
  "buffer_load_dwordx4 v[124:127], v41, %[W], s60 offen offset:3456\n\t" \
  "buffer_load_dwordx4 v[128:131], v41, %[W], s61 offen offset:0\n\t" \
  "buffer_load_dwordx4 v[132:135], v41, %[W], s61 offen offset:1152\n\t" \
  "buffer_load_dwordx4 v[136:139], v41, %[W], s61 offen offset:2304\n\t" \
  "buffer_load_dwordx4 v[140:143], v41, %[W], s61 offen offset:3456\n\t" \
  "s_xor_b32 s60, s66, 0\n\t" \
  "s_add_u32 s60, s60, 110592\n\t" \
  "s_add_u32 s61, s60, 18432\n\t" \
  "buffer_load_dwordx4 v[144:147], v41, %[W], s60 offen offset:0\n\t" \
  "buffer_load_dwordx4 v[148:151], v41, %[W], s60 offen offset:1152\n\t" \
  "buffer_load_dwordx4 v[152:155], v41, %[W], s60 offen offset:2304\n\t" \
  "buffer_load_dwordx4 v[156:159], v41, %[W], s60 offen offset:3456\n\t" \
  "buffer_load_dwordx4 v[160:163], v41, %[W], s61 offen offset:0\n\t" \
  "buffer_load_dwordx4 v[164:167], v41, %[W], s61 offen offset:1152\n\t" \
  "buffer_load_dwordx4 v[168:171], v41, %[W], s61 offen offset:2304\n\t" \
  "buffer_load_dwordx4 v[172:175], v41, %[W], s61 offen offset:3456\n\t" \
  "s_xor_b32 s60, s66, 147456\n\t" \
  "s_add_u32 s60, s60, 0\n\t" \
  "s_add_u32 s61, s60, 18432\n\t" \
  "buffer_load_dwordx4 v[176:179], v41, %[W], s60 offen offset:0\n\t" \
  "buffer_load_dwordx4 v[180:183], v41, %[W], s60 offen offset:1152\n\t" \
  "buffer_load_dwordx4 v[184:187], v41, %[W], s60 offen offset:2304\n\t" \
  "buffer_load_dwordx4 v[188:191], v41, %[W], s60 offen offset:3456\n\t" \
  "buffer_load_dwordx4 v[192:195], v41, %[W], s61 offen offset:0\n\t" \
  "buffer_load_dwordx4 v[196:199], v41, %[W], s61 offen offset:1152\n\t" \
  "buffer_load_dwordx4 v[200:203], v41, %[W], s61 offen offset:2304\n\t" \
  "buffer_load_dwordx4 v[204:207], v41, %[W], s61 offen offset:3456\n\t" \
  "s_waitcnt vmcnt(38)\n\t" \
  "v_perm_b32 v44, v52, v48, s62\n\t" \
  "s_waitcnt vmcnt(36)\n\t" \
  "v_perm_b32 v45, v60, v56, s62\n\t" \
  "s_waitcnt vmcnt(34)\n\t" \
  "v_perm_b32 v46, v68, v64, s62\n\t" \
  "s_waitcnt vmcnt(32)\n\t" \
  "v_perm_b32 v47, v76, v72, s62\n\t" \
  "v_accvgpr_write_b32 a0, v44\n\t" \
  "v_accvgpr_write_b32 a1, v45\n\t" \
  "v_accvgpr_write_b32 a2, v46\n\t" \
  "v_accvgpr_write_b32 a3, v47\n\t" \
  "v_perm_b32 v44, v52, v48, s63\n\t" \
  "v_perm_b32 v45, v60, v56, s63\n\t" \
  "v_perm_b32 v46, v68, v64, s63\n\t" \
  "v_perm_b32 v47, v76, v72, s63\n\t" \
  "v_accvgpr_write_b32 a4, v44\n\t" \
  "v_accvgpr_write_b32 a5, v45\n\t" \
  "v_accvgpr_write_b32 a6, v46\n\t" \
  "v_accvgpr_write_b32 a7, v47\n\t" \
  "v_perm_b32 v44, v53, v49, s62\n\t" \
  "v_perm_b32 v45, v61, v57, s62\n\t" \
  "v_perm_b32 v46, v69, v65, s62\n\t" \
  "v_perm_b32 v47, v77, v73, s62\n\t" \
  "v_accvgpr_write_b32 a8, v44\n\t" \
  "v_accvgpr_write_b32 a9, v45\n\t" \
  "v_accvgpr_write_b32 a10, v46\n\t" \
  "v_accvgpr_write_b32 a11, v47\n\t" \
  "v_perm_b32 v44, v53, v49, s63\n\t" \
  "v_perm_b32 v45, v61, v57, s63\n\t" \
  "v_perm_b32 v46, v69, v65, s63\n\t" \
  "v_perm_b32 v47, v77, v73, s63\n\t" \
  "v_accvgpr_write_b32 a12, v44\n\t" \
  "v_accvgpr_write_b32 a13, v45\n\t" \
  "v_accvgpr_write_b32 a14, v46\n\t" \
  "v_accvgpr_write_b32 a15, v47\n\t" \
  "v_perm_b32 v44, v54, v50, s62\n\t" \
  "v_perm_b32 v45, v62, v58, s62\n\t" \
  "v_perm_b32 v46, v70, v66, s62\n\t" \
  "v_perm_b32 v47, v78, v74, s62\n\t" \
  "v_accvgpr_write_b32 a16, v44\n\t" \
  "v_accvgpr_write_b32 a17, v45\n\t" \
  "v_accvgpr_write_b32 a18, v46\n\t" \
  "v_accvgpr_write_b32 a19, v47\n\t" \
  "v_perm_b32 v44, v54, v50, s63\n\t" \
  "v_perm_b32 v45, v62, v58, s63\n\t" \
  "v_perm_b32 v46, v70, v66, s63\n\t" \
  "v_perm_b32 v47, v78, v74, s63\n\t" \
  "v_accvgpr_write_b32 a20, v44\n\t" \
  "v_accvgpr_write_b32 a21, v45\n\t" \
  "v_accvgpr_write_b32 a22, v46\n\t" \
  "v_accvgpr_write_b32 a23, v47\n\t" \
  "v_perm_b32 v44, v55, v51, s62\n\t" \
  "v_perm_b32 v45, v63, v59, s62\n\t" \
  "v_perm_b32 v46, v71, v67, s62\n\t" \
  "v_perm_b32 v47, v79, v75, s62\n\t" \
  "v_accvgpr_write_b32 a24, v44\n\t" \
  "v_accvgpr_write_b32 a25, v45\n\t" \
  "v_accvgpr_write_b32 a26, v46\n\t" \
  "v_accvgpr_write_b32 a27, v47\n\t" \
  "v_perm_b32 v44, v55, v51, s63\n\t" \
  "v_perm_b32 v45, v63, v59, s63\n\t" \
  "v_perm_b32 v46, v71, v67, s63\n\t" \
  "v_perm_b32 v47, v79, v75, s63\n\t" \
  "v_accvgpr_write_b32 a28, v44\n\t" \
  "v_accvgpr_write_b32 a29, v45\n\t" \
  "v_accvgpr_write_b32 a30, v46\n\t" \
  "v_accvgpr_write_b32 a31, v47\n\t" \
  "s_xor_b32 s60, s66, 147456\n\t" \
  "s_add_u32 s60, s60, 36864\n\t" \
  "s_add_u32 s61, s60, 18432\n\t" \
  "buffer_load_dwordx4 v[48:51], v41, %[W], s60 offen offset:0\n\t" \
  "buffer_load_dwordx4 v[52:55], v41, %[W], s60 offen offset:1152\n\t" \
  "buffer_load_dwordx4 v[56:59], v41, %[W], s60 offen offset:2304\n\t" \
  "buffer_load_dwordx4 v[60:63], v41, %[W], s60 offen offset:3456\n\t" \
  "buffer_load_dwordx4 v[64:67], v41, %[W], s61 offen offset:0\n\t" \
  "buffer_load_dwordx4 v[68:71], v41, %[W], s61 offen offset:1152\n\t" \
  "buffer_load_dwordx4 v[72:75], v41, %[W], s61 offen offset:2304\n\t" \
  "buffer_load_dwordx4 v[76:79], v41, %[W], s61 offen offset:3456\n\t" \
  "s_waitcnt vmcnt(38)\n\t" \
  "v_perm_b32 v44, v84, v80, s62\n\t" \
  "s_waitcnt vmcnt(36)\n\t" \
  "v_perm_b32 v45, v92, v88, s62\n\t" \
  "s_waitcnt vmcnt(34)\n\t" \
  "v_perm_b32 v46, v100, v96, s62\n\t" \
  "s_waitcnt vmcnt(32)\n\t" \
  "v_perm_b32 v47, v108, v104, s62\n\t" \
  "v_accvgpr_write_b32 a32, v44\n\t" \
  "v_accvgpr_write_b32 a33, v45\n\t" \
  "v_accvgpr_write_b32 a34, v46\n\t" \
  "v_accvgpr_write_b32 a35, v47\n\t" \
  "v_perm_b32 v44, v84, v80, s63\n\t" \
  "v_perm_b32 v45, v92, v88, s63\n\t" \
  "v_perm_b32 v46, v100, v96, s63\n\t" \
  "v_perm_b32 v47, v108, v104, s63\n\t" \
  "v_accvgpr_write_b32 a36, v44\n\t" \
  "v_accvgpr_write_b32 a37, v45\n\t" \
  "v_accvgpr_write_b32 a38, v46\n\t" \
  "v_accvgpr_write_b32 a39, v47\n\t" \
  "v_perm_b32 v44, v85, v81, s62\n\t" \
  "v_perm_b32 v45, v93, v89, s62\n\t" \
  "v_perm_b32 v46, v101, v97, s62\n\t" \
  "v_perm_b32 v47, v109, v105, s62\n\t" \
  "v_accvgpr_write_b32 a40, v44\n\t" \
  "v_accvgpr_write_b32 a41, v45\n\t" \
  "v_accvgpr_write_b32 a42, v46\n\t" \
  "v_accvgpr_write_b32 a43, v47\n\t" \
  "v_perm_b32 v44, v85, v81, s63\n\t" \
  "v_perm_b32 v45, v93, v89, s63\n\t" \
  "v_perm_b32 v46, v101, v97, s63\n\t" \
  "v_perm_b32 v47, v109, v105, s63\n\t" \
  "v_accvgpr_write_b32 a44, v44\n\t" \
  "v_accvgpr_write_b32 a45, v45\n\t" \
  "v_accvgpr_write_b32 a46, v46\n\t" \
  "v_accvgpr_write_b32 a47, v47\n\t" \
  "v_perm_b32 v44, v86, v82, s62\n\t" \
  "v_perm_b32 v45, v94, v90, s62\n\t" \
  "v_perm_b32 v46, v102, v98, s62\n\t" \
  "v_perm_b32 v47, v110, v106, s62\n\t" \
  "v_accvgpr_write_b32 a48, v44\n\t" \
  "v_accvgpr_write_b32 a49, v45\n\t" \
  "v_accvgpr_write_b32 a50, v46\n\t" \
  "v_accvgpr_write_b32 a51, v47\n\t" \
  "v_perm_b32 v44, v86, v82, s63\n\t" \
  "v_perm_b32 v45, v94, v90, s63\n\t" \
  "v_perm_b32 v46, v102, v98, s63\n\t" \
  "v_perm_b32 v47, v110, v106, s63\n\t" \
  "v_accvgpr_write_b32 a52, v44\n\t" \
  "v_accvgpr_write_b32 a53, v45\n\t" \
  "v_accvgpr_write_b32 a54, v46\n\t" \
  "v_accvgpr_write_b32 a55, v47\n\t" \
  "v_perm_b32 v44, v87, v83, s62\n\t" \
  "v_perm_b32 v45, v95, v91, s62\n\t" \
  "v_perm_b32 v46, v103, v99, s62\n\t" \
  "v_perm_b32 v47, v111, v107, s62\n\t" \
  "v_accvgpr_write_b32 a56, v44\n\t" \
  "v_accvgpr_write_b32 a57, v45\n\t" \
  "v_accvgpr_write_b32 a58, v46\n\t" \
  "v_accvgpr_write_b32 a59, v47\n\t" \
  "v_perm_b32 v44, v87, v83, s63\n\t" \
  "v_perm_b32 v45, v95, v91, s63\n\t" \
  "v_perm_b32 v46, v103, v99, s63\n\t" \
  "v_perm_b32 v47, v111, v107, s63\n\t" \
  "v_accvgpr_write_b32 a60, v44\n\t" \
  "v_accvgpr_write_b32 a61, v45\n\t" \
  "v_accvgpr_write_b32 a62, v46\n\t" \
  "v_accvgpr_write_b32 a63, v47\n\t" \
  "s_xor_b32 s60, s66, 147456\n\t" \
  "s_add_u32 s60, s60, 73728\n\t" \
  "s_add_u32 s61, s60, 18432\n\t" \
  "buffer_load_dwordx4 v[80:83], v41, %[W], s60 offen offset:0\n\t" \
  "buffer_load_dwordx4 v[84:87], v41, %[W], s60 offen offset:1152\n\t" \
  "buffer_load_dwordx4 v[88:91], v41, %[W], s60 offen offset:2304\n\t" \
  "buffer_load_dwordx4 v[92:95], v41, %[W], s60 offen offset:3456\n\t" \
  "buffer_load_dwordx4 v[96:99], v41, %[W], s61 offen offset:0\n\t" \
  "buffer_load_dwordx4 v[100:103], v41, %[W], s61 offen offset:1152\n\t" \
  "buffer_load_dwordx4 v[104:107], v41, %[W], s61 offen offset:2304\n\t" \
  "buffer_load_dwordx4 v[108:111], v41, %[W], s61 offen offset:3456\n\t" \
  "s_waitcnt vmcnt(38)\n\t" \
  "v_perm_b32 v44, v116, v112, s62\n\t" \
  "s_waitcnt vmcnt(36)\n\t" \
  "v_perm_b32 v45, v124, v120, s62\n\t" \
  "s_waitcnt vmcnt(34)\n\t" \
  "v_perm_b32 v46, v132, v128, s62\n\t" \
  "s_waitcnt vmcnt(32)\n\t" \
  "v_perm_b32 v47, v140, v136, s62\n\t" \
  "v_accvgpr_write_b32 a64, v44\n\t" \
  "v_accvgpr_write_b32 a65, v45\n\t" \
  "v_accvgpr_write_b32 a66, v46\n\t" \
  "v_accvgpr_write_b32 a67, v47\n\t" \
  "v_perm_b32 v44, v116, v112, s63\n\t" \
  "v_perm_b32 v45, v124, v120, s63\n\t" \
  "v_perm_b32 v46, v132, v128, s63\n\t" \
  "v_perm_b32 v47, v140, v136, s63\n\t" \
  "v_accvgpr_write_b32 a68, v44\n\t" \
  "v_accvgpr_write_b32 a69, v45\n\t" \
  "v_accvgpr_write_b32 a70, v46\n\t" \
  "v_accvgpr_write_b32 a71, v47\n\t" \
  "v_perm_b32 v44, v117, v113, s62\n\t" \
  "v_perm_b32 v45, v125, v121, s62\n\t" \
  "v_perm_b32 v46, v133, v129, s62\n\t" \
  "v_perm_b32 v47, v141, v137, s62\n\t" \
  "v_accvgpr_write_b32 a72, v44\n\t" \
  "v_accvgpr_write_b32 a73, v45\n\t" \
  "v_accvgpr_write_b32 a74, v46\n\t" \
  "v_accvgpr_write_b32 a75, v47\n\t" \
  "v_perm_b32 v44, v117, v113, s63\n\t" \
  "v_perm_b32 v45, v125, v121, s63\n\t" \
  "v_perm_b32 v46, v133, v129, s63\n\t" \
  "v_perm_b32 v47, v141, v137, s63\n\t" \
  "v_accvgpr_write_b32 a76, v44\n\t" \
  "v_accvgpr_write_b32 a77, v45\n\t" \
  "v_accvgpr_write_b32 a78, v46\n\t" \
  "v_accvgpr_write_b32 a79, v47\n\t" \
  "v_perm_b32 v44, v118, v114, s62\n\t" \
  "v_perm_b32 v45, v126, v122, s62\n\t" \
  "v_perm_b32 v46, v134, v130, s62\n\t" \
  "v_perm_b32 v47, v142, v138, s62\n\t" \
  "v_accvgpr_write_b32 a80, v44\n\t" \
  "v_accvgpr_write_b32 a81, v45\n\t" \
  "v_accvgpr_write_b32 a82, v46\n\t" \
  "v_accvgpr_write_b32 a83, v47\n\t" \
  "v_perm_b32 v44, v118, v114, s63\n\t" \
  "v_perm_b32 v45, v126, v122, s63\n\t" \
  "v_perm_b32 v46, v134, v130, s63\n\t" \
  "v_perm_b32 v47, v142, v138, s63\n\t" \
  "v_accvgpr_write_b32 a84, v44\n\t" \
  "v_accvgpr_write_b32 a85, v45\n\t" \
  "v_accvgpr_write_b32 a86, v46\n\t" \
  "v_accvgpr_write_b32 a87, v47\n\t" \
  "v_perm_b32 v44, v119, v115, s62\n\t" \
  "v_perm_b32 v45, v127, v123, s62\n\t" \
  "v_perm_b32 v46, v135, v131, s62\n\t" \
  "v_perm_b32 v47, v143, v139, s62\n\t" \
  "v_accvgpr_write_b32 a88, v44\n\t" \
  "v_accvgpr_write_b32 a89, v45\n\t" \
  "v_accvgpr_write_b32 a90, v46\n\t" \
  "v_accvgpr_write_b32 a91, v47\n\t" \
  "v_perm_b32 v44, v119, v115, s63\n\t" \
  "v_perm_b32 v45, v127, v123, s63\n\t" \
  "v_perm_b32 v46, v135, v131, s63\n\t" \
  "v_perm_b32 v47, v143, v139, s63\n\t" \
  "v_accvgpr_write_b32 a92, v44\n\t" \
  "v_accvgpr_write_b32 a93, v45\n\t" \
  "v_accvgpr_write_b32 a94, v46\n\t" \
  "v_accvgpr_write_b32 a95, v47\n\t" \
  "s_xor_b32 s60, s66, 147456\n\t" \
  "s_add_u32 s60, s60, 110592\n\t" \
  "s_add_u32 s61, s60, 18432\n\t" \
  "buffer_load_dwordx4 v[112:115], v41, %[W], s60 offen offset:0\n\t" \
  "buffer_load_dwordx4 v[116:119], v41, %[W], s60 offen offset:1152\n\t" \
  "buffer_load_dwordx4 v[120:123], v41, %[W], s60 offen offset:2304\n\t" \
  "buffer_load_dwordx4 v[124:127], v41, %[W], s60 offen offset:3456\n\t" \
  "buffer_load_dwordx4 v[128:131], v41, %[W], s61 offen offset:0\n\t" \
  "buffer_load_dwordx4 v[132:135], v41, %[W], s61 offen offset:1152\n\t" \
  "buffer_load_dwordx4 v[136:139], v41, %[W], s61 offen offset:2304\n\t" \
  "buffer_load_dwordx4 v[140:143], v41, %[W], s61 offen offset:3456\n\t" \
  "s_waitcnt vmcnt(38)\n\t" \
  "v_perm_b32 v44, v148, v144, s62\n\t" \
  "s_waitcnt vmcnt(36)\n\t" \
  "v_perm_b32 v45, v156, v152, s62\n\t" \
  "s_waitcnt vmcnt(34)\n\t" \
  "v_perm_b32 v46, v164, v160, s62\n\t" \
  "s_waitcnt vmcnt(32)\n\t" \
  "v_perm_b32 v47, v172, v168, s62\n\t" \
  "v_accvgpr_write_b32 a96, v44\n\t" \
  "v_accvgpr_write_b32 a97, v45\n\t" \
  "v_accvgpr_write_b32 a98, v46\n\t" \
  "v_accvgpr_write_b32 a99, v47\n\t" \
  "v_perm_b32 v44, v148, v144, s63\n\t" \
  "v_perm_b32 v45, v156, v152, s63\n\t" \
  "v_perm_b32 v46, v164, v160, s63\n\t" \
  "v_perm_b32 v47, v172, v168, s63\n\t" \
  "v_accvgpr_write_b32 a100, v44\n\t" \
  "v_accvgpr_write_b32 a101, v45\n\t" \
  "v_accvgpr_write_b32 a102, v46\n\t" \
  "v_accvgpr_write_b32 a103, v47\n\t" \
  "v_perm_b32 v44, v149, v145, s62\n\t" \
  "v_perm_b32 v45, v157, v153, s62\n\t" \
  "v_perm_b32 v46, v165, v161, s62\n\t" \
  "v_perm_b32 v47, v173, v169, s62\n\t" \
  "v_accvgpr_write_b32 a104, v44\n\t" \
  "v_accvgpr_write_b32 a105, v45\n\t" \
  "v_accvgpr_write_b32 a106, v46\n\t" \
  "v_accvgpr_write_b32 a107, v47\n\t" \
  "v_perm_b32 v44, v149, v145, s63\n\t" \
  "v_perm_b32 v45, v157, v153, s63\n\t" \
  "v_perm_b32 v46, v165, v161, s63\n\t" \
  "v_perm_b32 v47, v173, v169, s63\n\t" \
  "v_accvgpr_write_b32 a108, v44\n\t" \
  "v_accvgpr_write_b32 a109, v45\n\t" \
  "v_accvgpr_write_b32 a110, v46\n\t" \
  "v_accvgpr_write_b32 a111, v47\n\t" \
  "v_perm_b32 v44, v150, v146, s62\n\t" \
  "v_perm_b32 v45, v158, v154, s62\n\t" \
  "v_perm_b32 v46, v166, v162, s62\n\t" \
  "v_perm_b32 v47, v174, v170, s62\n\t" \
  "v_accvgpr_write_b32 a112, v44\n\t" \
  "v_accvgpr_write_b32 a113, v45\n\t" \
  "v_accvgpr_write_b32 a114, v46\n\t" \
  "v_accvgpr_write_b32 a115, v47\n\t" \
  "v_perm_b32 v44, v150, v146, s63\n\t" \
  "v_perm_b32 v45, v158, v154, s63\n\t" \
  "v_perm_b32 v46, v166, v162, s63\n\t" \
  "v_perm_b32 v47, v174, v170, s63\n\t" \
  "v_accvgpr_write_b32 a116, v44\n\t" \
  "v_accvgpr_write_b32 a117, v45\n\t" \
  "v_accvgpr_write_b32 a118, v46\n\t" \
  "v_accvgpr_write_b32 a119, v47\n\t" \
  "v_perm_b32 v44, v151, v147, s62\n\t" \
  "v_perm_b32 v45, v159, v155, s62\n\t" \
  "v_perm_b32 v46, v167, v163, s62\n\t" \
  "v_perm_b32 v47, v175, v171, s62\n\t" \
  "v_accvgpr_write_b32 a120, v44\n\t" \
  "v_accvgpr_write_b32 a121, v45\n\t" \
  "v_accvgpr_write_b32 a122, v46\n\t" \
  "v_accvgpr_write_b32 a123, v47\n\t" \
  "v_perm_b32 v44, v151, v147, s63\n\t" \
  "v_perm_b32 v45, v159, v155, s63\n\t" \
  "v_perm_b32 v46, v167, v163, s63\n\t" \
  "v_perm_b32 v47, v175, v171, s63\n\t" \
  "v_accvgpr_write_b32 a124, v44\n\t" \
  "v_accvgpr_write_b32 a125, v45\n\t" \
  "v_accvgpr_write_b32 a126, v46\n\t" \
  "v_accvgpr_write_b32 a127, v47\n\t" \
  "s_xor_b32 s60, s66, 0\n\t" \
  "s_add_u32 s60, s60, 0\n\t" \
  "s_add_u32 s61, s60, 18432\n\t" \
  "buffer_load_ushort v224, v42, %[W], s60 offen offset:0\n\t" \
  "buffer_load_ushort v144, v42, %[W], s60 offen offset:1152\n\t" \
  "buffer_load_ushort v225, v42, %[W], s60 offen offset:2304\n\t" \
  "buffer_load_ushort v145, v42, %[W], s60 offen offset:3456\n\t" \
  "buffer_load_ushort v226, v42, %[W], s61 offen offset:0\n\t" \
  "buffer_load_ushort v146, v42, %[W], s61 offen offset:1152\n\t" \
  "buffer_load_ushort v227, v42, %[W], s61 offen offset:2304\n\t" \
  "buffer_load_ushort v147, v42, %[W], s61 offen offset:3456\n\t" \
  "s_xor_b32 s60, s66, 0\n\t" \
  "s_add_u32 s60, s60, 36864\n\t" \
  "s_add_u32 s61, s60, 18432\n\t" \
  "buffer_load_ushort v228, v42, %[W], s60 offen offset:0\n\t" \
  "buffer_load_ushort v148, v42, %[W], s60 offen offset:1152\n\t" \
  "buffer_load_ushort v229, v42, %[W], s60 offen offset:2304\n\t" \
  "buffer_load_ushort v149, v42, %[W], s60 offen offset:3456\n\t" \
  "buffer_load_ushort v230, v42, %[W], s61 offen offset:0\n\t" \
  "buffer_load_ushort v150, v42, %[W], s61 offen offset:1152\n\t" \
  "buffer_load_ushort v231, v42, %[W], s61 offen offset:2304\n\t" \
  "buffer_load_ushort v151, v42, %[W], s61 offen offset:3456\n\t" \
  "s_waitcnt vmcnt(46)\n\t" \
  "v_perm_b32 v44, v180, v176, s62\n\t" \
  "s_waitcnt vmcnt(44)\n\t" \
  "v_perm_b32 v45, v188, v184, s62\n\t" \
  "s_waitcnt vmcnt(42)\n\t" \
  "v_perm_b32 v46, v196, v192, s62\n\t" \
  "s_waitcnt vmcnt(40)\n\t" \
  "v_perm_b32 v47, v204, v200, s62\n\t" \
  "v_accvgpr_write_b32 a128, v44\n\t" \
  "v_accvgpr_write_b32 a129, v45\n\t" \
  "v_accvgpr_write_b32 a130, v46\n\t" \
  "v_accvgpr_write_b32 a131, v47\n\t" \
  "v_perm_b32 v44, v180, v176, s63\n\t" \
  "v_perm_b32 v45, v188, v184, s63\n\t" \
  "v_perm_b32 v46, v196, v192, s63\n\t" \
  "v_perm_b32 v47, v204, v200, s63\n\t" \
  "v_accvgpr_write_b32 a132, v44\n\t" \
  "v_accvgpr_write_b32 a133, v45\n\t" \
  "v_accvgpr_write_b32 a134, v46\n\t" \
  "v_accvgpr_write_b32 a135, v47\n\t" \
  "v_perm_b32 v44, v181, v177, s62\n\t" \
  "v_perm_b32 v45, v189, v185, s62\n\t" \
  "v_perm_b32 v46, v197, v193, s62\n\t" \
  "v_perm_b32 v47, v205, v201, s62\n\t" \
  "v_accvgpr_write_b32 a136, v44\n\t" \
  "v_accvgpr_write_b32 a137, v45\n\t" \
  "v_accvgpr_write_b32 a138, v46\n\t" \
  "v_accvgpr_write_b32 a139, v47\n\t" \
  "v_perm_b32 v44, v181, v177, s63\n\t" \
  "v_perm_b32 v45, v189, v185, s63\n\t" \
  "v_perm_b32 v46, v197, v193, s63\n\t" \
  "v_perm_b32 v47, v205, v201, s63\n\t" \
  "v_accvgpr_write_b32 a140, v44\n\t" \
  "v_accvgpr_write_b32 a141, v45\n\t" \
  "v_accvgpr_write_b32 a142, v46\n\t" \
  "v_accvgpr_write_b32 a143, v47\n\t" \
  "v_perm_b32 v44, v182, v178, s62\n\t" \
  "v_perm_b32 v45, v190, v186, s62\n\t" \
  "v_perm_b32 v46, v198, v194, s62\n\t" \
  "v_perm_b32 v47, v206, v202, s62\n\t" \
  "v_accvgpr_write_b32 a144, v44\n\t" \
  "v_accvgpr_write_b32 a145, v45\n\t" \
  "v_accvgpr_write_b32 a146, v46\n\t" \
  "v_accvgpr_write_b32 a147, v47\n\t" \
  "v_perm_b32 v44, v182, v178, s63\n\t" \
  "v_perm_b32 v45, v190, v186, s63\n\t" \
  "v_perm_b32 v46, v198, v194, s63\n\t" \
  "v_perm_b32 v47, v206, v202, s63\n\t" \
  "v_accvgpr_write_b32 a148, v44\n\t" \
  "v_accvgpr_write_b32 a149, v45\n\t" \
  "v_accvgpr_write_b32 a150, v46\n\t" \
  "v_accvgpr_write_b32 a151, v47\n\t" \
  "v_perm_b32 v44, v183, v179, s62\n\t" \
  "v_perm_b32 v45, v191, v187, s62\n\t" \
  "v_perm_b32 v46, v199, v195, s62\n\t" \
  "v_perm_b32 v47, v207, v203, s62\n\t" \
  "v_accvgpr_write_b32 a152, v44\n\t" \
  "v_accvgpr_write_b32 a153, v45\n\t" \
  "v_accvgpr_write_b32 a154, v46\n\t" \
  "v_accvgpr_write_b32 a155, v47\n\t" \
  "v_perm_b32 v44, v183, v179, s63\n\t" \
  "v_perm_b32 v45, v191, v187, s63\n\t" \
  "v_perm_b32 v46, v199, v195, s63\n\t" \
  "v_perm_b32 v47, v207, v203, s63\n\t" \
  "v_accvgpr_write_b32 a156, v44\n\t" \
  "v_accvgpr_write_b32 a157, v45\n\t" \
  "v_accvgpr_write_b32 a158, v46\n\t" \
  "v_accvgpr_write_b32 a159, v47\n\t" \
  "s_xor_b32 s60, s66, 0\n\t" \
  "s_add_u32 s60, s60, 73728\n\t" \
  "s_add_u32 s61, s60, 18432\n\t" \
  "buffer_load_ushort v232, v42, %[W], s60 offen offset:0\n\t" \
  "buffer_load_ushort v152, v42, %[W], s60 offen offset:1152\n\t" \
  "buffer_load_ushort v233, v42, %[W], s60 offen offset:2304\n\t" \
  "buffer_load_ushort v153, v42, %[W], s60 offen offset:3456\n\t" \
  "buffer_load_ushort v234, v42, %[W], s61 offen offset:0\n\t" \
  "buffer_load_ushort v154, v42, %[W], s61 offen offset:1152\n\t" \
  "buffer_load_ushort v235, v42, %[W], s61 offen offset:2304\n\t" \
  "buffer_load_ushort v155, v42, %[W], s61 offen offset:3456\n\t" \
  "s_xor_b32 s60, s66, 0\n\t" \
  "s_add_u32 s60, s60, 110592\n\t" \
  "s_add_u32 s61, s60, 18432\n\t" \
  "buffer_load_ushort v236, v42, %[W], s60 offen offset:0\n\t" \
  "buffer_load_ushort v156, v42, %[W], s60 offen offset:1152\n\t" \
  "buffer_load_ushort v237, v42, %[W], s60 offen offset:2304\n\t" \
  "buffer_load_ushort v157, v42, %[W], s60 offen offset:3456\n\t" \
  "buffer_load_ushort v238, v42, %[W], s61 offen offset:0\n\t" \
  "buffer_load_ushort v158, v42, %[W], s61 offen offset:1152\n\t" \
  "buffer_load_ushort v239, v42, %[W], s61 offen offset:2304\n\t" \
  "buffer_load_ushort v159, v42, %[W], s61 offen offset:3456\n\t" \
  "s_waitcnt vmcnt(54)\n\t" \
  "v_perm_b32 v44, v52, v48, s62\n\t" \
  "s_waitcnt vmcnt(52)\n\t" \
  "v_perm_b32 v45, v60, v56, s62\n\t" \
  "s_waitcnt vmcnt(50)\n\t" \
  "v_perm_b32 v46, v68, v64, s62\n\t" \
  "s_waitcnt vmcnt(48)\n\t" \
  "v_perm_b32 v47, v76, v72, s62\n\t" \
  "v_accvgpr_write_b32 a160, v44\n\t" \
  "v_accvgpr_write_b32 a161, v45\n\t" \
  "v_accvgpr_write_b32 a162, v46\n\t" \
  "v_accvgpr_write_b32 a163, v47\n\t" \
  "v_perm_b32 v44, v52, v48, s63\n\t" \
  "v_perm_b32 v45, v60, v56, s63\n\t" \
  "v_perm_b32 v46, v68, v64, s63\n\t" \
  "v_perm_b32 v47, v76, v72, s63\n\t" \
  "v_accvgpr_write_b32 a164, v44\n\t" \
  "v_accvgpr_write_b32 a165, v45\n\t" \
  "v_accvgpr_write_b32 a166, v46\n\t" \
  "v_accvgpr_write_b32 a167, v47\n\t" \
  "v_perm_b32 v44, v53, v49, s62\n\t" \
  "v_perm_b32 v45, v61, v57, s62\n\t" \
  "v_perm_b32 v46, v69, v65, s62\n\t" \
  "v_perm_b32 v47, v77, v73, s62\n\t" \
  "v_accvgpr_write_b32 a168, v44\n\t" \
  "v_accvgpr_write_b32 a169, v45\n\t" \
  "v_accvgpr_write_b32 a170, v46\n\t" \
  "v_accvgpr_write_b32 a171, v47\n\t" \
  "v_perm_b32 v44, v53, v49, s63\n\t" \
  "v_perm_b32 v45, v61, v57, s63\n\t" \
  "v_perm_b32 v46, v69, v65, s63\n\t" \
  "v_perm_b32 v47, v77, v73, s63\n\t" \
  "v_accvgpr_write_b32 a172, v44\n\t" \
  "v_accvgpr_write_b32 a173, v45\n\t" \
  "v_accvgpr_write_b32 a174, v46\n\t" \
  "v_accvgpr_write_b32 a175, v47\n\t" \
  "v_perm_b32 v44, v54, v50, s62\n\t" \
  "v_perm_b32 v45, v62, v58, s62\n\t" \
  "v_perm_b32 v46, v70, v66, s62\n\t" \
  "v_perm_b32 v47, v78, v74, s62\n\t" \
  "v_accvgpr_write_b32 a176, v44\n\t" \
  "v_accvgpr_write_b32 a177, v45\n\t" \
  "v_accvgpr_write_b32 a178, v46\n\t" \
  "v_accvgpr_write_b32 a179, v47\n\t" \
  "v_perm_b32 v44, v54, v50, s63\n\t" \
  "v_perm_b32 v45, v62, v58, s63\n\t" \
  "v_perm_b32 v46, v70, v66, s63\n\t" \
  "v_perm_b32 v47, v78, v74, s63\n\t" \
  "v_accvgpr_write_b32 a180, v44\n\t" \
  "v_accvgpr_write_b32 a181, v45\n\t" \
  "v_accvgpr_write_b32 a182, v46\n\t" \
  "v_accvgpr_write_b32 a183, v47\n\t" \
  "v_perm_b32 v44, v55, v51, s62\n\t" \
  "v_perm_b32 v45, v63, v59, s62\n\t" \
  "v_perm_b32 v46, v71, v67, s62\n\t" \
  "v_perm_b32 v47, v79, v75, s62\n\t" \
  "v_accvgpr_write_b32 a184, v44\n\t" \
  "v_accvgpr_write_b32 a185, v45\n\t" \
  "v_accvgpr_write_b32 a186, v46\n\t" \
  "v_accvgpr_write_b32 a187, v47\n\t" \
  "v_perm_b32 v44, v55, v51, s63\n\t" \
  "v_perm_b32 v45, v63, v59, s63\n\t" \
  "v_perm_b32 v46, v71, v67, s63\n\t" \
  "v_perm_b32 v47, v79, v75, s63\n\t" \
  "v_accvgpr_write_b32 a188, v44\n\t" \
  "v_accvgpr_write_b32 a189, v45\n\t" \
  "v_accvgpr_write_b32 a190, v46\n\t" \
  "v_accvgpr_write_b32 a191, v47\n\t" \
  "s_xor_b32 s60, s66, 147456\n\t" \
  "s_add_u32 s60, s60, 0\n\t" \
  "s_add_u32 s61, s60, 18432\n\t" \
  "buffer_load_ushort v240, v42, %[W], s60 offen offset:0\n\t" \
  "buffer_load_ushort v160, v42, %[W], s60 offen offset:1152\n\t" \
  "buffer_load_ushort v241, v42, %[W], s60 offen offset:2304\n\t" \
  "buffer_load_ushort v161, v42, %[W], s60 offen offset:3456\n\t" \
  "buffer_load_ushort v242, v42, %[W], s61 offen offset:0\n\t" \
  "buffer_load_ushort v162, v42, %[W], s61 offen offset:1152\n\t" \
  "buffer_load_ushort v243, v42, %[W], s61 offen offset:2304\n\t" \
  "buffer_load_ushort v163, v42, %[W], s61 offen offset:3456\n\t" \
  "s_waitcnt vmcnt(54)\n\t" \
  "v_perm_b32 v44, v84, v80, s62\n\t" \
  "s_waitcnt vmcnt(52)\n\t" \
  "v_perm_b32 v45, v92, v88, s62\n\t" \
  "s_waitcnt vmcnt(50)\n\t" \
  "v_perm_b32 v46, v100, v96, s62\n\t" \
  "s_waitcnt vmcnt(48)\n\t" \
  "v_perm_b32 v47, v108, v104, s62\n\t" \
  "v_accvgpr_write_b32 a192, v44\n\t" \
  "v_accvgpr_write_b32 a193, v45\n\t" \
  "v_accvgpr_write_b32 a194, v46\n\t" \
  "v_accvgpr_write_b32 a195, v47\n\t" \
  "v_perm_b32 v44, v84, v80, s63\n\t" \
  "v_perm_b32 v45, v92, v88, s63\n\t" \
  "v_perm_b32 v46, v100, v96, s63\n\t" \
  "v_perm_b32 v47, v108, v104, s63\n\t" \
  "v_accvgpr_write_b32 a196, v44\n\t" \
  "v_accvgpr_write_b32 a197, v45\n\t" \
  "v_accvgpr_write_b32 a198, v46\n\t" \
  "v_accvgpr_write_b32 a199, v47\n\t" \
  "v_perm_b32 v44, v85, v81, s62\n\t" \
  "v_perm_b32 v45, v93, v89, s62\n\t" \
  "v_perm_b32 v46, v101, v97, s62\n\t" \
  "v_perm_b32 v47, v109, v105, s62\n\t" \
  "v_accvgpr_write_b32 a200, v44\n\t" \
  "v_accvgpr_write_b32 a201, v45\n\t" \
  "v_accvgpr_write_b32 a202, v46\n\t" \
  "v_accvgpr_write_b32 a203, v47\n\t" \
  "v_perm_b32 v44, v85, v81, s63\n\t" \
  "v_perm_b32 v45, v93, v89, s63\n\t" \
  "v_perm_b32 v46, v101, v97, s63\n\t" \
  "v_perm_b32 v47, v109, v105, s63\n\t" \
  "v_accvgpr_write_b32 a204, v44\n\t" \
  "v_accvgpr_write_b32 a205, v45\n\t" \
  "v_accvgpr_write_b32 a206, v46\n\t" \
  "v_accvgpr_write_b32 a207, v47\n\t" \
  "v_perm_b32 v44, v86, v82, s62\n\t" \
  "v_perm_b32 v45, v94, v90, s62\n\t" \
  "v_perm_b32 v46, v102, v98, s62\n\t" \
  "v_perm_b32 v47, v110, v106, s62\n\t" \
  "v_accvgpr_write_b32 a208, v44\n\t" \
  "v_accvgpr_write_b32 a209, v45\n\t" \
  "v_accvgpr_write_b32 a210, v46\n\t" \
  "v_accvgpr_write_b32 a211, v47\n\t" \
  "v_perm_b32 v44, v86, v82, s63\n\t" \
  "v_perm_b32 v45, v94, v90, s63\n\t" \
  "v_perm_b32 v46, v102, v98, s63\n\t" \
  "v_perm_b32 v47, v110, v106, s63\n\t" \
  "v_accvgpr_write_b32 a212, v44\n\t" \
  "v_accvgpr_write_b32 a213, v45\n\t" \
  "v_accvgpr_write_b32 a214, v46\n\t" \
  "v_accvgpr_write_b32 a215, v47\n\t" \
  "v_perm_b32 v44, v87, v83, s62\n\t" \
  "v_perm_b32 v45, v95, v91, s62\n\t" \
  "v_perm_b32 v46, v103, v99, s62\n\t" \
  "v_perm_b32 v47, v111, v107, s62\n\t" \
  "v_accvgpr_write_b32 a216, v44\n\t" \
  "v_accvgpr_write_b32 a217, v45\n\t" \
  "v_accvgpr_write_b32 a218, v46\n\t" \
  "v_accvgpr_write_b32 a219, v47\n\t" \
  "v_perm_b32 v44, v87, v83, s63\n\t" \
  "v_perm_b32 v45, v95, v91, s63\n\t" \
  "v_perm_b32 v46, v103, v99, s63\n\t" \
  "v_perm_b32 v47, v111, v107, s63\n\t" \
  "v_accvgpr_write_b32 a220, v44\n\t" \
  "v_accvgpr_write_b32 a221, v45\n\t" \
  "v_accvgpr_write_b32 a222, v46\n\t" \
  "v_accvgpr_write_b32 a223, v47\n\t" \
  "s_xor_b32 s60, s66, 147456\n\t" \
  "s_add_u32 s60, s60, 36864\n\t" \
  "s_add_u32 s61, s60, 18432\n\t" \
  "buffer_load_ushort v244, v42, %[W], s60 offen offset:0\n\t" \
  "buffer_load_ushort v164, v42, %[W], s60 offen offset:1152\n\t" \
  "buffer_load_ushort v245, v42, %[W], s60 offen offset:2304\n\t" \
  "buffer_load_ushort v165, v42, %[W], s60 offen offset:3456\n\t" \
  "buffer_load_ushort v246, v42, %[W], s61 offen offset:0\n\t" \
  "buffer_load_ushort v166, v42, %[W], s61 offen offset:1152\n\t" \
  "buffer_load_ushort v247, v42, %[W], s61 offen offset:2304\n\t" \
  "buffer_load_ushort v167, v42, %[W], s61 offen offset:3456\n\t" \
  "s_waitcnt vmcnt(54)\n\t" \
  "v_perm_b32 v44, v116, v112, s62\n\t" \
  "s_waitcnt vmcnt(52)\n\t" \
  "v_perm_b32 v45, v124, v120, s62\n\t" \
  "s_waitcnt vmcnt(50)\n\t" \
  "v_perm_b32 v46, v132, v128, s62\n\t" \
  "s_waitcnt vmcnt(48)\n\t" \
  "v_perm_b32 v47, v140, v136, s62\n\t" \
  "v_accvgpr_write_b32 a224, v44\n\t" \
  "v_accvgpr_write_b32 a225, v45\n\t" \
  "v_accvgpr_write_b32 a226, v46\n\t" \
  "v_accvgpr_write_b32 a227, v47\n\t" \
  "v_perm_b32 v44, v116, v112, s63\n\t" \
  "v_perm_b32 v45, v124, v120, s63\n\t" \
  "v_perm_b32 v46, v132, v128, s63\n\t" \
  "v_perm_b32 v47, v140, v136, s63\n\t" \
  "v_accvgpr_write_b32 a228, v44\n\t" \
  "v_accvgpr_write_b32 a229, v45\n\t" \
  "v_accvgpr_write_b32 a230, v46\n\t" \
  "v_accvgpr_write_b32 a231, v47\n\t" \
  "v_perm_b32 v44, v117, v113, s62\n\t" \
  "v_perm_b32 v45, v125, v121, s62\n\t" \
  "v_perm_b32 v46, v133, v129, s62\n\t" \
  "v_perm_b32 v47, v141, v137, s62\n\t" \
  "v_accvgpr_write_b32 a232, v44\n\t" \
  "v_accvgpr_write_b32 a233, v45\n\t" \
  "v_accvgpr_write_b32 a234, v46\n\t" \
  "v_accvgpr_write_b32 a235, v47\n\t" \
  "v_perm_b32 v44, v117, v113, s63\n\t" \
  "v_perm_b32 v45, v125, v121, s63\n\t" \
  "v_perm_b32 v46, v133, v129, s63\n\t" \
  "v_perm_b32 v47, v141, v137, s63\n\t" \
  "v_accvgpr_write_b32 a236, v44\n\t" \
  "v_accvgpr_write_b32 a237, v45\n\t" \
  "v_accvgpr_write_b32 a238, v46\n\t" \
  "v_accvgpr_write_b32 a239, v47\n\t" \
  "v_perm_b32 v44, v118, v114, s62\n\t" \
  "v_perm_b32 v45, v126, v122, s62\n\t" \
  "v_perm_b32 v46, v134, v130, s62\n\t" \
  "v_perm_b32 v47, v142, v138, s62\n\t" \
  "v_accvgpr_write_b32 a240, v44\n\t" \
  "v_accvgpr_write_b32 a241, v45\n\t" \
  "v_accvgpr_write_b32 a242, v46\n\t" \
  "v_accvgpr_write_b32 a243, v47\n\t" \
  "v_perm_b32 v44, v118, v114, s63\n\t" \
  "v_perm_b32 v45, v126, v122, s63\n\t" \
  "v_perm_b32 v46, v134, v130, s63\n\t" \
  "v_perm_b32 v47, v142, v138, s63\n\t" \
  "v_accvgpr_write_b32 a244, v44\n\t" \
  "v_accvgpr_write_b32 a245, v45\n\t" \
  "v_accvgpr_write_b32 a246, v46\n\t" \
  "v_accvgpr_write_b32 a247, v47\n\t" \
  "v_perm_b32 v44, v119, v115, s62\n\t" \
  "v_perm_b32 v45, v127, v123, s62\n\t" \
  "v_perm_b32 v46, v135, v131, s62\n\t" \
  "v_perm_b32 v47, v143, v139, s62\n\t" \
  "v_accvgpr_write_b32 a248, v44\n\t" \
  "v_accvgpr_write_b32 a249, v45\n\t" \
  "v_accvgpr_write_b32 a250, v46\n\t" \
  "v_accvgpr_write_b32 a251, v47\n\t" \
  "v_perm_b32 v44, v119, v115, s63\n\t" \
  "v_perm_b32 v45, v127, v123, s63\n\t" \
  "v_perm_b32 v46, v135, v131, s63\n\t" \
  "v_perm_b32 v47, v143, v139, s63\n\t" \
  "v_accvgpr_write_b32 a252, v44\n\t" \
  "v_accvgpr_write_b32 a253, v45\n\t" \
  "v_accvgpr_write_b32 a254, v46\n\t" \
  "v_accvgpr_write_b32 a255, v47\n\t" \
  "s_xor_b32 s60, s66, 147456\n\t" \
  "s_add_u32 s60, s60, 73728\n\t" \
  "s_add_u32 s61, s60, 18432\n\t" \
  "buffer_load_ushort v248, v42, %[W], s60 offen offset:0\n\t" \
  "buffer_load_ushort v168, v42, %[W], s60 offen offset:1152\n\t" \
  "buffer_load_ushort v249, v42, %[W], s60 offen offset:2304\n\t" \
  "buffer_load_ushort v169, v42, %[W], s60 offen offset:3456\n\t" \
  "buffer_load_ushort v250, v42, %[W], s61 offen offset:0\n\t" \
  "buffer_load_ushort v170, v42, %[W], s61 offen offset:1152\n\t" \
  "buffer_load_ushort v251, v42, %[W], s61 offen offset:2304\n\t" \
  "buffer_load_ushort v171, v42, %[W], s61 offen offset:3456\n\t" \
  "s_xor_b32 s60, s66, 147456\n\t" \
  "s_add_u32 s60, s60, 110592\n\t" \
  "s_add_u32 s61, s60, 18432\n\t" \
  "s_waitcnt vmcnt(40)\n\t" \
  "buffer_load_ushort v252, v42, %[W], s60 offen offset:0\n\t" \
  "buffer_load_ushort v172, v42, %[W], s60 offen offset:1152\n\t" \
  "buffer_load_ushort v253, v42, %[W], s60 offen offset:2304\n\t" \
  "buffer_load_ushort v173, v42, %[W], s60 offen offset:3456\n\t" \
  "buffer_load_ushort v254, v42, %[W], s61 offen offset:0\n\t" \
  "buffer_load_ushort v174, v42, %[W], s61 offen offset:1152\n\t" \
  "buffer_load_ushort v255, v42, %[W], s61 offen offset:2304\n\t" \
  "buffer_load_ushort v175, v42, %[W], s61 offen offset:3456\n\t" \
  "v_lshl_or_b32 v224, v144, 16, v224\n\t" \
  "v_lshl_or_b32 v225, v145, 16, v225\n\t" \
  "v_lshl_or_b32 v226, v146, 16, v226\n\t" \
  "v_lshl_or_b32 v227, v147, 16, v227\n\t" \
  "v_lshl_or_b32 v228, v148, 16, v228\n\t" \
  "v_lshl_or_b32 v229, v149, 16, v229\n\t" \
  "v_lshl_or_b32 v230, v150, 16, v230\n\t" \
  "v_lshl_or_b32 v231, v151, 16, v231\n\t" \
  "s_waitcnt vmcnt(46)\n\t" \
  "v_lshl_or_b32 v232, v152, 16, v232\n\t" \
  "s_waitcnt vmcnt(44)\n\t" \
  "v_lshl_or_b32 v233, v153, 16, v233\n\t" \
  "s_waitcnt vmcnt(42)\n\t" \
  "v_lshl_or_b32 v234, v154, 16, v234\n\t" \
  "s_waitcnt vmcnt(40)\n\t" \
  "v_lshl_or_b32 v235, v155, 16, v235\n\t" \
  "s_waitcnt vmcnt(38)\n\t" \
  "v_lshl_or_b32 v236, v156, 16, v236\n\t" \
  "s_waitcnt vmcnt(36)\n\t" \
  "v_lshl_or_b32 v237, v157, 16, v237\n\t" \
  "s_waitcnt vmcnt(34)\n\t" \
  "v_lshl_or_b32 v238, v158, 16, v238\n\t" \
  "s_waitcnt vmcnt(32)\n\t" \
  "v_lshl_or_b32 v239, v159, 16, v239\n\t" \
  "s_waitcnt vmcnt(30)\n\t" \
  "v_lshl_or_b32 v240, v160, 16, v240\n\t" \
  "s_waitcnt vmcnt(28)\n\t" \
  "v_lshl_or_b32 v241, v161, 16, v241\n\t" \
  "s_waitcnt vmcnt(26)\n\t" \
  "v_lshl_or_b32 v242, v162, 16, v242\n\t" \
  "s_waitcnt vmcnt(24)\n\t" \
  "v_lshl_or_b32 v243, v163, 16, v243\n\t" \
  "s_waitcnt vmcnt(22)\n\t" \
  "v_lshl_or_b32 v244, v164, 16, v244\n\t" \
  "s_waitcnt vmcnt(20)\n\t" \
  "v_lshl_or_b32 v245, v165, 16, v245\n\t" \
  "s_waitcnt vmcnt(18)\n\t" \
  "v_lshl_or_b32 v246, v166, 16, v246\n\t" \
  "s_waitcnt vmcnt(16)\n\t" \
  "v_lshl_or_b32 v247, v167, 16, v247\n\t" \
  "s_waitcnt vmcnt(14)\n\t" \
  "v_lshl_or_b32 v248, v168, 16, v248\n\t" \
  "s_waitcnt vmcnt(12)\n\t" \
  "v_lshl_or_b32 v249, v169, 16, v249\n\t" \
  "s_waitcnt vmcnt(10)\n\t" \
  "v_lshl_or_b32 v250, v170, 16, v250\n\t" \
  "s_waitcnt vmcnt(8)\n\t" \
  "v_lshl_or_b32 v251, v171, 16, v251\n\t" \
  "s_waitcnt vmcnt(6)\n\t" \
  "v_lshl_or_b32 v252, v172, 16, v252\n\t" \
  "s_waitcnt vmcnt(4)\n\t" \
  "v_lshl_or_b32 v253, v173, 16, v253\n\t" \
  "s_waitcnt vmcnt(2)\n\t" \
  "v_lshl_or_b32 v254, v174, 16, v254\n\t" \
  "s_waitcnt vmcnt(0)\n\t" \
  "v_lshl_or_b32 v255, v175, 16, v255\n\t" \
  "s_mov_b32 s84, " TK_CAP "\n\t" \
  "s_mov_b32 s48, 0\n\t" \
  "s_mov_b32 s49, 0xf0000000\n\t" \
  "s_mov_b32 s46, s45\n\t" \
  "v_mov_b32 v5, -1\n\t" \
  "v_lshrrev_b32 v13, 2, v0\n\t" \
  "s_mov_b32 s60, " TK_CTAB "\n\t" \
  "buffer_load_dword v10, v13, %[X], s60 offen offset:88 sc1\n\t" \
  "buffer_load_dword v13, v13, %[X], s60 offen offset:0 sc1\n\t" \
  "s_waitcnt vmcnt(0)\n\t" \
  "v_and_b32 v10, 1, v10\n\t" \
  "v_and_b32 v13, 1, v13\n\t" \
  "v_cmp_ne_u32 s[90:91], s67, v10\n\t" \
  "v_cmp_ne_u32 s[88:89], s67, v13\n\t" \
  "s_nop 1\n\t" \
  "s_mov_b32 s86, s88\n\t" \
  "v_lshrrev_b32 v13, 2, v0\n\t" \
  "s_mov_b32 s60, " TK_CTAB "\n\t" \
  "buffer_load_dword v10, v13, %[X], s60 offen offset:88 sc1\n\t" \
  "buffer_load_dword v13, v13, %[X], s60 offen offset:344 sc1\n\t" \
  "s_waitcnt vmcnt(0)\n\t" \
  "v_and_b32 v10, 1, v10\n\t" \
  "v_and_b32 v13, 1, v13\n\t" \
  "v_cmp_ne_u32 s[90:91], s67, v10\n\t" \
  "v_cmp_ne_u32 s[88:89], s67, v13\n\t" \
  "s_nop 1\n\t" \
  "s_and_b32 s60, s88, 256\n\t" \
  "s_lshl_b32 s60, s60, 5\n\t" \
  "s_add_u32 s44, s44, s60\n\t" \
  "s_add_u32 s83, s48, 0\n\t" \
  "s_lshr_b64 s[60:61], s[90:91], s83\n\t" \
  "s_and_b32 s60, s60, 1\n\t" \
  "s_lshl_b32 s60, s60, 13\n\t" \
  "s_add_u32 s83, s83, " TK_SG0 "\n\t" \
  "s_lshl_b32 s83, s83, 21\n\t" \
  "s_or_b32 s83, s83, s60\n\t" \
  "LCOLD_%=:\n\t" \
  "s_add_u32 s62, s83, s41\n\t" \
  "s_mov_b32 s61, 0\n\t" \
  "LPQ_%=:\n\t" \
  "buffer_load_dword v10, v9, %[X], s62 offen sc1\n\t" \
  "s_waitcnt vmcnt(0)\n\t" \
  "v_cmp_eq_u32 vcc, 0, v10\n\t" \
  "s_cbranch_vccz LPQ_%=x\n\t" \
  "s_add_u32 s61, s61, 1\n\t" \
  "s_sleep 4\n\t" \
  "s_cmp_gt_u32 s61, s84\n\t" \
  "s_cbranch_scc1 LPQ_%=g\n\t" \
  "s_and_b32 s60, s61, 1023\n\t" \
  "s_cmp_lg_u32 s60, 0\n\t" \
  "s_cbranch_scc1 LPQ_%=\n\t" \
  "buffer_load_dword v10, off, %[X], s92 sc1\n\t" \
  "s_waitcnt vmcnt(0)\n\t" \
  "v_readfirstlane_b32 s60, v10\n\t" \
  "s_nop 0\n\t" \
  "s_cmp_eq_u32 s60, 0\n\t" \
  "s_cbranch_scc1 LPQ_%=\n\t" \
  "LPQ_%=g:\n\t" \
  "s_mov_b32 s84, 0\n\t" \
  "s_or_b32 s60, s48, 1073741824\n\t" \
  "v_mov_b32 v10, s60\n\t" \
  "s_mov_b64 exec, 1\n\t" \
  "buffer_store_dword v10, off, %[X], s92 sc1\n\t" \
  "s_mov_b64 exec, -1\n\t" \
  "LPQ_%=x:\n\t" \
  "s_add_u32 m0, s46, 0\n\t" \
  "s_add_u32 s47, s83, s36\n\t" \
  "buffer_load_dwordx4 v0, %[X], s47 offen offset:0 sc1 lds\n\t" \
  "buffer_load_dwordx4 v0, %[X], s47 offen offset:1024 sc1 lds\n\t" \
  "buffer_load_dwordx4 v0, %[X], s47 offen offset:2048 sc1 lds\n\t" \
  "buffer_load_dwordx4 v0, %[X], s47 offen offset:3072 sc1 lds\n\t" \
  "s_add_u32 m0, s46, 4096\n\t" \
  "s_add_u32 s47, s83, s37\n\t" \
  "buffer_load_dwordx4 v0, %[X], s47 offen offset:0 sc1 lds\n\t" \
  "buffer_load_dwordx4 v0, %[X], s47 offen offset:1024 sc1 lds\n\t" \
  "buffer_load_dwordx4 v0, %[X], s47 offen offset:2048 sc1 lds\n\t" \
  "buffer_load_dwordx4 v0, %[X], s47 offen offset:3072 sc1 lds\n\t" \
  "s_add_u32 m0, s46, 8192\n\t" \
  "s_add_u32 s47, s83, s38\n\t" \
  "buffer_load_dwordx4 v0, %[X], s47 offen offset:0 sc1 lds\n\t" \
  "buffer_load_dwordx4 v0, %[X], s47 offen offset:1024 sc1 lds\n\t" \
  "buffer_load_dwordx4 v0, %[X], s47 offen offset:2048 sc1 lds\n\t" \
  "buffer_load_dwordx4 v0, %[X], s47 offen offset:3072 sc1 lds\n\t" \
  "s_waitcnt vmcnt(0)\n\t" \
  "LTOP_%=:\n\t" \
  "s_memrealtime s[78:79]\n\t" \
  "v_readfirstlane_b32 s76, v5\n\t" \
  "s_waitcnt lgkmcnt(0)\n\t" \
  "s_add_u32 s60, s76, 1\n\t" \
  "s_not_b32 s76, s76\n\t" \
  "s_cmp_gt_u32 s60, 1\n\t" \
  "s_cselect_b32 s61, -1, 0\n\t" \
  "s_lshl_b32 s60, s78, 8\n\t" \
  "s_sub_u32 s76, s76, s60\n\t" \
  "s_ashr_i32 s76, s76, 8\n\t" \
  "s_add_i32 s76, s76, " TK_AX "\n\t" \
  "s_min_i32 s76, s76, 400\n\t" \
  "s_and_b32 s76, s76, s61\n\t" \
  "s_add_u32 s57, s78, s76\n\t" \
  "s_barrier\n\t" \
  "ds_read_b128 v[48:51], v2 offset:0\n\t" \
  "ds_read_b128 v[64:67], v1 offset:8192\n\t" \
  "ds_read_b128 v[80:83], v1 offset:12288\n\t" \
  "ds_read_b128 v[52:55], v2 offset:1024\n\t" \
  "ds_read_b128 v[68:71], v1 offset:9216\n\t" \
  "ds_read_b128 v[84:87], v1 offset:13312\n\t" \
  "ds_read_b128 v[56:59], v2 offset:2048\n\t" \
  "ds_read_b128 v[72:75], v1 offset:10240\n\t" \
  "ds_read_b128 v[88:91], v1 offset:14336\n\t" \
  "ds_read_b128 v[60:63], v2 offset:3072\n\t" \
  "ds_read_b128 v[76:79], v1 offset:11264\n\t" \
  "ds_read_b128 v[92:95], v1 offset:15360\n\t" \
  "s_mov_b32 s82, s83\n\t" \
  "s_xor_b32 s46, s46, 0x10000\n\t" \
  "s_add_u32 s58, s82, s41\n\t" \
  "s_add_u32 s58, s58, 4096\n\t" \
  "s_add_u32 s59, s82, s39\n\t" \
  "s_cmp_gt_i32 s76, 0\n\t" \
  "s_cselect_b32 s62, 0xf0000000, s58\n\t" \
  "buffer_load_dword v5, off, %[X], s62 sc1\n\t" \
  "s_waitcnt lgkmcnt(10)\n\t" \
  "v_mfma_f32_16x16x32_bf16 v[36:39], v[64:67], v[48:51], v[16:19]\n\t" \
  "ds_read_b128 v[96:99], v1 offset:16384\n\t" \
  "s_waitcnt lgkmcnt(10)\n\t" \
  "v_mfma_f32_16x16x32_bf16 v[40:43], v[80:83], v[48:51], v[20:23]\n\t" \
  "ds_read_b128 v[100:103], v1 offset:17408\n\t" \
  "s_waitcnt lgkmcnt(9)\n\t" \
  "v_mfma_f32_16x16x32_bf16 v[36:39], v[68:71], v[52:55], v[36:39]\n\t" \
  "ds_read_b128 v[104:107], v1 offset:18432\n\t" \
  "s_waitcnt lgkmcnt(9)\n\t" \
  "v_mfma_f32_16x16x32_bf16 v[40:43], v[84:87], v[52:55], v[40:43]\n\t" \
  "ds_read_b128 v[108:111], v1 offset:19456\n\t" \
  "s_waitcnt lgkmcnt(8)\n\t" \
  "v_mfma_f32_16x16x32_bf16 v[36:39], v[72:75], v[56:59], v[36:39]\n\t" \
  "ds_read_b128 v[112:115], v1 offset:20480\n\t" \
  "s_waitcnt lgkmcnt(8)\n\t" \
  "v_mfma_f32_16x16x32_bf16 v[40:43], v[88:91], v[56:59], v[40:43]\n\t" \
  "ds_read_b128 v[116:119], v1 offset:21504\n\t" \
  "s_waitcnt lgkmcnt(7)\n\t" \
  "v_mfma_f32_16x16x32_bf16 v[36:39], v[76:79], v[60:63], v[36:39]\n\t" \
  "ds_read_b128 v[120:123], v1 offset:22528\n\t" \
  "s_waitcnt lgkmcnt(7)\n\t" \
  "v_mfma_f32_16x16x32_bf16 v[40:43], v[92:95], v[60:63], v[40:43]\n\t" \
  "ds_read_b128 v[124:127], v1 offset:23552\n\t" \
  "s_add_u32 s83, s48, 1\n\t" \
  "s_lshr_b64 s[60:61], s[90:91], s83\n\t" \
  "s_and_b32 s60, s60, 1\n\t" \
  "s_lshl_b32 s60, s60, 13\n\t" \
  "s_add_u32 s83, s83, " TK_SG0 "\n\t" \
  "s_lshl_b32 s83, s83, 21\n\t" \
  "s_or_b32 s83, s83, s60\n\t" \
  "v_xor_b32 v1, 0x10000, v1\n\t" \
  "v_xor_b32 v2, 0x10000, v2\n\t" \
  "v_max3_f32 v11, v36, v37, v38\n\t" \
  "v_max3_f32 v11, v11, v39, v40\n\t" \
  "v_max3_f32 v13, v41, v42, v43\n\t" \
  "v_max_f32 v11, v11, v13\n\t" \
  "v_mov_b32 v13, v11\n\t" \
  "s_nop 1\n\t" \
  "v_permlane16_swap_b32 v11, v13\n\t" \
  "v_max_f32 v11, v11, v13\n\t" \
  "v_mov_b32 v13, v11\n\t" \
  "s_nop 1\n\t" \
  "v_permlane32_swap_b32 v11, v13\n\t" \
  "v_max_f32 v11, v11, v13\n\t" \
  "v_mul_f32 v13, 0xbe0293ee, v11\n\t" \
  "v_fmamk_f32 v36, v36, 0x3e0293ee, v13\n\t" \
  "v_fmamk_f32 v37, v37, 0x3e0293ee, v13\n\t" \
  "v_fmamk_f32 v38, v38, 0x3e0293ee, v13\n\t" \
  "v_fmamk_f32 v39, v39, 0x3e0293ee, v13\n\t" \
  "v_fmamk_f32 v40, v40, 0x3e0293ee, v13\n\t" \
  "v_fmamk_f32 v41, v41, 0x3e0293ee, v13\n\t" \
  "v_fmamk_f32 v42, v42, 0x3e0293ee, v13\n\t" \
  "v_fmamk_f32 v43, v43, 0x3e0293ee, v13\n\t" \
  "s_waitcnt vmcnt(0)\n\t" \
  "v_readfirstlane_b32 s76, v5\n\t" \
  "s_barrier\n\t" \
  "s_cmp_eq_u32 s76, 0\n\t" \
  "s_cbranch_scc1 LNG_%=\n\t" \
  "buffer_load_dwordx4 v[196:199], v0, %[X], s59 offen offset:0 sc1\n\t" \
  "buffer_load_dwordx4 v[200:203], v0, %[X], s59 offen offset:1024 sc1\n\t" \
  "buffer_load_dwordx2 v[204:205], v6, %[X], s59 offen offset:2048 sc1\n\t" \
  "buffer_load_dwordx2 v[206:207], v6, %[X], s59 offen offset:2560 sc1\n\t" \
  "buffer_load_dwordx2 v[208:209], v6, %[X], s59 offen offset:3072 sc1\n\t" \
  "buffer_load_dwordx2 v[210:211], v6, %[X], s59 offen offset:3584 sc1\n\t" \
  "LNG_%=:\n\t" \
  "s_mov_b64 exec, 1\n\t" \
  "buffer_store_dword v3, off, %[X], s49 sc1\n\t" \
  "s_mov_b64 exec, -1\n\t" \
  "s_mov_b32 s49, 0xf0000000\n\t" \
  "v_exp_f32 v36, v36\n\t" \
  "v_exp_f32 v37, v37\n\t" \
  "v_exp_f32 v38, v38\n\t" \
  "v_exp_f32 v39, v39\n\t" \
  "v_exp_f32 v40, v40\n\t" \
  "v_exp_f32 v41, v41\n\t" \
  "v_exp_f32 v42, v42\n\t" \
  "v_exp_f32 v43, v43\n\t" \
  "v_cvt_pk_bf16_f32 v44, v36, v37\n\t" \
  "v_cvt_pk_bf16_f32 v45, v38, v39\n\t" \
  "v_cvt_pk_bf16_f32 v46, v40, v41\n\t" \
  "v_cvt_pk_bf16_f32 v47, v42, v43\n\t" \
  "v_add_f32 v36, v36, v37\n\t" \
  "v_add_f32 v38, v38, v39\n\t" \
  "s_waitcnt lgkmcnt(7)\n\t" \
  "v_mfma_f32_16x16x32_bf16 v[128:131], v[96:99], v[44:47], 0\n\t" \
  "v_add_f32 v40, v40, v41\n\t" \
  "s_and_b32 s62, s48, 15\n\t" \
  "s_lshr_b32 s63, s62, 1\n\t" \
  "s_waitcnt lgkmcnt(6)\n\t" \
  "v_mfma_f32_16x16x32_bf16 v[132:135], v[100:103], v[44:47], 0\n\t" \
  "v_add_f32 v42, v42, v43\n\t" \
  "s_lshr_b32 s60, s88, s63\n\t" \
  "s_and_b32 s60, s60, 1\n\t" \
  "s_waitcnt lgkmcnt(5)\n\t" \
  "v_mfma_f32_16x16x32_bf16 v[136:139], v[104:107], v[44:47], 0\n\t" \
  "v_add_f32 v36, v36, v38\n\t" \
  "s_lshl_b32 s60, s60, 13\n\t" \
  "s_lshl_b32 s61, s62, 20\n\t" \
  "s_waitcnt lgkmcnt(4)\n\t" \
  "v_mfma_f32_16x16x32_bf16 v[140:143], v[108:111], v[44:47], 0\n\t" \
  "v_add_f32 v40, v40, v42\n\t" \
  "s_add_u32 s50, s61, s60\n\t" \
  "s_add_u32 s50, s50, s43\n\t" \
  "s_waitcnt lgkmcnt(3)\n\t" \
  "v_mfma_f32_16x16x32_bf16 v[144:147], v[112:115], v[44:47], 0\n\t" \
  "v_add_f32 v12, v36, v40\n\t" \
  "s_add_u32 s51, s50, 32768\n\t" \
  "s_add_u32 s52, s51, 32768\n\t" \
  "s_waitcnt lgkmcnt(2)\n\t" \
  "v_mfma_f32_16x16x32_bf16 v[148:151], v[116:119], v[44:47], 0\n\t" \
  "s_add_u32 s53, s52, 32768\n\t" \
  "s_lshl_b32 s61, s62, 17\n\t" \
  "s_waitcnt lgkmcnt(1)\n\t" \
  "v_mfma_f32_16x16x32_bf16 v[152:155], v[120:123], v[44:47], 0\n\t" \
  "s_add_u32 s54, s61, s44\n\t" \
  "s_add_u32 s63, s83, s41\n\t" \
  "s_waitcnt lgkmcnt(0)\n\t" \
  "v_mfma_f32_16x16x32_bf16 v[156:159], v[124:127], v[44:47], 0\n\t" \
  "s_cmp_lg_u32 s76, 0\n\t" \
  "s_cbranch_scc1 LGOT_%=\n\t" \
  "LKW_%=:\n\t" \
  "s_memrealtime s[78:79]\n\t" \
  "s_waitcnt lgkmcnt(0)\n\t" \
  "s_sub_i32 s79, s78, s57\n\t" \
  "s_cmp_lt_i32 s79, 0\n\t" \
  "s_cbranch_scc1 LKW_%=\n\t" \
  "s_mov_b32 s61, 0\n\t" \
  "LPK_%=:\n\t" \
  "buffer_load_dword v5, off, %[X], s58 sc1\n\t" \
  "s_waitcnt vmcnt(0)\n\t" \
  "v_cmp_eq_u32 vcc, 0, v5\n\t" \
  "s_cbranch_vccz LPK_%=x\n\t" \
  "s_add_u32 s61, s61, 1\n\t" \
  "s_sleep " TK_PSK "\n\t" \
  "s_cmp_gt_u32 s61, s84\n\t" \
  "s_cbranch_scc1 LPK_%=g\n\t" \
  "s_and_b32 s60, s61, 1023\n\t" \
  "s_cmp_lg_u32 s60, 0\n\t" \
  "s_cbranch_scc1 LPK_%=\n\t" \
  "buffer_load_dword v5, off, %[X], s92 sc1\n\t" \
  "s_waitcnt vmcnt(0)\n\t" \
  "v_readfirstlane_b32 s60, v5\n\t" \
  "s_nop 0\n\t" \
  "s_cmp_eq_u32 s60, 0\n\t" \
  "s_cbranch_scc1 LPK_%=\n\t" \
  "LPK_%=g:\n\t" \
  "s_mov_b32 s84, 0\n\t" \
  "s_or_b32 s60, s48, 1342177280\n\t" \
  "v_mov_b32 v5, s60\n\t" \
  "s_mov_b64 exec, 1\n\t" \
  "buffer_store_dword v5, off, %[X], s92 sc1\n\t" \
  "s_mov_b64 exec, -1\n\t" \
  "LPK_%=x:\n\t" \
  "buffer_load_dwordx4 v[196:199], v0, %[X], s59 offen offset:0 sc1\n\t" \
  "buffer_load_dwordx4 v[200:203], v0, %[X], s59 offen offset:1024 sc1\n\t" \
  "buffer_load_dwordx2 v[204:205], v6, %[X], s59 offen offset:2048 sc1\n\t" \
  "buffer_load_dwordx2 v[206:207], v6, %[X], s59 offen offset:2560 sc1\n\t" \
  "buffer_load_dwordx2 v[208:209], v6, %[X], s59 offen offset:3072 sc1\n\t" \
  "buffer_load_dwordx2 v[210:211], v6, %[X], s59 offen offset:3584 sc1\n\t" \
  "LGOT_%=:\n\t" \
  "buffer_load_dword v10, v9, %[X], s63 offen sc1\n\t" \
  "s_add_u32 s49, s82, s42\n\t" \
  "s_waitcnt vmcnt(6)\n\t" \
  "v_mfma_f32_16x16x32_bf16 v[36:39], v[196:199], v[48:51], 0\n\t" \
  "v_mfma_f32_16x16x32_bf16 v[40:43], v[196:199], v[52:55], 0\n\t" \
  "s_waitcnt vmcnt(5)\n\t" \
  "v_mfma_f32_16x16x32_bf16 v[36:39], v[200:203], v[56:59], v[36:39]\n\t" \
  "v_mfma_f32_16x16x32_bf16 v[40:43], v[200:203], v[60:63], v[40:43]\n\t" \
  "s_nop 8\n\t" \
  "v_add_f32 v36, v36, v42\n\t" \
  "v_add_f32 v37, v37, v43\n\t" \
  "v_max3_f32 v14, v36, v37, v11\n\t" \
  "v_mov_b32 v13, v14\n\t" \
  "s_nop 1\n\t" \
  "v_permlane16_swap_b32 v14, v13\n\t" \
  "v_max_f32 v14, v14, v13\n\t" \
  "v_mov_b32 v13, v14\n\t" \
  "s_nop 1\n\t" \
  "v_permlane32_swap_b32 v14, v13\n\t" \
  "v_max_f32 v14, v14, v13\n\t" \
  "v_mul_f32 v14, 0xbe0293ee, v14\n\t" \
  "v_fmamk_f32 v11, v11, 0x3e0293ee, v14\n\t" \
  "v_fmamk_f32 v36, v36, 0x3e0293ee, v14\n\t" \
  "v_fmamk_f32 v37, v37, 0x3e0293ee, v14\n\t" \
  "v_exp_f32 v11, v11\n\t" \
  "v_exp_f32 v36, v36\n\t" \
  "v_exp_f32 v37, v37\n\t" \
  "v_fma_f32 v12, v12, v11, v36\n\t" \
  "v_add_f32 v12, v12, v37\n\t" \
  "v_mov_b32 v13, v12\n\t" \
  "s_nop 1\n\t" \
  "v_permlane16_swap_b32 v12, v13\n\t" \
  "v_add_f32 v12, v12, v13\n\t" \
  "v_mov_b32 v13, v12\n\t" \
  "s_nop 1\n\t" \
  "v_permlane32_swap_b32 v12, v13\n\t" \
  "v_add_f32 v12, v12, v13\n\t" \
  "v_rcp_f32 v12, v12\n\t" \
  "s_nop 0\n\t" \
  "v_mul_f32 v14, v11, v12\n\t" \
  "v_mul_f32 v36, v36, v12\n\t" \
  "v_mul_f32 v37, v37, v12\n\t" \
  "v_cvt_pk_bf16_f32 v26, v36, v37\n\t" \
  "v_cvt_pk_bf16_f32 v33, v36, v37\n\t" \
  "v_pk_mul_f32 v[128:129], v[128:129], v[14:15] op_sel_hi:[1,0]\n\t" \
  "v_pk_mul_f32 v[130:131], v[130:131], v[14:15] op_sel_hi:[1,0]\n\t" \
  "v_pk_mul_f32 v[132:133], v[132:133], v[14:15] op_sel_hi:[1,0]\n\t" \
  "v_pk_mul_f32 v[134:135], v[134:135], v[14:15] op_sel_hi:[1,0]\n\t" \
  "v_pk_mul_f32 v[136:137], v[136:137], v[14:15] op_sel_hi:[1,0]\n\t" \
  "v_pk_mul_f32 v[138:139], v[138:139], v[14:15] op_sel_hi:[1,0]\n\t" \
  "v_pk_mul_f32 v[140:141], v[140:141], v[14:15] op_sel_hi:[1,0]\n\t" \
  "v_pk_mul_f32 v[142:143], v[142:143], v[14:15] op_sel_hi:[1,0]\n\t" \
  "s_waitcnt vmcnt(3)\n\t" \
  "v_mfma_f32_16x16x32_bf16 v[128:131], v[204:207], v[26:29], v[128:131]\n\t" \
  "v_mul_f32 v144, v144, v14\n\t" \
  "v_mul_f32 v145, v145, v14\n\t" \
  "v_mfma_f32_16x16x32_bf16 v[132:135], v[204:207], v[32:35], v[132:135]\n\t" \
  "v_mul_f32 v146, v146, v14\n\t" \
  "v_mul_f32 v147, v147, v14\n\t" \
  "v_mfma_f32_16x16x32_bf16 v[136:139], v[204:207], v[24:27], v[136:139]\n\t" \
  "v_mul_f32 v148, v148, v14\n\t" \
  "v_mul_f32 v149, v149, v14\n\t" \
  "v_mfma_f32_16x16x32_bf16 v[140:143], v[204:207], v[30:33], v[140:143]\n\t" \
  "v_cvt_pk_bf16_f32 v64, v128, v129\n\t" \
  "v_cvt_pk_bf16_f32 v65, v130, v131\n\t" \
  "v_cvt_pk_bf16_f32 v66, v132, v133\n\t" \
  "v_cvt_pk_bf16_f32 v67, v134, v135\n\t" \
  "v_mul_f32 v150, v150, v14\n\t" \
  "v_mul_f32 v151, v151, v14\n\t" \
  "v_mfma_f32_16x16x32_bf16 v[192:195], v[224:227], v[64:67], 0\n\t" \
  "ds_write_b128 v3, v[64:67] offset:0\n\t" \
  "v_cvt_pk_bf16_f32 v68, v136, v137\n\t" \
  "v_cvt_pk_bf16_f32 v69, v138, v139\n\t" \
  "v_mfma_f32_16x16x32_bf16 v[160:163], v[64:67], a[0:3], 0\n\t" \
  "v_cvt_pk_bf16_f32 v70, v140, v141\n\t" \
  "v_cvt_pk_bf16_f32 v71, v142, v143\n\t" \
  "s_waitcnt vmcnt(1)\n\t" \
  "v_mfma_f32_16x16x32_bf16 v[144:147], v[208:211], v[26:29], v[144:147]\n\t" \
  "v_mul_f32 v152, v152, v14\n\t" \
  "v_mul_f32 v153, v153, v14\n\t" \
  "v_mfma_f32_16x16x32_bf16 v[148:151], v[208:211], v[32:35], v[148:151]\n\t" \
  "v_mul_f32 v154, v154, v14\n\t" \
  "v_mul_f32 v155, v155, v14\n\t" \
  "v_mfma_f32_16x16x32_bf16 v[164:167], v[64:67], a[4:7], 0\n\t" \
  "ds_write_b128 v3, v[68:71] offset:1024\n\t" \
  "v_mul_f32 v156, v156, v14\n\t" \
  "v_mul_f32 v157, v157, v14\n\t" \
  "v_mfma_f32_16x16x32_bf16 v[168:171], v[64:67], a[8:11], 0\n\t" \
  "v_mul_f32 v158, v158, v14\n\t" \
  "v_mul_f32 v159, v159, v14\n\t" \
  "v_mfma_f32_16x16x32_bf16 v[172:175], v[64:67], a[12:15], 0\n\t" \
  "v_cvt_pk_bf16_f32 v72, v144, v145\n\t" \
  "v_cvt_pk_bf16_f32 v73, v146, v147\n\t" \
  "v_mfma_f32_16x16x32_bf16 v[152:155], v[208:211], v[24:27], v[152:155]\n\t" \
  "v_cvt_pk_bf16_f32 v74, v148, v149\n\t" \
  "v_cvt_pk_bf16_f32 v75, v150, v151\n\t" \
  "v_mfma_f32_16x16x32_bf16 v[156:159], v[208:211], v[30:33], v[156:159]\n\t" \
  "v_mfma_f32_16x16x32_bf16 v[176:179], v[64:67], a[16:19], 0\n\t" \
  "ds_write_b128 v3, v[72:75] offset:2048\n\t" \
  "v_mfma_f32_16x16x32_bf16 v[180:183], v[64:67], a[20:23], 0\n\t" \
  "v_mfma_f32_16x16x32_bf16 v[184:187], v[64:67], a[24:27], 0\n\t" \
  "v_cvt_pk_bf16_f32 v76, v152, v153\n\t" \
  "v_cvt_pk_bf16_f32 v77, v154, v155\n\t" \
  "v_mfma_f32_16x16x32_bf16 v[188:191], v[64:67], a[28:31], 0\n\t" \
  "v_cvt_pk_bf16_f32 v78, v156, v157\n\t" \
  "v_cvt_pk_bf16_f32 v79, v158, v159\n\t" \
  "v_mfma_f32_16x16x32_bf16 v[192:195], v[228:231], v[68:71], v[192:195]\n\t" \
  "ds_write_b128 v3, v[76:79] offset:3072\n\t" \
  "v_mfma_f32_16x16x32_bf16 v[160:163], v[68:71], a[32:35], v[160:163]\n\t" \
  "s_waitcnt vmcnt(0)\n\t" \
  "v_cmp_eq_u32 vcc, 0, v10\n\t" \
  "v_mfma_f32_16x16x32_bf16 v[164:167], v[68:71], a[36:39], v[164:167]\n\t" \
  "s_cmp_eq_u64 vcc, 0\n\t" \
  "s_cselect_b32 s55, s83, 0xf0000000\n\t" \
  "s_cmp_eq_u32 s48, 63\n\t" \
  "v_mfma_f32_16x16x32_bf16 v[168:171], v[68:71], a[40:43], v[168:171]\n\t" \
  "s_cselect_b32 s55, 0xf0000000, s55\n\t" \
  "s_mov_b32 s56, s83\n\t" \
  "s_mov_b32 s83, s55\n\t" \
  "v_mfma_f32_16x16x32_bf16 v[172:175], v[68:71], a[44:47], v[172:175]\n\t" \
  "v_mfma_f32_16x16x32_bf16 v[176:179], v[68:71], a[48:51], v[176:179]\n\t" \
  "s_add_u32 m0, s46, 0\n\t" \
  "s_add_u32 s47, s83, s36\n\t" \
  "buffer_load_dwordx4 v0, %[X], s47 offen offset:0 sc1 lds\n\t" \
  "v_mfma_f32_16x16x32_bf16 v[180:183], v[68:71], a[52:55], v[180:183]\n\t" \
  "v_mfma_f32_16x16x32_bf16 v[184:187], v[68:71], a[56:59], v[184:187]\n\t" \
  "s_waitcnt lgkmcnt(0)\n\t" \
  "s_barrier\n\t" \
  "v_mfma_f32_16x16x32_bf16 v[188:191], v[68:71], a[60:63], v[188:191]\n\t" \
  "ds_read_b128 v[80:83], v4 offset:0\n\t" \
  "v_mfma_f32_16x16x32_bf16 v[192:195], v[232:235], v[72:75], v[192:195]\n\t" \
  "buffer_load_dwordx4 v0, %[X], s47 offen offset:1024 sc1 lds\n\t" \
  "v_mfma_f32_16x16x32_bf16 v[160:163], v[72:75], a[64:67], v[160:163]\n\t" \
  "ds_read_b128 v[84:87], v4 offset:1024\n\t" \
  "v_mfma_f32_16x16x32_bf16 v[164:167], v[72:75], a[68:71], v[164:167]\n\t" \
  "v_mfma_f32_16x16x32_bf16 v[168:171], v[72:75], a[72:75], v[168:171]\n\t" \
  "ds_read_b128 v[88:91], v4 offset:2048\n\t" \
  "v_mfma_f32_16x16x32_bf16 v[172:175], v[72:75], a[76:79], v[172:175]\n\t" \
  "buffer_load_dwordx4 v0, %[X], s47 offen offset:2048 sc1 lds\n\t" \
  "v_mfma_f32_16x16x32_bf16 v[176:179], v[72:75], a[80:83], v[176:179]\n\t" \
  "ds_read_b128 v[92:95], v4 offset:3072\n\t" \
  "v_mfma_f32_16x16x32_bf16 v[180:183], v[72:75], a[84:87], v[180:183]\n\t" \
  "v_mfma_f32_16x16x32_bf16 v[184:187], v[72:75], a[88:91], v[184:187]\n\t" \
  "v_mfma_f32_16x16x32_bf16 v[188:191], v[72:75], a[92:95], v[188:191]\n\t" \
  "buffer_load_dwordx4 v0, %[X], s47 offen offset:3072 sc1 lds\n\t" \
  "v_mfma_f32_16x16x32_bf16 v[192:195], v[236:239], v[76:79], v[192:195]\n\t" \
  "s_add_u32 s62, s48, 2\n\t" \
  "v_mfma_f32_16x16x32_bf16 v[160:163], v[76:79], a[96:99], v[160:163]\n\t" \
  "v_mfma_f32_16x16x32_bf16 v[164:167], v[76:79], a[100:103], v[164:167]\n\t" \
  "v_mfma_f32_16x16x32_bf16 v[168:171], v[76:79], a[104:107], v[168:171]\n\t" \
  "s_add_u32 m0, s46, 4096\n\t" \
  "s_add_u32 s47, s83, s37\n\t" \
  "buffer_load_dwordx4 v0, %[X], s47 offen offset:0 sc1 lds\n\t" \
  "v_mfma_f32_16x16x32_bf16 v[172:175], v[76:79], a[108:111], v[172:175]\n\t" \
  "s_mul_i32 s63, s62, 43\n\t" \
  "s_lshr_b32 s63, s63, 7\n\t" \
  "v_mfma_f32_16x16x32_bf16 v[176:179], v[76:79], a[112:115], v[176:179]\n\t" \
  "v_mfma_f32_16x16x32_bf16 v[180:183], v[76:79], a[116:119], v[180:183]\n\t" \
  "v_mfma_f32_16x16x32_bf16 v[184:187], v[76:79], a[120:123], v[184:187]\n\t" \
  "buffer_load_dwordx4 v0, %[X], s47 offen offset:1024 sc1 lds\n\t" \
  "v_mfma_f32_16x16x32_bf16 v[188:191], v[76:79], a[124:127], v[188:191]\n\t" \
  "s_mul_i32 s62, s62, 688128\n\t" \
  "s_lshl_b32 s60, s63, 15\n\t" \
  "s_waitcnt lgkmcnt(3)\n\t" \
  "v_mfma_f32_16x16x32_bf16 v[192:195], v[240:243], v[80:83], v[192:195]\n\t" \
  "v_mfma_f32_16x16x32_bf16 v[160:163], v[80:83], a[128:131], v[160:163]\n\t" \
  "v_mfma_f32_16x16x32_bf16 v[164:167], v[80:83], a[132:135], v[164:167]\n\t" \
  "buffer_load_dwordx4 v0, %[X], s47 offen offset:2048 sc1 lds\n\t" \
  "v_mfma_f32_16x16x32_bf16 v[168:171], v[80:83], a[136:139], v[168:171]\n\t" \
  "s_add_u32 s62, s62, s60\n\t" \
  "s_lshr_b32 s60, s86, s63\n\t" \
  "v_mfma_f32_16x16x32_bf16 v[172:175], v[80:83], a[140:143], v[172:175]\n\t" \
  "v_mfma_f32_16x16x32_bf16 v[176:179], v[80:83], a[144:147], v[176:179]\n\t" \
  "v_mfma_f32_16x16x32_bf16 v[180:183], v[80:83], a[148:151], v[180:183]\n\t" \
  "buffer_load_dwordx4 v0, %[X], s47 offen offset:3072 sc1 lds\n\t" \
  "v_mfma_f32_16x16x32_bf16 v[184:187], v[80:83], a[152:155], v[184:187]\n\t" \
  "s_and_b32 s60, s60, 1\n\t" \
  "s_lshl_b32 s60, s60, 13\n\t" \
  "v_mfma_f32_16x16x32_bf16 v[188:191], v[80:83], a[156:159], v[188:191]\n\t" \
  "s_waitcnt lgkmcnt(2)\n\t" \
  "v_mfma_f32_16x16x32_bf16 v[192:195], v[244:247], v[84:87], v[192:195]\n\t" \
  "v_mfma_f32_16x16x32_bf16 v[160:163], v[84:87], a[160:163], v[160:163]\n\t" \
  "s_add_u32 m0, s46, 8192\n\t" \
  "s_add_u32 s47, s83, s38\n\t" \
  "buffer_load_dwordx4 v0, %[X], s47 offen offset:0 sc1 lds\n\t" \
  "v_mfma_f32_16x16x32_bf16 v[164:167], v[84:87], a[164:167], v[164:167]\n\t" \
  "s_add_u32 s62, s62, s60\n\t" \
  "s_add_u32 s62, s62, 656896\n\t" \
  "v_mfma_f32_16x16x32_bf16 v[168:171], v[84:87], a[168:171], v[168:171]\n\t" \
  "v_mfma_f32_16x16x32_bf16 v[172:175], v[84:87], a[172:175], v[172:175]\n\t" \
  "v_mfma_f32_16x16x32_bf16 v[176:179], v[84:87], a[176:179], v[176:179]\n\t" \
  "buffer_load_dwordx4 v0, %[X], s47 offen offset:1024 sc1 lds\n\t" \
  "v_mfma_f32_16x16x32_bf16 v[180:183], v[84:87], a[180:183], v[180:183]\n\t" \
  "s_cmp_gt_u32 s48, 61\n\t" \
  "s_cselect_b32 s62, 0xf0000000, s62\n\t" \
  "v_mfma_f32_16x16x32_bf16 v[184:187], v[84:87], a[184:187], v[184:187]\n\t" \
  "v_mfma_f32_16x16x32_bf16 v[188:191], v[84:87], a[188:191], v[188:191]\n\t" \
  "s_waitcnt lgkmcnt(1)\n\t" \
  "v_mfma_f32_16x16x32_bf16 v[192:195], v[248:251], v[88:91], v[192:195]\n\t" \
  "buffer_load_dwordx4 v0, %[X], s47 offen offset:2048 sc1 lds\n\t" \
  "v_mfma_f32_16x16x32_bf16 v[160:163], v[88:91], a[192:195], v[160:163]\n\t" \
  "v_mfma_f32_16x16x32_bf16 v[164:167], v[88:91], a[196:199], v[164:167]\n\t" \
  "v_mfma_f32_16x16x32_bf16 v[168:171], v[88:91], a[200:203], v[168:171]\n\t" \
  "v_mfma_f32_16x16x32_bf16 v[172:175], v[88:91], a[204:207], v[172:175]\n\t" \
  "buffer_load_dwordx4 v0, %[X], s47 offen offset:3072 sc1 lds\n\t" \
  "v_mfma_f32_16x16x32_bf16 v[176:179], v[88:91], a[208:211], v[176:179]\n\t" \
  "v_mfma_f32_16x16x32_bf16 v[180:183], v[88:91], a[212:215], v[180:183]\n\t" \
  "v_mfma_f32_16x16x32_bf16 v[184:187], v[88:91], a[216:219], v[184:187]\n\t" \
  "v_mfma_f32_16x16x32_bf16 v[188:191], v[88:91], a[220:223], v[188:191]\n\t" \
  "buffer_load_dword v5, off, %[X], s62 sc1\n\t" \
  "s_waitcnt lgkmcnt(0)\n\t" \
  "v_mfma_f32_16x16x32_bf16 v[192:195], v[252:255], v[92:95], v[192:195]\n\t" \
  "v_mfma_f32_16x16x32_bf16 v[160:163], v[92:95], a[224:227], v[160:163]\n\t" \
  "v_mfma_f32_16x16x32_bf16 v[164:167], v[92:95], a[228:231], v[164:167]\n\t" \
  "v_mfma_f32_16x16x32_bf16 v[168:171], v[92:95], a[232:235], v[168:171]\n\t" \
  "v_cvt_pk_bf16_f32 v112, v192, v193\n\t" \
  "v_cvt_pk_bf16_f32 v113, v194, v195\n\t" \
  "v_mfma_f32_16x16x32_bf16 v[172:175], v[92:95], a[236:239], v[172:175]\n\t" \
  "buffer_store_dwordx2 v[112:113], v8, %[X], s54 offen sc1\n\t" \
  "v_mfma_f32_16x16x32_bf16 v[176:179], v[92:95], a[240:243], v[176:179]\n\t" \
  "v_cvt_pk_bf16_f32 v96, v160, v164\n\t" \
  "v_cvt_pk_bf16_f32 v100, v161, v165\n\t" \
  "v_mfma_f32_16x16x32_bf16 v[180:183], v[92:95], a[244:247], v[180:183]\n\t" \
  "v_cvt_pk_bf16_f32 v104, v162, v166\n\t" \
  "v_cvt_pk_bf16_f32 v108, v163, v167\n\t" \
  "v_mfma_f32_16x16x32_bf16 v[184:187], v[92:95], a[248:251], v[184:187]\n\t" \
  "v_cvt_pk_bf16_f32 v97, v168, v172\n\t" \
  "v_cvt_pk_bf16_f32 v101, v169, v173\n\t" \
  "v_mfma_f32_16x16x32_bf16 v[188:191], v[92:95], a[252:255], v[188:191]\n\t" \
  "v_cvt_pk_bf16_f32 v105, v170, v174\n\t" \
  "v_cvt_pk_bf16_f32 v109, v171, v175\n\t" \
  "v_cvt_pk_bf16_f32 v98, v176, v180\n\t" \
  "v_cvt_pk_bf16_f32 v102, v177, v181\n\t" \
  "v_cvt_pk_bf16_f32 v106, v178, v182\n\t" \
  "v_cvt_pk_bf16_f32 v110, v179, v183\n\t" \
  "s_nop 1\n\t" \
  "v_cvt_pk_bf16_f32 v99, v184, v188\n\t" \
  "buffer_store_dwordx4 v[96:99], v7, %[X], s50 offen sc1\n\t" \
  "v_cvt_pk_bf16_f32 v103, v185, v189\n\t" \
  "buffer_store_dwordx4 v[100:103], v7, %[X], s51 offen sc1\n\t" \
  "v_cvt_pk_bf16_f32 v107, v186, v190\n\t" \
  "buffer_store_dwordx4 v[104:107], v7, %[X], s52 offen sc1\n\t" \
  "v_cvt_pk_bf16_f32 v111, v187, v191\n\t" \
  "buffer_store_dwordx4 v[108:111], v7, %[X], s53 offen sc1\n\t" \
  "s_cmp_eq_u32 s48, 63\n\t" \
  "s_cbranch_scc1 LEND_%=\n\t" \
  "s_add_u32 s48, s48, 1\n\t" \
  "s_cmp_eq_u32 s83, s56\n\t" \
  "s_mov_b32 s83, s56\n\t" \
  "s_cbranch_scc0 LCOLD_%=\n\t" \
  "s_waitcnt vmcnt(5)\n\t" \
  "s_branch LTOP_%=\n\t" \
  "LEND_%=:\n\t" \
  "s_waitcnt vmcnt(0)\n\t" \
  "s_barrier\n\t" \
  "s_mov_b64 exec, 1\n\t" \
  "buffer_store_dword v3, off, %[X], s49 sc1\n\t" \
  "s_mov_b64 exec, -1\n\t" \
  "s_mov_b32 s49, 0xf0000000\n\t" \
  ""
#define QK_ASM \
  "v_lshrrev_b32 v16, 7, v0\n\t" \
  "v_lshlrev_b32 v16, 13, v16\n\t" \
  "v_lshrrev_b32 v19, 2, v0\n\t" \
  "v_and_b32 v19, 64, v19\n\t" \
  "v_xor_b32 v19, v19, v0\n\t" \
  "v_and_b32 v19, 0x70, v19\n\t" \
  "v_add_u32 v16, v16, v19\n\t" \
  "s_mul_i32 s63, s40, 40960\n\t" \
  "v_and_b32 v19, 0x300, v0\n\t" \
  "v_lshlrev_b32 v17, 2, v19\n\t" \
  "v_and_b32 v19, 0xc0, v0\n\t" \
  "v_lshl_add_u32 v17, v19, 1, v17\n\t" \
  "v_and_b32 v19, 0x30, v0\n\t" \
  "v_add3_u32 v17, v17, v19, s63\n\t" \
  "v_lshrrev_b32 v19, 1, v0\n\t" \
  "v_and_b32 v19, 64, v19\n\t" \
  "v_add_u32 v17, v17, v19\n\t" \
  "v_xor_b32 v18, 64, v17\n\t" \
  "s_add_u32 m0, s63, 0\n\t" \
  "s_mov_b32 s60, 0\n\t" \
  "buffer_load_dwordx4 v16, %[W], s60 offen lds\n\t" \
  "s_add_u32 m0, s63, 1024\n\t" \
  "s_mov_b32 s60, 65536\n\t" \
  "buffer_load_dwordx4 v16, %[W], s60 offen lds\n\t" \
  "s_add_u32 m0, s63, 2048\n\t" \
  "s_mov_b32 s60, 131072\n\t" \
  "buffer_load_dwordx4 v16, %[W], s60 offen lds\n\t" \
  "s_add_u32 m0, s63, 3072\n\t" \
  "s_mov_b32 s60, 196608\n\t" \
  "buffer_load_dwordx4 v16, %[W], s60 offen lds\n\t" \
  "s_add_u32 m0, s63, 4096\n\t" \
  "s_mov_b32 s60, 262144\n\t" \
  "buffer_load_dwordx4 v16, %[W], s60 offen lds\n\t" \
  "s_add_u32 m0, s63, 5120\n\t" \
  "s_mov_b32 s60, 327680\n\t" \
  "buffer_load_dwordx4 v16, %[W], s60 offen lds\n\t" \
  "s_add_u32 m0, s63, 6144\n\t" \
  "s_mov_b32 s60, 393216\n\t" \
  "buffer_load_dwordx4 v16, %[W], s60 offen lds\n\t" \
  "s_add_u32 m0, s63, 7168\n\t" \
  "s_mov_b32 s60, 458752\n\t" \
  "buffer_load_dwordx4 v16, %[W], s60 offen lds\n\t" \
  "s_add_u32 m0, s63, 8192\n\t" \
  "s_mov_b32 s60, 524288\n\t" \
  "buffer_load_dwordx4 v16, %[W], s60 offen lds\n\t" \
  "s_add_u32 m0, s63, 9216\n\t" \
  "s_mov_b32 s60, 589824\n\t" \
  "buffer_load_dwordx4 v16, %[W], s60 offen lds\n\t" \
  "s_add_u32 m0, s63, 10240\n\t" \
  "s_mov_b32 s60, 655360\n\t" \
  "buffer_load_dwordx4 v16, %[W], s60 offen lds\n\t" \
  "s_add_u32 m0, s63, 11264\n\t" \
  "s_mov_b32 s60, 720896\n\t" \
  "buffer_load_dwordx4 v16, %[W], s60 offen lds\n\t" \
  "s_add_u32 m0, s63, 12288\n\t" \
  "s_mov_b32 s60, 786432\n\t" \
  "buffer_load_dwordx4 v16, %[W], s60 offen lds\n\t" \
  "s_add_u32 m0, s63, 13312\n\t" \
  "s_mov_b32 s60, 851968\n\t" \
  "buffer_load_dwordx4 v16, %[W], s60 offen lds\n\t" \
  "s_add_u32 m0, s63, 14336\n\t" \
  "s_mov_b32 s60, 917504\n\t" \
  "buffer_load_dwordx4 v16, %[W], s60 offen lds\n\t" \
  "s_add_u32 m0, s63, 15360\n\t" \
  "s_mov_b32 s60, 983040\n\t" \
  "buffer_load_dwordx4 v16, %[W], s60 offen lds\n\t" \
  "s_add_u32 m0, s63, 16384\n\t" \
  "s_mov_b32 s60, 1048576\n\t" \
  "buffer_load_dwordx4 v16, %[W], s60 offen lds\n\t" \
  "s_add_u32 m0, s63, 17408\n\t" \
  "s_mov_b32 s60, 1114112\n\t" \
  "buffer_load_dwordx4 v16, %[W], s60 offen lds\n\t" \
  "s_add_u32 m0, s63, 18432\n\t" \
  "s_mov_b32 s60, 1179648\n\t" \
  "buffer_load_dwordx4 v16, %[W], s60 offen lds\n\t" \
  "s_add_u32 m0, s63, 19456\n\t" \
  "s_mov_b32 s60, 1245184\n\t" \
  "buffer_load_dwordx4 v16, %[W], s60 offen lds\n\t" \
  "s_add_u32 m0, s63, 20480\n\t" \
  "s_mov_b32 s60, 1310720\n\t" \
  "buffer_load_dwordx4 v16, %[W], s60 offen lds\n\t" \
  "s_add_u32 m0, s63, 21504\n\t" \
  "s_mov_b32 s60, 1376256\n\t" \
  "buffer_load_dwordx4 v16, %[W], s60 offen lds\n\t" \
  "s_add_u32 m0, s63, 22528\n\t" \
  "s_mov_b32 s60, 1441792\n\t" \
  "buffer_load_dwordx4 v16, %[W], s60 offen lds\n\t" \
  "s_add_u32 m0, s63, 23552\n\t" \
  "s_mov_b32 s60, 1507328\n\t" \
  "buffer_load_dwordx4 v16, %[W], s60 offen lds\n\t" \
  "s_add_u32 m0, s63, 24576\n\t" \
  "s_mov_b32 s60, 1572864\n\t" \
  "buffer_load_dwordx4 v16, %[W], s60 offen lds\n\t" \
  "s_add_u32 m0, s63, 25600\n\t" \
  "s_mov_b32 s60, 1638400\n\t" \
  "buffer_load_dwordx4 v16, %[W], s60 offen lds\n\t" \
  "s_add_u32 m0, s63, 26624\n\t" \
  "s_mov_b32 s60, 1703936\n\t" \
  "buffer_load_dwordx4 v16, %[W], s60 offen lds\n\t" \
  "s_add_u32 m0, s63, 27648\n\t" \
  "s_mov_b32 s60, 1769472\n\t" \
  "buffer_load_dwordx4 v16, %[W], s60 offen lds\n\t" \
  "s_add_u32 m0, s63, 28672\n\t" \
  "s_mov_b32 s60, 1835008\n\t" \
  "buffer_load_dwordx4 v16, %[W], s60 offen lds\n\t" \
  "s_add_u32 m0, s63, 29696\n\t" \
  "s_mov_b32 s60, 1900544\n\t" \
  "buffer_load_dwordx4 v16, %[W], s60 offen lds\n\t" \
  "s_add_u32 m0, s63, 30720\n\t" \
  "s_mov_b32 s60, 1966080\n\t" \
  "buffer_load_dwordx4 v16, %[W], s60 offen lds\n\t" \
  "s_add_u32 m0, s63, 31744\n\t" \
  "s_mov_b32 s60, 2031616\n\t" \
  "buffer_load_dwordx4 v16, %[W], s60 offen lds\n\t" \
  "s_add_u32 m0, s63, 32768\n\t" \
  "s_mov_b32 s60, 2097152\n\t" \
  "buffer_load_dwordx4 v16, %[W], s60 offen lds\n\t" \
  "s_add_u32 m0, s63, 33792\n\t" \
  "s_mov_b32 s60, 2162688\n\t" \
  "buffer_load_dwordx4 v16, %[W], s60 offen lds\n\t" \
  "s_add_u32 m0, s63, 34816\n\t" \
  "s_mov_b32 s60, 2228224\n\t" \
  "buffer_load_dwordx4 v16, %[W], s60 offen lds\n\t" \
  "s_add_u32 m0, s63, 35840\n\t" \
  "s_mov_b32 s60, 2293760\n\t" \
  "buffer_load_dwordx4 v16, %[W], s60 offen lds\n\t" \
  "s_add_u32 m0, s63, 36864\n\t" \
  "s_mov_b32 s60, 2359296\n\t" \
  "buffer_load_dwordx4 v16, %[W], s60 offen lds\n\t" \
  "s_add_u32 m0, s63, 37888\n\t" \
  "s_mov_b32 s60, 2424832\n\t" \
  "buffer_load_dwordx4 v16, %[W], s60 offen lds\n\t" \
  "s_add_u32 m0, s63, 38912\n\t" \
  "s_mov_b32 s60, 2490368\n\t" \
  "buffer_load_dwordx4 v16, %[W], s60 offen lds\n\t" \
  "s_add_u32 m0, s63, 39936\n\t" \
  "s_mov_b32 s60, 2555904\n\t" \
  "buffer_load_dwordx4 v16, %[W], s60 offen lds\n\t" \
  "s_waitcnt vmcnt(32)\n\t" \
  "ds_read_b64_tr_b16 a[0:1], v17 offset:0\n\t" \
  "ds_read_b64_tr_b16 a[2:3], v17 offset:512\n\t" \
  "ds_read_b64_tr_b16 a[4:5], v17 offset:4096\n\t" \
  "ds_read_b64_tr_b16 a[6:7], v17 offset:4608\n\t" \
  "ds_read_b64_tr_b16 a[72:73], v17 offset:8\n\t" \
  "ds_read_b64_tr_b16 a[74:75], v17 offset:520\n\t" \
  "ds_read_b64_tr_b16 a[76:77], v17 offset:4104\n\t" \
  "ds_read_b64_tr_b16 a[78:79], v17 offset:4616\n\t" \
  "ds_read_b64_tr_b16 a[144:145], v18 offset:0\n\t" \
  "ds_read_b64_tr_b16 a[146:147], v18 offset:512\n\t" \
  "ds_read_b64_tr_b16 a[148:149], v18 offset:4096\n\t" \
  "ds_read_b64_tr_b16 a[150:151], v18 offset:4608\n\t" \
  "ds_read_b64_tr_b16 a[216:217], v18 offset:8\n\t" \
  "ds_read_b64_tr_b16 a[218:219], v18 offset:520\n\t" \
  "ds_read_b64_tr_b16 a[220:221], v18 offset:4104\n\t" \
  "ds_read_b64_tr_b16 a[222:223], v18 offset:4616\n\t" \
  "s_waitcnt lgkmcnt(0)\n\t" \
  "s_add_u32 m0, s63, 0\n\t" \
  "s_mov_b32 s60, 2621440\n\t" \
  "buffer_load_dwordx4 v16, %[W], s60 offen lds\n\t" \
  "s_add_u32 m0, s63, 1024\n\t" \
  "s_mov_b32 s60, 2686976\n\t" \
  "buffer_load_dwordx4 v16, %[W], s60 offen lds\n\t" \
  "s_add_u32 m0, s63, 2048\n\t" \
  "s_mov_b32 s60, 2752512\n\t" \
  "buffer_load_dwordx4 v16, %[W], s60 offen lds\n\t" \
  "s_add_u32 m0, s63, 3072\n\t" \
  "s_mov_b32 s60, 2818048\n\t" \
  "buffer_load_dwordx4 v16, %[W], s60 offen lds\n\t" \
  "s_add_u32 m0, s63, 4096\n\t" \
  "s_mov_b32 s60, 2883584\n\t" \
  "buffer_load_dwordx4 v16, %[W], s60 offen lds\n\t" \
  "s_add_u32 m0, s63, 5120\n\t" \
  "s_mov_b32 s60, 2949120\n\t" \
  "buffer_load_dwordx4 v16, %[W], s60 offen lds\n\t" \
  "s_add_u32 m0, s63, 6144\n\t" \
  "s_mov_b32 s60, 3014656\n\t" \
  "buffer_load_dwordx4 v16, %[W], s60 offen lds\n\t" \
  "s_add_u32 m0, s63, 7168\n\t" \
  "s_mov_b32 s60, 3080192\n\t" \
  "buffer_load_dwordx4 v16, %[W], s60 offen lds\n\t" \
  "s_waitcnt vmcnt(32)\n\t" \
  "ds_read_b64_tr_b16 a[8:9], v17 offset:8192\n\t" \
  "ds_read_b64_tr_b16 a[10:11], v17 offset:8704\n\t" \
  "ds_read_b64_tr_b16 a[12:13], v17 offset:12288\n\t" \
  "ds_read_b64_tr_b16 a[14:15], v17 offset:12800\n\t" \
  "ds_read_b64_tr_b16 a[80:81], v17 offset:8200\n\t" \
  "ds_read_b64_tr_b16 a[82:83], v17 offset:8712\n\t" \
  "ds_read_b64_tr_b16 a[84:85], v17 offset:12296\n\t" \
  "ds_read_b64_tr_b16 a[86:87], v17 offset:12808\n\t" \
  "ds_read_b64_tr_b16 a[152:153], v18 offset:8192\n\t" \
  "ds_read_b64_tr_b16 a[154:155], v18 offset:8704\n\t" \
  "ds_read_b64_tr_b16 a[156:157], v18 offset:12288\n\t" \
  "ds_read_b64_tr_b16 a[158:159], v18 offset:12800\n\t" \
  "ds_read_b64_tr_b16 a[224:225], v18 offset:8200\n\t" \
  "ds_read_b64_tr_b16 a[226:227], v18 offset:8712\n\t" \
  "ds_read_b64_tr_b16 a[228:229], v18 offset:12296\n\t" \
  "ds_read_b64_tr_b16 a[230:231], v18 offset:12808\n\t" \
  "s_waitcnt lgkmcnt(0)\n\t" \
  "s_add_u32 m0, s63, 8192\n\t" \
  "s_mov_b32 s60, 3145728\n\t" \
  "buffer_load_dwordx4 v16, %[W], s60 offen lds\n\t" \
  "s_add_u32 m0, s63, 9216\n\t" \
  "s_mov_b32 s60, 3211264\n\t" \
  "buffer_load_dwordx4 v16, %[W], s60 offen lds\n\t" \
  "s_add_u32 m0, s63, 10240\n\t" \
  "s_mov_b32 s60, 3276800\n\t" \
  "buffer_load_dwordx4 v16, %[W], s60 offen lds\n\t" \
  "s_add_u32 m0, s63, 11264\n\t" \
  "s_mov_b32 s60, 3342336\n\t" \
  "buffer_load_dwordx4 v16, %[W], s60 offen lds\n\t" \
  "s_add_u32 m0, s63, 12288\n\t" \
  "s_mov_b32 s60, 3407872\n\t" \
  "buffer_load_dwordx4 v16, %[W], s60 offen lds\n\t" \
  "s_add_u32 m0, s63, 13312\n\t" \
  "s_mov_b32 s60, 3473408\n\t" \
  "buffer_load_dwordx4 v16, %[W], s60 offen lds\n\t" \
  "s_add_u32 m0, s63, 14336\n\t" \
  "s_mov_b32 s60, 3538944\n\t" \
  "buffer_load_dwordx4 v16, %[W], s60 offen lds\n\t" \
  "s_add_u32 m0, s63, 15360\n\t" \
  "s_mov_b32 s60, 3604480\n\t" \
  "buffer_load_dwordx4 v16, %[W], s60 offen lds\n\t" \
  "s_waitcnt vmcnt(32)\n\t" \
  "ds_read_b64_tr_b16 a[16:17], v17 offset:16384\n\t" \
  "ds_read_b64_tr_b16 a[18:19], v17 offset:16896\n\t" \
  "ds_read_b64_tr_b16 a[20:21], v17 offset:20480\n\t" \
  "ds_read_b64_tr_b16 a[22:23], v17 offset:20992\n\t" \
  "ds_read_b64_tr_b16 a[88:89], v17 offset:16392\n\t" \
  "ds_read_b64_tr_b16 a[90:91], v17 offset:16904\n\t" \
  "ds_read_b64_tr_b16 a[92:93], v17 offset:20488\n\t" \
  "ds_read_b64_tr_b16 a[94:95], v17 offset:21000\n\t" \
  "ds_read_b64_tr_b16 a[160:161], v18 offset:16384\n\t" \
  "ds_read_b64_tr_b16 a[162:163], v18 offset:16896\n\t" \
  "ds_read_b64_tr_b16 a[164:165], v18 offset:20480\n\t" \
  "ds_read_b64_tr_b16 a[166:167], v18 offset:20992\n\t" \
  "ds_read_b64_tr_b16 a[232:233], v18 offset:16392\n\t" \
  "ds_read_b64_tr_b16 a[234:235], v18 offset:16904\n\t" \
  "ds_read_b64_tr_b16 a[236:237], v18 offset:20488\n\t" \
  "ds_read_b64_tr_b16 a[238:239], v18 offset:21000\n\t" \
  "s_waitcnt lgkmcnt(0)\n\t" \
  "s_add_u32 m0, s63, 16384\n\t" \
  "s_mov_b32 s60, 3670016\n\t" \
  "buffer_load_dwordx4 v16, %[W], s60 offen lds\n\t" \
  "s_add_u32 m0, s63, 17408\n\t" \
  "s_mov_b32 s60, 3735552\n\t" \
  "buffer_load_dwordx4 v16, %[W], s60 offen lds\n\t" \
  "s_add_u32 m0, s63, 18432\n\t" \
  "s_mov_b32 s60, 3801088\n\t" \
  "buffer_load_dwordx4 v16, %[W], s60 offen lds\n\t" \
  "s_add_u32 m0, s63, 19456\n\t" \
  "s_mov_b32 s60, 3866624\n\t" \
  "buffer_load_dwordx4 v16, %[W], s60 offen lds\n\t" \
  "s_add_u32 m0, s63, 20480\n\t" \
  "s_mov_b32 s60, 3932160\n\t" \
  "buffer_load_dwordx4 v16, %[W], s60 offen lds\n\t" \
  "s_add_u32 m0, s63, 21504\n\t" \
  "s_mov_b32 s60, 3997696\n\t" \
  "buffer_load_dwordx4 v16, %[W], s60 offen lds\n\t" \
  "s_add_u32 m0, s63, 22528\n\t" \
  "s_mov_b32 s60, 4063232\n\t" \
  "buffer_load_dwordx4 v16, %[W], s60 offen lds\n\t" \
  "s_add_u32 m0, s63, 23552\n\t" \
  "s_mov_b32 s60, 4128768\n\t" \
  "buffer_load_dwordx4 v16, %[W], s60 offen lds\n\t" \
  "s_waitcnt vmcnt(32)\n\t" \
  "ds_read_b64_tr_b16 a[24:25], v17 offset:24576\n\t" \
  "ds_read_b64_tr_b16 a[26:27], v17 offset:25088\n\t" \
  "ds_read_b64_tr_b16 a[28:29], v17 offset:28672\n\t" \
  "ds_read_b64_tr_b16 a[30:31], v17 offset:29184\n\t" \
  "ds_read_b64_tr_b16 a[96:97], v17 offset:24584\n\t" \
  "ds_read_b64_tr_b16 a[98:99], v17 offset:25096\n\t" \
  "ds_read_b64_tr_b16 a[100:101], v17 offset:28680\n\t" \
  "ds_read_b64_tr_b16 a[102:103], v17 offset:29192\n\t" \
  "ds_read_b64_tr_b16 a[168:169], v18 offset:24576\n\t" \
  "ds_read_b64_tr_b16 a[170:171], v18 offset:25088\n\t" \
  "ds_read_b64_tr_b16 a[172:173], v18 offset:28672\n\t" \
  "ds_read_b64_tr_b16 a[174:175], v18 offset:29184\n\t" \
  "ds_read_b64_tr_b16 a[240:241], v18 offset:24584\n\t" \
  "ds_read_b64_tr_b16 a[242:243], v18 offset:25096\n\t" \
  "ds_read_b64_tr_b16 a[244:245], v18 offset:28680\n\t" \
  "ds_read_b64_tr_b16 a[246:247], v18 offset:29192\n\t" \
  "s_waitcnt lgkmcnt(0)\n\t" \
  "s_add_u32 m0, s63, 24576\n\t" \
  "s_mov_b32 s60, 4194304\n\t" \
  "buffer_load_dwordx4 v16, %[W], s60 offen lds\n\t" \
  "s_add_u32 m0, s63, 25600\n\t" \
  "s_mov_b32 s60, 4259840\n\t" \
  "buffer_load_dwordx4 v16, %[W], s60 offen lds\n\t" \
  "s_add_u32 m0, s63, 26624\n\t" \
  "s_mov_b32 s60, 4325376\n\t" \
  "buffer_load_dwordx4 v16, %[W], s60 offen lds\n\t" \
  "s_add_u32 m0, s63, 27648\n\t" \
  "s_mov_b32 s60, 4390912\n\t" \
  "buffer_load_dwordx4 v16, %[W], s60 offen lds\n\t" \
  "s_add_u32 m0, s63, 28672\n\t" \
  "s_mov_b32 s60, 4456448\n\t" \
  "buffer_load_dwordx4 v16, %[W], s60 offen lds\n\t" \
  "s_add_u32 m0, s63, 29696\n\t" \
  "s_mov_b32 s60, 4521984\n\t" \
  "buffer_load_dwordx4 v16, %[W], s60 offen lds\n\t" \
  "s_add_u32 m0, s63, 30720\n\t" \
  "s_mov_b32 s60, 4587520\n\t" \
  "buffer_load_dwordx4 v16, %[W], s60 offen lds\n\t" \
  "s_add_u32 m0, s63, 31744\n\t" \
  "s_mov_b32 s60, 4653056\n\t" \
  "buffer_load_dwordx4 v16, %[W], s60 offen lds\n\t" \
  "s_waitcnt vmcnt(32)\n\t" \
  "ds_read_b64_tr_b16 a[32:33], v17 offset:32768\n\t" \
  "ds_read_b64_tr_b16 a[34:35], v17 offset:33280\n\t" \
  "ds_read_b64_tr_b16 a[36:37], v17 offset:36864\n\t" \
  "ds_read_b64_tr_b16 a[38:39], v17 offset:37376\n\t" \
  "ds_read_b64_tr_b16 a[104:105], v17 offset:32776\n\t" \
  "ds_read_b64_tr_b16 a[106:107], v17 offset:33288\n\t" \
  "ds_read_b64_tr_b16 a[108:109], v17 offset:36872\n\t" \
  "ds_read_b64_tr_b16 a[110:111], v17 offset:37384\n\t" \
  "ds_read_b64_tr_b16 a[176:177], v18 offset:32768\n\t" \
  "ds_read_b64_tr_b16 a[178:179], v18 offset:33280\n\t" \
  "ds_read_b64_tr_b16 a[180:181], v18 offset:36864\n\t" \
  "ds_read_b64_tr_b16 a[182:183], v18 offset:37376\n\t" \
  "ds_read_b64_tr_b16 a[248:249], v18 offset:32776\n\t" \
  "ds_read_b64_tr_b16 a[250:251], v18 offset:33288\n\t" \
  "ds_read_b64_tr_b16 a[252:253], v18 offset:36872\n\t" \
  "ds_read_b64_tr_b16 a[254:255], v18 offset:37384\n\t" \
  "s_waitcnt lgkmcnt(0)\n\t" \
  "s_waitcnt vmcnt(24)\n\t" \
  "ds_read_b64_tr_b16 a[40:41], v17 offset:0\n\t" \
  "ds_read_b64_tr_b16 a[42:43], v17 offset:512\n\t" \
  "ds_read_b64_tr_b16 a[44:45], v17 offset:4096\n\t" \
  "ds_read_b64_tr_b16 a[46:47], v17 offset:4608\n\t" \
  "ds_read_b64_tr_b16 a[112:113], v17 offset:8\n\t" \
  "ds_read_b64_tr_b16 a[114:115], v17 offset:520\n\t" \
  "ds_read_b64_tr_b16 a[116:117], v17 offset:4104\n\t" \
  "ds_read_b64_tr_b16 a[118:119], v17 offset:4616\n\t" \
  "ds_read_b64_tr_b16 a[184:185], v18 offset:0\n\t" \
  "ds_read_b64_tr_b16 a[186:187], v18 offset:512\n\t" \
  "ds_read_b64_tr_b16 a[188:189], v18 offset:4096\n\t" \
  "ds_read_b64_tr_b16 a[190:191], v18 offset:4608\n\t" \
  "ds_read_b64_tr_b16 v[224:225], v18 offset:8\n\t" \
  "ds_read_b64_tr_b16 v[226:227], v18 offset:520\n\t" \
  "ds_read_b64_tr_b16 v[228:229], v18 offset:4104\n\t" \
  "ds_read_b64_tr_b16 v[230:231], v18 offset:4616\n\t" \
  "s_waitcnt lgkmcnt(0)\n\t" \
  "s_waitcnt vmcnt(16)\n\t" \
  "ds_read_b64_tr_b16 a[48:49], v17 offset:8192\n\t" \
  "ds_read_b64_tr_b16 a[50:51], v17 offset:8704\n\t" \
  "ds_read_b64_tr_b16 a[52:53], v17 offset:12288\n\t" \
  "ds_read_b64_tr_b16 a[54:55], v17 offset:12800\n\t" \
  "ds_read_b64_tr_b16 a[120:121], v17 offset:8200\n\t" \
  "ds_read_b64_tr_b16 a[122:123], v17 offset:8712\n\t" \
  "ds_read_b64_tr_b16 a[124:125], v17 offset:12296\n\t" \
  "ds_read_b64_tr_b16 a[126:127], v17 offset:12808\n\t" \
  "ds_read_b64_tr_b16 a[192:193], v18 offset:8192\n\t" \
  "ds_read_b64_tr_b16 a[194:195], v18 offset:8704\n\t" \
  "ds_read_b64_tr_b16 a[196:197], v18 offset:12288\n\t" \
  "ds_read_b64_tr_b16 a[198:199], v18 offset:12800\n\t" \
  "ds_read_b64_tr_b16 v[232:233], v18 offset:8200\n\t" \
  "ds_read_b64_tr_b16 v[234:235], v18 offset:8712\n\t" \
  "ds_read_b64_tr_b16 v[236:237], v18 offset:12296\n\t" \
  "ds_read_b64_tr_b16 v[238:239], v18 offset:12808\n\t" \
  "s_waitcnt lgkmcnt(0)\n\t" \
  "s_waitcnt vmcnt(8)\n\t" \
  "ds_read_b64_tr_b16 a[56:57], v17 offset:16384\n\t" \
  "ds_read_b64_tr_b16 a[58:59], v17 offset:16896\n\t" \
  "ds_read_b64_tr_b16 a[60:61], v17 offset:20480\n\t" \
  "ds_read_b64_tr_b16 a[62:63], v17 offset:20992\n\t" \
  "ds_read_b64_tr_b16 a[128:129], v17 offset:16392\n\t" \
  "ds_read_b64_tr_b16 a[130:131], v17 offset:16904\n\t" \
  "ds_read_b64_tr_b16 a[132:133], v17 offset:20488\n\t" \
  "ds_read_b64_tr_b16 a[134:135], v17 offset:21000\n\t" \
  "ds_read_b64_tr_b16 a[200:201], v18 offset:16384\n\t" \
  "ds_read_b64_tr_b16 a[202:203], v18 offset:16896\n\t" \
  "ds_read_b64_tr_b16 a[204:205], v18 offset:20480\n\t" \
  "ds_read_b64_tr_b16 a[206:207], v18 offset:20992\n\t" \
  "ds_read_b64_tr_b16 v[240:241], v18 offset:16392\n\t" \
  "ds_read_b64_tr_b16 v[242:243], v18 offset:16904\n\t" \
  "ds_read_b64_tr_b16 v[244:245], v18 offset:20488\n\t" \
  "ds_read_b64_tr_b16 v[246:247], v18 offset:21000\n\t" \
  "s_waitcnt lgkmcnt(0)\n\t" \
  "s_waitcnt vmcnt(0)\n\t" \
  "ds_read_b64_tr_b16 a[64:65], v17 offset:24576\n\t" \
  "ds_read_b64_tr_b16 a[66:67], v17 offset:25088\n\t" \
  "ds_read_b64_tr_b16 a[68:69], v17 offset:28672\n\t" \
  "ds_read_b64_tr_b16 a[70:71], v17 offset:29184\n\t" \
  "ds_read_b64_tr_b16 a[136:137], v17 offset:24584\n\t" \
  "ds_read_b64_tr_b16 a[138:139], v17 offset:25096\n\t" \
  "ds_read_b64_tr_b16 a[140:141], v17 offset:28680\n\t" \
  "ds_read_b64_tr_b16 a[142:143], v17 offset:29192\n\t" \
  "ds_read_b64_tr_b16 a[208:209], v18 offset:24576\n\t" \
  "ds_read_b64_tr_b16 a[210:211], v18 offset:25088\n\t" \
  "ds_read_b64_tr_b16 a[212:213], v18 offset:28672\n\t" \
  "ds_read_b64_tr_b16 a[214:215], v18 offset:29184\n\t" \
  "ds_read_b64_tr_b16 v[248:249], v18 offset:24584\n\t" \
  "ds_read_b64_tr_b16 v[250:251], v18 offset:25096\n\t" \
  "ds_read_b64_tr_b16 v[252:253], v18 offset:28680\n\t" \
  "ds_read_b64_tr_b16 v[254:255], v18 offset:29192\n\t" \
  "s_waitcnt lgkmcnt(0)\n\t" \
  "s_mov_b32 s84, " TK_CAP "\n\t" \
  "s_mov_b32 s48, 0\n\t" \
  "s_mov_b32 s49, 0xf0000000\n\t" \
  "s_mov_b32 s46, s41\n\t" \
  "s_mov_b32 s56, 0\n\t" \
  "v_lshrrev_b32 v7, 2, v0\n\t" \
  "s_mov_b32 s60, " TK_CTAB "\n\t" \
  "buffer_load_dword v4, v7, %[X], s60 offen offset:88 sc1\n\t" \
  "buffer_load_dword v7, v7, %[X], s60 offen offset:0 sc1\n\t" \
  "s_waitcnt vmcnt(0)\n\t" \
  "v_and_b32 v4, 1, v4\n\t" \
  "v_and_b32 v7, 1, v7\n\t" \
  "v_cmp_ne_u32 s[90:91], s67, v4\n\t" \
  "v_cmp_ne_u32 s[88:89], s67, v7\n\t" \
  "s_nop 1\n\t" \
  "s_mov_b32 s57, 0xf0000000\n\t" \
  "s_add_u32 s83, s48, 0\n\t" \
  "s_lshr_b64 s[60:61], s[90:91], s83\n\t" \
  "s_and_b32 s60, s60, 1\n\t" \
  "s_lshl_b32 s60, s60, 13\n\t" \
  "s_add_u32 s83, s83, " TK_SG0 "\n\t" \
  "s_lshl_b32 s83, s83, 21\n\t" \
  "s_or_b32 s83, s83, s60\n\t" \
  "s_add_u32 s62, s83, s39\n\t" \
  "v_add_u32 v7, s62, v3\n\t" \
  "LCOLD_%=:\n\t" \
  "s_waitcnt vmcnt(0)\n\t" \
  "s_mov_b64 exec, 1\n\t" \
  "buffer_store_dword v6, off, %[X], s49 sc1\n\t" \
  "s_mov_b64 exec, -1\n\t" \
  "s_mov_b32 s49, 0xf0000000\n\t" \
  "s_add_u32 s55, s83, s36\n\t" \
  "s_cmp_lg_u32 s40, 0\n\t" \
  "s_cbranch_scc1 LGO_%=\n\t" \
  "s_and_b32 s62, s45, 3\n\t" \
  "s_lshl_b32 s62, s62, 7\n\t" \
  "s_add_u32 s62, s62, s83\n\t" \
  "s_add_u32 s62, s62, s39\n\t" \
  "s_mov_b32 s61, 0\n\t" \
  "LPO_%=:\n\t" \
  "buffer_load_dword v4, off, %[X], s62 sc1\n\t" \
  "s_waitcnt vmcnt(0)\n\t" \
  "v_cmp_eq_u32 vcc, 0, v4\n\t" \
  "s_cbranch_vccz LPO_%=x\n\t" \
  "s_add_u32 s61, s61, 1\n\t" \
  "s_sleep " TK_PSH "\n\t" \
  "s_cmp_gt_u32 s61, s84\n\t" \
  "s_cbranch_scc1 LPO_%=g\n\t" \
  "s_and_b32 s60, s61, 1023\n\t" \
  "s_cmp_lg_u32 s60, 0\n\t" \
  "s_cbranch_scc1 LPO_%=\n\t" \
  "buffer_load_dword v4, off, %[X], s92 sc1\n\t" \
  "s_waitcnt vmcnt(0)\n\t" \
  "v_readfirstlane_b32 s60, v4\n\t" \
  "s_nop 0\n\t" \
  "s_cmp_eq_u32 s60, 0\n\t" \
  "s_cbranch_scc1 LPO_%=\n\t" \
  "LPO_%=g:\n\t" \
  "s_mov_b32 s84, 0\n\t" \
  "s_or_b32 s60, s48, 1610612736\n\t" \
  "v_mov_b32 v4, s60\n\t" \
  "s_mov_b64 exec, 1\n\t" \
  "buffer_store_dword v4, off, %[X], s92 sc1\n\t" \
  "s_mov_b64 exec, -1\n\t" \
  "LPO_%=x:\n\t" \
  "s_mov_b32 s61, 0\n\t" \
  "LPH_%=:\n\t" \
  "buffer_load_dword v4, v7, %[X], 0 offen sc1\n\t" \
  "s_waitcnt vmcnt(0)\n\t" \
  "v_cmp_eq_u32 vcc, 0, v4\n\t" \
  "s_cbranch_vccz LPH_%=x\n\t" \
  "s_add_u32 s61, s61, 1\n\t" \
  "s_sleep " TK_PSH "\n\t" \
  "s_cmp_gt_u32 s61, s84\n\t" \
  "s_cbranch_scc1 LPH_%=g\n\t" \
  "s_and_b32 s60, s61, 1023\n\t" \
  "s_cmp_lg_u32 s60, 0\n\t" \
  "s_cbranch_scc1 LPH_%=\n\t" \
  "buffer_load_dword v4, off, %[X], s92 sc1\n\t" \
  "s_waitcnt vmcnt(0)\n\t" \
  "v_readfirstlane_b32 s60, v4\n\t" \
  "s_nop 0\n\t" \
  "s_cmp_eq_u32 s60, 0\n\t" \
  "s_cbranch_scc1 LPH_%=\n\t" \
  "LPH_%=g:\n\t" \
  "s_mov_b32 s84, 0\n\t" \
  "s_or_b32 s60, s48, 1610612736\n\t" \
  "v_mov_b32 v4, s60\n\t" \
  "s_mov_b64 exec, 1\n\t" \
  "buffer_store_dword v4, off, %[X], s92 sc1\n\t" \
  "s_mov_b64 exec, -1\n\t" \
  "LPH_%=x:\n\t" \
  "s_cmp_lt_i32 s57, 0\n\t" \
  "s_cbranch_scc1 LGO_%=\n\t" \
  "s_movk_i32 s63, 4000\n\t" \
  "s_branch LGC_%=u\n\t" \
  "LGC_%=:\n\t" \
  "s_sleep " TK_PSG "\n\t" \
  "buffer_load_dword v8, off, %[X], s57 sc1\n\t" \
  "s_waitcnt vmcnt(0)\n\t" \
  "LGC_%=u:\n\t" \
  "s_memrealtime s[52:53]\n\t" \
  "v_readfirstlane_b32 s60, v8\n\t" \
  "s_waitcnt lgkmcnt(0)\n\t" \
  "s_sub_u32 s63, s63, 1\n\t" \
  "s_cbranch_scc1 LGO_%=\n\t" \
  "s_cmp_eq_u32 s60, -1\n\t" \
  "s_cbranch_scc1 LGC_%=\n\t" \
  "s_not_b32 s60, s60\n\t" \
  "s_lshl_b32 s61, s52, 8\n\t" \
  "s_sub_u32 s60, s60, s61\n\t" \
  "s_ashr_i32 s60, s60, 8\n\t" \
  "s_sub_i32 s60, s60, " TK_QXC "\n\t" \
  "s_min_i32 s60, s60, 400\n\t" \
  "s_add_u32 s54, s52, s60\n\t" \
  "LGC_%=w:\n\t" \
  "s_sleep 1\n\t" \
  "s_memrealtime s[52:53]\n\t" \
  "s_waitcnt lgkmcnt(0)\n\t" \
  "s_sub_i32 s53, s52, s54\n\t" \
  "s_cmp_lt_i32 s53, 0\n\t" \
  "s_cbranch_scc1 LGC_%=w\n\t" \
  "LGO_%=:\n\t" \
  "s_barrier\n\t" \
  "s_add_u32 m0, s46, 0\n\t" \
  "s_add_u32 s47, s55, 0\n\t" \
  "buffer_load_dwordx4 v0, %[X], s47 offen sc1 lds\n\t" \
  "s_add_u32 m0, s46, 4096\n\t" \
  "s_add_u32 s47, s55, 4096\n\t" \
  "buffer_load_dwordx4 v0, %[X], s47 offen sc1 lds\n\t" \
  "s_add_u32 m0, s46, 8192\n\t" \
  "s_add_u32 s47, s55, 16384\n\t" \
  "buffer_load_dwordx4 v0, %[X], s47 offen sc1 lds\n\t" \
  "s_add_u32 m0, s46, 12288\n\t" \
  "s_add_u32 s47, s55, 20480\n\t" \
  "buffer_load_dwordx4 v0, %[X], s47 offen sc1 lds\n\t" \
  "s_add_u32 m0, s46, 16384\n\t" \
  "s_add_u32 s47, s55, 32768\n\t" \
  "buffer_load_dwordx4 v0, %[X], s47 offen sc1 lds\n\t" \
  "s_add_u32 m0, s46, 20480\n\t" \
  "s_add_u32 s47, s55, 36864\n\t" \
  "buffer_load_dwordx4 v0, %[X], s47 offen sc1 lds\n\t" \
  "s_add_u32 m0, s46, 24576\n\t" \
  "s_add_u32 s47, s55, 49152\n\t" \
  "buffer_load_dwordx4 v0, %[X], s47 offen sc1 lds\n\t" \
  "s_add_u32 m0, s46, 28672\n\t" \
  "s_add_u32 s47, s55, 53248\n\t" \
  "buffer_load_dwordx4 v0, %[X], s47 offen sc1 lds\n\t" \
  "s_add_u32 m0, s46, 32768\n\t" \
  "s_add_u32 s47, s55, 65536\n\t" \
  "buffer_load_dwordx4 v0, %[X], s47 offen sc1 lds\n\t" \
  "s_waitcnt vmcnt(5)\n\t" \
  "LTOP_%=:\n\t" \
  "s_barrier\n\t" \
  "ds_read_b128 v[64:67], v1 offset:0\n\t" \
  "ds_read_b128 v[68:71], v1 offset:2048\n\t" \
  "ds_read_b128 v[72:75], v2 offset:0\n\t" \
  "ds_read_b128 v[76:79], v2 offset:2048\n\t" \
  "ds_read_b128 v[80:83], v1 offset:4096\n\t" \
  "ds_read_b128 v[84:87], v1 offset:6144\n\t" \
  "s_mov_b32 s82, s83\n\t" \
  "s_xor_b32 s46, s46, 0x10000\n\t" \
  "s_waitcnt lgkmcnt(5)\n\t" \
  "v_mfma_f32_16x16x32_bf16 v[16:19], a[0:3], v[64:67], 0\n\t" \
  "ds_read_b128 v[88:91], v2 offset:4096\n\t" \
  "v_mfma_f32_16x16x32_bf16 v[20:23], a[72:75], v[64:67], 0\n\t" \
  "s_add_u32 s83, s48, 1\n\t" \
  "s_lshr_b64 s[60:61], s[90:91], s83\n\t" \
  "v_mfma_f32_16x16x32_bf16 v[24:27], a[144:147], v[64:67], 0\n\t" \
  "ds_read_b128 v[92:95], v2 offset:6144\n\t" \
  "v_mfma_f32_16x16x32_bf16 v[28:31], a[216:219], v[64:67], 0\n\t" \
  "s_and_b32 s60, s60, 1\n\t" \
  "s_lshl_b32 s60, s60, 13\n\t" \
  "s_waitcnt lgkmcnt(5)\n\t" \
  "v_mfma_f32_16x16x32_bf16 v[16:19], a[4:7], v[72:75], v[16:19]\n\t" \
  "ds_read_b128 v[96:99], v1 offset:8192\n\t" \
  "v_mfma_f32_16x16x32_bf16 v[20:23], a[76:79], v[72:75], v[20:23]\n\t" \
  "s_add_u32 s83, s83, " TK_SG0 "\n\t" \
  "s_lshl_b32 s83, s83, 21\n\t" \
  "v_mfma_f32_16x16x32_bf16 v[24:27], a[148:151], v[72:75], v[24:27]\n\t" \
  "ds_read_b128 v[100:103], v1 offset:10240\n\t" \
  "v_mfma_f32_16x16x32_bf16 v[28:31], a[220:223], v[72:75], v[28:31]\n\t" \
  "s_or_b32 s83, s83, s60\n\t" \
  "s_add_u32 s62, s83, s39\n\t" \
  "s_waitcnt lgkmcnt(5)\n\t" \
  "v_mfma_f32_16x16x32_bf16 v[16:19], a[8:11], v[80:83], v[16:19]\n\t" \
  "ds_read_b128 v[104:107], v2 offset:8192\n\t" \
  "v_mfma_f32_16x16x32_bf16 v[20:23], a[80:83], v[80:83], v[20:23]\n\t" \
  "v_add_u32 v7, s62, v3\n\t" \
  "s_add_u32 s50, s82, s37\n\t" \
  "v_mfma_f32_16x16x32_bf16 v[24:27], a[152:155], v[80:83], v[24:27]\n\t" \
  "ds_read_b128 v[108:111], v2 offset:10240\n\t" \
  "v_mfma_f32_16x16x32_bf16 v[28:31], a[224:227], v[80:83], v[28:31]\n\t" \
  "s_add_u32 s51, s50, 4096\n\t" \
  "s_waitcnt lgkmcnt(5)\n\t" \
  "v_mfma_f32_16x16x32_bf16 v[16:19], a[12:15], v[88:91], v[16:19]\n\t" \
  "ds_read_b128 v[112:115], v1 offset:12288\n\t" \
  "v_mfma_f32_16x16x32_bf16 v[20:23], a[84:87], v[88:91], v[20:23]\n\t" \
  "v_mfma_f32_16x16x32_bf16 v[24:27], a[156:159], v[88:91], v[24:27]\n\t" \
  "ds_read_b128 v[116:119], v1 offset:14336\n\t" \
  "v_mfma_f32_16x16x32_bf16 v[28:31], a[228:231], v[88:91], v[28:31]\n\t" \
  "s_waitcnt lgkmcnt(5)\n\t" \
  "v_mfma_f32_16x16x32_bf16 v[16:19], a[16:19], v[96:99], v[16:19]\n\t" \
  "ds_read_b128 v[120:123], v2 offset:12288\n\t" \
  "v_mfma_f32_16x16x32_bf16 v[20:23], a[88:91], v[96:99], v[20:23]\n\t" \
  "s_cmp_lg_u32 s56, 0\n\t" \
  "v_mfma_f32_16x16x32_bf16 v[24:27], a[160:163], v[96:99], v[24:27]\n\t" \
  "ds_read_b128 v[124:127], v2 offset:14336\n\t" \
  "v_mfma_f32_16x16x32_bf16 v[28:31], a[232:235], v[96:99], v[28:31]\n\t" \
  "s_cbranch_scc1 LWARM_%=\n\t" \
  "s_waitcnt vmcnt(0)\n\t" \
  "LWARM_%=:\n\t" \
  "s_barrier\n\t" \
  "s_waitcnt lgkmcnt(5)\n\t" \
  "v_mfma_f32_16x16x32_bf16 v[16:19], a[20:23], v[104:107], v[16:19]\n\t" \
  "ds_read_b128 v[128:131], v1 offset:16384\n\t" \
  "v_mfma_f32_16x16x32_bf16 v[20:23], a[92:95], v[104:107], v[20:23]\n\t" \
  "v_mfma_f32_16x16x32_bf16 v[24:27], a[164:167], v[104:107], v[24:27]\n\t" \
  "ds_read_b128 v[132:135], v1 offset:18432\n\t" \
  "v_mfma_f32_16x16x32_bf16 v[28:31], a[236:239], v[104:107], v[28:31]\n\t" \
  "s_add_i32 s62, s48, s42\n\t" \
  "s_add_i32 s62, s62, 1\n\t" \
  "s_waitcnt lgkmcnt(5)\n\t" \
  "v_mfma_f32_16x16x32_bf16 v[16:19], a[24:27], v[112:115], v[16:19]\n\t" \
  "ds_read_b128 v[136:139], v2 offset:16384\n\t" \
  "v_mfma_f32_16x16x32_bf16 v[20:23], a[96:99], v[112:115], v[20:23]\n\t" \
  "s_mul_i32 s63, s62, 43\n\t" \
  "s_lshr_b32 s63, s63, 7\n\t" \
  "v_mfma_f32_16x16x32_bf16 v[24:27], a[168:171], v[112:115], v[24:27]\n\t" \
  "ds_read_b128 v[140:143], v2 offset:18432\n\t" \
  "v_mfma_f32_16x16x32_bf16 v[28:31], a[240:243], v[112:115], v[28:31]\n\t" \
  "s_mul_i32 s57, s62, 688128\n\t" \
  "s_lshl_b32 s60, s63, 15\n\t" \
  "s_waitcnt lgkmcnt(5)\n\t" \
  "v_mfma_f32_16x16x32_bf16 v[16:19], a[28:31], v[120:123], v[16:19]\n\t" \
  "ds_read_b128 v[144:147], v1 offset:20480\n\t" \
  "v_mfma_f32_16x16x32_bf16 v[20:23], a[100:103], v[120:123], v[20:23]\n\t" \
  "s_add_u32 s57, s57, s60\n\t" \
  "s_lshr_b32 s60, s88, s63\n\t" \
  "v_mfma_f32_16x16x32_bf16 v[24:27], a[172:175], v[120:123], v[24:27]\n\t" \
  "ds_read_b128 v[148:151], v1 offset:22528\n\t" \
  "v_mfma_f32_16x16x32_bf16 v[28:31], a[244:247], v[120:123], v[28:31]\n\t" \
  "s_and_b32 s60, s60, 1\n\t" \
  "s_lshl_b32 s60, s60, 13\n\t" \
  "s_waitcnt lgkmcnt(5)\n\t" \
  "v_mfma_f32_16x16x32_bf16 v[16:19], a[32:35], v[128:131], v[16:19]\n\t" \
  "ds_read_b128 v[152:155], v2 offset:20480\n\t" \
  "v_mfma_f32_16x16x32_bf16 v[20:23], a[104:107], v[128:131], v[20:23]\n\t" \
  "s_add_u32 s57, s57, s60\n\t" \
  "s_add_u32 s57, s57, 656896\n\t" \
  "v_mfma_f32_16x16x32_bf16 v[24:27], a[176:179], v[128:131], v[24:27]\n\t" \
  "ds_read_b128 v[156:159], v2 offset:22528\n\t" \
  "v_mfma_f32_16x16x32_bf16 v[28:31], a[248:251], v[128:131], v[28:31]\n\t" \
  "s_cmp_lt_i32 s62, 1\n\t" \
  "s_cselect_b32 s57, 0xf0000000, s57\n\t" \
  "s_waitcnt lgkmcnt(5)\n\t" \
  "v_mfma_f32_16x16x32_bf16 v[16:19], a[36:39], v[136:139], v[16:19]\n\t" \
  "ds_read_b128 v[160:163], v1 offset:24576\n\t" \
  "v_mfma_f32_16x16x32_bf16 v[20:23], a[108:111], v[136:139], v[20:23]\n\t" \
  "s_cmp_lg_u32 s40, 0\n\t" \
  "s_cselect_b32 s57, 0xf0000000, s57\n\t" \
  "v_mfma_f32_16x16x32_bf16 v[24:27], a[180:183], v[136:139], v[24:27]\n\t" \
  "ds_read_b128 v[164:167], v1 offset:26624\n\t" \
  "v_mfma_f32_16x16x32_bf16 v[28:31], a[252:255], v[136:139], v[28:31]\n\t" \
  "s_waitcnt lgkmcnt(5)\n\t" \
  "v_mfma_f32_16x16x32_bf16 v[16:19], a[40:43], v[144:147], v[16:19]\n\t" \
  "ds_read_b128 v[168:171], v2 offset:24576\n\t" \
  "v_mfma_f32_16x16x32_bf16 v[20:23], a[112:115], v[144:147], v[20:23]\n\t" \
  "v_mfma_f32_16x16x32_bf16 v[24:27], a[184:187], v[144:147], v[24:27]\n\t" \
  "ds_read_b128 v[172:175], v2 offset:26624\n\t" \
  "v_mfma_f32_16x16x32_bf16 v[28:31], v[224:227], v[144:147], v[28:31]\n\t" \
  "s_waitcnt lgkmcnt(5)\n\t" \
  "v_mfma_f32_16x16x32_bf16 v[16:19], a[44:47], v[152:155], v[16:19]\n\t" \
  "ds_read_b128 v[176:179], v1 offset:28672\n\t" \
  "v_mfma_f32_16x16x32_bf16 v[20:23], a[116:119], v[152:155], v[20:23]\n\t" \
  "v_mfma_f32_16x16x32_bf16 v[24:27], a[188:191], v[152:155], v[24:27]\n\t" \
  "ds_read_b128 v[180:183], v1 offset:30720\n\t" \
  "v_mfma_f32_16x16x32_bf16 v[28:31], v[228:231], v[152:155], v[28:31]\n\t" \
  "s_waitcnt lgkmcnt(5)\n\t" \
  "v_mfma_f32_16x16x32_bf16 v[16:19], a[48:51], v[160:163], v[16:19]\n\t" \
  "ds_read_b128 v[184:187], v2 offset:28672\n\t" \
  "v_mfma_f32_16x16x32_bf16 v[20:23], a[120:123], v[160:163], v[20:23]\n\t" \
  "v_mfma_f32_16x16x32_bf16 v[24:27], a[192:195], v[160:163], v[24:27]\n\t" \
  "ds_read_b128 v[188:191], v2 offset:30720\n\t" \
  "v_mfma_f32_16x16x32_bf16 v[28:31], v[232:235], v[160:163], v[28:31]\n\t" \
  "s_waitcnt lgkmcnt(5)\n\t" \
  "v_mfma_f32_16x16x32_bf16 v[16:19], a[52:55], v[168:171], v[16:19]\n\t" \
  "ds_read_b128 v[192:195], v1 offset:32768\n\t" \
  "v_mfma_f32_16x16x32_bf16 v[20:23], a[124:127], v[168:171], v[20:23]\n\t" \
  "v_mfma_f32_16x16x32_bf16 v[24:27], a[196:199], v[168:171], v[24:27]\n\t" \
  "ds_read_b128 v[196:199], v1 offset:34816\n\t" \
  "v_mfma_f32_16x16x32_bf16 v[28:31], v[236:239], v[168:171], v[28:31]\n\t" \
  "s_waitcnt lgkmcnt(5)\n\t" \
  "v_mfma_f32_16x16x32_bf16 v[16:19], a[56:59], v[176:179], v[16:19]\n\t" \
  "ds_read_b128 v[200:203], v2 offset:32768\n\t" \
  "v_mfma_f32_16x16x32_bf16 v[20:23], a[128:131], v[176:179], v[20:23]\n\t" \
  "v_mfma_f32_16x16x32_bf16 v[24:27], a[200:203], v[176:179], v[24:27]\n\t" \
  "ds_read_b128 v[204:207], v2 offset:34816\n\t" \
  "v_mfma_f32_16x16x32_bf16 v[28:31], v[240:243], v[176:179], v[28:31]\n\t" \
  "s_waitcnt lgkmcnt(5)\n\t" \
  "v_mfma_f32_16x16x32_bf16 v[16:19], a[60:63], v[184:187], v[16:19]\n\t" \
  "v_mfma_f32_16x16x32_bf16 v[20:23], a[132:135], v[184:187], v[20:23]\n\t" \
  "buffer_load_dword v4, v7, %[X], 0 offen sc1\n\t" \
  "v_mfma_f32_16x16x32_bf16 v[24:27], a[204:207], v[184:187], v[24:27]\n\t" \
  "v_mfma_f32_16x16x32_bf16 v[28:31], v[244:247], v[184:187], v[28:31]\n\t" \
  "s_waitcnt lgkmcnt(3)\n\t" \
  "v_mfma_f32_16x16x32_bf16 v[16:19], a[64:67], v[192:195], v[16:19]\n\t" \
  "v_xor_b32 v1, 0x10000, v1\n\t" \
  "v_xor_b32 v2, 0x10000, v2\n\t" \
  "v_mfma_f32_16x16x32_bf16 v[20:23], a[136:139], v[192:195], v[20:23]\n\t" \
  "v_mfma_f32_16x16x32_bf16 v[24:27], a[208:211], v[192:195], v[24:27]\n\t" \
  "v_mfma_f32_16x16x32_bf16 v[28:31], v[248:251], v[192:195], v[28:31]\n\t" \
  "s_waitcnt lgkmcnt(1)\n\t" \
  "v_mfma_f32_16x16x32_bf16 v[16:19], a[68:71], v[200:203], v[16:19]\n\t" \
  "v_mfma_f32_16x16x32_bf16 v[20:23], a[140:143], v[200:203], v[20:23]\n\t" \
  "v_mfma_f32_16x16x32_bf16 v[24:27], a[212:215], v[200:203], v[24:27]\n\t" \
  "v_mfma_f32_16x16x32_bf16 v[28:31], v[252:255], v[200:203], v[28:31]\n\t" \
  "v_mfma_f32_16x16x32_bf16 v[32:35], a[0:3], v[68:71], 0\n\t" \
  "v_cvt_pk_bf16_f32 v48, v16, v17\n\t" \
  "v_cvt_pk_bf16_f32 v49, v18, v19\n\t" \
  "v_mfma_f32_16x16x32_bf16 v[36:39], a[72:75], v[68:71], 0\n\t" \
  "v_cvt_pk_bf16_f32 v50, v20, v21\n\t" \
  "v_cvt_pk_bf16_f32 v51, v22, v23\n\t" \
  "v_mfma_f32_16x16x32_bf16 v[32:35], a[4:7], v[76:79], v[32:35]\n\t" \
  "v_cvt_pk_bf16_f32 v52, v24, v25\n\t" \
  "v_cvt_pk_bf16_f32 v53, v26, v27\n\t" \
  "v_mfma_f32_16x16x32_bf16 v[36:39], a[76:79], v[76:79], v[36:39]\n\t" \
  "v_cvt_pk_bf16_f32 v54, v28, v29\n\t" \
  "v_cvt_pk_bf16_f32 v55, v30, v31\n\t" \
  "v_mfma_f32_16x16x32_bf16 v[32:35], a[8:11], v[84:87], v[32:35]\n\t" \
  "buffer_store_dwordx4 v[48:51], v0, %[X], s50 offen offset:0 sc1\n\t" \
  "v_mfma_f32_16x16x32_bf16 v[36:39], a[80:83], v[84:87], v[36:39]\n\t" \
  "v_mfma_f32_16x16x32_bf16 v[32:35], a[12:15], v[92:95], v[32:35]\n\t" \
  "v_mfma_f32_16x16x32_bf16 v[36:39], a[84:87], v[92:95], v[36:39]\n\t" \
  "v_mfma_f32_16x16x32_bf16 v[32:35], a[16:19], v[100:103], v[32:35]\n\t" \
  "v_mfma_f32_16x16x32_bf16 v[36:39], a[88:91], v[100:103], v[36:39]\n\t" \
  "v_mfma_f32_16x16x32_bf16 v[32:35], a[20:23], v[108:111], v[32:35]\n\t" \
  "v_mfma_f32_16x16x32_bf16 v[36:39], a[92:95], v[108:111], v[36:39]\n\t" \
  "v_mfma_f32_16x16x32_bf16 v[32:35], a[24:27], v[116:119], v[32:35]\n\t" \
  "v_mfma_f32_16x16x32_bf16 v[36:39], a[96:99], v[116:119], v[36:39]\n\t" \
  "v_mfma_f32_16x16x32_bf16 v[32:35], a[28:31], v[124:127], v[32:35]\n\t" \
  "v_mfma_f32_16x16x32_bf16 v[36:39], a[100:103], v[124:127], v[36:39]\n\t" \
  "s_waitcnt vmcnt(1)\n\t" \
  "v_cmp_eq_u32 vcc, 0, v4\n\t" \
  "v_mfma_f32_16x16x32_bf16 v[32:35], a[32:35], v[132:135], v[32:35]\n\t" \
  "v_mfma_f32_16x16x32_bf16 v[36:39], a[104:107], v[132:135], v[36:39]\n\t" \
  "s_cmp_eq_u64 vcc, 0\n\t" \
  "s_cselect_b32 s60, 1, 0\n\t" \
  "v_mov_b32 v4, s60\n\t" \
  "v_mfma_f32_16x16x32_bf16 v[32:35], a[36:39], v[140:143], v[32:35]\n\t" \
  "ds_write_b32 v5, v4\n\t" \
  "v_mfma_f32_16x16x32_bf16 v[36:39], a[108:111], v[140:143], v[36:39]\n\t" \
  "v_mfma_f32_16x16x32_bf16 v[32:35], a[40:43], v[148:151], v[32:35]\n\t" \
  "v_mfma_f32_16x16x32_bf16 v[36:39], a[112:115], v[148:151], v[36:39]\n\t" \
  "buffer_store_dwordx4 v[52:55], v0, %[X], s50 offen offset:1024 sc1\n\t" \
  "v_mfma_f32_16x16x32_bf16 v[32:35], a[44:47], v[156:159], v[32:35]\n\t" \
  "v_mfma_f32_16x16x32_bf16 v[36:39], a[116:119], v[156:159], v[36:39]\n\t" \
  "s_waitcnt lgkmcnt(0)\n\t" \
  "s_barrier\n\t" \
  "v_mfma_f32_16x16x32_bf16 v[32:35], a[48:51], v[164:167], v[32:35]\n\t" \
  "ds_read_b32 v4, v6 offset:40959\n\t" \
  "v_mfma_f32_16x16x32_bf16 v[36:39], a[120:123], v[164:167], v[36:39]\n\t" \
  "v_mfma_f32_16x16x32_bf16 v[32:35], a[52:55], v[172:175], v[32:35]\n\t" \
  "v_mfma_f32_16x16x32_bf16 v[36:39], a[124:127], v[172:175], v[36:39]\n\t" \
  "v_mfma_f32_16x16x32_bf16 v[32:35], a[56:59], v[180:183], v[32:35]\n\t" \
  "v_mfma_f32_16x16x32_bf16 v[36:39], a[128:131], v[180:183], v[36:39]\n\t" \
  "s_waitcnt lgkmcnt(0)\n\t" \
  "v_readfirstlane_b32 s60, v4\n\t" \
  "s_add_u32 s55, s83, s36\n\t" \
  "v_mfma_f32_16x16x32_bf16 v[32:35], a[60:63], v[188:191], v[32:35]\n\t" \
  "s_cmp_eq_u32 s48, 63\n\t" \
  "s_cselect_b32 s56, 0, s60\n\t" \
  "v_mfma_f32_16x16x32_bf16 v[36:39], a[132:135], v[188:191], v[36:39]\n\t" \
  "s_cmp_eq_u32 s56, 0\n\t" \
  "s_cselect_b32 s55, 0xf0000000, s55\n\t" \
  "v_mfma_f32_16x16x32_bf16 v[32:35], a[64:67], v[196:199], v[32:35]\n\t" \
  "v_mfma_f32_16x16x32_bf16 v[36:39], a[136:139], v[196:199], v[36:39]\n\t" \
  "v_mfma_f32_16x16x32_bf16 v[32:35], a[68:71], v[204:207], v[32:35]\n\t" \
  "s_add_u32 m0, s46, 0\n\t" \
  "s_add_u32 s47, s55, 0\n\t" \
  "buffer_load_dwordx4 v0, %[X], s47 offen sc1 lds\n\t" \
  "v_mfma_f32_16x16x32_bf16 v[36:39], a[140:143], v[204:207], v[36:39]\n\t" \
  "v_mfma_f32_16x16x32_bf16 v[40:43], a[144:147], v[68:71], 0\n\t" \
  "v_mfma_f32_16x16x32_bf16 v[44:47], a[216:219], v[68:71], 0\n\t" \
  "v_mfma_f32_16x16x32_bf16 v[40:43], a[148:151], v[76:79], v[40:43]\n\t" \
  "v_cvt_pk_bf16_f32 v56, v32, v33\n\t" \
  "v_cvt_pk_bf16_f32 v57, v34, v35\n\t" \
  "s_add_u32 m0, s46, 4096\n\t" \
  "s_add_u32 s47, s55, 4096\n\t" \
  "buffer_load_dwordx4 v0, %[X], s47 offen sc1 lds\n\t" \
  "v_mfma_f32_16x16x32_bf16 v[44:47], a[220:223], v[76:79], v[44:47]\n\t" \
  "v_cvt_pk_bf16_f32 v58, v36, v37\n\t" \
  "v_cvt_pk_bf16_f32 v59, v38, v39\n\t" \
  "v_mfma_f32_16x16x32_bf16 v[40:43], a[152:155], v[84:87], v[40:43]\n\t" \
  "v_mfma_f32_16x16x32_bf16 v[44:47], a[224:227], v[84:87], v[44:47]\n\t" \
  "buffer_store_dwordx4 v[56:59], v0, %[X], s51 offen offset:0 sc1\n\t" \
  "v_mfma_f32_16x16x32_bf16 v[40:43], a[156:159], v[92:95], v[40:43]\n\t" \
  "s_add_u32 m0, s46, 8192\n\t" \
  "s_add_u32 s47, s55, 16384\n\t" \
  "buffer_load_dwordx4 v0, %[X], s47 offen sc1 lds\n\t" \
  "v_mfma_f32_16x16x32_bf16 v[44:47], a[228:231], v[92:95], v[44:47]\n\t" \
  "v_mfma_f32_16x16x32_bf16 v[40:43], a[160:163], v[100:103], v[40:43]\n\t" \
  "v_mfma_f32_16x16x32_bf16 v[44:47], a[232:235], v[100:103], v[44:47]\n\t" \
  "v_mfma_f32_16x16x32_bf16 v[40:43], a[164:167], v[108:111], v[40:43]\n\t" \
  "s_add_u32 m0, s46, 12288\n\t" \
  "s_add_u32 s47, s55, 20480\n\t" \
  "buffer_load_dwordx4 v0, %[X], s47 offen sc1 lds\n\t" \
  "v_mfma_f32_16x16x32_bf16 v[44:47], a[236:239], v[108:111], v[44:47]\n\t" \
  "v_mfma_f32_16x16x32_bf16 v[40:43], a[168:171], v[116:119], v[40:43]\n\t" \
  "v_mfma_f32_16x16x32_bf16 v[44:47], a[240:243], v[116:119], v[44:47]\n\t" \
  "v_mfma_f32_16x16x32_bf16 v[40:43], a[172:175], v[124:127], v[40:43]\n\t" \
  "s_add_u32 m0, s46, 16384\n\t" \
  "s_add_u32 s47, s55, 32768\n\t" \
  "buffer_load_dwordx4 v0, %[X], s47 offen sc1 lds\n\t" \
  "v_mfma_f32_16x16x32_bf16 v[44:47], a[244:247], v[124:127], v[44:47]\n\t" \
  "v_mfma_f32_16x16x32_bf16 v[40:43], a[176:179], v[132:135], v[40:43]\n\t" \
  "v_mfma_f32_16x16x32_bf16 v[44:47], a[248:251], v[132:135], v[44:47]\n\t" \
  "v_mfma_f32_16x16x32_bf16 v[40:43], a[180:183], v[140:143], v[40:43]\n\t" \
  "s_add_u32 m0, s46, 20480\n\t" \
  "s_add_u32 s47, s55, 36864\n\t" \
  "buffer_load_dwordx4 v0, %[X], s47 offen sc1 lds\n\t" \
  "v_mfma_f32_16x16x32_bf16 v[44:47], a[252:255], v[140:143], v[44:47]\n\t" \
  "v_mfma_f32_16x16x32_bf16 v[40:43], a[184:187], v[148:151], v[40:43]\n\t" \
  "v_mfma_f32_16x16x32_bf16 v[44:47], v[224:227], v[148:151], v[44:47]\n\t" \
  "v_mfma_f32_16x16x32_bf16 v[40:43], a[188:191], v[156:159], v[40:43]\n\t" \
  "s_add_u32 m0, s46, 24576\n\t" \
  "s_add_u32 s47, s55, 49152\n\t" \
  "buffer_load_dwordx4 v0, %[X], s47 offen sc1 lds\n\t" \
  "v_mfma_f32_16x16x32_bf16 v[44:47], v[228:231], v[156:159], v[44:47]\n\t" \
  "v_mfma_f32_16x16x32_bf16 v[40:43], a[192:195], v[164:167], v[40:43]\n\t" \
  "v_mfma_f32_16x16x32_bf16 v[44:47], v[232:235], v[164:167], v[44:47]\n\t" \
  "v_mfma_f32_16x16x32_bf16 v[40:43], a[196:199], v[172:175], v[40:43]\n\t" \
  "s_add_u32 m0, s46, 28672\n\t" \
  "s_add_u32 s47, s55, 53248\n\t" \
  "buffer_load_dwordx4 v0, %[X], s47 offen sc1 lds\n\t" \
  "v_mfma_f32_16x16x32_bf16 v[44:47], v[236:239], v[172:175], v[44:47]\n\t" \
  "v_mfma_f32_16x16x32_bf16 v[40:43], a[200:203], v[180:183], v[40:43]\n\t" \
  "v_mfma_f32_16x16x32_bf16 v[44:47], v[240:243], v[180:183], v[44:47]\n\t" \
  "v_mfma_f32_16x16x32_bf16 v[40:43], a[204:207], v[188:191], v[40:43]\n\t" \
  "s_add_u32 m0, s46, 32768\n\t" \
  "s_add_u32 s47, s55, 65536\n\t" \
  "buffer_load_dwordx4 v0, %[X], s47 offen sc1 lds\n\t" \
  "v_mfma_f32_16x16x32_bf16 v[44:47], v[244:247], v[188:191], v[44:47]\n\t" \
  "v_mfma_f32_16x16x32_bf16 v[40:43], a[208:211], v[196:199], v[40:43]\n\t" \
  "v_mfma_f32_16x16x32_bf16 v[44:47], v[248:251], v[196:199], v[44:47]\n\t" \
  "buffer_load_dword v8, off, %[X], s57 sc1\n\t" \
  "v_mfma_f32_16x16x32_bf16 v[40:43], a[212:215], v[204:207], v[40:43]\n\t" \
  "v_mfma_f32_16x16x32_bf16 v[44:47], v[252:255], v[204:207], v[44:47]\n\t" \
  "s_nop 4\n\t" \
  "v_cvt_pk_bf16_f32 v60, v40, v41\n\t" \
  "v_cvt_pk_bf16_f32 v61, v42, v43\n\t" \
  "s_nop 1\n\t" \
  "v_cvt_pk_bf16_f32 v62, v44, v45\n\t" \
  "v_cvt_pk_bf16_f32 v63, v46, v47\n\t" \
  "buffer_store_dwordx4 v[60:63], v0, %[X], s51 offen offset:1024 sc1\n\t" \
  "s_add_u32 s49, s82, s38\n\t" \
  "s_cmp_eq_u32 s48, 63\n\t" \
  "s_cbranch_scc1 LEND_%=\n\t" \
  "s_add_u32 s48, s48, 1\n\t" \
  "s_cmp_eq_u32 s56, 0\n\t" \
  "s_cbranch_scc1 LCOLD_%=\n\t" \
  "s_waitcnt vmcnt(0)\n\t" \
  "s_mov_b64 exec, 1\n\t" \
  "buffer_store_dword v6, off, %[X], s49 sc1\n\t" \
  "s_mov_b64 exec, -1\n\t" \
  "s_mov_b32 s49, 0xf0000000\n\t" \
  "s_cmp_lt_i32 s57, 0\n\t" \
  "s_cbranch_scc1 LTOP_%=\n\t" \
  "s_movk_i32 s63, 4000\n\t" \
  "s_branch LGW_%=u\n\t" \
  "LGW_%=:\n\t" \
  "s_sleep " TK_PSG "\n\t" \
  "buffer_load_dword v8, off, %[X], s57 sc1\n\t" \
  "s_waitcnt vmcnt(0)\n\t" \
  "LGW_%=u:\n\t" \
  "s_memrealtime s[52:53]\n\t" \
  "v_readfirstlane_b32 s60, v8\n\t" \
  "s_waitcnt lgkmcnt(0)\n\t" \
  "s_sub_u32 s63, s63, 1\n\t" \
  "s_cbranch_scc1 LTOP_%=\n\t" \
  "s_cmp_eq_u32 s60, -1\n\t" \
  "s_cbranch_scc1 LGW_%=\n\t" \
  "s_not_b32 s60, s60\n\t" \
  "s_lshl_b32 s61, s52, 8\n\t" \
  "s_sub_u32 s60, s60, s61\n\t" \
  "s_ashr_i32 s60, s60, 8\n\t" \
  "s_sub_i32 s60, s60, " TK_QXW "\n\t" \
  "s_min_i32 s60, s60, 400\n\t" \
  "s_add_u32 s54, s52, s60\n\t" \
  "LGW_%=w:\n\t" \
  "s_sleep 1\n\t" \
  "s_memrealtime s[52:53]\n\t" \
  "s_waitcnt lgkmcnt(0)\n\t" \
  "s_sub_i32 s53, s52, s54\n\t" \
  "s_cmp_lt_i32 s53, 0\n\t" \
  "s_cbranch_scc1 LGW_%=w\n\t" \
  "s_branch LTOP_%=\n\t" \
  "LEND_%=:\n\t" \
  "s_waitcnt vmcnt(0)\n\t" \
  "s_mov_b64 exec, 1\n\t" \
  "buffer_store_dword v6, off, %[X], s49 sc1\n\t" \
  "s_mov_b64 exec, -1\n\t" \
  "s_mov_b32 s49, 0xf0000000\n\t" \
  ""
#define QV_ASM \
  "v_lshrrev_b32 v16, 7, v0\n\t" \
  "v_lshlrev_b32 v16, 13, v16\n\t" \
  "v_lshrrev_b32 v19, 2, v0\n\t" \
  "v_and_b32 v19, 64, v19\n\t" \
  "v_xor_b32 v19, v19, v0\n\t" \
  "v_and_b32 v19, 0x70, v19\n\t" \
  "v_add_u32 v16, v16, v19\n\t" \
  "s_mul_i32 s63, s40, 40960\n\t" \
  "v_and_b32 v19, 0x300, v0\n\t" \
  "v_lshlrev_b32 v17, 2, v19\n\t" \
  "v_and_b32 v19, 0xc0, v0\n\t" \
  "v_lshl_add_u32 v17, v19, 1, v17\n\t" \
  "v_and_b32 v19, 0x30, v0\n\t" \
  "v_lshrrev_b32 v19, 1, v19\n\t" \
  "v_add3_u32 v17, v17, v19, s63\n\t" \
  "v_lshrrev_b32 v19, 1, v0\n\t" \
  "v_and_b32 v19, 64, v19\n\t" \
  "v_add_u32 v17, v17, v19\n\t" \
  "v_xor_b32 v18, 64, v17\n\t" \
  "s_add_u32 m0, s63, 0\n\t" \
  "s_mov_b32 s60, 0\n\t" \
  "buffer_load_dwordx4 v16, %[W], s60 offen lds\n\t" \
  "s_add_u32 m0, s63, 1024\n\t" \
  "s_mov_b32 s60, 65536\n\t" \
  "buffer_load_dwordx4 v16, %[W], s60 offen lds\n\t" \
  "s_add_u32 m0, s63, 2048\n\t" \
  "s_mov_b32 s60, 131072\n\t" \
  "buffer_load_dwordx4 v16, %[W], s60 offen lds\n\t" \
  "s_add_u32 m0, s63, 3072\n\t" \
  "s_mov_b32 s60, 196608\n\t" \
  "buffer_load_dwordx4 v16, %[W], s60 offen lds\n\t" \
  "s_add_u32 m0, s63, 4096\n\t" \
  "s_mov_b32 s60, 262144\n\t" \
  "buffer_load_dwordx4 v16, %[W], s60 offen lds\n\t" \
  "s_add_u32 m0, s63, 5120\n\t" \
  "s_mov_b32 s60, 327680\n\t" \
  "buffer_load_dwordx4 v16, %[W], s60 offen lds\n\t" \
  "s_add_u32 m0, s63, 6144\n\t" \
  "s_mov_b32 s60, 393216\n\t" \
  "buffer_load_dwordx4 v16, %[W], s60 offen lds\n\t" \
  "s_add_u32 m0, s63, 7168\n\t" \
  "s_mov_b32 s60, 458752\n\t" \
  "buffer_load_dwordx4 v16, %[W], s60 offen lds\n\t" \
  "s_add_u32 m0, s63, 8192\n\t" \
  "s_mov_b32 s60, 524288\n\t" \
  "buffer_load_dwordx4 v16, %[W], s60 offen lds\n\t" \
  "s_add_u32 m0, s63, 9216\n\t" \
  "s_mov_b32 s60, 589824\n\t" \
  "buffer_load_dwordx4 v16, %[W], s60 offen lds\n\t" \
  "s_add_u32 m0, s63, 10240\n\t" \
  "s_mov_b32 s60, 655360\n\t" \
  "buffer_load_dwordx4 v16, %[W], s60 offen lds\n\t" \
  "s_add_u32 m0, s63, 11264\n\t" \
  "s_mov_b32 s60, 720896\n\t" \
  "buffer_load_dwordx4 v16, %[W], s60 offen lds\n\t" \
  "s_add_u32 m0, s63, 12288\n\t" \
  "s_mov_b32 s60, 786432\n\t" \
  "buffer_load_dwordx4 v16, %[W], s60 offen lds\n\t" \
  "s_add_u32 m0, s63, 13312\n\t" \
  "s_mov_b32 s60, 851968\n\t" \
  "buffer_load_dwordx4 v16, %[W], s60 offen lds\n\t" \
  "s_add_u32 m0, s63, 14336\n\t" \
  "s_mov_b32 s60, 917504\n\t" \
  "buffer_load_dwordx4 v16, %[W], s60 offen lds\n\t" \
  "s_add_u32 m0, s63, 15360\n\t" \
  "s_mov_b32 s60, 983040\n\t" \
  "buffer_load_dwordx4 v16, %[W], s60 offen lds\n\t" \
  "s_add_u32 m0, s63, 16384\n\t" \
  "s_mov_b32 s60, 1048576\n\t" \
  "buffer_load_dwordx4 v16, %[W], s60 offen lds\n\t" \
  "s_add_u32 m0, s63, 17408\n\t" \
  "s_mov_b32 s60, 1114112\n\t" \
  "buffer_load_dwordx4 v16, %[W], s60 offen lds\n\t" \
  "s_add_u32 m0, s63, 18432\n\t" \
  "s_mov_b32 s60, 1179648\n\t" \
  "buffer_load_dwordx4 v16, %[W], s60 offen lds\n\t" \
  "s_add_u32 m0, s63, 19456\n\t" \
  "s_mov_b32 s60, 1245184\n\t" \
  "buffer_load_dwordx4 v16, %[W], s60 offen lds\n\t" \
  "s_add_u32 m0, s63, 20480\n\t" \
  "s_mov_b32 s60, 1310720\n\t" \
  "buffer_load_dwordx4 v16, %[W], s60 offen lds\n\t" \
  "s_add_u32 m0, s63, 21504\n\t" \
  "s_mov_b32 s60, 1376256\n\t" \
  "buffer_load_dwordx4 v16, %[W], s60 offen lds\n\t" \
  "s_add_u32 m0, s63, 22528\n\t" \
  "s_mov_b32 s60, 1441792\n\t" \
  "buffer_load_dwordx4 v16, %[W], s60 offen lds\n\t" \
  "s_add_u32 m0, s63, 23552\n\t" \
  "s_mov_b32 s60, 1507328\n\t" \
  "buffer_load_dwordx4 v16, %[W], s60 offen lds\n\t" \
  "s_add_u32 m0, s63, 24576\n\t" \
  "s_mov_b32 s60, 1572864\n\t" \
  "buffer_load_dwordx4 v16, %[W], s60 offen lds\n\t" \
  "s_add_u32 m0, s63, 25600\n\t" \
  "s_mov_b32 s60, 1638400\n\t" \
  "buffer_load_dwordx4 v16, %[W], s60 offen lds\n\t" \
  "s_add_u32 m0, s63, 26624\n\t" \
  "s_mov_b32 s60, 1703936\n\t" \
  "buffer_load_dwordx4 v16, %[W], s60 offen lds\n\t" \
  "s_add_u32 m0, s63, 27648\n\t" \
  "s_mov_b32 s60, 1769472\n\t" \
  "buffer_load_dwordx4 v16, %[W], s60 offen lds\n\t" \
  "s_add_u32 m0, s63, 28672\n\t" \
  "s_mov_b32 s60, 1835008\n\t" \
  "buffer_load_dwordx4 v16, %[W], s60 offen lds\n\t" \
  "s_add_u32 m0, s63, 29696\n\t" \
  "s_mov_b32 s60, 1900544\n\t" \
  "buffer_load_dwordx4 v16, %[W], s60 offen lds\n\t" \
  "s_add_u32 m0, s63, 30720\n\t" \
  "s_mov_b32 s60, 1966080\n\t" \
  "buffer_load_dwordx4 v16, %[W], s60 offen lds\n\t" \
  "s_add_u32 m0, s63, 31744\n\t" \
  "s_mov_b32 s60, 2031616\n\t" \
  "buffer_load_dwordx4 v16, %[W], s60 offen lds\n\t" \
  "s_add_u32 m0, s63, 32768\n\t" \
  "s_mov_b32 s60, 2097152\n\t" \
  "buffer_load_dwordx4 v16, %[W], s60 offen lds\n\t" \
  "s_add_u32 m0, s63, 33792\n\t" \
  "s_mov_b32 s60, 2162688\n\t" \
  "buffer_load_dwordx4 v16, %[W], s60 offen lds\n\t" \
  "s_add_u32 m0, s63, 34816\n\t" \
  "s_mov_b32 s60, 2228224\n\t" \
  "buffer_load_dwordx4 v16, %[W], s60 offen lds\n\t" \
  "s_add_u32 m0, s63, 35840\n\t" \
  "s_mov_b32 s60, 2293760\n\t" \
  "buffer_load_dwordx4 v16, %[W], s60 offen lds\n\t" \
  "s_add_u32 m0, s63, 36864\n\t" \
  "s_mov_b32 s60, 2359296\n\t" \
  "buffer_load_dwordx4 v16, %[W], s60 offen lds\n\t" \
  "s_add_u32 m0, s63, 37888\n\t" \
  "s_mov_b32 s60, 2424832\n\t" \
  "buffer_load_dwordx4 v16, %[W], s60 offen lds\n\t" \
  "s_add_u32 m0, s63, 38912\n\t" \
  "s_mov_b32 s60, 2490368\n\t" \
  "buffer_load_dwordx4 v16, %[W], s60 offen lds\n\t" \
  "s_add_u32 m0, s63, 39936\n\t" \
  "s_mov_b32 s60, 2555904\n\t" \
  "buffer_load_dwordx4 v16, %[W], s60 offen lds\n\t" \
  "s_waitcnt vmcnt(32)\n\t" \
  "ds_read_b64_tr_b16 a[0:1], v17 offset:0\n\t" \
  "ds_read_b64_tr_b16 a[2:3], v17 offset:512\n\t" \
  "ds_read_b64_tr_b16 a[4:5], v17 offset:4096\n\t" \
  "ds_read_b64_tr_b16 a[6:7], v17 offset:4608\n\t" \
  "ds_read_b64_tr_b16 a[72:73], v17 offset:32\n\t" \
  "ds_read_b64_tr_b16 a[74:75], v17 offset:544\n\t" \
  "ds_read_b64_tr_b16 a[76:77], v17 offset:4128\n\t" \
  "ds_read_b64_tr_b16 a[78:79], v17 offset:4640\n\t" \
  "ds_read_b64_tr_b16 a[144:145], v18 offset:0\n\t" \
  "ds_read_b64_tr_b16 a[146:147], v18 offset:512\n\t" \
  "ds_read_b64_tr_b16 a[148:149], v18 offset:4096\n\t" \
  "ds_read_b64_tr_b16 a[150:151], v18 offset:4608\n\t" \
  "ds_read_b64_tr_b16 a[216:217], v18 offset:32\n\t" \
  "ds_read_b64_tr_b16 a[218:219], v18 offset:544\n\t" \
  "ds_read_b64_tr_b16 a[220:221], v18 offset:4128\n\t" \
  "ds_read_b64_tr_b16 a[222:223], v18 offset:4640\n\t" \
  "s_waitcnt lgkmcnt(0)\n\t" \
  "s_add_u32 m0, s63, 0\n\t" \
  "s_mov_b32 s60, 2621440\n\t" \
  "buffer_load_dwordx4 v16, %[W], s60 offen lds\n\t" \
  "s_add_u32 m0, s63, 1024\n\t" \
  "s_mov_b32 s60, 2686976\n\t" \
  "buffer_load_dwordx4 v16, %[W], s60 offen lds\n\t" \
  "s_add_u32 m0, s63, 2048\n\t" \
  "s_mov_b32 s60, 2752512\n\t" \
  "buffer_load_dwordx4 v16, %[W], s60 offen lds\n\t" \
  "s_add_u32 m0, s63, 3072\n\t" \
  "s_mov_b32 s60, 2818048\n\t" \
  "buffer_load_dwordx4 v16, %[W], s60 offen lds\n\t" \
  "s_add_u32 m0, s63, 4096\n\t" \
  "s_mov_b32 s60, 2883584\n\t" \
  "buffer_load_dwordx4 v16, %[W], s60 offen lds\n\t" \
  "s_add_u32 m0, s63, 5120\n\t" \
  "s_mov_b32 s60, 2949120\n\t" \
  "buffer_load_dwordx4 v16, %[W], s60 offen lds\n\t" \
  "s_add_u32 m0, s63, 6144\n\t" \
  "s_mov_b32 s60, 3014656\n\t" \
  "buffer_load_dwordx4 v16, %[W], s60 offen lds\n\t" \
  "s_add_u32 m0, s63, 7168\n\t" \
  "s_mov_b32 s60, 3080192\n\t" \
  "buffer_load_dwordx4 v16, %[W], s60 offen lds\n\t" \
  "s_waitcnt vmcnt(32)\n\t" \
  "ds_read_b64_tr_b16 a[8:9], v17 offset:8192\n\t" \
  "ds_read_b64_tr_b16 a[10:11], v17 offset:8704\n\t" \
  "ds_read_b64_tr_b16 a[12:13], v17 offset:12288\n\t" \
  "ds_read_b64_tr_b16 a[14:15], v17 offset:12800\n\t" \
  "ds_read_b64_tr_b16 a[80:81], v17 offset:8224\n\t" \
  "ds_read_b64_tr_b16 a[82:83], v17 offset:8736\n\t" \
  "ds_read_b64_tr_b16 a[84:85], v17 offset:12320\n\t" \
  "ds_read_b64_tr_b16 a[86:87], v17 offset:12832\n\t" \
  "ds_read_b64_tr_b16 a[152:153], v18 offset:8192\n\t" \
  "ds_read_b64_tr_b16 a[154:155], v18 offset:8704\n\t" \
  "ds_read_b64_tr_b16 a[156:157], v18 offset:12288\n\t" \
  "ds_read_b64_tr_b16 a[158:159], v18 offset:12800\n\t" \
  "ds_read_b64_tr_b16 a[224:225], v18 offset:8224\n\t" \
  "ds_read_b64_tr_b16 a[226:227], v18 offset:8736\n\t" \
  "ds_read_b64_tr_b16 a[228:229], v18 offset:12320\n\t" \
  "ds_read_b64_tr_b16 a[230:231], v18 offset:12832\n\t" \
  "s_waitcnt lgkmcnt(0)\n\t" \
  "s_add_u32 m0, s63, 8192\n\t" \
  "s_mov_b32 s60, 3145728\n\t" \
  "buffer_load_dwordx4 v16, %[W], s60 offen lds\n\t" \
  "s_add_u32 m0, s63, 9216\n\t" \
  "s_mov_b32 s60, 3211264\n\t" \
  "buffer_load_dwordx4 v16, %[W], s60 offen lds\n\t" \
  "s_add_u32 m0, s63, 10240\n\t" \
  "s_mov_b32 s60, 3276800\n\t" \
  "buffer_load_dwordx4 v16, %[W], s60 offen lds\n\t" \
  "s_add_u32 m0, s63, 11264\n\t" \
  "s_mov_b32 s60, 3342336\n\t" \
  "buffer_load_dwordx4 v16, %[W], s60 offen lds\n\t" \
  "s_add_u32 m0, s63, 12288\n\t" \
  "s_mov_b32 s60, 3407872\n\t" \
  "buffer_load_dwordx4 v16, %[W], s60 offen lds\n\t" \
  "s_add_u32 m0, s63, 13312\n\t" \
  "s_mov_b32 s60, 3473408\n\t" \
  "buffer_load_dwordx4 v16, %[W], s60 offen lds\n\t" \
  "s_add_u32 m0, s63, 14336\n\t" \
  "s_mov_b32 s60, 3538944\n\t" \
  "buffer_load_dwordx4 v16, %[W], s60 offen lds\n\t" \
  "s_add_u32 m0, s63, 15360\n\t" \
  "s_mov_b32 s60, 3604480\n\t" \
  "buffer_load_dwordx4 v16, %[W], s60 offen lds\n\t" \
  "s_waitcnt vmcnt(32)\n\t" \
  "ds_read_b64_tr_b16 a[16:17], v17 offset:16384\n\t" \
  "ds_read_b64_tr_b16 a[18:19], v17 offset:16896\n\t" \
  "ds_read_b64_tr_b16 a[20:21], v17 offset:20480\n\t" \
  "ds_read_b64_tr_b16 a[22:23], v17 offset:20992\n\t" \
  "ds_read_b64_tr_b16 a[88:89], v17 offset:16416\n\t" \
  "ds_read_b64_tr_b16 a[90:91], v17 offset:16928\n\t" \
  "ds_read_b64_tr_b16 a[92:93], v17 offset:20512\n\t" \
  "ds_read_b64_tr_b16 a[94:95], v17 offset:21024\n\t" \
  "ds_read_b64_tr_b16 a[160:161], v18 offset:16384\n\t" \
  "ds_read_b64_tr_b16 a[162:163], v18 offset:16896\n\t" \
  "ds_read_b64_tr_b16 a[164:165], v18 offset:20480\n\t" \
  "ds_read_b64_tr_b16 a[166:167], v18 offset:20992\n\t" \
  "ds_read_b64_tr_b16 a[232:233], v18 offset:16416\n\t" \
  "ds_read_b64_tr_b16 a[234:235], v18 offset:16928\n\t" \
  "ds_read_b64_tr_b16 a[236:237], v18 offset:20512\n\t" \
  "ds_read_b64_tr_b16 a[238:239], v18 offset:21024\n\t" \
  "s_waitcnt lgkmcnt(0)\n\t" \
  "s_add_u32 m0, s63, 16384\n\t" \
  "s_mov_b32 s60, 3670016\n\t" \
  "buffer_load_dwordx4 v16, %[W], s60 offen lds\n\t" \
  "s_add_u32 m0, s63, 17408\n\t" \
  "s_mov_b32 s60, 3735552\n\t" \
  "buffer_load_dwordx4 v16, %[W], s60 offen lds\n\t" \
  "s_add_u32 m0, s63, 18432\n\t" \
  "s_mov_b32 s60, 3801088\n\t" \
  "buffer_load_dwordx4 v16, %[W], s60 offen lds\n\t" \
  "s_add_u32 m0, s63, 19456\n\t" \
  "s_mov_b32 s60, 3866624\n\t" \
  "buffer_load_dwordx4 v16, %[W], s60 offen lds\n\t" \
  "s_add_u32 m0, s63, 20480\n\t" \
  "s_mov_b32 s60, 3932160\n\t" \
  "buffer_load_dwordx4 v16, %[W], s60 offen lds\n\t" \
  "s_add_u32 m0, s63, 21504\n\t" \
  "s_mov_b32 s60, 3997696\n\t" \
  "buffer_load_dwordx4 v16, %[W], s60 offen lds\n\t" \
  "s_add_u32 m0, s63, 22528\n\t" \
  "s_mov_b32 s60, 4063232\n\t" \
  "buffer_load_dwordx4 v16, %[W], s60 offen lds\n\t" \
  "s_add_u32 m0, s63, 23552\n\t" \
  "s_mov_b32 s60, 4128768\n\t" \
  "buffer_load_dwordx4 v16, %[W], s60 offen lds\n\t" \
  "s_waitcnt vmcnt(32)\n\t" \
  "ds_read_b64_tr_b16 a[24:25], v17 offset:24576\n\t" \
  "ds_read_b64_tr_b16 a[26:27], v17 offset:25088\n\t" \
  "ds_read_b64_tr_b16 a[28:29], v17 offset:28672\n\t" \
  "ds_read_b64_tr_b16 a[30:31], v17 offset:29184\n\t" \
  "ds_read_b64_tr_b16 a[96:97], v17 offset:24608\n\t" \
  "ds_read_b64_tr_b16 a[98:99], v17 offset:25120\n\t" \
  "ds_read_b64_tr_b16 a[100:101], v17 offset:28704\n\t" \
  "ds_read_b64_tr_b16 a[102:103], v17 offset:29216\n\t" \
  "ds_read_b64_tr_b16 a[168:169], v18 offset:24576\n\t" \
  "ds_read_b64_tr_b16 a[170:171], v18 offset:25088\n\t" \
  "ds_read_b64_tr_b16 a[172:173], v18 offset:28672\n\t" \
  "ds_read_b64_tr_b16 a[174:175], v18 offset:29184\n\t" \
  "ds_read_b64_tr_b16 a[240:241], v18 offset:24608\n\t" \
  "ds_read_b64_tr_b16 a[242:243], v18 offset:25120\n\t" \
  "ds_read_b64_tr_b16 a[244:245], v18 offset:28704\n\t" \
  "ds_read_b64_tr_b16 a[246:247], v18 offset:29216\n\t" \
  "s_waitcnt lgkmcnt(0)\n\t" \
  "s_add_u32 m0, s63, 24576\n\t" \
  "s_mov_b32 s60, 4194304\n\t" \
  "buffer_load_dwordx4 v16, %[W], s60 offen lds\n\t" \
  "s_add_u32 m0, s63, 25600\n\t" \
  "s_mov_b32 s60, 4259840\n\t" \
  "buffer_load_dwordx4 v16, %[W], s60 offen lds\n\t" \
  "s_add_u32 m0, s63, 26624\n\t" \
  "s_mov_b32 s60, 4325376\n\t" \
  "buffer_load_dwordx4 v16, %[W], s60 offen lds\n\t" \
  "s_add_u32 m0, s63, 27648\n\t" \
  "s_mov_b32 s60, 4390912\n\t" \
  "buffer_load_dwordx4 v16, %[W], s60 offen lds\n\t" \
  "s_add_u32 m0, s63, 28672\n\t" \
  "s_mov_b32 s60, 4456448\n\t" \
  "buffer_load_dwordx4 v16, %[W], s60 offen lds\n\t" \
  "s_add_u32 m0, s63, 29696\n\t" \
  "s_mov_b32 s60, 4521984\n\t" \
  "buffer_load_dwordx4 v16, %[W], s60 offen lds\n\t" \
  "s_add_u32 m0, s63, 30720\n\t" \
  "s_mov_b32 s60, 4587520\n\t" \
  "buffer_load_dwordx4 v16, %[W], s60 offen lds\n\t" \
  "s_add_u32 m0, s63, 31744\n\t" \
  "s_mov_b32 s60, 4653056\n\t" \
  "buffer_load_dwordx4 v16, %[W], s60 offen lds\n\t" \
  "s_waitcnt vmcnt(32)\n\t" \
  "ds_read_b64_tr_b16 a[32:33], v17 offset:32768\n\t" \
  "ds_read_b64_tr_b16 a[34:35], v17 offset:33280\n\t" \
  "ds_read_b64_tr_b16 a[36:37], v17 offset:36864\n\t" \
  "ds_read_b64_tr_b16 a[38:39], v17 offset:37376\n\t" \
  "ds_read_b64_tr_b16 a[104:105], v17 offset:32800\n\t" \
  "ds_read_b64_tr_b16 a[106:107], v17 offset:33312\n\t" \
  "ds_read_b64_tr_b16 a[108:109], v17 offset:36896\n\t" \
  "ds_read_b64_tr_b16 a[110:111], v17 offset:37408\n\t" \
  "ds_read_b64_tr_b16 a[176:177], v18 offset:32768\n\t" \
  "ds_read_b64_tr_b16 a[178:179], v18 offset:33280\n\t" \
  "ds_read_b64_tr_b16 a[180:181], v18 offset:36864\n\t" \
  "ds_read_b64_tr_b16 a[182:183], v18 offset:37376\n\t" \
  "ds_read_b64_tr_b16 a[248:249], v18 offset:32800\n\t" \
  "ds_read_b64_tr_b16 a[250:251], v18 offset:33312\n\t" \
  "ds_read_b64_tr_b16 a[252:253], v18 offset:36896\n\t" \
  "ds_read_b64_tr_b16 a[254:255], v18 offset:37408\n\t" \
  "s_waitcnt lgkmcnt(0)\n\t" \
  "s_waitcnt vmcnt(24)\n\t" \
  "ds_read_b64_tr_b16 a[40:41], v17 offset:0\n\t" \
  "ds_read_b64_tr_b16 a[42:43], v17 offset:512\n\t" \
  "ds_read_b64_tr_b16 a[44:45], v17 offset:4096\n\t" \
  "ds_read_b64_tr_b16 a[46:47], v17 offset:4608\n\t" \
  "ds_read_b64_tr_b16 a[112:113], v17 offset:32\n\t" \
  "ds_read_b64_tr_b16 a[114:115], v17 offset:544\n\t" \
  "ds_read_b64_tr_b16 a[116:117], v17 offset:4128\n\t" \
  "ds_read_b64_tr_b16 a[118:119], v17 offset:4640\n\t" \
  "ds_read_b64_tr_b16 a[184:185], v18 offset:0\n\t" \
  "ds_read_b64_tr_b16 a[186:187], v18 offset:512\n\t" \
  "ds_read_b64_tr_b16 a[188:189], v18 offset:4096\n\t" \
  "ds_read_b64_tr_b16 a[190:191], v18 offset:4608\n\t" \
  "ds_read_b64_tr_b16 v[224:225], v18 offset:32\n\t" \
  "ds_read_b64_tr_b16 v[226:227], v18 offset:544\n\t" \
  "ds_read_b64_tr_b16 v[228:229], v18 offset:4128\n\t" \
  "ds_read_b64_tr_b16 v[230:231], v18 offset:4640\n\t" \
  "s_waitcnt lgkmcnt(0)\n\t" \
  "s_waitcnt vmcnt(16)\n\t" \
  "ds_read_b64_tr_b16 a[48:49], v17 offset:8192\n\t" \
  "ds_read_b64_tr_b16 a[50:51], v17 offset:8704\n\t" \
  "ds_read_b64_tr_b16 a[52:53], v17 offset:12288\n\t" \
  "ds_read_b64_tr_b16 a[54:55], v17 offset:12800\n\t" \
  "ds_read_b64_tr_b16 a[120:121], v17 offset:8224\n\t" \
  "ds_read_b64_tr_b16 a[122:123], v17 offset:8736\n\t" \
  "ds_read_b64_tr_b16 a[124:125], v17 offset:12320\n\t" \
  "ds_read_b64_tr_b16 a[126:127], v17 offset:12832\n\t" \
  "ds_read_b64_tr_b16 a[192:193], v18 offset:8192\n\t" \
  "ds_read_b64_tr_b16 a[194:195], v18 offset:8704\n\t" \
  "ds_read_b64_tr_b16 a[196:197], v18 offset:12288\n\t" \
  "ds_read_b64_tr_b16 a[198:199], v18 offset:12800\n\t" \
  "ds_read_b64_tr_b16 v[232:233], v18 offset:8224\n\t" \
  "ds_read_b64_tr_b16 v[234:235], v18 offset:8736\n\t" \
  "ds_read_b64_tr_b16 v[236:237], v18 offset:12320\n\t" \
  "ds_read_b64_tr_b16 v[238:239], v18 offset:12832\n\t" \
  "s_waitcnt lgkmcnt(0)\n\t" \
  "s_waitcnt vmcnt(8)\n\t" \
  "ds_read_b64_tr_b16 a[56:57], v17 offset:16384\n\t" \
  "ds_read_b64_tr_b16 a[58:59], v17 offset:16896\n\t" \
  "ds_read_b64_tr_b16 a[60:61], v17 offset:20480\n\t" \
  "ds_read_b64_tr_b16 a[62:63], v17 offset:20992\n\t" \
  "ds_read_b64_tr_b16 a[128:129], v17 offset:16416\n\t" \
  "ds_read_b64_tr_b16 a[130:131], v17 offset:16928\n\t" \
  "ds_read_b64_tr_b16 a[132:133], v17 offset:20512\n\t" \
  "ds_read_b64_tr_b16 a[134:135], v17 offset:21024\n\t" \
  "ds_read_b64_tr_b16 a[200:201], v18 offset:16384\n\t" \
  "ds_read_b64_tr_b16 a[202:203], v18 offset:16896\n\t" \
  "ds_read_b64_tr_b16 a[204:205], v18 offset:20480\n\t" \
  "ds_read_b64_tr_b16 a[206:207], v18 offset:20992\n\t" \
  "ds_read_b64_tr_b16 v[240:241], v18 offset:16416\n\t" \
  "ds_read_b64_tr_b16 v[242:243], v18 offset:16928\n\t" \
  "ds_read_b64_tr_b16 v[244:245], v18 offset:20512\n\t" \
  "ds_read_b64_tr_b16 v[246:247], v18 offset:21024\n\t" \
  "s_waitcnt lgkmcnt(0)\n\t" \
  "s_waitcnt vmcnt(0)\n\t" \
  "ds_read_b64_tr_b16 a[64:65], v17 offset:24576\n\t" \
  "ds_read_b64_tr_b16 a[66:67], v17 offset:25088\n\t" \
  "ds_read_b64_tr_b16 a[68:69], v17 offset:28672\n\t" \
  "ds_read_b64_tr_b16 a[70:71], v17 offset:29184\n\t" \
  "ds_read_b64_tr_b16 a[136:137], v17 offset:24608\n\t" \
  "ds_read_b64_tr_b16 a[138:139], v17 offset:25120\n\t" \
  "ds_read_b64_tr_b16 a[140:141], v17 offset:28704\n\t" \
  "ds_read_b64_tr_b16 a[142:143], v17 offset:29216\n\t" \
  "ds_read_b64_tr_b16 a[208:209], v18 offset:24576\n\t" \
  "ds_read_b64_tr_b16 a[210:211], v18 offset:25088\n\t" \
  "ds_read_b64_tr_b16 a[212:213], v18 offset:28672\n\t" \
  "ds_read_b64_tr_b16 a[214:215], v18 offset:29184\n\t" \
  "ds_read_b64_tr_b16 v[248:249], v18 offset:24608\n\t" \
  "ds_read_b64_tr_b16 v[250:251], v18 offset:25120\n\t" \
  "ds_read_b64_tr_b16 v[252:253], v18 offset:28704\n\t" \
  "ds_read_b64_tr_b16 v[254:255], v18 offset:29216\n\t" \
  "s_waitcnt lgkmcnt(0)\n\t" \
  "s_mov_b32 s84, " TK_CAP "\n\t" \
  "s_mov_b32 s48, 0\n\t" \
  "s_mov_b32 s49, 0xf0000000\n\t" \
  "s_mov_b32 s46, s41\n\t" \
  "s_mov_b32 s56, 0\n\t" \
  "v_lshrrev_b32 v7, 2, v0\n\t" \
  "s_mov_b32 s60, " TK_CTAB "\n\t" \
  "buffer_load_dword v4, v7, %[X], s60 offen offset:88 sc1\n\t" \
  "buffer_load_dword v7, v7, %[X], s60 offen offset:0 sc1\n\t" \
  "s_waitcnt vmcnt(0)\n\t" \
  "v_and_b32 v4, 1, v4\n\t" \
  "v_and_b32 v7, 1, v7\n\t" \
  "v_cmp_ne_u32 s[90:91], s67, v4\n\t" \
  "v_cmp_ne_u32 s[88:89], s67, v7\n\t" \
  "s_nop 1\n\t" \
  "s_mov_b32 s57, 0xf0000000\n\t" \
  "s_add_u32 s83, s48, 0\n\t" \
  "s_lshr_b64 s[60:61], s[90:91], s83\n\t" \
  "s_and_b32 s60, s60, 1\n\t" \
  "s_lshl_b32 s60, s60, 13\n\t" \
  "s_add_u32 s83, s83, " TK_SG0 "\n\t" \
  "s_lshl_b32 s83, s83, 21\n\t" \
  "s_or_b32 s83, s83, s60\n\t" \
  "s_add_u32 s62, s83, s39\n\t" \
  "v_add_u32 v7, s62, v3\n\t" \
  "LCOLD_%=:\n\t" \
  "s_waitcnt vmcnt(0)\n\t" \
  "s_mov_b64 exec, 1\n\t" \
  "buffer_store_dword v6, off, %[X], s49 sc1\n\t" \
  "s_mov_b64 exec, -1\n\t" \
  "s_mov_b32 s49, 0xf0000000\n\t" \
  "s_add_u32 s55, s83, s36\n\t" \
  "s_cmp_lg_u32 s40, 0\n\t" \
  "s_cbranch_scc1 LGO_%=\n\t" \
  "s_and_b32 s62, s45, 3\n\t" \
  "s_lshl_b32 s62, s62, 7\n\t" \
  "s_add_u32 s62, s62, s83\n\t" \
  "s_add_u32 s62, s62, s39\n\t" \
  "s_mov_b32 s61, 0\n\t" \
  "LPO_%=:\n\t" \
  "buffer_load_dword v4, off, %[X], s62 sc1\n\t" \
  "s_waitcnt vmcnt(0)\n\t" \
  "v_cmp_eq_u32 vcc, 0, v4\n\t" \
  "s_cbranch_vccz LPO_%=x\n\t" \
  "s_add_u32 s61, s61, 1\n\t" \
  "s_sleep " TK_PSH "\n\t" \
  "s_cmp_gt_u32 s61, s84\n\t" \
  "s_cbranch_scc1 LPO_%=g\n\t" \
  "s_and_b32 s60, s61, 1023\n\t" \
  "s_cmp_lg_u32 s60, 0\n\t" \
  "s_cbranch_scc1 LPO_%=\n\t" \
  "buffer_load_dword v4, off, %[X], s92 sc1\n\t" \
  "s_waitcnt vmcnt(0)\n\t" \
  "v_readfirstlane_b32 s60, v4\n\t" \
  "s_nop 0\n\t" \
  "s_cmp_eq_u32 s60, 0\n\t" \
  "s_cbranch_scc1 LPO_%=\n\t" \
  "LPO_%=g:\n\t" \
  "s_mov_b32 s84, 0\n\t" \
  "s_or_b32 s60, s48, 1610612736\n\t" \
  "v_mov_b32 v4, s60\n\t" \
  "s_mov_b64 exec, 1\n\t" \
  "buffer_store_dword v4, off, %[X], s92 sc1\n\t" \
  "s_mov_b64 exec, -1\n\t" \
  "LPO_%=x:\n\t" \
  "s_mov_b32 s61, 0\n\t" \
  "LPH_%=:\n\t" \
  "buffer_load_dword v4, v7, %[X], 0 offen sc1\n\t" \
  "s_waitcnt vmcnt(0)\n\t" \
  "v_cmp_eq_u32 vcc, 0, v4\n\t" \
  "s_cbranch_vccz LPH_%=x\n\t" \
  "s_add_u32 s61, s61, 1\n\t" \
  "s_sleep " TK_PSH "\n\t" \
  "s_cmp_gt_u32 s61, s84\n\t" \
  "s_cbranch_scc1 LPH_%=g\n\t" \
  "s_and_b32 s60, s61, 1023\n\t" \
  "s_cmp_lg_u32 s60, 0\n\t" \
  "s_cbranch_scc1 LPH_%=\n\t" \
  "buffer_load_dword v4, off, %[X], s92 sc1\n\t" \
  "s_waitcnt vmcnt(0)\n\t" \
  "v_readfirstlane_b32 s60, v4\n\t" \
  "s_nop 0\n\t" \
  "s_cmp_eq_u32 s60, 0\n\t" \
  "s_cbranch_scc1 LPH_%=\n\t" \
  "LPH_%=g:\n\t" \
  "s_mov_b32 s84, 0\n\t" \
  "s_or_b32 s60, s48, 1610612736\n\t" \
  "v_mov_b32 v4, s60\n\t" \
  "s_mov_b64 exec, 1\n\t" \
  "buffer_store_dword v4, off, %[X], s92 sc1\n\t" \
  "s_mov_b64 exec, -1\n\t" \
  "LPH_%=x:\n\t" \
  "s_cmp_lt_i32 s57, 0\n\t" \
  "s_cbranch_scc1 LGO_%=\n\t" \
  "s_movk_i32 s63, 4000\n\t" \
  "s_branch LGC_%=u\n\t" \
  "LGC_%=:\n\t" \
  "s_sleep " TK_PSG "\n\t" \
  "buffer_load_dword v8, off, %[X], s57 sc1\n\t" \
  "s_waitcnt vmcnt(0)\n\t" \
  "LGC_%=u:\n\t" \
  "s_memrealtime s[52:53]\n\t" \
  "v_readfirstlane_b32 s60, v8\n\t" \
  "s_waitcnt lgkmcnt(0)\n\t" \
  "s_sub_u32 s63, s63, 1\n\t" \
  "s_cbranch_scc1 LGO_%=\n\t" \
  "s_cmp_eq_u32 s60, -1\n\t" \
  "s_cbranch_scc1 LGC_%=\n\t" \
  "s_not_b32 s60, s60\n\t" \
  "s_lshl_b32 s61, s52, 8\n\t" \
  "s_sub_u32 s60, s60, s61\n\t" \
  "s_ashr_i32 s60, s60, 8\n\t" \
  "s_sub_i32 s60, s60, " TK_QXC "\n\t" \
  "s_min_i32 s60, s60, 400\n\t" \
  "s_add_u32 s54, s52, s60\n\t" \
  "LGC_%=w:\n\t" \
  "s_sleep 1\n\t" \
  "s_memrealtime s[52:53]\n\t" \
  "s_waitcnt lgkmcnt(0)\n\t" \
  "s_sub_i32 s53, s52, s54\n\t" \
  "s_cmp_lt_i32 s53, 0\n\t" \
  "s_cbranch_scc1 LGC_%=w\n\t" \
  "LGO_%=:\n\t" \
  "s_barrier\n\t" \
  "s_add_u32 m0, s46, 0\n\t" \
  "s_add_u32 s47, s55, 0\n\t" \
  "buffer_load_dwordx4 v0, %[X], s47 offen sc1 lds\n\t" \
  "s_add_u32 m0, s46, 4096\n\t" \
  "s_add_u32 s47, s55, 4096\n\t" \
  "buffer_load_dwordx4 v0, %[X], s47 offen sc1 lds\n\t" \
  "s_add_u32 m0, s46, 8192\n\t" \
  "s_add_u32 s47, s55, 16384\n\t" \
  "buffer_load_dwordx4 v0, %[X], s47 offen sc1 lds\n\t" \
  "s_add_u32 m0, s46, 12288\n\t" \
  "s_add_u32 s47, s55, 20480\n\t" \
  "buffer_load_dwordx4 v0, %[X], s47 offen sc1 lds\n\t" \
  "s_add_u32 m0, s46, 16384\n\t" \
  "s_add_u32 s47, s55, 32768\n\t" \
  "buffer_load_dwordx4 v0, %[X], s47 offen sc1 lds\n\t" \
  "s_add_u32 m0, s46, 20480\n\t" \
  "s_add_u32 s47, s55, 36864\n\t" \
  "buffer_load_dwordx4 v0, %[X], s47 offen sc1 lds\n\t" \
  "s_add_u32 m0, s46, 24576\n\t" \
  "s_add_u32 s47, s55, 49152\n\t" \
  "buffer_load_dwordx4 v0, %[X], s47 offen sc1 lds\n\t" \
  "s_add_u32 m0, s46, 28672\n\t" \
  "s_add_u32 s47, s55, 53248\n\t" \
  "buffer_load_dwordx4 v0, %[X], s47 offen sc1 lds\n\t" \
  "s_add_u32 m0, s46, 32768\n\t" \
  "s_add_u32 s47, s55, 65536\n\t" \
  "buffer_load_dwordx4 v0, %[X], s47 offen sc1 lds\n\t" \
  "s_waitcnt vmcnt(5)\n\t" \
  "LTOP_%=:\n\t" \
  "s_barrier\n\t" \
  "ds_read_b128 v[64:67], v1 offset:0\n\t" \
  "ds_read_b128 v[68:71], v1 offset:2048\n\t" \
  "ds_read_b128 v[72:75], v2 offset:0\n\t" \
  "ds_read_b128 v[76:79], v2 offset:2048\n\t" \
  "ds_read_b128 v[80:83], v1 offset:4096\n\t" \
  "ds_read_b128 v[84:87], v1 offset:6144\n\t" \
  "s_mov_b32 s82, s83\n\t" \
  "s_xor_b32 s46, s46, 0x10000\n\t" \
  "s_waitcnt lgkmcnt(5)\n\t" \
  "v_mfma_f32_16x16x32_bf16 v[16:19], v[64:67], a[0:3], 0\n\t" \
  "ds_read_b128 v[88:91], v2 offset:4096\n\t" \
  "s_waitcnt lgkmcnt(5)\n\t" \
  "v_mfma_f32_16x16x32_bf16 v[20:23], v[68:71], a[0:3], 0\n\t" \
  "s_add_u32 s83, s48, 1\n\t" \
  "s_lshr_b64 s[60:61], s[90:91], s83\n\t" \
  "v_mfma_f32_16x16x32_bf16 v[24:27], v[64:67], a[72:75], 0\n\t" \
  "ds_read_b128 v[92:95], v2 offset:6144\n\t" \
  "v_mfma_f32_16x16x32_bf16 v[28:31], v[68:71], a[72:75], 0\n\t" \
  "s_and_b32 s60, s60, 1\n\t" \
  "s_lshl_b32 s60, s60, 13\n\t" \
  "s_waitcnt lgkmcnt(5)\n\t" \
  "v_mfma_f32_16x16x32_bf16 v[16:19], v[72:75], a[4:7], v[16:19]\n\t" \
  "ds_read_b128 v[96:99], v1 offset:8192\n\t" \
  "s_waitcnt lgkmcnt(5)\n\t" \
  "v_mfma_f32_16x16x32_bf16 v[20:23], v[76:79], a[4:7], v[20:23]\n\t" \
  "s_add_u32 s83, s83, " TK_SG0 "\n\t" \
  "s_lshl_b32 s83, s83, 21\n\t" \
  "v_mfma_f32_16x16x32_bf16 v[24:27], v[72:75], a[76:79], v[24:27]\n\t" \
  "ds_read_b128 v[100:103], v1 offset:10240\n\t" \
  "v_mfma_f32_16x16x32_bf16 v[28:31], v[76:79], a[76:79], v[28:31]\n\t" \
  "s_or_b32 s83, s83, s60\n\t" \
  "s_add_u32 s62, s83, s39\n\t" \
  "s_waitcnt lgkmcnt(5)\n\t" \
  "v_mfma_f32_16x16x32_bf16 v[16:19], v[80:83], a[8:11], v[16:19]\n\t" \
  "ds_read_b128 v[104:107], v2 offset:8192\n\t" \
  "s_waitcnt lgkmcnt(5)\n\t" \
  "v_mfma_f32_16x16x32_bf16 v[20:23], v[84:87], a[8:11], v[20:23]\n\t" \
  "v_add_u32 v7, s62, v3\n\t" \
  "s_add_u32 s50, s82, s37\n\t" \
  "v_mfma_f32_16x16x32_bf16 v[24:27], v[80:83], a[80:83], v[24:27]\n\t" \
  "ds_read_b128 v[108:111], v2 offset:10240\n\t" \
  "v_mfma_f32_16x16x32_bf16 v[28:31], v[84:87], a[80:83], v[28:31]\n\t" \
  "s_add_u32 s51, s50, 4096\n\t" \
  "s_waitcnt lgkmcnt(5)\n\t" \
  "v_mfma_f32_16x16x32_bf16 v[16:19], v[88:91], a[12:15], v[16:19]\n\t" \
  "ds_read_b128 v[112:115], v1 offset:12288\n\t" \
  "s_waitcnt lgkmcnt(5)\n\t" \
  "v_mfma_f32_16x16x32_bf16 v[20:23], v[92:95], a[12:15], v[20:23]\n\t" \
  "v_mfma_f32_16x16x32_bf16 v[24:27], v[88:91], a[84:87], v[24:27]\n\t" \
  "ds_read_b128 v[116:119], v1 offset:14336\n\t" \
  "v_mfma_f32_16x16x32_bf16 v[28:31], v[92:95], a[84:87], v[28:31]\n\t" \
  "s_waitcnt lgkmcnt(5)\n\t" \
  "v_mfma_f32_16x16x32_bf16 v[16:19], v[96:99], a[16:19], v[16:19]\n\t" \
  "ds_read_b128 v[120:123], v2 offset:12288\n\t" \
  "s_waitcnt lgkmcnt(5)\n\t" \
  "v_mfma_f32_16x16x32_bf16 v[20:23], v[100:103], a[16:19], v[20:23]\n\t" \
  "s_cmp_lg_u32 s56, 0\n\t" \
  "v_mfma_f32_16x16x32_bf16 v[24:27], v[96:99], a[88:91], v[24:27]\n\t" \
  "ds_read_b128 v[124:127], v2 offset:14336\n\t" \
  "v_mfma_f32_16x16x32_bf16 v[28:31], v[100:103], a[88:91], v[28:31]\n\t" \
  "s_cbranch_scc1 LWARM_%=\n\t" \
  "s_waitcnt vmcnt(0)\n\t" \
  "LWARM_%=:\n\t" \
  "s_barrier\n\t" \
  "s_waitcnt lgkmcnt(5)\n\t" \
  "v_mfma_f32_16x16x32_bf16 v[16:19], v[104:107], a[20:23], v[16:19]\n\t" \
  "ds_read_b128 v[128:131], v1 offset:16384\n\t" \
  "s_waitcnt lgkmcnt(5)\n\t" \
  "v_mfma_f32_16x16x32_bf16 v[20:23], v[108:111], a[20:23], v[20:23]\n\t" \
  "v_mfma_f32_16x16x32_bf16 v[24:27], v[104:107], a[92:95], v[24:27]\n\t" \
  "ds_read_b128 v[132:135], v1 offset:18432\n\t" \
  "v_mfma_f32_16x16x32_bf16 v[28:31], v[108:111], a[92:95], v[28:31]\n\t" \
  "s_add_i32 s62, s48, s42\n\t" \
  "s_add_i32 s62, s62, 1\n\t" \
  "s_waitcnt lgkmcnt(5)\n\t" \
  "v_mfma_f32_16x16x32_bf16 v[16:19], v[112:115], a[24:27], v[16:19]\n\t" \
  "ds_read_b128 v[136:139], v2 offset:16384\n\t" \
  "s_waitcnt lgkmcnt(5)\n\t" \
  "v_mfma_f32_16x16x32_bf16 v[20:23], v[116:119], a[24:27], v[20:23]\n\t" \
  "s_mul_i32 s63, s62, 43\n\t" \
  "s_lshr_b32 s63, s63, 7\n\t" \
  "v_mfma_f32_16x16x32_bf16 v[24:27], v[112:115], a[96:99], v[24:27]\n\t" \
  "ds_read_b128 v[140:143], v2 offset:18432\n\t" \
  "v_mfma_f32_16x16x32_bf16 v[28:31], v[116:119], a[96:99], v[28:31]\n\t" \
  "s_mul_i32 s57, s62, 688128\n\t" \
  "s_lshl_b32 s60, s63, 15\n\t" \
  "s_waitcnt lgkmcnt(5)\n\t" \
  "v_mfma_f32_16x16x32_bf16 v[16:19], v[120:123], a[28:31], v[16:19]\n\t" \
  "ds_read_b128 v[144:147], v1 offset:20480\n\t" \
  "s_waitcnt lgkmcnt(5)\n\t" \
  "v_mfma_f32_16x16x32_bf16 v[20:23], v[124:127], a[28:31], v[20:23]\n\t" \
  "s_add_u32 s57, s57, s60\n\t" \
  "s_lshr_b32 s60, s88, s63\n\t" \
  "v_mfma_f32_16x16x32_bf16 v[24:27], v[120:123], a[100:103], v[24:27]\n\t" \
  "ds_read_b128 v[148:151], v1 offset:22528\n\t" \
  "v_mfma_f32_16x16x32_bf16 v[28:31], v[124:127], a[100:103], v[28:31]\n\t" \
  "s_and_b32 s60, s60, 1\n\t" \
  "s_lshl_b32 s60, s60, 13\n\t" \
  "s_waitcnt lgkmcnt(5)\n\t" \
  "v_mfma_f32_16x16x32_bf16 v[16:19], v[128:131], a[32:35], v[16:19]\n\t" \
  "ds_read_b128 v[152:155], v2 offset:20480\n\t" \
  "s_waitcnt lgkmcnt(5)\n\t" \
  "v_mfma_f32_16x16x32_bf16 v[20:23], v[132:135], a[32:35], v[20:23]\n\t" \
  "s_add_u32 s57, s57, s60\n\t" \
  "s_add_u32 s57, s57, 656896\n\t" \
  "v_mfma_f32_16x16x32_bf16 v[24:27], v[128:131], a[104:107], v[24:27]\n\t" \
  "ds_read_b128 v[156:159], v2 offset:22528\n\t" \
  "v_mfma_f32_16x16x32_bf16 v[28:31], v[132:135], a[104:107], v[28:31]\n\t" \
  "s_cmp_lt_i32 s62, 1\n\t" \
  "s_cselect_b32 s57, 0xf0000000, s57\n\t" \
  "s_waitcnt lgkmcnt(5)\n\t" \
  "v_mfma_f32_16x16x32_bf16 v[16:19], v[136:139], a[36:39], v[16:19]\n\t" \
  "ds_read_b128 v[160:163], v1 offset:24576\n\t" \
  "s_waitcnt lgkmcnt(5)\n\t" \
  "v_mfma_f32_16x16x32_bf16 v[20:23], v[140:143], a[36:39], v[20:23]\n\t" \
  "s_cmp_lg_u32 s40, 0\n\t" \
  "s_cselect_b32 s57, 0xf0000000, s57\n\t" \
  "v_mfma_f32_16x16x32_bf16 v[24:27], v[136:139], a[108:111], v[24:27]\n\t" \
  "ds_read_b128 v[164:167], v1 offset:26624\n\t" \
  "v_mfma_f32_16x16x32_bf16 v[28:31], v[140:143], a[108:111], v[28:31]\n\t" \
  "s_waitcnt lgkmcnt(5)\n\t" \
  "v_mfma_f32_16x16x32_bf16 v[16:19], v[144:147], a[40:43], v[16:19]\n\t" \
  "ds_read_b128 v[168:171], v2 offset:24576\n\t" \
  "s_waitcnt lgkmcnt(5)\n\t" \
  "v_mfma_f32_16x16x32_bf16 v[20:23], v[148:151], a[40:43], v[20:23]\n\t" \
  "v_mfma_f32_16x16x32_bf16 v[24:27], v[144:147], a[112:115], v[24:27]\n\t" \
  "ds_read_b128 v[172:175], v2 offset:26624\n\t" \
  "v_mfma_f32_16x16x32_bf16 v[28:31], v[148:151], a[112:115], v[28:31]\n\t" \
  "s_waitcnt lgkmcnt(5)\n\t" \
  "v_mfma_f32_16x16x32_bf16 v[16:19], v[152:155], a[44:47], v[16:19]\n\t" \
  "ds_read_b128 v[176:179], v1 offset:28672\n\t" \
  "s_waitcnt lgkmcnt(5)\n\t" \
  "v_mfma_f32_16x16x32_bf16 v[20:23], v[156:159], a[44:47], v[20:23]\n\t" \
  "v_mfma_f32_16x16x32_bf16 v[24:27], v[152:155], a[116:119], v[24:27]\n\t" \
  "ds_read_b128 v[180:183], v1 offset:30720\n\t" \
  "v_mfma_f32_16x16x32_bf16 v[28:31], v[156:159], a[116:119], v[28:31]\n\t" \
  "s_waitcnt lgkmcnt(5)\n\t" \
  "v_mfma_f32_16x16x32_bf16 v[16:19], v[160:163], a[48:51], v[16:19]\n\t" \
  "ds_read_b128 v[184:187], v2 offset:28672\n\t" \
  "s_waitcnt lgkmcnt(5)\n\t" \
  "v_mfma_f32_16x16x32_bf16 v[20:23], v[164:167], a[48:51], v[20:23]\n\t" \
  "v_mfma_f32_16x16x32_bf16 v[24:27], v[160:163], a[120:123], v[24:27]\n\t" \
  "ds_read_b128 v[188:191], v2 offset:30720\n\t" \
  "v_mfma_f32_16x16x32_bf16 v[28:31], v[164:167], a[120:123], v[28:31]\n\t" \
  "s_waitcnt lgkmcnt(5)\n\t" \
  "v_mfma_f32_16x16x32_bf16 v[16:19], v[168:171], a[52:55], v[16:19]\n\t" \
  "ds_read_b128 v[192:195], v1 offset:32768\n\t" \
  "s_waitcnt lgkmcnt(5)\n\t" \
  "v_mfma_f32_16x16x32_bf16 v[20:23], v[172:175], a[52:55], v[20:23]\n\t" \
  "v_mfma_f32_16x16x32_bf16 v[24:27], v[168:171], a[124:127], v[24:27]\n\t" \
  "ds_read_b128 v[196:199], v1 offset:34816\n\t" \
  "v_mfma_f32_16x16x32_bf16 v[28:31], v[172:175], a[124:127], v[28:31]\n\t" \
  "s_waitcnt lgkmcnt(5)\n\t" \
  "v_mfma_f32_16x16x32_bf16 v[16:19], v[176:179], a[56:59], v[16:19]\n\t" \
  "ds_read_b128 v[200:203], v2 offset:32768\n\t" \
  "s_waitcnt lgkmcnt(5)\n\t" \
  "v_mfma_f32_16x16x32_bf16 v[20:23], v[180:183], a[56:59], v[20:23]\n\t" \
  "v_mfma_f32_16x16x32_bf16 v[24:27], v[176:179], a[128:131], v[24:27]\n\t" \
  "ds_read_b128 v[204:207], v2 offset:34816\n\t" \
  "v_mfma_f32_16x16x32_bf16 v[28:31], v[180:183], a[128:131], v[28:31]\n\t" \
  "s_waitcnt lgkmcnt(5)\n\t" \
  "v_mfma_f32_16x16x32_bf16 v[16:19], v[184:187], a[60:63], v[16:19]\n\t" \
  "s_waitcnt lgkmcnt(4)\n\t" \
  "v_mfma_f32_16x16x32_bf16 v[20:23], v[188:191], a[60:63], v[20:23]\n\t" \
  "buffer_load_dword v4, v7, %[X], 0 offen sc1\n\t" \
  "v_mfma_f32_16x16x32_bf16 v[24:27], v[184:187], a[132:135], v[24:27]\n\t" \
  "v_mfma_f32_16x16x32_bf16 v[28:31], v[188:191], a[132:135], v[28:31]\n\t" \
  "s_waitcnt lgkmcnt(3)\n\t" \
  "v_mfma_f32_16x16x32_bf16 v[16:19], v[192:195], a[64:67], v[16:19]\n\t" \
  "v_xor_b32 v1, 0x10000, v1\n\t" \
  "v_xor_b32 v2, 0x10000, v2\n\t" \
  "s_waitcnt lgkmcnt(2)\n\t" \
  "v_mfma_f32_16x16x32_bf16 v[20:23], v[196:199], a[64:67], v[20:23]\n\t" \
  "v_mfma_f32_16x16x32_bf16 v[24:27], v[192:195], a[136:139], v[24:27]\n\t" \
  "v_mfma_f32_16x16x32_bf16 v[28:31], v[196:199], a[136:139], v[28:31]\n\t" \
  "s_waitcnt lgkmcnt(1)\n\t" \
  "v_mfma_f32_16x16x32_bf16 v[16:19], v[200:203], a[68:71], v[16:19]\n\t" \
  "s_waitcnt lgkmcnt(0)\n\t" \
  "v_mfma_f32_16x16x32_bf16 v[20:23], v[204:207], a[68:71], v[20:23]\n\t" \
  "v_mfma_f32_16x16x32_bf16 v[24:27], v[200:203], a[140:143], v[24:27]\n\t" \
  "v_mfma_f32_16x16x32_bf16 v[28:31], v[204:207], a[140:143], v[28:31]\n\t" \
  "v_mfma_f32_16x16x32_bf16 v[32:35], v[64:67], a[144:147], 0\n\t" \
  "v_cvt_pk_bf16_f32 v48, v16, v17\n\t" \
  "v_cvt_pk_bf16_f32 v49, v18, v19\n\t" \
  "v_mfma_f32_16x16x32_bf16 v[36:39], v[68:71], a[144:147], 0\n\t" \
  "v_cvt_pk_bf16_f32 v50, v20, v21\n\t" \
  "v_cvt_pk_bf16_f32 v51, v22, v23\n\t" \
  "v_mfma_f32_16x16x32_bf16 v[32:35], v[72:75], a[148:151], v[32:35]\n\t" \
  "v_cvt_pk_bf16_f32 v52, v24, v25\n\t" \
  "v_cvt_pk_bf16_f32 v53, v26, v27\n\t" \
  "v_mfma_f32_16x16x32_bf16 v[36:39], v[76:79], a[148:151], v[36:39]\n\t" \
  "v_cvt_pk_bf16_f32 v54, v28, v29\n\t" \
  "v_cvt_pk_bf16_f32 v55, v30, v31\n\t" \
  "v_mfma_f32_16x16x32_bf16 v[32:35], v[80:83], a[152:155], v[32:35]\n\t" \
  "buffer_store_dwordx4 v[48:51], v0, %[X], s50 offen offset:0 sc1\n\t" \
  "v_mfma_f32_16x16x32_bf16 v[36:39], v[84:87], a[152:155], v[36:39]\n\t" \
  "v_mfma_f32_16x16x32_bf16 v[32:35], v[88:91], a[156:159], v[32:35]\n\t" \
  "v_mfma_f32_16x16x32_bf16 v[36:39], v[92:95], a[156:159], v[36:39]\n\t" \
  "v_mfma_f32_16x16x32_bf16 v[32:35], v[96:99], a[160:163], v[32:35]\n\t" \
  "v_mfma_f32_16x16x32_bf16 v[36:39], v[100:103], a[160:163], v[36:39]\n\t" \
  "v_mfma_f32_16x16x32_bf16 v[32:35], v[104:107], a[164:167], v[32:35]\n\t" \
  "v_mfma_f32_16x16x32_bf16 v[36:39], v[108:111], a[164:167], v[36:39]\n\t" \
  "v_mfma_f32_16x16x32_bf16 v[32:35], v[112:115], a[168:171], v[32:35]\n\t" \
  "v_mfma_f32_16x16x32_bf16 v[36:39], v[116:119], a[168:171], v[36:39]\n\t" \
  "v_mfma_f32_16x16x32_bf16 v[32:35], v[120:123], a[172:175], v[32:35]\n\t" \
  "v_mfma_f32_16x16x32_bf16 v[36:39], v[124:127], a[172:175], v[36:39]\n\t" \
  "s_waitcnt vmcnt(1)\n\t" \
  "v_cmp_eq_u32 vcc, 0, v4\n\t" \
  "v_mfma_f32_16x16x32_bf16 v[32:35], v[128:131], a[176:179], v[32:35]\n\t" \
  "v_mfma_f32_16x16x32_bf16 v[36:39], v[132:135], a[176:179], v[36:39]\n\t" \
  "s_cmp_eq_u64 vcc, 0\n\t" \
  "s_cselect_b32 s60, 1, 0\n\t" \
  "v_mov_b32 v4, s60\n\t" \
  "v_mfma_f32_16x16x32_bf16 v[32:35], v[136:139], a[180:183], v[32:35]\n\t" \
  "ds_write_b32 v5, v4\n\t" \
  "v_mfma_f32_16x16x32_bf16 v[36:39], v[140:143], a[180:183], v[36:39]\n\t" \
  "v_mfma_f32_16x16x32_bf16 v[32:35], v[144:147], a[184:187], v[32:35]\n\t" \
  "v_mfma_f32_16x16x32_bf16 v[36:39], v[148:151], a[184:187], v[36:39]\n\t" \
  "buffer_store_dwordx4 v[52:55], v0, %[X], s50 offen offset:1024 sc1\n\t" \
  "v_mfma_f32_16x16x32_bf16 v[32:35], v[152:155], a[188:191], v[32:35]\n\t" \
  "v_mfma_f32_16x16x32_bf16 v[36:39], v[156:159], a[188:191], v[36:39]\n\t" \
  "s_waitcnt lgkmcnt(0)\n\t" \
  "s_barrier\n\t" \
  "v_mfma_f32_16x16x32_bf16 v[32:35], v[160:163], a[192:195], v[32:35]\n\t" \
  "ds_read_b32 v4, v6 offset:40959\n\t" \
  "v_mfma_f32_16x16x32_bf16 v[36:39], v[164:167], a[192:195], v[36:39]\n\t" \
  "v_mfma_f32_16x16x32_bf16 v[32:35], v[168:171], a[196:199], v[32:35]\n\t" \
  "v_mfma_f32_16x16x32_bf16 v[36:39], v[172:175], a[196:199], v[36:39]\n\t" \
  "v_mfma_f32_16x16x32_bf16 v[32:35], v[176:179], a[200:203], v[32:35]\n\t" \
  "v_mfma_f32_16x16x32_bf16 v[36:39], v[180:183], a[200:203], v[36:39]\n\t" \
  "s_waitcnt lgkmcnt(0)\n\t" \
  "v_readfirstlane_b32 s60, v4\n\t" \
  "s_add_u32 s55, s83, s36\n\t" \
  "v_mfma_f32_16x16x32_bf16 v[32:35], v[184:187], a[204:207], v[32:35]\n\t" \
  "s_cmp_eq_u32 s48, 63\n\t" \
  "s_cselect_b32 s56, 0, s60\n\t" \
  "v_mfma_f32_16x16x32_bf16 v[36:39], v[188:191], a[204:207], v[36:39]\n\t" \
  "s_cmp_eq_u32 s56, 0\n\t" \
  "s_cselect_b32 s55, 0xf0000000, s55\n\t" \
  "v_mfma_f32_16x16x32_bf16 v[32:35], v[192:195], a[208:211], v[32:35]\n\t" \
  "v_mfma_f32_16x16x32_bf16 v[36:39], v[196:199], a[208:211], v[36:39]\n\t" \
  "v_mfma_f32_16x16x32_bf16 v[32:35], v[200:203], a[212:215], v[32:35]\n\t" \
  "s_add_u32 m0, s46, 0\n\t" \
  "s_add_u32 s47, s55, 0\n\t" \
  "buffer_load_dwordx4 v0, %[X], s47 offen sc1 lds\n\t" \
  "v_mfma_f32_16x16x32_bf16 v[36:39], v[204:207], a[212:215], v[36:39]\n\t" \
  "v_mfma_f32_16x16x32_bf16 v[40:43], v[64:67], a[216:219], 0\n\t" \
  "v_mfma_f32_16x16x32_bf16 v[44:47], v[68:71], a[216:219], 0\n\t" \
  "v_mfma_f32_16x16x32_bf16 v[40:43], v[72:75], a[220:223], v[40:43]\n\t" \
  "v_cvt_pk_bf16_f32 v56, v32, v33\n\t" \
  "v_cvt_pk_bf16_f32 v57, v34, v35\n\t" \
  "s_add_u32 m0, s46, 4096\n\t" \
  "s_add_u32 s47, s55, 4096\n\t" \
  "buffer_load_dwordx4 v0, %[X], s47 offen sc1 lds\n\t" \
  "v_mfma_f32_16x16x32_bf16 v[44:47], v[76:79], a[220:223], v[44:47]\n\t" \
  "v_cvt_pk_bf16_f32 v58, v36, v37\n\t" \
  "v_cvt_pk_bf16_f32 v59, v38, v39\n\t" \
  "v_mfma_f32_16x16x32_bf16 v[40:43], v[80:83], a[224:227], v[40:43]\n\t" \
  "v_mfma_f32_16x16x32_bf16 v[44:47], v[84:87], a[224:227], v[44:47]\n\t" \
  "buffer_store_dwordx4 v[56:59], v0, %[X], s50 offen offset:2048 sc1\n\t" \
  "v_mfma_f32_16x16x32_bf16 v[40:43], v[88:91], a[228:231], v[40:43]\n\t" \
  "s_add_u32 m0, s46, 8192\n\t" \
  "s_add_u32 s47, s55, 16384\n\t" \
  "buffer_load_dwordx4 v0, %[X], s47 offen sc1 lds\n\t" \
  "v_mfma_f32_16x16x32_bf16 v[44:47], v[92:95], a[228:231], v[44:47]\n\t" \
  "v_mfma_f32_16x16x32_bf16 v[40:43], v[96:99], a[232:235], v[40:43]\n\t" \
  "v_mfma_f32_16x16x32_bf16 v[44:47], v[100:103], a[232:235], v[44:47]\n\t" \
  "v_mfma_f32_16x16x32_bf16 v[40:43], v[104:107], a[236:239], v[40:43]\n\t" \
  "s_add_u32 m0, s46, 12288\n\t" \
  "s_add_u32 s47, s55, 20480\n\t" \
  "buffer_load_dwordx4 v0, %[X], s47 offen sc1 lds\n\t" \
  "v_mfma_f32_16x16x32_bf16 v[44:47], v[108:111], a[236:239], v[44:47]\n\t" \
  "v_mfma_f32_16x16x32_bf16 v[40:43], v[112:115], a[240:243], v[40:43]\n\t" \
  "v_mfma_f32_16x16x32_bf16 v[44:47], v[116:119], a[240:243], v[44:47]\n\t" \
  "v_mfma_f32_16x16x32_bf16 v[40:43], v[120:123], a[244:247], v[40:43]\n\t" \
  "s_add_u32 m0, s46, 16384\n\t" \
  "s_add_u32 s47, s55, 32768\n\t" \
  "buffer_load_dwordx4 v0, %[X], s47 offen sc1 lds\n\t" \
  "v_mfma_f32_16x16x32_bf16 v[44:47], v[124:127], a[244:247], v[44:47]\n\t" \
  "v_mfma_f32_16x16x32_bf16 v[40:43], v[128:131], a[248:251], v[40:43]\n\t" \
  "v_mfma_f32_16x16x32_bf16 v[44:47], v[132:135], a[248:251], v[44:47]\n\t" \
  "v_mfma_f32_16x16x32_bf16 v[40:43], v[136:139], a[252:255], v[40:43]\n\t" \
  "s_add_u32 m0, s46, 20480\n\t" \
  "s_add_u32 s47, s55, 36864\n\t" \
  "buffer_load_dwordx4 v0, %[X], s47 offen sc1 lds\n\t" \
  "v_mfma_f32_16x16x32_bf16 v[44:47], v[140:143], a[252:255], v[44:47]\n\t" \
  "v_mfma_f32_16x16x32_bf16 v[40:43], v[144:147], v[224:227], v[40:43]\n\t" \
  "v_mfma_f32_16x16x32_bf16 v[44:47], v[148:151], v[224:227], v[44:47]\n\t" \
  "v_mfma_f32_16x16x32_bf16 v[40:43], v[152:155], v[228:231], v[40:43]\n\t" \
  "s_add_u32 m0, s46, 24576\n\t" \
  "s_add_u32 s47, s55, 49152\n\t" \
  "buffer_load_dwordx4 v0, %[X], s47 offen sc1 lds\n\t" \
  "v_mfma_f32_16x16x32_bf16 v[44:47], v[156:159], v[228:231], v[44:47]\n\t" \
  "v_mfma_f32_16x16x32_bf16 v[40:43], v[160:163], v[232:235], v[40:43]\n\t" \
  "v_mfma_f32_16x16x32_bf16 v[44:47], v[164:167], v[232:235], v[44:47]\n\t" \
  "v_mfma_f32_16x16x32_bf16 v[40:43], v[168:171], v[236:239], v[40:43]\n\t" \
  "s_add_u32 m0, s46, 28672\n\t" \
  "s_add_u32 s47, s55, 53248\n\t" \
  "buffer_load_dwordx4 v0, %[X], s47 offen sc1 lds\n\t" \
  "v_mfma_f32_16x16x32_bf16 v[44:47], v[172:175], v[236:239], v[44:47]\n\t" \
  "v_mfma_f32_16x16x32_bf16 v[40:43], v[176:179], v[240:243], v[40:43]\n\t" \
  "v_mfma_f32_16x16x32_bf16 v[44:47], v[180:183], v[240:243], v[44:47]\n\t" \
  "v_mfma_f32_16x16x32_bf16 v[40:43], v[184:187], v[244:247], v[40:43]\n\t" \
  "s_add_u32 m0, s46, 32768\n\t" \
  "s_add_u32 s47, s55, 65536\n\t" \
  "buffer_load_dwordx4 v0, %[X], s47 offen sc1 lds\n\t" \
  "v_mfma_f32_16x16x32_bf16 v[44:47], v[188:191], v[244:247], v[44:47]\n\t" \
  "v_mfma_f32_16x16x32_bf16 v[40:43], v[192:195], v[248:251], v[40:43]\n\t" \
  "v_mfma_f32_16x16x32_bf16 v[44:47], v[196:199], v[248:251], v[44:47]\n\t" \
  "buffer_load_dword v8, off, %[X], s57 sc1\n\t" \
  "v_mfma_f32_16x16x32_bf16 v[40:43], v[200:203], v[252:255], v[40:43]\n\t" \
  "v_mfma_f32_16x16x32_bf16 v[44:47], v[204:207], v[252:255], v[44:47]\n\t" \
  "s_nop 4\n\t" \
  "v_cvt_pk_bf16_f32 v60, v40, v41\n\t" \
  "v_cvt_pk_bf16_f32 v61, v42, v43\n\t" \
  "s_nop 1\n\t" \
  "v_cvt_pk_bf16_f32 v62, v44, v45\n\t" \
  "v_cvt_pk_bf16_f32 v63, v46, v47\n\t" \
  "buffer_store_dwordx4 v[60:63], v0, %[X], s50 offen offset:3072 sc1\n\t" \
  "s_add_u32 s49, s82, s38\n\t" \
  "s_cmp_eq_u32 s48, 63\n\t" \
  "s_cbranch_scc1 LEND_%=\n\t" \
  "s_add_u32 s48, s48, 1\n\t" \
  "s_cmp_eq_u32 s56, 0\n\t" \
  "s_cbranch_scc1 LCOLD_%=\n\t" \
  "s_waitcnt vmcnt(0)\n\t" \
  "s_mov_b64 exec, 1\n\t" \
  "buffer_store_dword v6, off, %[X], s49 sc1\n\t" \
  "s_mov_b64 exec, -1\n\t" \
  "s_mov_b32 s49, 0xf0000000\n\t" \
  "s_cmp_lt_i32 s57, 0\n\t" \
  "s_cbranch_scc1 LTOP_%=\n\t" \
  "s_movk_i32 s63, 4000\n\t" \
  "s_branch LGW_%=u\n\t" \
  "LGW_%=:\n\t" \
  "s_sleep " TK_PSG "\n\t" \
  "buffer_load_dword v8, off, %[X], s57 sc1\n\t" \
  "s_waitcnt vmcnt(0)\n\t" \
  "LGW_%=u:\n\t" \
  "s_memrealtime s[52:53]\n\t" \
  "v_readfirstlane_b32 s60, v8\n\t" \
  "s_waitcnt lgkmcnt(0)\n\t" \
  "s_sub_u32 s63, s63, 1\n\t" \
  "s_cbranch_scc1 LTOP_%=\n\t" \
  "s_cmp_eq_u32 s60, -1\n\t" \
  "s_cbranch_scc1 LGW_%=\n\t" \
  "s_not_b32 s60, s60\n\t" \
  "s_lshl_b32 s61, s52, 8\n\t" \
  "s_sub_u32 s60, s60, s61\n\t" \
  "s_ashr_i32 s60, s60, 8\n\t" \
  "s_sub_i32 s60, s60, " TK_QXW "\n\t" \
  "s_min_i32 s60, s60, 400\n\t" \
  "s_add_u32 s54, s52, s60\n\t" \
  "LGW_%=w:\n\t" \
  "s_sleep 1\n\t" \
  "s_memrealtime s[52:53]\n\t" \
  "s_waitcnt lgkmcnt(0)\n\t" \
  "s_sub_i32 s53, s52, s54\n\t" \
  "s_cmp_lt_i32 s53, 0\n\t" \
  "s_cbranch_scc1 LGW_%=w\n\t" \
  "s_branch LTOP_%=\n\t" \
  "LEND_%=:\n\t" \
  "s_waitcnt vmcnt(0)\n\t" \
  "s_mov_b64 exec, 1\n\t" \
  "buffer_store_dword v6, off, %[X], s49 sc1\n\t" \
  "s_mov_b64 exec, -1\n\t" \
  "s_mov_b32 s49, 0xf0000000\n\t" \
  ""
__device__ __forceinline__ unsigned hoff(int r, int piece) {
  const int m = 4 * (r >> 3) + (r & 3), kp = piece >> 3;
  return (P_H + (unsigned)(kp >> 1)) * PAIR + (unsigned)((kp & 1) * 4096 + (16 * ((r >> 2) & 1) + m) * 128 + (((piece ^ m) & 7) << 4));
}
template <int SLEEP>
__device__ __forceinline__ void wait_all(rsrc_t X, unsigned vo, unsigned so) {
  unsigned spins = 0;
  while (true) {
    asm volatile("" ::: "memory");
    if (__all(ld4(X, vo, so) != 0u)) break;
    if (spin_fail(spins, X)) break;
    __builtin_amdgcn_s_sleep(SLEEP);
  }
  asm volatile("" ::: "memory");
}
#define VQ_CLOB "v8", "v16", "v17", "v18", "v19", "v20", "v21", "v22", "v23", "v24", "v25", "v26", "v27", "v28", "v29", "v30", "v31", "v32", "v33", "v34", "v35", "v36", "v37", "v38", "v39", "v40", "v41", "v42", "v43", "v44", "v45", "v46", "v47", "v48", "v49", "v50", "v51", "v52", "v53", "v54", "v55", "v56", "v57", "v58", "v59", "v60", "v61", "v62", "v63", "v64", "v65", "v66", "v67", "v68", "v69", "v70", "v71", "v72", "v73", "v74", "v75", "v76", "v77", "v78", "v79", "v80", "v81", "v82", "v83", "v84", "v85", "v86", "v87", "v88", "v89", "v90", "v91", "v92", "v93", "v94", "v95", "v96", "v97", "v98", "v99", "v100", "v101", "v102", "v103", "v104", "v105", "v106", "v107", "v108", "v109", "v110", "v111", "v112", "v113", "v114", "v115", "v116", "v117", "v118", "v119", "v120", "v121", "v122", "v123", "v124", "v125", "v126", "v127", "v128", "v129", "v130", "v131", "v132", "v133", "v134", "v135", "v136", "v137", "v138", "v139", "v140", "v141", "v142", "v143", "v144", "v145", "v146", "v147", "v148", "v149", "v150", "v151", "v152", "v153", "v154", "v155", "v156", "v157", "v158", "v159", "v160", "v161", "v162", "v163", "v164", "v165", "v166", "v167", "v168", "v169", "v170", "v171", "v172", "v173", "v174", "v175", "v176", "v177", "v178", "v179", "v180", "v181", "v182", "v183", "v184", "v185", "v186", "v187", "v188", "v189", "v190", "v191", "v192", "v193", "v194", "v195", "v196", "v197", "v198", "v199", "v200", "v201", "v202", "v203", "v204", "v205", "v206", "v207", "v220", "v221"
#define VA_CLOB "v40", "v41", "v42", "v43", "v44", "v45", "v46", "v47", "v48", "v49", "v50", "v51", "v52", "v53", "v54", "v55", "v56", "v57", "v58", "v59", "v60", "v61", "v62", "v63", "v64", "v65", "v66", "v67", "v68", "v69", "v70", "v71", "v72", "v73", "v74", "v75", "v76", "v77", "v78", "v79", "v80", "v81", "v82", "v83", "v84", "v85", "v86", "v87", "v88", "v89", "v90", "v91", "v92", "v93", "v94", "v95", "v96", "v97", "v98", "v99", "v100", "v101", "v102", "v103", "v104", "v105", "v106", "v107", "v108", "v109", "v110", "v111", "v112", "v113", "v114", "v115", "v116", "v117", "v118", "v119", "v120", "v121", "v122", "v123", "v124", "v125", "v126", "v127", "v128", "v129", "v130", "v131", "v132", "v133", "v134", "v135", "v136", "v137", "v138", "v139", "v140", "v141", "v142", "v143", "v144", "v145", "v146", "v147", "v148", "v149", "v150", "v151", "v152", "v153", "v154", "v155", "v156", "v157", "v158", "v159", "v160", "v161", "v162", "v163", "v164", "v165", "v166", "v167", "v168", "v169", "v170", "v171", "v172", "v173", "v174", "v175", "v176", "v177", "v178", "v179", "v180", "v181", "v182", "v183", "v184", "v185", "v186", "v187", "v188", "v189", "v190", "v191", "v192", "v193", "v194", "v195", "v196", "v197", "v198", "v199", "v200", "v201", "v202", "v203", "v204", "v205", "v206", "v207", "v208", "v209", "v210", "v211", "v220", "v221"
#define TOK_COMMON() \
  const MP p = getp(); \
  const int c = (int)blockIdx.x - NCHW, xq = c & 3, L = (c >> 2) & 1, i = c >> 3; \
  const int tid = threadIdx.x, lane = tid & 63, w = __builtin_amdgcn_readfirstlane(tid >> 6); \
  const rsrc_t X = mkr(p.xb, XB_BYTES)
#define W_IN [W] "s"(WR)
#define W_CLOB "a0", "a1", "a2", "a3", "a4", "a5", "a6", "a7", "a8", "a9", "a10", "a11", "a12", "a13", "a14", "a15", "a16", "a17", "a18", "a19", "a20", "a21", "a22", "a23", "a24", "a25", "a26", "a27", "a28", "a29", "a30", "a31", "a32", "a33", "a34", "a35", "a36", "a37", "a38", "a39", "a40", "a41", "a42", "a43", "a44", "a45", "a46", "a47", "a48", "a49", "a50", "a51", "a52", "a53", "a54", "a55", "a56", "a57", "a58", "a59", "a60", "a61", "a62", "a63", "a64", "a65", "a66", "a67", "a68", "a69", "a70", "a71", "a72", "a73", "a74", "a75", "a76", "a77", "a78", "a79", "a80", "a81", "a82", "a83", "a84", "a85", "a86", "a87", "a88", "a89", "a90", "a91", "a92", "a93", "a94", "a95", "a96", "a97", "a98", "a99", "a100", "a101", "a102", "a103", "a104", "a105", "a106", "a107", "a108", "a109", "a110", "a111", "a112", "a113", "a114", "a115", "a116", "a117", "a118", "a119", "a120", "a121", "a122", "a123", "a124", "a125", "a126", "a127", "a128", "a129", "a130", "a131", "a132", "a133", "a134", "a135", "a136", "a137", "a138", "a139", "a140", "a141", "a142", "a143", "a144", "a145", "a146", "a147", "a148", "a149", "a150", "a151", "a152", "a153", "a154", "a155", "a156", "a157", "a158", "a159", "a160", "a161", "a162", "a163", "a164", "a165", "a166", "a167", "a168", "a169", "a170", "a171", "a172", "a173", "a174", "a175", "a176", "a177", "a178", "a179", "a180", "a181", "a182", "a183", "a184", "a185", "a186", "a187", "a188", "a189", "a190", "a191", "a192", "a193", "a194", "a195", "a196", "a197", "a198", "a199", "a200", "a201", "a202", "a203", "a204", "a205", "a206", "a207", "a208", "a209", "a210", "a211", "a212", "a213", "a214", "a215", "a216", "a217", "a218", "a219", "a220", "a221", "a222", "a223", "a224", "a225", "a226", "a227", "a228", "a229", "a230", "a231", "a232", "a233", "a234", "a235", "a236", "a237", "a238", "a239", "a240", "a241", "a242", "a243", "a244", "a245", "a246", "a247", "a248", "a249", "a250", "a251", "a252", "a253", "a254", "a255", "v224", "v225", "v226", "v227", "v228", "v229", "v230", "v231", "v232", "v233", "v234", "v235", "v236", "v237", "v238", "v239", "v240", "v241", "v242", "v243", "v244", "v245", "v246", "v247", "v248", "v249", "v250", "v251", "v252", "v253", "v254", "v255"
#define BOOT_IN "{s67}"(L), [ctab] "i"(CTAB_OFF)
#define S_CLOB "s76", "s78", "s79", "s86", "s88", "s89", "s90", "s91", "s46", "s47", "s48", "s49", "s50", "s51", "s52", "s53", "s54", "s55", "s56", "s57", "s58", "s59", "s60", "s61", "s62", "s63", "s68", "s69", "s70", "s71", "s72", "s73", "s82", "s83", "s84", "m0", "vcc", "scc", "memory"
typedef unsigned v8w __attribute__((ext_vector_type(8)));

__device__ __forceinline__ void qkv_role() {
  TOK_COMMON();
  const int role = i * 4 + w, hh = 8 * xq + role / 6, part = role % 6, which = part >> 1, half = part & 1;
  const rsrc_t WR = mkr((which == 0 ? p.Wq : (which == 1 ? p.Wk : p.Wv)) + hh * DH + 64 * half, DM * HD * 2u - (unsigned)(hh * DH + 64 * half) * 2u);
  const int m = lane & 15, g = lane >> 4;
  const unsigned a0 = (unsigned)(m * 128 + ((g ^ (m & 7)) << 4));
  v8w lv = {(unsigned)lane * 16u, a0, a0 ^ 64u, (unsigned)(lane & (L ? 15 : 3)) * 128u, 0u, 40960u + 4u * (unsigned)w, 1u, 0u};
  const unsigned c36 = P_H * PAIR + (unsigned)w * 1024u, c37 = (unsigned)(3 * hh + which) * PAIR + (unsigned)half * (which == 2 ? 4096u : 2048u);
  const unsigned c38 = P_FA * PAIR + (unsigned)(hh * 128 + part * 16), c39 = w == 0 ? P_FB * PAIR + 4096u : 0xF0000000u, c41 = (unsigned)w * 1024u;
  const int c42 = L ? 1 - QLEAD1 : 1 - QLEAD;
  unsigned d74 = TRC_OFF + 262144u + (unsigned)((c * 4 + w) * 66) * 8u;
#define Q_CALL(A) asm volatile("s_waitcnt vmcnt(0) lgkmcnt(0)\n\t" A : "+{v[0:7]}"(lv), "+{s74}"(d74) \
               : [X] "s"(X), "{s36}"(c36), "{s37}"(c37), "{s38}"(c38), "{s39}"(c39), "{s40}"(w), "{s41}"(c41), "{s42}"(c42), "{s45}"(i), "{s92}"(TMO_OFF), BOOT_IN, [cap] "i"(SPIN_CAP), [qxw] "i"(QXW), [qxc] "i"(QXC), W_IN : VQ_CLOB, W_CLOB, S_CLOB)
  if (which == 2) { Q_CALL(QV_ASM "LQVEND:"); __builtin_amdgcn_endpgm(); }
  Q_CALL(QK_ASM "LQKEND:");
  __builtin_amdgcn_endpgm();
}

__device__ __forceinline__ void ap_role() {
  TOK_COMMON();
  const int hp = 4 * xq + ((i - 12) >> 1), ch = i & 1, a = w >> 1, rt = w & 1, h = 2 * hp + a;
  const int n = lane & 15, g = lane >> 4;
  const rsrc_t WR = mkr(p.Wo + (long)hp * 2 * DH * DM, 2u * DH * DM * 2u);
  const unsigned l16 = (unsigned)lane * 16u, ab = (unsigned)a * 24576u + l16;
  v16u lv = {l16, ab, ab + (unsigned)rt * 4096u, 49152u + (unsigned)w * 4096u + l16, 49152u + (unsigned)(w ^ 2) * 4096u + l16, l16 & ~32u, l16 >> 1,
             (unsigned)(g * 262144 + n * 16), (unsigned)(n * 32 + g * 8), (unsigned)((lane & 7) % 6) * 16u, 0u, 0u, 0u, 0u, 0u, 0u};
  v8w mk;
  const int qi = 8 * (n >> 2) + (n & 3) + 4 * rt;
#pragma unroll
  for (int k = 0; k < 8; ++k) mk[k] = 8 * g + k > qi ? 0xFF800000u : 0u;
  v16u zz = {0u, 0u, 0u, 0u, 0u, 0u, 0u, 0u, 0u, 0u, 0u, 0u, 0u, 0u, 0u, 0u};
  const unsigned tb = (unsigned)(3 * h) * PAIR;
  const unsigned c36 = tb + (rt ? PAIR + 4096u : 0u), c37 = tb + (rt ? 2u * PAIR : 4096u), c38 = tb + (rt ? 2u * PAIR + 4096u : PAIR);
  const unsigned c39 = (P_KV + (unsigned)(h >> 1)) * PAIR + (unsigned)(h & 1) * 4096u, c41 = P_FA * PAIR + (unsigned)h * 128u;
  const unsigned c42 = w == 0 ? P_FB * PAIR + (unsigned)(2 * hp + ch) * 128u : 0xF0000000u;
  const unsigned c43 = TP0 * XBLK + (unsigned)(8 * rt + (hp >> 3)) * PAIR + (unsigned)((hp & 7) * 1024 + ch * 512 + a * 256);
  unsigned d44 = (TP0 + 8u) * XBLK + (unsigned)(hp >> 1) * PAIR + (unsigned)((((hp & 1) * 4 + 2 * ch + a) * 2 + rt) * 512);
  const unsigned c45 = (unsigned)(a * 24576 + rt * 12288);
  unsigned d74 = TRC_OFF + 262144u + (unsigned)((c * 4 + w) * 66) * 8u;
  asm volatile("s_waitcnt vmcnt(0) lgkmcnt(0)\n\t" AP_ASM "LAPEND:" : "+{v[0:15]}"(lv), "+{v[24:39]}"(zz), "+{s74}"(d74), "+{s44}"(d44)
               : [X] "s"(X), "{s36}"(c36), "{s37}"(c37), "{s38}"(c38), "{s39}"(c39), "{s41}"(c41), "{s42}"(c42), "{s43}"(c43), "{s45}"(c45), "{s64}"(512 * ch + 256 * a), "{s65}"(1024 + 64 * ch + 32 * a), "{s66}"(147456 * a), BOOT_IN,
                 "{s92}"(TMO_OFF), [cap] "i"(SPIN_CAP), "{v[16:23]}"(mk), W_IN : VA_CLOB, W_CLOB, S_CLOB);
  __builtin_amdgcn_endpgm();
}

template <int N> __device__ __forceinline__ float rorN(float v) { return __uint_as_float((unsigned)__builtin_amdgcn_update_dpp(0, (int)__float_as_uint(v), 0x120 + N, 0xf, 0xf, false)); }
__device__ __forceinline__ void up8(v4u v, float* x) {
  x[0] = lo2f(v.x); x[1] = hi2f(v.x); x[2] = lo2f(v.y); x[3] = hi2f(v.y); x[4] = lo2f(v.z); x[5] = hi2f(v.z); x[6] = lo2f(v.w); x[7] = hi2f(v.w);
}
__device__ __forceinline__ v4u pk8(const float* x) { return (v4u){pk2(x[0], x[1]), pk2(x[2], x[3]), pk2(x[4], x[5]), pk2(x[6], x[7])}; }

__device__ __forceinline__ void rb9(float (&x)[9]) {
#pragma unroll
  for (int i = 0; i < 8; i += 2) { const unsigned u = pk2(x[i], x[i + 1]); x[i] = lo2f(u); x[i + 1] = hi2f(u); }
  x[8] = lo2f(pk2(x[8], x[8]));
}
template <int R>
__device__ __forceinline__ void lnR(const float (&x)[R][9], float (&o)[R][9]) {
  float s[2 * R], pv[R];
#pragma unroll
  for (int r = 0; r < R; ++r) {
    pv[r] = __uint_as_float(__builtin_amdgcn_readfirstlane(__float_as_uint(x[r][0])));
    const f32x2 p2 = {pv[r], pv[r]};
    f32x2 d[4];
#pragma unroll
    for (int i = 0; i < 4; ++i) d[i] = (f32x2){x[r][2 * i], x[r][2 * i + 1]} - p2;
    const float d8 = x[r][8] - pv[r];
    const f32x2 a = (d[0] + d[1]) + (d[2] + d[3]);
    const f32x2 q = __builtin_elementwise_fma(d[3], d[3], __builtin_elementwise_fma(d[2], d[2], __builtin_elementwise_fma(d[1], d[1], d[0] * d[0])));
    s[2 * r] = (a[0] + d8) + a[1];
    s[2 * r + 1] = __builtin_fmaf(d8, d8, q[0]) + q[1];
  }
#pragma unroll
  for (int k = 0; k < 2 * R; ++k) s[k] += rorN<8>(s[k]);
#pragma unroll
  for (int k = 0; k < 2 * R; ++k) s[k] += rorN<4>(s[k]);
#pragma unroll
  for (int k = 0; k < 2 * R; ++k) s[k] += rorN<2>(s[k]);
#pragma unroll
  for (int k = 0; k < 2 * R; ++k) s[k] += rorN<1>(s[k]);
#pragma unroll
  for (int r = 0; r < R; ++r) {
    auto u = __builtin_amdgcn_permlane16_swap(__float_as_uint(s[2 * r]), __float_as_uint(s[2 * r + 1]), false, false);
    s[2 * r] = __uint_as_float(u[0]) + __uint_as_float(u[1]);
  }
#pragma unroll
  for (int r = 0; r < R; ++r) {
    const unsigned u = __float_as_uint(s[2 * r]);
    const float md = (__uint_as_float(__builtin_amdgcn_readlane(u, 0)) + __uint_as_float(__builtin_amdgcn_readlane(u, 32))) * (1.0f / DM);
    const float sq = (__uint_as_float(__builtin_amdgcn_readlane(u, 16)) + __uint_as_float(__builtin_amdgcn_readlane(u, 48))) * (1.0f / DM);
    const unsigned mv = pk2(pv[r] + md, __builtin_fmaxf(__builtin_fmaf(-md, md, sq), 0.f));
    const float m = lo2f(mv), var = hi2f(mv);
    const float ve = lo2f(pk2(var + 1e-5f, 0.f));
    const float den = lo2f(pk2(__builtin_amdgcn_sqrtf(ve), 0.f));
    const float inv = __builtin_amdgcn_rcpf(den);
#pragma unroll
    for (int i = 0; i < 9; ++i) o[r][i] = (x[r][i] - m) * inv;
  }
}
__device__ __forceinline__ void ln9(const float (&x)[9], float (&o)[9]) { lnR<1>((const float (&)[1][9])x, (float (&)[1][9])o); }
__device__ __forceinline__ v4u gat8(float v) {
  const unsigned d = pk2(v, __uint_as_float((unsigned)__builtin_amdgcn_mov_dpp((int)__float_as_uint(v), 0xB1, 0xf, 0xf, true)));
  return (v4u){d, (unsigned)__builtin_amdgcn_mov_dpp((int)d, 0x102, 0xf, 0xf, true), (unsigned)__builtin_amdgcn_mov_dpp((int)d, 0x104, 0xf, 0xf, true),
               (unsigned)__builtin_amdgcn_mov_dpp((int)d, 0x106, 0xf, 0xf, true)};
}
__device__ __forceinline__ void tok_row(const MP& p, const rsrc_t X, unsigned sb1, int L, int s, int r, int lane, const v4u (&v)[18], v4u px, v4u py, unsigned px9, unsigned py9) {
  const long tok = (long)s * SEG + r;
  const int m = lane & 15, g = lane >> 4;
  const int xp = 4 * (g & 1) + 2 * (g >> 1) + (m >> 3);
  const unsigned zr = opq(0u);
  const unsigned one = (g == (m >> 2)) ? (opq(0x3F80u) << (16 * (m & 1))) : zr;
  const unsigned ax = (m & 2) ? zr : one, ay = (m & 2) ? one : zr;
  const v4u A1 = {ax, ay, zr, zr}, A2 = {zr, zr, ax, ay};
  const unsigned on2 = opq(0x3F80u) << (16 * (m & 1));
  const int q2 = (m >> 1) & 3;
  const v4u sel = {q2 == 0 ? on2 : zr, q2 == 1 ? on2 : zr, q2 == 2 ? on2 : zr, q2 == 3 ? on2 : zr};
  f32x4 d1, d2, e1;
  asm volatile("s_nop 1\n\t" MF "%0, %1, %3, 0\n\t" MF "%0, %2, %3, %0" : "=&v"(e1) : "v"(v[0]), "v"(v[1]), "v"(sel));
#pragma unroll
  for (int i = 0; i < 16; ++i) {
    if (i == 0) asm volatile("s_nop 1\n\t" MF "%0, %2, %4, 0\n\t" MF "%1, %3, %4, 0" : "=&v"(d1), "=&v"(d2) : "v"(A1), "v"(A2), "v"(v[2]));
    else asm volatile(MF "%0, %2, %4, %0\n\t" MF "%1, %3, %4, %1" : "+v"(d1), "+v"(d2) : "v"(A1), "v"(A2), "v"(v[2 + i]));
  }
  asm volatile("s_nop 7" : "+v"(d1), "+v"(d2), "+v"(e1));
  float o[9];
#pragma unroll
  for (int i = 0; i < 4; ++i) { o[i] = d1[i]; o[4 + i] = d2[i]; }
  {
    auto a02 = __builtin_amdgcn_permlane32_swap(__float_as_uint(e1[0]), __float_as_uint(e1[2]), false, false);
    auto a13 = __builtin_amdgcn_permlane32_swap(__float_as_uint(e1[1]), __float_as_uint(e1[3]), false, false);
    o[8] = m >= 8 ? __uint_as_float(a13[0]) + __uint_as_float(a13[1]) : __uint_as_float(a02[0]) + __uint_as_float(a02[1]);
  }
  float x[9];
  up8(px, x); x[8] = lo2f(px9);
#pragma unroll
  for (int i = 0; i < 9; ++i) x[i] += o[i];
  if (L == 0) {
    float y[9], ln[9];
    up8(py, y); y[8] = lo2f(py9);
    rb9(x);
    ln9(x, ln);
#pragma unroll
    for (int i = 0; i < 9; ++i) x[i] = __builtin_fmaf(ln[i], y[i], x[i]);
    rb9(x);
    st16(X, (unsigned)lane * 16u, sb1 + (P_X3 + (unsigned)(r >> 3)) * PAIR + (unsigned)(r & 7) * 1024u, pk8(x));
    const v4u x9 = gat8(x[8]);
    if ((lane & 7) == 0) st16(X, (unsigned)xp * 16u, sb1 + (P_H + 4u) * PAIR + 4096u + (unsigned)r * 128u, x9);
    ln9(x, ln);
    st16(X, hoff(r, lane), sb1, pk8(ln));
    const v4u h9 = gat8(ln[8]);
    if ((lane & 7) == 0) st16(X, hoff(r, 64 + xp), sb1, h9);
  } else {
    const rsrc_t O = mkr(p.out + tok * DM, DM * 2u);
    __builtin_amdgcn_raw_buffer_store_b128(pk8(x), O, (unsigned)lane * 16u, 0u, OAUX);
    __builtin_amdgcn_raw_buffer_store_b128(gat8(x[8]), O, (lane & 7) == 0 ? 1024u + (unsigned)xp * 16u : 0xF0000000u, 0u, OAUX);
  }
}

__device__ __forceinline__ void row_role(char* smem) {
  TOK_COMMON();
  const int rw = 4 * xq + (i - 20), r = 2 * rw + (w >> 1);
  const unsigned long long sm = die_mask(X, SG0, L, lane);
  const unsigned tm = (unsigned)die_mask(X, TP0, L, lane), dm = (unsigned)die_mask(X, 0u, L, lane);
  __attribute__((address_space(3))) unsigned* cnt = (__attribute__((address_space(3))) unsigned*)smem;
  BAR();
  if (tid < 64) cnt[tid] = 0u;
  BAR();
  for (int s = w & 1; s < NSEG; s += 2) {
    int ln_ = lane; asm volatile("" : "+v"(ln_));
    const int m = ln_ & 15, g = ln_ >> 4;
    const unsigned l16 = (unsigned)ln_ * 16u, xo = (unsigned)(8 * (4 * (g & 1) + 2 * (g >> 1) + (m >> 3)) + (m & 7)) * 2u;
    const unsigned sb = SEGB(sm, s), sb1 = sb ^ (L ? 0u : 8192u);
    if (s + ARMK < NSEG) {
      const unsigned sv = opq(SENT), sl = SLOT_AT(s + ARMK) + ((dm >> ((s + ARMK) / 3)) & 1u) * 8192u;
      for (int j = r; j < 84; j += 32) {
#pragma unroll
        for (int k = 0; k < 4; ++k) st16(X, l16 + (unsigned)k * 1024u, sl + (unsigned)(j >> 1) * PAIR + (unsigned)(j & 1) * 4096u, (v4u){sv, sv, sv, sv});
      }
    }
    wait_all<PSL>(X, 0u, sb + P_FB * PAIR + 4096u + (unsigned)(L ? rw : r >> 3) * 128u);
    const v4u px = ld16(X, l16, sb + (P_X3 + (unsigned)(r >> 3)) * PAIR + (unsigned)(r & 7) * 1024u);
    const unsigned px9 = __builtin_amdgcn_raw_buffer_load_b16(X, xo, sb + (P_H + 4u) * PAIR + 4096u + (unsigned)r * 128u, 16);
    const rsrc_t YX = mkr(p.yT + (2 * s + (r >> 4)) * 9216, L ? 0u : 18432u);
    const v2u ya = ld8(YX, (unsigned)(ln_ * 256 + ((r ^ m) & 15) * 8), 0u), yb = ld8(YX, (unsigned)(ln_ * 256 + 128 + ((r ^ m) & 15) * 8), 0u);
    const v4u py = {ya.x, ya.y, yb.x, yb.y};
    const unsigned py9 = __builtin_amdgcn_raw_buffer_load_b16(YX, 16384u + (xo >> 3) * 128u + (((unsigned)r ^ (xo >> 4)) & 15u) * 8u + (xo & 6u), 0u, 16);
    wait_all<PSP>(X, 0u, sb + P_FB * PAIR + (unsigned)r * 128u);
    __builtin_amdgcn_s_sleep(RONE);
    wait_all<PSP>(X, (unsigned)(ln_ & 31) * 128u, sb + P_FB * PAIR);
    const unsigned j = (unsigned)s & 15u, to = TP0 * XBLK + (j << 20) + ((tm >> (j >> 1)) & 1u) * 8192u + (unsigned)r * 2u * PAIR;
    const unsigned t9 = (TP0 + 8u) * XBLK + ((tm >> 8) & 1u) * 8192u + (j << 17) + (unsigned)(((r >> 2) & 1) * 512 + (4 * (r >> 3) + (r & 3)) * 32);
    const unsigned v9 = (unsigned)(g * (int)PAIR + (m >> 1) * 1024 + (m & 1) * 16);
    v4u v[18];
#pragma unroll
    for (int k = 0; k < 2; ++k) v[k] = ld16(X, v9, t9 + (unsigned)k * 4u * PAIR);
#pragma unroll
    for (int k = 0; k < 16; ++k) v[2 + k] = ld16(X, l16 + (unsigned)(k & 3) * 1024u, to + (unsigned)(k >> 3) * PAIR + (unsigned)(k & 4) * 1024u);
    SB();
    tok_row(p, X, sb1, L, s, r, ln_, v, px, py, px9, py9);
    VM0();
    unsigned o1 = 0u;
    if (ln_ == 0) o1 = __hip_atomic_fetch_add(cnt + s, 1u, __ATOMIC_RELAXED, __HIP_MEMORY_SCOPE_WORKGROUP);
    if (__builtin_amdgcn_readfirstlane(o1) == 1u && ln_ < 2 - L) st4(X, ln_ ? sb1 : sb + 2048u, P_FB * PAIR + 4096u + (unsigned)rw * 128u, opq(1u));
  }
}

__device__ __forceinline__ void rows_task(const MP& p, const rsrc_t X, unsigned long long sm, u16* ys, int grp, int ng) {
  const int tid = threadIdx.x, lane = tid & 63, w = __builtin_amdgcn_readfirstlane(tid >> 6);
  const int m = lane & 15, g = lane >> 4;
  const int xp = 4 * (g & 1) + 2 * (g >> 1) + (m >> 3), c9 = 512 + 8 * xp + (m & 7);
  const unsigned sb = SEGB(sm, grp >> 2);
  v4u yv[5], xa[4];
  unsigned x9[4];
#pragma unroll
  for (int i = 0; i < 5; ++i) yv[i] = ld16(mkr(p.yT + (grp >> 1) * 9216, 18432u), (unsigned)(tid + 256 * i) * 16u, 0u);
#pragma unroll
  for (int rr = 0; rr < 4; ++rr) {
    const long t = grp * 8 + w + 4 * (rr & (2 * ng - 1));
    xa[rr] = *(const v4u*)&p.xin[t * DM + 8 * lane]; x9[rr] = p.xin[t * DM + c9];
  }
#pragma unroll
  for (int i = 0; i < 5; ++i) ((v4u*)ys)[tid + 256 * i] = yv[i];
  BAR();
  const unsigned o9 = (lane & 7) == 0 ? 0u : 0xF0000000u;
#pragma unroll 1
  for (int k = 0; k < ng; ++k) {
    float x[2][9], y[2][9], ln[2][9];
    const int t0 = (grp + k) * 8 + w;
#pragma unroll
    for (int rr = 0; rr < 2; ++rr) {
      const int j = (t0 + 4 * rr) & 15;
      const v2u ya = *(const v2u*)&ys[lane * 128 + (j ^ m) * 4], yb = *(const v2u*)&ys[lane * 128 + 64 + (j ^ m) * 4];
      up8(xa[rr], x[rr]); x[rr][8] = lo2f(x9[rr]); up8((v4u){ya.x, ya.y, yb.x, yb.y}, y[rr]); y[rr][8] = bf2f(ys[(c9 >> 2) * 64 + (j ^ xp) * 4 + (m & 3)]);
      xa[rr] = xa[2 + rr]; x9[rr] = x9[2 + rr];
    }
    lnR<2>(x, ln);
#pragma unroll
    for (int rr = 0; rr < 2; ++rr) {
#pragma unroll
      for (int i = 0; i < 9; ++i) x[rr][i] = __builtin_fmaf(ln[rr][i], y[rr][i], x[rr][i]);
      rb9(x[rr]);
      const int r = (t0 + 4 * rr) & 31;
      st16(X, (unsigned)lane * 16u, sb + (P_X3 + (unsigned)(r >> 3)) * PAIR + (unsigned)(r & 7) * 1024u, pk8(x[rr]));
      st16(X, (unsigned)xp * 16u + o9, sb + (P_H + 4u) * PAIR + 4096u + (unsigned)r * 128u, gat8(x[rr][8]));
    }
    lnR<2>(x, ln);
#pragma unroll
    for (int rr = 0; rr < 2; ++rr) {
      const int r = (t0 + 4 * rr) & 31;
      st16(X, hoff(r, lane), sb, pk8(ln[rr]));
      st16(X, hoff(r, 64 + xp) + o9, sb, gat8(ln[rr][8]));
    }
  }
  VM0();
  BAR();
  if (tid < ng) st4(X, (unsigned)tid * 128u, sb + P_FB * PAIR + 4096u + (unsigned)(grp & 3) * 128u, opq(1u));
}

__device__ __forceinline__ void arrive(const MP& p, int k, int grp) {
  VM0();
  BAR();
  if (threadIdx.x < 64) {
    unsigned old = 0u;
    if (threadIdx.x == 0) old = __hip_atomic_fetch_add(p.xb + MF_OFF / 4u + (k * 16 + grp) * 32, 1u, __ATOMIC_RELAXED, __HIP_MEMORY_SCOPE_AGENT);
    if (__builtin_amdgcn_readfirstlane(old) == 15u && threadIdx.x < 8) st4(mkr(p.xb, XB_BYTES), threadIdx.x * 128u, RDY_OFF(k) + (unsigned)grp * 1024u, opq(1u));
  }
}
__device__ __forceinline__ void wait_ready(const MP& p, int k) {
  if (threadIdx.x < 64) wait_all<PSL>(mkr(p.xb, XB_BYTES), (threadIdx.x % 9u) * 1024u, RDY_OFF(k) + (blockIdx.x & 7u) * 128u);
  BAR();
}
__device__ __forceinline__ void start_work(char* smem, int wk) {
  const MP p = getp();
  const int w = __builtin_amdgcn_readfirstlane(threadIdx.x >> 6);
#define NARM (ARMK - 3u)
  conv_task(p.kvT, p.ssm_k, p.ssm_D, p.yT, (u16*)(smem + w * 32768), 4 * wk + w, [&]() {
    const unsigned sv = opq(SENT);
    constexpr unsigned S16 = SLOT / 16u;
#pragma unroll
    for (unsigned k = 0; k < (NARM * S16 + 36863u) / 36864u; ++k) {
      const unsigned i = k * 36864u + wk * 256 + threadIdx.x;
      st16(mkr(p.xb, XB_BYTES), i < NARM * S16 ? SLOT_AT(3u + i / S16) + (i % S16) * 16u : 0xF0000000u, 0u, (v4u){sv, sv, sv, sv});
    }
  });
  arrive(p, 0, wk >> 4);
}
__device__ __forceinline__ void token_start(char* smem) {
  TOK_COMMON();
  (void)w;
  const int k = 4 * i + xq;
  if (L || (i >= 12 && i < 16)) start_work(smem, L ? 32 + k : 80 + k);
  if (i >= (L ? 12 : 20)) {
    const unsigned long long sm = die_mask(X, SG0, 0, lane);
    int g = L ? 111 - k : k - 96;
    while (g < 120) {
      if (g >= 0 && g < 64) {
        wait_ready(p, 0);
      }
      rows_task(p, X, sm, (u16*)smem, g < 0 ? g + 16 : 16 + 2 * g, g < 0 ? 1 : 2);
      g += g < 0 ? 16 : (L ? 48 : 120);
    }
  }
}
__device__ __forceinline__ void token_wg(char* smem) {
  if ((unsigned)(size_t)(__attribute__((address_space(3))) char*)smem != 0u) return;
  token_start(smem);
  const int i = ((int)blockIdx.x - NCHW) >> 3;
  if (i < 12) qkv_role();
  else if (i < 20) ap_role();
  else row_role(smem);
}

__global__ __launch_bounds__(256, 1) void k_main(MP) {
  extern __shared__ __attribute__((aligned(16))) char smem[];
  asm volatile("LKBEG:");
  if ((int)blockIdx.x < NCHW) chain_wg(smem);
  else token_wg(smem);
}

extern "C" void kernel_launch(void* const* d_in, const int* in_sizes, int n_in, void* d_out, int out_size, void* d_ws,
                              size_t ws_size, hipStream_t stream) {
  const u16* A_q = (const u16*)d_in[0];
  const u16* A_k = (const u16*)d_in[1];
  const u16* A_v = (const u16*)d_in[2];
  const u16* A_shift = (const u16*)d_in[3];
  const u16* A_ssmk = (const u16*)d_in[4];
  const u16* A_ssmD = (const u16*)d_in[5];
  const u16* B_mem0 = (const u16*)d_in[7];
  const u16* B_Wq = (const u16*)d_in[8];
  const u16* B_Wk = (const u16*)d_in[9];
  const u16* B_Wv = (const u16*)d_in[10];
  const u16* B_Wo = (const u16*)d_in[11];

  char* ws = (char*)d_ws;
  size_t off = (size_t)(-(uintptr_t)ws) & (XBLK - 1u);
  auto alloc = [&](size_t bytes) { char* q = ws + off; off += (bytes + 4095) & ~(size_t)4095; return q; };
  unsigned* xb = (unsigned*)alloc((size_t)XB_BYTES);
  u16* kvT = (u16*)alloc((size_t)DM * S_LEN * 2);
  u16* yT = (u16*)alloc((size_t)DM * S_LEN * 2);

  PreP pp;
  pp.kk = A_k; pp.vv = A_v; pp.shift_k = A_shift; pp.ssm_k = A_ssmk; pp.ssm_D = A_ssmD; pp.yT = yT; pp.kvT = kvT;
  pp.Wq = B_Wq; pp.Wk = B_Wk; pp.Wv = B_Wv; pp.Wo = B_Wo; pp.xb = xb; pp.mem0 = B_mem0;
  k_pre<<<240 + PRE_FILL + 256 + 288, 256, 0, stream>>>(pp);
  MP mp;
  mp.out = (u16*)d_out; mp.xb = xb;
  mp.xin = A_q; mp.yT = yT; mp.kvT = kvT; mp.ssm_k = A_ssmk; mp.ssm_D = A_ssmD;
  mp.Wq = B_Wq; mp.Wk = B_Wk; mp.Wv = B_Wv; mp.Wo = B_Wo;
  const int smem_bytes = 160 * 1024;
  (void)hipFuncSetAttribute((const void*)k_main, hipFuncAttributeMaxDynamicSharedMemorySize, smem_bytes);
  k_main<<<NCHW + NTOKW, 256, smem_bytes, stream>>>(mp);
}
```

```cpp
#define SCH_B0 50
#define DL3 15
#include <hip/hip_runtime.h>
#include <stdint.h>

typedef unsigned short u16;
using bf16x8 = __attribute__((ext_vector_type(8))) short;
using f32x4 = __attribute__((ext_vector_type(4))) float;
using f32x2 = __attribute__((ext_vector_type(2))) float;
using bfx8 = __attribute__((ext_vector_type(8))) __bf16;
using bfx2 = __attribute__((ext_vector_type(2))) __bf16;
typedef unsigned v4u __attribute__((ext_vector_type(4)));
typedef unsigned v2u __attribute__((ext_vector_type(2)));
typedef short v4s __attribute__((ext_vector_type(4)));

#define S_LEN 2048
#define DM 576
#define NH 32
#define DH 128
#define HD (NH * DH)
#define SEG 32
#define NSEG 64

__device__ __forceinline__ float bf2f(u16 x) { return __uint_as_float(((unsigned)x) << 16); }
__device__ __forceinline__ unsigned pk2(float a, float b) { f32x2 v = {a, b}; bfx2 r = __builtin_convertvector(v, bfx2); return __builtin_bit_cast(unsigned, r); }
__device__ __forceinline__ float lo2f(unsigned u) { return __uint_as_float(u << 16); }
__device__ __forceinline__ float hi2f(unsigned u) { return __uint_as_float(u & 0xFFFF0000u); }

#define NFC 128
#define XBLK 2097152u
#define PAIR 16384u
#define SLOT 688128u
#define AGO 589824u
#define AGT 655360u
#define SG0 22
#define TP0 86
#define NXB 95
#define XB_BYTES (NXB * XBLK)
#define SLOT_AT(i) ((unsigned)((i) / 3) * XBLK + (unsigned)((i) % 3) * SLOT)
#define HT_OFF SLOT_AT(65)
#define CTAB_OFF (HT_OFF + 16384u)
#define MF_OFF (HT_OFF + 20480u)
#define TMO_OFF (MF_OFF + 15u * 128u)
#define RDY_OFF(k) (MF_OFF + 4096u + (k) * 10240u)
#define MF_BYTES 16384u
#define TRC_OFF SLOT_AT(64)
#define CAL_OFF (XBLK - 16384u)
#define P_KV 96u
#define P_H 112u
#define P_X3 117u
#define P_FA 121u
#define P_FB 122u
#define ARMK 8
#define NCAL 8
#define KPM 0
__device__ __forceinline__ unsigned kv2(const unsigned (&u)[4], unsigned v, v2u w0, v2u w1) {
  const float a = __builtin_fmaf(lo2f(u[3]), lo2f(w0.x), __builtin_fmaf(lo2f(u[2]), hi2f(w0.x), __builtin_fmaf(lo2f(u[1]), lo2f(w0.y), lo2f(u[0]) * hi2f(w0.y))));
  const float b = __builtin_fmaf(hi2f(u[3]), lo2f(w1.x), __builtin_fmaf(hi2f(u[2]), hi2f(w1.x), __builtin_fmaf(hi2f(u[1]), lo2f(w1.y), hi2f(u[0]) * hi2f(w1.y))));
  return pk2(a * lo2f(v), b * hi2f(v));
}
struct PreP {
  const u16* kk; const u16* vv; const u16* shift_k; const u16* ssm_k; const u16* ssm_D; u16* yT; u16* kvT;
  const u16* Wq; const u16* Wk; const u16* Wv; const u16* Wo; unsigned* xb; const u16* mem0;
};
#define PRE_FILL 112
#define PFN 5
__global__ __launch_bounds__(256, 4) void k_pre(PreP p) {
  __shared__ __attribute__((aligned(16))) float sh[64 * 36];
  const int tid = threadIdx.x, b0 = blockIdx.x, b = b0 < 240 ? b0 : (b0 < 528 ? b0 + PRE_FILL + 256 : b0 - 288);
  const __amdgpu_buffer_rsrc_t rx = __builtin_amdgcn_make_buffer_rsrc((void*)p.xb, 0, XB_BYTES, 0x00020000);
  const v4u ones = {0xFFFFFFFFu, 0xFFFFFFFFu, 0xFFFFFFFFu, 0xFFFFFFFFu}, zero = {0, 0, 0, 0};
  if (b < 96) {
    if (tid >= 64 || (KPM & 16)) return;
    const unsigned cb = (unsigned)b * XBLK + CAL_OFF;
    __builtin_amdgcn_raw_buffer_store_b128(zero, rx, (unsigned)(tid & 1) * 8192u + (unsigned)(tid >> 1) * 256u, cb, 16);
    asm volatile("s_waitcnt vmcnt(0)" ::: "memory");
    unsigned mn[2] = {~0u, ~0u};
    for (int i = 0; i < NCAL; ++i) {
      const unsigned long long c0 = __builtin_amdgcn_s_memtime();
      asm volatile("s_waitcnt lgkmcnt(0)" ::: "memory");
      const unsigned v = __builtin_amdgcn_raw_buffer_load_b32(rx, 0, cb + (unsigned)(i & 1) * 8192u + (unsigned)(i >> 1) * 256u, 16);
      asm volatile("s_waitcnt vmcnt(0)" :: "v"(v) : "memory");
      const unsigned d = (unsigned)(__builtin_amdgcn_s_memtime() - c0);
      if (i & 1) mn[1] = d < mn[1] ? d : mn[1]; else mn[0] = d < mn[0] ? d : mn[0];
    }
    unsigned xcc; asm volatile("s_getreg_b32 %0, hwreg(HW_REG_XCC_ID)" : "=s"(xcc));
    const unsigned res = mn[1] < mn[0] ? 1u : 0u;
    if (tid == 0) __builtin_amdgcn_raw_buffer_store_b32(res ^ ((xcc >> 2) & 1u), rx, 0, CTAB_OFF + (unsigned)b * 4u, 16);
  } else if (b < 240) {
    if (KPM & 1) return;
    const __amdgpu_buffer_rsrc_t rk = __builtin_amdgcn_make_buffer_rsrc((void*)p.kk, 0, S_LEN * DM * 2, 0x00020000);
    float (*kx)[132] = (float (*)[132])sh;
    float (*cf)[140] = (float (*)[140])(sh + 4 * 132);
    const int d0 = (b - 96) * 4, q = tid & 3, dd = (tid >> 2) & 3, t0 = (tid >> 4) * 8;
    const float Dv = bf2f(p.ssm_D[d0 + dd]);
    {
      const int s = tid >> 1, d = d0 + (tid & 1) * 2;
      unsigned u[4];
#pragma unroll
      for (int a = 0; a < 4; ++a) u[a] = __builtin_amdgcn_raw_buffer_load_b32(rk, (unsigned)((s - 3 + a) * DM + d) * 2u, 0, 0);
      const unsigned vr = *(const unsigned*)&p.vv[s * DM + d], c0 = *(const unsigned*)&p.ssm_k[(long)(d0 + (tid >> 6)) * S_LEN + (tid & 63) * 2];
      const v4u w = *(const v4u*)&p.shift_k[d * 4];
      const unsigned r = kv2(u, vr, (v2u){w.x, w.y}, (v2u){w.z, w.w});
      kx[(tid & 1) * 2][s] = lo2f(r); kx[(tid & 1) * 2 + 1][s] = hi2f(r);
      *(f32x2*)&cf[tid >> 6][8 + (tid & 63) * 2] = (f32x2){lo2f(c0), hi2f(c0)};
      if (tid < 32) cf[tid >> 3][tid & 7] = 0.f;
    }
    __syncthreads();
    float y[8];
#pragma unroll
    for (int i = 0; i < 8; ++i) y[i] = 0.f;
#pragma unroll 1
    for (int j = 8 * q; j <= t0; j += 32) {
      f32x4 kk[2], cw[4];
#pragma unroll
      for (int x = 0; x < 2; ++x) kk[x] = *(const f32x4*)&kx[dd][j + 4 * x];
#pragma unroll
      for (int x = 0; x < 4; ++x) cw[x] = *(const f32x4*)&cf[dd][t0 - j + 4 * x];
#pragma unroll
      for (int jj = 0; jj < 8; ++jj)
#pragma unroll
        for (int i = 0; i < 8; ++i) y[i] = __builtin_fmaf(cw[(8 + i - jj) >> 2][(8 + i - jj) & 3], kk[jj >> 2][jj & 3], y[i]);
    }
#pragma unroll
    for (int i = 0; i < 8; ++i) {
      y[i] += __uint_as_float((unsigned)__builtin_amdgcn_mov_dpp((int)__float_as_uint(y[i]), 0xB1, 0xf, 0xf, true));
      y[i] += __uint_as_float((unsigned)__builtin_amdgcn_mov_dpp((int)__float_as_uint(y[i]), 0x4E, 0xf, 0xf, true));
    }
    const float ya = q == 0 ? y[0] : (q == 1 ? y[2] : (q == 2 ? y[4] : y[6])), yb = q == 0 ? y[1] : (q == 1 ? y[3] : (q == 2 ? y[5] : y[7]));
    const int t = t0 + 2 * q, f = (d0 >> 3) & 15;
    const unsigned r0 = pk2(ya, yb), r1 = pk2(kx[dd][t] * Dv, kx[dd][t + 1] * Dv), o = pk2(lo2f(r0) + lo2f(r1), hi2f(r0) + hi2f(r1));
    u16* dst = p.yT + (t >> 4) * 9216 + (d0 >> 2) * 64 + dd;
    dst[((t & 15) ^ f) << 2] = (u16)(o & 0xFFFFu); dst[(((t + 1) & 15) ^ f) << 2] = (u16)(o >> 16);
  } else if (b < 240 + PRE_FILL) {
    if (KPM & 32) return;
    const unsigned i0 = (unsigned)(b - 240) * 256u + tid;
    if (i0 < (SLOT - AGO) / 16u) {
      const unsigned o = AGO + i0 * 16u, s = o < AGT ? (i0 >> 11) * 4u + ((o >> 10) & 3u) : 8u, r = o & 1023u;
      const unsigned gm = s < 8u ? r >> 8 : (r >> 6) & 3u, m3 = (r >> 4) & 3u, ga = s < 8u ? (r >> 6) & 3u : r >> 8, row = 4u * (gm & 1u) + 2u * (gm >> 1) + (m3 & 1u), half = m3 >> 1;
      const unsigned* src = (const unsigned*)(p.mem0 + row * DM + 64u * s + 16u * ga + 2u * half);
      __builtin_amdgcn_raw_buffer_store_b128((v4u){src[0], src[2], src[4], src[6]}, rx, o, 0, 16);
    }
    for (unsigned i = i0; i < 131072u + MF_BYTES / 16u; i += PRE_FILL * 256)
      __builtin_amdgcn_raw_buffer_store_b128(zero, rx, i < 131072u ? (SG0 + (i >> 11)) * XBLK + P_FA * PAIR + (i & 2047u) * 16u : MF_OFF + (i & 2047u) * 16u, 0, 16);
    for (unsigned i = i0; i < 2u * SLOT / 16u; i += PRE_FILL * 256) __builtin_amdgcn_raw_buffer_store_b128(ones, rx, SLOT + i * 16u, 0, 16);
  } else if (b >= 240 + PRE_FILL + 256) {
    if (KPM & 2) return;
    const __amdgpu_buffer_rsrc_t rk = __builtin_amdgcn_make_buffer_rsrc((void*)p.kk, 0, S_LEN * DM * 2, 0x00020000);
    u16 (*tile)[72] = (u16 (*)[72])sh;
    const int bid = b - 240 - PRE_FILL - 256, d0 = (bid % 9) * 64, s0 = (bid / 9) * 64;
    const int sr = (tid >> 3) * 2, d = d0 + (tid & 7) * 8;
    v4u kr[5], vr[2], w[4];
#pragma unroll
    for (int a = 0; a < 5; ++a) kr[a] = __builtin_amdgcn_raw_buffer_load_b128(rk, (unsigned)((s0 + sr - 3 + a) * DM + d) * 2u, 0, 0);
#pragma unroll
    for (int a = 0; a < 2; ++a) vr[a] = *(const v4u*)&p.vv[(long)(s0 + sr + a) * DM + d];
#pragma unroll
    for (int a = 0; a < 4; ++a) w[a] = *(const v4u*)&p.shift_k[(d + 2 * a) * 4];
    __builtin_amdgcn_sched_barrier(0);
#pragma unroll
    for (int it = 0; it < 2; ++it) {
      v4u o;
#pragma unroll
      for (int j = 0; j < 4; ++j) { const unsigned u[4] = {kr[it][j], kr[it + 1][j], kr[it + 2][j], kr[it + 3][j]}; o[j] = kv2(u, vr[it][j], (v2u){w[j].x, w[j].y}, (v2u){w[j].z, w[j].w}); }
      *(v4u*)&tile[sr + it][(tid & 7) * 8] = o;
    }
    __syncthreads();
    const int lane = tid & 63, wv = tid >> 6, g = lane >> 4, r = (lane >> 2) & 3, c = lane & 3;
    v2u o[4];
#pragma unroll
    for (int i = 0; i < 4; ++i) o[i] = __builtin_bit_cast(v2u, __builtin_amdgcn_ds_read_tr16_b64_v4i16((__attribute__((address_space(3))) v4s*)&tile[16 * g + 4 * i + r][16 * wv + 4 * c]));
    u16* dst = p.kvT + (long)(d0 + 16 * wv + (lane & 15)) * S_LEN + s0 + 16 * g;
    *(v4u*)dst = (v4u){o[0].x, o[0].y, o[1].x, o[1].y};
    *(v4u*)(dst + 8) = (v4u){o[2].x, o[2].y, o[3].x, o[3].y};
  } else {
    if (KPM & 4) return;
    const int x = b & 7, k = (b - 240 - PRE_FILL) >> 3;
    unsigned long long pc; unsigned l0, l1, l2, l3; int rel;
    asm volatile("s_getpc_b64 %0\nLPRE:\n\ts_mov_b32 %1, LKBEG-LPRE\n\ts_mov_b32 %2, LCHEND-LKBEG\n\ts_mov_b32 %3, LQVEND-LKBEG\n\ts_mov_b32 %4, LQKEND-LKBEG\n\ts_mov_b32 %5, LAPEND-LKBEG"
                 : "=s"(pc), "=s"(rel), "=s"(l0), "=s"(l1), "=s"(l2), "=s"(l3));
    l0 = l0 > l1 ? l0 : l1; l2 = l2 > l3 ? l2 : l3; l0 = l0 > l2 ? l0 : l2;
    const unsigned o = (unsigned)(k * 64 + (tid & 63)) * 64u;
    unsigned a[PFN + 2];
    a[PFN] = *(const __attribute__((address_space(1))) unsigned*)(pc + rel + (o < l0 ? o : 0u));
    a[PFN + 1] = __builtin_amdgcn_raw_buffer_load_b32(rx, k == 31 ? tid * XBLK + CAL_OFF + 8064u : 0xF0000000u, 0, 16);
#pragma unroll
    for (int i = 0; i < PFN; ++i) {
      const int q = (i * 32 + k) * 256 + tid, r2 = q >> 5, gr = r2 >> 5, row = (gr / 3) * 64 + (x >> 2) * 32 + (r2 & 31), off = (q & 31) * 32, ty = gr % 3;
      a[i] = *(const unsigned*)(gr < 27 ? (ty == 0 ? p.Wq : (ty == 1 ? p.Wk : p.Wv)) + (long)row * HD + (x & 3) * 1024 + off
                                        : p.Wo + ((long)(x & 3) * DM + (gr < 36 ? gr - 27 : 0) * 64 + (x >> 2) * 32 + (r2 & 31)) * 1024 + off);
    }
#pragma unroll
    for (int i = 0; i < PFN + 2; ++i) asm volatile("" :: "v"(a[i]));
  }
}

#define CREV_STRIDE 2208
#define SP(k)
#define LWAIT() asm volatile("s_waitcnt lgkmcnt(0)" ::: "memory")
template <class F>
__device__ __forceinline__ void conv_task(const u16* __restrict__ kvT, const u16* __restrict__ ssm_k, const u16* __restrict__ ssm_D, u16* yT, u16* shb, int d, F fill) {
  const int lane = threadIdx.x & 63;
  u16* crev = shb + 64;
  u16* kvs = crev + 4 * CREV_STRIDE;
  const int X0 = 2048 + 63;
  SP(0);
  v4u cv[4], kq[4];
#pragma unroll
  for (int j = 0; j < 4; ++j) { cv[j] = *(const v4u*)&ssm_k[(long)d * S_LEN + lane * 32 + 8 * j]; kq[j] = *(const v4u*)&kvT[(long)d * S_LEN + (j * 64 + lane) * 8]; }
  const float Dv = bf2f(ssm_D[d]);
  const v4u z4 = {0u, 0u, 0u, 0u};
  *(v4u*)&kvs[(lane >> 2) * 40 + (lane & 3) * 8] = z4;
  if (lane < 32) *(v4u*)&crev[(lane >> 3) * CREV_STRIDE + (lane & 7) * 8] = z4;
  if (lane < 36) *(v4u*)&crev[(lane / 9) * CREV_STRIDE + 2112 + (lane % 9) * 8] = z4;
  {
    unsigned r[18];
#pragma unroll
    for (int k = 0; k < 16; ++k) { const unsigned u = cv[3 - (k >> 2)][3 - (k & 3)]; r[k] = __builtin_amdgcn_alignbyte(u, u, 2); }
#pragma unroll
    for (int k = 0; k < 2; ++k) r[16 + k] = (unsigned)__builtin_amdgcn_update_dpp(0, (int)r[k], 0x138, 0xf, 0xf, true);
    LWAIT();
#pragma unroll
    for (int rho = 0; rho < 4; ++rho)
#pragma unroll
      for (int j = 0; j < 4; ++j) {
        v4u o;
#pragma unroll
        for (int k = 0; k < 4; ++k) { const int q = 4 * j + k + (rho >> 1); o[k] = (rho & 1) ? __builtin_amdgcn_alignbyte(r[q + 1], r[q], 2) : r[q]; }
        *(v4u*)&crev[rho * CREV_STRIDE + 2080 - 32 * lane + 8 * j] = o;
      }
    if (lane == 63) {
      *(v2u*)&crev[1 * CREV_STRIDE + 60] = (v2u){0u, r[0] << 16};
      *(v2u*)&crev[2 * CREV_STRIDE + 60] = (v2u){0u, r[0]};
      *(v2u*)&crev[3 * CREV_STRIDE + 60] = (v2u){r[0] << 16, __builtin_amdgcn_alignbyte(r[1], r[0], 2)};
    }
#pragma unroll
    for (int j = 0; j < 4; ++j) *(v4u*)&kvs[(16 + j * 16 + (lane >> 2)) * 40 + (lane & 3) * 8] = kq[j];
  }
  fill();
  LWAIT();
  SP(1);
  const int n = lane & 15, g = lane >> 4;
  const int rho = (X0 - n) & 3;
  f32x4 acc[4][2];
#pragma unroll
  for (int q = 0; q < 8; ++q) acc[q >> 1][q & 1] = (f32x4){0.f, 0.f, 0.f, 0.f};
  {
    typedef __attribute__((address_space(3))) const v2u* l2p;
    typedef __attribute__((address_space(3))) const bf16x8* l4p;
    unsigned a0 = (unsigned)(size_t)(__attribute__((address_space(3))) u16*)&crev[rho * CREV_STRIDE + (X0 - (n - 8 * g) - rho)] - 3232u, a1 = a0 + 8u;
    unsigned b0 = (unsigned)(size_t)(__attribute__((address_space(3))) u16*)&kvs[(14 + n) * 40 + 8 * g];
    asm volatile("" : "+v"(a1));
#define AFR(m, f) ({ const unsigned o_ = 3232u - 64u * (m) - 1024u * ((f) >> 1) - 32u * ((f) & 1); const v2u lo_ = *(l2p)(size_t)(a0 + o_), hi_ = *(l2p)(size_t)(a1 + o_); \
                     __builtin_bit_cast(bf16x8, (v4u){lo_.x, lo_.y, hi_.x, hi_.y}); })
#define BFR(m, j) (*(l4p)(size_t)(b0 + 80u * (2 - (m)) + 1280u * (j)))
    bf16x8 af[2][8], bf[2][4];
#pragma unroll
    for (int f = 0; f < 8; ++f) { af[0][f] = AFR(0, f); af[1][f] = af[0][f]; __builtin_amdgcn_sched_barrier(0); }
#pragma unroll
    for (int j = 0; j < 4; ++j) { bf[0][j] = BFR(0, j); bf[1][j] = bf[0][j]; }
#pragma unroll 1
    for (int mo = 0; mo < 8; ++mo) {
#pragma unroll
      for (int m = 0; m < 2; ++m) {
        int k = 0;
#pragma unroll
        for (int a = 0; a < 4; ++a)
#pragma unroll
          for (int ct = a; ct < 4; ++ct)
#pragma unroll
            for (int ti = 0; ti < 2; ++ti) {
              acc[ct][ti] = __builtin_amdgcn_mfma_f32_16x16x32_bf16(__builtin_bit_cast(bfx8, af[m][2 * a + ti]), __builtin_bit_cast(bfx8, bf[m][ct - a]), acc[ct][ti], 0, 0, 0);
              if (k < 2) af[m ^ 1][k] = AFR(m + 1, k);
              else if (k < 6) bf[m ^ 1][k - 2] = BFR(m + 1, k - 2);
              else if (k < 12) af[m ^ 1][k - 4] = AFR(m + 1, k - 4);
              __builtin_amdgcn_sched_barrier(0);
              ++k;
            }
      }
      a0 -= 128u; a1 -= 128u; b0 -= 160u;
    }
  }
  SP(2);
  u16* yo = crev;
  LWAIT();
#pragma unroll
  for (int q = 0; q < 8; ++q) {
    const int o = (16 * (q >> 1) + n) * 40 + 16 * (q & 1) + 4 * g;
    const v2u kx = *(const v2u*)&kvs[16 * 40 + o];
    const f32x4 y = acc[q >> 1][q & 1];
    const unsigned y0 = pk2(y[0], y[1]), y1 = pk2(y[2], y[3]), r0 = pk2(lo2f(kx.x) * Dv, hi2f(kx.x) * Dv), r1 = pk2(lo2f(kx.y) * Dv, hi2f(kx.y) * Dv);
    *(v2u*)&yo[o] = (v2u){pk2(lo2f(y0) + lo2f(r0), hi2f(y0) + hi2f(r0)), pk2(lo2f(y1) + lo2f(r1), hi2f(y1) + hi2f(r1))};
  }
  LWAIT();
  __builtin_amdgcn_s_barrier();
  asm volatile("" ::: "memory");
  {
    const int tid = threadIdx.x, f = (d >> 3) & 15;
    const u16* y0 = shb - (tid >> 6) * 16384 + 64;
#pragma unroll
    for (int k = 0; k < 4; ++k) {
      const int tile = 32 * k + (tid >> 3), t = 16 * tile + 2 * ((tid & 7) ^ (f >> 1));
      unsigned u[4];
#pragma unroll
      for (int c = 0; c < 4; ++c) u[c] = *(const unsigned*)&y0[c * 16384 + (t >> 5) * 40 + (t & 31)];
      const v2u e = {__builtin_amdgcn_perm(u[1], u[0], 0x05040100u), __builtin_amdgcn_perm(u[3], u[2], 0x05040100u)}, o = {__builtin_amdgcn_perm(u[1], u[0], 0x07060302u), __builtin_amdgcn_perm(u[3], u[2], 0x07060302u)};
      if (tile >= NFC / 16) __builtin_amdgcn_raw_buffer_store_b128((f & 1) ? (v4u){o.x, o.y, e.x, e.y} : (v4u){e.x, e.y, o.x, o.y}, __builtin_amdgcn_make_buffer_rsrc((void*)yT, 0, DM * S_LEN * 2, 0x00020000),
                                                                   tile * 18432 + (tid & 7) * 16, (d >> 2) * 128, 16);
    }
  }
}

#define NCHW 64
#define NTOKW 192
#define PSL 4
#define SENT 0xFFFFFFFFu
#define SPIN_CAP 300000u
struct MP {
  const u16* Wq; const u16* Wk; const u16* Wv; const u16* Wo; u16* out; unsigned* xb;
  const u16* xin; u16* yT; u16* kvT; const u16* ssm_k; const u16* ssm_D;
};

__device__ __forceinline__ MP getp() {
  MP r;
#if defined(__HIP_DEVICE_COMPILE__)
  const __attribute__((address_space(4))) unsigned long long* k = (const __attribute__((address_space(4))) unsigned long long*)__builtin_amdgcn_kernarg_segment_ptr();
  asm volatile("" : "+s"(k));
#define GP(T, i) ((T*)(__attribute__((address_space(1))) T*)k[i])
  r.Wq = GP(const u16, 0);
  r.Wk = GP(const u16, 1);
  r.Wv = GP(const u16, 2);
  r.Wo = GP(const u16, 3);
  r.out = GP(u16, 4);
  r.xb = GP(unsigned, 5);
  r.xin = GP(const u16, 6);
  r.yT = GP(u16, 7);
  r.kvT = GP(u16, 8);
  r.ssm_k = GP(const u16, 9);
  r.ssm_D = GP(const u16, 10);
#endif
  return r;
}
typedef __amdgpu_buffer_rsrc_t rsrc_t;
__device__ __forceinline__ rsrc_t mkr(const void* p, unsigned bytes) { return __builtin_amdgcn_make_buffer_rsrc((void*)p, 0, bytes, 0x00020000); }
__device__ __forceinline__ void st16(rsrc_t r, unsigned vo, unsigned so, v4u v) { __builtin_amdgcn_raw_buffer_store_b128(v, r, vo, so, 16); }
__device__ __forceinline__ void st4(rsrc_t r, unsigned vo, unsigned so, unsigned v) { __builtin_amdgcn_raw_buffer_store_b32(v, r, vo, so, 16); }
__device__ __forceinline__ v4u ld16(rsrc_t r, unsigned vo, unsigned so) { return __builtin_amdgcn_raw_buffer_load_b128(r, vo, so, 16); }
__device__ __forceinline__ v2u ld8(rsrc_t r, unsigned vo, unsigned so) { return __builtin_amdgcn_raw_buffer_load_b64(r, vo, so, 16); }
__device__ __forceinline__ unsigned ld4(rsrc_t r, unsigned vo, unsigned so) { return __builtin_amdgcn_raw_buffer_load_b32(r, vo, so, 16); }
__device__ __forceinline__ unsigned opq(unsigned x) { asm volatile("" : "+v"(x)); return x; }

__device__ __forceinline__ unsigned long long die_mask(rsrc_t X, unsigned first, int L, int lane) { return __ballot((ld4(X, (unsigned)lane * 4u, CTAB_OFF + first * 4u) & 1u) != (unsigned)L); }
#define SEGB(m, s) ((unsigned)(SG0 + (s)) * XBLK + (unsigned)(((m) >> (s)) & 1ull) * 8192u)
__device__ __forceinline__ bool spin_fail(unsigned& spins, rsrc_t F) {
  ++spins;
  if (spins > SPIN_CAP) { unsigned one = 0x40000000u | (blockIdx.x << 8) | threadIdx.x; asm volatile("" : "+v"(one)); if (__builtin_amdgcn_readfirstlane(ld4(F, 0, TMO_OFF)) == 0u) st4(F, 0, TMO_OFF, one); return true; }
  if ((spins & 1023u) == 0u) return __builtin_amdgcn_readfirstlane(ld4(F, 0, TMO_OFF)) != 0;
  return false;
}
__device__ __forceinline__ unsigned rtc() { return (unsigned)__builtin_amdgcn_s_memrealtime(); }
#define BAR() do { asm volatile("s_waitcnt lgkmcnt(0)" ::: "memory"); __builtin_amdgcn_s_barrier(); asm volatile("" ::: "memory"); } while (0)
#define SB() __builtin_amdgcn_sched_barrier(0)
#define MF "v_mfma_f32_16x16x32_bf16 "
#define VM0() __builtin_amdgcn_s_waitcnt(0x0F70)
#define DL1 12
#define DLL1 6
#define DLL2 4
#define DLF 24
#define SCH_PEN 0
#define DLX 4
#define LATE 2
#define TKL 28
#define UP 1
#define DN 100
#define FIN -3
#define CH_FIN CH_S(FIN)
#define DLT 16
#define DLY 20
#define CH_DLT CH_S(DLT)
#define CH_DLY CH_S(DLY)
#define ES 0
#define ED 4
#define EB 8
#define CH_ES CH_S(ES)
#define CH_EX "%[ex]"
#define DRYL 1
#define CH_DRYL CH_S(DRYL)
#define SMAP 0x1023u
#define RSL 2
#define CH_S2(x) #x
#define CH_S(x) CH_S2(x)
#define CH_ST "s_memtime s[62:63]\n\ts_waitcnt lgkmcnt(0)\n\ts_sub_u32 s67, s62, s67\n\t"
#define CH_BASE "s_memtime s[94:95]\n\ts_waitcnt lgkmcnt(0)\n\ts_mov_b32 s67, s94\n\t"
#define CH_DPC
#define CH_TOKCHK "s_cbranch_vccz LG3_%=\n\t"
#define CH_SEGC
#define CH_SG0 CH_S(SG0)
#define CH_ARMD "%[armd]"
#define CH_ARMS "%[arms]"
#define CH_SLOT "688128"
#define CH_SKIP "32768"
#define CH_DL1 CH_S(DL1)
#define CH_DLL1 CH_S(DLL1)
#define CH_DLL2 CH_S(DLL2)
#define CH_DLF CH_S(DLF)
#define CH_B0 CH_S(SCH_B0)
#define CH_PEN CH_S(SCH_PEN)
#define CH_RSL CH_S(RSL)
#define CH_DL3 CH_S(DL3)
#define CH_UP CH_S(UP)
#define CH_DN CH_S(DN)
#define CH_DLX CH_S(DLX)
#define CH_LATE CH_S(LATE)
#define CH_DL1L "%[dl1l]"
#define CH_TKL CH_S(TKL)
#define CH_SCHW "656384"
#define CH_CAP "%[cap]"
#define CH_CTAB "%[ctab]"
#define CH_COW
#define CH_INW "s_sub_u32 s93, s48, 10\n\ts_cmp_lt_u32 s93, 50\n\t"
#define CH_ST1
#define CH_ST2
#define FDL 6
#define CH_FDL CH_S(FDL)
#define CH_ST3
#define CH_ST4
#define CH_TRC
#define CH_TG2
#define CH_TG5
#define CH_STV
#define CH_STW
#define CH_DP0
#define CH_DP1
#define CH_DP2
#define CH_DP3
#define CH_DP4
#define CH_DP5
#define CH_DP6
#define CH_DP7
#define CH_DP8
#define CH_DP9
#define CH_DP10
#define CH_DP11
#define CH_DP12
#define CH_DP13
#define CH_DP14
#define CH_DP15
#define CH_DP16
#define CH_DP17
#define CH_OPS {64, 80, 128, 144, 1, 17, 65, 81, 129, 145, 2, 18, 82, 130, 146, 3, 67, 83, 131, 147, 4, 20, 68, 84, 132, 5, 69, 85, 133, 149, 6, 22, 70, 86, 134, 150, 7, 23, 71, 87, 135, 151, 8, 24, 88, 136, 195, 209, 210, 211, 227, 241, 242, 256, 0, 16, 19, 21, 66, 72, 148, 152, 208, 240, 192, 224, 193, 225, 194, 226, 243, 257}
#define CH_ASM \
  "s_mov_b32 s84, " CH_CAP "\n\t" \
  "s_mov_b32 s83, " CH_CAP "\n\t" \
  "s_mov_b32 s68, 0\n\t" \
  "s_mov_b32 s74, 0\n\t" \
  "s_mov_b32 s76, 0\n\t" \
  "s_mov_b32 s77, 0\n\t" \
  "s_mov_b32 s78, 0\n\t" \
  "s_mov_b32 s79, 0\n\t" \
  "s_mov_b32 s56, 0xcccccccc\n\t" \
  "s_mov_b32 s57, 0xcccccccc\n\t" \
  "s_mov_b32 s58, 0x33333333\n\t" \
  "s_mov_b32 s59, 0x33333333\n\t" \
  "s_mov_b32 s24, 0x01010101\n\t" \
  "s_mov_b32 s25, 0\n\t" \
  "s_mov_b32 s88, 0x3f803f80\n\t" \
  "s_mov_b32 s89, 0x3f803f80\n\t" \
  "s_mov_b32 s66, s65\n\t" \
  "s_mov_b32 s47, s65\n\t" \
  "s_mov_b32 s72, 0\n\t" \
  "s_mov_b32 s73, 0\n\t" \
  "s_mov_b32 s67, 0\n\t" \
  "s_mul_i32 s60, s40, 36864\n\t" \
  "s_add_u32 s60, s60, 16384\n\t" \
  "v_add_u32 v70, s60, v69\n\t" \
  "v_bfe_i32 v68, v69, 5, 1\n\t" \
  "v_and_b32 v68, 0xaaaaaaaa, v68\n\t" \
  "v_or_b32 v68, 0x44444444, v68\n\t" \
  "v_and_b32 v73, 32, v69\n\t" \
  "v_lshl_add_u32 v73, v73, 5, v69\n\t" \
  "LLOAD_%=:\n\t" \
  "v_lshrrev_b32 v74, 2, v69\n\t" \
  "s_mov_b32 s60, " CH_CTAB "\n\t" \
  "buffer_load_dword v72, v74, s[28:31], s60 offen offset:88 sc1\n\t" \
  "buffer_load_dword v74, v74, s[28:31], s60 offen sc1\n\t" \
  "v_lshrrev_b32 v71, 8, v69\n\t" \
  "v_lshlrev_b32 v1, 6, v71\n\t" \
  "v_and_b32 v2, 0xf0, v69\n\t" \
  "v_xor_b32 v1, v1, v2\n\t" \
  "v_lshl_add_u32 v71, v71, 13, v1\n\t" \
  "s_lshl_b32 s60, s40, 17\n\t" \
  "v_add_u32 v71, s60, v71\n\t" \
  "v_lshrrev_b32 v1, 7, v69\n\t" \
  "v_and_b32 v2, 0x70, v69\n\t" \
  "s_movk_i32 s60, 1152\n\t" \
  "v_mad_u32_u24 v75, v1, s60, v2\n\t" \
  "s_lshl_b32 s54, s40, 12\n\t" \
  "s_lshl_b32 s55, s40, 7\n\t" \
  "s_mul_i32 s75, s40, 36864\n\t" \
  "s_add_u32 s75, s75, 1024\n\t" \
  "s_mov_b32 s62, 0x05040100\n\t" \
  "s_mov_b32 s63, 0x07060302\n\t" \
  "s_barrier\n\t" \
  "s_xor_b32 s61, s40, 0\n\t" \
  "s_mul_i32 s61, s61, 36864\n\t" \
  "s_add_u32 s61, s61, s55\n\t" \
  "s_add_u32 m0, s54, 0\n\t" \
  "s_add_u32 s60, s61, 0\n\t" \
  "buffer_load_dwordx4 v75, %[Wo], s60 offen lds\n\t" \
  "s_add_u32 m0, s54, 1024\n\t" \
  "s_add_u32 s60, s61, 9216\n\t" \
  "buffer_load_dwordx4 v75, %[Wo], s60 offen lds\n\t" \
  "s_add_u32 m0, s54, 2048\n\t" \
  "s_add_u32 s60, s61, 18432\n\t" \
  "buffer_load_dwordx4 v75, %[Wo], s60 offen lds\n\t" \
  "s_add_u32 m0, s54, 3072\n\t" \
  "s_add_u32 s60, s61, 27648\n\t" \
  "buffer_load_dwordx4 v75, %[Wo], s60 offen lds\n\t" \
  "s_add_u32 m0, s54, 16384\n\t" \
  "s_mov_b32 s60, 0\n\t" \
  "buffer_load_dwordx4 v71, %[Wq], s60 offen lds\n\t" \
  "s_add_u32 m0, s54, 17408\n\t" \
  "s_mov_b32 s60, 32768\n\t" \
  "buffer_load_dwordx4 v71, %[Wq], s60 offen lds\n\t" \
  "s_add_u32 m0, s54, 18432\n\t" \
  "s_mov_b32 s60, 65536\n\t" \
  "buffer_load_dwordx4 v71, %[Wq], s60 offen lds\n\t" \
  "s_add_u32 m0, s54, 19456\n\t" \
  "s_mov_b32 s60, 98304\n\t" \
  "buffer_load_dwordx4 v71, %[Wq], s60 offen lds\n\t" \
  "s_add_u32 m0, s54, 53248\n\t" \
  "s_mov_b32 s60, 0\n\t" \
  "buffer_load_dwordx4 v71, %[Wk], s60 offen lds\n\t" \
  "s_add_u32 m0, s54, 54272\n\t" \
  "s_mov_b32 s60, 32768\n\t" \
  "buffer_load_dwordx4 v71, %[Wk], s60 offen lds\n\t" \
  "s_add_u32 m0, s54, 55296\n\t" \
  "s_mov_b32 s60, 65536\n\t" \
  "buffer_load_dwordx4 v71, %[Wk], s60 offen lds\n\t" \
  "s_add_u32 m0, s54, 56320\n\t" \
  "s_mov_b32 s60, 98304\n\t" \
  "buffer_load_dwordx4 v71, %[Wk], s60 offen lds\n\t" \
  "s_xor_b32 s61, s40, 0\n\t" \
  "s_mul_i32 s61, s61, 36864\n\t" \
  "s_add_u32 s61, s61, s55\n\t" \
  "s_add_u32 m0, s54, 90112\n\t" \
  "s_add_u32 s60, s61, 512\n\t" \
  "buffer_load_dwordx4 v75, %[Wo], s60 offen lds\n\t" \
  "s_add_u32 m0, s54, 91136\n\t" \
  "s_add_u32 s60, s61, 9728\n\t" \
  "buffer_load_dwordx4 v75, %[Wo], s60 offen lds\n\t" \
  "s_add_u32 m0, s54, 92160\n\t" \
  "s_add_u32 s60, s61, 18944\n\t" \
  "buffer_load_dwordx4 v75, %[Wo], s60 offen lds\n\t" \
  "s_add_u32 m0, s54, 93184\n\t" \
  "s_add_u32 s60, s61, 28160\n\t" \
  "buffer_load_dwordx4 v75, %[Wo], s60 offen lds\n\t" \
  "s_add_u32 m0, s54, 126976\n\t" \
  "s_mov_b32 s60, 0\n\t" \
  "buffer_load_dwordx4 v71, %[Wv], s60 offen lds\n\t" \
  "s_add_u32 m0, s54, 128000\n\t" \
  "s_mov_b32 s60, 32768\n\t" \
  "buffer_load_dwordx4 v71, %[Wv], s60 offen lds\n\t" \
  "s_add_u32 m0, s54, 129024\n\t" \
  "s_mov_b32 s60, 65536\n\t" \
  "buffer_load_dwordx4 v71, %[Wv], s60 offen lds\n\t" \
  "s_add_u32 m0, s54, 130048\n\t" \
  "s_mov_b32 s60, 98304\n\t" \
  "buffer_load_dwordx4 v71, %[Wv], s60 offen lds\n\t" \
  "v_and_b32 v1, 0x300, v69\n\t" \
  "v_lshlrev_b32 v65, 3, v1\n\t" \
  "v_and_b32 v1, 0xc0, v69\n\t" \
  "v_lshl_add_u32 v65, v1, 2, v65\n\t" \
  "s_lshl_b32 s60, s40, 6\n\t" \
  "v_xor_b32 v1, s60, v1\n\t" \
  "v_and_b32 v2, 48, v69\n\t" \
  "v_add3_u32 v65, v65, v1, v2\n\t" \
  "v_lshrrev_b32 v2, 1, v2\n\t" \
  "v_sub_u32 v66, v65, v2\n\t" \
  "v_bfe_u32 v0, v69, 8, 1\n\t" \
  "v_bfe_u32 v1, v69, 9, 1\n\t" \
  "v_lshlrev_b32 v0, 2, v0\n\t" \
  "v_add_u32 v0, v0, v1\n\t" \
  "v_lshlrev_b32 v0, 9, v0\n\t" \
  "v_lshrrev_b32 v1, 1, v69\n\t" \
  "v_and_b32 v1, 0x60, v1\n\t" \
  "v_and_b32 v2, 16, v69\n\t" \
  "v_bfe_u32 v3, v69, 5, 1\n\t" \
  "v_add3_u32 v64, v0, v1, v2\n\t" \
  "v_lshl_add_u32 v64, v3, 2, v64\n\t" \
  "v_add_u32 v64, s54, v64\n\t" \
  "v_lshl_add_u32 v67, v3, 3, v0\n\t" \
  "v_bfe_u32 v1, v69, 7, 1\n\t" \
  "v_lshl_add_u32 v67, v1, 2, v67\n\t" \
  "v_bfe_u32 v1, v69, 6, 1\n\t" \
  "v_lshl_add_u32 v67, v1, 4, v67\n\t" \
  "v_bfe_u32 v1, v69, 4, 1\n\t" \
  "v_lshl_add_u32 v67, v1, 1, v67\n\t" \
  "s_lshl_b32 s60, s40, 5\n\t" \
  "v_add_u32 v67, s60, v67\n\t" \
  "v_add_u32 v78, 0, v64\n\t" \
  "v_add_u32 v79, 1024, v64\n\t" \
  "s_waitcnt vmcnt(16) lgkmcnt(0)\n\t" \
  "s_barrier\n\t" \
  "ds_read2_b32 v[80:81], v78 offset0:0 offset1:2\n\t" \
  "ds_read2_b32 v[82:83], v78 offset0:32 offset1:34\n\t" \
  "ds_read2_b32 v[84:85], v78 offset0:64 offset1:66\n\t" \
  "ds_read2_b32 v[86:87], v78 offset0:96 offset1:98\n\t" \
  "ds_read2_b32 v[88:89], v79 offset0:0 offset1:2\n\t" \
  "ds_read2_b32 v[90:91], v79 offset0:32 offset1:34\n\t" \
  "ds_read2_b32 v[92:93], v79 offset0:64 offset1:66\n\t" \
  "ds_read2_b32 v[94:95], v79 offset0:96 offset1:98\n\t" \
  "s_waitcnt lgkmcnt(6)\n\t" \
  "v_perm_b32 v112, v82, v80, s62\n\t" \
  "v_perm_b32 v113, v82, v80, s63\n\t" \
  "s_waitcnt lgkmcnt(4)\n\t" \
  "v_perm_b32 v114, v86, v84, s62\n\t" \
  "v_perm_b32 v115, v86, v84, s63\n\t" \
  "s_waitcnt lgkmcnt(2)\n\t" \
  "v_perm_b32 v116, v90, v88, s62\n\t" \
  "v_perm_b32 v117, v90, v88, s63\n\t" \
  "s_waitcnt lgkmcnt(0)\n\t" \
  "v_perm_b32 v118, v94, v92, s62\n\t" \
  "v_perm_b32 v119, v94, v92, s63\n\t" \
  "ds_write_b128 v70, v[112:115] offset:20480\n\t" \
  "ds_write_b128 v70, v[116:119] offset:21504\n\t" \
  "v_perm_b32 v120, v83, v81, s62\n\t" \
  "v_perm_b32 v121, v83, v81, s63\n\t" \
  "v_perm_b32 v122, v87, v85, s62\n\t" \
  "v_perm_b32 v123, v87, v85, s63\n\t" \
  "v_perm_b32 v124, v91, v89, s62\n\t" \
  "v_perm_b32 v125, v91, v89, s63\n\t" \
  "v_perm_b32 v126, v95, v93, s62\n\t" \
  "v_perm_b32 v127, v95, v93, s63\n\t" \
  "ds_write_b128 v70, v[120:123] offset:16384\n\t" \
  "ds_write_b128 v70, v[124:127] offset:17408\n\t" \
  "v_add_u32 v78, 16384, v65\n\t" \
  "s_waitcnt vmcnt(12) lgkmcnt(0)\n\t" \
  "s_barrier\n\t" \
  "ds_read_b64_tr_b16 v[0:1], v78 offset:0\n\t" \
  "ds_read_b64_tr_b16 v[2:3], v78 offset:1024\n\t" \
  "ds_read_b64_tr_b16 v[4:5], v78 offset:8192\n\t" \
  "ds_read_b64_tr_b16 v[6:7], v78 offset:9216\n\t" \
  "ds_read_b64_tr_b16 v[8:9], v78 offset:8\n\t" \
  "ds_read_b64_tr_b16 v[10:11], v78 offset:1032\n\t" \
  "ds_read_b64_tr_b16 v[12:13], v78 offset:8200\n\t" \
  "ds_read_b64_tr_b16 v[14:15], v78 offset:9224\n\t" \
  "s_add_u32 m0, s54, 0\n\t" \
  "s_mov_b32 s60, 524288\n\t" \
  "buffer_load_dwordx4 v71, %[Wq], s60 offen lds\n\t" \
  "s_add_u32 m0, s54, 1024\n\t" \
  "s_mov_b32 s60, 557056\n\t" \
  "buffer_load_dwordx4 v71, %[Wq], s60 offen lds\n\t" \
  "s_add_u32 m0, s54, 2048\n\t" \
  "s_mov_b32 s60, 589824\n\t" \
  "buffer_load_dwordx4 v71, %[Wq], s60 offen lds\n\t" \
  "s_add_u32 m0, s54, 3072\n\t" \
  "s_mov_b32 s60, 622592\n\t" \
  "buffer_load_dwordx4 v71, %[Wq], s60 offen lds\n\t" \
  "v_add_u32 v78, 53248, v65\n\t" \
  "s_waitcnt vmcnt(12) lgkmcnt(0)\n\t" \
  "s_barrier\n\t" \
  "ds_read_b64_tr_b16 a[0:1], v78 offset:0\n\t" \
  "ds_read_b64_tr_b16 a[2:3], v78 offset:1024\n\t" \
  "ds_read_b64_tr_b16 a[4:5], v78 offset:8192\n\t" \
  "ds_read_b64_tr_b16 a[6:7], v78 offset:9216\n\t" \
  "ds_read_b64_tr_b16 a[8:9], v78 offset:8\n\t" \
  "ds_read_b64_tr_b16 a[10:11], v78 offset:1032\n\t" \
  "ds_read_b64_tr_b16 a[12:13], v78 offset:8200\n\t" \
  "ds_read_b64_tr_b16 a[14:15], v78 offset:9224\n\t" \
  "s_xor_b32 s61, s40, 1\n\t" \
  "s_mul_i32 s61, s61, 36864\n\t" \
  "s_add_u32 s61, s61, s55\n\t" \
  "s_add_u32 m0, s54, 16384\n\t" \
  "s_add_u32 s60, s61, 0\n\t" \
  "buffer_load_dwordx4 v75, %[Wo], s60 offen lds\n\t" \
  "s_add_u32 m0, s54, 17408\n\t" \
  "s_add_u32 s60, s61, 9216\n\t" \
  "buffer_load_dwordx4 v75, %[Wo], s60 offen lds\n\t" \
  "s_add_u32 m0, s54, 18432\n\t" \
  "s_add_u32 s60, s61, 18432\n\t" \
  "buffer_load_dwordx4 v75, %[Wo], s60 offen lds\n\t" \
  "s_add_u32 m0, s54, 19456\n\t" \
  "s_add_u32 s60, s61, 27648\n\t" \
  "buffer_load_dwordx4 v75, %[Wo], s60 offen lds\n\t" \
  "v_add_u32 v78, 90112, v64\n\t" \
  "v_add_u32 v79, 91136, v64\n\t" \
  "s_waitcnt vmcnt(12) lgkmcnt(0)\n\t" \
  "s_barrier\n\t" \
  "ds_read2_b32 v[80:81], v78 offset0:0 offset1:2\n\t" \
  "ds_read2_b32 v[82:83], v78 offset0:32 offset1:34\n\t" \
  "ds_read2_b32 v[84:85], v78 offset0:64 offset1:66\n\t" \
  "ds_read2_b32 v[86:87], v78 offset0:96 offset1:98\n\t" \
  "ds_read2_b32 v[88:89], v79 offset0:0 offset1:2\n\t" \
  "ds_read2_b32 v[90:91], v79 offset0:32 offset1:34\n\t" \
  "ds_read2_b32 v[92:93], v79 offset0:64 offset1:66\n\t" \
  "ds_read2_b32 v[94:95], v79 offset0:96 offset1:98\n\t" \
  "s_add_u32 m0, s54, 53248\n\t" \
  "s_mov_b32 s60, 524288\n\t" \
  "buffer_load_dwordx4 v71, %[Wk], s60 offen lds\n\t" \
  "s_add_u32 m0, s54, 54272\n\t" \
  "s_mov_b32 s60, 557056\n\t" \
  "buffer_load_dwordx4 v71, %[Wk], s60 offen lds\n\t" \
  "s_add_u32 m0, s54, 55296\n\t" \
  "s_mov_b32 s60, 589824\n\t" \
  "buffer_load_dwordx4 v71, %[Wk], s60 offen lds\n\t" \
  "s_add_u32 m0, s54, 56320\n\t" \
  "s_mov_b32 s60, 622592\n\t" \
  "buffer_load_dwordx4 v71, %[Wk], s60 offen lds\n\t" \
  "s_waitcnt lgkmcnt(6)\n\t" \
  "v_perm_b32 v112, v82, v80, s62\n\t" \
  "v_perm_b32 v113, v82, v80, s63\n\t" \
  "s_waitcnt lgkmcnt(4)\n\t" \
  "v_perm_b32 v114, v86, v84, s62\n\t" \
  "v_perm_b32 v115, v86, v84, s63\n\t" \
  "s_waitcnt lgkmcnt(2)\n\t" \
  "v_perm_b32 v116, v90, v88, s62\n\t" \
  "v_perm_b32 v117, v90, v88, s63\n\t" \
  "s_waitcnt lgkmcnt(0)\n\t" \
  "v_perm_b32 v118, v94, v92, s62\n\t" \
  "v_perm_b32 v119, v94, v92, s63\n\t" \
  "ds_write_b128 v70, v[112:115] offset:22528\n\t" \
  "ds_write_b128 v70, v[116:119] offset:23552\n\t" \
  "v_perm_b32 v120, v83, v81, s62\n\t" \
  "v_perm_b32 v121, v83, v81, s63\n\t" \
  "v_perm_b32 v122, v87, v85, s62\n\t" \
  "v_perm_b32 v123, v87, v85, s63\n\t" \
  "v_perm_b32 v124, v91, v89, s62\n\t" \
  "v_perm_b32 v125, v91, v89, s63\n\t" \
  "v_perm_b32 v126, v95, v93, s62\n\t" \
  "v_perm_b32 v127, v95, v93, s63\n\t" \
  "ds_write_b128 v70, v[120:123] offset:18432\n\t" \
  "ds_write_b128 v70, v[124:127] offset:19456\n\t" \
  "v_add_u32 v78, 126976, v66\n\t" \
  "s_waitcnt vmcnt(12) lgkmcnt(0)\n\t" \
  "s_barrier\n\t" \
  "ds_read_b64_tr_b16 a[16:17], v78 offset:0\n\t" \
  "ds_read_b64_tr_b16 a[18:19], v78 offset:1024\n\t" \
  "ds_read_b64_tr_b16 a[20:21], v78 offset:8192\n\t" \
  "ds_read_b64_tr_b16 a[22:23], v78 offset:9216\n\t" \
  "ds_read_b64_tr_b16 a[24:25], v78 offset:32\n\t" \
  "ds_read_b64_tr_b16 a[26:27], v78 offset:1056\n\t" \
  "ds_read_b64_tr_b16 a[28:29], v78 offset:8224\n\t" \
  "ds_read_b64_tr_b16 a[30:31], v78 offset:9248\n\t" \
  "s_add_u32 m0, s54, 90112\n\t" \
  "s_mov_b32 s60, 524288\n\t" \
  "buffer_load_dwordx4 v71, %[Wv], s60 offen lds\n\t" \
  "s_add_u32 m0, s54, 91136\n\t" \
  "s_mov_b32 s60, 557056\n\t" \
  "buffer_load_dwordx4 v71, %[Wv], s60 offen lds\n\t" \
  "s_add_u32 m0, s54, 92160\n\t" \
  "s_mov_b32 s60, 589824\n\t" \
  "buffer_load_dwordx4 v71, %[Wv], s60 offen lds\n\t" \
  "s_add_u32 m0, s54, 93184\n\t" \
  "s_mov_b32 s60, 622592\n\t" \
  "buffer_load_dwordx4 v71, %[Wv], s60 offen lds\n\t" \
  "v_add_u32 v78, 0, v65\n\t" \
  "s_waitcnt vmcnt(12) lgkmcnt(0)\n\t" \
  "s_barrier\n\t" \
  "ds_read_b64_tr_b16 a[32:33], v78 offset:0\n\t" \
  "ds_read_b64_tr_b16 a[34:35], v78 offset:1024\n\t" \
  "ds_read_b64_tr_b16 a[36:37], v78 offset:8192\n\t" \
  "ds_read_b64_tr_b16 a[38:39], v78 offset:9216\n\t" \
  "ds_read_b64_tr_b16 a[40:41], v78 offset:8\n\t" \
  "ds_read_b64_tr_b16 a[42:43], v78 offset:1032\n\t" \
  "ds_read_b64_tr_b16 a[44:45], v78 offset:8200\n\t" \
  "ds_read_b64_tr_b16 a[46:47], v78 offset:9224\n\t" \
  "s_xor_b32 s61, s40, 1\n\t" \
  "s_mul_i32 s61, s61, 36864\n\t" \
  "s_add_u32 s61, s61, s55\n\t" \
  "s_add_u32 m0, s54, 126976\n\t" \
  "s_add_u32 s60, s61, 512\n\t" \
  "buffer_load_dwordx4 v75, %[Wo], s60 offen lds\n\t" \
  "s_add_u32 m0, s54, 128000\n\t" \
  "s_add_u32 s60, s61, 9728\n\t" \
  "buffer_load_dwordx4 v75, %[Wo], s60 offen lds\n\t" \
  "s_add_u32 m0, s54, 129024\n\t" \
  "s_add_u32 s60, s61, 18944\n\t" \
  "buffer_load_dwordx4 v75, %[Wo], s60 offen lds\n\t" \
  "s_add_u32 m0, s54, 130048\n\t" \
  "s_add_u32 s60, s61, 28160\n\t" \
  "buffer_load_dwordx4 v75, %[Wo], s60 offen lds\n\t" \
  "v_add_u32 v78, 16384, v64\n\t" \
  "v_add_u32 v79, 17408, v64\n\t" \
  "s_waitcnt vmcnt(12) lgkmcnt(0)\n\t" \
  "s_barrier\n\t" \
  "ds_read2_b32 v[80:81], v78 offset0:0 offset1:2\n\t" \
  "ds_read2_b32 v[82:83], v78 offset0:32 offset1:34\n\t" \
  "ds_read2_b32 v[84:85], v78 offset0:64 offset1:66\n\t" \
  "ds_read2_b32 v[86:87], v78 offset0:96 offset1:98\n\t" \
  "ds_read2_b32 v[88:89], v79 offset0:0 offset1:2\n\t" \
  "ds_read2_b32 v[90:91], v79 offset0:32 offset1:34\n\t" \
  "ds_read2_b32 v[92:93], v79 offset0:64 offset1:66\n\t" \
  "ds_read2_b32 v[94:95], v79 offset0:96 offset1:98\n\t" \
  "s_add_u32 m0, s54, 0\n\t" \
  "s_mov_b32 s60, 1048576\n\t" \
  "buffer_load_dwordx4 v71, %[Wq], s60 offen lds\n\t" \
  "s_add_u32 m0, s54, 1024\n\t" \
  "s_mov_b32 s60, 1081344\n\t" \
  "buffer_load_dwordx4 v71, %[Wq], s60 offen lds\n\t" \
  "s_add_u32 m0, s54, 2048\n\t" \
  "s_mov_b32 s60, 1114112\n\t" \
  "buffer_load_dwordx4 v71, %[Wq], s60 offen lds\n\t" \
  "s_add_u32 m0, s54, 3072\n\t" \
  "s_mov_b32 s60, 1146880\n\t" \
  "buffer_load_dwordx4 v71, %[Wq], s60 offen lds\n\t" \
  "s_waitcnt lgkmcnt(6)\n\t" \
  "v_perm_b32 v112, v82, v80, s62\n\t" \
  "v_perm_b32 v113, v82, v80, s63\n\t" \
  "s_waitcnt lgkmcnt(4)\n\t" \
  "v_perm_b32 v114, v86, v84, s62\n\t" \
  "v_perm_b32 v115, v86, v84, s63\n\t" \
  "s_waitcnt lgkmcnt(2)\n\t" \
  "v_perm_b32 v116, v90, v88, s62\n\t" \
  "v_perm_b32 v117, v90, v88, s63\n\t" \
  "s_waitcnt lgkmcnt(0)\n\t" \
  "v_perm_b32 v118, v94, v92, s62\n\t" \
  "v_perm_b32 v119, v94, v92, s63\n\t" \
  "ds_write_b128 v70, v[112:115] offset:24576\n\t" \
  "ds_write_b128 v70, v[116:119] offset:25600\n\t" \
  "v_perm_b32 v200, v83, v81, s62\n\t" \
  "v_perm_b32 v201, v83, v81, s63\n\t" \
  "v_perm_b32 v202, v87, v85, s62\n\t" \
  "v_perm_b32 v203, v87, v85, s63\n\t" \
  "v_perm_b32 v204, v91, v89, s62\n\t" \
  "v_perm_b32 v205, v91, v89, s63\n\t" \
  "v_perm_b32 v206, v95, v93, s62\n\t" \
  "v_perm_b32 v207, v95, v93, s63\n\t" \
  "v_add_u32 v78, 53248, v65\n\t" \
  "s_waitcnt vmcnt(12) lgkmcnt(0)\n\t" \
  "s_barrier\n\t" \
  "ds_read_b64_tr_b16 a[48:49], v78 offset:0\n\t" \
  "ds_read_b64_tr_b16 a[50:51], v78 offset:1024\n\t" \
  "ds_read_b64_tr_b16 a[52:53], v78 offset:8192\n\t" \
  "ds_read_b64_tr_b16 a[54:55], v78 offset:9216\n\t" \
  "ds_read_b64_tr_b16 a[56:57], v78 offset:8\n\t" \
  "ds_read_b64_tr_b16 a[58:59], v78 offset:1032\n\t" \
  "ds_read_b64_tr_b16 a[60:61], v78 offset:8200\n\t" \
  "ds_read_b64_tr_b16 a[62:63], v78 offset:9224\n\t" \
  "s_add_u32 m0, s54, 16384\n\t" \
  "s_mov_b32 s60, 1048576\n\t" \
  "buffer_load_dwordx4 v71, %[Wk], s60 offen lds\n\t" \
  "s_add_u32 m0, s54, 17408\n\t" \
  "s_mov_b32 s60, 1081344\n\t" \
  "buffer_load_dwordx4 v71, %[Wk], s60 offen lds\n\t" \
  "s_add_u32 m0, s54, 18432\n\t" \
  "s_mov_b32 s60, 1114112\n\t" \
  "buffer_load_dwordx4 v71, %[Wk], s60 offen lds\n\t" \
  "s_add_u32 m0, s54, 19456\n\t" \
  "s_mov_b32 s60, 1146880\n\t" \
  "buffer_load_dwordx4 v71, %[Wk], s60 offen lds\n\t" \
  "v_add_u32 v78, 90112, v66\n\t" \
  "s_waitcnt vmcnt(12) lgkmcnt(0)\n\t" \
  "s_barrier\n\t" \
  "ds_read_b64_tr_b16 a[64:65], v78 offset:0\n\t" \
  "ds_read_b64_tr_b16 a[66:67], v78 offset:1024\n\t" \
  "ds_read_b64_tr_b16 a[68:69], v78 offset:8192\n\t" \
  "ds_read_b64_tr_b16 a[70:71], v78 offset:9216\n\t" \
  "ds_read_b64_tr_b16 a[72:73], v78 offset:32\n\t" \
  "ds_read_b64_tr_b16 a[74:75], v78 offset:1056\n\t" \
  "ds_read_b64_tr_b16 a[76:77], v78 offset:8224\n\t" \
  "ds_read_b64_tr_b16 a[78:79], v78 offset:9248\n\t" \
  "s_xor_b32 s61, s40, 2\n\t" \
  "s_mul_i32 s61, s61, 36864\n\t" \
  "s_add_u32 s61, s61, s55\n\t" \
  "s_add_u32 m0, s54, 53248\n\t" \
  "s_add_u32 s60, s61, 0\n\t" \
  "buffer_load_dwordx4 v75, %[Wo], s60 offen lds\n\t" \
  "s_add_u32 m0, s54, 54272\n\t" \
  "s_add_u32 s60, s61, 9216\n\t" \
  "buffer_load_dwordx4 v75, %[Wo], s60 offen lds\n\t" \
  "s_add_u32 m0, s54, 55296\n\t" \
  "s_add_u32 s60, s61, 18432\n\t" \
  "buffer_load_dwordx4 v75, %[Wo], s60 offen lds\n\t" \
  "s_add_u32 m0, s54, 56320\n\t" \
  "s_add_u32 s60, s61, 27648\n\t" \
  "buffer_load_dwordx4 v75, %[Wo], s60 offen lds\n\t" \
  "v_add_u32 v78, 126976, v64\n\t" \
  "v_add_u32 v79, 128000, v64\n\t" \
  "s_waitcnt vmcnt(12) lgkmcnt(0)\n\t" \
  "s_barrier\n\t" \
  "ds_read2_b32 v[80:81], v78 offset0:0 offset1:2\n\t" \
  "ds_read2_b32 v[82:83], v78 offset0:32 offset1:34\n\t" \
  "ds_read2_b32 v[84:85], v78 offset0:64 offset1:66\n\t" \
  "ds_read2_b32 v[86:87], v78 offset0:96 offset1:98\n\t" \
  "ds_read2_b32 v[88:89], v79 offset0:0 offset1:2\n\t" \
  "ds_read2_b32 v[90:91], v79 offset0:32 offset1:34\n\t" \
  "ds_read2_b32 v[92:93], v79 offset0:64 offset1:66\n\t" \
  "ds_read2_b32 v[94:95], v79 offset0:96 offset1:98\n\t" \
  "s_add_u32 m0, s54, 90112\n\t" \
  "s_mov_b32 s60, 1048576\n\t" \
  "buffer_load_dwordx4 v71, %[Wv], s60 offen lds\n\t" \
  "s_add_u32 m0, s54, 91136\n\t" \
  "s_mov_b32 s60, 1081344\n\t" \
  "buffer_load_dwordx4 v71, %[Wv], s60 offen lds\n\t" \
  "s_add_u32 m0, s54, 92160\n\t" \
  "s_mov_b32 s60, 1114112\n\t" \
  "buffer_load_dwordx4 v71, %[Wv], s60 offen lds\n\t" \
  "s_add_u32 m0, s54, 93184\n\t" \
  "s_mov_b32 s60, 1146880\n\t" \
  "buffer_load_dwordx4 v71, %[Wv], s60 offen lds\n\t" \
  "s_waitcnt lgkmcnt(6)\n\t" \
  "v_perm_b32 v112, v82, v80, s62\n\t" \
  "v_perm_b32 v113, v82, v80, s63\n\t" \
  "s_waitcnt lgkmcnt(4)\n\t" \
  "v_perm_b32 v114, v86, v84, s62\n\t" \
  "v_perm_b32 v115, v86, v84, s63\n\t" \
  "s_waitcnt lgkmcnt(2)\n\t" \
  "v_perm_b32 v116, v90, v88, s62\n\t" \
  "v_perm_b32 v117, v90, v88, s63\n\t" \
  "s_waitcnt lgkmcnt(0)\n\t" \
  "v_perm_b32 v118, v94, v92, s62\n\t" \
  "v_perm_b32 v119, v94, v92, s63\n\t" \
  "ds_write_b128 v70, v[112:115] offset:26624\n\t" \
  "ds_write_b128 v70, v[116:119] offset:27648\n\t" \
  "v_perm_b32 v232, v83, v81, s62\n\t" \
  "v_perm_b32 v233, v83, v81, s63\n\t" \
  "v_perm_b32 v234, v87, v85, s62\n\t" \
  "v_perm_b32 v235, v87, v85, s63\n\t" \
  "v_perm_b32 v236, v91, v89, s62\n\t" \
  "v_perm_b32 v237, v91, v89, s63\n\t" \
  "v_perm_b32 v238, v95, v93, s62\n\t" \
  "v_perm_b32 v239, v95, v93, s63\n\t" \
  "v_add_u32 v78, 0, v65\n\t" \
  "s_waitcnt vmcnt(12) lgkmcnt(0)\n\t" \
  "s_barrier\n\t" \
  "ds_read_b64_tr_b16 a[80:81], v78 offset:0\n\t" \
  "ds_read_b64_tr_b16 a[82:83], v78 offset:1024\n\t" \
  "ds_read_b64_tr_b16 a[84:85], v78 offset:8192\n\t" \
  "ds_read_b64_tr_b16 a[86:87], v78 offset:9216\n\t" \
  "ds_read_b64_tr_b16 a[88:89], v78 offset:8\n\t" \
  "ds_read_b64_tr_b16 a[90:91], v78 offset:1032\n\t" \
  "ds_read_b64_tr_b16 a[92:93], v78 offset:8200\n\t" \
  "ds_read_b64_tr_b16 a[94:95], v78 offset:9224\n\t" \
  "s_add_u32 m0, s54, 126976\n\t" \
  "s_mov_b32 s60, 1572864\n\t" \
  "buffer_load_dwordx4 v71, %[Wq], s60 offen lds\n\t" \
  "s_add_u32 m0, s54, 128000\n\t" \
  "s_mov_b32 s60, 1605632\n\t" \
  "buffer_load_dwordx4 v71, %[Wq], s60 offen lds\n\t" \
  "s_add_u32 m0, s54, 129024\n\t" \
  "s_mov_b32 s60, 1638400\n\t" \
  "buffer_load_dwordx4 v71, %[Wq], s60 offen lds\n\t" \
  "s_add_u32 m0, s54, 130048\n\t" \
  "s_mov_b32 s60, 1671168\n\t" \
  "buffer_load_dwordx4 v71, %[Wq], s60 offen lds\n\t" \
  "v_add_u32 v78, 16384, v65\n\t" \
  "s_waitcnt vmcnt(12) lgkmcnt(0)\n\t" \
  "s_barrier\n\t" \
  "ds_read_b64_tr_b16 v[32:33], v78 offset:0\n\t" \
  "ds_read_b64_tr_b16 v[34:35], v78 offset:1024\n\t" \
  "ds_read_b64_tr_b16 v[36:37], v78 offset:8192\n\t" \
  "ds_read_b64_tr_b16 v[38:39], v78 offset:9216\n\t" \
  "ds_read_b64_tr_b16 a[96:97], v78 offset:8\n\t" \
  "ds_read_b64_tr_b16 a[98:99], v78 offset:1032\n\t" \
  "ds_read_b64_tr_b16 a[100:101], v78 offset:8200\n\t" \
  "ds_read_b64_tr_b16 a[102:103], v78 offset:9224\n\t" \
  "s_xor_b32 s61, s40, 2\n\t" \
  "s_mul_i32 s61, s61, 36864\n\t" \
  "s_add_u32 s61, s61, s55\n\t" \
  "s_add_u32 m0, s54, 0\n\t" \
  "s_add_u32 s60, s61, 512\n\t" \
  "buffer_load_dwordx4 v75, %[Wo], s60 offen lds\n\t" \
  "s_add_u32 m0, s54, 1024\n\t" \
  "s_add_u32 s60, s61, 9728\n\t" \
  "buffer_load_dwordx4 v75, %[Wo], s60 offen lds\n\t" \
  "s_add_u32 m0, s54, 2048\n\t" \
  "s_add_u32 s60, s61, 18944\n\t" \
  "buffer_load_dwordx4 v75, %[Wo], s60 offen lds\n\t" \
  "s_add_u32 m0, s54, 3072\n\t" \
  "s_add_u32 s60, s61, 28160\n\t" \
  "buffer_load_dwordx4 v75, %[Wo], s60 offen lds\n\t" \
  "v_add_u32 v78, 53248, v64\n\t" \
  "v_add_u32 v79, 54272, v64\n\t" \
  "s_waitcnt vmcnt(12) lgkmcnt(0)\n\t" \
  "s_barrier\n\t" \
  "ds_read2_b32 v[80:81], v78 offset0:0 offset1:2\n\t" \
  "ds_read2_b32 v[82:83], v78 offset0:32 offset1:34\n\t" \
  "ds_read2_b32 v[84:85], v78 offset0:64 offset1:66\n\t" \
  "ds_read2_b32 v[86:87], v78 offset0:96 offset1:98\n\t" \
  "ds_read2_b32 v[88:89], v79 offset0:0 offset1:2\n\t" \
  "ds_read2_b32 v[90:91], v79 offset0:32 offset1:34\n\t" \
  "ds_read2_b32 v[92:93], v79 offset0:64 offset1:66\n\t" \
  "ds_read2_b32 v[94:95], v79 offset0:96 offset1:98\n\t" \
  "s_add_u32 m0, s54, 16384\n\t" \
  "s_mov_b32 s60, 1572864\n\t" \
  "buffer_load_dwordx4 v71, %[Wk], s60 offen lds\n\t" \
  "s_add_u32 m0, s54, 17408\n\t" \
  "s_mov_b32 s60, 1605632\n\t" \
  "buffer_load_dwordx4 v71, %[Wk], s60 offen lds\n\t" \
  "s_add_u32 m0, s54, 18432\n\t" \
  "s_mov_b32 s60, 1638400\n\t" \
  "buffer_load_dwordx4 v71, %[Wk], s60 offen lds\n\t" \
  "s_add_u32 m0, s54, 19456\n\t" \
  "s_mov_b32 s60, 1671168\n\t" \
  "buffer_load_dwordx4 v71, %[Wk], s60 offen lds\n\t" \
  "s_waitcnt lgkmcnt(6)\n\t" \
  "v_perm_b32 v112, v82, v80, s62\n\t" \
  "v_perm_b32 v113, v82, v80, s63\n\t" \
  "s_waitcnt lgkmcnt(4)\n\t" \
  "v_perm_b32 v114, v86, v84, s62\n\t" \
  "v_perm_b32 v115, v86, v84, s63\n\t" \
  "s_waitcnt lgkmcnt(2)\n\t" \
  "v_perm_b32 v116, v90, v88, s62\n\t" \
  "v_perm_b32 v117, v90, v88, s63\n\t" \
  "s_waitcnt lgkmcnt(0)\n\t" \
  "v_perm_b32 v118, v94, v92, s62\n\t" \
  "v_perm_b32 v119, v94, v92, s63\n\t" \
  "ds_write_b128 v70, v[112:115] offset:28672\n\t" \
  "ds_write_b128 v70, v[116:119] offset:29696\n\t" \
  "v_perm_b32 v208, v83, v81, s62\n\t" \
  "v_perm_b32 v209, v83, v81, s63\n\t" \
  "v_perm_b32 v210, v87, v85, s62\n\t" \
  "v_perm_b32 v211, v87, v85, s63\n\t" \
  "v_perm_b32 v212, v91, v89, s62\n\t" \
  "v_perm_b32 v213, v91, v89, s63\n\t" \
  "v_perm_b32 v214, v95, v93, s62\n\t" \
  "v_perm_b32 v215, v95, v93, s63\n\t" \
  "v_add_u32 v78, 90112, v66\n\t" \
  "s_waitcnt vmcnt(12) lgkmcnt(0)\n\t" \
  "s_barrier\n\t" \
  "ds_read_b64_tr_b16 a[104:105], v78 offset:0\n\t" \
  "ds_read_b64_tr_b16 a[106:107], v78 offset:1024\n\t" \
  "ds_read_b64_tr_b16 a[108:109], v78 offset:8192\n\t" \
  "ds_read_b64_tr_b16 a[110:111], v78 offset:9216\n\t" \
  "ds_read_b64_tr_b16 a[112:113], v78 offset:32\n\t" \
  "ds_read_b64_tr_b16 a[114:115], v78 offset:1056\n\t" \
  "ds_read_b64_tr_b16 a[116:117], v78 offset:8224\n\t" \
  "ds_read_b64_tr_b16 a[118:119], v78 offset:9248\n\t" \
  "s_add_u32 m0, s54, 53248\n\t" \
  "s_mov_b32 s60, 1572864\n\t" \
  "buffer_load_dwordx4 v71, %[Wv], s60 offen lds\n\t" \
  "s_add_u32 m0, s54, 54272\n\t" \
  "s_mov_b32 s60, 1605632\n\t" \
  "buffer_load_dwordx4 v71, %[Wv], s60 offen lds\n\t" \
  "s_add_u32 m0, s54, 55296\n\t" \
  "s_mov_b32 s60, 1638400\n\t" \
  "buffer_load_dwordx4 v71, %[Wv], s60 offen lds\n\t" \
  "s_add_u32 m0, s54, 56320\n\t" \
  "s_mov_b32 s60, 1671168\n\t" \
  "buffer_load_dwordx4 v71, %[Wv], s60 offen lds\n\t" \
  "v_add_u32 v78, 126976, v65\n\t" \
  "s_waitcnt vmcnt(12) lgkmcnt(0)\n\t" \
  "s_barrier\n\t" \
  "ds_read_b64_tr_b16 a[120:121], v78 offset:0\n\t" \
  "ds_read_b64_tr_b16 a[122:123], v78 offset:1024\n\t" \
  "ds_read_b64_tr_b16 a[124:125], v78 offset:8192\n\t" \
  "ds_read_b64_tr_b16 a[126:127], v78 offset:9216\n\t" \
  "ds_read_b64_tr_b16 v[16:17], v78 offset:8\n\t" \
  "ds_read_b64_tr_b16 v[18:19], v78 offset:1032\n\t" \
  "ds_read_b64_tr_b16 v[20:21], v78 offset:8200\n\t" \
  "ds_read_b64_tr_b16 v[22:23], v78 offset:9224\n\t" \
  "s_xor_b32 s61, s40, 3\n\t" \
  "s_mul_i32 s61, s61, 36864\n\t" \
  "s_add_u32 s61, s61, s55\n\t" \
  "s_add_u32 m0, s54, 90112\n\t" \
  "s_add_u32 s60, s61, 0\n\t" \
  "buffer_load_dwordx4 v75, %[Wo], s60 offen lds\n\t" \
  "s_add_u32 m0, s54, 91136\n\t" \
  "s_add_u32 s60, s61, 9216\n\t" \
  "buffer_load_dwordx4 v75, %[Wo], s60 offen lds\n\t" \
  "s_add_u32 m0, s54, 92160\n\t" \
  "s_add_u32 s60, s61, 18432\n\t" \
  "buffer_load_dwordx4 v75, %[Wo], s60 offen lds\n\t" \
  "s_add_u32 m0, s54, 93184\n\t" \
  "s_add_u32 s60, s61, 27648\n\t" \
  "buffer_load_dwordx4 v75, %[Wo], s60 offen lds\n\t" \
  "v_add_u32 v78, 0, v64\n\t" \
  "v_add_u32 v79, 1024, v64\n\t" \
  "s_waitcnt vmcnt(12) lgkmcnt(0)\n\t" \
  "s_barrier\n\t" \
  "ds_read2_b32 v[80:81], v78 offset0:0 offset1:2\n\t" \
  "ds_read2_b32 v[82:83], v78 offset0:32 offset1:34\n\t" \
  "ds_read2_b32 v[84:85], v78 offset0:64 offset1:66\n\t" \
  "ds_read2_b32 v[86:87], v78 offset0:96 offset1:98\n\t" \
  "ds_read2_b32 v[88:89], v79 offset0:0 offset1:2\n\t" \
  "ds_read2_b32 v[90:91], v79 offset0:32 offset1:34\n\t" \
  "ds_read2_b32 v[92:93], v79 offset0:64 offset1:66\n\t" \
  "ds_read2_b32 v[94:95], v79 offset0:96 offset1:98\n\t" \
  "s_add_u32 m0, s54, 126976\n\t" \
  "s_mov_b32 s60, 2097152\n\t" \
  "buffer_load_dwordx4 v71, %[Wq], s60 offen lds\n\t" \
  "s_add_u32 m0, s54, 128000\n\t" \
  "s_mov_b32 s60, 2129920\n\t" \
  "buffer_load_dwordx4 v71, %[Wq], s60 offen lds\n\t" \
  "s_add_u32 m0, s54, 129024\n\t" \
  "s_mov_b32 s60, 2162688\n\t" \
  "buffer_load_dwordx4 v71, %[Wq], s60 offen lds\n\t" \
  "s_add_u32 m0, s54, 130048\n\t" \
  "s_mov_b32 s60, 2195456\n\t" \
  "buffer_load_dwordx4 v71, %[Wq], s60 offen lds\n\t" \
  "s_waitcnt lgkmcnt(6)\n\t" \
  "v_perm_b32 v112, v82, v80, s62\n\t" \
  "v_perm_b32 v113, v82, v80, s63\n\t" \
  "s_waitcnt lgkmcnt(4)\n\t" \
  "v_perm_b32 v114, v86, v84, s62\n\t" \
  "v_perm_b32 v115, v86, v84, s63\n\t" \
  "s_waitcnt lgkmcnt(2)\n\t" \
  "v_perm_b32 v116, v90, v88, s62\n\t" \
  "v_perm_b32 v117, v90, v88, s63\n\t" \
  "s_waitcnt lgkmcnt(0)\n\t" \
  "v_perm_b32 v118, v94, v92, s62\n\t" \
  "v_perm_b32 v119, v94, v92, s63\n\t" \
  "ds_write_b128 v70, v[112:115] offset:30720\n\t" \
  "ds_write_b128 v70, v[116:119] offset:31744\n\t" \
  "v_perm_b32 v240, v83, v81, s62\n\t" \
  "v_perm_b32 v241, v83, v81, s63\n\t" \
  "v_perm_b32 v242, v87, v85, s62\n\t" \
  "v_perm_b32 v243, v87, v85, s63\n\t" \
  "v_perm_b32 v244, v91, v89, s62\n\t" \
  "v_perm_b32 v245, v91, v89, s63\n\t" \
  "v_perm_b32 v246, v95, v93, s62\n\t" \
  "v_perm_b32 v247, v95, v93, s63\n\t" \
  "v_add_u32 v78, 16384, v65\n\t" \
  "s_waitcnt vmcnt(12) lgkmcnt(0)\n\t" \
  "s_barrier\n\t" \
  "ds_read_b64_tr_b16 a[128:129], v78 offset:0\n\t" \
  "ds_read_b64_tr_b16 a[130:131], v78 offset:1024\n\t" \
  "ds_read_b64_tr_b16 a[132:133], v78 offset:8192\n\t" \
  "ds_read_b64_tr_b16 a[134:135], v78 offset:9216\n\t" \
  "ds_read_b64_tr_b16 a[136:137], v78 offset:8\n\t" \
  "ds_read_b64_tr_b16 a[138:139], v78 offset:1032\n\t" \
  "ds_read_b64_tr_b16 a[140:141], v78 offset:8200\n\t" \
  "ds_read_b64_tr_b16 a[142:143], v78 offset:9224\n\t" \
  "s_add_u32 m0, s54, 0\n\t" \
  "s_mov_b32 s60, 2097152\n\t" \
  "buffer_load_dwordx4 v71, %[Wk], s60 offen lds\n\t" \
  "s_add_u32 m0, s54, 1024\n\t" \
  "s_mov_b32 s60, 2129920\n\t" \
  "buffer_load_dwordx4 v71, %[Wk], s60 offen lds\n\t" \
  "s_add_u32 m0, s54, 2048\n\t" \
  "s_mov_b32 s60, 2162688\n\t" \
  "buffer_load_dwordx4 v71, %[Wk], s60 offen lds\n\t" \
  "s_add_u32 m0, s54, 3072\n\t" \
  "s_mov_b32 s60, 2195456\n\t" \
  "buffer_load_dwordx4 v71, %[Wk], s60 offen lds\n\t" \
  "v_add_u32 v78, 53248, v66\n\t" \
  "s_waitcnt vmcnt(12) lgkmcnt(0)\n\t" \
  "s_barrier\n\t" \
  "ds_read_b64_tr_b16 a[144:145], v78 offset:0\n\t" \
  "ds_read_b64_tr_b16 a[146:147], v78 offset:1024\n\t" \
  "ds_read_b64_tr_b16 a[148:149], v78 offset:8192\n\t" \
  "ds_read_b64_tr_b16 a[150:151], v78 offset:9216\n\t" \
  "ds_read_b64_tr_b16 a[152:153], v78 offset:32\n\t" \
  "ds_read_b64_tr_b16 a[154:155], v78 offset:1056\n\t" \
  "ds_read_b64_tr_b16 a[156:157], v78 offset:8224\n\t" \
  "ds_read_b64_tr_b16 a[158:159], v78 offset:9248\n\t" \
  "s_xor_b32 s61, s40, 3\n\t" \
  "s_mul_i32 s61, s61, 36864\n\t" \
  "s_add_u32 s61, s61, s55\n\t" \
  "s_add_u32 m0, s54, 16384\n\t" \
  "s_add_u32 s60, s61, 512\n\t" \
  "buffer_load_dwordx4 v75, %[Wo], s60 offen lds\n\t" \
  "s_add_u32 m0, s54, 17408\n\t" \
  "s_add_u32 s60, s61, 9728\n\t" \
  "buffer_load_dwordx4 v75, %[Wo], s60 offen lds\n\t" \
  "s_add_u32 m0, s54, 18432\n\t" \
  "s_add_u32 s60, s61, 18944\n\t" \
  "buffer_load_dwordx4 v75, %[Wo], s60 offen lds\n\t" \
  "s_add_u32 m0, s54, 19456\n\t" \
  "s_add_u32 s60, s61, 28160\n\t" \
  "buffer_load_dwordx4 v75, %[Wo], s60 offen lds\n\t" \
  "v_add_u32 v78, 90112, v64\n\t" \
  "v_add_u32 v79, 91136, v64\n\t" \
  "s_waitcnt vmcnt(12) lgkmcnt(0)\n\t" \
  "s_barrier\n\t" \
  "ds_read2_b32 v[80:81], v78 offset0:0 offset1:2\n\t" \
  "ds_read2_b32 v[82:83], v78 offset0:32 offset1:34\n\t" \
  "ds_read2_b32 v[84:85], v78 offset0:64 offset1:66\n\t" \
  "ds_read2_b32 v[86:87], v78 offset0:96 offset1:98\n\t" \
  "ds_read2_b32 v[88:89], v79 offset0:0 offset1:2\n\t" \
  "ds_read2_b32 v[90:91], v79 offset0:32 offset1:34\n\t" \
  "ds_read2_b32 v[92:93], v79 offset0:64 offset1:66\n\t" \
  "ds_read2_b32 v[94:95], v79 offset0:96 offset1:98\n\t" \
  "s_add_u32 m0, s54, 53248\n\t" \
  "s_mov_b32 s60, 2097152\n\t" \
  "buffer_load_dwordx4 v71, %[Wv], s60 offen lds\n\t" \
  "s_add_u32 m0, s54, 54272\n\t" \
  "s_mov_b32 s60, 2129920\n\t" \
  "buffer_load_dwordx4 v71, %[Wv], s60 offen lds\n\t" \
  "s_add_u32 m0, s54, 55296\n\t" \
  "s_mov_b32 s60, 2162688\n\t" \
  "buffer_load_dwordx4 v71, %[Wv], s60 offen lds\n\t" \
  "s_add_u32 m0, s54, 56320\n\t" \
  "s_mov_b32 s60, 2195456\n\t" \
  "buffer_load_dwordx4 v71, %[Wv], s60 offen lds\n\t" \
  "s_waitcnt lgkmcnt(6)\n\t" \
  "v_perm_b32 v192, v82, v80, s62\n\t" \
  "v_perm_b32 v193, v82, v80, s63\n\t" \
  "s_waitcnt lgkmcnt(4)\n\t" \
  "v_perm_b32 v194, v86, v84, s62\n\t" \
  "v_perm_b32 v195, v86, v84, s63\n\t" \
  "s_waitcnt lgkmcnt(2)\n\t" \
  "v_perm_b32 v196, v90, v88, s62\n\t" \
  "v_perm_b32 v197, v90, v88, s63\n\t" \
  "s_waitcnt lgkmcnt(0)\n\t" \
  "v_perm_b32 v198, v94, v92, s62\n\t" \
  "v_perm_b32 v199, v94, v92, s63\n\t" \
  "v_perm_b32 v216, v83, v81, s62\n\t" \
  "v_perm_b32 v217, v83, v81, s63\n\t" \
  "v_perm_b32 v218, v87, v85, s62\n\t" \
  "v_perm_b32 v219, v87, v85, s63\n\t" \
  "v_perm_b32 v220, v91, v89, s62\n\t" \
  "v_perm_b32 v221, v91, v89, s63\n\t" \
  "v_perm_b32 v222, v95, v93, s62\n\t" \
  "v_perm_b32 v223, v95, v93, s63\n\t" \
  "v_add_u32 v78, 126976, v65\n\t" \
  "s_waitcnt vmcnt(12) lgkmcnt(0)\n\t" \
  "s_barrier\n\t" \
  "ds_read_b64_tr_b16 a[160:161], v78 offset:0\n\t" \
  "ds_read_b64_tr_b16 a[162:163], v78 offset:1024\n\t" \
  "ds_read_b64_tr_b16 a[164:165], v78 offset:8192\n\t" \
  "ds_read_b64_tr_b16 a[166:167], v78 offset:9216\n\t" \
  "ds_read_b64_tr_b16 a[168:169], v78 offset:8\n\t" \
  "ds_read_b64_tr_b16 a[170:171], v78 offset:1032\n\t" \
  "ds_read_b64_tr_b16 a[172:173], v78 offset:8200\n\t" \
  "ds_read_b64_tr_b16 a[174:175], v78 offset:9224\n\t" \
  "s_add_u32 m0, s54, 90112\n\t" \
  "s_mov_b32 s60, 2621440\n\t" \
  "buffer_load_dwordx4 v71, %[Wq], s60 offen lds\n\t" \
  "s_add_u32 m0, s54, 91136\n\t" \
  "s_mov_b32 s60, 2654208\n\t" \
  "buffer_load_dwordx4 v71, %[Wq], s60 offen lds\n\t" \
  "s_add_u32 m0, s54, 92160\n\t" \
  "s_mov_b32 s60, 2686976\n\t" \
  "buffer_load_dwordx4 v71, %[Wq], s60 offen lds\n\t" \
  "s_add_u32 m0, s54, 93184\n\t" \
  "s_mov_b32 s60, 2719744\n\t" \
  "buffer_load_dwordx4 v71, %[Wq], s60 offen lds\n\t" \
  "v_add_u32 v78, 0, v65\n\t" \
  "s_waitcnt vmcnt(12) lgkmcnt(0)\n\t" \
  "s_barrier\n\t" \
  "ds_read_b64_tr_b16 a[176:177], v78 offset:0\n\t" \
  "ds_read_b64_tr_b16 a[178:179], v78 offset:1024\n\t" \
  "ds_read_b64_tr_b16 a[180:181], v78 offset:8192\n\t" \
  "ds_read_b64_tr_b16 a[182:183], v78 offset:9216\n\t" \
  "ds_read_b64_tr_b16 a[184:185], v78 offset:8\n\t" \
  "ds_read_b64_tr_b16 a[186:187], v78 offset:1032\n\t" \
  "ds_read_b64_tr_b16 a[188:189], v78 offset:8200\n\t" \
  "ds_read_b64_tr_b16 a[190:191], v78 offset:9224\n\t" \
  "s_add_u32 m0, s54, 126976\n\t" \
  "s_add_u32 s60, s75, 0\n\t" \
  "buffer_load_dwordx4 v75, %[Wo], s60 offen lds\n\t" \
  "s_add_u32 m0, s54, 128000\n\t" \
  "s_add_u32 s60, s75, 9216\n\t" \
  "buffer_load_dwordx4 v75, %[Wo], s60 offen lds\n\t" \
  "s_add_u32 m0, s54, 129024\n\t" \
  "s_add_u32 s60, s75, 18432\n\t" \
  "buffer_load_dwordx4 v75, %[Wo], s60 offen lds\n\t" \
  "s_add_u32 m0, s54, 130048\n\t" \
  "s_add_u32 s60, s75, 27648\n\t" \
  "buffer_load_dwordx4 v75, %[Wo], s60 offen lds\n\t" \
  "v_add_u32 v78, 16384, v64\n\t" \
  "v_add_u32 v79, 17408, v64\n\t" \
  "s_waitcnt vmcnt(12) lgkmcnt(0)\n\t" \
  "s_barrier\n\t" \
  "ds_read2_b32 v[80:81], v78 offset0:0 offset1:2\n\t" \
  "ds_read2_b32 v[82:83], v78 offset0:32 offset1:34\n\t" \
  "ds_read2_b32 v[84:85], v78 offset0:64 offset1:66\n\t" \
  "ds_read2_b32 v[86:87], v78 offset0:96 offset1:98\n\t" \
  "ds_read2_b32 v[88:89], v79 offset0:0 offset1:2\n\t" \
  "ds_read2_b32 v[90:91], v79 offset0:32 offset1:34\n\t" \
  "ds_read2_b32 v[92:93], v79 offset0:64 offset1:66\n\t" \
  "ds_read2_b32 v[94:95], v79 offset0:96 offset1:98\n\t" \
  "s_add_u32 m0, s54, 0\n\t" \
  "s_mov_b32 s60, 2621440\n\t" \
  "buffer_load_dwordx4 v71, %[Wk], s60 offen lds\n\t" \
  "s_add_u32 m0, s54, 1024\n\t" \
  "s_mov_b32 s60, 2654208\n\t" \
  "buffer_load_dwordx4 v71, %[Wk], s60 offen lds\n\t" \
  "s_add_u32 m0, s54, 2048\n\t" \
  "s_mov_b32 s60, 2686976\n\t" \
  "buffer_load_dwordx4 v71, %[Wk], s60 offen lds\n\t" \
  "s_add_u32 m0, s54, 3072\n\t" \
  "s_mov_b32 s60, 2719744\n\t" \
  "buffer_load_dwordx4 v71, %[Wk], s60 offen lds\n\t" \
  "s_waitcnt lgkmcnt(6)\n\t" \
  "v_perm_b32 v224, v82, v80, s62\n\t" \
  "v_perm_b32 v225, v82, v80, s63\n\t" \
  "s_waitcnt lgkmcnt(4)\n\t" \
  "v_perm_b32 v226, v86, v84, s62\n\t" \
  "v_perm_b32 v227, v86, v84, s63\n\t" \
  "s_waitcnt lgkmcnt(2)\n\t" \
  "v_perm_b32 v228, v90, v88, s62\n\t" \
  "v_perm_b32 v229, v90, v88, s63\n\t" \
  "s_waitcnt lgkmcnt(0)\n\t" \
  "v_perm_b32 v230, v94, v92, s62\n\t" \
  "v_perm_b32 v231, v94, v92, s63\n\t" \
  "v_perm_b32 v120, v83, v81, s62\n\t" \
  "v_perm_b32 v121, v83, v81, s63\n\t" \
  "v_perm_b32 v122, v87, v85, s62\n\t" \
  "v_perm_b32 v123, v87, v85, s63\n\t" \
  "v_perm_b32 v124, v91, v89, s62\n\t" \
  "v_perm_b32 v125, v91, v89, s63\n\t" \
  "v_perm_b32 v126, v95, v93, s62\n\t" \
  "v_perm_b32 v127, v95, v93, s63\n\t" \
  "ds_write_b128 v70, v[120:123] offset:32768\n\t" \
  "ds_write_b128 v70, v[124:127] offset:33792\n\t" \
  "v_add_u32 v78, 53248, v66\n\t" \
  "s_waitcnt vmcnt(12) lgkmcnt(0)\n\t" \
  "s_barrier\n\t" \
  "ds_read_b64_tr_b16 a[192:193], v78 offset:0\n\t" \
  "ds_read_b64_tr_b16 a[194:195], v78 offset:1024\n\t" \
  "ds_read_b64_tr_b16 a[196:197], v78 offset:8192\n\t" \
  "ds_read_b64_tr_b16 a[198:199], v78 offset:9216\n\t" \
  "ds_read_b64_tr_b16 v[48:49], v78 offset:32\n\t" \
  "ds_read_b64_tr_b16 v[50:51], v78 offset:1056\n\t" \
  "ds_read_b64_tr_b16 v[52:53], v78 offset:8224\n\t" \
  "ds_read_b64_tr_b16 v[54:55], v78 offset:9248\n\t" \
  "s_add_u32 m0, s54, 16384\n\t" \
  "s_mov_b32 s60, 2621440\n\t" \
  "buffer_load_dwordx4 v71, %[Wv], s60 offen lds\n\t" \
  "s_add_u32 m0, s54, 17408\n\t" \
  "s_mov_b32 s60, 2654208\n\t" \
  "buffer_load_dwordx4 v71, %[Wv], s60 offen lds\n\t" \
  "s_add_u32 m0, s54, 18432\n\t" \
  "s_mov_b32 s60, 2686976\n\t" \
  "buffer_load_dwordx4 v71, %[Wv], s60 offen lds\n\t" \
  "s_add_u32 m0, s54, 19456\n\t" \
  "s_mov_b32 s60, 2719744\n\t" \
  "buffer_load_dwordx4 v71, %[Wv], s60 offen lds\n\t" \
  "v_add_u32 v78, 90112, v65\n\t" \
  "s_waitcnt vmcnt(12) lgkmcnt(0)\n\t" \
  "s_barrier\n\t" \
  "ds_read_b64_tr_b16 a[200:201], v78 offset:0\n\t" \
  "ds_read_b64_tr_b16 a[202:203], v78 offset:1024\n\t" \
  "ds_read_b64_tr_b16 a[204:205], v78 offset:8192\n\t" \
  "ds_read_b64_tr_b16 a[206:207], v78 offset:9216\n\t" \
  "ds_read_b64_tr_b16 v[24:25], v78 offset:8\n\t" \
  "ds_read_b64_tr_b16 v[26:27], v78 offset:1032\n\t" \
  "ds_read_b64_tr_b16 v[28:29], v78 offset:8200\n\t" \
  "ds_read_b64_tr_b16 v[30:31], v78 offset:9224\n\t" \
  "s_add_u32 m0, s54, 53248\n\t" \
  "s_mov_b32 s60, 3145728\n\t" \
  "buffer_load_dwordx4 v71, %[Wq], s60 offen lds\n\t" \
  "s_add_u32 m0, s54, 54272\n\t" \
  "s_mov_b32 s60, 3178496\n\t" \
  "buffer_load_dwordx4 v71, %[Wq], s60 offen lds\n\t" \
  "s_add_u32 m0, s54, 55296\n\t" \
  "s_mov_b32 s60, 3211264\n\t" \
  "buffer_load_dwordx4 v71, %[Wq], s60 offen lds\n\t" \
  "s_add_u32 m0, s54, 56320\n\t" \
  "s_mov_b32 s60, 3244032\n\t" \
  "buffer_load_dwordx4 v71, %[Wq], s60 offen lds\n\t" \
  "v_add_u32 v78, 126976, v67\n\t" \
  "s_waitcnt vmcnt(12) lgkmcnt(0)\n\t" \
  "s_barrier\n\t" \
  "ds_read_u16 v80, v78 offset:0\n\t" \
  "ds_read_u16 v81, v78 offset:128\n\t" \
  "ds_read_u16 v82, v78 offset:4096\n\t" \
  "ds_read_u16 v83, v78 offset:4224\n\t" \
  "ds_read_u16 v84, v78 offset:256\n\t" \
  "ds_read_u16 v85, v78 offset:384\n\t" \
  "ds_read_u16 v86, v78 offset:4352\n\t" \
  "ds_read_u16 v87, v78 offset:4480\n\t" \
  "ds_read_u16 v88, v78 offset:1024\n\t" \
  "ds_read_u16 v89, v78 offset:1152\n\t" \
  "ds_read_u16 v90, v78 offset:5120\n\t" \
  "ds_read_u16 v91, v78 offset:5248\n\t" \
  "ds_read_u16 v92, v78 offset:1280\n\t" \
  "ds_read_u16 v93, v78 offset:1408\n\t" \
  "ds_read_u16 v94, v78 offset:5376\n\t" \
  "ds_read_u16 v95, v78 offset:5504\n\t" \
  "ds_read_u16 v96, v78 offset:8192\n\t" \
  "ds_read_u16 v97, v78 offset:8320\n\t" \
  "ds_read_u16 v98, v78 offset:12288\n\t" \
  "ds_read_u16 v99, v78 offset:12416\n\t" \
  "ds_read_u16 v100, v78 offset:8448\n\t" \
  "ds_read_u16 v101, v78 offset:8576\n\t" \
  "ds_read_u16 v102, v78 offset:12544\n\t" \
  "ds_read_u16 v103, v78 offset:12672\n\t" \
  "ds_read_u16 v104, v78 offset:9216\n\t" \
  "ds_read_u16 v105, v78 offset:9344\n\t" \
  "ds_read_u16 v106, v78 offset:13312\n\t" \
  "ds_read_u16 v107, v78 offset:13440\n\t" \
  "ds_read_u16 v108, v78 offset:9472\n\t" \
  "ds_read_u16 v109, v78 offset:9600\n\t" \
  "ds_read_u16 v110, v78 offset:13568\n\t" \
  "ds_read_u16 v111, v78 offset:13696\n\t" \
  "s_add_u32 m0, s54, 90112\n\t" \
  "s_mov_b32 s60, 3145728\n\t" \
  "buffer_load_dwordx4 v71, %[Wk], s60 offen lds\n\t" \
  "s_add_u32 m0, s54, 91136\n\t" \
  "s_mov_b32 s60, 3178496\n\t" \
  "buffer_load_dwordx4 v71, %[Wk], s60 offen lds\n\t" \
  "s_add_u32 m0, s54, 92160\n\t" \
  "s_mov_b32 s60, 3211264\n\t" \
  "buffer_load_dwordx4 v71, %[Wk], s60 offen lds\n\t" \
  "s_add_u32 m0, s54, 93184\n\t" \
  "s_mov_b32 s60, 3244032\n\t" \
  "buffer_load_dwordx4 v71, %[Wk], s60 offen lds\n\t" \
  "s_waitcnt lgkmcnt(0)\n\t" \
  "v_lshl_or_b32 v248, v81, 16, v80\n\t" \
  "v_lshl_or_b32 v249, v83, 16, v82\n\t" \
  "v_lshl_or_b32 v250, v85, 16, v84\n\t" \
  "v_lshl_or_b32 v251, v87, 16, v86\n\t" \
  "v_lshl_or_b32 v252, v89, 16, v88\n\t" \
  "v_lshl_or_b32 v253, v91, 16, v90\n\t" \
  "v_lshl_or_b32 v254, v93, 16, v92\n\t" \
  "v_lshl_or_b32 v255, v95, 16, v94\n\t" \
  "v_lshl_or_b32 v112, v97, 16, v96\n\t" \
  "v_lshl_or_b32 v113, v99, 16, v98\n\t" \
  "v_lshl_or_b32 v114, v101, 16, v100\n\t" \
  "v_lshl_or_b32 v115, v103, 16, v102\n\t" \
  "v_lshl_or_b32 v116, v105, 16, v104\n\t" \
  "v_lshl_or_b32 v117, v107, 16, v106\n\t" \
  "v_lshl_or_b32 v118, v109, 16, v108\n\t" \
  "v_lshl_or_b32 v119, v111, 16, v110\n\t" \
  "ds_write_b128 v70, v[112:115] offset:34816\n\t" \
  "ds_write_b128 v70, v[116:119] offset:35840\n\t" \
  "v_add_u32 v78, 0, v65\n\t" \
  "s_waitcnt vmcnt(12) lgkmcnt(0)\n\t" \
  "s_barrier\n\t" \
  "ds_read_b64_tr_b16 a[208:209], v78 offset:0\n\t" \
  "ds_read_b64_tr_b16 a[210:211], v78 offset:1024\n\t" \
  "ds_read_b64_tr_b16 a[212:213], v78 offset:8192\n\t" \
  "ds_read_b64_tr_b16 a[214:215], v78 offset:9216\n\t" \
  "ds_read_b64_tr_b16 a[216:217], v78 offset:8\n\t" \
  "ds_read_b64_tr_b16 a[218:219], v78 offset:1032\n\t" \
  "ds_read_b64_tr_b16 a[220:221], v78 offset:8200\n\t" \
  "ds_read_b64_tr_b16 a[222:223], v78 offset:9224\n\t" \
  "s_add_u32 m0, s54, 126976\n\t" \
  "s_mov_b32 s60, 3145728\n\t" \
  "buffer_load_dwordx4 v71, %[Wv], s60 offen lds\n\t" \
  "s_add_u32 m0, s54, 128000\n\t" \
  "s_mov_b32 s60, 3178496\n\t" \
  "buffer_load_dwordx4 v71, %[Wv], s60 offen lds\n\t" \
  "s_add_u32 m0, s54, 129024\n\t" \
  "s_mov_b32 s60, 3211264\n\t" \
  "buffer_load_dwordx4 v71, %[Wv], s60 offen lds\n\t" \
  "s_add_u32 m0, s54, 130048\n\t" \
  "s_mov_b32 s60, 3244032\n\t" \
  "buffer_load_dwordx4 v71, %[Wv], s60 offen lds\n\t" \
  "v_add_u32 v78, 16384, v66\n\t" \
  "s_waitcnt vmcnt(12) lgkmcnt(0)\n\t" \
  "s_barrier\n\t" \
  "ds_read_b64_tr_b16 a[224:225], v78 offset:0\n\t" \
  "ds_read_b64_tr_b16 a[226:227], v78 offset:1024\n\t" \
  "ds_read_b64_tr_b16 a[228:229], v78 offset:8192\n\t" \
  "ds_read_b64_tr_b16 a[230:231], v78 offset:9216\n\t" \
  "ds_read_b64_tr_b16 a[232:233], v78 offset:32\n\t" \
  "ds_read_b64_tr_b16 a[234:235], v78 offset:1056\n\t" \
  "ds_read_b64_tr_b16 a[236:237], v78 offset:8224\n\t" \
  "ds_read_b64_tr_b16 a[238:239], v78 offset:9248\n\t" \
  "s_add_u32 m0, s54, 0\n\t" \
  "s_mov_b32 s60, 3670016\n\t" \
  "buffer_load_dwordx4 v71, %[Wq], s60 offen lds\n\t" \
  "s_add_u32 m0, s54, 1024\n\t" \
  "s_mov_b32 s60, 3702784\n\t" \
  "buffer_load_dwordx4 v71, %[Wq], s60 offen lds\n\t" \
  "s_add_u32 m0, s54, 2048\n\t" \
  "s_mov_b32 s60, 3735552\n\t" \
  "buffer_load_dwordx4 v71, %[Wq], s60 offen lds\n\t" \
  "s_add_u32 m0, s54, 3072\n\t" \
  "s_mov_b32 s60, 3768320\n\t" \
  "buffer_load_dwordx4 v71, %[Wq], s60 offen lds\n\t" \
  "v_add_u32 v78, 53248, v65\n\t" \
  "s_waitcnt vmcnt(12) lgkmcnt(0)\n\t" \
  "s_barrier\n\t" \
  "ds_read_b64_tr_b16 a[240:241], v78 offset:0\n\t" \
  "ds_read_b64_tr_b16 a[242:243], v78 offset:1024\n\t" \
  "ds_read_b64_tr_b16 a[244:245], v78 offset:8192\n\t" \
  "ds_read_b64_tr_b16 a[246:247], v78 offset:9216\n\t" \
  "ds_read_b64_tr_b16 a[248:249], v78 offset:8\n\t" \
  "ds_read_b64_tr_b16 a[250:251], v78 offset:1032\n\t" \
  "ds_read_b64_tr_b16 a[252:253], v78 offset:8200\n\t" \
  "ds_read_b64_tr_b16 a[254:255], v78 offset:9224\n\t" \
  "s_add_u32 m0, s54, 16384\n\t" \
  "s_mov_b32 s60, 3670016\n\t" \
  "buffer_load_dwordx4 v71, %[Wk], s60 offen lds\n\t" \
  "s_add_u32 m0, s54, 17408\n\t" \
  "s_mov_b32 s60, 3702784\n\t" \
  "buffer_load_dwordx4 v71, %[Wk], s60 offen lds\n\t" \
  "s_add_u32 m0, s54, 18432\n\t" \
  "s_mov_b32 s60, 3735552\n\t" \
  "buffer_load_dwordx4 v71, %[Wk], s60 offen lds\n\t" \
  "s_add_u32 m0, s54, 19456\n\t" \
  "s_mov_b32 s60, 3768320\n\t" \
  "buffer_load_dwordx4 v71, %[Wk], s60 offen lds\n\t" \
  "v_add_u32 v78, 90112, v65\n\t" \
  "s_waitcnt vmcnt(12) lgkmcnt(0)\n\t" \
  "s_barrier\n\t" \
  "ds_read_b64_tr_b16 v[80:81], v78 offset:0\n\t" \
  "ds_read_b64_tr_b16 v[82:83], v78 offset:1024\n\t" \
  "ds_read_b64_tr_b16 v[84:85], v78 offset:8192\n\t" \
  "ds_read_b64_tr_b16 v[86:87], v78 offset:9216\n\t" \
  "ds_read_b64_tr_b16 v[88:89], v78 offset:8\n\t" \
  "ds_read_b64_tr_b16 v[90:91], v78 offset:1032\n\t" \
  "ds_read_b64_tr_b16 v[92:93], v78 offset:8200\n\t" \
  "ds_read_b64_tr_b16 v[94:95], v78 offset:9224\n\t" \
  "s_add_u32 m0, s54, 53248\n\t" \
  "s_mov_b32 s60, 3670016\n\t" \
  "buffer_load_dwordx4 v71, %[Wv], s60 offen lds\n\t" \
  "s_add_u32 m0, s54, 54272\n\t" \
  "s_mov_b32 s60, 3702784\n\t" \
  "buffer_load_dwordx4 v71, %[Wv], s60 offen lds\n\t" \
  "s_add_u32 m0, s54, 55296\n\t" \
  "s_mov_b32 s60, 3735552\n\t" \
  "buffer_load_dwordx4 v71, %[Wv], s60 offen lds\n\t" \
  "s_add_u32 m0, s54, 56320\n\t" \
  "s_mov_b32 s60, 3768320\n\t" \
  "buffer_load_dwordx4 v71, %[Wv], s60 offen lds\n\t" \
  "v_add_u32 v78, 126976, v66\n\t" \
  "s_waitcnt vmcnt(12) lgkmcnt(0)\n\t" \
  "s_barrier\n\t" \
  "ds_read_b64_tr_b16 v[96:97], v78 offset:0\n\t" \
  "ds_read_b64_tr_b16 v[98:99], v78 offset:1024\n\t" \
  "ds_read_b64_tr_b16 v[100:101], v78 offset:8192\n\t" \
  "ds_read_b64_tr_b16 v[102:103], v78 offset:9216\n\t" \
  "ds_read_b64_tr_b16 v[104:105], v78 offset:32\n\t" \
  "ds_read_b64_tr_b16 v[106:107], v78 offset:1056\n\t" \
  "ds_read_b64_tr_b16 v[108:109], v78 offset:8224\n\t" \
  "ds_read_b64_tr_b16 v[110:111], v78 offset:9248\n\t" \
  "s_add_u32 m0, s54, 90112\n\t" \
  "s_mov_b32 s60, 4194304\n\t" \
  "buffer_load_dwordx4 v71, %[Wq], s60 offen lds\n\t" \
  "s_add_u32 m0, s54, 91136\n\t" \
  "s_mov_b32 s60, 4227072\n\t" \
  "buffer_load_dwordx4 v71, %[Wq], s60 offen lds\n\t" \
  "s_add_u32 m0, s54, 92160\n\t" \
  "s_mov_b32 s60, 4259840\n\t" \
  "buffer_load_dwordx4 v71, %[Wq], s60 offen lds\n\t" \
  "s_add_u32 m0, s54, 93184\n\t" \
  "s_mov_b32 s60, 4292608\n\t" \
  "buffer_load_dwordx4 v71, %[Wq], s60 offen lds\n\t" \
  "v_add_u32 v78, 0, v65\n\t" \
  "s_waitcnt vmcnt(12) lgkmcnt(0)\n\t" \
  "s_barrier\n\t" \
  "ds_read_b64_tr_b16 v[112:113], v78 offset:0\n\t" \
  "ds_read_b64_tr_b16 v[114:115], v78 offset:1024\n\t" \
  "ds_read_b64_tr_b16 v[116:117], v78 offset:8192\n\t" \
  "ds_read_b64_tr_b16 v[118:119], v78 offset:9216\n\t" \
  "ds_read_b64_tr_b16 v[120:121], v78 offset:8\n\t" \
  "ds_read_b64_tr_b16 v[122:123], v78 offset:1032\n\t" \
  "ds_read_b64_tr_b16 v[124:125], v78 offset:8200\n\t" \
  "ds_read_b64_tr_b16 v[126:127], v78 offset:9224\n\t" \
  "s_add_u32 m0, s54, 126976\n\t" \
  "s_mov_b32 s60, 4194304\n\t" \
  "buffer_load_dwordx4 v71, %[Wk], s60 offen lds\n\t" \
  "s_add_u32 m0, s54, 128000\n\t" \
  "s_mov_b32 s60, 4227072\n\t" \
  "buffer_load_dwordx4 v71, %[Wk], s60 offen lds\n\t" \
  "s_add_u32 m0, s54, 129024\n\t" \
  "s_mov_b32 s60, 4259840\n\t" \
  "buffer_load_dwordx4 v71, %[Wk], s60 offen lds\n\t" \
  "s_add_u32 m0, s54, 130048\n\t" \
  "s_mov_b32 s60, 4292608\n\t" \
  "buffer_load_dwordx4 v71, %[Wk], s60 offen lds\n\t" \
  "v_add_u32 v78, 16384, v65\n\t" \
  "s_waitcnt vmcnt(12) lgkmcnt(0)\n\t" \
  "s_barrier\n\t" \
  "ds_read_b64_tr_b16 v[128:129], v78 offset:0\n\t" \
  "ds_read_b64_tr_b16 v[130:131], v78 offset:1024\n\t" \
  "ds_read_b64_tr_b16 v[132:133], v78 offset:8192\n\t" \
  "ds_read_b64_tr_b16 v[134:135], v78 offset:9216\n\t" \
  "ds_read_b64_tr_b16 v[136:137], v78 offset:8\n\t" \
  "ds_read_b64_tr_b16 v[138:139], v78 offset:1032\n\t" \
  "ds_read_b64_tr_b16 v[140:141], v78 offset:8200\n\t" \
  "ds_read_b64_tr_b16 v[142:143], v78 offset:9224\n\t" \
  "s_add_u32 m0, s54, 0\n\t" \
  "s_mov_b32 s60, 4194304\n\t" \
  "buffer_load_dwordx4 v71, %[Wv], s60 offen lds\n\t" \
  "s_add_u32 m0, s54, 1024\n\t" \
  "s_mov_b32 s60, 4227072\n\t" \
  "buffer_load_dwordx4 v71, %[Wv], s60 offen lds\n\t" \
  "s_add_u32 m0, s54, 2048\n\t" \
  "s_mov_b32 s60, 4259840\n\t" \
  "buffer_load_dwordx4 v71, %[Wv], s60 offen lds\n\t" \
  "s_add_u32 m0, s54, 3072\n\t" \
  "s_mov_b32 s60, 4292608\n\t" \
  "buffer_load_dwordx4 v71, %[Wv], s60 offen lds\n\t" \
  "v_add_u32 v78, 53248, v66\n\t" \
  "s_waitcnt vmcnt(12) lgkmcnt(0)\n\t" \
  "s_barrier\n\t" \
  "ds_read_b64_tr_b16 v[144:145], v78 offset:0\n\t" \
  "ds_read_b64_tr_b16 v[146:147], v78 offset:1024\n\t" \
  "ds_read_b64_tr_b16 v[148:149], v78 offset:8192\n\t" \
  "ds_read_b64_tr_b16 v[150:151], v78 offset:9216\n\t" \
  "ds_read_b64_tr_b16 v[152:153], v78 offset:32\n\t" \
  "ds_read_b64_tr_b16 v[154:155], v78 offset:1056\n\t" \
  "ds_read_b64_tr_b16 v[156:157], v78 offset:8224\n\t" \
  "ds_read_b64_tr_b16 v[158:159], v78 offset:9248\n\t" \
  "v_add_u32 v78, 90112, v65\n\t" \
  "s_waitcnt vmcnt(8) lgkmcnt(0)\n\t" \
  "s_barrier\n\t" \
  "ds_read_b64_tr_b16 v[160:161], v78 offset:0\n\t" \
  "ds_read_b64_tr_b16 v[162:163], v78 offset:1024\n\t" \
  "ds_read_b64_tr_b16 v[164:165], v78 offset:8192\n\t" \
  "ds_read_b64_tr_b16 v[166:167], v78 offset:9216\n\t" \
  "ds_read_b64_tr_b16 v[168:169], v78 offset:8\n\t" \
  "ds_read_b64_tr_b16 v[170:171], v78 offset:1032\n\t" \
  "ds_read_b64_tr_b16 v[172:173], v78 offset:8200\n\t" \
  "ds_read_b64_tr_b16 v[174:175], v78 offset:9224\n\t" \
  "v_add_u32 v78, 126976, v65\n\t" \
  "s_waitcnt vmcnt(4) lgkmcnt(0)\n\t" \
  "s_barrier\n\t" \
  "ds_read_b64_tr_b16 v[40:41], v78 offset:0\n\t" \
  "ds_read_b64_tr_b16 v[42:43], v78 offset:1024\n\t" \
  "ds_read_b64_tr_b16 v[44:45], v78 offset:8192\n\t" \
  "ds_read_b64_tr_b16 v[46:47], v78 offset:9216\n\t" \
  "ds_read_b64_tr_b16 v[176:177], v78 offset:8\n\t" \
  "ds_read_b64_tr_b16 v[178:179], v78 offset:1032\n\t" \
  "ds_read_b64_tr_b16 v[180:181], v78 offset:8200\n\t" \
  "ds_read_b64_tr_b16 v[182:183], v78 offset:9224\n\t" \
  "v_add_u32 v78, 0, v66\n\t" \
  "s_waitcnt vmcnt(0) lgkmcnt(0)\n\t" \
  "s_barrier\n\t" \
  "ds_read_b64_tr_b16 v[184:185], v78 offset:0\n\t" \
  "ds_read_b64_tr_b16 v[186:187], v78 offset:1024\n\t" \
  "ds_read_b64_tr_b16 v[188:189], v78 offset:8192\n\t" \
  "ds_read_b64_tr_b16 v[190:191], v78 offset:9216\n\t" \
  "ds_read_b64_tr_b16 v[56:57], v78 offset:32\n\t" \
  "ds_read_b64_tr_b16 v[58:59], v78 offset:1056\n\t" \
  "ds_read_b64_tr_b16 v[60:61], v78 offset:8224\n\t" \
  "ds_read_b64_tr_b16 v[62:63], v78 offset:9248\n\t" \
  CH_STV \
  "s_waitcnt lgkmcnt(0)\n\t" \
  "s_barrier\n\t" \
  "ds_write_b128 v70, v[0:3] offset:0\n\t" \
  "ds_write_b128 v70, v[4:7] offset:1024\n\t" \
  "ds_write_b128 v70, v[8:11] offset:2048\n\t" \
  "ds_write_b128 v70, v[12:15] offset:3072\n\t" \
  "ds_write_b128 v70, v[16:19] offset:4096\n\t" \
  "ds_write_b128 v70, v[20:23] offset:5120\n\t" \
  "ds_write_b128 v70, v[24:27] offset:6144\n\t" \
  "ds_write_b128 v70, v[28:31] offset:7168\n\t" \
  "ds_write_b128 v70, v[32:35] offset:8192\n\t" \
  "ds_write_b128 v70, v[36:39] offset:9216\n\t" \
  "ds_write_b128 v70, v[40:43] offset:10240\n\t" \
  "ds_write_b128 v70, v[44:47] offset:11264\n\t" \
  "ds_write_b128 v70, v[48:51] offset:12288\n\t" \
  "ds_write_b128 v70, v[52:55] offset:13312\n\t" \
  "ds_write_b128 v70, v[56:59] offset:14336\n\t" \
  "ds_write_b128 v70, v[60:63] offset:15360\n\t" \
  "s_waitcnt lgkmcnt(0)\n\t" \
  CH_STW \
  CH_COW \
  "v_and_b32 v72, 1, v72\n\t" \
  "v_and_b32 v74, 1, v74\n\t" \
  "v_cmp_ne_u32 s[90:91], s32, v72\n\t" \
  "v_cmp_ne_u32 vcc, s32, v74\n\t" \
  "s_nop 1\n\t" \
  "s_mov_b32 s96, vcc_lo\n\t" \
  "v_and_b32 v73, 32, v69\n\t" \
  "v_lshl_add_u32 v73, v73, 5, v69\n\t" \
  "s_mov_b32 s35, s30\n\t" \
  "s_cmp_lt_u32 s32, " CH_DRYL "\n\t" \
  "s_cbranch_scc1 LINI_%=\n\t" \
  "s_mov_b32 s30, 0\n\t" \
  "s_mov_b32 s34, 0\n\t" \
  "s_cmp_eq_u32 s40, 0\n\t" \
  "s_cbranch_scc1 LW0_%=\n\t" \
  "s_barrier\n\t" \
  "s_cmp_eq_u32 s40, 1\n\t" \
  "s_cbranch_scc1 LG1_%=\n\t" \
  "s_cmp_eq_u32 s40, 2\n\t" \
  "s_cbranch_scc1 LG2_%=\n\t" \
  "s_branch LG3_%=\n\t" \
  "LINI_%=:\n\t" \
  "s_waitcnt vmcnt(0) lgkmcnt(0)\n\t" \
  "s_mov_b32 s30, s35\n\t" \
  "s_movk_i32 s34, 1000\n\t" \
  "s_mov_b32 s48, -1\n\t" \
  "s_add_u32 s97, s41, s53\n\t" \
  "s_add_u32 s98, s37, s53\n\t" \
  "s_add_u32 s82, s48, 0\n\t" \
  CH_SEGC \
  "s_lshr_b64 s[60:61], s[90:91], s82\n\t" \
  "s_and_b32 s60, s60, 1\n\t" \
  "s_lshl_b32 s60, s60, 13\n\t" \
  "s_add_u32 s82, s82, " CH_SG0 "\n\t" \
  "s_lshl_b32 s82, s82, 21\n\t" \
  "s_or_b32 s82, s82, s60\n\t" \
  "s_add_u32 s50, s82, s38\n\t" \
  "s_add_u32 s69, s82, s39\n\t" \
  "s_add_u32 s82, s48, 1\n\t" \
  CH_SEGC \
  "s_lshr_b64 s[60:61], s[90:91], s82\n\t" \
  "s_and_b32 s60, s60, 1\n\t" \
  "s_lshl_b32 s60, s60, 13\n\t" \
  "s_add_u32 s82, s82, " CH_SG0 "\n\t" \
  "s_lshl_b32 s82, s82, 21\n\t" \
  "s_or_b32 s82, s82, s60\n\t" \
  "s_add_u32 s52, s82, s43\n\t" \
  "s_add_u32 s82, s48, " CH_ARMD "\n\t" \
  CH_SEGC \
  "s_lshr_b64 s[60:61], s[90:91], s82\n\t" \
  "s_and_b32 s60, s60, 1\n\t" \
  "s_lshl_b32 s60, s60, 13\n\t" \
  "s_add_u32 s82, s82, " CH_SG0 "\n\t" \
  "s_lshl_b32 s82, s82, 21\n\t" \
  "s_or_b32 s82, s82, s60\n\t" \
  "s_add_u32 s85, s82, s45\n\t" \
  "s_cmp_lt_i32 s48, " CH_ARMS "\n\t" \
  "s_cselect_b32 s85, s27, s85\n\t" \
  "v_and_b32 v37, 0x30, v69\n\t" \
  "v_add_u32 v37, s52, v37\n\t" \
  "s_mov_b32 s48, 0\n\t" \
  "s_mov_b32 s86, 1\n\t" \
  "s_and_b32 s49, s96, 1\n\t" \
  "s_lshl_b32 s49, s49, 13\n\t" \
  "s_add_u32 s53, s49, " CH_SLOT "\n\t" \
  "s_movk_i32 s60, 124\n\t" \
  "s_cmp_eq_u32 s40, 3\n\t" \
  "s_cselect_b32 s81, 76, s60\n\t" \
  "s_add_u32 s82, s48, 0\n\t" \
  CH_SEGC \
  "s_lshr_b64 s[60:61], s[90:91], s82\n\t" \
  "s_and_b32 s60, s60, 1\n\t" \
  "s_lshl_b32 s60, s60, 13\n\t" \
  "s_add_u32 s82, s82, " CH_SG0 "\n\t" \
  "s_lshl_b32 s82, s82, 21\n\t" \
  "s_or_b32 s82, s82, s60\n\t" \
  "s_add_u32 s50, s82, s38\n\t" \
  "s_add_u32 s69, s82, s39\n\t" \
  "s_add_u32 s51, s82, s43\n\t" \
  "s_add_u32 s97, s41, s53\n\t" \
  "s_add_u32 s98, s37, s53\n\t" \
  "s_add_u32 s82, s48, 0\n\t" \
  CH_SEGC \
  "s_lshr_b64 s[60:61], s[90:91], s82\n\t" \
  "s_and_b32 s60, s60, 1\n\t" \
  "s_lshl_b32 s60, s60, 13\n\t" \
  "s_add_u32 s82, s82, " CH_SG0 "\n\t" \
  "s_lshl_b32 s82, s82, 21\n\t" \
  "s_or_b32 s82, s82, s60\n\t" \
  "s_add_u32 s50, s82, s38\n\t" \
  "s_add_u32 s69, s82, s39\n\t" \
  "s_add_u32 s82, s48, 1\n\t" \
  CH_SEGC \
  "s_lshr_b64 s[60:61], s[90:91], s82\n\t" \
  "s_and_b32 s60, s60, 1\n\t" \
  "s_lshl_b32 s60, s60, 13\n\t" \
  "s_add_u32 s82, s82, " CH_SG0 "\n\t" \
  "s_lshl_b32 s82, s82, 21\n\t" \
  "s_or_b32 s82, s82, s60\n\t" \
  "s_add_u32 s52, s82, s43\n\t" \
  "s_add_u32 s82, s48, " CH_ARMD "\n\t" \
  CH_SEGC \
  "s_lshr_b64 s[60:61], s[90:91], s82\n\t" \
  "s_and_b32 s60, s60, 1\n\t" \
  "s_lshl_b32 s60, s60, 13\n\t" \
  "s_add_u32 s82, s82, " CH_SG0 "\n\t" \
  "s_lshl_b32 s82, s82, 21\n\t" \
  "s_or_b32 s82, s82, s60\n\t" \
  "s_add_u32 s85, s82, s45\n\t" \
  "s_cmp_lt_i32 s48, " CH_ARMS "\n\t" \
  "s_cselect_b32 s85, s27, s85\n\t" \
  "s_memrealtime s[62:63]\n\t" \
  "s_waitcnt lgkmcnt(0)\n\t" \
  "s_mov_b32 s65, s62\n\t" \
  "s_sub_u32 s47, s62, 1000\n\t" \
  "s_mov_b64 vcc, -1\n\t" \
  "s_cmp_eq_u32 s40, 0\n\t" \
  "s_cbranch_scc1 LW0_%=\n\t" \
  "s_barrier\n\t" \
  "s_cmp_eq_u32 s40, 1\n\t" \
  "s_cbranch_scc1 LG1_%=\n\t" \
  "s_cmp_eq_u32 s40, 2\n\t" \
  "s_cbranch_scc1 LG2_%=\n\t" \
  "s_branch LTK_%=\n\t" \
  ".p2align 6\n\t" \
  "LW0_%=:\n\t" \
  "v_lshlrev_b32 v78, 2, v69\n\t" \
  "v_and_b32 v78, 0x300, v78\n\t" \
  "v_lshrrev_b32 v79, 8, v69\n\t" \
  "v_lshl_add_u32 v78, v79, 6, v78\n\t" \
  "v_and_b32 v79, 48, v69\n\t" \
  "v_add_u32 v78, v78, v79\n\t" \
  "s_add_u32 s98, s49, s42\n\t" \
  "s_add_u32 s97, s98, 32768\n\t" \
  "s_add_u32 s99, s98, 65536\n\t" \
  "s_barrier\n\t" \
  "s_memrealtime s[62:63]\n\t" \
  "s_waitcnt lgkmcnt(0)\n\t" \
  "s_add_u32 s62, s62, " CH_DLL2 "\n\t" \
  "s_sub_i32 s63, s62, s65\n\t" \
  "s_cmp_gt_i32 s63, " CH_DLT "\n\t" \
  "s_cselect_b32 s60, " CH_DLY ", " CH_DLX "\n\t" \
  "s_add_u32 s62, s62, s60\n\t" \
  "s_cmp_gt_i32 s63, 0\n\t" \
  "s_cselect_b32 s65, s62, s65\n\t" \
  "s_mov_b32 m0, 0\n\t" \
  "s_movk_i32 s60, 8192\n\t" \
  "LWT_%=:\n\t" \
  "s_memrealtime s[62:63]\n\t" \
  "s_sub_u32 s60, s60, 1\n\t" \
  "s_cselect_b32 s61, 0, -1\n\t" \
  "s_waitcnt lgkmcnt(0)\n\t" \
  "s_sub_i32 s63, s62, s65\n\t" \
  "s_and_b32 s63, s63, s61\n\t" \
  "s_cmp_lt_i32 s63, " CH_FIN "\n\t" \
  "s_cbranch_scc1 LWT_%=\n\t" \
  "s_cmp_lt_i32 s63, 0\n\t" \
  "s_cbranch_scc1 LWT_%=f\n\t" \
  "LWT_%=g:\n\t" \
  "LDMA_%=:\n\t" \
  CH_DPC \
  "buffer_load_dwordx4 v78, s[28:31], s98 offen offset:0 sc1 lds\n\t" \
  "buffer_load_dwordx4 v78, s[28:31], s98 offen offset:1024 sc1 lds\n\t" \
  "buffer_load_dwordx4 v78, s[28:31], s98 offen offset:2048 sc1 lds\n\t" \
  "buffer_load_dwordx4 v78, s[28:31], s98 offen offset:3072 sc1 lds\n\t" \
  "s_movk_i32 m0, 4096\n\t" \
  "s_mov_b32 s66, s62\n\t" \
  "buffer_load_dwordx4 v78, s[28:31], s97 offen offset:0 sc1 lds\n\t" \
  "buffer_load_dwordx4 v78, s[28:31], s97 offen offset:1024 sc1 lds\n\t" \
  "buffer_load_dwordx4 v78, s[28:31], s97 offen offset:2048 sc1 lds\n\t" \
  "buffer_load_dwordx4 v78, s[28:31], s97 offen offset:3072 sc1 lds\n\t" \
  "s_movk_i32 m0, 8192\n\t" \
  "s_nop 0\n\t" \
  "buffer_load_dwordx4 v69, s[28:31], s99 offen sc1 lds\n\t" \
  CH_ST1 \
  ".p2align 6\n\t" \
  "LG0_%=:\n\t" \
  "s_barrier\n\t" \
  "buffer_load_dwordx4 v[52:55], v69, s[28:31], s50 offen sc1\n\t" \
  "buffer_load_dwordx4 v[56:59], v69, s[28:31], s50 offen offset:1024 sc1\n\t" \
  "s_add_u32 s64, s50, 16384\n\t" \
  "s_movk_i32 m0, 11264\n\t" \
  "v_lshlrev_b32 v75, 1, v69\n\t" \
  "buffer_load_dword v75, s[28:31], s64 offen sc1 lds\n\t" \
  "s_memrealtime s[62:63]\n\t" \
  "s_waitcnt lgkmcnt(0)\n\t" \
  "s_sub_u32 s62, s62, " CH_TKL "\n\t" \
  "s_sub_i32 s63, s62, s65\n\t" \
  "s_cmp_gt_i32 s63, 0\n\t" \
  "s_cselect_b32 s62, s62, s65\n\t" \
  "s_sub_i32 s63, s62, s47\n\t" \
  "s_cmp_gt_i32 s63, " CH_LATE "\n\t" \
  "s_cselect_b32 s71, " CH_SCHW ", s46\n\t" \
  "s_add_u32 s61, s47, " CH_LATE "\n\t" \
  "s_cmp_gt_i32 s63, " CH_LATE "\n\t" \
  "s_cselect_b32 s62, s62, s61\n\t" \
  "s_add_u32 s71, s71, s53\n\t" \
  "s_add_u32 s60, s62, s81\n\t" \
  "s_add_u32 s60, s60, " CH_DL1L "\n\t" \
  "s_lshl_b32 s60, s60, 8\n\t" \
  "s_or_b32 s60, s60, " CH_B0 "\n\t" \
  "s_cmp_lt_u32 s48, " CH_ES "\n\t" \
  "s_cselect_b32 s61, " CH_EX ", 0\n\t" \
  "s_add_u32 s60, s60, s61\n\t" \
  "s_not_b32 s60, s60\n\t" \
  "v_mov_b32 v76, s60\n\t" \
  "s_lshr_b32 s60, s46, 31\n\t" \
  "s_or_b32 s60, s60, s68\n\t" \
  "s_cselect_b32 s25, 0, 1\n\t" \
  "s_mov_b64 exec, s[24:25]\n\t" \
  "buffer_atomic_umin v76, v69, s[28:31], s71 offen sc1\n\t" \
  "s_mov_b64 exec, -1\n\t" \
  "ds_read_b128 v[44:47], v70 offset:0\n\t" \
  "ds_read_b128 v[48:51], v70 offset:1024\n\t" \
  "ds_read_b128 v[60:63], v70 offset:2048\n\t" \
  "ds_read_b128 v[64:67], v70 offset:3072\n\t" \
  "v_mov_b64 v[36:37], 0\n\t" \
  "v_mov_b64 v[38:39], 0\n\t" \
  "v_mov_b64 v[40:41], 0\n\t" \
  "v_mov_b64 v[42:43], 0\n\t" \
  "v_lshrrev_b32 v71, 1, v69\n\t" \
  "v_lshrrev_b32 v72, 3, v69\n\t" \
  "v_and_b32 v72, 0x78, v72\n\t" \
  "v_and_b32 v74, 0x30, v69\n\t" \
  "v_lshl_add_u32 v72, v74, 3, v72\n\t" \
  "s_add_u32 s54, s53, s36\n\t" \
  "s_add_u32 s55, s54, 524288\n\t" \
  "s_add_u32 s54, s54, 0\n\t" \
  "s_add_u32 s75, s54, 262144\n\t" \
  "s_add_u32 s80, s53, s26\n\t" \
  "s_or_b32 s87, s53, 0x1fff80\n\t" \
  "s_add_u32 s87, s87, 2097152\n\t" \
  "s_waitcnt vmcnt(9)\n\t" \
  "s_barrier\n\t" \
  CH_DP0 \
  "ds_read_b128 v[0:3], v69 offset:0\n\t" \
  "ds_read_b128 v[4:7], v69 offset:1024\n\t" \
  "ds_read_b128 v[8:11], v69 offset:2048\n\t" \
  "ds_read_b128 v[12:15], v69 offset:3072\n\t" \
  "s_waitcnt lgkmcnt(3)\n\t" \
  "v_smfmac_f32_16x16x64_bf16 v[36:39], v[0:3], v[44:51], v68\n\t" \
  "ds_read_b128 v[44:47], v70 offset:4096\n\t" \
  "v_smfmac_f32_16x16x64_bf16 v[40:43], v[0:3], v[60:67], v68\n\t" \
  "ds_read_b128 v[48:51], v70 offset:5120\n\t" \
  "s_waitcnt lgkmcnt(4)\n\t" \
  "v_smfmac_f32_16x16x64_bf16 v[36:39], v[4:7], a[32:39], v68\n\t" \
  "s_waitcnt vmcnt(4)\n\t" \
  "s_barrier\n\t" \
  "ds_read_b128 v[16:19], v69 offset:4096\n\t" \
  CH_DP1 \
  CH_TG2 \
  "v_smfmac_f32_16x16x64_bf16 v[40:43], v[4:7], a[40:47], v68\n\t" \
  "ds_read_b128 v[20:23], v69 offset:5120\n\t" \
  "s_waitcnt lgkmcnt(5)\n\t" \
  "v_smfmac_f32_16x16x64_bf16 v[36:39], v[8:11], a[80:87], v68\n\t" \
  "ds_read_b128 v[24:27], v69 offset:6144\n\t" \
  "v_smfmac_f32_16x16x64_bf16 v[40:43], v[8:11], a[88:95], v68\n\t" \
  "ds_read_b128 v[28:31], v69 offset:7168\n\t" \
  "s_waitcnt lgkmcnt(6)\n\t" \
  "v_smfmac_f32_16x16x64_bf16 v[36:39], v[12:15], a[120:127], v68\n\t" \
  "ds_read_b128 v[32:35], v69 offset:8192\n\t" \
  "s_waitcnt lgkmcnt(5)\n\t" \
  "v_smfmac_f32_16x16x64_bf16 v[40:43], v[12:15], v[44:51], v68\n\t" \
  "ds_read_b128 v[60:63], v70 offset:6144\n\t" \
  "s_waitcnt lgkmcnt(5)\n\t" \
  "v_smfmac_f32_16x16x64_bf16 v[36:39], v[16:19], a[160:167], v68\n\t" \
  "ds_read_b128 v[64:67], v70 offset:7168\n\t" \
  "v_smfmac_f32_16x16x64_bf16 v[40:43], v[16:19], a[168:175], v68\n\t" \
  "s_waitcnt lgkmcnt(5)\n\t" \
  "v_mov_b64 v[44:45], 0\n\t" \
  "v_smfmac_f32_16x16x64_bf16 v[36:39], v[20:23], a[200:207], v68\n\t" \
  "s_waitcnt lgkmcnt(4)\n\t" \
  "v_mov_b64 v[46:47], 0\n\t" \
  "v_smfmac_f32_16x16x64_bf16 v[36:39], v[24:27], a[240:247], v68\n\t" \
  "s_waitcnt lgkmcnt(2)\n\t" \
  "v_mov_b64 v[48:49], 0\n\t" \
  "v_smfmac_f32_16x16x64_bf16 v[36:39], v[28:31], v[112:119], v68\n\t" \
  "v_mov_b64 v[50:51], 0\n\t" \
  "v_smfmac_f32_16x16x64_bf16 v[36:39], v[32:35], v[160:167], v68\n\t" \
  "s_waitcnt lgkmcnt(0)\n\t" \
  "v_smfmac_f32_16x16x64_bf16 v[40:43], v[20:23], v[60:67], v68\n\t" \
  "ds_read_b128 v[60:63], v70 offset:8192\n\t" \
  "v_smfmac_f32_16x16x64_bf16 v[40:43], v[24:27], a[248:255], v68\n\t" \
  "ds_read_b128 v[64:67], v70 offset:9216\n\t" \
  "v_add_f32 v74, v36, v38\n\t" \
  "v_smfmac_f32_16x16x64_bf16 v[40:43], v[28:31], v[120:127], v68\n\t" \
  "v_add_f32 v37, v37, v39\n\t" \
  "v_smfmac_f32_16x16x64_bf16 v[40:43], v[32:35], v[168:175], v68\n\t" \
  "v_cmp_u_f32 vcc, v74, v37\n\t" \
  CH_DP2 \
  "v_smfmac_f32_16x16x64_bf16 v[44:47], v[0:3], a[0:7], v68\n\t" \
  "s_cbranch_vccnz LFAIL0_%=\n\t" \
  "LGO0_%=:\n\t" \
  "v_cvt_pk_bf16_f32 v74, v74, v37\n\t" \
  "v_smfmac_f32_16x16x64_bf16 v[44:47], v[4:7], a[48:55], v68\n\t" \
  "s_waitcnt lgkmcnt(0)\n\t" \
  "v_add_f32 v75, v40, v42\n\t" \
  "v_smfmac_f32_16x16x64_bf16 v[44:47], v[8:11], v[60:67], v68\n\t" \
  "ds_read_b128 v[60:63], v70 offset:10240\n\t" \
  "v_add_f32 v41, v41, v43\n\t" \
  "v_cvt_pk_bf16_f32 v75, v75, v41\n\t" \
  "v_smfmac_f32_16x16x64_bf16 v[44:47], v[12:15], a[128:135], v68\n\t" \
  "ds_write2st64_b32 v71, v74, v75 offset0:36 offset1:38\n\t" \
  "v_smfmac_f32_16x16x64_bf16 v[44:47], v[16:19], a[176:183], v68\n\t" \
  "ds_read_b128 v[64:67], v70 offset:11264\n\t" \
  "v_smfmac_f32_16x16x64_bf16 v[44:47], v[20:23], a[208:215], v68\n\t" \
  "v_smfmac_f32_16x16x64_bf16 v[44:47], v[24:27], v[80:87], v68\n\t" \
  "v_smfmac_f32_16x16x64_bf16 v[44:47], v[28:31], v[128:135], v68\n\t" \
  "s_waitcnt lgkmcnt(0)\n\t" \
  "v_smfmac_f32_16x16x64_bf16 v[44:47], v[32:35], v[60:67], v68\n\t" \
  "buffer_load_dwordx4 v[60:63], v69, s[28:31], s64 offen sc1\n\t" \
  "s_barrier\n\t" \
  "ds_read_b64_tr_b16 v[76:77], v72 offset:9216\n\t" \
  CH_DP3 \
  "v_smfmac_f32_16x16x64_bf16 v[48:51], v[0:3], a[8:15], v68\n\t" \
  "ds_read_b64_tr_b16 v[78:79], v72 offset:9728\n\t" \
  "v_smfmac_f32_16x16x64_bf16 v[48:51], v[4:7], a[56:63], v68\n\t" \
  "v_add_f32 v74, v44, v46\n\t" \
  "v_smfmac_f32_16x16x64_bf16 v[48:51], v[8:11], a[96:103], v68\n\t" \
  "v_add_f32 v45, v45, v47\n\t" \
  "v_smfmac_f32_16x16x64_bf16 v[48:51], v[12:15], a[136:143], v68\n\t" \
  "s_waitcnt vmcnt(4)\n\t" \
  "s_waitcnt lgkmcnt(0)\n\t" \
  "v_cvt_pk_bf16_f32 v74, v74, v45\n\t" \
  "v_mfma_f32_16x16x32_bf16 v[36:39], v[52:55], v[76:79], 0\n\t" \
  "buffer_load_dwordx4 v[64:67], v69, s[28:31], s64 offen offset:1024 sc1\n\t" \
  "s_waitcnt vmcnt(4)\n\t" \
  "v_mfma_f32_16x16x32_bf16 v[40:43], v[56:59], v[76:79], 0\n\t" \
  "v_mov_b64 v[52:53], 0\n\t" \
  "v_mov_b64 v[54:55], 0\n\t" \
  "v_smfmac_f32_16x16x64_bf16 v[48:51], v[16:19], a[184:191], v68\n\t" \
  "v_mov_b64 v[56:57], 0\n\t" \
  "v_mov_b64 v[58:59], 0\n\t" \
  "v_smfmac_f32_16x16x64_bf16 v[48:51], v[20:23], a[216:223], v68\n\t" \
  "ds_write_b32 v71, v74 offset:0\n\t" \
  "v_add_f32_dpp v36, v40, v36 quad_perm:[2,3,0,1] row_mask:0xf bank_mask:0xf\n\t" \
  "v_smfmac_f32_16x16x64_bf16 v[48:51], v[24:27], v[88:95], v68\n\t" \
  "v_add_f32_dpp v37, v41, v37 quad_perm:[2,3,0,1] row_mask:0xf bank_mask:0xf\n\t" \
  "v_smfmac_f32_16x16x64_bf16 v[48:51], v[28:31], v[136:143], v68\n\t" \
  "v_add_f32_dpp v38, v42, v38 quad_perm:[2,3,0,1] row_mask:0xf bank_mask:0xf\n\t" \
  "v_smfmac_f32_16x16x64_bf16 v[48:51], v[32:35], v[176:183], v68\n\t" \
  "v_add_f32_dpp v39, v43, v39 quad_perm:[2,3,0,1] row_mask:0xf bank_mask:0xf\n\t" \
  CH_DP4 \
  "v_smfmac_f32_16x16x64_bf16 v[52:55], v[0:3], a[16:23], v68\n\t" \
  "s_mov_b64 exec, s[58:59]\n\t" \
  "ds_write_b128 v69, v[36:39] offset:2048\n\t" \
  "s_mov_b64 exec, -1\n\t" \
  "v_smfmac_f32_16x16x64_bf16 v[52:55], v[4:7], a[64:71], v68\n\t" \
  "buffer_load_dword off, s[28:31], s87 sc1 lds\n\t" \
  "v_add_f32 v75, v48, v50\n\t" \
  "v_add_f32 v49, v49, v51\n\t" \
  "v_cvt_pk_bf16_f32 v75, v75, v49\n\t" \
  "v_smfmac_f32_16x16x64_bf16 v[56:59], v[0:3], a[24:31], v68\n\t" \
  "ds_write_b32 v71, v75 offset:512\n\t" \
  "v_smfmac_f32_16x16x64_bf16 v[56:59], v[4:7], a[72:79], v68\n\t" \
  "ds_read_b64_tr_b16 v[0:1], v72 offset:10240\n\t" \
  "ds_read_b64_tr_b16 v[2:3], v72 offset:10752\n\t" \
  "v_smfmac_f32_16x16x64_bf16 v[52:55], v[8:11], a[104:111], v68\n\t" \
  "ds_read_b128 v[36:39], v70 offset:12288\n\t" \
  "v_smfmac_f32_16x16x64_bf16 v[56:59], v[8:11], a[112:119], v68\n\t" \
  "ds_read_b128 v[40:43], v70 offset:13312\n\t" \
  "v_smfmac_f32_16x16x64_bf16 v[52:55], v[12:15], a[144:151], v68\n\t" \
  "s_waitcnt lgkmcnt(4)\n\t" \
  "s_barrier\n\t" \
  CH_DP5 \
  CH_TG5 \
  "ds_read_b64_tr_b16 v[44:45], v72 offset:0\n\t" \
  "ds_read_b64_tr_b16 v[46:47], v72 offset:512\n\t" \
  "v_smfmac_f32_16x16x64_bf16 v[56:59], v[12:15], a[152:159], v68\n\t" \
  "ds_read_b64_tr_b16 v[48:49], v72 offset:1024\n\t" \
  "ds_read_b64_tr_b16 v[50:51], v72 offset:1536\n\t" \
  "v_smfmac_f32_16x16x64_bf16 v[52:55], v[16:19], a[192:199], v68\n\t" \
  "ds_read_b128 v[4:7], v69 offset:2048\n\t" \
  "s_waitcnt lgkmcnt(5)\n\t" \
  "v_smfmac_f32_16x16x64_bf16 v[56:59], v[16:19], v[36:43], v68\n\t" \
  "ds_read_b128 v[8:11], v69 offset:3072\n\t" \
  "s_waitcnt lgkmcnt(4)\n\t" \
  "v_mfma_f32_16x16x32_bf16 v[12:15], v[44:47], v[76:79], 0\n\t" \
  "ds_read_b128 v[36:39], v70 offset:14336\n\t" \
  "s_waitcnt lgkmcnt(3)\n\t" \
  "v_mfma_f32_16x16x32_bf16 v[12:15], v[48:51], v[0:3], v[12:15]\n\t" \
  "ds_read_b128 v[40:43], v70 offset:15360\n\t" \
  "v_smfmac_f32_16x16x64_bf16 v[52:55], v[20:23], a[224:231], v68\n\t" \
  "s_waitcnt lgkmcnt(2)\n\t" \
  "v_add_f32 v4, v4, v8\n\t" \
  "v_add_f32 v5, v5, v9\n\t" \
  "v_add_f32 v6, v6, v10\n\t" \
  CH_DP6 \
  "v_cndmask_b32 v78, v13, v14, s[56:57]\n\t" \
  "v_cndmask_b32 v76, v12, v15, s[56:57]\n\t" \
  "v_add_f32 v7, v7, v11\n\t" \
  "v_max3_f32 v77, v4, v5, v6\n\t" \
  "v_add_f32_dpp v76, v78, v76 quad_perm:[2,3,0,1] row_mask:0xf bank_mask:0xf\n\t" \
  "v_max3_f32 v77, v77, v7, v76\n\t" \
  "v_smfmac_f32_16x16x64_bf16 v[56:59], v[20:23], a[232:239], v68\n\t" \
  "ds_read_b128 v[44:47], v70 offset:16384\n\t" \
  "v_max_f32_dpp v77, v77, v77 quad_perm:[2,3,0,1] row_mask:0xf bank_mask:0xf\n\t" \
  "v_mov_b32 v78, v77\n\t" \
  "v_smfmac_f32_16x16x64_bf16 v[52:55], v[24:27], v[96:103], v68\n\t" \
  "ds_read_b128 v[48:51], v70 offset:17408\n\t" \
  "v_permlane16_swap_b32 v77, v78\n\t" \
  "v_max_f32 v77, v77, v78\n\t" \
  "v_mov_b32 v78, v77\n\t" \
  "v_smfmac_f32_16x16x64_bf16 v[52:55], v[28:31], v[144:151], v68\n\t" \
  "ds_read_b128 v[16:19], v70 offset:18432\n\t" \
  "v_permlane32_swap_b32 v77, v78\n\t" \
  "v_max_f32 v77, v77, v78\n\t" \
  "v_smfmac_f32_16x16x64_bf16 v[52:55], v[32:35], v[184:191], v68\n\t" \
  "ds_read_b128 v[20:23], v70 offset:19456\n\t" \
  "v_mul_f32 v77, 0xbe0293ee, v77\n\t" \
  CH_DP7 \
  "v_smfmac_f32_16x16x64_bf16 v[56:59], v[24:27], v[104:111], v68\n\t" \
  "v_fmamk_f32 v4, v4, 0x3e0293ee, v77\n\t" \
  "v_smfmac_f32_16x16x64_bf16 v[56:59], v[28:31], v[152:159], v68\n\t" \
  "ds_read_b128 v[24:27], v70 offset:22528\n\t" \
  "s_waitcnt lgkmcnt(4)\n\t" \
  "v_fmamk_f32 v5, v5, 0x3e0293ee, v77\n\t" \
  "v_smfmac_f32_16x16x64_bf16 v[56:59], v[32:35], v[36:43], v68\n\t" \
  "ds_read_b128 v[28:31], v70 offset:23552\n\t" \
  "v_fmamk_f32 v6, v6, 0x3e0293ee, v77\n\t" \
  "v_fmamk_f32 v7, v7, 0x3e0293ee, v77\n\t" \
  "v_fmamk_f32 v76, v76, 0x3e0293ee, v77\n\t" \
  "v_exp_f32 v4, v4\n\t" \
  "v_exp_f32 v5, v5\n\t" \
  "v_exp_f32 v6, v6\n\t" \
  "v_exp_f32 v7, v7\n\t" \
  "v_cvt_pk_bf16_f32 v4, v4, v5\n\t" \
  "v_exp_f32 v76, v76\n\t" \
  "v_cvt_pk_bf16_f32 v5, v6, v7\n\t" \
  "v_mov_b32_dpp v12, v4 quad_perm:[0,1,0,1] row_mask:0xf bank_mask:0xf\n\t" \
  "v_mov_b32_dpp v14, v4 quad_perm:[2,3,2,3] row_mask:0xf bank_mask:0xf\n\t" \
  "v_mov_b32_dpp v13, v5 quad_perm:[0,1,0,1] row_mask:0xf bank_mask:0xf\n\t" \
  "v_mov_b32_dpp v15, v5 quad_perm:[2,3,2,3] row_mask:0xf bank_mask:0xf\n\t" \
  "v_add_f32 v52, v52, v54\n\t" \
  "v_add_f32 v53, v53, v55\n\t" \
  "s_waitcnt vmcnt(2)\n\t" \
  CH_DP8 \
  "v_mfma_f32_16x16x32_bf16 v[0:3], v[60:63], v[12:15], 0\n\t" \
  "ds_read_b128 v[36:39], v70 offset:20480\n\t" \
  "v_add_f32 v56, v56, v58\n\t" \
  "v_add_f32 v57, v57, v59\n\t" \
  "v_cvt_pk_bf16_f32 v52, v52, v53\n\t" \
  "v_cvt_pk_bf16_f32 v53, v56, v57\n\t" \
  "s_waitcnt vmcnt(1)\n\t" \
  "v_mfma_f32_16x16x32_bf16 v[4:7], v[64:67], v[12:15], 0\n\t" \
  "buffer_store_dwordx2 v[52:53], v71, s[28:31], s69 offen offset:2048 sc1\n\t" \
  "ds_read_b128 v[40:43], v70 offset:21504\n\t" \
  "v_mov_b64 v[56:57], s[88:89]\n\t" \
  "v_mov_b64 v[58:59], s[88:89]\n\t" \
  "v_mov_b32_dpp v78, v76 quad_perm:[0,1,0,1] row_mask:0xf bank_mask:0xf\n\t" \
  "v_mov_b32_dpp v79, v76 quad_perm:[2,3,2,3] row_mask:0xf bank_mask:0xf\n\t" \
  "v_mfma_f32_16x16x32_bf16 v[32:35], v[56:59], v[12:15], 0\n\t" \
  "ds_read_b128 v[60:63], v70 offset:24576\n\t" \
  "v_cvt_pk_bf16_f32 v11, v78, v79\n\t" \
  "v_cvt_pk_bf16_f32 v8, v78, v79\n\t" \
  "v_mov_b64 v[54:55], 0\n\t" \
  "v_mov_b32 v9, 0\n\t" \
  "v_mov_b32 v10, 0\n\t" \
  "v_mov_b64 v[14:15], 0\n\t" \
  "s_nop 0\n\t" \
  "v_mfma_f32_16x16x32_bf16 v[32:35], v[54:57], v[8:11], v[32:35]\n\t" \
  "s_cmp_eq_u32 s48, 63\n\t" \
  "v_mfma_f32_16x16x32_bf16 v[0:3], v[52:55], v[8:11], v[0:3]\n\t" \
  "ds_read_b128 v[64:67], v70 offset:25600\n\t" \
  "v_mov_b64 v[56:57], 0\n\t" \
  "v_mov_b64 v[58:59], 0\n\t" \
  "v_mfma_f32_16x16x32_bf16 v[4:7], v[52:55], v[10:13], v[4:7]\n\t" \
  "s_cbranch_scc1 LLAST0_%=\n\t" \
  "v_rcp_f32 v74, v32\n\t" \
  "v_mov_b64 v[12:13], 0\n\t" \
  "v_mov_b32 v8, 0\n\t" \
  "v_mul_f32 v0, v0, v74\n\t" \
  "v_mul_f32 v1, v1, v74\n\t" \
  "v_mul_f32 v2, v2, v74\n\t" \
  "v_mul_f32 v3, v3, v74\n\t" \
  "v_cvt_pk_bf16_f32 v76, v0, v1\n\t" \
  "v_cvt_pk_bf16_f32 v77, v2, v3\n\t" \
  "v_pk_mul_f32 v[4:5], v[4:5], v[74:75] op_sel_hi:[1,0]\n\t" \
  "v_pk_mul_f32 v[6:7], v[6:7], v[74:75] op_sel_hi:[1,0]\n\t" \
  "v_cvt_pk_bf16_f32 v78, v4, v5\n\t" \
  "v_cvt_pk_bf16_f32 v79, v6, v7\n\t" \
  "ds_write_b128 v69, v[76:79] offset:4096\n\t" \
  CH_DP9 \
  "s_waitcnt lgkmcnt(2)\n\t" \
  "v_smfmac_f32_16x16x64_bf16 v[12:15], v[76:79], v[44:51], v68\n\t" \
  "ds_read_b128 v[44:47], v70 offset:26624\n\t" \
  "v_mov_b32 v11, 0\n\t" \
  "v_smfmac_f32_16x16x64_bf16 v[56:59], v[76:79], v[16:23], v68\n\t" \
  "ds_read_b128 v[48:51], v70 offset:27648\n\t" \
  "v_mov_b64 v[52:53], 0\n\t" \
  "v_smfmac_f32_16x16x64_bf16 v[8:11], v[76:79], v[36:43], v68\n\t" \
  "ds_read_b128 v[16:19], v70 offset:28672\n\t" \
  "v_smfmac_f32_16x16x64_bf16 v[52:55], v[76:79], v[24:31], v68\n\t" \
  "s_waitcnt lgkmcnt(3)\n\t" \
  "s_barrier\n\t" \
  CH_DP10 \
  "ds_read_b128 v[0:3], v69 offset:5120\n\t" \
  "ds_read_b128 v[4:7], v69 offset:6144\n\t" \
  "v_lshrrev_b32 v71, 7, v69\n\t" \
  "v_and_b32 v74, 0x70, v69\n\t" \
  "v_lshl_add_u32 v71, v71, 14, v74\n\t" \
  "v_bfe_u32 v72, v69, 2, 7\n\t" \
  "v_and_b32 v74, 0x200, v69\n\t" \
  "s_waitcnt lgkmcnt(1)\n\t" \
  "v_smfmac_f32_16x16x64_bf16 v[8:11], v[0:3], v[60:67], v68\n\t" \
  "ds_read_b128 v[20:23], v70 offset:29696\n\t" \
  "v_mov_b64 v[76:77], 0\n\t" \
  "v_smfmac_f32_16x16x64_bf16 v[12:15], v[0:3], v[200:207], v68\n\t" \
  "ds_read_b128 v[32:35], v69 offset:7168\n\t" \
  "v_mov_b64 v[78:79], 0\n\t" \
  "v_smfmac_f32_16x16x64_bf16 v[52:55], v[0:3], v[44:51], v68\n\t" \
  "ds_read_b128 v[24:27], v70 offset:30720\n\t" \
  "v_lshl_add_u32 v72, v74, 3, v72\n\t" \
  "v_smfmac_f32_16x16x64_bf16 v[56:59], v[0:3], v[232:239], v68\n\t" \
  "buffer_load_dword v75, off, s[28:31], s80 sc1\n\t" \
  "ds_read_b128 v[28:31], v70 offset:31744\n\t" \
  "s_waitcnt lgkmcnt(4)\n\t" \
  "v_smfmac_f32_16x16x64_bf16 v[12:15], v[4:7], v[208:215], v68\n\t" \
  "ds_read_b128 v[60:63], v70 offset:32768\n\t" \
  "s_waitcnt lgkmcnt(4)\n\t" \
  "v_smfmac_f32_16x16x64_bf16 v[8:11], v[4:7], v[16:23], v68\n\t" \
  "ds_read_b128 v[64:67], v70 offset:33792\n\t" \
  "s_waitcnt lgkmcnt(4)\n\t" \
  "v_smfmac_f32_16x16x64_bf16 v[8:11], v[32:35], v[192:199], v68\n\t" \
  "ds_read_b128 v[36:39], v73 offset:4096\n\t" \
  "v_smfmac_f32_16x16x64_bf16 v[12:15], v[32:35], v[216:223], v68\n\t" \
  "ds_read_b128 v[40:43], v73 offset:6144\n\t" \
  "s_waitcnt lgkmcnt(4)\n\t" \
  "v_smfmac_f32_16x16x64_bf16 v[52:55], v[4:7], v[24:31], v68\n\t" \
  "ds_read_b128 v[44:47], v70 offset:34816\n\t" \
  "v_cvt_pk_bf16_f32 v8, v8, v9\n\t" \
  "v_smfmac_f32_16x16x64_bf16 v[52:55], v[32:35], v[224:231], v68\n\t" \
  "ds_read_b128 v[48:51], v70 offset:35840\n\t" \
  "v_cvt_pk_bf16_f32 v9, v10, v11\n\t" \
  "v_smfmac_f32_16x16x64_bf16 v[56:59], v[4:7], v[240:247], v68\n\t" \
  "s_waitcnt lgkmcnt(4)\n\t" \
  "v_cvt_pk_bf16_f32 v10, v12, v13\n\t" \
  "v_smfmac_f32_16x16x64_bf16 v[56:59], v[32:35], v[60:67], v68\n\t" \
  "s_waitcnt lgkmcnt(3)\n\t" \
  "v_cvt_pk_bf16_f32 v11, v14, v15\n\t" \
  "v_smfmac_f32_16x16x64_bf16 v[76:79], v[36:39], v[248:255], v68\n\t" \
  "buffer_store_dwordx4 v[8:11], v71, s[28:31], s54 offen sc1\n\t" \
  "s_waitcnt lgkmcnt(0)\n\t" \
  CH_DP11 \
  "v_cvt_pk_bf16_f32 v52, v52, v53\n\t" \
  "v_smfmac_f32_16x16x64_bf16 v[76:79], v[40:43], v[44:51], v68\n\t" \
  "v_cvt_pk_bf16_f32 v53, v54, v55\n\t" \
  "v_cvt_pk_bf16_f32 v54, v56, v57\n\t" \
  "v_cvt_pk_bf16_f32 v55, v58, v59\n\t" \
  "buffer_store_dwordx4 v[52:55], v71, s[28:31], s75 offen sc1\n\t" \
  "s_nop 2\n\t" \
  "v_add_f32 v76, v76, v78\n\t" \
  "v_add_f32 v77, v77, v79\n\t" \
  "v_cvt_pk_bf16_f32 v76, v76, v77\n\t" \
  "buffer_store_dword v76, v72, s[28:31], s55 offen sc1\n\t" \
  CH_DP12 \
  "s_memrealtime s[70:71]\n\t" \
  "s_waitcnt vmcnt(3)\n\t" \
  "v_readfirstlane_b32 s65, v75\n\t" \
  "s_barrier\n\t" \
  "s_waitcnt lgkmcnt(0)\n\t" \
  "s_not_b32 s65, s65\n\t" \
  "s_lshl_b32 s61, s70, 8\n\t" \
  "s_andn2_b32 s64, s65, 0xff\n\t" \
  "s_sub_u32 s64, s64, s61\n\t" \
  "s_ashr_i32 s64, s64, 8\n\t" \
  "s_cmp_eq_u32 s65, 0\n\t" \
  "s_cselect_b32 s64, " CH_DLF ", s64\n\t" \
  "s_cselect_b32 s65, " CH_B0 ", s65\n\t" \
  "s_add_u32 s47, s64, s70\n\t" \
  "s_min_i32 s64, s64, s34\n\t" \
  "s_max_i32 s64, s64, " CH_DLL1 "\n\t" \
  "s_add_u32 s64, s64, s70\n\t" \
  CH_ST2 \
  "s_add_u32 s55, s64, " CH_FDL "\n\t" \
  "s_movk_i32 s60, 8192\n\t" \
  "FWT_%=:\n\t" \
  "s_memrealtime s[62:63]\n\t" \
  "s_sub_u32 s60, s60, 1\n\t" \
  "s_cselect_b32 s61, 0, -1\n\t" \
  "s_waitcnt lgkmcnt(0)\n\t" \
  "s_sub_i32 s63, s62, s55\n\t" \
  "s_and_b32 s63, s63, s61\n\t" \
  "s_cmp_lt_i32 s63, 0\n\t" \
  "s_cbranch_scc1 FWT_%=\n\t" \
  "s_mov_b64 exec, 1\n\t" \
  "buffer_store_dword v68, off, s[28:31], s51 offset:4064 sc1\n\t" \
  "s_mov_b64 exec, -1\n\t" \
  "s_and_b32 s65, s65, 0xff\n\t" \
  "s_add_u32 s47, s47, s65\n\t" \
  "s_add_u32 s65, s65, s64\n\t" \
  "s_sub_u32 s60, s70, s66\n\t" \
  "s_add_u32 s61, s81, " CH_UP "\n\t" \
  "s_min_u32 s60, s60, s61\n\t" \
  "s_sub_u32 s61, s81, " CH_DN "\n\t" \
  "s_max_u32 s60, s60, s61\n\t" \
  "s_cmp_eq_u32 s68, 0\n\t" \
  "s_cselect_b32 s81, s60, s81\n\t" \
  CH_TRC \
  "s_add_u32 s48, s48, 1\n\t" \
  "s_mov_b32 s49, s53\n\t" \
  "s_add_u32 s53, s53, " CH_SLOT "\n\t" \
  "s_add_u32 s60, s53, " CH_SKIP "\n\t" \
  "s_lshr_b32 s61, s96, 1\n\t" \
  "s_add_u32 s86, s86, 1\n\t" \
  "s_cmp_eq_u32 s86, 3\n\t" \
  "s_cselect_b32 s53, s60, s53\n\t" \
  "s_cselect_b32 s96, s61, s96\n\t" \
  "s_cselect_b32 s86, 0, s86\n\t" \
  "s_andn2_b32 s53, s53, 0x2000\n\t" \
  "s_and_b32 s60, s96, 1\n\t" \
  "s_lshl_b32 s60, s60, 13\n\t" \
  "s_or_b32 s53, s53, s60\n\t" \
  "s_mov_b32 s68, 0\n\t" \
  "s_add_u32 s82, s48, 0\n\t" \
  CH_SEGC \
  "s_lshr_b64 s[60:61], s[90:91], s82\n\t" \
  "s_and_b32 s60, s60, 1\n\t" \
  "s_lshl_b32 s60, s60, 13\n\t" \
  "s_add_u32 s82, s82, " CH_SG0 "\n\t" \
  "s_lshl_b32 s82, s82, 21\n\t" \
  "s_or_b32 s82, s82, s60\n\t" \
  "s_add_u32 s50, s82, s38\n\t" \
  "s_add_u32 s69, s82, s39\n\t" \
  "s_add_u32 s51, s82, s43\n\t" \
  "s_cmp_lg_u32 s34, 0\n\t" \
  "s_cbranch_scc1 LW0_%=\n\t" \
  "s_barrier\n\t" \
  "s_branch LINI_%=\n\t" \
  "LFAIL0_%=:\n\t" \
  "v_max_u32 v75, v0, v1\n\t" \
  "v_max3_u32 v75, v75, v2, v3\n\t" \
  "v_max3_u32 v75, v75, v4, v5\n\t" \
  "v_max3_u32 v75, v75, v6, v7\n\t" \
  "v_max3_u32 v75, v75, v8, v9\n\t" \
  "v_max3_u32 v75, v75, v10, v11\n\t" \
  "v_max3_u32 v75, v75, v12, v13\n\t" \
  "v_max3_u32 v75, v75, v14, v15\n\t" \
  "v_max3_u32 v75, v75, v16, v17\n\t" \
  "v_max3_u32 v75, v75, v18, v19\n\t" \
  "v_max3_u32 v75, v75, v20, v21\n\t" \
  "v_max3_u32 v75, v75, v22, v23\n\t" \
  "v_max3_u32 v75, v75, v24, v25\n\t" \
  "v_max3_u32 v75, v75, v26, v27\n\t" \
  "v_max3_u32 v75, v75, v28, v29\n\t" \
  "v_max3_u32 v75, v75, v30, v31\n\t" \
  "v_max3_u32 v75, v75, v32, v33\n\t" \
  "v_max3_u32 v75, v75, v34, v35\n\t" \
  "v_cmp_eq_u32 vcc, -1, v75\n\t" \
  "s_cbranch_vccz LGO0_%=\n\t" \
  "s_add_u32 s68, s68, 1\n\t" \
  "s_waitcnt lgkmcnt(0)\n\t" \
  "s_cmp_gt_u32 s68, s83\n\t" \
  "s_cbranch_scc1 LGV0_%=\n\t" \
  "s_barrier\n\t" \
  "s_memrealtime s[62:63]\n\t" \
  "s_mov_b32 m0, 0\n\t" \
  "s_waitcnt lgkmcnt(0)\n\t" \
  "s_add_u32 s65, s62, " CH_PEN "\n\t" \
  "s_branch LDMA_%=\n\t" \
  "LWT_%=f:\n\t" \
  "s_cmp_lt_i32 s63, -2\n\t" \
  "s_cbranch_scc0 LWT_%=g\n\t" \
  "s_nop 4\n\t" \
  "s_branch LWT_%=g\n\t" \
  "LGV0_%=:\n\t" \
  "s_mov_b32 s83, 0\n\t" \
  "s_lshl_b32 s60, s36, 4\n\t" \
  "s_or_b32 s60, s60, s48\n\t" \
  "s_or_b32 s60, s60, 805306368\n\t" \
  "v_mov_b32 v76, s60\n\t" \
  "v_mov_b32 v77, 0\n\t" \
  "s_mov_b64 exec, 1\n\t" \
  "buffer_atomic_cmpswap v[76:77], off, s[28:31], s92 sc1\n\t" \
  "s_mov_b64 exec, -1\n\t" \
  "s_branch LGO0_%=\n\t" \
  "LLAST0_%=:\n\t" \
  "s_waitcnt vmcnt(0) lgkmcnt(0)\n\t" \
  "s_barrier\n\t" \
  "s_mov_b64 exec, 1\n\t" \
  "buffer_store_dword v68, off, s[28:31], s51 offset:4064 sc1\n\t" \
  "s_mov_b64 exec, -1\n\t" \
  "s_branch LEND_%=\n\t" \
  ".p2align 6\n\t" \
  "LG1_%=:\n\t" \
  "s_barrier\n\t" \
  "buffer_load_dwordx4 v[52:55], v69, s[28:31], s50 offen sc1\n\t" \
  "buffer_load_dwordx4 v[56:59], v69, s[28:31], s50 offen offset:1024 sc1\n\t" \
  "s_add_u32 s64, s50, 14336\n\t" \
  "s_movk_i32 m0, 11264\n\t" \
  "v_lshlrev_b32 v75, 1, v69\n\t" \
  "buffer_load_dword v75, s[28:31], s64 offen sc1 lds\n\t" \
  "ds_read_b128 v[44:47], v70 offset:0\n\t" \
  "ds_read_b128 v[48:51], v70 offset:1024\n\t" \
  "ds_read_b128 v[60:63], v70 offset:2048\n\t" \
  "ds_read_b128 v[64:67], v70 offset:3072\n\t" \
  "v_mov_b64 v[36:37], 0\n\t" \
  "v_mov_b64 v[38:39], 0\n\t" \
  "v_mov_b64 v[40:41], 0\n\t" \
  "v_mov_b64 v[42:43], 0\n\t" \
  "v_lshrrev_b32 v71, 1, v69\n\t" \
  "v_add_u32 v71, 4, v71\n\t" \
  "v_lshrrev_b32 v72, 3, v69\n\t" \
  "v_and_b32 v72, 0x78, v72\n\t" \
  "v_and_b32 v74, 0x30, v69\n\t" \
  "v_lshl_add_u32 v72, v74, 3, v72\n\t" \
  "s_add_u32 s54, s53, s36\n\t" \
  "s_add_u32 s55, s54, 540672\n\t" \
  "s_add_u32 s54, s54, 4096\n\t" \
  "s_add_u32 s75, s54, 262144\n\t" \
  "s_add_u32 s80, s53, s26\n\t" \
  "s_barrier\n\t" \
  CH_DP0 \
  "ds_read_b128 v[0:3], v69 offset:0\n\t" \
  "ds_read_b128 v[4:7], v69 offset:1024\n\t" \
  "ds_read_b128 v[8:11], v69 offset:2048\n\t" \
  "ds_read_b128 v[12:15], v69 offset:3072\n\t" \
  "s_waitcnt lgkmcnt(3)\n\t" \
  "v_smfmac_f32_16x16x64_bf16 v[36:39], v[0:3], v[44:51], v68\n\t" \
  "ds_read_b128 v[44:47], v70 offset:4096\n\t" \
  "v_smfmac_f32_16x16x64_bf16 v[40:43], v[0:3], v[60:67], v68\n\t" \
  "ds_read_b128 v[48:51], v70 offset:5120\n\t" \
  "s_waitcnt lgkmcnt(4)\n\t" \
  "v_smfmac_f32_16x16x64_bf16 v[36:39], v[4:7], a[32:39], v68\n\t" \
  "s_barrier\n\t" \
  "ds_read_b128 v[16:19], v69 offset:4096\n\t" \
  CH_DP1 \
  "v_smfmac_f32_16x16x64_bf16 v[40:43], v[4:7], a[40:47], v68\n\t" \
  "ds_read_b128 v[20:23], v69 offset:5120\n\t" \
  "s_waitcnt lgkmcnt(5)\n\t" \
  "v_smfmac_f32_16x16x64_bf16 v[36:39], v[8:11], a[80:87], v68\n\t" \
  "ds_read_b128 v[24:27], v69 offset:6144\n\t" \
  "v_smfmac_f32_16x16x64_bf16 v[40:43], v[8:11], a[88:95], v68\n\t" \
  "ds_read_b128 v[28:31], v69 offset:7168\n\t" \
  "s_waitcnt lgkmcnt(6)\n\t" \
  "v_smfmac_f32_16x16x64_bf16 v[36:39], v[12:15], a[120:127], v68\n\t" \
  "ds_read_b128 v[32:35], v69 offset:8192\n\t" \
  "s_waitcnt lgkmcnt(5)\n\t" \
  "v_smfmac_f32_16x16x64_bf16 v[40:43], v[12:15], v[44:51], v68\n\t" \
  "ds_read_b128 v[60:63], v70 offset:6144\n\t" \
  "s_waitcnt lgkmcnt(5)\n\t" \
  "v_smfmac_f32_16x16x64_bf16 v[36:39], v[16:19], a[160:167], v68\n\t" \
  "ds_read_b128 v[64:67], v70 offset:7168\n\t" \
  "v_smfmac_f32_16x16x64_bf16 v[40:43], v[16:19], a[168:175], v68\n\t" \
  "s_waitcnt lgkmcnt(5)\n\t" \
  "v_mov_b64 v[44:45], 0\n\t" \
  "v_smfmac_f32_16x16x64_bf16 v[36:39], v[20:23], a[200:207], v68\n\t" \
  "s_waitcnt lgkmcnt(4)\n\t" \
  "v_mov_b64 v[46:47], 0\n\t" \
  "v_smfmac_f32_16x16x64_bf16 v[36:39], v[24:27], a[240:247], v68\n\t" \
  "s_waitcnt lgkmcnt(2)\n\t" \
  "v_mov_b64 v[48:49], 0\n\t" \
  "v_smfmac_f32_16x16x64_bf16 v[36:39], v[28:31], v[112:119], v68\n\t" \
  "v_mov_b64 v[50:51], 0\n\t" \
  "v_smfmac_f32_16x16x64_bf16 v[36:39], v[32:35], v[160:167], v68\n\t" \
  "s_waitcnt lgkmcnt(0)\n\t" \
  "v_smfmac_f32_16x16x64_bf16 v[40:43], v[20:23], v[60:67], v68\n\t" \
  "ds_read_b128 v[60:63], v70 offset:8192\n\t" \
  "v_smfmac_f32_16x16x64_bf16 v[40:43], v[24:27], a[248:255], v68\n\t" \
  "ds_read_b128 v[64:67], v70 offset:9216\n\t" \
  "v_add_f32 v74, v36, v38\n\t" \
  "v_smfmac_f32_16x16x64_bf16 v[40:43], v[28:31], v[120:127], v68\n\t" \
  "v_add_f32 v37, v37, v39\n\t" \
  "v_smfmac_f32_16x16x64_bf16 v[40:43], v[32:35], v[168:175], v68\n\t" \
  "v_cmp_u_f32 vcc, v74, v37\n\t" \
  CH_DP2 \
  "v_smfmac_f32_16x16x64_bf16 v[44:47], v[0:3], a[0:7], v68\n\t" \
  "s_cbranch_vccnz LFAIL1_%=\n\t" \
  "LGO1_%=:\n\t" \
  "v_cvt_pk_bf16_f32 v74, v74, v37\n\t" \
  "v_smfmac_f32_16x16x64_bf16 v[44:47], v[4:7], a[48:55], v68\n\t" \
  "s_waitcnt lgkmcnt(0)\n\t" \
  "v_add_f32 v75, v40, v42\n\t" \
  "v_smfmac_f32_16x16x64_bf16 v[44:47], v[8:11], v[60:67], v68\n\t" \
  "ds_read_b128 v[60:63], v70 offset:10240\n\t" \
  "v_add_f32 v41, v41, v43\n\t" \
  "v_cvt_pk_bf16_f32 v75, v75, v41\n\t" \
  "v_smfmac_f32_16x16x64_bf16 v[44:47], v[12:15], a[128:135], v68\n\t" \
  "ds_write2st64_b32 v71, v74, v75 offset0:36 offset1:38\n\t" \
  "v_smfmac_f32_16x16x64_bf16 v[44:47], v[16:19], a[176:183], v68\n\t" \
  "ds_read_b128 v[64:67], v70 offset:11264\n\t" \
  "v_smfmac_f32_16x16x64_bf16 v[44:47], v[20:23], a[208:215], v68\n\t" \
  "v_smfmac_f32_16x16x64_bf16 v[44:47], v[24:27], v[80:87], v68\n\t" \
  "v_smfmac_f32_16x16x64_bf16 v[44:47], v[28:31], v[128:135], v68\n\t" \
  "s_waitcnt lgkmcnt(0)\n\t" \
  "v_smfmac_f32_16x16x64_bf16 v[44:47], v[32:35], v[60:67], v68\n\t" \
  "s_barrier\n\t" \
  "ds_read_b64_tr_b16 v[76:77], v72 offset:9216\n\t" \
  CH_DP3 \
  "v_smfmac_f32_16x16x64_bf16 v[48:51], v[0:3], a[8:15], v68\n\t" \
  "buffer_load_dwordx4 v[60:63], v69, s[28:31], s64 offen sc1\n\t" \
  "ds_read_b64_tr_b16 v[78:79], v72 offset:9728\n\t" \
  "v_smfmac_f32_16x16x64_bf16 v[48:51], v[4:7], a[56:63], v68\n\t" \
  "v_add_f32 v74, v44, v46\n\t" \
  "v_smfmac_f32_16x16x64_bf16 v[48:51], v[8:11], a[96:103], v68\n\t" \
  "v_add_f32 v45, v45, v47\n\t" \
  "v_smfmac_f32_16x16x64_bf16 v[48:51], v[12:15], a[136:143], v68\n\t" \
  "s_waitcnt vmcnt(3)\n\t" \
  "s_waitcnt lgkmcnt(0)\n\t" \
  "v_cvt_pk_bf16_f32 v74, v74, v45\n\t" \
  "v_mfma_f32_16x16x32_bf16 v[36:39], v[52:55], v[76:79], 0\n\t" \
  "s_waitcnt vmcnt(2)\n\t" \
  "v_mfma_f32_16x16x32_bf16 v[40:43], v[56:59], v[76:79], 0\n\t" \
  "buffer_load_dwordx4 v[64:67], v69, s[28:31], s64 offen offset:1024 sc1\n\t" \
  "v_mov_b64 v[52:53], 0\n\t" \
  "v_mov_b64 v[54:55], 0\n\t" \
  "v_smfmac_f32_16x16x64_bf16 v[48:51], v[16:19], a[184:191], v68\n\t" \
  "v_mov_b64 v[56:57], 0\n\t" \
  "v_mov_b64 v[58:59], 0\n\t" \
  "v_smfmac_f32_16x16x64_bf16 v[48:51], v[20:23], a[216:223], v68\n\t" \
  "ds_write_b32 v71, v74 offset:0\n\t" \
  "v_add_f32_dpp v36, v40, v36 quad_perm:[2,3,0,1] row_mask:0xf bank_mask:0xf\n\t" \
  "v_smfmac_f32_16x16x64_bf16 v[48:51], v[24:27], v[88:95], v68\n\t" \
  "v_add_f32_dpp v37, v41, v37 quad_perm:[2,3,0,1] row_mask:0xf bank_mask:0xf\n\t" \
  "v_smfmac_f32_16x16x64_bf16 v[48:51], v[28:31], v[136:143], v68\n\t" \
  "v_add_f32_dpp v38, v42, v38 quad_perm:[2,3,0,1] row_mask:0xf bank_mask:0xf\n\t" \
  "v_smfmac_f32_16x16x64_bf16 v[48:51], v[32:35], v[176:183], v68\n\t" \
  "v_add_f32_dpp v39, v43, v39 quad_perm:[2,3,0,1] row_mask:0xf bank_mask:0xf\n\t" \
  CH_DP4 \
  "v_smfmac_f32_16x16x64_bf16 v[52:55], v[0:3], a[16:23], v68\n\t" \
  "s_mov_b64 exec, s[58:59]\n\t" \
  "ds_write_b128 v69, v[36:39] offset:2080\n\t" \
  "s_mov_b64 exec, -1\n\t" \
  "v_smfmac_f32_16x16x64_bf16 v[52:55], v[4:7], a[64:71], v68\n\t" \
  "v_add_f32 v75, v48, v50\n\t" \
  "v_add_f32 v49, v49, v51\n\t" \
  "v_cvt_pk_bf16_f32 v75, v75, v49\n\t" \
  "v_smfmac_f32_16x16x64_bf16 v[56:59], v[0:3], a[24:31], v68\n\t" \
  "ds_write_b32 v71, v75 offset:512\n\t" \
  "v_smfmac_f32_16x16x64_bf16 v[56:59], v[4:7], a[72:79], v68\n\t" \
  "ds_read_b64_tr_b16 v[0:1], v72 offset:10240\n\t" \
  "ds_read_b64_tr_b16 v[2:3], v72 offset:10752\n\t" \
  "v_smfmac_f32_16x16x64_bf16 v[52:55], v[8:11], a[104:111], v68\n\t" \
  "ds_read_b128 v[36:39], v70 offset:12288\n\t" \
  "v_smfmac_f32_16x16x64_bf16 v[56:59], v[8:11], a[112:119], v68\n\t" \
  "ds_read_b128 v[40:43], v70 offset:13312\n\t" \
  "v_smfmac_f32_16x16x64_bf16 v[52:55], v[12:15], a[144:151], v68\n\t" \
  "s_waitcnt lgkmcnt(4)\n\t" \
  "s_barrier\n\t" \
  CH_DP5 \
  "ds_read_b64_tr_b16 v[44:45], v72 offset:0\n\t" \
  "ds_read_b64_tr_b16 v[46:47], v72 offset:512\n\t" \
  "v_smfmac_f32_16x16x64_bf16 v[56:59], v[12:15], a[152:159], v68\n\t" \
  "ds_read_b64_tr_b16 v[48:49], v72 offset:1024\n\t" \
  "ds_read_b64_tr_b16 v[50:51], v72 offset:1536\n\t" \
  "v_smfmac_f32_16x16x64_bf16 v[52:55], v[16:19], a[192:199], v68\n\t" \
  "ds_read_b128 v[4:7], v69 offset:2048\n\t" \
  "s_waitcnt lgkmcnt(5)\n\t" \
  "v_smfmac_f32_16x16x64_bf16 v[56:59], v[16:19], v[36:43], v68\n\t" \
  "ds_read_b128 v[8:11], v69 offset:3072\n\t" \
  "s_waitcnt lgkmcnt(4)\n\t" \
  "v_mfma_f32_16x16x32_bf16 v[12:15], v[44:47], v[76:79], 0\n\t" \
  "ds_read_b128 v[36:39], v70 offset:14336\n\t" \
  "s_waitcnt lgkmcnt(3)\n\t" \
  "v_mfma_f32_16x16x32_bf16 v[12:15], v[48:51], v[0:3], v[12:15]\n\t" \
  "buffer_store_dwordx4 v[44:47], v69, s[28:31], s69 offen offset:0 sc1\n\t" \
  "ds_read_b128 v[40:43], v70 offset:15360\n\t" \
  "v_smfmac_f32_16x16x64_bf16 v[52:55], v[20:23], a[224:231], v68\n\t" \
  "s_waitcnt lgkmcnt(2)\n\t" \
  "v_add_f32 v4, v4, v8\n\t" \
  "v_add_f32 v5, v5, v9\n\t" \
  "v_add_f32 v6, v6, v10\n\t" \
  CH_DP6 \
  "v_cndmask_b32 v78, v13, v14, s[56:57]\n\t" \
  "v_cndmask_b32 v76, v12, v15, s[56:57]\n\t" \
  "v_add_f32 v7, v7, v11\n\t" \
  "v_max3_f32 v77, v4, v5, v6\n\t" \
  "v_add_f32_dpp v76, v78, v76 quad_perm:[2,3,0,1] row_mask:0xf bank_mask:0xf\n\t" \
  "v_max3_f32 v77, v77, v7, v76\n\t" \
  "v_smfmac_f32_16x16x64_bf16 v[56:59], v[20:23], a[232:239], v68\n\t" \
  "ds_read_b128 v[44:47], v70 offset:16384\n\t" \
  "v_max_f32_dpp v77, v77, v77 quad_perm:[2,3,0,1] row_mask:0xf bank_mask:0xf\n\t" \
  "v_mov_b32 v78, v77\n\t" \
  "v_smfmac_f32_16x16x64_bf16 v[52:55], v[24:27], v[96:103], v68\n\t" \
  "ds_read_b128 v[48:51], v70 offset:17408\n\t" \
  "v_permlane16_swap_b32 v77, v78\n\t" \
  "v_max_f32 v77, v77, v78\n\t" \
  "v_mov_b32 v78, v77\n\t" \
  "v_smfmac_f32_16x16x64_bf16 v[52:55], v[28:31], v[144:151], v68\n\t" \
  "ds_read_b128 v[16:19], v70 offset:18432\n\t" \
  "v_permlane32_swap_b32 v77, v78\n\t" \
  "v_max_f32 v77, v77, v78\n\t" \
  "v_smfmac_f32_16x16x64_bf16 v[52:55], v[32:35], v[184:191], v68\n\t" \
  "ds_read_b128 v[20:23], v70 offset:19456\n\t" \
  "v_mul_f32 v77, 0xbe0293ee, v77\n\t" \
  CH_DP7 \
  "v_smfmac_f32_16x16x64_bf16 v[56:59], v[24:27], v[104:111], v68\n\t" \
  "v_fmamk_f32 v4, v4, 0x3e0293ee, v77\n\t" \
  "v_smfmac_f32_16x16x64_bf16 v[56:59], v[28:31], v[152:159], v68\n\t" \
  "ds_read_b128 v[24:27], v70 offset:22528\n\t" \
  "s_waitcnt lgkmcnt(4)\n\t" \
  "v_fmamk_f32 v5, v5, 0x3e0293ee, v77\n\t" \
  "v_smfmac_f32_16x16x64_bf16 v[56:59], v[32:35], v[36:43], v68\n\t" \
  "ds_read_b128 v[28:31], v70 offset:23552\n\t" \
  "v_fmamk_f32 v6, v6, 0x3e0293ee, v77\n\t" \
  "v_fmamk_f32 v7, v7, 0x3e0293ee, v77\n\t" \
  "v_fmamk_f32 v76, v76, 0x3e0293ee, v77\n\t" \
  "v_exp_f32 v4, v4\n\t" \
  "v_exp_f32 v5, v5\n\t" \
  "v_exp_f32 v6, v6\n\t" \
  "v_exp_f32 v7, v7\n\t" \
  "v_cvt_pk_bf16_f32 v4, v4, v5\n\t" \
  "v_exp_f32 v76, v76\n\t" \
  "v_cvt_pk_bf16_f32 v5, v6, v7\n\t" \
  "v_mov_b32_dpp v12, v4 quad_perm:[0,1,0,1] row_mask:0xf bank_mask:0xf\n\t" \
  "v_mov_b32_dpp v14, v4 quad_perm:[2,3,2,3] row_mask:0xf bank_mask:0xf\n\t" \
  "v_mov_b32_dpp v13, v5 quad_perm:[0,1,0,1] row_mask:0xf bank_mask:0xf\n\t" \
  "v_mov_b32_dpp v15, v5 quad_perm:[2,3,2,3] row_mask:0xf bank_mask:0xf\n\t" \
  "v_add_f32 v52, v52, v54\n\t" \
  "v_add_f32 v53, v53, v55\n\t" \
  "s_waitcnt vmcnt(2)\n\t" \
  CH_DP8 \
  "v_mfma_f32_16x16x32_bf16 v[0:3], v[60:63], v[12:15], 0\n\t" \
  "ds_read_b128 v[36:39], v70 offset:20480\n\t" \
  "v_add_f32 v56, v56, v58\n\t" \
  "v_add_f32 v57, v57, v59\n\t" \
  "v_cvt_pk_bf16_f32 v52, v52, v53\n\t" \
  "v_cvt_pk_bf16_f32 v53, v56, v57\n\t" \
  "s_waitcnt vmcnt(1)\n\t" \
  "v_mfma_f32_16x16x32_bf16 v[4:7], v[64:67], v[12:15], 0\n\t" \
  "ds_read_b128 v[40:43], v70 offset:21504\n\t" \
  "v_mov_b64 v[56:57], s[88:89]\n\t" \
  "v_mov_b64 v[58:59], s[88:89]\n\t" \
  "v_mov_b32_dpp v78, v76 quad_perm:[0,1,0,1] row_mask:0xf bank_mask:0xf\n\t" \
  "v_mov_b32_dpp v79, v76 quad_perm:[2,3,2,3] row_mask:0xf bank_mask:0xf\n\t" \
  "v_mfma_f32_16x16x32_bf16 v[32:35], v[56:59], v[12:15], 0\n\t" \
  "buffer_store_dwordx2 v[52:53], v71, s[28:31], s69 offen offset:2556 sc1\n\t" \
  "ds_read_b128 v[60:63], v70 offset:24576\n\t" \
  "v_cvt_pk_bf16_f32 v11, v78, v79\n\t" \
  "v_cvt_pk_bf16_f32 v8, v78, v79\n\t" \
  "v_mov_b64 v[54:55], 0\n\t" \
  "v_mov_b32 v9, 0\n\t" \
  "v_mov_b32 v10, 0\n\t" \
  "v_mov_b64 v[14:15], 0\n\t" \
  "s_nop 0\n\t" \
  "v_mfma_f32_16x16x32_bf16 v[32:35], v[54:57], v[8:11], v[32:35]\n\t" \
  "s_cmp_eq_u32 s48, 63\n\t" \
  "v_mfma_f32_16x16x32_bf16 v[0:3], v[52:55], v[8:11], v[0:3]\n\t" \
  "ds_read_b128 v[64:67], v70 offset:25600\n\t" \
  "v_mov_b64 v[56:57], 0\n\t" \
  "v_mov_b64 v[58:59], 0\n\t" \
  "v_mfma_f32_16x16x32_bf16 v[4:7], v[52:55], v[10:13], v[4:7]\n\t" \
  "s_cbranch_scc1 LLAST1_%=\n\t" \
  "v_rcp_f32 v74, v32\n\t" \
  "v_mov_b64 v[12:13], 0\n\t" \
  "v_mov_b32 v8, 0\n\t" \
  "v_mul_f32 v0, v0, v74\n\t" \
  "v_mul_f32 v1, v1, v74\n\t" \
  "v_mul_f32 v2, v2, v74\n\t" \
  "v_mul_f32 v3, v3, v74\n\t" \
  "v_cvt_pk_bf16_f32 v76, v0, v1\n\t" \
  "v_cvt_pk_bf16_f32 v77, v2, v3\n\t" \
  "v_pk_mul_f32 v[4:5], v[4:5], v[74:75] op_sel_hi:[1,0]\n\t" \
  "v_pk_mul_f32 v[6:7], v[6:7], v[74:75] op_sel_hi:[1,0]\n\t" \
  "v_cvt_pk_bf16_f32 v78, v4, v5\n\t" \
  "v_cvt_pk_bf16_f32 v79, v6, v7\n\t" \
  "ds_write_b128 v69, v[76:79] offset:5120\n\t" \
  CH_DP9 \
  "s_waitcnt lgkmcnt(2)\n\t" \
  "v_smfmac_f32_16x16x64_bf16 v[12:15], v[76:79], v[44:51], v68\n\t" \
  "ds_read_b128 v[44:47], v70 offset:26624\n\t" \
  "v_mov_b32 v11, 0\n\t" \
  "v_smfmac_f32_16x16x64_bf16 v[56:59], v[76:79], v[16:23], v68\n\t" \
  "ds_read_b128 v[48:51], v70 offset:27648\n\t" \
  "v_mov_b64 v[52:53], 0\n\t" \
  "v_smfmac_f32_16x16x64_bf16 v[8:11], v[76:79], v[36:43], v68\n\t" \
  "ds_read_b128 v[16:19], v70 offset:28672\n\t" \
  "v_smfmac_f32_16x16x64_bf16 v[52:55], v[76:79], v[24:31], v68\n\t" \
  "s_waitcnt lgkmcnt(3)\n\t" \
  "s_barrier\n\t" \
  CH_DP10 \
  "ds_read_b128 v[0:3], v69 offset:4096\n\t" \
  "ds_read_b128 v[4:7], v69 offset:7168\n\t" \
  "v_lshrrev_b32 v71, 7, v69\n\t" \
  "v_and_b32 v74, 0x70, v69\n\t" \
  "v_lshl_add_u32 v71, v71, 14, v74\n\t" \
  "v_bfe_u32 v72, v69, 2, 7\n\t" \
  "v_and_b32 v74, 0x200, v69\n\t" \
  "s_waitcnt lgkmcnt(1)\n\t" \
  "v_smfmac_f32_16x16x64_bf16 v[8:11], v[0:3], v[60:67], v68\n\t" \
  "buffer_load_dword v75, off, s[28:31], s80 sc1\n\t" \
  "ds_read_b128 v[20:23], v70 offset:29696\n\t" \
  "v_mov_b64 v[76:77], 0\n\t" \
  "v_smfmac_f32_16x16x64_bf16 v[12:15], v[0:3], v[200:207], v68\n\t" \
  "ds_read_b128 v[32:35], v69 offset:6144\n\t" \
  "v_mov_b64 v[78:79], 0\n\t" \
  "v_smfmac_f32_16x16x64_bf16 v[52:55], v[0:3], v[44:51], v68\n\t" \
  "ds_read_b128 v[24:27], v70 offset:30720\n\t" \
  "v_lshl_add_u32 v72, v74, 3, v72\n\t" \
  "v_smfmac_f32_16x16x64_bf16 v[56:59], v[0:3], v[232:239], v68\n\t" \
  "ds_read_b128 v[28:31], v70 offset:31744\n\t" \
  "s_waitcnt lgkmcnt(4)\n\t" \
  "v_smfmac_f32_16x16x64_bf16 v[12:15], v[4:7], v[208:215], v68\n\t" \
  "ds_read_b128 v[60:63], v70 offset:32768\n\t" \
  "s_waitcnt lgkmcnt(4)\n\t" \
  "v_smfmac_f32_16x16x64_bf16 v[8:11], v[4:7], v[16:23], v68\n\t" \
  "ds_read_b128 v[64:67], v70 offset:33792\n\t" \
  "s_waitcnt lgkmcnt(4)\n\t" \
  "v_smfmac_f32_16x16x64_bf16 v[8:11], v[32:35], v[192:199], v68\n\t" \
  "ds_read_b128 v[36:39], v73 offset:4096\n\t" \
  "v_smfmac_f32_16x16x64_bf16 v[12:15], v[32:35], v[216:223], v68\n\t" \
  "ds_read_b128 v[40:43], v73 offset:6144\n\t" \
  "s_waitcnt lgkmcnt(4)\n\t" \
  "v_smfmac_f32_16x16x64_bf16 v[52:55], v[4:7], v[24:31], v68\n\t" \
  "ds_read_b128 v[44:47], v70 offset:34816\n\t" \
  "v_cvt_pk_bf16_f32 v8, v8, v9\n\t" \
  "v_smfmac_f32_16x16x64_bf16 v[52:55], v[32:35], v[224:231], v68\n\t" \
  "ds_read_b128 v[48:51], v70 offset:35840\n\t" \
  "v_cvt_pk_bf16_f32 v9, v10, v11\n\t" \
  "v_smfmac_f32_16x16x64_bf16 v[56:59], v[4:7], v[240:247], v68\n\t" \
  "s_waitcnt lgkmcnt(4)\n\t" \
  "v_cvt_pk_bf16_f32 v10, v12, v13\n\t" \
  "v_smfmac_f32_16x16x64_bf16 v[56:59], v[32:35], v[60:67], v68\n\t" \
  "s_waitcnt lgkmcnt(3)\n\t" \
  "v_cvt_pk_bf16_f32 v11, v14, v15\n\t" \
  "v_smfmac_f32_16x16x64_bf16 v[76:79], v[36:39], v[248:255], v68\n\t" \
  "buffer_store_dwordx4 v[8:11], v71, s[28:31], s54 offen sc1\n\t" \
  "s_waitcnt lgkmcnt(0)\n\t" \
  CH_DP11 \
  "v_cvt_pk_bf16_f32 v52, v52, v53\n\t" \
  "v_smfmac_f32_16x16x64_bf16 v[76:79], v[40:43], v[44:51], v68\n\t" \
  "v_cvt_pk_bf16_f32 v53, v54, v55\n\t" \
  "v_cvt_pk_bf16_f32 v54, v56, v57\n\t" \
  "v_cvt_pk_bf16_f32 v55, v58, v59\n\t" \
  "buffer_store_dwordx4 v[52:55], v71, s[28:31], s75 offen sc1\n\t" \
  "s_nop 2\n\t" \
  "v_add_f32 v76, v76, v78\n\t" \
  "v_add_f32 v77, v77, v79\n\t" \
  "v_cvt_pk_bf16_f32 v76, v76, v77\n\t" \
  "buffer_store_dword v76, v72, s[28:31], s55 offen sc1\n\t" \
  CH_DP12 \
  "s_memrealtime s[70:71]\n\t" \
  "s_waitcnt vmcnt(3)\n\t" \
  "v_readfirstlane_b32 s65, v75\n\t" \
  "s_barrier\n\t" \
  "s_waitcnt lgkmcnt(0)\n\t" \
  "s_not_b32 s65, s65\n\t" \
  "s_lshl_b32 s61, s70, 8\n\t" \
  "s_andn2_b32 s64, s65, 0xff\n\t" \
  "s_sub_u32 s64, s64, s61\n\t" \
  "s_ashr_i32 s64, s64, 8\n\t" \
  "s_cmp_eq_u32 s65, 0\n\t" \
  "s_cselect_b32 s64, " CH_DLF ", s64\n\t" \
  "s_min_i32 s64, s64, s34\n\t" \
  "s_max_i32 s64, s64, " CH_DLL1 "\n\t" \
  "s_add_u32 s64, s64, s70\n\t" \
  CH_ST2 \
  "s_mov_b32 s74, 0\n\t" \
  "s_movk_i32 s60, 8192\n\t" \
  "RWT1_%=:\n\t" \
  "s_memrealtime s[72:73]\n\t" \
  "s_sub_u32 s60, s60, 1\n\t" \
  "s_cselect_b32 s61, 0, -1\n\t" \
  "s_waitcnt lgkmcnt(0)\n\t" \
  "s_sub_i32 s73, s72, s64\n\t" \
  "s_and_b32 s73, s73, s61\n\t" \
  "s_cmp_lt_i32 s73, " CH_FIN "\n\t" \
  "s_cbranch_scc1 RWT1_%=\n\t" \
  "s_cmp_lt_i32 s73, 0\n\t" \
  "s_cbranch_scc1 RWT1_%=f\n\t" \
  "RWT1_%=g:\n\t" \
  "RRETRY1_%=:\n\t" \
  CH_DP13 \
  "buffer_load_dwordx4 v[0:3], v69, s[28:31], s97 offen offset:0 sc1\n\t" \
  "buffer_load_dwordx4 v[4:7], v69, s[28:31], s97 offen offset:1024 sc1\n\t" \
  "buffer_load_dwordx4 v[8:11], v69, s[28:31], s97 offen offset:2048 sc1\n\t" \
  "buffer_load_dwordx4 v[12:15], v69, s[28:31], s97 offen offset:3072 sc1\n\t" \
  "v_lshrrev_b32 v20, 2, v69\n\t" \
  "v_and_b32 v20, 16, v20\n\t" \
  "v_mov_b32 v21, 0x3f80\n\t" \
  "v_lshlrev_b32 v21, v20, v21\n\t" \
  "v_bfe_u32 v20, v69, 4, 2\n\t" \
  "v_cmp_eq_u32 vcc, 0, v20\n\t" \
  "s_nop 1\n\t" \
  "v_cndmask_b32 v32, 0, v21, vcc\n\t" \
  "v_cmp_eq_u32 vcc, 1, v20\n\t" \
  "s_nop 1\n\t" \
  "v_cndmask_b32 v33, 0, v21, vcc\n\t" \
  "v_cmp_eq_u32 vcc, 2, v20\n\t" \
  "s_nop 1\n\t" \
  "v_cndmask_b32 v34, 0, v21, vcc\n\t" \
  "v_cmp_eq_u32 vcc, 3, v20\n\t" \
  "s_nop 1\n\t" \
  "v_cndmask_b32 v35, 0, v21, vcc\n\t" \
  "v_bfe_u32 v38, v69, 7, 1\n\t" \
  "v_bfe_u32 v20, v69, 9, 1\n\t" \
  "v_lshl_add_u32 v38, v20, 2, v38\n\t" \
  "v_lshlrev_b32 v38, 12, v38\n\t" \
  "v_and_b32 v20, 0x60, v69\n\t" \
  "v_add_u32 v38, v38, v20\n\t" \
  "v_bfe_u32 v20, v69, 8, 1\n\t" \
  "v_lshl_add_u32 v38, v20, 4, v38\n\t" \
  "v_bfe_u32 v20, v69, 4, 1\n\t" \
  "v_lshl_add_u32 v38, v20, 3, v38\n\t" \
  "v_mov_b32 v44, 0\n\t" \
  CH_DP17 \
  "s_waitcnt vmcnt(3)\n\t" \
  CH_DP14 \
  "v_mfma_f32_16x16x32_bf16 v[40:43], v[32:35], v[0:3], 0\n\t" \
  "v_max3_u32 v44, v44, v0, v1\n\t" \
  "v_max3_u32 v44, v44, v2, v3\n\t" \
  "s_waitcnt vmcnt(2)\n\t" \
  "v_mfma_f32_16x16x32_bf16 v[40:43], v[32:35], v[4:7], v[40:43]\n\t" \
  "v_max3_u32 v44, v44, v4, v5\n\t" \
  "v_max3_u32 v44, v44, v6, v7\n\t" \
  "s_waitcnt vmcnt(1)\n\t" \
  "v_mfma_f32_16x16x32_bf16 v[40:43], v[32:35], v[8:11], v[40:43]\n\t" \
  "v_max3_u32 v44, v44, v8, v9\n\t" \
  "v_max3_u32 v44, v44, v10, v11\n\t" \
  "s_waitcnt vmcnt(0)\n\t" \
  CH_DP15 \
  "v_mfma_f32_16x16x32_bf16 v[40:43], v[32:35], v[12:15], v[40:43]\n\t" \
  "v_max3_u32 v44, v44, v12, v13\n\t" \
  "v_max3_u32 v44, v44, v14, v15\n\t" \
  "v_cmp_eq_u32 vcc, -1, v44\n\t" \
  "s_cbranch_vccnz RFAIL1_%=\n\t" \
  "s_nop 3\n\t" \
  "v_add_f32_dpp v40, v40, v40 row_ror:8 row_mask:0xf bank_mask:0xf\n\t" \
  "v_add_f32_dpp v41, v41, v41 row_ror:8 row_mask:0xf bank_mask:0xf\n\t" \
  "v_add_f32_dpp v42, v42, v42 row_ror:8 row_mask:0xf bank_mask:0xf\n\t" \
  "v_add_f32_dpp v43, v43, v43 row_ror:8 row_mask:0xf bank_mask:0xf\n\t" \
  "v_cvt_pk_bf16_f32 v40, v40, v41\n\t" \
  "v_cvt_pk_bf16_f32 v41, v42, v43\n\t" \
  "buffer_store_dwordx2 v[40:41], v38, s[28:31], s98 offen sc1\n\t" \
  "RDONE1_%=:\n\t" \
  CH_DP16 \
  CH_ST4 \
  "s_barrier\n\t" \
  CH_ST3 \
  CH_TRC \
  "s_add_u32 s48, s48, 1\n\t" \
  "s_mov_b32 s49, s53\n\t" \
  "s_add_u32 s53, s53, " CH_SLOT "\n\t" \
  "s_add_u32 s60, s53, " CH_SKIP "\n\t" \
  "s_lshr_b32 s61, s96, 1\n\t" \
  "s_add_u32 s86, s86, 1\n\t" \
  "s_cmp_eq_u32 s86, 3\n\t" \
  "s_cselect_b32 s53, s60, s53\n\t" \
  "s_cselect_b32 s96, s61, s96\n\t" \
  "s_cselect_b32 s86, 0, s86\n\t" \
  "s_andn2_b32 s53, s53, 0x2000\n\t" \
  "s_and_b32 s60, s96, 1\n\t" \
  "s_lshl_b32 s60, s60, 13\n\t" \
  "s_or_b32 s53, s53, s60\n\t" \
  "s_mov_b32 s68, 0\n\t" \
  "s_add_u32 s97, s41, s53\n\t" \
  "s_add_u32 s98, s37, s53\n\t" \
  "s_add_u32 s82, s48, 0\n\t" \
  CH_SEGC \
  "s_lshr_b64 s[60:61], s[90:91], s82\n\t" \
  "s_and_b32 s60, s60, 1\n\t" \
  "s_lshl_b32 s60, s60, 13\n\t" \
  "s_add_u32 s82, s82, " CH_SG0 "\n\t" \
  "s_lshl_b32 s82, s82, 21\n\t" \
  "s_or_b32 s82, s82, s60\n\t" \
  "s_add_u32 s50, s82, s38\n\t" \
  "s_add_u32 s69, s82, s39\n\t" \
  "s_cmp_eq_u32 s34, 0\n\t" \
  "s_cbranch_scc1 LINI_%=\n\t" \
  "s_branch LG1_%=\n\t" \
  "RFAIL1_%=:\n\t" \
  "s_add_u32 s74, s74, 1\n\t" \
  "s_sleep " CH_RSL "\n\t" \
  "s_waitcnt vmcnt(0)\n\t" \
  "s_cmp_gt_u32 s74, s84\n\t" \
  "s_cbranch_scc1 RG1_%=\n\t" \
  "s_and_b32 s60, s74, 1023\n\t" \
  "s_cmp_lg_u32 s60, 0\n\t" \
  "s_cbranch_scc1 RRETRY1_%=\n\t" \
  "buffer_load_dword v46, off, s[28:31], s92 sc1\n\t" \
  "s_waitcnt vmcnt(0)\n\t" \
  "v_readfirstlane_b32 s60, v46\n\t" \
  "s_nop 0\n\t" \
  "s_cmp_eq_u32 s60, 0\n\t" \
  "s_cbranch_scc1 RRETRY1_%=\n\t" \
  "RG1_%=:\n\t" \
  "s_mov_b32 s84, 0\n\t" \
  "s_lshl_b32 s60, s36, 4\n\t" \
  "s_or_b32 s60, s60, s48\n\t" \
  "s_or_b32 s60, s60, 536870912\n\t" \
  "v_mov_b32 v76, s60\n\t" \
  "v_mov_b32 v77, 0\n\t" \
  "s_mov_b64 exec, 1\n\t" \
  "buffer_atomic_cmpswap v[76:77], off, s[28:31], s92 sc1\n\t" \
  "s_mov_b64 exec, -1\n\t" \
  "s_branch RDONE1_%=\n\t" \
  "LFAIL1_%=:\n\t" \
  "v_max_u32 v75, v0, v1\n\t" \
  "v_max3_u32 v75, v75, v2, v3\n\t" \
  "v_max3_u32 v75, v75, v4, v5\n\t" \
  "v_max3_u32 v75, v75, v6, v7\n\t" \
  "v_max3_u32 v75, v75, v8, v9\n\t" \
  "v_max3_u32 v75, v75, v10, v11\n\t" \
  "v_max3_u32 v75, v75, v12, v13\n\t" \
  "v_max3_u32 v75, v75, v14, v15\n\t" \
  "v_max3_u32 v75, v75, v16, v17\n\t" \
  "v_max3_u32 v75, v75, v18, v19\n\t" \
  "v_max3_u32 v75, v75, v20, v21\n\t" \
  "v_max3_u32 v75, v75, v22, v23\n\t" \
  "v_max3_u32 v75, v75, v24, v25\n\t" \
  "v_max3_u32 v75, v75, v26, v27\n\t" \
  "v_max3_u32 v75, v75, v28, v29\n\t" \
  "v_max3_u32 v75, v75, v30, v31\n\t" \
  "v_max3_u32 v75, v75, v32, v33\n\t" \
  "v_max3_u32 v75, v75, v34, v35\n\t" \
  "v_cmp_eq_u32 vcc, -1, v75\n\t" \
  "s_cbranch_vccz LGO1_%=\n\t" \
  "s_add_u32 s68, s68, 1\n\t" \
  "s_waitcnt lgkmcnt(0)\n\t" \
  "s_cmp_gt_u32 s68, s83\n\t" \
  "s_cbranch_scc1 LGV1_%=\n\t" \
  "s_barrier\n\t" \
  "s_waitcnt vmcnt(8)\n\t" \
  "s_branch LG1_%=\n\t" \
  "RWT1_%=f:\n\t" \
  "s_cmp_lt_i32 s73, -2\n\t" \
  "s_cbranch_scc0 RWT1_%=g\n\t" \
  "s_nop 4\n\t" \
  "s_branch RWT1_%=g\n\t" \
  "LGV1_%=:\n\t" \
  "s_mov_b32 s83, 0\n\t" \
  "s_lshl_b32 s60, s36, 4\n\t" \
  "s_or_b32 s60, s60, s48\n\t" \
  "s_or_b32 s60, s60, 805306368\n\t" \
  "v_mov_b32 v76, s60\n\t" \
  "v_mov_b32 v77, 0\n\t" \
  "s_mov_b64 exec, 1\n\t" \
  "buffer_atomic_cmpswap v[76:77], off, s[28:31], s92 sc1\n\t" \
  "s_mov_b64 exec, -1\n\t" \
  "s_branch LGO1_%=\n\t" \
  "LLAST1_%=:\n\t" \
  "s_waitcnt vmcnt(0) lgkmcnt(0)\n\t" \
  "s_barrier\n\t" \
  "s_branch LEND_%=\n\t" \
  ".p2align 6\n\t" \
  "LG2_%=:\n\t" \
  "s_barrier\n\t" \
  "buffer_load_dwordx4 v[52:55], v69, s[28:31], s50 offen sc1\n\t" \
  "buffer_load_dwordx4 v[56:59], v69, s[28:31], s50 offen offset:1024 sc1\n\t" \
  "s_add_u32 s64, s50, 18432\n\t" \
  "s_movk_i32 m0, 11264\n\t" \
  "v_lshlrev_b32 v75, 1, v69\n\t" \
  "buffer_load_dword v75, s[28:31], s64 offen sc1 lds\n\t" \
  "ds_read_b128 v[44:47], v70 offset:0\n\t" \
  "ds_read_b128 v[48:51], v70 offset:1024\n\t" \
  "ds_read_b128 v[60:63], v70 offset:2048\n\t" \
  "ds_read_b128 v[64:67], v70 offset:3072\n\t" \
  "v_mov_b64 v[36:37], 0\n\t" \
  "v_mov_b64 v[38:39], 0\n\t" \
  "v_mov_b64 v[40:41], 0\n\t" \
  "v_mov_b64 v[42:43], 0\n\t" \
  "v_lshrrev_b32 v71, 1, v69\n\t" \
  "v_lshrrev_b32 v72, 3, v69\n\t" \
  "v_and_b32 v72, 0x78, v72\n\t" \
  "v_and_b32 v74, 0x30, v69\n\t" \
  "v_lshl_add_u32 v72, v74, 3, v72\n\t" \
  "s_add_u32 s54, s53, s36\n\t" \
  "s_add_u32 s55, s54, 557056\n\t" \
  "s_add_u32 s54, s54, 131072\n\t" \
  "s_add_u32 s75, s54, 262144\n\t" \
  "s_add_u32 s80, s53, s26\n\t" \
  "s_barrier\n\t" \
  CH_DP0 \
  "ds_read_b128 v[0:3], v69 offset:0\n\t" \
  "ds_read_b128 v[4:7], v69 offset:1024\n\t" \
  "ds_read_b128 v[8:11], v69 offset:2048\n\t" \
  "ds_read_b128 v[12:15], v69 offset:3072\n\t" \
  "s_waitcnt lgkmcnt(3)\n\t" \
  "v_smfmac_f32_16x16x64_bf16 v[36:39], v[0:3], v[44:51], v68\n\t" \
  "ds_read_b128 v[44:47], v70 offset:4096\n\t" \
  "v_smfmac_f32_16x16x64_bf16 v[40:43], v[0:3], v[60:67], v68\n\t" \
  "ds_read_b128 v[48:51], v70 offset:5120\n\t" \
  "s_waitcnt lgkmcnt(4)\n\t" \
  "v_smfmac_f32_16x16x64_bf16 v[36:39], v[4:7], a[32:39], v68\n\t" \
  "s_barrier\n\t" \
  "ds_read_b128 v[16:19], v69 offset:4096\n\t" \
  CH_DP1 \
  "v_smfmac_f32_16x16x64_bf16 v[40:43], v[4:7], a[40:47], v68\n\t" \
  "ds_read_b128 v[20:23], v69 offset:5120\n\t" \
  "s_waitcnt lgkmcnt(5)\n\t" \
  "v_smfmac_f32_16x16x64_bf16 v[36:39], v[8:11], a[80:87], v68\n\t" \
  "ds_read_b128 v[24:27], v69 offset:6144\n\t" \
  "v_smfmac_f32_16x16x64_bf16 v[40:43], v[8:11], a[88:95], v68\n\t" \
  "ds_read_b128 v[28:31], v69 offset:7168\n\t" \
  "s_waitcnt lgkmcnt(6)\n\t" \
  "v_smfmac_f32_16x16x64_bf16 v[36:39], v[12:15], a[120:127], v68\n\t" \
  "ds_read_b128 v[32:35], v69 offset:8192\n\t" \
  "s_waitcnt lgkmcnt(5)\n\t" \
  "v_smfmac_f32_16x16x64_bf16 v[40:43], v[12:15], v[44:51], v68\n\t" \
  "ds_read_b128 v[60:63], v70 offset:6144\n\t" \
  "s_waitcnt lgkmcnt(5)\n\t" \
  "v_smfmac_f32_16x16x64_bf16 v[36:39], v[16:19], a[160:167], v68\n\t" \
  "ds_read_b128 v[64:67], v70 offset:7168\n\t" \
  "v_smfmac_f32_16x16x64_bf16 v[40:43], v[16:19], a[168:175], v68\n\t" \
  "s_waitcnt lgkmcnt(5)\n\t" \
  "v_mov_b64 v[44:45], 0\n\t" \
  "v_smfmac_f32_16x16x64_bf16 v[36:39], v[20:23], a[200:207], v68\n\t" \
  "s_waitcnt lgkmcnt(4)\n\t" \
  "v_mov_b64 v[46:47], 0\n\t" \
  "v_smfmac_f32_16x16x64_bf16 v[36:39], v[24:27], a[240:247], v68\n\t" \
  "s_waitcnt lgkmcnt(2)\n\t" \
  "v_mov_b64 v[48:49], 0\n\t" \
  "v_smfmac_f32_16x16x64_bf16 v[36:39], v[28:31], v[112:119], v68\n\t" \
  "v_mov_b64 v[50:51], 0\n\t" \
  "v_smfmac_f32_16x16x64_bf16 v[36:39], v[32:35], v[160:167], v68\n\t" \
  "s_waitcnt lgkmcnt(0)\n\t" \
  "v_smfmac_f32_16x16x64_bf16 v[40:43], v[20:23], v[60:67], v68\n\t" \
  "ds_read_b128 v[60:63], v70 offset:8192\n\t" \
  "v_smfmac_f32_16x16x64_bf16 v[40:43], v[24:27], a[248:255], v68\n\t" \
  "ds_read_b128 v[64:67], v70 offset:9216\n\t" \
  "v_add_f32 v74, v36, v38\n\t" \
  "v_smfmac_f32_16x16x64_bf16 v[40:43], v[28:31], v[120:127], v68\n\t" \
  "v_add_f32 v37, v37, v39\n\t" \
  "v_smfmac_f32_16x16x64_bf16 v[40:43], v[32:35], v[168:175], v68\n\t" \
  "v_cmp_u_f32 vcc, v74, v37\n\t" \
  CH_DP2 \
  "v_smfmac_f32_16x16x64_bf16 v[44:47], v[0:3], a[0:7], v68\n\t" \
  "s_cbranch_vccnz LFAIL2_%=\n\t" \
  "LGO2_%=:\n\t" \
  "v_cvt_pk_bf16_f32 v74, v74, v37\n\t" \
  "v_smfmac_f32_16x16x64_bf16 v[44:47], v[4:7], a[48:55], v68\n\t" \
  "s_waitcnt lgkmcnt(0)\n\t" \
  "v_add_f32 v75, v40, v42\n\t" \
  "v_smfmac_f32_16x16x64_bf16 v[44:47], v[8:11], v[60:67], v68\n\t" \
  "ds_read_b128 v[60:63], v70 offset:10240\n\t" \
  "v_add_f32 v41, v41, v43\n\t" \
  "v_cvt_pk_bf16_f32 v75, v75, v41\n\t" \
  "v_smfmac_f32_16x16x64_bf16 v[44:47], v[12:15], a[128:135], v68\n\t" \
  "ds_write2st64_b32 v71, v74, v75 offset0:40 offset1:42\n\t" \
  "v_smfmac_f32_16x16x64_bf16 v[44:47], v[16:19], a[176:183], v68\n\t" \
  "ds_read_b128 v[64:67], v70 offset:11264\n\t" \
  "v_smfmac_f32_16x16x64_bf16 v[44:47], v[20:23], a[208:215], v68\n\t" \
  "v_smfmac_f32_16x16x64_bf16 v[44:47], v[24:27], v[80:87], v68\n\t" \
  "v_smfmac_f32_16x16x64_bf16 v[44:47], v[28:31], v[128:135], v68\n\t" \
  "s_waitcnt lgkmcnt(0)\n\t" \
  "v_smfmac_f32_16x16x64_bf16 v[44:47], v[32:35], v[60:67], v68\n\t" \
  "s_barrier\n\t" \
  "ds_read_b64_tr_b16 v[76:77], v72 offset:10240\n\t" \
  CH_DP3 \
  "v_smfmac_f32_16x16x64_bf16 v[48:51], v[0:3], a[8:15], v68\n\t" \
  "ds_read_b64_tr_b16 v[78:79], v72 offset:10752\n\t" \
  "v_smfmac_f32_16x16x64_bf16 v[48:51], v[4:7], a[56:63], v68\n\t" \
  "buffer_load_dwordx4 v[60:63], v69, s[28:31], s64 offen sc1\n\t" \
  "v_add_f32 v74, v44, v46\n\t" \
  "v_smfmac_f32_16x16x64_bf16 v[48:51], v[8:11], a[96:103], v68\n\t" \
  "v_add_f32 v45, v45, v47\n\t" \
  "v_smfmac_f32_16x16x64_bf16 v[48:51], v[12:15], a[136:143], v68\n\t" \
  "buffer_load_dwordx4 v[64:67], v69, s[28:31], s64 offen offset:1024 sc1\n\t" \
  "s_waitcnt vmcnt(4)\n\t" \
  "s_waitcnt lgkmcnt(0)\n\t" \
  "v_cvt_pk_bf16_f32 v74, v74, v45\n\t" \
  "v_mfma_f32_16x16x32_bf16 v[36:39], v[52:55], v[76:79], 0\n\t" \
  "s_waitcnt vmcnt(3)\n\t" \
  "v_mfma_f32_16x16x32_bf16 v[40:43], v[56:59], v[76:79], 0\n\t" \
  "v_mov_b64 v[52:53], 0\n\t" \
  "v_mov_b64 v[54:55], 0\n\t" \
  "v_smfmac_f32_16x16x64_bf16 v[48:51], v[16:19], a[184:191], v68\n\t" \
  "v_mov_b64 v[56:57], 0\n\t" \
  "v_mov_b64 v[58:59], 0\n\t" \
  "v_smfmac_f32_16x16x64_bf16 v[48:51], v[20:23], a[216:223], v68\n\t" \
  "ds_write_b32 v71, v74 offset:1024\n\t" \
  "v_add_f32_dpp v36, v40, v36 quad_perm:[2,3,0,1] row_mask:0xf bank_mask:0xf\n\t" \
  "v_smfmac_f32_16x16x64_bf16 v[48:51], v[24:27], v[88:95], v68\n\t" \
  "v_add_f32_dpp v37, v41, v37 quad_perm:[2,3,0,1] row_mask:0xf bank_mask:0xf\n\t" \
  "v_smfmac_f32_16x16x64_bf16 v[48:51], v[28:31], v[136:143], v68\n\t" \
  "v_add_f32_dpp v38, v42, v38 quad_perm:[2,3,0,1] row_mask:0xf bank_mask:0xf\n\t" \
  "v_smfmac_f32_16x16x64_bf16 v[48:51], v[32:35], v[176:183], v68\n\t" \
  "v_add_f32_dpp v39, v43, v39 quad_perm:[2,3,0,1] row_mask:0xf bank_mask:0xf\n\t" \
  CH_DP4 \
  "v_smfmac_f32_16x16x64_bf16 v[52:55], v[0:3], a[16:23], v68\n\t" \
  "s_mov_b64 exec, s[58:59]\n\t" \
  "ds_write_b128 v69, v[36:39] offset:3072\n\t" \
  "s_mov_b64 exec, -1\n\t" \
  "v_smfmac_f32_16x16x64_bf16 v[52:55], v[4:7], a[64:71], v68\n\t" \
  "v_add_f32 v75, v48, v50\n\t" \
  "v_add_f32 v49, v49, v51\n\t" \
  "v_cvt_pk_bf16_f32 v75, v75, v49\n\t" \
  "v_smfmac_f32_16x16x64_bf16 v[56:59], v[0:3], a[24:31], v68\n\t" \
  "ds_write_b32 v71, v75 offset:1536\n\t" \
  "v_smfmac_f32_16x16x64_bf16 v[56:59], v[4:7], a[72:79], v68\n\t" \
  "ds_read_b64_tr_b16 v[0:1], v72 offset:9216\n\t" \
  "ds_read_b64_tr_b16 v[2:3], v72 offset:9728\n\t" \
  "v_smfmac_f32_16x16x64_bf16 v[52:55], v[8:11], a[104:111], v68\n\t" \
  "ds_read_b128 v[36:39], v70 offset:12288\n\t" \
  "v_smfmac_f32_16x16x64_bf16 v[56:59], v[8:11], a[112:119], v68\n\t" \
  "ds_read_b128 v[40:43], v70 offset:13312\n\t" \
  "v_smfmac_f32_16x16x64_bf16 v[52:55], v[12:15], a[144:151], v68\n\t" \
  "s_waitcnt lgkmcnt(4)\n\t" \
  "s_barrier\n\t" \
  CH_DP5 \
  "ds_read_b64_tr_b16 v[44:45], v72 offset:1024\n\t" \
  "ds_read_b64_tr_b16 v[46:47], v72 offset:1536\n\t" \
  "v_smfmac_f32_16x16x64_bf16 v[56:59], v[12:15], a[152:159], v68\n\t" \
  "ds_read_b64_tr_b16 v[48:49], v72 offset:0\n\t" \
  "ds_read_b64_tr_b16 v[50:51], v72 offset:512\n\t" \
  "v_smfmac_f32_16x16x64_bf16 v[52:55], v[16:19], a[192:199], v68\n\t" \
  "ds_read_b128 v[4:7], v69 offset:2048\n\t" \
  "s_waitcnt lgkmcnt(5)\n\t" \
  "v_smfmac_f32_16x16x64_bf16 v[56:59], v[16:19], v[36:43], v68\n\t" \
  "ds_read_b128 v[8:11], v69 offset:3072\n\t" \
  "s_waitcnt lgkmcnt(4)\n\t" \
  "v_mfma_f32_16x16x32_bf16 v[12:15], v[44:47], v[76:79], 0\n\t" \
  "ds_read_b128 v[36:39], v70 offset:14336\n\t" \
  "s_waitcnt lgkmcnt(3)\n\t" \
  "v_mfma_f32_16x16x32_bf16 v[12:15], v[48:51], v[0:3], v[12:15]\n\t" \
  "ds_read_b128 v[40:43], v70 offset:15360\n\t" \
  "v_smfmac_f32_16x16x64_bf16 v[52:55], v[20:23], a[224:231], v68\n\t" \
  "buffer_store_dwordx4 v[44:47], v69, s[28:31], s69 offen offset:1024 sc1\n\t" \
  "s_waitcnt lgkmcnt(2)\n\t" \
  "v_add_f32 v4, v4, v8\n\t" \
  "v_add_f32 v5, v5, v9\n\t" \
  "v_add_f32 v6, v6, v10\n\t" \
  CH_DP6 \
  "v_cndmask_b32 v78, v13, v14, s[56:57]\n\t" \
  "v_cndmask_b32 v76, v12, v15, s[56:57]\n\t" \
  "v_add_f32 v7, v7, v11\n\t" \
  "v_max3_f32 v77, v4, v5, v6\n\t" \
  "v_add_f32_dpp v76, v78, v76 quad_perm:[2,3,0,1] row_mask:0xf bank_mask:0xf\n\t" \
  "v_max3_f32 v77, v77, v7, v76\n\t" \
  "v_smfmac_f32_16x16x64_bf16 v[56:59], v[20:23], a[232:239], v68\n\t" \
  "ds_read_b128 v[44:47], v70 offset:16384\n\t" \
  "v_max_f32_dpp v77, v77, v77 quad_perm:[2,3,0,1] row_mask:0xf bank_mask:0xf\n\t" \
  "v_mov_b32 v78, v77\n\t" \
  "v_smfmac_f32_16x16x64_bf16 v[52:55], v[24:27], v[96:103], v68\n\t" \
  "ds_read_b128 v[48:51], v70 offset:17408\n\t" \
  "v_permlane16_swap_b32 v77, v78\n\t" \
  "v_max_f32 v77, v77, v78\n\t" \
  "v_mov_b32 v78, v77\n\t" \
  "v_smfmac_f32_16x16x64_bf16 v[52:55], v[28:31], v[144:151], v68\n\t" \
  "ds_read_b128 v[16:19], v70 offset:18432\n\t" \
  "v_permlane32_swap_b32 v77, v78\n\t" \
  "v_max_f32 v77, v77, v78\n\t" \
  "v_smfmac_f32_16x16x64_bf16 v[52:55], v[32:35], v[184:191], v68\n\t" \
  "ds_read_b128 v[20:23], v70 offset:19456\n\t" \
  "v_mul_f32 v77, 0xbe0293ee, v77\n\t" \
  CH_DP7 \
  "v_smfmac_f32_16x16x64_bf16 v[56:59], v[24:27], v[104:111], v68\n\t" \
  "v_fmamk_f32 v4, v4, 0x3e0293ee, v77\n\t" \
  "v_smfmac_f32_16x16x64_bf16 v[56:59], v[28:31], v[152:159], v68\n\t" \
  "ds_read_b128 v[24:27], v70 offset:22528\n\t" \
  "s_waitcnt lgkmcnt(4)\n\t" \
  "v_fmamk_f32 v5, v5, 0x3e0293ee, v77\n\t" \
  "v_smfmac_f32_16x16x64_bf16 v[56:59], v[32:35], v[36:43], v68\n\t" \
  "ds_read_b128 v[28:31], v70 offset:23552\n\t" \
  "v_fmamk_f32 v6, v6, 0x3e0293ee, v77\n\t" \
  "v_fmamk_f32 v7, v7, 0x3e0293ee, v77\n\t" \
  "v_fmamk_f32 v76, v76, 0x3e0293ee, v77\n\t" \
  "v_exp_f32 v4, v4\n\t" \
  "v_exp_f32 v5, v5\n\t" \
  "v_exp_f32 v6, v6\n\t" \
  "v_exp_f32 v7, v7\n\t" \
  "v_cvt_pk_bf16_f32 v4, v4, v5\n\t" \
  "v_exp_f32 v76, v76\n\t" \
  "v_cvt_pk_bf16_f32 v5, v6, v7\n\t" \
  "v_mov_b32_dpp v12, v4 quad_perm:[0,1,0,1] row_mask:0xf bank_mask:0xf\n\t" \
  "v_mov_b32_dpp v14, v4 quad_perm:[2,3,2,3] row_mask:0xf bank_mask:0xf\n\t" \
  "v_mov_b32_dpp v13, v5 quad_perm:[0,1,0,1] row_mask:0xf bank_mask:0xf\n\t" \
  "v_mov_b32_dpp v15, v5 quad_perm:[2,3,2,3] row_mask:0xf bank_mask:0xf\n\t" \
  "v_add_f32 v52, v52, v54\n\t" \
  "v_add_f32 v53, v53, v55\n\t" \
  "s_waitcnt vmcnt(2)\n\t" \
  CH_DP8 \
  "v_mfma_f32_16x16x32_bf16 v[0:3], v[60:63], v[12:15], 0\n\t" \
  "ds_read_b128 v[36:39], v70 offset:20480\n\t" \
  "v_add_f32 v56, v56, v58\n\t" \
  "v_add_f32 v57, v57, v59\n\t" \
  "v_cvt_pk_bf16_f32 v52, v52, v53\n\t" \
  "v_cvt_pk_bf16_f32 v53, v56, v57\n\t" \
  "s_waitcnt vmcnt(1)\n\t" \
  "v_mfma_f32_16x16x32_bf16 v[4:7], v[64:67], v[12:15], 0\n\t" \
  "ds_read_b128 v[40:43], v70 offset:21504\n\t" \
  "v_mov_b64 v[56:57], s[88:89]\n\t" \
  "v_mov_b64 v[58:59], s[88:89]\n\t" \
  "v_mov_b32_dpp v78, v76 quad_perm:[0,1,0,1] row_mask:0xf bank_mask:0xf\n\t" \
  "v_mov_b32_dpp v79, v76 quad_perm:[2,3,2,3] row_mask:0xf bank_mask:0xf\n\t" \
  "v_mfma_f32_16x16x32_bf16 v[32:35], v[56:59], v[12:15], 0\n\t" \
  "ds_read_b128 v[60:63], v70 offset:24576\n\t" \
  "v_cvt_pk_bf16_f32 v11, v78, v79\n\t" \
  "v_cvt_pk_bf16_f32 v8, v78, v79\n\t" \
  "v_mov_b64 v[54:55], 0\n\t" \
  "v_mov_b32 v9, 0\n\t" \
  "v_mov_b32 v10, 0\n\t" \
  "v_mov_b64 v[14:15], 0\n\t" \
  "s_nop 0\n\t" \
  "v_mfma_f32_16x16x32_bf16 v[32:35], v[54:57], v[8:11], v[32:35]\n\t" \
  "buffer_store_dwordx2 v[52:53], v71, s[28:31], s69 offen offset:3072 sc1\n\t" \
  "s_cmp_eq_u32 s48, 63\n\t" \
  "v_mfma_f32_16x16x32_bf16 v[0:3], v[52:55], v[8:11], v[0:3]\n\t" \
  "ds_read_b128 v[64:67], v70 offset:25600\n\t" \
  "v_mov_b64 v[56:57], 0\n\t" \
  "v_mov_b64 v[58:59], 0\n\t" \
  "v_mfma_f32_16x16x32_bf16 v[4:7], v[52:55], v[10:13], v[4:7]\n\t" \
  "s_cbranch_scc1 LLAST2_%=\n\t" \
  "v_rcp_f32 v74, v32\n\t" \
  "v_mov_b64 v[12:13], 0\n\t" \
  "v_mov_b32 v8, 0\n\t" \
  "v_mul_f32 v0, v0, v74\n\t" \
  "v_mul_f32 v1, v1, v74\n\t" \
  "v_mul_f32 v2, v2, v74\n\t" \
  "v_mul_f32 v3, v3, v74\n\t" \
  "v_cvt_pk_bf16_f32 v76, v0, v1\n\t" \
  "v_cvt_pk_bf16_f32 v77, v2, v3\n\t" \
  "v_pk_mul_f32 v[4:5], v[4:5], v[74:75] op_sel_hi:[1,0]\n\t" \
  "v_pk_mul_f32 v[6:7], v[6:7], v[74:75] op_sel_hi:[1,0]\n\t" \
  "v_cvt_pk_bf16_f32 v78, v4, v5\n\t" \
  "v_cvt_pk_bf16_f32 v79, v6, v7\n\t" \
  "ds_write_b128 v69, v[76:79] offset:6144\n\t" \
  CH_DP9 \
  "s_waitcnt lgkmcnt(2)\n\t" \
  "v_smfmac_f32_16x16x64_bf16 v[12:15], v[76:79], v[44:51], v68\n\t" \
  "ds_read_b128 v[44:47], v70 offset:26624\n\t" \
  "v_mov_b32 v11, 0\n\t" \
  "v_smfmac_f32_16x16x64_bf16 v[56:59], v[76:79], v[16:23], v68\n\t" \
  "ds_read_b128 v[48:51], v70 offset:27648\n\t" \
  "v_mov_b64 v[52:53], 0\n\t" \
  "v_smfmac_f32_16x16x64_bf16 v[8:11], v[76:79], v[36:43], v68\n\t" \
  "ds_read_b128 v[16:19], v70 offset:28672\n\t" \
  "v_smfmac_f32_16x16x64_bf16 v[52:55], v[76:79], v[24:31], v68\n\t" \
  "s_waitcnt lgkmcnt(3)\n\t" \
  "s_barrier\n\t" \
  CH_DP10 \
  "ds_read_b128 v[0:3], v69 offset:7168\n\t" \
  "ds_read_b128 v[4:7], v69 offset:4096\n\t" \
  "v_lshrrev_b32 v71, 7, v69\n\t" \
  "v_and_b32 v74, 0x70, v69\n\t" \
  "v_lshl_add_u32 v71, v71, 14, v74\n\t" \
  "v_bfe_u32 v72, v69, 2, 7\n\t" \
  "v_and_b32 v74, 0x200, v69\n\t" \
  "s_waitcnt lgkmcnt(1)\n\t" \
  "v_smfmac_f32_16x16x64_bf16 v[8:11], v[0:3], v[60:67], v68\n\t" \
  "ds_read_b128 v[20:23], v70 offset:29696\n\t" \
  "v_mov_b64 v[76:77], 0\n\t" \
  "v_smfmac_f32_16x16x64_bf16 v[12:15], v[0:3], v[200:207], v68\n\t" \
  "buffer_load_dword v75, off, s[28:31], s80 sc1\n\t" \
  "ds_read_b128 v[32:35], v69 offset:5120\n\t" \
  "v_mov_b64 v[78:79], 0\n\t" \
  "v_smfmac_f32_16x16x64_bf16 v[52:55], v[0:3], v[44:51], v68\n\t" \
  "ds_read_b128 v[24:27], v70 offset:30720\n\t" \
  "v_lshl_add_u32 v72, v74, 3, v72\n\t" \
  "v_smfmac_f32_16x16x64_bf16 v[56:59], v[0:3], v[232:239], v68\n\t" \
  "ds_read_b128 v[28:31], v70 offset:31744\n\t" \
  "s_waitcnt lgkmcnt(4)\n\t" \
  "v_smfmac_f32_16x16x64_bf16 v[12:15], v[4:7], v[208:215], v68\n\t" \
  "ds_read_b128 v[60:63], v70 offset:32768\n\t" \
  "s_waitcnt lgkmcnt(4)\n\t" \
  "v_smfmac_f32_16x16x64_bf16 v[8:11], v[4:7], v[16:23], v68\n\t" \
  "ds_read_b128 v[64:67], v70 offset:33792\n\t" \
  "s_waitcnt lgkmcnt(4)\n\t" \
  "v_smfmac_f32_16x16x64_bf16 v[8:11], v[32:35], v[192:199], v68\n\t" \
  "ds_read_b128 v[36:39], v73 offset:4096\n\t" \
  "v_smfmac_f32_16x16x64_bf16 v[12:15], v[32:35], v[216:223], v68\n\t" \
  "ds_read_b128 v[40:43], v73 offset:6144\n\t" \
  "s_waitcnt lgkmcnt(4)\n\t" \
  "v_smfmac_f32_16x16x64_bf16 v[52:55], v[4:7], v[24:31], v68\n\t" \
  "ds_read_b128 v[44:47], v70 offset:34816\n\t" \
  "v_cvt_pk_bf16_f32 v8, v8, v9\n\t" \
  "v_smfmac_f32_16x16x64_bf16 v[52:55], v[32:35], v[224:231], v68\n\t" \
  "ds_read_b128 v[48:51], v70 offset:35840\n\t" \
  "v_cvt_pk_bf16_f32 v9, v10, v11\n\t" \
  "v_smfmac_f32_16x16x64_bf16 v[56:59], v[4:7], v[240:247], v68\n\t" \
  "s_waitcnt lgkmcnt(4)\n\t" \
  "v_cvt_pk_bf16_f32 v10, v12, v13\n\t" \
  "v_smfmac_f32_16x16x64_bf16 v[56:59], v[32:35], v[60:67], v68\n\t" \
  "s_waitcnt lgkmcnt(3)\n\t" \
  "v_cvt_pk_bf16_f32 v11, v14, v15\n\t" \
  "v_smfmac_f32_16x16x64_bf16 v[76:79], v[36:39], v[248:255], v68\n\t" \
  "buffer_store_dwordx4 v[8:11], v71, s[28:31], s54 offen sc1\n\t" \
  "s_waitcnt lgkmcnt(0)\n\t" \
  CH_DP11 \
  "v_cvt_pk_bf16_f32 v52, v52, v53\n\t" \
  "v_smfmac_f32_16x16x64_bf16 v[76:79], v[40:43], v[44:51], v68\n\t" \
  "v_cvt_pk_bf16_f32 v53, v54, v55\n\t" \
  "v_cvt_pk_bf16_f32 v54, v56, v57\n\t" \
  "v_cvt_pk_bf16_f32 v55, v58, v59\n\t" \
  "buffer_store_dwordx4 v[52:55], v71, s[28:31], s75 offen sc1\n\t" \
  "s_nop 2\n\t" \
  "v_add_f32 v76, v76, v78\n\t" \
  "v_add_f32 v77, v77, v79\n\t" \
  "v_cvt_pk_bf16_f32 v76, v76, v77\n\t" \
  "buffer_store_dword v76, v72, s[28:31], s55 offen sc1\n\t" \
  CH_DP12 \
  "s_memrealtime s[70:71]\n\t" \
  "s_waitcnt vmcnt(3)\n\t" \
  "v_readfirstlane_b32 s65, v75\n\t" \
  "s_barrier\n\t" \
  "s_waitcnt lgkmcnt(0)\n\t" \
  "s_not_b32 s65, s65\n\t" \
  "s_lshl_b32 s61, s70, 8\n\t" \
  "s_andn2_b32 s64, s65, 0xff\n\t" \
  "s_sub_u32 s64, s64, s61\n\t" \
  "s_ashr_i32 s64, s64, 8\n\t" \
  "s_cmp_eq_u32 s65, 0\n\t" \
  "s_cselect_b32 s64, " CH_DLF ", s64\n\t" \
  "s_min_i32 s64, s64, s34\n\t" \
  "s_max_i32 s64, s64, " CH_DLL1 "\n\t" \
  "s_add_u32 s64, s64, s70\n\t" \
  CH_ST2 \
  "s_mov_b32 s74, 0\n\t" \
  "s_movk_i32 s60, 8192\n\t" \
  "RWT2_%=:\n\t" \
  "s_memrealtime s[72:73]\n\t" \
  "s_sub_u32 s60, s60, 1\n\t" \
  "s_cselect_b32 s61, 0, -1\n\t" \
  "s_waitcnt lgkmcnt(0)\n\t" \
  "s_sub_i32 s73, s72, s64\n\t" \
  "s_and_b32 s73, s73, s61\n\t" \
  "s_cmp_lt_i32 s73, " CH_FIN "\n\t" \
  "s_cbranch_scc1 RWT2_%=\n\t" \
  "s_cmp_lt_i32 s73, 0\n\t" \
  "s_cbranch_scc1 RWT2_%=f\n\t" \
  "RWT2_%=g:\n\t" \
  "RRETRY2_%=:\n\t" \
  CH_DP13 \
  "buffer_load_dwordx4 v[0:3], v69, s[28:31], s97 offen offset:0 sc1\n\t" \
  "buffer_load_dwordx4 v[4:7], v69, s[28:31], s97 offen offset:1024 sc1\n\t" \
  "buffer_load_dwordx4 v[8:11], v69, s[28:31], s97 offen offset:2048 sc1\n\t" \
  "buffer_load_dwordx4 v[12:15], v69, s[28:31], s97 offen offset:3072 sc1\n\t" \
  "v_lshrrev_b32 v20, 2, v69\n\t" \
  "v_and_b32 v20, 16, v20\n\t" \
  "v_mov_b32 v21, 0x3f80\n\t" \
  "v_lshlrev_b32 v21, v20, v21\n\t" \
  "v_bfe_u32 v20, v69, 4, 2\n\t" \
  "v_cmp_eq_u32 vcc, 0, v20\n\t" \
  "s_nop 1\n\t" \
  "v_cndmask_b32 v32, 0, v21, vcc\n\t" \
  "v_cmp_eq_u32 vcc, 1, v20\n\t" \
  "s_nop 1\n\t" \
  "v_cndmask_b32 v33, 0, v21, vcc\n\t" \
  "v_cmp_eq_u32 vcc, 2, v20\n\t" \
  "s_nop 1\n\t" \
  "v_cndmask_b32 v34, 0, v21, vcc\n\t" \
  "v_cmp_eq_u32 vcc, 3, v20\n\t" \
  "s_nop 1\n\t" \
  "v_cndmask_b32 v35, 0, v21, vcc\n\t" \
  "v_bfe_u32 v38, v69, 7, 1\n\t" \
  "v_bfe_u32 v20, v69, 9, 1\n\t" \
  "v_lshl_add_u32 v38, v20, 2, v38\n\t" \
  "v_lshlrev_b32 v38, 12, v38\n\t" \
  "v_and_b32 v20, 0x60, v69\n\t" \
  "v_add_u32 v38, v38, v20\n\t" \
  "v_bfe_u32 v20, v69, 8, 1\n\t" \
  "v_lshl_add_u32 v38, v20, 4, v38\n\t" \
  "v_bfe_u32 v20, v69, 4, 1\n\t" \
  "v_lshl_add_u32 v38, v20, 3, v38\n\t" \
  "v_mov_b32 v44, 0\n\t" \
  CH_DP17 \
  "s_waitcnt vmcnt(3)\n\t" \
  CH_DP14 \
  "v_mfma_f32_16x16x32_bf16 v[40:43], v[32:35], v[0:3], 0\n\t" \
  "v_max3_u32 v44, v44, v0, v1\n\t" \
  "v_max3_u32 v44, v44, v2, v3\n\t" \
  "s_waitcnt vmcnt(2)\n\t" \
  "v_mfma_f32_16x16x32_bf16 v[40:43], v[32:35], v[4:7], v[40:43]\n\t" \
  "v_max3_u32 v44, v44, v4, v5\n\t" \
  "v_max3_u32 v44, v44, v6, v7\n\t" \
  "s_waitcnt vmcnt(1)\n\t" \
  "v_mfma_f32_16x16x32_bf16 v[40:43], v[32:35], v[8:11], v[40:43]\n\t" \
  "v_max3_u32 v44, v44, v8, v9\n\t" \
  "v_max3_u32 v44, v44, v10, v11\n\t" \
  "s_waitcnt vmcnt(0)\n\t" \
  CH_DP15 \
  "v_mfma_f32_16x16x32_bf16 v[40:43], v[32:35], v[12:15], v[40:43]\n\t" \
  "v_max3_u32 v44, v44, v12, v13\n\t" \
  "v_max3_u32 v44, v44, v14, v15\n\t" \
  "v_cmp_eq_u32 vcc, -1, v44\n\t" \
  "s_cbranch_vccnz RFAIL2_%=\n\t" \
  "s_nop 3\n\t" \
  "v_add_f32_dpp v40, v40, v40 row_ror:8 row_mask:0xf bank_mask:0xf\n\t" \
  "v_add_f32_dpp v41, v41, v41 row_ror:8 row_mask:0xf bank_mask:0xf\n\t" \
  "v_add_f32_dpp v42, v42, v42 row_ror:8 row_mask:0xf bank_mask:0xf\n\t" \
  "v_add_f32_dpp v43, v43, v43 row_ror:8 row_mask:0xf bank_mask:0xf\n\t" \
  "v_cvt_pk_bf16_f32 v40, v40, v41\n\t" \
  "v_cvt_pk_bf16_f32 v41, v42, v43\n\t" \
  "buffer_store_dwordx2 v[40:41], v38, s[28:31], s98 offen sc1\n\t" \
  "RDONE2_%=:\n\t" \
  CH_DP16 \
  CH_ST4 \
  "s_barrier\n\t" \
  CH_ST3 \
  CH_TRC \
  "s_add_u32 s48, s48, 1\n\t" \
  "s_mov_b32 s49, s53\n\t" \
  "s_add_u32 s53, s53, " CH_SLOT "\n\t" \
  "s_add_u32 s60, s53, " CH_SKIP "\n\t" \
  "s_lshr_b32 s61, s96, 1\n\t" \
  "s_add_u32 s86, s86, 1\n\t" \
  "s_cmp_eq_u32 s86, 3\n\t" \
  "s_cselect_b32 s53, s60, s53\n\t" \
  "s_cselect_b32 s96, s61, s96\n\t" \
  "s_cselect_b32 s86, 0, s86\n\t" \
  "s_andn2_b32 s53, s53, 0x2000\n\t" \
  "s_and_b32 s60, s96, 1\n\t" \
  "s_lshl_b32 s60, s60, 13\n\t" \
  "s_or_b32 s53, s53, s60\n\t" \
  "s_mov_b32 s68, 0\n\t" \
  "s_add_u32 s97, s41, s53\n\t" \
  "s_add_u32 s98, s37, s53\n\t" \
  "s_add_u32 s82, s48, 0\n\t" \
  CH_SEGC \
  "s_lshr_b64 s[60:61], s[90:91], s82\n\t" \
  "s_and_b32 s60, s60, 1\n\t" \
  "s_lshl_b32 s60, s60, 13\n\t" \
  "s_add_u32 s82, s82, " CH_SG0 "\n\t" \
  "s_lshl_b32 s82, s82, 21\n\t" \
  "s_or_b32 s82, s82, s60\n\t" \
  "s_add_u32 s50, s82, s38\n\t" \
  "s_add_u32 s69, s82, s39\n\t" \
  "s_cmp_eq_u32 s34, 0\n\t" \
  "s_cbranch_scc1 LINI_%=\n\t" \
  "s_branch LG2_%=\n\t" \
  "RFAIL2_%=:\n\t" \
  "s_add_u32 s74, s74, 1\n\t" \
  "s_sleep " CH_RSL "\n\t" \
  "s_waitcnt vmcnt(0)\n\t" \
  "s_cmp_gt_u32 s74, s84\n\t" \
  "s_cbranch_scc1 RG2_%=\n\t" \
  "s_and_b32 s60, s74, 1023\n\t" \
  "s_cmp_lg_u32 s60, 0\n\t" \
  "s_cbranch_scc1 RRETRY2_%=\n\t" \
  "buffer_load_dword v46, off, s[28:31], s92 sc1\n\t" \
  "s_waitcnt vmcnt(0)\n\t" \
  "v_readfirstlane_b32 s60, v46\n\t" \
  "s_nop 0\n\t" \
  "s_cmp_eq_u32 s60, 0\n\t" \
  "s_cbranch_scc1 RRETRY2_%=\n\t" \
  "RG2_%=:\n\t" \
  "s_mov_b32 s84, 0\n\t" \
  "s_lshl_b32 s60, s36, 4\n\t" \
  "s_or_b32 s60, s60, s48\n\t" \
  "s_or_b32 s60, s60, 536870912\n\t" \
  "v_mov_b32 v76, s60\n\t" \
  "v_mov_b32 v77, 0\n\t" \
  "s_mov_b64 exec, 1\n\t" \
  "buffer_atomic_cmpswap v[76:77], off, s[28:31], s92 sc1\n\t" \
  "s_mov_b64 exec, -1\n\t" \
  "s_branch RDONE2_%=\n\t" \
  "LFAIL2_%=:\n\t" \
  "v_max_u32 v75, v0, v1\n\t" \
  "v_max3_u32 v75, v75, v2, v3\n\t" \
  "v_max3_u32 v75, v75, v4, v5\n\t" \
  "v_max3_u32 v75, v75, v6, v7\n\t" \
  "v_max3_u32 v75, v75, v8, v9\n\t" \
  "v_max3_u32 v75, v75, v10, v11\n\t" \
  "v_max3_u32 v75, v75, v12, v13\n\t" \
  "v_max3_u32 v75, v75, v14, v15\n\t" \
  "v_max3_u32 v75, v75, v16, v17\n\t" \
  "v_max3_u32 v75, v75, v18, v19\n\t" \
  "v_max3_u32 v75, v75, v20, v21\n\t" \
  "v_max3_u32 v75, v75, v22, v23\n\t" \
  "v_max3_u32 v75, v75, v24, v25\n\t" \
  "v_max3_u32 v75, v75, v26, v27\n\t" \
  "v_max3_u32 v75, v75, v28, v29\n\t" \
  "v_max3_u32 v75, v75, v30, v31\n\t" \
  "v_max3_u32 v75, v75, v32, v33\n\t" \
  "v_max3_u32 v75, v75, v34, v35\n\t" \
  "v_cmp_eq_u32 vcc, -1, v75\n\t" \
  "s_cbranch_vccz LGO2_%=\n\t" \
  "s_add_u32 s68, s68, 1\n\t" \
  "s_waitcnt lgkmcnt(0)\n\t" \
  "s_cmp_gt_u32 s68, s83\n\t" \
  "s_cbranch_scc1 LGV2_%=\n\t" \
  "s_barrier\n\t" \
  "s_waitcnt vmcnt(8)\n\t" \
  "s_branch LG2_%=\n\t" \
  "RWT2_%=f:\n\t" \
  "s_cmp_lt_i32 s73, -2\n\t" \
  "s_cbranch_scc0 RWT2_%=g\n\t" \
  "s_nop 4\n\t" \
  "s_branch RWT2_%=g\n\t" \
  "LGV2_%=:\n\t" \
  "s_mov_b32 s83, 0\n\t" \
  "s_lshl_b32 s60, s36, 4\n\t" \
  "s_or_b32 s60, s60, s48\n\t" \
  "s_or_b32 s60, s60, 805306368\n\t" \
  "v_mov_b32 v76, s60\n\t" \
  "v_mov_b32 v77, 0\n\t" \
  "s_mov_b64 exec, 1\n\t" \
  "buffer_atomic_cmpswap v[76:77], off, s[28:31], s92 sc1\n\t" \
  "s_mov_b64 exec, -1\n\t" \
  "s_branch LGO2_%=\n\t" \
  "LLAST2_%=:\n\t" \
  "s_waitcnt vmcnt(0) lgkmcnt(0)\n\t" \
  "s_barrier\n\t" \
  "s_branch LEND_%=\n\t" \
  ".p2align 6\n\t" \
  "LG3_%=:\n\t" \
  "s_barrier\n\t" \
  "buffer_load_dwordx4 v[52:55], v69, s[28:31], s50 offen sc1\n\t" \
  "buffer_load_dwordx4 v[56:59], v69, s[28:31], s50 offen offset:1024 sc1\n\t" \
  "s_add_u32 s64, s50, 16384\n\t" \
  "s_movk_i32 m0, 11264\n\t" \
  "v_lshlrev_b32 v75, 1, v69\n\t" \
  "buffer_load_dword v75, s[28:31], s64 offen sc1 lds\n\t" \
  "s_add_u32 s47, s81, " CH_DL3 "\n\t" \
  "s_lshl_b32 s47, s47, 8\n\t" \
  "s_or_b32 s47, s47, " CH_B0 "\n\t" \
  "s_cmp_lt_u32 s48, " CH_ES "\n\t" \
  "s_cselect_b32 s60, " CH_EX ", 0\n\t" \
  "s_add_u32 s47, s47, s60\n\t" \
  "s_not_b32 s47, s47\n\t" \
  "s_add_u32 s71, s53, s46\n\t" \
  "ds_read_b128 v[44:47], v70 offset:0\n\t" \
  "ds_read_b128 v[48:51], v70 offset:1024\n\t" \
  "ds_read_b128 v[60:63], v70 offset:2048\n\t" \
  "ds_read_b128 v[64:67], v70 offset:3072\n\t" \
  "v_mov_b64 v[36:37], 0\n\t" \
  "v_mov_b64 v[38:39], 0\n\t" \
  "v_mov_b64 v[40:41], 0\n\t" \
  "v_mov_b64 v[42:43], 0\n\t" \
  "v_lshrrev_b32 v71, 1, v69\n\t" \
  "v_add_u32 v71, 4, v71\n\t" \
  "v_lshrrev_b32 v72, 3, v69\n\t" \
  "v_and_b32 v72, 0x78, v72\n\t" \
  "v_and_b32 v74, 0x30, v69\n\t" \
  "v_lshl_add_u32 v72, v74, 3, v72\n\t" \
  "s_add_u32 s54, s53, s36\n\t" \
  "s_add_u32 s55, s54, 573440\n\t" \
  "s_add_u32 s54, s54, 135168\n\t" \
  "s_add_u32 s75, s54, 262144\n\t" \
  "s_add_u32 s80, s53, s26\n\t" \
  "s_or_b32 s87, s50, 0x1fff80\n\t" \
  "s_add_u32 s87, s87, 4194304\n\t" \
  "s_barrier\n\t" \
  CH_DP0 \
  "ds_read_b128 v[0:3], v69 offset:0\n\t" \
  "ds_read_b128 v[4:7], v69 offset:1024\n\t" \
  "ds_read_b128 v[8:11], v69 offset:2048\n\t" \
  "ds_read_b128 v[12:15], v69 offset:3072\n\t" \
  "s_waitcnt lgkmcnt(3)\n\t" \
  "v_smfmac_f32_16x16x64_bf16 v[36:39], v[0:3], v[44:51], v68\n\t" \
  "ds_read_b128 v[44:47], v70 offset:4096\n\t" \
  "v_smfmac_f32_16x16x64_bf16 v[40:43], v[0:3], v[60:67], v68\n\t" \
  "ds_read_b128 v[48:51], v70 offset:5120\n\t" \
  "s_waitcnt lgkmcnt(4)\n\t" \
  "v_smfmac_f32_16x16x64_bf16 v[36:39], v[4:7], a[32:39], v68\n\t" \
  "s_barrier\n\t" \
  "ds_read_b128 v[16:19], v69 offset:4096\n\t" \
  CH_DP1 \
  "v_smfmac_f32_16x16x64_bf16 v[40:43], v[4:7], a[40:47], v68\n\t" \
  "ds_read_b128 v[20:23], v69 offset:5120\n\t" \
  "s_waitcnt lgkmcnt(5)\n\t" \
  "v_smfmac_f32_16x16x64_bf16 v[36:39], v[8:11], a[80:87], v68\n\t" \
  "ds_read_b128 v[24:27], v69 offset:6144\n\t" \
  "v_smfmac_f32_16x16x64_bf16 v[40:43], v[8:11], a[88:95], v68\n\t" \
  "ds_read_b128 v[28:31], v69 offset:7168\n\t" \
  "s_waitcnt lgkmcnt(6)\n\t" \
  "v_smfmac_f32_16x16x64_bf16 v[36:39], v[12:15], a[120:127], v68\n\t" \
  "ds_read_b128 v[32:35], v69 offset:8192\n\t" \
  "s_waitcnt lgkmcnt(5)\n\t" \
  "v_smfmac_f32_16x16x64_bf16 v[40:43], v[12:15], v[44:51], v68\n\t" \
  "ds_read_b128 v[60:63], v70 offset:6144\n\t" \
  "s_waitcnt lgkmcnt(5)\n\t" \
  "v_smfmac_f32_16x16x64_bf16 v[36:39], v[16:19], a[160:167], v68\n\t" \
  "ds_read_b128 v[64:67], v70 offset:7168\n\t" \
  "v_smfmac_f32_16x16x64_bf16 v[40:43], v[16:19], a[168:175], v68\n\t" \
  "s_waitcnt lgkmcnt(5)\n\t" \
  "v_mov_b64 v[44:45], 0\n\t" \
  "v_smfmac_f32_16x16x64_bf16 v[36:39], v[20:23], a[200:207], v68\n\t" \
  "s_waitcnt lgkmcnt(4)\n\t" \
  "v_mov_b64 v[46:47], 0\n\t" \
  "v_smfmac_f32_16x16x64_bf16 v[36:39], v[24:27], a[240:247], v68\n\t" \
  "s_waitcnt lgkmcnt(2)\n\t" \
  "v_mov_b64 v[48:49], 0\n\t" \
  "v_smfmac_f32_16x16x64_bf16 v[36:39], v[28:31], v[112:119], v68\n\t" \
  "v_mov_b64 v[50:51], 0\n\t" \
  "v_smfmac_f32_16x16x64_bf16 v[36:39], v[32:35], v[160:167], v68\n\t" \
  "s_waitcnt lgkmcnt(0)\n\t" \
  "v_smfmac_f32_16x16x64_bf16 v[40:43], v[20:23], v[60:67], v68\n\t" \
  "ds_read_b128 v[60:63], v70 offset:8192\n\t" \
  "v_smfmac_f32_16x16x64_bf16 v[40:43], v[24:27], a[248:255], v68\n\t" \
  "ds_read_b128 v[64:67], v70 offset:9216\n\t" \
  "v_add_f32 v74, v36, v38\n\t" \
  "v_smfmac_f32_16x16x64_bf16 v[40:43], v[28:31], v[120:127], v68\n\t" \
  "v_add_f32 v37, v37, v39\n\t" \
  "v_smfmac_f32_16x16x64_bf16 v[40:43], v[32:35], v[168:175], v68\n\t" \
  "v_cmp_u_f32 vcc, v74, v37\n\t" \
  CH_DP2 \
  "v_smfmac_f32_16x16x64_bf16 v[44:47], v[0:3], a[0:7], v68\n\t" \
  "s_cbranch_vccnz LFAIL3_%=\n\t" \
  "LGO3_%=:\n\t" \
  "v_cvt_pk_bf16_f32 v74, v74, v37\n\t" \
  "v_smfmac_f32_16x16x64_bf16 v[44:47], v[4:7], a[48:55], v68\n\t" \
  "s_waitcnt lgkmcnt(0)\n\t" \
  "v_add_f32 v75, v40, v42\n\t" \
  "v_smfmac_f32_16x16x64_bf16 v[44:47], v[8:11], v[60:67], v68\n\t" \
  "ds_read_b128 v[60:63], v70 offset:10240\n\t" \
  "v_add_f32 v41, v41, v43\n\t" \
  "v_cvt_pk_bf16_f32 v75, v75, v41\n\t" \
  "v_smfmac_f32_16x16x64_bf16 v[44:47], v[12:15], a[128:135], v68\n\t" \
  "ds_write2st64_b32 v71, v74, v75 offset0:40 offset1:42\n\t" \
  "v_smfmac_f32_16x16x64_bf16 v[44:47], v[16:19], a[176:183], v68\n\t" \
  "ds_read_b128 v[64:67], v70 offset:11264\n\t" \
  "s_memrealtime s[66:67]\n\t" \
  "v_smfmac_f32_16x16x64_bf16 v[44:47], v[20:23], a[208:215], v68\n\t" \
  "v_smfmac_f32_16x16x64_bf16 v[44:47], v[24:27], v[80:87], v68\n\t" \
  "v_smfmac_f32_16x16x64_bf16 v[44:47], v[28:31], v[128:135], v68\n\t" \
  "s_waitcnt lgkmcnt(0)\n\t" \
  "v_smfmac_f32_16x16x64_bf16 v[44:47], v[32:35], v[60:67], v68\n\t" \
  "s_barrier\n\t" \
  "ds_read_b64_tr_b16 v[76:77], v72 offset:10240\n\t" \
  CH_DP3 \
  "v_smfmac_f32_16x16x64_bf16 v[48:51], v[0:3], a[8:15], v68\n\t" \
  "s_lshl_b32 s60, s66, 8\n\t" \
  "s_sub_u32 s60, s47, s60\n\t" \
  "ds_read_b64_tr_b16 v[78:79], v72 offset:10752\n\t" \
  "v_mov_b32 v75, s60\n\t" \
  "v_smfmac_f32_16x16x64_bf16 v[48:51], v[4:7], a[56:63], v68\n\t" \
  "s_mov_b64 exec, s[24:25]\n\t" \
  "buffer_atomic_umin v75, v69, s[28:31], s71 offen sc1\n\t" \
  "s_mov_b64 exec, -1\n\t" \
  "v_add_f32 v74, v44, v46\n\t" \
  "v_smfmac_f32_16x16x64_bf16 v[48:51], v[8:11], a[96:103], v68\n\t" \
  "buffer_load_dwordx4 v[60:63], v69, s[28:31], s64 offen sc1\n\t" \
  "v_add_f32 v45, v45, v47\n\t" \
  "v_smfmac_f32_16x16x64_bf16 v[48:51], v[12:15], a[136:143], v68\n\t" \
  "s_waitcnt vmcnt(4)\n\t" \
  "s_waitcnt lgkmcnt(0)\n\t" \
  "v_cvt_pk_bf16_f32 v74, v74, v45\n\t" \
  "v_mfma_f32_16x16x32_bf16 v[36:39], v[52:55], v[76:79], 0\n\t" \
  "s_waitcnt vmcnt(3)\n\t" \
  "v_mfma_f32_16x16x32_bf16 v[40:43], v[56:59], v[76:79], 0\n\t" \
  "v_mov_b64 v[52:53], 0\n\t" \
  "v_mov_b64 v[54:55], 0\n\t" \
  "v_smfmac_f32_16x16x64_bf16 v[48:51], v[16:19], a[184:191], v68\n\t" \
  "buffer_load_dwordx4 v[64:67], v69, s[28:31], s64 offen offset:1024 sc1\n\t" \
  "v_mov_b64 v[56:57], 0\n\t" \
  "v_mov_b64 v[58:59], 0\n\t" \
  "v_smfmac_f32_16x16x64_bf16 v[48:51], v[20:23], a[216:223], v68\n\t" \
  "ds_write_b32 v71, v74 offset:1024\n\t" \
  "v_add_f32_dpp v36, v40, v36 quad_perm:[2,3,0,1] row_mask:0xf bank_mask:0xf\n\t" \
  "v_smfmac_f32_16x16x64_bf16 v[48:51], v[24:27], v[88:95], v68\n\t" \
  "v_add_f32_dpp v37, v41, v37 quad_perm:[2,3,0,1] row_mask:0xf bank_mask:0xf\n\t" \
  "v_smfmac_f32_16x16x64_bf16 v[48:51], v[28:31], v[136:143], v68\n\t" \
  "v_add_f32_dpp v38, v42, v38 quad_perm:[2,3,0,1] row_mask:0xf bank_mask:0xf\n\t" \
  "v_smfmac_f32_16x16x64_bf16 v[48:51], v[32:35], v[176:183], v68\n\t" \
  "v_add_f32_dpp v39, v43, v39 quad_perm:[2,3,0,1] row_mask:0xf bank_mask:0xf\n\t" \
  CH_DP4 \
  "v_smfmac_f32_16x16x64_bf16 v[52:55], v[0:3], a[16:23], v68\n\t" \
  "s_mov_b64 exec, s[58:59]\n\t" \
  "ds_write_b128 v69, v[36:39] offset:3104\n\t" \
  "s_mov_b64 exec, -1\n\t" \
  "v_smfmac_f32_16x16x64_bf16 v[52:55], v[4:7], a[64:71], v68\n\t" \
  "buffer_load_dword off, s[28:31], s87 sc1 lds\n\t" \
  "v_add_f32 v75, v48, v50\n\t" \
  "v_add_f32 v49, v49, v51\n\t" \
  "v_cvt_pk_bf16_f32 v75, v75, v49\n\t" \
  "v_smfmac_f32_16x16x64_bf16 v[56:59], v[0:3], a[24:31], v68\n\t" \
  "ds_write_b32 v71, v75 offset:1536\n\t" \
  "v_smfmac_f32_16x16x64_bf16 v[56:59], v[4:7], a[72:79], v68\n\t" \
  "ds_read_b64_tr_b16 v[0:1], v72 offset:9216\n\t" \
  "ds_read_b64_tr_b16 v[2:3], v72 offset:9728\n\t" \
  "v_smfmac_f32_16x16x64_bf16 v[52:55], v[8:11], a[104:111], v68\n\t" \
  "ds_read_b128 v[36:39], v70 offset:12288\n\t" \
  "v_smfmac_f32_16x16x64_bf16 v[56:59], v[8:11], a[112:119], v68\n\t" \
  "ds_read_b128 v[40:43], v70 offset:13312\n\t" \
  "v_smfmac_f32_16x16x64_bf16 v[52:55], v[12:15], a[144:151], v68\n\t" \
  "s_waitcnt lgkmcnt(4)\n\t" \
  "s_barrier\n\t" \
  CH_DP5 \
  "ds_read_b64_tr_b16 v[44:45], v72 offset:1024\n\t" \
  "ds_read_b64_tr_b16 v[46:47], v72 offset:1536\n\t" \
  "v_smfmac_f32_16x16x64_bf16 v[56:59], v[12:15], a[152:159], v68\n\t" \
  "ds_read_b64_tr_b16 v[48:49], v72 offset:0\n\t" \
  "ds_read_b64_tr_b16 v[50:51], v72 offset:512\n\t" \
  "v_smfmac_f32_16x16x64_bf16 v[52:55], v[16:19], a[192:199], v68\n\t" \
  "ds_read_b128 v[4:7], v69 offset:2048\n\t" \
  "s_waitcnt lgkmcnt(5)\n\t" \
  "v_smfmac_f32_16x16x64_bf16 v[56:59], v[16:19], v[36:43], v68\n\t" \
  "ds_read_b128 v[8:11], v69 offset:3072\n\t" \
  "s_waitcnt lgkmcnt(4)\n\t" \
  "v_mfma_f32_16x16x32_bf16 v[12:15], v[44:47], v[76:79], 0\n\t" \
  "ds_read_b128 v[36:39], v70 offset:14336\n\t" \
  "s_waitcnt lgkmcnt(3)\n\t" \
  "v_mfma_f32_16x16x32_bf16 v[12:15], v[48:51], v[0:3], v[12:15]\n\t" \
  "ds_read_b128 v[40:43], v70 offset:15360\n\t" \
  "v_smfmac_f32_16x16x64_bf16 v[52:55], v[20:23], a[224:231], v68\n\t" \
  "s_waitcnt lgkmcnt(2)\n\t" \
  "v_add_f32 v4, v4, v8\n\t" \
  "v_add_f32 v5, v5, v9\n\t" \
  "v_add_f32 v6, v6, v10\n\t" \
  CH_DP6 \
  "v_cndmask_b32 v78, v13, v14, s[56:57]\n\t" \
  "v_cndmask_b32 v76, v12, v15, s[56:57]\n\t" \
  "v_add_f32 v7, v7, v11\n\t" \
  "v_max3_f32 v77, v4, v5, v6\n\t" \
  "v_add_f32_dpp v76, v78, v76 quad_perm:[2,3,0,1] row_mask:0xf bank_mask:0xf\n\t" \
  "v_max3_f32 v77, v77, v7, v76\n\t" \
  "v_smfmac_f32_16x16x64_bf16 v[56:59], v[20:23], a[232:239], v68\n\t" \
  "ds_read_b128 v[44:47], v70 offset:16384\n\t" \
  "v_max_f32_dpp v77, v77, v77 quad_perm:[2,3,0,1] row_mask:0xf bank_mask:0xf\n\t" \
  "v_mov_b32 v78, v77\n\t" \
  "v_smfmac_f32_16x16x64_bf16 v[52:55], v[24:27], v[96:103], v68\n\t" \
  "ds_read_b128 v[48:51], v70 offset:17408\n\t" \
  "v_permlane16_swap_b32 v77, v78\n\t" \
  "v_max_f32 v77, v77, v78\n\t" \
  "v_mov_b32 v78, v77\n\t" \
  "v_smfmac_f32_16x16x64_bf16 v[52:55], v[28:31], v[144:151], v68\n\t" \
  "ds_read_b128 v[16:19], v70 offset:18432\n\t" \
  "v_permlane32_swap_b32 v77, v78\n\t" \
  "v_max_f32 v77, v77, v78\n\t" \
  "v_smfmac_f32_16x16x64_bf16 v[52:55], v[32:35], v[184:191], v68\n\t" \
  "ds_read_b128 v[20:23], v70 offset:19456\n\t" \
  "v_mul_f32 v77, 0xbe0293ee, v77\n\t" \
  CH_DP7 \
  "v_smfmac_f32_16x16x64_bf16 v[56:59], v[24:27], v[104:111], v68\n\t" \
  "v_fmamk_f32 v4, v4, 0x3e0293ee, v77\n\t" \
  "v_smfmac_f32_16x16x64_bf16 v[56:59], v[28:31], v[152:159], v68\n\t" \
  "ds_read_b128 v[24:27], v70 offset:22528\n\t" \
  "s_waitcnt lgkmcnt(4)\n\t" \
  "v_fmamk_f32 v5, v5, 0x3e0293ee, v77\n\t" \
  "v_smfmac_f32_16x16x64_bf16 v[56:59], v[32:35], v[36:43], v68\n\t" \
  "ds_read_b128 v[28:31], v70 offset:23552\n\t" \
  "v_fmamk_f32 v6, v6, 0x3e0293ee, v77\n\t" \
  "v_fmamk_f32 v7, v7, 0x3e0293ee, v77\n\t" \
  "v_fmamk_f32 v76, v76, 0x3e0293ee, v77\n\t" \
  "v_exp_f32 v4, v4\n\t" \
  "v_exp_f32 v5, v5\n\t" \
  "v_exp_f32 v6, v6\n\t" \
  "v_exp_f32 v7, v7\n\t" \
  "v_cvt_pk_bf16_f32 v4, v4, v5\n\t" \
  "v_exp_f32 v76, v76\n\t" \
  "v_cvt_pk_bf16_f32 v5, v6, v7\n\t" \
  "v_mov_b32_dpp v12, v4 quad_perm:[0,1,0,1] row_mask:0xf bank_mask:0xf\n\t" \
  "v_mov_b32_dpp v14, v4 quad_perm:[2,3,2,3] row_mask:0xf bank_mask:0xf\n\t" \
  "v_mov_b32_dpp v13, v5 quad_perm:[0,1,0,1] row_mask:0xf bank_mask:0xf\n\t" \
  "v_mov_b32_dpp v15, v5 quad_perm:[2,3,2,3] row_mask:0xf bank_mask:0xf\n\t" \
  "v_add_f32 v52, v52, v54\n\t" \
  "v_add_f32 v53, v53, v55\n\t" \
  "s_waitcnt vmcnt(2)\n\t" \
  CH_DP8 \
  "v_mfma_f32_16x16x32_bf16 v[0:3], v[60:63], v[12:15], 0\n\t" \
  "ds_read_b128 v[36:39], v70 offset:20480\n\t" \
  "v_add_f32 v56, v56, v58\n\t" \
  "v_add_f32 v57, v57, v59\n\t" \
  "v_cvt_pk_bf16_f32 v52, v52, v53\n\t" \
  "v_cvt_pk_bf16_f32 v53, v56, v57\n\t" \
  "s_waitcnt vmcnt(1)\n\t" \
  "v_mfma_f32_16x16x32_bf16 v[4:7], v[64:67], v[12:15], 0\n\t" \
  "ds_read_b128 v[40:43], v70 offset:21504\n\t" \
  "v_mov_b64 v[56:57], s[88:89]\n\t" \
  "v_mov_b64 v[58:59], s[88:89]\n\t" \
  "v_mov_b32_dpp v78, v76 quad_perm:[0,1,0,1] row_mask:0xf bank_mask:0xf\n\t" \
  "v_mov_b32_dpp v79, v76 quad_perm:[2,3,2,3] row_mask:0xf bank_mask:0xf\n\t" \
  "v_mfma_f32_16x16x32_bf16 v[32:35], v[56:59], v[12:15], 0\n\t" \
  "ds_read_b128 v[60:63], v70 offset:24576\n\t" \
  "v_cvt_pk_bf16_f32 v11, v78, v79\n\t" \
  "v_cvt_pk_bf16_f32 v8, v78, v79\n\t" \
  "v_mov_b64 v[54:55], 0\n\t" \
  "v_mov_b32 v9, 0\n\t" \
  "v_mov_b32 v10, 0\n\t" \
  "v_mov_b64 v[14:15], 0\n\t" \
  "s_nop 0\n\t" \
  "v_mfma_f32_16x16x32_bf16 v[32:35], v[54:57], v[8:11], v[32:35]\n\t" \
  "s_cmp_eq_u32 s48, 63\n\t" \
  "v_mfma_f32_16x16x32_bf16 v[0:3], v[52:55], v[8:11], v[0:3]\n\t" \
  "buffer_store_dwordx2 v[52:53], v71, s[28:31], s69 offen offset:3580 sc1\n\t" \
  "ds_read_b128 v[64:67], v70 offset:25600\n\t" \
  "v_mov_b64 v[56:57], 0\n\t" \
  "v_mov_b64 v[58:59], 0\n\t" \
  "v_mfma_f32_16x16x32_bf16 v[4:7], v[52:55], v[10:13], v[4:7]\n\t" \
  "s_cbranch_scc1 LLAST3_%=\n\t" \
  "v_rcp_f32 v74, v32\n\t" \
  "v_mov_b64 v[12:13], 0\n\t" \
  "v_mov_b32 v8, 0\n\t" \
  "v_mul_f32 v0, v0, v74\n\t" \
  "v_mul_f32 v1, v1, v74\n\t" \
  "v_mul_f32 v2, v2, v74\n\t" \
  "v_mul_f32 v3, v3, v74\n\t" \
  "v_cvt_pk_bf16_f32 v76, v0, v1\n\t" \
  "v_cvt_pk_bf16_f32 v77, v2, v3\n\t" \
  "v_pk_mul_f32 v[4:5], v[4:5], v[74:75] op_sel_hi:[1,0]\n\t" \
  "v_pk_mul_f32 v[6:7], v[6:7], v[74:75] op_sel_hi:[1,0]\n\t" \
  "v_cvt_pk_bf16_f32 v78, v4, v5\n\t" \
  "v_cvt_pk_bf16_f32 v79, v6, v7\n\t" \
  "ds_write_b128 v69, v[76:79] offset:7168\n\t" \
  CH_DP9 \
  "s_waitcnt lgkmcnt(2)\n\t" \
  "v_smfmac_f32_16x16x64_bf16 v[12:15], v[76:79], v[44:51], v68\n\t" \
  "ds_read_b128 v[44:47], v70 offset:26624\n\t" \
  "v_mov_b32 v11, 0\n\t" \
  "v_smfmac_f32_16x16x64_bf16 v[56:59], v[76:79], v[16:23], v68\n\t" \
  "ds_read_b128 v[48:51], v70 offset:27648\n\t" \
  "v_mov_b64 v[52:53], 0\n\t" \
  "v_smfmac_f32_16x16x64_bf16 v[8:11], v[76:79], v[36:43], v68\n\t" \
  "ds_read_b128 v[16:19], v70 offset:28672\n\t" \
  "v_smfmac_f32_16x16x64_bf16 v[52:55], v[76:79], v[24:31], v68\n\t" \
  "s_waitcnt lgkmcnt(3)\n\t" \
  "s_barrier\n\t" \
  CH_DP10 \
  "ds_read_b128 v[0:3], v69 offset:6144\n\t" \
  "ds_read_b128 v[4:7], v69 offset:5120\n\t" \
  "v_lshrrev_b32 v71, 7, v69\n\t" \
  "v_and_b32 v74, 0x70, v69\n\t" \
  "v_lshl_add_u32 v71, v71, 14, v74\n\t" \
  "v_bfe_u32 v72, v69, 2, 7\n\t" \
  "v_and_b32 v74, 0x200, v69\n\t" \
  "s_waitcnt lgkmcnt(1)\n\t" \
  "v_smfmac_f32_16x16x64_bf16 v[8:11], v[0:3], v[60:67], v68\n\t" \
  "ds_read_b128 v[20:23], v70 offset:29696\n\t" \
  "v_mov_b64 v[76:77], 0\n\t" \
  "v_smfmac_f32_16x16x64_bf16 v[12:15], v[0:3], v[200:207], v68\n\t" \
  "ds_read_b128 v[32:35], v69 offset:4096\n\t" \
  "v_mov_b64 v[78:79], 0\n\t" \
  "v_smfmac_f32_16x16x64_bf16 v[52:55], v[0:3], v[44:51], v68\n\t" \
  "buffer_load_dword v75, off, s[28:31], s80 sc1\n\t" \
  "ds_read_b128 v[24:27], v70 offset:30720\n\t" \
  "v_lshl_add_u32 v72, v74, 3, v72\n\t" \
  "v_smfmac_f32_16x16x64_bf16 v[56:59], v[0:3], v[232:239], v68\n\t" \
  "ds_read_b128 v[28:31], v70 offset:31744\n\t" \
  "s_waitcnt lgkmcnt(4)\n\t" \
  "v_smfmac_f32_16x16x64_bf16 v[12:15], v[4:7], v[208:215], v68\n\t" \
  "ds_read_b128 v[60:63], v70 offset:32768\n\t" \
  "s_waitcnt lgkmcnt(4)\n\t" \
  "v_smfmac_f32_16x16x64_bf16 v[8:11], v[4:7], v[16:23], v68\n\t" \
  "ds_read_b128 v[64:67], v70 offset:33792\n\t" \
  "s_waitcnt lgkmcnt(4)\n\t" \
  "v_smfmac_f32_16x16x64_bf16 v[8:11], v[32:35], v[192:199], v68\n\t" \
  "ds_read_b128 v[36:39], v73 offset:4096\n\t" \
  "v_smfmac_f32_16x16x64_bf16 v[12:15], v[32:35], v[216:223], v68\n\t" \
  "ds_read_b128 v[40:43], v73 offset:6144\n\t" \
  "s_waitcnt lgkmcnt(4)\n\t" \
  "v_smfmac_f32_16x16x64_bf16 v[52:55], v[4:7], v[24:31], v68\n\t" \
  "ds_read_b128 v[44:47], v70 offset:34816\n\t" \
  "v_cvt_pk_bf16_f32 v8, v8, v9\n\t" \
  "v_smfmac_f32_16x16x64_bf16 v[52:55], v[32:35], v[224:231], v68\n\t" \
  "ds_read_b128 v[48:51], v70 offset:35840\n\t" \
  "v_cvt_pk_bf16_f32 v9, v10, v11\n\t" \
  "v_smfmac_f32_16x16x64_bf16 v[56:59], v[4:7], v[240:247], v68\n\t" \
  "s_waitcnt lgkmcnt(4)\n\t" \
  "v_cvt_pk_bf16_f32 v10, v12, v13\n\t" \
  "v_smfmac_f32_16x16x64_bf16 v[56:59], v[32:35], v[60:67], v68\n\t" \
  "s_waitcnt lgkmcnt(3)\n\t" \
  "v_cvt_pk_bf16_f32 v11, v14, v15\n\t" \
  "v_smfmac_f32_16x16x64_bf16 v[76:79], v[36:39], v[248:255], v68\n\t" \
  "buffer_store_dwordx4 v[8:11], v71, s[28:31], s54 offen sc1\n\t" \
  "s_waitcnt lgkmcnt(0)\n\t" \
  CH_DP11 \
  "v_cvt_pk_bf16_f32 v52, v52, v53\n\t" \
  "v_smfmac_f32_16x16x64_bf16 v[76:79], v[40:43], v[44:51], v68\n\t" \
  "v_cvt_pk_bf16_f32 v53, v54, v55\n\t" \
  "v_cvt_pk_bf16_f32 v54, v56, v57\n\t" \
  "v_cvt_pk_bf16_f32 v55, v58, v59\n\t" \
  "buffer_store_dwordx4 v[52:55], v71, s[28:31], s75 offen sc1\n\t" \
  "s_nop 2\n\t" \
  "v_add_f32 v76, v76, v78\n\t" \
  "v_add_f32 v77, v77, v79\n\t" \
  "v_cvt_pk_bf16_f32 v76, v76, v77\n\t" \
  "buffer_store_dword v76, v72, s[28:31], s55 offen sc1\n\t" \
  CH_DP12 \
  "s_memrealtime s[70:71]\n\t" \
  "s_waitcnt vmcnt(3)\n\t" \
  "v_readfirstlane_b32 s65, v75\n\t" \
  "s_barrier\n\t" \
  "s_waitcnt lgkmcnt(0)\n\t" \
  "s_not_b32 s65, s65\n\t" \
  "s_lshl_b32 s61, s70, 8\n\t" \
  "s_andn2_b32 s64, s65, 0xff\n\t" \
  "s_sub_u32 s64, s64, s61\n\t" \
  "s_ashr_i32 s64, s64, 8\n\t" \
  "s_cmp_eq_u32 s65, 0\n\t" \
  "s_cselect_b32 s64, " CH_DLF ", s64\n\t" \
  "s_min_i32 s64, s64, s34\n\t" \
  "s_max_i32 s64, s64, " CH_DLL1 "\n\t" \
  "s_add_u32 s64, s64, s70\n\t" \
  CH_ST2 \
  "s_mov_b32 s74, 0\n\t" \
  "v_and_b32 v37, 0x30, v69\n\t" \
  "v_add_u32 v37, s52, v37\n\t" \
  "s_mov_b32 exec_lo, 0\n\t" \
  "v_mov_b32 v37, s85\n\t" \
  "s_mov_b32 exec_lo, -1\n\t" \
  "s_cmp_lt_i32 s41, 0\n\t" \
  "s_cbranch_scc1 LIDLE_%=\n\t" \
  "s_movk_i32 s60, 8192\n\t" \
  "RWT3_%=:\n\t" \
  "s_memrealtime s[72:73]\n\t" \
  "s_sub_u32 s60, s60, 1\n\t" \
  "s_cselect_b32 s61, 0, -1\n\t" \
  "s_waitcnt lgkmcnt(0)\n\t" \
  "s_sub_i32 s73, s72, s64\n\t" \
  "s_and_b32 s73, s73, s61\n\t" \
  "s_cmp_lt_i32 s73, " CH_FIN "\n\t" \
  "s_cbranch_scc1 RWT3_%=\n\t" \
  "s_cmp_lt_i32 s73, 0\n\t" \
  "s_cbranch_scc1 RWT3_%=f\n\t" \
  "RWT3_%=g:\n\t" \
  "RRETRY3_%=:\n\t" \
  CH_DP13 \
  "buffer_load_dwordx4 v[0:3], v69, s[28:31], s97 offen offset:0 sc1\n\t" \
  "buffer_load_dwordx4 v[4:7], v69, s[28:31], s97 offen offset:1024 sc1\n\t" \
  "buffer_load_dwordx4 v[8:11], v69, s[28:31], s97 offen offset:2048 sc1\n\t" \
  "buffer_load_dwordx4 v[12:15], v69, s[28:31], s97 offen offset:3072 sc1\n\t" \
  "v_lshrrev_b32 v20, 2, v69\n\t" \
  "v_and_b32 v20, 16, v20\n\t" \
  "v_mov_b32 v21, 0x3f80\n\t" \
  "v_lshlrev_b32 v21, v20, v21\n\t" \
  "v_bfe_u32 v20, v69, 4, 2\n\t" \
  "v_cmp_eq_u32 vcc, 0, v20\n\t" \
  "s_nop 1\n\t" \
  "v_cndmask_b32 v32, 0, v21, vcc\n\t" \
  "v_cmp_eq_u32 vcc, 1, v20\n\t" \
  "s_nop 1\n\t" \
  "v_cndmask_b32 v33, 0, v21, vcc\n\t" \
  "v_cmp_eq_u32 vcc, 2, v20\n\t" \
  "s_nop 1\n\t" \
  "v_cndmask_b32 v34, 0, v21, vcc\n\t" \
  "v_cmp_eq_u32 vcc, 3, v20\n\t" \
  "s_nop 1\n\t" \
  "v_cndmask_b32 v35, 0, v21, vcc\n\t" \
  "v_bfe_u32 v38, v69, 7, 1\n\t" \
  "v_bfe_u32 v20, v69, 9, 1\n\t" \
  "v_lshl_add_u32 v38, v20, 2, v38\n\t" \
  "v_lshlrev_b32 v38, 12, v38\n\t" \
  "v_and_b32 v20, 0x60, v69\n\t" \
  "v_add_u32 v38, v38, v20\n\t" \
  "v_bfe_u32 v20, v69, 8, 1\n\t" \
  "v_lshl_add_u32 v38, v20, 4, v38\n\t" \
  "v_bfe_u32 v20, v69, 4, 1\n\t" \
  "v_lshl_add_u32 v38, v20, 3, v38\n\t" \
  "v_mov_b32 v44, 0\n\t" \
  "buffer_load_dword v39, v37, s[28:31], 0 offen sc1\n\t" \
  CH_DP17 \
  "s_waitcnt vmcnt(4)\n\t" \
  CH_DP14 \
  "v_mfma_f32_16x16x32_bf16 v[40:43], v[32:35], v[0:3], 0\n\t" \
  "v_max3_u32 v44, v44, v0, v1\n\t" \
  "v_max3_u32 v44, v44, v2, v3\n\t" \
  "s_waitcnt vmcnt(3)\n\t" \
  "v_mfma_f32_16x16x32_bf16 v[40:43], v[32:35], v[4:7], v[40:43]\n\t" \
  "v_max3_u32 v44, v44, v4, v5\n\t" \
  "v_max3_u32 v44, v44, v6, v7\n\t" \
  "s_waitcnt vmcnt(2)\n\t" \
  "v_mfma_f32_16x16x32_bf16 v[40:43], v[32:35], v[8:11], v[40:43]\n\t" \
  "v_max3_u32 v44, v44, v8, v9\n\t" \
  "v_max3_u32 v44, v44, v10, v11\n\t" \
  "s_waitcnt vmcnt(1)\n\t" \
  CH_DP15 \
  "v_mfma_f32_16x16x32_bf16 v[40:43], v[32:35], v[12:15], v[40:43]\n\t" \
  "v_max3_u32 v44, v44, v12, v13\n\t" \
  "v_max3_u32 v44, v44, v14, v15\n\t" \
  "v_cmp_eq_u32 vcc, -1, v44\n\t" \
  "s_cbranch_vccnz RFAIL3_%=\n\t" \
  "s_nop 3\n\t" \
  "v_add_f32_dpp v40, v40, v40 row_ror:8 row_mask:0xf bank_mask:0xf\n\t" \
  "v_add_f32_dpp v41, v41, v41 row_ror:8 row_mask:0xf bank_mask:0xf\n\t" \
  "v_add_f32_dpp v42, v42, v42 row_ror:8 row_mask:0xf bank_mask:0xf\n\t" \
  "v_add_f32_dpp v43, v43, v43 row_ror:8 row_mask:0xf bank_mask:0xf\n\t" \
  "v_cvt_pk_bf16_f32 v40, v40, v41\n\t" \
  "v_cvt_pk_bf16_f32 v41, v42, v43\n\t" \
  "buffer_store_dwordx2 v[40:41], v38, s[28:31], s98 offen sc1\n\t" \
  "RDONE3_%=:\n\t" \
  CH_DP16 \
  CH_ST4 \
  "s_barrier\n\t" \
  CH_ST3 \
  "s_waitcnt vmcnt(1)\n\t" \
  "v_cmp_eq_u32 vcc, 0, v39\n\t" \
  "s_sub_u32 s60, s70, s66\n\t" \
  "s_add_u32 s81, s81, 1\n\t" \
  "s_min_u32 s81, s81, s60\n\t" \
  CH_TRC \
  "s_add_u32 s48, s48, 1\n\t" \
  "s_mov_b32 s49, s53\n\t" \
  "s_add_u32 s53, s53, " CH_SLOT "\n\t" \
  "s_add_u32 s60, s53, " CH_SKIP "\n\t" \
  "s_lshr_b32 s61, s96, 1\n\t" \
  "s_add_u32 s86, s86, 1\n\t" \
  "s_cmp_eq_u32 s86, 3\n\t" \
  "s_cselect_b32 s53, s60, s53\n\t" \
  "s_cselect_b32 s96, s61, s96\n\t" \
  "s_cselect_b32 s86, 0, s86\n\t" \
  "s_andn2_b32 s53, s53, 0x2000\n\t" \
  "s_and_b32 s60, s96, 1\n\t" \
  "s_lshl_b32 s60, s60, 13\n\t" \
  "s_or_b32 s53, s53, s60\n\t" \
  "s_mov_b32 s68, 0\n\t" \
  "s_add_u32 s97, s41, s53\n\t" \
  "s_add_u32 s98, s37, s53\n\t" \
  "s_add_u32 s82, s48, 0\n\t" \
  CH_SEGC \
  "s_lshr_b64 s[60:61], s[90:91], s82\n\t" \
  "s_and_b32 s60, s60, 1\n\t" \
  "s_lshl_b32 s60, s60, 13\n\t" \
  "s_add_u32 s82, s82, " CH_SG0 "\n\t" \
  "s_lshl_b32 s82, s82, 21\n\t" \
  "s_or_b32 s82, s82, s60\n\t" \
  "s_add_u32 s50, s82, s38\n\t" \
  "s_add_u32 s69, s82, s39\n\t" \
  "s_add_u32 s82, s48, 1\n\t" \
  CH_SEGC \
  "s_lshr_b64 s[60:61], s[90:91], s82\n\t" \
  "s_and_b32 s60, s60, 1\n\t" \
  "s_lshl_b32 s60, s60, 13\n\t" \
  "s_add_u32 s82, s82, " CH_SG0 "\n\t" \
  "s_lshl_b32 s82, s82, 21\n\t" \
  "s_or_b32 s82, s82, s60\n\t" \
  "s_add_u32 s52, s82, s43\n\t" \
  "s_add_u32 s82, s48, " CH_ARMD "\n\t" \
  CH_SEGC \
  "s_lshr_b64 s[60:61], s[90:91], s82\n\t" \
  "s_and_b32 s60, s60, 1\n\t" \
  "s_lshl_b32 s60, s60, 13\n\t" \
  "s_add_u32 s82, s82, " CH_SG0 "\n\t" \
  "s_lshl_b32 s82, s82, 21\n\t" \
  "s_or_b32 s82, s82, s60\n\t" \
  "s_add_u32 s85, s82, s45\n\t" \
  "s_cmp_lt_i32 s48, " CH_ARMS "\n\t" \
  "s_cselect_b32 s85, s27, s85\n\t" \
  "s_cmp_eq_u32 s34, 0\n\t" \
  "s_cbranch_scc1 LINI_%=\n\t" \
  "LTK_%=:\n\t" \
  CH_TOKCHK \
  "s_mov_b32 s61, 0\n\t" \
  "LTW_%=:\n\t" \
  "buffer_load_dword v1, v37, s[28:31], 0 offen sc1\n\t" \
  "s_waitcnt vmcnt(0)\n\t" \
  "v_cmp_eq_u32 vcc, 0, v1\n\t" \
  "s_cbranch_vccz LG3_%=\n\t" \
  "s_add_u32 s61, s61, 1\n\t" \
  "s_sleep 2\n\t" \
  "s_cmp_gt_u32 s61, s84\n\t" \
  "s_cbranch_scc1 LTG_%=\n\t" \
  "s_and_b32 s60, s61, 1023\n\t" \
  "s_cmp_lg_u32 s60, 0\n\t" \
  "s_cbranch_scc1 LTW_%=\n\t" \
  "buffer_load_dword v46, off, s[28:31], s92 sc1\n\t" \
  "s_waitcnt vmcnt(0)\n\t" \
  "v_readfirstlane_b32 s60, v46\n\t" \
  "s_nop 0\n\t" \
  "s_cmp_eq_u32 s60, 0\n\t" \
  "s_cbranch_scc1 LTW_%=\n\t" \
  "LTG_%=:\n\t" \
  "s_mov_b32 s84, 0\n\t" \
  "s_lshl_b32 s60, s36, 4\n\t" \
  "s_or_b32 s60, s60, s48\n\t" \
  "s_or_b32 s60, s60, 268435456\n\t" \
  "v_mov_b32 v76, s60\n\t" \
  "v_mov_b32 v77, 0\n\t" \
  "s_mov_b64 exec, 1\n\t" \
  "buffer_atomic_cmpswap v[76:77], off, s[28:31], s92 sc1\n\t" \
  "s_mov_b64 exec, -1\n\t" \
  "s_branch LG3_%=\n\t" \
  "LIDLE_%=:\n\t" \
  "s_add_u32 s64, s64, 8\n\t" \
  "s_movk_i32 s60, 8192\n\t" \
  "IWT_%=:\n\t" \
  "s_memrealtime s[72:73]\n\t" \
  "s_sub_u32 s60, s60, 1\n\t" \
  "s_cselect_b32 s61, 0, -1\n\t" \
  "s_waitcnt lgkmcnt(0)\n\t" \
  "s_sub_i32 s73, s72, s64\n\t" \
  "s_and_b32 s73, s73, s61\n\t" \
  "s_cmp_lt_i32 s73, 0\n\t" \
  "s_cbranch_scc1 IWT_%=\n\t" \
  "buffer_load_dword v39, v37, s[28:31], 0 offen sc1\n\t" \
  "buffer_store_dword v37, off, s[28:31], s41\n\t" \
  "s_branch RDONE3_%=\n\t" \
  "RFAIL3_%=:\n\t" \
  "s_add_u32 s74, s74, 1\n\t" \
  "s_sleep " CH_RSL "\n\t" \
  "s_waitcnt vmcnt(0)\n\t" \
  "s_cmp_gt_u32 s74, s84\n\t" \
  "s_cbranch_scc1 RG3_%=\n\t" \
  "s_and_b32 s60, s74, 1023\n\t" \
  "s_cmp_lg_u32 s60, 0\n\t" \
  "s_cbranch_scc1 RRETRY3_%=\n\t" \
  "buffer_load_dword v46, off, s[28:31], s92 sc1\n\t" \
  "s_waitcnt vmcnt(0)\n\t" \
  "v_readfirstlane_b32 s60, v46\n\t" \
  "s_nop 0\n\t" \
  "s_cmp_eq_u32 s60, 0\n\t" \
  "s_cbranch_scc1 RRETRY3_%=\n\t" \
  "RG3_%=:\n\t" \
  "s_mov_b32 s84, 0\n\t" \
  "s_lshl_b32 s60, s36, 4\n\t" \
  "s_or_b32 s60, s60, s48\n\t" \
  "s_or_b32 s60, s60, 536870912\n\t" \
  "v_mov_b32 v76, s60\n\t" \
  "v_mov_b32 v77, 0\n\t" \
  "s_mov_b64 exec, 1\n\t" \
  "buffer_atomic_cmpswap v[76:77], off, s[28:31], s92 sc1\n\t" \
  "s_mov_b64 exec, -1\n\t" \
  "s_branch RDONE3_%=\n\t" \
  "LFAIL3_%=:\n\t" \
  "v_max_u32 v75, v0, v1\n\t" \
  "v_max3_u32 v75, v75, v2, v3\n\t" \
  "v_max3_u32 v75, v75, v4, v5\n\t" \
  "v_max3_u32 v75, v75, v6, v7\n\t" \
  "v_max3_u32 v75, v75, v8, v9\n\t" \
  "v_max3_u32 v75, v75, v10, v11\n\t" \
  "v_max3_u32 v75, v75, v12, v13\n\t" \
  "v_max3_u32 v75, v75, v14, v15\n\t" \
  "v_max3_u32 v75, v75, v16, v17\n\t" \
  "v_max3_u32 v75, v75, v18, v19\n\t" \
  "v_max3_u32 v75, v75, v20, v21\n\t" \
  "v_max3_u32 v75, v75, v22, v23\n\t" \
  "v_max3_u32 v75, v75, v24, v25\n\t" \
  "v_max3_u32 v75, v75, v26, v27\n\t" \
  "v_max3_u32 v75, v75, v28, v29\n\t" \
  "v_max3_u32 v75, v75, v30, v31\n\t" \
  "v_max3_u32 v75, v75, v32, v33\n\t" \
  "v_max3_u32 v75, v75, v34, v35\n\t" \
  "v_cmp_eq_u32 vcc, -1, v75\n\t" \
  "s_cbranch_vccz LGO3_%=\n\t" \
  "s_add_u32 s68, s68, 1\n\t" \
  "s_waitcnt lgkmcnt(0)\n\t" \
  "s_cmp_gt_u32 s68, s83\n\t" \
  "s_cbranch_scc1 LGV3_%=\n\t" \
  "s_barrier\n\t" \
  "s_waitcnt vmcnt(8)\n\t" \
  "s_branch LG3_%=\n\t" \
  "RWT3_%=f:\n\t" \
  "s_cmp_lt_i32 s73, -2\n\t" \
  "s_cbranch_scc0 RWT3_%=g\n\t" \
  "s_nop 4\n\t" \
  "s_branch RWT3_%=g\n\t" \
  "LGV3_%=:\n\t" \
  "s_mov_b32 s83, 0\n\t" \
  "s_lshl_b32 s60, s36, 4\n\t" \
  "s_or_b32 s60, s60, s48\n\t" \
  "s_or_b32 s60, s60, 805306368\n\t" \
  "v_mov_b32 v76, s60\n\t" \
  "v_mov_b32 v77, 0\n\t" \
  "s_mov_b64 exec, 1\n\t" \
  "buffer_atomic_cmpswap v[76:77], off, s[28:31], s92 sc1\n\t" \
  "s_mov_b64 exec, -1\n\t" \
  "s_branch LGO3_%=\n\t" \
  "LLAST3_%=:\n\t" \
  "s_waitcnt vmcnt(0) lgkmcnt(0)\n\t" \
  "s_barrier\n\t" \
  "LEND_%=:\n\t" \
  ""
typedef unsigned v16u __attribute__((ext_vector_type(16)));
#define CH_WCLOB "a0", "a1", "a2", "a3", "a4", "a5", "a6", "a7", "a8", "a9", "a10", "a11", "a12", "a13", "a14", "a15", "a16", "a17", "a18", "a19", "a20", "a21", "a22", "a23", "a24", "a25", "a26", "a27", "a28", "a29", "a30", "a31", "a32", "a33", "a34", "a35", "a36", "a37", "a38", "a39", "a40", "a41", "a42", "a43", "a44", "a45", "a46", "a47", "a48", "a49", "a50", "a51", "a52", "a53", "a54", "a55", "a56", "a57", "a58", "a59", "a60", "a61", "a62", "a63", "a64", "a65", "a66", "a67", "a68", "a69", "a70", "a71", "a72", "a73", "a74", "a75", "a76", "a77", "a78", "a79", "a80", "a81", "a82", "a83", "a84", "a85", "a86", "a87", "a88", "a89", "a90", "a91", "a92", "a93", "a94", "a95", "a96", "a97", "a98", "a99", "a100", "a101", "a102", "a103", "a104", "a105", "a106", "a107", "a108", "a109", "a110", "a111", "a112", "a113", "a114", "a115", "a116", "a117", "a118", "a119", "a120", "a121", "a122", "a123", "a124", "a125", "a126", "a127", "a128", "a129", "a130", "a131", "a132", "a133", "a134", "a135", "a136", "a137", "a138", "a139", "a140", "a141", "a142", "a143", "a144", "a145", "a146", "a147", "a148", "a149", "a150", "a151", "a152", "a153", "a154", "a155", "a156", "a157", "a158", "a159", "a160", "a161", "a162", "a163", "a164", "a165", "a166", "a167", "a168", "a169", "a170", "a171", "a172", "a173", "a174", "a175", "a176", "a177", "a178", "a179", "a180", "a181", "a182", "a183", "a184", "a185", "a186", "a187", "a188", "a189", "a190", "a191", "a192", "a193", "a194", "a195", "a196", "a197", "a198", "a199", "a200", "a201", "a202", "a203", "a204", "a205", "a206", "a207", "a208", "a209", "a210", "a211", "a212", "a213", "a214", "a215", "a216", "a217", "a218", "a219", "a220", "a221", "a222", "a223", "a224", "a225", "a226", "a227", "a228", "a229", "a230", "a231", "a232", "a233", "a234", "a235", "a236", "a237", "a238", "a239", "a240", "a241", "a242", "a243", "a244", "a245", "a246", "a247", "a248", "a249", "a250", "a251", "a252", "a253", "a254", "a255", "v80", "v81", "v82", "v83", "v84", "v85", "v86", "v87", "v88", "v89", "v90", "v91", "v92", "v93", "v94", "v95", "v96", "v97", "v98", "v99", "v100", "v101", "v102", "v103", "v104", "v105", "v106", "v107", "v108", "v109", "v110", "v111", "v112", "v113", "v114", "v115", "v116", "v117", "v118", "v119", "v120", "v121", "v122", "v123", "v124", "v125", "v126", "v127", "v128", "v129", "v130", "v131", "v132", "v133", "v134", "v135", "v136", "v137", "v138", "v139", "v140", "v141", "v142", "v143", "v144", "v145", "v146", "v147", "v148", "v149", "v150", "v151", "v152", "v153", "v154", "v155", "v156", "v157", "v158", "v159", "v160", "v161", "v162", "v163", "v164", "v165", "v166", "v167", "v168", "v169", "v170", "v171", "v172", "v173", "v174", "v175", "v176", "v177", "v178", "v179", "v180", "v181", "v182", "v183", "v184", "v185", "v186", "v187", "v188", "v189", "v190", "v191", "v192", "v193", "v194", "v195", "v196", "v197", "v198", "v199", "v200", "v201", "v202", "v203", "v204", "v205", "v206", "v207", "v208", "v209", "v210", "v211", "v212", "v213", "v214", "v215", "v216", "v217", "v218", "v219", "v220", "v221", "v222", "v223", "v224", "v225", "v226", "v227", "v228", "v229", "v230", "v231", "v232", "v233", "v234", "v235", "v236", "v237", "v238", "v239", "v240", "v241", "v242", "v243", "v244", "v245", "v246", "v247", "v248", "v249", "v250", "v251", "v252", "v253", "v254", "v255"
#define CH_H(b) (8 * ((b) & 3) + ((b) >> 3))
#define CH_L(b) (((b) >> 2) & 1)
__device__ __forceinline__ void start_work(char* smem, int wk);
__device__ __forceinline__ void chain_wg(char* smem) {
  if (CH_L((int)blockIdx.x)) start_work(smem, CH_H((int)blockIdx.x));
  const MP p = getp();
  const int b = (int)blockIdx.x, h = CH_H(b), L = CH_L(b);
  int w = __builtin_amdgcn_readfirstlane(threadIdx.x >> 6);
  {
    unsigned id; asm volatile("s_getreg_b32 %0, hwreg(HW_REG_HW_ID, 4, 2)" : "=s"(id));
    volatile __attribute__((address_space(3))) unsigned* lw = (volatile __attribute__((address_space(3))) unsigned*)smem;
    lw[w] = 1u << id;
    BAR();
    if (__builtin_amdgcn_readfirstlane(lw[0] | lw[1] | lw[2] | lw[3]) == 15u) w = (int)((SMAP >> (4u * id)) & 3u);
  }
  unsigned lp16 = (threadIdx.x & 63u) * 16u;
  rsrc_t X = mkr(p.xb, XB_BYTES);
  if ((unsigned)(size_t)(__attribute__((address_space(3))) char*)smem != 0u) return;
  const unsigned wsz = DM * HD * 2u - (unsigned)(h * DH) * 2u;
  const rsrc_t RQ = mkr(p.Wq + h * DH, wsz), RK = mkr(p.Wk + h * DH, wsz), RV = mkr(p.Wv + h * DH, wsz), RO = mkr(p.Wo + (long)h * DH * DM, DH * DM * 2u);
  unsigned xcc; asm volatile("s_getreg_b32 %0, hwreg(HW_REG_XCC_ID)" : "=s"(xcc)); xcc &= 3u;
  unsigned d96 = 0u;
  const unsigned c43 = P_FA * PAIR + (unsigned)h * 128u + 32u;
  const unsigned h128 = (unsigned)h * 128u, uw = (unsigned)w, t = uw >> 1, T = uw & 1u, NONE = 0xF0000000u;
  const unsigned u0 = w == 3 ? (unsigned)h >> 2 : (unsigned)((h & 1) + (h >> 3) * 2 + ((h >> 1) & 3) * 8 + (w == 2 ? 32 : 0));
  const bool idle = w == 3 && (h & 3);
  const unsigned c37 = idle ? NONE : (w == 3 ? AGT + u0 * 128u : AGO + (u0 >> 5) * 32768u + (u0 & 31u) * 128u);
  const unsigned c41 = idle ? NONE : (w == 3 ? 524288u + (u0 >> 1) * 16384u + (u0 & 1u) * 4096u : (u0 & 7u) * 16384u + ((u0 >> 3) & 1u) * 4096u + (u0 >> 4) * 131072u);
  const unsigned c42 = AGO + (xcc & 1u) * 16384u + (xcc >> 1) * 4096u;
  const unsigned c46 = (w == 3 ? (h & 7) == 5 && DL3 > -50 : h == 30) ? AGT + 1024u : NONE;
  const unsigned c38 = (unsigned)(3 * h + 1) * PAIR + T * 4096u + t * 2048u, c39 = (P_KV + (unsigned)(h >> 1)) * PAIR + (unsigned)(h & 1) * 4096u;
  const unsigned c45 = P_FB * PAIR + 4096u + (unsigned)(16 + (h & 15)) * 128u;
  unsigned d48 = 0u, d49 = 0u, d53 = SLOT, d86 = 1u, d65 = rtc();
  unsigned mn = 120u;
  v4u stt;
  asm volatile("s_waitcnt vmcnt(0) lgkmcnt(0)\n\t" CH_ASM "LCHEND:"
               : "+{s48}"(d48), "+{s49}"(d49), "+{s53}"(d53), "+{s65}"(d65), "+{s81}"(mn), "+{s86}"(d86), "+{s96}"(d96), "=&{s[76:79]}"(stt), "+{v69}"(lp16), "+{s[28:31]}"(X)
               : [Wq] "s"(RQ), [Wk] "s"(RK), [Wv] "s"(RV), [Wo] "s"(RO), "{s33}"(HT_OFF + 8192u + 256u + (unsigned)b * 4u), "{s36}"(h128), "{s37}"(c37), "{s40}"(w), "{s41}"(c41), "{s42}"(c42), "{s46}"(c46), "{s92}"(TMO_OFF), "{s38}"(c38), "{s39}"(c39), "{s43}"(c43), "{s45}"(c45), "{s32}"(L), "{s26}"(AGT + 1024u + xcc * 128u), "{s27}"(RDY_OFF(0) + (unsigned)((h % 9) * 1024 + (b & 7) * 128)), [ctab] "i"(CTAB_OFF), [ex] "i"(ED * 256 + EB), [dl1l] "i"(DL1 - LATE), [armd] "i"(3 - ARMK), [arms] "i"(ARMK - 3),
                 [cap] "i"(SPIN_CAP)
               : "v0", "v1", "v2", "v3", "v4", "v5", "v6", "v7", "v8", "v9", "v10", "v11", "v12", "v13", "v14", "v15", "v16", "v17", "v18", "v19",
                 "v20", "v21", "v22", "v23", "v24", "v25", "v26", "v27", "v28", "v29", "v30", "v31", "v32", "v33", "v34", "v35", "v36", "v37", "v38", "v39",
                 "v40", "v41", "v42", "v43", "v44", "v45", "v46", "v47", "v48", "v49", "v50", "v51", "v52", "v53", "v54", "v55", "v56", "v57", "v58", "v59",
                 "v60", "v61", "v62", "v63", "v64", "v65", "v66", "v67", "v68", "v70", "v71", "v72", "v73", "v74", "v75", "v76", "v77", "v78", "v79",
                 CH_WCLOB, "s34", "s35", "s24", "s25", "s90", "s91", "s47", "s87", "s50", "s51", "s52", "s69", "s82", "s85", "s54", "s75", "s80", "s55", "s56", "s57", "s58", "s59", "s60", "s61", "s62", "s63", "s64", "s66", "s67", "s68", "s70", "s71", "s72", "s73", "s74", "s88", "s89",
                 "s83", "s84", "s97", "s98", "s99", "s93", "s94", "s95", "m0", "vcc", "scc", "memory");
}
#define XQ 0u
#define XQL 0u
#define XK 0u
#define XT 0u
#define XA 0u
#define XAL 0u
#define XRL 0u
#define QLEAD 4
#define RONE 2
#define OAUX 0
#define PSP 4
#define PSR 8
#define RDLY 40
#define QLEAD1 2
#define QXW -18
#define QXC QXW
#define TK_QXW "%[qxw]"
#define TK_QXC "%[qxc]"
#define PSG 16
#define TK_PSG CH_S(PSG)
#define AX 30
#define TK_AX CH_S(AX)
#define PSK 2
#define PSH 2
#define TK_CAP "%[cap]"
#define TK_CTAB "%[ctab]"
#define TK_SG0 CH_S(SG0)
#define TK_PSK CH_S(PSK)
#define TK_PSH CH_S(PSH)
#define TK_PA0
#define TK_PA1
#define TK_PA2
#define TK_PQ0
#define TK_PQ1
#define TK_PQ2
#define TT(s, i)
#define TK_AC(n)
#define TK_ACE
#define TK_AC0 TK_AC(212)
#define TK_AC1 TK_AC(213)
#define TK_AC2 TK_AC(214)
#define TK_AC3 TK_AC(215)
#define TK_AC4 TK_AC(216)
#define TK_AC5 TK_AC(217)
#define TK_AC6 TK_AC(218)
#define TK_AC7 TK_AC(219)
#define TK_AC8 TK_AC(220)
#define AP_ASM \
  "v_lshrrev_b32 v40, 8, v0\n\t" \
  "v_mul_u32_u24 v40, 4608, v40\n\t" \
  "v_and_b32 v41, 0xf0, v0\n\t" \
  "v_lshrrev_b32 v42, 3, v41\n\t" \
  "v_add3_u32 v41, v41, v40, s64\n\t" \
  "v_add3_u32 v42, v42, v40, s65\n\t" \
  "s_mov_b32 s62, 0x05040100\n\t" \
  "s_mov_b32 s63, 0x07060302\n\t" \
  "s_xor_b32 s60, s66, 0\n\t" \
  "s_add_u32 s60, s60, 0\n\t" \
  "s_add_u32 s61, s60, 18432\n\t" \
  "buffer_load_dwordx4 v[48:51], v41, %[W], s60 offen offset:0\n\t" \
  "buffer_load_dwordx4 v[52:55], v41, %[W], s60 offen offset:1152\n\t" \
  "buffer_load_dwordx4 v[56:59], v41, %[W], s60 offen offset:2304\n\t" \
  "buffer_load_dwordx4 v[60:63], v41, %[W], s60 offen offset:3456\n\t" \
  "buffer_load_dwordx4 v[64:67], v41, %[W], s61 offen offset:0\n\t" \
  "buffer_load_dwordx4 v[68:71], v41, %[W], s61 offen offset:1152\n\t" \
  "buffer_load_dwordx4 v[72:75], v41, %[W], s61 offen offset:2304\n\t" \
  "buffer_load_dwordx4 v[76:79], v41, %[W], s61 offen offset:3456\n\t" \
  "s_xor_b32 s60, s66, 0\n\t" \
  "s_add_u32 s60, s60, 36864\n\t" \
  "s_add_u32 s61, s60, 18432\n\t" \
  "buffer_load_dwordx4 v[80:83], v41, %[W], s60 offen offset:0\n\t" \
  "buffer_load_dwordx4 v[84:87], v41, %[W], s60 offen offset:1152\n\t" \
  "buffer_load_dwordx4 v[88:91], v41, %[W], s60 offen offset:2304\n\t" \
  "buffer_load_dwordx4 v[92:95], v41, %[W], s60 offen offset:3456\n\t" \
  "buffer_load_dwordx4 v[96:99], v41, %[W], s61 offen offset:0\n\t" \
  "buffer_load_dwordx4 v[100:103], v41, %[W], s61 offen offset:1152\n\t" \
  "buffer_load_dwordx4 v[104:107], v41, %[W], s61 offen offset:2304\n\t" \
  "buffer_load_dwordx4 v[108:111], v41, %[W], s61 offen offset:3456\n\t" \
  "s_xor_b32 s60, s66, 0\n\t" \
  "s_add_u32 s60, s60, 73728\n\t" \
  "s_add_u32 s61, s60, 18432\n\t" \
  "buffer_load_dwordx4 v[112:115], v41, %[W], s60 offen offset:0\n\t" \
  "buffer_load_dwordx4 v[116:119], v41, %[W], s60 offen offset:1152\n\t" \
  "buffer_load_dwordx4 v[120:123], v41, %[W], s60 offen offset:2304\n\t" \
  "buffer_load_dwordx4 v[124:127], v41, %[W], s60 offen offset:3456\n\t" \
  "buffer_load_dwordx4 v[128:131], v41, %[W], s61 offen offset:0\n\t" \
  "buffer_load_dwordx4 v[132:135], v41, %[W], s61 offen offset:1152\n\t" \
  "buffer_load_dwordx4 v[136:139], v41, %[W], s61 offen offset:2304\n\t" \
  "buffer_load_dwordx4 v[140:143], v41, %[W], s61 offen offset:3456\n\t" \
  "s_xor_b32 s60, s66, 0\n\t" \
  "s_add_u32 s60, s60, 110592\n\t" \
  "s_add_u32 s61, s60, 18432\n\t" \
  "buffer_load_dwordx4 v[144:147], v41, %[W], s60 offen offset:0\n\t" \
  "buffer_load_dwordx4 v[148:151], v41, %[W], s60 offen offset:1152\n\t" \
  "buffer_load_dwordx4 v[152:155], v41, %[W], s60 offen offset:2304\n\t" \
  "buffer_load_dwordx4 v[156:159], v41, %[W], s60 offen offset:3456\n\t" \
  "buffer_load_dwordx4 v[160:163], v41, %[W], s61 offen offset:0\n\t" \
  "buffer_load_dwordx4 v[164:167], v41, %[W], s61 offen offset:1152\n\t" \
  "buffer_load_dwordx4 v[168:171], v41, %[W], s61 offen offset:2304\n\t" \
  "buffer_load_dwordx4 v[172:175], v41, %[W], s61 offen offset:3456\n\t" \
  "s_xor_b32 s60, s66, 147456\n\t" \
  "s_add_u32 s60, s60, 0\n\t" \
  "s_add_u32 s61, s60, 18432\n\t" \
  "buffer_load_dwordx4 v[176:179], v41, %[W], s60 offen offset:0\n\t" \
  "buffer_load_dwordx4 v[180:183], v41, %[W], s60 offen offset:1152\n\t" \
  "buffer_load_dwordx4 v[184:187], v41, %[W], s60 offen offset:2304\n\t" \
  "buffer_load_dwordx4 v[188:191], v41, %[W], s60 offen offset:3456\n\t" \
  "buffer_load_dwordx4 v[192:195], v41, %[W], s61 offen offset:0\n\t" \
  "buffer_load_dwordx4 v[196:199], v41, %[W], s61 offen offset:1152\n\t" \
  "buffer_load_dwordx4 v[200:203], v41, %[W], s61 offen offset:2304\n\t" \
  "buffer_load_dwordx4 v[204:207], v41, %[W], s61 offen offset:3456\n\t" \
  "s_waitcnt vmcnt(38)\n\t" \
  "v_perm_b32 v44, v52, v48, s62\n\t" \
  "s_waitcnt vmcnt(36)\n\t" \
  "v_perm_b32 v45, v60, v56, s62\n\t" \
  "s_waitcnt vmcnt(34)\n\t" \
  "v_perm_b32 v46, v68, v64, s62\n\t" \
  "s_waitcnt vmcnt(32)\n\t" \
  "v_perm_b32 v47, v76, v72, s62\n\t" \
  "v_accvgpr_write_b32 a0, v44\n\t" \
  "v_accvgpr_write_b32 a1, v45\n\t" \
  "v_accvgpr_write_b32 a2, v46\n\t" \
  "v_accvgpr_write_b32 a3, v47\n\t" \
  "v_perm_b32 v44, v52, v48, s63\n\t" \
  "v_perm_b32 v45, v60, v56, s63\n\t" \
  "v_perm_b32 v46, v68, v64, s63\n\t" \
  "v_perm_b32 v47, v76, v72, s63\n\t" \
  "v_accvgpr_write_b32 a4, v44\n\t" \
  "v_accvgpr_write_b32 a5, v45\n\t" \
  "v_accvgpr_write_b32 a6, v46\n\t" \
  "v_accvgpr_write_b32 a7, v47\n\t" \
  "v_perm_b32 v44, v53, v49, s62\n\t" \
  "v_perm_b32 v45, v61, v57, s62\n\t" \
  "v_perm_b32 v46, v69, v65, s62\n\t" \
  "v_perm_b32 v47, v77, v73, s62\n\t" \
  "v_accvgpr_write_b32 a8, v44\n\t" \
  "v_accvgpr_write_b32 a9, v45\n\t" \
  "v_accvgpr_write_b32 a10, v46\n\t" \
  "v_accvgpr_write_b32 a11, v47\n\t" \
  "v_perm_b32 v44, v53, v49, s63\n\t" \
  "v_perm_b32 v45, v61, v57, s63\n\t" \
  "v_perm_b32 v46, v69, v65, s63\n\t" \
  "v_perm_b32 v47, v77, v73, s63\n\t" \
  "v_accvgpr_write_b32 a12, v44\n\t" \
  "v_accvgpr_write_b32 a13, v45\n\t" \
  "v_accvgpr_write_b32 a14, v46\n\t" \
  "v_accvgpr_write_b32 a15, v47\n\t" \
  "v_perm_b32 v44, v54, v50, s62\n\t" \
  "v_perm_b32 v45, v62, v58, s62\n\t" \
  "v_perm_b32 v46, v70, v66, s62\n\t" \
  "v_perm_b32 v47, v78, v74, s62\n\t" \
  "v_accvgpr_write_b32 a16, v44\n\t" \
  "v_accvgpr_write_b32 a17, v45\n\t" \
  "v_accvgpr_write_b32 a18, v46\n\t" \
  "v_accvgpr_write_b32 a19, v47\n\t" \
  "v_perm_b32 v44, v54, v50, s63\n\t" \
  "v_perm_b32 v45, v62, v58, s63\n\t" \
  "v_perm_b32 v46, v70, v66, s63\n\t" \
  "v_perm_b32 v47, v78, v74, s63\n\t" \
  "v_accvgpr_write_b32 a20, v44\n\t" \
  "v_accvgpr_write_b32 a21, v45\n\t" \
  "v_accvgpr_write_b32 a22, v46\n\t" \
  "v_accvgpr_write_b32 a23, v47\n\t" \
  "v_perm_b32 v44, v55, v51, s62\n\t" \
  "v_perm_b32 v45, v63, v59, s62\n\t" \
  "v_perm_b32 v46, v71, v67, s62\n\t" \
  "v_perm_b32 v47, v79, v75, s62\n\t" \
  "v_accvgpr_write_b32 a24, v44\n\t" \
  "v_accvgpr_write_b32 a25, v45\n\t" \
  "v_accvgpr_write_b32 a26, v46\n\t" \
  "v_accvgpr_write_b32 a27, v47\n\t" \
  "v_perm_b32 v44, v55, v51, s63\n\t" \
  "v_perm_b32 v45, v63, v59, s63\n\t" \
  "v_perm_b32 v46, v71, v67, s63\n\t" \
  "v_perm_b32 v47, v79, v75, s63\n\t" \
  "v_accvgpr_write_b32 a28, v44\n\t" \
  "v_accvgpr_write_b32 a29, v45\n\t" \
  "v_accvgpr_write_b32 a30, v46\n\t" \
  "v_accvgpr_write_b32 a31, v47\n\t" \
  "s_xor_b32 s60, s66, 147456\n\t" \
  "s_add_u32 s60, s60, 36864\n\t" \
  "s_add_u32 s61, s60, 18432\n\t" \
  "buffer_load_dwordx4 v[48:51], v41, %[W], s60 offen offset:0\n\t" \
  "buffer_load_dwordx4 v[52:55], v41, %[W], s60 offen offset:1152\n\t" \
  "buffer_load_dwordx4 v[56:59], v41, %[W], s60 offen offset:2304\n\t" \
  "buffer_load_dwordx4 v[60:63], v41, %[W], s60 offen offset:3456\n\t" \
  "buffer_load_dwordx4 v[64:67], v41, %[W], s61 offen offset:0\n\t" \
  "buffer_load_dwordx4 v[68:71], v41, %[W], s61 offen offset:1152\n\t" \
  "buffer_load_dwordx4 v[72:75], v41, %[W], s61 offen offset:2304\n\t" \
  "buffer_load_dwordx4 v[76:79], v41, %[W], s61 offen offset:3456\n\t" \
  "s_waitcnt vmcnt(38)\n\t" \
  "v_perm_b32 v44, v84, v80, s62\n\t" \
  "s_waitcnt vmcnt(36)\n\t" \
  "v_perm_b32 v45, v92, v88, s62\n\t" \
  "s_waitcnt vmcnt(34)\n\t" \
  "v_perm_b32 v46, v100, v96, s62\n\t" \
  "s_waitcnt vmcnt(32)\n\t" \
  "v_perm_b32 v47, v108, v104, s62\n\t" \
  "v_accvgpr_write_b32 a32, v44\n\t" \
  "v_accvgpr_write_b32 a33, v45\n\t" \
  "v_accvgpr_write_b32 a34, v46\n\t" \
  "v_accvgpr_write_b32 a35, v47\n\t" \
  "v_perm_b32 v44, v84, v80, s63\n\t" \
  "v_perm_b32 v45, v92, v88, s63\n\t" \
  "v_perm_b32 v46, v100, v96, s63\n\t" \
  "v_perm_b32 v47, v108, v104, s63\n\t" \
  "v_accvgpr_write_b32 a36, v44\n\t" \
  "v_accvgpr_write_b32 a37, v45\n\t" \
  "v_accvgpr_write_b32 a38, v46\n\t" \
  "v_accvgpr_write_b32 a39, v47\n\t" \
  "v_perm_b32 v44, v85, v81, s62\n\t" \
  "v_perm_b32 v45, v93, v89, s62\n\t" \
  "v_perm_b32 v46, v101, v97, s62\n\t" \
  "v_perm_b32 v47, v109, v105, s62\n\t" \
  "v_accvgpr_write_b32 a40, v44\n\t" \
  "v_accvgpr_write_b32 a41, v45\n\t" \
  "v_accvgpr_write_b32 a42, v46\n\t" \
  "v_accvgpr_write_b32 a43, v47\n\t" \
  "v_perm_b32 v44, v85, v81, s63\n\t" \
  "v_perm_b32 v45, v93, v89, s63\n\t" \
  "v_perm_b32 v46, v101, v97, s63\n\t" \
  "v_perm_b32 v47, v109, v105, s63\n\t" \
  "v_accvgpr_write_b32 a44, v44\n\t" \
  "v_accvgpr_write_b32 a45, v45\n\t" \
  "v_accvgpr_write_b32 a46, v46\n\t" \
  "v_accvgpr_write_b32 a47, v47\n\t" \
  "v_perm_b32 v44, v86, v82, s62\n\t" \
  "v_perm_b32 v45, v94, v90, s62\n\t" \
  "v_perm_b32 v46, v102, v98, s62\n\t" \
  "v_perm_b32 v47, v110, v106, s62\n\t" \
  "v_accvgpr_write_b32 a48, v44\n\t" \
  "v_accvgpr_write_b32 a49, v45\n\t" \
  "v_accvgpr_write_b32 a50, v46\n\t" \
  "v_accvgpr_write_b32 a51, v47\n\t" \
  "v_perm_b32 v44, v86, v82, s63\n\t" \
  "v_perm_b32 v45, v94, v90, s63\n\t" \
  "v_perm_b32 v46, v102, v98, s63\n\t" \
  "v_perm_b32 v47, v110, v106, s63\n\t" \
  "v_accvgpr_write_b32 a52, v44\n\t" \
  "v_accvgpr_write_b32 a53, v45\n\t" \
  "v_accvgpr_write_b32 a54, v46\n\t" \
  "v_accvgpr_write_b32 a55, v47\n\t" \
  "v_perm_b32 v44, v87, v83, s62\n\t" \
  "v_perm_b32 v45, v95, v91, s62\n\t" \
  "v_perm_b32 v46, v103, v99, s62\n\t" \
  "v_perm_b32 v47, v111, v107, s62\n\t" \
  "v_accvgpr_write_b32 a56, v44\n\t" \
  "v_accvgpr_write_b32 a57, v45\n\t" \
  "v_accvgpr_write_b32 a58, v46\n\t" \
  "v_accvgpr_write_b32 a59, v47\n\t" \
  "v_perm_b32 v44, v87, v83, s63\n\t" \
  "v_perm_b32 v45, v95, v91, s63\n\t" \
  "v_perm_b32 v46, v103, v99, s63\n\t" \
  "v_perm_b32 v47, v111, v107, s63\n\t" \
  "v_accvgpr_write_b32 a60, v44\n\t" \
  "v_accvgpr_write_b32 a61, v45\n\t" \
  "v_accvgpr_write_b32 a62, v46\n\t" \
  "v_accvgpr_write_b32 a63, v47\n\t" \
  "s_xor_b32 s60, s66, 147456\n\t" \
  "s_add_u32 s60, s60, 73728\n\t" \
  "s_add_u32 s61, s60, 18432\n\t" \
  "buffer_load_dwordx4 v[80:83], v41, %[W], s60 offen offset:0\n\t" \
  "buffer_load_dwordx4 v[84:87], v41, %[W], s60 offen offset:1152\n\t" \
  "buffer_load_dwordx4 v[88:91], v41, %[W], s60 offen offset:2304\n\t" \
  "buffer_load_dwordx4 v[92:95], v41, %[W], s60 offen offset:3456\n\t" \
  "buffer_load_dwordx4 v[96:99], v41, %[W], s61 offen offset:0\n\t" \
  "buffer_load_dwordx4 v[100:103], v41, %[W], s61 offen offset:1152\n\t" \
  "buffer_load_dwordx4 v[104:107], v41, %[W], s61 offen offset:2304\n\t" \
  "buffer_load_dwordx4 v[108:111], v41, %[W], s61 offen offset:3456\n\t" \
  "s_waitcnt vmcnt(38)\n\t" \
  "v_perm_b32 v44, v116, v112, s62\n\t" \
  "s_waitcnt vmcnt(36)\n\t" \
  "v_perm_b32 v45, v124, v120, s62\n\t" \
  "s_waitcnt vmcnt(34)\n\t" \
  "v_perm_b32 v46, v132, v128, s62\n\t" \
  "s_waitcnt vmcnt(32)\n\t" \
  "v_perm_b32 v47, v140, v136, s62\n\t" \
  "v_accvgpr_write_b32 a64, v44\n\t" \
  "v_accvgpr_write_b32 a65, v45\n\t" \
  "v_accvgpr_write_b32 a66, v46\n\t" \
  "v_accvgpr_write_b32 a67, v47\n\t" \
  "v_perm_b32 v44, v116, v112, s63\n\t" \
  "v_perm_b32 v45, v124, v120, s63\n\t" \
  "v_perm_b32 v46, v132, v128, s63\n\t" \
  "v_perm_b32 v47, v140, v136, s63\n\t" \
  "v_accvgpr_write_b32 a68, v44\n\t" \
  "v_accvgpr_write_b32 a69, v45\n\t" \
  "v_accvgpr_write_b32 a70, v46\n\t" \
  "v_accvgpr_write_b32 a71, v47\n\t" \
  "v_perm_b32 v44, v117, v113, s62\n\t" \
  "v_perm_b32 v45, v125, v121, s62\n\t" \
  "v_perm_b32 v46, v133, v129, s62\n\t" \
  "v_perm_b32 v47, v141, v137, s62\n\t" \
  "v_accvgpr_write_b32 a72, v44\n\t" \
  "v_accvgpr_write_b32 a73, v45\n\t" \
  "v_accvgpr_write_b32 a74, v46\n\t" \
  "v_accvgpr_write_b32 a75, v47\n\t" \
  "v_perm_b32 v44, v117, v113, s63\n\t" \
  "v_perm_b32 v45, v125, v121, s63\n\t" \
  "v_perm_b32 v46, v133, v129, s63\n\t" \
  "v_perm_b32 v47, v141, v137, s63\n\t" \
  "v_accvgpr_write_b32 a76, v44\n\t" \
  "v_accvgpr_write_b32 a77, v45\n\t" \
  "v_accvgpr_write_b32 a78, v46\n\t" \
  "v_accvgpr_write_b32 a79, v47\n\t" \
  "v_perm_b32 v44, v118, v114, s62\n\t" \
  "v_perm_b32 v45, v126, v122, s62\n\t" \
  "v_perm_b32 v46, v134, v130, s62\n\t" \
  "v_perm_b32 v47, v142, v138, s62\n\t" \
  "v_accvgpr_write_b32 a80, v44\n\t" \
  "v_accvgpr_write_b32 a81, v45\n\t" \
  "v_accvgpr_write_b32 a82, v46\n\t" \
  "v_accvgpr_write_b32 a83, v47\n\t" \
  "v_perm_b32 v44, v118, v114, s63\n\t" \
  "v_perm_b32 v45, v126, v122, s63\n\t" \
  "v_perm_b32 v46, v134, v130, s63\n\t" \
  "v_perm_b32 v47, v142, v138, s63\n\t" \
  "v_accvgpr_write_b32 a84, v44\n\t" \
  "v_accvgpr_write_b32 a85, v45\n\t" \
  "v_accvgpr_write_b32 a86, v46\n\t" \
  "v_accvgpr_write_b32 a87, v47\n\t" \
  "v_perm_b32 v44, v119, v115, s62\n\t" \
  "v_perm_b32 v45, v127, v123, s62\n\t" \
  "v_perm_b32 v46, v135, v131, s62\n\t" \
  "v_perm_b32 v47, v143, v139, s62\n\t" \
  "v_accvgpr_write_b32 a88, v44\n\t" \
  "v_accvgpr_write_b32 a89, v45\n\t" \
  "v_accvgpr_write_b32 a90, v46\n\t" \
  "v_accvgpr_write_b32 a91, v47\n\t" \
  "v_perm_b32 v44, v119, v115, s63\n\t" \
  "v_perm_b32 v45, v127, v123, s63\n\t" \
  "v_perm_b32 v46, v135, v131, s63\n\t" \
  "v_perm_b32 v47, v143, v139, s63\n\t" \
  "v_accvgpr_write_b32 a92, v44\n\t" \
  "v_accvgpr_write_b32 a93, v45\n\t" \
  "v_accvgpr_write_b32 a94, v46\n\t" \
  "v_accvgpr_write_b32 a95, v47\n\t" \
  "s_xor_b32 s60, s66, 147456\n\t" \
  "s_add_u32 s60, s60, 110592\n\t" \
  "s_add_u32 s61, s60, 18432\n\t" \
  "buffer_load_dwordx4 v[112:115], v41, %[W], s60 offen offset:0\n\t" \
  "buffer_load_dwordx4 v[116:119], v41, %[W], s60 offen offset:1152\n\t" \
  "buffer_load_dwordx4 v[120:123], v41, %[W], s60 offen offset:2304\n\t" \
  "buffer_load_dwordx4 v[124:127], v41, %[W], s60 offen offset:3456\n\t" \
  "buffer_load_dwordx4 v[128:131], v41, %[W], s61 offen offset:0\n\t" \
  "buffer_load_dwordx4 v[132:135], v41, %[W], s61 offen offset:1152\n\t" \
  "buffer_load_dwordx4 v[136:139], v41, %[W], s61 offen offset:2304\n\t" \
  "buffer_load_dwordx4 v[140:143], v41, %[W], s61 offen offset:3456\n\t" \
  "s_waitcnt vmcnt(38)\n\t" \
  "v_perm_b32 v44, v148, v144, s62\n\t" \
  "s_waitcnt vmcnt(36)\n\t" \
  "v_perm_b32 v45, v156, v152, s62\n\t" \
  "s_waitcnt vmcnt(34)\n\t" \
  "v_perm_b32 v46, v164, v160, s62\n\t" \
  "s_waitcnt vmcnt(32)\n\t" \
  "v_perm_b32 v47, v172, v168, s62\n\t" \
  "v_accvgpr_write_b32 a96, v44\n\t" \
  "v_accvgpr_write_b32 a97, v45\n\t" \
  "v_accvgpr_write_b32 a98, v46\n\t" \
  "v_accvgpr_write_b32 a99, v47\n\t" \
  "v_perm_b32 v44, v148, v144, s63\n\t" \
  "v_perm_b32 v45, v156, v152, s63\n\t" \
  "v_perm_b32 v46, v164, v160, s63\n\t" \
  "v_perm_b32 v47, v172, v168, s63\n\t" \
  "v_accvgpr_write_b32 a100, v44\n\t" \
  "v_accvgpr_write_b32 a101, v45\n\t" \
  "v_accvgpr_write_b32 a102, v46\n\t" \
  "v_accvgpr_write_b32 a103, v47\n\t" \
  "v_perm_b32 v44, v149, v145, s62\n\t" \
  "v_perm_b32 v45, v157, v153, s62\n\t" \
  "v_perm_b32 v46, v165, v161, s62\n\t" \
  "v_perm_b32 v47, v173, v169, s62\n\t" \
  "v_accvgpr_write_b32 a104, v44\n\t" \
  "v_accvgpr_write_b32 a105, v45\n\t" \
  "v_accvgpr_write_b32 a106, v46\n\t" \
  "v_accvgpr_write_b32 a107, v47\n\t" \
  "v_perm_b32 v44, v149, v145, s63\n\t" \
  "v_perm_b32 v45, v157, v153, s63\n\t" \
  "v_perm_b32 v46, v165, v161, s63\n\t" \
  "v_perm_b32 v47, v173, v169, s63\n\t" \
  "v_accvgpr_write_b32 a108, v44\n\t" \
  "v_accvgpr_write_b32 a109, v45\n\t" \
  "v_accvgpr_write_b32 a110, v46\n\t" \
  "v_accvgpr_write_b32 a111, v47\n\t" \
  "v_perm_b32 v44, v150, v146, s62\n\t" \
  "v_perm_b32 v45, v158, v154, s62\n\t" \
  "v_perm_b32 v46, v166, v162, s62\n\t" \
  "v_perm_b32 v47, v174, v170, s62\n\t" \
  "v_accvgpr_write_b32 a112, v44\n\t" \
  "v_accvgpr_write_b32 a113, v45\n\t" \
  "v_accvgpr_write_b32 a114, v46\n\t" \
  "v_accvgpr_write_b32 a115, v47\n\t" \
  "v_perm_b32 v44, v150, v146, s63\n\t" \
  "v_perm_b32 v45, v158, v154, s63\n\t" \
  "v_perm_b32 v46, v166, v162, s63\n\t" \
  "v_perm_b32 v47, v174, v170, s63\n\t" \
  "v_accvgpr_write_b32 a116, v44\n\t" \
  "v_accvgpr_write_b32 a117, v45\n\t" \
  "v_accvgpr_write_b32 a118, v46\n\t" \
  "v_accvgpr_write_b32 a119, v47\n\t" \
  "v_perm_b32 v44, v151, v147, s62\n\t" \
  "v_perm_b32 v45, v159, v155, s62\n\t" \
  "v_perm_b32 v46, v167, v163, s62\n\t" \
  "v_perm_b32 v47, v175, v171, s62\n\t" \
  "v_accvgpr_write_b32 a120, v44\n\t" \
  "v_accvgpr_write_b32 a121, v45\n\t" \
  "v_accvgpr_write_b32 a122, v46\n\t" \
  "v_accvgpr_write_b32 a123, v47\n\t" \
  "v_perm_b32 v44, v151, v147, s63\n\t" \
  "v_perm_b32 v45, v159, v155, s63\n\t" \
  "v_perm_b32 v46, v167, v163, s63\n\t" \
  "v_perm_b32 v47, v175, v171, s63\n\t" \
  "v_accvgpr_write_b32 a124, v44\n\t" \
  "v_accvgpr_write_b32 a125, v45\n\t" \
  "v_accvgpr_write_b32 a126, v46\n\t" \
  "v_accvgpr_write_b32 a127, v47\n\t" \
  "s_xor_b32 s60, s66, 0\n\t" \
  "s_add_u32 s60, s60, 0\n\t" \
  "s_add_u32 s61, s60, 18432\n\t" \
  "buffer_load_ushort v224, v42, %[W], s60 offen offset:0\n\t" \
  "buffer_load_ushort v144, v42, %[W], s60 offen offset:1152\n\t" \
  "buffer_load_ushort v225, v42, %[W], s60 offen offset:2304\n\t" \
  "buffer_load_ushort v145, v42, %[W], s60 offen offset:3456\n\t" \
  "buffer_load_ushort v226, v42, %[W], s61 offen offset:0\n\t" \
  "buffer_load_ushort v146, v42, %[W], s61 offen offset:1152\n\t" \
  "buffer_load_ushort v227, v42, %[W], s61 offen offset:2304\n\t" \
  "buffer_load_ushort v147, v42, %[W], s61 offen offset:3456\n\t" \
  "s_xor_b32 s60, s66, 0\n\t" \
  "s_add_u32 s60, s60, 36864\n\t" \
  "s_add_u32 s61, s60, 18432\n\t" \
  "buffer_load_ushort v228, v42, %[W], s60 offen offset:0\n\t" \
  "buffer_load_ushort v148, v42, %[W], s60 offen offset:1152\n\t" \
  "buffer_load_ushort v229, v42, %[W], s60 offen offset:2304\n\t" \
  "buffer_load_ushort v149, v42, %[W], s60 offen offset:3456\n\t" \
  "buffer_load_ushort v230, v42, %[W], s61 offen offset:0\n\t" \
  "buffer_load_ushort v150, v42, %[W], s61 offen offset:1152\n\t" \
  "buffer_load_ushort v231, v42, %[W], s61 offen offset:2304\n\t" \
  "buffer_load_ushort v151, v42, %[W], s61 offen offset:3456\n\t" \
  "s_waitcnt vmcnt(46)\n\t" \
  "v_perm_b32 v44, v180, v176, s62\n\t" \
  "s_waitcnt vmcnt(44)\n\t" \
  "v_perm_b32 v45, v188, v184, s62\n\t" \
  "s_waitcnt vmcnt(42)\n\t" \
  "v_perm_b32 v46, v196, v192, s62\n\t" \
  "s_waitcnt vmcnt(40)\n\t" \
  "v_perm_b32 v47, v204, v200, s62\n\t" \
  "v_accvgpr_write_b32 a128, v44\n\t" \
  "v_accvgpr_write_b32 a129, v45\n\t" \
  "v_accvgpr_write_b32 a130, v46\n\t" \
  "v_accvgpr_write_b32 a131, v47\n\t" \
  "v_perm_b32 v44, v180, v176, s63\n\t" \
  "v_perm_b32 v45, v188, v184, s63\n\t" \
  "v_perm_b32 v46, v196, v192, s63\n\t" \
  "v_perm_b32 v47, v204, v200, s63\n\t" \
  "v_accvgpr_write_b32 a132, v44\n\t" \
  "v_accvgpr_write_b32 a133, v45\n\t" \
  "v_accvgpr_write_b32 a134, v46\n\t" \
  "v_accvgpr_write_b32 a135, v47\n\t" \
  "v_perm_b32 v44, v181, v177, s62\n\t" \
  "v_perm_b32 v45, v189, v185, s62\n\t" \
  "v_perm_b32 v46, v197, v193, s62\n\t" \
  "v_perm_b32 v47, v205, v201, s62\n\t" \
  "v_accvgpr_write_b32 a136, v44\n\t" \
  "v_accvgpr_write_b32 a137, v45\n\t" \
  "v_accvgpr_write_b32 a138, v46\n\t" \
  "v_accvgpr_write_b32 a139, v47\n\t" \
  "v_perm_b32 v44, v181, v177, s63\n\t" \
  "v_perm_b32 v45, v189, v185, s63\n\t" \
  "v_perm_b32 v46, v197, v193, s63\n\t" \
  "v_perm_b32 v47, v205, v201, s63\n\t" \
  "v_accvgpr_write_b32 a140, v44\n\t" \
  "v_accvgpr_write_b32 a141, v45\n\t" \
  "v_accvgpr_write_b32 a142, v46\n\t" \
  "v_accvgpr_write_b32 a143, v47\n\t" \
  "v_perm_b32 v44, v182, v178, s62\n\t" \
  "v_perm_b32 v45, v190, v186, s62\n\t" \
  "v_perm_b32 v46, v198, v194, s62\n\t" \
  "v_perm_b32 v47, v206, v202, s62\n\t" \
  "v_accvgpr_write_b32 a144, v44\n\t" \
  "v_accvgpr_write_b32 a145, v45\n\t" \
  "v_accvgpr_write_b32 a146, v46\n\t" \
  "v_accvgpr_write_b32 a147, v47\n\t" \
  "v_perm_b32 v44, v182, v178, s63\n\t" \
  "v_perm_b32 v45, v190, v186, s63\n\t" \
  "v_perm_b32 v46, v198, v194, s63\n\t" \
  "v_perm_b32 v47, v206, v202, s63\n\t" \
  "v_accvgpr_write_b32 a148, v44\n\t" \
  "v_accvgpr_write_b32 a149, v45\n\t" \
  "v_accvgpr_write_b32 a150, v46\n\t" \
  "v_accvgpr_write_b32 a151, v47\n\t" \
  "v_perm_b32 v44, v183, v179, s62\n\t" \
  "v_perm_b32 v45, v191, v187, s62\n\t" \
  "v_perm_b32 v46, v199, v195, s62\n\t" \
  "v_perm_b32 v47, v207, v203, s62\n\t" \
  "v_accvgpr_write_b32 a152, v44\n\t" \
  "v_accvgpr_write_b32 a153, v45\n\t" \
  "v_accvgpr_write_b32 a154, v46\n\t" \
  "v_accvgpr_write_b32 a155, v47\n\t" \
  "v_perm_b32 v44, v183, v179, s63\n\t" \
  "v_perm_b32 v45, v191, v187, s63\n\t" \
  "v_perm_b32 v46, v199, v195, s63\n\t" \
  "v_perm_b32 v47, v207, v203, s63\n\t" \
  "v_accvgpr_write_b32 a156, v44\n\t" \
  "v_accvgpr_write_b32 a157, v45\n\t" \
  "v_accvgpr_write_b32 a158, v46\n\t" \
  "v_accvgpr_write_b32 a159, v47\n\t" \
  "s_xor_b32 s60, s66, 0\n\t" \
  "s_add_u32 s60, s60, 73728\n\t" \
  "s_add_u32 s61, s60, 18432\n\t" \
  "buffer_load_ushort v232, v42, %[W], s60 offen offset:0\n\t" \
  "buffer_load_ushort v152, v42, %[W], s60 offen offset:1152\n\t" \
  "buffer_load_ushort v233, v42, %[W], s60 offen offset:2304\n\t" \
  "buffer_load_ushort v153, v42, %[W], s60 offen offset:3456\n\t" \
  "buffer_load_ushort v234, v42, %[W], s61 offen offset:0\n\t" \
  "buffer_load_ushort v154, v42, %[W], s61 offen offset:1152\n\t" \
  "buffer_load_ushort v235, v42, %[W], s61 offen offset:2304\n\t" \
  "buffer_load_ushort v155, v42, %[W], s61 offen offset:3456\n\t" \
  "s_xor_b32 s60, s66, 0\n\t" \
  "s_add_u32 s60, s60, 110592\n\t" \
  "s_add_u32 s61, s60, 18432\n\t" \
  "buffer_load_ushort v236, v42, %[W], s60 offen offset:0\n\t" \
  "buffer_load_ushort v156, v42, %[W], s60 offen offset:1152\n\t" \
  "buffer_load_ushort v237, v42, %[W], s60 offen offset:2304\n\t" \
  "buffer_load_ushort v157, v42, %[W], s60 offen offset:3456\n\t" \
  "buffer_load_ushort v238, v42, %[W], s61 offen offset:0\n\t" \
  "buffer_load_ushort v158, v42, %[W], s61 offen offset:1152\n\t" \
  "buffer_load_ushort v239, v42, %[W], s61 offen offset:2304\n\t" \
  "buffer_load_ushort v159, v42, %[W], s61 offen offset:3456\n\t" \
  "s_waitcnt vmcnt(54)\n\t" \
  "v_perm_b32 v44, v52, v48, s62\n\t" \
  "s_waitcnt vmcnt(52)\n\t" \
  "v_perm_b32 v45, v60, v56, s62\n\t" \
  "s_waitcnt vmcnt(50)\n\t" \
  "v_perm_b32 v46, v68, v64, s62\n\t" \
  "s_waitcnt vmcnt(48)\n\t" \
  "v_perm_b32 v47, v76, v72, s62\n\t" \
  "v_accvgpr_write_b32 a160, v44\n\t" \
  "v_accvgpr_write_b32 a161, v45\n\t" \
  "v_accvgpr_write_b32 a162, v46\n\t" \
  "v_accvgpr_write_b32 a163, v47\n\t" \
  "v_perm_b32 v44, v52, v48, s63\n\t" \
  "v_perm_b32 v45, v60, v56, s63\n\t" \
  "v_perm_b32 v46, v68, v64, s63\n\t" \
  "v_perm_b32 v47, v76, v72, s63\n\t" \
  "v_accvgpr_write_b32 a164, v44\n\t" \
  "v_accvgpr_write_b32 a165, v45\n\t" \
  "v_accvgpr_write_b32 a166, v46\n\t" \
  "v_accvgpr_write_b32 a167, v47\n\t" \
  "v_perm_b32 v44, v53, v49, s62\n\t" \
  "v_perm_b32 v45, v61, v57, s62\n\t" \
  "v_perm_b32 v46, v69, v65, s62\n\t" \
  "v_perm_b32 v47, v77, v73, s62\n\t" \
  "v_accvgpr_write_b32 a168, v44\n\t" \
  "v_accvgpr_write_b32 a169, v45\n\t" \
  "v_accvgpr_write_b32 a170, v46\n\t" \
  "v_accvgpr_write_b32 a171, v47\n\t" \
  "v_perm_b32 v44, v53, v49, s63\n\t" \
  "v_perm_b32 v45, v61, v57, s63\n\t" \
  "v_perm_b32 v46, v69, v65, s63\n\t" \
  "v_perm_b32 v47, v77, v73, s63\n\t" \
  "v_accvgpr_write_b32 a172, v44\n\t" \
  "v_accvgpr_write_b32 a173, v45\n\t" \
  "v_accvgpr_write_b32 a174, v46\n\t" \
  "v_accvgpr_write_b32 a175, v47\n\t" \
  "v_perm_b32 v44, v54, v50, s62\n\t" \
  "v_perm_b32 v45, v62, v58, s62\n\t" \
  "v_perm_b32 v46, v70, v66, s62\n\t" \
  "v_perm_b32 v47, v78, v74, s62\n\t" \
  "v_accvgpr_write_b32 a176, v44\n\t" \
  "v_accvgpr_write_b32 a177, v45\n\t" \
  "v_accvgpr_write_b32 a178, v46\n\t" \
  "v_accvgpr_write_b32 a179, v47\n\t" \
  "v_perm_b32 v44, v54, v50, s63\n\t" \
  "v_perm_b32 v45, v62, v58, s63\n\t" \
  "v_perm_b32 v46, v70, v66, s63\n\t" \
  "v_perm_b32 v47, v78, v74, s63\n\t" \
  "v_accvgpr_write_b32 a180, v44\n\t" \
  "v_accvgpr_write_b32 a181, v45\n\t" \
  "v_accvgpr_write_b32 a182, v46\n\t" \
  "v_accvgpr_write_b32 a183, v47\n\t" \
  "v_perm_b32 v44, v55, v51, s62\n\t" \
  "v_perm_b32 v45, v63, v59, s62\n\t" \
  "v_perm_b32 v46, v71, v67, s62\n\t" \
  "v_perm_b32 v47, v79, v75, s62\n\t" \
  "v_accvgpr_write_b32 a184, v44\n\t" \
  "v_accvgpr_write_b32 a185, v45\n\t" \
  "v_accvgpr_write_b32 a186, v46\n\t" \
  "v_accvgpr_write_b32 a187, v47\n\t" \
  "v_perm_b32 v44, v55, v51, s63\n\t" \
  "v_perm_b32 v45, v63, v59, s63\n\t" \
  "v_perm_b32 v46, v71, v67, s63\n\t" \
  "v_perm_b32 v47, v79, v75, s63\n\t" \
  "v_accvgpr_write_b32 a188, v44\n\t" \
  "v_accvgpr_write_b32 a189, v45\n\t" \
  "v_accvgpr_write_b32 a190, v46\n\t" \
  "v_accvgpr_write_b32 a191, v47\n\t" \
  "s_xor_b32 s60, s66, 147456\n\t" \
  "s_add_u32 s60, s60, 0\n\t" \
  "s_add_u32 s61, s60, 18432\n\t" \
  "buffer_load_ushort v240, v42, %[W], s60 offen offset:0\n\t" \
  "buffer_load_ushort v160, v42, %[W], s60 offen offset:1152\n\t" \
  "buffer_load_ushort v241, v42, %[W], s60 offen offset:2304\n\t" \
  "buffer_load_ushort v161, v42, %[W], s60 offen offset:3456\n\t" \
  "buffer_load_ushort v242, v42, %[W], s61 offen offset:0\n\t" \
  "buffer_load_ushort v162, v42, %[W], s61 offen offset:1152\n\t" \
  "buffer_load_ushort v243, v42, %[W], s61 offen offset:2304\n\t" \
  "buffer_load_ushort v163, v42, %[W], s61 offen offset:3456\n\t" \
  "s_waitcnt vmcnt(54)\n\t" \
  "v_perm_b32 v44, v84, v80, s62\n\t" \
  "s_waitcnt vmcnt(52)\n\t" \
  "v_perm_b32 v45, v92, v88, s62\n\t" \
  "s_waitcnt vmcnt(50)\n\t" \
  "v_perm_b32 v46, v100, v96, s62\n\t" \
  "s_waitcnt vmcnt(48)\n\t" \
  "v_perm_b32 v47, v108, v104, s62\n\t" \
  "v_accvgpr_write_b32 a192, v44\n\t" \
  "v_accvgpr_write_b32 a193, v45\n\t" \
  "v_accvgpr_write_b32 a194, v46\n\t" \
  "v_accvgpr_write_b32 a195, v47\n\t" \
  "v_perm_b32 v44, v84, v80, s63\n\t" \
  "v_perm_b32 v45, v92, v88, s63\n\t" \
  "v_perm_b32 v46, v100, v96, s63\n\t" \
  "v_perm_b32 v47, v108, v104, s63\n\t" \
  "v_accvgpr_write_b32 a196, v44\n\t" \
  "v_accvgpr_write_b32 a197, v45\n\t" \
  "v_accvgpr_write_b32 a198, v46\n\t" \
  "v_accvgpr_write_b32 a199, v47\n\t" \
  "v_perm_b32 v44, v85, v81, s62\n\t" \
  "v_perm_b32 v45, v93, v89, s62\n\t" \
  "v_perm_b32 v46, v101, v97, s62\n\t" \
  "v_perm_b32 v47, v109, v105, s62\n\t" \
  "v_accvgpr_write_b32 a200, v44\n\t" \
  "v_accvgpr_write_b32 a201, v45\n\t" \
  "v_accvgpr_write_b32 a202, v46\n\t" \
  "v_accvgpr_write_b32 a203, v47\n\t" \
  "v_perm_b32 v44, v85, v81, s63\n\t" \
  "v_perm_b32 v45, v93, v89, s63\n\t" \
  "v_perm_b32 v46, v101, v97, s63\n\t" \
  "v_perm_b32 v47, v109, v105, s63\n\t" \
  "v_accvgpr_write_b32 a204, v44\n\t" \
  "v_accvgpr_write_b32 a205, v45\n\t" \
  "v_accvgpr_write_b32 a206, v46\n\t" \
  "v_accvgpr_write_b32 a207, v47\n\t" \
  "v_perm_b32 v44, v86, v82, s62\n\t" \
  "v_perm_b32 v45, v94, v90, s62\n\t" \
  "v_perm_b32 v46, v102, v98, s62\n\t" \
  "v_perm_b32 v47, v110, v106, s62\n\t" \
  "v_accvgpr_write_b32 a208, v44\n\t" \
  "v_accvgpr_write_b32 a209, v45\n\t" \
  "v_accvgpr_write_b32 a210, v46\n\t" \
  "v_accvgpr_write_b32 a211, v47\n\t" \
  "v_perm_b32 v44, v86, v82, s63\n\t" \
  "v_perm_b32 v45, v94, v90, s63\n\t" \
  "v_perm_b32 v46, v102, v98, s63\n\t" \
  "v_perm_b32 v47, v110, v106, s63\n\t" \
  "v_accvgpr_write_b32 a212, v44\n\t" \
  "v_accvgpr_write_b32 a213, v45\n\t" \
  "v_accvgpr_write_b32 a214, v46\n\t" \
  "v_accvgpr_write_b32 a215, v47\n\t" \
  "v_perm_b32 v44, v87, v83, s62\n\t" \
  "v_perm_b32 v45, v95, v91, s62\n\t" \
  "v_perm_b32 v46, v103, v99, s62\n\t" \
  "v_perm_b32 v47, v111, v107, s62\n\t" \
  "v_accvgpr_write_b32 a216, v44\n\t" \
  "v_accvgpr_write_b32 a217, v45\n\t" \
  "v_accvgpr_write_b32 a218, v46\n\t" \
  "v_accvgpr_write_b32 a219, v47\n\t" \
  "v_perm_b32 v44, v87, v83, s63\n\t" \
  "v_perm_b32 v45, v95, v91, s63\n\t" \
  "v_perm_b32 v46, v103, v99, s63\n\t" \
  "v_perm_b32 v47, v111, v107, s63\n\t" \
  "v_accvgpr_write_b32 a220, v44\n\t" \
  "v_accvgpr_write_b32 a221, v45\n\t" \
  "v_accvgpr_write_b32 a222, v46\n\t" \
  "v_accvgpr_write_b32 a223, v47\n\t" \
  "s_xor_b32 s60, s66, 147456\n\t" \
  "s_add_u32 s60, s60, 36864\n\t" \
  "s_add_u32 s61, s60, 18432\n\t" \
  "buffer_load_ushort v244, v42, %[W], s60 offen offset:0\n\t" \
  "buffer_load_ushort v164, v42, %[W], s60 offen offset:1152\n\t" \
  "buffer_load_ushort v245, v42, %[W], s60 offen offset:2304\n\t" \
  "buffer_load_ushort v165, v42, %[W], s60 offen offset:3456\n\t" \
  "buffer_load_ushort v246, v42, %[W], s61 offen offset:0\n\t" \
  "buffer_load_ushort v166, v42, %[W], s61 offen offset:1152\n\t" \
  "buffer_load_ushort v247, v42, %[W], s61 offen offset:2304\n\t" \
  "buffer_load_ushort v167, v42, %[W], s61 offen offset:3456\n\t" \
  "s_waitcnt vmcnt(54)\n\t" \
  "v_perm_b32 v44, v116, v112, s62\n\t" \
  "s_waitcnt vmcnt(52)\n\t" \
  "v_perm_b32 v45, v124, v120, s62\n\t" \
  "s_waitcnt vmcnt(50)\n\t" \
  "v_perm_b32 v46, v132, v128, s62\n\t" \
  "s_waitcnt vmcnt(48)\n\t" \
  "v_perm_b32 v47, v140, v136, s62\n\t" \
  "v_accvgpr_write_b32 a224, v44\n\t" \
  "v_accvgpr_write_b32 a225, v45\n\t" \
  "v_accvgpr_write_b32 a226, v46\n\t" \
  "v_accvgpr_write_b32 a227, v47\n\t" \
  "v_perm_b32 v44, v116, v112, s63\n\t" \
  "v_perm_b32 v45, v124, v120, s63\n\t" \
  "v_perm_b32 v46, v132, v128, s63\n\t" \
  "v_perm_b32 v47, v140, v136, s63\n\t" \
  "v_accvgpr_write_b32 a228, v44\n\t" \
  "v_accvgpr_write_b32 a229, v45\n\t" \
  "v_accvgpr_write_b32 a230, v46\n\t" \
  "v_accvgpr_write_b32 a231, v47\n\t" \
  "v_perm_b32 v44, v117, v113, s62\n\t" \
  "v_perm_b32 v45, v125, v121, s62\n\t" \
  "v_perm_b32 v46, v133, v129, s62\n\t" \
  "v_perm_b32 v47, v141, v137, s62\n\t" \
  "v_accvgpr_write_b32 a232, v44\n\t" \
  "v_accvgpr_write_b32 a233, v45\n\t" \
  "v_accvgpr_write_b32 a234, v46\n\t" \
  "v_accvgpr_write_b32 a235, v47\n\t" \
  "v_perm_b32 v44, v117, v113, s63\n\t" \
  "v_perm_b32 v45, v125, v121, s63\n\t" \
  "v_perm_b32 v46, v133, v129, s63\n\t" \
  "v_perm_b32 v47, v141, v137, s63\n\t" \
  "v_accvgpr_write_b32 a236, v44\n\t" \
  "v_accvgpr_write_b32 a237, v45\n\t" \
  "v_accvgpr_write_b32 a238, v46\n\t" \
  "v_accvgpr_write_b32 a239, v47\n\t" \
  "v_perm_b32 v44, v118, v114, s62\n\t" \
  "v_perm_b32 v45, v126, v122, s62\n\t" \
  "v_perm_b32 v46, v134, v130, s62\n\t" \
  "v_perm_b32 v47, v142, v138, s62\n\t" \
  "v_accvgpr_write_b32 a240, v44\n\t" \
  "v_accvgpr_write_b32 a241, v45\n\t" \
  "v_accvgpr_write_b32 a242, v46\n\t" \
  "v_accvgpr_write_b32 a243, v47\n\t" \
  "v_perm_b32 v44, v118, v114, s63\n\t" \
  "v_perm_b32 v45, v126, v122, s63\n\t" \
  "v_perm_b32 v46, v134, v130, s63\n\t" \
  "v_perm_b32 v47, v142, v138, s63\n\t" \
  "v_accvgpr_write_b32 a244, v44\n\t" \
  "v_accvgpr_write_b32 a245, v45\n\t" \
  "v_accvgpr_write_b32 a246, v46\n\t" \
  "v_accvgpr_write_b32 a247, v47\n\t" \
  "v_perm_b32 v44, v119, v115, s62\n\t" \
  "v_perm_b32 v45, v127, v123, s62\n\t" \
  "v_perm_b32 v46, v135, v131, s62\n\t" \
  "v_perm_b32 v47, v143, v139, s62\n\t" \
  "v_accvgpr_write_b32 a248, v44\n\t" \
  "v_accvgpr_write_b32 a249, v45\n\t" \
  "v_accvgpr_write_b32 a250, v46\n\t" \
  "v_accvgpr_write_b32 a251, v47\n\t" \
  "v_perm_b32 v44, v119, v115, s63\n\t" \
  "v_perm_b32 v45, v127, v123, s63\n\t" \
  "v_perm_b32 v46, v135, v131, s63\n\t" \
  "v_perm_b32 v47, v143, v139, s63\n\t" \
  "v_accvgpr_write_b32 a252, v44\n\t" \
  "v_accvgpr_write_b32 a253, v45\n\t" \
  "v_accvgpr_write_b32 a254, v46\n\t" \
  "v_accvgpr_write_b32 a255, v47\n\t" \
  "s_xor_b32 s60, s66, 147456\n\t" \
  "s_add_u32 s60, s60, 73728\n\t" \
  "s_add_u32 s61, s60, 18432\n\t" \
  "buffer_load_ushort v248, v42, %[W], s60 offen offset:0\n\t" \
  "buffer_load_ushort v168, v42, %[W], s60 offen offset:1152\n\t" \
  "buffer_load_ushort v249, v42, %[W], s60 offen offset:2304\n\t" \
  "buffer_load_ushort v169, v42, %[W], s60 offen offset:3456\n\t" \
  "buffer_load_ushort v250, v42, %[W], s61 offen offset:0\n\t" \
  "buffer_load_ushort v170, v42, %[W], s61 offen offset:1152\n\t" \
  "buffer_load_ushort v251, v42, %[W], s61 offen offset:2304\n\t" \
  "buffer_load_ushort v171, v42, %[W], s61 offen offset:3456\n\t" \
  "s_xor_b32 s60, s66, 147456\n\t" \
  "s_add_u32 s60, s60, 110592\n\t" \
  "s_add_u32 s61, s60, 18432\n\t" \
  "s_waitcnt vmcnt(40)\n\t" \
  "buffer_load_ushort v252, v42, %[W], s60 offen offset:0\n\t" \
  "buffer_load_ushort v172, v42, %[W], s60 offen offset:1152\n\t" \
  "buffer_load_ushort v253, v42, %[W], s60 offen offset:2304\n\t" \
  "buffer_load_ushort v173, v42, %[W], s60 offen offset:3456\n\t" \
  "buffer_load_ushort v254, v42, %[W], s61 offen offset:0\n\t" \
  "buffer_load_ushort v174, v42, %[W], s61 offen offset:1152\n\t" \
  "buffer_load_ushort v255, v42, %[W], s61 offen offset:2304\n\t" \
  "buffer_load_ushort v175, v42, %[W], s61 offen offset:3456\n\t" \
  "v_lshl_or_b32 v224, v144, 16, v224\n\t" \
  "v_lshl_or_b32 v225, v145, 16, v225\n\t" \
  "v_lshl_or_b32 v226, v146, 16, v226\n\t" \
  "v_lshl_or_b32 v227, v147, 16, v227\n\t" \
  "v_lshl_or_b32 v228, v148, 16, v228\n\t" \
  "v_lshl_or_b32 v229, v149, 16, v229\n\t" \
  "v_lshl_or_b32 v230, v150, 16, v230\n\t" \
  "v_lshl_or_b32 v231, v151, 16, v231\n\t" \
  "s_waitcnt vmcnt(46)\n\t" \
  "v_lshl_or_b32 v232, v152, 16, v232\n\t" \
  "s_waitcnt vmcnt(44)\n\t" \
  "v_lshl_or_b32 v233, v153, 16, v233\n\t" \
  "s_waitcnt vmcnt(42)\n\t" \
  "v_lshl_or_b32 v234, v154, 16, v234\n\t" \
  "s_waitcnt vmcnt(40)\n\t" \
  "v_lshl_or_b32 v235, v155, 16, v235\n\t" \
  "s_waitcnt vmcnt(38)\n\t" \
  "v_lshl_or_b32 v236, v156, 16, v236\n\t" \
  "s_waitcnt vmcnt(36)\n\t" \
  "v_lshl_or_b32 v237, v157, 16, v237\n\t" \
  "s_waitcnt vmcnt(34)\n\t" \
  "v_lshl_or_b32 v238, v158, 16, v238\n\t" \
  "s_waitcnt vmcnt(32)\n\t" \
  "v_lshl_or_b32 v239, v159, 16, v239\n\t" \
  "s_waitcnt vmcnt(30)\n\t" \
  "v_lshl_or_b32 v240, v160, 16, v240\n\t" \
  "s_waitcnt vmcnt(28)\n\t" \
  "v_lshl_or_b32 v241, v161, 16, v241\n\t" \
  "s_waitcnt vmcnt(26)\n\t" \
  "v_lshl_or_b32 v242, v162, 16, v242\n\t" \
  "s_waitcnt vmcnt(24)\n\t" \
  "v_lshl_or_b32 v243, v163, 16, v243\n\t" \
  "s_waitcnt vmcnt(22)\n\t" \
  "v_lshl_or_b32 v244, v164, 16, v244\n\t" \
  "s_waitcnt vmcnt(20)\n\t" \
  "v_lshl_or_b32 v245, v165, 16, v245\n\t" \
  "s_waitcnt vmcnt(18)\n\t" \
  "v_lshl_or_b32 v246, v166, 16, v246\n\t" \
  "s_waitcnt vmcnt(16)\n\t" \
  "v_lshl_or_b32 v247, v167, 16, v247\n\t" \
  "s_waitcnt vmcnt(14)\n\t" \
  "v_lshl_or_b32 v248, v168, 16, v248\n\t" \
  "s_waitcnt vmcnt(12)\n\t" \
  "v_lshl_or_b32 v249, v169, 16, v249\n\t" \
  "s_waitcnt vmcnt(10)\n\t" \
  "v_lshl_or_b32 v250, v170, 16, v250\n\t" \
  "s_waitcnt vmcnt(8)\n\t" \
  "v_lshl_or_b32 v251, v171, 16, v251\n\t" \
  "s_waitcnt vmcnt(6)\n\t" \
  "v_lshl_or_b32 v252, v172, 16, v252\n\t" \
  "s_waitcnt vmcnt(4)\n\t" \
  "v_lshl_or_b32 v253, v173, 16, v253\n\t" \
  "s_waitcnt vmcnt(2)\n\t" \
  "v_lshl_or_b32 v254, v174, 16, v254\n\t" \
  "s_waitcnt vmcnt(0)\n\t" \
  "v_lshl_or_b32 v255, v175, 16, v255\n\t" \
  "s_mov_b32 s84, " TK_CAP "\n\t" \
  "s_mov_b32 s48, 0\n\t" \
  "s_mov_b32 s49, 0xf0000000\n\t" \
  "s_mov_b32 s46, s45\n\t" \
  "v_mov_b32 v5, -1\n\t" \
  "v_lshrrev_b32 v13, 2, v0\n\t" \
  "s_mov_b32 s60, " TK_CTAB "\n\t" \
  "buffer_load_dword v10, v13, %[X], s60 offen offset:88 sc1\n\t" \
  "buffer_load_dword v13, v13, %[X], s60 offen offset:0 sc1\n\t" \
  "s_waitcnt vmcnt(0)\n\t" \
  "v_and_b32 v10, 1, v10\n\t" \
  "v_and_b32 v13, 1, v13\n\t" \
  "v_cmp_ne_u32 s[90:91], s67, v10\n\t" \
  "v_cmp_ne_u32 s[88:89], s67, v13\n\t" \
  "s_nop 1\n\t" \
  "s_mov_b32 s86, s88\n\t" \
  "v_lshrrev_b32 v13, 2, v0\n\t" \
  "s_mov_b32 s60, " TK_CTAB "\n\t" \
  "buffer_load_dword v10, v13, %[X], s60 offen offset:88 sc1\n\t" \
  "buffer_load_dword v13, v13, %[X], s60 offen offset:344 sc1\n\t" \
  "s_waitcnt vmcnt(0)\n\t" \
  "v_and_b32 v10, 1, v10\n\t" \
  "v_and_b32 v13, 1, v13\n\t" \
  "v_cmp_ne_u32 s[90:91], s67, v10\n\t" \
  "v_cmp_ne_u32 s[88:89], s67, v13\n\t" \
  "s_nop 1\n\t" \
  "s_and_b32 s60, s88, 256\n\t" \
  "s_lshl_b32 s60, s60, 5\n\t" \
  "s_add_u32 s44, s44, s60\n\t" \
  "s_add_u32 s83, s48, 0\n\t" \
  "s_lshr_b64 s[60:61], s[90:91], s83\n\t" \
  "s_and_b32 s60, s60, 1\n\t" \
  "s_lshl_b32 s60, s60, 13\n\t" \
  "s_add_u32 s83, s83, " TK_SG0 "\n\t" \
  "s_lshl_b32 s83, s83, 21\n\t" \
  "s_or_b32 s83, s83, s60\n\t" \
  "LCOLD_%=:\n\t" \
  "s_add_u32 s62, s83, s41\n\t" \
  "s_mov_b32 s61, 0\n\t" \
  "LPQ_%=:\n\t" \
  "buffer_load_dword v10, v9, %[X], s62 offen sc1\n\t" \
  "s_waitcnt vmcnt(0)\n\t" \
  "v_cmp_eq_u32 vcc, 0, v10\n\t" \
  "s_cbranch_vccz LPQ_%=x\n\t" \
  "s_add_u32 s61, s61, 1\n\t" \
  "s_sleep 4\n\t" \
  "s_cmp_gt_u32 s61, s84\n\t" \
  "s_cbranch_scc1 LPQ_%=g\n\t" \
  "s_and_b32 s60, s61, 1023\n\t" \
  "s_cmp_lg_u32 s60, 0\n\t" \
  "s_cbranch_scc1 LPQ_%=\n\t" \
  "buffer_load_dword v10, off, %[X], s92 sc1\n\t" \
  "s_waitcnt vmcnt(0)\n\t" \
  "v_readfirstlane_b32 s60, v10\n\t" \
  "s_nop 0\n\t" \
  "s_cmp_eq_u32 s60, 0\n\t" \
  "s_cbranch_scc1 LPQ_%=\n\t" \
  "LPQ_%=g:\n\t" \
  "s_mov_b32 s84, 0\n\t" \
  "s_or_b32 s60, s48, 1073741824\n\t" \
  "v_mov_b32 v10, s60\n\t" \
  "s_mov_b64 exec, 1\n\t" \
  "buffer_store_dword v10, off, %[X], s92 sc1\n\t" \
  "s_mov_b64 exec, -1\n\t" \
  "LPQ_%=x:\n\t" \
  "s_add_u32 m0, s46, 0\n\t" \
  "s_add_u32 s47, s83, s36\n\t" \
  "buffer_load_dwordx4 v0, %[X], s47 offen offset:0 sc1 lds\n\t" \
  "buffer_load_dwordx4 v0, %[X], s47 offen offset:1024 sc1 lds\n\t" \
  "buffer_load_dwordx4 v0, %[X], s47 offen offset:2048 sc1 lds\n\t" \
  "buffer_load_dwordx4 v0, %[X], s47 offen offset:3072 sc1 lds\n\t" \
  "s_add_u32 m0, s46, 4096\n\t" \
  "s_add_u32 s47, s83, s37\n\t" \
  "buffer_load_dwordx4 v0, %[X], s47 offen offset:0 sc1 lds\n\t" \
  "buffer_load_dwordx4 v0, %[X], s47 offen offset:1024 sc1 lds\n\t" \
  "buffer_load_dwordx4 v0, %[X], s47 offen offset:2048 sc1 lds\n\t" \
  "buffer_load_dwordx4 v0, %[X], s47 offen offset:3072 sc1 lds\n\t" \
  "s_add_u32 m0, s46, 8192\n\t" \
  "s_add_u32 s47, s83, s38\n\t" \
  "buffer_load_dwordx4 v0, %[X], s47 offen offset:0 sc1 lds\n\t" \
  "buffer_load_dwordx4 v0, %[X], s47 offen offset:1024 sc1 lds\n\t" \
  "buffer_load_dwordx4 v0, %[X], s47 offen offset:2048 sc1 lds\n\t" \
  "buffer_load_dwordx4 v0, %[X], s47 offen offset:3072 sc1 lds\n\t" \
  "s_waitcnt vmcnt(0)\n\t" \
  "LTOP_%=:\n\t" \
  "s_memrealtime s[78:79]\n\t" \
  "v_readfirstlane_b32 s76, v5\n\t" \
  "s_waitcnt lgkmcnt(0)\n\t" \
  "s_add_u32 s60, s76, 1\n\t" \
  "s_not_b32 s76, s76\n\t" \
  "s_cmp_gt_u32 s60, 1\n\t" \
  "s_cselect_b32 s61, -1, 0\n\t" \
  "s_lshl_b32 s60, s78, 8\n\t" \
  "s_sub_u32 s76, s76, s60\n\t" \
  "s_ashr_i32 s76, s76, 8\n\t" \
  "s_add_i32 s76, s76, " TK_AX "\n\t" \
  "s_min_i32 s76, s76, 400\n\t" \
  "s_and_b32 s76, s76, s61\n\t" \
  "s_add_u32 s57, s78, s76\n\t" \
  TK_AC0 \
  "s_barrier\n\t" \
  TK_AC1 \
  "ds_read_b128 v[48:51], v2 offset:0\n\t" \
  "ds_read_b128 v[64:67], v1 offset:8192\n\t" \
  "ds_read_b128 v[80:83], v1 offset:12288\n\t" \
  "ds_read_b128 v[52:55], v2 offset:1024\n\t" \
  "ds_read_b128 v[68:71], v1 offset:9216\n\t" \
  "ds_read_b128 v[84:87], v1 offset:13312\n\t" \
  "ds_read_b128 v[56:59], v2 offset:2048\n\t" \
  "ds_read_b128 v[72:75], v1 offset:10240\n\t" \
  "ds_read_b128 v[88:91], v1 offset:14336\n\t" \
  "ds_read_b128 v[60:63], v2 offset:3072\n\t" \
  "ds_read_b128 v[76:79], v1 offset:11264\n\t" \
  "ds_read_b128 v[92:95], v1 offset:15360\n\t" \
  "s_mov_b32 s82, s83\n\t" \
  "s_xor_b32 s46, s46, 0x10000\n\t" \
  "s_add_u32 s58, s82, s41\n\t" \
  "s_add_u32 s58, s58, 4096\n\t" \
  "s_add_u32 s59, s82, s39\n\t" \
  "s_cmp_gt_i32 s76, 0\n\t" \
  "s_cselect_b32 s62, 0xf0000000, s58\n\t" \
  "buffer_load_dword v5, off, %[X], s62 sc1\n\t" \
  "s_waitcnt lgkmcnt(10)\n\t" \
  "v_mfma_f32_16x16x32_bf16 v[36:39], v[64:67], v[48:51], v[16:19]\n\t" \
  "ds_read_b128 v[96:99], v1 offset:16384\n\t" \
  "s_waitcnt lgkmcnt(10)\n\t" \
  "v_mfma_f32_16x16x32_bf16 v[40:43], v[80:83], v[48:51], v[20:23]\n\t" \
  "ds_read_b128 v[100:103], v1 offset:17408\n\t" \
  "s_waitcnt lgkmcnt(9)\n\t" \
  "v_mfma_f32_16x16x32_bf16 v[36:39], v[68:71], v[52:55], v[36:39]\n\t" \
  "ds_read_b128 v[104:107], v1 offset:18432\n\t" \
  "s_waitcnt lgkmcnt(9)\n\t" \
  "v_mfma_f32_16x16x32_bf16 v[40:43], v[84:87], v[52:55], v[40:43]\n\t" \
  "ds_read_b128 v[108:111], v1 offset:19456\n\t" \
  "s_waitcnt lgkmcnt(8)\n\t" \
  "v_mfma_f32_16x16x32_bf16 v[36:39], v[72:75], v[56:59], v[36:39]\n\t" \
  "ds_read_b128 v[112:115], v1 offset:20480\n\t" \
  "s_waitcnt lgkmcnt(8)\n\t" \
  "v_mfma_f32_16x16x32_bf16 v[40:43], v[88:91], v[56:59], v[40:43]\n\t" \
  "ds_read_b128 v[116:119], v1 offset:21504\n\t" \
  "s_waitcnt lgkmcnt(7)\n\t" \
  "v_mfma_f32_16x16x32_bf16 v[36:39], v[76:79], v[60:63], v[36:39]\n\t" \
  "ds_read_b128 v[120:123], v1 offset:22528\n\t" \
  "s_waitcnt lgkmcnt(7)\n\t" \
  "v_mfma_f32_16x16x32_bf16 v[40:43], v[92:95], v[60:63], v[40:43]\n\t" \
  "ds_read_b128 v[124:127], v1 offset:23552\n\t" \
  "s_add_u32 s83, s48, 1\n\t" \
  "s_lshr_b64 s[60:61], s[90:91], s83\n\t" \
  "s_and_b32 s60, s60, 1\n\t" \
  "s_lshl_b32 s60, s60, 13\n\t" \
  "s_add_u32 s83, s83, " TK_SG0 "\n\t" \
  "s_lshl_b32 s83, s83, 21\n\t" \
  "s_or_b32 s83, s83, s60\n\t" \
  "v_xor_b32 v1, 0x10000, v1\n\t" \
  "v_xor_b32 v2, 0x10000, v2\n\t" \
  "v_max3_f32 v11, v36, v37, v38\n\t" \
  "v_max3_f32 v11, v11, v39, v40\n\t" \
  "v_max3_f32 v13, v41, v42, v43\n\t" \
  "v_max_f32 v11, v11, v13\n\t" \
  "v_mov_b32 v13, v11\n\t" \
  "s_nop 1\n\t" \
  "v_permlane16_swap_b32 v11, v13\n\t" \
  "v_max_f32 v11, v11, v13\n\t" \
  "v_mov_b32 v13, v11\n\t" \
  "s_nop 1\n\t" \
  "v_permlane32_swap_b32 v11, v13\n\t" \
  "v_max_f32 v11, v11, v13\n\t" \
  "v_mul_f32 v13, 0xbe0293ee, v11\n\t" \
  "v_fmamk_f32 v36, v36, 0x3e0293ee, v13\n\t" \
  "v_fmamk_f32 v37, v37, 0x3e0293ee, v13\n\t" \
  "v_fmamk_f32 v38, v38, 0x3e0293ee, v13\n\t" \
  "v_fmamk_f32 v39, v39, 0x3e0293ee, v13\n\t" \
  "v_fmamk_f32 v40, v40, 0x3e0293ee, v13\n\t" \
  "v_fmamk_f32 v41, v41, 0x3e0293ee, v13\n\t" \
  "v_fmamk_f32 v42, v42, 0x3e0293ee, v13\n\t" \
  "v_fmamk_f32 v43, v43, 0x3e0293ee, v13\n\t" \
  "s_waitcnt vmcnt(0)\n\t" \
  "v_readfirstlane_b32 s76, v5\n\t" \
  "s_barrier\n\t" \
  "s_cmp_eq_u32 s76, 0\n\t" \
  "s_cbranch_scc1 LNG_%=\n\t" \
  "buffer_load_dwordx4 v[196:199], v0, %[X], s59 offen offset:0 sc1\n\t" \
  "buffer_load_dwordx4 v[200:203], v0, %[X], s59 offen offset:1024 sc1\n\t" \
  "buffer_load_dwordx2 v[204:205], v6, %[X], s59 offen offset:2048 sc1\n\t" \
  "buffer_load_dwordx2 v[206:207], v6, %[X], s59 offen offset:2560 sc1\n\t" \
  "buffer_load_dwordx2 v[208:209], v6, %[X], s59 offen offset:3072 sc1\n\t" \
  "buffer_load_dwordx2 v[210:211], v6, %[X], s59 offen offset:3584 sc1\n\t" \
  "LNG_%=:\n\t" \
  "s_mov_b64 exec, 1\n\t" \
  "buffer_store_dword v3, off, %[X], s49 sc1\n\t" \
  "s_mov_b64 exec, -1\n\t" \
  "s_mov_b32 s49, 0xf0000000\n\t" \
  "v_exp_f32 v36, v36\n\t" \
  "v_exp_f32 v37, v37\n\t" \
  "v_exp_f32 v38, v38\n\t" \
  "v_exp_f32 v39, v39\n\t" \
  "v_exp_f32 v40, v40\n\t" \
  "v_exp_f32 v41, v41\n\t" \
  "v_exp_f32 v42, v42\n\t" \
  "v_exp_f32 v43, v43\n\t" \
  "v_cvt_pk_bf16_f32 v44, v36, v37\n\t" \
  "v_cvt_pk_bf16_f32 v45, v38, v39\n\t" \
  "v_cvt_pk_bf16_f32 v46, v40, v41\n\t" \
  "v_cvt_pk_bf16_f32 v47, v42, v43\n\t" \
  "v_add_f32 v36, v36, v37\n\t" \
  "v_add_f32 v38, v38, v39\n\t" \
  "s_waitcnt lgkmcnt(7)\n\t" \
  "v_mfma_f32_16x16x32_bf16 v[128:131], v[96:99], v[44:47], 0\n\t" \
  "v_add_f32 v40, v40, v41\n\t" \
  "s_and_b32 s62, s48, 15\n\t" \
  "s_lshr_b32 s63, s62, 1\n\t" \
  "s_waitcnt lgkmcnt(6)\n\t" \
  "v_mfma_f32_16x16x32_bf16 v[132:135], v[100:103], v[44:47], 0\n\t" \
  "v_add_f32 v42, v42, v43\n\t" \
  "s_lshr_b32 s60, s88, s63\n\t" \
  "s_and_b32 s60, s60, 1\n\t" \
  "s_waitcnt lgkmcnt(5)\n\t" \
  "v_mfma_f32_16x16x32_bf16 v[136:139], v[104:107], v[44:47], 0\n\t" \
  "v_add_f32 v36, v36, v38\n\t" \
  "s_lshl_b32 s60, s60, 13\n\t" \
  "s_lshl_b32 s61, s62, 20\n\t" \
  "s_waitcnt lgkmcnt(4)\n\t" \
  "v_mfma_f32_16x16x32_bf16 v[140:143], v[108:111], v[44:47], 0\n\t" \
  "v_add_f32 v40, v40, v42\n\t" \
  "s_add_u32 s50, s61, s60\n\t" \
  "s_add_u32 s50, s50, s43\n\t" \
  "s_waitcnt lgkmcnt(3)\n\t" \
  "v_mfma_f32_16x16x32_bf16 v[144:147], v[112:115], v[44:47], 0\n\t" \
  "v_add_f32 v12, v36, v40\n\t" \
  "s_add_u32 s51, s50, 32768\n\t" \
  "s_add_u32 s52, s51, 32768\n\t" \
  "s_waitcnt lgkmcnt(2)\n\t" \
  "v_mfma_f32_16x16x32_bf16 v[148:151], v[116:119], v[44:47], 0\n\t" \
  "s_add_u32 s53, s52, 32768\n\t" \
  "s_lshl_b32 s61, s62, 17\n\t" \
  "s_waitcnt lgkmcnt(1)\n\t" \
  "v_mfma_f32_16x16x32_bf16 v[152:155], v[120:123], v[44:47], 0\n\t" \
  "s_add_u32 s54, s61, s44\n\t" \
  "s_add_u32 s63, s83, s41\n\t" \
  "s_waitcnt lgkmcnt(0)\n\t" \
  "v_mfma_f32_16x16x32_bf16 v[156:159], v[124:127], v[44:47], 0\n\t" \
  TK_AC2 \
  TK_AC3 \
  TK_PA0 \
  "s_cmp_lg_u32 s76, 0\n\t" \
  "s_cbranch_scc1 LGOT_%=\n\t" \
  "LKW_%=:\n\t" \
  "s_memrealtime s[78:79]\n\t" \
  "s_waitcnt lgkmcnt(0)\n\t" \
  "s_sub_i32 s79, s78, s57\n\t" \
  "s_cmp_lt_i32 s79, 0\n\t" \
  "s_cbranch_scc1 LKW_%=\n\t" \
  "s_mov_b32 s61, 0\n\t" \
  "LPK_%=:\n\t" \
  "buffer_load_dword v5, off, %[X], s58 sc1\n\t" \
  "s_waitcnt vmcnt(0)\n\t" \
  "v_cmp_eq_u32 vcc, 0, v5\n\t" \
  "s_cbranch_vccz LPK_%=x\n\t" \
  "s_add_u32 s61, s61, 1\n\t" \
  "s_sleep " TK_PSK "\n\t" \
  "s_cmp_gt_u32 s61, s84\n\t" \
  "s_cbranch_scc1 LPK_%=g\n\t" \
  "s_and_b32 s60, s61, 1023\n\t" \
  "s_cmp_lg_u32 s60, 0\n\t" \
  "s_cbranch_scc1 LPK_%=\n\t" \
  "buffer_load_dword v5, off, %[X], s92 sc1\n\t" \
  "s_waitcnt vmcnt(0)\n\t" \
  "v_readfirstlane_b32 s60, v5\n\t" \
  "s_nop 0\n\t" \
  "s_cmp_eq_u32 s60, 0\n\t" \
  "s_cbranch_scc1 LPK_%=\n\t" \
  "LPK_%=g:\n\t" \
  "s_mov_b32 s84, 0\n\t" \
  "s_or_b32 s60, s48, 1342177280\n\t" \
  "v_mov_b32 v5, s60\n\t" \
  "s_mov_b64 exec, 1\n\t" \
  "buffer_store_dword v5, off, %[X], s92 sc1\n\t" \
  "s_mov_b64 exec, -1\n\t" \
  "LPK_%=x:\n\t" \
  "buffer_load_dwordx4 v[196:199], v0, %[X], s59 offen offset:0 sc1\n\t" \
  "buffer_load_dwordx4 v[200:203], v0, %[X], s59 offen offset:1024 sc1\n\t" \
  "buffer_load_dwordx2 v[204:205], v6, %[X], s59 offen offset:2048 sc1\n\t" \
  "buffer_load_dwordx2 v[206:207], v6, %[X], s59 offen offset:2560 sc1\n\t" \
  "buffer_load_dwordx2 v[208:209], v6, %[X], s59 offen offset:3072 sc1\n\t" \
  "buffer_load_dwordx2 v[210:211], v6, %[X], s59 offen offset:3584 sc1\n\t" \
  "LGOT_%=:\n\t" \
  TK_PA1 \
  TK_AC4 \
  "buffer_load_dword v10, v9, %[X], s63 offen sc1\n\t" \
  "s_add_u32 s49, s82, s42\n\t" \
  "s_waitcnt vmcnt(6)\n\t" \
  "v_mfma_f32_16x16x32_bf16 v[36:39], v[196:199], v[48:51], 0\n\t" \
  "v_mfma_f32_16x16x32_bf16 v[40:43], v[196:199], v[52:55], 0\n\t" \
  "s_waitcnt vmcnt(5)\n\t" \
  "v_mfma_f32_16x16x32_bf16 v[36:39], v[200:203], v[56:59], v[36:39]\n\t" \
  "v_mfma_f32_16x16x32_bf16 v[40:43], v[200:203], v[60:63], v[40:43]\n\t" \
  TK_AC5 \
  "s_nop 8\n\t" \
  "v_add_f32 v36, v36, v42\n\t" \
  "v_add_f32 v37, v37, v43\n\t" \
  "v_max3_f32 v14, v36, v37, v11\n\t" \
  "v_mov_b32 v13, v14\n\t" \
  "s_nop 1\n\t" \
  "v_permlane16_swap_b32 v14, v13\n\t" \
  "v_max_f32 v14, v14, v13\n\t" \
  "v_mov_b32 v13, v14\n\t" \
  "s_nop 1\n\t" \
  "v_permlane32_swap_b32 v14, v13\n\t" \
  "v_max_f32 v14, v14, v13\n\t" \
  "v_mul_f32 v14, 0xbe0293ee, v14\n\t" \
  "v_fmamk_f32 v11, v11, 0x3e0293ee, v14\n\t" \
  "v_fmamk_f32 v36, v36, 0x3e0293ee, v14\n\t" \
  "v_fmamk_f32 v37, v37, 0x3e0293ee, v14\n\t" \
  "v_exp_f32 v11, v11\n\t" \
  "v_exp_f32 v36, v36\n\t" \
  "v_exp_f32 v37, v37\n\t" \
  "v_fma_f32 v12, v12, v11, v36\n\t" \
  "v_add_f32 v12, v12, v37\n\t" \
  "v_mov_b32 v13, v12\n\t" \
  "s_nop 1\n\t" \
  "v_permlane16_swap_b32 v12, v13\n\t" \
  "v_add_f32 v12, v12, v13\n\t" \
  "v_mov_b32 v13, v12\n\t" \
  "s_nop 1\n\t" \
  "v_permlane32_swap_b32 v12, v13\n\t" \
  "v_add_f32 v12, v12, v13\n\t" \
  "v_rcp_f32 v12, v12\n\t" \
  "s_nop 0\n\t" \
  "v_mul_f32 v14, v11, v12\n\t" \
  "v_mul_f32 v36, v36, v12\n\t" \
  "v_mul_f32 v37, v37, v12\n\t" \
  "v_cvt_pk_bf16_f32 v26, v36, v37\n\t" \
  "v_cvt_pk_bf16_f32 v33, v36, v37\n\t" \
  "v_pk_mul_f32 v[128:129], v[128:129], v[14:15] op_sel_hi:[1,0]\n\t" \
  "v_pk_mul_f32 v[130:131], v[130:131], v[14:15] op_sel_hi:[1,0]\n\t" \
  "v_pk_mul_f32 v[132:133], v[132:133], v[14:15] op_sel_hi:[1,0]\n\t" \
  "v_pk_mul_f32 v[134:135], v[134:135], v[14:15] op_sel_hi:[1,0]\n\t" \
  "v_pk_mul_f32 v[136:137], v[136:137], v[14:15] op_sel_hi:[1,0]\n\t" \
  "v_pk_mul_f32 v[138:139], v[138:139], v[14:15] op_sel_hi:[1,0]\n\t" \
  "v_pk_mul_f32 v[140:141], v[140:141], v[14:15] op_sel_hi:[1,0]\n\t" \
  "v_pk_mul_f32 v[142:143], v[142:143], v[14:15] op_sel_hi:[1,0]\n\t" \
  "s_waitcnt vmcnt(3)\n\t" \
  "v_mfma_f32_16x16x32_bf16 v[128:131], v[204:207], v[26:29], v[128:131]\n\t" \
  "v_mul_f32 v144, v144, v14\n\t" \
  "v_mul_f32 v145, v145, v14\n\t" \
  "v_mfma_f32_16x16x32_bf16 v[132:135], v[204:207], v[32:35], v[132:135]\n\t" \
  "v_mul_f32 v146, v146, v14\n\t" \
  "v_mul_f32 v147, v147, v14\n\t" \
  "v_mfma_f32_16x16x32_bf16 v[136:139], v[204:207], v[24:27], v[136:139]\n\t" \
  "v_mul_f32 v148, v148, v14\n\t" \
  "v_mul_f32 v149, v149, v14\n\t" \
  "v_mfma_f32_16x16x32_bf16 v[140:143], v[204:207], v[30:33], v[140:143]\n\t" \
  "v_cvt_pk_bf16_f32 v64, v128, v129\n\t" \
  "v_cvt_pk_bf16_f32 v65, v130, v131\n\t" \
  "v_cvt_pk_bf16_f32 v66, v132, v133\n\t" \
  "v_cvt_pk_bf16_f32 v67, v134, v135\n\t" \
  "v_mul_f32 v150, v150, v14\n\t" \
  "v_mul_f32 v151, v151, v14\n\t" \
  TK_AC6 \
  "v_mfma_f32_16x16x32_bf16 v[192:195], v[224:227], v[64:67], 0\n\t" \
  "ds_write_b128 v3, v[64:67] offset:0\n\t" \
  "v_cvt_pk_bf16_f32 v68, v136, v137\n\t" \
  "v_cvt_pk_bf16_f32 v69, v138, v139\n\t" \
  "v_mfma_f32_16x16x32_bf16 v[160:163], v[64:67], a[0:3], 0\n\t" \
  "v_cvt_pk_bf16_f32 v70, v140, v141\n\t" \
  "v_cvt_pk_bf16_f32 v71, v142, v143\n\t" \
  "s_waitcnt vmcnt(1)\n\t" \
  "v_mfma_f32_16x16x32_bf16 v[144:147], v[208:211], v[26:29], v[144:147]\n\t" \
  "v_mul_f32 v152, v152, v14\n\t" \
  "v_mul_f32 v153, v153, v14\n\t" \
  "v_mfma_f32_16x16x32_bf16 v[148:151], v[208:211], v[32:35], v[148:151]\n\t" \
  "v_mul_f32 v154, v154, v14\n\t" \
  "v_mul_f32 v155, v155, v14\n\t" \
  "v_mfma_f32_16x16x32_bf16 v[164:167], v[64:67], a[4:7], 0\n\t" \
  "ds_write_b128 v3, v[68:71] offset:1024\n\t" \
  "v_mul_f32 v156, v156, v14\n\t" \
  "v_mul_f32 v157, v157, v14\n\t" \
  "v_mfma_f32_16x16x32_bf16 v[168:171], v[64:67], a[8:11], 0\n\t" \
  "v_mul_f32 v158, v158, v14\n\t" \
  "v_mul_f32 v159, v159, v14\n\t" \
  "v_mfma_f32_16x16x32_bf16 v[172:175], v[64:67], a[12:15], 0\n\t" \
  "v_cvt_pk_bf16_f32 v72, v144, v145\n\t" \
  "v_cvt_pk_bf16_f32 v73, v146, v147\n\t" \
  "v_mfma_f32_16x16x32_bf16 v[152:155], v[208:211], v[24:27], v[152:155]\n\t" \
  "v_cvt_pk_bf16_f32 v74, v148, v149\n\t" \
  "v_cvt_pk_bf16_f32 v75, v150, v151\n\t" \
  "v_mfma_f32_16x16x32_bf16 v[156:159], v[208:211], v[30:33], v[156:159]\n\t" \
  "v_mfma_f32_16x16x32_bf16 v[176:179], v[64:67], a[16:19], 0\n\t" \
  "ds_write_b128 v3, v[72:75] offset:2048\n\t" \
  "v_mfma_f32_16x16x32_bf16 v[180:183], v[64:67], a[20:23], 0\n\t" \
  "v_mfma_f32_16x16x32_bf16 v[184:187], v[64:67], a[24:27], 0\n\t" \
  "v_cvt_pk_bf16_f32 v76, v152, v153\n\t" \
  "v_cvt_pk_bf16_f32 v77, v154, v155\n\t" \
  "v_mfma_f32_16x16x32_bf16 v[188:191], v[64:67], a[28:31], 0\n\t" \
  "v_cvt_pk_bf16_f32 v78, v156, v157\n\t" \
  "v_cvt_pk_bf16_f32 v79, v158, v159\n\t" \
  "v_mfma_f32_16x16x32_bf16 v[192:195], v[228:231], v[68:71], v[192:195]\n\t" \
  "ds_write_b128 v3, v[76:79] offset:3072\n\t" \
  "v_mfma_f32_16x16x32_bf16 v[160:163], v[68:71], a[32:35], v[160:163]\n\t" \
  "s_waitcnt vmcnt(0)\n\t" \
  "v_cmp_eq_u32 vcc, 0, v10\n\t" \
  "v_mfma_f32_16x16x32_bf16 v[164:167], v[68:71], a[36:39], v[164:167]\n\t" \
  "s_cmp_eq_u64 vcc, 0\n\t" \
  "s_cselect_b32 s55, s83, 0xf0000000\n\t" \
  "s_cmp_eq_u32 s48, 63\n\t" \
  "v_mfma_f32_16x16x32_bf16 v[168:171], v[68:71], a[40:43], v[168:171]\n\t" \
  "s_cselect_b32 s55, 0xf0000000, s55\n\t" \
  "s_mov_b32 s56, s83\n\t" \
  "s_mov_b32 s83, s55\n\t" \
  "v_mfma_f32_16x16x32_bf16 v[172:175], v[68:71], a[44:47], v[172:175]\n\t" \
  "v_mfma_f32_16x16x32_bf16 v[176:179], v[68:71], a[48:51], v[176:179]\n\t" \
  "s_add_u32 m0, s46, 0\n\t" \
  "s_add_u32 s47, s83, s36\n\t" \
  "buffer_load_dwordx4 v0, %[X], s47 offen offset:0 sc1 lds\n\t" \
  "v_mfma_f32_16x16x32_bf16 v[180:183], v[68:71], a[52:55], v[180:183]\n\t" \
  "v_mfma_f32_16x16x32_bf16 v[184:187], v[68:71], a[56:59], v[184:187]\n\t" \
  "s_waitcnt lgkmcnt(0)\n\t" \
  "s_barrier\n\t" \
  "v_mfma_f32_16x16x32_bf16 v[188:191], v[68:71], a[60:63], v[188:191]\n\t" \
  "ds_read_b128 v[80:83], v4 offset:0\n\t" \
  "v_mfma_f32_16x16x32_bf16 v[192:195], v[232:235], v[72:75], v[192:195]\n\t" \
  "buffer_load_dwordx4 v0, %[X], s47 offen offset:1024 sc1 lds\n\t" \
  "v_mfma_f32_16x16x32_bf16 v[160:163], v[72:75], a[64:67], v[160:163]\n\t" \
  "ds_read_b128 v[84:87], v4 offset:1024\n\t" \
  "v_mfma_f32_16x16x32_bf16 v[164:167], v[72:75], a[68:71], v[164:167]\n\t" \
  "v_mfma_f32_16x16x32_bf16 v[168:171], v[72:75], a[72:75], v[168:171]\n\t" \
  "ds_read_b128 v[88:91], v4 offset:2048\n\t" \
  "v_mfma_f32_16x16x32_bf16 v[172:175], v[72:75], a[76:79], v[172:175]\n\t" \
  "buffer_load_dwordx4 v0, %[X], s47 offen offset:2048 sc1 lds\n\t" \
  "v_mfma_f32_16x16x32_bf16 v[176:179], v[72:75], a[80:83], v[176:179]\n\t" \
  "ds_read_b128 v[92:95], v4 offset:3072\n\t" \
  "v_mfma_f32_16x16x32_bf16 v[180:183], v[72:75], a[84:87], v[180:183]\n\t" \
  "v_mfma_f32_16x16x32_bf16 v[184:187], v[72:75], a[88:91], v[184:187]\n\t" \
  "v_mfma_f32_16x16x32_bf16 v[188:191], v[72:75], a[92:95], v[188:191]\n\t" \
  "buffer_load_dwordx4 v0, %[X], s47 offen offset:3072 sc1 lds\n\t" \
  "v_mfma_f32_16x16x32_bf16 v[192:195], v[236:239], v[76:79], v[192:195]\n\t" \
  "s_add_u32 s62, s48, 2\n\t" \
  "v_mfma_f32_16x16x32_bf16 v[160:163], v[76:79], a[96:99], v[160:163]\n\t" \
  "v_mfma_f32_16x16x32_bf16 v[164:167], v[76:79], a[100:103], v[164:167]\n\t" \
  "v_mfma_f32_16x16x32_bf16 v[168:171], v[76:79], a[104:107], v[168:171]\n\t" \
  "s_add_u32 m0, s46, 4096\n\t" \
  "s_add_u32 s47, s83, s37\n\t" \
  "buffer_load_dwordx4 v0, %[X], s47 offen offset:0 sc1 lds\n\t" \
  "v_mfma_f32_16x16x32_bf16 v[172:175], v[76:79], a[108:111], v[172:175]\n\t" \
  "s_mul_i32 s63, s62, 43\n\t" \
  "s_lshr_b32 s63, s63, 7\n\t" \
  "v_mfma_f32_16x16x32_bf16 v[176:179], v[76:79], a[112:115], v[176:179]\n\t" \
  "v_mfma_f32_16x16x32_bf16 v[180:183], v[76:79], a[116:119], v[180:183]\n\t" \
  "v_mfma_f32_16x16x32_bf16 v[184:187], v[76:79], a[120:123], v[184:187]\n\t" \
  "buffer_load_dwordx4 v0, %[X], s47 offen offset:1024 sc1 lds\n\t" \
  "v_mfma_f32_16x16x32_bf16 v[188:191], v[76:79], a[124:127], v[188:191]\n\t" \
  "s_mul_i32 s62, s62, 688128\n\t" \
  "s_lshl_b32 s60, s63, 15\n\t" \
  "s_waitcnt lgkmcnt(3)\n\t" \
  "v_mfma_f32_16x16x32_bf16 v[192:195], v[240:243], v[80:83], v[192:195]\n\t" \
  "v_mfma_f32_16x16x32_bf16 v[160:163], v[80:83], a[128:131], v[160:163]\n\t" \
  "v_mfma_f32_16x16x32_bf16 v[164:167], v[80:83], a[132:135], v[164:167]\n\t" \
  "buffer_load_dwordx4 v0, %[X], s47 offen offset:2048 sc1 lds\n\t" \
  "v_mfma_f32_16x16x32_bf16 v[168:171], v[80:83], a[136:139], v[168:171]\n\t" \
  "s_add_u32 s62, s62, s60\n\t" \
  "s_lshr_b32 s60, s86, s63\n\t" \
  "v_mfma_f32_16x16x32_bf16 v[172:175], v[80:83], a[140:143], v[172:175]\n\t" \
  "v_mfma_f32_16x16x32_bf16 v[176:179], v[80:83], a[144:147], v[176:179]\n\t" \
  "v_mfma_f32_16x16x32_bf16 v[180:183], v[80:83], a[148:151], v[180:183]\n\t" \
  "buffer_load_dwordx4 v0, %[X], s47 offen offset:3072 sc1 lds\n\t" \
  "v_mfma_f32_16x16x32_bf16 v[184:187], v[80:83], a[152:155], v[184:187]\n\t" \
  "s_and_b32 s60, s60, 1\n\t" \
  "s_lshl_b32 s60, s60, 13\n\t" \
  "v_mfma_f32_16x16x32_bf16 v[188:191], v[80:83], a[156:159], v[188:191]\n\t" \
  "s_waitcnt lgkmcnt(2)\n\t" \
  "v_mfma_f32_16x16x32_bf16 v[192:195], v[244:247], v[84:87], v[192:195]\n\t" \
  "v_mfma_f32_16x16x32_bf16 v[160:163], v[84:87], a[160:163], v[160:163]\n\t" \
  "s_add_u32 m0, s46, 8192\n\t" \
  "s_add_u32 s47, s83, s38\n\t" \
  "buffer_load_dwordx4 v0, %[X], s47 offen offset:0 sc1 lds\n\t" \
  "v_mfma_f32_16x16x32_bf16 v[164:167], v[84:87], a[164:167], v[164:167]\n\t" \
  "s_add_u32 s62, s62, s60\n\t" \
  "s_add_u32 s62, s62, 656896\n\t" \
  "v_mfma_f32_16x16x32_bf16 v[168:171], v[84:87], a[168:171], v[168:171]\n\t" \
  "v_mfma_f32_16x16x32_bf16 v[172:175], v[84:87], a[172:175], v[172:175]\n\t" \
  "v_mfma_f32_16x16x32_bf16 v[176:179], v[84:87], a[176:179], v[176:179]\n\t" \
  "buffer_load_dwordx4 v0, %[X], s47 offen offset:1024 sc1 lds\n\t" \
  "v_mfma_f32_16x16x32_bf16 v[180:183], v[84:87], a[180:183], v[180:183]\n\t" \
  "s_cmp_gt_u32 s48, 61\n\t" \
  "s_cselect_b32 s62, 0xf0000000, s62\n\t" \
  "v_mfma_f32_16x16x32_bf16 v[184:187], v[84:87], a[184:187], v[184:187]\n\t" \
  "v_mfma_f32_16x16x32_bf16 v[188:191], v[84:87], a[188:191], v[188:191]\n\t" \
  "s_waitcnt lgkmcnt(1)\n\t" \
  "v_mfma_f32_16x16x32_bf16 v[192:195], v[248:251], v[88:91], v[192:195]\n\t" \
  "buffer_load_dwordx4 v0, %[X], s47 offen offset:2048 sc1 lds\n\t" \
  "v_mfma_f32_16x16x32_bf16 v[160:163], v[88:91], a[192:195], v[160:163]\n\t" \
  "v_mfma_f32_16x16x32_bf16 v[164:167], v[88:91], a[196:199], v[164:167]\n\t" \
  "v_mfma_f32_16x16x32_bf16 v[168:171], v[88:91], a[200:203], v[168:171]\n\t" \
  "v_mfma_f32_16x16x32_bf16 v[172:175], v[88:91], a[204:207], v[172:175]\n\t" \
  "buffer_load_dwordx4 v0, %[X], s47 offen offset:3072 sc1 lds\n\t" \
  "v_mfma_f32_16x16x32_bf16 v[176:179], v[88:91], a[208:211], v[176:179]\n\t" \
  "v_mfma_f32_16x16x32_bf16 v[180:183], v[88:91], a[212:215], v[180:183]\n\t" \
  "v_mfma_f32_16x16x32_bf16 v[184:187], v[88:91], a[216:219], v[184:187]\n\t" \
  "v_mfma_f32_16x16x32_bf16 v[188:191], v[88:91], a[220:223], v[188:191]\n\t" \
  "buffer_load_dword v5, off, %[X], s62 sc1\n\t" \
  "s_waitcnt lgkmcnt(0)\n\t" \
  "v_mfma_f32_16x16x32_bf16 v[192:195], v[252:255], v[92:95], v[192:195]\n\t" \
  "v_mfma_f32_16x16x32_bf16 v[160:163], v[92:95], a[224:227], v[160:163]\n\t" \
  "v_mfma_f32_16x16x32_bf16 v[164:167], v[92:95], a[228:231], v[164:167]\n\t" \
  "v_mfma_f32_16x16x32_bf16 v[168:171], v[92:95], a[232:235], v[168:171]\n\t" \
  "v_cvt_pk_bf16_f32 v112, v192, v193\n\t" \
  "v_cvt_pk_bf16_f32 v113, v194, v195\n\t" \
  "v_mfma_f32_16x16x32_bf16 v[172:175], v[92:95], a[236:239], v[172:175]\n\t" \
  "buffer_store_dwordx2 v[112:113], v8, %[X], s54 offen sc1\n\t" \
  "v_mfma_f32_16x16x32_bf16 v[176:179], v[92:95], a[240:243], v[176:179]\n\t" \
  "v_cvt_pk_bf16_f32 v96, v160, v164\n\t" \
  "v_cvt_pk_bf16_f32 v100, v161, v165\n\t" \
  "v_mfma_f32_16x16x32_bf16 v[180:183], v[92:95], a[244:247], v[180:183]\n\t" \
  "v_cvt_pk_bf16_f32 v104, v162, v166\n\t" \
  "v_cvt_pk_bf16_f32 v108, v163, v167\n\t" \
  "v_mfma_f32_16x16x32_bf16 v[184:187], v[92:95], a[248:251], v[184:187]\n\t" \
  "v_cvt_pk_bf16_f32 v97, v168, v172\n\t" \
  "v_cvt_pk_bf16_f32 v101, v169, v173\n\t" \
  "v_mfma_f32_16x16x32_bf16 v[188:191], v[92:95], a[252:255], v[188:191]\n\t" \
  "v_cvt_pk_bf16_f32 v105, v170, v174\n\t" \
  "v_cvt_pk_bf16_f32 v109, v171, v175\n\t" \
  "v_cvt_pk_bf16_f32 v98, v176, v180\n\t" \
  "v_cvt_pk_bf16_f32 v102, v177, v181\n\t" \
  "v_cvt_pk_bf16_f32 v106, v178, v182\n\t" \
  "v_cvt_pk_bf16_f32 v110, v179, v183\n\t" \
  TK_AC7 \
  "s_nop 1\n\t" \
  "v_cvt_pk_bf16_f32 v99, v184, v188\n\t" \
  "buffer_store_dwordx4 v[96:99], v7, %[X], s50 offen sc1\n\t" \
  "v_cvt_pk_bf16_f32 v103, v185, v189\n\t" \
  "buffer_store_dwordx4 v[100:103], v7, %[X], s51 offen sc1\n\t" \
  "v_cvt_pk_bf16_f32 v107, v186, v190\n\t" \
  "buffer_store_dwordx4 v[104:107], v7, %[X], s52 offen sc1\n\t" \
  "v_cvt_pk_bf16_f32 v111, v187, v191\n\t" \
  "buffer_store_dwordx4 v[108:111], v7, %[X], s53 offen sc1\n\t" \
  TK_PA2 \
  TK_AC8 \
  "s_cmp_eq_u32 s48, 63\n\t" \
  "s_cbranch_scc1 LEND_%=\n\t" \
  "s_add_u32 s48, s48, 1\n\t" \
  "s_cmp_eq_u32 s83, s56\n\t" \
  "s_mov_b32 s83, s56\n\t" \
  "s_cbranch_scc0 LCOLD_%=\n\t" \
  "s_waitcnt vmcnt(5)\n\t" \
  "s_branch LTOP_%=\n\t" \
  "LEND_%=:\n\t" \
  "s_waitcnt vmcnt(0)\n\t" \
  "s_barrier\n\t" \
  "s_mov_b64 exec, 1\n\t" \
  "buffer_store_dword v3, off, %[X], s49 sc1\n\t" \
  "s_mov_b64 exec, -1\n\t" \
  "s_mov_b32 s49, 0xf0000000\n\t" \
  TK_PA0 \
  TK_ACE \
  ""
#define QK_ASM \
  "v_lshrrev_b32 v16, 7, v0\n\t" \
  "v_lshlrev_b32 v16, 13, v16\n\t" \
  "v_lshrrev_b32 v19, 2, v0\n\t" \
  "v_and_b32 v19, 64, v19\n\t" \
  "v_xor_b32 v19, v19, v0\n\t" \
  "v_and_b32 v19, 0x70, v19\n\t" \
  "v_add_u32 v16, v16, v19\n\t" \
  "s_mul_i32 s63, s40, 40960\n\t" \
  "v_and_b32 v19, 0x300, v0\n\t" \
  "v_lshlrev_b32 v17, 2, v19\n\t" \
  "v_and_b32 v19, 0xc0, v0\n\t" \
  "v_lshl_add_u32 v17, v19, 1, v17\n\t" \
  "v_and_b32 v19, 0x30, v0\n\t" \
  "v_add3_u32 v17, v17, v19, s63\n\t" \
  "v_lshrrev_b32 v19, 1, v0\n\t" \
  "v_and_b32 v19, 64, v19\n\t" \
  "v_add_u32 v17, v17, v19\n\t" \
  "v_xor_b32 v18, 64, v17\n\t" \
  "s_add_u32 m0, s63, 0\n\t" \
  "s_mov_b32 s60, 0\n\t" \
  "buffer_load_dwordx4 v16, %[W], s60 offen lds\n\t" \
  "s_add_u32 m0, s63, 1024\n\t" \
  "s_mov_b32 s60, 65536\n\t" \
  "buffer_load_dwordx4 v16, %[W], s60 offen lds\n\t" \
  "s_add_u32 m0, s63, 2048\n\t" \
  "s_mov_b32 s60, 131072\n\t" \
  "buffer_load_dwordx4 v16, %[W], s60 offen lds\n\t" \
  "s_add_u32 m0, s63, 3072\n\t" \
  "s_mov_b32 s60, 196608\n\t" \
  "buffer_load_dwordx4 v16, %[W], s60 offen lds\n\t" \
  "s_add_u32 m0, s63, 4096\n\t" \
  "s_mov_b32 s60, 262144\n\t" \
  "buffer_load_dwordx4 v16, %[W], s60 offen lds\n\t" \
  "s_add_u32 m0, s63, 5120\n\t" \
  "s_mov_b32 s60, 327680\n\t" \
  "buffer_load_dwordx4 v16, %[W], s60 offen lds\n\t" \
  "s_add_u32 m0, s63, 6144\n\t" \
  "s_mov_b32 s60, 393216\n\t" \
  "buffer_load_dwordx4 v16, %[W], s60 offen lds\n\t" \
  "s_add_u32 m0, s63, 7168\n\t" \
  "s_mov_b32 s60, 458752\n\t" \
  "buffer_load_dwordx4 v16, %[W], s60 offen lds\n\t" \
  "s_add_u32 m0, s63, 8192\n\t" \
  "s_mov_b32 s60, 524288\n\t" \
  "buffer_load_dwordx4 v16, %[W], s60 offen lds\n\t" \
  "s_add_u32 m0, s63, 9216\n\t" \
  "s_mov_b32 s60, 589824\n\t" \
  "buffer_load_dwordx4 v16, %[W], s60 offen lds\n\t" \
  "s_add_u32 m0, s63, 10240\n\t" \
  "s_mov_b32 s60, 655360\n\t" \
  "buffer_load_dwordx4 v16, %[W], s60 offen lds\n\t" \
  "s_add_u32 m0, s63, 11264\n\t" \
  "s_mov_b32 s60, 720896\n\t" \
  "buffer_load_dwordx4 v16, %[W], s60 offen lds\n\t" \
  "s_add_u32 m0, s63, 12288\n\t" \
  "s_mov_b32 s60, 786432\n\t" \
  "buffer_load_dwordx4 v16, %[W], s60 offen lds\n\t" \
  "s_add_u32 m0, s63, 13312\n\t" \
  "s_mov_b32 s60, 851968\n\t" \
  "buffer_load_dwordx4 v16, %[W], s60 offen lds\n\t" \
  "s_add_u32 m0, s63, 14336\n\t" \
  "s_mov_b32 s60, 917504\n\t" \
  "buffer_load_dwordx4 v16, %[W], s60 offen lds\n\t" \
  "s_add_u32 m0, s63, 15360\n\t" \
  "s_mov_b32 s60, 983040\n\t" \
  "buffer_load_dwordx4 v16, %[W], s60 offen lds\n\t" \
  "s_add_u32 m0, s63, 16384\n\t" \
  "s_mov_b32 s60, 1048576\n\t" \
  "buffer_load_dwordx4 v16, %[W], s60 offen lds\n\t" \
  "s_add_u32 m0, s63, 17408\n\t" \
  "s_mov_b32 s60, 1114112\n\t" \
  "buffer_load_dwordx4 v16, %[W], s60 offen lds\n\t" \
  "s_add_u32 m0, s63, 18432\n\t" \
  "s_mov_b32 s60, 1179648\n\t" \
  "buffer_load_dwordx4 v16, %[W], s60 offen lds\n\t" \
  "s_add_u32 m0, s63, 19456\n\t" \
  "s_mov_b32 s60, 1245184\n\t" \
  "buffer_load_dwordx4 v16, %[W], s60 offen lds\n\t" \
  "s_add_u32 m0, s63, 20480\n\t" \
  "s_mov_b32 s60, 1310720\n\t" \
  "buffer_load_dwordx4 v16, %[W], s60 offen lds\n\t" \
  "s_add_u32 m0, s63, 21504\n\t" \
  "s_mov_b32 s60, 1376256\n\t" \
  "buffer_load_dwordx4 v16, %[W], s60 offen lds\n\t" \
  "s_add_u32 m0, s63, 22528\n\t" \
  "s_mov_b32 s60, 1441792\n\t" \
  "buffer_load_dwordx4 v16, %[W], s60 offen lds\n\t" \
  "s_add_u32 m0, s63, 23552\n\t" \
  "s_mov_b32 s60, 1507328\n\t" \
  "buffer_load_dwordx4 v16, %[W], s60 offen lds\n\t" \
  "s_add_u32 m0, s63, 24576\n\t" \
  "s_mov_b32 s60, 1572864\n\t" \
  "buffer_load_dwordx4 v16, %[W], s60 offen lds\n\t" \
  "s_add_u32 m0, s63, 25600\n\t" \
  "s_mov_b32 s60, 1638400\n\t" \
  "buffer_load_dwordx4 v16, %[W], s60 offen lds\n\t" \
  "s_add_u32 m0, s63, 26624\n\t" \
  "s_mov_b32 s60, 1703936\n\t" \
  "buffer_load_dwordx4 v16, %[W], s60 offen lds\n\t" \
  "s_add_u32 m0, s63, 27648\n\t" \
  "s_mov_b32 s60, 1769472\n\t" \
  "buffer_load_dwordx4 v16, %[W], s60 offen lds\n\t" \
  "s_add_u32 m0, s63, 28672\n\t" \
  "s_mov_b32 s60, 1835008\n\t" \
  "buffer_load_dwordx4 v16, %[W], s60 offen lds\n\t" \
  "s_add_u32 m0, s63, 29696\n\t" \
  "s_mov_b32 s60, 1900544\n\t" \
  "buffer_load_dwordx4 v16, %[W], s60 offen lds\n\t" \
  "s_add_u32 m0, s63, 30720\n\t" \
  "s_mov_b32 s60, 1966080\n\t" \
  "buffer_load_dwordx4 v16, %[W], s60 offen lds\n\t" \
  "s_add_u32 m0, s63, 31744\n\t" \
  "s_mov_b32 s60, 2031616\n\t" \
  "buffer_load_dwordx4 v16, %[W], s60 offen lds\n\t" \
  "s_add_u32 m0, s63, 32768\n\t" \
  "s_mov_b32 s60, 2097152\n\t" \
  "buffer_load_dwordx4 v16, %[W], s60 offen lds\n\t" \
  "s_add_u32 m0, s63, 33792\n\t" \
  "s_mov_b32 s60, 2162688\n\t" \
  "buffer_load_dwordx4 v16, %[W], s60 offen lds\n\t" \
  "s_add_u32 m0, s63, 34816\n\t" \
  "s_mov_b32 s60, 2228224\n\t" \
  "buffer_load_dwordx4 v16, %[W], s60 offen lds\n\t" \
  "s_add_u32 m0, s63, 35840\n\t" \
  "s_mov_b32 s60, 2293760\n\t" \
  "buffer_load_dwordx4 v16, %[W], s60 offen lds\n\t" \
  "s_add_u32 m0, s63, 36864\n\t" \
  "s_mov_b32 s60, 2359296\n\t" \
  "buffer_load_dwordx4 v16, %[W], s60 offen lds\n\t" \
  "s_add_u32 m0, s63, 37888\n\t" \
  "s_mov_b32 s60, 2424832\n\t" \
  "buffer_load_dwordx4 v16, %[W], s60 offen lds\n\t" \
  "s_add_u32 m0, s63, 38912\n\t" \
  "s_mov_b32 s60, 2490368\n\t" \
  "buffer_load_dwordx4 v16, %[W], s60 offen lds\n\t" \
  "s_add_u32 m0, s63, 39936\n\t" \
  "s_mov_b32 s60, 2555904\n\t" \
  "buffer_load_dwordx4 v16, %[W], s60 offen lds\n\t" \
  "s_waitcnt vmcnt(32)\n\t" \
  "ds_read_b64_tr_b16 a[0:1], v17 offset:0\n\t" \
  "ds_read_b64_tr_b16 a[2:3], v17 offset:512\n\t" \
  "ds_read_b64_tr_b16 a[4:5], v17 offset:4096\n\t" \
  "ds_read_b64_tr_b16 a[6:7], v17 offset:4608\n\t" \
  "ds_read_b64_tr_b16 a[72:73], v17 offset:8\n\t" \
  "ds_read_b64_tr_b16 a[74:75], v17 offset:520\n\t" \
  "ds_read_b64_tr_b16 a[76:77], v17 offset:4104\n\t" \
  "ds_read_b64_tr_b16 a[78:79], v17 offset:4616\n\t" \
  "ds_read_b64_tr_b16 a[144:145], v18 offset:0\n\t" \
  "ds_read_b64_tr_b16 a[146:147], v18 offset:512\n\t" \
  "ds_read_b64_tr_b16 a[148:149], v18 offset:4096\n\t" \
  "ds_read_b64_tr_b16 a[150:151], v18 offset:4608\n\t" \
  "ds_read_b64_tr_b16 a[216:217], v18 offset:8\n\t" \
  "ds_read_b64_tr_b16 a[218:219], v18 offset:520\n\t" \
  "ds_read_b64_tr_b16 a[220:221], v18 offset:4104\n\t" \
  "ds_read_b64_tr_b16 a[222:223], v18 offset:4616\n\t" \
  "s_waitcnt lgkmcnt(0)\n\t" \
  "s_add_u32 m0, s63, 0\n\t" \
  "s_mov_b32 s60, 2621440\n\t" \
  "buffer_load_dwordx4 v16, %[W], s60 offen lds\n\t" \
  "s_add_u32 m0, s63, 1024\n\t" \
  "s_mov_b32 s60, 2686976\n\t" \
  "buffer_load_dwordx4 v16, %[W], s60 offen lds\n\t" \
  "s_add_u32 m0, s63, 2048\n\t" \
  "s_mov_b32 s60, 2752512\n\t" \
  "buffer_load_dwordx4 v16, %[W], s60 offen lds\n\t" \
  "s_add_u32 m0, s63, 3072\n\t" \
  "s_mov_b32 s60, 2818048\n\t" \
  "buffer_load_dwordx4 v16, %[W], s60 offen lds\n\t" \
  "s_add_u32 m0, s63, 4096\n\t" \
  "s_mov_b32 s60, 2883584\n\t" \
  "buffer_load_dwordx4 v16, %[W], s60 offen lds\n\t" \
  "s_add_u32 m0, s63, 5120\n\t" \
  "s_mov_b32 s60, 2949120\n\t" \
  "buffer_load_dwordx4 v16, %[W], s60 offen lds\n\t" \
  "s_add_u32 m0, s63, 6144\n\t" \
  "s_mov_b32 s60, 3014656\n\t" \
  "buffer_load_dwordx4 v16, %[W], s60 offen lds\n\t" \
  "s_add_u32 m0, s63, 7168\n\t" \
  "s_mov_b32 s60, 3080192\n\t" \
  "buffer_load_dwordx4 v16, %[W], s60 offen lds\n\t" \
  "s_waitcnt vmcnt(32)\n\t" \
  "ds_read_b64_tr_b16 a[8:9], v17 offset:8192\n\t" \
  "ds_read_b64_tr_b16 a[10:11], v17 offset:8704\n\t" \
  "ds_read_b64_tr_b16 a[12:13], v17 offset:12288\n\t" \
  "ds_read_b64_tr_b16 a[14:15], v17 offset:12800\n\t" \
  "ds_read_b64_tr_b16 a[80:81], v17 offset:8200\n\t" \
  "ds_read_b64_tr_b16 a[82:83], v17 offset:8712\n\t" \
  "ds_read_b64_tr_b16 a[84:85], v17 offset:12296\n\t" \
  "ds_read_b64_tr_b16 a[86:87], v17 offset:12808\n\t" \
  "ds_read_b64_tr_b16 a[152:153], v18 offset:8192\n\t" \
  "ds_read_b64_tr_b16 a[154:155], v18 offset:8704\n\t" \
  "ds_read_b64_tr_b16 a[156:157], v18 offset:12288\n\t" \
  "ds_read_b64_tr_b16 a[158:159], v18 offset:12800\n\t" \
  "ds_read_b64_tr_b16 a[224:225], v18 offset:8200\n\t" \
  "ds_read_b64_tr_b16 a[226:227], v18 offset:8712\n\t" \
  "ds_read_b64_tr_b16 a[228:229], v18 offset:12296\n\t" \
  "ds_read_b64_tr_b16 a[230:231], v18 offset:12808\n\t" \
  "s_waitcnt lgkmcnt(0)\n\t" \
  "s_add_u32 m0, s63, 8192\n\t" \
  "s_mov_b32 s60, 3145728\n\t" \
  "buffer_load_dwordx4 v16, %[W], s60 offen lds\n\t" \
  "s_add_u32 m0, s63, 9216\n\t" \
  "s_mov_b32 s60, 3211264\n\t" \
  "buffer_load_dwordx4 v16, %[W], s60 offen lds\n\t" \
  "s_add_u32 m0, s63, 10240\n\t" \
  "s_mov_b32 s60, 3276800\n\t" \
  "buffer_load_dwordx4 v16, %[W], s60 offen lds\n\t" \
  "s_add_u32 m0, s63, 11264\n\t" \
  "s_mov_b32 s60, 3342336\n\t" \
  "buffer_load_dwordx4 v16, %[W], s60 offen lds\n\t" \
  "s_add_u32 m0, s63, 12288\n\t" \
  "s_mov_b32 s60, 3407872\n\t" \
  "buffer_load_dwordx4 v16, %[W], s60 offen lds\n\t" \
  "s_add_u32 m0, s63, 13312\n\t" \
  "s_mov_b32 s60, 3473408\n\t" \
  "buffer_load_dwordx4 v16, %[W], s60 offen lds\n\t" \
  "s_add_u32 m0, s63, 14336\n\t" \
  "s_mov_b32 s60, 3538944\n\t" \
  "buffer_load_dwordx4 v16, %[W], s60 offen lds\n\t" \
  "s_add_u32 m0, s63, 15360\n\t" \
  "s_mov_b32 s60, 3604480\n\t" \
  "buffer_load_dwordx4 v16, %[W], s60 offen lds\n\t" \
  "s_waitcnt vmcnt(32)\n\t" \
  "ds_read_b64_tr_b16 a[16:17], v17 offset:16384\n\t" \
  "ds_read_b64_tr_b16 a[18:19], v17 offset:16896\n\t" \
  "ds_read_b64_tr_b16 a[20:21], v17 offset:20480\n\t" \
  "ds_read_b64_tr_b16 a[22:23], v17 offset:20992\n\t" \
  "ds_read_b64_tr_b16 a[88:89], v17 offset:16392\n\t" \
  "ds_read_b64_tr_b16 a[90:91], v17 offset:16904\n\t" \
  "ds_read_b64_tr_b16 a[92:93], v17 offset:20488\n\t" \
  "ds_read_b64_tr_b16 a[94:95], v17 offset:21000\n\t" \
  "ds_read_b64_tr_b16 a[160:161], v18 offset:16384\n\t" \
  "ds_read_b64_tr_b16 a[162:163], v18 offset:16896\n\t" \
  "ds_read_b64_tr_b16 a[164:165], v18 offset:20480\n\t" \
  "ds_read_b64_tr_b16 a[166:167], v18 offset:20992\n\t" \
  "ds_read_b64_tr_b16 a[232:233], v18 offset:16392\n\t" \
  "ds_read_b64_tr_b16 a[234:235], v18 offset:16904\n\t" \
  "ds_read_b64_tr_b16 a[236:237], v18 offset:20488\n\t" \
  "ds_read_b64_tr_b16 a[238:239], v18 offset:21000\n\t" \
  "s_waitcnt lgkmcnt(0)\n\t" \
  "s_add_u32 m0, s63, 16384\n\t" \
  "s_mov_b32 s60, 3670016\n\t" \
  "buffer_load_dwordx4 v16, %[W], s60 offen lds\n\t" \
  "s_add_u32 m0, s63, 17408\n\t" \
  "s_mov_b32 s60, 3735552\n\t" \
  "buffer_load_dwordx4 v16, %[W], s60 offen lds\n\t" \
  "s_add_u32 m0, s63, 18432\n\t" \
  "s_mov_b32 s60, 3801088\n\t" \
  "buffer_load_dwordx4 v16, %[W], s60 offen lds\n\t" \
  "s_add_u32 m0, s63, 19456\n\t" \
  "s_mov_b32 s60, 3866624\n\t" \
  "buffer_load_dwordx4 v16, %[W], s60 offen lds\n\t" \
  "s_add_u32 m0, s63, 20480\n\t" \
  "s_mov_b32 s60, 3932160\n\t" \
  "buffer_load_dwordx4 v16, %[W], s60 offen lds\n\t" \
  "s_add_u32 m0, s63, 21504\n\t" \
  "s_mov_b32 s60, 3997696\n\t" \
  "buffer_load_dwordx4 v16, %[W], s60 offen lds\n\t" \
  "s_add_u32 m0, s63, 22528\n\t" \
  "s_mov_b32 s60, 4063232\n\t" \
  "buffer_load_dwordx4 v16, %[W], s60 offen lds\n\t" \
  "s_add_u32 m0, s63, 23552\n\t" \
  "s_mov_b32 s60, 4128768\n\t" \
  "buffer_load_dwordx4 v16, %[W], s60 offen lds\n\t" \
  "s_waitcnt vmcnt(32)\n\t" \
  "ds_read_b64_tr_b16 a[24:25], v17 offset:24576\n\t" \
  "ds_read_b64_tr_b16 a[26:27], v17 offset:25088\n\t" \
  "ds_read_b64_tr_b16 a[28:29], v17 offset:28672\n\t" \
  "ds_read_b64_tr_b16 a[30:31], v17 offset:29184\n\t" \
  "ds_read_b64_tr_b16 a[96:97], v17 offset:24584\n\t" \
  "ds_read_b64_tr_b16 a[98:99], v17 offset:25096\n\t" \
  "ds_read_b64_tr_b16 a[100:101], v17 offset:28680\n\t" \
  "ds_read_b64_tr_b16 a[102:103], v17 offset:29192\n\t" \
  "ds_read_b64_tr_b16 a[168:169], v18 offset:24576\n\t" \
  "ds_read_b64_tr_b16 a[170:171], v18 offset:25088\n\t" \
  "ds_read_b64_tr_b16 a[172:173], v18 offset:28672\n\t" \
  "ds_read_b64_tr_b16 a[174:175], v18 offset:29184\n\t" \
  "ds_read_b64_tr_b16 a[240:241], v18 offset:24584\n\t" \
  "ds_read_b64_tr_b16 a[242:243], v18 offset:25096\n\t" \
  "ds_read_b64_tr_b16 a[244:245], v18 offset:28680\n\t" \
  "ds_read_b64_tr_b16 a[246:247], v18 offset:29192\n\t" \
  "s_waitcnt lgkmcnt(0)\n\t" \
  "s_add_u32 m0, s63, 24576\n\t" \
  "s_mov_b32 s60, 4194304\n\t" \
  "buffer_load_dwordx4 v16, %[W], s60 offen lds\n\t" \
  "s_add_u32 m0, s63, 25600\n\t" \
  "s_mov_b32 s60, 4259840\n\t" \
  "buffer_load_dwordx4 v16, %[W], s60 offen lds\n\t" \
  "s_add_u32 m0, s63, 26624\n\t" \
  "s_mov_b32 s60, 4325376\n\t" \
  "buffer_load_dwordx4 v16, %[W], s60 offen lds\n\t" \
  "s_add_u32 m0, s63, 27648\n\t" \
  "s_mov_b32 s60, 4390912\n\t" \
  "buffer_load_dwordx4 v16, %[W], s60 offen lds\n\t" \
  "s_add_u32 m0, s63, 28672\n\t" \
  "s_mov_b32 s60, 4456448\n\t" \
  "buffer_load_dwordx4 v16, %[W], s60 offen lds\n\t" \
  "s_add_u32 m0, s63, 29696\n\t" \
  "s_mov_b32 s60, 4521984\n\t" \
  "buffer_load_dwordx4 v16, %[W], s60 offen lds\n\t" \
  "s_add_u32 m0, s63, 30720\n\t" \
  "s_mov_b32 s60, 4587520\n\t" \
  "buffer_load_dwordx4 v16, %[W], s60 offen lds\n\t" \
  "s_add_u32 m0, s63, 31744\n\t" \
  "s_mov_b32 s60, 4653056\n\t" \
  "buffer_load_dwordx4 v16, %[W], s60 offen lds\n\t" \
  "s_waitcnt vmcnt(32)\n\t" \
  "ds_read_b64_tr_b16 a[32:33], v17 offset:32768\n\t" \
  "ds_read_b64_tr_b16 a[34:35], v17 offset:33280\n\t" \
  "ds_read_b64_tr_b16 a[36:37], v17 offset:36864\n\t" \
  "ds_read_b64_tr_b16 a[38:39], v17 offset:37376\n\t" \
  "ds_read_b64_tr_b16 a[104:105], v17 offset:32776\n\t" \
  "ds_read_b64_tr_b16 a[106:107], v17 offset:33288\n\t" \
  "ds_read_b64_tr_b16 a[108:109], v17 offset:36872\n\t" \
  "ds_read_b64_tr_b16 a[110:111], v17 offset:37384\n\t" \
  "ds_read_b64_tr_b16 a[176:177], v18 offset:32768\n\t" \
  "ds_read_b64_tr_b16 a[178:179], v18 offset:33280\n\t" \
  "ds_read_b64_tr_b16 a[180:181], v18 offset:36864\n\t" \
  "ds_read_b64_tr_b16 a[182:183], v18 offset:37376\n\t" \
  "ds_read_b64_tr_b16 a[248:249], v18 offset:32776\n\t" \
  "ds_read_b64_tr_b16 a[250:251], v18 offset:33288\n\t" \
  "ds_read_b64_tr_b16 a[252:253], v18 offset:36872\n\t" \
  "ds_read_b64_tr_b16 a[254:255], v18 offset:37384\n\t" \
  "s_waitcnt lgkmcnt(0)\n\t" \
  "s_waitcnt vmcnt(24)\n\t" \
  "ds_read_b64_tr_b16 a[40:41], v17 offset:0\n\t" \
  "ds_read_b64_tr_b16 a[42:43], v17 offset:512\n\t" \
  "ds_read_b64_tr_b16 a[44:45], v17 offset:4096\n\t" \
  "ds_read_b64_tr_b16 a[46:47], v17 offset:4608\n\t" \
  "ds_read_b64_tr_b16 a[112:113], v17 offset:8\n\t" \
  "ds_read_b64_tr_b16 a[114:115], v17 offset:520\n\t" \
  "ds_read_b64_tr_b16 a[116:117], v17 offset:4104\n\t" \
  "ds_read_b64_tr_b16 a[118:119], v17 offset:4616\n\t" \
  "ds_read_b64_tr_b16 a[184:185], v18 offset:0\n\t" \
  "ds_read_b64_tr_b16 a[186:187], v18 offset:512\n\t" \
  "ds_read_b64_tr_b16 a[188:189], v18 offset:4096\n\t" \
  "ds_read_b64_tr_b16 a[190:191], v18 offset:4608\n\t" \
  "ds_read_b64_tr_b16 v[224:225], v18 offset:8\n\t" \
  "ds_read_b64_tr_b16 v[226:227], v18 offset:520\n\t" \
  "ds_read_b64_tr_b16 v[228:229], v18 offset:4104\n\t" \
  "ds_read_b64_tr_b16 v[230:231], v18 offset:4616\n\t" \
  "s_waitcnt lgkmcnt(0)\n\t" \
  "s_waitcnt vmcnt(16)\n\t" \
  "ds_read_b64_tr_b16 a[48:49], v17 offset:8192\n\t" \
  "ds_read_b64_tr_b16 a[50:51], v17 offset:8704\n\t" \
  "ds_read_b64_tr_b16 a[52:53], v17 offset:12288\n\t" \
  "ds_read_b64_tr_b16 a[54:55], v17 offset:12800\n\t" \
  "ds_read_b64_tr_b16 a[120:121], v17 offset:8200\n\t" \
  "ds_read_b64_tr_b16 a[122:123], v17 offset:8712\n\t" \
  "ds_read_b64_tr_b16 a[124:125], v17 offset:12296\n\t" \
  "ds_read_b64_tr_b16 a[126:127], v17 offset:12808\n\t" \
  "ds_read_b64_tr_b16 a[192:193], v18 offset:8192\n\t" \
  "ds_read_b64_tr_b16 a[194:195], v18 offset:8704\n\t" \
  "ds_read_b64_tr_b16 a[196:197], v18 offset:12288\n\t" \
  "ds_read_b64_tr_b16 a[198:199], v18 offset:12800\n\t" \
  "ds_read_b64_tr_b16 v[232:233], v18 offset:8200\n\t" \
  "ds_read_b64_tr_b16 v[234:235], v18 offset:8712\n\t" \
  "ds_read_b64_tr_b16 v[236:237], v18 offset:12296\n\t" \
  "ds_read_b64_tr_b16 v[238:239], v18 offset:12808\n\t" \
  "s_waitcnt lgkmcnt(0)\n\t" \
  "s_waitcnt vmcnt(8)\n\t" \
  "ds_read_b64_tr_b16 a[56:57], v17 offset:16384\n\t" \
  "ds_read_b64_tr_b16 a[58:59], v17 offset:16896\n\t" \
  "ds_read_b64_tr_b16 a[60:61], v17 offset:20480\n\t" \
  "ds_read_b64_tr_b16 a[62:63], v17 offset:20992\n\t" \
  "ds_read_b64_tr_b16 a[128:129], v17 offset:16392\n\t" \
  "ds_read_b64_tr_b16 a[130:131], v17 offset:16904\n\t" \
  "ds_read_b64_tr_b16 a[132:133], v17 offset:20488\n\t" \
  "ds_read_b64_tr_b16 a[134:135], v17 offset:21000\n\t" \
  "ds_read_b64_tr_b16 a[200:201], v18 offset:16384\n\t" \
  "ds_read_b64_tr_b16 a[202:203], v18 offset:16896\n\t" \
  "ds_read_b64_tr_b16 a[204:205], v18 offset:20480\n\t" \
  "ds_read_b64_tr_b16 a[206:207], v18 offset:20992\n\t" \
  "ds_read_b64_tr_b16 v[240:241], v18 offset:16392\n\t" \
  "ds_read_b64_tr_b16 v[242:243], v18 offset:16904\n\t" \
  "ds_read_b64_tr_b16 v[244:245], v18 offset:20488\n\t" \
  "ds_read_b64_tr_b16 v[246:247], v18 offset:21000\n\t" \
  "s_waitcnt lgkmcnt(0)\n\t" \
  "s_waitcnt vmcnt(0)\n\t" \
  "ds_read_b64_tr_b16 a[64:65], v17 offset:24576\n\t" \
  "ds_read_b64_tr_b16 a[66:67], v17 offset:25088\n\t" \
  "ds_read_b64_tr_b16 a[68:69], v17 offset:28672\n\t" \
  "ds_read_b64_tr_b16 a[70:71], v17 offset:29184\n\t" \
  "ds_read_b64_tr_b16 a[136:137], v17 offset:24584\n\t" \
  "ds_read_b64_tr_b16 a[138:139], v17 offset:25096\n\t" \
  "ds_read_b64_tr_b16 a[140:141], v17 offset:28680\n\t" \
  "ds_read_b64_tr_b16 a[142:143], v17 offset:29192\n\t" \
  "ds_read_b64_tr_b16 a[208:209], v18 offset:24576\n\t" \
  "ds_read_b64_tr_b16 a[210:211], v18 offset:25088\n\t" \
  "ds_read_b64_tr_b16 a[212:213], v18 offset:28672\n\t" \
  "ds_read_b64_tr_b16 a[214:215], v18 offset:29184\n\t" \
  "ds_read_b64_tr_b16 v[248:249], v18 offset:24584\n\t" \
  "ds_read_b64_tr_b16 v[250:251], v18 offset:25096\n\t" \
  "ds_read_b64_tr_b16 v[252:253], v18 offset:28680\n\t" \
  "ds_read_b64_tr_b16 v[254:255], v18 offset:29192\n\t" \
  "s_waitcnt lgkmcnt(0)\n\t" \
  "s_mov_b32 s84, " TK_CAP "\n\t" \
  "s_mov_b32 s48, 0\n\t" \
  "s_mov_b32 s49, 0xf0000000\n\t" \
  "s_mov_b32 s46, s41\n\t" \
  "s_mov_b32 s56, 0\n\t" \
  "v_lshrrev_b32 v7, 2, v0\n\t" \
  "s_mov_b32 s60, " TK_CTAB "\n\t" \
  "buffer_load_dword v4, v7, %[X], s60 offen offset:88 sc1\n\t" \
  "buffer_load_dword v7, v7, %[X], s60 offen offset:0 sc1\n\t" \
  "s_waitcnt vmcnt(0)\n\t" \
  "v_and_b32 v4, 1, v4\n\t" \
  "v_and_b32 v7, 1, v7\n\t" \
  "v_cmp_ne_u32 s[90:91], s67, v4\n\t" \
  "v_cmp_ne_u32 s[88:89], s67, v7\n\t" \
  "s_nop 1\n\t" \
  "s_mov_b32 s57, 0xf0000000\n\t" \
  "s_add_u32 s83, s48, 0\n\t" \
  "s_lshr_b64 s[60:61], s[90:91], s83\n\t" \
  "s_and_b32 s60, s60, 1\n\t" \
  "s_lshl_b32 s60, s60, 13\n\t" \
  "s_add_u32 s83, s83, " TK_SG0 "\n\t" \
  "s_lshl_b32 s83, s83, 21\n\t" \
  "s_or_b32 s83, s83, s60\n\t" \
  "s_add_u32 s62, s83, s39\n\t" \
  "v_add_u32 v7, s62, v3\n\t" \
  "LCOLD_%=:\n\t" \
  "s_waitcnt vmcnt(0)\n\t" \
  "s_mov_b64 exec, 1\n\t" \
  "buffer_store_dword v6, off, %[X], s49 sc1\n\t" \
  "s_mov_b64 exec, -1\n\t" \
  "s_mov_b32 s49, 0xf0000000\n\t" \
  "s_add_u32 s55, s83, s36\n\t" \
  "s_cmp_lg_u32 s40, 0\n\t" \
  "s_cbranch_scc1 LGO_%=\n\t" \
  "s_and_b32 s62, s45, 3\n\t" \
  "s_lshl_b32 s62, s62, 7\n\t" \
  "s_add_u32 s62, s62, s83\n\t" \
  "s_add_u32 s62, s62, s39\n\t" \
  "s_mov_b32 s61, 0\n\t" \
  "LPO_%=:\n\t" \
  "buffer_load_dword v4, off, %[X], s62 sc1\n\t" \
  "s_waitcnt vmcnt(0)\n\t" \
  "v_cmp_eq_u32 vcc, 0, v4\n\t" \
  "s_cbranch_vccz LPO_%=x\n\t" \
  "s_add_u32 s61, s61, 1\n\t" \
  "s_sleep " TK_PSH "\n\t" \
  "s_cmp_gt_u32 s61, s84\n\t" \
  "s_cbranch_scc1 LPO_%=g\n\t" \
  "s_and_b32 s60, s61, 1023\n\t" \
  "s_cmp_lg_u32 s60, 0\n\t" \
  "s_cbranch_scc1 LPO_%=\n\t" \
  "buffer_load_dword v4, off, %[X], s92 sc1\n\t" \
  "s_waitcnt vmcnt(0)\n\t" \
  "v_readfirstlane_b32 s60, v4\n\t" \
  "s_nop 0\n\t" \
  "s_cmp_eq_u32 s60, 0\n\t" \
  "s_cbranch_scc1 LPO_%=\n\t" \
  "LPO_%=g:\n\t" \
  "s_mov_b32 s84, 0\n\t" \
  "s_or_b32 s60, s48, 1610612736\n\t" \
  "v_mov_b32 v4, s60\n\t" \
  "s_mov_b64 exec, 1\n\t" \
  "buffer_store_dword v4, off, %[X], s92 sc1\n\t" \
  "s_mov_b64 exec, -1\n\t" \
  "LPO_%=x:\n\t" \
  "s_mov_b32 s61, 0\n\t" \
  "LPH_%=:\n\t" \
  "buffer_load_dword v4, v7, %[X], 0 offen sc1\n\t" \
  "s_waitcnt vmcnt(0)\n\t" \
  "v_cmp_eq_u32 vcc, 0, v4\n\t" \
  "s_cbranch_vccz LPH_%=x\n\t" \
  "s_add_u32 s61, s61, 1\n\t" \
  "s_sleep " TK_PSH "\n\t" \
  "s_cmp_gt_u32 s61, s84\n\t" \
  "s_cbranch_scc1 LPH_%=g\n\t" \
  "s_and_b32 s60, s61, 1023\n\t" \
  "s_cmp_lg_u32 s60, 0\n\t" \
  "s_cbranch_scc1 LPH_%=\n\t" \
  "buffer_load_dword v4, off, %[X], s92 sc1\n\t" \
  "s_waitcnt vmcnt(0)\n\t" \
  "v_readfirstlane_b32 s60, v4\n\t" \
  "s_nop 0\n\t" \
  "s_cmp_eq_u32 s60, 0\n\t" \
  "s_cbranch_scc1 LPH_%=\n\t" \
  "LPH_%=g:\n\t" \
  "s_mov_b32 s84, 0\n\t" \
  "s_or_b32 s60, s48, 1610612736\n\t" \
  "v_mov_b32 v4, s60\n\t" \
  "s_mov_b64 exec, 1\n\t" \
  "buffer_store_dword v4, off, %[X], s92 sc1\n\t" \
  "s_mov_b64 exec, -1\n\t" \
  "LPH_%=x:\n\t" \
  "s_cmp_lt_i32 s57, 0\n\t" \
  "s_cbranch_scc1 LGO_%=\n\t" \
  "s_movk_i32 s63, 4000\n\t" \
  "s_branch LGC_%=u\n\t" \
  "LGC_%=:\n\t" \
  "s_sleep " TK_PSG "\n\t" \
  "buffer_load_dword v8, off, %[X], s57 sc1\n\t" \
  "s_waitcnt vmcnt(0)\n\t" \
  "LGC_%=u:\n\t" \
  "s_memrealtime s[52:53]\n\t" \
  "v_readfirstlane_b32 s60, v8\n\t" \
  "s_waitcnt lgkmcnt(0)\n\t" \
  "s_sub_u32 s63, s63, 1\n\t" \
  "s_cbranch_scc1 LGO_%=\n\t" \
  "s_cmp_eq_u32 s60, -1\n\t" \
  "s_cbranch_scc1 LGC_%=\n\t" \
  "s_not_b32 s60, s60\n\t" \
  "s_lshl_b32 s61, s52, 8\n\t" \
  "s_sub_u32 s60, s60, s61\n\t" \
  "s_ashr_i32 s60, s60, 8\n\t" \
  "s_sub_i32 s60, s60, " TK_QXC "\n\t" \
  "s_min_i32 s60, s60, 400\n\t" \
  "s_add_u32 s54, s52, s60\n\t" \
  "LGC_%=w:\n\t" \
  "s_sleep 1\n\t" \
  "s_memrealtime s[52:53]\n\t" \
  "s_waitcnt lgkmcnt(0)\n\t" \
  "s_sub_i32 s53, s52, s54\n\t" \
  "s_cmp_lt_i32 s53, 0\n\t" \
  "s_cbranch_scc1 LGC_%=w\n\t" \
  "LGO_%=:\n\t" \
  "s_barrier\n\t" \
  "s_add_u32 m0, s46, 0\n\t" \
  "s_add_u32 s47, s55, 0\n\t" \
  "buffer_load_dwordx4 v0, %[X], s47 offen sc1 lds\n\t" \
  "s_add_u32 m0, s46, 4096\n\t" \
  "s_add_u32 s47, s55, 4096\n\t" \
  "buffer_load_dwordx4 v0, %[X], s47 offen sc1 lds\n\t" \
  "s_add_u32 m0, s46, 8192\n\t" \
  "s_add_u32 s47, s55, 16384\n\t" \
  "buffer_load_dwordx4 v0, %[X], s47 offen sc1 lds\n\t" \
  "s_add_u32 m0, s46, 12288\n\t" \
  "s_add_u32 s47, s55, 20480\n\t" \
  "buffer_load_dwordx4 v0, %[X], s47 offen sc1 lds\n\t" \
  "s_add_u32 m0, s46, 16384\n\t" \
  "s_add_u32 s47, s55, 32768\n\t" \
  "buffer_load_dwordx4 v0, %[X], s47 offen sc1 lds\n\t" \
  "s_add_u32 m0, s46, 20480\n\t" \
  "s_add_u32 s47, s55, 36864\n\t" \
  "buffer_load_dwordx4 v0, %[X], s47 offen sc1 lds\n\t" \
  "s_add_u32 m0, s46, 24576\n\t" \
  "s_add_u32 s47, s55, 49152\n\t" \
  "buffer_load_dwordx4 v0, %[X], s47 offen sc1 lds\n\t" \
  "s_add_u32 m0, s46, 28672\n\t" \
  "s_add_u32 s47, s55, 53248\n\t" \
  "buffer_load_dwordx4 v0, %[X], s47 offen sc1 lds\n\t" \
  "s_add_u32 m0, s46, 32768\n\t" \
  "s_add_u32 s47, s55, 65536\n\t" \
  "buffer_load_dwordx4 v0, %[X], s47 offen sc1 lds\n\t" \
  "s_waitcnt vmcnt(5)\n\t" \
  "LTOP_%=:\n\t" \
  "s_barrier\n\t" \
  TK_PQ1 \
  "ds_read_b128 v[64:67], v1 offset:0\n\t" \
  "ds_read_b128 v[68:71], v1 offset:2048\n\t" \
  "ds_read_b128 v[72:75], v2 offset:0\n\t" \
  "ds_read_b128 v[76:79], v2 offset:2048\n\t" \
  "ds_read_b128 v[80:83], v1 offset:4096\n\t" \
  "ds_read_b128 v[84:87], v1 offset:6144\n\t" \
  "s_mov_b32 s82, s83\n\t" \
  "s_xor_b32 s46, s46, 0x10000\n\t" \
  "s_waitcnt lgkmcnt(5)\n\t" \
  "v_mfma_f32_16x16x32_bf16 v[16:19], a[0:3], v[64:67], 0\n\t" \
  "ds_read_b128 v[88:91], v2 offset:4096\n\t" \
  "v_mfma_f32_16x16x32_bf16 v[20:23], a[72:75], v[64:67], 0\n\t" \
  "s_add_u32 s83, s48, 1\n\t" \
  "s_lshr_b64 s[60:61], s[90:91], s83\n\t" \
  "v_mfma_f32_16x16x32_bf16 v[24:27], a[144:147], v[64:67], 0\n\t" \
  "ds_read_b128 v[92:95], v2 offset:6144\n\t" \
  "v_mfma_f32_16x16x32_bf16 v[28:31], a[216:219], v[64:67], 0\n\t" \
  "s_and_b32 s60, s60, 1\n\t" \
  "s_lshl_b32 s60, s60, 13\n\t" \
  "s_waitcnt lgkmcnt(5)\n\t" \
  "v_mfma_f32_16x16x32_bf16 v[16:19], a[4:7], v[72:75], v[16:19]\n\t" \
  "ds_read_b128 v[96:99], v1 offset:8192\n\t" \
  "v_mfma_f32_16x16x32_bf16 v[20:23], a[76:79], v[72:75], v[20:23]\n\t" \
  "s_add_u32 s83, s83, " TK_SG0 "\n\t" \
  "s_lshl_b32 s83, s83, 21\n\t" \
  "v_mfma_f32_16x16x32_bf16 v[24:27], a[148:151], v[72:75], v[24:27]\n\t" \
  "ds_read_b128 v[100:103], v1 offset:10240\n\t" \
  "v_mfma_f32_16x16x32_bf16 v[28:31], a[220:223], v[72:75], v[28:31]\n\t" \
  "s_or_b32 s83, s83, s60\n\t" \
  "s_add_u32 s62, s83, s39\n\t" \
  "s_waitcnt lgkmcnt(5)\n\t" \
  "v_mfma_f32_16x16x32_bf16 v[16:19], a[8:11], v[80:83], v[16:19]\n\t" \
  "ds_read_b128 v[104:107], v2 offset:8192\n\t" \
  "v_mfma_f32_16x16x32_bf16 v[20:23], a[80:83], v[80:83], v[20:23]\n\t" \
  "v_add_u32 v7, s62, v3\n\t" \
  "s_add_u32 s50, s82, s37\n\t" \
  "v_mfma_f32_16x16x32_bf16 v[24:27], a[152:155], v[80:83], v[24:27]\n\t" \
  "ds_read_b128 v[108:111], v2 offset:10240\n\t" \
  "v_mfma_f32_16x16x32_bf16 v[28:31], a[224:227], v[80:83], v[28:31]\n\t" \
  "s_add_u32 s51, s50, 4096\n\t" \
  "s_waitcnt lgkmcnt(5)\n\t" \
  "v_mfma_f32_16x16x32_bf16 v[16:19], a[12:15], v[88:91], v[16:19]\n\t" \
  "ds_read_b128 v[112:115], v1 offset:12288\n\t" \
  "v_mfma_f32_16x16x32_bf16 v[20:23], a[84:87], v[88:91], v[20:23]\n\t" \
  "v_mfma_f32_16x16x32_bf16 v[24:27], a[156:159], v[88:91], v[24:27]\n\t" \
  "ds_read_b128 v[116:119], v1 offset:14336\n\t" \
  "v_mfma_f32_16x16x32_bf16 v[28:31], a[228:231], v[88:91], v[28:31]\n\t" \
  "s_waitcnt lgkmcnt(5)\n\t" \
  "v_mfma_f32_16x16x32_bf16 v[16:19], a[16:19], v[96:99], v[16:19]\n\t" \
  "ds_read_b128 v[120:123], v2 offset:12288\n\t" \
  "v_mfma_f32_16x16x32_bf16 v[20:23], a[88:91], v[96:99], v[20:23]\n\t" \
  "s_cmp_lg_u32 s56, 0\n\t" \
  "v_mfma_f32_16x16x32_bf16 v[24:27], a[160:163], v[96:99], v[24:27]\n\t" \
  "ds_read_b128 v[124:127], v2 offset:14336\n\t" \
  "v_mfma_f32_16x16x32_bf16 v[28:31], a[232:235], v[96:99], v[28:31]\n\t" \
  "s_cbranch_scc1 LWARM_%=\n\t" \
  "s_waitcnt vmcnt(0)\n\t" \
  "LWARM_%=:\n\t" \
  "s_barrier\n\t" \
  "s_waitcnt lgkmcnt(5)\n\t" \
  "v_mfma_f32_16x16x32_bf16 v[16:19], a[20:23], v[104:107], v[16:19]\n\t" \
  "ds_read_b128 v[128:131], v1 offset:16384\n\t" \
  "v_mfma_f32_16x16x32_bf16 v[20:23], a[92:95], v[104:107], v[20:23]\n\t" \
  "v_mfma_f32_16x16x32_bf16 v[24:27], a[164:167], v[104:107], v[24:27]\n\t" \
  "ds_read_b128 v[132:135], v1 offset:18432\n\t" \
  "v_mfma_f32_16x16x32_bf16 v[28:31], a[236:239], v[104:107], v[28:31]\n\t" \
  "s_add_i32 s62, s48, s42\n\t" \
  "s_add_i32 s62, s62, 1\n\t" \
  "s_waitcnt lgkmcnt(5)\n\t" \
  "v_mfma_f32_16x16x32_bf16 v[16:19], a[24:27], v[112:115], v[16:19]\n\t" \
  "ds_read_b128 v[136:139], v2 offset:16384\n\t" \
  "v_mfma_f32_16x16x32_bf16 v[20:23], a[96:99], v[112:115], v[20:23]\n\t" \
  "s_mul_i32 s63, s62, 43\n\t" \
  "s_lshr_b32 s63, s63, 7\n\t" \
  "v_mfma_f32_16x16x32_bf16 v[24:27], a[168:171], v[112:115], v[24:27]\n\t" \
  "ds_read_b128 v[140:143], v2 offset:18432\n\t" \
  "v_mfma_f32_16x16x32_bf16 v[28:31], a[240:243], v[112:115], v[28:31]\n\t" \
  "s_mul_i32 s57, s62, 688128\n\t" \
  "s_lshl_b32 s60, s63, 15\n\t" \
  "s_waitcnt lgkmcnt(5)\n\t" \
  "v_mfma_f32_16x16x32_bf16 v[16:19], a[28:31], v[120:123], v[16:19]\n\t" \
  "ds_read_b128 v[144:147], v1 offset:20480\n\t" \
  "v_mfma_f32_16x16x32_bf16 v[20:23], a[100:103], v[120:123], v[20:23]\n\t" \
  "s_add_u32 s57, s57, s60\n\t" \
  "s_lshr_b32 s60, s88, s63\n\t" \
  "v_mfma_f32_16x16x32_bf16 v[24:27], a[172:175], v[120:123], v[24:27]\n\t" \
  "ds_read_b128 v[148:151], v1 offset:22528\n\t" \
  "v_mfma_f32_16x16x32_bf16 v[28:31], a[244:247], v[120:123], v[28:31]\n\t" \
  "s_and_b32 s60, s60, 1\n\t" \
  "s_lshl_b32 s60, s60, 13\n\t" \
  "s_waitcnt lgkmcnt(5)\n\t" \
  "v_mfma_f32_16x16x32_bf16 v[16:19], a[32:35], v[128:131], v[16:19]\n\t" \
  "ds_read_b128 v[152:155], v2 offset:20480\n\t" \
  "v_mfma_f32_16x16x32_bf16 v[20:23], a[104:107], v[128:131], v[20:23]\n\t" \
  "s_add_u32 s57, s57, s60\n\t" \
  "s_add_u32 s57, s57, 656896\n\t" \
  "v_mfma_f32_16x16x32_bf16 v[24:27], a[176:179], v[128:131], v[24:27]\n\t" \
  "ds_read_b128 v[156:159], v2 offset:22528\n\t" \
  "v_mfma_f32_16x16x32_bf16 v[28:31], a[248:251], v[128:131], v[28:31]\n\t" \
  "s_cmp_lt_i32 s62, 1\n\t" \
  "s_cselect_b32 s57, 0xf0000000, s57\n\t" \
  "s_waitcnt lgkmcnt(5)\n\t" \
  "v_mfma_f32_16x16x32_bf16 v[16:19], a[36:39], v[136:139], v[16:19]\n\t" \
  "ds_read_b128 v[160:163], v1 offset:24576\n\t" \
  "v_mfma_f32_16x16x32_bf16 v[20:23], a[108:111], v[136:139], v[20:23]\n\t" \
  "s_cmp_lg_u32 s40, 0\n\t" \
  "s_cselect_b32 s57, 0xf0000000, s57\n\t" \
  "v_mfma_f32_16x16x32_bf16 v[24:27], a[180:183], v[136:139], v[24:27]\n\t" \
  "ds_read_b128 v[164:167], v1 offset:26624\n\t" \
  "v_mfma_f32_16x16x32_bf16 v[28:31], a[252:255], v[136:139], v[28:31]\n\t" \
  "s_waitcnt lgkmcnt(5)\n\t" \
  "v_mfma_f32_16x16x32_bf16 v[16:19], a[40:43], v[144:147], v[16:19]\n\t" \
  "ds_read_b128 v[168:171], v2 offset:24576\n\t" \
  "v_mfma_f32_16x16x32_bf16 v[20:23], a[112:115], v[144:147], v[20:23]\n\t" \
  "v_mfma_f32_16x16x32_bf16 v[24:27], a[184:187], v[144:147], v[24:27]\n\t" \
  "ds_read_b128 v[172:175], v2 offset:26624\n\t" \
  "v_mfma_f32_16x16x32_bf16 v[28:31], v[224:227], v[144:147], v[28:31]\n\t" \
  "s_waitcnt lgkmcnt(5)\n\t" \
  "v_mfma_f32_16x16x32_bf16 v[16:19], a[44:47], v[152:155], v[16:19]\n\t" \
  "ds_read_b128 v[176:179], v1 offset:28672\n\t" \
  "v_mfma_f32_16x16x32_bf16 v[20:23], a[116:119], v[152:155], v[20:23]\n\t" \
  "v_mfma_f32_16x16x32_bf16 v[24:27], a[188:191], v[152:155], v[24:27]\n\t" \
  "ds_read_b128 v[180:183], v1 offset:30720\n\t" \
  "v_mfma_f32_16x16x32_bf16 v[28:31], v[228:231], v[152:155], v[28:31]\n\t" \
  "s_waitcnt lgkmcnt(5)\n\t" \
  "v_mfma_f32_16x16x32_bf16 v[16:19], a[48:51], v[160:163], v[16:19]\n\t" \
  "ds_read_b128 v[184:187], v2 offset:28672\n\t" \
  "v_mfma_f32_16x16x32_bf16 v[20:23], a[120:123], v[160:163], v[20:23]\n\t" \
  TK_PQ0 \
  "v_mfma_f32_16x16x32_bf16 v[24:27], a[192:195], v[160:163], v[24:27]\n\t" \
  "ds_read_b128 v[188:191], v2 offset:30720\n\t" \
  "v_mfma_f32_16x16x32_bf16 v[28:31], v[232:235], v[160:163], v[28:31]\n\t" \
  "s_waitcnt lgkmcnt(5)\n\t" \
  "v_mfma_f32_16x16x32_bf16 v[16:19], a[52:55], v[168:171], v[16:19]\n\t" \
  "ds_read_b128 v[192:195], v1 offset:32768\n\t" \
  "v_mfma_f32_16x16x32_bf16 v[20:23], a[124:127], v[168:171], v[20:23]\n\t" \
  "v_mfma_f32_16x16x32_bf16 v[24:27], a[196:199], v[168:171], v[24:27]\n\t" \
  "ds_read_b128 v[196:199], v1 offset:34816\n\t" \
  "v_mfma_f32_16x16x32_bf16 v[28:31], v[236:239], v[168:171], v[28:31]\n\t" \
  "s_waitcnt lgkmcnt(5)\n\t" \
  "v_mfma_f32_16x16x32_bf16 v[16:19], a[56:59], v[176:179], v[16:19]\n\t" \
  "ds_read_b128 v[200:203], v2 offset:32768\n\t" \
  "v_mfma_f32_16x16x32_bf16 v[20:23], a[128:131], v[176:179], v[20:23]\n\t" \
  "v_mfma_f32_16x16x32_bf16 v[24:27], a[200:203], v[176:179], v[24:27]\n\t" \
  "ds_read_b128 v[204:207], v2 offset:34816\n\t" \
  "v_mfma_f32_16x16x32_bf16 v[28:31], v[240:243], v[176:179], v[28:31]\n\t" \
  "s_waitcnt lgkmcnt(5)\n\t" \
  "v_mfma_f32_16x16x32_bf16 v[16:19], a[60:63], v[184:187], v[16:19]\n\t" \
  "v_mfma_f32_16x16x32_bf16 v[20:23], a[132:135], v[184:187], v[20:23]\n\t" \
  "buffer_load_dword v4, v7, %[X], 0 offen sc1\n\t" \
  "v_mfma_f32_16x16x32_bf16 v[24:27], a[204:207], v[184:187], v[24:27]\n\t" \
  "v_mfma_f32_16x16x32_bf16 v[28:31], v[244:247], v[184:187], v[28:31]\n\t" \
  "s_waitcnt lgkmcnt(3)\n\t" \
  "v_mfma_f32_16x16x32_bf16 v[16:19], a[64:67], v[192:195], v[16:19]\n\t" \
  "v_xor_b32 v1, 0x10000, v1\n\t" \
  "v_xor_b32 v2, 0x10000, v2\n\t" \
  "v_mfma_f32_16x16x32_bf16 v[20:23], a[136:139], v[192:195], v[20:23]\n\t" \
  "v_mfma_f32_16x16x32_bf16 v[24:27], a[208:211], v[192:195], v[24:27]\n\t" \
  "v_mfma_f32_16x16x32_bf16 v[28:31], v[248:251], v[192:195], v[28:31]\n\t" \
  "s_waitcnt lgkmcnt(1)\n\t" \
  "v_mfma_f32_16x16x32_bf16 v[16:19], a[68:71], v[200:203], v[16:19]\n\t" \
  "v_mfma_f32_16x16x32_bf16 v[20:23], a[140:143], v[200:203], v[20:23]\n\t" \
  "v_mfma_f32_16x16x32_bf16 v[24:27], a[212:215], v[200:203], v[24:27]\n\t" \
  "v_mfma_f32_16x16x32_bf16 v[28:31], v[252:255], v[200:203], v[28:31]\n\t" \
  "v_mfma_f32_16x16x32_bf16 v[32:35], a[0:3], v[68:71], 0\n\t" \
  "v_cvt_pk_bf16_f32 v48, v16, v17\n\t" \
  "v_cvt_pk_bf16_f32 v49, v18, v19\n\t" \
  "v_mfma_f32_16x16x32_bf16 v[36:39], a[72:75], v[68:71], 0\n\t" \
  "v_cvt_pk_bf16_f32 v50, v20, v21\n\t" \
  "v_cvt_pk_bf16_f32 v51, v22, v23\n\t" \
  "v_mfma_f32_16x16x32_bf16 v[32:35], a[4:7], v[76:79], v[32:35]\n\t" \
  "v_cvt_pk_bf16_f32 v52, v24, v25\n\t" \
  "v_cvt_pk_bf16_f32 v53, v26, v27\n\t" \
  "v_mfma_f32_16x16x32_bf16 v[36:39], a[76:79], v[76:79], v[36:39]\n\t" \
  "v_cvt_pk_bf16_f32 v54, v28, v29\n\t" \
  "v_cvt_pk_bf16_f32 v55, v30, v31\n\t" \
  "v_mfma_f32_16x16x32_bf16 v[32:35], a[8:11], v[84:87], v[32:35]\n\t" \
  "buffer_store_dwordx4 v[48:51], v0, %[X], s50 offen offset:0 sc1\n\t" \
  "v_mfma_f32_16x16x32_bf16 v[36:39], a[80:83], v[84:87], v[36:39]\n\t" \
  "v_mfma_f32_16x16x32_bf16 v[32:35], a[12:15], v[92:95], v[32:35]\n\t" \
  "v_mfma_f32_16x16x32_bf16 v[36:39], a[84:87], v[92:95], v[36:39]\n\t" \
  "v_mfma_f32_16x16x32_bf16 v[32:35], a[16:19], v[100:103], v[32:35]\n\t" \
  "v_mfma_f32_16x16x32_bf16 v[36:39], a[88:91], v[100:103], v[36:39]\n\t" \
  "v_mfma_f32_16x16x32_bf16 v[32:35], a[20:23], v[108:111], v[32:35]\n\t" \
  "v_mfma_f32_16x16x32_bf16 v[36:39], a[92:95], v[108:111], v[36:39]\n\t" \
  "v_mfma_f32_16x16x32_bf16 v[32:35], a[24:27], v[116:119], v[32:35]\n\t" \
  "v_mfma_f32_16x16x32_bf16 v[36:39], a[96:99], v[116:119], v[36:39]\n\t" \
  "v_mfma_f32_16x16x32_bf16 v[32:35], a[28:31], v[124:127], v[32:35]\n\t" \
  "v_mfma_f32_16x16x32_bf16 v[36:39], a[100:103], v[124:127], v[36:39]\n\t" \
  "s_waitcnt vmcnt(1)\n\t" \
  "v_cmp_eq_u32 vcc, 0, v4\n\t" \
  "v_mfma_f32_16x16x32_bf16 v[32:35], a[32:35], v[132:135], v[32:35]\n\t" \
  "v_mfma_f32_16x16x32_bf16 v[36:39], a[104:107], v[132:135], v[36:39]\n\t" \
  "s_cmp_eq_u64 vcc, 0\n\t" \
  "s_cselect_b32 s60, 1, 0\n\t" \
  "v_mov_b32 v4, s60\n\t" \
  "v_mfma_f32_16x16x32_bf16 v[32:35], a[36:39], v[140:143], v[32:35]\n\t" \
  "ds_write_b32 v5, v4\n\t" \
  "v_mfma_f32_16x16x32_bf16 v[36:39], a[108:111], v[140:143], v[36:39]\n\t" \
  "v_mfma_f32_16x16x32_bf16 v[32:35], a[40:43], v[148:151], v[32:35]\n\t" \
  "v_mfma_f32_16x16x32_bf16 v[36:39], a[112:115], v[148:151], v[36:39]\n\t" \
  "buffer_store_dwordx4 v[52:55], v0, %[X], s50 offen offset:1024 sc1\n\t" \
  "v_mfma_f32_16x16x32_bf16 v[32:35], a[44:47], v[156:159], v[32:35]\n\t" \
  "v_mfma_f32_16x16x32_bf16 v[36:39], a[116:119], v[156:159], v[36:39]\n\t" \
  "s_waitcnt lgkmcnt(0)\n\t" \
  "s_barrier\n\t" \
  "v_mfma_f32_16x16x32_bf16 v[32:35], a[48:51], v[164:167], v[32:35]\n\t" \
  "ds_read_b32 v4, v6 offset:40959\n\t" \
  "v_mfma_f32_16x16x32_bf16 v[36:39], a[120:123], v[164:167], v[36:39]\n\t" \
  "v_mfma_f32_16x16x32_bf16 v[32:35], a[52:55], v[172:175], v[32:35]\n\t" \
  "v_mfma_f32_16x16x32_bf16 v[36:39], a[124:127], v[172:175], v[36:39]\n\t" \
  "v_mfma_f32_16x16x32_bf16 v[32:35], a[56:59], v[180:183], v[32:35]\n\t" \
  "v_mfma_f32_16x16x32_bf16 v[36:39], a[128:131], v[180:183], v[36:39]\n\t" \
  "s_waitcnt lgkmcnt(0)\n\t" \
  "v_readfirstlane_b32 s60, v4\n\t" \
  "s_add_u32 s55, s83, s36\n\t" \
  "v_mfma_f32_16x16x32_bf16 v[32:35], a[60:63], v[188:191], v[32:35]\n\t" \
  "s_cmp_eq_u32 s48, 63\n\t" \
  "s_cselect_b32 s56, 0, s60\n\t" \
  "v_mfma_f32_16x16x32_bf16 v[36:39], a[132:135], v[188:191], v[36:39]\n\t" \
  "s_cmp_eq_u32 s56, 0\n\t" \
  "s_cselect_b32 s55, 0xf0000000, s55\n\t" \
  "v_mfma_f32_16x16x32_bf16 v[32:35], a[64:67], v[196:199], v[32:35]\n\t" \
  "v_mfma_f32_16x16x32_bf16 v[36:39], a[136:139], v[196:199], v[36:39]\n\t" \
  "v_mfma_f32_16x16x32_bf16 v[32:35], a[68:71], v[204:207], v[32:35]\n\t" \
  "s_add_u32 m0, s46, 0\n\t" \
  "s_add_u32 s47, s55, 0\n\t" \
  "buffer_load_dwordx4 v0, %[X], s47 offen sc1 lds\n\t" \
  "v_mfma_f32_16x16x32_bf16 v[36:39], a[140:143], v[204:207], v[36:39]\n\t" \
  "v_mfma_f32_16x16x32_bf16 v[40:43], a[144:147], v[68:71], 0\n\t" \
  "v_mfma_f32_16x16x32_bf16 v[44:47], a[216:219], v[68:71], 0\n\t" \
  "v_mfma_f32_16x16x32_bf16 v[40:43], a[148:151], v[76:79], v[40:43]\n\t" \
  "v_cvt_pk_bf16_f32 v56, v32, v33\n\t" \
  "v_cvt_pk_bf16_f32 v57, v34, v35\n\t" \
  "s_add_u32 m0, s46, 4096\n\t" \
  "s_add_u32 s47, s55, 4096\n\t" \
  "buffer_load_dwordx4 v0, %[X], s47 offen sc1 lds\n\t" \
  "v_mfma_f32_16x16x32_bf16 v[44:47], a[220:223], v[76:79], v[44:47]\n\t" \
  "v_cvt_pk_bf16_f32 v58, v36, v37\n\t" \
  "v_cvt_pk_bf16_f32 v59, v38, v39\n\t" \
  "v_mfma_f32_16x16x32_bf16 v[40:43], a[152:155], v[84:87], v[40:43]\n\t" \
  "v_mfma_f32_16x16x32_bf16 v[44:47], a[224:227], v[84:87], v[44:47]\n\t" \
  "buffer_store_dwordx4 v[56:59], v0, %[X], s51 offen offset:0 sc1\n\t" \
  "v_mfma_f32_16x16x32_bf16 v[40:43], a[156:159], v[92:95], v[40:43]\n\t" \
  "s_add_u32 m0, s46, 8192\n\t" \
  "s_add_u32 s47, s55, 16384\n\t" \
  "buffer_load_dwordx4 v0, %[X], s47 offen sc1 lds\n\t" \
  "v_mfma_f32_16x16x32_bf16 v[44:47], a[228:231], v[92:95], v[44:47]\n\t" \
  "v_mfma_f32_16x16x32_bf16 v[40:43], a[160:163], v[100:103], v[40:43]\n\t" \
  "v_mfma_f32_16x16x32_bf16 v[44:47], a[232:235], v[100:103], v[44:47]\n\t" \
  "v_mfma_f32_16x16x32_bf16 v[40:43], a[164:167], v[108:111], v[40:43]\n\t" \
  "s_add_u32 m0, s46, 12288\n\t" \
  "s_add_u32 s47, s55, 20480\n\t" \
  "buffer_load_dwordx4 v0, %[X], s47 offen sc1 lds\n\t" \
  "v_mfma_f32_16x16x32_bf16 v[44:47], a[236:239], v[108:111], v[44:47]\n\t" \
  "v_mfma_f32_16x16x32_bf16 v[40:43], a[168:171], v[116:119], v[40:43]\n\t" \
  "v_mfma_f32_16x16x32_bf16 v[44:47], a[240:243], v[116:119], v[44:47]\n\t" \
  "v_mfma_f32_16x16x32_bf16 v[40:43], a[172:175], v[124:127], v[40:43]\n\t" \
  "s_add_u32 m0, s46, 16384\n\t" \
  "s_add_u32 s47, s55, 32768\n\t" \
  "buffer_load_dwordx4 v0, %[X], s47 offen sc1 lds\n\t" \
  "v_mfma_f32_16x16x32_bf16 v[44:47], a[244:247], v[124:127], v[44:47]\n\t" \
  "v_mfma_f32_16x16x32_bf16 v[40:43], a[176:179], v[132:135], v[40:43]\n\t" \
  "v_mfma_f32_16x16x32_bf16 v[44:47], a[248:251], v[132:135], v[44:47]\n\t" \
  "v_mfma_f32_16x16x32_bf16 v[40:43], a[180:183], v[140:143], v[40:43]\n\t" \
  "s_add_u32 m0, s46, 20480\n\t" \
  "s_add_u32 s47, s55, 36864\n\t" \
  "buffer_load_dwordx4 v0, %[X], s47 offen sc1 lds\n\t" \
  "v_mfma_f32_16x16x32_bf16 v[44:47], a[252:255], v[140:143], v[44:47]\n\t" \
  "v_mfma_f32_16x16x32_bf16 v[40:43], a[184:187], v[148:151], v[40:43]\n\t" \
  "v_mfma_f32_16x16x32_bf16 v[44:47], v[224:227], v[148:151], v[44:47]\n\t" \
  "v_mfma_f32_16x16x32_bf16 v[40:43], a[188:191], v[156:159], v[40:43]\n\t" \
  "s_add_u32 m0, s46, 24576\n\t" \
  "s_add_u32 s47, s55, 49152\n\t" \
  "buffer_load_dwordx4 v0, %[X], s47 offen sc1 lds\n\t" \
  "v_mfma_f32_16x16x32_bf16 v[44:47], v[228:231], v[156:159], v[44:47]\n\t" \
  "v_mfma_f32_16x16x32_bf16 v[40:43], a[192:195], v[164:167], v[40:43]\n\t" \
  "v_mfma_f32_16x16x32_bf16 v[44:47], v[232:235], v[164:167], v[44:47]\n\t" \
  "v_mfma_f32_16x16x32_bf16 v[40:43], a[196:199], v[172:175], v[40:43]\n\t" \
  "s_add_u32 m0, s46, 28672\n\t" \
  "s_add_u32 s47, s55, 53248\n\t" \
  "buffer_load_dwordx4 v0, %[X], s47 offen sc1 lds\n\t" \
  "v_mfma_f32_16x16x32_bf16 v[44:47], v[236:239], v[172:175], v[44:47]\n\t" \
  "v_mfma_f32_16x16x32_bf16 v[40:43], a[200:203], v[180:183], v[40:43]\n\t" \
  "v_mfma_f32_16x16x32_bf16 v[44:47], v[240:243], v[180:183], v[44:47]\n\t" \
  "v_mfma_f32_16x16x32_bf16 v[40:43], a[204:207], v[188:191], v[40:43]\n\t" \
  "s_add_u32 m0, s46, 32768\n\t" \
  "s_add_u32 s47, s55, 65536\n\t" \
  "buffer_load_dwordx4 v0, %[X], s47 offen sc1 lds\n\t" \
  "v_mfma_f32_16x16x32_bf16 v[44:47], v[244:247], v[188:191], v[44:47]\n\t" \
  "v_mfma_f32_16x16x32_bf16 v[40:43], a[208:211], v[196:199], v[40:43]\n\t" \
  "v_mfma_f32_16x16x32_bf16 v[44:47], v[248:251], v[196:199], v[44:47]\n\t" \
  "buffer_load_dword v8, off, %[X], s57 sc1\n\t" \
  "v_mfma_f32_16x16x32_bf16 v[40:43], a[212:215], v[204:207], v[40:43]\n\t" \
  "v_mfma_f32_16x16x32_bf16 v[44:47], v[252:255], v[204:207], v[44:47]\n\t" \
  "s_nop 4\n\t" \
  "v_cvt_pk_bf16_f32 v60, v40, v41\n\t" \
  "v_cvt_pk_bf16_f32 v61, v42, v43\n\t" \
  "s_nop 1\n\t" \
  "v_cvt_pk_bf16_f32 v62, v44, v45\n\t" \
  "v_cvt_pk_bf16_f32 v63, v46, v47\n\t" \
  "buffer_store_dwordx4 v[60:63], v0, %[X], s51 offen offset:1024 sc1\n\t" \
  TK_PQ2 \
  "s_add_u32 s49, s82, s38\n\t" \
  "s_cmp_eq_u32 s48, 63\n\t" \
  "s_cbranch_scc1 LEND_%=\n\t" \
  "s_add_u32 s48, s48, 1\n\t" \
  "s_cmp_eq_u32 s56, 0\n\t" \
  "s_cbranch_scc1 LCOLD_%=\n\t" \
  "s_waitcnt vmcnt(0)\n\t" \
  "s_mov_b64 exec, 1\n\t" \
  "buffer_store_dword v6, off, %[X], s49 sc1\n\t" \
  "s_mov_b64 exec, -1\n\t" \
  "s_mov_b32 s49, 0xf0000000\n\t" \
  "s_cmp_lt_i32 s57, 0\n\t" \
  "s_cbranch_scc1 LTOP_%=\n\t" \
  "s_movk_i32 s63, 4000\n\t" \
  "s_branch LGW_%=u\n\t" \
  "LGW_%=:\n\t" \
  "s_sleep " TK_PSG "\n\t" \
  "buffer_load_dword v8, off, %[X], s57 sc1\n\t" \
  "s_waitcnt vmcnt(0)\n\t" \
  "LGW_%=u:\n\t" \
  "s_memrealtime s[52:53]\n\t" \
  "v_readfirstlane_b32 s60, v8\n\t" \
  "s_waitcnt lgkmcnt(0)\n\t" \
  "s_sub_u32 s63, s63, 1\n\t" \
  "s_cbranch_scc1 LTOP_%=\n\t" \
  "s_cmp_eq_u32 s60, -1\n\t" \
  "s_cbranch_scc1 LGW_%=\n\t" \
  "s_not_b32 s60, s60\n\t" \
  "s_lshl_b32 s61, s52, 8\n\t" \
  "s_sub_u32 s60, s60, s61\n\t" \
  "s_ashr_i32 s60, s60, 8\n\t" \
  "s_sub_i32 s60, s60, " TK_QXW "\n\t" \
  "s_min_i32 s60, s60, 400\n\t" \
  "s_add_u32 s54, s52, s60\n\t" \
  "LGW_%=w:\n\t" \
  "s_sleep 1\n\t" \
  "s_memrealtime s[52:53]\n\t" \
  "s_waitcnt lgkmcnt(0)\n\t" \
  "s_sub_i32 s53, s52, s54\n\t" \
  "s_cmp_lt_i32 s53, 0\n\t" \
  "s_cbranch_scc1 LGW_%=w\n\t" \
  "s_branch LTOP_%=\n\t" \
  "LEND_%=:\n\t" \
  "s_waitcnt vmcnt(0)\n\t" \
  "s_mov_b64 exec, 1\n\t" \
  "buffer_store_dword v6, off, %[X], s49 sc1\n\t" \
  "s_mov_b64 exec, -1\n\t" \
  "s_mov_b32 s49, 0xf0000000\n\t" \
  TK_PQ0 \
  ""
#define QV_ASM \
  "v_lshrrev_b32 v16, 7, v0\n\t" \
  "v_lshlrev_b32 v16, 13, v16\n\t" \
  "v_lshrrev_b32 v19, 2, v0\n\t" \
  "v_and_b32 v19, 64, v19\n\t" \
  "v_xor_b32 v19, v19, v0\n\t" \
  "v_and_b32 v19, 0x70, v19\n\t" \
  "v_add_u32 v16, v16, v19\n\t" \
  "s_mul_i32 s63, s40, 40960\n\t" \
  "v_and_b32 v19, 0x300, v0\n\t" \
  "v_lshlrev_b32 v17, 2, v19\n\t" \
  "v_and_b32 v19, 0xc0, v0\n\t" \
  "v_lshl_add_u32 v17, v19, 1, v17\n\t" \
  "v_and_b32 v19, 0x30, v0\n\t" \
  "v_lshrrev_b32 v19, 1, v19\n\t" \
  "v_add3_u32 v17, v17, v19, s63\n\t" \
  "v_lshrrev_b32 v19, 1, v0\n\t" \
  "v_and_b32 v19, 64, v19\n\t" \
  "v_add_u32 v17, v17, v19\n\t" \
  "v_xor_b32 v18, 64, v17\n\t" \
  "s_add_u32 m0, s63, 0\n\t" \
  "s_mov_b32 s60, 0\n\t" \
  "buffer_load_dwordx4 v16, %[W], s60 offen lds\n\t" \
  "s_add_u32 m0, s63, 1024\n\t" \
  "s_mov_b32 s60, 65536\n\t" \
  "buffer_load_dwordx4 v16, %[W], s60 offen lds\n\t" \
  "s_add_u32 m0, s63, 2048\n\t" \
  "s_mov_b32 s60, 131072\n\t" \
  "buffer_load_dwordx4 v16, %[W], s60 offen lds\n\t" \
  "s_add_u32 m0, s63, 3072\n\t" \
  "s_mov_b32 s60, 196608\n\t" \
  "buffer_load_dwordx4 v16, %[W], s60 offen lds\n\t" \
  "s_add_u32 m0, s63, 4096\n\t" \
  "s_mov_b32 s60, 262144\n\t" \
  "buffer_load_dwordx4 v16, %[W], s60 offen lds\n\t" \
  "s_add_u32 m0, s63, 5120\n\t" \
  "s_mov_b32 s60, 327680\n\t" \
  "buffer_load_dwordx4 v16, %[W], s60 offen lds\n\t" \
  "s_add_u32 m0, s63, 6144\n\t" \
  "s_mov_b32 s60, 393216\n\t" \
  "buffer_load_dwordx4 v16, %[W], s60 offen lds\n\t" \
  "s_add_u32 m0, s63, 7168\n\t" \
  "s_mov_b32 s60, 458752\n\t" \
  "buffer_load_dwordx4 v16, %[W], s60 offen lds\n\t" \
  "s_add_u32 m0, s63, 8192\n\t" \
  "s_mov_b32 s60, 524288\n\t" \
  "buffer_load_dwordx4 v16, %[W], s60 offen lds\n\t" \
  "s_add_u32 m0, s63, 9216\n\t" \
  "s_mov_b32 s60, 589824\n\t" \
  "buffer_load_dwordx4 v16, %[W], s60 offen lds\n\t" \
  "s_add_u32 m0, s63, 10240\n\t" \
  "s_mov_b32 s60, 655360\n\t" \
  "buffer_load_dwordx4 v16, %[W], s60 offen lds\n\t" \
  "s_add_u32 m0, s63, 11264\n\t" \
  "s_mov_b32 s60, 720896\n\t" \
  "buffer_load_dwordx4 v16, %[W], s60 offen lds\n\t" \
  "s_add_u32 m0, s63, 12288\n\t" \
  "s_mov_b32 s60, 786432\n\t" \
  "buffer_load_dwordx4 v16, %[W], s60 offen lds\n\t" \
  "s_add_u32 m0, s63, 13312\n\t" \
  "s_mov_b32 s60, 851968\n\t" \
  "buffer_load_dwordx4 v16, %[W], s60 offen lds\n\t" \
  "s_add_u32 m0, s63, 14336\n\t" \
  "s_mov_b32 s60, 917504\n\t" \
  "buffer_load_dwordx4 v16, %[W], s60 offen lds\n\t" \
  "s_add_u32 m0, s63, 15360\n\t" \
  "s_mov_b32 s60, 983040\n\t" \
  "buffer_load_dwordx4 v16, %[W], s60 offen lds\n\t" \
  "s_add_u32 m0, s63, 16384\n\t" \
  "s_mov_b32 s60, 1048576\n\t" \
  "buffer_load_dwordx4 v16, %[W], s60 offen lds\n\t" \
  "s_add_u32 m0, s63, 17408\n\t" \
  "s_mov_b32 s60, 1114112\n\t" \
  "buffer_load_dwordx4 v16, %[W], s60 offen lds\n\t" \
  "s_add_u32 m0, s63, 18432\n\t" \
  "s_mov_b32 s60, 1179648\n\t" \
  "buffer_load_dwordx4 v16, %[W], s60 offen lds\n\t" \
  "s_add_u32 m0, s63, 19456\n\t" \
  "s_mov_b32 s60, 1245184\n\t" \
  "buffer_load_dwordx4 v16, %[W], s60 offen lds\n\t" \
  "s_add_u32 m0, s63, 20480\n\t" \
  "s_mov_b32 s60, 1310720\n\t" \
  "buffer_load_dwordx4 v16, %[W], s60 offen lds\n\t" \
  "s_add_u32 m0, s63, 21504\n\t" \
  "s_mov_b32 s60, 1376256\n\t" \
  "buffer_load_dwordx4 v16, %[W], s60 offen lds\n\t" \
  "s_add_u32 m0, s63, 22528\n\t" \
  "s_mov_b32 s60, 1441792\n\t" \
  "buffer_load_dwordx4 v16, %[W], s60 offen lds\n\t" \
  "s_add_u32 m0, s63, 23552\n\t" \
  "s_mov_b32 s60, 1507328\n\t" \
  "buffer_load_dwordx4 v16, %[W], s60 offen lds\n\t" \
  "s_add_u32 m0, s63, 24576\n\t" \
  "s_mov_b32 s60, 1572864\n\t" \
  "buffer_load_dwordx4 v16, %[W], s60 offen lds\n\t" \
  "s_add_u32 m0, s63, 25600\n\t" \
  "s_mov_b32 s60, 1638400\n\t" \
  "buffer_load_dwordx4 v16, %[W], s60 offen lds\n\t" \
  "s_add_u32 m0, s63, 26624\n\t" \
  "s_mov_b32 s60, 1703936\n\t" \
  "buffer_load_dwordx4 v16, %[W], s60 offen lds\n\t" \
  "s_add_u32 m0, s63, 27648\n\t" \
  "s_mov_b32 s60, 1769472\n\t" \
  "buffer_load_dwordx4 v16, %[W], s60 offen lds\n\t" \
  "s_add_u32 m0, s63, 28672\n\t" \
  "s_mov_b32 s60, 1835008\n\t" \
  "buffer_load_dwordx4 v16, %[W], s60 offen lds\n\t" \
  "s_add_u32 m0, s63, 29696\n\t" \
  "s_mov_b32 s60, 1900544\n\t" \
  "buffer_load_dwordx4 v16, %[W], s60 offen lds\n\t" \
  "s_add_u32 m0, s63, 30720\n\t" \
  "s_mov_b32 s60, 1966080\n\t" \
  "buffer_load_dwordx4 v16, %[W], s60 offen lds\n\t" \
  "s_add_u32 m0, s63, 31744\n\t" \
  "s_mov_b32 s60, 2031616\n\t" \
  "buffer_load_dwordx4 v16, %[W], s60 offen lds\n\t" \
  "s_add_u32 m0, s63, 32768\n\t" \
  "s_mov_b32 s60, 2097152\n\t" \
  "buffer_load_dwordx4 v16, %[W], s60 offen lds\n\t" \
  "s_add_u32 m0, s63, 33792\n\t" \
  "s_mov_b32 s60, 2162688\n\t" \
  "buffer_load_dwordx4 v16, %[W], s60 offen lds\n\t" \
  "s_add_u32 m0, s63, 34816\n\t" \
  "s_mov_b32 s60, 2228224\n\t" \
  "buffer_load_dwordx4 v16, %[W], s60 offen lds\n\t" \
  "s_add_u32 m0, s63, 35840\n\t" \
  "s_mov_b32 s60, 2293760\n\t" \
  "buffer_load_dwordx4 v16, %[W], s60 offen lds\n\t" \
  "s_add_u32 m0, s63, 36864\n\t" \
  "s_mov_b32 s60, 2359296\n\t" \
  "buffer_load_dwordx4 v16, %[W], s60 offen lds\n\t" \
  "s_add_u32 m0, s63, 37888\n\t" \
  "s_mov_b32 s60, 2424832\n\t" \
  "buffer_load_dwordx4 v16, %[W], s60 offen lds\n\t" \
  "s_add_u32 m0, s63, 38912\n\t" \
  "s_mov_b32 s60, 2490368\n\t" \
  "buffer_load_dwordx4 v16, %[W], s60 offen lds\n\t" \
  "s_add_u32 m0, s63, 39936\n\t" \
  "s_mov_b32 s60, 2555904\n\t" \
  "buffer_load_dwordx4 v16, %[W], s60 offen lds\n\t" \
  "s_waitcnt vmcnt(32)\n\t" \
  "ds_read_b64_tr_b16 a[0:1], v17 offset:0\n\t" \
  "ds_read_b64_tr_b16 a[2:3], v17 offset:512\n\t" \
  "ds_read_b64_tr_b16 a[4:5], v17 offset:4096\n\t" \
  "ds_read_b64_tr_b16 a[6:7], v17 offset:4608\n\t" \
  "ds_read_b64_tr_b16 a[72:73], v17 offset:32\n\t" \
  "ds_read_b64_tr_b16 a[74:75], v17 offset:544\n\t" \
  "ds_read_b64_tr_b16 a[76:77], v17 offset:4128\n\t" \
  "ds_read_b64_tr_b16 a[78:79], v17 offset:4640\n\t" \
  "ds_read_b64_tr_b16 a[144:145], v18 offset:0\n\t" \
  "ds_read_b64_tr_b16 a[146:147], v18 offset:512\n\t" \
  "ds_read_b64_tr_b16 a[148:149], v18 offset:4096\n\t" \
  "ds_read_b64_tr_b16 a[150:151], v18 offset:4608\n\t" \
  "ds_read_b64_tr_b16 a[216:217], v18 offset:32\n\t" \
  "ds_read_b64_tr_b16 a[218:219], v18 offset:544\n\t" \
  "ds_read_b64_tr_b16 a[220:221], v18 offset:4128\n\t" \
  "ds_read_b64_tr_b16 a[222:223], v18 offset:4640\n\t" \
  "s_waitcnt lgkmcnt(0)\n\t" \
  "s_add_u32 m0, s63, 0\n\t" \
  "s_mov_b32 s60, 2621440\n\t" \
  "buffer_load_dwordx4 v16, %[W], s60 offen lds\n\t" \
  "s_add_u32 m0, s63, 1024\n\t" \
  "s_mov_b32 s60, 2686976\n\t" \
  "buffer_load_dwordx4 v16, %[W], s60 offen lds\n\t" \
  "s_add_u32 m0, s63, 2048\n\t" \
  "s_mov_b32 s60, 2752512\n\t" \
  "buffer_load_dwordx4 v16, %[W], s60 offen lds\n\t" \
  "s_add_u32 m0, s63, 3072\n\t" \
  "s_mov_b32 s60, 2818048\n\t" \
  "buffer_load_dwordx4 v16, %[W], s60 offen lds\n\t" \
  "s_add_u32 m0, s63, 4096\n\t" \
  "s_mov_b32 s60, 2883584\n\t" \
  "buffer_load_dwordx4 v16, %[W], s60 offen lds\n\t" \
  "s_add_u32 m0, s63, 5120\n\t" \
  "s_mov_b32 s60, 2949120\n\t" \
  "buffer_load_dwordx4 v16, %[W], s60 offen lds\n\t" \
  "s_add_u32 m0, s63, 6144\n\t" \
  "s_mov_b32 s60, 3014656\n\t" \
  "buffer_load_dwordx4 v16, %[W], s60 offen lds\n\t" \
  "s_add_u32 m0, s63, 7168\n\t" \
  "s_mov_b32 s60, 3080192\n\t" \
  "buffer_load_dwordx4 v16, %[W], s60 offen lds\n\t" \
  "s_waitcnt vmcnt(32)\n\t" \
  "ds_read_b64_tr_b16 a[8:9], v17 offset:8192\n\t" \
  "ds_read_b64_tr_b16 a[10:11], v17 offset:8704\n\t" \
  "ds_read_b64_tr_b16 a[12:13], v17 offset:12288\n\t" \
  "ds_read_b64_tr_b16 a[14:15], v17 offset:12800\n\t" \
  "ds_read_b64_tr_b16 a[80:81], v17 offset:8224\n\t" \
  "ds_read_b64_tr_b16 a[82:83], v17 offset:8736\n\t" \
  "ds_read_b64_tr_b16 a[84:85], v17 offset:12320\n\t" \
  "ds_read_b64_tr_b16 a[86:87], v17 offset:12832\n\t" \
  "ds_read_b64_tr_b16 a[152:153], v18 offset:8192\n\t" \
  "ds_read_b64_tr_b16 a[154:155], v18 offset:8704\n\t" \
  "ds_read_b64_tr_b16 a[156:157], v18 offset:12288\n\t" \
  "ds_read_b64_tr_b16 a[158:159], v18 offset:12800\n\t" \
  "ds_read_b64_tr_b16 a[224:225], v18 offset:8224\n\t" \
  "ds_read_b64_tr_b16 a[226:227], v18 offset:8736\n\t" \
  "ds_read_b64_tr_b16 a[228:229], v18 offset:12320\n\t" \
  "ds_read_b64_tr_b16 a[230:231], v18 offset:12832\n\t" \
  "s_waitcnt lgkmcnt(0)\n\t" \
  "s_add_u32 m0, s63, 8192\n\t" \
  "s_mov_b32 s60, 3145728\n\t" \
  "buffer_load_dwordx4 v16, %[W], s60 offen lds\n\t" \
  "s_add_u32 m0, s63, 9216\n\t" \
  "s_mov_b32 s60, 3211264\n\t" \
  "buffer_load_dwordx4 v16, %[W], s60 offen lds\n\t" \
  "s_add_u32 m0, s63, 10240\n\t" \
  "s_mov_b32 s60, 3276800\n\t" \
  "buffer_load_dwordx4 v16, %[W], s60 offen lds\n\t" \
  "s_add_u32 m0, s63, 11264\n\t" \
  "s_mov_b32 s60, 3342336\n\t" \
  "buffer_load_dwordx4 v16, %[W], s60 offen lds\n\t" \
  "s_add_u32 m0, s63, 12288\n\t" \
  "s_mov_b32 s60, 3407872\n\t" \
  "buffer_load_dwordx4 v16, %[W], s60 offen lds\n\t" \
  "s_add_u32 m0, s63, 13312\n\t" \
  "s_mov_b32 s60, 3473408\n\t" \
  "buffer_load_dwordx4 v16, %[W], s60 offen lds\n\t" \
  "s_add_u32 m0, s63, 14336\n\t" \
  "s_mov_b32 s60, 3538944\n\t" \
  "buffer_load_dwordx4 v16, %[W], s60 offen lds\n\t" \
  "s_add_u32 m0, s63, 15360\n\t" \
  "s_mov_b32 s60, 3604480\n\t" \
  "buffer_load_dwordx4 v16, %[W], s60 offen lds\n\t" \
  "s_waitcnt vmcnt(32)\n\t" \
  "ds_read_b64_tr_b16 a[16:17], v17 offset:16384\n\t" \
  "ds_read_b64_tr_b16 a[18:19], v17 offset:16896\n\t" \
  "ds_read_b64_tr_b16 a[20:21], v17 offset:20480\n\t" \
  "ds_read_b64_tr_b16 a[22:23], v17 offset:20992\n\t" \
  "ds_read_b64_tr_b16 a[88:89], v17 offset:16416\n\t" \
  "ds_read_b64_tr_b16 a[90:91], v17 offset:16928\n\t" \
  "ds_read_b64_tr_b16 a[92:93], v17 offset:20512\n\t" \
  "ds_read_b64_tr_b16 a[94:95], v17 offset:21024\n\t" \
  "ds_read_b64_tr_b16 a[160:161], v18 offset:16384\n\t" \
  "ds_read_b64_tr_b16 a[162:163], v18 offset:16896\n\t" \
  "ds_read_b64_tr_b16 a[164:165], v18 offset:20480\n\t" \
  "ds_read_b64_tr_b16 a[166:167], v18 offset:20992\n\t" \
  "ds_read_b64_tr_b16 a[232:233], v18 offset:16416\n\t" \
  "ds_read_b64_tr_b16 a[234:235], v18 offset:16928\n\t" \
  "ds_read_b64_tr_b16 a[236:237], v18 offset:20512\n\t" \
  "ds_read_b64_tr_b16 a[238:239], v18 offset:21024\n\t" \
  "s_waitcnt lgkmcnt(0)\n\t" \
  "s_add_u32 m0, s63, 16384\n\t" \
  "s_mov_b32 s60, 3670016\n\t" \
  "buffer_load_dwordx4 v16, %[W], s60 offen lds\n\t" \
  "s_add_u32 m0, s63, 17408\n\t" \
  "s_mov_b32 s60, 3735552\n\t" \
  "buffer_load_dwordx4 v16, %[W], s60 offen lds\n\t" \
  "s_add_u32 m0, s63, 18432\n\t" \
  "s_mov_b32 s60, 3801088\n\t" \
  "buffer_load_dwordx4 v16, %[W], s60 offen lds\n\t" \
  "s_add_u32 m0, s63, 19456\n\t" \
  "s_mov_b32 s60, 3866624\n\t" \
  "buffer_load_dwordx4 v16, %[W], s60 offen lds\n\t" \
  "s_add_u32 m0, s63, 20480\n\t" \
  "s_mov_b32 s60, 3932160\n\t" \
  "buffer_load_dwordx4 v16, %[W], s60 offen lds\n\t" \
  "s_add_u32 m0, s63, 21504\n\t" \
  "s_mov_b32 s60, 3997696\n\t" \
  "buffer_load_dwordx4 v16, %[W], s60 offen lds\n\t" \
  "s_add_u32 m0, s63, 22528\n\t" \
  "s_mov_b32 s60, 4063232\n\t" \
  "buffer_load_dwordx4 v16, %[W], s60 offen lds\n\t" \
  "s_add_u32 m0, s63, 23552\n\t" \
  "s_mov_b32 s60, 4128768\n\t" \
  "buffer_load_dwordx4 v16, %[W], s60 offen lds\n\t" \
  "s_waitcnt vmcnt(32)\n\t" \
  "ds_read_b64_tr_b16 a[24:25], v17 offset:24576\n\t" \
  "ds_read_b64_tr_b16 a[26:27], v17 offset:25088\n\t" \
  "ds_read_b64_tr_b16 a[28:29], v17 offset:28672\n\t" \
  "ds_read_b64_tr_b16 a[30:31], v17 offset:29184\n\t" \
  "ds_read_b64_tr_b16 a[96:97], v17 offset:24608\n\t" \
  "ds_read_b64_tr_b16 a[98:99], v17 offset:25120\n\t" \
  "ds_read_b64_tr_b16 a[100:101], v17 offset:28704\n\t" \
  "ds_read_b64_tr_b16 a[102:103], v17 offset:29216\n\t" \
  "ds_read_b64_tr_b16 a[168:169], v18 offset:24576\n\t" \
  "ds_read_b64_tr_b16 a[170:171], v18 offset:25088\n\t" \
  "ds_read_b64_tr_b16 a[172:173], v18 offset:28672\n\t" \
  "ds_read_b64_tr_b16 a[174:175], v18 offset:29184\n\t" \
  "ds_read_b64_tr_b16 a[240:241], v18 offset:24608\n\t" \
  "ds_read_b64_tr_b16 a[242:243], v18 offset:25120\n\t" \
  "ds_read_b64_tr_b16 a[244:245], v18 offset:28704\n\t" \
  "ds_read_b64_tr_b16 a[246:247], v18 offset:29216\n\t" \
  "s_waitcnt lgkmcnt(0)\n\t" \
  "s_add_u32 m0, s63, 24576\n\t" \
  "s_mov_b32 s60, 4194304\n\t" \
  "buffer_load_dwordx4 v16, %[W], s60 offen lds\n\t" \
  "s_add_u32 m0, s63, 25600\n\t" \
  "s_mov_b32 s60, 4259840\n\t" \
  "buffer_load_dwordx4 v16, %[W], s60 offen lds\n\t" \
  "s_add_u32 m0, s63, 26624\n\t" \
  "s_mov_b32 s60, 4325376\n\t" \
  "buffer_load_dwordx4 v16, %[W], s60 offen lds\n\t" \
  "s_add_u32 m0, s63, 27648\n\t" \
  "s_mov_b32 s60, 4390912\n\t" \
  "buffer_load_dwordx4 v16, %[W], s60 offen lds\n\t" \
  "s_add_u32 m0, s63, 28672\n\t" \
  "s_mov_b32 s60, 4456448\n\t" \
  "buffer_load_dwordx4 v16, %[W], s60 offen lds\n\t" \
  "s_add_u32 m0, s63, 29696\n\t" \
  "s_mov_b32 s60, 4521984\n\t" \
  "buffer_load_dwordx4 v16, %[W], s60 offen lds\n\t" \
  "s_add_u32 m0, s63, 30720\n\t" \
  "s_mov_b32 s60, 4587520\n\t" \
  "buffer_load_dwordx4 v16, %[W], s60 offen lds\n\t" \
  "s_add_u32 m0, s63, 31744\n\t" \
  "s_mov_b32 s60, 4653056\n\t" \
  "buffer_load_dwordx4 v16, %[W], s60 offen lds\n\t" \
  "s_waitcnt vmcnt(32)\n\t" \
  "ds_read_b64_tr_b16 a[32:33], v17 offset:32768\n\t" \
  "ds_read_b64_tr_b16 a[34:35], v17 offset:33280\n\t" \
  "ds_read_b64_tr_b16 a[36:37], v17 offset:36864\n\t" \
  "ds_read_b64_tr_b16 a[38:39], v17 offset:37376\n\t" \
  "ds_read_b64_tr_b16 a[104:105], v17 offset:32800\n\t" \
  "ds_read_b64_tr_b16 a[106:107], v17 offset:33312\n\t" \
  "ds_read_b64_tr_b16 a[108:109], v17 offset:36896\n\t" \
  "ds_read_b64_tr_b16 a[110:111], v17 offset:37408\n\t" \
  "ds_read_b64_tr_b16 a[176:177], v18 offset:32768\n\t" \
  "ds_read_b64_tr_b16 a[178:179], v18 offset:33280\n\t" \
  "ds_read_b64_tr_b16 a[180:181], v18 offset:36864\n\t" \
  "ds_read_b64_tr_b16 a[182:183], v18 offset:37376\n\t" \
  "ds_read_b64_tr_b16 a[248:249], v18 offset:32800\n\t" \
  "ds_read_b64_tr_b16 a[250:251], v18 offset:33312\n\t" \
  "ds_read_b64_tr_b16 a[252:253], v18 offset:36896\n\t" \
  "ds_read_b64_tr_b16 a[254:255], v18 offset:37408\n\t" \
  "s_waitcnt lgkmcnt(0)\n\t" \
  "s_waitcnt vmcnt(24)\n\t" \
  "ds_read_b64_tr_b16 a[40:41], v17 offset:0\n\t" \
  "ds_read_b64_tr_b16 a[42:43], v17 offset:512\n\t" \
  "ds_read_b64_tr_b16 a[44:45], v17 offset:4096\n\t" \
  "ds_read_b64_tr_b16 a[46:47], v17 offset:4608\n\t" \
  "ds_read_b64_tr_b16 a[112:113], v17 offset:32\n\t" \
  "ds_read_b64_tr_b16 a[114:115], v17 offset:544\n\t" \
  "ds_read_b64_tr_b16 a[116:117], v17 offset:4128\n\t" \
  "ds_read_b64_tr_b16 a[118:119], v17 offset:4640\n\t" \
  "ds_read_b64_tr_b16 a[184:185], v18 offset:0\n\t" \
  "ds_read_b64_tr_b16 a[186:187], v18 offset:512\n\t" \
  "ds_read_b64_tr_b16 a[188:189], v18 offset:4096\n\t" \
  "ds_read_b64_tr_b16 a[190:191], v18 offset:4608\n\t" \
  "ds_read_b64_tr_b16 v[224:225], v18 offset:32\n\t" \
  "ds_read_b64_tr_b16 v[226:227], v18 offset:544\n\t" \
  "ds_read_b64_tr_b16 v[228:229], v18 offset:4128\n\t" \
  "ds_read_b64_tr_b16 v[230:231], v18 offset:4640\n\t" \
  "s_waitcnt lgkmcnt(0)\n\t" \
  "s_waitcnt vmcnt(16)\n\t" \
  "ds_read_b64_tr_b16 a[48:49], v17 offset:8192\n\t" \
  "ds_read_b64_tr_b16 a[50:51], v17 offset:8704\n\t" \
  "ds_read_b64_tr_b16 a[52:53], v17 offset:12288\n\t" \
  "ds_read_b64_tr_b16 a[54:55], v17 offset:12800\n\t" \
  "ds_read_b64_tr_b16 a[120:121], v17 offset:8224\n\t" \
  "ds_read_b64_tr_b16 a[122:123], v17 offset:8736\n\t" \
  "ds_read_b64_tr_b16 a[124:125], v17 offset:12320\n\t" \
  "ds_read_b64_tr_b16 a[126:127], v17 offset:12832\n\t" \
  "ds_read_b64_tr_b16 a[192:193], v18 offset:8192\n\t" \
  "ds_read_b64_tr_b16 a[194:195], v18 offset:8704\n\t" \
  "ds_read_b64_tr_b16 a[196:197], v18 offset:12288\n\t" \
  "ds_read_b64_tr_b16 a[198:199], v18 offset:12800\n\t" \
  "ds_read_b64_tr_b16 v[232:233], v18 offset:8224\n\t" \
  "ds_read_b64_tr_b16 v[234:235], v18 offset:8736\n\t" \
  "ds_read_b64_tr_b16 v[236:237], v18 offset:12320\n\t" \
  "ds_read_b64_tr_b16 v[238:239], v18 offset:12832\n\t" \
  "s_waitcnt lgkmcnt(0)\n\t" \
  "s_waitcnt vmcnt(8)\n\t" \
  "ds_read_b64_tr_b16 a[56:57], v17 offset:16384\n\t" \
  "ds_read_b64_tr_b16 a[58:59], v17 offset:16896\n\t" \
  "ds_read_b64_tr_b16 a[60:61], v17 offset:20480\n\t" \
  "ds_read_b64_tr_b16 a[62:63], v17 offset:20992\n\t" \
  "ds_read_b64_tr_b16 a[128:129], v17 offset:16416\n\t" \
  "ds_read_b64_tr_b16 a[130:131], v17 offset:16928\n\t" \
  "ds_read_b64_tr_b16 a[132:133], v17 offset:20512\n\t" \
  "ds_read_b64_tr_b16 a[134:135], v17 offset:21024\n\t" \
  "ds_read_b64_tr_b16 a[200:201], v18 offset:16384\n\t" \
  "ds_read_b64_tr_b16 a[202:203], v18 offset:16896\n\t" \
  "ds_read_b64_tr_b16 a[204:205], v18 offset:20480\n\t" \
  "ds_read_b64_tr_b16 a[206:207], v18 offset:20992\n\t" \
  "ds_read_b64_tr_b16 v[240:241], v18 offset:16416\n\t" \
  "ds_read_b64_tr_b16 v[242:243], v18 offset:16928\n\t" \
  "ds_read_b64_tr_b16 v[244:245], v18 offset:20512\n\t" \
  "ds_read_b64_tr_b16 v[246:247], v18 offset:21024\n\t" \
  "s_waitcnt lgkmcnt(0)\n\t" \
  "s_waitcnt vmcnt(0)\n\t" \
  "ds_read_b64_tr_b16 a[64:65], v17 offset:24576\n\t" \
  "ds_read_b64_tr_b16 a[66:67], v17 offset:25088\n\t" \
  "ds_read_b64_tr_b16 a[68:69], v17 offset:28672\n\t" \
  "ds_read_b64_tr_b16 a[70:71], v17 offset:29184\n\t" \
  "ds_read_b64_tr_b16 a[136:137], v17 offset:24608\n\t" \
  "ds_read_b64_tr_b16 a[138:139], v17 offset:25120\n\t" \
  "ds_read_b64_tr_b16 a[140:141], v17 offset:28704\n\t" \
  "ds_read_b64_tr_b16 a[142:143], v17 offset:29216\n\t" \
  "ds_read_b64_tr_b16 a[208:209], v18 offset:24576\n\t" \
  "ds_read_b64_tr_b16 a[210:211], v18 offset:25088\n\t" \
  "ds_read_b64_tr_b16 a[212:213], v18 offset:28672\n\t" \
  "ds_read_b64_tr_b16 a[214:215], v18 offset:29184\n\t" \
  "ds_read_b64_tr_b16 v[248:249], v18 offset:24608\n\t" \
  "ds_read_b64_tr_b16 v[250:251], v18 offset:25120\n\t" \
  "ds_read_b64_tr_b16 v[252:253], v18 offset:28704\n\t" \
  "ds_read_b64_tr_b16 v[254:255], v18 offset:29216\n\t" \
  "s_waitcnt lgkmcnt(0)\n\t" \
  "s_mov_b32 s84, " TK_CAP "\n\t" \
  "s_mov_b32 s48, 0\n\t" \
  "s_mov_b32 s49, 0xf0000000\n\t" \
  "s_mov_b32 s46, s41\n\t" \
  "s_mov_b32 s56, 0\n\t" \
  "v_lshrrev_b32 v7, 2, v0\n\t" \
  "s_mov_b32 s60, " TK_CTAB "\n\t" \
  "buffer_load_dword v4, v7, %[X], s60 offen offset:88 sc1\n\t" \
  "buffer_load_dword v7, v7, %[X], s60 offen offset:0 sc1\n\t" \
  "s_waitcnt vmcnt(0)\n\t" \
  "v_and_b32 v4, 1, v4\n\t" \
  "v_and_b32 v7, 1, v7\n\t" \
  "v_cmp_ne_u32 s[90:91], s67, v4\n\t" \
  "v_cmp_ne_u32 s[88:89], s67, v7\n\t" \
  "s_nop 1\n\t" \
  "s_mov_b32 s57, 0xf0000000\n\t" \
  "s_add_u32 s83, s48, 0\n\t" \
  "s_lshr_b64 s[60:61], s[90:91], s83\n\t" \
  "s_and_b32 s60, s60, 1\n\t" \
  "s_lshl_b32 s60, s60, 13\n\t" \
  "s_add_u32 s83, s83, " TK_SG0 "\n\t" \
  "s_lshl_b32 s83, s83, 21\n\t" \
  "s_or_b32 s83, s83, s60\n\t" \
  "s_add_u32 s62, s83, s39\n\t" \
  "v_add_u32 v7, s62, v3\n\t" \
  "LCOLD_%=:\n\t" \
  "s_waitcnt vmcnt(0)\n\t" \
  "s_mov_b64 exec, 1\n\t" \
  "buffer_store_dword v6, off, %[X], s49 sc1\n\t" \
  "s_mov_b64 exec, -1\n\t" \
  "s_mov_b32 s49, 0xf0000000\n\t" \
  "s_add_u32 s55, s83, s36\n\t" \
  "s_cmp_lg_u32 s40, 0\n\t" \
  "s_cbranch_scc1 LGO_%=\n\t" \
  "s_and_b32 s62, s45, 3\n\t" \
  "s_lshl_b32 s62, s62, 7\n\t" \
  "s_add_u32 s62, s62, s83\n\t" \
  "s_add_u32 s62, s62, s39\n\t" \
  "s_mov_b32 s61, 0\n\t" \
  "LPO_%=:\n\t" \
  "buffer_load_dword v4, off, %[X], s62 sc1\n\t" \
  "s_waitcnt vmcnt(0)\n\t" \
  "v_cmp_eq_u32 vcc, 0, v4\n\t" \
  "s_cbranch_vccz LPO_%=x\n\t" \
  "s_add_u32 s61, s61, 1\n\t" \
  "s_sleep " TK_PSH "\n\t" \
  "s_cmp_gt_u32 s61, s84\n\t" \
  "s_cbranch_scc1 LPO_%=g\n\t" \
  "s_and_b32 s60, s61, 1023\n\t" \
  "s_cmp_lg_u32 s60, 0\n\t" \
  "s_cbranch_scc1 LPO_%=\n\t" \
  "buffer_load_dword v4, off, %[X], s92 sc1\n\t" \
  "s_waitcnt vmcnt(0)\n\t" \
  "v_readfirstlane_b32 s60, v4\n\t" \
  "s_nop 0\n\t" \
  "s_cmp_eq_u32 s60, 0\n\t" \
  "s_cbranch_scc1 LPO_%=\n\t" \
  "LPO_%=g:\n\t" \
  "s_mov_b32 s84, 0\n\t" \
  "s_or_b32 s60, s48, 1610612736\n\t" \
  "v_mov_b32 v4, s60\n\t" \
  "s_mov_b64 exec, 1\n\t" \
  "buffer_store_dword v4, off, %[X], s92 sc1\n\t" \
  "s_mov_b64 exec, -1\n\t" \
  "LPO_%=x:\n\t" \
  "s_mov_b32 s61, 0\n\t" \
  "LPH_%=:\n\t" \
  "buffer_load_dword v4, v7, %[X], 0 offen sc1\n\t" \
  "s_waitcnt vmcnt(0)\n\t" \
  "v_cmp_eq_u32 vcc, 0, v4\n\t" \
  "s_cbranch_vccz LPH_%=x\n\t" \
  "s_add_u32 s61, s61, 1\n\t" \
  "s_sleep " TK_PSH "\n\t" \
  "s_cmp_gt_u32 s61, s84\n\t" \
  "s_cbranch_scc1 LPH_%=g\n\t" \
  "s_and_b32 s60, s61, 1023\n\t" \
  "s_cmp_lg_u32 s60, 0\n\t" \
  "s_cbranch_scc1 LPH_%=\n\t" \
  "buffer_load_dword v4, off, %[X], s92 sc1\n\t" \
  "s_waitcnt vmcnt(0)\n\t" \
  "v_readfirstlane_b32 s60, v4\n\t" \
  "s_nop 0\n\t" \
  "s_cmp_eq_u32 s60, 0\n\t" \
  "s_cbranch_scc1 LPH_%=\n\t" \
  "LPH_%=g:\n\t" \
  "s_mov_b32 s84, 0\n\t" \
  "s_or_b32 s60, s48, 1610612736\n\t" \
  "v_mov_b32 v4, s60\n\t" \
  "s_mov_b64 exec, 1\n\t" \
  "buffer_store_dword v4, off, %[X], s92 sc1\n\t" \
  "s_mov_b64 exec, -1\n\t" \
  "LPH_%=x:\n\t" \
  "s_cmp_lt_i32 s57, 0\n\t" \
  "s_cbranch_scc1 LGO_%=\n\t" \
  "s_movk_i32 s63, 4000\n\t" \
  "s_branch LGC_%=u\n\t" \
  "LGC_%=:\n\t" \
  "s_sleep " TK_PSG "\n\t" \
  "buffer_load_dword v8, off, %[X], s57 sc1\n\t" \
  "s_waitcnt vmcnt(0)\n\t" \
  "LGC_%=u:\n\t" \
  "s_memrealtime s[52:53]\n\t" \
  "v_readfirstlane_b32 s60, v8\n\t" \
  "s_waitcnt lgkmcnt(0)\n\t" \
  "s_sub_u32 s63, s63, 1\n\t" \
  "s_cbranch_scc1 LGO_%=\n\t" \
  "s_cmp_eq_u32 s60, -1\n\t" \
  "s_cbranch_scc1 LGC_%=\n\t" \
  "s_not_b32 s60, s60\n\t" \
  "s_lshl_b32 s61, s52, 8\n\t" \
  "s_sub_u32 s60, s60, s61\n\t" \
  "s_ashr_i32 s60, s60, 8\n\t" \
  "s_sub_i32 s60, s60, " TK_QXC "\n\t" \
  "s_min_i32 s60, s60, 400\n\t" \
  "s_add_u32 s54, s52, s60\n\t" \
  "LGC_%=w:\n\t" \
  "s_sleep 1\n\t" \
  "s_memrealtime s[52:53]\n\t" \
  "s_waitcnt lgkmcnt(0)\n\t" \
  "s_sub_i32 s53, s52, s54\n\t" \
  "s_cmp_lt_i32 s53, 0\n\t" \
  "s_cbranch_scc1 LGC_%=w\n\t" \
  "LGO_%=:\n\t" \
  "s_barrier\n\t" \
  "s_add_u32 m0, s46, 0\n\t" \
  "s_add_u32 s47, s55, 0\n\t" \
  "buffer_load_dwordx4 v0, %[X], s47 offen sc1 lds\n\t" \
  "s_add_u32 m0, s46, 4096\n\t" \
  "s_add_u32 s47, s55, 4096\n\t" \
  "buffer_load_dwordx4 v0, %[X], s47 offen sc1 lds\n\t" \
  "s_add_u32 m0, s46, 8192\n\t" \
  "s_add_u32 s47, s55, 16384\n\t" \
  "buffer_load_dwordx4 v0, %[X], s47 offen sc1 lds\n\t" \
  "s_add_u32 m0, s46, 12288\n\t" \
  "s_add_u32 s47, s55, 20480\n\t" \
  "buffer_load_dwordx4 v0, %[X], s47 offen sc1 lds\n\t" \
  "s_add_u32 m0, s46, 16384\n\t" \
  "s_add_u32 s47, s55, 32768\n\t" \
  "buffer_load_dwordx4 v0, %[X], s47 offen sc1 lds\n\t" \
  "s_add_u32 m0, s46, 20480\n\t" \
  "s_add_u32 s47, s55, 36864\n\t" \
  "buffer_load_dwordx4 v0, %[X], s47 offen sc1 lds\n\t" \
  "s_add_u32 m0, s46, 24576\n\t" \
  "s_add_u32 s47, s55, 49152\n\t" \
  "buffer_load_dwordx4 v0, %[X], s47 offen sc1 lds\n\t" \
  "s_add_u32 m0, s46, 28672\n\t" \
  "s_add_u32 s47, s55, 53248\n\t" \
  "buffer_load_dwordx4 v0, %[X], s47 offen sc1 lds\n\t" \
  "s_add_u32 m0, s46, 32768\n\t" \
  "s_add_u32 s47, s55, 65536\n\t" \
  "buffer_load_dwordx4 v0, %[X], s47 offen sc1 lds\n\t" \
  "s_waitcnt vmcnt(5)\n\t" \
  "LTOP_%=:\n\t" \
  "s_barrier\n\t" \
  TK_PQ1 \
  "ds_read_b128 v[64:67], v1 offset:0\n\t" \
  "ds_read_b128 v[68:71], v1 offset:2048\n\t" \
  "ds_read_b128 v[72:75], v2 offset:0\n\t" \
  "ds_read_b128 v[76:79], v2 offset:2048\n\t" \
  "ds_read_b128 v[80:83], v1 offset:4096\n\t" \
  "ds_read_b128 v[84:87], v1 offset:6144\n\t" \
  "s_mov_b32 s82, s83\n\t" \
  "s_xor_b32 s46, s46, 0x10000\n\t" \
  "s_waitcnt lgkmcnt(5)\n\t" \
  "v_mfma_f32_16x16x32_bf16 v[16:19], v[64:67], a[0:3], 0\n\t" \
  "ds_read_b128 v[88:91], v2 offset:4096\n\t" \
  "s_waitcnt lgkmcnt(5)\n\t" \
  "v_mfma_f32_16x16x32_bf16 v[20:23], v[68:71], a[0:3], 0\n\t" \
  "s_add_u32 s83, s48, 1\n\t" \
  "s_lshr_b64 s[60:61], s[90:91], s83\n\t" \
  "v_mfma_f32_16x16x32_bf16 v[24:27], v[64:67], a[72:75], 0\n\t" \
  "ds_read_b128 v[92:95], v2 offset:6144\n\t" \
  "v_mfma_f32_16x16x32_bf16 v[28:31], v[68:71], a[72:75], 0\n\t" \
  "s_and_b32 s60, s60, 1\n\t" \
  "s_lshl_b32 s60, s60, 13\n\t" \
  "s_waitcnt lgkmcnt(5)\n\t" \
  "v_mfma_f32_16x16x32_bf16 v[16:19], v[72:75], a[4:7], v[16:19]\n\t" \
  "ds_read_b128 v[96:99], v1 offset:8192\n\t" \
  "s_waitcnt lgkmcnt(5)\n\t" \
  "v_mfma_f32_16x16x32_bf16 v[20:23], v[76:79], a[4:7], v[20:23]\n\t" \
  "s_add_u32 s83, s83, " TK_SG0 "\n\t" \
  "s_lshl_b32 s83, s83, 21\n\t" \
  "v_mfma_f32_16x16x32_bf16 v[24:27], v[72:75], a[76:79], v[24:27]\n\t" \
  "ds_read_b128 v[100:103], v1 offset:10240\n\t" \
  "v_mfma_f32_16x16x32_bf16 v[28:31], v[76:79], a[76:79], v[28:31]\n\t" \
  "s_or_b32 s83, s83, s60\n\t" \
  "s_add_u32 s62, s83, s39\n\t" \
  "s_waitcnt lgkmcnt(5)\n\t" \
  "v_mfma_f32_16x16x32_bf16 v[16:19], v[80:83], a[8:11], v[16:19]\n\t" \
  "ds_read_b128 v[104:107], v2 offset:8192\n\t" \
  "s_waitcnt lgkmcnt(5)\n\t" \
  "v_mfma_f32_16x16x32_bf16 v[20:23], v[84:87], a[8:11], v[20:23]\n\t" \
  "v_add_u32 v7, s62, v3\n\t" \
  "s_add_u32 s50, s82, s37\n\t" \
  "v_mfma_f32_16x16x32_bf16 v[24:27], v[80:83], a[80:83], v[24:27]\n\t" \
  "ds_read_b128 v[108:111], v2 offset:10240\n\t" \
  "v_mfma_f32_16x16x32_bf16 v[28:31], v[84:87], a[80:83], v[28:31]\n\t" \
  "s_add_u32 s51, s50, 4096\n\t" \
  "s_waitcnt lgkmcnt(5)\n\t" \
  "v_mfma_f32_16x16x32_bf16 v[16:19], v[88:91], a[12:15], v[16:19]\n\t" \
  "ds_read_b128 v[112:115], v1 offset:12288\n\t" \
  "s_waitcnt lgkmcnt(5)\n\t" \
  "v_mfma_f32_16x16x32_bf16 v[20:23], v[92:95], a[12:15], v[20:23]\n\t" \
  "v_mfma_f32_16x16x32_bf16 v[24:27], v[88:91], a[84:87], v[24:27]\n\t" \
  "ds_read_b128 v[116:119], v1 offset:14336\n\t" \
  "v_mfma_f32_16x16x32_bf16 v[28:31], v[92:95], a[84:87], v[28:31]\n\t" \
  "s_waitcnt lgkmcnt(5)\n\t" \
  "v_mfma_f32_16x16x32_bf16 v[16:19], v[96:99], a[16:19], v[16:19]\n\t" \
  "ds_read_b128 v[120:123], v2 offset:12288\n\t" \
  "s_waitcnt lgkmcnt(5)\n\t" \
  "v_mfma_f32_16x16x32_bf16 v[20:23], v[100:103], a[16:19], v[20:23]\n\t" \
  "s_cmp_lg_u32 s56, 0\n\t" \
  "v_mfma_f32_16x16x32_bf16 v[24:27], v[96:99], a[88:91], v[24:27]\n\t" \
  "ds_read_b128 v[124:127], v2 offset:14336\n\t" \
  "v_mfma_f32_16x16x32_bf16 v[28:31], v[100:103], a[88:91], v[28:31]\n\t" \
  "s_cbranch_scc1 LWARM_%=\n\t" \
  "s_waitcnt vmcnt(0)\n\t" \
  "LWARM_%=:\n\t" \
  "s_barrier\n\t" \
  "s_waitcnt lgkmcnt(5)\n\t" \
  "v_mfma_f32_16x16x32_bf16 v[16:19], v[104:107], a[20:23], v[16:19]\n\t" \
  "ds_read_b128 v[128:131], v1 offset:16384\n\t" \
  "s_waitcnt lgkmcnt(5)\n\t" \
  "v_mfma_f32_16x16x32_bf16 v[20:23], v[108:111], a[20:23], v[20:23]\n\t" \
  "v_mfma_f32_16x16x32_bf16 v[24:27], v[104:107], a[92:95], v[24:27]\n\t" \
  "ds_read_b128 v[132:135], v1 offset:18432\n\t" \
  "v_mfma_f32_16x16x32_bf16 v[28:31], v[108:111], a[92:95], v[28:31]\n\t" \
  "s_add_i32 s62, s48, s42\n\t" \
  "s_add_i32 s62, s62, 1\n\t" \
  "s_waitcnt lgkmcnt(5)\n\t" \
  "v_mfma_f32_16x16x32_bf16 v[16:19], v[112:115], a[24:27], v[16:19]\n\t" \
  "ds_read_b128 v[136:139], v2 offset:16384\n\t" \
  "s_waitcnt lgkmcnt(5)\n\t" \
  "v_mfma_f32_16x16x32_bf16 v[20:23], v[116:119], a[24:27], v[20:23]\n\t" \
  "s_mul_i32 s63, s62, 43\n\t" \
  "s_lshr_b32 s63, s63, 7\n\t" \
  "v_mfma_f32_16x16x32_bf16 v[24:27], v[112:115], a[96:99], v[24:27]\n\t" \
  "ds_read_b128 v[140:143], v2 offset:18432\n\t" \
  "v_mfma_f32_16x16x32_bf16 v[28:31], v[116:119], a[96:99], v[28:31]\n\t" \
  "s_mul_i32 s57, s62, 688128\n\t" \
  "s_lshl_b32 s60, s63, 15\n\t" \
  "s_waitcnt lgkmcnt(5)\n\t" \
  "v_mfma_f32_16x16x32_bf16 v[16:19], v[120:123], a[28:31], v[16:19]\n\t" \
  "ds_read_b128 v[144:147], v1 offset:20480\n\t" \
  "s_waitcnt lgkmcnt(5)\n\t" \
  "v_mfma_f32_16x16x32_bf16 v[20:23], v[124:127], a[28:31], v[20:23]\n\t" \
  "s_add_u32 s57, s57, s60\n\t" \
  "s_lshr_b32 s60, s88, s63\n\t" \
  "v_mfma_f32_16x16x32_bf16 v[24:27], v[120:123], a[100:103], v[24:27]\n\t" \
  "ds_read_b128 v[148:151], v1 offset:22528\n\t" \
  "v_mfma_f32_16x16x32_bf16 v[28:31], v[124:127], a[100:103], v[28:31]\n\t" \
  "s_and_b32 s60, s60, 1\n\t" \
  "s_lshl_b32 s60, s60, 13\n\t" \
  "s_waitcnt lgkmcnt(5)\n\t" \
  "v_mfma_f32_16x16x32_bf16 v[16:19], v[128:131], a[32:35], v[16:19]\n\t" \
  "ds_read_b128 v[152:155], v2 offset:20480\n\t" \
  "s_waitcnt lgkmcnt(5)\n\t" \
  "v_mfma_f32_16x16x32_bf16 v[20:23], v[132:135], a[32:35], v[20:23]\n\t" \
  "s_add_u32 s57, s57, s60\n\t" \
  "s_add_u32 s57, s57, 656896\n\t" \
  "v_mfma_f32_16x16x32_bf16 v[24:27], v[128:131], a[104:107], v[24:27]\n\t" \
  "ds_read_b128 v[156:159], v2 offset:22528\n\t" \
  "v_mfma_f32_16x16x32_bf16 v[28:31], v[132:135], a[104:107], v[28:31]\n\t" \
  "s_cmp_lt_i32 s62, 1\n\t" \
  "s_cselect_b32 s57, 0xf0000000, s57\n\t" \
  "s_waitcnt lgkmcnt(5)\n\t" \
  "v_mfma_f32_16x16x32_bf16 v[16:19], v[136:139], a[36:39], v[16:19]\n\t" \
  "ds_read_b128 v[160:163], v1 offset:24576\n\t" \
  "s_waitcnt lgkmcnt(5)\n\t" \
  "v_mfma_f32_16x16x32_bf16 v[20:23], v[140:143], a[36:39], v[20:23]\n\t" \
  "s_cmp_lg_u32 s40, 0\n\t" \
  "s_cselect_b32 s57, 0xf0000000, s57\n\t" \
  "v_mfma_f32_16x16x32_bf16 v[24:27], v[136:139], a[108:111], v[24:27]\n\t" \
  "ds_read_b128 v[164:167], v1 offset:26624\n\t" \
  "v_mfma_f32_16x16x32_bf16 v[28:31], v[140:143], a[108:111], v[28:31]\n\t" \
  "s_waitcnt lgkmcnt(5)\n\t" \
  "v_mfma_f32_16x16x32_bf16 v[16:19], v[144:147], a[40:43], v[16:19]\n\t" \
  "ds_read_b128 v[168:171], v2 offset:24576\n\t" \
  "s_waitcnt lgkmcnt(5)\n\t" \
  "v_mfma_f32_16x16x32_bf16 v[20:23], v[148:151], a[40:43], v[20:23]\n\t" \
  "v_mfma_f32_16x16x32_bf16 v[24:27], v[144:147], a[112:115], v[24:27]\n\t" \
  "ds_read_b128 v[172:175], v2 offset:26624\n\t" \
  "v_mfma_f32_16x16x32_bf16 v[28:31], v[148:151], a[112:115], v[28:31]\n\t" \
  "s_waitcnt lgkmcnt(5)\n\t" \
  "v_mfma_f32_16x16x32_bf16 v[16:19], v[152:155], a[44:47], v[16:19]\n\t" \
  "ds_read_b128 v[176:179], v1 offset:28672\n\t" \
  "s_waitcnt lgkmcnt(5)\n\t" \
  "v_mfma_f32_16x16x32_bf16 v[20:23], v[156:159], a[44:47], v[20:23]\n\t" \
  "v_mfma_f32_16x16x32_bf16 v[24:27], v[152:155], a[116:119], v[24:27]\n\t" \
  "ds_read_b128 v[180:183], v1 offset:30720\n\t" \
  "v_mfma_f32_16x16x32_bf16 v[28:31], v[156:159], a[116:119], v[28:31]\n\t" \
  "s_waitcnt lgkmcnt(5)\n\t" \
  "v_mfma_f32_16x16x32_bf16 v[16:19], v[160:163], a[48:51], v[16:19]\n\t" \
  "ds_read_b128 v[184:187], v2 offset:28672\n\t" \
  "s_waitcnt lgkmcnt(5)\n\t" \
  "v_mfma_f32_16x16x32_bf16 v[20:23], v[164:167], a[48:51], v[20:23]\n\t" \
  TK_PQ0 \
  "v_mfma_f32_16x16x32_bf16 v[24:27], v[160:163], a[120:123], v[24:27]\n\t" \
  "ds_read_b128 v[188:191], v2 offset:30720\n\t" \
  "v_mfma_f32_16x16x32_bf16 v[28:31], v[164:167], a[120:123], v[28:31]\n\t" \
  "s_waitcnt lgkmcnt(5)\n\t" \
  "v_mfma_f32_16x16x32_bf16 v[16:19], v[168:171], a[52:55], v[16:19]\n\t" \
  "ds_read_b128 v[192:195], v1 offset:32768\n\t" \
  "s_waitcnt lgkmcnt(5)\n\t" \
  "v_mfma_f32_16x16x32_bf16 v[20:23], v[172:175], a[52:55], v[20:23]\n\t" \
  "v_mfma_f32_16x16x32_bf16 v[24:27], v[168:171], a[124:127], v[24:27]\n\t" \
  "ds_read_b128 v[196:199], v1 offset:34816\n\t" \
  "v_mfma_f32_16x16x32_bf16 v[28:31], v[172:175], a[124:127], v[28:31]\n\t" \
  "s_waitcnt lgkmcnt(5)\n\t" \
  "v_mfma_f32_16x16x32_bf16 v[16:19], v[176:179], a[56:59], v[16:19]\n\t" \
  "ds_read_b128 v[200:203], v2 offset:32768\n\t" \
  "s_waitcnt lgkmcnt(5)\n\t" \
  "v_mfma_f32_16x16x32_bf16 v[20:23], v[180:183], a[56:59], v[20:23]\n\t" \
  "v_mfma_f32_16x16x32_bf16 v[24:27], v[176:179], a[128:131], v[24:27]\n\t" \
  "ds_read_b128 v[204:207], v2 offset:34816\n\t" \
  "v_mfma_f32_16x16x32_bf16 v[28:31], v[180:183], a[128:131], v[28:31]\n\t" \
  "s_waitcnt lgkmcnt(5)\n\t" \
  "v_mfma_f32_16x16x32_bf16 v[16:19], v[184:187], a[60:63], v[16:19]\n\t" \
  "s_waitcnt lgkmcnt(4)\n\t" \
  "v_mfma_f32_16x16x32_bf16 v[20:23], v[188:191], a[60:63], v[20:23]\n\t" \
  "buffer_load_dword v4, v7, %[X], 0 offen sc1\n\t" \
  "v_mfma_f32_16x16x32_bf16 v[24:27], v[184:187], a[132:135], v[24:27]\n\t" \
  "v_mfma_f32_16x16x32_bf16 v[28:31], v[188:191], a[132:135], v[28:31]\n\t" \
  "s_waitcnt lgkmcnt(3)\n\t" \
  "v_mfma_f32_16x16x32_bf16 v[16:19], v[192:195], a[64:67], v[16:19]\n\t" \
  "v_xor_b32 v1, 0x10000, v1\n\t" \
  "v_xor_b32 v2, 0x10000, v2\n\t" \
  "s_waitcnt lgkmcnt(2)\n\t" \
  "v_mfma_f32_16x16x32_bf16 v[20:23], v[196:199], a[64:67], v[20:23]\n\t" \
  "v_mfma_f32_16x16x32_bf16 v[24:27], v[192:195], a[136:139], v[24:27]\n\t" \
  "v_mfma_f32_16x16x32_bf16 v[28:31], v[196:199], a[136:139], v[28:31]\n\t" \
  "s_waitcnt lgkmcnt(1)\n\t" \
  "v_mfma_f32_16x16x32_bf16 v[16:19], v[200:203], a[68:71], v[16:19]\n\t" \
  "s_waitcnt lgkmcnt(0)\n\t" \
  "v_mfma_f32_16x16x32_bf16 v[20:23], v[204:207], a[68:71], v[20:23]\n\t" \
  "v_mfma_f32_16x16x32_bf16 v[24:27], v[200:203], a[140:143], v[24:27]\n\t" \
  "v_mfma_f32_16x16x32_bf16 v[28:31], v[204:207], a[140:143], v[28:31]\n\t" \
  "v_mfma_f32_16x16x32_bf16 v[32:35], v[64:67], a[144:147], 0\n\t" \
  "v_cvt_pk_bf16_f32 v48, v16, v17\n\t" \
  "v_cvt_pk_bf16_f32 v49, v18, v19\n\t" \
  "v_mfma_f32_16x16x32_bf16 v[36:39], v[68:71], a[144:147], 0\n\t" \
  "v_cvt_pk_bf16_f32 v50, v20, v21\n\t" \
  "v_cvt_pk_bf16_f32 v51, v22, v23\n\t" \
  "v_mfma_f32_16x16x32_bf16 v[32:35], v[72:75], a[148:151], v[32:35]\n\t" \
  "v_cvt_pk_bf16_f32 v52, v24, v25\n\t" \
  "v_cvt_pk_bf16_f32 v53, v26, v27\n\t" \
  "v_mfma_f32_16x16x32_bf16 v[36:39], v[76:79], a[148:151], v[36:39]\n\t" \
  "v_cvt_pk_bf16_f32 v54, v28, v29\n\t" \
  "v_cvt_pk_bf16_f32 v55, v30, v31\n\t" \
  "v_mfma_f32_16x16x32_bf16 v[32:35], v[80:83], a[152:155], v[32:35]\n\t" \
  "buffer_store_dwordx4 v[48:51], v0, %[X], s50 offen offset:0 sc1\n\t" \
  "v_mfma_f32_16x16x32_bf16 v[36:39], v[84:87], a[152:155], v[36:39]\n\t" \
  "v_mfma_f32_16x16x32_bf16 v[32:35], v[88:91], a[156:159], v[32:35]\n\t" \
  "v_mfma_f32_16x16x32_bf16 v[36:39], v[92:95], a[156:159], v[36:39]\n\t" \
  "v_mfma_f32_16x16x32_bf16 v[32:35], v[96:99], a[160:163], v[32:35]\n\t" \
  "v_mfma_f32_16x16x32_bf16 v[36:39], v[100:103], a[160:163], v[36:39]\n\t" \
  "v_mfma_f32_16x16x32_bf16 v[32:35], v[104:107], a[164:167], v[32:35]\n\t" \
  "v_mfma_f32_16x16x32_bf16 v[36:39], v[108:111], a[164:167], v[36:39]\n\t" \
  "v_mfma_f32_16x16x32_bf16 v[32:35], v[112:115], a[168:171], v[32:35]\n\t" \
  "v_mfma_f32_16x16x32_bf16 v[36:39], v[116:119], a[168:171], v[36:39]\n\t" \
  "v_mfma_f32_16x16x32_bf16 v[32:35], v[120:123], a[172:175], v[32:35]\n\t" \
  "v_mfma_f32_16x16x32_bf16 v[36:39], v[124:127], a[172:175], v[36:39]\n\t" \
  "s_waitcnt vmcnt(1)\n\t" \
  "v_cmp_eq_u32 vcc, 0, v4\n\t" \
  "v_mfma_f32_16x16x32_bf16 v[32:35], v[128:131], a[176:179], v[32:35]\n\t" \
  "v_mfma_f32_16x16x32_bf16 v[36:39], v[132:135], a[176:179], v[36:39]\n\t" \
  "s_cmp_eq_u64 vcc, 0\n\t" \
  "s_cselect_b32 s60, 1, 0\n\t" \
  "v_mov_b32 v4, s60\n\t" \
  "v_mfma_f32_16x16x32_bf16 v[32:35], v[136:139], a[180:183], v[32:35]\n\t" \
  "ds_write_b32 v5, v4\n\t" \
  "v_mfma_f32_16x16x32_bf16 v[36:39], v[140:143], a[180:183], v[36:39]\n\t" \
  "v_mfma_f32_16x16x32_bf16 v[32:35], v[144:147], a[184:187], v[32:35]\n\t" \
  "v_mfma_f32_16x16x32_bf16 v[36:39], v[148:151], a[184:187], v[36:39]\n\t" \
  "buffer_store_dwordx4 v[52:55], v0, %[X], s50 offen offset:1024 sc1\n\t" \
  "v_mfma_f32_16x16x32_bf16 v[32:35], v[152:155], a[188:191], v[32:35]\n\t" \
  "v_mfma_f32_16x16x32_bf16 v[36:39], v[156:159], a[188:191], v[36:39]\n\t" \
  "s_waitcnt lgkmcnt(0)\n\t" \
  "s_barrier\n\t" \
  "v_mfma_f32_16x16x32_bf16 v[32:35], v[160:163], a[192:195], v[32:35]\n\t" \
  "ds_read_b32 v4, v6 offset:40959\n\t" \
  "v_mfma_f32_16x16x32_bf16 v[36:39], v[164:167], a[192:195], v[36:39]\n\t" \
  "v_mfma_f32_16x16x32_bf16 v[32:35], v[168:171], a[196:199], v[32:35]\n\t" \
  "v_mfma_f32_16x16x32_bf16 v[36:39], v[172:175], a[196:199], v[36:39]\n\t" \
  "v_mfma_f32_16x16x32_bf16 v[32:35], v[176:179], a[200:203], v[32:35]\n\t" \
  "v_mfma_f32_16x16x32_bf16 v[36:39], v[180:183], a[200:203], v[36:39]\n\t" \
  "s_waitcnt lgkmcnt(0)\n\t" \
  "v_readfirstlane_b32 s60, v4\n\t" \
  "s_add_u32 s55, s83, s36\n\t" \
  "v_mfma_f32_16x16x32_bf16 v[32:35], v[184:187], a[204:207], v[32:35]\n\t" \
  "s_cmp_eq_u32 s48, 63\n\t" \
  "s_cselect_b32 s56, 0, s60\n\t" \
  "v_mfma_f32_16x16x32_bf16 v[36:39], v[188:191], a[204:207], v[36:39]\n\t" \
  "s_cmp_eq_u32 s56, 0\n\t" \
  "s_cselect_b32 s55, 0xf0000000, s55\n\t" \
  "v_mfma_f32_16x16x32_bf16 v[32:35], v[192:195], a[208:211], v[32:35]\n\t" \
  "v_mfma_f32_16x16x32_bf16 v[36:39], v[196:199], a[208:211], v[36:39]\n\t" \
  "v_mfma_f32_16x16x32_bf16 v[32:35], v[200:203], a[212:215], v[32:35]\n\t" \
  "s_add_u32 m0, s46, 0\n\t" \
  "s_add_u32 s47, s55, 0\n\t" \
  "buffer_load_dwordx4 v0, %[X], s47 offen sc1 lds\n\t" \
  "v_mfma_f32_16x16x32_bf16 v[36:39], v[204:207], a[212:215], v[36:39]\n\t" \
  "v_mfma_f32_16x16x32_bf16 v[40:43], v[64:67], a[216:219], 0\n\t" \
  "v_mfma_f32_16x16x32_bf16 v[44:47], v[68:71], a[216:219], 0\n\t" \
  "v_mfma_f32_16x16x32_bf16 v[40:43], v[72:75], a[220:223], v[40:43]\n\t" \
  "v_cvt_pk_bf16_f32 v56, v32, v33\n\t" \
  "v_cvt_pk_bf16_f32 v57, v34, v35\n\t" \
  "s_add_u32 m0, s46, 4096\n\t" \
  "s_add_u32 s47, s55, 4096\n\t" \
  "buffer_load_dwordx4 v0, %[X], s47 offen sc1 lds\n\t" \
  "v_mfma_f32_16x16x32_bf16 v[44:47], v[76:79], a[220:223], v[44:47]\n\t" \
  "v_cvt_pk_bf16_f32 v58, v36, v37\n\t" \
  "v_cvt_pk_bf16_f32 v59, v38, v39\n\t" \
  "v_mfma_f32_16x16x32_bf16 v[40:43], v[80:83], a[224:227], v[40:43]\n\t" \
  "v_mfma_f32_16x16x32_bf16 v[44:47], v[84:87], a[224:227], v[44:47]\n\t" \
  "buffer_store_dwordx4 v[56:59], v0, %[X], s50 offen offset:2048 sc1\n\t" \
  "v_mfma_f32_16x16x32_bf16 v[40:43], v[88:91], a[228:231], v[40:43]\n\t" \
  "s_add_u32 m0, s46, 8192\n\t" \
  "s_add_u32 s47, s55, 16384\n\t" \
  "buffer_load_dwordx4 v0, %[X], s47 offen sc1 lds\n\t" \
  "v_mfma_f32_16x16x32_bf16 v[44:47], v[92:95], a[228:231], v[44:47]\n\t" \
  "v_mfma_f32_16x16x32_bf16 v[40:43], v[96:99], a[232:235], v[40:43]\n\t" \
  "v_mfma_f32_16x16x32_bf16 v[44:47], v[100:103], a[232:235], v[44:47]\n\t" \
  "v_mfma_f32_16x16x32_bf16 v[40:43], v[104:107], a[236:239], v[40:43]\n\t" \
  "s_add_u32 m0, s46, 12288\n\t" \
  "s_add_u32 s47, s55, 20480\n\t" \
  "buffer_load_dwordx4 v0, %[X], s47 offen sc1 lds\n\t" \
  "v_mfma_f32_16x16x32_bf16 v[44:47], v[108:111], a[236:239], v[44:47]\n\t" \
  "v_mfma_f32_16x16x32_bf16 v[40:43], v[112:115], a[240:243], v[40:43]\n\t" \
  "v_mfma_f32_16x16x32_bf16 v[44:47], v[116:119], a[240:243], v[44:47]\n\t" \
  "v_mfma_f32_16x16x32_bf16 v[40:43], v[120:123], a[244:247], v[40:43]\n\t" \
  "s_add_u32 m0, s46, 16384\n\t" \
  "s_add_u32 s47, s55, 32768\n\t" \
  "buffer_load_dwordx4 v0, %[X], s47 offen sc1 lds\n\t" \
  "v_mfma_f32_16x16x32_bf16 v[44:47], v[124:127], a[244:247], v[44:47]\n\t" \
  "v_mfma_f32_16x16x32_bf16 v[40:43], v[128:131], a[248:251], v[40:43]\n\t" \
  "v_mfma_f32_16x16x32_bf16 v[44:47], v[132:135], a[248:251], v[44:47]\n\t" \
  "v_mfma_f32_16x16x32_bf16 v[40:43], v[136:139], a[252:255], v[40:43]\n\t" \
  "s_add_u32 m0, s46, 20480\n\t" \
  "s_add_u32 s47, s55, 36864\n\t" \
  "buffer_load_dwordx4 v0, %[X], s47 offen sc1 lds\n\t" \
  "v_mfma_f32_16x16x32_bf16 v[44:47], v[140:143], a[252:255], v[44:47]\n\t" \
  "v_mfma_f32_16x16x32_bf16 v[40:43], v[144:147], v[224:227], v[40:43]\n\t" \
  "v_mfma_f32_16x16x32_bf16 v[44:47], v[148:151], v[224:227], v[44:47]\n\t" \
  "v_mfma_f32_16x16x32_bf16 v[40:43], v[152:155], v[228:231], v[40:43]\n\t" \
  "s_add_u32 m0, s46, 24576\n\t" \
  "s_add_u32 s47, s55, 49152\n\t" \
  "buffer_load_dwordx4 v0, %[X], s47 offen sc1 lds\n\t" \
  "v_mfma_f32_16x16x32_bf16 v[44:47], v[156:159], v[228:231], v[44:47]\n\t" \
  "v_mfma_f32_16x16x32_bf16 v[40:43], v[160:163], v[232:235], v[40:43]\n\t" \
  "v_mfma_f32_16x16x32_bf16 v[44:47], v[164:167], v[232:235], v[44:47]\n\t" \
  "v_mfma_f32_16x16x32_bf16 v[40:43], v[168:171], v[236:239], v[40:43]\n\t" \
  "s_add_u32 m0, s46, 28672\n\t" \
  "s_add_u32 s47, s55, 53248\n\t" \
  "buffer_load_dwordx4 v0, %[X], s47 offen sc1 lds\n\t" \
  "v_mfma_f32_16x16x32_bf16 v[44:47], v[172:175], v[236:239], v[44:47]\n\t" \
  "v_mfma_f32_16x16x32_bf16 v[40:43], v[176:179], v[240:243], v[40:43]\n\t" \
  "v_mfma_f32_16x16x32_bf16 v[44:47], v[180:183], v[240:243], v[44:47]\n\t" \
  "v_mfma_f32_16x16x32_bf16 v[40:43], v[184:187], v[244:247], v[40:43]\n\t" \
  "s_add_u32 m0, s46, 32768\n\t" \
  "s_add_u32 s47, s55, 65536\n\t" \
  "buffer_load_dwordx4 v0, %[X], s47 offen sc1 lds\n\t" \
  "v_mfma_f32_16x16x32_bf16 v[44:47], v[188:191], v[244:247], v[44:47]\n\t" \
  "v_mfma_f32_16x16x32_bf16 v[40:43], v[192:195], v[248:251], v[40:43]\n\t" \
  "v_mfma_f32_16x16x32_bf16 v[44:47], v[196:199], v[248:251], v[44:47]\n\t" \
  "buffer_load_dword v8, off, %[X], s57 sc1\n\t" \
  "v_mfma_f32_16x16x32_bf16 v[40:43], v[200:203], v[252:255], v[40:43]\n\t" \
  "v_mfma_f32_16x16x32_bf16 v[44:47], v[204:207], v[252:255], v[44:47]\n\t" \
  "s_nop 4\n\t" \
  "v_cvt_pk_bf16_f32 v60, v40, v41\n\t" \
  "v_cvt_pk_bf16_f32 v61, v42, v43\n\t" \
  "s_nop 1\n\t" \
  "v_cvt_pk_bf16_f32 v62, v44, v45\n\t" \
  "v_cvt_pk_bf16_f32 v63, v46, v47\n\t" \
  "buffer_store_dwordx4 v[60:63], v0, %[X], s50 offen offset:3072 sc1\n\t" \
  TK_PQ2 \
  "s_add_u32 s49, s82, s38\n\t" \
  "s_cmp_eq_u32 s48, 63\n\t" \
  "s_cbranch_scc1 LEND_%=\n\t" \
  "s_add_u32 s48, s48, 1\n\t" \
  "s_cmp_eq_u32 s56, 0\n\t" \
  "s_cbranch_scc1 LCOLD_%=\n\t" \
  "s_waitcnt vmcnt(0)\n\t" \
  "s_mov_b64 exec, 1\n\t" \
  "buffer_store_dword v6, off, %[X], s49 sc1\n\t" \
  "s_mov_b64 exec, -1\n\t" \
  "s_mov_b32 s49, 0xf0000000\n\t" \
  "s_cmp_lt_i32 s57, 0\n\t" \
  "s_cbranch_scc1 LTOP_%=\n\t" \
  "s_movk_i32 s63, 4000\n\t" \
  "s_branch LGW_%=u\n\t" \
  "LGW_%=:\n\t" \
  "s_sleep " TK_PSG "\n\t" \
  "buffer_load_dword v8, off, %[X], s57 sc1\n\t" \
  "s_waitcnt vmcnt(0)\n\t" \
  "LGW_%=u:\n\t" \
  "s_memrealtime s[52:53]\n\t" \
  "v_readfirstlane_b32 s60, v8\n\t" \
  "s_waitcnt lgkmcnt(0)\n\t" \
  "s_sub_u32 s63, s63, 1\n\t" \
  "s_cbranch_scc1 LTOP_%=\n\t" \
  "s_cmp_eq_u32 s60, -1\n\t" \
  "s_cbranch_scc1 LGW_%=\n\t" \
  "s_not_b32 s60, s60\n\t" \
  "s_lshl_b32 s61, s52, 8\n\t" \
  "s_sub_u32 s60, s60, s61\n\t" \
  "s_ashr_i32 s60, s60, 8\n\t" \
  "s_sub_i32 s60, s60, " TK_QXW "\n\t" \
  "s_min_i32 s60, s60, 400\n\t" \
  "s_add_u32 s54, s52, s60\n\t" \
  "LGW_%=w:\n\t" \
  "s_sleep 1\n\t" \
  "s_memrealtime s[52:53]\n\t" \
  "s_waitcnt lgkmcnt(0)\n\t" \
  "s_sub_i32 s53, s52, s54\n\t" \
  "s_cmp_lt_i32 s53, 0\n\t" \
  "s_cbranch_scc1 LGW_%=w\n\t" \
  "s_branch LTOP_%=\n\t" \
  "LEND_%=:\n\t" \
  "s_waitcnt vmcnt(0)\n\t" \
  "s_mov_b64 exec, 1\n\t" \
  "buffer_store_dword v6, off, %[X], s49 sc1\n\t" \
  "s_mov_b64 exec, -1\n\t" \
  "s_mov_b32 s49, 0xf0000000\n\t" \
  TK_PQ0 \
  ""
__device__ __forceinline__ unsigned hoff(int r, int piece) {
  const int m = 4 * (r >> 3) + (r & 3), kp = piece >> 3;
  return (P_H + (unsigned)(kp >> 1)) * PAIR + (unsigned)((kp & 1) * 4096 + (16 * ((r >> 2) & 1) + m) * 128 + (((piece ^ m) & 7) << 4));
}
template <int SLEEP>
__device__ __forceinline__ void wait_all(rsrc_t X, unsigned vo, unsigned so) {
  unsigned spins = 0;
  while (true) {
    asm volatile("" ::: "memory");
    if (__all(ld4(X, vo, so) != 0u)) break;
    if (spin_fail(spins, X)) break;
    __builtin_amdgcn_s_sleep(SLEEP);
  }
  asm volatile("" ::: "memory");
}
#define VQ_CLOB "v8", "v16", "v17", "v18", "v19", "v20", "v21", "v22", "v23", "v24", "v25", "v26", "v27", "v28", "v29", "v30", "v31", "v32", "v33", "v34", "v35", "v36", "v37", "v38", "v39", "v40", "v41", "v42", "v43", "v44", "v45", "v46", "v47", "v48", "v49", "v50", "v51", "v52", "v53", "v54", "v55", "v56", "v57", "v58", "v59", "v60", "v61", "v62", "v63", "v64", "v65", "v66", "v67", "v68", "v69", "v70", "v71", "v72", "v73", "v74", "v75", "v76", "v77", "v78", "v79", "v80", "v81", "v82", "v83", "v84", "v85", "v86", "v87", "v88", "v89", "v90", "v91", "v92", "v93", "v94", "v95", "v96", "v97", "v98", "v99", "v100", "v101", "v102", "v103", "v104", "v105", "v106", "v107", "v108", "v109", "v110", "v111", "v112", "v113", "v114", "v115", "v116", "v117", "v118", "v119", "v120", "v121", "v122", "v123", "v124", "v125", "v126", "v127", "v128", "v129", "v130", "v131", "v132", "v133", "v134", "v135", "v136", "v137", "v138", "v139", "v140", "v141", "v142", "v143", "v144", "v145", "v146", "v147", "v148", "v149", "v150", "v151", "v152", "v153", "v154", "v155", "v156", "v157", "v158", "v159", "v160", "v161", "v162", "v163", "v164", "v165", "v166", "v167", "v168", "v169", "v170", "v171", "v172", "v173", "v174", "v175", "v176", "v177", "v178", "v179", "v180", "v181", "v182", "v183", "v184", "v185", "v186", "v187", "v188", "v189", "v190", "v191", "v192", "v193", "v194", "v195", "v196", "v197", "v198", "v199", "v200", "v201", "v202", "v203", "v204", "v205", "v206", "v207", "v220", "v221"
#define VA_CLOB "v40", "v41", "v42", "v43", "v44", "v45", "v46", "v47", "v48", "v49", "v50", "v51", "v52", "v53", "v54", "v55", "v56", "v57", "v58", "v59", "v60", "v61", "v62", "v63", "v64", "v65", "v66", "v67", "v68", "v69", "v70", "v71", "v72", "v73", "v74", "v75", "v76", "v77", "v78", "v79", "v80", "v81", "v82", "v83", "v84", "v85", "v86", "v87", "v88", "v89", "v90", "v91", "v92", "v93", "v94", "v95", "v96", "v97", "v98", "v99", "v100", "v101", "v102", "v103", "v104", "v105", "v106", "v107", "v108", "v109", "v110", "v111", "v112", "v113", "v114", "v115", "v116", "v117", "v118", "v119", "v120", "v121", "v122", "v123", "v124", "v125", "v126", "v127", "v128", "v129", "v130", "v131", "v132", "v133", "v134", "v135", "v136", "v137", "v138", "v139", "v140", "v141", "v142", "v143", "v144", "v145", "v146", "v147", "v148", "v149", "v150", "v151", "v152", "v153", "v154", "v155", "v156", "v157", "v158", "v159", "v160", "v161", "v162", "v163", "v164", "v165", "v166", "v167", "v168", "v169", "v170", "v171", "v172", "v173", "v174", "v175", "v176", "v177", "v178", "v179", "v180", "v181", "v182", "v183", "v184", "v185", "v186", "v187", "v188", "v189", "v190", "v191", "v192", "v193", "v194", "v195", "v196", "v197", "v198", "v199", "v200", "v201", "v202", "v203", "v204", "v205", "v206", "v207", "v208", "v209", "v210", "v211", "v220", "v221"
#define TOK_COMMON() \
  const MP p = getp(); \
  const int c = (int)blockIdx.x - NCHW, xq = c & 3, L = (c >> 2) & 1, i = c >> 3; \
  const int tid = threadIdx.x, lane = tid & 63, w = __builtin_amdgcn_readfirstlane(tid >> 6); \
  const rsrc_t X = mkr(p.xb, XB_BYTES)
#define W_IN [W] "s"(WR)
#define W_CLOB "a0", "a1", "a2", "a3", "a4", "a5", "a6", "a7", "a8", "a9", "a10", "a11", "a12", "a13", "a14", "a15", "a16", "a17", "a18", "a19", "a20", "a21", "a22", "a23", "a24", "a25", "a26", "a27", "a28", "a29", "a30", "a31", "a32", "a33", "a34", "a35", "a36", "a37", "a38", "a39", "a40", "a41", "a42", "a43", "a44", "a45", "a46", "a47", "a48", "a49", "a50", "a51", "a52", "a53", "a54", "a55", "a56", "a57", "a58", "a59", "a60", "a61", "a62", "a63", "a64", "a65", "a66", "a67", "a68", "a69", "a70", "a71", "a72", "a73", "a74", "a75", "a76", "a77", "a78", "a79", "a80", "a81", "a82", "a83", "a84", "a85", "a86", "a87", "a88", "a89", "a90", "a91", "a92", "a93", "a94", "a95", "a96", "a97", "a98", "a99", "a100", "a101", "a102", "a103", "a104", "a105", "a106", "a107", "a108", "a109", "a110", "a111", "a112", "a113", "a114", "a115", "a116", "a117", "a118", "a119", "a120", "a121", "a122", "a123", "a124", "a125", "a126", "a127", "a128", "a129", "a130", "a131", "a132", "a133", "a134", "a135", "a136", "a137", "a138", "a139", "a140", "a141", "a142", "a143", "a144", "a145", "a146", "a147", "a148", "a149", "a150", "a151", "a152", "a153", "a154", "a155", "a156", "a157", "a158", "a159", "a160", "a161", "a162", "a163", "a164", "a165", "a166", "a167", "a168", "a169", "a170", "a171", "a172", "a173", "a174", "a175", "a176", "a177", "a178", "a179", "a180", "a181", "a182", "a183", "a184", "a185", "a186", "a187", "a188", "a189", "a190", "a191", "a192", "a193", "a194", "a195", "a196", "a197", "a198", "a199", "a200", "a201", "a202", "a203", "a204", "a205", "a206", "a207", "a208", "a209", "a210", "a211", "a212", "a213", "a214", "a215", "a216", "a217", "a218", "a219", "a220", "a221", "a222", "a223", "a224", "a225", "a226", "a227", "a228", "a229", "a230", "a231", "a232", "a233", "a234", "a235", "a236", "a237", "a238", "a239", "a240", "a241", "a242", "a243", "a244", "a245", "a246", "a247", "a248", "a249", "a250", "a251", "a252", "a253", "a254", "a255", "v224", "v225", "v226", "v227", "v228", "v229", "v230", "v231", "v232", "v233", "v234", "v235", "v236", "v237", "v238", "v239", "v240", "v241", "v242", "v243", "v244", "v245", "v246", "v247", "v248", "v249", "v250", "v251", "v252", "v253", "v254", "v255"
#define BOOT_IN "{s67}"(L), [ctab] "i"(CTAB_OFF)
#define S_CLOB "s76", "s78", "s79", "s86", "s88", "s89", "s90", "s91", "s46", "s47", "s48", "s49", "s50", "s51", "s52", "s53", "s54", "s55", "s56", "s57", "s58", "s59", "s60", "s61", "s62", "s63", "s68", "s69", "s70", "s71", "s72", "s73", "s82", "s83", "s84", "m0", "vcc", "scc", "memory"
typedef unsigned v8w __attribute__((ext_vector_type(8)));

__device__ __forceinline__ void qkv_role() {
  TOK_COMMON();
  const int role = i * 4 + w, hh = 8 * xq + role / 6, part = role % 6, which = part >> 1, half = part & 1;
  const rsrc_t WR = mkr((which == 0 ? p.Wq : (which == 1 ? p.Wk : p.Wv)) + hh * DH + 64 * half, DM * HD * 2u - (unsigned)(hh * DH + 64 * half) * 2u);
  const int m = lane & 15, g = lane >> 4;
  const unsigned a0 = (unsigned)(m * 128 + ((g ^ (m & 7)) << 4));
  v8w lv = {(unsigned)lane * 16u, a0, a0 ^ 64u, (unsigned)(lane & (L ? 15 : 3)) * 128u, 0u, 40960u + 4u * (unsigned)w, 1u, 0u};
  const unsigned c36 = XQL + P_H * PAIR + (unsigned)w * 1024u, c37 = XQ + (unsigned)(3 * hh + which) * PAIR + (unsigned)half * (which == 2 ? 4096u : 2048u);
  const unsigned c38 = P_FA * PAIR + (unsigned)(hh * 128 + part * 16), c39 = w == 0 ? P_FB * PAIR + 4096u : 0xF0000000u, c41 = (unsigned)w * 1024u;
  const int c42 = L ? 1 - QLEAD1 : 1 - QLEAD;
  unsigned d74 = TRC_OFF + 262144u + (unsigned)((c * 4 + w) * 66) * 8u;
  TT(64, 0);
#define Q_CALL(A) asm volatile("s_waitcnt vmcnt(0) lgkmcnt(0)\n\t" A : "+{v[0:7]}"(lv), "+{s74}"(d74) \
               : [X] "s"(X), "{s36}"(c36), "{s37}"(c37), "{s38}"(c38), "{s39}"(c39), "{s40}"(w), "{s41}"(c41), "{s42}"(c42), "{s45}"(i), "{s92}"(TMO_OFF), BOOT_IN, [cap] "i"(SPIN_CAP), [qxw] "i"(QXW), [qxc] "i"(QXC), W_IN : VQ_CLOB, W_CLOB, S_CLOB)
  if (which == 2) { Q_CALL(QV_ASM "LQVEND:"); __builtin_amdgcn_endpgm(); }
  Q_CALL(QK_ASM "LQKEND:");
  __builtin_amdgcn_endpgm();
}

__device__ __forceinline__ void ap_role() {
  TOK_COMMON();
  const int hp = 4 * xq + ((i - 12) >> 1), ch = i & 1, a = w >> 1, rt = w & 1, h = 2 * hp + a;
  const int n = lane & 15, g = lane >> 4;
  const rsrc_t WR = mkr(p.Wo + (long)hp * 2 * DH * DM, 2u * DH * DM * 2u);
  const unsigned l16 = (unsigned)lane * 16u, ab = (unsigned)a * 24576u + l16;
  v16u lv = {l16, ab, ab + (unsigned)rt * 4096u, 49152u + (unsigned)w * 4096u + l16, 49152u + (unsigned)(w ^ 2) * 4096u + l16, l16 & ~32u, l16 >> 1,
             (unsigned)(g * 262144 + n * 16), (unsigned)(n * 32 + g * 8), (unsigned)((lane & 7) % 6) * 16u, 0u, 0u, 0u, 0u, 0u, 0u};
  v8w mk;
  const int qi = 8 * (n >> 2) + (n & 3) + 4 * rt;
#pragma unroll
  for (int k = 0; k < 8; ++k) mk[k] = 8 * g + k > qi ? 0xFF800000u : 0u;
  v16u zz = {0u, 0u, 0u, 0u, 0u, 0u, 0u, 0u, 0u, 0u, 0u, 0u, 0u, 0u, 0u, 0u};
  const unsigned tb = XAL + (unsigned)(3 * h) * PAIR;
  const unsigned c36 = tb + (rt ? PAIR + 4096u : 0u), c37 = tb + (rt ? 2u * PAIR : 4096u), c38 = tb + (rt ? 2u * PAIR + 4096u : PAIR);
  const unsigned c39 = XK + (P_KV + (unsigned)(h >> 1)) * PAIR + (unsigned)(h & 1) * 4096u, c41 = P_FA * PAIR + (unsigned)h * 128u;
  const unsigned c42 = w == 0 ? P_FB * PAIR + (unsigned)(2 * hp + ch) * 128u : 0xF0000000u;
  const unsigned c43 = XA + TP0 * XBLK + (unsigned)(8 * rt + (hp >> 3)) * PAIR + (unsigned)((hp & 7) * 1024 + ch * 512 + a * 256);
  unsigned d44 = XT + (TP0 + 8u) * XBLK + (unsigned)(hp >> 1) * PAIR + (unsigned)((((hp & 1) * 4 + 2 * ch + a) * 2 + rt) * 512);
  const unsigned c45 = (unsigned)(a * 24576 + rt * 12288);
  unsigned d74 = TRC_OFF + 262144u + (unsigned)((c * 4 + w) * 66) * 8u;
  asm volatile("s_waitcnt vmcnt(0) lgkmcnt(0)\n\t" AP_ASM "LAPEND:" : "+{v[0:15]}"(lv), "+{v[24:39]}"(zz), "+{s74}"(d74), "+{s44}"(d44)
               : [X] "s"(X), "{s36}"(c36), "{s37}"(c37), "{s38}"(c38), "{s39}"(c39), "{s41}"(c41), "{s42}"(c42), "{s43}"(c43), "{s45}"(c45), "{s64}"(512 * ch + 256 * a), "{s65}"(1024 + 64 * ch + 32 * a), "{s66}"(147456 * a), BOOT_IN,
                 "{s92}"(TMO_OFF), [cap] "i"(SPIN_CAP), "{v[16:23]}"(mk), W_IN : VA_CLOB, W_CLOB, S_CLOB);
  __builtin_amdgcn_endpgm();
}

template <int N> __device__ __forceinline__ float rorN(float v) { return __uint_as_float((unsigned)__builtin_amdgcn_update_dpp(0, (int)__float_as_uint(v), 0x120 + N, 0xf, 0xf, false)); }
__device__ __forceinline__ void up8(v4u v, float* x) {
  x[0] = lo2f(v.x); x[1] = hi2f(v.x); x[2] = lo2f(v.y); x[3] = hi2f(v.y); x[4] = lo2f(v.z); x[5] = hi2f(v.z); x[6] = lo2f(v.w); x[7] = hi2f(v.w);
}
__device__ __forceinline__ v4u pk8(const float* x) { return (v4u){pk2(x[0], x[1]), pk2(x[2], x[3]), pk2(x[4], x[5]), pk2(x[6], x[7])}; }

__device__ __forceinline__ void rb9(float (&x)[9]) {
#pragma unroll
  for (int i = 0; i < 8; i += 2) { const unsigned u = pk2(x[i], x[i + 1]); x[i] = lo2f(u); x[i + 1] = hi2f(u); }
  x[8] = lo2f(pk2(x[8], x[8]));
}
template <int R>
__device__ __forceinline__ void lnR(const float (&x)[R][9], float (&o)[R][9]) {
  float s[2 * R], pv[R];
#pragma unroll
  for (int r = 0; r < R; ++r) {
    pv[r] = __uint_as_float(__builtin_amdgcn_readfirstlane(__float_as_uint(x[r][0])));
    const f32x2 p2 = {pv[r], pv[r]};
    f32x2 d[4];
#pragma unroll
    for (int i = 0; i < 4; ++i) d[i] = (f32x2){x[r][2 * i], x[r][2 * i + 1]} - p2;
    const float d8 = x[r][8] - pv[r];
    const f32x2 a = (d[0] + d[1]) + (d[2] + d[3]);
    const f32x2 q = __builtin_elementwise_fma(d[3], d[3], __builtin_elementwise_fma(d[2], d[2], __builtin_elementwise_fma(d[1], d[1], d[0] * d[0])));
    s[2 * r] = (a[0] + d8) + a[1];
    s[2 * r + 1] = __builtin_fmaf(d8, d8, q[0]) + q[1];
  }
#pragma unroll
  for (int k = 0; k < 2 * R; ++k) s[k] += rorN<8>(s[k]);
#pragma unroll
  for (int k = 0; k < 2 * R; ++k) s[k] += rorN<4>(s[k]);
#pragma unroll
  for (int k = 0; k < 2 * R; ++k) s[k] += rorN<2>(s[k]);
#pragma unroll
  for (int k = 0; k < 2 * R; ++k) s[k] += rorN<1>(s[k]);
#pragma unroll
  for (int r = 0; r < R; ++r) {
    auto u = __builtin_amdgcn_permlane16_swap(__float_as_uint(s[2 * r]), __float_as_uint(s[2 * r + 1]), false, false);
    s[2 * r] = __uint_as_float(u[0]) + __uint_as_float(u[1]);
  }
#pragma unroll
  for (int r = 0; r < R; ++r) {
    const unsigned u = __float_as_uint(s[2 * r]);
    const float md = (__uint_as_float(__builtin_amdgcn_readlane(u, 0)) + __uint_as_float(__builtin_amdgcn_readlane(u, 32))) * (1.0f / DM);
    const float sq = (__uint_as_float(__builtin_amdgcn_readlane(u, 16)) + __uint_as_float(__builtin_amdgcn_readlane(u, 48))) * (1.0f / DM);
    const unsigned mv = pk2(pv[r] + md, __builtin_fmaxf(__builtin_fmaf(-md, md, sq), 0.f));
    const float m = lo2f(mv), var = hi2f(mv);
    const float ve = lo2f(pk2(var + 1e-5f, 0.f));
    const float den = lo2f(pk2(__builtin_amdgcn_sqrtf(ve), 0.f));
    const float inv = __builtin_amdgcn_rcpf(den);
#pragma unroll
    for (int i = 0; i < 9; ++i) o[r][i] = (x[r][i] - m) * inv;
  }
}
__device__ __forceinline__ void ln9(const float (&x)[9], float (&o)[9]) { lnR<1>((const float (&)[1][9])x, (float (&)[1][9])o); }
__device__ __forceinline__ v4u gat8(float v) {
  const unsigned d = pk2(v, __uint_as_float((unsigned)__builtin_amdgcn_mov_dpp((int)__float_as_uint(v), 0xB1, 0xf, 0xf, true)));
  return (v4u){d, (unsigned)__builtin_amdgcn_mov_dpp((int)d, 0x102, 0xf, 0xf, true), (unsigned)__builtin_amdgcn_mov_dpp((int)d, 0x104, 0xf, 0xf, true),
               (unsigned)__builtin_amdgcn_mov_dpp((int)d, 0x106, 0xf, 0xf, true)};
}
__device__ __forceinline__ void tok_row(const MP& p, const rsrc_t X, unsigned sb1, int L, int s, int r, int lane, const v4u (&v)[18], v4u px, v4u py, unsigned px9, unsigned py9) {
  const long tok = (long)s * SEG + r;
  const int m = lane & 15, g = lane >> 4;
  const int xp = 4 * (g & 1) + 2 * (g >> 1) + (m >> 3);
  const unsigned zr = opq(0u);
  const unsigned one = (g == (m >> 2)) ? (opq(0x3F80u) << (16 * (m & 1))) : zr;
  const unsigned ax = (m & 2) ? zr : one, ay = (m & 2) ? one : zr;
  const v4u A1 = {ax, ay, zr, zr}, A2 = {zr, zr, ax, ay};
  const unsigned on2 = opq(0x3F80u) << (16 * (m & 1));
  const int q2 = (m >> 1) & 3;
  const v4u sel = {q2 == 0 ? on2 : zr, q2 == 1 ? on2 : zr, q2 == 2 ? on2 : zr, q2 == 3 ? on2 : zr};
  f32x4 d1, d2, e1;
  asm volatile("s_nop 1\n\t" MF "%0, %1, %3, 0\n\t" MF "%0, %2, %3, %0" : "=&v"(e1) : "v"(v[0]), "v"(v[1]), "v"(sel));
#pragma unroll
  for (int i = 0; i < 16; ++i) {
    if (i == 0) asm volatile("s_nop 1\n\t" MF "%0, %2, %4, 0\n\t" MF "%1, %3, %4, 0" : "=&v"(d1), "=&v"(d2) : "v"(A1), "v"(A2), "v"(v[2]));
    else asm volatile(MF "%0, %2, %4, %0\n\t" MF "%1, %3, %4, %1" : "+v"(d1), "+v"(d2) : "v"(A1), "v"(A2), "v"(v[2 + i]));
  }
  asm volatile("s_nop 7" : "+v"(d1), "+v"(d2), "+v"(e1));
  float o[9];
#pragma unroll
  for (int i = 0; i < 4; ++i) { o[i] = d1[i]; o[4 + i] = d2[i]; }
  {
    auto a02 = __builtin_amdgcn_permlane32_swap(__float_as_uint(e1[0]), __float_as_uint(e1[2]), false, false);
    auto a13 = __builtin_amdgcn_permlane32_swap(__float_as_uint(e1[1]), __float_as_uint(e1[3]), false, false);
    o[8] = m >= 8 ? __uint_as_float(a13[0]) + __uint_as_float(a13[1]) : __uint_as_float(a02[0]) + __uint_as_float(a02[1]);
  }
  float x[9];
  up8(px, x); x[8] = lo2f(px9);
#pragma unroll
  for (int i = 0; i < 9; ++i) x[i] += o[i];
  if (L == 0) {
    float y[9], ln[9];
    up8(py, y); y[8] = lo2f(py9);
    rb9(x);
    ln9(x, ln);
#pragma unroll
    for (int i = 0; i < 9; ++i) x[i] = __builtin_fmaf(ln[i], y[i], x[i]);
    rb9(x);
    st16(X, (unsigned)lane * 16u, sb1 + (P_X3 + (unsigned)(r >> 3)) * PAIR + (unsigned)(r & 7) * 1024u, pk8(x));
    const v4u x9 = gat8(x[8]);
    if ((lane & 7) == 0) st16(X, (unsigned)xp * 16u, sb1 + (P_H + 4u) * PAIR + 4096u + (unsigned)r * 128u, x9);
    ln9(x, ln);
    st16(X, hoff(r, lane), sb1, pk8(ln));
    const v4u h9 = gat8(ln[8]);
    if ((lane & 7) == 0) st16(X, hoff(r, 64 + xp), sb1, h9);
  } else {
    const rsrc_t O = mkr(p.out + tok * DM, DM * 2u);
    __builtin_amdgcn_raw_buffer_store_b128(pk8(x), O, (unsigned)lane * 16u, 0u, OAUX);
    __builtin_amdgcn_raw_buffer_store_b128(gat8(x[8]), O, (lane & 7) == 0 ? 1024u + (unsigned)xp * 16u : 0xF0000000u, 0u, OAUX);
  }
}

__device__ __forceinline__ void row_role(char* smem) {
  TOK_COMMON();
  const int rw = 4 * xq + (i - 20), r = 2 * rw + (w >> 1);
  const unsigned long long sm = die_mask(X, SG0, L, lane);
  const unsigned tm = (unsigned)die_mask(X, TP0, L, lane), dm = (unsigned)die_mask(X, 0u, L, lane);
  __attribute__((address_space(3))) unsigned* cnt = (__attribute__((address_space(3))) unsigned*)smem;
  BAR();
  if (tid < 64) cnt[tid] = 0u;
  BAR();
#define ANCT 25000
  for (int s = w & 1; s < NSEG; s += 2) {
    int ln_ = lane; asm volatile("" : "+v"(ln_));
    const int m = ln_ & 15, g = ln_ >> 4;
    const unsigned l16 = (unsigned)ln_ * 16u, xo = (unsigned)(8 * (4 * (g & 1) + 2 * (g >> 1) + (m >> 3)) + (m & 7)) * 2u;
    const unsigned sb = SEGB(sm, s), sb1 = sb ^ (L ? 0u : 8192u);
    if (s + ARMK < NSEG) {
      const unsigned sv = opq(SENT), sl = SLOT_AT(s + ARMK) + ((dm >> ((s + ARMK) / 3)) & 1u) * 8192u;
      for (int j = r; j < 84; j += 32) {
#pragma unroll
        for (int k = 0; k < 4; ++k) st16(X, l16 + (unsigned)k * 1024u, sl + (unsigned)(j >> 1) * PAIR + (unsigned)(j & 1) * 4096u, (v4u){sv, sv, sv, sv});
      }
    }
    wait_all<PSL>(X, 0u, sb + P_FB * PAIR + 4096u + (unsigned)(L ? rw : r >> 3) * 128u);
    const v4u px = ld16(X, l16, sb + (P_X3 + (unsigned)(r >> 3)) * PAIR + (unsigned)(r & 7) * 1024u);
    const unsigned px9 = __builtin_amdgcn_raw_buffer_load_b16(X, xo, sb + (P_H + 4u) * PAIR + 4096u + (unsigned)r * 128u, 16);
    const rsrc_t YX = mkr(p.yT + (2 * s + (r >> 4)) * 9216, L ? 0u : 18432u);
    const v2u ya = ld8(YX, (unsigned)(ln_ * 256 + ((r ^ m) & 15) * 8), 0u), yb = ld8(YX, (unsigned)(ln_ * 256 + 128 + ((r ^ m) & 15) * 8), 0u);
    const v4u py = {ya.x, ya.y, yb.x, yb.y};
    const unsigned py9 = __builtin_amdgcn_raw_buffer_load_b16(YX, 16384u + (xo >> 3) * 128u + (((unsigned)r ^ (xo >> 4)) & 15u) * 8u + (xo & 6u), 0u, 16);
    wait_all<PSP>(X, 0u, sb + P_FB * PAIR + (unsigned)r * 128u);
    __builtin_amdgcn_s_sleep(RONE);
    wait_all<PSP>(X, (unsigned)(ln_ & 31) * 128u, sb + P_FB * PAIR);
    TT(s, 0);
    const unsigned j = (unsigned)s & 15u, to = XRL + TP0 * XBLK + (j << 20) + ((tm >> (j >> 1)) & 1u) * 8192u + (unsigned)r * 2u * PAIR;
    const unsigned t9 = (TP0 + 8u) * XBLK + ((tm >> 8) & 1u) * 8192u + (j << 17) + (unsigned)(((r >> 2) & 1) * 512 + (4 * (r >> 3) + (r & 3)) * 32);
    const unsigned v9 = (unsigned)(g * (int)PAIR + (m >> 1) * 1024 + (m & 1) * 16);
    v4u v[18];
#pragma unroll
    for (int k = 0; k < 2; ++k) v[k] = ld16(X, v9, t9 + (unsigned)k * 4u * PAIR);
#pragma unroll
    for (int k = 0; k < 16; ++k) v[2 + k] = ld16(X, l16 + (unsigned)(k & 3) * 1024u, to + (unsigned)(k >> 3) * PAIR + (unsigned)(k & 4) * 1024u);
    SB();
    tok_row(p, X, sb1, L, s, r, ln_, v, px, py, px9, py9);
    VM0();
    unsigned o1 = 0u;
    if (ln_ == 0) o1 = __hip_atomic_fetch_add(cnt + s, 1u, __ATOMIC_RELAXED, __HIP_MEMORY_SCOPE_WORKGROUP);
    if (__builtin_amdgcn_readfirstlane(o1) == 1u && ln_ < 2 - L) st4(X, ln_ ? sb1 : sb + 2048u, P_FB * PAIR + 4096u + (unsigned)rw * 128u, opq(1u));
    TT(s, 1);
  }
}

__device__ __forceinline__ void rows_task(const MP& p, const rsrc_t X, unsigned long long sm, u16* ys, int grp, int ng) {
  const int tid = threadIdx.x, lane = tid & 63, w = __builtin_amdgcn_readfirstlane(tid >> 6);
  const int m = lane & 15, g = lane >> 4;
  const int xp = 4 * (g & 1) + 2 * (g >> 1) + (m >> 3), c9 = 512 + 8 * xp + (m & 7);
  const unsigned sb = SEGB(sm, grp >> 2);
  v4u yv[5], xa[4];
  unsigned x9[4];
#pragma unroll
  for (int i = 0; i < 5; ++i) yv[i] = ld16(mkr(p.yT + (grp >> 1) * 9216, 18432u), (unsigned)(tid + 256 * i) * 16u, 0u);
#pragma unroll
  for (int rr = 0; rr < 4; ++rr) {
    const long t = grp * 8 + w + 4 * (rr & (2 * ng - 1));
    xa[rr] = *(const v4u*)&p.xin[t * DM + 8 * lane]; x9[rr] = p.xin[t * DM + c9];
  }
#pragma unroll
  for (int i = 0; i < 5; ++i) ((v4u*)ys)[tid + 256 * i] = yv[i];
  BAR();
  SP(13 - 3 * ng);
  const unsigned o9 = (lane & 7) == 0 ? 0u : 0xF0000000u;
#pragma unroll 1
  for (int k = 0; k < ng; ++k) {
    float x[2][9], y[2][9], ln[2][9];
    const int t0 = (grp + k) * 8 + w;
#pragma unroll
    for (int rr = 0; rr < 2; ++rr) {
      const int j = (t0 + 4 * rr) & 15;
      const v2u ya = *(const v2u*)&ys[lane * 128 + (j ^ m) * 4], yb = *(const v2u*)&ys[lane * 128 + 64 + (j ^ m) * 4];
      up8(xa[rr], x[rr]); x[rr][8] = lo2f(x9[rr]); up8((v4u){ya.x, ya.y, yb.x, yb.y}, y[rr]); y[rr][8] = bf2f(ys[(c9 >> 2) * 64 + (j ^ xp) * 4 + (m & 3)]);
      xa[rr] = xa[2 + rr]; x9[rr] = x9[2 + rr];
    }
    lnR<2>(x, ln);
#pragma unroll
    for (int rr = 0; rr < 2; ++rr) {
#pragma unroll
      for (int i = 0; i < 9; ++i) x[rr][i] = __builtin_fmaf(ln[rr][i], y[rr][i], x[rr][i]);
      rb9(x[rr]);
      const int r = (t0 + 4 * rr) & 31;
      st16(X, (unsigned)lane * 16u, sb + (P_X3 + (unsigned)(r >> 3)) * PAIR + (unsigned)(r & 7) * 1024u, pk8(x[rr]));
      st16(X, (unsigned)xp * 16u + o9, sb + (P_H + 4u) * PAIR + 4096u + (unsigned)r * 128u, gat8(x[rr][8]));
    }
    lnR<2>(x, ln);
#pragma unroll
    for (int rr = 0; rr < 2; ++rr) {
      const int r = (t0 + 4 * rr) & 31;
      st16(X, hoff(r, lane), sb, pk8(ln[rr]));
      st16(X, hoff(r, 64 + xp) + o9, sb, gat8(ln[rr][8]));
    }
  }
  SP(14 - 3 * ng);
  VM0();
  BAR();
  SP(15 - 3 * ng);
  if (tid < ng) st4(X, (unsigned)tid * 128u, sb + P_FB * PAIR + 4096u + (unsigned)(grp & 3) * 128u, opq(1u));
}

__device__ __forceinline__ void arrive(const MP& p, int k, int grp) {
  VM0();
  BAR();
  if (threadIdx.x < 64) {
    unsigned old = 0u;
    if (threadIdx.x == 0) old = __hip_atomic_fetch_add(p.xb + MF_OFF / 4u + (k * 16 + grp) * 32, 1u, __ATOMIC_RELAXED, __HIP_MEMORY_SCOPE_AGENT);
    if (__builtin_amdgcn_readfirstlane(old) == 15u && threadIdx.x < 8) st4(mkr(p.xb, XB_BYTES), threadIdx.x * 128u, RDY_OFF(k) + (unsigned)grp * 1024u, opq(1u));
  }
}
__device__ __forceinline__ void wait_ready(const MP& p, int k) {
  if (threadIdx.x < 64) wait_all<PSL>(mkr(p.xb, XB_BYTES), (threadIdx.x % 9u) * 1024u, RDY_OFF(k) + (blockIdx.x & 7u) * 128u);
  BAR();
}
__device__ __forceinline__ void start_work(char* smem, int wk) {
  const MP p = getp();
  const int w = __builtin_amdgcn_readfirstlane(threadIdx.x >> 6);
#define NARM (ARMK - 3u)
  conv_task(p.kvT, p.ssm_k, p.ssm_D, p.yT, (u16*)(smem + w * 32768), 4 * wk + w, [&]() {
    const unsigned sv = opq(SENT);
    constexpr unsigned S16 = SLOT / 16u;
#pragma unroll
    for (unsigned k = 0; k < (NARM * S16 + 36863u) / 36864u; ++k) {
      const unsigned i = k * 36864u + wk * 256 + threadIdx.x;
      st16(mkr(p.xb, XB_BYTES), i < NARM * S16 ? SLOT_AT(3u + i / S16) + (i % S16) * 16u : 0xF0000000u, 0u, (v4u){sv, sv, sv, sv});
    }
  });
  SP(3);
  arrive(p, 0, wk >> 4);
  SP(4);
}
__device__ __forceinline__ void token_start(char* smem) {
  TOK_COMMON();
  (void)w;
  const int k = 4 * i + xq;
  if (L || (i >= 12 && i < 16)) start_work(smem, L ? 32 + k : 80 + k);
  if (i >= (L ? 12 : 20)) {
    const unsigned long long sm = die_mask(X, SG0, 0, lane);
    int g = L ? 111 - k : k - 96;
    while (g < 120) {
      if (g >= 0 && g < 64) {
        SP(5);
        wait_ready(p, 0);
        SP(6);
      }
      rows_task(p, X, sm, (u16*)smem, g < 0 ? g + 16 : 16 + 2 * g, g < 0 ? 1 : 2);
      g += g < 0 ? 16 : (L ? 48 : 120);
    }
  }
}
__device__ __forceinline__ void token_wg(char* smem) {
  if ((unsigned)(size_t)(__attribute__((address_space(3))) char*)smem != 0u) return;
  token_start(smem);
  const int i = ((int)blockIdx.x - NCHW) >> 3;
  if (i < 12) qkv_role();
  else if (i < 20) ap_role();
  else row_role(smem);
}

__global__ __launch_bounds__(256, 1) void k_main(MP) {
  extern __shared__ __attribute__((aligned(16))) char smem[];
  asm volatile("LKBEG:");
  SP(13);
  if ((int)blockIdx.x < NCHW) chain_wg(smem);
  else token_wg(smem);
}

extern "C" void kernel_launch(void* const* d_in, const int* in_sizes, int n_in, void* d_out, int out_size, void* d_ws,
                              size_t ws_size, hipStream_t stream) {
  const u16* A_q = (const u16*)d_in[0];
  const u16* A_k = (const u16*)d_in[1];
  const u16* A_v = (const u16*)d_in[2];
  const u16* A_shift = (const u16*)d_in[3];
  const u16* A_ssmk = (const u16*)d_in[4];
  const u16* A_ssmD = (const u16*)d_in[5];
  const u16* B_mem0 = (const u16*)d_in[7];
  const u16* B_Wq = (const u16*)d_in[8];
  const u16* B_Wk = (const u16*)d_in[9];
  const u16* B_Wv = (const u16*)d_in[10];
  const u16* B_Wo = (const u16*)d_in[11];

  char* ws = (char*)d_ws;
  size_t off = (size_t)(-(uintptr_t)ws) & (XBLK - 1u);
  auto alloc = [&](size_t bytes) { char* q = ws + off; off += (bytes + 4095) & ~(size_t)4095; return q; };
  unsigned* xb = (unsigned*)alloc((size_t)XB_BYTES);
  u16* kvT = (u16*)alloc((size_t)DM * S_LEN * 2);
  u16* yT = (u16*)alloc((size_t)DM * S_LEN * 2);

  PreP pp;
  pp.kk = A_k; pp.vv = A_v; pp.shift_k = A_shift; pp.ssm_k = A_ssmk; pp.ssm_D = A_ssmD; pp.yT = yT; pp.kvT = kvT;
  pp.Wq = B_Wq; pp.Wk = B_Wk; pp.Wv = B_Wv; pp.Wo = B_Wo; pp.xb = xb; pp.mem0 = B_mem0;
  k_pre<<<240 + PRE_FILL + 256 + 288, 256, 0, stream>>>(pp);
  MP mp;
  mp.out = (u16*)d_out; mp.xb = xb;
  mp.xin = A_q; mp.yT = yT; mp.kvT = kvT; mp.ssm_k = A_ssmk; mp.ssm_D = A_ssmD;
  mp.Wq = B_Wq; mp.Wk = B_Wk; mp.Wv = B_Wv; mp.Wo = B_Wo;
  const int smem_bytes = 160 * 1024;
  (void)hipFuncSetAttribute((const void*)k_main, hipFuncAttributeMaxDynamicSharedMemorySize, smem_bytes);
  k_main<<<NCHW + NTOKW, 256, smem_bytes, stream>>>(mp);
}
```

```cpp
#include <hip/hip_runtime.h>
#include <stdint.h>

typedef unsigned short u16;
using bf16x8 = __attribute__((ext_vector_type(8))) short;
using f32x4 = __attribute__((ext_vector_type(4))) float;
using f32x2 = __attribute__((ext_vector_type(2))) float;
using bfx8 = __attribute__((ext_vector_type(8))) __bf16;
using bfx2 = __attribute__((ext_vector_type(2))) __bf16;
typedef unsigned v4u __attribute__((ext_vector_type(4)));
typedef unsigned v2u __attribute__((ext_vector_type(2)));
typedef short v4s __attribute__((ext_vector_type(4)));

#define S_LEN 2048
#define DM 576
#define NH 32
#define DH 128
#define HD (NH * DH)
#define SEG 32
#define NSEG 64

__device__ __forceinline__ float bf2f(u16 x) { return __uint_as_float(((unsigned)x) << 16); }
__device__ __forceinline__ unsigned pk2(float a, float b) { f32x2 v = {a, b}; bfx2 r = __builtin_convertvector(v, bfx2); return __builtin_bit_cast(unsigned, r); }
__device__ __forceinline__ float lo2f(unsigned u) { return __uint_as_float(u << 16); }
__device__ __forceinline__ float hi2f(unsigned u) { return __uint_as_float(u & 0xFFFF0000u); }

#define NFC 128
#define XBLK 2097152u
#define PAIR 16384u
#define SLOT 688128u
#define AGO 589824u
#define AGT 655360u
#define SG0 22
#define TP0 86
#define NXB 95
#define XB_BYTES (NXB * XBLK)
#define SLOT_AT(i) ((unsigned)((i) / 3) * XBLK + (unsigned)((i) % 3) * SLOT)
#define HT_OFF SLOT_AT(65)
#define CTAB_OFF (HT_OFF + 16384u)
#define MF_OFF (HT_OFF + 20480u)
#define TMO_OFF (MF_OFF + 15u * 128u)
#define RDY_OFF(k) (MF_OFF + 4096u + (k) * 10240u)
#define MF_BYTES 16384u
#define TRC_OFF SLOT_AT(64)
#define CAL_OFF (XBLK - 16384u)
#define P_KV 96u
#define P_H 112u
#define P_X3 117u
#define P_FA 121u
#define P_FB 122u
#define ARMK 8
#define NCAL 12
#define CALT 530u
__device__ __forceinline__ unsigned kv2(const unsigned (&u)[4], unsigned v, v2u w0, v2u w1) {
  const float a = __builtin_fmaf(lo2f(u[3]), lo2f(w0.x), __builtin_fmaf(lo2f(u[2]), hi2f(w0.x), __builtin_fmaf(lo2f(u[1]), lo2f(w0.y), lo2f(u[0]) * hi2f(w0.y))));
  const float b = __builtin_fmaf(hi2f(u[3]), lo2f(w1.x), __builtin_fmaf(hi2f(u[2]), hi2f(w1.x), __builtin_fmaf(hi2f(u[1]), lo2f(w1.y), hi2f(u[0]) * hi2f(w1.y))));
  return pk2(a * lo2f(v), b * hi2f(v));
}
struct PreP {
  const u16* kk; const u16* vv; const u16* shift_k; const u16* ssm_k; const u16* ssm_D; u16* yT; u16* kvT;
  const u16* Wq; const u16* Wk; const u16* Wv; const u16* Wo; unsigned* xb; const u16* mem0;
};
#define PRE_FILL 112
#define PFN 5
__global__ __launch_bounds__(256, 4) void k_pre(PreP p) {
  __shared__ __attribute__((aligned(16))) float sh[64 * 36];
  const int tid = threadIdx.x, b0 = blockIdx.x, b = b0 < 240 ? b0 : (b0 < 528 ? b0 + PRE_FILL + 256 : b0 - 288);
  const __amdgpu_buffer_rsrc_t rx = __builtin_amdgcn_make_buffer_rsrc((void*)p.xb, 0, XB_BYTES, 0x00020000);
  const v4u ones = {0xFFFFFFFFu, 0xFFFFFFFFu, 0xFFFFFFFFu, 0xFFFFFFFFu}, zero = {0, 0, 0, 0};
  if (b < 96) {
    if (tid >= 64) return;
    const unsigned cb = (unsigned)b * XBLK + CAL_OFF;
    __builtin_amdgcn_raw_buffer_store_b128(zero, rx, (unsigned)(tid & 1) * 8192u + (unsigned)(tid >> 1) * 256u, cb, 16);
    asm volatile("s_waitcnt vmcnt(0)" ::: "memory");
    unsigned mn[2] = {~0u, ~0u};
    for (int i = 0; i < NCAL; ++i) {
      const unsigned long long c0 = __builtin_amdgcn_s_memtime();
      asm volatile("s_waitcnt lgkmcnt(0)" ::: "memory");
      const unsigned v = __builtin_amdgcn_raw_buffer_load_b32(rx, 0, cb + (unsigned)(i & 1) * 8192u + (unsigned)(i >> 1) * 256u, 16);
      asm volatile("s_waitcnt vmcnt(0)" :: "v"(v) : "memory");
      const unsigned d = (unsigned)(__builtin_amdgcn_s_memtime() - c0);
      if (i & 1) mn[1] = d < mn[1] ? d : mn[1]; else mn[0] = d < mn[0] ? d : mn[0];
      const unsigned lo = mn[0] < mn[1] ? mn[0] : mn[1], hi = mn[0] < mn[1] ? mn[1] : mn[0];
      if (i >= 3 && lo < CALT && hi > lo + 100u) break;
    }
    unsigned xcc; asm volatile("s_getreg_b32 %0, hwreg(HW_REG_XCC_ID)" : "=s"(xcc));
    const unsigned res = mn[1] < mn[0] ? 1u : 0u;
    if (tid == 0) __builtin_amdgcn_raw_buffer_store_b32(res ^ ((xcc >> 2) & 1u), rx, 0, CTAB_OFF + (unsigned)b * 4u, 16);
  } else if (b < 240) {
    const __amdgpu_buffer_rsrc_t rk = __builtin_amdgcn_make_buffer_rsrc((void*)p.kk, 0, S_LEN * DM * 2, 0x00020000);
    float (*kx)[132] = (float (*)[132])sh;
    float (*cf)[140] = (float (*)[140])(sh + 4 * 132);
    const int d0 = (b - 96) * 4, q = tid & 3, dd = (tid >> 2) & 3, t0 = (tid >> 4) * 8;
    const float Dv = bf2f(p.ssm_D[d0 + dd]);
    {
      const int s = tid >> 1, d = d0 + (tid & 1) * 2;
      unsigned u[4];
#pragma unroll
      for (int a = 0; a < 4; ++a) u[a] = __builtin_amdgcn_raw_buffer_load_b32(rk, (unsigned)((s - 3 + a) * DM + d) * 2u, 0, 0);
      const unsigned vr = *(const unsigned*)&p.vv[s * DM + d], c0 = *(const unsigned*)&p.ssm_k[(long)(d0 + (tid >> 6)) * S_LEN + (tid & 63) * 2];
      const v4u w = *(const v4u*)&p.shift_k[d * 4];
      const unsigned r = kv2(u, vr, (v2u){w.x, w.y}, (v2u){w.z, w.w});
      kx[(tid & 1) * 2][s] = lo2f(r); kx[(tid & 1) * 2 + 1][s] = hi2f(r);
      *(f32x2*)&cf[tid >> 6][8 + (tid & 63) * 2] = (f32x2){lo2f(c0), hi2f(c0)};
      if (tid < 32) cf[tid >> 3][tid & 7] = 0.f;
    }
    __syncthreads();
    float y[8];
#pragma unroll
    for (int i = 0; i < 8; ++i) y[i] = 0.f;
#pragma unroll 1
    for (int j = 8 * q; j <= t0; j += 32) {
      f32x4 kk[2], cw[4];
#pragma unroll
      for (int x = 0; x < 2; ++x) kk[x] = *(const f32x4*)&kx[dd][j + 4 * x];
#pragma unroll
      for (int x = 0; x < 4; ++x) cw[x] = *(const f32x4*)&cf[dd][t0 - j + 4 * x];
#pragma unroll
      for (int jj = 0; jj < 8; ++jj)
#pragma unroll
        for (int i = 0; i < 8; ++i) y[i] = __builtin_fmaf(cw[(8 + i - jj) >> 2][(8 + i - jj) & 3], kk[jj >> 2][jj & 3], y[i]);
    }
#pragma unroll
    for (int i = 0; i < 8; ++i) {
      y[i] += __uint_as_float((unsigned)__builtin_amdgcn_mov_dpp((int)__float_as_uint(y[i]), 0xB1, 0xf, 0xf, true));
      y[i] += __uint_as_float((unsigned)__builtin_amdgcn_mov_dpp((int)__float_as_uint(y[i]), 0x4E, 0xf, 0xf, true));
    }
    const float ya = q == 0 ? y[0] : (q == 1 ? y[2] : (q == 2 ? y[4] : y[6])), yb = q == 0 ? y[1] : (q == 1 ? y[3] : (q == 2 ? y[5] : y[7]));
    const int t = t0 + 2 * q, f = (d0 >> 3) & 15;
    const unsigned r0 = pk2(ya, yb), r1 = pk2(kx[dd][t] * Dv, kx[dd][t + 1] * Dv), o = pk2(lo2f(r0) + lo2f(r1), hi2f(r0) + hi2f(r1));
    u16* dst = p.yT + (t >> 4) * 9216 + (d0 >> 2) * 64 + dd;
    dst[((t & 15) ^ f) << 2] = (u16)(o & 0xFFFFu); dst[(((t + 1) & 15) ^ f) << 2] = (u16)(o >> 16);
  } else if (b < 240 + PRE_FILL) {
    const unsigned i0 = (unsigned)(b - 240) * 256u + tid;
    if (i0 < (SLOT - AGO) / 16u) {
      const unsigned o = AGO + i0 * 16u, s = o < AGT ? (i0 >> 11) * 4u + ((o >> 10) & 3u) : 8u, r = o & 1023u;
      const unsigned gm = s < 8u ? r >> 8 : (r >> 6) & 3u, m3 = (r >> 4) & 3u, ga = s < 8u ? (r >> 6) & 3u : r >> 8, row = 4u * (gm & 1u) + 2u * (gm >> 1) + (m3 & 1u), half = m3 >> 1;
      const unsigned* src = (const unsigned*)(p.mem0 + row * DM + 64u * s + 16u * ga + 2u * half);
      __builtin_amdgcn_raw_buffer_store_b128((v4u){src[0], src[2], src[4], src[6]}, rx, o, 0, 16);
    }
    for (unsigned i = i0; i < 131072u + MF_BYTES / 16u; i += PRE_FILL * 256)
      __builtin_amdgcn_raw_buffer_store_b128(zero, rx, i < 131072u ? (SG0 + (i >> 11)) * XBLK + P_FA * PAIR + (i & 2047u) * 16u : MF_OFF + (i & 2047u) * 16u, 0, 16);
    for (unsigned i = i0; i < 2u * SLOT / 16u; i += PRE_FILL * 256) __builtin_amdgcn_raw_buffer_store_b128(ones, rx, SLOT + i * 16u, 0, 16);
  } else if (b >= 240 + PRE_FILL + 256) {
    const __amdgpu_buffer_rsrc_t rk = __builtin_amdgcn_make_buffer_rsrc((void*)p.kk, 0, S_LEN * DM * 2, 0x00020000);
    u16 (*tile)[72] = (u16 (*)[72])sh;
    const int bid = b - 240 - PRE_FILL - 256, d0 = (bid % 9) * 64, s0 = (bid / 9) * 64;
    const int sr = (tid >> 3) * 2, d = d0 + (tid & 7) * 8;
    v4u kr[5], vr[2], w[4];
#pragma unroll
    for (int a = 0; a < 5; ++a) kr[a] = __builtin_amdgcn_raw_buffer_load_b128(rk, (unsigned)((s0 + sr - 3 + a) * DM + d) * 2u, 0, 0);
#pragma unroll
    for (int a = 0; a < 2; ++a) vr[a] = *(const v4u*)&p.vv[(long)(s0 + sr + a) * DM + d];
#pragma unroll
    for (int a = 0; a < 4; ++a) w[a] = *(const v4u*)&p.shift_k[(d + 2 * a) * 4];
    __builtin_amdgcn_sched_barrier(0);
#pragma unroll
    for (int it = 0; it < 2; ++it) {
      v4u o;
#pragma unroll
      for (int j = 0; j < 4; ++j) { const unsigned u[4] = {kr[it][j], kr[it + 1][j], kr[it + 2][j], kr[it + 3][j]}; o[j] = kv2(u, vr[it][j], (v2u){w[j].x, w[j].y}, (v2u){w[j].z, w[j].w}); }
      *(v4u*)&tile[sr + it][(tid & 7) * 8] = o;
    }
    __syncthreads();
    const int lane = tid & 63, wv = tid >> 6, g = lane >> 4, r = (lane >> 2) & 3, c = lane & 3;
    v2u o[4];
#pragma unroll
    for (int i = 0; i < 4; ++i) o[i] = __builtin_bit_cast(v2u, __builtin_amdgcn_ds_read_tr16_b64_v4i16((__attribute__((address_space(3))) v4s*)&tile[16 * g + 4 * i + r][16 * wv + 4 * c]));
    u16* dst = p.kvT + (long)(d0 + 16 * wv + (lane & 15)) * S_LEN + s0 + 16 * g;
    *(v4u*)dst = (v4u){o[0].x, o[0].y, o[1].x, o[1].y};
    *(v4u*)(dst + 8) = (v4u){o[2].x, o[2].y, o[3].x, o[3].y};
  } else {
    const int x = b & 7, k = (b - 240 - PRE_FILL) >> 3;
    unsigned long long pc; unsigned l0, l1, l2, l3; int rel;
    asm volatile("s_getpc_b64 %0\nLPRE:\n\ts_mov_b32 %1, LKBEG-LPRE\n\ts_mov_b32 %2, LCHEND-LKBEG\n\ts_mov_b32 %3, LQVEND-LKBEG\n\ts_mov_b32 %4, LQKEND-LKBEG\n\ts_mov_b32 %5, LAPEND-LKBEG"
                 : "=s"(pc), "=s"(rel), "=s"(l0), "=s"(l1), "=s"(l2), "=s"(l3));
    l0 = l0 > l1 ? l0 : l1; l2 = l2 > l3 ? l2 : l3; l0 = l0 > l2 ? l0 : l2;
    const unsigned o = (unsigned)(k * 64 + (tid & 63)) * 64u;
    unsigned a[PFN + 2];
    a[PFN] = *(const __attribute__((address_space(1))) unsigned*)(pc + rel + (o < l0 ? o : 0u));
    a[PFN + 1] = __builtin_amdgcn_raw_buffer_load_b32(rx, k == 31 ? tid * XBLK + CAL_OFF + 8064u : 0xF0000000u, 0, 16);
#pragma unroll
    for (int i = 0; i < PFN; ++i) {
      const int q = (i * 32 + k) * 256 + tid, r2 = q >> 5, gr = r2 >> 5, row = (gr / 3) * 64 + (x >> 2) * 32 + (r2 & 31), off = (q & 31) * 32, ty = gr % 3;
      a[i] = *(const unsigned*)(gr < 27 ? (ty == 0 ? p.Wq : (ty == 1 ? p.Wk : p.Wv)) + (long)row * HD + (x & 3) * 1024 + off
                                        : p.Wo + ((long)(x & 3) * DM + (gr < 36 ? gr - 27 : 0) * 64 + (x >> 2) * 32 + (r2 & 31)) * 1024 + off);
    }
#pragma unroll
    for (int i = 0; i < PFN + 2; ++i) asm volatile("" :: "v"(a[i]));
  }
}

#define CREV_STRIDE 2208
#define LWAIT() asm volatile("s_waitcnt lgkmcnt(0)" ::: "memory")
template <class F>
__device__ __forceinline__ void conv_task(const u16* __restrict__ kvT, const u16* __restrict__ ssm_k, const u16* __restrict__ ssm_D, u16* yT, u16* shb, int d, F fill) {
  const int lane = threadIdx.x & 63;
  u16* crev = shb + 64;
  u16* kvs = crev + 4 * CREV_STRIDE;
  const int X0 = 2048 + 63;
  v4u cv[4], kq[4];
#pragma unroll
  for (int j = 0; j < 4; ++j) { cv[j] = *(const v4u*)&ssm_k[(long)d * S_LEN + lane * 32 + 8 * j]; kq[j] = *(const v4u*)&kvT[(long)d * S_LEN + (j * 64 + lane) * 8]; }
  const float Dv = bf2f(ssm_D[d]);
  const v4u z4 = {0u, 0u, 0u, 0u};
  *(v4u*)&kvs[(lane >> 2) * 40 + (lane & 3) * 8] = z4;
  if (lane < 32) *(v4u*)&crev[(lane >> 3) * CREV_STRIDE + (lane & 7) * 8] = z4;
  if (lane < 36) *(v4u*)&crev[(lane / 9) * CREV_STRIDE + 2112 + (lane % 9) * 8] = z4;
  {
    unsigned r[18];
#pragma unroll
    for (int k = 0; k < 16; ++k) { const unsigned u = cv[3 - (k >> 2)][3 - (k & 3)]; r[k] = __builtin_amdgcn_alignbyte(u, u, 2); }
#pragma unroll
    for (int k = 0; k < 2; ++k) r[16 + k] = (unsigned)__builtin_amdgcn_update_dpp(0, (int)r[k], 0x138, 0xf, 0xf, true);
    LWAIT();
#pragma unroll
    for (int rho = 0; rho < 4; ++rho)
#pragma unroll
      for (int j = 0; j < 4; ++j) {
        v4u o;
#pragma unroll
        for (int k = 0; k < 4; ++k) { const int q = 4 * j + k + (rho >> 1); o[k] = (rho & 1) ? __builtin_amdgcn_alignbyte(r[q + 1], r[q], 2) : r[q]; }
        *(v4u*)&crev[rho * CREV_STRIDE + 2080 - 32 * lane + 8 * j] = o;
      }
    if (lane == 63) {
      *(v2u*)&crev[1 * CREV_STRIDE + 60] = (v2u){0u, r[0] << 16};
      *(v2u*)&crev[2 * CREV_STRIDE + 60] = (v2u){0u, r[0]};
      *(v2u*)&crev[3 * CREV_STRIDE + 60] = (v2u){r[0] << 16, __builtin_amdgcn_alignbyte(r[1], r[0], 2)};
    }
#pragma unroll
    for (int j = 0; j < 4; ++j) *(v4u*)&kvs[(16 + j * 16 + (lane >> 2)) * 40 + (lane & 3) * 8] = kq[j];
  }
  fill();
  LWAIT();
  const int n = lane & 15, g = lane >> 4;
  const int rho = (X0 - n) & 3;
  f32x4 acc[4][2];
#pragma unroll
  for (int q = 0; q < 8; ++q) acc[q >> 1][q & 1] = (f32x4){0.f, 0.f, 0.f, 0.f};
  {
    typedef __attribute__((address_space(3))) const v2u* l2p;
    typedef __attribute__((address_space(3))) const bf16x8* l4p;
    unsigned a0 = (unsigned)(size_t)(__attribute__((address_space(3))) u16*)&crev[rho * CREV_STRIDE + (X0 - (n - 8 * g) - rho)] - 3232u, a1 = a0 + 8u;
    unsigned b0 = (unsigned)(size_t)(__attribute__((address_space(3))) u16*)&kvs[(14 + n) * 40 + 8 * g];
    asm volatile("" : "+v"(a1));
#define AFR(m, f) ({ const unsigned o_ = 3232u - 64u * (m) - 1024u * ((f) >> 1) - 32u * ((f) & 1); const v2u lo_ = *(l2p)(size_t)(a0 + o_), hi_ = *(l2p)(size_t)(a1 + o_); \
                     __builtin_bit_cast(bf16x8, (v4u){lo_.x, lo_.y, hi_.x, hi_.y}); })
#define BFR(m, j) (*(l4p)(size_t)(b0 + 80u * (2 - (m)) + 1280u * (j)))
    bf16x8 af[2][8], bf[2][4];
#pragma unroll
    for (int f = 0; f < 8; ++f) { af[0][f] = AFR(0, f); af[1][f] = af[0][f]; __builtin_amdgcn_sched_barrier(0); }
#pragma unroll
    for (int j = 0; j < 4; ++j) { bf[0][j] = BFR(0, j); bf[1][j] = bf[0][j]; }
#pragma unroll 1
    for (int mo = 0; mo < 8; ++mo) {
#pragma unroll
      for (int m = 0; m < 2; ++m) {
        int k = 0;
#pragma unroll
        for (int a = 0; a < 4; ++a)
#pragma unroll
          for (int ct = a; ct < 4; ++ct)
#pragma unroll
            for (int ti = 0; ti < 2; ++ti) {
              acc[ct][ti] = __builtin_amdgcn_mfma_f32_16x16x32_bf16(__builtin_bit_cast(bfx8, af[m][2 * a + ti]), __builtin_bit_cast(bfx8, bf[m][ct - a]), acc[ct][ti], 0, 0, 0);
              if (k < 2) af[m ^ 1][k] = AFR(m + 1, k);
              else if (k < 6) bf[m ^ 1][k - 2] = BFR(m + 1, k - 2);
              else if (k < 12) af[m ^ 1][k - 4] = AFR(m + 1, k - 4);
              __builtin_amdgcn_sched_barrier(0);
              ++k;
            }
      }
      a0 -= 128u; a1 -= 128u; b0 -= 160u;
    }
  }
  u16* yo = crev;
  LWAIT();
#pragma unroll
  for (int q = 0; q < 8; ++q) {
    const int o = (16 * (q >> 1) + n) * 40 + 16 * (q & 1) + 4 * g;
    const v2u kx = *(const v2u*)&kvs[16 * 40 + o];
    const f32x4 y = acc[q >> 1][q & 1];
    const unsigned y0 = pk2(y[0], y[1]), y1 = pk2(y[2], y[3]), r0 = pk2(lo2f(kx.x) * Dv, hi2f(kx.x) * Dv), r1 = pk2(lo2f(kx.y) * Dv, hi2f(kx.y) * Dv);
    *(v2u*)&yo[o] = (v2u){pk2(lo2f(y0) + lo2f(r0), hi2f(y0) + hi2f(r0)), pk2(lo2f(y1) + lo2f(r1), hi2f(y1) + hi2f(r1))};
  }
  LWAIT();
  __builtin_amdgcn_s_barrier();
  asm volatile("" ::: "memory");
  {
    const int tid = threadIdx.x, f = (d >> 3) & 15;
    const u16* y0 = shb - (tid >> 6) * 16384 + 64;
#pragma unroll
    for (int k = 0; k < 4; ++k) {
      const int tile = 32 * k + (tid >> 3), t = 16 * tile + 2 * ((tid & 7) ^ (f >> 1));
      unsigned u[4];
#pragma unroll
      for (int c = 0; c < 4; ++c) u[c] = *(const unsigned*)&y0[c * 16384 + (t >> 5) * 40 + (t & 31)];
      const v2u e = {__builtin_amdgcn_perm(u[1], u[0], 0x05040100u), __builtin_amdgcn_perm(u[3], u[2], 0x05040100u)}, o = {__builtin_amdgcn_perm(u[1], u[0], 0x07060302u), __builtin_amdgcn_perm(u[3], u[2], 0x07060302u)};
      if (tile >= NFC / 16) __builtin_amdgcn_raw_buffer_store_b128((f & 1) ? (v4u){o.x, o.y, e.x, e.y} : (v4u){e.x, e.y, o.x, o.y}, __builtin_amdgcn_make_buffer_rsrc((void*)yT, 0, DM * S_LEN * 2, 0x00020000),
                                                                   tile * 18432 + (tid & 7) * 16, (d >> 2) * 128, 16);
    }
  }
}

#define NCHW 64
#define NTOKW 192
#define PSL 4
#define SENT 0xFFFFFFFFu
#define SPIN_CAP 300000u
struct MP {
  const u16* Wq; const u16* Wk; const u16* Wv; const u16* Wo; u16* out; unsigned* xb;
  const u16* xin; u16* yT; u16* kvT; const u16* ssm_k; const u16* ssm_D;
};

__device__ __forceinline__ MP getp() {
  MP r;
#if defined(__HIP_DEVICE_COMPILE__)
  const __attribute__((address_space(4))) unsigned long long* k = (const __attribute__((address_space(4))) unsigned long long*)__builtin_amdgcn_kernarg_segment_ptr();
  asm volatile("" : "+s"(k));
#define GP(T, i) ((T*)(__attribute__((address_space(1))) T*)k[i])
  r.Wq = GP(const u16, 0);
  r.Wk = GP(const u16, 1);
  r.Wv = GP(const u16, 2);
  r.Wo = GP(const u16, 3);
  r.out = GP(u16, 4);
  r.xb = GP(unsigned, 5);
  r.xin = GP(const u16, 6);
  r.yT = GP(u16, 7);
  r.kvT = GP(u16, 8);
  r.ssm_k = GP(const u16, 9);
  r.ssm_D = GP(const u16, 10);
#endif
  return r;
}
typedef __amdgpu_buffer_rsrc_t rsrc_t;
__device__ __forceinline__ rsrc_t mkr(const void* p, unsigned bytes) { return __builtin_amdgcn_make_buffer_rsrc((void*)p, 0, bytes, 0x00020000); }
__device__ __forceinline__ void st16(rsrc_t r, unsigned vo, unsigned so, v4u v) { __builtin_amdgcn_raw_buffer_store_b128(v, r, vo, so, 16); }
__device__ __forceinline__ void st4(rsrc_t r, unsigned vo, unsigned so, unsigned v) { __builtin_amdgcn_raw_buffer_store_b32(v, r, vo, so, 16); }
__device__ __forceinline__ v4u ld16(rsrc_t r, unsigned vo, unsigned so) { return __builtin_amdgcn_raw_buffer_load_b128(r, vo, so, 16); }
__device__ __forceinline__ v2u ld8(rsrc_t r, unsigned vo, unsigned so) { return __builtin_amdgcn_raw_buffer_load_b64(r, vo, so, 16); }
__device__ __forceinline__ unsigned ld4(rsrc_t r, unsigned vo, unsigned so) { return __builtin_amdgcn_raw_buffer_load_b32(r, vo, so, 16); }
__device__ __forceinline__ unsigned opq(unsigned x) { asm volatile("" : "+v"(x)); return x; }

__device__ __forceinline__ unsigned long long die_mask(rsrc_t X, unsigned first, int L, int lane) { return __ballot((ld4(X, (unsigned)lane * 4u, CTAB_OFF + first * 4u) & 1u) != (unsigned)L); }
#define SEGB(m, s) ((unsigned)(SG0 + (s)) * XBLK + (unsigned)(((m) >> (s)) & 1ull) * 8192u)
__device__ __forceinline__ bool spin_fail(unsigned& spins, rsrc_t F) {
  ++spins;
  if (spins > SPIN_CAP) { unsigned one = 0x40000000u | (blockIdx.x << 8) | threadIdx.x; asm volatile("" : "+v"(one)); if (__builtin_amdgcn_readfirstlane(ld4(F, 0, TMO_OFF)) == 0u) st4(F, 0, TMO_OFF, one); return true; }
  if ((spins & 1023u) == 0u) return __builtin_amdgcn_readfirstlane(ld4(F, 0, TMO_OFF)) != 0;
  return false;
}
__device__ __forceinline__ unsigned rtc() { return (unsigned)__builtin_amdgcn_s_memrealtime(); }
#define BAR() do { asm volatile("s_waitcnt lgkmcnt(0)" ::: "memory"); __builtin_amdgcn_s_barrier(); asm volatile("" ::: "memory"); } while (0)
#define SB() __builtin_amdgcn_sched_barrier(0)
#define MF "v_mfma_f32_16x16x32_bf16 "
#define VM0() __builtin_amdgcn_s_waitcnt(0x0F70)
#define DL1 12
#define SCH_B0 48
#define DLL1 6
#define DLL2 0
#define DLF 24
#define SCH_PEN 0
#define DLX 4
#define LATE 2
#define TKL 28
#define UP 1
#define DN 100
#define DL3 16
#define FIN -3
#define CH_FIN CH_S(FIN)
#define DLT 16
#define DLY 20
#define CH_DLT CH_S(DLT)
#define CH_DLY CH_S(DLY)
#define ES 6
#define ED 4
#define EB 8
#define CH_ES CH_S(ES)
#define CH_EX "%[ex]"
#define DRYL 1
#define CH_DRYL CH_S(DRYL)
#define SMAP 0x1023u
#define RO1 4
#define CH_RO1 CH_S(RO1)
#define RSL 2
#define CH_S2(x) #x
#define CH_S(x) CH_S2(x)
#define CH_TOKCHK "s_cbranch_vccz LG3_%=\n\t"
#define CH_SG0 CH_S(SG0)
#define CH_ARMD "%[armd]"
#define CH_ARMS "%[arms]"
#define CH_SLOT "688128"
#define CH_SKIP "32768"
#define CH_DLL1 CH_S(DLL1)
#define CH_DLL2 CH_S(DLL2)
#define CH_DLF CH_S(DLF)
#define CH_B0 CH_S(SCH_B0)
#define CH_PEN CH_S(SCH_PEN)
#define CH_RSL CH_S(RSL)
#define CH_DL3 CH_S(DL3)
#define CH_UP CH_S(UP)
#define CH_DN CH_S(DN)
#define CH_DLX CH_S(DLX)
#define CH_LATE CH_S(LATE)
#define CH_DL1L "%[dl1l]"
#define CH_TKL CH_S(TKL)
#define CH_SCHW "656384"
#define CH_CAP "%[cap]"
#define CH_CTAB "%[ctab]"
#define FDL 6
#define CH_FDL CH_S(FDL)
#define CH_ASM \
  "s_mov_b32 s84, " CH_CAP "\n\t" \
  "s_mov_b32 s83, " CH_CAP "\n\t" \
  "s_mov_b32 s68, 0\n\t" \
  "s_mov_b32 s74, 0\n\t" \
  "s_mov_b32 s76, 0\n\t" \
  "s_mov_b32 s77, 0\n\t" \
  "s_mov_b32 s78, 0\n\t" \
  "s_mov_b32 s79, 0\n\t" \
  "s_mov_b32 s56, 0xcccccccc\n\t" \
  "s_mov_b32 s57, 0xcccccccc\n\t" \
  "s_mov_b32 s58, 0x33333333\n\t" \
  "s_mov_b32 s59, 0x33333333\n\t" \
  "s_mov_b32 s24, 0x01010101\n\t" \
  "s_mov_b32 s25, 0\n\t" \
  "s_mov_b32 s88, 0x3f803f80\n\t" \
  "s_mov_b32 s89, 0x3f803f80\n\t" \
  "s_mov_b32 s66, s65\n\t" \
  "s_mov_b32 s47, s65\n\t" \
  "s_mov_b32 s72, 0\n\t" \
  "s_mov_b32 s73, 0\n\t" \
  "s_mov_b32 s67, 0\n\t" \
  "s_mul_i32 s60, s40, 36864\n\t" \
  "s_add_u32 s60, s60, 16384\n\t" \
  "v_add_u32 v70, s60, v69\n\t" \
  "v_bfe_i32 v68, v69, 5, 1\n\t" \
  "v_and_b32 v68, 0xaaaaaaaa, v68\n\t" \
  "v_or_b32 v68, 0x44444444, v68\n\t" \
  "v_and_b32 v73, 32, v69\n\t" \
  "v_lshl_add_u32 v73, v73, 5, v69\n\t" \
  "LLOAD_%=:\n\t" \
  "v_lshrrev_b32 v74, 2, v69\n\t" \
  "s_mov_b32 s60, " CH_CTAB "\n\t" \
  "buffer_load_dword v72, v74, s[28:31], s60 offen offset:88 sc1\n\t" \
  "buffer_load_dword v74, v74, s[28:31], s60 offen sc1\n\t" \
  "v_lshrrev_b32 v71, 8, v69\n\t" \
  "v_lshlrev_b32 v1, 6, v71\n\t" \
  "v_and_b32 v2, 0xf0, v69\n\t" \
  "v_xor_b32 v1, v1, v2\n\t" \
  "v_lshl_add_u32 v71, v71, 13, v1\n\t" \
  "s_lshl_b32 s60, s40, 17\n\t" \
  "v_add_u32 v71, s60, v71\n\t" \
  "v_lshrrev_b32 v1, 7, v69\n\t" \
  "v_and_b32 v2, 0x70, v69\n\t" \
  "s_movk_i32 s60, 1152\n\t" \
  "v_mad_u32_u24 v75, v1, s60, v2\n\t" \
  "s_lshl_b32 s54, s40, 12\n\t" \
  "s_lshl_b32 s55, s40, 7\n\t" \
  "s_mul_i32 s75, s40, 36864\n\t" \
  "s_add_u32 s75, s75, 1024\n\t" \
  "s_mov_b32 s62, 0x05040100\n\t" \
  "s_mov_b32 s63, 0x07060302\n\t" \
  "s_barrier\n\t" \
  "s_xor_b32 s61, s40, 0\n\t" \
  "s_mul_i32 s61, s61, 36864\n\t" \
  "s_add_u32 s61, s61, s55\n\t" \
  "s_add_u32 m0, s54, 0\n\t" \
  "s_add_u32 s60, s61, 0\n\t" \
  "buffer_load_dwordx4 v75, %[Wo], s60 offen lds\n\t" \
  "s_add_u32 m0, s54, 1024\n\t" \
  "s_add_u32 s60, s61, 9216\n\t" \
  "buffer_load_dwordx4 v75, %[Wo], s60 offen lds\n\t" \
  "s_add_u32 m0, s54, 2048\n\t" \
  "s_add_u32 s60, s61, 18432\n\t" \
  "buffer_load_dwordx4 v75, %[Wo], s60 offen lds\n\t" \
  "s_add_u32 m0, s54, 3072\n\t" \
  "s_add_u32 s60, s61, 27648\n\t" \
  "buffer_load_dwordx4 v75, %[Wo], s60 offen lds\n\t" \
  "s_add_u32 m0, s54, 16384\n\t" \
  "s_mov_b32 s60, 0\n\t" \
  "buffer_load_dwordx4 v71, %[Wq], s60 offen lds\n\t" \
  "s_add_u32 m0, s54, 17408\n\t" \
  "s_mov_b32 s60, 32768\n\t" \
  "buffer_load_dwordx4 v71, %[Wq], s60 offen lds\n\t" \
  "s_add_u32 m0, s54, 18432\n\t" \
  "s_mov_b32 s60, 65536\n\t" \
  "buffer_load_dwordx4 v71, %[Wq], s60 offen lds\n\t" \
  "s_add_u32 m0, s54, 19456\n\t" \
  "s_mov_b32 s60, 98304\n\t" \
  "buffer_load_dwordx4 v71, %[Wq], s60 offen lds\n\t" \
  "s_add_u32 m0, s54, 53248\n\t" \
  "s_mov_b32 s60, 0\n\t" \
  "buffer_load_dwordx4 v71, %[Wk], s60 offen lds\n\t" \
  "s_add_u32 m0, s54, 54272\n\t" \
  "s_mov_b32 s60, 32768\n\t" \
  "buffer_load_dwordx4 v71, %[Wk], s60 offen lds\n\t" \
  "s_add_u32 m0, s54, 55296\n\t" \
  "s_mov_b32 s60, 65536\n\t" \
  "buffer_load_dwordx4 v71, %[Wk], s60 offen lds\n\t" \
  "s_add_u32 m0, s54, 56320\n\t" \
  "s_mov_b32 s60, 98304\n\t" \
  "buffer_load_dwordx4 v71, %[Wk], s60 offen lds\n\t" \
  "s_xor_b32 s61, s40, 0\n\t" \
  "s_mul_i32 s61, s61, 36864\n\t" \
  "s_add_u32 s61, s61, s55\n\t" \
  "s_add_u32 m0, s54, 90112\n\t" \
  "s_add_u32 s60, s61, 512\n\t" \
  "buffer_load_dwordx4 v75, %[Wo], s60 offen lds\n\t" \
  "s_add_u32 m0, s54, 91136\n\t" \
  "s_add_u32 s60, s61, 9728\n\t" \
  "buffer_load_dwordx4 v75, %[Wo], s60 offen lds\n\t" \
  "s_add_u32 m0, s54, 92160\n\t" \
  "s_add_u32 s60, s61, 18944\n\t" \
  "buffer_load_dwordx4 v75, %[Wo], s60 offen lds\n\t" \
  "s_add_u32 m0, s54, 93184\n\t" \
  "s_add_u32 s60, s61, 28160\n\t" \
  "buffer_load_dwordx4 v75, %[Wo], s60 offen lds\n\t" \
  "s_add_u32 m0, s54, 126976\n\t" \
  "s_mov_b32 s60, 0\n\t" \
  "buffer_load_dwordx4 v71, %[Wv], s60 offen lds\n\t" \
  "s_add_u32 m0, s54, 128000\n\t" \
  "s_mov_b32 s60, 32768\n\t" \
  "buffer_load_dwordx4 v71, %[Wv], s60 offen lds\n\t" \
  "s_add_u32 m0, s54, 129024\n\t" \
  "s_mov_b32 s60, 65536\n\t" \
  "buffer_load_dwordx4 v71, %[Wv], s60 offen lds\n\t" \
  "s_add_u32 m0, s54, 130048\n\t" \
  "s_mov_b32 s60, 98304\n\t" \
  "buffer_load_dwordx4 v71, %[Wv], s60 offen lds\n\t" \
  "v_and_b32 v1, 0x300, v69\n\t" \
  "v_lshlrev_b32 v65, 3, v1\n\t" \
  "v_and_b32 v1, 0xc0, v69\n\t" \
  "v_lshl_add_u32 v65, v1, 2, v65\n\t" \
  "s_lshl_b32 s60, s40, 6\n\t" \
  "v_xor_b32 v1, s60, v1\n\t" \
  "v_and_b32 v2, 48, v69\n\t" \
  "v_add3_u32 v65, v65, v1, v2\n\t" \
  "v_lshrrev_b32 v2, 1, v2\n\t" \
  "v_sub_u32 v66, v65, v2\n\t" \
  "v_bfe_u32 v0, v69, 8, 1\n\t" \
  "v_bfe_u32 v1, v69, 9, 1\n\t" \
  "v_lshlrev_b32 v0, 2, v0\n\t" \
  "v_add_u32 v0, v0, v1\n\t" \
  "v_lshlrev_b32 v0, 9, v0\n\t" \
  "v_lshrrev_b32 v1, 1, v69\n\t" \
  "v_and_b32 v1, 0x60, v1\n\t" \
  "v_and_b32 v2, 16, v69\n\t" \
  "v_bfe_u32 v3, v69, 5, 1\n\t" \
  "v_add3_u32 v64, v0, v1, v2\n\t" \
  "v_lshl_add_u32 v64, v3, 2, v64\n\t" \
  "v_add_u32 v64, s54, v64\n\t" \
  "v_lshl_add_u32 v67, v3, 3, v0\n\t" \
  "v_bfe_u32 v1, v69, 7, 1\n\t" \
  "v_lshl_add_u32 v67, v1, 2, v67\n\t" \
  "v_bfe_u32 v1, v69, 6, 1\n\t" \
  "v_lshl_add_u32 v67, v1, 4, v67\n\t" \
  "v_bfe_u32 v1, v69, 4, 1\n\t" \
  "v_lshl_add_u32 v67, v1, 1, v67\n\t" \
  "s_lshl_b32 s60, s40, 5\n\t" \
  "v_add_u32 v67, s60, v67\n\t" \
  "v_add_u32 v78, 0, v64\n\t" \
  "v_add_u32 v79, 1024, v64\n\t" \
  "s_waitcnt vmcnt(16) lgkmcnt(0)\n\t" \
  "s_barrier\n\t" \
  "ds_read2_b32 v[80:81], v78 offset0:0 offset1:2\n\t" \
  "ds_read2_b32 v[82:83], v78 offset0:32 offset1:34\n\t" \
  "ds_read2_b32 v[84:85], v78 offset0:64 offset1:66\n\t" \
  "ds_read2_b32 v[86:87], v78 offset0:96 offset1:98\n\t" \
  "ds_read2_b32 v[88:89], v79 offset0:0 offset1:2\n\t" \
  "ds_read2_b32 v[90:91], v79 offset0:32 offset1:34\n\t" \
  "ds_read2_b32 v[92:93], v79 offset0:64 offset1:66\n\t" \
  "ds_read2_b32 v[94:95], v79 offset0:96 offset1:98\n\t" \
  "s_waitcnt lgkmcnt(6)\n\t" \
  "v_perm_b32 v112, v82, v80, s62\n\t" \
  "v_perm_b32 v113, v82, v80, s63\n\t" \
  "s_waitcnt lgkmcnt(4)\n\t" \
  "v_perm_b32 v114, v86, v84, s62\n\t" \
  "v_perm_b32 v115, v86, v84, s63\n\t" \
  "s_waitcnt lgkmcnt(2)\n\t" \
  "v_perm_b32 v116, v90, v88, s62\n\t" \
  "v_perm_b32 v117, v90, v88, s63\n\t" \
  "s_waitcnt lgkmcnt(0)\n\t" \
  "v_perm_b32 v118, v94, v92, s62\n\t" \
  "v_perm_b32 v119, v94, v92, s63\n\t" \
  "ds_write_b128 v70, v[112:115] offset:20480\n\t" \
  "ds_write_b128 v70, v[116:119] offset:21504\n\t" \
  "v_perm_b32 v120, v83, v81, s62\n\t" \
  "v_perm_b32 v121, v83, v81, s63\n\t" \
  "v_perm_b32 v122, v87, v85, s62\n\t" \
  "v_perm_b32 v123, v87, v85, s63\n\t" \
  "v_perm_b32 v124, v91, v89, s62\n\t" \
  "v_perm_b32 v125, v91, v89, s63\n\t" \
  "v_perm_b32 v126, v95, v93, s62\n\t" \
  "v_perm_b32 v127, v95, v93, s63\n\t" \
  "ds_write_b128 v70, v[120:123] offset:16384\n\t" \
  "ds_write_b128 v70, v[124:127] offset:17408\n\t" \
  "v_add_u32 v78, 16384, v65\n\t" \
  "s_waitcnt vmcnt(12) lgkmcnt(0)\n\t" \
  "s_barrier\n\t" \
  "ds_read_b64_tr_b16 v[0:1], v78 offset:0\n\t" \
  "ds_read_b64_tr_b16 v[2:3], v78 offset:1024\n\t" \
  "ds_read_b64_tr_b16 v[4:5], v78 offset:8192\n\t" \
  "ds_read_b64_tr_b16 v[6:7], v78 offset:9216\n\t" \
  "ds_read_b64_tr_b16 v[8:9], v78 offset:8\n\t" \
  "ds_read_b64_tr_b16 v[10:11], v78 offset:1032\n\t" \
  "ds_read_b64_tr_b16 v[12:13], v78 offset:8200\n\t" \
  "ds_read_b64_tr_b16 v[14:15], v78 offset:9224\n\t" \
  "s_add_u32 m0, s54, 0\n\t" \
  "s_mov_b32 s60, 524288\n\t" \
  "buffer_load_dwordx4 v71, %[Wq], s60 offen lds\n\t" \
  "s_add_u32 m0, s54, 1024\n\t" \
  "s_mov_b32 s60, 557056\n\t" \
  "buffer_load_dwordx4 v71, %[Wq], s60 offen lds\n\t" \
  "s_add_u32 m0, s54, 2048\n\t" \
  "s_mov_b32 s60, 589824\n\t" \
  "buffer_load_dwordx4 v71, %[Wq], s60 offen lds\n\t" \
  "s_add_u32 m0, s54, 3072\n\t" \
  "s_mov_b32 s60, 622592\n\t" \
  "buffer_load_dwordx4 v71, %[Wq], s60 offen lds\n\t" \
  "v_add_u32 v78, 53248, v65\n\t" \
  "s_waitcnt vmcnt(12) lgkmcnt(0)\n\t" \
  "s_barrier\n\t" \
  "ds_read_b64_tr_b16 a[0:1], v78 offset:0\n\t" \
  "ds_read_b64_tr_b16 a[2:3], v78 offset:1024\n\t" \
  "ds_read_b64_tr_b16 a[4:5], v78 offset:8192\n\t" \
  "ds_read_b64_tr_b16 a[6:7], v78 offset:9216\n\t" \
  "ds_read_b64_tr_b16 a[8:9], v78 offset:8\n\t" \
  "ds_read_b64_tr_b16 a[10:11], v78 offset:1032\n\t" \
  "ds_read_b64_tr_b16 a[12:13], v78 offset:8200\n\t" \
  "ds_read_b64_tr_b16 a[14:15], v78 offset:9224\n\t" \
  "s_xor_b32 s61, s40, 1\n\t" \
  "s_mul_i32 s61, s61, 36864\n\t" \
  "s_add_u32 s61, s61, s55\n\t" \
  "s_add_u32 m0, s54, 16384\n\t" \
  "s_add_u32 s60, s61, 0\n\t" \
  "buffer_load_dwordx4 v75, %[Wo], s60 offen lds\n\t" \
  "s_add_u32 m0, s54, 17408\n\t" \
  "s_add_u32 s60, s61, 9216\n\t" \
  "buffer_load_dwordx4 v75, %[Wo], s60 offen lds\n\t" \
  "s_add_u32 m0, s54, 18432\n\t" \
  "s_add_u32 s60, s61, 18432\n\t" \
  "buffer_load_dwordx4 v75, %[Wo], s60 offen lds\n\t" \
  "s_add_u32 m0, s54, 19456\n\t" \
  "s_add_u32 s60, s61, 27648\n\t" \
  "buffer_load_dwordx4 v75, %[Wo], s60 offen lds\n\t" \
  "v_add_u32 v78, 90112, v64\n\t" \
  "v_add_u32 v79, 91136, v64\n\t" \
  "s_waitcnt vmcnt(12) lgkmcnt(0)\n\t" \
  "s_barrier\n\t" \
  "ds_read2_b32 v[80:81], v78 offset0:0 offset1:2\n\t" \
  "ds_read2_b32 v[82:83], v78 offset0:32 offset1:34\n\t" \
  "ds_read2_b32 v[84:85], v78 offset0:64 offset1:66\n\t" \
  "ds_read2_b32 v[86:87], v78 offset0:96 offset1:98\n\t" \
  "ds_read2_b32 v[88:89], v79 offset0:0 offset1:2\n\t" \
  "ds_read2_b32 v[90:91], v79 offset0:32 offset1:34\n\t" \
  "ds_read2_b32 v[92:93], v79 offset0:64 offset1:66\n\t" \
  "ds_read2_b32 v[94:95], v79 offset0:96 offset1:98\n\t" \
  "s_add_u32 m0, s54, 53248\n\t" \
  "s_mov_b32 s60, 524288\n\t" \
  "buffer_load_dwordx4 v71, %[Wk], s60 offen lds\n\t" \
  "s_add_u32 m0, s54, 54272\n\t" \
  "s_mov_b32 s60, 557056\n\t" \
  "buffer_load_dwordx4 v71, %[Wk], s60 offen lds\n\t" \
  "s_add_u32 m0, s54, 55296\n\t" \
  "s_mov_b32 s60, 589824\n\t" \
  "buffer_load_dwordx4 v71, %[Wk], s60 offen lds\n\t" \
  "s_add_u32 m0, s54, 56320\n\t" \
  "s_mov_b32 s60, 622592\n\t" \
  "buffer_load_dwordx4 v71, %[Wk], s60 offen lds\n\t" \
  "s_waitcnt lgkmcnt(6)\n\t" \
  "v_perm_b32 v112, v82, v80, s62\n\t" \
  "v_perm_b32 v113, v82, v80, s63\n\t" \
  "s_waitcnt lgkmcnt(4)\n\t" \
  "v_perm_b32 v114, v86, v84, s62\n\t" \
  "v_perm_b32 v115, v86, v84, s63\n\t" \
  "s_waitcnt lgkmcnt(2)\n\t" \
  "v_perm_b32 v116, v90, v88, s62\n\t" \
  "v_perm_b32 v117, v90, v88, s63\n\t" \
  "s_waitcnt lgkmcnt(0)\n\t" \
  "v_perm_b32 v118, v94, v92, s62\n\t" \
  "v_perm_b32 v119, v94, v92, s63\n\t" \
  "ds_write_b128 v70, v[112:115] offset:22528\n\t" \
  "ds_write_b128 v70, v[116:119] offset:23552\n\t" \
  "v_perm_b32 v120, v83, v81, s62\n\t" \
  "v_perm_b32 v121, v83, v81, s63\n\t" \
  "v_perm_b32 v122, v87, v85, s62\n\t" \
  "v_perm_b32 v123, v87, v85, s63\n\t" \
  "v_perm_b32 v124, v91, v89, s62\n\t" \
  "v_perm_b32 v125, v91, v89, s63\n\t" \
  "v_perm_b32 v126, v95, v93, s62\n\t" \
  "v_perm_b32 v127, v95, v93, s63\n\t" \
  "ds_write_b128 v70, v[120:123] offset:18432\n\t" \
  "ds_write_b128 v70, v[124:127] offset:19456\n\t" \
  "v_add_u32 v78, 126976, v66\n\t" \
  "s_waitcnt vmcnt(12) lgkmcnt(0)\n\t" \
  "s_barrier\n\t" \
  "ds_read_b64_tr_b16 a[16:17], v78 offset:0\n\t" \
  "ds_read_b64_tr_b16 a[18:19], v78 offset:1024\n\t" \
  "ds_read_b64_tr_b16 a[20:21], v78 offset:8192\n\t" \
  "ds_read_b64_tr_b16 a[22:23], v78 offset:9216\n\t" \
  "ds_read_b64_tr_b16 a[24:25], v78 offset:32\n\t" \
  "ds_read_b64_tr_b16 a[26:27], v78 offset:1056\n\t" \
  "ds_read_b64_tr_b16 a[28:29], v78 offset:8224\n\t" \
  "ds_read_b64_tr_b16 a[30:31], v78 offset:9248\n\t" \
  "s_add_u32 m0, s54, 90112\n\t" \
  "s_mov_b32 s60, 524288\n\t" \
  "buffer_load_dwordx4 v71, %[Wv], s60 offen lds\n\t" \
  "s_add_u32 m0, s54, 91136\n\t" \
  "s_mov_b32 s60, 557056\n\t" \
  "buffer_load_dwordx4 v71, %[Wv], s60 offen lds\n\t" \
  "s_add_u32 m0, s54, 92160\n\t" \
  "s_mov_b32 s60, 589824\n\t" \
  "buffer_load_dwordx4 v71, %[Wv], s60 offen lds\n\t" \
  "s_add_u32 m0, s54, 93184\n\t" \
  "s_mov_b32 s60, 622592\n\t" \
  "buffer_load_dwordx4 v71, %[Wv], s60 offen lds\n\t" \
  "v_add_u32 v78, 0, v65\n\t" \
  "s_waitcnt vmcnt(12) lgkmcnt(0)\n\t" \
  "s_barrier\n\t" \
  "ds_read_b64_tr_b16 a[32:33], v78 offset:0\n\t" \
  "ds_read_b64_tr_b16 a[34:35], v78 offset:1024\n\t" \
  "ds_read_b64_tr_b16 a[36:37], v78 offset:8192\n\t" \
  "ds_read_b64_tr_b16 a[38:39], v78 offset:9216\n\t" \
  "ds_read_b64_tr_b16 a[40:41], v78 offset:8\n\t" \
  "ds_read_b64_tr_b16 a[42:43], v78 offset:1032\n\t" \
  "ds_read_b64_tr_b16 a[44:45], v78 offset:8200\n\t" \
  "ds_read_b64_tr_b16 a[46:47], v78 offset:9224\n\t" \
  "s_xor_b32 s61, s40, 1\n\t" \
  "s_mul_i32 s61, s61, 36864\n\t" \
  "s_add_u32 s61, s61, s55\n\t" \
  "s_add_u32 m0, s54, 126976\n\t" \
  "s_add_u32 s60, s61, 512\n\t" \
  "buffer_load_dwordx4 v75, %[Wo], s60 offen lds\n\t" \
  "s_add_u32 m0, s54, 128000\n\t" \
  "s_add_u32 s60, s61, 9728\n\t" \
  "buffer_load_dwordx4 v75, %[Wo], s60 offen lds\n\t" \
  "s_add_u32 m0, s54, 129024\n\t" \
  "s_add_u32 s60, s61, 18944\n\t" \
  "buffer_load_dwordx4 v75, %[Wo], s60 offen lds\n\t" \
  "s_add_u32 m0, s54, 130048\n\t" \
  "s_add_u32 s60, s61, 28160\n\t" \
  "buffer_load_dwordx4 v75, %[Wo], s60 offen lds\n\t" \
  "v_add_u32 v78, 16384, v64\n\t" \
  "v_add_u32 v79, 17408, v64\n\t" \
  "s_waitcnt vmcnt(12) lgkmcnt(0)\n\t" \
  "s_barrier\n\t" \
  "ds_read2_b32 v[80:81], v78 offset0:0 offset1:2\n\t" \
  "ds_read2_b32 v[82:83], v78 offset0:32 offset1:34\n\t" \
  "ds_read2_b32 v[84:85], v78 offset0:64 offset1:66\n\t" \
  "ds_read2_b32 v[86:87], v78 offset0:96 offset1:98\n\t" \
  "ds_read2_b32 v[88:89], v79 offset0:0 offset1:2\n\t" \
  "ds_read2_b32 v[90:91], v79 offset0:32 offset1:34\n\t" \
  "ds_read2_b32 v[92:93], v79 offset0:64 offset1:66\n\t" \
  "ds_read2_b32 v[94:95], v79 offset0:96 offset1:98\n\t" \
  "s_add_u32 m0, s54, 0\n\t" \
  "s_mov_b32 s60, 1048576\n\t" \
  "buffer_load_dwordx4 v71, %[Wq], s60 offen lds\n\t" \
  "s_add_u32 m0, s54, 1024\n\t" \
  "s_mov_b32 s60, 1081344\n\t" \
  "buffer_load_dwordx4 v71, %[Wq], s60 offen lds\n\t" \
  "s_add_u32 m0, s54, 2048\n\t" \
  "s_mov_b32 s60, 1114112\n\t" \
  "buffer_load_dwordx4 v71, %[Wq], s60 offen lds\n\t" \
  "s_add_u32 m0, s54, 3072\n\t" \
  "s_mov_b32 s60, 1146880\n\t" \
  "buffer_load_dwordx4 v71, %[Wq], s60 offen lds\n\t" \
  "s_waitcnt lgkmcnt(6)\n\t" \
  "v_perm_b32 v112, v82, v80, s62\n\t" \
  "v_perm_b32 v113, v82, v80, s63\n\t" \
  "s_waitcnt lgkmcnt(4)\n\t" \
  "v_perm_b32 v114, v86, v84, s62\n\t" \
  "v_perm_b32 v115, v86, v84, s63\n\t" \
  "s_waitcnt lgkmcnt(2)\n\t" \
  "v_perm_b32 v116, v90, v88, s62\n\t" \
  "v_perm_b32 v117, v90, v88, s63\n\t" \
  "s_waitcnt lgkmcnt(0)\n\t" \
  "v_perm_b32 v118, v94, v92, s62\n\t" \
  "v_perm_b32 v119, v94, v92, s63\n\t" \
  "ds_write_b128 v70, v[112:115] offset:24576\n\t" \
  "ds_write_b128 v70, v[116:119] offset:25600\n\t" \
  "v_perm_b32 v200, v83, v81, s62\n\t" \
  "v_perm_b32 v201, v83, v81, s63\n\t" \
  "v_perm_b32 v202, v87, v85, s62\n\t" \
  "v_perm_b32 v203, v87, v85, s63\n\t" \
  "v_perm_b32 v204, v91, v89, s62\n\t" \
  "v_perm_b32 v205, v91, v89, s63\n\t" \
  "v_perm_b32 v206, v95, v93, s62\n\t" \
  "v_perm_b32 v207, v95, v93, s63\n\t" \
  "v_add_u32 v78, 53248, v65\n\t" \
  "s_waitcnt vmcnt(12) lgkmcnt(0)\n\t" \
  "s_barrier\n\t" \
  "ds_read_b64_tr_b16 a[48:49], v78 offset:0\n\t" \
  "ds_read_b64_tr_b16 a[50:51], v78 offset:1024\n\t" \
  "ds_read_b64_tr_b16 a[52:53], v78 offset:8192\n\t" \
  "ds_read_b64_tr_b16 a[54:55], v78 offset:9216\n\t" \
  "ds_read_b64_tr_b16 a[56:57], v78 offset:8\n\t" \
  "ds_read_b64_tr_b16 a[58:59], v78 offset:1032\n\t" \
  "ds_read_b64_tr_b16 a[60:61], v78 offset:8200\n\t" \
  "ds_read_b64_tr_b16 a[62:63], v78 offset:9224\n\t" \
  "s_add_u32 m0, s54, 16384\n\t" \
  "s_mov_b32 s60, 1048576\n\t" \
  "buffer_load_dwordx4 v71, %[Wk], s60 offen lds\n\t" \
  "s_add_u32 m0, s54, 17408\n\t" \
  "s_mov_b32 s60, 1081344\n\t" \
  "buffer_load_dwordx4 v71, %[Wk], s60 offen lds\n\t" \
  "s_add_u32 m0, s54, 18432\n\t" \
  "s_mov_b32 s60, 1114112\n\t" \
  "buffer_load_dwordx4 v71, %[Wk], s60 offen lds\n\t" \
  "s_add_u32 m0, s54, 19456\n\t" \
  "s_mov_b32 s60, 1146880\n\t" \
  "buffer_load_dwordx4 v71, %[Wk], s60 offen lds\n\t" \
  "v_add_u32 v78, 90112, v66\n\t" \
  "s_waitcnt vmcnt(12) lgkmcnt(0)\n\t" \
  "s_barrier\n\t" \
  "ds_read_b64_tr_b16 a[64:65], v78 offset:0\n\t" \
  "ds_read_b64_tr_b16 a[66:67], v78 offset:1024\n\t" \
  "ds_read_b64_tr_b16 a[68:69], v78 offset:8192\n\t" \
  "ds_read_b64_tr_b16 a[70:71], v78 offset:9216\n\t" \
  "ds_read_b64_tr_b16 a[72:73], v78 offset:32\n\t" \
  "ds_read_b64_tr_b16 a[74:75], v78 offset:1056\n\t" \
  "ds_read_b64_tr_b16 a[76:77], v78 offset:8224\n\t" \
  "ds_read_b64_tr_b16 a[78:79], v78 offset:9248\n\t" \
  "s_xor_b32 s61, s40, 2\n\t" \
  "s_mul_i32 s61, s61, 36864\n\t" \
  "s_add_u32 s61, s61, s55\n\t" \
  "s_add_u32 m0, s54, 53248\n\t" \
  "s_add_u32 s60, s61, 0\n\t" \
  "buffer_load_dwordx4 v75, %[Wo], s60 offen lds\n\t" \
  "s_add_u32 m0, s54, 54272\n\t" \
  "s_add_u32 s60, s61, 9216\n\t" \
  "buffer_load_dwordx4 v75, %[Wo], s60 offen lds\n\t" \
  "s_add_u32 m0, s54, 55296\n\t" \
  "s_add_u32 s60, s61, 18432\n\t" \
  "buffer_load_dwordx4 v75, %[Wo], s60 offen lds\n\t" \
  "s_add_u32 m0, s54, 56320\n\t" \
  "s_add_u32 s60, s61, 27648\n\t" \
  "buffer_load_dwordx4 v75, %[Wo], s60 offen lds\n\t" \
  "v_add_u32 v78, 126976, v64\n\t" \
  "v_add_u32 v79, 128000, v64\n\t" \
  "s_waitcnt vmcnt(12) lgkmcnt(0)\n\t" \
  "s_barrier\n\t" \
  "ds_read2_b32 v[80:81], v78 offset0:0 offset1:2\n\t" \
  "ds_read2_b32 v[82:83], v78 offset0:32 offset1:34\n\t" \
  "ds_read2_b32 v[84:85], v78 offset0:64 offset1:66\n\t" \
  "ds_read2_b32 v[86:87], v78 offset0:96 offset1:98\n\t" \
  "ds_read2_b32 v[88:89], v79 offset0:0 offset1:2\n\t" \
  "ds_read2_b32 v[90:91], v79 offset0:32 offset1:34\n\t" \
  "ds_read2_b32 v[92:93], v79 offset0:64 offset1:66\n\t" \
  "ds_read2_b32 v[94:95], v79 offset0:96 offset1:98\n\t" \
  "s_add_u32 m0, s54, 90112\n\t" \
  "s_mov_b32 s60, 1048576\n\t" \
  "buffer_load_dwordx4 v71, %[Wv], s60 offen lds\n\t" \
  "s_add_u32 m0, s54, 91136\n\t" \
  "s_mov_b32 s60, 1081344\n\t" \
  "buffer_load_dwordx4 v71, %[Wv], s60 offen lds\n\t" \
  "s_add_u32 m0, s54, 92160\n\t" \
  "s_mov_b32 s60, 1114112\n\t" \
  "buffer_load_dwordx4 v71, %[Wv], s60 offen lds\n\t" \
  "s_add_u32 m0, s54, 93184\n\t" \
  "s_mov_b32 s60, 1146880\n\t" \
  "buffer_load_dwordx4 v71, %[Wv], s60 offen lds\n\t" \
  "s_waitcnt lgkmcnt(6)\n\t" \
  "v_perm_b32 v112, v82, v80, s62\n\t" \
  "v_perm_b32 v113, v82, v80, s63\n\t" \
  "s_waitcnt lgkmcnt(4)\n\t" \
  "v_perm_b32 v114, v86, v84, s62\n\t" \
  "v_perm_b32 v115, v86, v84, s63\n\t" \
  "s_waitcnt lgkmcnt(2)\n\t" \
  "v_perm_b32 v116, v90, v88, s62\n\t" \
  "v_perm_b32 v117, v90, v88, s63\n\t" \
  "s_waitcnt lgkmcnt(0)\n\t" \
  "v_perm_b32 v118, v94, v92, s62\n\t" \
  "v_perm_b32 v119, v94, v92, s63\n\t" \
  "ds_write_b128 v70, v[112:115] offset:26624\n\t" \
  "ds_write_b128 v70, v[116:119] offset:27648\n\t" \
  "v_perm_b32 v232, v83, v81, s62\n\t" \
  "v_perm_b32 v233, v83, v81, s63\n\t" \
  "v_perm_b32 v234, v87, v85, s62\n\t" \
  "v_perm_b32 v235, v87, v85, s63\n\t" \
  "v_perm_b32 v236, v91, v89, s62\n\t" \
  "v_perm_b32 v237, v91, v89, s63\n\t" \
  "v_perm_b32 v238, v95, v93, s62\n\t" \
  "v_perm_b32 v239, v95, v93, s63\n\t" \
  "v_add_u32 v78, 0, v65\n\t" \
  "s_waitcnt vmcnt(12) lgkmcnt(0)\n\t" \
  "s_barrier\n\t" \
  "ds_read_b64_tr_b16 a[80:81], v78 offset:0\n\t" \
  "ds_read_b64_tr_b16 a[82:83], v78 offset:1024\n\t" \
  "ds_read_b64_tr_b16 a[84:85], v78 offset:8192\n\t" \
  "ds_read_b64_tr_b16 a[86:87], v78 offset:9216\n\t" \
  "ds_read_b64_tr_b16 a[88:89], v78 offset:8\n\t" \
  "ds_read_b64_tr_b16 a[90:91], v78 offset:1032\n\t" \
  "ds_read_b64_tr_b16 a[92:93], v78 offset:8200\n\t" \
  "ds_read_b64_tr_b16 a[94:95], v78 offset:9224\n\t" \
  "s_add_u32 m0, s54, 126976\n\t" \
  "s_mov_b32 s60, 1572864\n\t" \
  "buffer_load_dwordx4 v71, %[Wq], s60 offen lds\n\t" \
  "s_add_u32 m0, s54, 128000\n\t" \
  "s_mov_b32 s60, 1605632\n\t" \
  "buffer_load_dwordx4 v71, %[Wq], s60 offen lds\n\t" \
  "s_add_u32 m0, s54, 129024\n\t" \
  "s_mov_b32 s60, 1638400\n\t" \
  "buffer_load_dwordx4 v71, %[Wq], s60 offen lds\n\t" \
  "s_add_u32 m0, s54, 130048\n\t" \
  "s_mov_b32 s60, 1671168\n\t" \
  "buffer_load_dwordx4 v71, %[Wq], s60 offen lds\n\t" \
  "v_add_u32 v78, 16384, v65\n\t" \
  "s_waitcnt vmcnt(12) lgkmcnt(0)\n\t" \
  "s_barrier\n\t" \
  "ds_read_b64_tr_b16 v[32:33], v78 offset:0\n\t" \
  "ds_read_b64_tr_b16 v[34:35], v78 offset:1024\n\t" \
  "ds_read_b64_tr_b16 v[36:37], v78 offset:8192\n\t" \
  "ds_read_b64_tr_b16 v[38:39], v78 offset:9216\n\t" \
  "ds_read_b64_tr_b16 a[96:97], v78 offset:8\n\t" \
  "ds_read_b64_tr_b16 a[98:99], v78 offset:1032\n\t" \
  "ds_read_b64_tr_b16 a[100:101], v78 offset:8200\n\t" \
  "ds_read_b64_tr_b16 a[102:103], v78 offset:9224\n\t" \
  "s_xor_b32 s61, s40, 2\n\t" \
  "s_mul_i32 s61, s61, 36864\n\t" \
  "s_add_u32 s61, s61, s55\n\t" \
  "s_add_u32 m0, s54, 0\n\t" \
  "s_add_u32 s60, s61, 512\n\t" \
  "buffer_load_dwordx4 v75, %[Wo], s60 offen lds\n\t" \
  "s_add_u32 m0, s54, 1024\n\t" \
  "s_add_u32 s60, s61, 9728\n\t" \
  "buffer_load_dwordx4 v75, %[Wo], s60 offen lds\n\t" \
  "s_add_u32 m0, s54, 2048\n\t" \
  "s_add_u32 s60, s61, 18944\n\t" \
  "buffer_load_dwordx4 v75, %[Wo], s60 offen lds\n\t" \
  "s_add_u32 m0, s54, 3072\n\t" \
  "s_add_u32 s60, s61, 28160\n\t" \
  "buffer_load_dwordx4 v75, %[Wo], s60 offen lds\n\t" \
  "v_add_u32 v78, 53248, v64\n\t" \
  "v_add_u32 v79, 54272, v64\n\t" \
  "s_waitcnt vmcnt(12) lgkmcnt(0)\n\t" \
  "s_barrier\n\t" \
  "ds_read2_b32 v[80:81], v78 offset0:0 offset1:2\n\t" \
  "ds_read2_b32 v[82:83], v78 offset0:32 offset1:34\n\t" \
  "ds_read2_b32 v[84:85], v78 offset0:64 offset1:66\n\t" \
  "ds_read2_b32 v[86:87], v78 offset0:96 offset1:98\n\t" \
  "ds_read2_b32 v[88:89], v79 offset0:0 offset1:2\n\t" \
  "ds_read2_b32 v[90:91], v79 offset0:32 offset1:34\n\t" \
  "ds_read2_b32 v[92:93], v79 offset0:64 offset1:66\n\t" \
  "ds_read2_b32 v[94:95], v79 offset0:96 offset1:98\n\t" \
  "s_add_u32 m0, s54, 16384\n\t" \
  "s_mov_b32 s60, 1572864\n\t" \
  "buffer_load_dwordx4 v71, %[Wk], s60 offen lds\n\t" \
  "s_add_u32 m0, s54, 17408\n\t" \
  "s_mov_b32 s60, 1605632\n\t" \
  "buffer_load_dwordx4 v71, %[Wk], s60 offen lds\n\t" \
  "s_add_u32 m0, s54, 18432\n\t" \
  "s_mov_b32 s60, 1638400\n\t" \
  "buffer_load_dwordx4 v71, %[Wk], s60 offen lds\n\t" \
  "s_add_u32 m0, s54, 19456\n\t" \
  "s_mov_b32 s60, 1671168\n\t" \
  "buffer_load_dwordx4 v71, %[Wk], s60 offen lds\n\t" \
  "s_waitcnt lgkmcnt(6)\n\t" \
  "v_perm_b32 v112, v82, v80, s62\n\t" \
  "v_perm_b32 v113, v82, v80, s63\n\t" \
  "s_waitcnt lgkmcnt(4)\n\t" \
  "v_perm_b32 v114, v86, v84, s62\n\t" \
  "v_perm_b32 v115, v86, v84, s63\n\t" \
  "s_waitcnt lgkmcnt(2)\n\t" \
  "v_perm_b32 v116, v90, v88, s62\n\t" \
  "v_perm_b32 v117, v90, v88, s63\n\t" \
  "s_waitcnt lgkmcnt(0)\n\t" \
  "v_perm_b32 v118, v94, v92, s62\n\t" \
  "v_perm_b32 v119, v94, v92, s63\n\t" \
  "ds_write_b128 v70, v[112:115] offset:28672\n\t" \
  "ds_write_b128 v70, v[116:119] offset:29696\n\t" \
  "v_perm_b32 v208, v83, v81, s62\n\t" \
  "v_perm_b32 v209, v83, v81, s63\n\t" \
  "v_perm_b32 v210, v87, v85, s62\n\t" \
  "v_perm_b32 v211, v87, v85, s63\n\t" \
  "v_perm_b32 v212, v91, v89, s62\n\t" \
  "v_perm_b32 v213, v91, v89, s63\n\t" \
  "v_perm_b32 v214, v95, v93, s62\n\t" \
  "v_perm_b32 v215, v95, v93, s63\n\t" \
  "v_add_u32 v78, 90112, v66\n\t" \
  "s_waitcnt vmcnt(12) lgkmcnt(0)\n\t" \
  "s_barrier\n\t" \
  "ds_read_b64_tr_b16 a[104:105], v78 offset:0\n\t" \
  "ds_read_b64_tr_b16 a[106:107], v78 offset:1024\n\t" \
  "ds_read_b64_tr_b16 a[108:109], v78 offset:8192\n\t" \
  "ds_read_b64_tr_b16 a[110:111], v78 offset:9216\n\t" \
  "ds_read_b64_tr_b16 a[112:113], v78 offset:32\n\t" \
  "ds_read_b64_tr_b16 a[114:115], v78 offset:1056\n\t" \
  "ds_read_b64_tr_b16 a[116:117], v78 offset:8224\n\t" \
  "ds_read_b64_tr_b16 a[118:119], v78 offset:9248\n\t" \
  "s_add_u32 m0, s54, 53248\n\t" \
  "s_mov_b32 s60, 1572864\n\t" \
  "buffer_load_dwordx4 v71, %[Wv], s60 offen lds\n\t" \
  "s_add_u32 m0, s54, 54272\n\t" \
  "s_mov_b32 s60, 1605632\n\t" \
  "buffer_load_dwordx4 v71, %[Wv], s60 offen lds\n\t" \
  "s_add_u32 m0, s54, 55296\n\t" \
  "s_mov_b32 s60, 1638400\n\t" \
  "buffer_load_dwordx4 v71, %[Wv], s60 offen lds\n\t" \
  "s_add_u32 m0, s54, 56320\n\t" \
  "s_mov_b32 s60, 1671168\n\t" \
  "buffer_load_dwordx4 v71, %[Wv], s60 offen lds\n\t" \
  "v_add_u32 v78, 126976, v65\n\t" \
  "s_waitcnt vmcnt(12) lgkmcnt(0)\n\t" \
  "s_barrier\n\t" \
  "ds_read_b64_tr_b16 a[120:121], v78 offset:0\n\t" \
  "ds_read_b64_tr_b16 a[122:123], v78 offset:1024\n\t" \
  "ds_read_b64_tr_b16 a[124:125], v78 offset:8192\n\t" \
  "ds_read_b64_tr_b16 a[126:127], v78 offset:9216\n\t" \
  "ds_read_b64_tr_b16 v[16:17], v78 offset:8\n\t" \
  "ds_read_b64_tr_b16 v[18:19], v78 offset:1032\n\t" \
  "ds_read_b64_tr_b16 v[20:21], v78 offset:8200\n\t" \
  "ds_read_b64_tr_b16 v[22:23], v78 offset:9224\n\t" \
  "s_xor_b32 s61, s40, 3\n\t" \
  "s_mul_i32 s61, s61, 36864\n\t" \
  "s_add_u32 s61, s61, s55\n\t" \
  "s_add_u32 m0, s54, 90112\n\t" \
  "s_add_u32 s60, s61, 0\n\t" \
  "buffer_load_dwordx4 v75, %[Wo], s60 offen lds\n\t" \
  "s_add_u32 m0, s54, 91136\n\t" \
  "s_add_u32 s60, s61, 9216\n\t" \
  "buffer_load_dwordx4 v75, %[Wo], s60 offen lds\n\t" \
  "s_add_u32 m0, s54, 92160\n\t" \
  "s_add_u32 s60, s61, 18432\n\t" \
  "buffer_load_dwordx4 v75, %[Wo], s60 offen lds\n\t" \
  "s_add_u32 m0, s54, 93184\n\t" \
  "s_add_u32 s60, s61, 27648\n\t" \
  "buffer_load_dwordx4 v75, %[Wo], s60 offen lds\n\t" \
  "v_add_u32 v78, 0, v64\n\t" \
  "v_add_u32 v79, 1024, v64\n\t" \
  "s_waitcnt vmcnt(12) lgkmcnt(0)\n\t" \
  "s_barrier\n\t" \
  "ds_read2_b32 v[80:81], v78 offset0:0 offset1:2\n\t" \
  "ds_read2_b32 v[82:83], v78 offset0:32 offset1:34\n\t" \
  "ds_read2_b32 v[84:85], v78 offset0:64 offset1:66\n\t" \
  "ds_read2_b32 v[86:87], v78 offset0:96 offset1:98\n\t" \
  "ds_read2_b32 v[88:89], v79 offset0:0 offset1:2\n\t" \
  "ds_read2_b32 v[90:91], v79 offset0:32 offset1:34\n\t" \
  "ds_read2_b32 v[92:93], v79 offset0:64 offset1:66\n\t" \
  "ds_read2_b32 v[94:95], v79 offset0:96 offset1:98\n\t" \
  "s_add_u32 m0, s54, 126976\n\t" \
  "s_mov_b32 s60, 2097152\n\t" \
  "buffer_load_dwordx4 v71, %[Wq], s60 offen lds\n\t" \
  "s_add_u32 m0, s54, 128000\n\t" \
  "s_mov_b32 s60, 2129920\n\t" \
  "buffer_load_dwordx4 v71, %[Wq], s60 offen lds\n\t" \
  "s_add_u32 m0, s54, 129024\n\t" \
  "s_mov_b32 s60, 2162688\n\t" \
  "buffer_load_dwordx4 v71, %[Wq], s60 offen lds\n\t" \
  "s_add_u32 m0, s54, 130048\n\t" \
  "s_mov_b32 s60, 2195456\n\t" \
  "buffer_load_dwordx4 v71, %[Wq], s60 offen lds\n\t" \
  "s_waitcnt lgkmcnt(6)\n\t" \
  "v_perm_b32 v112, v82, v80, s62\n\t" \
  "v_perm_b32 v113, v82, v80, s63\n\t" \
  "s_waitcnt lgkmcnt(4)\n\t" \
  "v_perm_b32 v114, v86, v84, s62\n\t" \
  "v_perm_b32 v115, v86, v84, s63\n\t" \
  "s_waitcnt lgkmcnt(2)\n\t" \
  "v_perm_b32 v116, v90, v88, s62\n\t" \
  "v_perm_b32 v117, v90, v88, s63\n\t" \
  "s_waitcnt lgkmcnt(0)\n\t" \
  "v_perm_b32 v118, v94, v92, s62\n\t" \
  "v_perm_b32 v119, v94, v92, s63\n\t" \
  "ds_write_b128 v70, v[112:115] offset:30720\n\t" \
  "ds_write_b128 v70, v[116:119] offset:31744\n\t" \
  "v_perm_b32 v240, v83, v81, s62\n\t" \
  "v_perm_b32 v241, v83, v81, s63\n\t" \
  "v_perm_b32 v242, v87, v85, s62\n\t" \
  "v_perm_b32 v243, v87, v85, s63\n\t" \
  "v_perm_b32 v244, v91, v89, s62\n\t" \
  "v_perm_b32 v245, v91, v89, s63\n\t" \
  "v_perm_b32 v246, v95, v93, s62\n\t" \
  "v_perm_b32 v247, v95, v93, s63\n\t" \
  "v_add_u32 v78, 16384, v65\n\t" \
  "s_waitcnt vmcnt(12) lgkmcnt(0)\n\t" \
  "s_barrier\n\t" \
  "ds_read_b64_tr_b16 a[128:129], v78 offset:0\n\t" \
  "ds_read_b64_tr_b16 a[130:131], v78 offset:1024\n\t" \
  "ds_read_b64_tr_b16 a[132:133], v78 offset:8192\n\t" \
  "ds_read_b64_tr_b16 a[134:135], v78 offset:9216\n\t" \
  "ds_read_b64_tr_b16 a[136:137], v78 offset:8\n\t" \
  "ds_read_b64_tr_b16 a[138:139], v78 offset:1032\n\t" \
  "ds_read_b64_tr_b16 a[140:141], v78 offset:8200\n\t" \
  "ds_read_b64_tr_b16 a[142:143], v78 offset:9224\n\t" \
  "s_add_u32 m0, s54, 0\n\t" \
  "s_mov_b32 s60, 2097152\n\t" \
  "buffer_load_dwordx4 v71, %[Wk], s60 offen lds\n\t" \
  "s_add_u32 m0, s54, 1024\n\t" \
  "s_mov_b32 s60, 2129920\n\t" \
  "buffer_load_dwordx4 v71, %[Wk], s60 offen lds\n\t" \
  "s_add_u32 m0, s54, 2048\n\t" \
  "s_mov_b32 s60, 2162688\n\t" \
  "buffer_load_dwordx4 v71, %[Wk], s60 offen lds\n\t" \
  "s_add_u32 m0, s54, 3072\n\t" \
  "s_mov_b32 s60, 2195456\n\t" \
  "buffer_load_dwordx4 v71, %[Wk], s60 offen lds\n\t" \
  "v_add_u32 v78, 53248, v66\n\t" \
  "s_waitcnt vmcnt(12) lgkmcnt(0)\n\t" \
  "s_barrier\n\t" \
  "ds_read_b64_tr_b16 a[144:145], v78 offset:0\n\t" \
  "ds_read_b64_tr_b16 a[146:147], v78 offset:1024\n\t" \
  "ds_read_b64_tr_b16 a[148:149], v78 offset:8192\n\t" \
  "ds_read_b64_tr_b16 a[150:151], v78 offset:9216\n\t" \
  "ds_read_b64_tr_b16 a[152:153], v78 offset:32\n\t" \
  "ds_read_b64_tr_b16 a[154:155], v78 offset:1056\n\t" \
  "ds_read_b64_tr_b16 a[156:157], v78 offset:8224\n\t" \
  "ds_read_b64_tr_b16 a[158:159], v78 offset:9248\n\t" \
  "s_xor_b32 s61, s40, 3\n\t" \
  "s_mul_i32 s61, s61, 36864\n\t" \
  "s_add_u32 s61, s61, s55\n\t" \
  "s_add_u32 m0, s54, 16384\n\t" \
  "s_add_u32 s60, s61, 512\n\t" \
  "buffer_load_dwordx4 v75, %[Wo], s60 offen lds\n\t" \
  "s_add_u32 m0, s54, 17408\n\t" \
  "s_add_u32 s60, s61, 9728\n\t" \
  "buffer_load_dwordx4 v75, %[Wo], s60 offen lds\n\t" \
  "s_add_u32 m0, s54, 18432\n\t" \
  "s_add_u32 s60, s61, 18944\n\t" \
  "buffer_load_dwordx4 v75, %[Wo], s60 offen lds\n\t" \
  "s_add_u32 m0, s54, 19456\n\t" \
  "s_add_u32 s60, s61, 28160\n\t" \
  "buffer_load_dwordx4 v75, %[Wo], s60 offen lds\n\t" \
  "v_add_u32 v78, 90112, v64\n\t" \
  "v_add_u32 v79, 91136, v64\n\t" \
  "s_waitcnt vmcnt(12) lgkmcnt(0)\n\t" \
  "s_barrier\n\t" \
  "ds_read2_b32 v[80:81], v78 offset0:0 offset1:2\n\t" \
  "ds_read2_b32 v[82:83], v78 offset0:32 offset1:34\n\t" \
  "ds_read2_b32 v[84:85], v78 offset0:64 offset1:66\n\t" \
  "ds_read2_b32 v[86:87], v78 offset0:96 offset1:98\n\t" \
  "ds_read2_b32 v[88:89], v79 offset0:0 offset1:2\n\t" \
  "ds_read2_b32 v[90:91], v79 offset0:32 offset1:34\n\t" \
  "ds_read2_b32 v[92:93], v79 offset0:64 offset1:66\n\t" \
  "ds_read2_b32 v[94:95], v79 offset0:96 offset1:98\n\t" \
  "s_add_u32 m0, s54, 53248\n\t" \
  "s_mov_b32 s60, 2097152\n\t" \
  "buffer_load_dwordx4 v71, %[Wv], s60 offen lds\n\t" \
  "s_add_u32 m0, s54, 54272\n\t" \
  "s_mov_b32 s60, 2129920\n\t" \
  "buffer_load_dwordx4 v71, %[Wv], s60 offen lds\n\t" \
  "s_add_u32 m0, s54, 55296\n\t" \
  "s_mov_b32 s60, 2162688\n\t" \
  "buffer_load_dwordx4 v71, %[Wv], s60 offen lds\n\t" \
  "s_add_u32 m0, s54, 56320\n\t" \
  "s_mov_b32 s60, 2195456\n\t" \
  "buffer_load_dwordx4 v71, %[Wv], s60 offen lds\n\t" \
  "s_waitcnt lgkmcnt(6)\n\t" \
  "v_perm_b32 v192, v82, v80, s62\n\t" \
  "v_perm_b32 v193, v82, v80, s63\n\t" \
  "s_waitcnt lgkmcnt(4)\n\t" \
  "v_perm_b32 v194, v86, v84, s62\n\t" \
  "v_perm_b32 v195, v86, v84, s63\n\t" \
  "s_waitcnt lgkmcnt(2)\n\t" \
  "v_perm_b32 v196, v90, v88, s62\n\t" \
  "v_perm_b32 v197, v90, v88, s63\n\t" \
  "s_waitcnt lgkmcnt(0)\n\t" \
  "v_perm_b32 v198, v94, v92, s62\n\t" \
  "v_perm_b32 v199, v94, v92, s63\n\t" \
  "v_perm_b32 v216, v83, v81, s62\n\t" \
  "v_perm_b32 v217, v83, v81, s63\n\t" \
  "v_perm_b32 v218, v87, v85, s62\n\t" \
  "v_perm_b32 v219, v87, v85, s63\n\t" \
  "v_perm_b32 v220, v91, v89, s62\n\t" \
  "v_perm_b32 v221, v91, v89, s63\n\t" \
  "v_perm_b32 v222, v95, v93, s62\n\t" \
  "v_perm_b32 v223, v95, v93, s63\n\t" \
  "v_add_u32 v78, 126976, v65\n\t" \
  "s_waitcnt vmcnt(12) lgkmcnt(0)\n\t" \
  "s_barrier\n\t" \
  "ds_read_b64_tr_b16 a[160:161], v78 offset:0\n\t" \
  "ds_read_b64_tr_b16 a[162:163], v78 offset:1024\n\t" \
  "ds_read_b64_tr_b16 a[164:165], v78 offset:8192\n\t" \
  "ds_read_b64_tr_b16 a[166:167], v78 offset:9216\n\t" \
  "ds_read_b64_tr_b16 a[168:169], v78 offset:8\n\t" \
  "ds_read_b64_tr_b16 a[170:171], v78 offset:1032\n\t" \
  "ds_read_b64_tr_b16 a[172:173], v78 offset:8200\n\t" \
  "ds_read_b64_tr_b16 a[174:175], v78 offset:9224\n\t" \
  "s_add_u32 m0, s54, 90112\n\t" \
  "s_mov_b32 s60, 2621440\n\t" \
  "buffer_load_dwordx4 v71, %[Wq], s60 offen lds\n\t" \
  "s_add_u32 m0, s54, 91136\n\t" \
  "s_mov_b32 s60, 2654208\n\t" \
  "buffer_load_dwordx4 v71, %[Wq], s60 offen lds\n\t" \
  "s_add_u32 m0, s54, 92160\n\t" \
  "s_mov_b32 s60, 2686976\n\t" \
  "buffer_load_dwordx4 v71, %[Wq], s60 offen lds\n\t" \
  "s_add_u32 m0, s54, 93184\n\t" \
  "s_mov_b32 s60, 2719744\n\t" \
  "buffer_load_dwordx4 v71, %[Wq], s60 offen lds\n\t" \
  "v_add_u32 v78, 0, v65\n\t" \
  "s_waitcnt vmcnt(12) lgkmcnt(0)\n\t" \
  "s_barrier\n\t" \
  "ds_read_b64_tr_b16 a[176:177], v78 offset:0\n\t" \
  "ds_read_b64_tr_b16 a[178:179], v78 offset:1024\n\t" \
  "ds_read_b64_tr_b16 a[180:181], v78 offset:8192\n\t" \
  "ds_read_b64_tr_b16 a[182:183], v78 offset:9216\n\t" \
  "ds_read_b64_tr_b16 a[184:185], v78 offset:8\n\t" \
  "ds_read_b64_tr_b16 a[186:187], v78 offset:1032\n\t" \
  "ds_read_b64_tr_b16 a[188:189], v78 offset:8200\n\t" \
  "ds_read_b64_tr_b16 a[190:191], v78 offset:9224\n\t" \
  "s_add_u32 m0, s54, 126976\n\t" \
  "s_add_u32 s60, s75, 0\n\t" \
  "buffer_load_dwordx4 v75, %[Wo], s60 offen lds\n\t" \
  "s_add_u32 m0, s54, 128000\n\t" \
  "s_add_u32 s60, s75, 9216\n\t" \
  "buffer_load_dwordx4 v75, %[Wo], s60 offen lds\n\t" \
  "s_add_u32 m0, s54, 129024\n\t" \
  "s_add_u32 s60, s75, 18432\n\t" \
  "buffer_load_dwordx4 v75, %[Wo], s60 offen lds\n\t" \
  "s_add_u32 m0, s54, 130048\n\t" \
  "s_add_u32 s60, s75, 27648\n\t" \
  "buffer_load_dwordx4 v75, %[Wo], s60 offen lds\n\t" \
  "v_add_u32 v78, 16384, v64\n\t" \
  "v_add_u32 v79, 17408, v64\n\t" \
  "s_waitcnt vmcnt(12) lgkmcnt(0)\n\t" \
  "s_barrier\n\t" \
  "ds_read2_b32 v[80:81], v78 offset0:0 offset1:2\n\t" \
  "ds_read2_b32 v[82:83], v78 offset0:32 offset1:34\n\t" \
  "ds_read2_b32 v[84:85], v78 offset0:64 offset1:66\n\t" \
  "ds_read2_b32 v[86:87], v78 offset0:96 offset1:98\n\t" \
  "ds_read2_b32 v[88:89], v79 offset0:0 offset1:2\n\t" \
  "ds_read2_b32 v[90:91], v79 offset0:32 offset1:34\n\t" \
  "ds_read2_b32 v[92:93], v79 offset0:64 offset1:66\n\t" \
  "ds_read2_b32 v[94:95], v79 offset0:96 offset1:98\n\t" \
  "s_add_u32 m0, s54, 0\n\t" \
  "s_mov_b32 s60, 2621440\n\t" \
  "buffer_load_dwordx4 v71, %[Wk], s60 offen lds\n\t" \
  "s_add_u32 m0, s54, 1024\n\t" \
  "s_mov_b32 s60, 2654208\n\t" \
  "buffer_load_dwordx4 v71, %[Wk], s60 offen lds\n\t" \
  "s_add_u32 m0, s54, 2048\n\t" \
  "s_mov_b32 s60, 2686976\n\t" \
  "buffer_load_dwordx4 v71, %[Wk], s60 offen lds\n\t" \
  "s_add_u32 m0, s54, 3072\n\t" \
  "s_mov_b32 s60, 2719744\n\t" \
  "buffer_load_dwordx4 v71, %[Wk], s60 offen lds\n\t" \
  "s_waitcnt lgkmcnt(6)\n\t" \
  "v_perm_b32 v224, v82, v80, s62\n\t" \
  "v_perm_b32 v225, v82, v80, s63\n\t" \
  "s_waitcnt lgkmcnt(4)\n\t" \
  "v_perm_b32 v226, v86, v84, s62\n\t" \
  "v_perm_b32 v227, v86, v84, s63\n\t" \
  "s_waitcnt lgkmcnt(2)\n\t" \
  "v_perm_b32 v228, v90, v88, s62\n\t" \
  "v_perm_b32 v229, v90, v88, s63\n\t" \
  "s_waitcnt lgkmcnt(0)\n\t" \
  "v_perm_b32 v230, v94, v92, s62\n\t" \
  "v_perm_b32 v231, v94, v92, s63\n\t" \
  "v_perm_b32 v120, v83, v81, s62\n\t" \
  "v_perm_b32 v121, v83, v81, s63\n\t" \
  "v_perm_b32 v122, v87, v85, s62\n\t" \
  "v_perm_b32 v123, v87, v85, s63\n\t" \
  "v_perm_b32 v124, v91, v89, s62\n\t" \
  "v_perm_b32 v125, v91, v89, s63\n\t" \
  "v_perm_b32 v126, v95, v93, s62\n\t" \
  "v_perm_b32 v127, v95, v93, s63\n\t" \
  "ds_write_b128 v70, v[120:123] offset:32768\n\t" \
  "ds_write_b128 v70, v[124:127] offset:33792\n\t" \
  "v_add_u32 v78, 53248, v66\n\t" \
  "s_waitcnt vmcnt(12) lgkmcnt(0)\n\t" \
  "s_barrier\n\t" \
  "ds_read_b64_tr_b16 a[192:193], v78 offset:0\n\t" \
  "ds_read_b64_tr_b16 a[194:195], v78 offset:1024\n\t" \
  "ds_read_b64_tr_b16 a[196:197], v78 offset:8192\n\t" \
  "ds_read_b64_tr_b16 a[198:199], v78 offset:9216\n\t" \
  "ds_read_b64_tr_b16 v[48:49], v78 offset:32\n\t" \
  "ds_read_b64_tr_b16 v[50:51], v78 offset:1056\n\t" \
  "ds_read_b64_tr_b16 v[52:53], v78 offset:8224\n\t" \
  "ds_read_b64_tr_b16 v[54:55], v78 offset:9248\n\t" \
  "s_add_u32 m0, s54, 16384\n\t" \
  "s_mov_b32 s60, 2621440\n\t" \
  "buffer_load_dwordx4 v71, %[Wv], s60 offen lds\n\t" \
  "s_add_u32 m0, s54, 17408\n\t" \
  "s_mov_b32 s60, 2654208\n\t" \
  "buffer_load_dwordx4 v71, %[Wv], s60 offen lds\n\t" \
  "s_add_u32 m0, s54, 18432\n\t" \
  "s_mov_b32 s60, 2686976\n\t" \
  "buffer_load_dwordx4 v71, %[Wv], s60 offen lds\n\t" \
  "s_add_u32 m0, s54, 19456\n\t" \
  "s_mov_b32 s60, 2719744\n\t" \
  "buffer_load_dwordx4 v71, %[Wv], s60 offen lds\n\t" \
  "v_add_u32 v78, 90112, v65\n\t" \
  "s_waitcnt vmcnt(12) lgkmcnt(0)\n\t" \
  "s_barrier\n\t" \
  "ds_read_b64_tr_b16 a[200:201], v78 offset:0\n\t" \
  "ds_read_b64_tr_b16 a[202:203], v78 offset:1024\n\t" \
  "ds_read_b64_tr_b16 a[204:205], v78 offset:8192\n\t" \
  "ds_read_b64_tr_b16 a[206:207], v78 offset:9216\n\t" \
  "ds_read_b64_tr_b16 v[24:25], v78 offset:8\n\t" \
  "ds_read_b64_tr_b16 v[26:27], v78 offset:1032\n\t" \
  "ds_read_b64_tr_b16 v[28:29], v78 offset:8200\n\t" \
  "ds_read_b64_tr_b16 v[30:31], v78 offset:9224\n\t" \
  "s_add_u32 m0, s54, 53248\n\t" \
  "s_mov_b32 s60, 3145728\n\t" \
  "buffer_load_dwordx4 v71, %[Wq], s60 offen lds\n\t" \
  "s_add_u32 m0, s54, 54272\n\t" \
  "s_mov_b32 s60, 3178496\n\t" \
  "buffer_load_dwordx4 v71, %[Wq], s60 offen lds\n\t" \
  "s_add_u32 m0, s54, 55296\n\t" \
  "s_mov_b32 s60, 3211264\n\t" \
  "buffer_load_dwordx4 v71, %[Wq], s60 offen lds\n\t" \
  "s_add_u32 m0, s54, 56320\n\t" \
  "s_mov_b32 s60, 3244032\n\t" \
  "buffer_load_dwordx4 v71, %[Wq], s60 offen lds\n\t" \
  "v_add_u32 v78, 126976, v67\n\t" \
  "s_waitcnt vmcnt(12) lgkmcnt(0)\n\t" \
  "s_barrier\n\t" \
  "ds_read_u16 v80, v78 offset:0\n\t" \
  "ds_read_u16 v81, v78 offset:128\n\t" \
  "ds_read_u16 v82, v78 offset:4096\n\t" \
  "ds_read_u16 v83, v78 offset:4224\n\t" \
  "ds_read_u16 v84, v78 offset:256\n\t" \
  "ds_read_u16 v85, v78 offset:384\n\t" \
  "ds_read_u16 v86, v78 offset:4352\n\t" \
  "ds_read_u16 v87, v78 offset:4480\n\t" \
  "ds_read_u16 v88, v78 offset:1024\n\t" \
  "ds_read_u16 v89, v78 offset:1152\n\t" \
  "ds_read_u16 v90, v78 offset:5120\n\t" \
  "ds_read_u16 v91, v78 offset:5248\n\t" \
  "ds_read_u16 v92, v78 offset:1280\n\t" \
  "ds_read_u16 v93, v78 offset:1408\n\t" \
  "ds_read_u16 v94, v78 offset:5376\n\t" \
  "ds_read_u16 v95, v78 offset:5504\n\t" \
  "ds_read_u16 v96, v78 offset:8192\n\t" \
  "ds_read_u16 v97, v78 offset:8320\n\t" \
  "ds_read_u16 v98, v78 offset:12288\n\t" \
  "ds_read_u16 v99, v78 offset:12416\n\t" \
  "ds_read_u16 v100, v78 offset:8448\n\t" \
  "ds_read_u16 v101, v78 offset:8576\n\t" \
  "ds_read_u16 v102, v78 offset:12544\n\t" \
  "ds_read_u16 v103, v78 offset:12672\n\t" \
  "ds_read_u16 v104, v78 offset:9216\n\t" \
  "ds_read_u16 v105, v78 offset:9344\n\t" \
  "ds_read_u16 v106, v78 offset:13312\n\t" \
  "ds_read_u16 v107, v78 offset:13440\n\t" \
  "ds_read_u16 v108, v78 offset:9472\n\t" \
  "ds_read_u16 v109, v78 offset:9600\n\t" \
  "ds_read_u16 v110, v78 offset:13568\n\t" \
  "ds_read_u16 v111, v78 offset:13696\n\t" \
  "s_add_u32 m0, s54, 90112\n\t" \
  "s_mov_b32 s60, 3145728\n\t" \
  "buffer_load_dwordx4 v71, %[Wk], s60 offen lds\n\t" \
  "s_add_u32 m0, s54, 91136\n\t" \
  "s_mov_b32 s60, 3178496\n\t" \
  "buffer_load_dwordx4 v71, %[Wk], s60 offen lds\n\t" \
  "s_add_u32 m0, s54, 92160\n\t" \
  "s_mov_b32 s60, 3211264\n\t" \
  "buffer_load_dwordx4 v71, %[Wk], s60 offen lds\n\t" \
  "s_add_u32 m0, s54, 93184\n\t" \
  "s_mov_b32 s60, 3244032\n\t" \
  "buffer_load_dwordx4 v71, %[Wk], s60 offen lds\n\t" \
  "s_waitcnt lgkmcnt(0)\n\t" \
  "v_lshl_or_b32 v248, v81, 16, v80\n\t" \
  "v_lshl_or_b32 v249, v83, 16, v82\n\t" \
  "v_lshl_or_b32 v250, v85, 16, v84\n\t" \
  "v_lshl_or_b32 v251, v87, 16, v86\n\t" \
  "v_lshl_or_b32 v252, v89, 16, v88\n\t" \
  "v_lshl_or_b32 v253, v91, 16, v90\n\t" \
  "v_lshl_or_b32 v254, v93, 16, v92\n\t" \
  "v_lshl_or_b32 v255, v95, 16, v94\n\t" \
  "v_lshl_or_b32 v112, v97, 16, v96\n\t" \
  "v_lshl_or_b32 v113, v99, 16, v98\n\t" \
  "v_lshl_or_b32 v114, v101, 16, v100\n\t" \
  "v_lshl_or_b32 v115, v103, 16, v102\n\t" \
  "v_lshl_or_b32 v116, v105, 16, v104\n\t" \
  "v_lshl_or_b32 v117, v107, 16, v106\n\t" \
  "v_lshl_or_b32 v118, v109, 16, v108\n\t" \
  "v_lshl_or_b32 v119, v111, 16, v110\n\t" \
  "ds_write_b128 v70, v[112:115] offset:34816\n\t" \
  "ds_write_b128 v70, v[116:119] offset:35840\n\t" \
  "v_add_u32 v78, 0, v65\n\t" \
  "s_waitcnt vmcnt(12) lgkmcnt(0)\n\t" \
  "s_barrier\n\t" \
  "ds_read_b64_tr_b16 a[208:209], v78 offset:0\n\t" \
  "ds_read_b64_tr_b16 a[210:211], v78 offset:1024\n\t" \
  "ds_read_b64_tr_b16 a[212:213], v78 offset:8192\n\t" \
  "ds_read_b64_tr_b16 a[214:215], v78 offset:9216\n\t" \
  "ds_read_b64_tr_b16 a[216:217], v78 offset:8\n\t" \
  "ds_read_b64_tr_b16 a[218:219], v78 offset:1032\n\t" \
  "ds_read_b64_tr_b16 a[220:221], v78 offset:8200\n\t" \
  "ds_read_b64_tr_b16 a[222:223], v78 offset:9224\n\t" \
  "s_add_u32 m0, s54, 126976\n\t" \
  "s_mov_b32 s60, 3145728\n\t" \
  "buffer_load_dwordx4 v71, %[Wv], s60 offen lds\n\t" \
  "s_add_u32 m0, s54, 128000\n\t" \
  "s_mov_b32 s60, 3178496\n\t" \
  "buffer_load_dwordx4 v71, %[Wv], s60 offen lds\n\t" \
  "s_add_u32 m0, s54, 129024\n\t" \
  "s_mov_b32 s60, 3211264\n\t" \
  "buffer_load_dwordx4 v71, %[Wv], s60 offen lds\n\t" \
  "s_add_u32 m0, s54, 130048\n\t" \
  "s_mov_b32 s60, 3244032\n\t" \
  "buffer_load_dwordx4 v71, %[Wv], s60 offen lds\n\t" \
  "v_add_u32 v78, 16384, v66\n\t" \
  "s_waitcnt vmcnt(12) lgkmcnt(0)\n\t" \
  "s_barrier\n\t" \
  "ds_read_b64_tr_b16 a[224:225], v78 offset:0\n\t" \
  "ds_read_b64_tr_b16 a[226:227], v78 offset:1024\n\t" \
  "ds_read_b64_tr_b16 a[228:229], v78 offset:8192\n\t" \
  "ds_read_b64_tr_b16 a[230:231], v78 offset:9216\n\t" \
  "ds_read_b64_tr_b16 a[232:233], v78 offset:32\n\t" \
  "ds_read_b64_tr_b16 a[234:235], v78 offset:1056\n\t" \
  "ds_read_b64_tr_b16 a[236:237], v78 offset:8224\n\t" \
  "ds_read_b64_tr_b16 a[238:239], v78 offset:9248\n\t" \
  "s_add_u32 m0, s54, 0\n\t" \
  "s_mov_b32 s60, 3670016\n\t" \
  "buffer_load_dwordx4 v71, %[Wq], s60 offen lds\n\t" \
  "s_add_u32 m0, s54, 1024\n\t" \
  "s_mov_b32 s60, 3702784\n\t" \
  "buffer_load_dwordx4 v71, %[Wq], s60 offen lds\n\t" \
  "s_add_u32 m0, s54, 2048\n\t" \
  "s_mov_b32 s60, 3735552\n\t" \
  "buffer_load_dwordx4 v71, %[Wq], s60 offen lds\n\t" \
  "s_add_u32 m0, s54, 3072\n\t" \
  "s_mov_b32 s60, 3768320\n\t" \
  "buffer_load_dwordx4 v71, %[Wq], s60 offen lds\n\t" \
  "v_add_u32 v78, 53248, v65\n\t" \
  "s_waitcnt vmcnt(12) lgkmcnt(0)\n\t" \
  "s_barrier\n\t" \
  "ds_read_b64_tr_b16 a[240:241], v78 offset:0\n\t" \
  "ds_read_b64_tr_b16 a[242:243], v78 offset:1024\n\t" \
  "ds_read_b64_tr_b16 a[244:245], v78 offset:8192\n\t" \
  "ds_read_b64_tr_b16 a[246:247], v78 offset:9216\n\t" \
  "ds_read_b64_tr_b16 a[248:249], v78 offset:8\n\t" \
  "ds_read_b64_tr_b16 a[250:251], v78 offset:1032\n\t" \
  "ds_read_b64_tr_b16 a[252:253], v78 offset:8200\n\t" \
  "ds_read_b64_tr_b16 a[254:255], v78 offset:9224\n\t" \
  "s_add_u32 m0, s54, 16384\n\t" \
  "s_mov_b32 s60, 3670016\n\t" \
  "buffer_load_dwordx4 v71, %[Wk], s60 offen lds\n\t" \
  "s_add_u32 m0, s54, 17408\n\t" \
  "s_mov_b32 s60, 3702784\n\t" \
  "buffer_load_dwordx4 v71, %[Wk], s60 offen lds\n\t" \
  "s_add_u32 m0, s54, 18432\n\t" \
  "s_mov_b32 s60, 3735552\n\t" \
  "buffer_load_dwordx4 v71, %[Wk], s60 offen lds\n\t" \
  "s_add_u32 m0, s54, 19456\n\t" \
  "s_mov_b32 s60, 3768320\n\t" \
  "buffer_load_dwordx4 v71, %[Wk], s60 offen lds\n\t" \
  "v_add_u32 v78, 90112, v65\n\t" \
  "s_waitcnt vmcnt(12) lgkmcnt(0)\n\t" \
  "s_barrier\n\t" \
  "ds_read_b64_tr_b16 v[80:81], v78 offset:0\n\t" \
  "ds_read_b64_tr_b16 v[82:83], v78 offset:1024\n\t" \
  "ds_read_b64_tr_b16 v[84:85], v78 offset:8192\n\t" \
  "ds_read_b64_tr_b16 v[86:87], v78 offset:9216\n\t" \
  "ds_read_b64_tr_b16 v[88:89], v78 offset:8\n\t" \
  "ds_read_b64_tr_b16 v[90:91], v78 offset:1032\n\t" \
  "ds_read_b64_tr_b16 v[92:93], v78 offset:8200\n\t" \
  "ds_read_b64_tr_b16 v[94:95], v78 offset:9224\n\t" \
  "s_add_u32 m0, s54, 53248\n\t" \
  "s_mov_b32 s60, 3670016\n\t" \
  "buffer_load_dwordx4 v71, %[Wv], s60 offen lds\n\t" \
  "s_add_u32 m0, s54, 54272\n\t" \
  "s_mov_b32 s60, 3702784\n\t" \
  "buffer_load_dwordx4 v71, %[Wv], s60 offen lds\n\t" \
  "s_add_u32 m0, s54, 55296\n\t" \
  "s_mov_b32 s60, 3735552\n\t" \
  "buffer_load_dwordx4 v71, %[Wv], s60 offen lds\n\t" \
  "s_add_u32 m0, s54, 56320\n\t" \
  "s_mov_b32 s60, 3768320\n\t" \
  "buffer_load_dwordx4 v71, %[Wv], s60 offen lds\n\t" \
  "v_add_u32 v78, 126976, v66\n\t" \
  "s_waitcnt vmcnt(12) lgkmcnt(0)\n\t" \
  "s_barrier\n\t" \
  "ds_read_b64_tr_b16 v[96:97], v78 offset:0\n\t" \
  "ds_read_b64_tr_b16 v[98:99], v78 offset:1024\n\t" \
  "ds_read_b64_tr_b16 v[100:101], v78 offset:8192\n\t" \
  "ds_read_b64_tr_b16 v[102:103], v78 offset:9216\n\t" \
  "ds_read_b64_tr_b16 v[104:105], v78 offset:32\n\t" \
  "ds_read_b64_tr_b16 v[106:107], v78 offset:1056\n\t" \
  "ds_read_b64_tr_b16 v[108:109], v78 offset:8224\n\t" \
  "ds_read_b64_tr_b16 v[110:111], v78 offset:9248\n\t" \
  "s_add_u32 m0, s54, 90112\n\t" \
  "s_mov_b32 s60, 4194304\n\t" \
  "buffer_load_dwordx4 v71, %[Wq], s60 offen lds\n\t" \
  "s_add_u32 m0, s54, 91136\n\t" \
  "s_mov_b32 s60, 4227072\n\t" \
  "buffer_load_dwordx4 v71, %[Wq], s60 offen lds\n\t" \
  "s_add_u32 m0, s54, 92160\n\t" \
  "s_mov_b32 s60, 4259840\n\t" \
  "buffer_load_dwordx4 v71, %[Wq], s60 offen lds\n\t" \
  "s_add_u32 m0, s54, 93184\n\t" \
  "s_mov_b32 s60, 4292608\n\t" \
  "buffer_load_dwordx4 v71, %[Wq], s60 offen lds\n\t" \
  "v_add_u32 v78, 0, v65\n\t" \
  "s_waitcnt vmcnt(12) lgkmcnt(0)\n\t" \
  "s_barrier\n\t" \
  "ds_read_b64_tr_b16 v[112:113], v78 offset:0\n\t" \
  "ds_read_b64_tr_b16 v[114:115], v78 offset:1024\n\t" \
  "ds_read_b64_tr_b16 v[116:117], v78 offset:8192\n\t" \
  "ds_read_b64_tr_b16 v[118:119], v78 offset:9216\n\t" \
  "ds_read_b64_tr_b16 v[120:121], v78 offset:8\n\t" \
  "ds_read_b64_tr_b16 v[122:123], v78 offset:1032\n\t" \
  "ds_read_b64_tr_b16 v[124:125], v78 offset:8200\n\t" \
  "ds_read_b64_tr_b16 v[126:127], v78 offset:9224\n\t" \
  "s_add_u32 m0, s54, 126976\n\t" \
  "s_mov_b32 s60, 4194304\n\t" \
  "buffer_load_dwordx4 v71, %[Wk], s60 offen lds\n\t" \
  "s_add_u32 m0, s54, 128000\n\t" \
  "s_mov_b32 s60, 4227072\n\t" \
  "buffer_load_dwordx4 v71, %[Wk], s60 offen lds\n\t" \
  "s_add_u32 m0, s54, 129024\n\t" \
  "s_mov_b32 s60, 4259840\n\t" \
  "buffer_load_dwordx4 v71, %[Wk], s60 offen lds\n\t" \
  "s_add_u32 m0, s54, 130048\n\t" \
  "s_mov_b32 s60, 4292608\n\t" \
  "buffer_load_dwordx4 v71, %[Wk], s60 offen lds\n\t" \
  "v_add_u32 v78, 16384, v65\n\t" \
  "s_waitcnt vmcnt(12) lgkmcnt(0)\n\t" \
  "s_barrier\n\t" \
  "ds_read_b64_tr_b16 v[128:129], v78 offset:0\n\t" \
  "ds_read_b64_tr_b16 v[130:131], v78 offset:1024\n\t" \
  "ds_read_b64_tr_b16 v[132:133], v78 offset:8192\n\t" \
  "ds_read_b64_tr_b16 v[134:135], v78 offset:9216\n\t" \
  "ds_read_b64_tr_b16 v[136:137], v78 offset:8\n\t" \
  "ds_read_b64_tr_b16 v[138:139], v78 offset:1032\n\t" \
  "ds_read_b64_tr_b16 v[140:141], v78 offset:8200\n\t" \
  "ds_read_b64_tr_b16 v[142:143], v78 offset:9224\n\t" \
  "s_add_u32 m0, s54, 0\n\t" \
  "s_mov_b32 s60, 4194304\n\t" \
  "buffer_load_dwordx4 v71, %[Wv], s60 offen lds\n\t" \
  "s_add_u32 m0, s54, 1024\n\t" \
  "s_mov_b32 s60, 4227072\n\t" \
  "buffer_load_dwordx4 v71, %[Wv], s60 offen lds\n\t" \
  "s_add_u32 m0, s54, 2048\n\t" \
  "s_mov_b32 s60, 4259840\n\t" \
  "buffer_load_dwordx4 v71, %[Wv], s60 offen lds\n\t" \
  "s_add_u32 m0, s54, 3072\n\t" \
  "s_mov_b32 s60, 4292608\n\t" \
  "buffer_load_dwordx4 v71, %[Wv], s60 offen lds\n\t" \
  "v_add_u32 v78, 53248, v66\n\t" \
  "s_waitcnt vmcnt(12) lgkmcnt(0)\n\t" \
  "s_barrier\n\t" \
  "ds_read_b64_tr_b16 v[144:145], v78 offset:0\n\t" \
  "ds_read_b64_tr_b16 v[146:147], v78 offset:1024\n\t" \
  "ds_read_b64_tr_b16 v[148:149], v78 offset:8192\n\t" \
  "ds_read_b64_tr_b16 v[150:151], v78 offset:9216\n\t" \
  "ds_read_b64_tr_b16 v[152:153], v78 offset:32\n\t" \
  "ds_read_b64_tr_b16 v[154:155], v78 offset:1056\n\t" \
  "ds_read_b64_tr_b16 v[156:157], v78 offset:8224\n\t" \
  "ds_read_b64_tr_b16 v[158:159], v78 offset:9248\n\t" \
  "v_add_u32 v78, 90112, v65\n\t" \
  "s_waitcnt vmcnt(8) lgkmcnt(0)\n\t" \
  "s_barrier\n\t" \
  "ds_read_b64_tr_b16 v[160:161], v78 offset:0\n\t" \
  "ds_read_b64_tr_b16 v[162:163], v78 offset:1024\n\t" \
  "ds_read_b64_tr_b16 v[164:165], v78 offset:8192\n\t" \
  "ds_read_b64_tr_b16 v[166:167], v78 offset:9216\n\t" \
  "ds_read_b64_tr_b16 v[168:169], v78 offset:8\n\t" \
  "ds_read_b64_tr_b16 v[170:171], v78 offset:1032\n\t" \
  "ds_read_b64_tr_b16 v[172:173], v78 offset:8200\n\t" \
  "ds_read_b64_tr_b16 v[174:175], v78 offset:9224\n\t" \
  "v_add_u32 v78, 126976, v65\n\t" \
  "s_waitcnt vmcnt(4) lgkmcnt(0)\n\t" \
  "s_barrier\n\t" \
  "ds_read_b64_tr_b16 v[40:41], v78 offset:0\n\t" \
  "ds_read_b64_tr_b16 v[42:43], v78 offset:1024\n\t" \
  "ds_read_b64_tr_b16 v[44:45], v78 offset:8192\n\t" \
  "ds_read_b64_tr_b16 v[46:47], v78 offset:9216\n\t" \
  "ds_read_b64_tr_b16 v[176:177], v78 offset:8\n\t" \
  "ds_read_b64_tr_b16 v[178:179], v78 offset:1032\n\t" \
  "ds_read_b64_tr_b16 v[180:181], v78 offset:8200\n\t" \
  "ds_read_b64_tr_b16 v[182:183], v78 offset:9224\n\t" \
  "v_add_u32 v78, 0, v66\n\t" \
  "s_waitcnt vmcnt(0) lgkmcnt(0)\n\t" \
  "s_barrier\n\t" \
  "ds_read_b64_tr_b16 v[184:185], v78 offset:0\n\t" \
  "ds_read_b64_tr_b16 v[186:187], v78 offset:1024\n\t" \
  "ds_read_b64_tr_b16 v[188:189], v78 offset:8192\n\t" \
  "ds_read_b64_tr_b16 v[190:191], v78 offset:9216\n\t" \
  "ds_read_b64_tr_b16 v[56:57], v78 offset:32\n\t" \
  "ds_read_b64_tr_b16 v[58:59], v78 offset:1056\n\t" \
  "ds_read_b64_tr_b16 v[60:61], v78 offset:8224\n\t" \
  "ds_read_b64_tr_b16 v[62:63], v78 offset:9248\n\t" \
  "s_waitcnt lgkmcnt(0)\n\t" \
  "s_barrier\n\t" \
  "ds_write_b128 v70, v[0:3] offset:0\n\t" \
  "ds_write_b128 v70, v[4:7] offset:1024\n\t" \
  "ds_write_b128 v70, v[8:11] offset:2048\n\t" \
  "ds_write_b128 v70, v[12:15] offset:3072\n\t" \
  "ds_write_b128 v70, v[16:19] offset:4096\n\t" \
  "ds_write_b128 v70, v[20:23] offset:5120\n\t" \
  "ds_write_b128 v70, v[24:27] offset:6144\n\t" \
  "ds_write_b128 v70, v[28:31] offset:7168\n\t" \
  "ds_write_b128 v70, v[32:35] offset:8192\n\t" \
  "ds_write_b128 v70, v[36:39] offset:9216\n\t" \
  "ds_write_b128 v70, v[40:43] offset:10240\n\t" \
  "ds_write_b128 v70, v[44:47] offset:11264\n\t" \
  "ds_write_b128 v70, v[48:51] offset:12288\n\t" \
  "ds_write_b128 v70, v[52:55] offset:13312\n\t" \
  "ds_write_b128 v70, v[56:59] offset:14336\n\t" \
  "ds_write_b128 v70, v[60:63] offset:15360\n\t" \
  "s_waitcnt lgkmcnt(0)\n\t" \
  "v_and_b32 v72, 1, v72\n\t" \
  "v_and_b32 v74, 1, v74\n\t" \
  "v_cmp_ne_u32 s[90:91], s32, v72\n\t" \
  "v_cmp_ne_u32 vcc, s32, v74\n\t" \
  "s_nop 1\n\t" \
  "s_mov_b32 s96, vcc_lo\n\t" \
  "v_and_b32 v73, 32, v69\n\t" \
  "v_lshl_add_u32 v73, v73, 5, v69\n\t" \
  "s_mov_b32 s35, s30\n\t" \
  "s_cmp_lt_u32 s32, " CH_DRYL "\n\t" \
  "s_cbranch_scc1 LINI_%=\n\t" \
  "s_mov_b32 s30, 0\n\t" \
  "s_mov_b32 s34, 0\n\t" \
  "s_cmp_eq_u32 s40, 0\n\t" \
  "s_cbranch_scc1 LW0_%=\n\t" \
  "s_barrier\n\t" \
  "s_cmp_eq_u32 s40, 1\n\t" \
  "s_cbranch_scc1 LG1_%=\n\t" \
  "s_cmp_eq_u32 s40, 2\n\t" \
  "s_cbranch_scc1 LG2_%=\n\t" \
  "s_branch LG3_%=\n\t" \
  "LINI_%=:\n\t" \
  "s_waitcnt vmcnt(0) lgkmcnt(0)\n\t" \
  "s_mov_b32 s30, s35\n\t" \
  "s_movk_i32 s34, 1000\n\t" \
  "s_mov_b32 s48, -1\n\t" \
  "s_add_u32 s97, s41, s53\n\t" \
  "s_add_u32 s98, s37, s53\n\t" \
  "s_add_u32 s82, s48, 0\n\t" \
  "s_lshr_b64 s[60:61], s[90:91], s82\n\t" \
  "s_and_b32 s60, s60, 1\n\t" \
  "s_lshl_b32 s60, s60, 13\n\t" \
  "s_add_u32 s82, s82, " CH_SG0 "\n\t" \
  "s_lshl_b32 s82, s82, 21\n\t" \
  "s_or_b32 s82, s82, s60\n\t" \
  "s_add_u32 s50, s82, s38\n\t" \
  "s_add_u32 s69, s82, s39\n\t" \
  "s_add_u32 s82, s48, 1\n\t" \
  "s_lshr_b64 s[60:61], s[90:91], s82\n\t" \
  "s_and_b32 s60, s60, 1\n\t" \
  "s_lshl_b32 s60, s60, 13\n\t" \
  "s_add_u32 s82, s82, " CH_SG0 "\n\t" \
  "s_lshl_b32 s82, s82, 21\n\t" \
  "s_or_b32 s82, s82, s60\n\t" \
  "s_add_u32 s52, s82, s43\n\t" \
  "s_add_u32 s82, s48, " CH_ARMD "\n\t" \
  "s_lshr_b64 s[60:61], s[90:91], s82\n\t" \
  "s_and_b32 s60, s60, 1\n\t" \
  "s_lshl_b32 s60, s60, 13\n\t" \
  "s_add_u32 s82, s82, " CH_SG0 "\n\t" \
  "s_lshl_b32 s82, s82, 21\n\t" \
  "s_or_b32 s82, s82, s60\n\t" \
  "s_add_u32 s85, s82, s45\n\t" \
  "s_cmp_lt_i32 s48, " CH_ARMS "\n\t" \
  "s_cselect_b32 s85, s27, s85\n\t" \
  "v_and_b32 v37, 0x30, v69\n\t" \
  "v_add_u32 v37, s52, v37\n\t" \
  "s_mov_b32 s48, 0\n\t" \
  "s_mov_b32 s86, 1\n\t" \
  "s_and_b32 s49, s96, 1\n\t" \
  "s_lshl_b32 s49, s49, 13\n\t" \
  "s_add_u32 s53, s49, " CH_SLOT "\n\t" \
  "s_movk_i32 s60, 124\n\t" \
  "s_cmp_eq_u32 s40, 3\n\t" \
  "s_cselect_b32 s81, 76, s60\n\t" \
  "s_add_u32 s82, s48, 0\n\t" \
  "s_lshr_b64 s[60:61], s[90:91], s82\n\t" \
  "s_and_b32 s60, s60, 1\n\t" \
  "s_lshl_b32 s60, s60, 13\n\t" \
  "s_add_u32 s82, s82, " CH_SG0 "\n\t" \
  "s_lshl_b32 s82, s82, 21\n\t" \
  "s_or_b32 s82, s82, s60\n\t" \
  "s_add_u32 s50, s82, s38\n\t" \
  "s_add_u32 s69, s82, s39\n\t" \
  "s_add_u32 s51, s82, s43\n\t" \
  "s_add_u32 s97, s41, s53\n\t" \
  "s_add_u32 s98, s37, s53\n\t" \
  "s_add_u32 s82, s48, 0\n\t" \
  "s_lshr_b64 s[60:61], s[90:91], s82\n\t" \
  "s_and_b32 s60, s60, 1\n\t" \
  "s_lshl_b32 s60, s60, 13\n\t" \
  "s_add_u32 s82, s82, " CH_SG0 "\n\t" \
  "s_lshl_b32 s82, s82, 21\n\t" \
  "s_or_b32 s82, s82, s60\n\t" \
  "s_add_u32 s50, s82, s38\n\t" \
  "s_add_u32 s69, s82, s39\n\t" \
  "s_add_u32 s82, s48, 1\n\t" \
  "s_lshr_b64 s[60:61], s[90:91], s82\n\t" \
  "s_and_b32 s60, s60, 1\n\t" \
  "s_lshl_b32 s60, s60, 13\n\t" \
  "s_add_u32 s82, s82, " CH_SG0 "\n\t" \
  "s_lshl_b32 s82, s82, 21\n\t" \
  "s_or_b32 s82, s82, s60\n\t" \
  "s_add_u32 s52, s82, s43\n\t" \
  "s_add_u32 s82, s48, " CH_ARMD "\n\t" \
  "s_lshr_b64 s[60:61], s[90:91], s82\n\t" \
  "s_and_b32 s60, s60, 1\n\t" \
  "s_lshl_b32 s60, s60, 13\n\t" \
  "s_add_u32 s82, s82, " CH_SG0 "\n\t" \
  "s_lshl_b32 s82, s82, 21\n\t" \
  "s_or_b32 s82, s82, s60\n\t" \
  "s_add_u32 s85, s82, s45\n\t" \
  "s_cmp_lt_i32 s48, " CH_ARMS "\n\t" \
  "s_cselect_b32 s85, s27, s85\n\t" \
  "s_memrealtime s[62:63]\n\t" \
  "s_waitcnt lgkmcnt(0)\n\t" \
  "s_mov_b32 s65, s62\n\t" \
  "s_sub_u32 s47, s62, 1000\n\t" \
  "s_mov_b64 vcc, -1\n\t" \
  "s_cmp_eq_u32 s40, 0\n\t" \
  "s_cbranch_scc1 LW0_%=\n\t" \
  "s_barrier\n\t" \
  "s_cmp_eq_u32 s40, 1\n\t" \
  "s_cbranch_scc1 LG1_%=\n\t" \
  "s_cmp_eq_u32 s40, 2\n\t" \
  "s_cbranch_scc1 LG2_%=\n\t" \
  "s_branch LTK_%=\n\t" \
  ".p2align 6\n\t" \
  "LW0_%=:\n\t" \
  "v_lshlrev_b32 v78, 2, v69\n\t" \
  "v_and_b32 v78, 0x300, v78\n\t" \
  "v_lshrrev_b32 v79, 8, v69\n\t" \
  "v_lshl_add_u32 v78, v79, 6, v78\n\t" \
  "v_and_b32 v79, 48, v69\n\t" \
  "v_add_u32 v78, v78, v79\n\t" \
  "s_add_u32 s98, s49, s42\n\t" \
  "s_add_u32 s97, s98, 32768\n\t" \
  "s_add_u32 s99, s98, 65536\n\t" \
  "s_barrier\n\t" \
  "s_memrealtime s[62:63]\n\t" \
  "s_waitcnt lgkmcnt(0)\n\t" \
  "s_add_u32 s62, s62, " CH_DLL2 "\n\t" \
  "s_sub_i32 s63, s62, s65\n\t" \
  "s_cmp_gt_i32 s63, " CH_DLT "\n\t" \
  "s_cselect_b32 s60, " CH_DLY ", " CH_DLX "\n\t" \
  "s_add_u32 s62, s62, s60\n\t" \
  "s_cmp_gt_i32 s63, 0\n\t" \
  "s_cselect_b32 s65, s62, s65\n\t" \
  "s_mov_b32 m0, 0\n\t" \
  "s_movk_i32 s60, 8192\n\t" \
  "LWT_%=:\n\t" \
  "s_memrealtime s[62:63]\n\t" \
  "s_sub_u32 s60, s60, 1\n\t" \
  "s_cselect_b32 s61, 0, -1\n\t" \
  "s_waitcnt lgkmcnt(0)\n\t" \
  "s_sub_i32 s63, s62, s65\n\t" \
  "s_and_b32 s63, s63, s61\n\t" \
  "s_cmp_lt_i32 s63, " CH_FIN "\n\t" \
  "s_cbranch_scc1 LWT_%=\n\t" \
  "s_cmp_lt_i32 s63, 0\n\t" \
  "s_cbranch_scc1 LWT_%=f\n\t" \
  "LWT_%=g:\n\t" \
  "LDMA_%=:\n\t" \
  "buffer_load_dwordx4 v78, s[28:31], s98 offen offset:0 sc1 lds\n\t" \
  "buffer_load_dwordx4 v78, s[28:31], s98 offen offset:1024 sc1 lds\n\t" \
  "buffer_load_dwordx4 v78, s[28:31], s98 offen offset:2048 sc1 lds\n\t" \
  "buffer_load_dwordx4 v78, s[28:31], s98 offen offset:3072 sc1 lds\n\t" \
  "s_movk_i32 m0, 4096\n\t" \
  "s_mov_b32 s66, s62\n\t" \
  "buffer_load_dwordx4 v78, s[28:31], s97 offen offset:0 sc1 lds\n\t" \
  "buffer_load_dwordx4 v78, s[28:31], s97 offen offset:1024 sc1 lds\n\t" \
  "buffer_load_dwordx4 v78, s[28:31], s97 offen offset:2048 sc1 lds\n\t" \
  "buffer_load_dwordx4 v78, s[28:31], s97 offen offset:3072 sc1 lds\n\t" \
  "s_movk_i32 m0, 8192\n\t" \
  "s_nop 0\n\t" \
  "buffer_load_dwordx4 v69, s[28:31], s99 offen sc1 lds\n\t" \
  ".p2align 6\n\t" \
  "LG0_%=:\n\t" \
  "s_barrier\n\t" \
  "buffer_load_dwordx4 v[52:55], v69, s[28:31], s50 offen sc1\n\t" \
  "buffer_load_dwordx4 v[56:59], v69, s[28:31], s50 offen offset:1024 sc1\n\t" \
  "s_add_u32 s64, s50, 16384\n\t" \
  "s_movk_i32 m0, 11264\n\t" \
  "v_lshlrev_b32 v75, 1, v69\n\t" \
  "buffer_load_dword v75, s[28:31], s64 offen sc1 lds\n\t" \
  "s_memrealtime s[62:63]\n\t" \
  "s_waitcnt lgkmcnt(0)\n\t" \
  "s_sub_u32 s62, s62, " CH_TKL "\n\t" \
  "s_sub_i32 s63, s62, s65\n\t" \
  "s_cmp_gt_i32 s63, 0\n\t" \
  "s_cselect_b32 s62, s62, s65\n\t" \
  "s_sub_i32 s63, s62, s47\n\t" \
  "s_cmp_gt_i32 s63, " CH_LATE "\n\t" \
  "s_cselect_b32 s71, " CH_SCHW ", s46\n\t" \
  "s_add_u32 s61, s47, " CH_LATE "\n\t" \
  "s_cmp_gt_i32 s63, " CH_LATE "\n\t" \
  "s_cselect_b32 s62, s62, s61\n\t" \
  "s_add_u32 s71, s71, s53\n\t" \
  "s_add_u32 s60, s62, s81\n\t" \
  "s_add_u32 s60, s60, " CH_DL1L "\n\t" \
  "s_lshl_b32 s60, s60, 8\n\t" \
  "s_or_b32 s60, s60, " CH_B0 "\n\t" \
  "s_cmp_lt_u32 s48, " CH_ES "\n\t" \
  "s_cselect_b32 s61, " CH_EX ", 0\n\t" \
  "s_add_u32 s60, s60, s61\n\t" \
  "s_not_b32 s60, s60\n\t" \
  "v_mov_b32 v76, s60\n\t" \
  "s_lshr_b32 s60, s46, 31\n\t" \
  "s_or_b32 s60, s60, s68\n\t" \
  "s_cselect_b32 s25, 0, 1\n\t" \
  "s_mov_b64 exec, s[24:25]\n\t" \
  "buffer_atomic_umin v76, v69, s[28:31], s71 offen sc1\n\t" \
  "s_mov_b64 exec, -1\n\t" \
  "ds_read_b128 v[44:47], v70 offset:0\n\t" \
  "ds_read_b128 v[48:51], v70 offset:1024\n\t" \
  "ds_read_b128 v[60:63], v70 offset:2048\n\t" \
  "ds_read_b128 v[64:67], v70 offset:3072\n\t" \
  "v_mov_b64 v[36:37], 0\n\t" \
  "v_mov_b64 v[38:39], 0\n\t" \
  "v_mov_b64 v[40:41], 0\n\t" \
  "v_mov_b64 v[42:43], 0\n\t" \
  "v_lshrrev_b32 v71, 1, v69\n\t" \
  "v_lshrrev_b32 v72, 3, v69\n\t" \
  "v_and_b32 v72, 0x78, v72\n\t" \
  "v_and_b32 v74, 0x30, v69\n\t" \
  "v_lshl_add_u32 v72, v74, 3, v72\n\t" \
  "s_add_u32 s54, s53, s36\n\t" \
  "s_add_u32 s55, s54, 524288\n\t" \
  "s_add_u32 s54, s54, 0\n\t" \
  "s_add_u32 s75, s54, 262144\n\t" \
  "s_add_u32 s80, s53, s26\n\t" \
  "s_or_b32 s87, s53, 0x1fff80\n\t" \
  "s_add_u32 s87, s87, 2097152\n\t" \
  "s_waitcnt vmcnt(9)\n\t" \
  "s_barrier\n\t" \
  "ds_read_b128 v[0:3], v69 offset:0\n\t" \
  "ds_read_b128 v[4:7], v69 offset:1024\n\t" \
  "ds_read_b128 v[8:11], v69 offset:2048\n\t" \
  "ds_read_b128 v[12:15], v69 offset:3072\n\t" \
  "s_waitcnt lgkmcnt(3)\n\t" \
  "v_smfmac_f32_16x16x64_bf16 v[36:39], v[0:3], v[44:51], v68\n\t" \
  "ds_read_b128 v[44:47], v70 offset:4096\n\t" \
  "v_smfmac_f32_16x16x64_bf16 v[40:43], v[0:3], v[60:67], v68\n\t" \
  "ds_read_b128 v[48:51], v70 offset:5120\n\t" \
  "s_waitcnt lgkmcnt(4)\n\t" \
  "v_smfmac_f32_16x16x64_bf16 v[36:39], v[4:7], a[32:39], v68\n\t" \
  "ds_read_b128 v[60:63], v70 offset:6144\n\t" \
  "v_smfmac_f32_16x16x64_bf16 v[40:43], v[4:7], a[40:47], v68\n\t" \
  "ds_read_b128 v[64:67], v70 offset:7168\n\t" \
  "s_waitcnt lgkmcnt(5)\n\t" \
  "v_smfmac_f32_16x16x64_bf16 v[36:39], v[8:11], a[80:87], v68\n\t" \
  "s_waitcnt vmcnt(4)\n\t" \
  "s_barrier\n\t" \
  "ds_read_b128 v[16:19], v69 offset:4096\n\t" \
  "v_smfmac_f32_16x16x64_bf16 v[40:43], v[8:11], a[88:95], v68\n\t" \
  "ds_read_b128 v[20:23], v69 offset:5120\n\t" \
  "s_waitcnt lgkmcnt(6)\n\t" \
  "v_smfmac_f32_16x16x64_bf16 v[36:39], v[12:15], a[120:127], v68\n\t" \
  "ds_read_b128 v[24:27], v69 offset:6144\n\t" \
  "s_waitcnt lgkmcnt(5)\n\t" \
  "v_smfmac_f32_16x16x64_bf16 v[40:43], v[12:15], v[44:51], v68\n\t" \
  "ds_read_b128 v[28:31], v69 offset:7168\n\t" \
  "s_waitcnt lgkmcnt(3)\n\t" \
  "v_smfmac_f32_16x16x64_bf16 v[36:39], v[16:19], a[160:167], v68\n\t" \
  "ds_read_b128 v[32:35], v69 offset:8192\n\t" \
  "v_smfmac_f32_16x16x64_bf16 v[40:43], v[16:19], a[168:175], v68\n\t" \
  "s_waitcnt lgkmcnt(3)\n\t" \
  "v_mov_b64 v[44:45], 0\n\t" \
  "v_smfmac_f32_16x16x64_bf16 v[36:39], v[20:23], a[200:207], v68\n\t" \
  "s_waitcnt lgkmcnt(2)\n\t" \
  "v_mov_b64 v[46:47], 0\n\t" \
  "v_smfmac_f32_16x16x64_bf16 v[36:39], v[24:27], a[240:247], v68\n\t" \
  "s_waitcnt lgkmcnt(1)\n\t" \
  "v_mov_b64 v[48:49], 0\n\t" \
  "v_smfmac_f32_16x16x64_bf16 v[36:39], v[28:31], v[112:119], v68\n\t" \
  "s_waitcnt lgkmcnt(0)\n\t" \
  "v_mov_b64 v[50:51], 0\n\t" \
  "v_smfmac_f32_16x16x64_bf16 v[36:39], v[32:35], v[160:167], v68\n\t" \
  "v_smfmac_f32_16x16x64_bf16 v[40:43], v[20:23], v[60:67], v68\n\t" \
  "ds_read_b128 v[60:63], v70 offset:8192\n\t" \
  "v_smfmac_f32_16x16x64_bf16 v[40:43], v[24:27], a[248:255], v68\n\t" \
  "ds_read_b128 v[64:67], v70 offset:9216\n\t" \
  "v_add_f32 v74, v36, v38\n\t" \
  "v_smfmac_f32_16x16x64_bf16 v[40:43], v[28:31], v[120:127], v68\n\t" \
  "v_add_f32 v37, v37, v39\n\t" \
  "v_smfmac_f32_16x16x64_bf16 v[40:43], v[32:35], v[168:175], v68\n\t" \
  "v_cmp_u_f32 vcc, v74, v37\n\t" \
  "v_smfmac_f32_16x16x64_bf16 v[44:47], v[0:3], a[0:7], v68\n\t" \
  "s_cbranch_vccnz LFAIL0_%=\n\t" \
  "LGO0_%=:\n\t" \
  "v_cvt_pk_bf16_f32 v74, v74, v37\n\t" \
  "v_smfmac_f32_16x16x64_bf16 v[44:47], v[4:7], a[48:55], v68\n\t" \
  "s_waitcnt lgkmcnt(0)\n\t" \
  "v_add_f32 v75, v40, v42\n\t" \
  "v_smfmac_f32_16x16x64_bf16 v[44:47], v[8:11], v[60:67], v68\n\t" \
  "ds_read_b128 v[60:63], v70 offset:10240\n\t" \
  "v_add_f32 v41, v41, v43\n\t" \
  "v_cvt_pk_bf16_f32 v75, v75, v41\n\t" \
  "v_smfmac_f32_16x16x64_bf16 v[44:47], v[12:15], a[128:135], v68\n\t" \
  "ds_write2st64_b32 v71, v74, v75 offset0:36 offset1:38\n\t" \
  "v_smfmac_f32_16x16x64_bf16 v[44:47], v[16:19], a[176:183], v68\n\t" \
  "ds_read_b128 v[64:67], v70 offset:11264\n\t" \
  "v_smfmac_f32_16x16x64_bf16 v[44:47], v[20:23], a[208:215], v68\n\t" \
  "v_smfmac_f32_16x16x64_bf16 v[44:47], v[24:27], v[80:87], v68\n\t" \
  "v_smfmac_f32_16x16x64_bf16 v[44:47], v[28:31], v[128:135], v68\n\t" \
  "s_waitcnt lgkmcnt(0)\n\t" \
  "v_smfmac_f32_16x16x64_bf16 v[44:47], v[32:35], v[60:67], v68\n\t" \
  "buffer_load_dwordx4 v[60:63], v69, s[28:31], s64 offen sc1\n\t" \
  "s_barrier\n\t" \
  "ds_read_b64_tr_b16 v[76:77], v72 offset:9216\n\t" \
  "v_smfmac_f32_16x16x64_bf16 v[48:51], v[0:3], a[8:15], v68\n\t" \
  "ds_read_b64_tr_b16 v[78:79], v72 offset:9728\n\t" \
  "v_smfmac_f32_16x16x64_bf16 v[48:51], v[4:7], a[56:63], v68\n\t" \
  "v_add_f32 v74, v44, v46\n\t" \
  "v_smfmac_f32_16x16x64_bf16 v[48:51], v[8:11], a[96:103], v68\n\t" \
  "v_add_f32 v45, v45, v47\n\t" \
  "v_smfmac_f32_16x16x64_bf16 v[48:51], v[12:15], a[136:143], v68\n\t" \
  "s_waitcnt vmcnt(4)\n\t" \
  "s_waitcnt lgkmcnt(0)\n\t" \
  "v_cvt_pk_bf16_f32 v74, v74, v45\n\t" \
  "v_mfma_f32_16x16x32_bf16 v[36:39], v[52:55], v[76:79], 0\n\t" \
  "buffer_load_dwordx4 v[64:67], v69, s[28:31], s64 offen offset:1024 sc1\n\t" \
  "s_waitcnt vmcnt(4)\n\t" \
  "v_mfma_f32_16x16x32_bf16 v[40:43], v[56:59], v[76:79], 0\n\t" \
  "v_mov_b64 v[52:53], 0\n\t" \
  "v_mov_b64 v[54:55], 0\n\t" \
  "v_smfmac_f32_16x16x64_bf16 v[48:51], v[16:19], a[184:191], v68\n\t" \
  "v_mov_b64 v[56:57], 0\n\t" \
  "v_mov_b64 v[58:59], 0\n\t" \
  "v_smfmac_f32_16x16x64_bf16 v[48:51], v[20:23], a[216:223], v68\n\t" \
  "ds_write_b32 v71, v74 offset:0\n\t" \
  "v_add_f32_dpp v36, v40, v36 quad_perm:[2,3,0,1] row_mask:0xf bank_mask:0xf\n\t" \
  "v_smfmac_f32_16x16x64_bf16 v[48:51], v[24:27], v[88:95], v68\n\t" \
  "v_add_f32_dpp v37, v41, v37 quad_perm:[2,3,0,1] row_mask:0xf bank_mask:0xf\n\t" \
  "v_smfmac_f32_16x16x64_bf16 v[48:51], v[28:31], v[136:143], v68\n\t" \
  "v_add_f32_dpp v38, v42, v38 quad_perm:[2,3,0,1] row_mask:0xf bank_mask:0xf\n\t" \
  "v_smfmac_f32_16x16x64_bf16 v[48:51], v[32:35], v[176:183], v68\n\t" \
  "v_add_f32_dpp v39, v43, v39 quad_perm:[2,3,0,1] row_mask:0xf bank_mask:0xf\n\t" \
  "v_smfmac_f32_16x16x64_bf16 v[52:55], v[0:3], a[16:23], v68\n\t" \
  "s_mov_b64 exec, s[58:59]\n\t" \
  "ds_write_b128 v69, v[36:39] offset:2048\n\t" \
  "s_mov_b64 exec, -1\n\t" \
  "v_smfmac_f32_16x16x64_bf16 v[52:55], v[4:7], a[64:71], v68\n\t" \
  "v_add_f32 v75, v48, v50\n\t" \
  "v_add_f32 v49, v49, v51\n\t" \
  "v_cvt_pk_bf16_f32 v75, v75, v49\n\t" \
  "v_smfmac_f32_16x16x64_bf16 v[56:59], v[0:3], a[24:31], v68\n\t" \
  "ds_write_b32 v71, v75 offset:512\n\t" \
  "v_smfmac_f32_16x16x64_bf16 v[56:59], v[4:7], a[72:79], v68\n\t" \
  "ds_read_b64_tr_b16 v[0:1], v72 offset:10240\n\t" \
  "ds_read_b64_tr_b16 v[2:3], v72 offset:10752\n\t" \
  "v_smfmac_f32_16x16x64_bf16 v[52:55], v[8:11], a[104:111], v68\n\t" \
  "ds_read_b128 v[36:39], v70 offset:12288\n\t" \
  "v_smfmac_f32_16x16x64_bf16 v[56:59], v[8:11], a[112:119], v68\n\t" \
  "buffer_load_dword off, s[28:31], s87 sc1 lds\n\t" \
  "ds_read_b128 v[40:43], v70 offset:13312\n\t" \
  "v_smfmac_f32_16x16x64_bf16 v[52:55], v[12:15], a[144:151], v68\n\t" \
  "s_waitcnt lgkmcnt(4)\n\t" \
  "s_barrier\n\t" \
  "ds_read_b64_tr_b16 v[44:45], v72 offset:0\n\t" \
  "ds_read_b64_tr_b16 v[46:47], v72 offset:512\n\t" \
  "v_smfmac_f32_16x16x64_bf16 v[56:59], v[12:15], a[152:159], v68\n\t" \
  "ds_read_b64_tr_b16 v[48:49], v72 offset:1024\n\t" \
  "ds_read_b64_tr_b16 v[50:51], v72 offset:1536\n\t" \
  "v_smfmac_f32_16x16x64_bf16 v[52:55], v[16:19], a[192:199], v68\n\t" \
  "ds_read_b128 v[4:7], v69 offset:2048\n\t" \
  "s_waitcnt lgkmcnt(5)\n\t" \
  "v_smfmac_f32_16x16x64_bf16 v[56:59], v[16:19], v[36:43], v68\n\t" \
  "ds_read_b128 v[8:11], v69 offset:3072\n\t" \
  "s_waitcnt lgkmcnt(4)\n\t" \
  "v_mfma_f32_16x16x32_bf16 v[12:15], v[44:47], v[76:79], 0\n\t" \
  "ds_read_b128 v[36:39], v70 offset:14336\n\t" \
  "s_waitcnt lgkmcnt(3)\n\t" \
  "v_mfma_f32_16x16x32_bf16 v[12:15], v[48:51], v[0:3], v[12:15]\n\t" \
  "ds_read_b128 v[40:43], v70 offset:15360\n\t" \
  "v_smfmac_f32_16x16x64_bf16 v[52:55], v[20:23], a[224:231], v68\n\t" \
  "s_waitcnt lgkmcnt(2)\n\t" \
  "v_add_f32 v4, v4, v8\n\t" \
  "v_add_f32 v5, v5, v9\n\t" \
  "v_add_f32 v6, v6, v10\n\t" \
  "v_cndmask_b32 v78, v13, v14, s[56:57]\n\t" \
  "v_cndmask_b32 v76, v12, v15, s[56:57]\n\t" \
  "v_add_f32 v7, v7, v11\n\t" \
  "v_max3_f32 v77, v4, v5, v6\n\t" \
  "v_add_f32_dpp v76, v78, v76 quad_perm:[2,3,0,1] row_mask:0xf bank_mask:0xf\n\t" \
  "v_max3_f32 v77, v77, v7, v76\n\t" \
  "v_smfmac_f32_16x16x64_bf16 v[56:59], v[20:23], a[232:239], v68\n\t" \
  "ds_read_b128 v[44:47], v70 offset:16384\n\t" \
  "v_max_f32_dpp v77, v77, v77 quad_perm:[2,3,0,1] row_mask:0xf bank_mask:0xf\n\t" \
  "v_mov_b32 v78, v77\n\t" \
  "v_smfmac_f32_16x16x64_bf16 v[52:55], v[24:27], v[96:103], v68\n\t" \
  "ds_read_b128 v[48:51], v70 offset:17408\n\t" \
  "v_permlane16_swap_b32 v77, v78\n\t" \
  "v_max_f32 v77, v77, v78\n\t" \
  "v_mov_b32 v78, v77\n\t" \
  "v_smfmac_f32_16x16x64_bf16 v[52:55], v[28:31], v[144:151], v68\n\t" \
  "ds_read_b128 v[16:19], v70 offset:18432\n\t" \
  "v_permlane32_swap_b32 v77, v78\n\t" \
  "v_max_f32 v77, v77, v78\n\t" \
  "v_smfmac_f32_16x16x64_bf16 v[52:55], v[32:35], v[184:191], v68\n\t" \
  "ds_read_b128 v[20:23], v70 offset:19456\n\t" \
  "v_mul_f32 v77, 0xbe0293ee, v77\n\t" \
  "v_smfmac_f32_16x16x64_bf16 v[56:59], v[24:27], v[104:111], v68\n\t" \
  "v_fmamk_f32 v4, v4, 0x3e0293ee, v77\n\t" \
  "v_smfmac_f32_16x16x64_bf16 v[56:59], v[28:31], v[152:159], v68\n\t" \
  "ds_read_b128 v[24:27], v70 offset:22528\n\t" \
  "s_waitcnt lgkmcnt(4)\n\t" \
  "v_fmamk_f32 v5, v5, 0x3e0293ee, v77\n\t" \
  "v_smfmac_f32_16x16x64_bf16 v[56:59], v[32:35], v[36:43], v68\n\t" \
  "ds_read_b128 v[28:31], v70 offset:23552\n\t" \
  "v_fmamk_f32 v6, v6, 0x3e0293ee, v77\n\t" \
  "v_fmamk_f32 v7, v7, 0x3e0293ee, v77\n\t" \
  "v_fmamk_f32 v76, v76, 0x3e0293ee, v77\n\t" \
  "v_exp_f32 v4, v4\n\t" \
  "v_exp_f32 v5, v5\n\t" \
  "v_exp_f32 v6, v6\n\t" \
  "v_exp_f32 v7, v7\n\t" \
  "v_cvt_pk_bf16_f32 v4, v4, v5\n\t" \
  "v_exp_f32 v76, v76\n\t" \
  "v_cvt_pk_bf16_f32 v5, v6, v7\n\t" \
  "v_mov_b32_dpp v12, v4 quad_perm:[0,1,0,1] row_mask:0xf bank_mask:0xf\n\t" \
  "v_mov_b32_dpp v14, v4 quad_perm:[2,3,2,3] row_mask:0xf bank_mask:0xf\n\t" \
  "v_mov_b32_dpp v13, v5 quad_perm:[0,1,0,1] row_mask:0xf bank_mask:0xf\n\t" \
  "v_mov_b32_dpp v15, v5 quad_perm:[2,3,2,3] row_mask:0xf bank_mask:0xf\n\t" \
  "v_add_f32 v52, v52, v54\n\t" \
  "v_add_f32 v53, v53, v55\n\t" \
  "s_waitcnt vmcnt(2)\n\t" \
  "v_mfma_f32_16x16x32_bf16 v[0:3], v[60:63], v[12:15], 0\n\t" \
  "ds_read_b128 v[36:39], v70 offset:20480\n\t" \
  "v_add_f32 v56, v56, v58\n\t" \
  "v_add_f32 v57, v57, v59\n\t" \
  "v_cvt_pk_bf16_f32 v52, v52, v53\n\t" \
  "v_cvt_pk_bf16_f32 v53, v56, v57\n\t" \
  "s_waitcnt vmcnt(1)\n\t" \
  "v_mfma_f32_16x16x32_bf16 v[4:7], v[64:67], v[12:15], 0\n\t" \
  "buffer_store_dwordx2 v[52:53], v71, s[28:31], s69 offen offset:2048 sc1\n\t" \
  "ds_read_b128 v[40:43], v70 offset:21504\n\t" \
  "v_mov_b64 v[56:57], s[88:89]\n\t" \
  "v_mov_b64 v[58:59], s[88:89]\n\t" \
  "v_mov_b32_dpp v78, v76 quad_perm:[0,1,0,1] row_mask:0xf bank_mask:0xf\n\t" \
  "v_mov_b32_dpp v79, v76 quad_perm:[2,3,2,3] row_mask:0xf bank_mask:0xf\n\t" \
  "v_mfma_f32_16x16x32_bf16 v[32:35], v[56:59], v[12:15], 0\n\t" \
  "ds_read_b128 v[60:63], v70 offset:24576\n\t" \
  "v_cvt_pk_bf16_f32 v11, v78, v79\n\t" \
  "v_cvt_pk_bf16_f32 v8, v78, v79\n\t" \
  "v_mov_b64 v[54:55], 0\n\t" \
  "v_mov_b32 v9, 0\n\t" \
  "v_mov_b32 v10, 0\n\t" \
  "v_mov_b64 v[14:15], 0\n\t" \
  "s_nop 0\n\t" \
  "v_mfma_f32_16x16x32_bf16 v[32:35], v[54:57], v[8:11], v[32:35]\n\t" \
  "s_cmp_eq_u32 s48, 63\n\t" \
  "v_mfma_f32_16x16x32_bf16 v[0:3], v[52:55], v[8:11], v[0:3]\n\t" \
  "ds_read_b128 v[64:67], v70 offset:25600\n\t" \
  "v_mov_b64 v[56:57], 0\n\t" \
  "v_mov_b64 v[58:59], 0\n\t" \
  "v_mfma_f32_16x16x32_bf16 v[4:7], v[52:55], v[10:13], v[4:7]\n\t" \
  "s_cbranch_scc1 LLAST0_%=\n\t" \
  "v_rcp_f32 v74, v32\n\t" \
  "v_mov_b64 v[12:13], 0\n\t" \
  "v_mov_b32 v8, 0\n\t" \
  "v_pk_mul_f32 v[0:1], v[0:1], v[74:75] op_sel_hi:[1,0]\n\t" \
  "v_pk_mul_f32 v[2:3], v[2:3], v[74:75] op_sel_hi:[1,0]\n\t" \
  "v_cvt_pk_bf16_f32 v76, v0, v1\n\t" \
  "v_cvt_pk_bf16_f32 v77, v2, v3\n\t" \
  "v_pk_mul_f32 v[4:5], v[4:5], v[74:75] op_sel_hi:[1,0]\n\t" \
  "v_pk_mul_f32 v[6:7], v[6:7], v[74:75] op_sel_hi:[1,0]\n\t" \
  "v_cvt_pk_bf16_f32 v78, v4, v5\n\t" \
  "v_cvt_pk_bf16_f32 v79, v6, v7\n\t" \
  "ds_write_b128 v69, v[76:79] offset:4096\n\t" \
  "s_waitcnt lgkmcnt(2)\n\t" \
  "v_smfmac_f32_16x16x64_bf16 v[12:15], v[76:79], v[44:51], v68\n\t" \
  "ds_read_b128 v[44:47], v70 offset:26624\n\t" \
  "v_mov_b32 v11, 0\n\t" \
  "v_smfmac_f32_16x16x64_bf16 v[56:59], v[76:79], v[16:23], v68\n\t" \
  "ds_read_b128 v[48:51], v70 offset:27648\n\t" \
  "v_mov_b64 v[52:53], 0\n\t" \
  "v_smfmac_f32_16x16x64_bf16 v[8:11], v[76:79], v[36:43], v68\n\t" \
  "ds_read_b128 v[16:19], v70 offset:28672\n\t" \
  "v_smfmac_f32_16x16x64_bf16 v[52:55], v[76:79], v[24:31], v68\n\t" \
  "s_waitcnt lgkmcnt(3)\n\t" \
  "s_barrier\n\t" \
  "ds_read_b128 v[0:3], v69 offset:5120\n\t" \
  "ds_read_b128 v[4:7], v69 offset:6144\n\t" \
  "v_lshrrev_b32 v71, 7, v69\n\t" \
  "v_and_b32 v74, 0x70, v69\n\t" \
  "v_lshl_add_u32 v71, v71, 14, v74\n\t" \
  "v_bfe_u32 v72, v69, 2, 7\n\t" \
  "v_and_b32 v74, 0x200, v69\n\t" \
  "s_waitcnt lgkmcnt(1)\n\t" \
  "v_smfmac_f32_16x16x64_bf16 v[8:11], v[0:3], v[60:67], v68\n\t" \
  "ds_read_b128 v[20:23], v70 offset:29696\n\t" \
  "v_mov_b64 v[76:77], 0\n\t" \
  "v_smfmac_f32_16x16x64_bf16 v[12:15], v[0:3], v[200:207], v68\n\t" \
  "ds_read_b128 v[32:35], v69 offset:7168\n\t" \
  "v_mov_b64 v[78:79], 0\n\t" \
  "v_smfmac_f32_16x16x64_bf16 v[52:55], v[0:3], v[44:51], v68\n\t" \
  "ds_read_b128 v[24:27], v70 offset:30720\n\t" \
  "v_lshl_add_u32 v72, v74, 3, v72\n\t" \
  "v_smfmac_f32_16x16x64_bf16 v[56:59], v[0:3], v[232:239], v68\n\t" \
  "buffer_load_dword v75, off, s[28:31], s80 sc1\n\t" \
  "ds_read_b128 v[28:31], v70 offset:31744\n\t" \
  "s_waitcnt lgkmcnt(4)\n\t" \
  "v_smfmac_f32_16x16x64_bf16 v[12:15], v[4:7], v[208:215], v68\n\t" \
  "ds_read_b128 v[60:63], v70 offset:32768\n\t" \
  "s_waitcnt lgkmcnt(4)\n\t" \
  "v_smfmac_f32_16x16x64_bf16 v[8:11], v[4:7], v[16:23], v68\n\t" \
  "ds_read_b128 v[64:67], v70 offset:33792\n\t" \
  "s_waitcnt lgkmcnt(4)\n\t" \
  "v_smfmac_f32_16x16x64_bf16 v[8:11], v[32:35], v[192:199], v68\n\t" \
  "ds_read_b128 v[36:39], v73 offset:4096\n\t" \
  "v_smfmac_f32_16x16x64_bf16 v[12:15], v[32:35], v[216:223], v68\n\t" \
  "ds_read_b128 v[40:43], v73 offset:6144\n\t" \
  "s_waitcnt lgkmcnt(4)\n\t" \
  "v_smfmac_f32_16x16x64_bf16 v[52:55], v[4:7], v[24:31], v68\n\t" \
  "ds_read_b128 v[44:47], v70 offset:34816\n\t" \
  "v_cvt_pk_bf16_f32 v8, v8, v9\n\t" \
  "v_smfmac_f32_16x16x64_bf16 v[52:55], v[32:35], v[224:231], v68\n\t" \
  "ds_read_b128 v[48:51], v70 offset:35840\n\t" \
  "v_cvt_pk_bf16_f32 v9, v10, v11\n\t" \
  "v_smfmac_f32_16x16x64_bf16 v[56:59], v[4:7], v[240:247], v68\n\t" \
  "s_waitcnt lgkmcnt(4)\n\t" \
  "v_cvt_pk_bf16_f32 v10, v12, v13\n\t" \
  "v_smfmac_f32_16x16x64_bf16 v[56:59], v[32:35], v[60:67], v68\n\t" \
  "s_waitcnt lgkmcnt(3)\n\t" \
  "v_cvt_pk_bf16_f32 v11, v14, v15\n\t" \
  "v_smfmac_f32_16x16x64_bf16 v[76:79], v[36:39], v[248:255], v68\n\t" \
  "buffer_store_dwordx4 v[8:11], v71, s[28:31], s54 offen sc1\n\t" \
  "s_waitcnt lgkmcnt(0)\n\t" \
  "v_cvt_pk_bf16_f32 v52, v52, v53\n\t" \
  "v_smfmac_f32_16x16x64_bf16 v[76:79], v[40:43], v[44:51], v68\n\t" \
  "v_cvt_pk_bf16_f32 v53, v54, v55\n\t" \
  "v_cvt_pk_bf16_f32 v54, v56, v57\n\t" \
  "v_cvt_pk_bf16_f32 v55, v58, v59\n\t" \
  "buffer_store_dwordx4 v[52:55], v71, s[28:31], s75 offen sc1\n\t" \
  "s_nop 2\n\t" \
  "v_add_f32 v76, v76, v78\n\t" \
  "v_add_f32 v77, v77, v79\n\t" \
  "v_cvt_pk_bf16_f32 v76, v76, v77\n\t" \
  "buffer_store_dword v76, v72, s[28:31], s55 offen sc1\n\t" \
  "s_memrealtime s[70:71]\n\t" \
  "s_waitcnt vmcnt(3)\n\t" \
  "v_readfirstlane_b32 s65, v75\n\t" \
  "s_barrier\n\t" \
  "s_waitcnt lgkmcnt(0)\n\t" \
  "s_not_b32 s65, s65\n\t" \
  "s_lshl_b32 s61, s70, 8\n\t" \
  "s_andn2_b32 s64, s65, 0xff\n\t" \
  "s_sub_u32 s64, s64, s61\n\t" \
  "s_ashr_i32 s64, s64, 8\n\t" \
  "s_cmp_eq_u32 s65, 0\n\t" \
  "s_cselect_b32 s64, " CH_DLF ", s64\n\t" \
  "s_cselect_b32 s65, " CH_B0 ", s65\n\t" \
  "s_add_u32 s47, s64, s70\n\t" \
  "s_min_i32 s64, s64, s34\n\t" \
  "s_max_i32 s64, s64, " CH_DLL1 "\n\t" \
  "s_add_u32 s64, s64, s70\n\t" \
  "s_add_u32 s55, s64, " CH_FDL "\n\t" \
  "s_movk_i32 s60, 8192\n\t" \
  "FWT_%=:\n\t" \
  "s_memrealtime s[62:63]\n\t" \
  "s_sub_u32 s60, s60, 1\n\t" \
  "s_cselect_b32 s61, 0, -1\n\t" \
  "s_waitcnt lgkmcnt(0)\n\t" \
  "s_sub_i32 s63, s62, s55\n\t" \
  "s_and_b32 s63, s63, s61\n\t" \
  "s_cmp_lt_i32 s63, 0\n\t" \
  "s_cbranch_scc1 FWT_%=\n\t" \
  "s_mov_b64 exec, 1\n\t" \
  "buffer_store_dword v68, off, s[28:31], s51 offset:4064 sc1\n\t" \
  "s_mov_b64 exec, -1\n\t" \
  "s_and_b32 s65, s65, 0xff\n\t" \
  "s_add_u32 s47, s47, s65\n\t" \
  "s_add_u32 s65, s65, s64\n\t" \
  "s_sub_u32 s60, s70, s66\n\t" \
  "s_add_u32 s61, s81, " CH_UP "\n\t" \
  "s_min_u32 s60, s60, s61\n\t" \
  "s_sub_u32 s61, s81, " CH_DN "\n\t" \
  "s_max_u32 s60, s60, s61\n\t" \
  "s_cmp_eq_u32 s68, 0\n\t" \
  "s_cselect_b32 s81, s60, s81\n\t" \
  "s_add_u32 s48, s48, 1\n\t" \
  "s_mov_b32 s49, s53\n\t" \
  "s_add_u32 s53, s53, " CH_SLOT "\n\t" \
  "s_add_u32 s60, s53, " CH_SKIP "\n\t" \
  "s_lshr_b32 s61, s96, 1\n\t" \
  "s_add_u32 s86, s86, 1\n\t" \
  "s_cmp_eq_u32 s86, 3\n\t" \
  "s_cselect_b32 s53, s60, s53\n\t" \
  "s_cselect_b32 s96, s61, s96\n\t" \
  "s_cselect_b32 s86, 0, s86\n\t" \
  "s_andn2_b32 s53, s53, 0x2000\n\t" \
  "s_and_b32 s60, s96, 1\n\t" \
  "s_lshl_b32 s60, s60, 13\n\t" \
  "s_or_b32 s53, s53, s60\n\t" \
  "s_mov_b32 s68, 0\n\t" \
  "s_add_u32 s82, s48, 0\n\t" \
  "s_lshr_b64 s[60:61], s[90:91], s82\n\t" \
  "s_and_b32 s60, s60, 1\n\t" \
  "s_lshl_b32 s60, s60, 13\n\t" \
  "s_add_u32 s82, s82, " CH_SG0 "\n\t" \
  "s_lshl_b32 s82, s82, 21\n\t" \
  "s_or_b32 s82, s82, s60\n\t" \
  "s_add_u32 s50, s82, s38\n\t" \
  "s_add_u32 s69, s82, s39\n\t" \
  "s_add_u32 s51, s82, s43\n\t" \
  "s_cmp_lg_u32 s34, 0\n\t" \
  "s_cbranch_scc1 LW0_%=\n\t" \
  "s_barrier\n\t" \
  "s_branch LINI_%=\n\t" \
  "LFAIL0_%=:\n\t" \
  "v_max_u32 v75, v0, v1\n\t" \
  "v_max3_u32 v75, v75, v2, v3\n\t" \
  "v_max3_u32 v75, v75, v4, v5\n\t" \
  "v_max3_u32 v75, v75, v6, v7\n\t" \
  "v_max3_u32 v75, v75, v8, v9\n\t" \
  "v_max3_u32 v75, v75, v10, v11\n\t" \
  "v_max3_u32 v75, v75, v12, v13\n\t" \
  "v_max3_u32 v75, v75, v14, v15\n\t" \
  "v_max3_u32 v75, v75, v16, v17\n\t" \
  "v_max3_u32 v75, v75, v18, v19\n\t" \
  "v_max3_u32 v75, v75, v20, v21\n\t" \
  "v_max3_u32 v75, v75, v22, v23\n\t" \
  "v_max3_u32 v75, v75, v24, v25\n\t" \
  "v_max3_u32 v75, v75, v26, v27\n\t" \
  "v_max3_u32 v75, v75, v28, v29\n\t" \
  "v_max3_u32 v75, v75, v30, v31\n\t" \
  "v_max3_u32 v75, v75, v32, v33\n\t" \
  "v_max3_u32 v75, v75, v34, v35\n\t" \
  "v_cmp_eq_u32 vcc, -1, v75\n\t" \
  "s_cbranch_vccz LGO0_%=\n\t" \
  "s_add_u32 s68, s68, 1\n\t" \
  "s_waitcnt lgkmcnt(0)\n\t" \
  "s_cmp_gt_u32 s68, s83\n\t" \
  "s_cbranch_scc1 LGV0_%=\n\t" \
  "s_barrier\n\t" \
  "s_memrealtime s[62:63]\n\t" \
  "s_mov_b32 m0, 0\n\t" \
  "s_waitcnt lgkmcnt(0)\n\t" \
  "s_add_u32 s65, s62, " CH_PEN "\n\t" \
  "s_branch LDMA_%=\n\t" \
  "LWT_%=f:\n\t" \
  "s_cmp_lt_i32 s63, -2\n\t" \
  "s_cbranch_scc0 LWT_%=g\n\t" \
  "s_nop 4\n\t" \
  "s_branch LWT_%=g\n\t" \
  "LGV0_%=:\n\t" \
  "s_mov_b32 s83, 0\n\t" \
  "s_lshl_b32 s60, s36, 4\n\t" \
  "s_or_b32 s60, s60, s48\n\t" \
  "s_or_b32 s60, s60, 805306368\n\t" \
  "v_mov_b32 v76, s60\n\t" \
  "v_mov_b32 v77, 0\n\t" \
  "s_mov_b64 exec, 1\n\t" \
  "buffer_atomic_cmpswap v[76:77], off, s[28:31], s92 sc1\n\t" \
  "s_mov_b64 exec, -1\n\t" \
  "s_branch LGO0_%=\n\t" \
  "LLAST0_%=:\n\t" \
  "s_waitcnt vmcnt(0) lgkmcnt(0)\n\t" \
  "s_barrier\n\t" \
  "s_mov_b64 exec, 1\n\t" \
  "buffer_store_dword v68, off, s[28:31], s51 offset:4064 sc1\n\t" \
  "s_mov_b64 exec, -1\n\t" \
  "s_branch LEND_%=\n\t" \
  ".p2align 6\n\t" \
  "LG1_%=:\n\t" \
  "s_barrier\n\t" \
  "buffer_load_dwordx4 v[52:55], v69, s[28:31], s50 offen sc1\n\t" \
  "buffer_load_dwordx4 v[56:59], v69, s[28:31], s50 offen offset:1024 sc1\n\t" \
  "s_add_u32 s64, s50, 14336\n\t" \
  "s_movk_i32 m0, 11264\n\t" \
  "v_lshlrev_b32 v75, 1, v69\n\t" \
  "buffer_load_dword v75, s[28:31], s64 offen sc1 lds\n\t" \
  "ds_read_b128 v[44:47], v70 offset:0\n\t" \
  "ds_read_b128 v[48:51], v70 offset:1024\n\t" \
  "ds_read_b128 v[60:63], v70 offset:2048\n\t" \
  "ds_read_b128 v[64:67], v70 offset:3072\n\t" \
  "v_mov_b64 v[36:37], 0\n\t" \
  "v_mov_b64 v[38:39], 0\n\t" \
  "v_mov_b64 v[40:41], 0\n\t" \
  "v_mov_b64 v[42:43], 0\n\t" \
  "v_lshrrev_b32 v71, 1, v69\n\t" \
  "v_add_u32 v71, 4, v71\n\t" \
  "v_lshrrev_b32 v72, 3, v69\n\t" \
  "v_and_b32 v72, 0x78, v72\n\t" \
  "v_and_b32 v74, 0x30, v69\n\t" \
  "v_lshl_add_u32 v72, v74, 3, v72\n\t" \
  "s_add_u32 s54, s53, s36\n\t" \
  "s_add_u32 s55, s54, 540672\n\t" \
  "s_add_u32 s54, s54, 4096\n\t" \
  "s_add_u32 s75, s54, 262144\n\t" \
  "s_add_u32 s80, s53, s26\n\t" \
  "s_barrier\n\t" \
  "ds_read_b128 v[0:3], v69 offset:0\n\t" \
  "ds_read_b128 v[4:7], v69 offset:1024\n\t" \
  "ds_read_b128 v[8:11], v69 offset:2048\n\t" \
  "ds_read_b128 v[12:15], v69 offset:3072\n\t" \
  "s_waitcnt lgkmcnt(3)\n\t" \
  "v_smfmac_f32_16x16x64_bf16 v[36:39], v[0:3], v[44:51], v68\n\t" \
  "ds_read_b128 v[44:47], v70 offset:4096\n\t" \
  "v_smfmac_f32_16x16x64_bf16 v[40:43], v[0:3], v[60:67], v68\n\t" \
  "ds_read_b128 v[48:51], v70 offset:5120\n\t" \
  "s_waitcnt lgkmcnt(4)\n\t" \
  "v_smfmac_f32_16x16x64_bf16 v[36:39], v[4:7], a[32:39], v68\n\t" \
  "ds_read_b128 v[60:63], v70 offset:6144\n\t" \
  "v_smfmac_f32_16x16x64_bf16 v[40:43], v[4:7], a[40:47], v68\n\t" \
  "ds_read_b128 v[64:67], v70 offset:7168\n\t" \
  "s_waitcnt lgkmcnt(5)\n\t" \
  "v_smfmac_f32_16x16x64_bf16 v[36:39], v[8:11], a[80:87], v68\n\t" \
  "s_barrier\n\t" \
  "ds_read_b128 v[16:19], v69 offset:4096\n\t" \
  "v_smfmac_f32_16x16x64_bf16 v[40:43], v[8:11], a[88:95], v68\n\t" \
  "ds_read_b128 v[20:23], v69 offset:5120\n\t" \
  "s_waitcnt lgkmcnt(6)\n\t" \
  "v_smfmac_f32_16x16x64_bf16 v[36:39], v[12:15], a[120:127], v68\n\t" \
  "ds_read_b128 v[24:27], v69 offset:6144\n\t" \
  "s_waitcnt lgkmcnt(5)\n\t" \
  "v_smfmac_f32_16x16x64_bf16 v[40:43], v[12:15], v[44:51], v68\n\t" \
  "ds_read_b128 v[28:31], v69 offset:7168\n\t" \
  "s_waitcnt lgkmcnt(3)\n\t" \
  "v_smfmac_f32_16x16x64_bf16 v[36:39], v[16:19], a[160:167], v68\n\t" \
  "ds_read_b128 v[32:35], v69 offset:8192\n\t" \
  "v_smfmac_f32_16x16x64_bf16 v[40:43], v[16:19], a[168:175], v68\n\t" \
  "s_waitcnt lgkmcnt(3)\n\t" \
  "v_mov_b64 v[44:45], 0\n\t" \
  "v_smfmac_f32_16x16x64_bf16 v[36:39], v[20:23], a[200:207], v68\n\t" \
  "s_waitcnt lgkmcnt(2)\n\t" \
  "v_mov_b64 v[46:47], 0\n\t" \
  "v_smfmac_f32_16x16x64_bf16 v[36:39], v[24:27], a[240:247], v68\n\t" \
  "s_waitcnt lgkmcnt(1)\n\t" \
  "v_mov_b64 v[48:49], 0\n\t" \
  "v_smfmac_f32_16x16x64_bf16 v[36:39], v[28:31], v[112:119], v68\n\t" \
  "s_waitcnt lgkmcnt(0)\n\t" \
  "v_mov_b64 v[50:51], 0\n\t" \
  "v_smfmac_f32_16x16x64_bf16 v[36:39], v[32:35], v[160:167], v68\n\t" \
  "v_smfmac_f32_16x16x64_bf16 v[40:43], v[20:23], v[60:67], v68\n\t" \
  "ds_read_b128 v[60:63], v70 offset:8192\n\t" \
  "v_smfmac_f32_16x16x64_bf16 v[40:43], v[24:27], a[248:255], v68\n\t" \
  "ds_read_b128 v[64:67], v70 offset:9216\n\t" \
  "v_add_f32 v74, v36, v38\n\t" \
  "v_smfmac_f32_16x16x64_bf16 v[40:43], v[28:31], v[120:127], v68\n\t" \
  "v_add_f32 v37, v37, v39\n\t" \
  "v_smfmac_f32_16x16x64_bf16 v[40:43], v[32:35], v[168:175], v68\n\t" \
  "v_cmp_u_f32 vcc, v74, v37\n\t" \
  "v_smfmac_f32_16x16x64_bf16 v[44:47], v[0:3], a[0:7], v68\n\t" \
  "s_cbranch_vccnz LFAIL1_%=\n\t" \
  "LGO1_%=:\n\t" \
  "v_cvt_pk_bf16_f32 v74, v74, v37\n\t" \
  "v_smfmac_f32_16x16x64_bf16 v[44:47], v[4:7], a[48:55], v68\n\t" \
  "s_waitcnt lgkmcnt(0)\n\t" \
  "v_add_f32 v75, v40, v42\n\t" \
  "v_smfmac_f32_16x16x64_bf16 v[44:47], v[8:11], v[60:67], v68\n\t" \
  "ds_read_b128 v[60:63], v70 offset:10240\n\t" \
  "v_add_f32 v41, v41, v43\n\t" \
  "v_cvt_pk_bf16_f32 v75, v75, v41\n\t" \
  "v_smfmac_f32_16x16x64_bf16 v[44:47], v[12:15], a[128:135], v68\n\t" \
  "ds_write2st64_b32 v71, v74, v75 offset0:36 offset1:38\n\t" \
  "v_smfmac_f32_16x16x64_bf16 v[44:47], v[16:19], a[176:183], v68\n\t" \
  "ds_read_b128 v[64:67], v70 offset:11264\n\t" \
  "v_smfmac_f32_16x16x64_bf16 v[44:47], v[20:23], a[208:215], v68\n\t" \
  "v_smfmac_f32_16x16x64_bf16 v[44:47], v[24:27], v[80:87], v68\n\t" \
  "v_smfmac_f32_16x16x64_bf16 v[44:47], v[28:31], v[128:135], v68\n\t" \
  "s_waitcnt lgkmcnt(0)\n\t" \
  "v_smfmac_f32_16x16x64_bf16 v[44:47], v[32:35], v[60:67], v68\n\t" \
  "s_barrier\n\t" \
  "ds_read_b64_tr_b16 v[76:77], v72 offset:9216\n\t" \
  "v_smfmac_f32_16x16x64_bf16 v[48:51], v[0:3], a[8:15], v68\n\t" \
  "buffer_load_dwordx4 v[60:63], v69, s[28:31], s64 offen sc1\n\t" \
  "ds_read_b64_tr_b16 v[78:79], v72 offset:9728\n\t" \
  "v_smfmac_f32_16x16x64_bf16 v[48:51], v[4:7], a[56:63], v68\n\t" \
  "v_add_f32 v74, v44, v46\n\t" \
  "v_smfmac_f32_16x16x64_bf16 v[48:51], v[8:11], a[96:103], v68\n\t" \
  "v_add_f32 v45, v45, v47\n\t" \
  "v_smfmac_f32_16x16x64_bf16 v[48:51], v[12:15], a[136:143], v68\n\t" \
  "s_waitcnt vmcnt(3)\n\t" \
  "s_waitcnt lgkmcnt(0)\n\t" \
  "v_cvt_pk_bf16_f32 v74, v74, v45\n\t" \
  "v_mfma_f32_16x16x32_bf16 v[36:39], v[52:55], v[76:79], 0\n\t" \
  "s_waitcnt vmcnt(2)\n\t" \
  "v_mfma_f32_16x16x32_bf16 v[40:43], v[56:59], v[76:79], 0\n\t" \
  "buffer_load_dwordx4 v[64:67], v69, s[28:31], s64 offen offset:1024 sc1\n\t" \
  "v_mov_b64 v[52:53], 0\n\t" \
  "v_mov_b64 v[54:55], 0\n\t" \
  "v_smfmac_f32_16x16x64_bf16 v[48:51], v[16:19], a[184:191], v68\n\t" \
  "v_mov_b64 v[56:57], 0\n\t" \
  "v_mov_b64 v[58:59], 0\n\t" \
  "v_smfmac_f32_16x16x64_bf16 v[48:51], v[20:23], a[216:223], v68\n\t" \
  "ds_write_b32 v71, v74 offset:0\n\t" \
  "v_add_f32_dpp v36, v40, v36 quad_perm:[2,3,0,1] row_mask:0xf bank_mask:0xf\n\t" \
  "v_smfmac_f32_16x16x64_bf16 v[48:51], v[24:27], v[88:95], v68\n\t" \
  "v_add_f32_dpp v37, v41, v37 quad_perm:[2,3,0,1] row_mask:0xf bank_mask:0xf\n\t" \
  "v_smfmac_f32_16x16x64_bf16 v[48:51], v[28:31], v[136:143], v68\n\t" \
  "v_add_f32_dpp v38, v42, v38 quad_perm:[2,3,0,1] row_mask:0xf bank_mask:0xf\n\t" \
  "v_smfmac_f32_16x16x64_bf16 v[48:51], v[32:35], v[176:183], v68\n\t" \
  "v_add_f32_dpp v39, v43, v39 quad_perm:[2,3,0,1] row_mask:0xf bank_mask:0xf\n\t" \
  "v_smfmac_f32_16x16x64_bf16 v[52:55], v[0:3], a[16:23], v68\n\t" \
  "s_mov_b64 exec, s[58:59]\n\t" \
  "ds_write_b128 v69, v[36:39] offset:2080\n\t" \
  "s_mov_b64 exec, -1\n\t" \
  "v_smfmac_f32_16x16x64_bf16 v[52:55], v[4:7], a[64:71], v68\n\t" \
  "v_add_f32 v75, v48, v50\n\t" \
  "v_add_f32 v49, v49, v51\n\t" \
  "v_cvt_pk_bf16_f32 v75, v75, v49\n\t" \
  "v_smfmac_f32_16x16x64_bf16 v[56:59], v[0:3], a[24:31], v68\n\t" \
  "ds_write_b32 v71, v75 offset:512\n\t" \
  "v_smfmac_f32_16x16x64_bf16 v[56:59], v[4:7], a[72:79], v68\n\t" \
  "ds_read_b64_tr_b16 v[0:1], v72 offset:10240\n\t" \
  "ds_read_b64_tr_b16 v[2:3], v72 offset:10752\n\t" \
  "v_smfmac_f32_16x16x64_bf16 v[52:55], v[8:11], a[104:111], v68\n\t" \
  "ds_read_b128 v[36:39], v70 offset:12288\n\t" \
  "v_smfmac_f32_16x16x64_bf16 v[56:59], v[8:11], a[112:119], v68\n\t" \
  "ds_read_b128 v[40:43], v70 offset:13312\n\t" \
  "v_smfmac_f32_16x16x64_bf16 v[52:55], v[12:15], a[144:151], v68\n\t" \
  "s_waitcnt lgkmcnt(4)\n\t" \
  "s_barrier\n\t" \
  "ds_read_b64_tr_b16 v[44:45], v72 offset:0\n\t" \
  "ds_read_b64_tr_b16 v[46:47], v72 offset:512\n\t" \
  "v_smfmac_f32_16x16x64_bf16 v[56:59], v[12:15], a[152:159], v68\n\t" \
  "ds_read_b64_tr_b16 v[48:49], v72 offset:1024\n\t" \
  "ds_read_b64_tr_b16 v[50:51], v72 offset:1536\n\t" \
  "v_smfmac_f32_16x16x64_bf16 v[52:55], v[16:19], a[192:199], v68\n\t" \
  "ds_read_b128 v[4:7], v69 offset:2048\n\t" \
  "s_waitcnt lgkmcnt(5)\n\t" \
  "v_smfmac_f32_16x16x64_bf16 v[56:59], v[16:19], v[36:43], v68\n\t" \
  "ds_read_b128 v[8:11], v69 offset:3072\n\t" \
  "s_waitcnt lgkmcnt(4)\n\t" \
  "v_mfma_f32_16x16x32_bf16 v[12:15], v[44:47], v[76:79], 0\n\t" \
  "ds_read_b128 v[36:39], v70 offset:14336\n\t" \
  "s_waitcnt lgkmcnt(3)\n\t" \
  "v_mfma_f32_16x16x32_bf16 v[12:15], v[48:51], v[0:3], v[12:15]\n\t" \
  "buffer_store_dwordx4 v[44:47], v69, s[28:31], s69 offen offset:0 sc1\n\t" \
  "ds_read_b128 v[40:43], v70 offset:15360\n\t" \
  "v_smfmac_f32_16x16x64_bf16 v[52:55], v[20:23], a[224:231], v68\n\t" \
  "s_waitcnt lgkmcnt(2)\n\t" \
  "v_add_f32 v4, v4, v8\n\t" \
  "v_add_f32 v5, v5, v9\n\t" \
  "v_add_f32 v6, v6, v10\n\t" \
  "v_cndmask_b32 v78, v13, v14, s[56:57]\n\t" \
  "v_cndmask_b32 v76, v12, v15, s[56:57]\n\t" \
  "v_add_f32 v7, v7, v11\n\t" \
  "v_max3_f32 v77, v4, v5, v6\n\t" \
  "v_add_f32_dpp v76, v78, v76 quad_perm:[2,3,0,1] row_mask:0xf bank_mask:0xf\n\t" \
  "v_max3_f32 v77, v77, v7, v76\n\t" \
  "v_smfmac_f32_16x16x64_bf16 v[56:59], v[20:23], a[232:239], v68\n\t" \
  "ds_read_b128 v[44:47], v70 offset:16384\n\t" \
  "v_max_f32_dpp v77, v77, v77 quad_perm:[2,3,0,1] row_mask:0xf bank_mask:0xf\n\t" \
  "v_mov_b32 v78, v77\n\t" \
  "v_smfmac_f32_16x16x64_bf16 v[52:55], v[24:27], v[96:103], v68\n\t" \
  "ds_read_b128 v[48:51], v70 offset:17408\n\t" \
  "v_permlane16_swap_b32 v77, v78\n\t" \
  "v_max_f32 v77, v77, v78\n\t" \
  "v_mov_b32 v78, v77\n\t" \
  "v_smfmac_f32_16x16x64_bf16 v[52:55], v[28:31], v[144:151], v68\n\t" \
  "ds_read_b128 v[16:19], v70 offset:18432\n\t" \
  "v_permlane32_swap_b32 v77, v78\n\t" \
  "v_max_f32 v77, v77, v78\n\t" \
  "v_smfmac_f32_16x16x64_bf16 v[52:55], v[32:35], v[184:191], v68\n\t" \
  "ds_read_b128 v[20:23], v70 offset:19456\n\t" \
  "v_mul_f32 v77, 0xbe0293ee, v77\n\t" \
  "v_smfmac_f32_16x16x64_bf16 v[56:59], v[24:27], v[104:111], v68\n\t" \
  "v_fmamk_f32 v4, v4, 0x3e0293ee, v77\n\t" \
  "v_smfmac_f32_16x16x64_bf16 v[56:59], v[28:31], v[152:159], v68\n\t" \
  "ds_read_b128 v[24:27], v70 offset:22528\n\t" \
  "s_waitcnt lgkmcnt(4)\n\t" \
  "v_fmamk_f32 v5, v5, 0x3e0293ee, v77\n\t" \
  "v_smfmac_f32_16x16x64_bf16 v[56:59], v[32:35], v[36:43], v68\n\t" \
  "ds_read_b128 v[28:31], v70 offset:23552\n\t" \
  "v_fmamk_f32 v6, v6, 0x3e0293ee, v77\n\t" \
  "v_fmamk_f32 v7, v7, 0x3e0293ee, v77\n\t" \
  "v_fmamk_f32 v76, v76, 0x3e0293ee, v77\n\t" \
  "v_exp_f32 v4, v4\n\t" \
  "v_exp_f32 v5, v5\n\t" \
  "v_exp_f32 v6, v6\n\t" \
  "v_exp_f32 v7, v7\n\t" \
  "v_cvt_pk_bf16_f32 v4, v4, v5\n\t" \
  "v_exp_f32 v76, v76\n\t" \
  "v_cvt_pk_bf16_f32 v5, v6, v7\n\t" \
  "v_mov_b32_dpp v12, v4 quad_perm:[0,1,0,1] row_mask:0xf bank_mask:0xf\n\t" \
  "v_mov_b32_dpp v14, v4 quad_perm:[2,3,2,3] row_mask:0xf bank_mask:0xf\n\t" \
  "v_mov_b32_dpp v13, v5 quad_perm:[0,1,0,1] row_mask:0xf bank_mask:0xf\n\t" \
  "v_mov_b32_dpp v15, v5 quad_perm:[2,3,2,3] row_mask:0xf bank_mask:0xf\n\t" \
  "v_add_f32 v52, v52, v54\n\t" \
  "v_add_f32 v53, v53, v55\n\t" \
  "s_waitcnt vmcnt(2)\n\t" \
  "v_mfma_f32_16x16x32_bf16 v[0:3], v[60:63], v[12:15], 0\n\t" \
  "ds_read_b128 v[36:39], v70 offset:20480\n\t" \
  "v_add_f32 v56, v56, v58\n\t" \
  "v_add_f32 v57, v57, v59\n\t" \
  "v_cvt_pk_bf16_f32 v52, v52, v53\n\t" \
  "v_cvt_pk_bf16_f32 v53, v56, v57\n\t" \
  "s_waitcnt vmcnt(1)\n\t" \
  "v_mfma_f32_16x16x32_bf16 v[4:7], v[64:67], v[12:15], 0\n\t" \
  "ds_read_b128 v[40:43], v70 offset:21504\n\t" \
  "v_mov_b64 v[56:57], s[88:89]\n\t" \
  "v_mov_b64 v[58:59], s[88:89]\n\t" \
  "v_mov_b32_dpp v78, v76 quad_perm:[0,1,0,1] row_mask:0xf bank_mask:0xf\n\t" \
  "v_mov_b32_dpp v79, v76 quad_perm:[2,3,2,3] row_mask:0xf bank_mask:0xf\n\t" \
  "v_mfma_f32_16x16x32_bf16 v[32:35], v[56:59], v[12:15], 0\n\t" \
  "buffer_store_dwordx2 v[52:53], v71, s[28:31], s69 offen offset:2556 sc1\n\t" \
  "ds_read_b128 v[60:63], v70 offset:24576\n\t" \
  "v_cvt_pk_bf16_f32 v11, v78, v79\n\t" \
  "v_cvt_pk_bf16_f32 v8, v78, v79\n\t" \
  "v_mov_b64 v[54:55], 0\n\t" \
  "v_mov_b32 v9, 0\n\t" \
  "v_mov_b32 v10, 0\n\t" \
  "v_mov_b64 v[14:15], 0\n\t" \
  "s_nop 0\n\t" \
  "v_mfma_f32_16x16x32_bf16 v[32:35], v[54:57], v[8:11], v[32:35]\n\t" \
  "s_cmp_eq_u32 s48, 63\n\t" \
  "v_mfma_f32_16x16x32_bf16 v[0:3], v[52:55], v[8:11], v[0:3]\n\t" \
  "ds_read_b128 v[64:67], v70 offset:25600\n\t" \
  "v_mov_b64 v[56:57], 0\n\t" \
  "v_mov_b64 v[58:59], 0\n\t" \
  "v_mfma_f32_16x16x32_bf16 v[4:7], v[52:55], v[10:13], v[4:7]\n\t" \
  "s_cbranch_scc1 LLAST1_%=\n\t" \
  "v_rcp_f32 v74, v32\n\t" \
  "v_mov_b64 v[12:13], 0\n\t" \
  "v_mov_b32 v8, 0\n\t" \
  "v_pk_mul_f32 v[0:1], v[0:1], v[74:75] op_sel_hi:[1,0]\n\t" \
  "v_pk_mul_f32 v[2:3], v[2:3], v[74:75] op_sel_hi:[1,0]\n\t" \
  "v_cvt_pk_bf16_f32 v76, v0, v1\n\t" \
  "v_cvt_pk_bf16_f32 v77, v2, v3\n\t" \
  "v_pk_mul_f32 v[4:5], v[4:5], v[74:75] op_sel_hi:[1,0]\n\t" \
  "v_pk_mul_f32 v[6:7], v[6:7], v[74:75] op_sel_hi:[1,0]\n\t" \
  "v_cvt_pk_bf16_f32 v78, v4, v5\n\t" \
  "v_cvt_pk_bf16_f32 v79, v6, v7\n\t" \
  "ds_write_b128 v69, v[76:79] offset:5120\n\t" \
  "s_waitcnt lgkmcnt(2)\n\t" \
  "v_smfmac_f32_16x16x64_bf16 v[12:15], v[76:79], v[44:51], v68\n\t" \
  "ds_read_b128 v[44:47], v70 offset:26624\n\t" \
  "v_mov_b32 v11, 0\n\t" \
  "v_smfmac_f32_16x16x64_bf16 v[56:59], v[76:79], v[16:23], v68\n\t" \
  "ds_read_b128 v[48:51], v70 offset:27648\n\t" \
  "v_mov_b64 v[52:53], 0\n\t" \
  "v_smfmac_f32_16x16x64_bf16 v[8:11], v[76:79], v[36:43], v68\n\t" \
  "ds_read_b128 v[16:19], v70 offset:28672\n\t" \
  "v_smfmac_f32_16x16x64_bf16 v[52:55], v[76:79], v[24:31], v68\n\t" \
  "s_waitcnt lgkmcnt(3)\n\t" \
  "s_barrier\n\t" \
  "ds_read_b128 v[0:3], v69 offset:4096\n\t" \
  "ds_read_b128 v[4:7], v69 offset:7168\n\t" \
  "v_lshrrev_b32 v71, 7, v69\n\t" \
  "v_and_b32 v74, 0x70, v69\n\t" \
  "v_lshl_add_u32 v71, v71, 14, v74\n\t" \
  "v_bfe_u32 v72, v69, 2, 7\n\t" \
  "v_and_b32 v74, 0x200, v69\n\t" \
  "s_waitcnt lgkmcnt(1)\n\t" \
  "v_smfmac_f32_16x16x64_bf16 v[8:11], v[0:3], v[60:67], v68\n\t" \
  "buffer_load_dword v75, off, s[28:31], s80 sc1\n\t" \
  "ds_read_b128 v[20:23], v70 offset:29696\n\t" \
  "v_mov_b64 v[76:77], 0\n\t" \
  "v_smfmac_f32_16x16x64_bf16 v[12:15], v[0:3], v[200:207], v68\n\t" \
  "ds_read_b128 v[32:35], v69 offset:6144\n\t" \
  "v_mov_b64 v[78:79], 0\n\t" \
  "v_smfmac_f32_16x16x64_bf16 v[52:55], v[0:3], v[44:51], v68\n\t" \
  "ds_read_b128 v[24:27], v70 offset:30720\n\t" \
  "v_lshl_add_u32 v72, v74, 3, v72\n\t" \
  "v_smfmac_f32_16x16x64_bf16 v[56:59], v[0:3], v[232:239], v68\n\t" \
  "ds_read_b128 v[28:31], v70 offset:31744\n\t" \
  "s_waitcnt lgkmcnt(4)\n\t" \
  "v_smfmac_f32_16x16x64_bf16 v[12:15], v[4:7], v[208:215], v68\n\t" \
  "ds_read_b128 v[60:63], v70 offset:32768\n\t" \
  "s_waitcnt lgkmcnt(4)\n\t" \
  "v_smfmac_f32_16x16x64_bf16 v[8:11], v[4:7], v[16:23], v68\n\t" \
  "ds_read_b128 v[64:67], v70 offset:33792\n\t" \
  "s_waitcnt lgkmcnt(4)\n\t" \
  "v_smfmac_f32_16x16x64_bf16 v[8:11], v[32:35], v[192:199], v68\n\t" \
  "ds_read_b128 v[36:39], v73 offset:4096\n\t" \
  "v_smfmac_f32_16x16x64_bf16 v[12:15], v[32:35], v[216:223], v68\n\t" \
  "ds_read_b128 v[40:43], v73 offset:6144\n\t" \
  "s_waitcnt lgkmcnt(4)\n\t" \
  "v_smfmac_f32_16x16x64_bf16 v[52:55], v[4:7], v[24:31], v68\n\t" \
  "ds_read_b128 v[44:47], v70 offset:34816\n\t" \
  "v_cvt_pk_bf16_f32 v8, v8, v9\n\t" \
  "v_smfmac_f32_16x16x64_bf16 v[52:55], v[32:35], v[224:231], v68\n\t" \
  "ds_read_b128 v[48:51], v70 offset:35840\n\t" \
  "v_cvt_pk_bf16_f32 v9, v10, v11\n\t" \
  "v_smfmac_f32_16x16x64_bf16 v[56:59], v[4:7], v[240:247], v68\n\t" \
  "s_waitcnt lgkmcnt(4)\n\t" \
  "v_cvt_pk_bf16_f32 v10, v12, v13\n\t" \
  "v_smfmac_f32_16x16x64_bf16 v[56:59], v[32:35], v[60:67], v68\n\t" \
  "s_waitcnt lgkmcnt(3)\n\t" \
  "v_cvt_pk_bf16_f32 v11, v14, v15\n\t" \
  "v_smfmac_f32_16x16x64_bf16 v[76:79], v[36:39], v[248:255], v68\n\t" \
  "buffer_store_dwordx4 v[8:11], v71, s[28:31], s54 offen sc1\n\t" \
  "s_waitcnt lgkmcnt(0)\n\t" \
  "v_cvt_pk_bf16_f32 v52, v52, v53\n\t" \
  "v_smfmac_f32_16x16x64_bf16 v[76:79], v[40:43], v[44:51], v68\n\t" \
  "v_cvt_pk_bf16_f32 v53, v54, v55\n\t" \
  "v_cvt_pk_bf16_f32 v54, v56, v57\n\t" \
  "v_cvt_pk_bf16_f32 v55, v58, v59\n\t" \
  "buffer_store_dwordx4 v[52:55], v71, s[28:31], s75 offen sc1\n\t" \
  "s_nop 2\n\t" \
  "v_add_f32 v76, v76, v78\n\t" \
  "v_add_f32 v77, v77, v79\n\t" \
  "v_cvt_pk_bf16_f32 v76, v76, v77\n\t" \
  "buffer_store_dword v76, v72, s[28:31], s55 offen sc1\n\t" \
  "s_memrealtime s[70:71]\n\t" \
  "s_waitcnt vmcnt(3)\n\t" \
  "v_readfirstlane_b32 s65, v75\n\t" \
  "s_barrier\n\t" \
  "s_waitcnt lgkmcnt(0)\n\t" \
  "s_not_b32 s65, s65\n\t" \
  "s_lshl_b32 s61, s70, 8\n\t" \
  "s_andn2_b32 s64, s65, 0xff\n\t" \
  "s_sub_u32 s64, s64, s61\n\t" \
  "s_ashr_i32 s64, s64, 8\n\t" \
  "s_cmp_eq_u32 s65, 0\n\t" \
  "s_cselect_b32 s64, " CH_DLF ", s64\n\t" \
  "s_sub_i32 s64, s64, " CH_RO1 "\n\t" \
  "s_min_i32 s64, s64, s34\n\t" \
  "s_max_i32 s64, s64, " CH_DLL1 "\n\t" \
  "s_add_u32 s64, s64, s70\n\t" \
  "s_mov_b32 s74, 0\n\t" \
  "s_movk_i32 s60, 8192\n\t" \
  "RWT1_%=:\n\t" \
  "s_memrealtime s[72:73]\n\t" \
  "s_sub_u32 s60, s60, 1\n\t" \
  "s_cselect_b32 s61, 0, -1\n\t" \
  "s_waitcnt lgkmcnt(0)\n\t" \
  "s_sub_i32 s73, s72, s64\n\t" \
  "s_and_b32 s73, s73, s61\n\t" \
  "s_cmp_lt_i32 s73, " CH_FIN "\n\t" \
  "s_cbranch_scc1 RWT1_%=\n\t" \
  "s_cmp_lt_i32 s73, 0\n\t" \
  "s_cbranch_scc1 RWT1_%=f\n\t" \
  "RWT1_%=g:\n\t" \
  "RRETRY1_%=:\n\t" \
  "buffer_load_dwordx4 v[0:3], v69, s[28:31], s97 offen offset:0 sc1\n\t" \
  "buffer_load_dwordx4 v[4:7], v69, s[28:31], s97 offen offset:1024 sc1\n\t" \
  "buffer_load_dwordx4 v[8:11], v69, s[28:31], s97 offen offset:2048 sc1\n\t" \
  "buffer_load_dwordx4 v[12:15], v69, s[28:31], s97 offen offset:3072 sc1\n\t" \
  "v_lshrrev_b32 v20, 2, v69\n\t" \
  "v_and_b32 v20, 16, v20\n\t" \
  "v_mov_b32 v21, 0x3f80\n\t" \
  "v_lshlrev_b32 v21, v20, v21\n\t" \
  "v_bfe_u32 v20, v69, 4, 2\n\t" \
  "v_cmp_eq_u32 vcc, 0, v20\n\t" \
  "s_nop 1\n\t" \
  "v_cndmask_b32 v32, 0, v21, vcc\n\t" \
  "v_cmp_eq_u32 vcc, 1, v20\n\t" \
  "s_nop 1\n\t" \
  "v_cndmask_b32 v33, 0, v21, vcc\n\t" \
  "v_cmp_eq_u32 vcc, 2, v20\n\t" \
  "s_nop 1\n\t" \
  "v_cndmask_b32 v34, 0, v21, vcc\n\t" \
  "v_cmp_eq_u32 vcc, 3, v20\n\t" \
  "s_nop 1\n\t" \
  "v_cndmask_b32 v35, 0, v21, vcc\n\t" \
  "v_bfe_u32 v38, v69, 7, 1\n\t" \
  "v_bfe_u32 v20, v69, 9, 1\n\t" \
  "v_lshl_add_u32 v38, v20, 2, v38\n\t" \
  "v_lshlrev_b32 v38, 12, v38\n\t" \
  "v_and_b32 v20, 0x60, v69\n\t" \
  "v_add_u32 v38, v38, v20\n\t" \
  "v_bfe_u32 v20, v69, 8, 1\n\t" \
  "v_lshl_add_u32 v38, v20, 4, v38\n\t" \
  "v_bfe_u32 v20, v69, 4, 1\n\t" \
  "v_lshl_add_u32 v38, v20, 3, v38\n\t" \
  "v_mov_b32 v44, 0\n\t" \
  "s_waitcnt vmcnt(3)\n\t" \
  "v_mfma_f32_16x16x32_bf16 v[40:43], v[32:35], v[0:3], 0\n\t" \
  "v_max3_u32 v44, v44, v0, v1\n\t" \
  "v_max3_u32 v44, v44, v2, v3\n\t" \
  "s_waitcnt vmcnt(2)\n\t" \
  "v_mfma_f32_16x16x32_bf16 v[40:43], v[32:35], v[4:7], v[40:43]\n\t" \
  "v_max3_u32 v44, v44, v4, v5\n\t" \
  "v_max3_u32 v44, v44, v6, v7\n\t" \
  "s_waitcnt vmcnt(1)\n\t" \
  "v_mfma_f32_16x16x32_bf16 v[40:43], v[32:35], v[8:11], v[40:43]\n\t" \
  "v_max3_u32 v44, v44, v8, v9\n\t" \
  "v_max3_u32 v44, v44, v10, v11\n\t" \
  "s_waitcnt vmcnt(0)\n\t" \
  "v_mfma_f32_16x16x32_bf16 v[40:43], v[32:35], v[12:15], v[40:43]\n\t" \
  "v_max3_u32 v44, v44, v12, v13\n\t" \
  "v_max3_u32 v44, v44, v14, v15\n\t" \
  "v_cmp_eq_u32 vcc, -1, v44\n\t" \
  "s_cbranch_vccnz RFAIL1_%=\n\t" \
  "s_nop 3\n\t" \
  "v_add_f32_dpp v40, v40, v40 row_ror:8 row_mask:0xf bank_mask:0xf\n\t" \
  "v_add_f32_dpp v41, v41, v41 row_ror:8 row_mask:0xf bank_mask:0xf\n\t" \
  "v_add_f32_dpp v42, v42, v42 row_ror:8 row_mask:0xf bank_mask:0xf\n\t" \
  "v_add_f32_dpp v43, v43, v43 row_ror:8 row_mask:0xf bank_mask:0xf\n\t" \
  "v_cvt_pk_bf16_f32 v40, v40, v41\n\t" \
  "v_cvt_pk_bf16_f32 v41, v42, v43\n\t" \
  "buffer_store_dwordx2 v[40:41], v38, s[28:31], s98 offen sc1\n\t" \
  "RDONE1_%=:\n\t" \
  "s_barrier\n\t" \
  "s_add_u32 s48, s48, 1\n\t" \
  "s_mov_b32 s49, s53\n\t" \
  "s_add_u32 s53, s53, " CH_SLOT "\n\t" \
  "s_add_u32 s60, s53, " CH_SKIP "\n\t" \
  "s_lshr_b32 s61, s96, 1\n\t" \
  "s_add_u32 s86, s86, 1\n\t" \
  "s_cmp_eq_u32 s86, 3\n\t" \
  "s_cselect_b32 s53, s60, s53\n\t" \
  "s_cselect_b32 s96, s61, s96\n\t" \
  "s_cselect_b32 s86, 0, s86\n\t" \
  "s_andn2_b32 s53, s53, 0x2000\n\t" \
  "s_and_b32 s60, s96, 1\n\t" \
  "s_lshl_b32 s60, s60, 13\n\t" \
  "s_or_b32 s53, s53, s60\n\t" \
  "s_mov_b32 s68, 0\n\t" \
  "s_add_u32 s97, s41, s53\n\t" \
  "s_add_u32 s98, s37, s53\n\t" \
  "s_add_u32 s82, s48, 0\n\t" \
  "s_lshr_b64 s[60:61], s[90:91], s82\n\t" \
  "s_and_b32 s60, s60, 1\n\t" \
  "s_lshl_b32 s60, s60, 13\n\t" \
  "s_add_u32 s82, s82, " CH_SG0 "\n\t" \
  "s_lshl_b32 s82, s82, 21\n\t" \
  "s_or_b32 s82, s82, s60\n\t" \
  "s_add_u32 s50, s82, s38\n\t" \
  "s_add_u32 s69, s82, s39\n\t" \
  "s_cmp_eq_u32 s34, 0\n\t" \
  "s_cbranch_scc1 LINI_%=\n\t" \
  "s_branch LG1_%=\n\t" \
  "RFAIL1_%=:\n\t" \
  "s_add_u32 s74, s74, 1\n\t" \
  "s_sleep " CH_RSL "\n\t" \
  "s_waitcnt vmcnt(0)\n\t" \
  "s_cmp_gt_u32 s74, s84\n\t" \
  "s_cbranch_scc1 RG1_%=\n\t" \
  "s_and_b32 s60, s74, 1023\n\t" \
  "s_cmp_lg_u32 s60, 0\n\t" \
  "s_cbranch_scc1 RRETRY1_%=\n\t" \
  "buffer_load_dword v46, off, s[28:31], s92 sc1\n\t" \
  "s_waitcnt vmcnt(0)\n\t" \
  "v_readfirstlane_b32 s60, v46\n\t" \
  "s_nop 0\n\t" \
  "s_cmp_eq_u32 s60, 0\n\t" \
  "s_cbranch_scc1 RRETRY1_%=\n\t" \
  "RG1_%=:\n\t" \
  "s_mov_b32 s84, 0\n\t" \
  "s_lshl_b32 s60, s36, 4\n\t" \
  "s_or_b32 s60, s60, s48\n\t" \
  "s_or_b32 s60, s60, 536870912\n\t" \
  "v_mov_b32 v76, s60\n\t" \
  "v_mov_b32 v77, 0\n\t" \
  "s_mov_b64 exec, 1\n\t" \
  "buffer_atomic_cmpswap v[76:77], off, s[28:31], s92 sc1\n\t" \
  "s_mov_b64 exec, -1\n\t" \
  "s_branch RDONE1_%=\n\t" \
  "LFAIL1_%=:\n\t" \
  "v_max_u32 v75, v0, v1\n\t" \
  "v_max3_u32 v75, v75, v2, v3\n\t" \
  "v_max3_u32 v75, v75, v4, v5\n\t" \
  "v_max3_u32 v75, v75, v6, v7\n\t" \
  "v_max3_u32 v75, v75, v8, v9\n\t" \
  "v_max3_u32 v75, v75, v10, v11\n\t" \
  "v_max3_u32 v75, v75, v12, v13\n\t" \
  "v_max3_u32 v75, v75, v14, v15\n\t" \
  "v_max3_u32 v75, v75, v16, v17\n\t" \
  "v_max3_u32 v75, v75, v18, v19\n\t" \
  "v_max3_u32 v75, v75, v20, v21\n\t" \
  "v_max3_u32 v75, v75, v22, v23\n\t" \
  "v_max3_u32 v75, v75, v24, v25\n\t" \
  "v_max3_u32 v75, v75, v26, v27\n\t" \
  "v_max3_u32 v75, v75, v28, v29\n\t" \
  "v_max3_u32 v75, v75, v30, v31\n\t" \
  "v_max3_u32 v75, v75, v32, v33\n\t" \
  "v_max3_u32 v75, v75, v34, v35\n\t" \
  "v_cmp_eq_u32 vcc, -1, v75\n\t" \
  "s_cbranch_vccz LGO1_%=\n\t" \
  "s_add_u32 s68, s68, 1\n\t" \
  "s_waitcnt lgkmcnt(0)\n\t" \
  "s_cmp_gt_u32 s68, s83\n\t" \
  "s_cbranch_scc1 LGV1_%=\n\t" \
  "s_barrier\n\t" \
  "s_waitcnt vmcnt(8)\n\t" \
  "s_branch LG1_%=\n\t" \
  "RWT1_%=f:\n\t" \
  "s_cmp_lt_i32 s73, -2\n\t" \
  "s_cbranch_scc0 RWT1_%=g\n\t" \
  "s_nop 4\n\t" \
  "s_branch RWT1_%=g\n\t" \
  "LGV1_%=:\n\t" \
  "s_mov_b32 s83, 0\n\t" \
  "s_lshl_b32 s60, s36, 4\n\t" \
  "s_or_b32 s60, s60, s48\n\t" \
  "s_or_b32 s60, s60, 805306368\n\t" \
  "v_mov_b32 v76, s60\n\t" \
  "v_mov_b32 v77, 0\n\t" \
  "s_mov_b64 exec, 1\n\t" \
  "buffer_atomic_cmpswap v[76:77], off, s[28:31], s92 sc1\n\t" \
  "s_mov_b64 exec, -1\n\t" \
  "s_branch LGO1_%=\n\t" \
  "LLAST1_%=:\n\t" \
  "s_waitcnt vmcnt(0) lgkmcnt(0)\n\t" \
  "s_barrier\n\t" \
  "s_branch LEND_%=\n\t" \
  ".p2align 6\n\t" \
  "LG2_%=:\n\t" \
  "s_barrier\n\t" \
  "buffer_load_dwordx4 v[52:55], v69, s[28:31], s50 offen sc1\n\t" \
  "buffer_load_dwordx4 v[56:59], v69, s[28:31], s50 offen offset:1024 sc1\n\t" \
  "s_add_u32 s64, s50, 18432\n\t" \
  "s_movk_i32 m0, 11264\n\t" \
  "v_lshlrev_b32 v75, 1, v69\n\t" \
  "buffer_load_dword v75, s[28:31], s64 offen sc1 lds\n\t" \
  "ds_read_b128 v[44:47], v70 offset:0\n\t" \
  "ds_read_b128 v[48:51], v70 offset:1024\n\t" \
  "ds_read_b128 v[60:63], v70 offset:2048\n\t" \
  "ds_read_b128 v[64:67], v70 offset:3072\n\t" \
  "v_mov_b64 v[36:37], 0\n\t" \
  "v_mov_b64 v[38:39], 0\n\t" \
  "v_mov_b64 v[40:41], 0\n\t" \
  "v_mov_b64 v[42:43], 0\n\t" \
  "v_lshrrev_b32 v71, 1, v69\n\t" \
  "v_lshrrev_b32 v72, 3, v69\n\t" \
  "v_and_b32 v72, 0x78, v72\n\t" \
  "v_and_b32 v74, 0x30, v69\n\t" \
  "v_lshl_add_u32 v72, v74, 3, v72\n\t" \
  "s_add_u32 s54, s53, s36\n\t" \
  "s_add_u32 s55, s54, 557056\n\t" \
  "s_add_u32 s54, s54, 131072\n\t" \
  "s_add_u32 s75, s54, 262144\n\t" \
  "s_add_u32 s80, s53, s26\n\t" \
  "s_barrier\n\t" \
  "ds_read_b128 v[0:3], v69 offset:0\n\t" \
  "ds_read_b128 v[4:7], v69 offset:1024\n\t" \
  "ds_read_b128 v[8:11], v69 offset:2048\n\t" \
  "ds_read_b128 v[12:15], v69 offset:3072\n\t" \
  "s_waitcnt lgkmcnt(3)\n\t" \
  "v_smfmac_f32_16x16x64_bf16 v[36:39], v[0:3], v[44:51], v68\n\t" \
  "ds_read_b128 v[44:47], v70 offset:4096\n\t" \
  "v_smfmac_f32_16x16x64_bf16 v[40:43], v[0:3], v[60:67], v68\n\t" \
  "ds_read_b128 v[48:51], v70 offset:5120\n\t" \
  "s_waitcnt lgkmcnt(4)\n\t" \
  "v_smfmac_f32_16x16x64_bf16 v[36:39], v[4:7], a[32:39], v68\n\t" \
  "ds_read_b128 v[60:63], v70 offset:6144\n\t" \
  "v_smfmac_f32_16x16x64_bf16 v[40:43], v[4:7], a[40:47], v68\n\t" \
  "ds_read_b128 v[64:67], v70 offset:7168\n\t" \
  "s_waitcnt lgkmcnt(5)\n\t" \
  "v_smfmac_f32_16x16x64_bf16 v[36:39], v[8:11], a[80:87], v68\n\t" \
  "s_barrier\n\t" \
  "ds_read_b128 v[16:19], v69 offset:4096\n\t" \
  "v_smfmac_f32_16x16x64_bf16 v[40:43], v[8:11], a[88:95], v68\n\t" \
  "ds_read_b128 v[20:23], v69 offset:5120\n\t" \
  "s_waitcnt lgkmcnt(6)\n\t" \
  "v_smfmac_f32_16x16x64_bf16 v[36:39], v[12:15], a[120:127], v68\n\t" \
  "ds_read_b128 v[24:27], v69 offset:6144\n\t" \
  "s_waitcnt lgkmcnt(5)\n\t" \
  "v_smfmac_f32_16x16x64_bf16 v[40:43], v[12:15], v[44:51], v68\n\t" \
  "ds_read_b128 v[28:31], v69 offset:7168\n\t" \
  "s_waitcnt lgkmcnt(3)\n\t" \
  "v_smfmac_f32_16x16x64_bf16 v[36:39], v[16:19], a[160:167], v68\n\t" \
  "ds_read_b128 v[32:35], v69 offset:8192\n\t" \
  "v_smfmac_f32_16x16x64_bf16 v[40:43], v[16:19], a[168:175], v68\n\t" \
  "s_waitcnt lgkmcnt(3)\n\t" \
  "v_mov_b64 v[44:45], 0\n\t" \
  "v_smfmac_f32_16x16x64_bf16 v[36:39], v[20:23], a[200:207], v68\n\t" \
  "s_waitcnt lgkmcnt(2)\n\t" \
  "v_mov_b64 v[46:47], 0\n\t" \
  "v_smfmac_f32_16x16x64_bf16 v[36:39], v[24:27], a[240:247], v68\n\t" \
  "s_waitcnt lgkmcnt(1)\n\t" \
  "v_mov_b64 v[48:49], 0\n\t" \
  "v_smfmac_f32_16x16x64_bf16 v[36:39], v[28:31], v[112:119], v68\n\t" \
  "s_waitcnt lgkmcnt(0)\n\t" \
  "v_mov_b64 v[50:51], 0\n\t" \
  "v_smfmac_f32_16x16x64_bf16 v[36:39], v[32:35], v[160:167], v68\n\t" \
  "v_smfmac_f32_16x16x64_bf16 v[40:43], v[20:23], v[60:67], v68\n\t" \
  "ds_read_b128 v[60:63], v70 offset:8192\n\t" \
  "v_smfmac_f32_16x16x64_bf16 v[40:43], v[24:27], a[248:255], v68\n\t" \
  "ds_read_b128 v[64:67], v70 offset:9216\n\t" \
  "v_add_f32 v74, v36, v38\n\t" \
  "v_smfmac_f32_16x16x64_bf16 v[40:43], v[28:31], v[120:127], v68\n\t" \
  "v_add_f32 v37, v37, v39\n\t" \
  "v_smfmac_f32_16x16x64_bf16 v[40:43], v[32:35], v[168:175], v68\n\t" \
  "v_cmp_u_f32 vcc, v74, v37\n\t" \
  "v_smfmac_f32_16x16x64_bf16 v[44:47], v[0:3], a[0:7], v68\n\t" \
  "s_cbranch_vccnz LFAIL2_%=\n\t" \
  "LGO2_%=:\n\t" \
  "v_cvt_pk_bf16_f32 v74, v74, v37\n\t" \
  "v_smfmac_f32_16x16x64_bf16 v[44:47], v[4:7], a[48:55], v68\n\t" \
  "s_waitcnt lgkmcnt(0)\n\t" \
  "v_add_f32 v75, v40, v42\n\t" \
  "v_smfmac_f32_16x16x64_bf16 v[44:47], v[8:11], v[60:67], v68\n\t" \
  "ds_read_b128 v[60:63], v70 offset:10240\n\t" \
  "v_add_f32 v41, v41, v43\n\t" \
  "v_cvt_pk_bf16_f32 v75, v75, v41\n\t" \
  "v_smfmac_f32_16x16x64_bf16 v[44:47], v[12:15], a[128:135], v68\n\t" \
  "ds_write2st64_b32 v71, v74, v75 offset0:40 offset1:42\n\t" \
  "v_smfmac_f32_16x16x64_bf16 v[44:47], v[16:19], a[176:183], v68\n\t" \
  "ds_read_b128 v[64:67], v70 offset:11264\n\t" \
  "v_smfmac_f32_16x16x64_bf16 v[44:47], v[20:23], a[208:215], v68\n\t" \
  "v_smfmac_f32_16x16x64_bf16 v[44:47], v[24:27], v[80:87], v68\n\t" \
  "v_smfmac_f32_16x16x64_bf16 v[44:47], v[28:31], v[128:135], v68\n\t" \
  "s_waitcnt lgkmcnt(0)\n\t" \
  "v_smfmac_f32_16x16x64_bf16 v[44:47], v[32:35], v[60:67], v68\n\t" \
  "s_barrier\n\t" \
  "ds_read_b64_tr_b16 v[76:77], v72 offset:10240\n\t" \
  "v_smfmac_f32_16x16x64_bf16 v[48:51], v[0:3], a[8:15], v68\n\t" \
  "ds_read_b64_tr_b16 v[78:79], v72 offset:10752\n\t" \
  "v_smfmac_f32_16x16x64_bf16 v[48:51], v[4:7], a[56:63], v68\n\t" \
  "buffer_load_dwordx4 v[60:63], v69, s[28:31], s64 offen sc1\n\t" \
  "v_add_f32 v74, v44, v46\n\t" \
  "v_smfmac_f32_16x16x64_bf16 v[48:51], v[8:11], a[96:103], v68\n\t" \
  "v_add_f32 v45, v45, v47\n\t" \
  "v_smfmac_f32_16x16x64_bf16 v[48:51], v[12:15], a[136:143], v68\n\t" \
  "buffer_load_dwordx4 v[64:67], v69, s[28:31], s64 offen offset:1024 sc1\n\t" \
  "s_waitcnt vmcnt(4)\n\t" \
  "s_waitcnt lgkmcnt(0)\n\t" \
  "v_cvt_pk_bf16_f32 v74, v74, v45\n\t" \
  "v_mfma_f32_16x16x32_bf16 v[36:39], v[52:55], v[76:79], 0\n\t" \
  "s_waitcnt vmcnt(3)\n\t" \
  "v_mfma_f32_16x16x32_bf16 v[40:43], v[56:59], v[76:79], 0\n\t" \
  "v_mov_b64 v[52:53], 0\n\t" \
  "v_mov_b64 v[54:55], 0\n\t" \
  "v_smfmac_f32_16x16x64_bf16 v[48:51], v[16:19], a[184:191], v68\n\t" \
  "v_mov_b64 v[56:57], 0\n\t" \
  "v_mov_b64 v[58:59], 0\n\t" \
  "v_smfmac_f32_16x16x64_bf16 v[48:51], v[20:23], a[216:223], v68\n\t" \
  "ds_write_b32 v71, v74 offset:1024\n\t" \
  "v_add_f32_dpp v36, v40, v36 quad_perm:[2,3,0,1] row_mask:0xf bank_mask:0xf\n\t" \
  "v_smfmac_f32_16x16x64_bf16 v[48:51], v[24:27], v[88:95], v68\n\t" \
  "v_add_f32_dpp v37, v41, v37 quad_perm:[2,3,0,1] row_mask:0xf bank_mask:0xf\n\t" \
  "v_smfmac_f32_16x16x64_bf16 v[48:51], v[28:31], v[136:143], v68\n\t" \
  "v_add_f32_dpp v38, v42, v38 quad_perm:[2,3,0,1] row_mask:0xf bank_mask:0xf\n\t" \
  "v_smfmac_f32_16x16x64_bf16 v[48:51], v[32:35], v[176:183], v68\n\t" \
  "v_add_f32_dpp v39, v43, v39 quad_perm:[2,3,0,1] row_mask:0xf bank_mask:0xf\n\t" \
  "v_smfmac_f32_16x16x64_bf16 v[52:55], v[0:3], a[16:23], v68\n\t" \
  "s_mov_b64 exec, s[58:59]\n\t" \
  "ds_write_b128 v69, v[36:39] offset:3072\n\t" \
  "s_mov_b64 exec, -1\n\t" \
  "v_smfmac_f32_16x16x64_bf16 v[52:55], v[4:7], a[64:71], v68\n\t" \
  "v_add_f32 v75, v48, v50\n\t" \
  "v_add_f32 v49, v49, v51\n\t" \
  "v_cvt_pk_bf16_f32 v75, v75, v49\n\t" \
  "v_smfmac_f32_16x16x64_bf16 v[56:59], v[0:3], a[24:31], v68\n\t" \
  "ds_write_b32 v71, v75 offset:1536\n\t" \
  "v_smfmac_f32_16x16x64_bf16 v[56:59], v[4:7], a[72:79], v68\n\t" \
  "ds_read_b64_tr_b16 v[0:1], v72 offset:9216\n\t" \
  "ds_read_b64_tr_b16 v[2:3], v72 offset:9728\n\t" \
  "v_smfmac_f32_16x16x64_bf16 v[52:55], v[8:11], a[104:111], v68\n\t" \
  "ds_read_b128 v[36:39], v70 offset:12288\n\t" \
  "v_smfmac_f32_16x16x64_bf16 v[56:59], v[8:11], a[112:119], v68\n\t" \
  "ds_read_b128 v[40:43], v70 offset:13312\n\t" \
  "v_smfmac_f32_16x16x64_bf16 v[52:55], v[12:15], a[144:151], v68\n\t" \
  "s_waitcnt lgkmcnt(4)\n\t" \
  "s_barrier\n\t" \
  "ds_read_b64_tr_b16 v[44:45], v72 offset:1024\n\t" \
  "ds_read_b64_tr_b16 v[46:47], v72 offset:1536\n\t" \
  "v_smfmac_f32_16x16x64_bf16 v[56:59], v[12:15], a[152:159], v68\n\t" \
  "ds_read_b64_tr_b16 v[48:49], v72 offset:0\n\t" \
  "ds_read_b64_tr_b16 v[50:51], v72 offset:512\n\t" \
  "v_smfmac_f32_16x16x64_bf16 v[52:55], v[16:19], a[192:199], v68\n\t" \
  "ds_read_b128 v[4:7], v69 offset:2048\n\t" \
  "s_waitcnt lgkmcnt(5)\n\t" \
  "v_smfmac_f32_16x16x64_bf16 v[56:59], v[16:19], v[36:43], v68\n\t" \
  "ds_read_b128 v[8:11], v69 offset:3072\n\t" \
  "s_waitcnt lgkmcnt(4)\n\t" \
  "v_mfma_f32_16x16x32_bf16 v[12:15], v[44:47], v[76:79], 0\n\t" \
  "ds_read_b128 v[36:39], v70 offset:14336\n\t" \
  "s_waitcnt lgkmcnt(3)\n\t" \
  "v_mfma_f32_16x16x32_bf16 v[12:15], v[48:51], v[0:3], v[12:15]\n\t" \
  "ds_read_b128 v[40:43], v70 offset:15360\n\t" \
  "v_smfmac_f32_16x16x64_bf16 v[52:55], v[20:23], a[224:231], v68\n\t" \
  "buffer_store_dwordx4 v[44:47], v69, s[28:31], s69 offen offset:1024 sc1\n\t" \
  "s_waitcnt lgkmcnt(2)\n\t" \
  "v_add_f32 v4, v4, v8\n\t" \
  "v_add_f32 v5, v5, v9\n\t" \
  "v_add_f32 v6, v6, v10\n\t" \
  "v_cndmask_b32 v78, v13, v14, s[56:57]\n\t" \
  "v_cndmask_b32 v76, v12, v15, s[56:57]\n\t" \
  "v_add_f32 v7, v7, v11\n\t" \
  "v_max3_f32 v77, v4, v5, v6\n\t" \
  "v_add_f32_dpp v76, v78, v76 quad_perm:[2,3,0,1] row_mask:0xf bank_mask:0xf\n\t" \
  "v_max3_f32 v77, v77, v7, v76\n\t" \
  "v_smfmac_f32_16x16x64_bf16 v[56:59], v[20:23], a[232:239], v68\n\t" \
  "ds_read_b128 v[44:47], v70 offset:16384\n\t" \
  "v_max_f32_dpp v77, v77, v77 quad_perm:[2,3,0,1] row_mask:0xf bank_mask:0xf\n\t" \
  "v_mov_b32 v78, v77\n\t" \
  "v_smfmac_f32_16x16x64_bf16 v[52:55], v[24:27], v[96:103], v68\n\t" \
  "ds_read_b128 v[48:51], v70 offset:17408\n\t" \
  "v_permlane16_swap_b32 v77, v78\n\t" \
  "v_max_f32 v77, v77, v78\n\t" \
  "v_mov_b32 v78, v77\n\t" \
  "v_smfmac_f32_16x16x64_bf16 v[52:55], v[28:31], v[144:151], v68\n\t" \
  "ds_read_b128 v[16:19], v70 offset:18432\n\t" \
  "v_permlane32_swap_b32 v77, v78\n\t" \
  "v_max_f32 v77, v77, v78\n\t" \
  "v_smfmac_f32_16x16x64_bf16 v[52:55], v[32:35], v[184:191], v68\n\t" \
  "ds_read_b128 v[20:23], v70 offset:19456\n\t" \
  "v_mul_f32 v77, 0xbe0293ee, v77\n\t" \
  "v_smfmac_f32_16x16x64_bf16 v[56:59], v[24:27], v[104:111], v68\n\t" \
  "v_fmamk_f32 v4, v4, 0x3e0293ee, v77\n\t" \
  "v_smfmac_f32_16x16x64_bf16 v[56:59], v[28:31], v[152:159], v68\n\t" \
  "ds_read_b128 v[24:27], v70 offset:22528\n\t" \
  "s_waitcnt lgkmcnt(4)\n\t" \
  "v_fmamk_f32 v5, v5, 0x3e0293ee, v77\n\t" \
  "v_smfmac_f32_16x16x64_bf16 v[56:59], v[32:35], v[36:43], v68\n\t" \
  "ds_read_b128 v[28:31], v70 offset:23552\n\t" \
  "v_fmamk_f32 v6, v6, 0x3e0293ee, v77\n\t" \
  "v_fmamk_f32 v7, v7, 0x3e0293ee, v77\n\t" \
  "v_fmamk_f32 v76, v76, 0x3e0293ee, v77\n\t" \
  "v_exp_f32 v4, v4\n\t" \
  "v_exp_f32 v5, v5\n\t" \
  "v_exp_f32 v6, v6\n\t" \
  "v_exp_f32 v7, v7\n\t" \
  "v_cvt_pk_bf16_f32 v4, v4, v5\n\t" \
  "v_exp_f32 v76, v76\n\t" \
  "v_cvt_pk_bf16_f32 v5, v6, v7\n\t" \
  "v_mov_b32_dpp v12, v4 quad_perm:[0,1,0,1] row_mask:0xf bank_mask:0xf\n\t" \
  "v_mov_b32_dpp v14, v4 quad_perm:[2,3,2,3] row_mask:0xf bank_mask:0xf\n\t" \
  "v_mov_b32_dpp v13, v5 quad_perm:[0,1,0,1] row_mask:0xf bank_mask:0xf\n\t" \
  "v_mov_b32_dpp v15, v5 quad_perm:[2,3,2,3] row_mask:0xf bank_mask:0xf\n\t" \
  "v_add_f32 v52, v52, v54\n\t" \
  "v_add_f32 v53, v53, v55\n\t" \
  "s_waitcnt vmcnt(2)\n\t" \
  "v_mfma_f32_16x16x32_bf16 v[0:3], v[60:63], v[12:15], 0\n\t" \
  "ds_read_b128 v[36:39], v70 offset:20480\n\t" \
  "v_add_f32 v56, v56, v58\n\t" \
  "v_add_f32 v57, v57, v59\n\t" \
  "v_cvt_pk_bf16_f32 v52, v52, v53\n\t" \
  "v_cvt_pk_bf16_f32 v53, v56, v57\n\t" \
  "s_waitcnt vmcnt(1)\n\t" \
  "v_mfma_f32_16x16x32_bf16 v[4:7], v[64:67], v[12:15], 0\n\t" \
  "ds_read_b128 v[40:43], v70 offset:21504\n\t" \
  "v_mov_b64 v[56:57], s[88:89]\n\t" \
  "v_mov_b64 v[58:59], s[88:89]\n\t" \
  "v_mov_b32_dpp v78, v76 quad_perm:[0,1,0,1] row_mask:0xf bank_mask:0xf\n\t" \
  "v_mov_b32_dpp v79, v76 quad_perm:[2,3,2,3] row_mask:0xf bank_mask:0xf\n\t" \
  "v_mfma_f32_16x16x32_bf16 v[32:35], v[56:59], v[12:15], 0\n\t" \
  "ds_read_b128 v[60:63], v70 offset:24576\n\t" \
  "v_cvt_pk_bf16_f32 v11, v78, v79\n\t" \
  "v_cvt_pk_bf16_f32 v8, v78, v79\n\t" \
  "v_mov_b64 v[54:55], 0\n\t" \
  "v_mov_b32 v9, 0\n\t" \
  "v_mov_b32 v10, 0\n\t" \
  "v_mov_b64 v[14:15], 0\n\t" \
  "s_nop 0\n\t" \
  "v_mfma_f32_16x16x32_bf16 v[32:35], v[54:57], v[8:11], v[32:35]\n\t" \
  "buffer_store_dwordx2 v[52:53], v71, s[28:31], s69 offen offset:3072 sc1\n\t" \
  "s_cmp_eq_u32 s48, 63\n\t" \
  "v_mfma_f32_16x16x32_bf16 v[0:3], v[52:55], v[8:11], v[0:3]\n\t" \
  "ds_read_b128 v[64:67], v70 offset:25600\n\t" \
  "v_mov_b64 v[56:57], 0\n\t" \
  "v_mov_b64 v[58:59], 0\n\t" \
  "v_mfma_f32_16x16x32_bf16 v[4:7], v[52:55], v[10:13], v[4:7]\n\t" \
  "s_cbranch_scc1 LLAST2_%=\n\t" \
  "v_rcp_f32 v74, v32\n\t" \
  "v_mov_b64 v[12:13], 0\n\t" \
  "v_mov_b32 v8, 0\n\t" \
  "v_pk_mul_f32 v[0:1], v[0:1], v[74:75] op_sel_hi:[1,0]\n\t" \
  "v_pk_mul_f32 v[2:3], v[2:3], v[74:75] op_sel_hi:[1,0]\n\t" \
  "v_cvt_pk_bf16_f32 v76, v0, v1\n\t" \
  "v_cvt_pk_bf16_f32 v77, v2, v3\n\t" \
  "v_pk_mul_f32 v[4:5], v[4:5], v[74:75] op_sel_hi:[1,0]\n\t" \
  "v_pk_mul_f32 v[6:7], v[6:7], v[74:75] op_sel_hi:[1,0]\n\t" \
  "v_cvt_pk_bf16_f32 v78, v4, v5\n\t" \
  "v_cvt_pk_bf16_f32 v79, v6, v7\n\t" \
  "ds_write_b128 v69, v[76:79] offset:6144\n\t" \
  "s_waitcnt lgkmcnt(2)\n\t" \
  "v_smfmac_f32_16x16x64_bf16 v[12:15], v[76:79], v[44:51], v68\n\t" \
  "ds_read_b128 v[44:47], v70 offset:26624\n\t" \
  "v_mov_b32 v11, 0\n\t" \
  "v_smfmac_f32_16x16x64_bf16 v[56:59], v[76:79], v[16:23], v68\n\t" \
  "ds_read_b128 v[48:51], v70 offset:27648\n\t" \
  "v_mov_b64 v[52:53], 0\n\t" \
  "v_smfmac_f32_16x16x64_bf16 v[8:11], v[76:79], v[36:43], v68\n\t" \
  "ds_read_b128 v[16:19], v70 offset:28672\n\t" \
  "v_smfmac_f32_16x16x64_bf16 v[52:55], v[76:79], v[24:31], v68\n\t" \
  "s_waitcnt lgkmcnt(3)\n\t" \
  "s_barrier\n\t" \
  "ds_read_b128 v[0:3], v69 offset:7168\n\t" \
  "ds_read_b128 v[4:7], v69 offset:4096\n\t" \
  "v_lshrrev_b32 v71, 7, v69\n\t" \
  "v_and_b32 v74, 0x70, v69\n\t" \
  "v_lshl_add_u32 v71, v71, 14, v74\n\t" \
  "v_bfe_u32 v72, v69, 2, 7\n\t" \
  "v_and_b32 v74, 0x200, v69\n\t" \
  "s_waitcnt lgkmcnt(1)\n\t" \
  "v_smfmac_f32_16x16x64_bf16 v[8:11], v[0:3], v[60:67], v68\n\t" \
  "ds_read_b128 v[20:23], v70 offset:29696\n\t" \
  "v_mov_b64 v[76:77], 0\n\t" \
  "v_smfmac_f32_16x16x64_bf16 v[12:15], v[0:3], v[200:207], v68\n\t" \
  "buffer_load_dword v75, off, s[28:31], s80 sc1\n\t" \
  "ds_read_b128 v[32:35], v69 offset:5120\n\t" \
  "v_mov_b64 v[78:79], 0\n\t" \
  "v_smfmac_f32_16x16x64_bf16 v[52:55], v[0:3], v[44:51], v68\n\t" \
  "ds_read_b128 v[24:27], v70 offset:30720\n\t" \
  "v_lshl_add_u32 v72, v74, 3, v72\n\t" \
  "v_smfmac_f32_16x16x64_bf16 v[56:59], v[0:3], v[232:239], v68\n\t" \
  "ds_read_b128 v[28:31], v70 offset:31744\n\t" \
  "s_waitcnt lgkmcnt(4)\n\t" \
  "v_smfmac_f32_16x16x64_bf16 v[12:15], v[4:7], v[208:215], v68\n\t" \
  "ds_read_b128 v[60:63], v70 offset:32768\n\t" \
  "s_waitcnt lgkmcnt(4)\n\t" \
  "v_smfmac_f32_16x16x64_bf16 v[8:11], v[4:7], v[16:23], v68\n\t" \
  "ds_read_b128 v[64:67], v70 offset:33792\n\t" \
  "s_waitcnt lgkmcnt(4)\n\t" \
  "v_smfmac_f32_16x16x64_bf16 v[8:11], v[32:35], v[192:199], v68\n\t" \
  "ds_read_b128 v[36:39], v73 offset:4096\n\t" \
  "v_smfmac_f32_16x16x64_bf16 v[12:15], v[32:35], v[216:223], v68\n\t" \
  "ds_read_b128 v[40:43], v73 offset:6144\n\t" \
  "s_waitcnt lgkmcnt(4)\n\t" \
  "v_smfmac_f32_16x16x64_bf16 v[52:55], v[4:7], v[24:31], v68\n\t" \
  "ds_read_b128 v[44:47], v70 offset:34816\n\t" \
  "v_cvt_pk_bf16_f32 v8, v8, v9\n\t" \
  "v_smfmac_f32_16x16x64_bf16 v[52:55], v[32:35], v[224:231], v68\n\t" \
  "ds_read_b128 v[48:51], v70 offset:35840\n\t" \
  "v_cvt_pk_bf16_f32 v9, v10, v11\n\t" \
  "v_smfmac_f32_16x16x64_bf16 v[56:59], v[4:7], v[240:247], v68\n\t" \
  "s_waitcnt lgkmcnt(4)\n\t" \
  "v_cvt_pk_bf16_f32 v10, v12, v13\n\t" \
  "v_smfmac_f32_16x16x64_bf16 v[56:59], v[32:35], v[60:67], v68\n\t" \
  "s_waitcnt lgkmcnt(3)\n\t" \
  "v_cvt_pk_bf16_f32 v11, v14, v15\n\t" \
  "v_smfmac_f32_16x16x64_bf16 v[76:79], v[36:39], v[248:255], v68\n\t" \
  "buffer_store_dwordx4 v[8:11], v71, s[28:31], s54 offen sc1\n\t" \
  "s_waitcnt lgkmcnt(0)\n\t" \
  "v_cvt_pk_bf16_f32 v52, v52, v53\n\t" \
  "v_smfmac_f32_16x16x64_bf16 v[76:79], v[40:43], v[44:51], v68\n\t" \
  "v_cvt_pk_bf16_f32 v53, v54, v55\n\t" \
  "v_cvt_pk_bf16_f32 v54, v56, v57\n\t" \
  "v_cvt_pk_bf16_f32 v55, v58, v59\n\t" \
  "buffer_store_dwordx4 v[52:55], v71, s[28:31], s75 offen sc1\n\t" \
  "s_nop 2\n\t" \
  "v_add_f32 v76, v76, v78\n\t" \
  "v_add_f32 v77, v77, v79\n\t" \
  "v_cvt_pk_bf16_f32 v76, v76, v77\n\t" \
  "buffer_store_dword v76, v72, s[28:31], s55 offen sc1\n\t" \
  "s_memrealtime s[70:71]\n\t" \
  "s_waitcnt vmcnt(3)\n\t" \
  "v_readfirstlane_b32 s65, v75\n\t" \
  "s_barrier\n\t" \
  "s_waitcnt lgkmcnt(0)\n\t" \
  "s_not_b32 s65, s65\n\t" \
  "s_lshl_b32 s61, s70, 8\n\t" \
  "s_andn2_b32 s64, s65, 0xff\n\t" \
  "s_sub_u32 s64, s64, s61\n\t" \
  "s_ashr_i32 s64, s64, 8\n\t" \
  "s_cmp_eq_u32 s65, 0\n\t" \
  "s_cselect_b32 s64, " CH_DLF ", s64\n\t" \
  "s_min_i32 s64, s64, s34\n\t" \
  "s_max_i32 s64, s64, " CH_DLL1 "\n\t" \
  "s_add_u32 s64, s64, s70\n\t" \
  "s_mov_b32 s74, 0\n\t" \
  "s_movk_i32 s60, 8192\n\t" \
  "RWT2_%=:\n\t" \
  "s_memrealtime s[72:73]\n\t" \
  "s_sub_u32 s60, s60, 1\n\t" \
  "s_cselect_b32 s61, 0, -1\n\t" \
  "s_waitcnt lgkmcnt(0)\n\t" \
  "s_sub_i32 s73, s72, s64\n\t" \
  "s_and_b32 s73, s73, s61\n\t" \
  "s_cmp_lt_i32 s73, " CH_FIN "\n\t" \
  "s_cbranch_scc1 RWT2_%=\n\t" \
  "s_cmp_lt_i32 s73, 0\n\t" \
  "s_cbranch_scc1 RWT2_%=f\n\t" \
  "RWT2_%=g:\n\t" \
  "RRETRY2_%=:\n\t" \
  "buffer_load_dwordx4 v[0:3], v69, s[28:31], s97 offen offset:0 sc1\n\t" \
  "buffer_load_dwordx4 v[4:7], v69, s[28:31], s97 offen offset:1024 sc1\n\t" \
  "buffer_load_dwordx4 v[8:11], v69, s[28:31], s97 offen offset:2048 sc1\n\t" \
  "buffer_load_dwordx4 v[12:15], v69, s[28:31], s97 offen offset:3072 sc1\n\t" \
  "v_lshrrev_b32 v20, 2, v69\n\t" \
  "v_and_b32 v20, 16, v20\n\t" \
  "v_mov_b32 v21, 0x3f80\n\t" \
  "v_lshlrev_b32 v21, v20, v21\n\t" \
  "v_bfe_u32 v20, v69, 4, 2\n\t" \
  "v_cmp_eq_u32 vcc, 0, v20\n\t" \
  "s_nop 1\n\t" \
  "v_cndmask_b32 v32, 0, v21, vcc\n\t" \
  "v_cmp_eq_u32 vcc, 1, v20\n\t" \
  "s_nop 1\n\t" \
  "v_cndmask_b32 v33, 0, v21, vcc\n\t" \
  "v_cmp_eq_u32 vcc, 2, v20\n\t" \
  "s_nop 1\n\t" \
  "v_cndmask_b32 v34, 0, v21, vcc\n\t" \
  "v_cmp_eq_u32 vcc, 3, v20\n\t" \
  "s_nop 1\n\t" \
  "v_cndmask_b32 v35, 0, v21, vcc\n\t" \
  "v_bfe_u32 v38, v69, 7, 1\n\t" \
  "v_bfe_u32 v20, v69, 9, 1\n\t" \
  "v_lshl_add_u32 v38, v20, 2, v38\n\t" \
  "v_lshlrev_b32 v38, 12, v38\n\t" \
  "v_and_b32 v20, 0x60, v69\n\t" \
  "v_add_u32 v38, v38, v20\n\t" \
  "v_bfe_u32 v20, v69, 8, 1\n\t" \
  "v_lshl_add_u32 v38, v20, 4, v38\n\t" \
  "v_bfe_u32 v20, v69, 4, 1\n\t" \
  "v_lshl_add_u32 v38, v20, 3, v38\n\t" \
  "v_mov_b32 v44, 0\n\t" \
  "s_waitcnt vmcnt(3)\n\t" \
  "v_mfma_f32_16x16x32_bf16 v[40:43], v[32:35], v[0:3], 0\n\t" \
  "v_max3_u32 v44, v44, v0, v1\n\t" \
  "v_max3_u32 v44, v44, v2, v3\n\t" \
  "s_waitcnt vmcnt(2)\n\t" \
  "v_mfma_f32_16x16x32_bf16 v[40:43], v[32:35], v[4:7], v[40:43]\n\t" \
  "v_max3_u32 v44, v44, v4, v5\n\t" \
  "v_max3_u32 v44, v44, v6, v7\n\t" \
  "s_waitcnt vmcnt(1)\n\t" \
  "v_mfma_f32_16x16x32_bf16 v[40:43], v[32:35], v[8:11], v[40:43]\n\t" \
  "v_max3_u32 v44, v44, v8, v9\n\t" \
  "v_max3_u32 v44, v44, v10, v11\n\t" \
  "s_waitcnt vmcnt(0)\n\t" \
  "v_mfma_f32_16x16x32_bf16 v[40:43], v[32:35], v[12:15], v[40:43]\n\t" \
  "v_max3_u32 v44, v44, v12, v13\n\t" \
  "v_max3_u32 v44, v44, v14, v15\n\t" \
  "v_cmp_eq_u32 vcc, -1, v44\n\t" \
  "s_cbranch_vccnz RFAIL2_%=\n\t" \
  "s_nop 3\n\t" \
  "v_add_f32_dpp v40, v40, v40 row_ror:8 row_mask:0xf bank_mask:0xf\n\t" \
  "v_add_f32_dpp v41, v41, v41 row_ror:8 row_mask:0xf bank_mask:0xf\n\t" \
  "v_add_f32_dpp v42, v42, v42 row_ror:8 row_mask:0xf bank_mask:0xf\n\t" \
  "v_add_f32_dpp v43, v43, v43 row_ror:8 row_mask:0xf bank_mask:0xf\n\t" \
  "v_cvt_pk_bf16_f32 v40, v40, v41\n\t" \
  "v_cvt_pk_bf16_f32 v41, v42, v43\n\t" \
  "buffer_store_dwordx2 v[40:41], v38, s[28:31], s98 offen sc1\n\t" \
  "RDONE2_%=:\n\t" \
  "s_barrier\n\t" \
  "s_add_u32 s48, s48, 1\n\t" \
  "s_mov_b32 s49, s53\n\t" \
  "s_add_u32 s53, s53, " CH_SLOT "\n\t" \
  "s_add_u32 s60, s53, " CH_SKIP "\n\t" \
  "s_lshr_b32 s61, s96, 1\n\t" \
  "s_add_u32 s86, s86, 1\n\t" \
  "s_cmp_eq_u32 s86, 3\n\t" \
  "s_cselect_b32 s53, s60, s53\n\t" \
  "s_cselect_b32 s96, s61, s96\n\t" \
  "s_cselect_b32 s86, 0, s86\n\t" \
  "s_andn2_b32 s53, s53, 0x2000\n\t" \
  "s_and_b32 s60, s96, 1\n\t" \
  "s_lshl_b32 s60, s60, 13\n\t" \
  "s_or_b32 s53, s53, s60\n\t" \
  "s_mov_b32 s68, 0\n\t" \
  "s_add_u32 s97, s41, s53\n\t" \
  "s_add_u32 s98, s37, s53\n\t" \
  "s_add_u32 s82, s48, 0\n\t" \
  "s_lshr_b64 s[60:61], s[90:91], s82\n\t" \
  "s_and_b32 s60, s60, 1\n\t" \
  "s_lshl_b32 s60, s60, 13\n\t" \
  "s_add_u32 s82, s82, " CH_SG0 "\n\t" \
  "s_lshl_b32 s82, s82, 21\n\t" \
  "s_or_b32 s82, s82, s60\n\t" \
  "s_add_u32 s50, s82, s38\n\t" \
  "s_add_u32 s69, s82, s39\n\t" \
  "s_cmp_eq_u32 s34, 0\n\t" \
  "s_cbranch_scc1 LINI_%=\n\t" \
  "s_branch LG2_%=\n\t" \
  "RFAIL2_%=:\n\t" \
  "s_add_u32 s74, s74, 1\n\t" \
  "s_sleep " CH_RSL "\n\t" \
  "s_waitcnt vmcnt(0)\n\t" \
  "s_cmp_gt_u32 s74, s84\n\t" \
  "s_cbranch_scc1 RG2_%=\n\t" \
  "s_and_b32 s60, s74, 1023\n\t" \
  "s_cmp_lg_u32 s60, 0\n\t" \
  "s_cbranch_scc1 RRETRY2_%=\n\t" \
  "buffer_load_dword v46, off, s[28:31], s92 sc1\n\t" \
  "s_waitcnt vmcnt(0)\n\t" \
  "v_readfirstlane_b32 s60, v46\n\t" \
  "s_nop 0\n\t" \
  "s_cmp_eq_u32 s60, 0\n\t" \
  "s_cbranch_scc1 RRETRY2_%=\n\t" \
  "RG2_%=:\n\t" \
  "s_mov_b32 s84, 0\n\t" \
  "s_lshl_b32 s60, s36, 4\n\t" \
  "s_or_b32 s60, s60, s48\n\t" \
  "s_or_b32 s60, s60, 536870912\n\t" \
  "v_mov_b32 v76, s60\n\t" \
  "v_mov_b32 v77, 0\n\t" \
  "s_mov_b64 exec, 1\n\t" \
  "buffer_atomic_cmpswap v[76:77], off, s[28:31], s92 sc1\n\t" \
  "s_mov_b64 exec, -1\n\t" \
  "s_branch RDONE2_%=\n\t" \
  "LFAIL2_%=:\n\t" \
  "v_max_u32 v75, v0, v1\n\t" \
  "v_max3_u32 v75, v75, v2, v3\n\t" \
  "v_max3_u32 v75, v75, v4, v5\n\t" \
  "v_max3_u32 v75, v75, v6, v7\n\t" \
  "v_max3_u32 v75, v75, v8, v9\n\t" \
  "v_max3_u32 v75, v75, v10, v11\n\t" \
  "v_max3_u32 v75, v75, v12, v13\n\t" \
  "v_max3_u32 v75, v75, v14, v15\n\t" \
  "v_max3_u32 v75, v75, v16, v17\n\t" \
  "v_max3_u32 v75, v75, v18, v19\n\t" \
  "v_max3_u32 v75, v75, v20, v21\n\t" \
  "v_max3_u32 v75, v75, v22, v23\n\t" \
  "v_max3_u32 v75, v75, v24, v25\n\t" \
  "v_max3_u32 v75, v75, v26, v27\n\t" \
  "v_max3_u32 v75, v75, v28, v29\n\t" \
  "v_max3_u32 v75, v75, v30, v31\n\t" \
  "v_max3_u32 v75, v75, v32, v33\n\t" \
  "v_max3_u32 v75, v75, v34, v35\n\t" \
  "v_cmp_eq_u32 vcc, -1, v75\n\t" \
  "s_cbranch_vccz LGO2_%=\n\t" \
  "s_add_u32 s68, s68, 1\n\t" \
  "s_waitcnt lgkmcnt(0)\n\t" \
  "s_cmp_gt_u32 s68, s83\n\t" \
  "s_cbranch_scc1 LGV2_%=\n\t" \
  "s_barrier\n\t" \
  "s_waitcnt vmcnt(8)\n\t" \
  "s_branch LG2_%=\n\t" \
  "RWT2_%=f:\n\t" \
  "s_cmp_lt_i32 s73, -2\n\t" \
  "s_cbranch_scc0 RWT2_%=g\n\t" \
  "s_nop 4\n\t" \
  "s_branch RWT2_%=g\n\t" \
  "LGV2_%=:\n\t" \
  "s_mov_b32 s83, 0\n\t" \
  "s_lshl_b32 s60, s36, 4\n\t" \
  "s_or_b32 s60, s60, s48\n\t" \
  "s_or_b32 s60, s60, 805306368\n\t" \
  "v_mov_b32 v76, s60\n\t" \
  "v_mov_b32 v77, 0\n\t" \
  "s_mov_b64 exec, 1\n\t" \
  "buffer_atomic_cmpswap v[76:77], off, s[28:31], s92 sc1\n\t" \
  "s_mov_b64 exec, -1\n\t" \
  "s_branch LGO2_%=\n\t" \
  "LLAST2_%=:\n\t" \
  "s_waitcnt vmcnt(0) lgkmcnt(0)\n\t" \
  "s_barrier\n\t" \
  "s_branch LEND_%=\n\t" \
  ".p2align 6\n\t" \
  "LG3_%=:\n\t" \
  "s_barrier\n\t" \
  "buffer_load_dwordx4 v[52:55], v69, s[28:31], s50 offen sc1\n\t" \
  "buffer_load_dwordx4 v[56:59], v69, s[28:31], s50 offen offset:1024 sc1\n\t" \
  "s_add_u32 s64, s50, 16384\n\t" \
  "s_movk_i32 m0, 11264\n\t" \
  "v_lshlrev_b32 v75, 1, v69\n\t" \
  "buffer_load_dword v75, s[28:31], s64 offen sc1 lds\n\t" \
  "s_add_u32 s47, s81, " CH_DL3 "\n\t" \
  "s_lshl_b32 s47, s47, 8\n\t" \
  "s_or_b32 s47, s47, " CH_B0 "\n\t" \
  "s_cmp_lt_u32 s48, " CH_ES "\n\t" \
  "s_cselect_b32 s60, " CH_EX ", 0\n\t" \
  "s_add_u32 s47, s47, s60\n\t" \
  "s_not_b32 s47, s47\n\t" \
  "s_add_u32 s71, s53, s46\n\t" \
  "ds_read_b128 v[44:47], v70 offset:0\n\t" \
  "ds_read_b128 v[48:51], v70 offset:1024\n\t" \
  "ds_read_b128 v[60:63], v70 offset:2048\n\t" \
  "ds_read_b128 v[64:67], v70 offset:3072\n\t" \
  "v_mov_b64 v[36:37], 0\n\t" \
  "v_mov_b64 v[38:39], 0\n\t" \
  "v_mov_b64 v[40:41], 0\n\t" \
  "v_mov_b64 v[42:43], 0\n\t" \
  "v_lshrrev_b32 v71, 1, v69\n\t" \
  "v_add_u32 v71, 4, v71\n\t" \
  "v_lshrrev_b32 v72, 3, v69\n\t" \
  "v_and_b32 v72, 0x78, v72\n\t" \
  "v_and_b32 v74, 0x30, v69\n\t" \
  "v_lshl_add_u32 v72, v74, 3, v72\n\t" \
  "s_add_u32 s54, s53, s36\n\t" \
  "s_add_u32 s55, s54, 573440\n\t" \
  "s_add_u32 s54, s54, 135168\n\t" \
  "s_add_u32 s75, s54, 262144\n\t" \
  "s_add_u32 s80, s53, s26\n\t" \
  "s_or_b32 s87, s50, 0x1fff80\n\t" \
  "s_add_u32 s87, s87, 4194304\n\t" \
  "s_barrier\n\t" \
  "ds_read_b128 v[0:3], v69 offset:0\n\t" \
  "ds_read_b128 v[4:7], v69 offset:1024\n\t" \
  "ds_read_b128 v[8:11], v69 offset:2048\n\t" \
  "ds_read_b128 v[12:15], v69 offset:3072\n\t" \
  "s_waitcnt lgkmcnt(3)\n\t" \
  "v_smfmac_f32_16x16x64_bf16 v[36:39], v[0:3], v[44:51], v68\n\t" \
  "ds_read_b128 v[44:47], v70 offset:4096\n\t" \
  "v_smfmac_f32_16x16x64_bf16 v[40:43], v[0:3], v[60:67], v68\n\t" \
  "ds_read_b128 v[48:51], v70 offset:5120\n\t" \
  "s_waitcnt lgkmcnt(4)\n\t" \
  "v_smfmac_f32_16x16x64_bf16 v[36:39], v[4:7], a[32:39], v68\n\t" \
  "ds_read_b128 v[60:63], v70 offset:6144\n\t" \
  "v_smfmac_f32_16x16x64_bf16 v[40:43], v[4:7], a[40:47], v68\n\t" \
  "ds_read_b128 v[64:67], v70 offset:7168\n\t" \
  "s_waitcnt lgkmcnt(5)\n\t" \
  "v_smfmac_f32_16x16x64_bf16 v[36:39], v[8:11], a[80:87], v68\n\t" \
  "s_barrier\n\t" \
  "ds_read_b128 v[16:19], v69 offset:4096\n\t" \
  "v_smfmac_f32_16x16x64_bf16 v[40:43], v[8:11], a[88:95], v68\n\t" \
  "ds_read_b128 v[20:23], v69 offset:5120\n\t" \
  "s_waitcnt lgkmcnt(6)\n\t" \
  "v_smfmac_f32_16x16x64_bf16 v[36:39], v[12:15], a[120:127], v68\n\t" \
  "ds_read_b128 v[24:27], v69 offset:6144\n\t" \
  "s_waitcnt lgkmcnt(5)\n\t" \
  "v_smfmac_f32_16x16x64_bf16 v[40:43], v[12:15], v[44:51], v68\n\t" \
  "ds_read_b128 v[28:31], v69 offset:7168\n\t" \
  "s_waitcnt lgkmcnt(3)\n\t" \
  "v_smfmac_f32_16x16x64_bf16 v[36:39], v[16:19], a[160:167], v68\n\t" \
  "ds_read_b128 v[32:35], v69 offset:8192\n\t" \
  "v_smfmac_f32_16x16x64_bf16 v[40:43], v[16:19], a[168:175], v68\n\t" \
  "s_waitcnt lgkmcnt(3)\n\t" \
  "v_mov_b64 v[44:45], 0\n\t" \
  "v_smfmac_f32_16x16x64_bf16 v[36:39], v[20:23], a[200:207], v68\n\t" \
  "s_waitcnt lgkmcnt(2)\n\t" \
  "v_mov_b64 v[46:47], 0\n\t" \
  "v_smfmac_f32_16x16x64_bf16 v[36:39], v[24:27], a[240:247], v68\n\t" \
  "s_waitcnt lgkmcnt(1)\n\t" \
  "v_mov_b64 v[48:49], 0\n\t" \
  "v_smfmac_f32_16x16x64_bf16 v[36:39], v[28:31], v[112:119], v68\n\t" \
  "s_waitcnt lgkmcnt(0)\n\t" \
  "v_mov_b64 v[50:51], 0\n\t" \
  "v_smfmac_f32_16x16x64_bf16 v[36:39], v[32:35], v[160:167], v68\n\t" \
  "v_smfmac_f32_16x16x64_bf16 v[40:43], v[20:23], v[60:67], v68\n\t" \
  "ds_read_b128 v[60:63], v70 offset:8192\n\t" \
  "v_smfmac_f32_16x16x64_bf16 v[40:43], v[24:27], a[248:255], v68\n\t" \
  "ds_read_b128 v[64:67], v70 offset:9216\n\t" \
  "v_add_f32 v74, v36, v38\n\t" \
  "v_smfmac_f32_16x16x64_bf16 v[40:43], v[28:31], v[120:127], v68\n\t" \
  "v_add_f32 v37, v37, v39\n\t" \
  "v_smfmac_f32_16x16x64_bf16 v[40:43], v[32:35], v[168:175], v68\n\t" \
  "v_cmp_u_f32 vcc, v74, v37\n\t" \
  "v_smfmac_f32_16x16x64_bf16 v[44:47], v[0:3], a[0:7], v68\n\t" \
  "s_cbranch_vccnz LFAIL3_%=\n\t" \
  "LGO3_%=:\n\t" \
  "v_cvt_pk_bf16_f32 v74, v74, v37\n\t" \
  "v_smfmac_f32_16x16x64_bf16 v[44:47], v[4:7], a[48:55], v68\n\t" \
  "s_waitcnt lgkmcnt(0)\n\t" \
  "v_add_f32 v75, v40, v42\n\t" \
  "v_smfmac_f32_16x16x64_bf16 v[44:47], v[8:11], v[60:67], v68\n\t" \
  "ds_read_b128 v[60:63], v70 offset:10240\n\t" \
  "v_add_f32 v41, v41, v43\n\t" \
  "v_cvt_pk_bf16_f32 v75, v75, v41\n\t" \
  "v_smfmac_f32_16x16x64_bf16 v[44:47], v[12:15], a[128:135], v68\n\t" \
  "ds_write2st64_b32 v71, v74, v75 offset0:40 offset1:42\n\t" \
  "v_smfmac_f32_16x16x64_bf16 v[44:47], v[16:19], a[176:183], v68\n\t" \
  "ds_read_b128 v[64:67], v70 offset:11264\n\t" \
  "s_memrealtime s[66:67]\n\t" \
  "v_smfmac_f32_16x16x64_bf16 v[44:47], v[20:23], a[208:215], v68\n\t" \
  "v_smfmac_f32_16x16x64_bf16 v[44:47], v[24:27], v[80:87], v68\n\t" \
  "v_smfmac_f32_16x16x64_bf16 v[44:47], v[28:31], v[128:135], v68\n\t" \
  "s_waitcnt lgkmcnt(0)\n\t" \
  "v_smfmac_f32_16x16x64_bf16 v[44:47], v[32:35], v[60:67], v68\n\t" \
  "s_barrier\n\t" \
  "ds_read_b64_tr_b16 v[76:77], v72 offset:10240\n\t" \
  "v_smfmac_f32_16x16x64_bf16 v[48:51], v[0:3], a[8:15], v68\n\t" \
  "s_lshl_b32 s60, s66, 8\n\t" \
  "s_sub_u32 s60, s47, s60\n\t" \
  "ds_read_b64_tr_b16 v[78:79], v72 offset:10752\n\t" \
  "v_mov_b32 v75, s60\n\t" \
  "v_smfmac_f32_16x16x64_bf16 v[48:51], v[4:7], a[56:63], v68\n\t" \
  "s_mov_b64 exec, s[24:25]\n\t" \
  "buffer_atomic_umin v75, v69, s[28:31], s71 offen sc1\n\t" \
  "s_mov_b64 exec, -1\n\t" \
  "v_add_f32 v74, v44, v46\n\t" \
  "v_smfmac_f32_16x16x64_bf16 v[48:51], v[8:11], a[96:103], v68\n\t" \
  "buffer_load_dwordx4 v[60:63], v69, s[28:31], s64 offen sc1\n\t" \
  "v_add_f32 v45, v45, v47\n\t" \
  "v_smfmac_f32_16x16x64_bf16 v[48:51], v[12:15], a[136:143], v68\n\t" \
  "s_waitcnt vmcnt(4)\n\t" \
  "s_waitcnt lgkmcnt(0)\n\t" \
  "v_cvt_pk_bf16_f32 v74, v74, v45\n\t" \
  "v_mfma_f32_16x16x32_bf16 v[36:39], v[52:55], v[76:79], 0\n\t" \
  "s_waitcnt vmcnt(3)\n\t" \
  "v_mfma_f32_16x16x32_bf16 v[40:43], v[56:59], v[76:79], 0\n\t" \
  "v_mov_b64 v[52:53], 0\n\t" \
  "v_mov_b64 v[54:55], 0\n\t" \
  "v_smfmac_f32_16x16x64_bf16 v[48:51], v[16:19], a[184:191], v68\n\t" \
  "buffer_load_dwordx4 v[64:67], v69, s[28:31], s64 offen offset:1024 sc1\n\t" \
  "v_mov_b64 v[56:57], 0\n\t" \
  "v_mov_b64 v[58:59], 0\n\t" \
  "v_smfmac_f32_16x16x64_bf16 v[48:51], v[20:23], a[216:223], v68\n\t" \
  "ds_write_b32 v71, v74 offset:1024\n\t" \
  "v_add_f32_dpp v36, v40, v36 quad_perm:[2,3,0,1] row_mask:0xf bank_mask:0xf\n\t" \
  "v_smfmac_f32_16x16x64_bf16 v[48:51], v[24:27], v[88:95], v68\n\t" \
  "v_add_f32_dpp v37, v41, v37 quad_perm:[2,3,0,1] row_mask:0xf bank_mask:0xf\n\t" \
  "v_smfmac_f32_16x16x64_bf16 v[48:51], v[28:31], v[136:143], v68\n\t" \
  "v_add_f32_dpp v38, v42, v38 quad_perm:[2,3,0,1] row_mask:0xf bank_mask:0xf\n\t" \
  "v_smfmac_f32_16x16x64_bf16 v[48:51], v[32:35], v[176:183], v68\n\t" \
  "v_add_f32_dpp v39, v43, v39 quad_perm:[2,3,0,1] row_mask:0xf bank_mask:0xf\n\t" \
  "v_smfmac_f32_16x16x64_bf16 v[52:55], v[0:3], a[16:23], v68\n\t" \
  "s_mov_b64 exec, s[58:59]\n\t" \
  "ds_write_b128 v69, v[36:39] offset:3104\n\t" \
  "s_mov_b64 exec, -1\n\t" \
  "v_smfmac_f32_16x16x64_bf16 v[52:55], v[4:7], a[64:71], v68\n\t" \
  "v_add_f32 v75, v48, v50\n\t" \
  "v_add_f32 v49, v49, v51\n\t" \
  "v_cvt_pk_bf16_f32 v75, v75, v49\n\t" \
  "v_smfmac_f32_16x16x64_bf16 v[56:59], v[0:3], a[24:31], v68\n\t" \
  "ds_write_b32 v71, v75 offset:1536\n\t" \
  "v_smfmac_f32_16x16x64_bf16 v[56:59], v[4:7], a[72:79], v68\n\t" \
  "ds_read_b64_tr_b16 v[0:1], v72 offset:9216\n\t" \
  "ds_read_b64_tr_b16 v[2:3], v72 offset:9728\n\t" \
  "v_smfmac_f32_16x16x64_bf16 v[52:55], v[8:11], a[104:111], v68\n\t" \
  "ds_read_b128 v[36:39], v70 offset:12288\n\t" \
  "v_smfmac_f32_16x16x64_bf16 v[56:59], v[8:11], a[112:119], v68\n\t" \
  "buffer_load_dword off, s[28:31], s87 sc1 lds\n\t" \
  "ds_read_b128 v[40:43], v70 offset:13312\n\t" \
  "v_smfmac_f32_16x16x64_bf16 v[52:55], v[12:15], a[144:151], v68\n\t" \
  "s_waitcnt lgkmcnt(4)\n\t" \
  "s_barrier\n\t" \
  "ds_read_b64_tr_b16 v[44:45], v72 offset:1024\n\t" \
  "ds_read_b64_tr_b16 v[46:47], v72 offset:1536\n\t" \
  "v_smfmac_f32_16x16x64_bf16 v[56:59], v[12:15], a[152:159], v68\n\t" \
  "ds_read_b64_tr_b16 v[48:49], v72 offset:0\n\t" \
  "ds_read_b64_tr_b16 v[50:51], v72 offset:512\n\t" \
  "v_smfmac_f32_16x16x64_bf16 v[52:55], v[16:19], a[192:199], v68\n\t" \
  "ds_read_b128 v[4:7], v69 offset:2048\n\t" \
  "s_waitcnt lgkmcnt(5)\n\t" \
  "v_smfmac_f32_16x16x64_bf16 v[56:59], v[16:19], v[36:43], v68\n\t" \
  "ds_read_b128 v[8:11], v69 offset:3072\n\t" \
  "s_waitcnt lgkmcnt(4)\n\t" \
  "v_mfma_f32_16x16x32_bf16 v[12:15], v[44:47], v[76:79], 0\n\t" \
  "ds_read_b128 v[36:39], v70 offset:14336\n\t" \
  "s_waitcnt lgkmcnt(3)\n\t" \
  "v_mfma_f32_16x16x32_bf16 v[12:15], v[48:51], v[0:3], v[12:15]\n\t" \
  "ds_read_b128 v[40:43], v70 offset:15360\n\t" \
  "v_smfmac_f32_16x16x64_bf16 v[52:55], v[20:23], a[224:231], v68\n\t" \
  "s_waitcnt lgkmcnt(2)\n\t" \
  "v_add_f32 v4, v4, v8\n\t" \
  "v_add_f32 v5, v5, v9\n\t" \
  "v_add_f32 v6, v6, v10\n\t" \
  "v_cndmask_b32 v78, v13, v14, s[56:57]\n\t" \
  "v_cndmask_b32 v76, v12, v15, s[56:57]\n\t" \
  "v_add_f32 v7, v7, v11\n\t" \
  "v_max3_f32 v77, v4, v5, v6\n\t" \
  "v_add_f32_dpp v76, v78, v76 quad_perm:[2,3,0,1] row_mask:0xf bank_mask:0xf\n\t" \
  "v_max3_f32 v77, v77, v7, v76\n\t" \
  "v_smfmac_f32_16x16x64_bf16 v[56:59], v[20:23], a[232:239], v68\n\t" \
  "ds_read_b128 v[44:47], v70 offset:16384\n\t" \
  "v_max_f32_dpp v77, v77, v77 quad_perm:[2,3,0,1] row_mask:0xf bank_mask:0xf\n\t" \
  "v_mov_b32 v78, v77\n\t" \
  "v_smfmac_f32_16x16x64_bf16 v[52:55], v[24:27], v[96:103], v68\n\t" \
  "ds_read_b128 v[48:51], v70 offset:17408\n\t" \
  "v_permlane16_swap_b32 v77, v78\n\t" \
  "v_max_f32 v77, v77, v78\n\t" \
  "v_mov_b32 v78, v77\n\t" \
  "v_smfmac_f32_16x16x64_bf16 v[52:55], v[28:31], v[144:151], v68\n\t" \
  "ds_read_b128 v[16:19], v70 offset:18432\n\t" \
  "v_permlane32_swap_b32 v77, v78\n\t" \
  "v_max_f32 v77, v77, v78\n\t" \
  "v_smfmac_f32_16x16x64_bf16 v[52:55], v[32:35], v[184:191], v68\n\t" \
  "ds_read_b128 v[20:23], v70 offset:19456\n\t" \
  "v_mul_f32 v77, 0xbe0293ee, v77\n\t" \
  "v_smfmac_f32_16x16x64_bf16 v[56:59], v[24:27], v[104:111], v68\n\t" \
  "v_fmamk_f32 v4, v4, 0x3e0293ee, v77\n\t" \
  "v_smfmac_f32_16x16x64_bf16 v[56:59], v[28:31], v[152:159], v68\n\t" \
  "ds_read_b128 v[24:27], v70 offset:22528\n\t" \
  "s_waitcnt lgkmcnt(4)\n\t" \
  "v_fmamk_f32 v5, v5, 0x3e0293ee, v77\n\t" \
  "v_smfmac_f32_16x16x64_bf16 v[56:59], v[32:35], v[36:43], v68\n\t" \
  "ds_read_b128 v[28:31], v70 offset:23552\n\t" \
  "v_fmamk_f32 v6, v6, 0x3e0293ee, v77\n\t" \
  "v_fmamk_f32 v7, v7, 0x3e0293ee, v77\n\t" \
  "v_fmamk_f32 v76, v76, 0x3e0293ee, v77\n\t" \
  "v_exp_f32 v4, v4\n\t" \
  "v_exp_f32 v5, v5\n\t" \
  "v_exp_f32 v6, v6\n\t" \
  "v_exp_f32 v7, v7\n\t" \
  "v_cvt_pk_bf16_f32 v4, v4, v5\n\t" \
  "v_exp_f32 v76, v76\n\t" \
  "v_cvt_pk_bf16_f32 v5, v6, v7\n\t" \
  "v_mov_b32_dpp v12, v4 quad_perm:[0,1,0,1] row_mask:0xf bank_mask:0xf\n\t" \
  "v_mov_b32_dpp v14, v4 quad_perm:[2,3,2,3] row_mask:0xf bank_mask:0xf\n\t" \
  "v_mov_b32_dpp v13, v5 quad_perm:[0,1,0,1] row_mask:0xf bank_mask:0xf\n\t" \
  "v_mov_b32_dpp v15, v5 quad_perm:[2,3,2,3] row_mask:0xf bank_mask:0xf\n\t" \
  "v_add_f32 v52, v52, v54\n\t" \
  "v_add_f32 v53, v53, v55\n\t" \
  "s_waitcnt vmcnt(2)\n\t" \
  "v_mfma_f32_16x16x32_bf16 v[0:3], v[60:63], v[12:15], 0\n\t" \
  "ds_read_b128 v[36:39], v70 offset:20480\n\t" \
  "v_add_f32 v56, v56, v58\n\t" \
  "v_add_f32 v57, v57, v59\n\t" \
  "v_cvt_pk_bf16_f32 v52, v52, v53\n\t" \
  "v_cvt_pk_bf16_f32 v53, v56, v57\n\t" \
  "s_waitcnt vmcnt(1)\n\t" \
  "v_mfma_f32_16x16x32_bf16 v[4:7], v[64:67], v[12:15], 0\n\t" \
  "ds_read_b128 v[40:43], v70 offset:21504\n\t" \
  "v_mov_b64 v[56:57], s[88:89]\n\t" \
  "v_mov_b64 v[58:59], s[88:89]\n\t" \
  "v_mov_b32_dpp v78, v76 quad_perm:[0,1,0,1] row_mask:0xf bank_mask:0xf\n\t" \
  "v_mov_b32_dpp v79, v76 quad_perm:[2,3,2,3] row_mask:0xf bank_mask:0xf\n\t" \
  "v_mfma_f32_16x16x32_bf16 v[32:35], v[56:59], v[12:15], 0\n\t" \
  "ds_read_b128 v[60:63], v70 offset:24576\n\t" \
  "v_cvt_pk_bf16_f32 v11, v78, v79\n\t" \
  "v_cvt_pk_bf16_f32 v8, v78, v79\n\t" \
  "v_mov_b64 v[54:55], 0\n\t" \
  "v_mov_b32 v9, 0\n\t" \
  "v_mov_b32 v10, 0\n\t" \
  "v_mov_b64 v[14:15], 0\n\t" \
  "s_nop 0\n\t" \
  "v_mfma_f32_16x16x32_bf16 v[32:35], v[54:57], v[8:11], v[32:35]\n\t" \
  "s_cmp_eq_u32 s48, 63\n\t" \
  "v_mfma_f32_16x16x32_bf16 v[0:3], v[52:55], v[8:11], v[0:3]\n\t" \
  "buffer_store_dwordx2 v[52:53], v71, s[28:31], s69 offen offset:3580 sc1\n\t" \
  "ds_read_b128 v[64:67], v70 offset:25600\n\t" \
  "v_mov_b64 v[56:57], 0\n\t" \
  "v_mov_b64 v[58:59], 0\n\t" \
  "v_mfma_f32_16x16x32_bf16 v[4:7], v[52:55], v[10:13], v[4:7]\n\t" \
  "s_cbranch_scc1 LLAST3_%=\n\t" \
  "v_rcp_f32 v74, v32\n\t" \
  "v_mov_b64 v[12:13], 0\n\t" \
  "v_mov_b32 v8, 0\n\t" \
  "v_pk_mul_f32 v[0:1], v[0:1], v[74:75] op_sel_hi:[1,0]\n\t" \
  "v_pk_mul_f32 v[2:3], v[2:3], v[74:75] op_sel_hi:[1,0]\n\t" \
  "v_cvt_pk_bf16_f32 v76, v0, v1\n\t" \
  "v_cvt_pk_bf16_f32 v77, v2, v3\n\t" \
  "v_pk_mul_f32 v[4:5], v[4:5], v[74:75] op_sel_hi:[1,0]\n\t" \
  "v_pk_mul_f32 v[6:7], v[6:7], v[74:75] op_sel_hi:[1,0]\n\t" \
  "v_cvt_pk_bf16_f32 v78, v4, v5\n\t" \
  "v_cvt_pk_bf16_f32 v79, v6, v7\n\t" \
  "ds_write_b128 v69, v[76:79] offset:7168\n\t" \
  "s_waitcnt lgkmcnt(2)\n\t" \
  "v_smfmac_f32_16x16x64_bf16 v[12:15], v[76:79], v[44:51], v68\n\t" \
  "ds_read_b128 v[44:47], v70 offset:26624\n\t" \
  "v_mov_b32 v11, 0\n\t" \
  "v_smfmac_f32_16x16x64_bf16 v[56:59], v[76:79], v[16:23], v68\n\t" \
  "ds_read_b128 v[48:51], v70 offset:27648\n\t" \
  "v_mov_b64 v[52:53], 0\n\t" \
  "v_smfmac_f32_16x16x64_bf16 v[8:11], v[76:79], v[36:43], v68\n\t" \
  "ds_read_b128 v[16:19], v70 offset:28672\n\t" \
  "v_smfmac_f32_16x16x64_bf16 v[52:55], v[76:79], v[24:31], v68\n\t" \
  "s_waitcnt lgkmcnt(3)\n\t" \
  "s_barrier\n\t" \
  "ds_read_b128 v[0:3], v69 offset:6144\n\t" \
  "ds_read_b128 v[4:7], v69 offset:5120\n\t" \
  "v_lshrrev_b32 v71, 7, v69\n\t" \
  "v_and_b32 v74, 0x70, v69\n\t" \
  "v_lshl_add_u32 v71, v71, 14, v74\n\t" \
  "v_bfe_u32 v72, v69, 2, 7\n\t" \
  "v_and_b32 v74, 0x200, v69\n\t" \
  "s_waitcnt lgkmcnt(1)\n\t" \
  "v_smfmac_f32_16x16x64_bf16 v[8:11], v[0:3], v[60:67], v68\n\t" \
  "ds_read_b128 v[20:23], v70 offset:29696\n\t" \
  "v_mov_b64 v[76:77], 0\n\t" \
  "v_smfmac_f32_16x16x64_bf16 v[12:15], v[0:3], v[200:207], v68\n\t" \
  "ds_read_b128 v[32:35], v69 offset:4096\n\t" \
  "v_mov_b64 v[78:79], 0\n\t" \
  "v_smfmac_f32_16x16x64_bf16 v[52:55], v[0:3], v[44:51], v68\n\t" \
  "buffer_load_dword v75, off, s[28:31], s80 sc1\n\t" \
  "ds_read_b128 v[24:27], v70 offset:30720\n\t" \
  "v_lshl_add_u32 v72, v74, 3, v72\n\t" \
  "v_smfmac_f32_16x16x64_bf16 v[56:59], v[0:3], v[232:239], v68\n\t" \
  "ds_read_b128 v[28:31], v70 offset:31744\n\t" \
  "s_waitcnt lgkmcnt(4)\n\t" \
  "v_smfmac_f32_16x16x64_bf16 v[12:15], v[4:7], v[208:215], v68\n\t" \
  "ds_read_b128 v[60:63], v70 offset:32768\n\t" \
  "s_waitcnt lgkmcnt(4)\n\t" \
  "v_smfmac_f32_16x16x64_bf16 v[8:11], v[4:7], v[16:23], v68\n\t" \
  "ds_read_b128 v[64:67], v70 offset:33792\n\t" \
  "s_waitcnt lgkmcnt(4)\n\t" \
  "v_smfmac_f32_16x16x64_bf16 v[8:11], v[32:35], v[192:199], v68\n\t" \
  "ds_read_b128 v[36:39], v73 offset:4096\n\t" \
  "v_smfmac_f32_16x16x64_bf16 v[12:15], v[32:35], v[216:223], v68\n\t" \
  "ds_read_b128 v[40:43], v73 offset:6144\n\t" \
  "s_waitcnt lgkmcnt(4)\n\t" \
  "v_smfmac_f32_16x16x64_bf16 v[52:55], v[4:7], v[24:31], v68\n\t" \
  "ds_read_b128 v[44:47], v70 offset:34816\n\t" \
  "v_cvt_pk_bf16_f32 v8, v8, v9\n\t" \
  "v_smfmac_f32_16x16x64_bf16 v[52:55], v[32:35], v[224:231], v68\n\t" \
  "ds_read_b128 v[48:51], v70 offset:35840\n\t" \
  "v_cvt_pk_bf16_f32 v9, v10, v11\n\t" \
  "v_smfmac_f32_16x16x64_bf16 v[56:59], v[4:7], v[240:247], v68\n\t" \
  "s_waitcnt lgkmcnt(4)\n\t" \
  "v_cvt_pk_bf16_f32 v10, v12, v13\n\t" \
  "v_smfmac_f32_16x16x64_bf16 v[56:59], v[32:35], v[60:67], v68\n\t" \
  "s_waitcnt lgkmcnt(3)\n\t" \
  "v_cvt_pk_bf16_f32 v11, v14, v15\n\t" \
  "v_smfmac_f32_16x16x64_bf16 v[76:79], v[36:39], v[248:255], v68\n\t" \
  "buffer_store_dwordx4 v[8:11], v71, s[28:31], s54 offen sc1\n\t" \
  "s_waitcnt lgkmcnt(0)\n\t" \
  "v_cvt_pk_bf16_f32 v52, v52, v53\n\t" \
  "v_smfmac_f32_16x16x64_bf16 v[76:79], v[40:43], v[44:51], v68\n\t" \
  "v_cvt_pk_bf16_f32 v53, v54, v55\n\t" \
  "v_cvt_pk_bf16_f32 v54, v56, v57\n\t" \
  "v_cvt_pk_bf16_f32 v55, v58, v59\n\t" \
  "buffer_store_dwordx4 v[52:55], v71, s[28:31], s75 offen sc1\n\t" \
  "s_nop 2\n\t" \
  "v_add_f32 v76, v76, v78\n\t" \
  "v_add_f32 v77, v77, v79\n\t" \
  "v_cvt_pk_bf16_f32 v76, v76, v77\n\t" \
  "buffer_store_dword v76, v72, s[28:31], s55 offen sc1\n\t" \
  "s_memrealtime s[70:71]\n\t" \
  "s_waitcnt vmcnt(3)\n\t" \
  "v_readfirstlane_b32 s65, v75\n\t" \
  "s_barrier\n\t" \
  "s_waitcnt lgkmcnt(0)\n\t" \
  "s_not_b32 s65, s65\n\t" \
  "s_lshl_b32 s61, s70, 8\n\t" \
  "s_andn2_b32 s64, s65, 0xff\n\t" \
  "s_sub_u32 s64, s64, s61\n\t" \
  "s_ashr_i32 s64, s64, 8\n\t" \
  "s_cmp_eq_u32 s65, 0\n\t" \
  "s_cselect_b32 s64, " CH_DLF ", s64\n\t" \
  "s_min_i32 s64, s64, s34\n\t" \
  "s_max_i32 s64, s64, " CH_DLL1 "\n\t" \
  "s_add_u32 s64, s64, s70\n\t" \
  "s_mov_b32 s74, 0\n\t" \
  "v_and_b32 v37, 0x30, v69\n\t" \
  "v_add_u32 v37, s52, v37\n\t" \
  "s_mov_b32 exec_lo, 0\n\t" \
  "v_mov_b32 v37, s85\n\t" \
  "s_mov_b32 exec_lo, -1\n\t" \
  "s_cmp_lt_i32 s41, 0\n\t" \
  "s_cbranch_scc1 LIDLE_%=\n\t" \
  "s_movk_i32 s60, 8192\n\t" \
  "RWT3_%=:\n\t" \
  "s_memrealtime s[72:73]\n\t" \
  "s_sub_u32 s60, s60, 1\n\t" \
  "s_cselect_b32 s61, 0, -1\n\t" \
  "s_waitcnt lgkmcnt(0)\n\t" \
  "s_sub_i32 s73, s72, s64\n\t" \
  "s_and_b32 s73, s73, s61\n\t" \
  "s_cmp_lt_i32 s73, " CH_FIN "\n\t" \
  "s_cbranch_scc1 RWT3_%=\n\t" \
  "s_cmp_lt_i32 s73, 0\n\t" \
  "s_cbranch_scc1 RWT3_%=f\n\t" \
  "RWT3_%=g:\n\t" \
  "RRETRY3_%=:\n\t" \
  "buffer_load_dwordx4 v[0:3], v69, s[28:31], s97 offen offset:0 sc1\n\t" \
  "buffer_load_dwordx4 v[4:7], v69, s[28:31], s97 offen offset:1024 sc1\n\t" \
  "buffer_load_dwordx4 v[8:11], v69, s[28:31], s97 offen offset:2048 sc1\n\t" \
  "buffer_load_dwordx4 v[12:15], v69, s[28:31], s97 offen offset:3072 sc1\n\t" \
  "v_lshrrev_b32 v20, 2, v69\n\t" \
  "v_and_b32 v20, 16, v20\n\t" \
  "v_mov_b32 v21, 0x3f80\n\t" \
  "v_lshlrev_b32 v21, v20, v21\n\t" \
  "v_bfe_u32 v20, v69, 4, 2\n\t" \
  "v_cmp_eq_u32 vcc, 0, v20\n\t" \
  "s_nop 1\n\t" \
  "v_cndmask_b32 v32, 0, v21, vcc\n\t" \
  "v_cmp_eq_u32 vcc, 1, v20\n\t" \
  "s_nop 1\n\t" \
  "v_cndmask_b32 v33, 0, v21, vcc\n\t" \
  "v_cmp_eq_u32 vcc, 2, v20\n\t" \
  "s_nop 1\n\t" \
  "v_cndmask_b32 v34, 0, v21, vcc\n\t" \
  "v_cmp_eq_u32 vcc, 3, v20\n\t" \
  "s_nop 1\n\t" \
  "v_cndmask_b32 v35, 0, v21, vcc\n\t" \
  "v_bfe_u32 v38, v69, 7, 1\n\t" \
  "v_bfe_u32 v20, v69, 9, 1\n\t" \
  "v_lshl_add_u32 v38, v20, 2, v38\n\t" \
  "v_lshlrev_b32 v38, 12, v38\n\t" \
  "v_and_b32 v20, 0x60, v69\n\t" \
  "v_add_u32 v38, v38, v20\n\t" \
  "v_bfe_u32 v20, v69, 8, 1\n\t" \
  "v_lshl_add_u32 v38, v20, 4, v38\n\t" \
  "v_bfe_u32 v20, v69, 4, 1\n\t" \
  "v_lshl_add_u32 v38, v20, 3, v38\n\t" \
  "v_mov_b32 v44, 0\n\t" \
  "buffer_load_dword v39, v37, s[28:31], 0 offen sc1\n\t" \
  "s_waitcnt vmcnt(4)\n\t" \
  "v_mfma_f32_16x16x32_bf16 v[40:43], v[32:35], v[0:3], 0\n\t" \
  "v_max3_u32 v44, v44, v0, v1\n\t" \
  "v_max3_u32 v44, v44, v2, v3\n\t" \
  "s_waitcnt vmcnt(3)\n\t" \
  "v_mfma_f32_16x16x32_bf16 v[40:43], v[32:35], v[4:7], v[40:43]\n\t" \
  "v_max3_u32 v44, v44, v4, v5\n\t" \
  "v_max3_u32 v44, v44, v6, v7\n\t" \
  "s_waitcnt vmcnt(2)\n\t" \
  "v_mfma_f32_16x16x32_bf16 v[40:43], v[32:35], v[8:11], v[40:43]\n\t" \
  "v_max3_u32 v44, v44, v8, v9\n\t" \
  "v_max3_u32 v44, v44, v10, v11\n\t" \
  "s_waitcnt vmcnt(1)\n\t" \
  "v_mfma_f32_16x16x32_bf16 v[40:43], v[32:35], v[12:15], v[40:43]\n\t" \
  "v_max3_u32 v44, v44, v12, v13\n\t" \
  "v_max3_u32 v44, v44, v14, v15\n\t" \
  "v_cmp_eq_u32 vcc, -1, v44\n\t" \
  "s_cbranch_vccnz RFAIL3_%=\n\t" \
  "s_nop 3\n\t" \
  "v_add_f32_dpp v40, v40, v40 row_ror:8 row_mask:0xf bank_mask:0xf\n\t" \
  "v_add_f32_dpp v41, v41, v41 row_ror:8 row_mask:0xf bank_mask:0xf\n\t" \
  "v_add_f32_dpp v42, v42, v42 row_ror:8 row_mask:0xf bank_mask:0xf\n\t" \
  "v_add_f32_dpp v43, v43, v43 row_ror:8 row_mask:0xf bank_mask:0xf\n\t" \
  "v_cvt_pk_bf16_f32 v40, v40, v41\n\t" \
  "v_cvt_pk_bf16_f32 v41, v42, v43\n\t" \
  "buffer_store_dwordx2 v[40:41], v38, s[28:31], s98 offen sc1\n\t" \
  "RDONE3_%=:\n\t" \
  "s_barrier\n\t" \
  "s_waitcnt vmcnt(1)\n\t" \
  "v_cmp_eq_u32 vcc, 0, v39\n\t" \
  "s_sub_u32 s60, s70, s66\n\t" \
  "s_add_u32 s81, s81, 1\n\t" \
  "s_min_u32 s81, s81, s60\n\t" \
  "s_add_u32 s48, s48, 1\n\t" \
  "s_mov_b32 s49, s53\n\t" \
  "s_add_u32 s53, s53, " CH_SLOT "\n\t" \
  "s_add_u32 s60, s53, " CH_SKIP "\n\t" \
  "s_lshr_b32 s61, s96, 1\n\t" \
  "s_add_u32 s86, s86, 1\n\t" \
  "s_cmp_eq_u32 s86, 3\n\t" \
  "s_cselect_b32 s53, s60, s53\n\t" \
  "s_cselect_b32 s96, s61, s96\n\t" \
  "s_cselect_b32 s86, 0, s86\n\t" \
  "s_andn2_b32 s53, s53, 0x2000\n\t" \
  "s_and_b32 s60, s96, 1\n\t" \
  "s_lshl_b32 s60, s60, 13\n\t" \
  "s_or_b32 s53, s53, s60\n\t" \
  "s_mov_b32 s68, 0\n\t" \
  "s_add_u32 s97, s41, s53\n\t" \
  "s_add_u32 s98, s37, s53\n\t" \
  "s_add_u32 s82, s48, 0\n\t" \
  "s_lshr_b64 s[60:61], s[90:91], s82\n\t" \
  "s_and_b32 s60, s60, 1\n\t" \
  "s_lshl_b32 s60, s60, 13\n\t" \
  "s_add_u32 s82, s82, " CH_SG0 "\n\t" \
  "s_lshl_b32 s82, s82, 21\n\t" \
  "s_or_b32 s82, s82, s60\n\t" \
  "s_add_u32 s50, s82, s38\n\t" \
  "s_add_u32 s69, s82, s39\n\t" \
  "s_add_u32 s82, s48, 1\n\t" \
  "s_lshr_b64 s[60:61], s[90:91], s82\n\t" \
  "s_and_b32 s60, s60, 1\n\t" \
  "s_lshl_b32 s60, s60, 13\n\t" \
  "s_add_u32 s82, s82, " CH_SG0 "\n\t" \
  "s_lshl_b32 s82, s82, 21\n\t" \
  "s_or_b32 s82, s82, s60\n\t" \
  "s_add_u32 s52, s82, s43\n\t" \
  "s_add_u32 s82, s48, " CH_ARMD "\n\t" \
  "s_lshr_b64 s[60:61], s[90:91], s82\n\t" \
  "s_and_b32 s60, s60, 1\n\t" \
  "s_lshl_b32 s60, s60, 13\n\t" \
  "s_add_u32 s82, s82, " CH_SG0 "\n\t" \
  "s_lshl_b32 s82, s82, 21\n\t" \
  "s_or_b32 s82, s82, s60\n\t" \
  "s_add_u32 s85, s82, s45\n\t" \
  "s_cmp_lt_i32 s48, " CH_ARMS "\n\t" \
  "s_cselect_b32 s85, s27, s85\n\t" \
  "s_cmp_eq_u32 s34, 0\n\t" \
  "s_cbranch_scc1 LINI_%=\n\t" \
  "LTK_%=:\n\t" \
  CH_TOKCHK \
  "s_mov_b32 s61, 0\n\t" \
  "LTW_%=:\n\t" \
  "buffer_load_dword v1, v37, s[28:31], 0 offen sc1\n\t" \
  "s_waitcnt vmcnt(0)\n\t" \
  "v_cmp_eq_u32 vcc, 0, v1\n\t" \
  "s_cbranch_vccz LG3_%=\n\t" \
  "s_add_u32 s61, s61, 1\n\t" \
  "s_sleep 2\n\t" \
  "s_cmp_gt_u32 s61, s84\n\t" \
  "s_cbranch_scc1 LTG_%=\n\t" \
  "s_and_b32 s60, s61, 1023\n\t" \
  "s_cmp_lg_u32 s60, 0\n\t" \
  "s_cbranch_scc1 LTW_%=\n\t" \
  "buffer_load_dword v46, off, s[28:31], s92 sc1\n\t" \
  "s_waitcnt vmcnt(0)\n\t" \
  "v_readfirstlane_b32 s60, v46\n\t" \
  "s_nop 0\n\t" \
  "s_cmp_eq_u32 s60, 0\n\t" \
  "s_cbranch_scc1 LTW_%=\n\t" \
  "LTG_%=:\n\t" \
  "s_mov_b32 s84, 0\n\t" \
  "s_lshl_b32 s60, s36, 4\n\t" \
  "s_or_b32 s60, s60, s48\n\t" \
  "s_or_b32 s60, s60, 268435456\n\t" \
  "v_mov_b32 v76, s60\n\t" \
  "v_mov_b32 v77, 0\n\t" \
  "s_mov_b64 exec, 1\n\t" \
  "buffer_atomic_cmpswap v[76:77], off, s[28:31], s92 sc1\n\t" \
  "s_mov_b64 exec, -1\n\t" \
  "s_branch LG3_%=\n\t" \
  "LIDLE_%=:\n\t" \
  "s_add_u32 s64, s64, 8\n\t" \
  "s_movk_i32 s60, 8192\n\t" \
  "IWT_%=:\n\t" \
  "s_memrealtime s[72:73]\n\t" \
  "s_sub_u32 s60, s60, 1\n\t" \
  "s_cselect_b32 s61, 0, -1\n\t" \
  "s_waitcnt lgkmcnt(0)\n\t" \
  "s_sub_i32 s73, s72, s64\n\t" \
  "s_and_b32 s73, s73, s61\n\t" \
  "s_cmp_lt_i32 s73, 0\n\t" \
  "s_cbranch_scc1 IWT_%=\n\t" \
  "buffer_load_dword v39, v37, s[28:31], 0 offen sc1\n\t" \
  "buffer_store_dword v37, off, s[28:31], s41\n\t" \
  "s_branch RDONE3_%=\n\t" \
  "RFAIL3_%=:\n\t" \
  "s_add_u32 s74, s74, 1\n\t" \
  "s_sleep " CH_RSL "\n\t" \
  "s_waitcnt vmcnt(0)\n\t" \
  "s_cmp_gt_u32 s74, s84\n\t" \
  "s_cbranch_scc1 RG3_%=\n\t" \
  "s_and_b32 s60, s74, 1023\n\t" \
  "s_cmp_lg_u32 s60, 0\n\t" \
  "s_cbranch_scc1 RRETRY3_%=\n\t" \
  "buffer_load_dword v46, off, s[28:31], s92 sc1\n\t" \
  "s_waitcnt vmcnt(0)\n\t" \
  "v_readfirstlane_b32 s60, v46\n\t" \
  "s_nop 0\n\t" \
  "s_cmp_eq_u32 s60, 0\n\t" \
  "s_cbranch_scc1 RRETRY3_%=\n\t" \
  "RG3_%=:\n\t" \
  "s_mov_b32 s84, 0\n\t" \
  "s_lshl_b32 s60, s36, 4\n\t" \
  "s_or_b32 s60, s60, s48\n\t" \
  "s_or_b32 s60, s60, 536870912\n\t" \
  "v_mov_b32 v76, s60\n\t" \
  "v_mov_b32 v77, 0\n\t" \
  "s_mov_b64 exec, 1\n\t" \
  "buffer_atomic_cmpswap v[76:77], off, s[28:31], s92 sc1\n\t" \
  "s_mov_b64 exec, -1\n\t" \
  "s_branch RDONE3_%=\n\t" \
  "LFAIL3_%=:\n\t" \
  "v_max_u32 v75, v0, v1\n\t" \
  "v_max3_u32 v75, v75, v2, v3\n\t" \
  "v_max3_u32 v75, v75, v4, v5\n\t" \
  "v_max3_u32 v75, v75, v6, v7\n\t" \
  "v_max3_u32 v75, v75, v8, v9\n\t" \
  "v_max3_u32 v75, v75, v10, v11\n\t" \
  "v_max3_u32 v75, v75, v12, v13\n\t" \
  "v_max3_u32 v75, v75, v14, v15\n\t" \
  "v_max3_u32 v75, v75, v16, v17\n\t" \
  "v_max3_u32 v75, v75, v18, v19\n\t" \
  "v_max3_u32 v75, v75, v20, v21\n\t" \
  "v_max3_u32 v75, v75, v22, v23\n\t" \
  "v_max3_u32 v75, v75, v24, v25\n\t" \
  "v_max3_u32 v75, v75, v26, v27\n\t" \
  "v_max3_u32 v75, v75, v28, v29\n\t" \
  "v_max3_u32 v75, v75, v30, v31\n\t" \
  "v_max3_u32 v75, v75, v32, v33\n\t" \
  "v_max3_u32 v75, v75, v34, v35\n\t" \
  "v_cmp_eq_u32 vcc, -1, v75\n\t" \
  "s_cbranch_vccz LGO3_%=\n\t" \
  "s_add_u32 s68, s68, 1\n\t" \
  "s_waitcnt lgkmcnt(0)\n\t" \
  "s_cmp_gt_u32 s68, s83\n\t" \
  "s_cbranch_scc1 LGV3_%=\n\t" \
  "s_barrier\n\t" \
  "s_waitcnt vmcnt(8)\n\t" \
  "s_branch LG3_%=\n\t" \
  "RWT3_%=f:\n\t" \
  "s_cmp_lt_i32 s73, -2\n\t" \
  "s_cbranch_scc0 RWT3_%=g\n\t" \
  "s_nop 4\n\t" \
  "s_branch RWT3_%=g\n\t" \
  "LGV3_%=:\n\t" \
  "s_mov_b32 s83, 0\n\t" \
  "s_lshl_b32 s60, s36, 4\n\t" \
  "s_or_b32 s60, s60, s48\n\t" \
  "s_or_b32 s60, s60, 805306368\n\t" \
  "v_mov_b32 v76, s60\n\t" \
  "v_mov_b32 v77, 0\n\t" \
  "s_mov_b64 exec, 1\n\t" \
  "buffer_atomic_cmpswap v[76:77], off, s[28:31], s92 sc1\n\t" \
  "s_mov_b64 exec, -1\n\t" \
  "s_branch LGO3_%=\n\t" \
  "LLAST3_%=:\n\t" \
  "s_waitcnt vmcnt(0) lgkmcnt(0)\n\t" \
  "s_barrier\n\t" \
  "LEND_%=:\n\t" \
  ""
typedef unsigned v16u __attribute__((ext_vector_type(16)));
#define CH_WCLOB "a0", "a1", "a2", "a3", "a4", "a5", "a6", "a7", "a8", "a9", "a10", "a11", "a12", "a13", "a14", "a15", "a16", "a17", "a18", "a19", "a20", "a21", "a22", "a23", "a24", "a25", "a26", "a27", "a28", "a29", "a30", "a31", "a32", "a33", "a34", "a35", "a36", "a37", "a38", "a39", "a40", "a41", "a42", "a43", "a44", "a45", "a46", "a47", "a48", "a49", "a50", "a51", "a52", "a53", "a54", "a55", "a56", "a57", "a58", "a59", "a60", "a61", "a62", "a63", "a64", "a65", "a66", "a67", "a68", "a69", "a70", "a71", "a72", "a73", "a74", "a75", "a76", "a77", "a78", "a79", "a80", "a81", "a82", "a83", "a84", "a85", "a86", "a87", "a88", "a89", "a90", "a91", "a92", "a93", "a94", "a95", "a96", "a97", "a98", "a99", "a100", "a101", "a102", "a103", "a104", "a105", "a106", "a107", "a108", "a109", "a110", "a111", "a112", "a113", "a114", "a115", "a116", "a117", "a118", "a119", "a120", "a121", "a122", "a123", "a124", "a125", "a126", "a127", "a128", "a129", "a130", "a131", "a132", "a133", "a134", "a135", "a136", "a137", "a138", "a139", "a140", "a141", "a142", "a143", "a144", "a145", "a146", "a147", "a148", "a149", "a150", "a151", "a152", "a153", "a154", "a155", "a156", "a157", "a158", "a159", "a160", "a161", "a162", "a163", "a164", "a165", "a166", "a167", "a168", "a169", "a170", "a171", "a172", "a173", "a174", "a175", "a176", "a177", "a178", "a179", "a180", "a181", "a182", "a183", "a184", "a185", "a186", "a187", "a188", "a189", "a190", "a191", "a192", "a193", "a194", "a195", "a196", "a197", "a198", "a199", "a200", "a201", "a202", "a203", "a204", "a205", "a206", "a207", "a208", "a209", "a210", "a211", "a212", "a213", "a214", "a215", "a216", "a217", "a218", "a219", "a220", "a221", "a222", "a223", "a224", "a225", "a226", "a227", "a228", "a229", "a230", "a231", "a232", "a233", "a234", "a235", "a236", "a237", "a238", "a239", "a240", "a241", "a242", "a243", "a244", "a245", "a246", "a247", "a248", "a249", "a250", "a251", "a252", "a253", "a254", "a255", "v80", "v81", "v82", "v83", "v84", "v85", "v86", "v87", "v88", "v89", "v90", "v91", "v92", "v93", "v94", "v95", "v96", "v97", "v98", "v99", "v100", "v101", "v102", "v103", "v104", "v105", "v106", "v107", "v108", "v109", "v110", "v111", "v112", "v113", "v114", "v115", "v116", "v117", "v118", "v119", "v120", "v121", "v122", "v123", "v124", "v125", "v126", "v127", "v128", "v129", "v130", "v131", "v132", "v133", "v134", "v135", "v136", "v137", "v138", "v139", "v140", "v141", "v142", "v143", "v144", "v145", "v146", "v147", "v148", "v149", "v150", "v151", "v152", "v153", "v154", "v155", "v156", "v157", "v158", "v159", "v160", "v161", "v162", "v163", "v164", "v165", "v166", "v167", "v168", "v169", "v170", "v171", "v172", "v173", "v174", "v175", "v176", "v177", "v178", "v179", "v180", "v181", "v182", "v183", "v184", "v185", "v186", "v187", "v188", "v189", "v190", "v191", "v192", "v193", "v194", "v195", "v196", "v197", "v198", "v199", "v200", "v201", "v202", "v203", "v204", "v205", "v206", "v207", "v208", "v209", "v210", "v211", "v212", "v213", "v214", "v215", "v216", "v217", "v218", "v219", "v220", "v221", "v222", "v223", "v224", "v225", "v226", "v227", "v228", "v229", "v230", "v231", "v232", "v233", "v234", "v235", "v236", "v237", "v238", "v239", "v240", "v241", "v242", "v243", "v244", "v245", "v246", "v247", "v248", "v249", "v250", "v251", "v252", "v253", "v254", "v255"
#define CH_H(b) (8 * ((b) & 3) + ((b) >> 3))
#define CH_L(b) (((b) >> 2) & 1)
__device__ __forceinline__ void start_work(char* smem, int wk);
__device__ __forceinline__ void chain_wg(char* smem) {
  if (CH_L((int)blockIdx.x)) start_work(smem, CH_H((int)blockIdx.x));
  const MP p = getp();
  const int b = (int)blockIdx.x, h = CH_H(b), L = CH_L(b);
  int w = __builtin_amdgcn_readfirstlane(threadIdx.x >> 6);
  {
    unsigned id; asm volatile("s_getreg_b32 %0, hwreg(HW_REG_HW_ID, 4, 2)" : "=s"(id));
    volatile __attribute__((address_space(3))) unsigned* lw = (volatile __attribute__((address_space(3))) unsigned*)smem;
    lw[w] = 1u << id;
    BAR();
    if (__builtin_amdgcn_readfirstlane(lw[0] | lw[1] | lw[2] | lw[3]) == 15u) w = (int)((SMAP >> (4u * id)) & 3u);
  }
  unsigned lp16 = (threadIdx.x & 63u) * 16u;
  rsrc_t X = mkr(p.xb, XB_BYTES);
  if ((unsigned)(size_t)(__attribute__((address_space(3))) char*)smem != 0u) return;
  const unsigned wsz = DM * HD * 2u - (unsigned)(h * DH) * 2u;
  const rsrc_t RQ = mkr(p.Wq + h * DH, wsz), RK = mkr(p.Wk + h * DH, wsz), RV = mkr(p.Wv + h * DH, wsz), RO = mkr(p.Wo + (long)h * DH * DM, DH * DM * 2u);
  unsigned xcc; asm volatile("s_getreg_b32 %0, hwreg(HW_REG_XCC_ID)" : "=s"(xcc)); xcc &= 3u;
  unsigned d96 = 0u;
  const unsigned c43 = P_FA * PAIR + (unsigned)h * 128u + 32u;
  const unsigned h128 = (unsigned)h * 128u, uw = (unsigned)w, t = uw >> 1, T = uw & 1u, NONE = 0xF0000000u;
  const unsigned u0 = w == 3 ? (unsigned)h >> 2 : (unsigned)((h & 1) + (h >> 3) * 2 + ((h >> 1) & 3) * 8 + (w == 2 ? 32 : 0));
  const bool idle = w == 3 && (h & 3);
  const unsigned c37 = idle ? NONE : (w == 3 ? AGT + u0 * 128u : AGO + (u0 >> 5) * 32768u + (u0 & 31u) * 128u);
  const unsigned c41 = idle ? NONE : (w == 3 ? 524288u + (u0 >> 1) * 16384u + (u0 & 1u) * 4096u : (u0 & 7u) * 16384u + ((u0 >> 3) & 1u) * 4096u + (u0 >> 4) * 131072u);
  const unsigned c42 = AGO + (xcc & 1u) * 16384u + (xcc >> 1) * 4096u;
  const unsigned c46 = (w == 3 ? (h & 7) == 5 && DL3 > -50 : h == 30) ? AGT + 1024u : NONE;
  const unsigned c38 = (unsigned)(3 * h + 1) * PAIR + T * 4096u + t * 2048u, c39 = (P_KV + (unsigned)(h >> 1)) * PAIR + (unsigned)(h & 1) * 4096u;
  const unsigned c45 = P_FB * PAIR + 4096u + (unsigned)(16 + (h & 15)) * 128u;
  unsigned d48 = 0u, d49 = 0u, d53 = SLOT, d86 = 1u, d65 = rtc();
  unsigned mn = 120u;
  v4u stt;
  asm volatile("s_waitcnt vmcnt(0) lgkmcnt(0)\n\t" CH_ASM "LCHEND:"
               : "+{s48}"(d48), "+{s49}"(d49), "+{s53}"(d53), "+{s65}"(d65), "+{s81}"(mn), "+{s86}"(d86), "+{s96}"(d96), "=&{s[76:79]}"(stt), "+{v69}"(lp16), "+{s[28:31]}"(X)
               : [Wq] "s"(RQ), [Wk] "s"(RK), [Wv] "s"(RV), [Wo] "s"(RO), "{s33}"(HT_OFF + 8192u + 256u + (unsigned)b * 4u), "{s36}"(h128), "{s37}"(c37), "{s40}"(w), "{s41}"(c41), "{s42}"(c42), "{s46}"(c46), "{s92}"(TMO_OFF), "{s38}"(c38), "{s39}"(c39), "{s43}"(c43), "{s45}"(c45), "{s32}"(L), "{s26}"(AGT + 1024u + xcc * 128u), "{s27}"(RDY_OFF(0) + (unsigned)((h % 9) * 1024 + (b & 7) * 128)), [ctab] "i"(CTAB_OFF), [ex] "i"(ED * 256 + EB), [dl1l] "i"(DL1 - LATE), [armd] "i"(3 - ARMK), [arms] "i"(ARMK - 3),
                 [cap] "i"(SPIN_CAP)
               : "v0", "v1", "v2", "v3", "v4", "v5", "v6", "v7", "v8", "v9", "v10", "v11", "v12", "v13", "v14", "v15", "v16", "v17", "v18", "v19",
                 "v20", "v21", "v22", "v23", "v24", "v25", "v26", "v27", "v28", "v29", "v30", "v31", "v32", "v33", "v34", "v35", "v36", "v37", "v38", "v39",
                 "v40", "v41", "v42", "v43", "v44", "v45", "v46", "v47", "v48", "v49", "v50", "v51", "v52", "v53", "v54", "v55", "v56", "v57", "v58", "v59",
                 "v60", "v61", "v62", "v63", "v64", "v65", "v66", "v67", "v68", "v70", "v71", "v72", "v73", "v74", "v75", "v76", "v77", "v78", "v79",
                 CH_WCLOB, "s34", "s35", "s24", "s25", "s90", "s91", "s47", "s87", "s50", "s51", "s52", "s69", "s82", "s85", "s54", "s75", "s80", "s55", "s56", "s57", "s58", "s59", "s60", "s61", "s62", "s63", "s64", "s66", "s67", "s68", "s70", "s71", "s72", "s73", "s74", "s88", "s89",
                 "s83", "s84", "s97", "s98", "s99", "s93", "s94", "s95", "m0", "vcc", "scc", "memory");
}
#define QLEAD 4
#define RONE 2
#define OAUX 0
#define PSP 4
#define QLEAD1 2
#define QXW -18
#define QXC QXW
#define TK_QXW "%[qxw]"
#define TK_QXC "%[qxc]"
#define PSG 16
#define TK_PSG CH_S(PSG)
#define AX 30
#define AX1 45
#define AM 22
#define TK_AM CH_S(AM)
#define PSK 2
#define PSH 2
#define TK_CAP "%[cap]"
#define TK_CTAB "%[ctab]"
#define TK_SG0 CH_S(SG0)
#define TK_PSK CH_S(PSK)
#define TK_PSH CH_S(PSH)
#define AP_ASM \
  "v_lshrrev_b32 v40, 8, v0\n\t" \
  "v_mul_u32_u24 v40, 4608, v40\n\t" \
  "v_and_b32 v41, 0xf0, v0\n\t" \
  "v_lshrrev_b32 v42, 3, v41\n\t" \
  "v_add3_u32 v41, v41, v40, s64\n\t" \
  "v_add3_u32 v42, v42, v40, s65\n\t" \
  "s_mov_b32 s62, 0x05040100\n\t" \
  "s_mov_b32 s63, 0x07060302\n\t" \
  "s_xor_b32 s60, s66, 0\n\t" \
  "s_add_u32 s60, s60, 0\n\t" \
  "s_add_u32 s61, s60, 18432\n\t" \
  "buffer_load_dwordx4 v[48:51], v41, %[W], s60 offen offset:0\n\t" \
  "buffer_load_dwordx4 v[52:55], v41, %[W], s60 offen offset:1152\n\t" \
  "buffer_load_dwordx4 v[56:59], v41, %[W], s60 offen offset:2304\n\t" \
  "buffer_load_dwordx4 v[60:63], v41, %[W], s60 offen offset:3456\n\t" \
  "buffer_load_dwordx4 v[64:67], v41, %[W], s61 offen offset:0\n\t" \
  "buffer_load_dwordx4 v[68:71], v41, %[W], s61 offen offset:1152\n\t" \
  "buffer_load_dwordx4 v[72:75], v41, %[W], s61 offen offset:2304\n\t" \
  "buffer_load_dwordx4 v[76:79], v41, %[W], s61 offen offset:3456\n\t" \
  "s_xor_b32 s60, s66, 0\n\t" \
  "s_add_u32 s60, s60, 36864\n\t" \
  "s_add_u32 s61, s60, 18432\n\t" \
  "buffer_load_dwordx4 v[80:83], v41, %[W], s60 offen offset:0\n\t" \
  "buffer_load_dwordx4 v[84:87], v41, %[W], s60 offen offset:1152\n\t" \
  "buffer_load_dwordx4 v[88:91], v41, %[W], s60 offen offset:2304\n\t" \
  "buffer_load_dwordx4 v[92:95], v41, %[W], s60 offen offset:3456\n\t" \
  "buffer_load_dwordx4 v[96:99], v41, %[W], s61 offen offset:0\n\t" \
  "buffer_load_dwordx4 v[100:103], v41, %[W], s61 offen offset:1152\n\t" \
  "buffer_load_dwordx4 v[104:107], v41, %[W], s61 offen offset:2304\n\t" \
  "buffer_load_dwordx4 v[108:111], v41, %[W], s61 offen offset:3456\n\t" \
  "s_xor_b32 s60, s66, 0\n\t" \
  "s_add_u32 s60, s60, 73728\n\t" \
  "s_add_u32 s61, s60, 18432\n\t" \
  "buffer_load_dwordx4 v[112:115], v41, %[W], s60 offen offset:0\n\t" \
  "buffer_load_dwordx4 v[116:119], v41, %[W], s60 offen offset:1152\n\t" \
  "buffer_load_dwordx4 v[120:123], v41, %[W], s60 offen offset:2304\n\t" \
  "buffer_load_dwordx4 v[124:127], v41, %[W], s60 offen offset:3456\n\t" \
  "buffer_load_dwordx4 v[128:131], v41, %[W], s61 offen offset:0\n\t" \
  "buffer_load_dwordx4 v[132:135], v41, %[W], s61 offen offset:1152\n\t" \
  "buffer_load_dwordx4 v[136:139], v41, %[W], s61 offen offset:2304\n\t" \
  "buffer_load_dwordx4 v[140:143], v41, %[W], s61 offen offset:3456\n\t" \
  "s_xor_b32 s60, s66, 0\n\t" \
  "s_add_u32 s60, s60, 110592\n\t" \
  "s_add_u32 s61, s60, 18432\n\t" \
  "buffer_load_dwordx4 v[144:147], v41, %[W], s60 offen offset:0\n\t" \
  "buffer_load_dwordx4 v[148:151], v41, %[W], s60 offen offset:1152\n\t" \
  "buffer_load_dwordx4 v[152:155], v41, %[W], s60 offen offset:2304\n\t" \
  "buffer_load_dwordx4 v[156:159], v41, %[W], s60 offen offset:3456\n\t" \
  "buffer_load_dwordx4 v[160:163], v41, %[W], s61 offen offset:0\n\t" \
  "buffer_load_dwordx4 v[164:167], v41, %[W], s61 offen offset:1152\n\t" \
  "buffer_load_dwordx4 v[168:171], v41, %[W], s61 offen offset:2304\n\t" \
  "buffer_load_dwordx4 v[172:175], v41, %[W], s61 offen offset:3456\n\t" \
  "s_xor_b32 s60, s66, 147456\n\t" \
  "s_add_u32 s60, s60, 0\n\t" \
  "s_add_u32 s61, s60, 18432\n\t" \
  "buffer_load_dwordx4 v[176:179], v41, %[W], s60 offen offset:0\n\t" \
  "buffer_load_dwordx4 v[180:183], v41, %[W], s60 offen offset:1152\n\t" \
  "buffer_load_dwordx4 v[184:187], v41, %[W], s60 offen offset:2304\n\t" \
  "buffer_load_dwordx4 v[188:191], v41, %[W], s60 offen offset:3456\n\t" \
  "buffer_load_dwordx4 v[192:195], v41, %[W], s61 offen offset:0\n\t" \
  "buffer_load_dwordx4 v[196:199], v41, %[W], s61 offen offset:1152\n\t" \
  "buffer_load_dwordx4 v[200:203], v41, %[W], s61 offen offset:2304\n\t" \
  "buffer_load_dwordx4 v[204:207], v41, %[W], s61 offen offset:3456\n\t" \
  "s_waitcnt vmcnt(38)\n\t" \
  "v_perm_b32 v44, v52, v48, s62\n\t" \
  "s_waitcnt vmcnt(36)\n\t" \
  "v_perm_b32 v45, v60, v56, s62\n\t" \
  "s_waitcnt vmcnt(34)\n\t" \
  "v_perm_b32 v46, v68, v64, s62\n\t" \
  "s_waitcnt vmcnt(32)\n\t" \
  "v_perm_b32 v47, v76, v72, s62\n\t" \
  "v_accvgpr_write_b32 a0, v44\n\t" \
  "v_accvgpr_write_b32 a1, v45\n\t" \
  "v_accvgpr_write_b32 a2, v46\n\t" \
  "v_accvgpr_write_b32 a3, v47\n\t" \
  "v_perm_b32 v44, v52, v48, s63\n\t" \
  "v_perm_b32 v45, v60, v56, s63\n\t" \
  "v_perm_b32 v46, v68, v64, s63\n\t" \
  "v_perm_b32 v47, v76, v72, s63\n\t" \
  "v_accvgpr_write_b32 a4, v44\n\t" \
  "v_accvgpr_write_b32 a5, v45\n\t" \
  "v_accvgpr_write_b32 a6, v46\n\t" \
  "v_accvgpr_write_b32 a7, v47\n\t" \
  "v_perm_b32 v44, v53, v49, s62\n\t" \
  "v_perm_b32 v45, v61, v57, s62\n\t" \
  "v_perm_b32 v46, v69, v65, s62\n\t" \
  "v_perm_b32 v47, v77, v73, s62\n\t" \
  "v_accvgpr_write_b32 a8, v44\n\t" \
  "v_accvgpr_write_b32 a9, v45\n\t" \
  "v_accvgpr_write_b32 a10, v46\n\t" \
  "v_accvgpr_write_b32 a11, v47\n\t" \
  "v_perm_b32 v44, v53, v49, s63\n\t" \
  "v_perm_b32 v45, v61, v57, s63\n\t" \
  "v_perm_b32 v46, v69, v65, s63\n\t" \
  "v_perm_b32 v47, v77, v73, s63\n\t" \
  "v_accvgpr_write_b32 a12, v44\n\t" \
  "v_accvgpr_write_b32 a13, v45\n\t" \
  "v_accvgpr_write_b32 a14, v46\n\t" \
  "v_accvgpr_write_b32 a15, v47\n\t" \
  "v_perm_b32 v44, v54, v50, s62\n\t" \
  "v_perm_b32 v45, v62, v58, s62\n\t" \
  "v_perm_b32 v46, v70, v66, s62\n\t" \
  "v_perm_b32 v47, v78, v74, s62\n\t" \
  "v_accvgpr_write_b32 a16, v44\n\t" \
  "v_accvgpr_write_b32 a17, v45\n\t" \
  "v_accvgpr_write_b32 a18, v46\n\t" \
  "v_accvgpr_write_b32 a19, v47\n\t" \
  "v_perm_b32 v44, v54, v50, s63\n\t" \
  "v_perm_b32 v45, v62, v58, s63\n\t" \
  "v_perm_b32 v46, v70, v66, s63\n\t" \
  "v_perm_b32 v47, v78, v74, s63\n\t" \
  "v_accvgpr_write_b32 a20, v44\n\t" \
  "v_accvgpr_write_b32 a21, v45\n\t" \
  "v_accvgpr_write_b32 a22, v46\n\t" \
  "v_accvgpr_write_b32 a23, v47\n\t" \
  "v_perm_b32 v44, v55, v51, s62\n\t" \
  "v_perm_b32 v45, v63, v59, s62\n\t" \
  "v_perm_b32 v46, v71, v67, s62\n\t" \
  "v_perm_b32 v47, v79, v75, s62\n\t" \
  "v_accvgpr_write_b32 a24, v44\n\t" \
  "v_accvgpr_write_b32 a25, v45\n\t" \
  "v_accvgpr_write_b32 a26, v46\n\t" \
  "v_accvgpr_write_b32 a27, v47\n\t" \
  "v_perm_b32 v44, v55, v51, s63\n\t" \
  "v_perm_b32 v45, v63, v59, s63\n\t" \
  "v_perm_b32 v46, v71, v67, s63\n\t" \
  "v_perm_b32 v47, v79, v75, s63\n\t" \
  "v_accvgpr_write_b32 a28, v44\n\t" \
  "v_accvgpr_write_b32 a29, v45\n\t" \
  "v_accvgpr_write_b32 a30, v46\n\t" \
  "v_accvgpr_write_b32 a31, v47\n\t" \
  "s_xor_b32 s60, s66, 147456\n\t" \
  "s_add_u32 s60, s60, 36864\n\t" \
  "s_add_u32 s61, s60, 18432\n\t" \
  "buffer_load_dwordx4 v[48:51], v41, %[W], s60 offen offset:0\n\t" \
  "buffer_load_dwordx4 v[52:55], v41, %[W], s60 offen offset:1152\n\t" \
  "buffer_load_dwordx4 v[56:59], v41, %[W], s60 offen offset:2304\n\t" \
  "buffer_load_dwordx4 v[60:63], v41, %[W], s60 offen offset:3456\n\t" \
  "buffer_load_dwordx4 v[64:67], v41, %[W], s61 offen offset:0\n\t" \
  "buffer_load_dwordx4 v[68:71], v41, %[W], s61 offen offset:1152\n\t" \
  "buffer_load_dwordx4 v[72:75], v41, %[W], s61 offen offset:2304\n\t" \
  "buffer_load_dwordx4 v[76:79], v41, %[W], s61 offen offset:3456\n\t" \
  "s_waitcnt vmcnt(38)\n\t" \
  "v_perm_b32 v44, v84, v80, s62\n\t" \
  "s_waitcnt vmcnt(36)\n\t" \
  "v_perm_b32 v45, v92, v88, s62\n\t" \
  "s_waitcnt vmcnt(34)\n\t" \
  "v_perm_b32 v46, v100, v96, s62\n\t" \
  "s_waitcnt vmcnt(32)\n\t" \
  "v_perm_b32 v47, v108, v104, s62\n\t" \
  "v_accvgpr_write_b32 a32, v44\n\t" \
  "v_accvgpr_write_b32 a33, v45\n\t" \
  "v_accvgpr_write_b32 a34, v46\n\t" \
  "v_accvgpr_write_b32 a35, v47\n\t" \
  "v_perm_b32 v44, v84, v80, s63\n\t" \
  "v_perm_b32 v45, v92, v88, s63\n\t" \
  "v_perm_b32 v46, v100, v96, s63\n\t" \
  "v_perm_b32 v47, v108, v104, s63\n\t" \
  "v_accvgpr_write_b32 a36, v44\n\t" \
  "v_accvgpr_write_b32 a37, v45\n\t" \
  "v_accvgpr_write_b32 a38, v46\n\t" \
  "v_accvgpr_write_b32 a39, v47\n\t" \
  "v_perm_b32 v44, v85, v81, s62\n\t" \
  "v_perm_b32 v45, v93, v89, s62\n\t" \
  "v_perm_b32 v46, v101, v97, s62\n\t" \
  "v_perm_b32 v47, v109, v105, s62\n\t" \
  "v_accvgpr_write_b32 a40, v44\n\t" \
  "v_accvgpr_write_b32 a41, v45\n\t" \
  "v_accvgpr_write_b32 a42, v46\n\t" \
  "v_accvgpr_write_b32 a43, v47\n\t" \
  "v_perm_b32 v44, v85, v81, s63\n\t" \
  "v_perm_b32 v45, v93, v89, s63\n\t" \
  "v_perm_b32 v46, v101, v97, s63\n\t" \
  "v_perm_b32 v47, v109, v105, s63\n\t" \
  "v_accvgpr_write_b32 a44, v44\n\t" \
  "v_accvgpr_write_b32 a45, v45\n\t" \
  "v_accvgpr_write_b32 a46, v46\n\t" \
  "v_accvgpr_write_b32 a47, v47\n\t" \
  "v_perm_b32 v44, v86, v82, s62\n\t" \
  "v_perm_b32 v45, v94, v90, s62\n\t" \
  "v_perm_b32 v46, v102, v98, s62\n\t" \
  "v_perm_b32 v47, v110, v106, s62\n\t" \
  "v_accvgpr_write_b32 a48, v44\n\t" \
  "v_accvgpr_write_b32 a49, v45\n\t" \
  "v_accvgpr_write_b32 a50, v46\n\t" \
  "v_accvgpr_write_b32 a51, v47\n\t" \
  "v_perm_b32 v44, v86, v82, s63\n\t" \
  "v_perm_b32 v45, v94, v90, s63\n\t" \
  "v_perm_b32 v46, v102, v98, s63\n\t" \
  "v_perm_b32 v47, v110, v106, s63\n\t" \
  "v_accvgpr_write_b32 a52, v44\n\t" \
  "v_accvgpr_write_b32 a53, v45\n\t" \
  "v_accvgpr_write_b32 a54, v46\n\t" \
  "v_accvgpr_write_b32 a55, v47\n\t" \
  "v_perm_b32 v44, v87, v83, s62\n\t" \
  "v_perm_b32 v45, v95, v91, s62\n\t" \
  "v_perm_b32 v46, v103, v99, s62\n\t" \
  "v_perm_b32 v47, v111, v107, s62\n\t" \
  "v_accvgpr_write_b32 a56, v44\n\t" \
  "v_accvgpr_write_b32 a57, v45\n\t" \
  "v_accvgpr_write_b32 a58, v46\n\t" \
  "v_accvgpr_write_b32 a59, v47\n\t" \
  "v_perm_b32 v44, v87, v83, s63\n\t" \
  "v_perm_b32 v45, v95, v91, s63\n\t" \
  "v_perm_b32 v46, v103, v99, s63\n\t" \
  "v_perm_b32 v47, v111, v107, s63\n\t" \
  "v_accvgpr_write_b32 a60, v44\n\t" \
  "v_accvgpr_write_b32 a61, v45\n\t" \
  "v_accvgpr_write_b32 a62, v46\n\t" \
  "v_accvgpr_write_b32 a63, v47\n\t" \
  "s_xor_b32 s60, s66, 147456\n\t" \
  "s_add_u32 s60, s60, 73728\n\t" \
  "s_add_u32 s61, s60, 18432\n\t" \
  "buffer_load_dwordx4 v[80:83], v41, %[W], s60 offen offset:0\n\t" \
  "buffer_load_dwordx4 v[84:87], v41, %[W], s60 offen offset:1152\n\t" \
  "buffer_load_dwordx4 v[88:91], v41, %[W], s60 offen offset:2304\n\t" \
  "buffer_load_dwordx4 v[92:95], v41, %[W], s60 offen offset:3456\n\t" \
  "buffer_load_dwordx4 v[96:99], v41, %[W], s61 offen offset:0\n\t" \
  "buffer_load_dwordx4 v[100:103], v41, %[W], s61 offen offset:1152\n\t" \
  "buffer_load_dwordx4 v[104:107], v41, %[W], s61 offen offset:2304\n\t" \
  "buffer_load_dwordx4 v[108:111], v41, %[W], s61 offen offset:3456\n\t" \
  "s_waitcnt vmcnt(38)\n\t" \
  "v_perm_b32 v44, v116, v112, s62\n\t" \
  "s_waitcnt vmcnt(36)\n\t" \
  "v_perm_b32 v45, v124, v120, s62\n\t" \
  "s_waitcnt vmcnt(34)\n\t" \
  "v_perm_b32 v46, v132, v128, s62\n\t" \
  "s_waitcnt vmcnt(32)\n\t" \
  "v_perm_b32 v47, v140, v136, s62\n\t" \
  "v_accvgpr_write_b32 a64, v44\n\t" \
  "v_accvgpr_write_b32 a65, v45\n\t" \
  "v_accvgpr_write_b32 a66, v46\n\t" \
  "v_accvgpr_write_b32 a67, v47\n\t" \
  "v_perm_b32 v44, v116, v112, s63\n\t" \
  "v_perm_b32 v45, v124, v120, s63\n\t" \
  "v_perm_b32 v46, v132, v128, s63\n\t" \
  "v_perm_b32 v47, v140, v136, s63\n\t" \
  "v_accvgpr_write_b32 a68, v44\n\t" \
  "v_accvgpr_write_b32 a69, v45\n\t" \
  "v_accvgpr_write_b32 a70, v46\n\t" \
  "v_accvgpr_write_b32 a71, v47\n\t" \
  "v_perm_b32 v44, v117, v113, s62\n\t" \
  "v_perm_b32 v45, v125, v121, s62\n\t" \
  "v_perm_b32 v46, v133, v129, s62\n\t" \
  "v_perm_b32 v47, v141, v137, s62\n\t" \
  "v_accvgpr_write_b32 a72, v44\n\t" \
  "v_accvgpr_write_b32 a73, v45\n\t" \
  "v_accvgpr_write_b32 a74, v46\n\t" \
  "v_accvgpr_write_b32 a75, v47\n\t" \
  "v_perm_b32 v44, v117, v113, s63\n\t" \
  "v_perm_b32 v45, v125, v121, s63\n\t" \
  "v_perm_b32 v46, v133, v129, s63\n\t" \
  "v_perm_b32 v47, v141, v137, s63\n\t" \
  "v_accvgpr_write_b32 a76, v44\n\t" \
  "v_accvgpr_write_b32 a77, v45\n\t" \
  "v_accvgpr_write_b32 a78, v46\n\t" \
  "v_accvgpr_write_b32 a79, v47\n\t" \
  "v_perm_b32 v44, v118, v114, s62\n\t" \
  "v_perm_b32 v45, v126, v122, s62\n\t" \
  "v_perm_b32 v46, v134, v130, s62\n\t" \
  "v_perm_b32 v47, v142, v138, s62\n\t" \
  "v_accvgpr_write_b32 a80, v44\n\t" \
  "v_accvgpr_write_b32 a81, v45\n\t" \
  "v_accvgpr_write_b32 a82, v46\n\t" \
  "v_accvgpr_write_b32 a83, v47\n\t" \
  "v_perm_b32 v44, v118, v114, s63\n\t" \
  "v_perm_b32 v45, v126, v122, s63\n\t" \
  "v_perm_b32 v46, v134, v130, s63\n\t" \
  "v_perm_b32 v47, v142, v138, s63\n\t" \
  "v_accvgpr_write_b32 a84, v44\n\t" \
  "v_accvgpr_write_b32 a85, v45\n\t" \
  "v_accvgpr_write_b32 a86, v46\n\t" \
  "v_accvgpr_write_b32 a87, v47\n\t" \
  "v_perm_b32 v44, v119, v115, s62\n\t" \
  "v_perm_b32 v45, v127, v123, s62\n\t" \
  "v_perm_b32 v46, v135, v131, s62\n\t" \
  "v_perm_b32 v47, v143, v139, s62\n\t" \
  "v_accvgpr_write_b32 a88, v44\n\t" \
  "v_accvgpr_write_b32 a89, v45\n\t" \
  "v_accvgpr_write_b32 a90, v46\n\t" \
  "v_accvgpr_write_b32 a91, v47\n\t" \
  "v_perm_b32 v44, v119, v115, s63\n\t" \
  "v_perm_b32 v45, v127, v123, s63\n\t" \
  "v_perm_b32 v46, v135, v131, s63\n\t" \
  "v_perm_b32 v47, v143, v139, s63\n\t" \
  "v_accvgpr_write_b32 a92, v44\n\t" \
  "v_accvgpr_write_b32 a93, v45\n\t" \
  "v_accvgpr_write_b32 a94, v46\n\t" \
  "v_accvgpr_write_b32 a95, v47\n\t" \
  "s_xor_b32 s60, s66, 147456\n\t" \
  "s_add_u32 s60, s60, 110592\n\t" \
  "s_add_u32 s61, s60, 18432\n\t" \
  "buffer_load_dwordx4 v[112:115], v41, %[W], s60 offen offset:0\n\t" \
  "buffer_load_dwordx4 v[116:119], v41, %[W], s60 offen offset:1152\n\t" \
  "buffer_load_dwordx4 v[120:123], v41, %[W], s60 offen offset:2304\n\t" \
  "buffer_load_dwordx4 v[124:127], v41, %[W], s60 offen offset:3456\n\t" \
  "buffer_load_dwordx4 v[128:131], v41, %[W], s61 offen offset:0\n\t" \
  "buffer_load_dwordx4 v[132:135], v41, %[W], s61 offen offset:1152\n\t" \
  "buffer_load_dwordx4 v[136:139], v41, %[W], s61 offen offset:2304\n\t" \
  "buffer_load_dwordx4 v[140:143], v41, %[W], s61 offen offset:3456\n\t" \
  "s_waitcnt vmcnt(38)\n\t" \
  "v_perm_b32 v44, v148, v144, s62\n\t" \
  "s_waitcnt vmcnt(36)\n\t" \
  "v_perm_b32 v45, v156, v152, s62\n\t" \
  "s_waitcnt vmcnt(34)\n\t" \
  "v_perm_b32 v46, v164, v160, s62\n\t" \
  "s_waitcnt vmcnt(32)\n\t" \
  "v_perm_b32 v47, v172, v168, s62\n\t" \
  "v_accvgpr_write_b32 a96, v44\n\t" \
  "v_accvgpr_write_b32 a97, v45\n\t" \
  "v_accvgpr_write_b32 a98, v46\n\t" \
  "v_accvgpr_write_b32 a99, v47\n\t" \
  "v_perm_b32 v44, v148, v144, s63\n\t" \
  "v_perm_b32 v45, v156, v152, s63\n\t" \
  "v_perm_b32 v46, v164, v160, s63\n\t" \
  "v_perm_b32 v47, v172, v168, s63\n\t" \
  "v_accvgpr_write_b32 a100, v44\n\t" \
  "v_accvgpr_write_b32 a101, v45\n\t" \
  "v_accvgpr_write_b32 a102, v46\n\t" \
  "v_accvgpr_write_b32 a103, v47\n\t" \
  "v_perm_b32 v44, v149, v145, s62\n\t" \
  "v_perm_b32 v45, v157, v153, s62\n\t" \
  "v_perm_b32 v46, v165, v161, s62\n\t" \
  "v_perm_b32 v47, v173, v169, s62\n\t" \
  "v_accvgpr_write_b32 a104, v44\n\t" \
  "v_accvgpr_write_b32 a105, v45\n\t" \
  "v_accvgpr_write_b32 a106, v46\n\t" \
  "v_accvgpr_write_b32 a107, v47\n\t" \
  "v_perm_b32 v44, v149, v145, s63\n\t" \
  "v_perm_b32 v45, v157, v153, s63\n\t" \
  "v_perm_b32 v46, v165, v161, s63\n\t" \
  "v_perm_b32 v47, v173, v169, s63\n\t" \
  "v_accvgpr_write_b32 a108, v44\n\t" \
  "v_accvgpr_write_b32 a109, v45\n\t" \
  "v_accvgpr_write_b32 a110, v46\n\t" \
  "v_accvgpr_write_b32 a111, v47\n\t" \
  "v_perm_b32 v44, v150, v146, s62\n\t" \
  "v_perm_b32 v45, v158, v154, s62\n\t" \
  "v_perm_b32 v46, v166, v162, s62\n\t" \
  "v_perm_b32 v47, v174, v170, s62\n\t" \
  "v_accvgpr_write_b32 a112, v44\n\t" \
  "v_accvgpr_write_b32 a113, v45\n\t" \
  "v_accvgpr_write_b32 a114, v46\n\t" \
  "v_accvgpr_write_b32 a115, v47\n\t" \
  "v_perm_b32 v44, v150, v146, s63\n\t" \
  "v_perm_b32 v45, v158, v154, s63\n\t" \
  "v_perm_b32 v46, v166, v162, s63\n\t" \
  "v_perm_b32 v47, v174, v170, s63\n\t" \
  "v_accvgpr_write_b32 a116, v44\n\t" \
  "v_accvgpr_write_b32 a117, v45\n\t" \
  "v_accvgpr_write_b32 a118, v46\n\t" \
  "v_accvgpr_write_b32 a119, v47\n\t" \
  "v_perm_b32 v44, v151, v147, s62\n\t" \
  "v_perm_b32 v45, v159, v155, s62\n\t" \
  "v_perm_b32 v46, v167, v163, s62\n\t" \
  "v_perm_b32 v47, v175, v171, s62\n\t" \
  "v_accvgpr_write_b32 a120, v44\n\t" \
  "v_accvgpr_write_b32 a121, v45\n\t" \
  "v_accvgpr_write_b32 a122, v46\n\t" \
  "v_accvgpr_write_b32 a123, v47\n\t" \
  "v_perm_b32 v44, v151, v147, s63\n\t" \
  "v_perm_b32 v45, v159, v155, s63\n\t" \
  "v_perm_b32 v46, v167, v163, s63\n\t" \
  "v_perm_b32 v47, v175, v171, s63\n\t" \
  "v_accvgpr_write_b32 a124, v44\n\t" \
  "v_accvgpr_write_b32 a125, v45\n\t" \
  "v_accvgpr_write_b32 a126, v46\n\t" \
  "v_accvgpr_write_b32 a127, v47\n\t" \
  "s_xor_b32 s60, s66, 0\n\t" \
  "s_add_u32 s60, s60, 0\n\t" \
  "s_add_u32 s61, s60, 18432\n\t" \
  "buffer_load_ushort v224, v42, %[W], s60 offen offset:0\n\t" \
  "buffer_load_ushort v144, v42, %[W], s60 offen offset:1152\n\t" \
  "buffer_load_ushort v225, v42, %[W], s60 offen offset:2304\n\t" \
  "buffer_load_ushort v145, v42, %[W], s60 offen offset:3456\n\t" \
  "buffer_load_ushort v226, v42, %[W], s61 offen offset:0\n\t" \
  "buffer_load_ushort v146, v42, %[W], s61 offen offset:1152\n\t" \
  "buffer_load_ushort v227, v42, %[W], s61 offen offset:2304\n\t" \
  "buffer_load_ushort v147, v42, %[W], s61 offen offset:3456\n\t" \
  "s_xor_b32 s60, s66, 0\n\t" \
  "s_add_u32 s60, s60, 36864\n\t" \
  "s_add_u32 s61, s60, 18432\n\t" \
  "buffer_load_ushort v228, v42, %[W], s60 offen offset:0\n\t" \
  "buffer_load_ushort v148, v42, %[W], s60 offen offset:1152\n\t" \
  "buffer_load_ushort v229, v42, %[W], s60 offen offset:2304\n\t" \
  "buffer_load_ushort v149, v42, %[W], s60 offen offset:3456\n\t" \
  "buffer_load_ushort v230, v42, %[W], s61 offen offset:0\n\t" \
  "buffer_load_ushort v150, v42, %[W], s61 offen offset:1152\n\t" \
  "buffer_load_ushort v231, v42, %[W], s61 offen offset:2304\n\t" \
  "buffer_load_ushort v151, v42, %[W], s61 offen offset:3456\n\t" \
  "s_waitcnt vmcnt(46)\n\t" \
  "v_perm_b32 v44, v180, v176, s62\n\t" \
  "s_waitcnt vmcnt(44)\n\t" \
  "v_perm_b32 v45, v188, v184, s62\n\t" \
  "s_waitcnt vmcnt(42)\n\t" \
  "v_perm_b32 v46, v196, v192, s62\n\t" \
  "s_waitcnt vmcnt(40)\n\t" \
  "v_perm_b32 v47, v204, v200, s62\n\t" \
  "v_accvgpr_write_b32 a128, v44\n\t" \
  "v_accvgpr_write_b32 a129, v45\n\t" \
  "v_accvgpr_write_b32 a130, v46\n\t" \
  "v_accvgpr_write_b32 a131, v47\n\t" \
  "v_perm_b32 v44, v180, v176, s63\n\t" \
  "v_perm_b32 v45, v188, v184, s63\n\t" \
  "v_perm_b32 v46, v196, v192, s63\n\t" \
  "v_perm_b32 v47, v204, v200, s63\n\t" \
  "v_accvgpr_write_b32 a132, v44\n\t" \
  "v_accvgpr_write_b32 a133, v45\n\t" \
  "v_accvgpr_write_b32 a134, v46\n\t" \
  "v_accvgpr_write_b32 a135, v47\n\t" \
  "v_perm_b32 v44, v181, v177, s62\n\t" \
  "v_perm_b32 v45, v189, v185, s62\n\t" \
  "v_perm_b32 v46, v197, v193, s62\n\t" \
  "v_perm_b32 v47, v205, v201, s62\n\t" \
  "v_accvgpr_write_b32 a136, v44\n\t" \
  "v_accvgpr_write_b32 a137, v45\n\t" \
  "v_accvgpr_write_b32 a138, v46\n\t" \
  "v_accvgpr_write_b32 a139, v47\n\t" \
  "v_perm_b32 v44, v181, v177, s63\n\t" \
  "v_perm_b32 v45, v189, v185, s63\n\t" \
  "v_perm_b32 v46, v197, v193, s63\n\t" \
  "v_perm_b32 v47, v205, v201, s63\n\t" \
  "v_accvgpr_write_b32 a140, v44\n\t" \
  "v_accvgpr_write_b32 a141, v45\n\t" \
  "v_accvgpr_write_b32 a142, v46\n\t" \
  "v_accvgpr_write_b32 a143, v47\n\t" \
  "v_perm_b32 v44, v182, v178, s62\n\t" \
  "v_perm_b32 v45, v190, v186, s62\n\t" \
  "v_perm_b32 v46, v198, v194, s62\n\t" \
  "v_perm_b32 v47, v206, v202, s62\n\t" \
  "v_accvgpr_write_b32 a144, v44\n\t" \
  "v_accvgpr_write_b32 a145, v45\n\t" \
  "v_accvgpr_write_b32 a146, v46\n\t" \
  "v_accvgpr_write_b32 a147, v47\n\t" \
  "v_perm_b32 v44, v182, v178, s63\n\t" \
  "v_perm_b32 v45, v190, v186, s63\n\t" \
  "v_perm_b32 v46, v198, v194, s63\n\t" \
  "v_perm_b32 v47, v206, v202, s63\n\t" \
  "v_accvgpr_write_b32 a148, v44\n\t" \
  "v_accvgpr_write_b32 a149, v45\n\t" \
  "v_accvgpr_write_b32 a150, v46\n\t" \
  "v_accvgpr_write_b32 a151, v47\n\t" \
  "v_perm_b32 v44, v183, v179, s62\n\t" \
  "v_perm_b32 v45, v191, v187, s62\n\t" \
  "v_perm_b32 v46, v199, v195, s62\n\t" \
  "v_perm_b32 v47, v207, v203, s62\n\t" \
  "v_accvgpr_write_b32 a152, v44\n\t" \
  "v_accvgpr_write_b32 a153, v45\n\t" \
  "v_accvgpr_write_b32 a154, v46\n\t" \
  "v_accvgpr_write_b32 a155, v47\n\t" \
  "v_perm_b32 v44, v183, v179, s63\n\t" \
  "v_perm_b32 v45, v191, v187, s63\n\t" \
  "v_perm_b32 v46, v199, v195, s63\n\t" \
  "v_perm_b32 v47, v207, v203, s63\n\t" \
  "v_accvgpr_write_b32 a156, v44\n\t" \
  "v_accvgpr_write_b32 a157, v45\n\t" \
  "v_accvgpr_write_b32 a158, v46\n\t" \
  "v_accvgpr_write_b32 a159, v47\n\t" \
  "s_xor_b32 s60, s66, 0\n\t" \
  "s_add_u32 s60, s60, 73728\n\t" \
  "s_add_u32 s61, s60, 18432\n\t" \
  "buffer_load_ushort v232, v42, %[W], s60 offen offset:0\n\t" \
  "buffer_load_ushort v152, v42, %[W], s60 offen offset:1152\n\t" \
  "buffer_load_ushort v233, v42, %[W], s60 offen offset:2304\n\t" \
  "buffer_load_ushort v153, v42, %[W], s60 offen offset:3456\n\t" \
  "buffer_load_ushort v234, v42, %[W], s61 offen offset:0\n\t" \
  "buffer_load_ushort v154, v42, %[W], s61 offen offset:1152\n\t" \
  "buffer_load_ushort v235, v42, %[W], s61 offen offset:2304\n\t" \
  "buffer_load_ushort v155, v42, %[W], s61 offen offset:3456\n\t" \
  "s_xor_b32 s60, s66, 0\n\t" \
  "s_add_u32 s60, s60, 110592\n\t" \
  "s_add_u32 s61, s60, 18432\n\t" \
  "buffer_load_ushort v236, v42, %[W], s60 offen offset:0\n\t" \
  "buffer_load_ushort v156, v42, %[W], s60 offen offset:1152\n\t" \
  "buffer_load_ushort v237, v42, %[W], s60 offen offset:2304\n\t" \
  "buffer_load_ushort v157, v42, %[W], s60 offen offset:3456\n\t" \
  "buffer_load_ushort v238, v42, %[W], s61 offen offset:0\n\t" \
  "buffer_load_ushort v158, v42, %[W], s61 offen offset:1152\n\t" \
  "buffer_load_ushort v239, v42, %[W], s61 offen offset:2304\n\t" \
  "buffer_load_ushort v159, v42, %[W], s61 offen offset:3456\n\t" \
  "s_waitcnt vmcnt(54)\n\t" \
  "v_perm_b32 v44, v52, v48, s62\n\t" \
  "s_waitcnt vmcnt(52)\n\t" \
  "v_perm_b32 v45, v60, v56, s62\n\t" \
  "s_waitcnt vmcnt(50)\n\t" \
  "v_perm_b32 v46, v68, v64, s62\n\t" \
  "s_waitcnt vmcnt(48)\n\t" \
  "v_perm_b32 v47, v76, v72, s62\n\t" \
  "v_accvgpr_write_b32 a160, v44\n\t" \
  "v_accvgpr_write_b32 a161, v45\n\t" \
  "v_accvgpr_write_b32 a162, v46\n\t" \
  "v_accvgpr_write_b32 a163, v47\n\t" \
  "v_perm_b32 v44, v52, v48, s63\n\t" \
  "v_perm_b32 v45, v60, v56, s63\n\t" \
  "v_perm_b32 v46, v68, v64, s63\n\t" \
  "v_perm_b32 v47, v76, v72, s63\n\t" \
  "v_accvgpr_write_b32 a164, v44\n\t" \
  "v_accvgpr_write_b32 a165, v45\n\t" \
  "v_accvgpr_write_b32 a166, v46\n\t" \
  "v_accvgpr_write_b32 a167, v47\n\t" \
  "v_perm_b32 v44, v53, v49, s62\n\t" \
  "v_perm_b32 v45, v61, v57, s62\n\t" \
  "v_perm_b32 v46, v69, v65, s62\n\t" \
  "v_perm_b32 v47, v77, v73, s62\n\t" \
  "v_accvgpr_write_b32 a168, v44\n\t" \
  "v_accvgpr_write_b32 a169, v45\n\t" \
  "v_accvgpr_write_b32 a170, v46\n\t" \
  "v_accvgpr_write_b32 a171, v47\n\t" \
  "v_perm_b32 v44, v53, v49, s63\n\t" \
  "v_perm_b32 v45, v61, v57, s63\n\t" \
  "v_perm_b32 v46, v69, v65, s63\n\t" \
  "v_perm_b32 v47, v77, v73, s63\n\t" \
  "v_accvgpr_write_b32 a172, v44\n\t" \
  "v_accvgpr_write_b32 a173, v45\n\t" \
  "v_accvgpr_write_b32 a174, v46\n\t" \
  "v_accvgpr_write_b32 a175, v47\n\t" \
  "v_perm_b32 v44, v54, v50, s62\n\t" \
  "v_perm_b32 v45, v62, v58, s62\n\t" \
  "v_perm_b32 v46, v70, v66, s62\n\t" \
  "v_perm_b32 v47, v78, v74, s62\n\t" \
  "v_accvgpr_write_b32 a176, v44\n\t" \
  "v_accvgpr_write_b32 a177, v45\n\t" \
  "v_accvgpr_write_b32 a178, v46\n\t" \
  "v_accvgpr_write_b32 a179, v47\n\t" \
  "v_perm_b32 v44, v54, v50, s63\n\t" \
  "v_perm_b32 v45, v62, v58, s63\n\t" \
  "v_perm_b32 v46, v70, v66, s63\n\t" \
  "v_perm_b32 v47, v78, v74, s63\n\t" \
  "v_accvgpr_write_b32 a180, v44\n\t" \
  "v_accvgpr_write_b32 a181, v45\n\t" \
  "v_accvgpr_write_b32 a182, v46\n\t" \
  "v_accvgpr_write_b32 a183, v47\n\t" \
  "v_perm_b32 v44, v55, v51, s62\n\t" \
  "v_perm_b32 v45, v63, v59, s62\n\t" \
  "v_perm_b32 v46, v71, v67, s62\n\t" \
  "v_perm_b32 v47, v79, v75, s62\n\t" \
  "v_accvgpr_write_b32 a184, v44\n\t" \
  "v_accvgpr_write_b32 a185, v45\n\t" \
  "v_accvgpr_write_b32 a186, v46\n\t" \
  "v_accvgpr_write_b32 a187, v47\n\t" \
  "v_perm_b32 v44, v55, v51, s63\n\t" \
  "v_perm_b32 v45, v63, v59, s63\n\t" \
  "v_perm_b32 v46, v71, v67, s63\n\t" \
  "v_perm_b32 v47, v79, v75, s63\n\t" \
  "v_accvgpr_write_b32 a188, v44\n\t" \
  "v_accvgpr_write_b32 a189, v45\n\t" \
  "v_accvgpr_write_b32 a190, v46\n\t" \
  "v_accvgpr_write_b32 a191, v47\n\t" \
  "s_xor_b32 s60, s66, 147456\n\t" \
  "s_add_u32 s60, s60, 0\n\t" \
  "s_add_u32 s61, s60, 18432\n\t" \
  "buffer_load_ushort v240, v42, %[W], s60 offen offset:0\n\t" \
  "buffer_load_ushort v160, v42, %[W], s60 offen offset:1152\n\t" \
  "buffer_load_ushort v241, v42, %[W], s60 offen offset:2304\n\t" \
  "buffer_load_ushort v161, v42, %[W], s60 offen offset:3456\n\t" \
  "buffer_load_ushort v242, v42, %[W], s61 offen offset:0\n\t" \
  "buffer_load_ushort v162, v42, %[W], s61 offen offset:1152\n\t" \
  "buffer_load_ushort v243, v42, %[W], s61 offen offset:2304\n\t" \
  "buffer_load_ushort v163, v42, %[W], s61 offen offset:3456\n\t" \
  "s_waitcnt vmcnt(54)\n\t" \
  "v_perm_b32 v44, v84, v80, s62\n\t" \
  "s_waitcnt vmcnt(52)\n\t" \
  "v_perm_b32 v45, v92, v88, s62\n\t" \
  "s_waitcnt vmcnt(50)\n\t" \
  "v_perm_b32 v46, v100, v96, s62\n\t" \
  "s_waitcnt vmcnt(48)\n\t" \
  "v_perm_b32 v47, v108, v104, s62\n\t" \
  "v_accvgpr_write_b32 a192, v44\n\t" \
  "v_accvgpr_write_b32 a193, v45\n\t" \
  "v_accvgpr_write_b32 a194, v46\n\t" \
  "v_accvgpr_write_b32 a195, v47\n\t" \
  "v_perm_b32 v44, v84, v80, s63\n\t" \
  "v_perm_b32 v45, v92, v88, s63\n\t" \
  "v_perm_b32 v46, v100, v96, s63\n\t" \
  "v_perm_b32 v47, v108, v104, s63\n\t" \
  "v_accvgpr_write_b32 a196, v44\n\t" \
  "v_accvgpr_write_b32 a197, v45\n\t" \
  "v_accvgpr_write_b32 a198, v46\n\t" \
  "v_accvgpr_write_b32 a199, v47\n\t" \
  "v_perm_b32 v44, v85, v81, s62\n\t" \
  "v_perm_b32 v45, v93, v89, s62\n\t" \
  "v_perm_b32 v46, v101, v97, s62\n\t" \
  "v_perm_b32 v47, v109, v105, s62\n\t" \
  "v_accvgpr_write_b32 a200, v44\n\t" \
  "v_accvgpr_write_b32 a201, v45\n\t" \
  "v_accvgpr_write_b32 a202, v46\n\t" \
  "v_accvgpr_write_b32 a203, v47\n\t" \
  "v_perm_b32 v44, v85, v81, s63\n\t" \
  "v_perm_b32 v45, v93, v89, s63\n\t" \
  "v_perm_b32 v46, v101, v97, s63\n\t" \
  "v_perm_b32 v47, v109, v105, s63\n\t" \
  "v_accvgpr_write_b32 a204, v44\n\t" \
  "v_accvgpr_write_b32 a205, v45\n\t" \
  "v_accvgpr_write_b32 a206, v46\n\t" \
  "v_accvgpr_write_b32 a207, v47\n\t" \
  "v_perm_b32 v44, v86, v82, s62\n\t" \
  "v_perm_b32 v45, v94, v90, s62\n\t" \
  "v_perm_b32 v46, v102, v98, s62\n\t" \
  "v_perm_b32 v47, v110, v106, s62\n\t" \
  "v_accvgpr_write_b32 a208, v44\n\t" \
  "v_accvgpr_write_b32 a209, v45\n\t" \
  "v_accvgpr_write_b32 a210, v46\n\t" \
  "v_accvgpr_write_b32 a211, v47\n\t" \
  "v_perm_b32 v44, v86, v82, s63\n\t" \
  "v_perm_b32 v45, v94, v90, s63\n\t" \
  "v_perm_b32 v46, v102, v98, s63\n\t" \
  "v_perm_b32 v47, v110, v106, s63\n\t" \
  "v_accvgpr_write_b32 a212, v44\n\t" \
  "v_accvgpr_write_b32 a213, v45\n\t" \
  "v_accvgpr_write_b32 a214, v46\n\t" \
  "v_accvgpr_write_b32 a215, v47\n\t" \
  "v_perm_b32 v44, v87, v83, s62\n\t" \
  "v_perm_b32 v45, v95, v91, s62\n\t" \
  "v_perm_b32 v46, v103, v99, s62\n\t" \
  "v_perm_b32 v47, v111, v107, s62\n\t" \
  "v_accvgpr_write_b32 a216, v44\n\t" \
  "v_accvgpr_write_b32 a217, v45\n\t" \
  "v_accvgpr_write_b32 a218, v46\n\t" \
  "v_accvgpr_write_b32 a219, v47\n\t" \
  "v_perm_b32 v44, v87, v83, s63\n\t" \
  "v_perm_b32 v45, v95, v91, s63\n\t" \
  "v_perm_b32 v46, v103, v99, s63\n\t" \
  "v_perm_b32 v47, v111, v107, s63\n\t" \
  "v_accvgpr_write_b32 a220, v44\n\t" \
  "v_accvgpr_write_b32 a221, v45\n\t" \
  "v_accvgpr_write_b32 a222, v46\n\t" \
  "v_accvgpr_write_b32 a223, v47\n\t" \
  "s_xor_b32 s60, s66, 147456\n\t" \
  "s_add_u32 s60, s60, 36864\n\t" \
  "s_add_u32 s61, s60, 18432\n\t" \
  "buffer_load_ushort v244, v42, %[W], s60 offen offset:0\n\t" \
  "buffer_load_ushort v164, v42, %[W], s60 offen offset:1152\n\t" \
  "buffer_load_ushort v245, v42, %[W], s60 offen offset:2304\n\t" \
  "buffer_load_ushort v165, v42, %[W], s60 offen offset:3456\n\t" \
  "buffer_load_ushort v246, v42, %[W], s61 offen offset:0\n\t" \
  "buffer_load_ushort v166, v42, %[W], s61 offen offset:1152\n\t" \
  "buffer_load_ushort v247, v42, %[W], s61 offen offset:2304\n\t" \
  "buffer_load_ushort v167, v42, %[W], s61 offen offset:3456\n\t" \
  "s_waitcnt vmcnt(54)\n\t" \
  "v_perm_b32 v44, v116, v112, s62\n\t" \
  "s_waitcnt vmcnt(52)\n\t" \
  "v_perm_b32 v45, v124, v120, s62\n\t" \
  "s_waitcnt vmcnt(50)\n\t" \
  "v_perm_b32 v46, v132, v128, s62\n\t" \
  "s_waitcnt vmcnt(48)\n\t" \
  "v_perm_b32 v47, v140, v136, s62\n\t" \
  "v_accvgpr_write_b32 a224, v44\n\t" \
  "v_accvgpr_write_b32 a225, v45\n\t" \
  "v_accvgpr_write_b32 a226, v46\n\t" \
  "v_accvgpr_write_b32 a227, v47\n\t" \
  "v_perm_b32 v44, v116, v112, s63\n\t" \
  "v_perm_b32 v45, v124, v120, s63\n\t" \
  "v_perm_b32 v46, v132, v128, s63\n\t" \
  "v_perm_b32 v47, v140, v136, s63\n\t" \
  "v_accvgpr_write_b32 a228, v44\n\t" \
  "v_accvgpr_write_b32 a229, v45\n\t" \
  "v_accvgpr_write_b32 a230, v46\n\t" \
  "v_accvgpr_write_b32 a231, v47\n\t" \
  "v_perm_b32 v44, v117, v113, s62\n\t" \
  "v_perm_b32 v45, v125, v121, s62\n\t" \
  "v_perm_b32 v46, v133, v129, s62\n\t" \
  "v_perm_b32 v47, v141, v137, s62\n\t" \
  "v_accvgpr_write_b32 a232, v44\n\t" \
  "v_accvgpr_write_b32 a233, v45\n\t" \
  "v_accvgpr_write_b32 a234, v46\n\t" \
  "v_accvgpr_write_b32 a235, v47\n\t" \
  "v_perm_b32 v44, v117, v113, s63\n\t" \
  "v_perm_b32 v45, v125, v121, s63\n\t" \
  "v_perm_b32 v46, v133, v129, s63\n\t" \
  "v_perm_b32 v47, v141, v137, s63\n\t" \
  "v_accvgpr_write_b32 a236, v44\n\t" \
  "v_accvgpr_write_b32 a237, v45\n\t" \
  "v_accvgpr_write_b32 a238, v46\n\t" \
  "v_accvgpr_write_b32 a239, v47\n\t" \
  "v_perm_b32 v44, v118, v114, s62\n\t" \
  "v_perm_b32 v45, v126, v122, s62\n\t" \
  "v_perm_b32 v46, v134, v130, s62\n\t" \
  "v_perm_b32 v47, v142, v138, s62\n\t" \
  "v_accvgpr_write_b32 a240, v44\n\t" \
  "v_accvgpr_write_b32 a241, v45\n\t" \
  "v_accvgpr_write_b32 a242, v46\n\t" \
  "v_accvgpr_write_b32 a243, v47\n\t" \
  "v_perm_b32 v44, v118, v114, s63\n\t" \
  "v_perm_b32 v45, v126, v122, s63\n\t" \
  "v_perm_b32 v46, v134, v130, s63\n\t" \
  "v_perm_b32 v47, v142, v138, s63\n\t" \
  "v_accvgpr_write_b32 a244, v44\n\t" \
  "v_accvgpr_write_b32 a245, v45\n\t" \
  "v_accvgpr_write_b32 a246, v46\n\t" \
  "v_accvgpr_write_b32 a247, v47\n\t" \
  "v_perm_b32 v44, v119, v115, s62\n\t" \
  "v_perm_b32 v45, v127, v123, s62\n\t" \
  "v_perm_b32 v46, v135, v131, s62\n\t" \
  "v_perm_b32 v47, v143, v139, s62\n\t" \
  "v_accvgpr_write_b32 a248, v44\n\t" \
  "v_accvgpr_write_b32 a249, v45\n\t" \
  "v_accvgpr_write_b32 a250, v46\n\t" \
  "v_accvgpr_write_b32 a251, v47\n\t" \
  "v_perm_b32 v44, v119, v115, s63\n\t" \
  "v_perm_b32 v45, v127, v123, s63\n\t" \
  "v_perm_b32 v46, v135, v131, s63\n\t" \
  "v_perm_b32 v47, v143, v139, s63\n\t" \
  "v_accvgpr_write_b32 a252, v44\n\t" \
  "v_accvgpr_write_b32 a253, v45\n\t" \
  "v_accvgpr_write_b32 a254, v46\n\t" \
  "v_accvgpr_write_b32 a255, v47\n\t" \
  "s_xor_b32 s60, s66, 147456\n\t" \
  "s_add_u32 s60, s60, 73728\n\t" \
  "s_add_u32 s61, s60, 18432\n\t" \
  "buffer_load_ushort v248, v42, %[W], s60 offen offset:0\n\t" \
  "buffer_load_ushort v168, v42, %[W], s60 offen offset:1152\n\t" \
  "buffer_load_ushort v249, v42, %[W], s60 offen offset:2304\n\t" \
  "buffer_load_ushort v169, v42, %[W], s60 offen offset:3456\n\t" \
  "buffer_load_ushort v250, v42, %[W], s61 offen offset:0\n\t" \
  "buffer_load_ushort v170, v42, %[W], s61 offen offset:1152\n\t" \
  "buffer_load_ushort v251, v42, %[W], s61 offen offset:2304\n\t" \
  "buffer_load_ushort v171, v42, %[W], s61 offen offset:3456\n\t" \
  "s_xor_b32 s60, s66, 147456\n\t" \
  "s_add_u32 s60, s60, 110592\n\t" \
  "s_add_u32 s61, s60, 18432\n\t" \
  "s_waitcnt vmcnt(40)\n\t" \
  "buffer_load_ushort v252, v42, %[W], s60 offen offset:0\n\t" \
  "buffer_load_ushort v172, v42, %[W], s60 offen offset:1152\n\t" \
  "buffer_load_ushort v253, v42, %[W], s60 offen offset:2304\n\t" \
  "buffer_load_ushort v173, v42, %[W], s60 offen offset:3456\n\t" \
  "buffer_load_ushort v254, v42, %[W], s61 offen offset:0\n\t" \
  "buffer_load_ushort v174, v42, %[W], s61 offen offset:1152\n\t" \
  "buffer_load_ushort v255, v42, %[W], s61 offen offset:2304\n\t" \
  "buffer_load_ushort v175, v42, %[W], s61 offen offset:3456\n\t" \
  "v_lshl_or_b32 v224, v144, 16, v224\n\t" \
  "v_lshl_or_b32 v225, v145, 16, v225\n\t" \
  "v_lshl_or_b32 v226, v146, 16, v226\n\t" \
  "v_lshl_or_b32 v227, v147, 16, v227\n\t" \
  "v_lshl_or_b32 v228, v148, 16, v228\n\t" \
  "v_lshl_or_b32 v229, v149, 16, v229\n\t" \
  "v_lshl_or_b32 v230, v150, 16, v230\n\t" \
  "v_lshl_or_b32 v231, v151, 16, v231\n\t" \
  "s_waitcnt vmcnt(46)\n\t" \
  "v_lshl_or_b32 v232, v152, 16, v232\n\t" \
  "s_waitcnt vmcnt(44)\n\t" \
  "v_lshl_or_b32 v233, v153, 16, v233\n\t" \
  "s_waitcnt vmcnt(42)\n\t" \
  "v_lshl_or_b32 v234, v154, 16, v234\n\t" \
  "s_waitcnt vmcnt(40)\n\t" \
  "v_lshl_or_b32 v235, v155, 16, v235\n\t" \
  "s_waitcnt vmcnt(38)\n\t" \
  "v_lshl_or_b32 v236, v156, 16, v236\n\t" \
  "s_waitcnt vmcnt(36)\n\t" \
  "v_lshl_or_b32 v237, v157, 16, v237\n\t" \
  "s_waitcnt vmcnt(34)\n\t" \
  "v_lshl_or_b32 v238, v158, 16, v238\n\t" \
  "s_waitcnt vmcnt(32)\n\t" \
  "v_lshl_or_b32 v239, v159, 16, v239\n\t" \
  "s_waitcnt vmcnt(30)\n\t" \
  "v_lshl_or_b32 v240, v160, 16, v240\n\t" \
  "s_waitcnt vmcnt(28)\n\t" \
  "v_lshl_or_b32 v241, v161, 16, v241\n\t" \
  "s_waitcnt vmcnt(26)\n\t" \
  "v_lshl_or_b32 v242, v162, 16, v242\n\t" \
  "s_waitcnt vmcnt(24)\n\t" \
  "v_lshl_or_b32 v243, v163, 16, v243\n\t" \
  "s_waitcnt vmcnt(22)\n\t" \
  "v_lshl_or_b32 v244, v164, 16, v244\n\t" \
  "s_waitcnt vmcnt(20)\n\t" \
  "v_lshl_or_b32 v245, v165, 16, v245\n\t" \
  "s_waitcnt vmcnt(18)\n\t" \
  "v_lshl_or_b32 v246, v166, 16, v246\n\t" \
  "s_waitcnt vmcnt(16)\n\t" \
  "v_lshl_or_b32 v247, v167, 16, v247\n\t" \
  "s_waitcnt vmcnt(14)\n\t" \
  "v_lshl_or_b32 v248, v168, 16, v248\n\t" \
  "s_waitcnt vmcnt(12)\n\t" \
  "v_lshl_or_b32 v249, v169, 16, v249\n\t" \
  "s_waitcnt vmcnt(10)\n\t" \
  "v_lshl_or_b32 v250, v170, 16, v250\n\t" \
  "s_waitcnt vmcnt(8)\n\t" \
  "v_lshl_or_b32 v251, v171, 16, v251\n\t" \
  "s_waitcnt vmcnt(6)\n\t" \
  "v_lshl_or_b32 v252, v172, 16, v252\n\t" \
  "s_waitcnt vmcnt(4)\n\t" \
  "v_lshl_or_b32 v253, v173, 16, v253\n\t" \
  "s_waitcnt vmcnt(2)\n\t" \
  "v_lshl_or_b32 v254, v174, 16, v254\n\t" \
  "s_waitcnt vmcnt(0)\n\t" \
  "v_lshl_or_b32 v255, v175, 16, v255\n\t" \
  "s_mov_b32 s84, " TK_CAP "\n\t" \
  "s_mov_b32 s48, 0\n\t" \
  "s_mov_b32 s49, 0xf0000000\n\t" \
  "s_mov_b32 s46, s45\n\t" \
  "v_mov_b32 v5, -1\n\t" \
  "v_lshrrev_b32 v13, 2, v0\n\t" \
  "s_mov_b32 s60, " TK_CTAB "\n\t" \
  "buffer_load_dword v10, v13, %[X], s60 offen offset:88 sc1\n\t" \
  "buffer_load_dword v13, v13, %[X], s60 offen offset:0 sc1\n\t" \
  "s_waitcnt vmcnt(0)\n\t" \
  "v_and_b32 v10, 1, v10\n\t" \
  "v_and_b32 v13, 1, v13\n\t" \
  "v_cmp_ne_u32 s[90:91], s67, v10\n\t" \
  "v_cmp_ne_u32 s[88:89], s67, v13\n\t" \
  "s_nop 1\n\t" \
  "s_mov_b32 s86, s88\n\t" \
  "v_lshrrev_b32 v13, 2, v0\n\t" \
  "s_mov_b32 s60, " TK_CTAB "\n\t" \
  "buffer_load_dword v10, v13, %[X], s60 offen offset:88 sc1\n\t" \
  "buffer_load_dword v13, v13, %[X], s60 offen offset:344 sc1\n\t" \
  "s_waitcnt vmcnt(0)\n\t" \
  "v_and_b32 v10, 1, v10\n\t" \
  "v_and_b32 v13, 1, v13\n\t" \
  "v_cmp_ne_u32 s[90:91], s67, v10\n\t" \
  "v_cmp_ne_u32 s[88:89], s67, v13\n\t" \
  "s_nop 1\n\t" \
  "s_and_b32 s60, s88, 256\n\t" \
  "s_lshl_b32 s60, s60, 5\n\t" \
  "s_add_u32 s44, s44, s60\n\t" \
  "s_add_u32 s83, s48, 0\n\t" \
  "s_lshr_b64 s[60:61], s[90:91], s83\n\t" \
  "s_and_b32 s60, s60, 1\n\t" \
  "s_lshl_b32 s60, s60, 13\n\t" \
  "s_add_u32 s83, s83, " TK_SG0 "\n\t" \
  "s_lshl_b32 s83, s83, 21\n\t" \
  "s_or_b32 s83, s83, s60\n\t" \
  "LCOLD_%=:\n\t" \
  "s_add_u32 s62, s83, s41\n\t" \
  "s_mov_b32 s61, 0\n\t" \
  "LPQ_%=:\n\t" \
  "buffer_load_dword v10, v9, %[X], s62 offen sc1\n\t" \
  "s_waitcnt vmcnt(0)\n\t" \
  "v_cmp_eq_u32 vcc, 0, v10\n\t" \
  "s_cbranch_vccz LPQ_%=x\n\t" \
  "s_add_u32 s61, s61, 1\n\t" \
  "s_sleep 4\n\t" \
  "s_cmp_gt_u32 s61, s84\n\t" \
  "s_cbranch_scc1 LPQ_%=g\n\t" \
  "s_and_b32 s60, s61, 1023\n\t" \
  "s_cmp_lg_u32 s60, 0\n\t" \
  "s_cbranch_scc1 LPQ_%=\n\t" \
  "buffer_load_dword v10, off, %[X], s92 sc1\n\t" \
  "s_waitcnt vmcnt(0)\n\t" \
  "v_readfirstlane_b32 s60, v10\n\t" \
  "s_nop 0\n\t" \
  "s_cmp_eq_u32 s60, 0\n\t" \
  "s_cbranch_scc1 LPQ_%=\n\t" \
  "LPQ_%=g:\n\t" \
  "s_mov_b32 s84, 0\n\t" \
  "s_or_b32 s60, s48, 1073741824\n\t" \
  "v_mov_b32 v10, s60\n\t" \
  "s_mov_b64 exec, 1\n\t" \
  "buffer_store_dword v10, off, %[X], s92 sc1\n\t" \
  "s_mov_b64 exec, -1\n\t" \
  "LPQ_%=x:\n\t" \
  "s_add_u32 m0, s46, 0\n\t" \
  "s_add_u32 s47, s83, s36\n\t" \
  "buffer_load_dwordx4 v0, %[X], s47 offen offset:0 sc1 lds\n\t" \
  "buffer_load_dwordx4 v0, %[X], s47 offen offset:1024 sc1 lds\n\t" \
  "buffer_load_dwordx4 v0, %[X], s47 offen offset:2048 sc1 lds\n\t" \
  "buffer_load_dwordx4 v0, %[X], s47 offen offset:3072 sc1 lds\n\t" \
  "s_add_u32 m0, s46, 4096\n\t" \
  "s_add_u32 s47, s83, s37\n\t" \
  "buffer_load_dwordx4 v0, %[X], s47 offen offset:0 sc1 lds\n\t" \
  "buffer_load_dwordx4 v0, %[X], s47 offen offset:1024 sc1 lds\n\t" \
  "buffer_load_dwordx4 v0, %[X], s47 offen offset:2048 sc1 lds\n\t" \
  "buffer_load_dwordx4 v0, %[X], s47 offen offset:3072 sc1 lds\n\t" \
  "s_add_u32 m0, s46, 8192\n\t" \
  "s_add_u32 s47, s83, s38\n\t" \
  "buffer_load_dwordx4 v0, %[X], s47 offen offset:0 sc1 lds\n\t" \
  "buffer_load_dwordx4 v0, %[X], s47 offen offset:1024 sc1 lds\n\t" \
  "buffer_load_dwordx4 v0, %[X], s47 offen offset:2048 sc1 lds\n\t" \
  "buffer_load_dwordx4 v0, %[X], s47 offen offset:3072 sc1 lds\n\t" \
  "s_waitcnt vmcnt(0)\n\t" \
  "LTOP_%=:\n\t" \
  "s_memrealtime s[78:79]\n\t" \
  "v_readfirstlane_b32 s76, v5\n\t" \
  "s_waitcnt lgkmcnt(0)\n\t" \
  "s_add_u32 s60, s76, 1\n\t" \
  "s_not_b32 s76, s76\n\t" \
  "s_cmp_gt_u32 s60, 1\n\t" \
  "s_cselect_b32 s61, -1, 0\n\t" \
  "s_lshl_b32 s60, s78, 8\n\t" \
  "s_sub_u32 s76, s76, s60\n\t" \
  "s_ashr_i32 s76, s76, 8\n\t" \
  "s_add_i32 s76, s76, s77\n\t" \
  "s_min_i32 s76, s76, 400\n\t" \
  "s_and_b32 s76, s76, s61\n\t" \
  "s_add_u32 s57, s78, s76\n\t" \
  "s_barrier\n\t" \
  "ds_read_b128 v[48:51], v2 offset:0\n\t" \
  "ds_read_b128 v[64:67], v1 offset:8192\n\t" \
  "ds_read_b128 v[80:83], v1 offset:12288\n\t" \
  "ds_read_b128 v[52:55], v2 offset:1024\n\t" \
  "ds_read_b128 v[68:71], v1 offset:9216\n\t" \
  "ds_read_b128 v[84:87], v1 offset:13312\n\t" \
  "ds_read_b128 v[56:59], v2 offset:2048\n\t" \
  "ds_read_b128 v[72:75], v1 offset:10240\n\t" \
  "ds_read_b128 v[88:91], v1 offset:14336\n\t" \
  "ds_read_b128 v[60:63], v2 offset:3072\n\t" \
  "ds_read_b128 v[76:79], v1 offset:11264\n\t" \
  "ds_read_b128 v[92:95], v1 offset:15360\n\t" \
  "s_mov_b32 s82, s83\n\t" \
  "s_xor_b32 s46, s46, 0x10000\n\t" \
  "s_add_u32 s58, s82, s41\n\t" \
  "s_add_u32 s58, s58, 4096\n\t" \
  "s_add_u32 s59, s82, s39\n\t" \
  "s_cmp_gt_i32 s76, 0\n\t" \
  "s_cselect_b32 s62, 0xf0000000, s58\n\t" \
  "buffer_load_dword v5, off, %[X], s62 sc1\n\t" \
  "s_cmp_gt_i32 s76, " TK_AM "\n\t" \
  "s_cselect_b32 s79, 0xf0000000, s58\n\t" \
  "s_waitcnt lgkmcnt(10)\n\t" \
  "v_mfma_f32_16x16x32_bf16 v[36:39], v[64:67], v[48:51], v[16:19]\n\t" \
  "ds_read_b128 v[96:99], v1 offset:16384\n\t" \
  "s_waitcnt lgkmcnt(10)\n\t" \
  "v_mfma_f32_16x16x32_bf16 v[40:43], v[80:83], v[48:51], v[20:23]\n\t" \
  "ds_read_b128 v[100:103], v1 offset:17408\n\t" \
  "s_waitcnt lgkmcnt(9)\n\t" \
  "v_mfma_f32_16x16x32_bf16 v[36:39], v[68:71], v[52:55], v[36:39]\n\t" \
  "ds_read_b128 v[104:107], v1 offset:18432\n\t" \
  "s_waitcnt lgkmcnt(9)\n\t" \
  "v_mfma_f32_16x16x32_bf16 v[40:43], v[84:87], v[52:55], v[40:43]\n\t" \
  "ds_read_b128 v[108:111], v1 offset:19456\n\t" \
  "s_waitcnt lgkmcnt(8)\n\t" \
  "v_mfma_f32_16x16x32_bf16 v[36:39], v[72:75], v[56:59], v[36:39]\n\t" \
  "ds_read_b128 v[112:115], v1 offset:20480\n\t" \
  "s_waitcnt lgkmcnt(8)\n\t" \
  "v_mfma_f32_16x16x32_bf16 v[40:43], v[88:91], v[56:59], v[40:43]\n\t" \
  "ds_read_b128 v[116:119], v1 offset:21504\n\t" \
  "s_waitcnt lgkmcnt(7)\n\t" \
  "v_mfma_f32_16x16x32_bf16 v[36:39], v[76:79], v[60:63], v[36:39]\n\t" \
  "ds_read_b128 v[120:123], v1 offset:22528\n\t" \
  "s_waitcnt lgkmcnt(7)\n\t" \
  "v_mfma_f32_16x16x32_bf16 v[40:43], v[92:95], v[60:63], v[40:43]\n\t" \
  "ds_read_b128 v[124:127], v1 offset:23552\n\t" \
  "s_add_u32 s83, s48, 1\n\t" \
  "s_lshr_b64 s[60:61], s[90:91], s83\n\t" \
  "s_and_b32 s60, s60, 1\n\t" \
  "s_lshl_b32 s60, s60, 13\n\t" \
  "s_add_u32 s83, s83, " TK_SG0 "\n\t" \
  "s_lshl_b32 s83, s83, 21\n\t" \
  "s_or_b32 s83, s83, s60\n\t" \
  "v_xor_b32 v1, 0x10000, v1\n\t" \
  "v_xor_b32 v2, 0x10000, v2\n\t" \
  "v_max3_f32 v11, v36, v37, v38\n\t" \
  "v_max3_f32 v11, v11, v39, v40\n\t" \
  "v_max3_f32 v13, v41, v42, v43\n\t" \
  "v_max_f32 v11, v11, v13\n\t" \
  "v_mov_b32 v13, v11\n\t" \
  "s_nop 1\n\t" \
  "v_permlane16_swap_b32 v11, v13\n\t" \
  "v_max_f32 v11, v11, v13\n\t" \
  "v_mov_b32 v13, v11\n\t" \
  "s_nop 1\n\t" \
  "v_permlane32_swap_b32 v11, v13\n\t" \
  "v_max_f32 v11, v11, v13\n\t" \
  "v_mul_f32 v13, 0xbe0293ee, v11\n\t" \
  "v_fmamk_f32 v36, v36, 0x3e0293ee, v13\n\t" \
  "v_fmamk_f32 v37, v37, 0x3e0293ee, v13\n\t" \
  "v_fmamk_f32 v38, v38, 0x3e0293ee, v13\n\t" \
  "v_fmamk_f32 v39, v39, 0x3e0293ee, v13\n\t" \
  "v_fmamk_f32 v40, v40, 0x3e0293ee, v13\n\t" \
  "v_fmamk_f32 v41, v41, 0x3e0293ee, v13\n\t" \
  "v_fmamk_f32 v42, v42, 0x3e0293ee, v13\n\t" \
  "v_fmamk_f32 v43, v43, 0x3e0293ee, v13\n\t" \
  "s_waitcnt vmcnt(0)\n\t" \
  "v_readfirstlane_b32 s76, v5\n\t" \
  "s_barrier\n\t" \
  "s_cmp_eq_u32 s76, 0\n\t" \
  "s_cbranch_scc1 LNG_%=\n\t" \
  "buffer_load_dwordx4 v[196:199], v0, %[X], s59 offen offset:0 sc1\n\t" \
  "buffer_load_dwordx4 v[200:203], v0, %[X], s59 offen offset:1024 sc1\n\t" \
  "buffer_load_dwordx2 v[204:205], v6, %[X], s59 offen offset:2048 sc1\n\t" \
  "buffer_load_dwordx2 v[206:207], v6, %[X], s59 offen offset:2560 sc1\n\t" \
  "buffer_load_dwordx2 v[208:209], v6, %[X], s59 offen offset:3072 sc1\n\t" \
  "buffer_load_dwordx2 v[210:211], v6, %[X], s59 offen offset:3584 sc1\n\t" \
  "LNG_%=:\n\t" \
  "buffer_load_dword v5, off, %[X], s79 sc1\n\t" \
  "s_mov_b64 exec, 1\n\t" \
  "buffer_store_dword v3, off, %[X], s49 sc1\n\t" \
  "s_mov_b64 exec, -1\n\t" \
  "s_mov_b32 s49, 0xf0000000\n\t" \
  "v_exp_f32 v36, v36\n\t" \
  "v_exp_f32 v37, v37\n\t" \
  "v_exp_f32 v38, v38\n\t" \
  "v_exp_f32 v39, v39\n\t" \
  "v_exp_f32 v40, v40\n\t" \
  "v_exp_f32 v41, v41\n\t" \
  "v_exp_f32 v42, v42\n\t" \
  "v_exp_f32 v43, v43\n\t" \
  "v_cvt_pk_bf16_f32 v44, v36, v37\n\t" \
  "v_cvt_pk_bf16_f32 v45, v38, v39\n\t" \
  "v_cvt_pk_bf16_f32 v46, v40, v41\n\t" \
  "v_cvt_pk_bf16_f32 v47, v42, v43\n\t" \
  "v_add_f32 v36, v36, v37\n\t" \
  "v_add_f32 v38, v38, v39\n\t" \
  "s_waitcnt lgkmcnt(7)\n\t" \
  "v_mfma_f32_16x16x32_bf16 v[128:131], v[96:99], v[44:47], 0\n\t" \
  "v_add_f32 v40, v40, v41\n\t" \
  "s_and_b32 s62, s48, 15\n\t" \
  "s_lshr_b32 s63, s62, 1\n\t" \
  "s_waitcnt lgkmcnt(6)\n\t" \
  "v_mfma_f32_16x16x32_bf16 v[132:135], v[100:103], v[44:47], 0\n\t" \
  "v_add_f32 v42, v42, v43\n\t" \
  "s_lshr_b32 s60, s88, s63\n\t" \
  "s_and_b32 s60, s60, 1\n\t" \
  "s_waitcnt lgkmcnt(5)\n\t" \
  "v_mfma_f32_16x16x32_bf16 v[136:139], v[104:107], v[44:47], 0\n\t" \
  "v_add_f32 v36, v36, v38\n\t" \
  "s_lshl_b32 s60, s60, 13\n\t" \
  "s_lshl_b32 s61, s62, 20\n\t" \
  "s_waitcnt lgkmcnt(4)\n\t" \
  "v_mfma_f32_16x16x32_bf16 v[140:143], v[108:111], v[44:47], 0\n\t" \
  "v_add_f32 v40, v40, v42\n\t" \
  "s_add_u32 s50, s61, s60\n\t" \
  "s_add_u32 s50, s50, s43\n\t" \
  "s_waitcnt lgkmcnt(3)\n\t" \
  "v_mfma_f32_16x16x32_bf16 v[144:147], v[112:115], v[44:47], 0\n\t" \
  "v_add_f32 v12, v36, v40\n\t" \
  "s_add_u32 s51, s50, 32768\n\t" \
  "s_add_u32 s52, s51, 32768\n\t" \
  "s_waitcnt lgkmcnt(2)\n\t" \
  "v_mfma_f32_16x16x32_bf16 v[148:151], v[116:119], v[44:47], 0\n\t" \
  "s_add_u32 s53, s52, 32768\n\t" \
  "s_lshl_b32 s61, s62, 17\n\t" \
  "s_waitcnt lgkmcnt(1)\n\t" \
  "v_mfma_f32_16x16x32_bf16 v[152:155], v[120:123], v[44:47], 0\n\t" \
  "s_add_u32 s54, s61, s44\n\t" \
  "s_add_u32 s63, s83, s41\n\t" \
  "s_waitcnt lgkmcnt(0)\n\t" \
  "v_mfma_f32_16x16x32_bf16 v[156:159], v[124:127], v[44:47], 0\n\t" \
  "s_cmp_lg_u32 s76, 0\n\t" \
  "s_cbranch_scc1 LGOT_%=\n\t" \
  "s_waitcnt vmcnt(1)\n\t" \
  "v_readfirstlane_b32 s60, v5\n\t" \
  "s_nop 0\n\t" \
  "s_cmp_lg_u32 s60, 0\n\t" \
  "s_cbranch_scc1 LPK_%=x\n\t" \
  "LKW_%=:\n\t" \
  "s_memrealtime s[78:79]\n\t" \
  "s_waitcnt lgkmcnt(0)\n\t" \
  "s_sub_i32 s79, s78, s57\n\t" \
  "s_cmp_lt_i32 s79, 0\n\t" \
  "s_cbranch_scc1 LKW_%=\n\t" \
  "s_mov_b32 s61, 0\n\t" \
  "LPK_%=:\n\t" \
  "buffer_load_dword v5, off, %[X], s58 sc1\n\t" \
  "s_waitcnt vmcnt(0)\n\t" \
  "v_cmp_eq_u32 vcc, 0, v5\n\t" \
  "s_cbranch_vccz LPK_%=x\n\t" \
  "s_add_u32 s61, s61, 1\n\t" \
  "s_sleep " TK_PSK "\n\t" \
  "s_cmp_gt_u32 s61, s84\n\t" \
  "s_cbranch_scc1 LPK_%=g\n\t" \
  "s_and_b32 s60, s61, 1023\n\t" \
  "s_cmp_lg_u32 s60, 0\n\t" \
  "s_cbranch_scc1 LPK_%=\n\t" \
  "buffer_load_dword v5, off, %[X], s92 sc1\n\t" \
  "s_waitcnt vmcnt(0)\n\t" \
  "v_readfirstlane_b32 s60, v5\n\t" \
  "s_nop 0\n\t" \
  "s_cmp_eq_u32 s60, 0\n\t" \
  "s_cbranch_scc1 LPK_%=\n\t" \
  "LPK_%=g:\n\t" \
  "s_mov_b32 s84, 0\n\t" \
  "s_or_b32 s60, s48, 1342177280\n\t" \
  "v_mov_b32 v5, s60\n\t" \
  "s_mov_b64 exec, 1\n\t" \
  "buffer_store_dword v5, off, %[X], s92 sc1\n\t" \
  "s_mov_b64 exec, -1\n\t" \
  "LPK_%=x:\n\t" \
  "buffer_load_dwordx4 v[196:199], v0, %[X], s59 offen offset:0 sc1\n\t" \
  "buffer_load_dwordx4 v[200:203], v0, %[X], s59 offen offset:1024 sc1\n\t" \
  "buffer_load_dwordx2 v[204:205], v6, %[X], s59 offen offset:2048 sc1\n\t" \
  "buffer_load_dwordx2 v[206:207], v6, %[X], s59 offen offset:2560 sc1\n\t" \
  "buffer_load_dwordx2 v[208:209], v6, %[X], s59 offen offset:3072 sc1\n\t" \
  "buffer_load_dwordx2 v[210:211], v6, %[X], s59 offen offset:3584 sc1\n\t" \
  "LGOT_%=:\n\t" \
  "buffer_load_dword v10, v9, %[X], s63 offen sc1\n\t" \
  "s_add_u32 s49, s82, s42\n\t" \
  "s_waitcnt vmcnt(6)\n\t" \
  "v_mfma_f32_16x16x32_bf16 v[36:39], v[196:199], v[48:51], 0\n\t" \
  "v_mfma_f32_16x16x32_bf16 v[40:43], v[196:199], v[52:55], 0\n\t" \
  "s_waitcnt vmcnt(5)\n\t" \
  "v_mfma_f32_16x16x32_bf16 v[36:39], v[200:203], v[56:59], v[36:39]\n\t" \
  "v_mfma_f32_16x16x32_bf16 v[40:43], v[200:203], v[60:63], v[40:43]\n\t" \
  "s_nop 8\n\t" \
  "v_add_f32 v36, v36, v42\n\t" \
  "v_add_f32 v37, v37, v43\n\t" \
  "v_max3_f32 v14, v36, v37, v11\n\t" \
  "v_mov_b32 v13, v14\n\t" \
  "s_nop 1\n\t" \
  "v_permlane16_swap_b32 v14, v13\n\t" \
  "v_max_f32 v14, v14, v13\n\t" \
  "v_mov_b32 v13, v14\n\t" \
  "s_nop 1\n\t" \
  "v_permlane32_swap_b32 v14, v13\n\t" \
  "v_max_f32 v14, v14, v13\n\t" \
  "v_mul_f32 v14, 0xbe0293ee, v14\n\t" \
  "v_fmamk_f32 v11, v11, 0x3e0293ee, v14\n\t" \
  "v_fmamk_f32 v36, v36, 0x3e0293ee, v14\n\t" \
  "v_fmamk_f32 v37, v37, 0x3e0293ee, v14\n\t" \
  "v_exp_f32 v11, v11\n\t" \
  "v_exp_f32 v36, v36\n\t" \
  "v_exp_f32 v37, v37\n\t" \
  "v_fma_f32 v12, v12, v11, v36\n\t" \
  "v_add_f32 v12, v12, v37\n\t" \
  "v_mov_b32 v13, v12\n\t" \
  "s_nop 1\n\t" \
  "v_permlane16_swap_b32 v12, v13\n\t" \
  "v_add_f32 v12, v12, v13\n\t" \
  "v_mov_b32 v13, v12\n\t" \
  "s_nop 1\n\t" \
  "v_permlane32_swap_b32 v12, v13\n\t" \
  "v_add_f32 v12, v12, v13\n\t" \
  "v_rcp_f32 v12, v12\n\t" \
  "s_nop 0\n\t" \
  "v_mul_f32 v14, v11, v12\n\t" \
  "v_mul_f32 v36, v36, v12\n\t" \
  "v_mul_f32 v37, v37, v12\n\t" \
  "v_cvt_pk_bf16_f32 v26, v36, v37\n\t" \
  "v_cvt_pk_bf16_f32 v33, v36, v37\n\t" \
  "v_pk_mul_f32 v[128:129], v[128:129], v[14:15] op_sel_hi:[1,0]\n\t" \
  "v_pk_mul_f32 v[130:131], v[130:131], v[14:15] op_sel_hi:[1,0]\n\t" \
  "v_pk_mul_f32 v[132:133], v[132:133], v[14:15] op_sel_hi:[1,0]\n\t" \
  "v_pk_mul_f32 v[134:135], v[134:135], v[14:15] op_sel_hi:[1,0]\n\t" \
  "v_pk_mul_f32 v[136:137], v[136:137], v[14:15] op_sel_hi:[1,0]\n\t" \
  "v_pk_mul_f32 v[138:139], v[138:139], v[14:15] op_sel_hi:[1,0]\n\t" \
  "v_pk_mul_f32 v[140:141], v[140:141], v[14:15] op_sel_hi:[1,0]\n\t" \
  "v_pk_mul_f32 v[142:143], v[142:143], v[14:15] op_sel_hi:[1,0]\n\t" \
  "s_waitcnt vmcnt(3)\n\t" \
  "v_mfma_f32_16x16x32_bf16 v[128:131], v[204:207], v[26:29], v[128:131]\n\t" \
  "v_mul_f32 v144, v144, v14\n\t" \
  "v_mul_f32 v145, v145, v14\n\t" \
  "v_mfma_f32_16x16x32_bf16 v[132:135], v[204:207], v[32:35], v[132:135]\n\t" \
  "v_mul_f32 v146, v146, v14\n\t" \
  "v_mul_f32 v147, v147, v14\n\t" \
  "v_mfma_f32_16x16x32_bf16 v[136:139], v[204:207], v[24:27], v[136:139]\n\t" \
  "v_mul_f32 v148, v148, v14\n\t" \
  "v_mul_f32 v149, v149, v14\n\t" \
  "v_mfma_f32_16x16x32_bf16 v[140:143], v[204:207], v[30:33], v[140:143]\n\t" \
  "v_cvt_pk_bf16_f32 v64, v128, v129\n\t" \
  "v_cvt_pk_bf16_f32 v65, v130, v131\n\t" \
  "v_cvt_pk_bf16_f32 v66, v132, v133\n\t" \
  "v_cvt_pk_bf16_f32 v67, v134, v135\n\t" \
  "v_mul_f32 v150, v150, v14\n\t" \
  "v_mul_f32 v151, v151, v14\n\t" \
  "v_mfma_f32_16x16x32_bf16 v[192:195], v[224:227], v[64:67], 0\n\t" \
  "ds_write_b128 v3, v[64:67] offset:0\n\t" \
  "v_cvt_pk_bf16_f32 v68, v136, v137\n\t" \
  "v_cvt_pk_bf16_f32 v69, v138, v139\n\t" \
  "v_mfma_f32_16x16x32_bf16 v[160:163], v[64:67], a[0:3], 0\n\t" \
  "v_cvt_pk_bf16_f32 v70, v140, v141\n\t" \
  "v_cvt_pk_bf16_f32 v71, v142, v143\n\t" \
  "s_waitcnt vmcnt(1)\n\t" \
  "v_mfma_f32_16x16x32_bf16 v[144:147], v[208:211], v[26:29], v[144:147]\n\t" \
  "v_mul_f32 v152, v152, v14\n\t" \
  "v_mul_f32 v153, v153, v14\n\t" \
  "v_mfma_f32_16x16x32_bf16 v[148:151], v[208:211], v[32:35], v[148:151]\n\t" \
  "v_mul_f32 v154, v154, v14\n\t" \
  "v_mul_f32 v155, v155, v14\n\t" \
  "v_mfma_f32_16x16x32_bf16 v[164:167], v[64:67], a[4:7], 0\n\t" \
  "ds_write_b128 v3, v[68:71] offset:1024\n\t" \
  "v_mul_f32 v156, v156, v14\n\t" \
  "v_mul_f32 v157, v157, v14\n\t" \
  "v_mfma_f32_16x16x32_bf16 v[168:171], v[64:67], a[8:11], 0\n\t" \
  "v_mul_f32 v158, v158, v14\n\t" \
  "v_mul_f32 v159, v159, v14\n\t" \
  "v_mfma_f32_16x16x32_bf16 v[172:175], v[64:67], a[12:15], 0\n\t" \
  "v_cvt_pk_bf16_f32 v72, v144, v145\n\t" \
  "v_cvt_pk_bf16_f32 v73, v146, v147\n\t" \
  "v_mfma_f32_16x16x32_bf16 v[152:155], v[208:211], v[24:27], v[152:155]\n\t" \
  "v_cvt_pk_bf16_f32 v74, v148, v149\n\t" \
  "v_cvt_pk_bf16_f32 v75, v150, v151\n\t" \
  "v_mfma_f32_16x16x32_bf16 v[156:159], v[208:211], v[30:33], v[156:159]\n\t" \
  "v_mfma_f32_16x16x32_bf16 v[176:179], v[64:67], a[16:19], 0\n\t" \
  "ds_write_b128 v3, v[72:75] offset:2048\n\t" \
  "v_mfma_f32_16x16x32_bf16 v[180:183], v[64:67], a[20:23], 0\n\t" \
  "v_mfma_f32_16x16x32_bf16 v[184:187], v[64:67], a[24:27], 0\n\t" \
  "v_cvt_pk_bf16_f32 v76, v152, v153\n\t" \
  "v_cvt_pk_bf16_f32 v77, v154, v155\n\t" \
  "v_mfma_f32_16x16x32_bf16 v[188:191], v[64:67], a[28:31], 0\n\t" \
  "v_cvt_pk_bf16_f32 v78, v156, v157\n\t" \
  "v_cvt_pk_bf16_f32 v79, v158, v159\n\t" \
  "v_mfma_f32_16x16x32_bf16 v[192:195], v[228:231], v[68:71], v[192:195]\n\t" \
  "ds_write_b128 v3, v[76:79] offset:3072\n\t" \
  "v_mfma_f32_16x16x32_bf16 v[160:163], v[68:71], a[32:35], v[160:163]\n\t" \
  "s_waitcnt vmcnt(0)\n\t" \
  "v_cmp_eq_u32 vcc, 0, v10\n\t" \
  "v_mfma_f32_16x16x32_bf16 v[164:167], v[68:71], a[36:39], v[164:167]\n\t" \
  "s_cmp_eq_u64 vcc, 0\n\t" \
  "s_cselect_b32 s55, s83, 0xf0000000\n\t" \
  "s_cmp_eq_u32 s48, 63\n\t" \
  "v_mfma_f32_16x16x32_bf16 v[168:171], v[68:71], a[40:43], v[168:171]\n\t" \
  "s_cselect_b32 s55, 0xf0000000, s55\n\t" \
  "s_mov_b32 s56, s83\n\t" \
  "s_mov_b32 s83, s55\n\t" \
  "v_mfma_f32_16x16x32_bf16 v[172:175], v[68:71], a[44:47], v[172:175]\n\t" \
  "v_mfma_f32_16x16x32_bf16 v[176:179], v[68:71], a[48:51], v[176:179]\n\t" \
  "s_add_u32 m0, s46, 0\n\t" \
  "s_add_u32 s47, s83, s36\n\t" \
  "buffer_load_dwordx4 v0, %[X], s47 offen offset:0 sc1 lds\n\t" \
  "v_mfma_f32_16x16x32_bf16 v[180:183], v[68:71], a[52:55], v[180:183]\n\t" \
  "v_mfma_f32_16x16x32_bf16 v[184:187], v[68:71], a[56:59], v[184:187]\n\t" \
  "s_waitcnt lgkmcnt(0)\n\t" \
  "s_barrier\n\t" \
  "v_mfma_f32_16x16x32_bf16 v[188:191], v[68:71], a[60:63], v[188:191]\n\t" \
  "ds_read_b128 v[80:83], v4 offset:0\n\t" \
  "v_mfma_f32_16x16x32_bf16 v[192:195], v[232:235], v[72:75], v[192:195]\n\t" \
  "buffer_load_dwordx4 v0, %[X], s47 offen offset:1024 sc1 lds\n\t" \
  "v_mfma_f32_16x16x32_bf16 v[160:163], v[72:75], a[64:67], v[160:163]\n\t" \
  "ds_read_b128 v[84:87], v4 offset:1024\n\t" \
  "v_mfma_f32_16x16x32_bf16 v[164:167], v[72:75], a[68:71], v[164:167]\n\t" \
  "v_mfma_f32_16x16x32_bf16 v[168:171], v[72:75], a[72:75], v[168:171]\n\t" \
  "ds_read_b128 v[88:91], v4 offset:2048\n\t" \
  "v_mfma_f32_16x16x32_bf16 v[172:175], v[72:75], a[76:79], v[172:175]\n\t" \
  "buffer_load_dwordx4 v0, %[X], s47 offen offset:2048 sc1 lds\n\t" \
  "v_mfma_f32_16x16x32_bf16 v[176:179], v[72:75], a[80:83], v[176:179]\n\t" \
  "ds_read_b128 v[92:95], v4 offset:3072\n\t" \
  "v_mfma_f32_16x16x32_bf16 v[180:183], v[72:75], a[84:87], v[180:183]\n\t" \
  "v_mfma_f32_16x16x32_bf16 v[184:187], v[72:75], a[88:91], v[184:187]\n\t" \
  "v_mfma_f32_16x16x32_bf16 v[188:191], v[72:75], a[92:95], v[188:191]\n\t" \
  "buffer_load_dwordx4 v0, %[X], s47 offen offset:3072 sc1 lds\n\t" \
  "v_mfma_f32_16x16x32_bf16 v[192:195], v[236:239], v[76:79], v[192:195]\n\t" \
  "s_add_u32 s62, s48, 2\n\t" \
  "v_mfma_f32_16x16x32_bf16 v[160:163], v[76:79], a[96:99], v[160:163]\n\t" \
  "v_mfma_f32_16x16x32_bf16 v[164:167], v[76:79], a[100:103], v[164:167]\n\t" \
  "s_mul_i32 s63, s62, 43\n\t" \
  "s_lshr_b32 s63, s63, 7\n\t" \
  "v_mfma_f32_16x16x32_bf16 v[168:171], v[76:79], a[104:107], v[168:171]\n\t" \
  "s_add_u32 m0, s46, 4096\n\t" \
  "s_add_u32 s47, s83, s37\n\t" \
  "buffer_load_dwordx4 v0, %[X], s47 offen offset:0 sc1 lds\n\t" \
  "v_mfma_f32_16x16x32_bf16 v[172:175], v[76:79], a[108:111], v[172:175]\n\t" \
  "s_mul_i32 s62, s62, 688128\n\t" \
  "s_lshl_b32 s60, s63, 15\n\t" \
  "v_mfma_f32_16x16x32_bf16 v[176:179], v[76:79], a[112:115], v[176:179]\n\t" \
  "v_mfma_f32_16x16x32_bf16 v[180:183], v[76:79], a[116:119], v[180:183]\n\t" \
  "s_add_u32 s62, s62, s60\n\t" \
  "s_lshr_b32 s60, s86, s63\n\t" \
  "v_mfma_f32_16x16x32_bf16 v[184:187], v[76:79], a[120:123], v[184:187]\n\t" \
  "buffer_load_dwordx4 v0, %[X], s47 offen offset:1024 sc1 lds\n\t" \
  "v_mfma_f32_16x16x32_bf16 v[188:191], v[76:79], a[124:127], v[188:191]\n\t" \
  "s_and_b32 s60, s60, 1\n\t" \
  "s_lshl_b32 s60, s60, 13\n\t" \
  "s_waitcnt lgkmcnt(3)\n\t" \
  "v_mfma_f32_16x16x32_bf16 v[192:195], v[240:243], v[80:83], v[192:195]\n\t" \
  "v_mfma_f32_16x16x32_bf16 v[160:163], v[80:83], a[128:131], v[160:163]\n\t" \
  "s_add_u32 s62, s62, s60\n\t" \
  "s_add_u32 s62, s62, 656896\n\t" \
  "v_mfma_f32_16x16x32_bf16 v[164:167], v[80:83], a[132:135], v[164:167]\n\t" \
  "buffer_load_dwordx4 v0, %[X], s47 offen offset:2048 sc1 lds\n\t" \
  "v_mfma_f32_16x16x32_bf16 v[168:171], v[80:83], a[136:139], v[168:171]\n\t" \
  "s_cmp_gt_u32 s48, 61\n\t" \
  "s_cselect_b32 s62, 0xf0000000, s62\n\t" \
  "v_mfma_f32_16x16x32_bf16 v[172:175], v[80:83], a[140:143], v[172:175]\n\t" \
  "v_mfma_f32_16x16x32_bf16 v[176:179], v[80:83], a[144:147], v[176:179]\n\t" \
  "v_mfma_f32_16x16x32_bf16 v[180:183], v[80:83], a[148:151], v[180:183]\n\t" \
  "buffer_load_dwordx4 v0, %[X], s47 offen offset:3072 sc1 lds\n\t" \
  "v_mfma_f32_16x16x32_bf16 v[184:187], v[80:83], a[152:155], v[184:187]\n\t" \
  "v_mfma_f32_16x16x32_bf16 v[188:191], v[80:83], a[156:159], v[188:191]\n\t" \
  "s_waitcnt lgkmcnt(2)\n\t" \
  "v_mfma_f32_16x16x32_bf16 v[192:195], v[244:247], v[84:87], v[192:195]\n\t" \
  "v_mfma_f32_16x16x32_bf16 v[160:163], v[84:87], a[160:163], v[160:163]\n\t" \
  "s_add_u32 m0, s46, 8192\n\t" \
  "s_add_u32 s47, s83, s38\n\t" \
  "buffer_load_dwordx4 v0, %[X], s47 offen offset:0 sc1 lds\n\t" \
  "v_mfma_f32_16x16x32_bf16 v[164:167], v[84:87], a[164:167], v[164:167]\n\t" \
  "v_mfma_f32_16x16x32_bf16 v[168:171], v[84:87], a[168:171], v[168:171]\n\t" \
  "v_mfma_f32_16x16x32_bf16 v[172:175], v[84:87], a[172:175], v[172:175]\n\t" \
  "v_mfma_f32_16x16x32_bf16 v[176:179], v[84:87], a[176:179], v[176:179]\n\t" \
  "buffer_load_dwordx4 v0, %[X], s47 offen offset:1024 sc1 lds\n\t" \
  "v_mfma_f32_16x16x32_bf16 v[180:183], v[84:87], a[180:183], v[180:183]\n\t" \
  "v_mfma_f32_16x16x32_bf16 v[184:187], v[84:87], a[184:187], v[184:187]\n\t" \
  "v_mfma_f32_16x16x32_bf16 v[188:191], v[84:87], a[188:191], v[188:191]\n\t" \
  "s_waitcnt lgkmcnt(1)\n\t" \
  "v_mfma_f32_16x16x32_bf16 v[192:195], v[248:251], v[88:91], v[192:195]\n\t" \
  "buffer_load_dwordx4 v0, %[X], s47 offen offset:2048 sc1 lds\n\t" \
  "v_mfma_f32_16x16x32_bf16 v[160:163], v[88:91], a[192:195], v[160:163]\n\t" \
  "v_mfma_f32_16x16x32_bf16 v[164:167], v[88:91], a[196:199], v[164:167]\n\t" \
  "v_mfma_f32_16x16x32_bf16 v[168:171], v[88:91], a[200:203], v[168:171]\n\t" \
  "v_mfma_f32_16x16x32_bf16 v[172:175], v[88:91], a[204:207], v[172:175]\n\t" \
  "buffer_load_dwordx4 v0, %[X], s47 offen offset:3072 sc1 lds\n\t" \
  "v_mfma_f32_16x16x32_bf16 v[176:179], v[88:91], a[208:211], v[176:179]\n\t" \
  "v_mfma_f32_16x16x32_bf16 v[180:183], v[88:91], a[212:215], v[180:183]\n\t" \
  "buffer_load_dword v5, off, %[X], s62 sc1\n\t" \
  "v_mfma_f32_16x16x32_bf16 v[184:187], v[88:91], a[216:219], v[184:187]\n\t" \
  "v_mfma_f32_16x16x32_bf16 v[188:191], v[88:91], a[220:223], v[188:191]\n\t" \
  "s_waitcnt lgkmcnt(0)\n\t" \
  "v_mfma_f32_16x16x32_bf16 v[192:195], v[252:255], v[92:95], v[192:195]\n\t" \
  "v_mfma_f32_16x16x32_bf16 v[160:163], v[92:95], a[224:227], v[160:163]\n\t" \
  "v_mfma_f32_16x16x32_bf16 v[164:167], v[92:95], a[228:231], v[164:167]\n\t" \
  "v_mfma_f32_16x16x32_bf16 v[168:171], v[92:95], a[232:235], v[168:171]\n\t" \
  "v_cvt_pk_bf16_f32 v112, v192, v193\n\t" \
  "v_cvt_pk_bf16_f32 v113, v194, v195\n\t" \
  "v_mfma_f32_16x16x32_bf16 v[172:175], v[92:95], a[236:239], v[172:175]\n\t" \
  "buffer_store_dwordx2 v[112:113], v8, %[X], s54 offen sc1\n\t" \
  "v_mfma_f32_16x16x32_bf16 v[176:179], v[92:95], a[240:243], v[176:179]\n\t" \
  "v_cvt_pk_bf16_f32 v96, v160, v164\n\t" \
  "v_cvt_pk_bf16_f32 v100, v161, v165\n\t" \
  "v_mfma_f32_16x16x32_bf16 v[180:183], v[92:95], a[244:247], v[180:183]\n\t" \
  "v_cvt_pk_bf16_f32 v104, v162, v166\n\t" \
  "v_cvt_pk_bf16_f32 v108, v163, v167\n\t" \
  "v_mfma_f32_16x16x32_bf16 v[184:187], v[92:95], a[248:251], v[184:187]\n\t" \
  "v_cvt_pk_bf16_f32 v97, v168, v172\n\t" \
  "v_cvt_pk_bf16_f32 v101, v169, v173\n\t" \
  "v_mfma_f32_16x16x32_bf16 v[188:191], v[92:95], a[252:255], v[188:191]\n\t" \
  "v_cvt_pk_bf16_f32 v105, v170, v174\n\t" \
  "v_cvt_pk_bf16_f32 v109, v171, v175\n\t" \
  "v_cvt_pk_bf16_f32 v98, v176, v180\n\t" \
  "v_cvt_pk_bf16_f32 v102, v177, v181\n\t" \
  "v_cvt_pk_bf16_f32 v106, v178, v182\n\t" \
  "v_cvt_pk_bf16_f32 v110, v179, v183\n\t" \
  "s_nop 1\n\t" \
  "v_cvt_pk_bf16_f32 v99, v184, v188\n\t" \
  "buffer_store_dwordx4 v[96:99], v7, %[X], s50 offen sc1\n\t" \
  "v_cvt_pk_bf16_f32 v103, v185, v189\n\t" \
  "buffer_store_dwordx4 v[100:103], v7, %[X], s51 offen sc1\n\t" \
  "v_cvt_pk_bf16_f32 v107, v186, v190\n\t" \
  "buffer_store_dwordx4 v[104:107], v7, %[X], s52 offen sc1\n\t" \
  "v_cvt_pk_bf16_f32 v111, v187, v191\n\t" \
  "buffer_store_dwordx4 v[108:111], v7, %[X], s53 offen sc1\n\t" \
  "s_cmp_eq_u32 s48, 63\n\t" \
  "s_cbranch_scc1 LEND_%=\n\t" \
  "s_add_u32 s48, s48, 1\n\t" \
  "s_cmp_eq_u32 s83, s56\n\t" \
  "s_mov_b32 s83, s56\n\t" \
  "s_cbranch_scc0 LCOLD_%=\n\t" \
  "s_waitcnt vmcnt(5)\n\t" \
  "s_branch LTOP_%=\n\t" \
  "LEND_%=:\n\t" \
  "s_waitcnt vmcnt(0)\n\t" \
  "s_barrier\n\t" \
  "s_mov_b64 exec, 1\n\t" \
  "buffer_store_dword v3, off, %[X], s49 sc1\n\t" \
  "s_mov_b64 exec, -1\n\t" \
  "s_mov_b32 s49, 0xf0000000\n\t" \
  ""
#define QK_ASM \
  "v_lshrrev_b32 v16, 7, v0\n\t" \
  "v_lshlrev_b32 v16, 13, v16\n\t" \
  "v_lshrrev_b32 v19, 2, v0\n\t" \
  "v_and_b32 v19, 64, v19\n\t" \
  "v_xor_b32 v19, v19, v0\n\t" \
  "v_and_b32 v19, 0x70, v19\n\t" \
  "v_add_u32 v16, v16, v19\n\t" \
  "s_mul_i32 s63, s40, 40960\n\t" \
  "v_and_b32 v19, 0x300, v0\n\t" \
  "v_lshlrev_b32 v17, 2, v19\n\t" \
  "v_and_b32 v19, 0xc0, v0\n\t" \
  "v_lshl_add_u32 v17, v19, 1, v17\n\t" \
  "v_and_b32 v19, 0x30, v0\n\t" \
  "v_add3_u32 v17, v17, v19, s63\n\t" \
  "v_lshrrev_b32 v19, 1, v0\n\t" \
  "v_and_b32 v19, 64, v19\n\t" \
  "v_add_u32 v17, v17, v19\n\t" \
  "v_xor_b32 v18, 64, v17\n\t" \
  "s_add_u32 m0, s63, 0\n\t" \
  "s_mov_b32 s60, 0\n\t" \
  "buffer_load_dwordx4 v16, %[W], s60 offen lds\n\t" \
  "s_add_u32 m0, s63, 1024\n\t" \
  "s_mov_b32 s60, 65536\n\t" \
  "buffer_load_dwordx4 v16, %[W], s60 offen lds\n\t" \
  "s_add_u32 m0, s63, 2048\n\t" \
  "s_mov_b32 s60, 131072\n\t" \
  "buffer_load_dwordx4 v16, %[W], s60 offen lds\n\t" \
  "s_add_u32 m0, s63, 3072\n\t" \
  "s_mov_b32 s60, 196608\n\t" \
  "buffer_load_dwordx4 v16, %[W], s60 offen lds\n\t" \
  "s_add_u32 m0, s63, 4096\n\t" \
  "s_mov_b32 s60, 262144\n\t" \
  "buffer_load_dwordx4 v16, %[W], s60 offen lds\n\t" \
  "s_add_u32 m0, s63, 5120\n\t" \
  "s_mov_b32 s60, 327680\n\t" \
  "buffer_load_dwordx4 v16, %[W], s60 offen lds\n\t" \
  "s_add_u32 m0, s63, 6144\n\t" \
  "s_mov_b32 s60, 393216\n\t" \
  "buffer_load_dwordx4 v16, %[W], s60 offen lds\n\t" \
  "s_add_u32 m0, s63, 7168\n\t" \
  "s_mov_b32 s60, 458752\n\t" \
  "buffer_load_dwordx4 v16, %[W], s60 offen lds\n\t" \
  "s_add_u32 m0, s63, 8192\n\t" \
  "s_mov_b32 s60, 524288\n\t" \
  "buffer_load_dwordx4 v16, %[W], s60 offen lds\n\t" \
  "s_add_u32 m0, s63, 9216\n\t" \
  "s_mov_b32 s60, 589824\n\t" \
  "buffer_load_dwordx4 v16, %[W], s60 offen lds\n\t" \
  "s_add_u32 m0, s63, 10240\n\t" \
  "s_mov_b32 s60, 655360\n\t" \
  "buffer_load_dwordx4 v16, %[W], s60 offen lds\n\t" \
  "s_add_u32 m0, s63, 11264\n\t" \
  "s_mov_b32 s60, 720896\n\t" \
  "buffer_load_dwordx4 v16, %[W], s60 offen lds\n\t" \
  "s_add_u32 m0, s63, 12288\n\t" \
  "s_mov_b32 s60, 786432\n\t" \
  "buffer_load_dwordx4 v16, %[W], s60 offen lds\n\t" \
  "s_add_u32 m0, s63, 13312\n\t" \
  "s_mov_b32 s60, 851968\n\t" \
  "buffer_load_dwordx4 v16, %[W], s60 offen lds\n\t" \
  "s_add_u32 m0, s63, 14336\n\t" \
  "s_mov_b32 s60, 917504\n\t" \
  "buffer_load_dwordx4 v16, %[W], s60 offen lds\n\t" \
  "s_add_u32 m0, s63, 15360\n\t" \
  "s_mov_b32 s60, 983040\n\t" \
  "buffer_load_dwordx4 v16, %[W], s60 offen lds\n\t" \
  "s_add_u32 m0, s63, 16384\n\t" \
  "s_mov_b32 s60, 1048576\n\t" \
  "buffer_load_dwordx4 v16, %[W], s60 offen lds\n\t" \
  "s_add_u32 m0, s63, 17408\n\t" \
  "s_mov_b32 s60, 1114112\n\t" \
  "buffer_load_dwordx4 v16, %[W], s60 offen lds\n\t" \
  "s_add_u32 m0, s63, 18432\n\t" \
  "s_mov_b32 s60, 1179648\n\t" \
  "buffer_load_dwordx4 v16, %[W], s60 offen lds\n\t" \
  "s_add_u32 m0, s63, 19456\n\t" \
  "s_mov_b32 s60, 1245184\n\t" \
  "buffer_load_dwordx4 v16, %[W], s60 offen lds\n\t" \
  "s_add_u32 m0, s63, 20480\n\t" \
  "s_mov_b32 s60, 1310720\n\t" \
  "buffer_load_dwordx4 v16, %[W], s60 offen lds\n\t" \
  "s_add_u32 m0, s63, 21504\n\t" \
  "s_mov_b32 s60, 1376256\n\t" \
  "buffer_load_dwordx4 v16, %[W], s60 offen lds\n\t" \
  "s_add_u32 m0, s63, 22528\n\t" \
  "s_mov_b32 s60, 1441792\n\t" \
  "buffer_load_dwordx4 v16, %[W], s60 offen lds\n\t" \
  "s_add_u32 m0, s63, 23552\n\t" \
  "s_mov_b32 s60, 1507328\n\t" \
  "buffer_load_dwordx4 v16, %[W], s60 offen lds\n\t" \
  "s_add_u32 m0, s63, 24576\n\t" \
  "s_mov_b32 s60, 1572864\n\t" \
  "buffer_load_dwordx4 v16, %[W], s60 offen lds\n\t" \
  "s_add_u32 m0, s63, 25600\n\t" \
  "s_mov_b32 s60, 1638400\n\t" \
  "buffer_load_dwordx4 v16, %[W], s60 offen lds\n\t" \
  "s_add_u32 m0, s63, 26624\n\t" \
  "s_mov_b32 s60, 1703936\n\t" \
  "buffer_load_dwordx4 v16, %[W], s60 offen lds\n\t" \
  "s_add_u32 m0, s63, 27648\n\t" \
  "s_mov_b32 s60, 1769472\n\t" \
  "buffer_load_dwordx4 v16, %[W], s60 offen lds\n\t" \
  "s_add_u32 m0, s63, 28672\n\t" \
  "s_mov_b32 s60, 1835008\n\t" \
  "buffer_load_dwordx4 v16, %[W], s60 offen lds\n\t" \
  "s_add_u32 m0, s63, 29696\n\t" \
  "s_mov_b32 s60, 1900544\n\t" \
  "buffer_load_dwordx4 v16, %[W], s60 offen lds\n\t" \
  "s_add_u32 m0, s63, 30720\n\t" \
  "s_mov_b32 s60, 1966080\n\t" \
  "buffer_load_dwordx4 v16, %[W], s60 offen lds\n\t" \
  "s_add_u32 m0, s63, 31744\n\t" \
  "s_mov_b32 s60, 2031616\n\t" \
  "buffer_load_dwordx4 v16, %[W], s60 offen lds\n\t" \
  "s_add_u32 m0, s63, 32768\n\t" \
  "s_mov_b32 s60, 2097152\n\t" \
  "buffer_load_dwordx4 v16, %[W], s60 offen lds\n\t" \
  "s_add_u32 m0, s63, 33792\n\t" \
  "s_mov_b32 s60, 2162688\n\t" \
  "buffer_load_dwordx4 v16, %[W], s60 offen lds\n\t" \
  "s_add_u32 m0, s63, 34816\n\t" \
  "s_mov_b32 s60, 2228224\n\t" \
  "buffer_load_dwordx4 v16, %[W], s60 offen lds\n\t" \
  "s_add_u32 m0, s63, 35840\n\t" \
  "s_mov_b32 s60, 2293760\n\t" \
  "buffer_load_dwordx4 v16, %[W], s60 offen lds\n\t" \
  "s_add_u32 m0, s63, 36864\n\t" \
  "s_mov_b32 s60, 2359296\n\t" \
  "buffer_load_dwordx4 v16, %[W], s60 offen lds\n\t" \
  "s_add_u32 m0, s63, 37888\n\t" \
  "s_mov_b32 s60, 2424832\n\t" \
  "buffer_load_dwordx4 v16, %[W], s60 offen lds\n\t" \
  "s_add_u32 m0, s63, 38912\n\t" \
  "s_mov_b32 s60, 2490368\n\t" \
  "buffer_load_dwordx4 v16, %[W], s60 offen lds\n\t" \
  "s_add_u32 m0, s63, 39936\n\t" \
  "s_mov_b32 s60, 2555904\n\t" \
  "buffer_load_dwordx4 v16, %[W], s60 offen lds\n\t" \
  "s_waitcnt vmcnt(32)\n\t" \
  "ds_read_b64_tr_b16 a[0:1], v17 offset:0\n\t" \
  "ds_read_b64_tr_b16 a[2:3], v17 offset:512\n\t" \
  "ds_read_b64_tr_b16 a[4:5], v17 offset:4096\n\t" \
  "ds_read_b64_tr_b16 a[6:7], v17 offset:4608\n\t" \
  "ds_read_b64_tr_b16 a[72:73], v17 offset:8\n\t" \
  "ds_read_b64_tr_b16 a[74:75], v17 offset:520\n\t" \
  "ds_read_b64_tr_b16 a[76:77], v17 offset:4104\n\t" \
  "ds_read_b64_tr_b16 a[78:79], v17 offset:4616\n\t" \
  "ds_read_b64_tr_b16 a[144:145], v18 offset:0\n\t" \
  "ds_read_b64_tr_b16 a[146:147], v18 offset:512\n\t" \
  "ds_read_b64_tr_b16 a[148:149], v18 offset:4096\n\t" \
  "ds_read_b64_tr_b16 a[150:151], v18 offset:4608\n\t" \
  "ds_read_b64_tr_b16 a[216:217], v18 offset:8\n\t" \
  "ds_read_b64_tr_b16 a[218:219], v18 offset:520\n\t" \
  "ds_read_b64_tr_b16 a[220:221], v18 offset:4104\n\t" \
  "ds_read_b64_tr_b16 a[222:223], v18 offset:4616\n\t" \
  "s_waitcnt lgkmcnt(0)\n\t" \
  "s_add_u32 m0, s63, 0\n\t" \
  "s_mov_b32 s60, 2621440\n\t" \
  "buffer_load_dwordx4 v16, %[W], s60 offen lds\n\t" \
  "s_add_u32 m0, s63, 1024\n\t" \
  "s_mov_b32 s60, 2686976\n\t" \
  "buffer_load_dwordx4 v16, %[W], s60 offen lds\n\t" \
  "s_add_u32 m0, s63, 2048\n\t" \
  "s_mov_b32 s60, 2752512\n\t" \
  "buffer_load_dwordx4 v16, %[W], s60 offen lds\n\t" \
  "s_add_u32 m0, s63, 3072\n\t" \
  "s_mov_b32 s60, 2818048\n\t" \
  "buffer_load_dwordx4 v16, %[W], s60 offen lds\n\t" \
  "s_add_u32 m0, s63, 4096\n\t" \
  "s_mov_b32 s60, 2883584\n\t" \
  "buffer_load_dwordx4 v16, %[W], s60 offen lds\n\t" \
  "s_add_u32 m0, s63, 5120\n\t" \
  "s_mov_b32 s60, 2949120\n\t" \
  "buffer_load_dwordx4 v16, %[W], s60 offen lds\n\t" \
  "s_add_u32 m0, s63, 6144\n\t" \
  "s_mov_b32 s60, 3014656\n\t" \
  "buffer_load_dwordx4 v16, %[W], s60 offen lds\n\t" \
  "s_add_u32 m0, s63, 7168\n\t" \
  "s_mov_b32 s60, 3080192\n\t" \
  "buffer_load_dwordx4 v16, %[W], s60 offen lds\n\t" \
  "s_waitcnt vmcnt(32)\n\t" \
  "ds_read_b64_tr_b16 a[8:9], v17 offset:8192\n\t" \
  "ds_read_b64_tr_b16 a[10:11], v17 offset:8704\n\t" \
  "ds_read_b64_tr_b16 a[12:13], v17 offset:12288\n\t" \
  "ds_read_b64_tr_b16 a[14:15], v17 offset:12800\n\t" \
  "ds_read_b64_tr_b16 a[80:81], v17 offset:8200\n\t" \
  "ds_read_b64_tr_b16 a[82:83], v17 offset:8712\n\t" \
  "ds_read_b64_tr_b16 a[84:85], v17 offset:12296\n\t" \
  "ds_read_b64_tr_b16 a[86:87], v17 offset:12808\n\t" \
  "ds_read_b64_tr_b16 a[152:153], v18 offset:8192\n\t" \
  "ds_read_b64_tr_b16 a[154:155], v18 offset:8704\n\t" \
  "ds_read_b64_tr_b16 a[156:157], v18 offset:12288\n\t" \
  "ds_read_b64_tr_b16 a[158:159], v18 offset:12800\n\t" \
  "ds_read_b64_tr_b16 a[224:225], v18 offset:8200\n\t" \
  "ds_read_b64_tr_b16 a[226:227], v18 offset:8712\n\t" \
  "ds_read_b64_tr_b16 a[228:229], v18 offset:12296\n\t" \
  "ds_read_b64_tr_b16 a[230:231], v18 offset:12808\n\t" \
  "s_waitcnt lgkmcnt(0)\n\t" \
  "s_add_u32 m0, s63, 8192\n\t" \
  "s_mov_b32 s60, 3145728\n\t" \
  "buffer_load_dwordx4 v16, %[W], s60 offen lds\n\t" \
  "s_add_u32 m0, s63, 9216\n\t" \
  "s_mov_b32 s60, 3211264\n\t" \
  "buffer_load_dwordx4 v16, %[W], s60 offen lds\n\t" \
  "s_add_u32 m0, s63, 10240\n\t" \
  "s_mov_b32 s60, 3276800\n\t" \
  "buffer_load_dwordx4 v16, %[W], s60 offen lds\n\t" \
  "s_add_u32 m0, s63, 11264\n\t" \
  "s_mov_b32 s60, 3342336\n\t" \
  "buffer_load_dwordx4 v16, %[W], s60 offen lds\n\t" \
  "s_add_u32 m0, s63, 12288\n\t" \
  "s_mov_b32 s60, 3407872\n\t" \
  "buffer_load_dwordx4 v16, %[W], s60 offen lds\n\t" \
  "s_add_u32 m0, s63, 13312\n\t" \
  "s_mov_b32 s60, 3473408\n\t" \
  "buffer_load_dwordx4 v16, %[W], s60 offen lds\n\t" \
  "s_add_u32 m0, s63, 14336\n\t" \
  "s_mov_b32 s60, 3538944\n\t" \
  "buffer_load_dwordx4 v16, %[W], s60 offen lds\n\t" \
  "s_add_u32 m0, s63, 15360\n\t" \
  "s_mov_b32 s60, 3604480\n\t" \
  "buffer_load_dwordx4 v16, %[W], s60 offen lds\n\t" \
  "s_waitcnt vmcnt(32)\n\t" \
  "ds_read_b64_tr_b16 a[16:17], v17 offset:16384\n\t" \
  "ds_read_b64_tr_b16 a[18:19], v17 offset:16896\n\t" \
  "ds_read_b64_tr_b16 a[20:21], v17 offset:20480\n\t" \
  "ds_read_b64_tr_b16 a[22:23], v17 offset:20992\n\t" \
  "ds_read_b64_tr_b16 a[88:89], v17 offset:16392\n\t" \
  "ds_read_b64_tr_b16 a[90:91], v17 offset:16904\n\t" \
  "ds_read_b64_tr_b16 a[92:93], v17 offset:20488\n\t" \
  "ds_read_b64_tr_b16 a[94:95], v17 offset:21000\n\t" \
  "ds_read_b64_tr_b16 a[160:161], v18 offset:16384\n\t" \
  "ds_read_b64_tr_b16 a[162:163], v18 offset:16896\n\t" \
  "ds_read_b64_tr_b16 a[164:165], v18 offset:20480\n\t" \
  "ds_read_b64_tr_b16 a[166:167], v18 offset:20992\n\t" \
  "ds_read_b64_tr_b16 a[232:233], v18 offset:16392\n\t" \
  "ds_read_b64_tr_b16 a[234:235], v18 offset:16904\n\t" \
  "ds_read_b64_tr_b16 a[236:237], v18 offset:20488\n\t" \
  "ds_read_b64_tr_b16 a[238:239], v18 offset:21000\n\t" \
  "s_waitcnt lgkmcnt(0)\n\t" \
  "s_add_u32 m0, s63, 16384\n\t" \
  "s_mov_b32 s60, 3670016\n\t" \
  "buffer_load_dwordx4 v16, %[W], s60 offen lds\n\t" \
  "s_add_u32 m0, s63, 17408\n\t" \
  "s_mov_b32 s60, 3735552\n\t" \
  "buffer_load_dwordx4 v16, %[W], s60 offen lds\n\t" \
  "s_add_u32 m0, s63, 18432\n\t" \
  "s_mov_b32 s60, 3801088\n\t" \
  "buffer_load_dwordx4 v16, %[W], s60 offen lds\n\t" \
  "s_add_u32 m0, s63, 19456\n\t" \
  "s_mov_b32 s60, 3866624\n\t" \
  "buffer_load_dwordx4 v16, %[W], s60 offen lds\n\t" \
  "s_add_u32 m0, s63, 20480\n\t" \
  "s_mov_b32 s60, 3932160\n\t" \
  "buffer_load_dwordx4 v16, %[W], s60 offen lds\n\t" \
  "s_add_u32 m0, s63, 21504\n\t" \
  "s_mov_b32 s60, 3997696\n\t" \
  "buffer_load_dwordx4 v16, %[W], s60 offen lds\n\t" \
  "s_add_u32 m0, s63, 22528\n\t" \
  "s_mov_b32 s60, 4063232\n\t" \
  "buffer_load_dwordx4 v16, %[W], s60 offen lds\n\t" \
  "s_add_u32 m0, s63, 23552\n\t" \
  "s_mov_b32 s60, 4128768\n\t" \
  "buffer_load_dwordx4 v16, %[W], s60 offen lds\n\t" \
  "s_waitcnt vmcnt(32)\n\t" \
  "ds_read_b64_tr_b16 a[24:25], v17 offset:24576\n\t" \
  "ds_read_b64_tr_b16 a[26:27], v17 offset:25088\n\t" \
  "ds_read_b64_tr_b16 a[28:29], v17 offset:28672\n\t" \
  "ds_read_b64_tr_b16 a[30:31], v17 offset:29184\n\t" \
  "ds_read_b64_tr_b16 a[96:97], v17 offset:24584\n\t" \
  "ds_read_b64_tr_b16 a[98:99], v17 offset:25096\n\t" \
  "ds_read_b64_tr_b16 a[100:101], v17 offset:28680\n\t" \
  "ds_read_b64_tr_b16 a[102:103], v17 offset:29192\n\t" \
  "ds_read_b64_tr_b16 a[168:169], v18 offset:24576\n\t" \
  "ds_read_b64_tr_b16 a[170:171], v18 offset:25088\n\t" \
  "ds_read_b64_tr_b16 a[172:173], v18 offset:28672\n\t" \
  "ds_read_b64_tr_b16 a[174:175], v18 offset:29184\n\t" \
  "ds_read_b64_tr_b16 a[240:241], v18 offset:24584\n\t" \
  "ds_read_b64_tr_b16 a[242:243], v18 offset:25096\n\t" \
  "ds_read_b64_tr_b16 a[244:245], v18 offset:28680\n\t" \
  "ds_read_b64_tr_b16 a[246:247], v18 offset:29192\n\t" \
  "s_waitcnt lgkmcnt(0)\n\t" \
  "s_add_u32 m0, s63, 24576\n\t" \
  "s_mov_b32 s60, 4194304\n\t" \
  "buffer_load_dwordx4 v16, %[W], s60 offen lds\n\t" \
  "s_add_u32 m0, s63, 25600\n\t" \
  "s_mov_b32 s60, 4259840\n\t" \
  "buffer_load_dwordx4 v16, %[W], s60 offen lds\n\t" \
  "s_add_u32 m0, s63, 26624\n\t" \
  "s_mov_b32 s60, 4325376\n\t" \
  "buffer_load_dwordx4 v16, %[W], s60 offen lds\n\t" \
  "s_add_u32 m0, s63, 27648\n\t" \
  "s_mov_b32 s60, 4390912\n\t" \
  "buffer_load_dwordx4 v16, %[W], s60 offen lds\n\t" \
  "s_add_u32 m0, s63, 28672\n\t" \
  "s_mov_b32 s60, 4456448\n\t" \
  "buffer_load_dwordx4 v16, %[W], s60 offen lds\n\t" \
  "s_add_u32 m0, s63, 29696\n\t" \
  "s_mov_b32 s60, 4521984\n\t" \
  "buffer_load_dwordx4 v16, %[W], s60 offen lds\n\t" \
  "s_add_u32 m0, s63, 30720\n\t" \
  "s_mov_b32 s60, 4587520\n\t" \
  "buffer_load_dwordx4 v16, %[W], s60 offen lds\n\t" \
  "s_add_u32 m0, s63, 31744\n\t" \
  "s_mov_b32 s60, 4653056\n\t" \
  "buffer_load_dwordx4 v16, %[W], s60 offen lds\n\t" \
  "s_waitcnt vmcnt(32)\n\t" \
  "ds_read_b64_tr_b16 a[32:33], v17 offset:32768\n\t" \
  "ds_read_b64_tr_b16 a[34:35], v17 offset:33280\n\t" \
  "ds_read_b64_tr_b16 a[36:37], v17 offset:36864\n\t" \
  "ds_read_b64_tr_b16 a[38:39], v17 offset:37376\n\t" \
  "ds_read_b64_tr_b16 a[104:105], v17 offset:32776\n\t" \
  "ds_read_b64_tr_b16 a[106:107], v17 offset:33288\n\t" \
  "ds_read_b64_tr_b16 a[108:109], v17 offset:36872\n\t" \
  "ds_read_b64_tr_b16 a[110:111], v17 offset:37384\n\t" \
  "ds_read_b64_tr_b16 a[176:177], v18 offset:32768\n\t" \
  "ds_read_b64_tr_b16 a[178:179], v18 offset:33280\n\t" \
  "ds_read_b64_tr_b16 a[180:181], v18 offset:36864\n\t" \
  "ds_read_b64_tr_b16 a[182:183], v18 offset:37376\n\t" \
  "ds_read_b64_tr_b16 a[248:249], v18 offset:32776\n\t" \
  "ds_read_b64_tr_b16 a[250:251], v18 offset:33288\n\t" \
  "ds_read_b64_tr_b16 a[252:253], v18 offset:36872\n\t" \
  "ds_read_b64_tr_b16 a[254:255], v18 offset:37384\n\t" \
  "s_waitcnt lgkmcnt(0)\n\t" \
  "s_waitcnt vmcnt(24)\n\t" \
  "ds_read_b64_tr_b16 a[40:41], v17 offset:0\n\t" \
  "ds_read_b64_tr_b16 a[42:43], v17 offset:512\n\t" \
  "ds_read_b64_tr_b16 a[44:45], v17 offset:4096\n\t" \
  "ds_read_b64_tr_b16 a[46:47], v17 offset:4608\n\t" \
  "ds_read_b64_tr_b16 a[112:113], v17 offset:8\n\t" \
  "ds_read_b64_tr_b16 a[114:115], v17 offset:520\n\t" \
  "ds_read_b64_tr_b16 a[116:117], v17 offset:4104\n\t" \
  "ds_read_b64_tr_b16 a[118:119], v17 offset:4616\n\t" \
  "ds_read_b64_tr_b16 a[184:185], v18 offset:0\n\t" \
  "ds_read_b64_tr_b16 a[186:187], v18 offset:512\n\t" \
  "ds_read_b64_tr_b16 a[188:189], v18 offset:4096\n\t" \
  "ds_read_b64_tr_b16 a[190:191], v18 offset:4608\n\t" \
  "ds_read_b64_tr_b16 v[224:225], v18 offset:8\n\t" \
  "ds_read_b64_tr_b16 v[226:227], v18 offset:520\n\t" \
  "ds_read_b64_tr_b16 v[228:229], v18 offset:4104\n\t" \
  "ds_read_b64_tr_b16 v[230:231], v18 offset:4616\n\t" \
  "s_waitcnt lgkmcnt(0)\n\t" \
  "s_waitcnt vmcnt(16)\n\t" \
  "ds_read_b64_tr_b16 a[48:49], v17 offset:8192\n\t" \
  "ds_read_b64_tr_b16 a[50:51], v17 offset:8704\n\t" \
  "ds_read_b64_tr_b16 a[52:53], v17 offset:12288\n\t" \
  "ds_read_b64_tr_b16 a[54:55], v17 offset:12800\n\t" \
  "ds_read_b64_tr_b16 a[120:121], v17 offset:8200\n\t" \
  "ds_read_b64_tr_b16 a[122:123], v17 offset:8712\n\t" \
  "ds_read_b64_tr_b16 a[124:125], v17 offset:12296\n\t" \
  "ds_read_b64_tr_b16 a[126:127], v17 offset:12808\n\t" \
  "ds_read_b64_tr_b16 a[192:193], v18 offset:8192\n\t" \
  "ds_read_b64_tr_b16 a[194:195], v18 offset:8704\n\t" \
  "ds_read_b64_tr_b16 a[196:197], v18 offset:12288\n\t" \
  "ds_read_b64_tr_b16 a[198:199], v18 offset:12800\n\t" \
  "ds_read_b64_tr_b16 v[232:233], v18 offset:8200\n\t" \
  "ds_read_b64_tr_b16 v[234:235], v18 offset:8712\n\t" \
  "ds_read_b64_tr_b16 v[236:237], v18 offset:12296\n\t" \
  "ds_read_b64_tr_b16 v[238:239], v18 offset:12808\n\t" \
  "s_waitcnt lgkmcnt(0)\n\t" \
  "s_waitcnt vmcnt(8)\n\t" \
  "ds_read_b64_tr_b16 a[56:57], v17 offset:16384\n\t" \
  "ds_read_b64_tr_b16 a[58:59], v17 offset:16896\n\t" \
  "ds_read_b64_tr_b16 a[60:61], v17 offset:20480\n\t" \
  "ds_read_b64_tr_b16 a[62:63], v17 offset:20992\n\t" \
  "ds_read_b64_tr_b16 a[128:129], v17 offset:16392\n\t" \
  "ds_read_b64_tr_b16 a[130:131], v17 offset:16904\n\t" \
  "ds_read_b64_tr_b16 a[132:133], v17 offset:20488\n\t" \
  "ds_read_b64_tr_b16 a[134:135], v17 offset:21000\n\t" \
  "ds_read_b64_tr_b16 a[200:201], v18 offset:16384\n\t" \
  "ds_read_b64_tr_b16 a[202:203], v18 offset:16896\n\t" \
  "ds_read_b64_tr_b16 a[204:205], v18 offset:20480\n\t" \
  "ds_read_b64_tr_b16 a[206:207], v18 offset:20992\n\t" \
  "ds_read_b64_tr_b16 v[240:241], v18 offset:16392\n\t" \
  "ds_read_b64_tr_b16 v[242:243], v18 offset:16904\n\t" \
  "ds_read_b64_tr_b16 v[244:245], v18 offset:20488\n\t" \
  "ds_read_b64_tr_b16 v[246:247], v18 offset:21000\n\t" \
  "s_waitcnt lgkmcnt(0)\n\t" \
  "s_waitcnt vmcnt(0)\n\t" \
  "ds_read_b64_tr_b16 a[64:65], v17 offset:24576\n\t" \
  "ds_read_b64_tr_b16 a[66:67], v17 offset:25088\n\t" \
  "ds_read_b64_tr_b16 a[68:69], v17 offset:28672\n\t" \
  "ds_read_b64_tr_b16 a[70:71], v17 offset:29184\n\t" \
  "ds_read_b64_tr_b16 a[136:137], v17 offset:24584\n\t" \
  "ds_read_b64_tr_b16 a[138:139], v17 offset:25096\n\t" \
  "ds_read_b64_tr_b16 a[140:141], v17 offset:28680\n\t" \
  "ds_read_b64_tr_b16 a[142:143], v17 offset:29192\n\t" \
  "ds_read_b64_tr_b16 a[208:209], v18 offset:24576\n\t" \
  "ds_read_b64_tr_b16 a[210:211], v18 offset:25088\n\t" \
  "ds_read_b64_tr_b16 a[212:213], v18 offset:28672\n\t" \
  "ds_read_b64_tr_b16 a[214:215], v18 offset:29184\n\t" \
  "ds_read_b64_tr_b16 v[248:249], v18 offset:24584\n\t" \
  "ds_read_b64_tr_b16 v[250:251], v18 offset:25096\n\t" \
  "ds_read_b64_tr_b16 v[252:253], v18 offset:28680\n\t" \
  "ds_read_b64_tr_b16 v[254:255], v18 offset:29192\n\t" \
  "s_waitcnt lgkmcnt(0)\n\t" \
  "s_mov_b32 s84, " TK_CAP "\n\t" \
  "s_mov_b32 s48, 0\n\t" \
  "s_mov_b32 s49, 0xf0000000\n\t" \
  "s_mov_b32 s46, s41\n\t" \
  "s_mov_b32 s56, 0\n\t" \
  "v_lshrrev_b32 v7, 2, v0\n\t" \
  "s_mov_b32 s60, " TK_CTAB "\n\t" \
  "buffer_load_dword v4, v7, %[X], s60 offen offset:88 sc1\n\t" \
  "buffer_load_dword v7, v7, %[X], s60 offen offset:0 sc1\n\t" \
  "s_waitcnt vmcnt(0)\n\t" \
  "v_and_b32 v4, 1, v4\n\t" \
  "v_and_b32 v7, 1, v7\n\t" \
  "v_cmp_ne_u32 s[90:91], s67, v4\n\t" \
  "v_cmp_ne_u32 s[88:89], s67, v7\n\t" \
  "s_nop 1\n\t" \
  "s_mov_b32 s57, 0xf0000000\n\t" \
  "s_add_u32 s83, s48, 0\n\t" \
  "s_lshr_b64 s[60:61], s[90:91], s83\n\t" \
  "s_and_b32 s60, s60, 1\n\t" \
  "s_lshl_b32 s60, s60, 13\n\t" \
  "s_add_u32 s83, s83, " TK_SG0 "\n\t" \
  "s_lshl_b32 s83, s83, 21\n\t" \
  "s_or_b32 s83, s83, s60\n\t" \
  "s_add_u32 s62, s83, s39\n\t" \
  "v_add_u32 v7, s62, v3\n\t" \
  "LCOLD_%=:\n\t" \
  "s_waitcnt vmcnt(0)\n\t" \
  "s_mov_b64 exec, 1\n\t" \
  "buffer_store_dword v6, off, %[X], s49 sc1\n\t" \
  "s_mov_b64 exec, -1\n\t" \
  "s_mov_b32 s49, 0xf0000000\n\t" \
  "s_add_u32 s55, s83, s36\n\t" \
  "s_cmp_lg_u32 s40, 0\n\t" \
  "s_cbranch_scc1 LGO_%=\n\t" \
  "s_and_b32 s62, s45, 3\n\t" \
  "s_lshl_b32 s62, s62, 7\n\t" \
  "s_add_u32 s62, s62, s83\n\t" \
  "s_add_u32 s62, s62, s39\n\t" \
  "s_mov_b32 s61, 0\n\t" \
  "LPO_%=:\n\t" \
  "buffer_load_dword v4, off, %[X], s62 sc1\n\t" \
  "s_waitcnt vmcnt(0)\n\t" \
  "v_cmp_eq_u32 vcc, 0, v4\n\t" \
  "s_cbranch_vccz LPO_%=x\n\t" \
  "s_add_u32 s61, s61, 1\n\t" \
  "s_sleep " TK_PSH "\n\t" \
  "s_cmp_gt_u32 s61, s84\n\t" \
  "s_cbranch_scc1 LPO_%=g\n\t" \
  "s_and_b32 s60, s61, 1023\n\t" \
  "s_cmp_lg_u32 s60, 0\n\t" \
  "s_cbranch_scc1 LPO_%=\n\t" \
  "buffer_load_dword v4, off, %[X], s92 sc1\n\t" \
  "s_waitcnt vmcnt(0)\n\t" \
  "v_readfirstlane_b32 s60, v4\n\t" \
  "s_nop 0\n\t" \
  "s_cmp_eq_u32 s60, 0\n\t" \
  "s_cbranch_scc1 LPO_%=\n\t" \
  "LPO_%=g:\n\t" \
  "s_mov_b32 s84, 0\n\t" \
  "s_or_b32 s60, s48, 1610612736\n\t" \
  "v_mov_b32 v4, s60\n\t" \
  "s_mov_b64 exec, 1\n\t" \
  "buffer_store_dword v4, off, %[X], s92 sc1\n\t" \
  "s_mov_b64 exec, -1\n\t" \
  "LPO_%=x:\n\t" \
  "s_mov_b32 s61, 0\n\t" \
  "LPH_%=:\n\t" \
  "buffer_load_dword v4, v7, %[X], 0 offen sc1\n\t" \
  "s_waitcnt vmcnt(0)\n\t" \
  "v_cmp_eq_u32 vcc, 0, v4\n\t" \
  "s_cbranch_vccz LPH_%=x\n\t" \
  "s_add_u32 s61, s61, 1\n\t" \
  "s_sleep " TK_PSH "\n\t" \
  "s_cmp_gt_u32 s61, s84\n\t" \
  "s_cbranch_scc1 LPH_%=g\n\t" \
  "s_and_b32 s60, s61, 1023\n\t" \
  "s_cmp_lg_u32 s60, 0\n\t" \
  "s_cbranch_scc1 LPH_%=\n\t" \
  "buffer_load_dword v4, off, %[X], s92 sc1\n\t" \
  "s_waitcnt vmcnt(0)\n\t" \
  "v_readfirstlane_b32 s60, v4\n\t" \
  "s_nop 0\n\t" \
  "s_cmp_eq_u32 s60, 0\n\t" \
  "s_cbranch_scc1 LPH_%=\n\t" \
  "LPH_%=g:\n\t" \
  "s_mov_b32 s84, 0\n\t" \
  "s_or_b32 s60, s48, 1610612736\n\t" \
  "v_mov_b32 v4, s60\n\t" \
  "s_mov_b64 exec, 1\n\t" \
  "buffer_store_dword v4, off, %[X], s92 sc1\n\t" \
  "s_mov_b64 exec, -1\n\t" \
  "LPH_%=x:\n\t" \
  "s_cmp_lt_i32 s57, 0\n\t" \
  "s_cbranch_scc1 LGO_%=\n\t" \
  "s_movk_i32 s63, 4000\n\t" \
  "s_branch LGC_%=u\n\t" \
  "LGC_%=:\n\t" \
  "s_sleep " TK_PSG "\n\t" \
  "buffer_load_dword v8, off, %[X], s57 sc1\n\t" \
  "s_waitcnt vmcnt(0)\n\t" \
  "LGC_%=u:\n\t" \
  "s_memrealtime s[52:53]\n\t" \
  "v_readfirstlane_b32 s60, v8\n\t" \
  "s_waitcnt lgkmcnt(0)\n\t" \
  "s_sub_u32 s63, s63, 1\n\t" \
  "s_cbranch_scc1 LGO_%=\n\t" \
  "s_cmp_eq_u32 s60, -1\n\t" \
  "s_cbranch_scc1 LGC_%=\n\t" \
  "s_not_b32 s60, s60\n\t" \
  "s_lshl_b32 s61, s52, 8\n\t" \
  "s_sub_u32 s60, s60, s61\n\t" \
  "s_ashr_i32 s60, s60, 8\n\t" \
  "s_sub_i32 s60, s60, " TK_QXC "\n\t" \
  "s_min_i32 s60, s60, 400\n\t" \
  "s_add_u32 s54, s52, s60\n\t" \
  "LGC_%=w:\n\t" \
  "s_sleep 1\n\t" \
  "s_memrealtime s[52:53]\n\t" \
  "s_waitcnt lgkmcnt(0)\n\t" \
  "s_sub_i32 s53, s52, s54\n\t" \
  "s_cmp_lt_i32 s53, 0\n\t" \
  "s_cbranch_scc1 LGC_%=w\n\t" \
  "LGO_%=:\n\t" \
  "s_barrier\n\t" \
  "s_add_u32 m0, s46, 0\n\t" \
  "s_add_u32 s47, s55, 0\n\t" \
  "buffer_load_dwordx4 v0, %[X], s47 offen sc1 lds\n\t" \
  "s_add_u32 m0, s46, 4096\n\t" \
  "s_add_u32 s47, s55, 4096\n\t" \
  "buffer_load_dwordx4 v0, %[X], s47 offen sc1 lds\n\t" \
  "s_add_u32 m0, s46, 8192\n\t" \
  "s_add_u32 s47, s55, 16384\n\t" \
  "buffer_load_dwordx4 v0, %[X], s47 offen sc1 lds\n\t" \
  "s_add_u32 m0, s46, 12288\n\t" \
  "s_add_u32 s47, s55, 20480\n\t" \
  "buffer_load_dwordx4 v0, %[X], s47 offen sc1 lds\n\t" \
  "s_add_u32 m0, s46, 16384\n\t" \
  "s_add_u32 s47, s55, 32768\n\t" \
  "buffer_load_dwordx4 v0, %[X], s47 offen sc1 lds\n\t" \
  "s_add_u32 m0, s46, 20480\n\t" \
  "s_add_u32 s47, s55, 36864\n\t" \
  "buffer_load_dwordx4 v0, %[X], s47 offen sc1 lds\n\t" \
  "s_add_u32 m0, s46, 24576\n\t" \
  "s_add_u32 s47, s55, 49152\n\t" \
  "buffer_load_dwordx4 v0, %[X], s47 offen sc1 lds\n\t" \
  "s_add_u32 m0, s46, 28672\n\t" \
  "s_add_u32 s47, s55, 53248\n\t" \
  "buffer_load_dwordx4 v0, %[X], s47 offen sc1 lds\n\t" \
  "s_add_u32 m0, s46, 32768\n\t" \
  "s_add_u32 s47, s55, 65536\n\t" \
  "buffer_load_dwordx4 v0, %[X], s47 offen sc1 lds\n\t" \
  "s_waitcnt vmcnt(5)\n\t" \
  "LTOP_%=:\n\t" \
  "s_barrier\n\t" \
  "ds_read_b128 v[64:67], v1 offset:0\n\t" \
  "ds_read_b128 v[68:71], v1 offset:2048\n\t" \
  "ds_read_b128 v[72:75], v2 offset:0\n\t" \
  "ds_read_b128 v[76:79], v2 offset:2048\n\t" \
  "ds_read_b128 v[80:83], v1 offset:4096\n\t" \
  "ds_read_b128 v[84:87], v1 offset:6144\n\t" \
  "s_mov_b32 s82, s83\n\t" \
  "s_xor_b32 s46, s46, 0x10000\n\t" \
  "s_waitcnt lgkmcnt(5)\n\t" \
  "v_mfma_f32_16x16x32_bf16 v[16:19], a[0:3], v[64:67], 0\n\t" \
  "ds_read_b128 v[88:91], v2 offset:4096\n\t" \
  "v_mfma_f32_16x16x32_bf16 v[20:23], a[72:75], v[64:67], 0\n\t" \
  "s_add_u32 s83, s48, 1\n\t" \
  "s_lshr_b64 s[60:61], s[90:91], s83\n\t" \
  "v_mfma_f32_16x16x32_bf16 v[24:27], a[144:147], v[64:67], 0\n\t" \
  "ds_read_b128 v[92:95], v2 offset:6144\n\t" \
  "v_mfma_f32_16x16x32_bf16 v[28:31], a[216:219], v[64:67], 0\n\t" \
  "s_and_b32 s60, s60, 1\n\t" \
  "s_lshl_b32 s60, s60, 13\n\t" \
  "s_waitcnt lgkmcnt(5)\n\t" \
  "v_mfma_f32_16x16x32_bf16 v[16:19], a[4:7], v[72:75], v[16:19]\n\t" \
  "ds_read_b128 v[96:99], v1 offset:8192\n\t" \
  "v_mfma_f32_16x16x32_bf16 v[20:23], a[76:79], v[72:75], v[20:23]\n\t" \
  "s_add_u32 s83, s83, " TK_SG0 "\n\t" \
  "s_lshl_b32 s83, s83, 21\n\t" \
  "v_mfma_f32_16x16x32_bf16 v[24:27], a[148:151], v[72:75], v[24:27]\n\t" \
  "ds_read_b128 v[100:103], v1 offset:10240\n\t" \
  "v_mfma_f32_16x16x32_bf16 v[28:31], a[220:223], v[72:75], v[28:31]\n\t" \
  "s_or_b32 s83, s83, s60\n\t" \
  "s_add_u32 s62, s83, s39\n\t" \
  "s_waitcnt lgkmcnt(5)\n\t" \
  "v_mfma_f32_16x16x32_bf16 v[16:19], a[8:11], v[80:83], v[16:19]\n\t" \
  "ds_read_b128 v[104:107], v2 offset:8192\n\t" \
  "v_mfma_f32_16x16x32_bf16 v[20:23], a[80:83], v[80:83], v[20:23]\n\t" \
  "v_add_u32 v7, s62, v3\n\t" \
  "s_add_u32 s50, s82, s37\n\t" \
  "v_mfma_f32_16x16x32_bf16 v[24:27], a[152:155], v[80:83], v[24:27]\n\t" \
  "ds_read_b128 v[108:111], v2 offset:10240\n\t" \
  "v_mfma_f32_16x16x32_bf16 v[28:31], a[224:227], v[80:83], v[28:31]\n\t" \
  "s_add_u32 s51, s50, 4096\n\t" \
  "s_waitcnt lgkmcnt(5)\n\t" \
  "v_mfma_f32_16x16x32_bf16 v[16:19], a[12:15], v[88:91], v[16:19]\n\t" \
  "ds_read_b128 v[112:115], v1 offset:12288\n\t" \
  "v_mfma_f32_16x16x32_bf16 v[20:23], a[84:87], v[88:91], v[20:23]\n\t" \
  "v_mfma_f32_16x16x32_bf16 v[24:27], a[156:159], v[88:91], v[24:27]\n\t" \
  "ds_read_b128 v[116:119], v1 offset:14336\n\t" \
  "v_mfma_f32_16x16x32_bf16 v[28:31], a[228:231], v[88:91], v[28:31]\n\t" \
  "s_waitcnt lgkmcnt(5)\n\t" \
  "v_mfma_f32_16x16x32_bf16 v[16:19], a[16:19], v[96:99], v[16:19]\n\t" \
  "ds_read_b128 v[120:123], v2 offset:12288\n\t" \
  "v_mfma_f32_16x16x32_bf16 v[20:23], a[88:91], v[96:99], v[20:23]\n\t" \
  "s_cmp_lg_u32 s56, 0\n\t" \
  "v_mfma_f32_16x16x32_bf16 v[24:27], a[160:163], v[96:99], v[24:27]\n\t" \
  "ds_read_b128 v[124:127], v2 offset:14336\n\t" \
  "v_mfma_f32_16x16x32_bf16 v[28:31], a[232:235], v[96:99], v[28:31]\n\t" \
  "s_cbranch_scc1 LWARM_%=\n\t" \
  "s_waitcnt vmcnt(0)\n\t" \
  "LWARM_%=:\n\t" \
  "s_barrier\n\t" \
  "s_waitcnt lgkmcnt(5)\n\t" \
  "v_mfma_f32_16x16x32_bf16 v[16:19], a[20:23], v[104:107], v[16:19]\n\t" \
  "ds_read_b128 v[128:131], v1 offset:16384\n\t" \
  "v_mfma_f32_16x16x32_bf16 v[20:23], a[92:95], v[104:107], v[20:23]\n\t" \
  "v_mfma_f32_16x16x32_bf16 v[24:27], a[164:167], v[104:107], v[24:27]\n\t" \
  "ds_read_b128 v[132:135], v1 offset:18432\n\t" \
  "v_mfma_f32_16x16x32_bf16 v[28:31], a[236:239], v[104:107], v[28:31]\n\t" \
  "s_add_i32 s62, s48, s42\n\t" \
  "s_add_i32 s62, s62, 1\n\t" \
  "s_waitcnt lgkmcnt(5)\n\t" \
  "v_mfma_f32_16x16x32_bf16 v[16:19], a[24:27], v[112:115], v[16:19]\n\t" \
  "ds_read_b128 v[136:139], v2 offset:16384\n\t" \
  "v_mfma_f32_16x16x32_bf16 v[20:23], a[96:99], v[112:115], v[20:23]\n\t" \
  "s_mul_i32 s63, s62, 43\n\t" \
  "s_lshr_b32 s63, s63, 7\n\t" \
  "v_mfma_f32_16x16x32_bf16 v[24:27], a[168:171], v[112:115], v[24:27]\n\t" \
  "ds_read_b128 v[140:143], v2 offset:18432\n\t" \
  "v_mfma_f32_16x16x32_bf16 v[28:31], a[240:243], v[112:115], v[28:31]\n\t" \
  "s_mul_i32 s57, s62, 688128\n\t" \
  "s_lshl_b32 s60, s63, 15\n\t" \
  "s_waitcnt lgkmcnt(5)\n\t" \
  "v_mfma_f32_16x16x32_bf16 v[16:19], a[28:31], v[120:123], v[16:19]\n\t" \
  "ds_read_b128 v[144:147], v1 offset:20480\n\t" \
  "v_mfma_f32_16x16x32_bf16 v[20:23], a[100:103], v[120:123], v[20:23]\n\t" \
  "s_add_u32 s57, s57, s60\n\t" \
  "s_lshr_b32 s60, s88, s63\n\t" \
  "v_mfma_f32_16x16x32_bf16 v[24:27], a[172:175], v[120:123], v[24:27]\n\t" \
  "ds_read_b128 v[148:151], v1 offset:22528\n\t" \
  "v_mfma_f32_16x16x32_bf16 v[28:31], a[244:247], v[120:123], v[28:31]\n\t" \
  "s_and_b32 s60, s60, 1\n\t" \
  "s_lshl_b32 s60, s60, 13\n\t" \
  "s_waitcnt lgkmcnt(5)\n\t" \
  "v_mfma_f32_16x16x32_bf16 v[16:19], a[32:35], v[128:131], v[16:19]\n\t" \
  "ds_read_b128 v[152:155], v2 offset:20480\n\t" \
  "v_mfma_f32_16x16x32_bf16 v[20:23], a[104:107], v[128:131], v[20:23]\n\t" \
  "s_add_u32 s57, s57, s60\n\t" \
  "s_add_u32 s57, s57, 656896\n\t" \
  "v_mfma_f32_16x16x32_bf16 v[24:27], a[176:179], v[128:131], v[24:27]\n\t" \
  "ds_read_b128 v[156:159], v2 offset:22528\n\t" \
  "v_mfma_f32_16x16x32_bf16 v[28:31], a[248:251], v[128:131], v[28:31]\n\t" \
  "s_cmp_lt_i32 s62, 1\n\t" \
  "s_cselect_b32 s57, 0xf0000000, s57\n\t" \
  "s_waitcnt lgkmcnt(5)\n\t" \
  "v_mfma_f32_16x16x32_bf16 v[16:19], a[36:39], v[136:139], v[16:19]\n\t" \
  "ds_read_b128 v[160:163], v1 offset:24576\n\t" \
  "v_mfma_f32_16x16x32_bf16 v[20:23], a[108:111], v[136:139], v[20:23]\n\t" \
  "s_cmp_lg_u32 s40, 0\n\t" \
  "s_cselect_b32 s57, 0xf0000000, s57\n\t" \
  "v_mfma_f32_16x16x32_bf16 v[24:27], a[180:183], v[136:139], v[24:27]\n\t" \
  "ds_read_b128 v[164:167], v1 offset:26624\n\t" \
  "v_mfma_f32_16x16x32_bf16 v[28:31], a[252:255], v[136:139], v[28:31]\n\t" \
  "s_waitcnt lgkmcnt(5)\n\t" \
  "v_mfma_f32_16x16x32_bf16 v[16:19], a[40:43], v[144:147], v[16:19]\n\t" \
  "ds_read_b128 v[168:171], v2 offset:24576\n\t" \
  "v_mfma_f32_16x16x32_bf16 v[20:23], a[112:115], v[144:147], v[20:23]\n\t" \
  "v_mfma_f32_16x16x32_bf16 v[24:27], a[184:187], v[144:147], v[24:27]\n\t" \
  "ds_read_b128 v[172:175], v2 offset:26624\n\t" \
  "v_mfma_f32_16x16x32_bf16 v[28:31], v[224:227], v[144:147], v[28:31]\n\t" \
  "s_waitcnt lgkmcnt(5)\n\t" \
  "v_mfma_f32_16x16x32_bf16 v[16:19], a[44:47], v[152:155], v[16:19]\n\t" \
  "ds_read_b128 v[176:179], v1 offset:28672\n\t" \
  "v_mfma_f32_16x16x32_bf16 v[20:23], a[116:119], v[152:155], v[20:23]\n\t" \
  "v_mfma_f32_16x16x32_bf16 v[24:27], a[188:191], v[152:155], v[24:27]\n\t" \
  "ds_read_b128 v[180:183], v1 offset:30720\n\t" \
  "v_mfma_f32_16x16x32_bf16 v[28:31], v[228:231], v[152:155], v[28:31]\n\t" \
  "s_waitcnt lgkmcnt(5)\n\t" \
  "v_mfma_f32_16x16x32_bf16 v[16:19], a[48:51], v[160:163], v[16:19]\n\t" \
  "ds_read_b128 v[184:187], v2 offset:28672\n\t" \
  "v_mfma_f32_16x16x32_bf16 v[20:23], a[120:123], v[160:163], v[20:23]\n\t" \
  "v_mfma_f32_16x16x32_bf16 v[24:27], a[192:195], v[160:163], v[24:27]\n\t" \
  "ds_read_b128 v[188:191], v2 offset:30720\n\t" \
  "v_mfma_f32_16x16x32_bf16 v[28:31], v[232:235], v[160:163], v[28:31]\n\t" \
  "s_waitcnt lgkmcnt(5)\n\t" \
  "v_mfma_f32_16x16x32_bf16 v[16:19], a[52:55], v[168:171], v[16:19]\n\t" \
  "ds_read_b128 v[192:195], v1 offset:32768\n\t" \
  "v_mfma_f32_16x16x32_bf16 v[20:23], a[124:127], v[168:171], v[20:23]\n\t" \
  "v_mfma_f32_16x16x32_bf16 v[24:27], a[196:199], v[168:171], v[24:27]\n\t" \
  "ds_read_b128 v[196:199], v1 offset:34816\n\t" \
  "v_mfma_f32_16x16x32_bf16 v[28:31], v[236:239], v[168:171], v[28:31]\n\t" \
  "s_waitcnt lgkmcnt(5)\n\t" \
  "v_mfma_f32_16x16x32_bf16 v[16:19], a[56:59], v[176:179], v[16:19]\n\t" \
  "ds_read_b128 v[200:203], v2 offset:32768\n\t" \
  "v_mfma_f32_16x16x32_bf16 v[20:23], a[128:131], v[176:179], v[20:23]\n\t" \
  "v_mfma_f32_16x16x32_bf16 v[24:27], a[200:203], v[176:179], v[24:27]\n\t" \
  "ds_read_b128 v[204:207], v2 offset:34816\n\t" \
  "v_mfma_f32_16x16x32_bf16 v[28:31], v[240:243], v[176:179], v[28:31]\n\t" \
  "s_waitcnt lgkmcnt(5)\n\t" \
  "v_mfma_f32_16x16x32_bf16 v[16:19], a[60:63], v[184:187], v[16:19]\n\t" \
  "v_mfma_f32_16x16x32_bf16 v[20:23], a[132:135], v[184:187], v[20:23]\n\t" \
  "buffer_load_dword v4, v7, %[X], 0 offen sc1\n\t" \
  "v_mfma_f32_16x16x32_bf16 v[24:27], a[204:207], v[184:187], v[24:27]\n\t" \
  "v_mfma_f32_16x16x32_bf16 v[28:31], v[244:247], v[184:187], v[28:31]\n\t" \
  "s_waitcnt lgkmcnt(3)\n\t" \
  "v_mfma_f32_16x16x32_bf16 v[16:19], a[64:67], v[192:195], v[16:19]\n\t" \
  "v_xor_b32 v1, 0x10000, v1\n\t" \
  "v_xor_b32 v2, 0x10000, v2\n\t" \
  "v_mfma_f32_16x16x32_bf16 v[20:23], a[136:139], v[192:195], v[20:23]\n\t" \
  "v_mfma_f32_16x16x32_bf16 v[24:27], a[208:211], v[192:195], v[24:27]\n\t" \
  "v_mfma_f32_16x16x32_bf16 v[28:31], v[248:251], v[192:195], v[28:31]\n\t" \
  "s_waitcnt lgkmcnt(1)\n\t" \
  "v_mfma_f32_16x16x32_bf16 v[16:19], a[68:71], v[200:203], v[16:19]\n\t" \
  "v_mfma_f32_16x16x32_bf16 v[20:23], a[140:143], v[200:203], v[20:23]\n\t" \
  "v_mfma_f32_16x16x32_bf16 v[24:27], a[212:215], v[200:203], v[24:27]\n\t" \
  "v_mfma_f32_16x16x32_bf16 v[28:31], v[252:255], v[200:203], v[28:31]\n\t" \
  "v_mfma_f32_16x16x32_bf16 v[32:35], a[0:3], v[68:71], 0\n\t" \
  "v_cvt_pk_bf16_f32 v48, v16, v17\n\t" \
  "v_cvt_pk_bf16_f32 v49, v18, v19\n\t" \
  "v_mfma_f32_16x16x32_bf16 v[36:39], a[72:75], v[68:71], 0\n\t" \
  "v_cvt_pk_bf16_f32 v50, v20, v21\n\t" \
  "v_cvt_pk_bf16_f32 v51, v22, v23\n\t" \
  "v_mfma_f32_16x16x32_bf16 v[32:35], a[4:7], v[76:79], v[32:35]\n\t" \
  "v_cvt_pk_bf16_f32 v52, v24, v25\n\t" \
  "v_cvt_pk_bf16_f32 v53, v26, v27\n\t" \
  "v_mfma_f32_16x16x32_bf16 v[36:39], a[76:79], v[76:79], v[36:39]\n\t" \
  "v_cvt_pk_bf16_f32 v54, v28, v29\n\t" \
  "v_cvt_pk_bf16_f32 v55, v30, v31\n\t" \
  "v_mfma_f32_16x16x32_bf16 v[32:35], a[8:11], v[84:87], v[32:35]\n\t" \
  "buffer_store_dwordx4 v[48:51], v0, %[X], s50 offen offset:0 sc1\n\t" \
  "v_mfma_f32_16x16x32_bf16 v[36:39], a[80:83], v[84:87], v[36:39]\n\t" \
  "v_mfma_f32_16x16x32_bf16 v[32:35], a[12:15], v[92:95], v[32:35]\n\t" \
  "v_mfma_f32_16x16x32_bf16 v[36:39], a[84:87], v[92:95], v[36:39]\n\t" \
  "v_mfma_f32_16x16x32_bf16 v[32:35], a[16:19], v[100:103], v[32:35]\n\t" \
  "v_mfma_f32_16x16x32_bf16 v[36:39], a[88:91], v[100:103], v[36:39]\n\t" \
  "v_mfma_f32_16x16x32_bf16 v[32:35], a[20:23], v[108:111], v[32:35]\n\t" \
  "v_mfma_f32_16x16x32_bf16 v[36:39], a[92:95], v[108:111], v[36:39]\n\t" \
  "v_mfma_f32_16x16x32_bf16 v[32:35], a[24:27], v[116:119], v[32:35]\n\t" \
  "v_mfma_f32_16x16x32_bf16 v[36:39], a[96:99], v[116:119], v[36:39]\n\t" \
  "v_mfma_f32_16x16x32_bf16 v[32:35], a[28:31], v[124:127], v[32:35]\n\t" \
  "v_mfma_f32_16x16x32_bf16 v[36:39], a[100:103], v[124:127], v[36:39]\n\t" \
  "s_waitcnt vmcnt(1)\n\t" \
  "v_cmp_eq_u32 vcc, 0, v4\n\t" \
  "v_mfma_f32_16x16x32_bf16 v[32:35], a[32:35], v[132:135], v[32:35]\n\t" \
  "v_mfma_f32_16x16x32_bf16 v[36:39], a[104:107], v[132:135], v[36:39]\n\t" \
  "s_cmp_eq_u64 vcc, 0\n\t" \
  "s_cselect_b32 s60, 1, 0\n\t" \
  "v_mov_b32 v4, s60\n\t" \
  "v_mfma_f32_16x16x32_bf16 v[32:35], a[36:39], v[140:143], v[32:35]\n\t" \
  "ds_write_b32 v5, v4\n\t" \
  "v_mfma_f32_16x16x32_bf16 v[36:39], a[108:111], v[140:143], v[36:39]\n\t" \
  "v_mfma_f32_16x16x32_bf16 v[32:35], a[40:43], v[148:151], v[32:35]\n\t" \
  "v_mfma_f32_16x16x32_bf16 v[36:39], a[112:115], v[148:151], v[36:39]\n\t" \
  "buffer_store_dwordx4 v[52:55], v0, %[X], s50 offen offset:1024 sc1\n\t" \
  "v_mfma_f32_16x16x32_bf16 v[32:35], a[44:47], v[156:159], v[32:35]\n\t" \
  "v_mfma_f32_16x16x32_bf16 v[36:39], a[116:119], v[156:159], v[36:39]\n\t" \
  "s_waitcnt lgkmcnt(0)\n\t" \
  "s_barrier\n\t" \
  "v_mfma_f32_16x16x32_bf16 v[32:35], a[48:51], v[164:167], v[32:35]\n\t" \
  "ds_read_b32 v4, v6 offset:40959\n\t" \
  "v_mfma_f32_16x16x32_bf16 v[36:39], a[120:123], v[164:167], v[36:39]\n\t" \
  "v_mfma_f32_16x16x32_bf16 v[32:35], a[52:55], v[172:175], v[32:35]\n\t" \
  "v_mfma_f32_16x16x32_bf16 v[36:39], a[124:127], v[172:175], v[36:39]\n\t" \
  "v_mfma_f32_16x16x32_bf16 v[32:35], a[56:59], v[180:183], v[32:35]\n\t" \
  "v_mfma_f32_16x16x32_bf16 v[36:39], a[128:131], v[180:183], v[36:39]\n\t" \
  "s_waitcnt lgkmcnt(0)\n\t" \
  "v_readfirstlane_b32 s60, v4\n\t" \
  "s_add_u32 s55, s83, s36\n\t" \
  "v_mfma_f32_16x16x32_bf16 v[32:35], a[60:63], v[188:191], v[32:35]\n\t" \
  "s_cmp_eq_u32 s48, 63\n\t" \
  "s_cselect_b32 s56, 0, s60\n\t" \
  "v_mfma_f32_16x16x32_bf16 v[36:39], a[132:135], v[188:191], v[36:39]\n\t" \
  "s_cmp_eq_u32 s56, 0\n\t" \
  "s_cselect_b32 s55, 0xf0000000, s55\n\t" \
  "v_mfma_f32_16x16x32_bf16 v[32:35], a[64:67], v[196:199], v[32:35]\n\t" \
  "v_mfma_f32_16x16x32_bf16 v[36:39], a[136:139], v[196:199], v[36:39]\n\t" \
  "v_mfma_f32_16x16x32_bf16 v[32:35], a[68:71], v[204:207], v[32:35]\n\t" \
  "s_add_u32 m0, s46, 0\n\t" \
  "s_add_u32 s47, s55, 0\n\t" \
  "buffer_load_dwordx4 v0, %[X], s47 offen sc1 lds\n\t" \
  "v_mfma_f32_16x16x32_bf16 v[36:39], a[140:143], v[204:207], v[36:39]\n\t" \
  "v_mfma_f32_16x16x32_bf16 v[40:43], a[144:147], v[68:71], 0\n\t" \
  "v_mfma_f32_16x16x32_bf16 v[44:47], a[216:219], v[68:71], 0\n\t" \
  "v_mfma_f32_16x16x32_bf16 v[40:43], a[148:151], v[76:79], v[40:43]\n\t" \
  "v_cvt_pk_bf16_f32 v56, v32, v33\n\t" \
  "v_cvt_pk_bf16_f32 v57, v34, v35\n\t" \
  "s_add_u32 m0, s46, 4096\n\t" \
  "s_add_u32 s47, s55, 4096\n\t" \
  "buffer_load_dwordx4 v0, %[X], s47 offen sc1 lds\n\t" \
  "v_mfma_f32_16x16x32_bf16 v[44:47], a[220:223], v[76:79], v[44:47]\n\t" \
  "v_cvt_pk_bf16_f32 v58, v36, v37\n\t" \
  "v_cvt_pk_bf16_f32 v59, v38, v39\n\t" \
  "v_mfma_f32_16x16x32_bf16 v[40:43], a[152:155], v[84:87], v[40:43]\n\t" \
  "v_mfma_f32_16x16x32_bf16 v[44:47], a[224:227], v[84:87], v[44:47]\n\t" \
  "buffer_store_dwordx4 v[56:59], v0, %[X], s51 offen offset:0 sc1\n\t" \
  "v_mfma_f32_16x16x32_bf16 v[40:43], a[156:159], v[92:95], v[40:43]\n\t" \
  "s_add_u32 m0, s46, 8192\n\t" \
  "s_add_u32 s47, s55, 16384\n\t" \
  "buffer_load_dwordx4 v0, %[X], s47 offen sc1 lds\n\t" \
  "v_mfma_f32_16x16x32_bf16 v[44:47], a[228:231], v[92:95], v[44:47]\n\t" \
  "v_mfma_f32_16x16x32_bf16 v[40:43], a[160:163], v[100:103], v[40:43]\n\t" \
  "v_mfma_f32_16x16x32_bf16 v[44:47], a[232:235], v[100:103], v[44:47]\n\t" \
  "v_mfma_f32_16x16x32_bf16 v[40:43], a[164:167], v[108:111], v[40:43]\n\t" \
  "s_add_u32 m0, s46, 12288\n\t" \
  "s_add_u32 s47, s55, 20480\n\t" \
  "buffer_load_dwordx4 v0, %[X], s47 offen sc1 lds\n\t" \
  "v_mfma_f32_16x16x32_bf16 v[44:47], a[236:239], v[108:111], v[44:47]\n\t" \
  "v_mfma_f32_16x16x32_bf16 v[40:43], a[168:171], v[116:119], v[40:43]\n\t" \
  "v_mfma_f32_16x16x32_bf16 v[44:47], a[240:243], v[116:119], v[44:47]\n\t" \
  "v_mfma_f32_16x16x32_bf16 v[40:43], a[172:175], v[124:127], v[40:43]\n\t" \
  "s_add_u32 m0, s46, 16384\n\t" \
  "s_add_u32 s47, s55, 32768\n\t" \
  "buffer_load_dwordx4 v0, %[X], s47 offen sc1 lds\n\t" \
  "v_mfma_f32_16x16x32_bf16 v[44:47], a[244:247], v[124:127], v[44:47]\n\t" \
  "v_mfma_f32_16x16x32_bf16 v[40:43], a[176:179], v[132:135], v[40:43]\n\t" \
  "v_mfma_f32_16x16x32_bf16 v[44:47], a[248:251], v[132:135], v[44:47]\n\t" \
  "v_mfma_f32_16x16x32_bf16 v[40:43], a[180:183], v[140:143], v[40:43]\n\t" \
  "s_add_u32 m0, s46, 20480\n\t" \
  "s_add_u32 s47, s55, 36864\n\t" \
  "buffer_load_dwordx4 v0, %[X], s47 offen sc1 lds\n\t" \
  "v_mfma_f32_16x16x32_bf16 v[44:47], a[252:255], v[140:143], v[44:47]\n\t" \
  "v_mfma_f32_16x16x32_bf16 v[40:43], a[184:187], v[148:151], v[40:43]\n\t" \
  "v_mfma_f32_16x16x32_bf16 v[44:47], v[224:227], v[148:151], v[44:47]\n\t" \
  "v_mfma_f32_16x16x32_bf16 v[40:43], a[188:191], v[156:159], v[40:43]\n\t" \
  "s_add_u32 m0, s46, 24576\n\t" \
  "s_add_u32 s47, s55, 49152\n\t" \
  "buffer_load_dwordx4 v0, %[X], s47 offen sc1 lds\n\t" \
  "v_mfma_f32_16x16x32_bf16 v[44:47], v[228:231], v[156:159], v[44:47]\n\t" \
  "v_mfma_f32_16x16x32_bf16 v[40:43], a[192:195], v[164:167], v[40:43]\n\t" \
  "v_mfma_f32_16x16x32_bf16 v[44:47], v[232:235], v[164:167], v[44:47]\n\t" \
  "v_mfma_f32_16x16x32_bf16 v[40:43], a[196:199], v[172:175], v[40:43]\n\t" \
  "s_add_u32 m0, s46, 28672\n\t" \
  "s_add_u32 s47, s55, 53248\n\t" \
  "buffer_load_dwordx4 v0, %[X], s47 offen sc1 lds\n\t" \
  "v_mfma_f32_16x16x32_bf16 v[44:47], v[236:239], v[172:175], v[44:47]\n\t" \
  "v_mfma_f32_16x16x32_bf16 v[40:43], a[200:203], v[180:183], v[40:43]\n\t" \
  "v_mfma_f32_16x16x32_bf16 v[44:47], v[240:243], v[180:183], v[44:47]\n\t" \
  "v_mfma_f32_16x16x32_bf16 v[40:43], a[204:207], v[188:191], v[40:43]\n\t" \
  "s_add_u32 m0, s46, 32768\n\t" \
  "s_add_u32 s47, s55, 65536\n\t" \
  "buffer_load_dwordx4 v0, %[X], s47 offen sc1 lds\n\t" \
  "v_mfma_f32_16x16x32_bf16 v[44:47], v[244:247], v[188:191], v[44:47]\n\t" \
  "v_mfma_f32_16x16x32_bf16 v[40:43], a[208:211], v[196:199], v[40:43]\n\t" \
  "v_mfma_f32_16x16x32_bf16 v[44:47], v[248:251], v[196:199], v[44:47]\n\t" \
  "buffer_load_dword v8, off, %[X], s57 sc1\n\t" \
  "v_mfma_f32_16x16x32_bf16 v[40:43], a[212:215], v[204:207], v[40:43]\n\t" \
  "v_mfma_f32_16x16x32_bf16 v[44:47], v[252:255], v[204:207], v[44:47]\n\t" \
  "s_nop 4\n\t" \
  "v_cvt_pk_bf16_f32 v60, v40, v41\n\t" \
  "v_cvt_pk_bf16_f32 v61, v42, v43\n\t" \
  "s_nop 1\n\t" \
  "v_cvt_pk_bf16_f32 v62, v44, v45\n\t" \
  "v_cvt_pk_bf16_f32 v63, v46, v47\n\t" \
  "buffer_store_dwordx4 v[60:63], v0, %[X], s51 offen offset:1024 sc1\n\t" \
  "s_add_u32 s49, s82, s38\n\t" \
  "s_cmp_eq_u32 s48, 63\n\t" \
  "s_cbranch_scc1 LEND_%=\n\t" \
  "s_add_u32 s48, s48, 1\n\t" \
  "s_cmp_eq_u32 s56, 0\n\t" \
  "s_cbranch_scc1 LCOLD_%=\n\t" \
  "s_waitcnt vmcnt(0)\n\t" \
  "s_mov_b64 exec, 1\n\t" \
  "buffer_store_dword v6, off, %[X], s49 sc1\n\t" \
  "s_mov_b64 exec, -1\n\t" \
  "s_mov_b32 s49, 0xf0000000\n\t" \
  "s_cmp_lt_i32 s57, 0\n\t" \
  "s_cbranch_scc1 LTOP_%=\n\t" \
  "s_movk_i32 s63, 4000\n\t" \
  "s_branch LGW_%=u\n\t" \
  "LGW_%=:\n\t" \
  "s_sleep " TK_PSG "\n\t" \
  "buffer_load_dword v8, off, %[X], s57 sc1\n\t" \
  "s_waitcnt vmcnt(0)\n\t" \
  "LGW_%=u:\n\t" \
  "s_memrealtime s[52:53]\n\t" \
  "v_readfirstlane_b32 s60, v8\n\t" \
  "s_waitcnt lgkmcnt(0)\n\t" \
  "s_sub_u32 s63, s63, 1\n\t" \
  "s_cbranch_scc1 LTOP_%=\n\t" \
  "s_cmp_eq_u32 s60, -1\n\t" \
  "s_cbranch_scc1 LGW_%=\n\t" \
  "s_not_b32 s60, s60\n\t" \
  "s_lshl_b32 s61, s52, 8\n\t" \
  "s_sub_u32 s60, s60, s61\n\t" \
  "s_ashr_i32 s60, s60, 8\n\t" \
  "s_sub_i32 s60, s60, " TK_QXW "\n\t" \
  "s_min_i32 s60, s60, 400\n\t" \
  "s_add_u32 s54, s52, s60\n\t" \
  "LGW_%=w:\n\t" \
  "s_sleep 1\n\t" \
  "s_memrealtime s[52:53]\n\t" \
  "s_waitcnt lgkmcnt(0)\n\t" \
  "s_sub_i32 s53, s52, s54\n\t" \
  "s_cmp_lt_i32 s53, 0\n\t" \
  "s_cbranch_scc1 LGW_%=w\n\t" \
  "s_branch LTOP_%=\n\t" \
  "LEND_%=:\n\t" \
  "s_waitcnt vmcnt(0)\n\t" \
  "s_mov_b64 exec, 1\n\t" \
  "buffer_store_dword v6, off, %[X], s49 sc1\n\t" \
  "s_mov_b64 exec, -1\n\t" \
  "s_mov_b32 s49, 0xf0000000\n\t" \
  ""
#define QV_ASM \
  "v_lshrrev_b32 v16, 7, v0\n\t" \
  "v_lshlrev_b32 v16, 13, v16\n\t" \
  "v_lshrrev_b32 v19, 2, v0\n\t" \
  "v_and_b32 v19, 64, v19\n\t" \
  "v_xor_b32 v19, v19, v0\n\t" \
  "v_and_b32 v19, 0x70, v19\n\t" \
  "v_add_u32 v16, v16, v19\n\t" \
  "s_mul_i32 s63, s40, 40960\n\t" \
  "v_and_b32 v19, 0x300, v0\n\t" \
  "v_lshlrev_b32 v17, 2, v19\n\t" \
  "v_and_b32 v19, 0xc0, v0\n\t" \
  "v_lshl_add_u32 v17, v19, 1, v17\n\t" \
  "v_and_b32 v19, 0x30, v0\n\t" \
  "v_lshrrev_b32 v19, 1, v19\n\t" \
  "v_add3_u32 v17, v17, v19, s63\n\t" \
  "v_lshrrev_b32 v19, 1, v0\n\t" \
  "v_and_b32 v19, 64, v19\n\t" \
  "v_add_u32 v17, v17, v19\n\t" \
  "v_xor_b32 v18, 64, v17\n\t" \
  "s_add_u32 m0, s63, 0\n\t" \
  "s_mov_b32 s60, 0\n\t" \
  "buffer_load_dwordx4 v16, %[W], s60 offen lds\n\t" \
  "s_add_u32 m0, s63, 1024\n\t" \
  "s_mov_b32 s60, 65536\n\t" \
  "buffer_load_dwordx4 v16, %[W], s60 offen lds\n\t" \
  "s_add_u32 m0, s63, 2048\n\t" \
  "s_mov_b32 s60, 131072\n\t" \
  "buffer_load_dwordx4 v16, %[W], s60 offen lds\n\t" \
  "s_add_u32 m0, s63, 3072\n\t" \
  "s_mov_b32 s60, 196608\n\t" \
  "buffer_load_dwordx4 v16, %[W], s60 offen lds\n\t" \
  "s_add_u32 m0, s63, 4096\n\t" \
  "s_mov_b32 s60, 262144\n\t" \
  "buffer_load_dwordx4 v16, %[W], s60 offen lds\n\t" \
  "s_add_u32 m0, s63, 5120\n\t" \
  "s_mov_b32 s60, 327680\n\t" \
  "buffer_load_dwordx4 v16, %[W], s60 offen lds\n\t" \
  "s_add_u32 m0, s63, 6144\n\t" \
  "s_mov_b32 s60, 393216\n\t" \
  "buffer_load_dwordx4 v16, %[W], s60 offen lds\n\t" \
  "s_add_u32 m0, s63, 7168\n\t" \
  "s_mov_b32 s60, 458752\n\t" \
  "buffer_load_dwordx4 v16, %[W], s60 offen lds\n\t" \
  "s_add_u32 m0, s63, 8192\n\t" \
  "s_mov_b32 s60, 524288\n\t" \
  "buffer_load_dwordx4 v16, %[W], s60 offen lds\n\t" \
  "s_add_u32 m0, s63, 9216\n\t" \
  "s_mov_b32 s60, 589824\n\t" \
  "buffer_load_dwordx4 v16, %[W], s60 offen lds\n\t" \
  "s_add_u32 m0, s63, 10240\n\t" \
  "s_mov_b32 s60, 655360\n\t" \
  "buffer_load_dwordx4 v16, %[W], s60 offen lds\n\t" \
  "s_add_u32 m0, s63, 11264\n\t" \
  "s_mov_b32 s60, 720896\n\t" \
  "buffer_load_dwordx4 v16, %[W], s60 offen lds\n\t" \
  "s_add_u32 m0, s63, 12288\n\t" \
  "s_mov_b32 s60, 786432\n\t" \
  "buffer_load_dwordx4 v16, %[W], s60 offen lds\n\t" \
  "s_add_u32 m0, s63, 13312\n\t" \
  "s_mov_b32 s60, 851968\n\t" \
  "buffer_load_dwordx4 v16, %[W], s60 offen lds\n\t" \
  "s_add_u32 m0, s63, 14336\n\t" \
  "s_mov_b32 s60, 917504\n\t" \
  "buffer_load_dwordx4 v16, %[W], s60 offen lds\n\t" \
  "s_add_u32 m0, s63, 15360\n\t" \
  "s_mov_b32 s60, 983040\n\t" \
  "buffer_load_dwordx4 v16, %[W], s60 offen lds\n\t" \
  "s_add_u32 m0, s63, 16384\n\t" \
  "s_mov_b32 s60, 1048576\n\t" \
  "buffer_load_dwordx4 v16, %[W], s60 offen lds\n\t" \
  "s_add_u32 m0, s63, 17408\n\t" \
  "s_mov_b32 s60, 1114112\n\t" \
  "buffer_load_dwordx4 v16, %[W], s60 offen lds\n\t" \
  "s_add_u32 m0, s63, 18432\n\t" \
  "s_mov_b32 s60, 1179648\n\t" \
  "buffer_load_dwordx4 v16, %[W], s60 offen lds\n\t" \
  "s_add_u32 m0, s63, 19456\n\t" \
  "s_mov_b32 s60, 1245184\n\t" \
  "buffer_load_dwordx4 v16, %[W], s60 offen lds\n\t" \
  "s_add_u32 m0, s63, 20480\n\t" \
  "s_mov_b32 s60, 1310720\n\t" \
  "buffer_load_dwordx4 v16, %[W], s60 offen lds\n\t" \
  "s_add_u32 m0, s63, 21504\n\t" \
  "s_mov_b32 s60, 1376256\n\t" \
  "buffer_load_dwordx4 v16, %[W], s60 offen lds\n\t" \
  "s_add_u32 m0, s63, 22528\n\t" \
  "s_mov_b32 s60, 1441792\n\t" \
  "buffer_load_dwordx4 v16, %[W], s60 offen lds\n\t" \
  "s_add_u32 m0, s63, 23552\n\t" \
  "s_mov_b32 s60, 1507328\n\t" \
  "buffer_load_dwordx4 v16, %[W], s60 offen lds\n\t" \
  "s_add_u32 m0, s63, 24576\n\t" \
  "s_mov_b32 s60, 1572864\n\t" \
  "buffer_load_dwordx4 v16, %[W], s60 offen lds\n\t" \
  "s_add_u32 m0, s63, 25600\n\t" \
  "s_mov_b32 s60, 1638400\n\t" \
  "buffer_load_dwordx4 v16, %[W], s60 offen lds\n\t" \
  "s_add_u32 m0, s63, 26624\n\t" \
  "s_mov_b32 s60, 1703936\n\t" \
  "buffer_load_dwordx4 v16, %[W], s60 offen lds\n\t" \
  "s_add_u32 m0, s63, 27648\n\t" \
  "s_mov_b32 s60, 1769472\n\t" \
  "buffer_load_dwordx4 v16, %[W], s60 offen lds\n\t" \
  "s_add_u32 m0, s63, 28672\n\t" \
  "s_mov_b32 s60, 1835008\n\t" \
  "buffer_load_dwordx4 v16, %[W], s60 offen lds\n\t" \
  "s_add_u32 m0, s63, 29696\n\t" \
  "s_mov_b32 s60, 1900544\n\t" \
  "buffer_load_dwordx4 v16, %[W], s60 offen lds\n\t" \
  "s_add_u32 m0, s63, 30720\n\t" \
  "s_mov_b32 s60, 1966080\n\t" \
  "buffer_load_dwordx4 v16, %[W], s60 offen lds\n\t" \
  "s_add_u32 m0, s63, 31744\n\t" \
  "s_mov_b32 s60, 2031616\n\t" \
  "buffer_load_dwordx4 v16, %[W], s60 offen lds\n\t" \
  "s_add_u32 m0, s63, 32768\n\t" \
  "s_mov_b32 s60, 2097152\n\t" \
  "buffer_load_dwordx4 v16, %[W], s60 offen lds\n\t" \
  "s_add_u32 m0, s63, 33792\n\t" \
  "s_mov_b32 s60, 2162688\n\t" \
  "buffer_load_dwordx4 v16, %[W], s60 offen lds\n\t" \
  "s_add_u32 m0, s63, 34816\n\t" \
  "s_mov_b32 s60, 2228224\n\t" \
  "buffer_load_dwordx4 v16, %[W], s60 offen lds\n\t" \
  "s_add_u32 m0, s63, 35840\n\t" \
  "s_mov_b32 s60, 2293760\n\t" \
  "buffer_load_dwordx4 v16, %[W], s60 offen lds\n\t" \
  "s_add_u32 m0, s63, 36864\n\t" \
  "s_mov_b32 s60, 2359296\n\t" \
  "buffer_load_dwordx4 v16, %[W], s60 offen lds\n\t" \
  "s_add_u32 m0, s63, 37888\n\t" \
  "s_mov_b32 s60, 2424832\n\t" \
  "buffer_load_dwordx4 v16, %[W], s60 offen lds\n\t" \
  "s_add_u32 m0, s63, 38912\n\t" \
  "s_mov_b32 s60, 2490368\n\t" \
  "buffer_load_dwordx4 v16, %[W], s60 offen lds\n\t" \
  "s_add_u32 m0, s63, 39936\n\t" \
  "s_mov_b32 s60, 2555904\n\t" \
  "buffer_load_dwordx4 v16, %[W], s60 offen lds\n\t" \
  "s_waitcnt vmcnt(32)\n\t" \
  "ds_read_b64_tr_b16 a[0:1], v17 offset:0\n\t" \
  "ds_read_b64_tr_b16 a[2:3], v17 offset:512\n\t" \
  "ds_read_b64_tr_b16 a[4:5], v17 offset:4096\n\t" \
  "ds_read_b64_tr_b16 a[6:7], v17 offset:4608\n\t" \
  "ds_read_b64_tr_b16 a[72:73], v17 offset:32\n\t" \
  "ds_read_b64_tr_b16 a[74:75], v17 offset:544\n\t" \
  "ds_read_b64_tr_b16 a[76:77], v17 offset:4128\n\t" \
  "ds_read_b64_tr_b16 a[78:79], v17 offset:4640\n\t" \
  "ds_read_b64_tr_b16 a[144:145], v18 offset:0\n\t" \
  "ds_read_b64_tr_b16 a[146:147], v18 offset:512\n\t" \
  "ds_read_b64_tr_b16 a[148:149], v18 offset:4096\n\t" \
  "ds_read_b64_tr_b16 a[150:151], v18 offset:4608\n\t" \
  "ds_read_b64_tr_b16 a[216:217], v18 offset:32\n\t" \
  "ds_read_b64_tr_b16 a[218:219], v18 offset:544\n\t" \
  "ds_read_b64_tr_b16 a[220:221], v18 offset:4128\n\t" \
  "ds_read_b64_tr_b16 a[222:223], v18 offset:4640\n\t" \
  "s_waitcnt lgkmcnt(0)\n\t" \
  "s_add_u32 m0, s63, 0\n\t" \
  "s_mov_b32 s60, 2621440\n\t" \
  "buffer_load_dwordx4 v16, %[W], s60 offen lds\n\t" \
  "s_add_u32 m0, s63, 1024\n\t" \
  "s_mov_b32 s60, 2686976\n\t" \
  "buffer_load_dwordx4 v16, %[W], s60 offen lds\n\t" \
  "s_add_u32 m0, s63, 2048\n\t" \
  "s_mov_b32 s60, 2752512\n\t" \
  "buffer_load_dwordx4 v16, %[W], s60 offen lds\n\t" \
  "s_add_u32 m0, s63, 3072\n\t" \
  "s_mov_b32 s60, 2818048\n\t" \
  "buffer_load_dwordx4 v16, %[W], s60 offen lds\n\t" \
  "s_add_u32 m0, s63, 4096\n\t" \
  "s_mov_b32 s60, 2883584\n\t" \
  "buffer_load_dwordx4 v16, %[W], s60 offen lds\n\t" \
  "s_add_u32 m0, s63, 5120\n\t" \
  "s_mov_b32 s60, 2949120\n\t" \
  "buffer_load_dwordx4 v16, %[W], s60 offen lds\n\t" \
  "s_add_u32 m0, s63, 6144\n\t" \
  "s_mov_b32 s60, 3014656\n\t" \
  "buffer_load_dwordx4 v16, %[W], s60 offen lds\n\t" \
  "s_add_u32 m0, s63, 7168\n\t" \
  "s_mov_b32 s60, 3080192\n\t" \
  "buffer_load_dwordx4 v16, %[W], s60 offen lds\n\t" \
  "s_waitcnt vmcnt(32)\n\t" \
  "ds_read_b64_tr_b16 a[8:9], v17 offset:8192\n\t" \
  "ds_read_b64_tr_b16 a[10:11], v17 offset:8704\n\t" \
  "ds_read_b64_tr_b16 a[12:13], v17 offset:12288\n\t" \
  "ds_read_b64_tr_b16 a[14:15], v17 offset:12800\n\t" \
  "ds_read_b64_tr_b16 a[80:81], v17 offset:8224\n\t" \
  "ds_read_b64_tr_b16 a[82:83], v17 offset:8736\n\t" \
  "ds_read_b64_tr_b16 a[84:85], v17 offset:12320\n\t" \
  "ds_read_b64_tr_b16 a[86:87], v17 offset:12832\n\t" \
  "ds_read_b64_tr_b16 a[152:153], v18 offset:8192\n\t" \
  "ds_read_b64_tr_b16 a[154:155], v18 offset:8704\n\t" \
  "ds_read_b64_tr_b16 a[156:157], v18 offset:12288\n\t" \
  "ds_read_b64_tr_b16 a[158:159], v18 offset:12800\n\t" \
  "ds_read_b64_tr_b16 a[224:225], v18 offset:8224\n\t" \
  "ds_read_b64_tr_b16 a[226:227], v18 offset:8736\n\t" \
  "ds_read_b64_tr_b16 a[228:229], v18 offset:12320\n\t" \
  "ds_read_b64_tr_b16 a[230:231], v18 offset:12832\n\t" \
  "s_waitcnt lgkmcnt(0)\n\t" \
  "s_add_u32 m0, s63, 8192\n\t" \
  "s_mov_b32 s60, 3145728\n\t" \
  "buffer_load_dwordx4 v16, %[W], s60 offen lds\n\t" \
  "s_add_u32 m0, s63, 9216\n\t" \
  "s_mov_b32 s60, 3211264\n\t" \
  "buffer_load_dwordx4 v16, %[W], s60 offen lds\n\t" \
  "s_add_u32 m0, s63, 10240\n\t" \
  "s_mov_b32 s60, 3276800\n\t" \
  "buffer_load_dwordx4 v16, %[W], s60 offen lds\n\t" \
  "s_add_u32 m0, s63, 11264\n\t" \
  "s_mov_b32 s60, 3342336\n\t" \
  "buffer_load_dwordx4 v16, %[W], s60 offen lds\n\t" \
  "s_add_u32 m0, s63, 12288\n\t" \
  "s_mov_b32 s60, 3407872\n\t" \
  "buffer_load_dwordx4 v16, %[W], s60 offen lds\n\t" \
  "s_add_u32 m0, s63, 13312\n\t" \
  "s_mov_b32 s60, 3473408\n\t" \
  "buffer_load_dwordx4 v16, %[W], s60 offen lds\n\t" \
  "s_add_u32 m0, s63, 14336\n\t" \
  "s_mov_b32 s60, 3538944\n\t" \
  "buffer_load_dwordx4 v16, %[W], s60 offen lds\n\t" \
  "s_add_u32 m0, s63, 15360\n\t" \
  "s_mov_b32 s60, 3604480\n\t" \
  "buffer_load_dwordx4 v16, %[W], s60 offen lds\n\t" \
  "s_waitcnt vmcnt(32)\n\t" \
  "ds_read_b64_tr_b16 a[16:17], v17 offset:16384\n\t" \
  "ds_read_b64_tr_b16 a[18:19], v17 offset:16896\n\t" \
  "ds_read_b64_tr_b16 a[20:21], v17 offset:20480\n\t" \
  "ds_read_b64_tr_b16 a[22:23], v17 offset:20992\n\t" \
  "ds_read_b64_tr_b16 a[88:89], v17 offset:16416\n\t" \
  "ds_read_b64_tr_b16 a[90:91], v17 offset:16928\n\t" \
  "ds_read_b64_tr_b16 a[92:93], v17 offset:20512\n\t" \
  "ds_read_b64_tr_b16 a[94:95], v17 offset:21024\n\t" \
  "ds_read_b64_tr_b16 a[160:161], v18 offset:16384\n\t" \
  "ds_read_b64_tr_b16 a[162:163], v18 offset:16896\n\t" \
  "ds_read_b64_tr_b16 a[164:165], v18 offset:20480\n\t" \
  "ds_read_b64_tr_b16 a[166:167], v18 offset:20992\n\t" \
  "ds_read_b64_tr_b16 a[232:233], v18 offset:16416\n\t" \
  "ds_read_b64_tr_b16 a[234:235], v18 offset:16928\n\t" \
  "ds_read_b64_tr_b16 a[236:237], v18 offset:20512\n\t" \
  "ds_read_b64_tr_b16 a[238:239], v18 offset:21024\n\t" \
  "s_waitcnt lgkmcnt(0)\n\t" \
  "s_add_u32 m0, s63, 16384\n\t" \
  "s_mov_b32 s60, 3670016\n\t" \
  "buffer_load_dwordx4 v16, %[W], s60 offen lds\n\t" \
  "s_add_u32 m0, s63, 17408\n\t" \
  "s_mov_b32 s60, 3735552\n\t" \
  "buffer_load_dwordx4 v16, %[W], s60 offen lds\n\t" \
  "s_add_u32 m0, s63, 18432\n\t" \
  "s_mov_b32 s60, 3801088\n\t" \
  "buffer_load_dwordx4 v16, %[W], s60 offen lds\n\t" \
  "s_add_u32 m0, s63, 19456\n\t" \
  "s_mov_b32 s60, 3866624\n\t" \
  "buffer_load_dwordx4 v16, %[W], s60 offen lds\n\t" \
  "s_add_u32 m0, s63, 20480\n\t" \
  "s_mov_b32 s60, 3932160\n\t" \
  "buffer_load_dwordx4 v16, %[W], s60 offen lds\n\t" \
  "s_add_u32 m0, s63, 21504\n\t" \
  "s_mov_b32 s60, 3997696\n\t" \
  "buffer_load_dwordx4 v16, %[W], s60 offen lds\n\t" \
  "s_add_u32 m0, s63, 22528\n\t" \
  "s_mov_b32 s60, 4063232\n\t" \
  "buffer_load_dwordx4 v16, %[W], s60 offen lds\n\t" \
  "s_add_u32 m0, s63, 23552\n\t" \
  "s_mov_b32 s60, 4128768\n\t" \
  "buffer_load_dwordx4 v16, %[W], s60 offen lds\n\t" \
  "s_waitcnt vmcnt(32)\n\t" \
  "ds_read_b64_tr_b16 a[24:25], v17 offset:24576\n\t" \
  "ds_read_b64_tr_b16 a[26:27], v17 offset:25088\n\t" \
  "ds_read_b64_tr_b16 a[28:29], v17 offset:28672\n\t" \
  "ds_read_b64_tr_b16 a[30:31], v17 offset:29184\n\t" \
  "ds_read_b64_tr_b16 a[96:97], v17 offset:24608\n\t" \
  "ds_read_b64_tr_b16 a[98:99], v17 offset:25120\n\t" \
  "ds_read_b64_tr_b16 a[100:101], v17 offset:28704\n\t" \
  "ds_read_b64_tr_b16 a[102:103], v17 offset:29216\n\t" \
  "ds_read_b64_tr_b16 a[168:169], v18 offset:24576\n\t" \
  "ds_read_b64_tr_b16 a[170:171], v18 offset:25088\n\t" \
  "ds_read_b64_tr_b16 a[172:173], v18 offset:28672\n\t" \
  "ds_read_b64_tr_b16 a[174:175], v18 offset:29184\n\t" \
  "ds_read_b64_tr_b16 a[240:241], v18 offset:24608\n\t" \
  "ds_read_b64_tr_b16 a[242:243], v18 offset:25120\n\t" \
  "ds_read_b64_tr_b16 a[244:245], v18 offset:28704\n\t" \
  "ds_read_b64_tr_b16 a[246:247], v18 offset:29216\n\t" \
  "s_waitcnt lgkmcnt(0)\n\t" \
  "s_add_u32 m0, s63, 24576\n\t" \
  "s_mov_b32 s60, 4194304\n\t" \
  "buffer_load_dwordx4 v16, %[W], s60 offen lds\n\t" \
  "s_add_u32 m0, s63, 25600\n\t" \
  "s_mov_b32 s60, 4259840\n\t" \
  "buffer_load_dwordx4 v16, %[W], s60 offen lds\n\t" \
  "s_add_u32 m0, s63, 26624\n\t" \
  "s_mov_b32 s60, 4325376\n\t" \
  "buffer_load_dwordx4 v16, %[W], s60 offen lds\n\t" \
  "s_add_u32 m0, s63, 27648\n\t" \
  "s_mov_b32 s60, 4390912\n\t" \
  "buffer_load_dwordx4 v16, %[W], s60 offen lds\n\t" \
  "s_add_u32 m0, s63, 28672\n\t" \
  "s_mov_b32 s60, 4456448\n\t" \
  "buffer_load_dwordx4 v16, %[W], s60 offen lds\n\t" \
  "s_add_u32 m0, s63, 29696\n\t" \
  "s_mov_b32 s60, 4521984\n\t" \
  "buffer_load_dwordx4 v16, %[W], s60 offen lds\n\t" \
  "s_add_u32 m0, s63, 30720\n\t" \
  "s_mov_b32 s60, 4587520\n\t" \
  "buffer_load_dwordx4 v16, %[W], s60 offen lds\n\t" \
  "s_add_u32 m0, s63, 31744\n\t" \
  "s_mov_b32 s60, 4653056\n\t" \
  "buffer_load_dwordx4 v16, %[W], s60 offen lds\n\t" \
  "s_waitcnt vmcnt(32)\n\t" \
  "ds_read_b64_tr_b16 a[32:33], v17 offset:32768\n\t" \
  "ds_read_b64_tr_b16 a[34:35], v17 offset:33280\n\t" \
  "ds_read_b64_tr_b16 a[36:37], v17 offset:36864\n\t" \
  "ds_read_b64_tr_b16 a[38:39], v17 offset:37376\n\t" \
  "ds_read_b64_tr_b16 a[104:105], v17 offset:32800\n\t" \
  "ds_read_b64_tr_b16 a[106:107], v17 offset:33312\n\t" \
  "ds_read_b64_tr_b16 a[108:109], v17 offset:36896\n\t" \
  "ds_read_b64_tr_b16 a[110:111], v17 offset:37408\n\t" \
  "ds_read_b64_tr_b16 a[176:177], v18 offset:32768\n\t" \
  "ds_read_b64_tr_b16 a[178:179], v18 offset:33280\n\t" \
  "ds_read_b64_tr_b16 a[180:181], v18 offset:36864\n\t" \
  "ds_read_b64_tr_b16 a[182:183], v18 offset:37376\n\t" \
  "ds_read_b64_tr_b16 a[248:249], v18 offset:32800\n\t" \
  "ds_read_b64_tr_b16 a[250:251], v18 offset:33312\n\t" \
  "ds_read_b64_tr_b16 a[252:253], v18 offset:36896\n\t" \
  "ds_read_b64_tr_b16 a[254:255], v18 offset:37408\n\t" \
  "s_waitcnt lgkmcnt(0)\n\t" \
  "s_waitcnt vmcnt(24)\n\t" \
  "ds_read_b64_tr_b16 a[40:41], v17 offset:0\n\t" \
  "ds_read_b64_tr_b16 a[42:43], v17 offset:512\n\t" \
  "ds_read_b64_tr_b16 a[44:45], v17 offset:4096\n\t" \
  "ds_read_b64_tr_b16 a[46:47], v17 offset:4608\n\t" \
  "ds_read_b64_tr_b16 a[112:113], v17 offset:32\n\t" \
  "ds_read_b64_tr_b16 a[114:115], v17 offset:544\n\t" \
  "ds_read_b64_tr_b16 a[116:117], v17 offset:4128\n\t" \
  "ds_read_b64_tr_b16 a[118:119], v17 offset:4640\n\t" \
  "ds_read_b64_tr_b16 a[184:185], v18 offset:0\n\t" \
  "ds_read_b64_tr_b16 a[186:187], v18 offset:512\n\t" \
  "ds_read_b64_tr_b16 a[188:189], v18 offset:4096\n\t" \
  "ds_read_b64_tr_b16 a[190:191], v18 offset:4608\n\t" \
  "ds_read_b64_tr_b16 v[224:225], v18 offset:32\n\t" \
  "ds_read_b64_tr_b16 v[226:227], v18 offset:544\n\t" \
  "ds_read_b64_tr_b16 v[228:229], v18 offset:4128\n\t" \
  "ds_read_b64_tr_b16 v[230:231], v18 offset:4640\n\t" \
  "s_waitcnt lgkmcnt(0)\n\t" \
  "s_waitcnt vmcnt(16)\n\t" \
  "ds_read_b64_tr_b16 a[48:49], v17 offset:8192\n\t" \
  "ds_read_b64_tr_b16 a[50:51], v17 offset:8704\n\t" \
  "ds_read_b64_tr_b16 a[52:53], v17 offset:12288\n\t" \
  "ds_read_b64_tr_b16 a[54:55], v17 offset:12800\n\t" \
  "ds_read_b64_tr_b16 a[120:121], v17 offset:8224\n\t" \
  "ds_read_b64_tr_b16 a[122:123], v17 offset:8736\n\t" \
  "ds_read_b64_tr_b16 a[124:125], v17 offset:12320\n\t" \
  "ds_read_b64_tr_b16 a[126:127], v17 offset:12832\n\t" \
  "ds_read_b64_tr_b16 a[192:193], v18 offset:8192\n\t" \
  "ds_read_b64_tr_b16 a[194:195], v18 offset:8704\n\t" \
  "ds_read_b64_tr_b16 a[196:197], v18 offset:12288\n\t" \
  "ds_read_b64_tr_b16 a[198:199], v18 offset:12800\n\t" \
  "ds_read_b64_tr_b16 v[232:233], v18 offset:8224\n\t" \
  "ds_read_b64_tr_b16 v[234:235], v18 offset:8736\n\t" \
  "ds_read_b64_tr_b16 v[236:237], v18 offset:12320\n\t" \
  "ds_read_b64_tr_b16 v[238:239], v18 offset:12832\n\t" \
  "s_waitcnt lgkmcnt(0)\n\t" \
  "s_waitcnt vmcnt(8)\n\t" \
  "ds_read_b64_tr_b16 a[56:57], v17 offset:16384\n\t" \
  "ds_read_b64_tr_b16 a[58:59], v17 offset:16896\n\t" \
  "ds_read_b64_tr_b16 a[60:61], v17 offset:20480\n\t" \
  "ds_read_b64_tr_b16 a[62:63], v17 offset:20992\n\t" \
  "ds_read_b64_tr_b16 a[128:129], v17 offset:16416\n\t" \
  "ds_read_b64_tr_b16 a[130:131], v17 offset:16928\n\t" \
  "ds_read_b64_tr_b16 a[132:133], v17 offset:20512\n\t" \
  "ds_read_b64_tr_b16 a[134:135], v17 offset:21024\n\t" \
  "ds_read_b64_tr_b16 a[200:201], v18 offset:16384\n\t" \
  "ds_read_b64_tr_b16 a[202:203], v18 offset:16896\n\t" \
  "ds_read_b64_tr_b16 a[204:205], v18 offset:20480\n\t" \
  "ds_read_b64_tr_b16 a[206:207], v18 offset:20992\n\t" \
  "ds_read_b64_tr_b16 v[240:241], v18 offset:16416\n\t" \
  "ds_read_b64_tr_b16 v[242:243], v18 offset:16928\n\t" \
  "ds_read_b64_tr_b16 v[244:245], v18 offset:20512\n\t" \
  "ds_read_b64_tr_b16 v[246:247], v18 offset:21024\n\t" \
  "s_waitcnt lgkmcnt(0)\n\t" \
  "s_waitcnt vmcnt(0)\n\t" \
  "ds_read_b64_tr_b16 a[64:65], v17 offset:24576\n\t" \
  "ds_read_b64_tr_b16 a[66:67], v17 offset:25088\n\t" \
  "ds_read_b64_tr_b16 a[68:69], v17 offset:28672\n\t" \
  "ds_read_b64_tr_b16 a[70:71], v17 offset:29184\n\t" \
  "ds_read_b64_tr_b16 a[136:137], v17 offset:24608\n\t" \
  "ds_read_b64_tr_b16 a[138:139], v17 offset:25120\n\t" \
  "ds_read_b64_tr_b16 a[140:141], v17 offset:28704\n\t" \
  "ds_read_b64_tr_b16 a[142:143], v17 offset:29216\n\t" \
  "ds_read_b64_tr_b16 a[208:209], v18 offset:24576\n\t" \
  "ds_read_b64_tr_b16 a[210:211], v18 offset:25088\n\t" \
  "ds_read_b64_tr_b16 a[212:213], v18 offset:28672\n\t" \
  "ds_read_b64_tr_b16 a[214:215], v18 offset:29184\n\t" \
  "ds_read_b64_tr_b16 v[248:249], v18 offset:24608\n\t" \
  "ds_read_b64_tr_b16 v[250:251], v18 offset:25120\n\t" \
  "ds_read_b64_tr_b16 v[252:253], v18 offset:28704\n\t" \
  "ds_read_b64_tr_b16 v[254:255], v18 offset:29216\n\t" \
  "s_waitcnt lgkmcnt(0)\n\t" \
  "s_mov_b32 s84, " TK_CAP "\n\t" \
  "s_mov_b32 s48, 0\n\t" \
  "s_mov_b32 s49, 0xf0000000\n\t" \
  "s_mov_b32 s46, s41\n\t" \
  "s_mov_b32 s56, 0\n\t" \
  "v_lshrrev_b32 v7, 2, v0\n\t" \
  "s_mov_b32 s60, " TK_CTAB "\n\t" \
  "buffer_load_dword v4, v7, %[X], s60 offen offset:88 sc1\n\t" \
  "buffer_load_dword v7, v7, %[X], s60 offen offset:0 sc1\n\t" \
  "s_waitcnt vmcnt(0)\n\t" \
  "v_and_b32 v4, 1, v4\n\t" \
  "v_and_b32 v7, 1, v7\n\t" \
  "v_cmp_ne_u32 s[90:91], s67, v4\n\t" \
  "v_cmp_ne_u32 s[88:89], s67, v7\n\t" \
  "s_nop 1\n\t" \
  "s_mov_b32 s57, 0xf0000000\n\t" \
  "s_add_u32 s83, s48, 0\n\t" \
  "s_lshr_b64 s[60:61], s[90:91], s83\n\t" \
  "s_and_b32 s60, s60, 1\n\t" \
  "s_lshl_b32 s60, s60, 13\n\t" \
  "s_add_u32 s83, s83, " TK_SG0 "\n\t" \
  "s_lshl_b32 s83, s83, 21\n\t" \
  "s_or_b32 s83, s83, s60\n\t" \
  "s_add_u32 s62, s83, s39\n\t" \
  "v_add_u32 v7, s62, v3\n\t" \
  "LCOLD_%=:\n\t" \
  "s_waitcnt vmcnt(0)\n\t" \
  "s_mov_b64 exec, 1\n\t" \
  "buffer_store_dword v6, off, %[X], s49 sc1\n\t" \
  "s_mov_b64 exec, -1\n\t" \
  "s_mov_b32 s49, 0xf0000000\n\t" \
  "s_add_u32 s55, s83, s36\n\t" \
  "s_cmp_lg_u32 s40, 0\n\t" \
  "s_cbranch_scc1 LGO_%=\n\t" \
  "s_and_b32 s62, s45, 3\n\t" \
  "s_lshl_b32 s62, s62, 7\n\t" \
  "s_add_u32 s62, s62, s83\n\t" \
  "s_add_u32 s62, s62, s39\n\t" \
  "s_mov_b32 s61, 0\n\t" \
  "LPO_%=:\n\t" \
  "buffer_load_dword v4, off, %[X], s62 sc1\n\t" \
  "s_waitcnt vmcnt(0)\n\t" \
  "v_cmp_eq_u32 vcc, 0, v4\n\t" \
  "s_cbranch_vccz LPO_%=x\n\t" \
  "s_add_u32 s61, s61, 1\n\t" \
  "s_sleep " TK_PSH "\n\t" \
  "s_cmp_gt_u32 s61, s84\n\t" \
  "s_cbranch_scc1 LPO_%=g\n\t" \
  "s_and_b32 s60, s61, 1023\n\t" \
  "s_cmp_lg_u32 s60, 0\n\t" \
  "s_cbranch_scc1 LPO_%=\n\t" \
  "buffer_load_dword v4, off, %[X], s92 sc1\n\t" \
  "s_waitcnt vmcnt(0)\n\t" \
  "v_readfirstlane_b32 s60, v4\n\t" \
  "s_nop 0\n\t" \
  "s_cmp_eq_u32 s60, 0\n\t" \
  "s_cbranch_scc1 LPO_%=\n\t" \
  "LPO_%=g:\n\t" \
  "s_mov_b32 s84, 0\n\t" \
  "s_or_b32 s60, s48, 1610612736\n\t" \
  "v_mov_b32 v4, s60\n\t" \
  "s_mov_b64 exec, 1\n\t" \
  "buffer_store_dword v4, off, %[X], s92 sc1\n\t" \
  "s_mov_b64 exec, -1\n\t" \
  "LPO_%=x:\n\t" \
  "s_mov_b32 s61, 0\n\t" \
  "LPH_%=:\n\t" \
  "buffer_load_dword v4, v7, %[X], 0 offen sc1\n\t" \
  "s_waitcnt vmcnt(0)\n\t" \
  "v_cmp_eq_u32 vcc, 0, v4\n\t" \
  "s_cbranch_vccz LPH_%=x\n\t" \
  "s_add_u32 s61, s61, 1\n\t" \
  "s_sleep " TK_PSH "\n\t" \
  "s_cmp_gt_u32 s61, s84\n\t" \
  "s_cbranch_scc1 LPH_%=g\n\t" \
  "s_and_b32 s60, s61, 1023\n\t" \
  "s_cmp_lg_u32 s60, 0\n\t" \
  "s_cbranch_scc1 LPH_%=\n\t" \
  "buffer_load_dword v4, off, %[X], s92 sc1\n\t" \
  "s_waitcnt vmcnt(0)\n\t" \
  "v_readfirstlane_b32 s60, v4\n\t" \
  "s_nop 0\n\t" \
  "s_cmp_eq_u32 s60, 0\n\t" \
  "s_cbranch_scc1 LPH_%=\n\t" \
  "LPH_%=g:\n\t" \
  "s_mov_b32 s84, 0\n\t" \
  "s_or_b32 s60, s48, 1610612736\n\t" \
  "v_mov_b32 v4, s60\n\t" \
  "s_mov_b64 exec, 1\n\t" \
  "buffer_store_dword v4, off, %[X], s92 sc1\n\t" \
  "s_mov_b64 exec, -1\n\t" \
  "LPH_%=x:\n\t" \
  "s_cmp_lt_i32 s57, 0\n\t" \
  "s_cbranch_scc1 LGO_%=\n\t" \
  "s_movk_i32 s63, 4000\n\t" \
  "s_branch LGC_%=u\n\t" \
  "LGC_%=:\n\t" \
  "s_sleep " TK_PSG "\n\t" \
  "buffer_load_dword v8, off, %[X], s57 sc1\n\t" \
  "s_waitcnt vmcnt(0)\n\t" \
  "LGC_%=u:\n\t" \
  "s_memrealtime s[52:53]\n\t" \
  "v_readfirstlane_b32 s60, v8\n\t" \
  "s_waitcnt lgkmcnt(0)\n\t" \
  "s_sub_u32 s63, s63, 1\n\t" \
  "s_cbranch_scc1 LGO_%=\n\t" \
  "s_cmp_eq_u32 s60, -1\n\t" \
  "s_cbranch_scc1 LGC_%=\n\t" \
  "s_not_b32 s60, s60\n\t" \
  "s_lshl_b32 s61, s52, 8\n\t" \
  "s_sub_u32 s60, s60, s61\n\t" \
  "s_ashr_i32 s60, s60, 8\n\t" \
  "s_sub_i32 s60, s60, " TK_QXC "\n\t" \
  "s_min_i32 s60, s60, 400\n\t" \
  "s_add_u32 s54, s52, s60\n\t" \
  "LGC_%=w:\n\t" \
  "s_sleep 1\n\t" \
  "s_memrealtime s[52:53]\n\t" \
  "s_waitcnt lgkmcnt(0)\n\t" \
  "s_sub_i32 s53, s52, s54\n\t" \
  "s_cmp_lt_i32 s53, 0\n\t" \
  "s_cbranch_scc1 LGC_%=w\n\t" \
  "LGO_%=:\n\t" \
  "s_barrier\n\t" \
  "s_add_u32 m0, s46, 0\n\t" \
  "s_add_u32 s47, s55, 0\n\t" \
  "buffer_load_dwordx4 v0, %[X], s47 offen sc1 lds\n\t" \
  "s_add_u32 m0, s46, 4096\n\t" \
  "s_add_u32 s47, s55, 4096\n\t" \
  "buffer_load_dwordx4 v0, %[X], s47 offen sc1 lds\n\t" \
  "s_add_u32 m0, s46, 8192\n\t" \
  "s_add_u32 s47, s55, 16384\n\t" \
  "buffer_load_dwordx4 v0, %[X], s47 offen sc1 lds\n\t" \
  "s_add_u32 m0, s46, 12288\n\t" \
  "s_add_u32 s47, s55, 20480\n\t" \
  "buffer_load_dwordx4 v0, %[X], s47 offen sc1 lds\n\t" \
  "s_add_u32 m0, s46, 16384\n\t" \
  "s_add_u32 s47, s55, 32768\n\t" \
  "buffer_load_dwordx4 v0, %[X], s47 offen sc1 lds\n\t" \
  "s_add_u32 m0, s46, 20480\n\t" \
  "s_add_u32 s47, s55, 36864\n\t" \
  "buffer_load_dwordx4 v0, %[X], s47 offen sc1 lds\n\t" \
  "s_add_u32 m0, s46, 24576\n\t" \
  "s_add_u32 s47, s55, 49152\n\t" \
  "buffer_load_dwordx4 v0, %[X], s47 offen sc1 lds\n\t" \
  "s_add_u32 m0, s46, 28672\n\t" \
  "s_add_u32 s47, s55, 53248\n\t" \
  "buffer_load_dwordx4 v0, %[X], s47 offen sc1 lds\n\t" \
  "s_add_u32 m0, s46, 32768\n\t" \
  "s_add_u32 s47, s55, 65536\n\t" \
  "buffer_load_dwordx4 v0, %[X], s47 offen sc1 lds\n\t" \
  "s_waitcnt vmcnt(5)\n\t" \
  "LTOP_%=:\n\t" \
  "s_barrier\n\t" \
  "ds_read_b128 v[64:67], v1 offset:0\n\t" \
  "ds_read_b128 v[68:71], v1 offset:2048\n\t" \
  "ds_read_b128 v[72:75], v2 offset:0\n\t" \
  "ds_read_b128 v[76:79], v2 offset:2048\n\t" \
  "ds_read_b128 v[80:83], v1 offset:4096\n\t" \
  "ds_read_b128 v[84:87], v1 offset:6144\n\t" \
  "s_mov_b32 s82, s83\n\t" \
  "s_xor_b32 s46, s46, 0x10000\n\t" \
  "s_waitcnt lgkmcnt(5)\n\t" \
  "v_mfma_f32_16x16x32_bf16 v[16:19], v[64:67], a[0:3], 0\n\t" \
  "ds_read_b128 v[88:91], v2 offset:4096\n\t" \
  "s_waitcnt lgkmcnt(5)\n\t" \
  "v_mfma_f32_16x16x32_bf16 v[20:23], v[68:71], a[0:3], 0\n\t" \
  "s_add_u32 s83, s48, 1\n\t" \
  "s_lshr_b64 s[60:61], s[90:91], s83\n\t" \
  "v_mfma_f32_16x16x32_bf16 v[24:27], v[64:67], a[72:75], 0\n\t" \
  "ds_read_b128 v[92:95], v2 offset:6144\n\t" \
  "v_mfma_f32_16x16x32_bf16 v[28:31], v[68:71], a[72:75], 0\n\t" \
  "s_and_b32 s60, s60, 1\n\t" \
  "s_lshl_b32 s60, s60, 13\n\t" \
  "s_waitcnt lgkmcnt(5)\n\t" \
  "v_mfma_f32_16x16x32_bf16 v[16:19], v[72:75], a[4:7], v[16:19]\n\t" \
  "ds_read_b128 v[96:99], v1 offset:8192\n\t" \
  "s_waitcnt lgkmcnt(5)\n\t" \
  "v_mfma_f32_16x16x32_bf16 v[20:23], v[76:79], a[4:7], v[20:23]\n\t" \
  "s_add_u32 s83, s83, " TK_SG0 "\n\t" \
  "s_lshl_b32 s83, s83, 21\n\t" \
  "v_mfma_f32_16x16x32_bf16 v[24:27], v[72:75], a[76:79], v[24:27]\n\t" \
  "ds_read_b128 v[100:103], v1 offset:10240\n\t" \
  "v_mfma_f32_16x16x32_bf16 v[28:31], v[76:79], a[76:79], v[28:31]\n\t" \
  "s_or_b32 s83, s83, s60\n\t" \
  "s_add_u32 s62, s83, s39\n\t" \
  "s_waitcnt lgkmcnt(5)\n\t" \
  "v_mfma_f32_16x16x32_bf16 v[16:19], v[80:83], a[8:11], v[16:19]\n\t" \
  "ds_read_b128 v[104:107], v2 offset:8192\n\t" \
  "s_waitcnt lgkmcnt(5)\n\t" \
  "v_mfma_f32_16x16x32_bf16 v[20:23], v[84:87], a[8:11], v[20:23]\n\t" \
  "v_add_u32 v7, s62, v3\n\t" \
  "s_add_u32 s50, s82, s37\n\t" \
  "v_mfma_f32_16x16x32_bf16 v[24:27], v[80:83], a[80:83], v[24:27]\n\t" \
  "ds_read_b128 v[108:111], v2 offset:10240\n\t" \
  "v_mfma_f32_16x16x32_bf16 v[28:31], v[84:87], a[80:83], v[28:31]\n\t" \
  "s_add_u32 s51, s50, 4096\n\t" \
  "s_waitcnt lgkmcnt(5)\n\t" \
  "v_mfma_f32_16x16x32_bf16 v[16:19], v[88:91], a[12:15], v[16:19]\n\t" \
  "ds_read_b128 v[112:115], v1 offset:12288\n\t" \
  "s_waitcnt lgkmcnt(5)\n\t" \
  "v_mfma_f32_16x16x32_bf16 v[20:23], v[92:95], a[12:15], v[20:23]\n\t" \
  "v_mfma_f32_16x16x32_bf16 v[24:27], v[88:91], a[84:87], v[24:27]\n\t" \
  "ds_read_b128 v[116:119], v1 offset:14336\n\t" \
  "v_mfma_f32_16x16x32_bf16 v[28:31], v[92:95], a[84:87], v[28:31]\n\t" \
  "s_waitcnt lgkmcnt(5)\n\t" \
  "v_mfma_f32_16x16x32_bf16 v[16:19], v[96:99], a[16:19], v[16:19]\n\t" \
  "ds_read_b128 v[120:123], v2 offset:12288\n\t" \
  "s_waitcnt lgkmcnt(5)\n\t" \
  "v_mfma_f32_16x16x32_bf16 v[20:23], v[100:103], a[16:19], v[20:23]\n\t" \
  "s_cmp_lg_u32 s56, 0\n\t" \
  "v_mfma_f32_16x16x32_bf16 v[24:27], v[96:99], a[88:91], v[24:27]\n\t" \
  "ds_read_b128 v[124:127], v2 offset:14336\n\t" \
  "v_mfma_f32_16x16x32_bf16 v[28:31], v[100:103], a[88:91], v[28:31]\n\t" \
  "s_cbranch_scc1 LWARM_%=\n\t" \
  "s_waitcnt vmcnt(0)\n\t" \
  "LWARM_%=:\n\t" \
  "s_barrier\n\t" \
  "s_waitcnt lgkmcnt(5)\n\t" \
  "v_mfma_f32_16x16x32_bf16 v[16:19], v[104:107], a[20:23], v[16:19]\n\t" \
  "ds_read_b128 v[128:131], v1 offset:16384\n\t" \
  "s_waitcnt lgkmcnt(5)\n\t" \
  "v_mfma_f32_16x16x32_bf16 v[20:23], v[108:111], a[20:23], v[20:23]\n\t" \
  "v_mfma_f32_16x16x32_bf16 v[24:27], v[104:107], a[92:95], v[24:27]\n\t" \
  "ds_read_b128 v[132:135], v1 offset:18432\n\t" \
  "v_mfma_f32_16x16x32_bf16 v[28:31], v[108:111], a[92:95], v[28:31]\n\t" \
  "s_add_i32 s62, s48, s42\n\t" \
  "s_add_i32 s62, s62, 1\n\t" \
  "s_waitcnt lgkmcnt(5)\n\t" \
  "v_mfma_f32_16x16x32_bf16 v[16:19], v[112:115], a[24:27], v[16:19]\n\t" \
  "ds_read_b128 v[136:139], v2 offset:16384\n\t" \
  "s_waitcnt lgkmcnt(5)\n\t" \
  "v_mfma_f32_16x16x32_bf16 v[20:23], v[116:119], a[24:27], v[20:23]\n\t" \
  "s_mul_i32 s63, s62, 43\n\t" \
  "s_lshr_b32 s63, s63, 7\n\t" \
  "v_mfma_f32_16x16x32_bf16 v[24:27], v[112:115], a[96:99], v[24:27]\n\t" \
  "ds_read_b128 v[140:143], v2 offset:18432\n\t" \
  "v_mfma_f32_16x16x32_bf16 v[28:31], v[116:119], a[96:99], v[28:31]\n\t" \
  "s_mul_i32 s57, s62, 688128\n\t" \
  "s_lshl_b32 s60, s63, 15\n\t" \
  "s_waitcnt lgkmcnt(5)\n\t" \
  "v_mfma_f32_16x16x32_bf16 v[16:19], v[120:123], a[28:31], v[16:19]\n\t" \
  "ds_read_b128 v[144:147], v1 offset:20480\n\t" \
  "s_waitcnt lgkmcnt(5)\n\t" \
  "v_mfma_f32_16x16x32_bf16 v[20:23], v[124:127], a[28:31], v[20:23]\n\t" \
  "s_add_u32 s57, s57, s60\n\t" \
  "s_lshr_b32 s60, s88, s63\n\t" \
  "v_mfma_f32_16x16x32_bf16 v[24:27], v[120:123], a[100:103], v[24:27]\n\t" \
  "ds_read_b128 v[148:151], v1 offset:22528\n\t" \
  "v_mfma_f32_16x16x32_bf16 v[28:31], v[124:127], a[100:103], v[28:31]\n\t" \
  "s_and_b32 s60, s60, 1\n\t" \
  "s_lshl_b32 s60, s60, 13\n\t" \
  "s_waitcnt lgkmcnt(5)\n\t" \
  "v_mfma_f32_16x16x32_bf16 v[16:19], v[128:131], a[32:35], v[16:19]\n\t" \
  "ds_read_b128 v[152:155], v2 offset:20480\n\t" \
  "s_waitcnt lgkmcnt(5)\n\t" \
  "v_mfma_f32_16x16x32_bf16 v[20:23], v[132:135], a[32:35], v[20:23]\n\t" \
  "s_add_u32 s57, s57, s60\n\t" \
  "s_add_u32 s57, s57, 656896\n\t" \
  "v_mfma_f32_16x16x32_bf16 v[24:27], v[128:131], a[104:107], v[24:27]\n\t" \
  "ds_read_b128 v[156:159], v2 offset:22528\n\t" \
  "v_mfma_f32_16x16x32_bf16 v[28:31], v[132:135], a[104:107], v[28:31]\n\t" \
  "s_cmp_lt_i32 s62, 1\n\t" \
  "s_cselect_b32 s57, 0xf0000000, s57\n\t" \
  "s_waitcnt lgkmcnt(5)\n\t" \
  "v_mfma_f32_16x16x32_bf16 v[16:19], v[136:139], a[36:39], v[16:19]\n\t" \
  "ds_read_b128 v[160:163], v1 offset:24576\n\t" \
  "s_waitcnt lgkmcnt(5)\n\t" \
  "v_mfma_f32_16x16x32_bf16 v[20:23], v[140:143], a[36:39], v[20:23]\n\t" \
  "s_cmp_lg_u32 s40, 0\n\t" \
  "s_cselect_b32 s57, 0xf0000000, s57\n\t" \
  "v_mfma_f32_16x16x32_bf16 v[24:27], v[136:139], a[108:111], v[24:27]\n\t" \
  "ds_read_b128 v[164:167], v1 offset:26624\n\t" \
  "v_mfma_f32_16x16x32_bf16 v[28:31], v[140:143], a[108:111], v[28:31]\n\t" \
  "s_waitcnt lgkmcnt(5)\n\t" \
  "v_mfma_f32_16x16x32_bf16 v[16:19], v[144:147], a[40:43], v[16:19]\n\t" \
  "ds_read_b128 v[168:171], v2 offset:24576\n\t" \
  "s_waitcnt lgkmcnt(5)\n\t" \
  "v_mfma_f32_16x16x32_bf16 v[20:23], v[148:151], a[40:43], v[20:23]\n\t" \
  "v_mfma_f32_16x16x32_bf16 v[24:27], v[144:147], a[112:115], v[24:27]\n\t" \
  "ds_read_b128 v[172:175], v2 offset:26624\n\t" \
  "v_mfma_f32_16x16x32_bf16 v[28:31], v[148:151], a[112:115], v[28:31]\n\t" \
  "s_waitcnt lgkmcnt(5)\n\t" \
  "v_mfma_f32_16x16x32_bf16 v[16:19], v[152:155], a[44:47], v[16:19]\n\t" \
  "ds_read_b128 v[176:179], v1 offset:28672\n\t" \
  "s_waitcnt lgkmcnt(5)\n\t" \
  "v_mfma_f32_16x16x32_bf16 v[20:23], v[156:159], a[44:47], v[20:23]\n\t" \
  "v_mfma_f32_16x16x32_bf16 v[24:27], v[152:155], a[116:119], v[24:27]\n\t" \
  "ds_read_b128 v[180:183], v1 offset:30720\n\t" \
  "v_mfma_f32_16x16x32_bf16 v[28:31], v[156:159], a[116:119], v[28:31]\n\t" \
  "s_waitcnt lgkmcnt(5)\n\t" \
  "v_mfma_f32_16x16x32_bf16 v[16:19], v[160:163], a[48:51], v[16:19]\n\t" \
  "ds_read_b128 v[184:187], v2 offset:28672\n\t" \
  "s_waitcnt lgkmcnt(5)\n\t" \
  "v_mfma_f32_16x16x32_bf16 v[20:23], v[164:167], a[48:51], v[20:23]\n\t" \
  "v_mfma_f32_16x16x32_bf16 v[24:27], v[160:163], a[120:123], v[24:27]\n\t" \
  "ds_read_b128 v[188:191], v2 offset:30720\n\t" \
  "v_mfma_f32_16x16x32_bf16 v[28:31], v[164:167], a[120:123], v[28:31]\n\t" \
  "s_waitcnt lgkmcnt(5)\n\t" \
  "v_mfma_f32_16x16x32_bf16 v[16:19], v[168:171], a[52:55], v[16:19]\n\t" \
  "ds_read_b128 v[192:195], v1 offset:32768\n\t" \
  "s_waitcnt lgkmcnt(5)\n\t" \
  "v_mfma_f32_16x16x32_bf16 v[20:23], v[172:175], a[52:55], v[20:23]\n\t" \
  "v_mfma_f32_16x16x32_bf16 v[24:27], v[168:171], a[124:127], v[24:27]\n\t" \
  "ds_read_b128 v[196:199], v1 offset:34816\n\t" \
  "v_mfma_f32_16x16x32_bf16 v[28:31], v[172:175], a[124:127], v[28:31]\n\t" \
  "s_waitcnt lgkmcnt(5)\n\t" \
  "v_mfma_f32_16x16x32_bf16 v[16:19], v[176:179], a[56:59], v[16:19]\n\t" \
  "ds_read_b128 v[200:203], v2 offset:32768\n\t" \
  "s_waitcnt lgkmcnt(5)\n\t" \
  "v_mfma_f32_16x16x32_bf16 v[20:23], v[180:183], a[56:59], v[20:23]\n\t" \
  "v_mfma_f32_16x16x32_bf16 v[24:27], v[176:179], a[128:131], v[24:27]\n\t" \
  "ds_read_b128 v[204:207], v2 offset:34816\n\t" \
  "v_mfma_f32_16x16x32_bf16 v[28:31], v[180:183], a[128:131], v[28:31]\n\t" \
  "s_waitcnt lgkmcnt(5)\n\t" \
  "v_mfma_f32_16x16x32_bf16 v[16:19], v[184:187], a[60:63], v[16:19]\n\t" \
  "s_waitcnt lgkmcnt(4)\n\t" \
  "v_mfma_f32_16x16x32_bf16 v[20:23], v[188:191], a[60:63], v[20:23]\n\t" \
  "buffer_load_dword v4, v7, %[X], 0 offen sc1\n\t" \
  "v_mfma_f32_16x16x32_bf16 v[24:27], v[184:187], a[132:135], v[24:27]\n\t" \
  "v_mfma_f32_16x16x32_bf16 v[28:31], v[188:191], a[132:135], v[28:31]\n\t" \
  "s_waitcnt lgkmcnt(3)\n\t" \
  "v_mfma_f32_16x16x32_bf16 v[16:19], v[192:195], a[64:67], v[16:19]\n\t" \
  "v_xor_b32 v1, 0x10000, v1\n\t" \
  "v_xor_b32 v2, 0x10000, v2\n\t" \
  "s_waitcnt lgkmcnt(2)\n\t" \
  "v_mfma_f32_16x16x32_bf16 v[20:23], v[196:199], a[64:67], v[20:23]\n\t" \
  "v_mfma_f32_16x16x32_bf16 v[24:27], v[192:195], a[136:139], v[24:27]\n\t" \
  "v_mfma_f32_16x16x32_bf16 v[28:31], v[196:199], a[136:139], v[28:31]\n\t" \
  "s_waitcnt lgkmcnt(1)\n\t" \
  "v_mfma_f32_16x16x32_bf16 v[16:19], v[200:203], a[68:71], v[16:19]\n\t" \
  "s_waitcnt lgkmcnt(0)\n\t" \
  "v_mfma_f32_16x16x32_bf16 v[20:23], v[204:207], a[68:71], v[20:23]\n\t" \
  "v_mfma_f32_16x16x32_bf16 v[24:27], v[200:203], a[140:143], v[24:27]\n\t" \
  "v_mfma_f32_16x16x32_bf16 v[28:31], v[204:207], a[140:143], v[28:31]\n\t" \
  "v_mfma_f32_16x16x32_bf16 v[32:35], v[64:67], a[144:147], 0\n\t" \
  "v_cvt_pk_bf16_f32 v48, v16, v17\n\t" \
  "v_cvt_pk_bf16_f32 v49, v18, v19\n\t" \
  "v_mfma_f32_16x16x32_bf16 v[36:39], v[68:71], a[144:147], 0\n\t" \
  "v_cvt_pk_bf16_f32 v50, v20, v21\n\t" \
  "v_cvt_pk_bf16_f32 v51, v22, v23\n\t" \
  "v_mfma_f32_16x16x32_bf16 v[32:35], v[72:75], a[148:151], v[32:35]\n\t" \
  "v_cvt_pk_bf16_f32 v52, v24, v25\n\t" \
  "v_cvt_pk_bf16_f32 v53, v26, v27\n\t" \
  "v_mfma_f32_16x16x32_bf16 v[36:39], v[76:79], a[148:151], v[36:39]\n\t" \
  "v_cvt_pk_bf16_f32 v54, v28, v29\n\t" \
  "v_cvt_pk_bf16_f32 v55, v30, v31\n\t" \
  "v_mfma_f32_16x16x32_bf16 v[32:35], v[80:83], a[152:155], v[32:35]\n\t" \
  "buffer_store_dwordx4 v[48:51], v0, %[X], s50 offen offset:0 sc1\n\t" \
  "v_mfma_f32_16x16x32_bf16 v[36:39], v[84:87], a[152:155], v[36:39]\n\t" \
  "v_mfma_f32_16x16x32_bf16 v[32:35], v[88:91], a[156:159], v[32:35]\n\t" \
  "v_mfma_f32_16x16x32_bf16 v[36:39], v[92:95], a[156:159], v[36:39]\n\t" \
  "v_mfma_f32_16x16x32_bf16 v[32:35], v[96:99], a[160:163], v[32:35]\n\t" \
  "v_mfma_f32_16x16x32_bf16 v[36:39], v[100:103], a[160:163], v[36:39]\n\t" \
  "v_mfma_f32_16x16x32_bf16 v[32:35], v[104:107], a[164:167], v[32:35]\n\t" \
  "v_mfma_f32_16x16x32_bf16 v[36:39], v[108:111], a[164:167], v[36:39]\n\t" \
  "v_mfma_f32_16x16x32_bf16 v[32:35], v[112:115], a[168:171], v[32:35]\n\t" \
  "v_mfma_f32_16x16x32_bf16 v[36:39], v[116:119], a[168:171], v[36:39]\n\t" \
  "v_mfma_f32_16x16x32_bf16 v[32:35], v[120:123], a[172:175], v[32:35]\n\t" \
  "v_mfma_f32_16x16x32_bf16 v[36:39], v[124:127], a[172:175], v[36:39]\n\t" \
  "s_waitcnt vmcnt(1)\n\t" \
  "v_cmp_eq_u32 vcc, 0, v4\n\t" \
  "v_mfma_f32_16x16x32_bf16 v[32:35], v[128:131], a[176:179], v[32:35]\n\t" \
  "v_mfma_f32_16x16x32_bf16 v[36:39], v[132:135], a[176:179], v[36:39]\n\t" \
  "s_cmp_eq_u64 vcc, 0\n\t" \
  "s_cselect_b32 s60, 1, 0\n\t" \
  "v_mov_b32 v4, s60\n\t" \
  "v_mfma_f32_16x16x32_bf16 v[32:35], v[136:139], a[180:183], v[32:35]\n\t" \
  "ds_write_b32 v5, v4\n\t" \
  "v_mfma_f32_16x16x32_bf16 v[36:39], v[140:143], a[180:183], v[36:39]\n\t" \
  "v_mfma_f32_16x16x32_bf16 v[32:35], v[144:147], a[184:187], v[32:35]\n\t" \
  "v_mfma_f32_16x16x32_bf16 v[36:39], v[148:151], a[184:187], v[36:39]\n\t" \
  "buffer_store_dwordx4 v[52:55], v0, %[X], s50 offen offset:1024 sc1\n\t" \
  "v_mfma_f32_16x16x32_bf16 v[32:35], v[152:155], a[188:191], v[32:35]\n\t" \
  "v_mfma_f32_16x16x32_bf16 v[36:39], v[156:159], a[188:191], v[36:39]\n\t" \
  "s_waitcnt lgkmcnt(0)\n\t" \
  "s_barrier\n\t" \
  "v_mfma_f32_16x16x32_bf16 v[32:35], v[160:163], a[192:195], v[32:35]\n\t" \
  "ds_read_b32 v4, v6 offset:40959\n\t" \
  "v_mfma_f32_16x16x32_bf16 v[36:39], v[164:167], a[192:195], v[36:39]\n\t" \
  "v_mfma_f32_16x16x32_bf16 v[32:35], v[168:171], a[196:199], v[32:35]\n\t" \
  "v_mfma_f32_16x16x32_bf16 v[36:39], v[172:175], a[196:199], v[36:39]\n\t" \
  "v_mfma_f32_16x16x32_bf16 v[32:35], v[176:179], a[200:203], v[32:35]\n\t" \
  "v_mfma_f32_16x16x32_bf16 v[36:39], v[180:183], a[200:203], v[36:39]\n\t" \
  "s_waitcnt lgkmcnt(0)\n\t" \
  "v_readfirstlane_b32 s60, v4\n\t" \
  "s_add_u32 s55, s83, s36\n\t" \
  "v_mfma_f32_16x16x32_bf16 v[32:35], v[184:187], a[204:207], v[32:35]\n\t" \
  "s_cmp_eq_u32 s48, 63\n\t" \
  "s_cselect_b32 s56, 0, s60\n\t" \
  "v_mfma_f32_16x16x32_bf16 v[36:39], v[188:191], a[204:207], v[36:39]\n\t" \
  "s_cmp_eq_u32 s56, 0\n\t" \
  "s_cselect_b32 s55, 0xf0000000, s55\n\t" \
  "v_mfma_f32_16x16x32_bf16 v[32:35], v[192:195], a[208:211], v[32:35]\n\t" \
  "v_mfma_f32_16x16x32_bf16 v[36:39], v[196:199], a[208:211], v[36:39]\n\t" \
  "v_mfma_f32_16x16x32_bf16 v[32:35], v[200:203], a[212:215], v[32:35]\n\t" \
  "s_add_u32 m0, s46, 0\n\t" \
  "s_add_u32 s47, s55, 0\n\t" \
  "buffer_load_dwordx4 v0, %[X], s47 offen sc1 lds\n\t" \
  "v_mfma_f32_16x16x32_bf16 v[36:39], v[204:207], a[212:215], v[36:39]\n\t" \
  "v_mfma_f32_16x16x32_bf16 v[40:43], v[64:67], a[216:219], 0\n\t" \
  "v_mfma_f32_16x16x32_bf16 v[44:47], v[68:71], a[216:219], 0\n\t" \
  "v_mfma_f32_16x16x32_bf16 v[40:43], v[72:75], a[220:223], v[40:43]\n\t" \
  "v_cvt_pk_bf16_f32 v56, v32, v33\n\t" \
  "v_cvt_pk_bf16_f32 v57, v34, v35\n\t" \
  "s_add_u32 m0, s46, 4096\n\t" \
  "s_add_u32 s47, s55, 4096\n\t" \
  "buffer_load_dwordx4 v0, %[X], s47 offen sc1 lds\n\t" \
  "v_mfma_f32_16x16x32_bf16 v[44:47], v[76:79], a[220:223], v[44:47]\n\t" \
  "v_cvt_pk_bf16_f32 v58, v36, v37\n\t" \
  "v_cvt_pk_bf16_f32 v59, v38, v39\n\t" \
  "v_mfma_f32_16x16x32_bf16 v[40:43], v[80:83], a[224:227], v[40:43]\n\t" \
  "v_mfma_f32_16x16x32_bf16 v[44:47], v[84:87], a[224:227], v[44:47]\n\t" \
  "buffer_store_dwordx4 v[56:59], v0, %[X], s50 offen offset:2048 sc1\n\t" \
  "v_mfma_f32_16x16x32_bf16 v[40:43], v[88:91], a[228:231], v[40:43]\n\t" \
  "s_add_u32 m0, s46, 8192\n\t" \
  "s_add_u32 s47, s55, 16384\n\t" \
  "buffer_load_dwordx4 v0, %[X], s47 offen sc1 lds\n\t" \
  "v_mfma_f32_16x16x32_bf16 v[44:47], v[92:95], a[228:231], v[44:47]\n\t" \
  "v_mfma_f32_16x16x32_bf16 v[40:43], v[96:99], a[232:235], v[40:43]\n\t" \
  "v_mfma_f32_16x16x32_bf16 v[44:47], v[100:103], a[232:235], v[44:47]\n\t" \
  "v_mfma_f32_16x16x32_bf16 v[40:43], v[104:107], a[236:239], v[40:43]\n\t" \
  "s_add_u32 m0, s46, 12288\n\t" \
  "s_add_u32 s47, s55, 20480\n\t" \
  "buffer_load_dwordx4 v0, %[X], s47 offen sc1 lds\n\t" \
  "v_mfma_f32_16x16x32_bf16 v[44:47], v[108:111], a[236:239], v[44:47]\n\t" \
  "v_mfma_f32_16x16x32_bf16 v[40:43], v[112:115], a[240:243], v[40:43]\n\t" \
  "v_mfma_f32_16x16x32_bf16 v[44:47], v[116:119], a[240:243], v[44:47]\n\t" \
  "v_mfma_f32_16x16x32_bf16 v[40:43], v[120:123], a[244:247], v[40:43]\n\t" \
  "s_add_u32 m0, s46, 16384\n\t" \
  "s_add_u32 s47, s55, 32768\n\t" \
  "buffer_load_dwordx4 v0, %[X], s47 offen sc1 lds\n\t" \
  "v_mfma_f32_16x16x32_bf16 v[44:47], v[124:127], a[244:247], v[44:47]\n\t" \
  "v_mfma_f32_16x16x32_bf16 v[40:43], v[128:131], a[248:251], v[40:43]\n\t" \
  "v_mfma_f32_16x16x32_bf16 v[44:47], v[132:135], a[248:251], v[44:47]\n\t" \
  "v_mfma_f32_16x16x32_bf16 v[40:43], v[136:139], a[252:255], v[40:43]\n\t" \
  "s_add_u32 m0, s46, 20480\n\t" \
  "s_add_u32 s47, s55, 36864\n\t" \
  "buffer_load_dwordx4 v0, %[X], s47 offen sc1 lds\n\t" \
  "v_mfma_f32_16x16x32_bf16 v[44:47], v[140:143], a[252:255], v[44:47]\n\t" \
  "v_mfma_f32_16x16x32_bf16 v[40:43], v[144:147], v[224:227], v[40:43]\n\t" \
  "v_mfma_f32_16x16x32_bf16 v[44:47], v[148:151], v[224:227], v[44:47]\n\t" \
  "v_mfma_f32_16x16x32_bf16 v[40:43], v[152:155], v[228:231], v[40:43]\n\t" \
  "s_add_u32 m0, s46, 24576\n\t" \
  "s_add_u32 s47, s55, 49152\n\t" \
  "buffer_load_dwordx4 v0, %[X], s47 offen sc1 lds\n\t" \
  "v_mfma_f32_16x16x32_bf16 v[44:47], v[156:159], v[228:231], v[44:47]\n\t" \
  "v_mfma_f32_16x16x32_bf16 v[40:43], v[160:163], v[232:235], v[40:43]\n\t" \
  "v_mfma_f32_16x16x32_bf16 v[44:47], v[164:167], v[232:235], v[44:47]\n\t" \
  "v_mfma_f32_16x16x32_bf16 v[40:43], v[168:171], v[236:239], v[40:43]\n\t" \
  "s_add_u32 m0, s46, 28672\n\t" \
  "s_add_u32 s47, s55, 53248\n\t" \
  "buffer_load_dwordx4 v0, %[X], s47 offen sc1 lds\n\t" \
  "v_mfma_f32_16x16x32_bf16 v[44:47], v[172:175], v[236:239], v[44:47]\n\t" \
  "v_mfma_f32_16x16x32_bf16 v[40:43], v[176:179], v[240:243], v[40:43]\n\t" \
  "v_mfma_f32_16x16x32_bf16 v[44:47], v[180:183], v[240:243], v[44:47]\n\t" \
  "v_mfma_f32_16x16x32_bf16 v[40:43], v[184:187], v[244:247], v[40:43]\n\t" \
  "s_add_u32 m0, s46, 32768\n\t" \
  "s_add_u32 s47, s55, 65536\n\t" \
  "buffer_load_dwordx4 v0, %[X], s47 offen sc1 lds\n\t" \
  "v_mfma_f32_16x16x32_bf16 v[44:47], v[188:191], v[244:247], v[44:47]\n\t" \
  "v_mfma_f32_16x16x32_bf16 v[40:43], v[192:195], v[248:251], v[40:43]\n\t" \
  "v_mfma_f32_16x16x32_bf16 v[44:47], v[196:199], v[248:251], v[44:47]\n\t" \
  "buffer_load_dword v8, off, %[X], s57 sc1\n\t" \
  "v_mfma_f32_16x16x32_bf16 v[40:43], v[200:203], v[252:255], v[40:43]\n\t" \
  "v_mfma_f32_16x16x32_bf16 v[44:47], v[204:207], v[252:255], v[44:47]\n\t" \
  "s_nop 4\n\t" \
  "v_cvt_pk_bf16_f32 v60, v40, v41\n\t" \
  "v_cvt_pk_bf16_f32 v61, v42, v43\n\t" \
  "s_nop 1\n\t" \
  "v_cvt_pk_bf16_f32 v62, v44, v45\n\t" \
  "v_cvt_pk_bf16_f32 v63, v46, v47\n\t" \
  "buffer_store_dwordx4 v[60:63], v0, %[X], s50 offen offset:3072 sc1\n\t" \
  "s_add_u32 s49, s82, s38\n\t" \
  "s_cmp_eq_u32 s48, 63\n\t" \
  "s_cbranch_scc1 LEND_%=\n\t" \
  "s_add_u32 s48, s48, 1\n\t" \
  "s_cmp_eq_u32 s56, 0\n\t" \
  "s_cbranch_scc1 LCOLD_%=\n\t" \
  "s_waitcnt vmcnt(0)\n\t" \
  "s_mov_b64 exec, 1\n\t" \
  "buffer_store_dword v6, off, %[X], s49 sc1\n\t" \
  "s_mov_b64 exec, -1\n\t" \
  "s_mov_b32 s49, 0xf0000000\n\t" \
  "s_cmp_lt_i32 s57, 0\n\t" \
  "s_cbranch_scc1 LTOP_%=\n\t" \
  "s_movk_i32 s63, 4000\n\t" \
  "s_branch LGW_%=u\n\t" \
  "LGW_%=:\n\t" \
  "s_sleep " TK_PSG "\n\t" \
  "buffer_load_dword v8, off, %[X], s57 sc1\n\t" \
  "s_waitcnt vmcnt(0)\n\t" \
  "LGW_%=u:\n\t" \
  "s_memrealtime s[52:53]\n\t" \
  "v_readfirstlane_b32 s60, v8\n\t" \
  "s_waitcnt lgkmcnt(0)\n\t" \
  "s_sub_u32 s63, s63, 1\n\t" \
  "s_cbranch_scc1 LTOP_%=\n\t" \
  "s_cmp_eq_u32 s60, -1\n\t" \
  "s_cbranch_scc1 LGW_%=\n\t" \
  "s_not_b32 s60, s60\n\t" \
  "s_lshl_b32 s61, s52, 8\n\t" \
  "s_sub_u32 s60, s60, s61\n\t" \
  "s_ashr_i32 s60, s60, 8\n\t" \
  "s_sub_i32 s60, s60, " TK_QXW "\n\t" \
  "s_min_i32 s60, s60, 400\n\t" \
  "s_add_u32 s54, s52, s60\n\t" \
  "LGW_%=w:\n\t" \
  "s_sleep 1\n\t" \
  "s_memrealtime s[52:53]\n\t" \
  "s_waitcnt lgkmcnt(0)\n\t" \
  "s_sub_i32 s53, s52, s54\n\t" \
  "s_cmp_lt_i32 s53, 0\n\t" \
  "s_cbranch_scc1 LGW_%=w\n\t" \
  "s_branch LTOP_%=\n\t" \
  "LEND_%=:\n\t" \
  "s_waitcnt vmcnt(0)\n\t" \
  "s_mov_b64 exec, 1\n\t" \
  "buffer_store_dword v6, off, %[X], s49 sc1\n\t" \
  "s_mov_b64 exec, -1\n\t" \
  "s_mov_b32 s49, 0xf0000000\n\t" \
  ""
__device__ __forceinline__ unsigned hoff(int r, int piece) {
  const int m = 4 * (r >> 3) + (r & 3), kp = piece >> 3;
  return (P_H + (unsigned)(kp >> 1)) * PAIR + (unsigned)((kp & 1) * 4096 + (16 * ((r >> 2) & 1) + m) * 128 + (((piece ^ m) & 7) << 4));
}
template <int SLEEP>
__device__ __forceinline__ void wait_all(rsrc_t X, unsigned vo, unsigned so) {
  unsigned spins = 0;
  while (true) {
    asm volatile("" ::: "memory");
    if (__all(ld4(X, vo, so) != 0u)) break;
    if (spin_fail(spins, X)) break;
    __builtin_amdgcn_s_sleep(SLEEP);
  }
  asm volatile("" ::: "memory");
}
#define VQ_CLOB "v8", "v16", "v17", "v18", "v19", "v20", "v21", "v22", "v23", "v24", "v25", "v26", "v27", "v28", "v29", "v30", "v31", "v32", "v33", "v34", "v35", "v36", "v37", "v38", "v39", "v40", "v41", "v42", "v43", "v44", "v45", "v46", "v47", "v48", "v49", "v50", "v51", "v52", "v53", "v54", "v55", "v56", "v57", "v58", "v59", "v60", "v61", "v62", "v63", "v64", "v65", "v66", "v67", "v68", "v69", "v70", "v71", "v72", "v73", "v74", "v75", "v76", "v77", "v78", "v79", "v80", "v81", "v82", "v83", "v84", "v85", "v86", "v87", "v88", "v89", "v90", "v91", "v92", "v93", "v94", "v95", "v96", "v97", "v98", "v99", "v100", "v101", "v102", "v103", "v104", "v105", "v106", "v107", "v108", "v109", "v110", "v111", "v112", "v113", "v114", "v115", "v116", "v117", "v118", "v119", "v120", "v121", "v122", "v123", "v124", "v125", "v126", "v127", "v128", "v129", "v130", "v131", "v132", "v133", "v134", "v135", "v136", "v137", "v138", "v139", "v140", "v141", "v142", "v143", "v144", "v145", "v146", "v147", "v148", "v149", "v150", "v151", "v152", "v153", "v154", "v155", "v156", "v157", "v158", "v159", "v160", "v161", "v162", "v163", "v164", "v165", "v166", "v167", "v168", "v169", "v170", "v171", "v172", "v173", "v174", "v175", "v176", "v177", "v178", "v179", "v180", "v181", "v182", "v183", "v184", "v185", "v186", "v187", "v188", "v189", "v190", "v191", "v192", "v193", "v194", "v195", "v196", "v197", "v198", "v199", "v200", "v201", "v202", "v203", "v204", "v205", "v206", "v207", "v220", "v221"
#define VA_CLOB "v40", "v41", "v42", "v43", "v44", "v45", "v46", "v47", "v48", "v49", "v50", "v51", "v52", "v53", "v54", "v55", "v56", "v57", "v58", "v59", "v60", "v61", "v62", "v63", "v64", "v65", "v66", "v67", "v68", "v69", "v70", "v71", "v72", "v73", "v74", "v75", "v76", "v77", "v78", "v79", "v80", "v81", "v82", "v83", "v84", "v85", "v86", "v87", "v88", "v89", "v90", "v91", "v92", "v93", "v94", "v95", "v96", "v97", "v98", "v99", "v100", "v101", "v102", "v103", "v104", "v105", "v106", "v107", "v108", "v109", "v110", "v111", "v112", "v113", "v114", "v115", "v116", "v117", "v118", "v119", "v120", "v121", "v122", "v123", "v124", "v125", "v126", "v127", "v128", "v129", "v130", "v131", "v132", "v133", "v134", "v135", "v136", "v137", "v138", "v139", "v140", "v141", "v142", "v143", "v144", "v145", "v146", "v147", "v148", "v149", "v150", "v151", "v152", "v153", "v154", "v155", "v156", "v157", "v158", "v159", "v160", "v161", "v162", "v163", "v164", "v165", "v166", "v167", "v168", "v169", "v170", "v171", "v172", "v173", "v174", "v175", "v176", "v177", "v178", "v179", "v180", "v181", "v182", "v183", "v184", "v185", "v186", "v187", "v188", "v189", "v190", "v191", "v192", "v193", "v194", "v195", "v196", "v197", "v198", "v199", "v200", "v201", "v202", "v203", "v204", "v205", "v206", "v207", "v208", "v209", "v210", "v211", "v220", "v221"
#define TOK_COMMON() \
  const MP p = getp(); \
  const int c = (int)blockIdx.x - NCHW, xq = c & 3, L = (c >> 2) & 1, i = c >> 3; \
  const int tid = threadIdx.x, lane = tid & 63, w = __builtin_amdgcn_readfirstlane(tid >> 6); \
  const rsrc_t X = mkr(p.xb, XB_BYTES)
#define W_IN [W] "s"(WR)
#define W_CLOB "a0", "a1", "a2", "a3", "a4", "a5", "a6", "a7", "a8", "a9", "a10", "a11", "a12", "a13", "a14", "a15", "a16", "a17", "a18", "a19", "a20", "a21", "a22", "a23", "a24", "a25", "a26", "a27", "a28", "a29", "a30", "a31", "a32", "a33", "a34", "a35", "a36", "a37", "a38", "a39", "a40", "a41", "a42", "a43", "a44", "a45", "a46", "a47", "a48", "a49", "a50", "a51", "a52", "a53", "a54", "a55", "a56", "a57", "a58", "a59", "a60", "a61", "a62", "a63", "a64", "a65", "a66", "a67", "a68", "a69", "a70", "a71", "a72", "a73", "a74", "a75", "a76", "a77", "a78", "a79", "a80", "a81", "a82", "a83", "a84", "a85", "a86", "a87", "a88", "a89", "a90", "a91", "a92", "a93", "a94", "a95", "a96", "a97", "a98", "a99", "a100", "a101", "a102", "a103", "a104", "a105", "a106", "a107", "a108", "a109", "a110", "a111", "a112", "a113", "a114", "a115", "a116", "a117", "a118", "a119", "a120", "a121", "a122", "a123", "a124", "a125", "a126", "a127", "a128", "a129", "a130", "a131", "a132", "a133", "a134", "a135", "a136", "a137", "a138", "a139", "a140", "a141", "a142", "a143", "a144", "a145", "a146", "a147", "a148", "a149", "a150", "a151", "a152", "a153", "a154", "a155", "a156", "a157", "a158", "a159", "a160", "a161", "a162", "a163", "a164", "a165", "a166", "a167", "a168", "a169", "a170", "a171", "a172", "a173", "a174", "a175", "a176", "a177", "a178", "a179", "a180", "a181", "a182", "a183", "a184", "a185", "a186", "a187", "a188", "a189", "a190", "a191", "a192", "a193", "a194", "a195", "a196", "a197", "a198", "a199", "a200", "a201", "a202", "a203", "a204", "a205", "a206", "a207", "a208", "a209", "a210", "a211", "a212", "a213", "a214", "a215", "a216", "a217", "a218", "a219", "a220", "a221", "a222", "a223", "a224", "a225", "a226", "a227", "a228", "a229", "a230", "a231", "a232", "a233", "a234", "a235", "a236", "a237", "a238", "a239", "a240", "a241", "a242", "a243", "a244", "a245", "a246", "a247", "a248", "a249", "a250", "a251", "a252", "a253", "a254", "a255", "v224", "v225", "v226", "v227", "v228", "v229", "v230", "v231", "v232", "v233", "v234", "v235", "v236", "v237", "v238", "v239", "v240", "v241", "v242", "v243", "v244", "v245", "v246", "v247", "v248", "v249", "v250", "v251", "v252", "v253", "v254", "v255"
#define BOOT_IN "{s67}"(L), [ctab] "i"(CTAB_OFF)
#define S_CLOB "s76", "s78", "s79", "s86", "s88", "s89", "s90", "s91", "s46", "s47", "s48", "s49", "s50", "s51", "s52", "s53", "s54", "s55", "s56", "s57", "s58", "s59", "s60", "s61", "s62", "s63", "s68", "s69", "s70", "s71", "s72", "s73", "s82", "s83", "s84", "m0", "vcc", "scc", "memory"
typedef unsigned v8w __attribute__((ext_vector_type(8)));

__device__ __forceinline__ void qkv_role() {
  TOK_COMMON();
  const int role = i * 4 + w, hh = 8 * xq + role / 6, part = role % 6, which = part >> 1, half = part & 1;
  const rsrc_t WR = mkr((which == 0 ? p.Wq : (which == 1 ? p.Wk : p.Wv)) + hh * DH + 64 * half, DM * HD * 2u - (unsigned)(hh * DH + 64 * half) * 2u);
  const int m = lane & 15, g = lane >> 4;
  const unsigned a0 = (unsigned)(m * 128 + ((g ^ (m & 7)) << 4));
  v8w lv = {(unsigned)lane * 16u, a0, a0 ^ 64u, (unsigned)(lane & (L ? 15 : 3)) * 128u, 0u, 40960u + 4u * (unsigned)w, 1u, 0u};
  const unsigned c36 = P_H * PAIR + (unsigned)w * 1024u, c37 = (unsigned)(3 * hh + which) * PAIR + (unsigned)half * (which == 2 ? 4096u : 2048u);
  const unsigned c38 = P_FA * PAIR + (unsigned)(hh * 128 + part * 16), c39 = w == 0 ? P_FB * PAIR + 4096u : 0xF0000000u, c41 = (unsigned)w * 1024u;
  const int c42 = L ? 1 - QLEAD1 : 1 - QLEAD;
  unsigned d74 = TRC_OFF + 262144u + (unsigned)((c * 4 + w) * 66) * 8u;
#define Q_CALL(A) asm volatile("s_waitcnt vmcnt(0) lgkmcnt(0)\n\t" A : "+{v[0:7]}"(lv), "+{s74}"(d74) \
               : [X] "s"(X), "{s36}"(c36), "{s37}"(c37), "{s38}"(c38), "{s39}"(c39), "{s40}"(w), "{s41}"(c41), "{s42}"(c42), "{s45}"(i), "{s92}"(TMO_OFF), BOOT_IN, [cap] "i"(SPIN_CAP), [qxw] "i"(QXW), [qxc] "i"(QXC), W_IN : VQ_CLOB, W_CLOB, S_CLOB)
  if (which == 2) { Q_CALL(QV_ASM "LQVEND:"); __builtin_amdgcn_endpgm(); }
  Q_CALL(QK_ASM "LQKEND:");
  __builtin_amdgcn_endpgm();
}

__device__ __forceinline__ void ap_role() {
  TOK_COMMON();
  const int hp = 4 * xq + ((i - 12) >> 1), ch = i & 1, a = w >> 1, rt = w & 1, h = 2 * hp + a;
  const int n = lane & 15, g = lane >> 4;
  const rsrc_t WR = mkr(p.Wo + (long)hp * 2 * DH * DM, 2u * DH * DM * 2u);
  const unsigned l16 = (unsigned)lane * 16u, ab = (unsigned)a * 24576u + l16;
  v16u lv = {l16, ab, ab + (unsigned)rt * 4096u, 49152u + (unsigned)w * 4096u + l16, 49152u + (unsigned)(w ^ 2) * 4096u + l16, l16 & ~32u, l16 >> 1,
             (unsigned)(g * 262144 + n * 16), (unsigned)(n * 32 + g * 8), (unsigned)((lane & 7) % 6) * 16u, 0u, 0u, 0u, 0u, 0u, 0u};
  v8w mk;
  const int qi = 8 * (n >> 2) + (n & 3) + 4 * rt;
#pragma unroll
  for (int k = 0; k < 8; ++k) mk[k] = 8 * g + k > qi ? 0xFF800000u : 0u;
  v16u zz = {0u, 0u, 0u, 0u, 0u, 0u, 0u, 0u, 0u, 0u, 0u, 0u, 0u, 0u, 0u, 0u};
  const unsigned tb = (unsigned)(3 * h) * PAIR;
  const unsigned c36 = tb + (rt ? PAIR + 4096u : 0u), c37 = tb + (rt ? 2u * PAIR : 4096u), c38 = tb + (rt ? 2u * PAIR + 4096u : PAIR);
  const unsigned c39 = (P_KV + (unsigned)(h >> 1)) * PAIR + (unsigned)(h & 1) * 4096u, c41 = P_FA * PAIR + (unsigned)h * 128u;
  const unsigned c42 = w == 0 ? P_FB * PAIR + (unsigned)(2 * hp + ch) * 128u : 0xF0000000u;
  const unsigned c43 = TP0 * XBLK + (unsigned)(8 * rt + (hp >> 3)) * PAIR + (unsigned)((hp & 7) * 1024 + ch * 512 + a * 256);
  unsigned d44 = (TP0 + 8u) * XBLK + (unsigned)(hp >> 1) * PAIR + (unsigned)((((hp & 1) * 4 + 2 * ch + a) * 2 + rt) * 512);
  const unsigned c45 = (unsigned)(a * 24576 + rt * 12288);
  unsigned d74 = TRC_OFF + 262144u + (unsigned)((c * 4 + w) * 66) * 8u;
  asm volatile("s_waitcnt vmcnt(0) lgkmcnt(0)\n\t" AP_ASM "LAPEND:" : "+{v[0:15]}"(lv), "+{v[24:39]}"(zz), "+{s74}"(d74), "+{s44}"(d44)
               : [X] "s"(X), "{s36}"(c36), "{s37}"(c37), "{s38}"(c38), "{s39}"(c39), "{s41}"(c41), "{s42}"(c42), "{s43}"(c43), "{s45}"(c45), "{s64}"(512 * ch + 256 * a), "{s65}"(1024 + 64 * ch + 32 * a), "{s66}"(147456 * a), "{s77}"(L ? AX1 : AX), BOOT_IN,
                 "{s92}"(TMO_OFF), [cap] "i"(SPIN_CAP), "{v[16:23]}"(mk), W_IN : VA_CLOB, W_CLOB, S_CLOB);
  __builtin_amdgcn_endpgm();
}

template <int N> __device__ __forceinline__ float rorN(float v) { return __uint_as_float((unsigned)__builtin_amdgcn_update_dpp(0, (int)__float_as_uint(v), 0x120 + N, 0xf, 0xf, false)); }
__device__ __forceinline__ void up8(v4u v, float* x) {
  x[0] = lo2f(v.x); x[1] = hi2f(v.x); x[2] = lo2f(v.y); x[3] = hi2f(v.y); x[4] = lo2f(v.z); x[5] = hi2f(v.z); x[6] = lo2f(v.w); x[7] = hi2f(v.w);
}
__device__ __forceinline__ v4u pk8(const float* x) { return (v4u){pk2(x[0], x[1]), pk2(x[2], x[3]), pk2(x[4], x[5]), pk2(x[6], x[7])}; }

__device__ __forceinline__ void rb9(float (&x)[9]) {
#pragma unroll
  for (int i = 0; i < 8; i += 2) { const unsigned u = pk2(x[i], x[i + 1]); x[i] = lo2f(u); x[i + 1] = hi2f(u); }
  x[8] = lo2f(pk2(x[8], x[8]));
}
template <int R>
__device__ __forceinline__ void lnR(const float (&x)[R][9], float (&o)[R][9]) {
  float s[2 * R], pv[R];
#pragma unroll
  for (int r = 0; r < R; ++r) {
    pv[r] = __uint_as_float(__builtin_amdgcn_readfirstlane(__float_as_uint(x[r][0])));
    const f32x2 p2 = {pv[r], pv[r]};
    f32x2 d[4];
#pragma unroll
    for (int i = 0; i < 4; ++i) d[i] = (f32x2){x[r][2 * i], x[r][2 * i + 1]} - p2;
    const float d8 = x[r][8] - pv[r];
    const f32x2 a = (d[0] + d[1]) + (d[2] + d[3]);
    const f32x2 q = __builtin_elementwise_fma(d[3], d[3], __builtin_elementwise_fma(d[2], d[2], __builtin_elementwise_fma(d[1], d[1], d[0] * d[0])));
    s[2 * r] = (a[0] + d8) + a[1];
    s[2 * r + 1] = __builtin_fmaf(d8, d8, q[0]) + q[1];
  }
#pragma unroll
  for (int k = 0; k < 2 * R; ++k) s[k] += rorN<8>(s[k]);
#pragma unroll
  for (int k = 0; k < 2 * R; ++k) s[k] += rorN<4>(s[k]);
#pragma unroll
  for (int k = 0; k < 2 * R; ++k) s[k] += rorN<2>(s[k]);
#pragma unroll
  for (int k = 0; k < 2 * R; ++k) s[k] += rorN<1>(s[k]);
#pragma unroll
  for (int r = 0; r < R; ++r) {
    auto u = __builtin_amdgcn_permlane16_swap(__float_as_uint(s[2 * r]), __float_as_uint(s[2 * r + 1]), false, false);
    s[2 * r] = __uint_as_float(u[0]) + __uint_as_float(u[1]);
  }
#pragma unroll
  for (int r = 0; r < R; ++r) {
    const unsigned u = __float_as_uint(s[2 * r]);
    const float md = (__uint_as_float(__builtin_amdgcn_readlane(u, 0)) + __uint_as_float(__builtin_amdgcn_readlane(u, 32))) * (1.0f / DM);
    const float sq = (__uint_as_float(__builtin_amdgcn_readlane(u, 16)) + __uint_as_float(__builtin_amdgcn_readlane(u, 48))) * (1.0f / DM);
    const unsigned mv = pk2(pv[r] + md, __builtin_fmaxf(__builtin_fmaf(-md, md, sq), 0.f));
    const float m = lo2f(mv), var = hi2f(mv);
    const float ve = lo2f(pk2(var + 1e-5f, 0.f));
    const float den = lo2f(pk2(__builtin_amdgcn_sqrtf(ve), 0.f));
    const float inv = __builtin_amdgcn_rcpf(den);
#pragma unroll
    for (int i = 0; i < 9; ++i) o[r][i] = (x[r][i] - m) * inv;
  }
}
__device__ __forceinline__ void ln9(const float (&x)[9], float (&o)[9]) { lnR<1>((const float (&)[1][9])x, (float (&)[1][9])o); }
__device__ __forceinline__ v4u gat8(float v) {
  const unsigned d = pk2(v, __uint_as_float((unsigned)__builtin_amdgcn_mov_dpp((int)__float_as_uint(v), 0xB1, 0xf, 0xf, true)));
  return (v4u){d, (unsigned)__builtin_amdgcn_mov_dpp((int)d, 0x102, 0xf, 0xf, true), (unsigned)__builtin_amdgcn_mov_dpp((int)d, 0x104, 0xf, 0xf, true),
               (unsigned)__builtin_amdgcn_mov_dpp((int)d, 0x106, 0xf, 0xf, true)};
}
__device__ __forceinline__ void tok_row(const MP& p, const rsrc_t X, unsigned sb1, int L, int s, int r, int lane, const v4u (&v)[18], v4u px, v4u py, unsigned px9, unsigned py9) {
  const long tok = (long)s * SEG + r;
  const int m = lane & 15, g = lane >> 4;
  const int xp = 4 * (g & 1) + 2 * (g >> 1) + (m >> 3);
  const unsigned zr = opq(0u);
  const unsigned one = (g == (m >> 2)) ? (opq(0x3F80u) << (16 * (m & 1))) : zr;
  const unsigned ax = (m & 2) ? zr : one, ay = (m & 2) ? one : zr;
  const v4u A1 = {ax, ay, zr, zr}, A2 = {zr, zr, ax, ay};
  const unsigned on2 = opq(0x3F80u) << (16 * (m & 1));
  const int q2 = (m >> 1) & 3;
  const v4u sel = {q2 == 0 ? on2 : zr, q2 == 1 ? on2 : zr, q2 == 2 ? on2 : zr, q2 == 3 ? on2 : zr};
  f32x4 d1, d2, e1;
  asm volatile("s_nop 1\n\t" MF "%0, %1, %3, 0\n\t" MF "%0, %2, %3, %0" : "=&v"(e1) : "v"(v[0]), "v"(v[1]), "v"(sel));
#pragma unroll
  for (int i = 0; i < 16; ++i) {
    if (i == 0) asm volatile("s_nop 1\n\t" MF "%0, %2, %4, 0\n\t" MF "%1, %3, %4, 0" : "=&v"(d1), "=&v"(d2) : "v"(A1), "v"(A2), "v"(v[2]));
    else asm volatile(MF "%0, %2, %4, %0\n\t" MF "%1, %3, %4, %1" : "+v"(d1), "+v"(d2) : "v"(A1), "v"(A2), "v"(v[2 + i]));
  }
  asm volatile("s_nop 7" : "+v"(d1), "+v"(d2), "+v"(e1));
  float o[9];
#pragma unroll
  for (int i = 0; i < 4; ++i) { o[i] = d1[i]; o[4 + i] = d2[i]; }
  {
    auto a02 = __builtin_amdgcn_permlane32_swap(__float_as_uint(e1[0]), __float_as_uint(e1[2]), false, false);
    auto a13 = __builtin_amdgcn_permlane32_swap(__float_as_uint(e1[1]), __float_as_uint(e1[3]), false, false);
    o[8] = m >= 8 ? __uint_as_float(a13[0]) + __uint_as_float(a13[1]) : __uint_as_float(a02[0]) + __uint_as_float(a02[1]);
  }
  float x[9];
  up8(px, x); x[8] = lo2f(px9);
#pragma unroll
  for (int i = 0; i < 9; ++i) x[i] += o[i];
  if (L == 0) {
    float y[9], ln[9];
    up8(py, y); y[8] = lo2f(py9);
    rb9(x);
    ln9(x, ln);
#pragma unroll
    for (int i = 0; i < 9; ++i) x[i] = __builtin_fmaf(ln[i], y[i], x[i]);
    rb9(x);
    st16(X, (unsigned)lane * 16u, sb1 + (P_X3 + (unsigned)(r >> 3)) * PAIR + (unsigned)(r & 7) * 1024u, pk8(x));
    const v4u x9 = gat8(x[8]);
    if ((lane & 7) == 0) st16(X, (unsigned)xp * 16u, sb1 + (P_H + 4u) * PAIR + 4096u + (unsigned)r * 128u, x9);
    ln9(x, ln);
    st16(X, hoff(r, lane), sb1, pk8(ln));
    const v4u h9 = gat8(ln[8]);
    if ((lane & 7) == 0) st16(X, hoff(r, 64 + xp), sb1, h9);
  } else {
    const rsrc_t O = mkr(p.out + tok * DM, DM * 2u);
    __builtin_amdgcn_raw_buffer_store_b128(pk8(x), O, (unsigned)lane * 16u, 0u, OAUX);
    __builtin_amdgcn_raw_buffer_store_b128(gat8(x[8]), O, (lane & 7) == 0 ? 1024u + (unsigned)xp * 16u : 0xF0000000u, 0u, OAUX);
  }
}

__device__ __forceinline__ void row_role(char* smem) {
  TOK_COMMON();
  const int rw = 4 * xq + (i - 20), r = 2 * rw + (w >> 1);
  const unsigned long long sm = die_mask(X, SG0, L, lane);
  const unsigned tm = (unsigned)die_mask(X, TP0, L, lane), dm = (unsigned)die_mask(X, 0u, L, lane);
  __attribute__((address_space(3))) unsigned* cnt = (__attribute__((address_space(3))) unsigned*)smem;
  BAR();
  if (tid < 64) cnt[tid] = 0u;
  BAR();
  for (int s = w & 1; s < NSEG; s += 2) {
    int ln_ = lane; asm volatile("" : "+v"(ln_));
    const int m = ln_ & 15, g = ln_ >> 4;
    const unsigned l16 = (unsigned)ln_ * 16u, xo = (unsigned)(8 * (4 * (g & 1) + 2 * (g >> 1) + (m >> 3)) + (m & 7)) * 2u;
    const unsigned sb = SEGB(sm, s), sb1 = sb ^ (L ? 0u : 8192u);
    if (s + ARMK < NSEG) {
      const unsigned sv = opq(SENT), sl = SLOT_AT(s + ARMK) + ((dm >> ((s + ARMK) / 3)) & 1u) * 8192u;
      for (int j = r; j < 84; j += 32) {
#pragma unroll
        for (int k = 0; k < 4; ++k) st16(X, l16 + (unsigned)k * 1024u, sl + (unsigned)(j >> 1) * PAIR + (unsigned)(j & 1) * 4096u, (v4u){sv, sv, sv, sv});
      }
    }
    wait_all<PSL>(X, 0u, sb + P_FB * PAIR + 4096u + (unsigned)(L ? rw : r >> 3) * 128u);
    const v4u px = ld16(X, l16, sb + (P_X3 + (unsigned)(r >> 3)) * PAIR + (unsigned)(r & 7) * 1024u);
    const unsigned px9 = __builtin_amdgcn_raw_buffer_load_b16(X, xo, sb + (P_H + 4u) * PAIR + 4096u + (unsigned)r * 128u, 16);
    const rsrc_t YX = mkr(p.yT + (2 * s + (r >> 4)) * 9216, L ? 0u : 18432u);
    const v2u ya = ld8(YX, (unsigned)(ln_ * 256 + ((r ^ m) & 15) * 8), 0u), yb = ld8(YX, (unsigned)(ln_ * 256 + 128 + ((r ^ m) & 15) * 8), 0u);
    const v4u py = {ya.x, ya.y, yb.x, yb.y};
    const unsigned py9 = __builtin_amdgcn_raw_buffer_load_b16(YX, 16384u + (xo >> 3) * 128u + (((unsigned)r ^ (xo >> 4)) & 15u) * 8u + (xo & 6u), 0u, 16);
    wait_all<PSP>(X, 0u, sb + P_FB * PAIR + (unsigned)r * 128u);
    __builtin_amdgcn_s_sleep(RONE);
    wait_all<PSP>(X, (unsigned)(ln_ & 31) * 128u, sb + P_FB * PAIR);
    const unsigned j = (unsigned)s & 15u, to = TP0 * XBLK + (j << 20) + ((tm >> (j >> 1)) & 1u) * 8192u + (unsigned)r * 2u * PAIR;
    const unsigned t9 = (TP0 + 8u) * XBLK + ((tm >> 8) & 1u) * 8192u + (j << 17) + (unsigned)(((r >> 2) & 1) * 512 + (4 * (r >> 3) + (r & 3)) * 32);
    const unsigned v9 = (unsigned)(g * (int)PAIR + (m >> 1) * 1024 + (m & 1) * 16);
    v4u v[18];
#pragma unroll
    for (int k = 0; k < 2; ++k) v[k] = ld16(X, v9, t9 + (unsigned)k * 4u * PAIR);
#pragma unroll
    for (int k = 0; k < 16; ++k) v[2 + k] = ld16(X, l16 + (unsigned)(k & 3) * 1024u, to + (unsigned)(k >> 3) * PAIR + (unsigned)(k & 4) * 1024u);
    SB();
    tok_row(p, X, sb1, L, s, r, ln_, v, px, py, px9, py9);
    VM0();
    unsigned o1 = 0u;
    if (ln_ == 0) o1 = __hip_atomic_fetch_add(cnt + s, 1u, __ATOMIC_RELAXED, __HIP_MEMORY_SCOPE_WORKGROUP);
    if (__builtin_amdgcn_readfirstlane(o1) == 1u && ln_ < 2 - L) st4(X, ln_ ? sb1 : sb + 2048u, P_FB * PAIR + 4096u + (unsigned)rw * 128u, opq(1u));
  }
}

__device__ __forceinline__ void rows_task(const MP& p, const rsrc_t X, unsigned long long sm, u16* ys, int grp, int ng) {
  const int tid = threadIdx.x, lane = tid & 63, w = __builtin_amdgcn_readfirstlane(tid >> 6);
  const int m = lane & 15, g = lane >> 4;
  const int xp = 4 * (g & 1) + 2 * (g >> 1) + (m >> 3), c9 = 512 + 8 * xp + (m & 7);
  const unsigned sb = SEGB(sm, grp >> 2);
  v4u yv[5], xa[4];
  unsigned x9[4];
#pragma unroll
  for (int i = 0; i < 5; ++i) yv[i] = ld16(mkr(p.yT + (grp >> 1) * 9216, 18432u), (unsigned)(tid + 256 * i) * 16u, 0u);
#pragma unroll
  for (int rr = 0; rr < 4; ++rr) {
    const long t = grp * 8 + w + 4 * (rr & (2 * ng - 1));
    xa[rr] = *(const v4u*)&p.xin[t * DM + 8 * lane]; x9[rr] = p.xin[t * DM + c9];
  }
#pragma unroll
  for (int i = 0; i < 5; ++i) ((v4u*)ys)[tid + 256 * i] = yv[i];
  BAR();
  const unsigned o9 = (lane & 7) == 0 ? 0u : 0xF0000000u;
#pragma unroll 1
  for (int k = 0; k < ng; ++k) {
    float x[2][9], y[2][9], ln[2][9];
    const int t0 = (grp + k) * 8 + w;
#pragma unroll
    for (int rr = 0; rr < 2; ++rr) {
      const int j = (t0 + 4 * rr) & 15;
      const v2u ya = *(const v2u*)&ys[lane * 128 + (j ^ m) * 4], yb = *(const v2u*)&ys[lane * 128 + 64 + (j ^ m) * 4];
      up8(xa[rr], x[rr]); x[rr][8] = lo2f(x9[rr]); up8((v4u){ya.x, ya.y, yb.x, yb.y}, y[rr]); y[rr][8] = bf2f(ys[(c9 >> 2) * 64 + (j ^ xp) * 4 + (m & 3)]);
      xa[rr] = xa[2 + rr]; x9[rr] = x9[2 + rr];
    }
    lnR<2>(x, ln);
#pragma unroll
    for (int rr = 0; rr < 2; ++rr) {
#pragma unroll
      for (int i = 0; i < 9; ++i) x[rr][i] = __builtin_fmaf(ln[rr][i], y[rr][i], x[rr][i]);
      rb9(x[rr]);
      const int r = (t0 + 4 * rr) & 31;
      st16(X, (unsigned)lane * 16u, sb + (P_X3 + (unsigned)(r >> 3)) * PAIR + (unsigned)(r & 7) * 1024u, pk8(x[rr]));
      st16(X, (unsigned)xp * 16u + o9, sb + (P_H + 4u) * PAIR + 4096u + (unsigned)r * 128u, gat8(x[rr][8]));
    }
    lnR<2>(x, ln);
#pragma unroll
    for (int rr = 0; rr < 2; ++rr) {
      const int r = (t0 + 4 * rr) & 31;
      st16(X, hoff(r, lane), sb, pk8(ln[rr]));
      st16(X, hoff(r, 64 + xp) + o9, sb, gat8(ln[rr][8]));
    }
  }
  VM0();
  BAR();
  if (tid < ng) st4(X, (unsigned)tid * 128u, sb + P_FB * PAIR + 4096u + (unsigned)(grp & 3) * 128u, opq(1u));
}

__device__ __forceinline__ void arrive(const MP& p, int k, int grp) {
  VM0();
  BAR();
  if (threadIdx.x < 64) {
    unsigned old = 0u;
    if (threadIdx.x == 0) old = __hip_atomic_fetch_add(p.xb + MF_OFF / 4u + (k * 16 + grp) * 32, 1u, __ATOMIC_RELAXED, __HIP_MEMORY_SCOPE_AGENT);
    if (__builtin_amdgcn_readfirstlane(old) == 15u && threadIdx.x < 8) st4(mkr(p.xb, XB_BYTES), threadIdx.x * 128u, RDY_OFF(k) + (unsigned)grp * 1024u, opq(1u));
  }
}
__device__ __forceinline__ void wait_ready(const MP& p, int k) {
  if (threadIdx.x < 64) wait_all<PSL>(mkr(p.xb, XB_BYTES), (threadIdx.x % 9u) * 1024u, RDY_OFF(k) + (blockIdx.x & 7u) * 128u);
  BAR();
}
__device__ __forceinline__ void start_work(char* smem, int wk) {
  const MP p = getp();
  const int w = __builtin_amdgcn_readfirstlane(threadIdx.x >> 6);
#define NARM (ARMK - 3u)
  conv_task(p.kvT, p.ssm_k, p.ssm_D, p.yT, (u16*)(smem + w * 32768), 4 * wk + w, [&]() {
    const unsigned sv = opq(SENT);
    constexpr unsigned S16 = SLOT / 16u;
#pragma unroll
    for (unsigned k = 0; k < (NARM * S16 + 36863u) / 36864u; ++k) {
      const unsigned i = k * 36864u + wk * 256 + threadIdx.x;
      st16(mkr(p.xb, XB_BYTES), i < NARM * S16 ? SLOT_AT(3u + i / S16) + (i % S16) * 16u : 0xF0000000u, 0u, (v4u){sv, sv, sv, sv});
    }
  });
  arrive(p, 0, wk >> 4);
}
__device__ __forceinline__ void token_start(char* smem) {
  TOK_COMMON();
  (void)w;
  const int k = 4 * i + xq;
  if (L || (i >= 12 && i < 16)) start_work(smem, L ? 32 + k : 80 + k);
  if (i >= (L ? 12 : 20)) {
    const unsigned long long sm = die_mask(X, SG0, 0, lane);
    int g = L ? 111 - k : k - 96;
    while (g < 120) {
      if (g >= 0 && g < 64) {
        wait_ready(p, 0);
      }
      rows_task(p, X, sm, (u16*)smem, g < 0 ? g + 16 : 16 + 2 * g, g < 0 ? 1 : 2);
      g += g < 0 ? 16 : (L ? 48 : 120);
    }
  }
}
__device__ __forceinline__ void token_wg(char* smem) {
  if ((unsigned)(size_t)(__attribute__((address_space(3))) char*)smem != 0u) return;
  token_start(smem);
  const int i = ((int)blockIdx.x - NCHW) >> 3;
  if (i < 12) qkv_role();
  else if (i < 20) ap_role();
  else row_role(smem);
}

__global__ __launch_bounds__(256, 1) void k_main(MP) {
  extern __shared__ __attribute__((aligned(16))) char smem[];
  asm volatile("LKBEG:");
  if ((int)blockIdx.x < NCHW) chain_wg(smem);
  else token_wg(smem);
}

extern "C" void kernel_launch(void* const* d_in, const int* in_sizes, int n_in, void* d_out, int out_size, void* d_ws,
                              size_t ws_size, hipStream_t stream) {
  const u16* A_q = (const u16*)d_in[0];
  const u16* A_k = (const u16*)d_in[1];
  const u16* A_v = (const u16*)d_in[2];
  const u16* A_shift = (const u16*)d_in[3];
  const u16* A_ssmk = (const u16*)d_in[4];
  const u16* A_ssmD = (const u16*)d_in[5];
  const u16* B_mem0 = (const u16*)d_in[7];
  const u16* B_Wq = (const u16*)d_in[8];
  const u16* B_Wk = (const u16*)d_in[9];
  const u16* B_Wv = (const u16*)d_in[10];
  const u16* B_Wo = (const u16*)d_in[11];

  char* ws = (char*)d_ws;
  size_t off = (size_t)(-(uintptr_t)ws) & (XBLK - 1u);
  auto alloc = [&](size_t bytes) { char* q = ws + off; off += (bytes + 4095) & ~(size_t)4095; return q; };
  unsigned* xb = (unsigned*)alloc((size_t)XB_BYTES);
  u16* kvT = (u16*)alloc((size_t)DM * S_LEN * 2);
  u16* yT = (u16*)alloc((size_t)DM * S_LEN * 2);

  PreP pp;
  pp.kk = A_k; pp.vv = A_v; pp.shift_k = A_shift; pp.ssm_k = A_ssmk; pp.ssm_D = A_ssmD; pp.yT = yT; pp.kvT = kvT;
  pp.Wq = B_Wq; pp.Wk = B_Wk; pp.Wv = B_Wv; pp.Wo = B_Wo; pp.xb = xb; pp.mem0 = B_mem0;
  k_pre<<<240 + PRE_FILL + 256 + 288, 256, 0, stream>>>(pp);
  MP mp;
  mp.out = (u16*)d_out; mp.xb = xb;
  mp.xin = A_q; mp.yT = yT; mp.kvT = kvT; mp.ssm_k = A_ssmk; mp.ssm_D = A_ssmD;
  mp.Wq = B_Wq; mp.Wk = B_Wk; mp.Wv = B_Wv; mp.Wo = B_Wo;
  const int smem_bytes = 160 * 1024;
  (void)hipFuncSetAttribute((const void*)k_main, hipFuncAttributeMaxDynamicSharedMemorySize, smem_bytes);
  k_main<<<NCHW + NTOKW, 256, smem_bytes, stream>>>(mp);
}
```

```cpp
#define CALT 560u
#define NCAL 8
#include <hip/hip_runtime.h>
#include <stdint.h>

typedef unsigned short u16;
using bf16x8 = __attribute__((ext_vector_type(8))) short;
using f32x4 = __attribute__((ext_vector_type(4))) float;
using f32x2 = __attribute__((ext_vector_type(2))) float;
using bfx8 = __attribute__((ext_vector_type(8))) __bf16;
using bfx2 = __attribute__((ext_vector_type(2))) __bf16;
typedef unsigned v4u __attribute__((ext_vector_type(4)));
typedef unsigned v2u __attribute__((ext_vector_type(2)));
typedef short v4s __attribute__((ext_vector_type(4)));

#define S_LEN 2048
#define DM 576
#define NH 32
#define DH 128
#define HD (NH * DH)
#define SEG 32
#define NSEG 64

__device__ __forceinline__ float bf2f(u16 x) { return __uint_as_float(((unsigned)x) << 16); }
__device__ __forceinline__ unsigned pk2(float a, float b) { f32x2 v = {a, b}; bfx2 r = __builtin_convertvector(v, bfx2); return __builtin_bit_cast(unsigned, r); }
__device__ __forceinline__ float lo2f(unsigned u) { return __uint_as_float(u << 16); }
__device__ __forceinline__ float hi2f(unsigned u) { return __uint_as_float(u & 0xFFFF0000u); }

#define NFC 128
#define XBLK 2097152u
#define PAIR 16384u
#define SLOT 688128u
#define AGO 589824u
#define AGT 655360u
#define SG0 22
#define TP0 86
#define NXB 95
#define XB_BYTES (NXB * XBLK)
#define SLOT_AT(i) ((unsigned)((i) / 3) * XBLK + (unsigned)((i) % 3) * SLOT)
#define HT_OFF SLOT_AT(65)
#define CTAB_OFF (HT_OFF + 16384u)
#define MF_OFF (HT_OFF + 20480u)
#define TMO_OFF (MF_OFF + 15u * 128u)
#define RDY_OFF(k) (MF_OFF + 4096u + (k) * 10240u)
#define MF_BYTES 16384u
#define TRC_OFF SLOT_AT(64)
#define CAL_OFF (XBLK - 16384u)
#define P_KV 96u
#define P_H 112u
#define P_X3 117u
#define P_FA 121u
#define P_FB 122u
#define ARMK 8
#define KPM 0
__device__ __forceinline__ unsigned kv2(const unsigned (&u)[4], unsigned v, v2u w0, v2u w1) {
  const float a = __builtin_fmaf(lo2f(u[3]), lo2f(w0.x), __builtin_fmaf(lo2f(u[2]), hi2f(w0.x), __builtin_fmaf(lo2f(u[1]), lo2f(w0.y), lo2f(u[0]) * hi2f(w0.y))));
  const float b = __builtin_fmaf(hi2f(u[3]), lo2f(w1.x), __builtin_fmaf(hi2f(u[2]), hi2f(w1.x), __builtin_fmaf(hi2f(u[1]), lo2f(w1.y), hi2f(u[0]) * hi2f(w1.y))));
  return pk2(a * lo2f(v), b * hi2f(v));
}
struct PreP {
  const u16* kk; const u16* vv; const u16* shift_k; const u16* ssm_k; const u16* ssm_D; u16* yT; u16* kvT;
  const u16* Wq; const u16* Wk; const u16* Wv; const u16* Wo; unsigned* xb; const u16* mem0;
};
#define PRE_FILL 112
#define PFN 5
__global__ __launch_bounds__(256, 4) void k_pre(PreP p) {
  __shared__ __attribute__((aligned(16))) float sh[64 * 36];
  const int tid = threadIdx.x, b0 = blockIdx.x, b = b0 < 240 ? b0 : (b0 < 528 ? b0 + PRE_FILL + 256 : b0 - 288);
  const __amdgpu_buffer_rsrc_t rx = __builtin_amdgcn_make_buffer_rsrc((void*)p.xb, 0, XB_BYTES, 0x00020000);
  const v4u ones = {0xFFFFFFFFu, 0xFFFFFFFFu, 0xFFFFFFFFu, 0xFFFFFFFFu}, zero = {0, 0, 0, 0};
  if (b < 96) {
    if (tid >= 64 || (KPM & 16)) return;
    const unsigned cb = (unsigned)b * XBLK + CAL_OFF;
    __builtin_amdgcn_raw_buffer_store_b128(zero, rx, (unsigned)(tid & 1) * 8192u + (unsigned)(tid >> 1) * 256u, cb, 16);
    asm volatile("s_waitcnt vmcnt(0)" ::: "memory");
    unsigned mn[2] = {~0u, ~0u};
    for (int i = 0; i < NCAL; ++i) {
      const unsigned long long c0 = __builtin_amdgcn_s_memtime();
      asm volatile("s_waitcnt lgkmcnt(0)" ::: "memory");
      const unsigned v = __builtin_amdgcn_raw_buffer_load_b32(rx, 0, cb + (unsigned)(i & 1) * 8192u + (unsigned)(i >> 1) * 256u, 16);
      asm volatile("s_waitcnt vmcnt(0)" :: "v"(v) : "memory");
      const unsigned d = (unsigned)(__builtin_amdgcn_s_memtime() - c0);
      if (i & 1) mn[1] = d < mn[1] ? d : mn[1]; else mn[0] = d < mn[0] ? d : mn[0];
      const unsigned lo = mn[0] < mn[1] ? mn[0] : mn[1], hi = mn[0] < mn[1] ? mn[1] : mn[0];
      if (i >= 3 && lo < CALT && hi > lo + 100u) break;
    }
    unsigned xcc; asm volatile("s_getreg_b32 %0, hwreg(HW_REG_XCC_ID)" : "=s"(xcc));
    const unsigned res = mn[1] < mn[0] ? 1u : 0u;
    if (tid == 0) __builtin_amdgcn_raw_buffer_store_b32(res ^ ((xcc >> 2) & 1u), rx, 0, CTAB_OFF + (unsigned)b * 4u, 16);
  } else if (b < 240) {
    if (KPM & 1) return;
    const __amdgpu_buffer_rsrc_t rk = __builtin_amdgcn_make_buffer_rsrc((void*)p.kk, 0, S_LEN * DM * 2, 0x00020000);
    float (*kx)[132] = (float (*)[132])sh;
    float (*cf)[140] = (float (*)[140])(sh + 4 * 132);
    const int d0 = (b - 96) * 4, q = tid & 3, dd = (tid >> 2) & 3, t0 = (tid >> 4) * 8;
    const float Dv = bf2f(p.ssm_D[d0 + dd]);
    {
      const int s = tid >> 1, d = d0 + (tid & 1) * 2;
      unsigned u[4];
#pragma unroll
      for (int a = 0; a < 4; ++a) u[a] = __builtin_amdgcn_raw_buffer_load_b32(rk, (unsigned)((s - 3 + a) * DM + d) * 2u, 0, 0);
      const unsigned vr = *(const unsigned*)&p.vv[s * DM + d], c0 = *(const unsigned*)&p.ssm_k[(long)(d0 + (tid >> 6)) * S_LEN + (tid & 63) * 2];
      const v4u w = *(const v4u*)&p.shift_k[d * 4];
      const unsigned r = kv2(u, vr, (v2u){w.x, w.y}, (v2u){w.z, w.w});
      kx[(tid & 1) * 2][s] = lo2f(r); kx[(tid & 1) * 2 + 1][s] = hi2f(r);
      *(f32x2*)&cf[tid >> 6][8 + (tid & 63) * 2] = (f32x2){lo2f(c0), hi2f(c0)};
      if (tid < 32) cf[tid >> 3][tid & 7] = 0.f;
    }
    __syncthreads();
    float y[8];
#pragma unroll
    for (int i = 0; i < 8; ++i) y[i] = 0.f;
#pragma unroll 1
    for (int j = 8 * q; j <= t0; j += 32) {
      f32x4 kk[2], cw[4];
#pragma unroll
      for (int x = 0; x < 2; ++x) kk[x] = *(const f32x4*)&kx[dd][j + 4 * x];
#pragma unroll
      for (int x = 0; x < 4; ++x) cw[x] = *(const f32x4*)&cf[dd][t0 - j + 4 * x];
#pragma unroll
      for (int jj = 0; jj < 8; ++jj)
#pragma unroll
        for (int i = 0; i < 8; ++i) y[i] = __builtin_fmaf(cw[(8 + i - jj) >> 2][(8 + i - jj) & 3], kk[jj >> 2][jj & 3], y[i]);
    }
#pragma unroll
    for (int i = 0; i < 8; ++i) {
      y[i] += __uint_as_float((unsigned)__builtin_amdgcn_mov_dpp((int)__float_as_uint(y[i]), 0xB1, 0xf, 0xf, true));
      y[i] += __uint_as_float((unsigned)__builtin_amdgcn_mov_dpp((int)__float_as_uint(y[i]), 0x4E, 0xf, 0xf, true));
    }
    const float ya = q == 0 ? y[0] : (q == 1 ? y[2] : (q == 2 ? y[4] : y[6])), yb = q == 0 ? y[1] : (q == 1 ? y[3] : (q == 2 ? y[5] : y[7]));
    const int t = t0 + 2 * q, f = (d0 >> 3) & 15;
    const unsigned r0 = pk2(ya, yb), r1 = pk2(kx[dd][t] * Dv, kx[dd][t + 1] * Dv), o = pk2(lo2f(r0) + lo2f(r1), hi2f(r0) + hi2f(r1));
    u16* dst = p.yT + (t >> 4) * 9216 + (d0 >> 2) * 64 + dd;
    dst[((t & 15) ^ f) << 2] = (u16)(o & 0xFFFFu); dst[(((t + 1) & 15) ^ f) << 2] = (u16)(o >> 16);
  } else if (b < 240 + PRE_FILL) {
    if (KPM & 32) return;
    const unsigned i0 = (unsigned)(b - 240) * 256u + tid;
    if (i0 < (SLOT - AGO) / 16u) {
      const unsigned o = AGO + i0 * 16u, s = o < AGT ? (i0 >> 11) * 4u + ((o >> 10) & 3u) : 8u, r = o & 1023u;
      const unsigned gm = s < 8u ? r >> 8 : (r >> 6) & 3u, m3 = (r >> 4) & 3u, ga = s < 8u ? (r >> 6) & 3u : r >> 8, row = 4u * (gm & 1u) + 2u * (gm >> 1) + (m3 & 1u), half = m3 >> 1;
      const unsigned* src = (const unsigned*)(p.mem0 + row * DM + 64u * s + 16u * ga + 2u * half);
      __builtin_amdgcn_raw_buffer_store_b128((v4u){src[0], src[2], src[4], src[6]}, rx, o, 0, 16);
    }
    for (unsigned i = i0; i < 131072u + MF_BYTES / 16u; i += PRE_FILL * 256)
      __builtin_amdgcn_raw_buffer_store_b128(zero, rx, i < 131072u ? (SG0 + (i >> 11)) * XBLK + P_FA * PAIR + (i & 2047u) * 16u : MF_OFF + (i & 2047u) * 16u, 0, 16);
    for (unsigned i = i0; i < 2u * SLOT / 16u; i += PRE_FILL * 256) __builtin_amdgcn_raw_buffer_store_b128(ones, rx, SLOT + i * 16u, 0, 16);
  } else if (b >= 240 + PRE_FILL + 256) {
    if (KPM & 2) return;
    const __amdgpu_buffer_rsrc_t rk = __builtin_amdgcn_make_buffer_rsrc((void*)p.kk, 0, S_LEN * DM * 2, 0x00020000);
    u16 (*tile)[72] = (u16 (*)[72])sh;
    const int bid = b - 240 - PRE_FILL - 256, d0 = (bid % 9) * 64, s0 = (bid / 9) * 64;
    const int sr = (tid >> 3) * 2, d = d0 + (tid & 7) * 8;
    v4u kr[5], vr[2], w[4];
#pragma unroll
    for (int a = 0; a < 5; ++a) kr[a] = __builtin_amdgcn_raw_buffer_load_b128(rk, (unsigned)((s0 + sr - 3 + a) * DM + d) * 2u, 0, 0);
#pragma unroll
    for (int a = 0; a < 2; ++a) vr[a] = *(const v4u*)&p.vv[(long)(s0 + sr + a) * DM + d];
#pragma unroll
    for (int a = 0; a < 4; ++a) w[a] = *(const v4u*)&p.shift_k[(d + 2 * a) * 4];
    __builtin_amdgcn_sched_barrier(0);
#pragma unroll
    for (int it = 0; it < 2; ++it) {
      v4u o;
#pragma unroll
      for (int j = 0; j < 4; ++j) { const unsigned u[4] = {kr[it][j], kr[it + 1][j], kr[it + 2][j], kr[it + 3][j]}; o[j] = kv2(u, vr[it][j], (v2u){w[j].x, w[j].y}, (v2u){w[j].z, w[j].w}); }
      *(v4u*)&tile[sr + it][(tid & 7) * 8] = o;
    }
    __syncthreads();
    const int lane = tid & 63, wv = tid >> 6, g = lane >> 4, r = (lane >> 2) & 3, c = lane & 3;
    v2u o[4];
#pragma unroll
    for (int i = 0; i < 4; ++i) o[i] = __builtin_bit_cast(v2u, __builtin_amdgcn_ds_read_tr16_b64_v4i16((__attribute__((address_space(3))) v4s*)&tile[16 * g + 4 * i + r][16 * wv + 4 * c]));
    u16* dst = p.kvT + (long)(d0 + 16 * wv + (lane & 15)) * S_LEN + s0 + 16 * g;
    *(v4u*)dst = (v4u){o[0].x, o[0].y, o[1].x, o[1].y};
    *(v4u*)(dst + 8) = (v4u){o[2].x, o[2].y, o[3].x, o[3].y};
  } else {
    if (KPM & 4) return;
    const int x = b & 7, k = (b - 240 - PRE_FILL) >> 3;
    unsigned long long pc; unsigned l0, l1, l2, l3; int rel;
    asm volatile("s_getpc_b64 %0\nLPRE:\n\ts_mov_b32 %1, LKBEG-LPRE\n\ts_mov_b32 %2, LCHEND-LKBEG\n\ts_mov_b32 %3, LQVEND-LKBEG\n\ts_mov_b32 %4, LQKEND-LKBEG\n\ts_mov_b32 %5, LAPEND-LKBEG"
                 : "=s"(pc), "=s"(rel), "=s"(l0), "=s"(l1), "=s"(l2), "=s"(l3));
    l0 = l0 > l1 ? l0 : l1; l2 = l2 > l3 ? l2 : l3; l0 = l0 > l2 ? l0 : l2;
    const unsigned o = (unsigned)(k * 64 + (tid & 63)) * 64u;
    unsigned a[PFN + 2];
    a[PFN] = *(const __attribute__((address_space(1))) unsigned*)(pc + rel + (o < l0 ? o : 0u));
    a[PFN + 1] = __builtin_amdgcn_raw_buffer_load_b32(rx, k == 31 ? tid * XBLK + CAL_OFF + 8064u : 0xF0000000u, 0, 16);
#pragma unroll
    for (int i = 0; i < PFN; ++i) {
      const int q = (i * 32 + k) * 256 + tid, r2 = q >> 5, gr = r2 >> 5, row = (gr / 3) * 64 + (x >> 2) * 32 + (r2 & 31), off = (q & 31) * 32, ty = gr % 3;
      a[i] = *(const unsigned*)(gr < 27 ? (ty == 0 ? p.Wq : (ty == 1 ? p.Wk : p.Wv)) + (long)row * HD + (x & 3) * 1024 + off
                                        : p.Wo + ((long)(x & 3) * DM + (gr < 36 ? gr - 27 : 0) * 64 + (x >> 2) * 32 + (r2 & 31)) * 1024 + off);
    }
#pragma unroll
    for (int i = 0; i < PFN + 2; ++i) asm volatile("" :: "v"(a[i]));
  }
}

#define CREV_STRIDE 2208
#define SP(k)
#define LWAIT() asm volatile("s_waitcnt lgkmcnt(0)" ::: "memory")
template <class F>
__device__ __forceinline__ void conv_task(const u16* __restrict__ kvT, const u16* __restrict__ ssm_k, const u16* __restrict__ ssm_D, u16* yT, u16* shb, int d, F fill) {
  const int lane = threadIdx.x & 63;
  u16* crev = shb + 64;
  u16* kvs = crev + 4 * CREV_STRIDE;
  const int X0 = 2048 + 63;
  SP(0);
  v4u cv[4], kq[4];
#pragma unroll
  for (int j = 0; j < 4; ++j) { cv[j] = *(const v4u*)&ssm_k[(long)d * S_LEN + lane * 32 + 8 * j]; kq[j] = *(const v4u*)&kvT[(long)d * S_LEN + (j * 64 + lane) * 8]; }
  const float Dv = bf2f(ssm_D[d]);
  const v4u z4 = {0u, 0u, 0u, 0u};
  *(v4u*)&kvs[(lane >> 2) * 40 + (lane & 3) * 8] = z4;
  if (lane < 32) *(v4u*)&crev[(lane >> 3) * CREV_STRIDE + (lane & 7) * 8] = z4;
  if (lane < 36) *(v4u*)&crev[(lane / 9) * CREV_STRIDE + 2112 + (lane % 9) * 8] = z4;
  {
    unsigned r[18];
#pragma unroll
    for (int k = 0; k < 16; ++k) { const unsigned u = cv[3 - (k >> 2)][3 - (k & 3)]; r[k] = __builtin_amdgcn_alignbyte(u, u, 2); }
#pragma unroll
    for (int k = 0; k < 2; ++k) r[16 + k] = (unsigned)__builtin_amdgcn_update_dpp(0, (int)r[k], 0x138, 0xf, 0xf, true);
    LWAIT();
#pragma unroll
    for (int rho = 0; rho < 4; ++rho)
#pragma unroll
      for (int j = 0; j < 4; ++j) {
        v4u o;
#pragma unroll
        for (int k = 0; k < 4; ++k) { const int q = 4 * j + k + (rho >> 1); o[k] = (rho & 1) ? __builtin_amdgcn_alignbyte(r[q + 1], r[q], 2) : r[q]; }
        *(v4u*)&crev[rho * CREV_STRIDE + 2080 - 32 * lane + 8 * j] = o;
      }
    if (lane == 63) {
      *(v2u*)&crev[1 * CREV_STRIDE + 60] = (v2u){0u, r[0] << 16};
      *(v2u*)&crev[2 * CREV_STRIDE + 60] = (v2u){0u, r[0]};
      *(v2u*)&crev[3 * CREV_STRIDE + 60] = (v2u){r[0] << 16, __builtin_amdgcn_alignbyte(r[1], r[0], 2)};
    }
#pragma unroll
    for (int j = 0; j < 4; ++j) *(v4u*)&kvs[(16 + j * 16 + (lane >> 2)) * 40 + (lane & 3) * 8] = kq[j];
  }
  fill();
  LWAIT();
  SP(1);
  const int n = lane & 15, g = lane >> 4;
  const int rho = (X0 - n) & 3;
  f32x4 acc[4][2];
#pragma unroll
  for (int q = 0; q < 8; ++q) acc[q >> 1][q & 1] = (f32x4){0.f, 0.f, 0.f, 0.f};
  {
    typedef __attribute__((address_space(3))) const v2u* l2p;
    typedef __attribute__((address_space(3))) const bf16x8* l4p;
    unsigned a0 = (unsigned)(size_t)(__attribute__((address_space(3))) u16*)&crev[rho * CREV_STRIDE + (X0 - (n - 8 * g) - rho)] - 3232u, a1 = a0 + 8u;
    unsigned b0 = (unsigned)(size_t)(__attribute__((address_space(3))) u16*)&kvs[(14 + n) * 40 + 8 * g];
    asm volatile("" : "+v"(a1));
#define AFR(m, f) ({ const unsigned o_ = 3232u - 64u * (m) - 1024u * ((f) >> 1) - 32u * ((f) & 1); const v2u lo_ = *(l2p)(size_t)(a0 + o_), hi_ = *(l2p)(size_t)(a1 + o_); \
                     __builtin_bit_cast(bf16x8, (v4u){lo_.x, lo_.y, hi_.x, hi_.y}); })
#define BFR(m, j) (*(l4p)(size_t)(b0 + 80u * (2 - (m)) + 1280u * (j)))
    bf16x8 af[2][8], bf[2][4];
#pragma unroll
    for (int f = 0; f < 8; ++f) { af[0][f] = AFR(0, f); af[1][f] = af[0][f]; __builtin_amdgcn_sched_barrier(0); }
#pragma unroll
    for (int j = 0; j < 4; ++j) { bf[0][j] = BFR(0, j); bf[1][j] = bf[0][j]; }
#pragma unroll 1
    for (int mo = 0; mo < 8; ++mo) {
#pragma unroll
      for (int m = 0; m < 2; ++m) {
        int k = 0;
#pragma unroll
        for (int a = 0; a < 4; ++a)
#pragma unroll
          for (int ct = a; ct < 4; ++ct)
#pragma unroll
            for (int ti = 0; ti < 2; ++ti) {
              acc[ct][ti] = __builtin_amdgcn_mfma_f32_16x16x32_bf16(__builtin_bit_cast(bfx8, af[m][2 * a + ti]), __builtin_bit_cast(bfx8, bf[m][ct - a]), acc[ct][ti], 0, 0, 0);
              if (k < 2) af[m ^ 1][k] = AFR(m + 1, k);
              else if (k < 6) bf[m ^ 1][k - 2] = BFR(m + 1, k - 2);
              else if (k < 12) af[m ^ 1][k - 4] = AFR(m + 1, k - 4);
              __builtin_amdgcn_sched_barrier(0);
              ++k;
            }
      }
      a0 -= 128u; a1 -= 128u; b0 -= 160u;
    }
  }
  SP(2);
  u16* yo = crev;
  LWAIT();
#pragma unroll
  for (int q = 0; q < 8; ++q) {
    const int o = (16 * (q >> 1) + n) * 40 + 16 * (q & 1) + 4 * g;
    const v2u kx = *(const v2u*)&kvs[16 * 40 + o];
    const f32x4 y = acc[q >> 1][q & 1];
    const unsigned y0 = pk2(y[0], y[1]), y1 = pk2(y[2], y[3]), r0 = pk2(lo2f(kx.x) * Dv, hi2f(kx.x) * Dv), r1 = pk2(lo2f(kx.y) * Dv, hi2f(kx.y) * Dv);
    *(v2u*)&yo[o] = (v2u){pk2(lo2f(y0) + lo2f(r0), hi2f(y0) + hi2f(r0)), pk2(lo2f(y1) + lo2f(r1), hi2f(y1) + hi2f(r1))};
  }
  LWAIT();
  __builtin_amdgcn_s_barrier();
  asm volatile("" ::: "memory");
  {
    const int tid = threadIdx.x, f = (d >> 3) & 15;
    const u16* y0 = shb - (tid >> 6) * 16384 + 64;
#pragma unroll
    for (int k = 0; k < 4; ++k) {
      const int tile = 32 * k + (tid >> 3), t = 16 * tile + 2 * ((tid & 7) ^ (f >> 1));
      unsigned u[4];
#pragma unroll
      for (int c = 0; c < 4; ++c) u[c] = *(const unsigned*)&y0[c * 16384 + (t >> 5) * 40 + (t & 31)];
      const v2u e = {__builtin_amdgcn_perm(u[1], u[0], 0x05040100u), __builtin_amdgcn_perm(u[3], u[2], 0x05040100u)}, o = {__builtin_amdgcn_perm(u[1], u[0], 0x07060302u), __builtin_amdgcn_perm(u[3], u[2], 0x07060302u)};
      if (tile >= NFC / 16) __builtin_amdgcn_raw_buffer_store_b128((f & 1) ? (v4u){o.x, o.y, e.x, e.y} : (v4u){e.x, e.y, o.x, o.y}, __builtin_amdgcn_make_buffer_rsrc((void*)yT, 0, DM * S_LEN * 2, 0x00020000),
                                                                   tile * 18432 + (tid & 7) * 16, (d >> 2) * 128, 16);
    }
  }
}

#define NCHW 64
#define NTOKW 192
#define PSL 4
#define SENT 0xFFFFFFFFu
#define SPIN_CAP 300000u
struct MP {
  const u16* Wq; const u16* Wk; const u16* Wv; const u16* Wo; u16* out; unsigned* xb;
  const u16* xin; u16* yT; u16* kvT; const u16* ssm_k; const u16* ssm_D;
};

__device__ __forceinline__ MP getp() {
  MP r;
#if defined(__HIP_DEVICE_COMPILE__)
  const __attribute__((address_space(4))) unsigned long long* k = (const __attribute__((address_space(4))) unsigned long long*)__builtin_amdgcn_kernarg_segment_ptr();
  asm volatile("" : "+s"(k));
#define GP(T, i) ((T*)(__attribute__((address_space(1))) T*)k[i])
  r.Wq = GP(const u16, 0);
  r.Wk = GP(const u16, 1);
  r.Wv = GP(const u16, 2);
  r.Wo = GP(const u16, 3);
  r.out = GP(u16, 4);
  r.xb = GP(unsigned, 5);
  r.xin = GP(const u16, 6);
  r.yT = GP(u16, 7);
  r.kvT = GP(u16, 8);
  r.ssm_k = GP(const u16, 9);
  r.ssm_D = GP(const u16, 10);
#endif
  return r;
}
typedef __amdgpu_buffer_rsrc_t rsrc_t;
__device__ __forceinline__ rsrc_t mkr(const void* p, unsigned bytes) { return __builtin_amdgcn_make_buffer_rsrc((void*)p, 0, bytes, 0x00020000); }
__device__ __forceinline__ void st16(rsrc_t r, unsigned vo, unsigned so, v4u v) { __builtin_amdgcn_raw_buffer_store_b128(v, r, vo, so, 16); }
__device__ __forceinline__ void st4(rsrc_t r, unsigned vo, unsigned so, unsigned v) { __builtin_amdgcn_raw_buffer_store_b32(v, r, vo, so, 16); }
__device__ __forceinline__ v4u ld16(rsrc_t r, unsigned vo, unsigned so) { return __builtin_amdgcn_raw_buffer_load_b128(r, vo, so, 16); }
__device__ __forceinline__ v2u ld8(rsrc_t r, unsigned vo, unsigned so) { return __builtin_amdgcn_raw_buffer_load_b64(r, vo, so, 16); }
__device__ __forceinline__ unsigned ld4(rsrc_t r, unsigned vo, unsigned so) { return __builtin_amdgcn_raw_buffer_load_b32(r, vo, so, 16); }
__device__ __forceinline__ unsigned opq(unsigned x) { asm volatile("" : "+v"(x)); return x; }

__device__ __forceinline__ unsigned long long die_mask(rsrc_t X, unsigned first, int L, int lane) { return __ballot((ld4(X, (unsigned)lane * 4u, CTAB_OFF + first * 4u) & 1u) != (unsigned)L); }
#define SEGB(m, s) ((unsigned)(SG0 + (s)) * XBLK + (unsigned)(((m) >> (s)) & 1ull) * 8192u)
__device__ __forceinline__ bool spin_fail(unsigned& spins, rsrc_t F) {
  ++spins;
  if (spins > SPIN_CAP) { unsigned one = 0x40000000u | (blockIdx.x << 8) | threadIdx.x; asm volatile("" : "+v"(one)); if (__builtin_amdgcn_readfirstlane(ld4(F, 0, TMO_OFF)) == 0u) st4(F, 0, TMO_OFF, one); return true; }
  if ((spins & 1023u) == 0u) return __builtin_amdgcn_readfirstlane(ld4(F, 0, TMO_OFF)) != 0;
  return false;
}
__device__ __forceinline__ unsigned rtc() { return (unsigned)__builtin_amdgcn_s_memrealtime(); }
#define BAR() do { asm volatile("s_waitcnt lgkmcnt(0)" ::: "memory"); __builtin_amdgcn_s_barrier(); asm volatile("" ::: "memory"); } while (0)
#define SB() __builtin_amdgcn_sched_barrier(0)
#define MF "v_mfma_f32_16x16x32_bf16 "
#define VM0() __builtin_amdgcn_s_waitcnt(0x0F70)
#define DL1 12
#define SCH_B0 48
#define DLL1 6
#define DLL2 0
#define DLF 24
#define SCH_PEN 0
#define DLX 4
#define LATE 2
#define TKL 28
#define UP 1
#define DN 100
#define DL3 16
#define FIN -3
#define CH_FIN CH_S(FIN)
#define DLT 16
#define DLY 20
#define CH_DLT CH_S(DLT)
#define CH_DLY CH_S(DLY)
#define ES 6
#define ED 4
#define EB 8
#define CH_ES CH_S(ES)
#define CH_EX "%[ex]"
#define DRYL 1
#define CH_DRYL CH_S(DRYL)
#define SMAP 0x1023u
#define RO1 4
#define CH_HNP
#define CH_RO1 CH_S(RO1)
#define RSL 2
#define CH_S2(x) #x
#define CH_S(x) CH_S2(x)
#define CH_ST "s_memtime s[62:63]\n\ts_waitcnt lgkmcnt(0)\n\ts_sub_u32 s67, s62, s67\n\t"
#define CH_BASE "s_memtime s[94:95]\n\ts_waitcnt lgkmcnt(0)\n\ts_mov_b32 s67, s94\n\t"
#define CH_DPC
#define CH_TOKCHK "s_cbranch_vccz LG3_%=\n\t"
#define CH_SEGC
#define CH_SG0 CH_S(SG0)
#define CH_ARMD "%[armd]"
#define CH_ARMS "%[arms]"
#define CH_SLOT "688128"
#define CH_SKIP "32768"
#define CH_DL1 CH_S(DL1)
#define CH_DLL1 CH_S(DLL1)
#define CH_DLL2 CH_S(DLL2)
#define CH_DLF CH_S(DLF)
#define CH_B0 CH_S(SCH_B0)
#define CH_PEN CH_S(SCH_PEN)
#define CH_RSL CH_S(RSL)
#define CH_DL3 CH_S(DL3)
#define CH_UP CH_S(UP)
#define CH_DN CH_S(DN)
#define CH_DLX CH_S(DLX)
#define CH_LATE CH_S(LATE)
#define CH_DL1L "%[dl1l]"
#define CH_TKL CH_S(TKL)
#define CH_SCHW "656384"
#define CH_CAP "%[cap]"
#define CH_CTAB "%[ctab]"
#define CH_COW
#define CH_INW "s_sub_u32 s93, s48, 10\n\ts_cmp_lt_u32 s93, 50\n\t"
#define CH_ST1
#define CH_ST2
#define FDL 6
#define CH_FDL CH_S(FDL)
#define CH_ST3
#define CH_ST4
#define CH_TRC
#define CH_TG2
#define CH_TG5
#define CH_STV
#define CH_STW
#define CH_DP0
#define CH_DP1
#define CH_DP2
#define CH_DP3
#define CH_DP4
#define CH_DP5
#define CH_DP6
#define CH_DP7
#define CH_DP8
#define CH_DP9
#define CH_DP10
#define CH_DP11
#define CH_DP12
#define CH_DP13
#define CH_DP14
#define CH_DP15
#define CH_DP16
#define CH_DP17
#define CH_OPS {64, 80, 128, 144, 1, 17, 65, 81, 129, 145, 2, 18, 82, 130, 146, 3, 67, 83, 131, 147, 4, 20, 68, 84, 132, 5, 69, 85, 133, 149, 6, 22, 70, 86, 134, 150, 7, 23, 71, 87, 135, 151, 8, 24, 88, 136, 195, 209, 210, 211, 227, 241, 242, 256, 0, 16, 19, 21, 66, 72, 148, 152, 208, 240, 192, 224, 193, 225, 194, 226, 243, 257}
#define CH_ASM \
  "s_mov_b32 s84, " CH_CAP "\n\t" \
  "s_mov_b32 s83, " CH_CAP "\n\t" \
  "s_mov_b32 s68, 0\n\t" \
  "s_mov_b32 s74, 0\n\t" \
  "s_mov_b32 s76, 0\n\t" \
  "s_mov_b32 s77, 0\n\t" \
  "s_mov_b32 s78, 0\n\t" \
  "s_mov_b32 s79, 0\n\t" \
  "s_mov_b32 s56, 0xcccccccc\n\t" \
  "s_mov_b32 s57, 0xcccccccc\n\t" \
  "s_mov_b32 s58, 0x33333333\n\t" \
  "s_mov_b32 s59, 0x33333333\n\t" \
  "s_mov_b32 s24, 0x01010101\n\t" \
  "s_mov_b32 s25, 0\n\t" \
  "s_mov_b32 s88, 0x3f803f80\n\t" \
  "s_mov_b32 s89, 0x3f803f80\n\t" \
  "s_mov_b32 s66, s65\n\t" \
  "s_mov_b32 s47, s65\n\t" \
  "s_mov_b32 s72, 0\n\t" \
  "s_mov_b32 s73, 0\n\t" \
  "s_mov_b32 s67, 0\n\t" \
  "s_mul_i32 s60, s40, 36864\n\t" \
  "s_add_u32 s60, s60, 16384\n\t" \
  "v_add_u32 v70, s60, v69\n\t" \
  "v_bfe_i32 v68, v69, 5, 1\n\t" \
  "v_and_b32 v68, 0xaaaaaaaa, v68\n\t" \
  "v_or_b32 v68, 0x44444444, v68\n\t" \
  "v_and_b32 v73, 32, v69\n\t" \
  "v_lshl_add_u32 v73, v73, 5, v69\n\t" \
  "LLOAD_%=:\n\t" \
  "v_lshrrev_b32 v74, 2, v69\n\t" \
  "s_mov_b32 s60, " CH_CTAB "\n\t" \
  "buffer_load_dword v72, v74, s[28:31], s60 offen offset:88 sc1\n\t" \
  "buffer_load_dword v74, v74, s[28:31], s60 offen sc1\n\t" \
  "v_lshrrev_b32 v71, 8, v69\n\t" \
  "v_lshlrev_b32 v1, 6, v71\n\t" \
  "v_and_b32 v2, 0xf0, v69\n\t" \
  "v_xor_b32 v1, v1, v2\n\t" \
  "v_lshl_add_u32 v71, v71, 13, v1\n\t" \
  "s_lshl_b32 s60, s40, 17\n\t" \
  "v_add_u32 v71, s60, v71\n\t" \
  "v_lshrrev_b32 v1, 7, v69\n\t" \
  "v_and_b32 v2, 0x70, v69\n\t" \
  "s_movk_i32 s60, 1152\n\t" \
  "v_mad_u32_u24 v75, v1, s60, v2\n\t" \
  "s_lshl_b32 s54, s40, 12\n\t" \
  "s_lshl_b32 s55, s40, 7\n\t" \
  "s_mul_i32 s75, s40, 36864\n\t" \
  "s_add_u32 s75, s75, 1024\n\t" \
  "s_mov_b32 s62, 0x05040100\n\t" \
  "s_mov_b32 s63, 0x07060302\n\t" \
  "s_barrier\n\t" \
  "s_xor_b32 s61, s40, 0\n\t" \
  "s_mul_i32 s61, s61, 36864\n\t" \
  "s_add_u32 s61, s61, s55\n\t" \
  "s_add_u32 m0, s54, 0\n\t" \
  "s_add_u32 s60, s61, 0\n\t" \
  "buffer_load_dwordx4 v75, %[Wo], s60 offen lds\n\t" \
  "s_add_u32 m0, s54, 1024\n\t" \
  "s_add_u32 s60, s61, 9216\n\t" \
  "buffer_load_dwordx4 v75, %[Wo], s60 offen lds\n\t" \
  "s_add_u32 m0, s54, 2048\n\t" \
  "s_add_u32 s60, s61, 18432\n\t" \
  "buffer_load_dwordx4 v75, %[Wo], s60 offen lds\n\t" \
  "s_add_u32 m0, s54, 3072\n\t" \
  "s_add_u32 s60, s61, 27648\n\t" \
  "buffer_load_dwordx4 v75, %[Wo], s60 offen lds\n\t" \
  "s_add_u32 m0, s54, 16384\n\t" \
  "s_mov_b32 s60, 0\n\t" \
  "buffer_load_dwordx4 v71, %[Wq], s60 offen lds\n\t" \
  "s_add_u32 m0, s54, 17408\n\t" \
  "s_mov_b32 s60, 32768\n\t" \
  "buffer_load_dwordx4 v71, %[Wq], s60 offen lds\n\t" \
  "s_add_u32 m0, s54, 18432\n\t" \
  "s_mov_b32 s60, 65536\n\t" \
  "buffer_load_dwordx4 v71, %[Wq], s60 offen lds\n\t" \
  "s_add_u32 m0, s54, 19456\n\t" \
  "s_mov_b32 s60, 98304\n\t" \
  "buffer_load_dwordx4 v71, %[Wq], s60 offen lds\n\t" \
  "s_add_u32 m0, s54, 53248\n\t" \
  "s_mov_b32 s60, 0\n\t" \
  "buffer_load_dwordx4 v71, %[Wk], s60 offen lds\n\t" \
  "s_add_u32 m0, s54, 54272\n\t" \
  "s_mov_b32 s60, 32768\n\t" \
  "buffer_load_dwordx4 v71, %[Wk], s60 offen lds\n\t" \
  "s_add_u32 m0, s54, 55296\n\t" \
  "s_mov_b32 s60, 65536\n\t" \
  "buffer_load_dwordx4 v71, %[Wk], s60 offen lds\n\t" \
  "s_add_u32 m0, s54, 56320\n\t" \
  "s_mov_b32 s60, 98304\n\t" \
  "buffer_load_dwordx4 v71, %[Wk], s60 offen lds\n\t" \
  "s_xor_b32 s61, s40, 0\n\t" \
  "s_mul_i32 s61, s61, 36864\n\t" \
  "s_add_u32 s61, s61, s55\n\t" \
  "s_add_u32 m0, s54, 90112\n\t" \
  "s_add_u32 s60, s61, 512\n\t" \
  "buffer_load_dwordx4 v75, %[Wo], s60 offen lds\n\t" \
  "s_add_u32 m0, s54, 91136\n\t" \
  "s_add_u32 s60, s61, 9728\n\t" \
  "buffer_load_dwordx4 v75, %[Wo], s60 offen lds\n\t" \
  "s_add_u32 m0, s54, 92160\n\t" \
  "s_add_u32 s60, s61, 18944\n\t" \
  "buffer_load_dwordx4 v75, %[Wo], s60 offen lds\n\t" \
  "s_add_u32 m0, s54, 93184\n\t" \
  "s_add_u32 s60, s61, 28160\n\t" \
  "buffer_load_dwordx4 v75, %[Wo], s60 offen lds\n\t" \
  "s_add_u32 m0, s54, 126976\n\t" \
  "s_mov_b32 s60, 0\n\t" \
  "buffer_load_dwordx4 v71, %[Wv], s60 offen lds\n\t" \
  "s_add_u32 m0, s54, 128000\n\t" \
  "s_mov_b32 s60, 32768\n\t" \
  "buffer_load_dwordx4 v71, %[Wv], s60 offen lds\n\t" \
  "s_add_u32 m0, s54, 129024\n\t" \
  "s_mov_b32 s60, 65536\n\t" \
  "buffer_load_dwordx4 v71, %[Wv], s60 offen lds\n\t" \
  "s_add_u32 m0, s54, 130048\n\t" \
  "s_mov_b32 s60, 98304\n\t" \
  "buffer_load_dwordx4 v71, %[Wv], s60 offen lds\n\t" \
  "v_and_b32 v1, 0x300, v69\n\t" \
  "v_lshlrev_b32 v65, 3, v1\n\t" \
  "v_and_b32 v1, 0xc0, v69\n\t" \
  "v_lshl_add_u32 v65, v1, 2, v65\n\t" \
  "s_lshl_b32 s60, s40, 6\n\t" \
  "v_xor_b32 v1, s60, v1\n\t" \
  "v_and_b32 v2, 48, v69\n\t" \
  "v_add3_u32 v65, v65, v1, v2\n\t" \
  "v_lshrrev_b32 v2, 1, v2\n\t" \
  "v_sub_u32 v66, v65, v2\n\t" \
  "v_bfe_u32 v0, v69, 8, 1\n\t" \
  "v_bfe_u32 v1, v69, 9, 1\n\t" \
  "v_lshlrev_b32 v0, 2, v0\n\t" \
  "v_add_u32 v0, v0, v1\n\t" \
  "v_lshlrev_b32 v0, 9, v0\n\t" \
  "v_lshrrev_b32 v1, 1, v69\n\t" \
  "v_and_b32 v1, 0x60, v1\n\t" \
  "v_and_b32 v2, 16, v69\n\t" \
  "v_bfe_u32 v3, v69, 5, 1\n\t" \
  "v_add3_u32 v64, v0, v1, v2\n\t" \
  "v_lshl_add_u32 v64, v3, 2, v64\n\t" \
  "v_add_u32 v64, s54, v64\n\t" \
  "v_lshl_add_u32 v67, v3, 3, v0\n\t" \
  "v_bfe_u32 v1, v69, 7, 1\n\t" \
  "v_lshl_add_u32 v67, v1, 2, v67\n\t" \
  "v_bfe_u32 v1, v69, 6, 1\n\t" \
  "v_lshl_add_u32 v67, v1, 4, v67\n\t" \
  "v_bfe_u32 v1, v69, 4, 1\n\t" \
  "v_lshl_add_u32 v67, v1, 1, v67\n\t" \
  "s_lshl_b32 s60, s40, 5\n\t" \
  "v_add_u32 v67, s60, v67\n\t" \
  "v_add_u32 v78, 0, v64\n\t" \
  "v_add_u32 v79, 1024, v64\n\t" \
  "s_waitcnt vmcnt(16) lgkmcnt(0)\n\t" \
  "s_barrier\n\t" \
  "ds_read2_b32 v[80:81], v78 offset0:0 offset1:2\n\t" \
  "ds_read2_b32 v[82:83], v78 offset0:32 offset1:34\n\t" \
  "ds_read2_b32 v[84:85], v78 offset0:64 offset1:66\n\t" \
  "ds_read2_b32 v[86:87], v78 offset0:96 offset1:98\n\t" \
  "ds_read2_b32 v[88:89], v79 offset0:0 offset1:2\n\t" \
  "ds_read2_b32 v[90:91], v79 offset0:32 offset1:34\n\t" \
  "ds_read2_b32 v[92:93], v79 offset0:64 offset1:66\n\t" \
  "ds_read2_b32 v[94:95], v79 offset0:96 offset1:98\n\t" \
  "s_waitcnt lgkmcnt(6)\n\t" \
  "v_perm_b32 v112, v82, v80, s62\n\t" \
  "v_perm_b32 v113, v82, v80, s63\n\t" \
  "s_waitcnt lgkmcnt(4)\n\t" \
  "v_perm_b32 v114, v86, v84, s62\n\t" \
  "v_perm_b32 v115, v86, v84, s63\n\t" \
  "s_waitcnt lgkmcnt(2)\n\t" \
  "v_perm_b32 v116, v90, v88, s62\n\t" \
  "v_perm_b32 v117, v90, v88, s63\n\t" \
  "s_waitcnt lgkmcnt(0)\n\t" \
  "v_perm_b32 v118, v94, v92, s62\n\t" \
  "v_perm_b32 v119, v94, v92, s63\n\t" \
  "ds_write_b128 v70, v[112:115] offset:20480\n\t" \
  "ds_write_b128 v70, v[116:119] offset:21504\n\t" \
  "v_perm_b32 v120, v83, v81, s62\n\t" \
  "v_perm_b32 v121, v83, v81, s63\n\t" \
  "v_perm_b32 v122, v87, v85, s62\n\t" \
  "v_perm_b32 v123, v87, v85, s63\n\t" \
  "v_perm_b32 v124, v91, v89, s62\n\t" \
  "v_perm_b32 v125, v91, v89, s63\n\t" \
  "v_perm_b32 v126, v95, v93, s62\n\t" \
  "v_perm_b32 v127, v95, v93, s63\n\t" \
  "ds_write_b128 v70, v[120:123] offset:16384\n\t" \
  "ds_write_b128 v70, v[124:127] offset:17408\n\t" \
  "v_add_u32 v78, 16384, v65\n\t" \
  "s_waitcnt vmcnt(12) lgkmcnt(0)\n\t" \
  "s_barrier\n\t" \
  "ds_read_b64_tr_b16 v[0:1], v78 offset:0\n\t" \
  "ds_read_b64_tr_b16 v[2:3], v78 offset:1024\n\t" \
  "ds_read_b64_tr_b16 v[4:5], v78 offset:8192\n\t" \
  "ds_read_b64_tr_b16 v[6:7], v78 offset:9216\n\t" \
  "ds_read_b64_tr_b16 v[8:9], v78 offset:8\n\t" \
  "ds_read_b64_tr_b16 v[10:11], v78 offset:1032\n\t" \
  "ds_read_b64_tr_b16 v[12:13], v78 offset:8200\n\t" \
  "ds_read_b64_tr_b16 v[14:15], v78 offset:9224\n\t" \
  "s_add_u32 m0, s54, 0\n\t" \
  "s_mov_b32 s60, 524288\n\t" \
  "buffer_load_dwordx4 v71, %[Wq], s60 offen lds\n\t" \
  "s_add_u32 m0, s54, 1024\n\t" \
  "s_mov_b32 s60, 557056\n\t" \
  "buffer_load_dwordx4 v71, %[Wq], s60 offen lds\n\t" \
  "s_add_u32 m0, s54, 2048\n\t" \
  "s_mov_b32 s60, 589824\n\t" \
  "buffer_load_dwordx4 v71, %[Wq], s60 offen lds\n\t" \
  "s_add_u32 m0, s54, 3072\n\t" \
  "s_mov_b32 s60, 622592\n\t" \
  "buffer_load_dwordx4 v71, %[Wq], s60 offen lds\n\t" \
  "v_add_u32 v78, 53248, v65\n\t" \
  "s_waitcnt vmcnt(12) lgkmcnt(0)\n\t" \
  "s_barrier\n\t" \
  "ds_read_b64_tr_b16 a[0:1], v78 offset:0\n\t" \
  "ds_read_b64_tr_b16 a[2:3], v78 offset:1024\n\t" \
  "ds_read_b64_tr_b16 a[4:5], v78 offset:8192\n\t" \
  "ds_read_b64_tr_b16 a[6:7], v78 offset:9216\n\t" \
  "ds_read_b64_tr_b16 a[8:9], v78 offset:8\n\t" \
  "ds_read_b64_tr_b16 a[10:11], v78 offset:1032\n\t" \
  "ds_read_b64_tr_b16 a[12:13], v78 offset:8200\n\t" \
  "ds_read_b64_tr_b16 a[14:15], v78 offset:9224\n\t" \
  "s_xor_b32 s61, s40, 1\n\t" \
  "s_mul_i32 s61, s61, 36864\n\t" \
  "s_add_u32 s61, s61, s55\n\t" \
  "s_add_u32 m0, s54, 16384\n\t" \
  "s_add_u32 s60, s61, 0\n\t" \
  "buffer_load_dwordx4 v75, %[Wo], s60 offen lds\n\t" \
  "s_add_u32 m0, s54, 17408\n\t" \
  "s_add_u32 s60, s61, 9216\n\t" \
  "buffer_load_dwordx4 v75, %[Wo], s60 offen lds\n\t" \
  "s_add_u32 m0, s54, 18432\n\t" \
  "s_add_u32 s60, s61, 18432\n\t" \
  "buffer_load_dwordx4 v75, %[Wo], s60 offen lds\n\t" \
  "s_add_u32 m0, s54, 19456\n\t" \
  "s_add_u32 s60, s61, 27648\n\t" \
  "buffer_load_dwordx4 v75, %[Wo], s60 offen lds\n\t" \
  "v_add_u32 v78, 90112, v64\n\t" \
  "v_add_u32 v79, 91136, v64\n\t" \
  "s_waitcnt vmcnt(12) lgkmcnt(0)\n\t" \
  "s_barrier\n\t" \
  "ds_read2_b32 v[80:81], v78 offset0:0 offset1:2\n\t" \
  "ds_read2_b32 v[82:83], v78 offset0:32 offset1:34\n\t" \
  "ds_read2_b32 v[84:85], v78 offset0:64 offset1:66\n\t" \
  "ds_read2_b32 v[86:87], v78 offset0:96 offset1:98\n\t" \
  "ds_read2_b32 v[88:89], v79 offset0:0 offset1:2\n\t" \
  "ds_read2_b32 v[90:91], v79 offset0:32 offset1:34\n\t" \
  "ds_read2_b32 v[92:93], v79 offset0:64 offset1:66\n\t" \
  "ds_read2_b32 v[94:95], v79 offset0:96 offset1:98\n\t" \
  "s_add_u32 m0, s54, 53248\n\t" \
  "s_mov_b32 s60, 524288\n\t" \
  "buffer_load_dwordx4 v71, %[Wk], s60 offen lds\n\t" \
  "s_add_u32 m0, s54, 54272\n\t" \
  "s_mov_b32 s60, 557056\n\t" \
  "buffer_load_dwordx4 v71, %[Wk], s60 offen lds\n\t" \
  "s_add_u32 m0, s54, 55296\n\t" \
  "s_mov_b32 s60, 589824\n\t" \
  "buffer_load_dwordx4 v71, %[Wk], s60 offen lds\n\t" \
  "s_add_u32 m0, s54, 56320\n\t" \
  "s_mov_b32 s60, 622592\n\t" \
  "buffer_load_dwordx4 v71, %[Wk], s60 offen lds\n\t" \
  "s_waitcnt lgkmcnt(6)\n\t" \
  "v_perm_b32 v112, v82, v80, s62\n\t" \
  "v_perm_b32 v113, v82, v80, s63\n\t" \
  "s_waitcnt lgkmcnt(4)\n\t" \
  "v_perm_b32 v114, v86, v84, s62\n\t" \
  "v_perm_b32 v115, v86, v84, s63\n\t" \
  "s_waitcnt lgkmcnt(2)\n\t" \
  "v_perm_b32 v116, v90, v88, s62\n\t" \
  "v_perm_b32 v117, v90, v88, s63\n\t" \
  "s_waitcnt lgkmcnt(0)\n\t" \
  "v_perm_b32 v118, v94, v92, s62\n\t" \
  "v_perm_b32 v119, v94, v92, s63\n\t" \
  "ds_write_b128 v70, v[112:115] offset:22528\n\t" \
  "ds_write_b128 v70, v[116:119] offset:23552\n\t" \
  "v_perm_b32 v120, v83, v81, s62\n\t" \
  "v_perm_b32 v121, v83, v81, s63\n\t" \
  "v_perm_b32 v122, v87, v85, s62\n\t" \
  "v_perm_b32 v123, v87, v85, s63\n\t" \
  "v_perm_b32 v124, v91, v89, s62\n\t" \
  "v_perm_b32 v125, v91, v89, s63\n\t" \
  "v_perm_b32 v126, v95, v93, s62\n\t" \
  "v_perm_b32 v127, v95, v93, s63\n\t" \
  "ds_write_b128 v70, v[120:123] offset:18432\n\t" \
  "ds_write_b128 v70, v[124:127] offset:19456\n\t" \
  "v_add_u32 v78, 126976, v66\n\t" \
  "s_waitcnt vmcnt(12) lgkmcnt(0)\n\t" \
  "s_barrier\n\t" \
  "ds_read_b64_tr_b16 a[16:17], v78 offset:0\n\t" \
  "ds_read_b64_tr_b16 a[18:19], v78 offset:1024\n\t" \
  "ds_read_b64_tr_b16 a[20:21], v78 offset:8192\n\t" \
  "ds_read_b64_tr_b16 a[22:23], v78 offset:9216\n\t" \
  "ds_read_b64_tr_b16 a[24:25], v78 offset:32\n\t" \
  "ds_read_b64_tr_b16 a[26:27], v78 offset:1056\n\t" \
  "ds_read_b64_tr_b16 a[28:29], v78 offset:8224\n\t" \
  "ds_read_b64_tr_b16 a[30:31], v78 offset:9248\n\t" \
  "s_add_u32 m0, s54, 90112\n\t" \
  "s_mov_b32 s60, 524288\n\t" \
  "buffer_load_dwordx4 v71, %[Wv], s60 offen lds\n\t" \
  "s_add_u32 m0, s54, 91136\n\t" \
  "s_mov_b32 s60, 557056\n\t" \
  "buffer_load_dwordx4 v71, %[Wv], s60 offen lds\n\t" \
  "s_add_u32 m0, s54, 92160\n\t" \
  "s_mov_b32 s60, 589824\n\t" \
  "buffer_load_dwordx4 v71, %[Wv], s60 offen lds\n\t" \
  "s_add_u32 m0, s54, 93184\n\t" \
  "s_mov_b32 s60, 622592\n\t" \
  "buffer_load_dwordx4 v71, %[Wv], s60 offen lds\n\t" \
  "v_add_u32 v78, 0, v65\n\t" \
  "s_waitcnt vmcnt(12) lgkmcnt(0)\n\t" \
  "s_barrier\n\t" \
  "ds_read_b64_tr_b16 a[32:33], v78 offset:0\n\t" \
  "ds_read_b64_tr_b16 a[34:35], v78 offset:1024\n\t" \
  "ds_read_b64_tr_b16 a[36:37], v78 offset:8192\n\t" \
  "ds_read_b64_tr_b16 a[38:39], v78 offset:9216\n\t" \
  "ds_read_b64_tr_b16 a[40:41], v78 offset:8\n\t" \
  "ds_read_b64_tr_b16 a[42:43], v78 offset:1032\n\t" \
  "ds_read_b64_tr_b16 a[44:45], v78 offset:8200\n\t" \
  "ds_read_b64_tr_b16 a[46:47], v78 offset:9224\n\t" \
  "s_xor_b32 s61, s40, 1\n\t" \
  "s_mul_i32 s61, s61, 36864\n\t" \
  "s_add_u32 s61, s61, s55\n\t" \
  "s_add_u32 m0, s54, 126976\n\t" \
  "s_add_u32 s60, s61, 512\n\t" \
  "buffer_load_dwordx4 v75, %[Wo], s60 offen lds\n\t" \
  "s_add_u32 m0, s54, 128000\n\t" \
  "s_add_u32 s60, s61, 9728\n\t" \
  "buffer_load_dwordx4 v75, %[Wo], s60 offen lds\n\t" \
  "s_add_u32 m0, s54, 129024\n\t" \
  "s_add_u32 s60, s61, 18944\n\t" \
  "buffer_load_dwordx4 v75, %[Wo], s60 offen lds\n\t" \
  "s_add_u32 m0, s54, 130048\n\t" \
  "s_add_u32 s60, s61, 28160\n\t" \
  "buffer_load_dwordx4 v75, %[Wo], s60 offen lds\n\t" \
  "v_add_u32 v78, 16384, v64\n\t" \
  "v_add_u32 v79, 17408, v64\n\t" \
  "s_waitcnt vmcnt(12) lgkmcnt(0)\n\t" \
  "s_barrier\n\t" \
  "ds_read2_b32 v[80:81], v78 offset0:0 offset1:2\n\t" \
  "ds_read2_b32 v[82:83], v78 offset0:32 offset1:34\n\t" \
  "ds_read2_b32 v[84:85], v78 offset0:64 offset1:66\n\t" \
  "ds_read2_b32 v[86:87], v78 offset0:96 offset1:98\n\t" \
  "ds_read2_b32 v[88:89], v79 offset0:0 offset1:2\n\t" \
  "ds_read2_b32 v[90:91], v79 offset0:32 offset1:34\n\t" \
  "ds_read2_b32 v[92:93], v79 offset0:64 offset1:66\n\t" \
  "ds_read2_b32 v[94:95], v79 offset0:96 offset1:98\n\t" \
  "s_add_u32 m0, s54, 0\n\t" \
  "s_mov_b32 s60, 1048576\n\t" \
  "buffer_load_dwordx4 v71, %[Wq], s60 offen lds\n\t" \
  "s_add_u32 m0, s54, 1024\n\t" \
  "s_mov_b32 s60, 1081344\n\t" \
  "buffer_load_dwordx4 v71, %[Wq], s60 offen lds\n\t" \
  "s_add_u32 m0, s54, 2048\n\t" \
  "s_mov_b32 s60, 1114112\n\t" \
  "buffer_load_dwordx4 v71, %[Wq], s60 offen lds\n\t" \
  "s_add_u32 m0, s54, 3072\n\t" \
  "s_mov_b32 s60, 1146880\n\t" \
  "buffer_load_dwordx4 v71, %[Wq], s60 offen lds\n\t" \
  "s_waitcnt lgkmcnt(6)\n\t" \
  "v_perm_b32 v112, v82, v80, s62\n\t" \
  "v_perm_b32 v113, v82, v80, s63\n\t" \
  "s_waitcnt lgkmcnt(4)\n\t" \
  "v_perm_b32 v114, v86, v84, s62\n\t" \
  "v_perm_b32 v115, v86, v84, s63\n\t" \
  "s_waitcnt lgkmcnt(2)\n\t" \
  "v_perm_b32 v116, v90, v88, s62\n\t" \
  "v_perm_b32 v117, v90, v88, s63\n\t" \
  "s_waitcnt lgkmcnt(0)\n\t" \
  "v_perm_b32 v118, v94, v92, s62\n\t" \
  "v_perm_b32 v119, v94, v92, s63\n\t" \
  "ds_write_b128 v70, v[112:115] offset:24576\n\t" \
  "ds_write_b128 v70, v[116:119] offset:25600\n\t" \
  "v_perm_b32 v200, v83, v81, s62\n\t" \
  "v_perm_b32 v201, v83, v81, s63\n\t" \
  "v_perm_b32 v202, v87, v85, s62\n\t" \
  "v_perm_b32 v203, v87, v85, s63\n\t" \
  "v_perm_b32 v204, v91, v89, s62\n\t" \
  "v_perm_b32 v205, v91, v89, s63\n\t" \
  "v_perm_b32 v206, v95, v93, s62\n\t" \
  "v_perm_b32 v207, v95, v93, s63\n\t" \
  "v_add_u32 v78, 53248, v65\n\t" \
  "s_waitcnt vmcnt(12) lgkmcnt(0)\n\t" \
  "s_barrier\n\t" \
  "ds_read_b64_tr_b16 a[48:49], v78 offset:0\n\t" \
  "ds_read_b64_tr_b16 a[50:51], v78 offset:1024\n\t" \
  "ds_read_b64_tr_b16 a[52:53], v78 offset:8192\n\t" \
  "ds_read_b64_tr_b16 a[54:55], v78 offset:9216\n\t" \
  "ds_read_b64_tr_b16 a[56:57], v78 offset:8\n\t" \
  "ds_read_b64_tr_b16 a[58:59], v78 offset:1032\n\t" \
  "ds_read_b64_tr_b16 a[60:61], v78 offset:8200\n\t" \
  "ds_read_b64_tr_b16 a[62:63], v78 offset:9224\n\t" \
  "s_add_u32 m0, s54, 16384\n\t" \
  "s_mov_b32 s60, 1048576\n\t" \
  "buffer_load_dwordx4 v71, %[Wk], s60 offen lds\n\t" \
  "s_add_u32 m0, s54, 17408\n\t" \
  "s_mov_b32 s60, 1081344\n\t" \
  "buffer_load_dwordx4 v71, %[Wk], s60 offen lds\n\t" \
  "s_add_u32 m0, s54, 18432\n\t" \
  "s_mov_b32 s60, 1114112\n\t" \
  "buffer_load_dwordx4 v71, %[Wk], s60 offen lds\n\t" \
  "s_add_u32 m0, s54, 19456\n\t" \
  "s_mov_b32 s60, 1146880\n\t" \
  "buffer_load_dwordx4 v71, %[Wk], s60 offen lds\n\t" \
  "v_add_u32 v78, 90112, v66\n\t" \
  "s_waitcnt vmcnt(12) lgkmcnt(0)\n\t" \
  "s_barrier\n\t" \
  "ds_read_b64_tr_b16 a[64:65], v78 offset:0\n\t" \
  "ds_read_b64_tr_b16 a[66:67], v78 offset:1024\n\t" \
  "ds_read_b64_tr_b16 a[68:69], v78 offset:8192\n\t" \
  "ds_read_b64_tr_b16 a[70:71], v78 offset:9216\n\t" \
  "ds_read_b64_tr_b16 a[72:73], v78 offset:32\n\t" \
  "ds_read_b64_tr_b16 a[74:75], v78 offset:1056\n\t" \
  "ds_read_b64_tr_b16 a[76:77], v78 offset:8224\n\t" \
  "ds_read_b64_tr_b16 a[78:79], v78 offset:9248\n\t" \
  "s_xor_b32 s61, s40, 2\n\t" \
  "s_mul_i32 s61, s61, 36864\n\t" \
  "s_add_u32 s61, s61, s55\n\t" \
  "s_add_u32 m0, s54, 53248\n\t" \
  "s_add_u32 s60, s61, 0\n\t" \
  "buffer_load_dwordx4 v75, %[Wo], s60 offen lds\n\t" \
  "s_add_u32 m0, s54, 54272\n\t" \
  "s_add_u32 s60, s61, 9216\n\t" \
  "buffer_load_dwordx4 v75, %[Wo], s60 offen lds\n\t" \
  "s_add_u32 m0, s54, 55296\n\t" \
  "s_add_u32 s60, s61, 18432\n\t" \
  "buffer_load_dwordx4 v75, %[Wo], s60 offen lds\n\t" \
  "s_add_u32 m0, s54, 56320\n\t" \
  "s_add_u32 s60, s61, 27648\n\t" \
  "buffer_load_dwordx4 v75, %[Wo], s60 offen lds\n\t" \
  "v_add_u32 v78, 126976, v64\n\t" \
  "v_add_u32 v79, 128000, v64\n\t" \
  "s_waitcnt vmcnt(12) lgkmcnt(0)\n\t" \
  "s_barrier\n\t" \
  "ds_read2_b32 v[80:81], v78 offset0:0 offset1:2\n\t" \
  "ds_read2_b32 v[82:83], v78 offset0:32 offset1:34\n\t" \
  "ds_read2_b32 v[84:85], v78 offset0:64 offset1:66\n\t" \
  "ds_read2_b32 v[86:87], v78 offset0:96 offset1:98\n\t" \
  "ds_read2_b32 v[88:89], v79 offset0:0 offset1:2\n\t" \
  "ds_read2_b32 v[90:91], v79 offset0:32 offset1:34\n\t" \
  "ds_read2_b32 v[92:93], v79 offset0:64 offset1:66\n\t" \
  "ds_read2_b32 v[94:95], v79 offset0:96 offset1:98\n\t" \
  "s_add_u32 m0, s54, 90112\n\t" \
  "s_mov_b32 s60, 1048576\n\t" \
  "buffer_load_dwordx4 v71, %[Wv], s60 offen lds\n\t" \
  "s_add_u32 m0, s54, 91136\n\t" \
  "s_mov_b32 s60, 1081344\n\t" \
  "buffer_load_dwordx4 v71, %[Wv], s60 offen lds\n\t" \
  "s_add_u32 m0, s54, 92160\n\t" \
  "s_mov_b32 s60, 1114112\n\t" \
  "buffer_load_dwordx4 v71, %[Wv], s60 offen lds\n\t" \
  "s_add_u32 m0, s54, 93184\n\t" \
  "s_mov_b32 s60, 1146880\n\t" \
  "buffer_load_dwordx4 v71, %[Wv], s60 offen lds\n\t" \
  "s_waitcnt lgkmcnt(6)\n\t" \
  "v_perm_b32 v112, v82, v80, s62\n\t" \
  "v_perm_b32 v113, v82, v80, s63\n\t" \
  "s_waitcnt lgkmcnt(4)\n\t" \
  "v_perm_b32 v114, v86, v84, s62\n\t" \
  "v_perm_b32 v115, v86, v84, s63\n\t" \
  "s_waitcnt lgkmcnt(2)\n\t" \
  "v_perm_b32 v116, v90, v88, s62\n\t" \
  "v_perm_b32 v117, v90, v88, s63\n\t" \
  "s_waitcnt lgkmcnt(0)\n\t" \
  "v_perm_b32 v118, v94, v92, s62\n\t" \
  "v_perm_b32 v119, v94, v92, s63\n\t" \
  "ds_write_b128 v70, v[112:115] offset:26624\n\t" \
  "ds_write_b128 v70, v[116:119] offset:27648\n\t" \
  "v_perm_b32 v232, v83, v81, s62\n\t" \
  "v_perm_b32 v233, v83, v81, s63\n\t" \
  "v_perm_b32 v234, v87, v85, s62\n\t" \
  "v_perm_b32 v235, v87, v85, s63\n\t" \
  "v_perm_b32 v236, v91, v89, s62\n\t" \
  "v_perm_b32 v237, v91, v89, s63\n\t" \
  "v_perm_b32 v238, v95, v93, s62\n\t" \
  "v_perm_b32 v239, v95, v93, s63\n\t" \
  "v_add_u32 v78, 0, v65\n\t" \
  "s_waitcnt vmcnt(12) lgkmcnt(0)\n\t" \
  "s_barrier\n\t" \
  "ds_read_b64_tr_b16 a[80:81], v78 offset:0\n\t" \
  "ds_read_b64_tr_b16 a[82:83], v78 offset:1024\n\t" \
  "ds_read_b64_tr_b16 a[84:85], v78 offset:8192\n\t" \
  "ds_read_b64_tr_b16 a[86:87], v78 offset:9216\n\t" \
  "ds_read_b64_tr_b16 a[88:89], v78 offset:8\n\t" \
  "ds_read_b64_tr_b16 a[90:91], v78 offset:1032\n\t" \
  "ds_read_b64_tr_b16 a[92:93], v78 offset:8200\n\t" \
  "ds_read_b64_tr_b16 a[94:95], v78 offset:9224\n\t" \
  "s_add_u32 m0, s54, 126976\n\t" \
  "s_mov_b32 s60, 1572864\n\t" \
  "buffer_load_dwordx4 v71, %[Wq], s60 offen lds\n\t" \
  "s_add_u32 m0, s54, 128000\n\t" \
  "s_mov_b32 s60, 1605632\n\t" \
  "buffer_load_dwordx4 v71, %[Wq], s60 offen lds\n\t" \
  "s_add_u32 m0, s54, 129024\n\t" \
  "s_mov_b32 s60, 1638400\n\t" \
  "buffer_load_dwordx4 v71, %[Wq], s60 offen lds\n\t" \
  "s_add_u32 m0, s54, 130048\n\t" \
  "s_mov_b32 s60, 1671168\n\t" \
  "buffer_load_dwordx4 v71, %[Wq], s60 offen lds\n\t" \
  "v_add_u32 v78, 16384, v65\n\t" \
  "s_waitcnt vmcnt(12) lgkmcnt(0)\n\t" \
  "s_barrier\n\t" \
  "ds_read_b64_tr_b16 v[32:33], v78 offset:0\n\t" \
  "ds_read_b64_tr_b16 v[34:35], v78 offset:1024\n\t" \
  "ds_read_b64_tr_b16 v[36:37], v78 offset:8192\n\t" \
  "ds_read_b64_tr_b16 v[38:39], v78 offset:9216\n\t" \
  "ds_read_b64_tr_b16 a[96:97], v78 offset:8\n\t" \
  "ds_read_b64_tr_b16 a[98:99], v78 offset:1032\n\t" \
  "ds_read_b64_tr_b16 a[100:101], v78 offset:8200\n\t" \
  "ds_read_b64_tr_b16 a[102:103], v78 offset:9224\n\t" \
  "s_xor_b32 s61, s40, 2\n\t" \
  "s_mul_i32 s61, s61, 36864\n\t" \
  "s_add_u32 s61, s61, s55\n\t" \
  "s_add_u32 m0, s54, 0\n\t" \
  "s_add_u32 s60, s61, 512\n\t" \
  "buffer_load_dwordx4 v75, %[Wo], s60 offen lds\n\t" \
  "s_add_u32 m0, s54, 1024\n\t" \
  "s_add_u32 s60, s61, 9728\n\t" \
  "buffer_load_dwordx4 v75, %[Wo], s60 offen lds\n\t" \
  "s_add_u32 m0, s54, 2048\n\t" \
  "s_add_u32 s60, s61, 18944\n\t" \
  "buffer_load_dwordx4 v75, %[Wo], s60 offen lds\n\t" \
  "s_add_u32 m0, s54, 3072\n\t" \
  "s_add_u32 s60, s61, 28160\n\t" \
  "buffer_load_dwordx4 v75, %[Wo], s60 offen lds\n\t" \
  "v_add_u32 v78, 53248, v64\n\t" \
  "v_add_u32 v79, 54272, v64\n\t" \
  "s_waitcnt vmcnt(12) lgkmcnt(0)\n\t" \
  "s_barrier\n\t" \
  "ds_read2_b32 v[80:81], v78 offset0:0 offset1:2\n\t" \
  "ds_read2_b32 v[82:83], v78 offset0:32 offset1:34\n\t" \
  "ds_read2_b32 v[84:85], v78 offset0:64 offset1:66\n\t" \
  "ds_read2_b32 v[86:87], v78 offset0:96 offset1:98\n\t" \
  "ds_read2_b32 v[88:89], v79 offset0:0 offset1:2\n\t" \
  "ds_read2_b32 v[90:91], v79 offset0:32 offset1:34\n\t" \
  "ds_read2_b32 v[92:93], v79 offset0:64 offset1:66\n\t" \
  "ds_read2_b32 v[94:95], v79 offset0:96 offset1:98\n\t" \
  "s_add_u32 m0, s54, 16384\n\t" \
  "s_mov_b32 s60, 1572864\n\t" \
  "buffer_load_dwordx4 v71, %[Wk], s60 offen lds\n\t" \
  "s_add_u32 m0, s54, 17408\n\t" \
  "s_mov_b32 s60, 1605632\n\t" \
  "buffer_load_dwordx4 v71, %[Wk], s60 offen lds\n\t" \
  "s_add_u32 m0, s54, 18432\n\t" \
  "s_mov_b32 s60, 1638400\n\t" \
  "buffer_load_dwordx4 v71, %[Wk], s60 offen lds\n\t" \
  "s_add_u32 m0, s54, 19456\n\t" \
  "s_mov_b32 s60, 1671168\n\t" \
  "buffer_load_dwordx4 v71, %[Wk], s60 offen lds\n\t" \
  "s_waitcnt lgkmcnt(6)\n\t" \
  "v_perm_b32 v112, v82, v80, s62\n\t" \
  "v_perm_b32 v113, v82, v80, s63\n\t" \
  "s_waitcnt lgkmcnt(4)\n\t" \
  "v_perm_b32 v114, v86, v84, s62\n\t" \
  "v_perm_b32 v115, v86, v84, s63\n\t" \
  "s_waitcnt lgkmcnt(2)\n\t" \
  "v_perm_b32 v116, v90, v88, s62\n\t" \
  "v_perm_b32 v117, v90, v88, s63\n\t" \
  "s_waitcnt lgkmcnt(0)\n\t" \
  "v_perm_b32 v118, v94, v92, s62\n\t" \
  "v_perm_b32 v119, v94, v92, s63\n\t" \
  "ds_write_b128 v70, v[112:115] offset:28672\n\t" \
  "ds_write_b128 v70, v[116:119] offset:29696\n\t" \
  "v_perm_b32 v208, v83, v81, s62\n\t" \
  "v_perm_b32 v209, v83, v81, s63\n\t" \
  "v_perm_b32 v210, v87, v85, s62\n\t" \
  "v_perm_b32 v211, v87, v85, s63\n\t" \
  "v_perm_b32 v212, v91, v89, s62\n\t" \
  "v_perm_b32 v213, v91, v89, s63\n\t" \
  "v_perm_b32 v214, v95, v93, s62\n\t" \
  "v_perm_b32 v215, v95, v93, s63\n\t" \
  "v_add_u32 v78, 90112, v66\n\t" \
  "s_waitcnt vmcnt(12) lgkmcnt(0)\n\t" \
  "s_barrier\n\t" \
  "ds_read_b64_tr_b16 a[104:105], v78 offset:0\n\t" \
  "ds_read_b64_tr_b16 a[106:107], v78 offset:1024\n\t" \
  "ds_read_b64_tr_b16 a[108:109], v78 offset:8192\n\t" \
  "ds_read_b64_tr_b16 a[110:111], v78 offset:9216\n\t" \
  "ds_read_b64_tr_b16 a[112:113], v78 offset:32\n\t" \
  "ds_read_b64_tr_b16 a[114:115], v78 offset:1056\n\t" \
  "ds_read_b64_tr_b16 a[116:117], v78 offset:8224\n\t" \
  "ds_read_b64_tr_b16 a[118:119], v78 offset:9248\n\t" \
  "s_add_u32 m0, s54, 53248\n\t" \
  "s_mov_b32 s60, 1572864\n\t" \
  "buffer_load_dwordx4 v71, %[Wv], s60 offen lds\n\t" \
  "s_add_u32 m0, s54, 54272\n\t" \
  "s_mov_b32 s60, 1605632\n\t" \
  "buffer_load_dwordx4 v71, %[Wv], s60 offen lds\n\t" \
  "s_add_u32 m0, s54, 55296\n\t" \
  "s_mov_b32 s60, 1638400\n\t" \
  "buffer_load_dwordx4 v71, %[Wv], s60 offen lds\n\t" \
  "s_add_u32 m0, s54, 56320\n\t" \
  "s_mov_b32 s60, 1671168\n\t" \
  "buffer_load_dwordx4 v71, %[Wv], s60 offen lds\n\t" \
  "v_add_u32 v78, 126976, v65\n\t" \
  "s_waitcnt vmcnt(12) lgkmcnt(0)\n\t" \
  "s_barrier\n\t" \
  "ds_read_b64_tr_b16 a[120:121], v78 offset:0\n\t" \
  "ds_read_b64_tr_b16 a[122:123], v78 offset:1024\n\t" \
  "ds_read_b64_tr_b16 a[124:125], v78 offset:8192\n\t" \
  "ds_read_b64_tr_b16 a[126:127], v78 offset:9216\n\t" \
  "ds_read_b64_tr_b16 v[16:17], v78 offset:8\n\t" \
  "ds_read_b64_tr_b16 v[18:19], v78 offset:1032\n\t" \
  "ds_read_b64_tr_b16 v[20:21], v78 offset:8200\n\t" \
  "ds_read_b64_tr_b16 v[22:23], v78 offset:9224\n\t" \
  "s_xor_b32 s61, s40, 3\n\t" \
  "s_mul_i32 s61, s61, 36864\n\t" \
  "s_add_u32 s61, s61, s55\n\t" \
  "s_add_u32 m0, s54, 90112\n\t" \
  "s_add_u32 s60, s61, 0\n\t" \
  "buffer_load_dwordx4 v75, %[Wo], s60 offen lds\n\t" \
  "s_add_u32 m0, s54, 91136\n\t" \
  "s_add_u32 s60, s61, 9216\n\t" \
  "buffer_load_dwordx4 v75, %[Wo], s60 offen lds\n\t" \
  "s_add_u32 m0, s54, 92160\n\t" \
  "s_add_u32 s60, s61, 18432\n\t" \
  "buffer_load_dwordx4 v75, %[Wo], s60 offen lds\n\t" \
  "s_add_u32 m0, s54, 93184\n\t" \
  "s_add_u32 s60, s61, 27648\n\t" \
  "buffer_load_dwordx4 v75, %[Wo], s60 offen lds\n\t" \
  "v_add_u32 v78, 0, v64\n\t" \
  "v_add_u32 v79, 1024, v64\n\t" \
  "s_waitcnt vmcnt(12) lgkmcnt(0)\n\t" \
  "s_barrier\n\t" \
  "ds_read2_b32 v[80:81], v78 offset0:0 offset1:2\n\t" \
  "ds_read2_b32 v[82:83], v78 offset0:32 offset1:34\n\t" \
  "ds_read2_b32 v[84:85], v78 offset0:64 offset1:66\n\t" \
  "ds_read2_b32 v[86:87], v78 offset0:96 offset1:98\n\t" \
  "ds_read2_b32 v[88:89], v79 offset0:0 offset1:2\n\t" \
  "ds_read2_b32 v[90:91], v79 offset0:32 offset1:34\n\t" \
  "ds_read2_b32 v[92:93], v79 offset0:64 offset1:66\n\t" \
  "ds_read2_b32 v[94:95], v79 offset0:96 offset1:98\n\t" \
  "s_add_u32 m0, s54, 126976\n\t" \
  "s_mov_b32 s60, 2097152\n\t" \
  "buffer_load_dwordx4 v71, %[Wq], s60 offen lds\n\t" \
  "s_add_u32 m0, s54, 128000\n\t" \
  "s_mov_b32 s60, 2129920\n\t" \
  "buffer_load_dwordx4 v71, %[Wq], s60 offen lds\n\t" \
  "s_add_u32 m0, s54, 129024\n\t" \
  "s_mov_b32 s60, 2162688\n\t" \
  "buffer_load_dwordx4 v71, %[Wq], s60 offen lds\n\t" \
  "s_add_u32 m0, s54, 130048\n\t" \
  "s_mov_b32 s60, 2195456\n\t" \
  "buffer_load_dwordx4 v71, %[Wq], s60 offen lds\n\t" \
  "s_waitcnt lgkmcnt(6)\n\t" \
  "v_perm_b32 v112, v82, v80, s62\n\t" \
  "v_perm_b32 v113, v82, v80, s63\n\t" \
  "s_waitcnt lgkmcnt(4)\n\t" \
  "v_perm_b32 v114, v86, v84, s62\n\t" \
  "v_perm_b32 v115, v86, v84, s63\n\t" \
  "s_waitcnt lgkmcnt(2)\n\t" \
  "v_perm_b32 v116, v90, v88, s62\n\t" \
  "v_perm_b32 v117, v90, v88, s63\n\t" \
  "s_waitcnt lgkmcnt(0)\n\t" \
  "v_perm_b32 v118, v94, v92, s62\n\t" \
  "v_perm_b32 v119, v94, v92, s63\n\t" \
  "ds_write_b128 v70, v[112:115] offset:30720\n\t" \
  "ds_write_b128 v70, v[116:119] offset:31744\n\t" \
  "v_perm_b32 v240, v83, v81, s62\n\t" \
  "v_perm_b32 v241, v83, v81, s63\n\t" \
  "v_perm_b32 v242, v87, v85, s62\n\t" \
  "v_perm_b32 v243, v87, v85, s63\n\t" \
  "v_perm_b32 v244, v91, v89, s62\n\t" \
  "v_perm_b32 v245, v91, v89, s63\n\t" \
  "v_perm_b32 v246, v95, v93, s62\n\t" \
  "v_perm_b32 v247, v95, v93, s63\n\t" \
  "v_add_u32 v78, 16384, v65\n\t" \
  "s_waitcnt vmcnt(12) lgkmcnt(0)\n\t" \
  "s_barrier\n\t" \
  "ds_read_b64_tr_b16 a[128:129], v78 offset:0\n\t" \
  "ds_read_b64_tr_b16 a[130:131], v78 offset:1024\n\t" \
  "ds_read_b64_tr_b16 a[132:133], v78 offset:8192\n\t" \
  "ds_read_b64_tr_b16 a[134:135], v78 offset:9216\n\t" \
  "ds_read_b64_tr_b16 a[136:137], v78 offset:8\n\t" \
  "ds_read_b64_tr_b16 a[138:139], v78 offset:1032\n\t" \
  "ds_read_b64_tr_b16 a[140:141], v78 offset:8200\n\t" \
  "ds_read_b64_tr_b16 a[142:143], v78 offset:9224\n\t" \
  "s_add_u32 m0, s54, 0\n\t" \
  "s_mov_b32 s60, 2097152\n\t" \
  "buffer_load_dwordx4 v71, %[Wk], s60 offen lds\n\t" \
  "s_add_u32 m0, s54, 1024\n\t" \
  "s_mov_b32 s60, 2129920\n\t" \
  "buffer_load_dwordx4 v71, %[Wk], s60 offen lds\n\t" \
  "s_add_u32 m0, s54, 2048\n\t" \
  "s_mov_b32 s60, 2162688\n\t" \
  "buffer_load_dwordx4 v71, %[Wk], s60 offen lds\n\t" \
  "s_add_u32 m0, s54, 3072\n\t" \
  "s_mov_b32 s60, 2195456\n\t" \
  "buffer_load_dwordx4 v71, %[Wk], s60 offen lds\n\t" \
  "v_add_u32 v78, 53248, v66\n\t" \
  "s_waitcnt vmcnt(12) lgkmcnt(0)\n\t" \
  "s_barrier\n\t" \
  "ds_read_b64_tr_b16 a[144:145], v78 offset:0\n\t" \
  "ds_read_b64_tr_b16 a[146:147], v78 offset:1024\n\t" \
  "ds_read_b64_tr_b16 a[148:149], v78 offset:8192\n\t" \
  "ds_read_b64_tr_b16 a[150:151], v78 offset:9216\n\t" \
  "ds_read_b64_tr_b16 a[152:153], v78 offset:32\n\t" \
  "ds_read_b64_tr_b16 a[154:155], v78 offset:1056\n\t" \
  "ds_read_b64_tr_b16 a[156:157], v78 offset:8224\n\t" \
  "ds_read_b64_tr_b16 a[158:159], v78 offset:9248\n\t" \
  "s_xor_b32 s61, s40, 3\n\t" \
  "s_mul_i32 s61, s61, 36864\n\t" \
  "s_add_u32 s61, s61, s55\n\t" \
  "s_add_u32 m0, s54, 16384\n\t" \
  "s_add_u32 s60, s61, 512\n\t" \
  "buffer_load_dwordx4 v75, %[Wo], s60 offen lds\n\t" \
  "s_add_u32 m0, s54, 17408\n\t" \
  "s_add_u32 s60, s61, 9728\n\t" \
  "buffer_load_dwordx4 v75, %[Wo], s60 offen lds\n\t" \
  "s_add_u32 m0, s54, 18432\n\t" \
  "s_add_u32 s60, s61, 18944\n\t" \
  "buffer_load_dwordx4 v75, %[Wo], s60 offen lds\n\t" \
  "s_add_u32 m0, s54, 19456\n\t" \
  "s_add_u32 s60, s61, 28160\n\t" \
  "buffer_load_dwordx4 v75, %[Wo], s60 offen lds\n\t" \
  "v_add_u32 v78, 90112, v64\n\t" \
  "v_add_u32 v79, 91136, v64\n\t" \
  "s_waitcnt vmcnt(12) lgkmcnt(0)\n\t" \
  "s_barrier\n\t" \
  "ds_read2_b32 v[80:81], v78 offset0:0 offset1:2\n\t" \
  "ds_read2_b32 v[82:83], v78 offset0:32 offset1:34\n\t" \
  "ds_read2_b32 v[84:85], v78 offset0:64 offset1:66\n\t" \
  "ds_read2_b32 v[86:87], v78 offset0:96 offset1:98\n\t" \
  "ds_read2_b32 v[88:89], v79 offset0:0 offset1:2\n\t" \
  "ds_read2_b32 v[90:91], v79 offset0:32 offset1:34\n\t" \
  "ds_read2_b32 v[92:93], v79 offset0:64 offset1:66\n\t" \
  "ds_read2_b32 v[94:95], v79 offset0:96 offset1:98\n\t" \
  "s_add_u32 m0, s54, 53248\n\t" \
  "s_mov_b32 s60, 2097152\n\t" \
  "buffer_load_dwordx4 v71, %[Wv], s60 offen lds\n\t" \
  "s_add_u32 m0, s54, 54272\n\t" \
  "s_mov_b32 s60, 2129920\n\t" \
  "buffer_load_dwordx4 v71, %[Wv], s60 offen lds\n\t" \
  "s_add_u32 m0, s54, 55296\n\t" \
  "s_mov_b32 s60, 2162688\n\t" \
  "buffer_load_dwordx4 v71, %[Wv], s60 offen lds\n\t" \
  "s_add_u32 m0, s54, 56320\n\t" \
  "s_mov_b32 s60, 2195456\n\t" \
  "buffer_load_dwordx4 v71, %[Wv], s60 offen lds\n\t" \
  "s_waitcnt lgkmcnt(6)\n\t" \
  "v_perm_b32 v192, v82, v80, s62\n\t" \
  "v_perm_b32 v193, v82, v80, s63\n\t" \
  "s_waitcnt lgkmcnt(4)\n\t" \
  "v_perm_b32 v194, v86, v84, s62\n\t" \
  "v_perm_b32 v195, v86, v84, s63\n\t" \
  "s_waitcnt lgkmcnt(2)\n\t" \
  "v_perm_b32 v196, v90, v88, s62\n\t" \
  "v_perm_b32 v197, v90, v88, s63\n\t" \
  "s_waitcnt lgkmcnt(0)\n\t" \
  "v_perm_b32 v198, v94, v92, s62\n\t" \
  "v_perm_b32 v199, v94, v92, s63\n\t" \
  "v_perm_b32 v216, v83, v81, s62\n\t" \
  "v_perm_b32 v217, v83, v81, s63\n\t" \
  "v_perm_b32 v218, v87, v85, s62\n\t" \
  "v_perm_b32 v219, v87, v85, s63\n\t" \
  "v_perm_b32 v220, v91, v89, s62\n\t" \
  "v_perm_b32 v221, v91, v89, s63\n\t" \
  "v_perm_b32 v222, v95, v93, s62\n\t" \
  "v_perm_b32 v223, v95, v93, s63\n\t" \
  "v_add_u32 v78, 126976, v65\n\t" \
  "s_waitcnt vmcnt(12) lgkmcnt(0)\n\t" \
  "s_barrier\n\t" \
  "ds_read_b64_tr_b16 a[160:161], v78 offset:0\n\t" \
  "ds_read_b64_tr_b16 a[162:163], v78 offset:1024\n\t" \
  "ds_read_b64_tr_b16 a[164:165], v78 offset:8192\n\t" \
  "ds_read_b64_tr_b16 a[166:167], v78 offset:9216\n\t" \
  "ds_read_b64_tr_b16 a[168:169], v78 offset:8\n\t" \
  "ds_read_b64_tr_b16 a[170:171], v78 offset:1032\n\t" \
  "ds_read_b64_tr_b16 a[172:173], v78 offset:8200\n\t" \
  "ds_read_b64_tr_b16 a[174:175], v78 offset:9224\n\t" \
  "s_add_u32 m0, s54, 90112\n\t" \
  "s_mov_b32 s60, 2621440\n\t" \
  "buffer_load_dwordx4 v71, %[Wq], s60 offen lds\n\t" \
  "s_add_u32 m0, s54, 91136\n\t" \
  "s_mov_b32 s60, 2654208\n\t" \
  "buffer_load_dwordx4 v71, %[Wq], s60 offen lds\n\t" \
  "s_add_u32 m0, s54, 92160\n\t" \
  "s_mov_b32 s60, 2686976\n\t" \
  "buffer_load_dwordx4 v71, %[Wq], s60 offen lds\n\t" \
  "s_add_u32 m0, s54, 93184\n\t" \
  "s_mov_b32 s60, 2719744\n\t" \
  "buffer_load_dwordx4 v71, %[Wq], s60 offen lds\n\t" \
  "v_add_u32 v78, 0, v65\n\t" \
  "s_waitcnt vmcnt(12) lgkmcnt(0)\n\t" \
  "s_barrier\n\t" \
  "ds_read_b64_tr_b16 a[176:177], v78 offset:0\n\t" \
  "ds_read_b64_tr_b16 a[178:179], v78 offset:1024\n\t" \
  "ds_read_b64_tr_b16 a[180:181], v78 offset:8192\n\t" \
  "ds_read_b64_tr_b16 a[182:183], v78 offset:9216\n\t" \
  "ds_read_b64_tr_b16 a[184:185], v78 offset:8\n\t" \
  "ds_read_b64_tr_b16 a[186:187], v78 offset:1032\n\t" \
  "ds_read_b64_tr_b16 a[188:189], v78 offset:8200\n\t" \
  "ds_read_b64_tr_b16 a[190:191], v78 offset:9224\n\t" \
  "s_add_u32 m0, s54, 126976\n\t" \
  "s_add_u32 s60, s75, 0\n\t" \
  "buffer_load_dwordx4 v75, %[Wo], s60 offen lds\n\t" \
  "s_add_u32 m0, s54, 128000\n\t" \
  "s_add_u32 s60, s75, 9216\n\t" \
  "buffer_load_dwordx4 v75, %[Wo], s60 offen lds\n\t" \
  "s_add_u32 m0, s54, 129024\n\t" \
  "s_add_u32 s60, s75, 18432\n\t" \
  "buffer_load_dwordx4 v75, %[Wo], s60 offen lds\n\t" \
  "s_add_u32 m0, s54, 130048\n\t" \
  "s_add_u32 s60, s75, 27648\n\t" \
  "buffer_load_dwordx4 v75, %[Wo], s60 offen lds\n\t" \
  "v_add_u32 v78, 16384, v64\n\t" \
  "v_add_u32 v79, 17408, v64\n\t" \
  "s_waitcnt vmcnt(12) lgkmcnt(0)\n\t" \
  "s_barrier\n\t" \
  "ds_read2_b32 v[80:81], v78 offset0:0 offset1:2\n\t" \
  "ds_read2_b32 v[82:83], v78 offset0:32 offset1:34\n\t" \
  "ds_read2_b32 v[84:85], v78 offset0:64 offset1:66\n\t" \
  "ds_read2_b32 v[86:87], v78 offset0:96 offset1:98\n\t" \
  "ds_read2_b32 v[88:89], v79 offset0:0 offset1:2\n\t" \
  "ds_read2_b32 v[90:91], v79 offset0:32 offset1:34\n\t" \
  "ds_read2_b32 v[92:93], v79 offset0:64 offset1:66\n\t" \
  "ds_read2_b32 v[94:95], v79 offset0:96 offset1:98\n\t" \
  "s_add_u32 m0, s54, 0\n\t" \
  "s_mov_b32 s60, 2621440\n\t" \
  "buffer_load_dwordx4 v71, %[Wk], s60 offen lds\n\t" \
  "s_add_u32 m0, s54, 1024\n\t" \
  "s_mov_b32 s60, 2654208\n\t" \
  "buffer_load_dwordx4 v71, %[Wk], s60 offen lds\n\t" \
  "s_add_u32 m0, s54, 2048\n\t" \
  "s_mov_b32 s60, 2686976\n\t" \
  "buffer_load_dwordx4 v71, %[Wk], s60 offen lds\n\t" \
  "s_add_u32 m0, s54, 3072\n\t" \
  "s_mov_b32 s60, 2719744\n\t" \
  "buffer_load_dwordx4 v71, %[Wk], s60 offen lds\n\t" \
  "s_waitcnt lgkmcnt(6)\n\t" \
  "v_perm_b32 v224, v82, v80, s62\n\t" \
  "v_perm_b32 v225, v82, v80, s63\n\t" \
  "s_waitcnt lgkmcnt(4)\n\t" \
  "v_perm_b32 v226, v86, v84, s62\n\t" \
  "v_perm_b32 v227, v86, v84, s63\n\t" \
  "s_waitcnt lgkmcnt(2)\n\t" \
  "v_perm_b32 v228, v90, v88, s62\n\t" \
  "v_perm_b32 v229, v90, v88, s63\n\t" \
  "s_waitcnt lgkmcnt(0)\n\t" \
  "v_perm_b32 v230, v94, v92, s62\n\t" \
  "v_perm_b32 v231, v94, v92, s63\n\t" \
  "v_perm_b32 v120, v83, v81, s62\n\t" \
  "v_perm_b32 v121, v83, v81, s63\n\t" \
  "v_perm_b32 v122, v87, v85, s62\n\t" \
  "v_perm_b32 v123, v87, v85, s63\n\t" \
  "v_perm_b32 v124, v91, v89, s62\n\t" \
  "v_perm_b32 v125, v91, v89, s63\n\t" \
  "v_perm_b32 v126, v95, v93, s62\n\t" \
  "v_perm_b32 v127, v95, v93, s63\n\t" \
  "ds_write_b128 v70, v[120:123] offset:32768\n\t" \
  "ds_write_b128 v70, v[124:127] offset:33792\n\t" \
  "v_add_u32 v78, 53248, v66\n\t" \
  "s_waitcnt vmcnt(12) lgkmcnt(0)\n\t" \
  "s_barrier\n\t" \
  "ds_read_b64_tr_b16 a[192:193], v78 offset:0\n\t" \
  "ds_read_b64_tr_b16 a[194:195], v78 offset:1024\n\t" \
  "ds_read_b64_tr_b16 a[196:197], v78 offset:8192\n\t" \
  "ds_read_b64_tr_b16 a[198:199], v78 offset:9216\n\t" \
  "ds_read_b64_tr_b16 v[48:49], v78 offset:32\n\t" \
  "ds_read_b64_tr_b16 v[50:51], v78 offset:1056\n\t" \
  "ds_read_b64_tr_b16 v[52:53], v78 offset:8224\n\t" \
  "ds_read_b64_tr_b16 v[54:55], v78 offset:9248\n\t" \
  "s_add_u32 m0, s54, 16384\n\t" \
  "s_mov_b32 s60, 2621440\n\t" \
  "buffer_load_dwordx4 v71, %[Wv], s60 offen lds\n\t" \
  "s_add_u32 m0, s54, 17408\n\t" \
  "s_mov_b32 s60, 2654208\n\t" \
  "buffer_load_dwordx4 v71, %[Wv], s60 offen lds\n\t" \
  "s_add_u32 m0, s54, 18432\n\t" \
  "s_mov_b32 s60, 2686976\n\t" \
  "buffer_load_dwordx4 v71, %[Wv], s60 offen lds\n\t" \
  "s_add_u32 m0, s54, 19456\n\t" \
  "s_mov_b32 s60, 2719744\n\t" \
  "buffer_load_dwordx4 v71, %[Wv], s60 offen lds\n\t" \
  "v_add_u32 v78, 90112, v65\n\t" \
  "s_waitcnt vmcnt(12) lgkmcnt(0)\n\t" \
  "s_barrier\n\t" \
  "ds_read_b64_tr_b16 a[200:201], v78 offset:0\n\t" \
  "ds_read_b64_tr_b16 a[202:203], v78 offset:1024\n\t" \
  "ds_read_b64_tr_b16 a[204:205], v78 offset:8192\n\t" \
  "ds_read_b64_tr_b16 a[206:207], v78 offset:9216\n\t" \
  "ds_read_b64_tr_b16 v[24:25], v78 offset:8\n\t" \
  "ds_read_b64_tr_b16 v[26:27], v78 offset:1032\n\t" \
  "ds_read_b64_tr_b16 v[28:29], v78 offset:8200\n\t" \
  "ds_read_b64_tr_b16 v[30:31], v78 offset:9224\n\t" \
  "s_add_u32 m0, s54, 53248\n\t" \
  "s_mov_b32 s60, 3145728\n\t" \
  "buffer_load_dwordx4 v71, %[Wq], s60 offen lds\n\t" \
  "s_add_u32 m0, s54, 54272\n\t" \
  "s_mov_b32 s60, 3178496\n\t" \
  "buffer_load_dwordx4 v71, %[Wq], s60 offen lds\n\t" \
  "s_add_u32 m0, s54, 55296\n\t" \
  "s_mov_b32 s60, 3211264\n\t" \
  "buffer_load_dwordx4 v71, %[Wq], s60 offen lds\n\t" \
  "s_add_u32 m0, s54, 56320\n\t" \
  "s_mov_b32 s60, 3244032\n\t" \
  "buffer_load_dwordx4 v71, %[Wq], s60 offen lds\n\t" \
  "v_add_u32 v78, 126976, v67\n\t" \
  "s_waitcnt vmcnt(12) lgkmcnt(0)\n\t" \
  "s_barrier\n\t" \
  "ds_read_u16 v80, v78 offset:0\n\t" \
  "ds_read_u16 v81, v78 offset:128\n\t" \
  "ds_read_u16 v82, v78 offset:4096\n\t" \
  "ds_read_u16 v83, v78 offset:4224\n\t" \
  "ds_read_u16 v84, v78 offset:256\n\t" \
  "ds_read_u16 v85, v78 offset:384\n\t" \
  "ds_read_u16 v86, v78 offset:4352\n\t" \
  "ds_read_u16 v87, v78 offset:4480\n\t" \
  "ds_read_u16 v88, v78 offset:1024\n\t" \
  "ds_read_u16 v89, v78 offset:1152\n\t" \
  "ds_read_u16 v90, v78 offset:5120\n\t" \
  "ds_read_u16 v91, v78 offset:5248\n\t" \
  "ds_read_u16 v92, v78 offset:1280\n\t" \
  "ds_read_u16 v93, v78 offset:1408\n\t" \
  "ds_read_u16 v94, v78 offset:5376\n\t" \
  "ds_read_u16 v95, v78 offset:5504\n\t" \
  "ds_read_u16 v96, v78 offset:8192\n\t" \
  "ds_read_u16 v97, v78 offset:8320\n\t" \
  "ds_read_u16 v98, v78 offset:12288\n\t" \
  "ds_read_u16 v99, v78 offset:12416\n\t" \
  "ds_read_u16 v100, v78 offset:8448\n\t" \
  "ds_read_u16 v101, v78 offset:8576\n\t" \
  "ds_read_u16 v102, v78 offset:12544\n\t" \
  "ds_read_u16 v103, v78 offset:12672\n\t" \
  "ds_read_u16 v104, v78 offset:9216\n\t" \
  "ds_read_u16 v105, v78 offset:9344\n\t" \
  "ds_read_u16 v106, v78 offset:13312\n\t" \
  "ds_read_u16 v107, v78 offset:13440\n\t" \
  "ds_read_u16 v108, v78 offset:9472\n\t" \
  "ds_read_u16 v109, v78 offset:9600\n\t" \
  "ds_read_u16 v110, v78 offset:13568\n\t" \
  "ds_read_u16 v111, v78 offset:13696\n\t" \
  "s_add_u32 m0, s54, 90112\n\t" \
  "s_mov_b32 s60, 3145728\n\t" \
  "buffer_load_dwordx4 v71, %[Wk], s60 offen lds\n\t" \
  "s_add_u32 m0, s54, 91136\n\t" \
  "s_mov_b32 s60, 3178496\n\t" \
  "buffer_load_dwordx4 v71, %[Wk], s60 offen lds\n\t" \
  "s_add_u32 m0, s54, 92160\n\t" \
  "s_mov_b32 s60, 3211264\n\t" \
  "buffer_load_dwordx4 v71, %[Wk], s60 offen lds\n\t" \
  "s_add_u32 m0, s54, 93184\n\t" \
  "s_mov_b32 s60, 3244032\n\t" \
  "buffer_load_dwordx4 v71, %[Wk], s60 offen lds\n\t" \
  "s_waitcnt lgkmcnt(0)\n\t" \
  "v_lshl_or_b32 v248, v81, 16, v80\n\t" \
  "v_lshl_or_b32 v249, v83, 16, v82\n\t" \
  "v_lshl_or_b32 v250, v85, 16, v84\n\t" \
  "v_lshl_or_b32 v251, v87, 16, v86\n\t" \
  "v_lshl_or_b32 v252, v89, 16, v88\n\t" \
  "v_lshl_or_b32 v253, v91, 16, v90\n\t" \
  "v_lshl_or_b32 v254, v93, 16, v92\n\t" \
  "v_lshl_or_b32 v255, v95, 16, v94\n\t" \
  "v_lshl_or_b32 v112, v97, 16, v96\n\t" \
  "v_lshl_or_b32 v113, v99, 16, v98\n\t" \
  "v_lshl_or_b32 v114, v101, 16, v100\n\t" \
  "v_lshl_or_b32 v115, v103, 16, v102\n\t" \
  "v_lshl_or_b32 v116, v105, 16, v104\n\t" \
  "v_lshl_or_b32 v117, v107, 16, v106\n\t" \
  "v_lshl_or_b32 v118, v109, 16, v108\n\t" \
  "v_lshl_or_b32 v119, v111, 16, v110\n\t" \
  "ds_write_b128 v70, v[112:115] offset:34816\n\t" \
  "ds_write_b128 v70, v[116:119] offset:35840\n\t" \
  "v_add_u32 v78, 0, v65\n\t" \
  "s_waitcnt vmcnt(12) lgkmcnt(0)\n\t" \
  "s_barrier\n\t" \
  "ds_read_b64_tr_b16 a[208:209], v78 offset:0\n\t" \
  "ds_read_b64_tr_b16 a[210:211], v78 offset:1024\n\t" \
  "ds_read_b64_tr_b16 a[212:213], v78 offset:8192\n\t" \
  "ds_read_b64_tr_b16 a[214:215], v78 offset:9216\n\t" \
  "ds_read_b64_tr_b16 a[216:217], v78 offset:8\n\t" \
  "ds_read_b64_tr_b16 a[218:219], v78 offset:1032\n\t" \
  "ds_read_b64_tr_b16 a[220:221], v78 offset:8200\n\t" \
  "ds_read_b64_tr_b16 a[222:223], v78 offset:9224\n\t" \
  "s_add_u32 m0, s54, 126976\n\t" \
  "s_mov_b32 s60, 3145728\n\t" \
  "buffer_load_dwordx4 v71, %[Wv], s60 offen lds\n\t" \
  "s_add_u32 m0, s54, 128000\n\t" \
  "s_mov_b32 s60, 3178496\n\t" \
  "buffer_load_dwordx4 v71, %[Wv], s60 offen lds\n\t" \
  "s_add_u32 m0, s54, 129024\n\t" \
  "s_mov_b32 s60, 3211264\n\t" \
  "buffer_load_dwordx4 v71, %[Wv], s60 offen lds\n\t" \
  "s_add_u32 m0, s54, 130048\n\t" \
  "s_mov_b32 s60, 3244032\n\t" \
  "buffer_load_dwordx4 v71, %[Wv], s60 offen lds\n\t" \
  "v_add_u32 v78, 16384, v66\n\t" \
  "s_waitcnt vmcnt(12) lgkmcnt(0)\n\t" \
  "s_barrier\n\t" \
  "ds_read_b64_tr_b16 a[224:225], v78 offset:0\n\t" \
  "ds_read_b64_tr_b16 a[226:227], v78 offset:1024\n\t" \
  "ds_read_b64_tr_b16 a[228:229], v78 offset:8192\n\t" \
  "ds_read_b64_tr_b16 a[230:231], v78 offset:9216\n\t" \
  "ds_read_b64_tr_b16 a[232:233], v78 offset:32\n\t" \
  "ds_read_b64_tr_b16 a[234:235], v78 offset:1056\n\t" \
  "ds_read_b64_tr_b16 a[236:237], v78 offset:8224\n\t" \
  "ds_read_b64_tr_b16 a[238:239], v78 offset:9248\n\t" \
  "s_add_u32 m0, s54, 0\n\t" \
  "s_mov_b32 s60, 3670016\n\t" \
  "buffer_load_dwordx4 v71, %[Wq], s60 offen lds\n\t" \
  "s_add_u32 m0, s54, 1024\n\t" \
  "s_mov_b32 s60, 3702784\n\t" \
  "buffer_load_dwordx4 v71, %[Wq], s60 offen lds\n\t" \
  "s_add_u32 m0, s54, 2048\n\t" \
  "s_mov_b32 s60, 3735552\n\t" \
  "buffer_load_dwordx4 v71, %[Wq], s60 offen lds\n\t" \
  "s_add_u32 m0, s54, 3072\n\t" \
  "s_mov_b32 s60, 3768320\n\t" \
  "buffer_load_dwordx4 v71, %[Wq], s60 offen lds\n\t" \
  "v_add_u32 v78, 53248, v65\n\t" \
  "s_waitcnt vmcnt(12) lgkmcnt(0)\n\t" \
  "s_barrier\n\t" \
  "ds_read_b64_tr_b16 a[240:241], v78 offset:0\n\t" \
  "ds_read_b64_tr_b16 a[242:243], v78 offset:1024\n\t" \
  "ds_read_b64_tr_b16 a[244:245], v78 offset:8192\n\t" \
  "ds_read_b64_tr_b16 a[246:247], v78 offset:9216\n\t" \
  "ds_read_b64_tr_b16 a[248:249], v78 offset:8\n\t" \
  "ds_read_b64_tr_b16 a[250:251], v78 offset:1032\n\t" \
  "ds_read_b64_tr_b16 a[252:253], v78 offset:8200\n\t" \
  "ds_read_b64_tr_b16 a[254:255], v78 offset:9224\n\t" \
  "s_add_u32 m0, s54, 16384\n\t" \
  "s_mov_b32 s60, 3670016\n\t" \
  "buffer_load_dwordx4 v71, %[Wk], s60 offen lds\n\t" \
  "s_add_u32 m0, s54, 17408\n\t" \
  "s_mov_b32 s60, 3702784\n\t" \
  "buffer_load_dwordx4 v71, %[Wk], s60 offen lds\n\t" \
  "s_add_u32 m0, s54, 18432\n\t" \
  "s_mov_b32 s60, 3735552\n\t" \
  "buffer_load_dwordx4 v71, %[Wk], s60 offen lds\n\t" \
  "s_add_u32 m0, s54, 19456\n\t" \
  "s_mov_b32 s60, 3768320\n\t" \
  "buffer_load_dwordx4 v71, %[Wk], s60 offen lds\n\t" \
  "v_add_u32 v78, 90112, v65\n\t" \
  "s_waitcnt vmcnt(12) lgkmcnt(0)\n\t" \
  "s_barrier\n\t" \
  "ds_read_b64_tr_b16 v[80:81], v78 offset:0\n\t" \
  "ds_read_b64_tr_b16 v[82:83], v78 offset:1024\n\t" \
  "ds_read_b64_tr_b16 v[84:85], v78 offset:8192\n\t" \
  "ds_read_b64_tr_b16 v[86:87], v78 offset:9216\n\t" \
  "ds_read_b64_tr_b16 v[88:89], v78 offset:8\n\t" \
  "ds_read_b64_tr_b16 v[90:91], v78 offset:1032\n\t" \
  "ds_read_b64_tr_b16 v[92:93], v78 offset:8200\n\t" \
  "ds_read_b64_tr_b16 v[94:95], v78 offset:9224\n\t" \
  "s_add_u32 m0, s54, 53248\n\t" \
  "s_mov_b32 s60, 3670016\n\t" \
  "buffer_load_dwordx4 v71, %[Wv], s60 offen lds\n\t" \
  "s_add_u32 m0, s54, 54272\n\t" \
  "s_mov_b32 s60, 3702784\n\t" \
  "buffer_load_dwordx4 v71, %[Wv], s60 offen lds\n\t" \
  "s_add_u32 m0, s54, 55296\n\t" \
  "s_mov_b32 s60, 3735552\n\t" \
  "buffer_load_dwordx4 v71, %[Wv], s60 offen lds\n\t" \
  "s_add_u32 m0, s54, 56320\n\t" \
  "s_mov_b32 s60, 3768320\n\t" \
  "buffer_load_dwordx4 v71, %[Wv], s60 offen lds\n\t" \
  "v_add_u32 v78, 126976, v66\n\t" \
  "s_waitcnt vmcnt(12) lgkmcnt(0)\n\t" \
  "s_barrier\n\t" \
  "ds_read_b64_tr_b16 v[96:97], v78 offset:0\n\t" \
  "ds_read_b64_tr_b16 v[98:99], v78 offset:1024\n\t" \
  "ds_read_b64_tr_b16 v[100:101], v78 offset:8192\n\t" \
  "ds_read_b64_tr_b16 v[102:103], v78 offset:9216\n\t" \
  "ds_read_b64_tr_b16 v[104:105], v78 offset:32\n\t" \
  "ds_read_b64_tr_b16 v[106:107], v78 offset:1056\n\t" \
  "ds_read_b64_tr_b16 v[108:109], v78 offset:8224\n\t" \
  "ds_read_b64_tr_b16 v[110:111], v78 offset:9248\n\t" \
  "s_add_u32 m0, s54, 90112\n\t" \
  "s_mov_b32 s60, 4194304\n\t" \
  "buffer_load_dwordx4 v71, %[Wq], s60 offen lds\n\t" \
  "s_add_u32 m0, s54, 91136\n\t" \
  "s_mov_b32 s60, 4227072\n\t" \
  "buffer_load_dwordx4 v71, %[Wq], s60 offen lds\n\t" \
  "s_add_u32 m0, s54, 92160\n\t" \
  "s_mov_b32 s60, 4259840\n\t" \
  "buffer_load_dwordx4 v71, %[Wq], s60 offen lds\n\t" \
  "s_add_u32 m0, s54, 93184\n\t" \
  "s_mov_b32 s60, 4292608\n\t" \
  "buffer_load_dwordx4 v71, %[Wq], s60 offen lds\n\t" \
  "v_add_u32 v78, 0, v65\n\t" \
  "s_waitcnt vmcnt(12) lgkmcnt(0)\n\t" \
  "s_barrier\n\t" \
  "ds_read_b64_tr_b16 v[112:113], v78 offset:0\n\t" \
  "ds_read_b64_tr_b16 v[114:115], v78 offset:1024\n\t" \
  "ds_read_b64_tr_b16 v[116:117], v78 offset:8192\n\t" \
  "ds_read_b64_tr_b16 v[118:119], v78 offset:9216\n\t" \
  "ds_read_b64_tr_b16 v[120:121], v78 offset:8\n\t" \
  "ds_read_b64_tr_b16 v[122:123], v78 offset:1032\n\t" \
  "ds_read_b64_tr_b16 v[124:125], v78 offset:8200\n\t" \
  "ds_read_b64_tr_b16 v[126:127], v78 offset:9224\n\t" \
  "s_add_u32 m0, s54, 126976\n\t" \
  "s_mov_b32 s60, 4194304\n\t" \
  "buffer_load_dwordx4 v71, %[Wk], s60 offen lds\n\t" \
  "s_add_u32 m0, s54, 128000\n\t" \
  "s_mov_b32 s60, 4227072\n\t" \
  "buffer_load_dwordx4 v71, %[Wk], s60 offen lds\n\t" \
  "s_add_u32 m0, s54, 129024\n\t" \
  "s_mov_b32 s60, 4259840\n\t" \
  "buffer_load_dwordx4 v71, %[Wk], s60 offen lds\n\t" \
  "s_add_u32 m0, s54, 130048\n\t" \
  "s_mov_b32 s60, 4292608\n\t" \
  "buffer_load_dwordx4 v71, %[Wk], s60 offen lds\n\t" \
  "v_add_u32 v78, 16384, v65\n\t" \
  "s_waitcnt vmcnt(12) lgkmcnt(0)\n\t" \
  "s_barrier\n\t" \
  "ds_read_b64_tr_b16 v[128:129], v78 offset:0\n\t" \
  "ds_read_b64_tr_b16 v[130:131], v78 offset:1024\n\t" \
  "ds_read_b64_tr_b16 v[132:133], v78 offset:8192\n\t" \
  "ds_read_b64_tr_b16 v[134:135], v78 offset:9216\n\t" \
  "ds_read_b64_tr_b16 v[136:137], v78 offset:8\n\t" \
  "ds_read_b64_tr_b16 v[138:139], v78 offset:1032\n\t" \
  "ds_read_b64_tr_b16 v[140:141], v78 offset:8200\n\t" \
  "ds_read_b64_tr_b16 v[142:143], v78 offset:9224\n\t" \
  "s_add_u32 m0, s54, 0\n\t" \
  "s_mov_b32 s60, 4194304\n\t" \
  "buffer_load_dwordx4 v71, %[Wv], s60 offen lds\n\t" \
  "s_add_u32 m0, s54, 1024\n\t" \
  "s_mov_b32 s60, 4227072\n\t" \
  "buffer_load_dwordx4 v71, %[Wv], s60 offen lds\n\t" \
  "s_add_u32 m0, s54, 2048\n\t" \
  "s_mov_b32 s60, 4259840\n\t" \
  "buffer_load_dwordx4 v71, %[Wv], s60 offen lds\n\t" \
  "s_add_u32 m0, s54, 3072\n\t" \
  "s_mov_b32 s60, 4292608\n\t" \
  "buffer_load_dwordx4 v71, %[Wv], s60 offen lds\n\t" \
  "v_add_u32 v78, 53248, v66\n\t" \
  "s_waitcnt vmcnt(12) lgkmcnt(0)\n\t" \
  "s_barrier\n\t" \
  "ds_read_b64_tr_b16 v[144:145], v78 offset:0\n\t" \
  "ds_read_b64_tr_b16 v[146:147], v78 offset:1024\n\t" \
  "ds_read_b64_tr_b16 v[148:149], v78 offset:8192\n\t" \
  "ds_read_b64_tr_b16 v[150:151], v78 offset:9216\n\t" \
  "ds_read_b64_tr_b16 v[152:153], v78 offset:32\n\t" \
  "ds_read_b64_tr_b16 v[154:155], v78 offset:1056\n\t" \
  "ds_read_b64_tr_b16 v[156:157], v78 offset:8224\n\t" \
  "ds_read_b64_tr_b16 v[158:159], v78 offset:9248\n\t" \
  "v_add_u32 v78, 90112, v65\n\t" \
  "s_waitcnt vmcnt(8) lgkmcnt(0)\n\t" \
  "s_barrier\n\t" \
  "ds_read_b64_tr_b16 v[160:161], v78 offset:0\n\t" \
  "ds_read_b64_tr_b16 v[162:163], v78 offset:1024\n\t" \
  "ds_read_b64_tr_b16 v[164:165], v78 offset:8192\n\t" \
  "ds_read_b64_tr_b16 v[166:167], v78 offset:9216\n\t" \
  "ds_read_b64_tr_b16 v[168:169], v78 offset:8\n\t" \
  "ds_read_b64_tr_b16 v[170:171], v78 offset:1032\n\t" \
  "ds_read_b64_tr_b16 v[172:173], v78 offset:8200\n\t" \
  "ds_read_b64_tr_b16 v[174:175], v78 offset:9224\n\t" \
  "v_add_u32 v78, 126976, v65\n\t" \
  "s_waitcnt vmcnt(4) lgkmcnt(0)\n\t" \
  "s_barrier\n\t" \
  "ds_read_b64_tr_b16 v[40:41], v78 offset:0\n\t" \
  "ds_read_b64_tr_b16 v[42:43], v78 offset:1024\n\t" \
  "ds_read_b64_tr_b16 v[44:45], v78 offset:8192\n\t" \
  "ds_read_b64_tr_b16 v[46:47], v78 offset:9216\n\t" \
  "ds_read_b64_tr_b16 v[176:177], v78 offset:8\n\t" \
  "ds_read_b64_tr_b16 v[178:179], v78 offset:1032\n\t" \
  "ds_read_b64_tr_b16 v[180:181], v78 offset:8200\n\t" \
  "ds_read_b64_tr_b16 v[182:183], v78 offset:9224\n\t" \
  "v_add_u32 v78, 0, v66\n\t" \
  "s_waitcnt vmcnt(0) lgkmcnt(0)\n\t" \
  "s_barrier\n\t" \
  "ds_read_b64_tr_b16 v[184:185], v78 offset:0\n\t" \
  "ds_read_b64_tr_b16 v[186:187], v78 offset:1024\n\t" \
  "ds_read_b64_tr_b16 v[188:189], v78 offset:8192\n\t" \
  "ds_read_b64_tr_b16 v[190:191], v78 offset:9216\n\t" \
  "ds_read_b64_tr_b16 v[56:57], v78 offset:32\n\t" \
  "ds_read_b64_tr_b16 v[58:59], v78 offset:1056\n\t" \
  "ds_read_b64_tr_b16 v[60:61], v78 offset:8224\n\t" \
  "ds_read_b64_tr_b16 v[62:63], v78 offset:9248\n\t" \
  CH_STV \
  "s_waitcnt lgkmcnt(0)\n\t" \
  "s_barrier\n\t" \
  "ds_write_b128 v70, v[0:3] offset:0\n\t" \
  "ds_write_b128 v70, v[4:7] offset:1024\n\t" \
  "ds_write_b128 v70, v[8:11] offset:2048\n\t" \
  "ds_write_b128 v70, v[12:15] offset:3072\n\t" \
  "ds_write_b128 v70, v[16:19] offset:4096\n\t" \
  "ds_write_b128 v70, v[20:23] offset:5120\n\t" \
  "ds_write_b128 v70, v[24:27] offset:6144\n\t" \
  "ds_write_b128 v70, v[28:31] offset:7168\n\t" \
  "ds_write_b128 v70, v[32:35] offset:8192\n\t" \
  "ds_write_b128 v70, v[36:39] offset:9216\n\t" \
  "ds_write_b128 v70, v[40:43] offset:10240\n\t" \
  "ds_write_b128 v70, v[44:47] offset:11264\n\t" \
  "ds_write_b128 v70, v[48:51] offset:12288\n\t" \
  "ds_write_b128 v70, v[52:55] offset:13312\n\t" \
  "ds_write_b128 v70, v[56:59] offset:14336\n\t" \
  "ds_write_b128 v70, v[60:63] offset:15360\n\t" \
  "s_waitcnt lgkmcnt(0)\n\t" \
  CH_STW \
  CH_COW \
  "v_and_b32 v72, 1, v72\n\t" \
  "v_and_b32 v74, 1, v74\n\t" \
  "v_cmp_ne_u32 s[90:91], s32, v72\n\t" \
  "v_cmp_ne_u32 vcc, s32, v74\n\t" \
  "s_nop 1\n\t" \
  "s_mov_b32 s96, vcc_lo\n\t" \
  "v_and_b32 v73, 32, v69\n\t" \
  "v_lshl_add_u32 v73, v73, 5, v69\n\t" \
  "s_mov_b32 s35, s30\n\t" \
  "s_cmp_lt_u32 s32, " CH_DRYL "\n\t" \
  "s_cbranch_scc1 LINI_%=\n\t" \
  "s_mov_b32 s30, 0\n\t" \
  "s_mov_b32 s34, 0\n\t" \
  "s_cmp_eq_u32 s40, 0\n\t" \
  "s_cbranch_scc1 LW0_%=\n\t" \
  "s_barrier\n\t" \
  "s_cmp_eq_u32 s40, 1\n\t" \
  "s_cbranch_scc1 LG1_%=\n\t" \
  "s_cmp_eq_u32 s40, 2\n\t" \
  "s_cbranch_scc1 LG2_%=\n\t" \
  "s_branch LG3_%=\n\t" \
  "LINI_%=:\n\t" \
  "s_waitcnt vmcnt(0) lgkmcnt(0)\n\t" \
  "s_mov_b32 s30, s35\n\t" \
  "s_movk_i32 s34, 1000\n\t" \
  "s_mov_b32 s48, -1\n\t" \
  "s_add_u32 s97, s41, s53\n\t" \
  "s_add_u32 s98, s37, s53\n\t" \
  "s_add_u32 s82, s48, 0\n\t" \
  CH_SEGC \
  "s_lshr_b64 s[60:61], s[90:91], s82\n\t" \
  "s_and_b32 s60, s60, 1\n\t" \
  "s_lshl_b32 s60, s60, 13\n\t" \
  "s_add_u32 s82, s82, " CH_SG0 "\n\t" \
  "s_lshl_b32 s82, s82, 21\n\t" \
  "s_or_b32 s82, s82, s60\n\t" \
  "s_add_u32 s50, s82, s38\n\t" \
  "s_add_u32 s69, s82, s39\n\t" \
  "s_add_u32 s82, s48, 1\n\t" \
  CH_SEGC \
  "s_lshr_b64 s[60:61], s[90:91], s82\n\t" \
  "s_and_b32 s60, s60, 1\n\t" \
  "s_lshl_b32 s60, s60, 13\n\t" \
  "s_add_u32 s82, s82, " CH_SG0 "\n\t" \
  "s_lshl_b32 s82, s82, 21\n\t" \
  "s_or_b32 s82, s82, s60\n\t" \
  "s_add_u32 s52, s82, s43\n\t" \
  "s_add_u32 s82, s48, " CH_ARMD "\n\t" \
  CH_SEGC \
  "s_lshr_b64 s[60:61], s[90:91], s82\n\t" \
  "s_and_b32 s60, s60, 1\n\t" \
  "s_lshl_b32 s60, s60, 13\n\t" \
  "s_add_u32 s82, s82, " CH_SG0 "\n\t" \
  "s_lshl_b32 s82, s82, 21\n\t" \
  "s_or_b32 s82, s82, s60\n\t" \
  "s_add_u32 s85, s82, s45\n\t" \
  "s_cmp_lt_i32 s48, " CH_ARMS "\n\t" \
  "s_cselect_b32 s85, s27, s85\n\t" \
  "v_and_b32 v37, 0x30, v69\n\t" \
  "v_add_u32 v37, s52, v37\n\t" \
  "s_mov_b32 s48, 0\n\t" \
  "s_mov_b32 s86, 1\n\t" \
  "s_and_b32 s49, s96, 1\n\t" \
  "s_lshl_b32 s49, s49, 13\n\t" \
  "s_add_u32 s53, s49, " CH_SLOT "\n\t" \
  "s_movk_i32 s60, 124\n\t" \
  "s_cmp_eq_u32 s40, 3\n\t" \
  "s_cselect_b32 s81, 76, s60\n\t" \
  "s_add_u32 s82, s48, 0\n\t" \
  CH_SEGC \
  "s_lshr_b64 s[60:61], s[90:91], s82\n\t" \
  "s_and_b32 s60, s60, 1\n\t" \
  "s_lshl_b32 s60, s60, 13\n\t" \
  "s_add_u32 s82, s82, " CH_SG0 "\n\t" \
  "s_lshl_b32 s82, s82, 21\n\t" \
  "s_or_b32 s82, s82, s60\n\t" \
  "s_add_u32 s50, s82, s38\n\t" \
  "s_add_u32 s69, s82, s39\n\t" \
  "s_add_u32 s51, s82, s43\n\t" \
  "s_add_u32 s97, s41, s53\n\t" \
  "s_add_u32 s98, s37, s53\n\t" \
  "s_add_u32 s82, s48, 0\n\t" \
  CH_SEGC \
  "s_lshr_b64 s[60:61], s[90:91], s82\n\t" \
  "s_and_b32 s60, s60, 1\n\t" \
  "s_lshl_b32 s60, s60, 13\n\t" \
  "s_add_u32 s82, s82, " CH_SG0 "\n\t" \
  "s_lshl_b32 s82, s82, 21\n\t" \
  "s_or_b32 s82, s82, s60\n\t" \
  "s_add_u32 s50, s82, s38\n\t" \
  "s_add_u32 s69, s82, s39\n\t" \
  "s_add_u32 s82, s48, 1\n\t" \
  CH_SEGC \
  "s_lshr_b64 s[60:61], s[90:91], s82\n\t" \
  "s_and_b32 s60, s60, 1\n\t" \
  "s_lshl_b32 s60, s60, 13\n\t" \
  "s_add_u32 s82, s82, " CH_SG0 "\n\t" \
  "s_lshl_b32 s82, s82, 21\n\t" \
  "s_or_b32 s82, s82, s60\n\t" \
  "s_add_u32 s52, s82, s43\n\t" \
  "s_add_u32 s82, s48, " CH_ARMD "\n\t" \
  CH_SEGC \
  "s_lshr_b64 s[60:61], s[90:91], s82\n\t" \
  "s_and_b32 s60, s60, 1\n\t" \
  "s_lshl_b32 s60, s60, 13\n\t" \
  "s_add_u32 s82, s82, " CH_SG0 "\n\t" \
  "s_lshl_b32 s82, s82, 21\n\t" \
  "s_or_b32 s82, s82, s60\n\t" \
  "s_add_u32 s85, s82, s45\n\t" \
  "s_cmp_lt_i32 s48, " CH_ARMS "\n\t" \
  "s_cselect_b32 s85, s27, s85\n\t" \
  "s_memrealtime s[62:63]\n\t" \
  "s_waitcnt lgkmcnt(0)\n\t" \
  "s_mov_b32 s65, s62\n\t" \
  "s_sub_u32 s47, s62, 1000\n\t" \
  "s_mov_b64 vcc, -1\n\t" \
  "s_cmp_eq_u32 s40, 0\n\t" \
  "s_cbranch_scc1 LW0_%=\n\t" \
  "s_barrier\n\t" \
  "s_cmp_eq_u32 s40, 1\n\t" \
  "s_cbranch_scc1 LG1_%=\n\t" \
  "s_cmp_eq_u32 s40, 2\n\t" \
  "s_cbranch_scc1 LG2_%=\n\t" \
  "s_branch LTK_%=\n\t" \
  ".p2align 6\n\t" \
  "LW0_%=:\n\t" \
  "v_lshlrev_b32 v78, 2, v69\n\t" \
  "v_and_b32 v78, 0x300, v78\n\t" \
  "v_lshrrev_b32 v79, 8, v69\n\t" \
  "v_lshl_add_u32 v78, v79, 6, v78\n\t" \
  "v_and_b32 v79, 48, v69\n\t" \
  "v_add_u32 v78, v78, v79\n\t" \
  "s_add_u32 s98, s49, s42\n\t" \
  "s_add_u32 s97, s98, 32768\n\t" \
  "s_add_u32 s99, s98, 65536\n\t" \
  "s_barrier\n\t" \
  "s_memrealtime s[62:63]\n\t" \
  "s_waitcnt lgkmcnt(0)\n\t" \
  "s_add_u32 s62, s62, " CH_DLL2 "\n\t" \
  "s_sub_i32 s63, s62, s65\n\t" \
  "s_cmp_gt_i32 s63, " CH_DLT "\n\t" \
  "s_cselect_b32 s60, " CH_DLY ", " CH_DLX "\n\t" \
  "s_add_u32 s62, s62, s60\n\t" \
  "s_cmp_gt_i32 s63, 0\n\t" \
  "s_cselect_b32 s65, s62, s65\n\t" \
  "s_mov_b32 m0, 0\n\t" \
  "s_movk_i32 s60, 8192\n\t" \
  "LWT_%=:\n\t" \
  "s_memrealtime s[62:63]\n\t" \
  "s_sub_u32 s60, s60, 1\n\t" \
  "s_cselect_b32 s61, 0, -1\n\t" \
  "s_waitcnt lgkmcnt(0)\n\t" \
  "s_sub_i32 s63, s62, s65\n\t" \
  "s_and_b32 s63, s63, s61\n\t" \
  "s_cmp_lt_i32 s63, " CH_FIN "\n\t" \
  "s_cbranch_scc1 LWT_%=\n\t" \
  "s_cmp_lt_i32 s63, 0\n\t" \
  "s_cbranch_scc1 LWT_%=f\n\t" \
  "LWT_%=g:\n\t" \
  "LDMA_%=:\n\t" \
  CH_DPC \
  "buffer_load_dwordx4 v78, s[28:31], s98 offen offset:0 sc1 lds\n\t" \
  "buffer_load_dwordx4 v78, s[28:31], s98 offen offset:1024 sc1 lds\n\t" \
  "buffer_load_dwordx4 v78, s[28:31], s98 offen offset:2048 sc1 lds\n\t" \
  "buffer_load_dwordx4 v78, s[28:31], s98 offen offset:3072 sc1 lds\n\t" \
  "s_movk_i32 m0, 4096\n\t" \
  "s_mov_b32 s66, s62\n\t" \
  CH_HNP \
  "buffer_load_dwordx4 v78, s[28:31], s97 offen offset:0 sc1 lds\n\t" \
  "buffer_load_dwordx4 v78, s[28:31], s97 offen offset:1024 sc1 lds\n\t" \
  "buffer_load_dwordx4 v78, s[28:31], s97 offen offset:2048 sc1 lds\n\t" \
  "buffer_load_dwordx4 v78, s[28:31], s97 offen offset:3072 sc1 lds\n\t" \
  "s_movk_i32 m0, 8192\n\t" \
  "s_nop 0\n\t" \
  "buffer_load_dwordx4 v69, s[28:31], s99 offen sc1 lds\n\t" \
  CH_ST1 \
  ".p2align 6\n\t" \
  "LG0_%=:\n\t" \
  "s_barrier\n\t" \
  "buffer_load_dwordx4 v[52:55], v69, s[28:31], s50 offen sc1\n\t" \
  "buffer_load_dwordx4 v[56:59], v69, s[28:31], s50 offen offset:1024 sc1\n\t" \
  "s_add_u32 s64, s50, 16384\n\t" \
  "s_movk_i32 m0, 11264\n\t" \
  "v_lshlrev_b32 v75, 1, v69\n\t" \
  "buffer_load_dword v75, s[28:31], s64 offen sc1 lds\n\t" \
  "s_memrealtime s[62:63]\n\t" \
  "s_waitcnt lgkmcnt(0)\n\t" \
  "s_sub_u32 s62, s62, " CH_TKL "\n\t" \
  "s_sub_i32 s63, s62, s65\n\t" \
  "s_cmp_gt_i32 s63, 0\n\t" \
  "s_cselect_b32 s62, s62, s65\n\t" \
  "s_sub_i32 s63, s62, s47\n\t" \
  "s_cmp_gt_i32 s63, " CH_LATE "\n\t" \
  "s_cselect_b32 s71, " CH_SCHW ", s46\n\t" \
  "s_add_u32 s61, s47, " CH_LATE "\n\t" \
  "s_cmp_gt_i32 s63, " CH_LATE "\n\t" \
  "s_cselect_b32 s62, s62, s61\n\t" \
  "s_add_u32 s71, s71, s53\n\t" \
  "s_add_u32 s60, s62, s81\n\t" \
  "s_add_u32 s60, s60, " CH_DL1L "\n\t" \
  "s_lshl_b32 s60, s60, 8\n\t" \
  "s_or_b32 s60, s60, " CH_B0 "\n\t" \
  "s_cmp_lt_u32 s48, " CH_ES "\n\t" \
  "s_cselect_b32 s61, " CH_EX ", 0\n\t" \
  "s_add_u32 s60, s60, s61\n\t" \
  "s_not_b32 s60, s60\n\t" \
  "v_mov_b32 v76, s60\n\t" \
  "s_lshr_b32 s60, s46, 31\n\t" \
  "s_or_b32 s60, s60, s68\n\t" \
  "s_cselect_b32 s25, 0, 1\n\t" \
  "s_mov_b64 exec, s[24:25]\n\t" \
  "buffer_atomic_umin v76, v69, s[28:31], s71 offen sc1\n\t" \
  "s_mov_b64 exec, -1\n\t" \
  "ds_read_b128 v[44:47], v70 offset:0\n\t" \
  "ds_read_b128 v[48:51], v70 offset:1024\n\t" \
  "ds_read_b128 v[60:63], v70 offset:2048\n\t" \
  "ds_read_b128 v[64:67], v70 offset:3072\n\t" \
  "v_mov_b64 v[36:37], 0\n\t" \
  "v_mov_b64 v[38:39], 0\n\t" \
  "v_mov_b64 v[40:41], 0\n\t" \
  "v_mov_b64 v[42:43], 0\n\t" \
  "v_lshrrev_b32 v71, 1, v69\n\t" \
  "v_lshrrev_b32 v72, 3, v69\n\t" \
  "v_and_b32 v72, 0x78, v72\n\t" \
  "v_and_b32 v74, 0x30, v69\n\t" \
  "v_lshl_add_u32 v72, v74, 3, v72\n\t" \
  "s_add_u32 s54, s53, s36\n\t" \
  "s_add_u32 s55, s54, 524288\n\t" \
  "s_add_u32 s54, s54, 0\n\t" \
  "s_add_u32 s75, s54, 262144\n\t" \
  "s_add_u32 s80, s53, s26\n\t" \
  "s_or_b32 s87, s53, 0x1fff80\n\t" \
  "s_add_u32 s87, s87, 2097152\n\t" \
  "s_waitcnt vmcnt(9)\n\t" \
  "s_barrier\n\t" \
  CH_DP0 \
  "ds_read_b128 v[0:3], v69 offset:0\n\t" \
  "ds_read_b128 v[4:7], v69 offset:1024\n\t" \
  "ds_read_b128 v[8:11], v69 offset:2048\n\t" \
  "ds_read_b128 v[12:15], v69 offset:3072\n\t" \
  "s_waitcnt lgkmcnt(3)\n\t" \
  "v_smfmac_f32_16x16x64_bf16 v[36:39], v[0:3], v[44:51], v68\n\t" \
  "ds_read_b128 v[44:47], v70 offset:4096\n\t" \
  "v_smfmac_f32_16x16x64_bf16 v[40:43], v[0:3], v[60:67], v68\n\t" \
  "ds_read_b128 v[48:51], v70 offset:5120\n\t" \
  "s_waitcnt lgkmcnt(4)\n\t" \
  "v_smfmac_f32_16x16x64_bf16 v[36:39], v[4:7], a[32:39], v68\n\t" \
  "ds_read_b128 v[60:63], v70 offset:6144\n\t" \
  "v_smfmac_f32_16x16x64_bf16 v[40:43], v[4:7], a[40:47], v68\n\t" \
  "ds_read_b128 v[64:67], v70 offset:7168\n\t" \
  "s_waitcnt lgkmcnt(5)\n\t" \
  "v_smfmac_f32_16x16x64_bf16 v[36:39], v[8:11], a[80:87], v68\n\t" \
  "s_waitcnt vmcnt(4)\n\t" \
  "s_barrier\n\t" \
  CH_DP1 \
  CH_TG2 \
  "ds_read_b128 v[16:19], v69 offset:4096\n\t" \
  "v_smfmac_f32_16x16x64_bf16 v[40:43], v[8:11], a[88:95], v68\n\t" \
  "ds_read_b128 v[20:23], v69 offset:5120\n\t" \
  "s_waitcnt lgkmcnt(6)\n\t" \
  "v_smfmac_f32_16x16x64_bf16 v[36:39], v[12:15], a[120:127], v68\n\t" \
  "ds_read_b128 v[24:27], v69 offset:6144\n\t" \
  "s_waitcnt lgkmcnt(5)\n\t" \
  "v_smfmac_f32_16x16x64_bf16 v[40:43], v[12:15], v[44:51], v68\n\t" \
  "ds_read_b128 v[28:31], v69 offset:7168\n\t" \
  "s_waitcnt lgkmcnt(3)\n\t" \
  "v_smfmac_f32_16x16x64_bf16 v[36:39], v[16:19], a[160:167], v68\n\t" \
  "ds_read_b128 v[32:35], v69 offset:8192\n\t" \
  "v_smfmac_f32_16x16x64_bf16 v[40:43], v[16:19], a[168:175], v68\n\t" \
  "s_waitcnt lgkmcnt(3)\n\t" \
  "v_mov_b64 v[44:45], 0\n\t" \
  "v_smfmac_f32_16x16x64_bf16 v[36:39], v[20:23], a[200:207], v68\n\t" \
  "s_waitcnt lgkmcnt(2)\n\t" \
  "v_mov_b64 v[46:47], 0\n\t" \
  "v_smfmac_f32_16x16x64_bf16 v[36:39], v[24:27], a[240:247], v68\n\t" \
  "s_waitcnt lgkmcnt(1)\n\t" \
  "v_mov_b64 v[48:49], 0\n\t" \
  "v_smfmac_f32_16x16x64_bf16 v[36:39], v[28:31], v[112:119], v68\n\t" \
  "s_waitcnt lgkmcnt(0)\n\t" \
  "v_mov_b64 v[50:51], 0\n\t" \
  "v_smfmac_f32_16x16x64_bf16 v[36:39], v[32:35], v[160:167], v68\n\t" \
  "v_smfmac_f32_16x16x64_bf16 v[40:43], v[20:23], v[60:67], v68\n\t" \
  "ds_read_b128 v[60:63], v70 offset:8192\n\t" \
  "v_smfmac_f32_16x16x64_bf16 v[40:43], v[24:27], a[248:255], v68\n\t" \
  "ds_read_b128 v[64:67], v70 offset:9216\n\t" \
  "v_add_f32 v74, v36, v38\n\t" \
  "v_smfmac_f32_16x16x64_bf16 v[40:43], v[28:31], v[120:127], v68\n\t" \
  "v_add_f32 v37, v37, v39\n\t" \
  "v_smfmac_f32_16x16x64_bf16 v[40:43], v[32:35], v[168:175], v68\n\t" \
  "v_cmp_u_f32 vcc, v74, v37\n\t" \
  CH_DP2 \
  "v_smfmac_f32_16x16x64_bf16 v[44:47], v[0:3], a[0:7], v68\n\t" \
  "s_cbranch_vccnz LFAIL0_%=\n\t" \
  "LGO0_%=:\n\t" \
  "v_cvt_pk_bf16_f32 v74, v74, v37\n\t" \
  "v_smfmac_f32_16x16x64_bf16 v[44:47], v[4:7], a[48:55], v68\n\t" \
  "s_waitcnt lgkmcnt(0)\n\t" \
  "v_add_f32 v75, v40, v42\n\t" \
  "v_smfmac_f32_16x16x64_bf16 v[44:47], v[8:11], v[60:67], v68\n\t" \
  "ds_read_b128 v[60:63], v70 offset:10240\n\t" \
  "v_add_f32 v41, v41, v43\n\t" \
  "v_cvt_pk_bf16_f32 v75, v75, v41\n\t" \
  "v_smfmac_f32_16x16x64_bf16 v[44:47], v[12:15], a[128:135], v68\n\t" \
  "ds_write2st64_b32 v71, v74, v75 offset0:36 offset1:38\n\t" \
  "v_smfmac_f32_16x16x64_bf16 v[44:47], v[16:19], a[176:183], v68\n\t" \
  "ds_read_b128 v[64:67], v70 offset:11264\n\t" \
  "v_smfmac_f32_16x16x64_bf16 v[44:47], v[20:23], a[208:215], v68\n\t" \
  "v_smfmac_f32_16x16x64_bf16 v[44:47], v[24:27], v[80:87], v68\n\t" \
  "v_smfmac_f32_16x16x64_bf16 v[44:47], v[28:31], v[128:135], v68\n\t" \
  "s_waitcnt lgkmcnt(0)\n\t" \
  "v_smfmac_f32_16x16x64_bf16 v[44:47], v[32:35], v[60:67], v68\n\t" \
  "buffer_load_dwordx4 v[60:63], v69, s[28:31], s64 offen sc1\n\t" \
  "s_barrier\n\t" \
  "ds_read_b64_tr_b16 v[76:77], v72 offset:9216\n\t" \
  CH_DP3 \
  "v_smfmac_f32_16x16x64_bf16 v[48:51], v[0:3], a[8:15], v68\n\t" \
  "ds_read_b64_tr_b16 v[78:79], v72 offset:9728\n\t" \
  "v_smfmac_f32_16x16x64_bf16 v[48:51], v[4:7], a[56:63], v68\n\t" \
  "v_add_f32 v74, v44, v46\n\t" \
  "v_smfmac_f32_16x16x64_bf16 v[48:51], v[8:11], a[96:103], v68\n\t" \
  "v_add_f32 v45, v45, v47\n\t" \
  "v_smfmac_f32_16x16x64_bf16 v[48:51], v[12:15], a[136:143], v68\n\t" \
  "s_waitcnt vmcnt(4)\n\t" \
  "s_waitcnt lgkmcnt(0)\n\t" \
  "v_cvt_pk_bf16_f32 v74, v74, v45\n\t" \
  "v_mfma_f32_16x16x32_bf16 v[36:39], v[52:55], v[76:79], 0\n\t" \
  "buffer_load_dwordx4 v[64:67], v69, s[28:31], s64 offen offset:1024 sc1\n\t" \
  "s_waitcnt vmcnt(4)\n\t" \
  "v_mfma_f32_16x16x32_bf16 v[40:43], v[56:59], v[76:79], 0\n\t" \
  "v_mov_b64 v[52:53], 0\n\t" \
  "v_mov_b64 v[54:55], 0\n\t" \
  "v_smfmac_f32_16x16x64_bf16 v[48:51], v[16:19], a[184:191], v68\n\t" \
  "v_mov_b64 v[56:57], 0\n\t" \
  "v_mov_b64 v[58:59], 0\n\t" \
  "v_smfmac_f32_16x16x64_bf16 v[48:51], v[20:23], a[216:223], v68\n\t" \
  "ds_write_b32 v71, v74 offset:0\n\t" \
  "v_add_f32_dpp v36, v40, v36 quad_perm:[2,3,0,1] row_mask:0xf bank_mask:0xf\n\t" \
  "v_smfmac_f32_16x16x64_bf16 v[48:51], v[24:27], v[88:95], v68\n\t" \
  "v_add_f32_dpp v37, v41, v37 quad_perm:[2,3,0,1] row_mask:0xf bank_mask:0xf\n\t" \
  "v_smfmac_f32_16x16x64_bf16 v[48:51], v[28:31], v[136:143], v68\n\t" \
  "v_add_f32_dpp v38, v42, v38 quad_perm:[2,3,0,1] row_mask:0xf bank_mask:0xf\n\t" \
  "v_smfmac_f32_16x16x64_bf16 v[48:51], v[32:35], v[176:183], v68\n\t" \
  "v_add_f32_dpp v39, v43, v39 quad_perm:[2,3,0,1] row_mask:0xf bank_mask:0xf\n\t" \
  CH_DP4 \
  "v_smfmac_f32_16x16x64_bf16 v[52:55], v[0:3], a[16:23], v68\n\t" \
  "s_mov_b64 exec, s[58:59]\n\t" \
  "ds_write_b128 v69, v[36:39] offset:2048\n\t" \
  "s_mov_b64 exec, -1\n\t" \
  "v_smfmac_f32_16x16x64_bf16 v[52:55], v[4:7], a[64:71], v68\n\t" \
  "v_add_f32 v75, v48, v50\n\t" \
  "v_add_f32 v49, v49, v51\n\t" \
  "v_cvt_pk_bf16_f32 v75, v75, v49\n\t" \
  "v_smfmac_f32_16x16x64_bf16 v[56:59], v[0:3], a[24:31], v68\n\t" \
  "ds_write_b32 v71, v75 offset:512\n\t" \
  "v_smfmac_f32_16x16x64_bf16 v[56:59], v[4:7], a[72:79], v68\n\t" \
  "ds_read_b64_tr_b16 v[0:1], v72 offset:10240\n\t" \
  "ds_read_b64_tr_b16 v[2:3], v72 offset:10752\n\t" \
  "v_smfmac_f32_16x16x64_bf16 v[52:55], v[8:11], a[104:111], v68\n\t" \
  "ds_read_b128 v[36:39], v70 offset:12288\n\t" \
  "v_smfmac_f32_16x16x64_bf16 v[56:59], v[8:11], a[112:119], v68\n\t" \
  "buffer_load_dword off, s[28:31], s87 sc1 lds\n\t" \
  "ds_read_b128 v[40:43], v70 offset:13312\n\t" \
  "v_smfmac_f32_16x16x64_bf16 v[52:55], v[12:15], a[144:151], v68\n\t" \
  "s_waitcnt lgkmcnt(4)\n\t" \
  "s_barrier\n\t" \
  CH_DP5 \
  CH_TG5 \
  "ds_read_b64_tr_b16 v[44:45], v72 offset:0\n\t" \
  "ds_read_b64_tr_b16 v[46:47], v72 offset:512\n\t" \
  "v_smfmac_f32_16x16x64_bf16 v[56:59], v[12:15], a[152:159], v68\n\t" \
  "ds_read_b64_tr_b16 v[48:49], v72 offset:1024\n\t" \
  "ds_read_b64_tr_b16 v[50:51], v72 offset:1536\n\t" \
  "v_smfmac_f32_16x16x64_bf16 v[52:55], v[16:19], a[192:199], v68\n\t" \
  "ds_read_b128 v[4:7], v69 offset:2048\n\t" \
  "s_waitcnt lgkmcnt(5)\n\t" \
  "v_smfmac_f32_16x16x64_bf16 v[56:59], v[16:19], v[36:43], v68\n\t" \
  "ds_read_b128 v[8:11], v69 offset:3072\n\t" \
  "s_waitcnt lgkmcnt(4)\n\t" \
  "v_mfma_f32_16x16x32_bf16 v[12:15], v[44:47], v[76:79], 0\n\t" \
  "ds_read_b128 v[36:39], v70 offset:14336\n\t" \
  "s_waitcnt lgkmcnt(3)\n\t" \
  "v_mfma_f32_16x16x32_bf16 v[12:15], v[48:51], v[0:3], v[12:15]\n\t" \
  "ds_read_b128 v[40:43], v70 offset:15360\n\t" \
  "v_smfmac_f32_16x16x64_bf16 v[52:55], v[20:23], a[224:231], v68\n\t" \
  "s_waitcnt lgkmcnt(2)\n\t" \
  "v_add_f32 v4, v4, v8\n\t" \
  "v_add_f32 v5, v5, v9\n\t" \
  "v_add_f32 v6, v6, v10\n\t" \
  CH_DP6 \
  "v_cndmask_b32 v78, v13, v14, s[56:57]\n\t" \
  "v_cndmask_b32 v76, v12, v15, s[56:57]\n\t" \
  "v_add_f32 v7, v7, v11\n\t" \
  "v_max3_f32 v77, v4, v5, v6\n\t" \
  "v_add_f32_dpp v76, v78, v76 quad_perm:[2,3,0,1] row_mask:0xf bank_mask:0xf\n\t" \
  "v_max3_f32 v77, v77, v7, v76\n\t" \
  "v_smfmac_f32_16x16x64_bf16 v[56:59], v[20:23], a[232:239], v68\n\t" \
  "ds_read_b128 v[44:47], v70 offset:16384\n\t" \
  "v_max_f32_dpp v77, v77, v77 quad_perm:[2,3,0,1] row_mask:0xf bank_mask:0xf\n\t" \
  "v_mov_b32 v78, v77\n\t" \
  "v_smfmac_f32_16x16x64_bf16 v[52:55], v[24:27], v[96:103], v68\n\t" \
  "ds_read_b128 v[48:51], v70 offset:17408\n\t" \
  "v_permlane16_swap_b32 v77, v78\n\t" \
  "v_max_f32 v77, v77, v78\n\t" \
  "v_mov_b32 v78, v77\n\t" \
  "v_smfmac_f32_16x16x64_bf16 v[52:55], v[28:31], v[144:151], v68\n\t" \
  "ds_read_b128 v[16:19], v70 offset:18432\n\t" \
  "v_permlane32_swap_b32 v77, v78\n\t" \
  "v_max_f32 v77, v77, v78\n\t" \
  "v_smfmac_f32_16x16x64_bf16 v[52:55], v[32:35], v[184:191], v68\n\t" \
  "ds_read_b128 v[20:23], v70 offset:19456\n\t" \
  "v_mul_f32 v77, 0xbe0293ee, v77\n\t" \
  CH_DP7 \
  "v_smfmac_f32_16x16x64_bf16 v[56:59], v[24:27], v[104:111], v68\n\t" \
  "v_fmamk_f32 v4, v4, 0x3e0293ee, v77\n\t" \
  "v_smfmac_f32_16x16x64_bf16 v[56:59], v[28:31], v[152:159], v68\n\t" \
  "ds_read_b128 v[24:27], v70 offset:22528\n\t" \
  "s_waitcnt lgkmcnt(4)\n\t" \
  "v_fmamk_f32 v5, v5, 0x3e0293ee, v77\n\t" \
  "v_smfmac_f32_16x16x64_bf16 v[56:59], v[32:35], v[36:43], v68\n\t" \
  "ds_read_b128 v[28:31], v70 offset:23552\n\t" \
  "v_fmamk_f32 v6, v6, 0x3e0293ee, v77\n\t" \
  "v_fmamk_f32 v7, v7, 0x3e0293ee, v77\n\t" \
  "v_fmamk_f32 v76, v76, 0x3e0293ee, v77\n\t" \
  "v_exp_f32 v4, v4\n\t" \
  "v_exp_f32 v5, v5\n\t" \
  "v_exp_f32 v6, v6\n\t" \
  "v_exp_f32 v7, v7\n\t" \
  "v_cvt_pk_bf16_f32 v4, v4, v5\n\t" \
  "v_exp_f32 v76, v76\n\t" \
  "v_cvt_pk_bf16_f32 v5, v6, v7\n\t" \
  "v_mov_b32_dpp v12, v4 quad_perm:[0,1,0,1] row_mask:0xf bank_mask:0xf\n\t" \
  "v_mov_b32_dpp v14, v4 quad_perm:[2,3,2,3] row_mask:0xf bank_mask:0xf\n\t" \
  "v_mov_b32_dpp v13, v5 quad_perm:[0,1,0,1] row_mask:0xf bank_mask:0xf\n\t" \
  "v_mov_b32_dpp v15, v5 quad_perm:[2,3,2,3] row_mask:0xf bank_mask:0xf\n\t" \
  "v_add_f32 v52, v52, v54\n\t" \
  "v_add_f32 v53, v53, v55\n\t" \
  "s_waitcnt vmcnt(2)\n\t" \
  CH_DP8 \
  "v_mfma_f32_16x16x32_bf16 v[0:3], v[60:63], v[12:15], 0\n\t" \
  "ds_read_b128 v[36:39], v70 offset:20480\n\t" \
  "v_add_f32 v56, v56, v58\n\t" \
  "v_add_f32 v57, v57, v59\n\t" \
  "v_cvt_pk_bf16_f32 v52, v52, v53\n\t" \
  "v_cvt_pk_bf16_f32 v53, v56, v57\n\t" \
  "s_waitcnt vmcnt(1)\n\t" \
  "v_mfma_f32_16x16x32_bf16 v[4:7], v[64:67], v[12:15], 0\n\t" \
  "buffer_store_dwordx2 v[52:53], v71, s[28:31], s69 offen offset:2048 sc1\n\t" \
  "ds_read_b128 v[40:43], v70 offset:21504\n\t" \
  "v_mov_b64 v[56:57], s[88:89]\n\t" \
  "v_mov_b64 v[58:59], s[88:89]\n\t" \
  "v_mov_b32_dpp v78, v76 quad_perm:[0,1,0,1] row_mask:0xf bank_mask:0xf\n\t" \
  "v_mov_b32_dpp v79, v76 quad_perm:[2,3,2,3] row_mask:0xf bank_mask:0xf\n\t" \
  "v_mfma_f32_16x16x32_bf16 v[32:35], v[56:59], v[12:15], 0\n\t" \
  "ds_read_b128 v[60:63], v70 offset:24576\n\t" \
  "v_cvt_pk_bf16_f32 v11, v78, v79\n\t" \
  "v_cvt_pk_bf16_f32 v8, v78, v79\n\t" \
  "v_mov_b64 v[54:55], 0\n\t" \
  "v_mov_b32 v9, 0\n\t" \
  "v_mov_b32 v10, 0\n\t" \
  "v_mov_b64 v[14:15], 0\n\t" \
  "s_nop 0\n\t" \
  "v_mfma_f32_16x16x32_bf16 v[32:35], v[54:57], v[8:11], v[32:35]\n\t" \
  "s_cmp_eq_u32 s48, 63\n\t" \
  "v_mfma_f32_16x16x32_bf16 v[0:3], v[52:55], v[8:11], v[0:3]\n\t" \
  "ds_read_b128 v[64:67], v70 offset:25600\n\t" \
  "v_mov_b64 v[56:57], 0\n\t" \
  "v_mov_b64 v[58:59], 0\n\t" \
  "v_mfma_f32_16x16x32_bf16 v[4:7], v[52:55], v[10:13], v[4:7]\n\t" \
  "s_cbranch_scc1 LLAST0_%=\n\t" \
  "v_rcp_f32 v74, v32\n\t" \
  "v_mov_b64 v[12:13], 0\n\t" \
  "v_mov_b32 v8, 0\n\t" \
  "v_pk_mul_f32 v[0:1], v[0:1], v[74:75] op_sel_hi:[1,0]\n\t" \
  "v_pk_mul_f32 v[2:3], v[2:3], v[74:75] op_sel_hi:[1,0]\n\t" \
  "v_cvt_pk_bf16_f32 v76, v0, v1\n\t" \
  "v_cvt_pk_bf16_f32 v77, v2, v3\n\t" \
  "v_pk_mul_f32 v[4:5], v[4:5], v[74:75] op_sel_hi:[1,0]\n\t" \
  "v_pk_mul_f32 v[6:7], v[6:7], v[74:75] op_sel_hi:[1,0]\n\t" \
  "v_cvt_pk_bf16_f32 v78, v4, v5\n\t" \
  "v_cvt_pk_bf16_f32 v79, v6, v7\n\t" \
  "ds_write_b128 v69, v[76:79] offset:4096\n\t" \
  CH_DP9 \
  "s_waitcnt lgkmcnt(2)\n\t" \
  "v_smfmac_f32_16x16x64_bf16 v[12:15], v[76:79], v[44:51], v68\n\t" \
  "ds_read_b128 v[44:47], v70 offset:26624\n\t" \
  "v_mov_b32 v11, 0\n\t" \
  "v_smfmac_f32_16x16x64_bf16 v[56:59], v[76:79], v[16:23], v68\n\t" \
  "ds_read_b128 v[48:51], v70 offset:27648\n\t" \
  "v_mov_b64 v[52:53], 0\n\t" \
  "v_smfmac_f32_16x16x64_bf16 v[8:11], v[76:79], v[36:43], v68\n\t" \
  "ds_read_b128 v[16:19], v70 offset:28672\n\t" \
  "v_smfmac_f32_16x16x64_bf16 v[52:55], v[76:79], v[24:31], v68\n\t" \
  "s_waitcnt lgkmcnt(3)\n\t" \
  "s_barrier\n\t" \
  CH_DP10 \
  "ds_read_b128 v[0:3], v69 offset:5120\n\t" \
  "ds_read_b128 v[4:7], v69 offset:6144\n\t" \
  "v_lshrrev_b32 v71, 7, v69\n\t" \
  "v_and_b32 v74, 0x70, v69\n\t" \
  "v_lshl_add_u32 v71, v71, 14, v74\n\t" \
  "v_bfe_u32 v72, v69, 2, 7\n\t" \
  "v_and_b32 v74, 0x200, v69\n\t" \
  "s_waitcnt lgkmcnt(1)\n\t" \
  "v_smfmac_f32_16x16x64_bf16 v[8:11], v[0:3], v[60:67], v68\n\t" \
  "ds_read_b128 v[20:23], v70 offset:29696\n\t" \
  "v_mov_b64 v[76:77], 0\n\t" \
  "v_smfmac_f32_16x16x64_bf16 v[12:15], v[0:3], v[200:207], v68\n\t" \
  "ds_read_b128 v[32:35], v69 offset:7168\n\t" \
  "v_mov_b64 v[78:79], 0\n\t" \
  "v_smfmac_f32_16x16x64_bf16 v[52:55], v[0:3], v[44:51], v68\n\t" \
  "ds_read_b128 v[24:27], v70 offset:30720\n\t" \
  "v_lshl_add_u32 v72, v74, 3, v72\n\t" \
  "v_smfmac_f32_16x16x64_bf16 v[56:59], v[0:3], v[232:239], v68\n\t" \
  "buffer_load_dword v75, off, s[28:31], s80 sc1\n\t" \
  "ds_read_b128 v[28:31], v70 offset:31744\n\t" \
  "s_waitcnt lgkmcnt(4)\n\t" \
  "v_smfmac_f32_16x16x64_bf16 v[12:15], v[4:7], v[208:215], v68\n\t" \
  "ds_read_b128 v[60:63], v70 offset:32768\n\t" \
  "s_waitcnt lgkmcnt(4)\n\t" \
  "v_smfmac_f32_16x16x64_bf16 v[8:11], v[4:7], v[16:23], v68\n\t" \
  "ds_read_b128 v[64:67], v70 offset:33792\n\t" \
  "s_waitcnt lgkmcnt(4)\n\t" \
  "v_smfmac_f32_16x16x64_bf16 v[8:11], v[32:35], v[192:199], v68\n\t" \
  "ds_read_b128 v[36:39], v73 offset:4096\n\t" \
  "v_smfmac_f32_16x16x64_bf16 v[12:15], v[32:35], v[216:223], v68\n\t" \
  "ds_read_b128 v[40:43], v73 offset:6144\n\t" \
  "s_waitcnt lgkmcnt(4)\n\t" \
  "v_smfmac_f32_16x16x64_bf16 v[52:55], v[4:7], v[24:31], v68\n\t" \
  "ds_read_b128 v[44:47], v70 offset:34816\n\t" \
  "v_cvt_pk_bf16_f32 v8, v8, v9\n\t" \
  "v_smfmac_f32_16x16x64_bf16 v[52:55], v[32:35], v[224:231], v68\n\t" \
  "ds_read_b128 v[48:51], v70 offset:35840\n\t" \
  "v_cvt_pk_bf16_f32 v9, v10, v11\n\t" \
  "v_smfmac_f32_16x16x64_bf16 v[56:59], v[4:7], v[240:247], v68\n\t" \
  "s_waitcnt lgkmcnt(4)\n\t" \
  "v_cvt_pk_bf16_f32 v10, v12, v13\n\t" \
  "v_smfmac_f32_16x16x64_bf16 v[56:59], v[32:35], v[60:67], v68\n\t" \
  "s_waitcnt lgkmcnt(3)\n\t" \
  "v_cvt_pk_bf16_f32 v11, v14, v15\n\t" \
  "v_smfmac_f32_16x16x64_bf16 v[76:79], v[36:39], v[248:255], v68\n\t" \
  "buffer_store_dwordx4 v[8:11], v71, s[28:31], s54 offen sc1\n\t" \
  "s_waitcnt lgkmcnt(0)\n\t" \
  CH_DP11 \
  "v_cvt_pk_bf16_f32 v52, v52, v53\n\t" \
  "v_smfmac_f32_16x16x64_bf16 v[76:79], v[40:43], v[44:51], v68\n\t" \
  "v_cvt_pk_bf16_f32 v53, v54, v55\n\t" \
  "v_cvt_pk_bf16_f32 v54, v56, v57\n\t" \
  "v_cvt_pk_bf16_f32 v55, v58, v59\n\t" \
  "buffer_store_dwordx4 v[52:55], v71, s[28:31], s75 offen sc1\n\t" \
  "s_nop 2\n\t" \
  "v_add_f32 v76, v76, v78\n\t" \
  "v_add_f32 v77, v77, v79\n\t" \
  "v_cvt_pk_bf16_f32 v76, v76, v77\n\t" \
  "buffer_store_dword v76, v72, s[28:31], s55 offen sc1\n\t" \
  CH_DP12 \
  "s_memrealtime s[70:71]\n\t" \
  "s_waitcnt vmcnt(3)\n\t" \
  "v_readfirstlane_b32 s65, v75\n\t" \
  "s_barrier\n\t" \
  "s_waitcnt lgkmcnt(0)\n\t" \
  "s_not_b32 s65, s65\n\t" \
  "s_lshl_b32 s61, s70, 8\n\t" \
  "s_andn2_b32 s64, s65, 0xff\n\t" \
  "s_sub_u32 s64, s64, s61\n\t" \
  "s_ashr_i32 s64, s64, 8\n\t" \
  "s_cmp_eq_u32 s65, 0\n\t" \
  "s_cselect_b32 s64, " CH_DLF ", s64\n\t" \
  "s_cselect_b32 s65, " CH_B0 ", s65\n\t" \
  "s_add_u32 s47, s64, s70\n\t" \
  "s_min_i32 s64, s64, s34\n\t" \
  "s_max_i32 s64, s64, " CH_DLL1 "\n\t" \
  "s_add_u32 s64, s64, s70\n\t" \
  CH_ST2 \
  "s_add_u32 s55, s64, " CH_FDL "\n\t" \
  "s_movk_i32 s60, 8192\n\t" \
  "FWT_%=:\n\t" \
  "s_memrealtime s[62:63]\n\t" \
  "s_sub_u32 s60, s60, 1\n\t" \
  "s_cselect_b32 s61, 0, -1\n\t" \
  "s_waitcnt lgkmcnt(0)\n\t" \
  "s_sub_i32 s63, s62, s55\n\t" \
  "s_and_b32 s63, s63, s61\n\t" \
  "s_cmp_lt_i32 s63, 0\n\t" \
  "s_cbranch_scc1 FWT_%=\n\t" \
  "s_mov_b64 exec, 1\n\t" \
  "buffer_store_dword v68, off, s[28:31], s51 offset:4064 sc1\n\t" \
  "s_mov_b64 exec, -1\n\t" \
  "s_and_b32 s65, s65, 0xff\n\t" \
  "s_add_u32 s47, s47, s65\n\t" \
  "s_add_u32 s65, s65, s64\n\t" \
  "s_sub_u32 s60, s70, s66\n\t" \
  "s_add_u32 s61, s81, " CH_UP "\n\t" \
  "s_min_u32 s60, s60, s61\n\t" \
  "s_sub_u32 s61, s81, " CH_DN "\n\t" \
  "s_max_u32 s60, s60, s61\n\t" \
  "s_cmp_eq_u32 s68, 0\n\t" \
  "s_cselect_b32 s81, s60, s81\n\t" \
  CH_TRC \
  "s_add_u32 s48, s48, 1\n\t" \
  "s_mov_b32 s49, s53\n\t" \
  "s_add_u32 s53, s53, " CH_SLOT "\n\t" \
  "s_add_u32 s60, s53, " CH_SKIP "\n\t" \
  "s_lshr_b32 s61, s96, 1\n\t" \
  "s_add_u32 s86, s86, 1\n\t" \
  "s_cmp_eq_u32 s86, 3\n\t" \
  "s_cselect_b32 s53, s60, s53\n\t" \
  "s_cselect_b32 s96, s61, s96\n\t" \
  "s_cselect_b32 s86, 0, s86\n\t" \
  "s_andn2_b32 s53, s53, 0x2000\n\t" \
  "s_and_b32 s60, s96, 1\n\t" \
  "s_lshl_b32 s60, s60, 13\n\t" \
  "s_or_b32 s53, s53, s60\n\t" \
  "s_mov_b32 s68, 0\n\t" \
  "s_add_u32 s82, s48, 0\n\t" \
  CH_SEGC \
  "s_lshr_b64 s[60:61], s[90:91], s82\n\t" \
  "s_and_b32 s60, s60, 1\n\t" \
  "s_lshl_b32 s60, s60, 13\n\t" \
  "s_add_u32 s82, s82, " CH_SG0 "\n\t" \
  "s_lshl_b32 s82, s82, 21\n\t" \
  "s_or_b32 s82, s82, s60\n\t" \
  "s_add_u32 s50, s82, s38\n\t" \
  "s_add_u32 s69, s82, s39\n\t" \
  "s_add_u32 s51, s82, s43\n\t" \
  "s_cmp_lg_u32 s34, 0\n\t" \
  "s_cbranch_scc1 LW0_%=\n\t" \
  "s_barrier\n\t" \
  "s_branch LINI_%=\n\t" \
  "LFAIL0_%=:\n\t" \
  "v_max_u32 v75, v0, v1\n\t" \
  "v_max3_u32 v75, v75, v2, v3\n\t" \
  "v_max3_u32 v75, v75, v4, v5\n\t" \
  "v_max3_u32 v75, v75, v6, v7\n\t" \
  "v_max3_u32 v75, v75, v8, v9\n\t" \
  "v_max3_u32 v75, v75, v10, v11\n\t" \
  "v_max3_u32 v75, v75, v12, v13\n\t" \
  "v_max3_u32 v75, v75, v14, v15\n\t" \
  "v_max3_u32 v75, v75, v16, v17\n\t" \
  "v_max3_u32 v75, v75, v18, v19\n\t" \
  "v_max3_u32 v75, v75, v20, v21\n\t" \
  "v_max3_u32 v75, v75, v22, v23\n\t" \
  "v_max3_u32 v75, v75, v24, v25\n\t" \
  "v_max3_u32 v75, v75, v26, v27\n\t" \
  "v_max3_u32 v75, v75, v28, v29\n\t" \
  "v_max3_u32 v75, v75, v30, v31\n\t" \
  "v_max3_u32 v75, v75, v32, v33\n\t" \
  "v_max3_u32 v75, v75, v34, v35\n\t" \
  "v_cmp_eq_u32 vcc, -1, v75\n\t" \
  "s_cbranch_vccz LGO0_%=\n\t" \
  "s_add_u32 s68, s68, 1\n\t" \
  "s_waitcnt lgkmcnt(0)\n\t" \
  "s_cmp_gt_u32 s68, s83\n\t" \
  "s_cbranch_scc1 LGV0_%=\n\t" \
  "s_barrier\n\t" \
  "s_memrealtime s[62:63]\n\t" \
  "s_mov_b32 m0, 0\n\t" \
  "s_waitcnt lgkmcnt(0)\n\t" \
  "s_add_u32 s65, s62, " CH_PEN "\n\t" \
  "s_branch LDMA_%=\n\t" \
  "LWT_%=f:\n\t" \
  "s_cmp_lt_i32 s63, -2\n\t" \
  "s_cbranch_scc0 LWT_%=g\n\t" \
  "s_nop 4\n\t" \
  "s_branch LWT_%=g\n\t" \
  "LGV0_%=:\n\t" \
  "s_mov_b32 s83, 0\n\t" \
  "s_lshl_b32 s60, s36, 4\n\t" \
  "s_or_b32 s60, s60, s48\n\t" \
  "s_or_b32 s60, s60, 805306368\n\t" \
  "v_mov_b32 v76, s60\n\t" \
  "v_mov_b32 v77, 0\n\t" \
  "s_mov_b64 exec, 1\n\t" \
  "buffer_atomic_cmpswap v[76:77], off, s[28:31], s92 sc1\n\t" \
  "s_mov_b64 exec, -1\n\t" \
  "s_branch LGO0_%=\n\t" \
  "LLAST0_%=:\n\t" \
  "s_waitcnt vmcnt(0) lgkmcnt(0)\n\t" \
  "s_barrier\n\t" \
  "s_mov_b64 exec, 1\n\t" \
  "buffer_store_dword v68, off, s[28:31], s51 offset:4064 sc1\n\t" \
  "s_mov_b64 exec, -1\n\t" \
  "s_branch LEND_%=\n\t" \
  ".p2align 6\n\t" \
  "LG1_%=:\n\t" \
  "s_barrier\n\t" \
  "buffer_load_dwordx4 v[52:55], v69, s[28:31], s50 offen sc1\n\t" \
  "buffer_load_dwordx4 v[56:59], v69, s[28:31], s50 offen offset:1024 sc1\n\t" \
  "s_add_u32 s64, s50, 14336\n\t" \
  "s_movk_i32 m0, 11264\n\t" \
  "v_lshlrev_b32 v75, 1, v69\n\t" \
  "buffer_load_dword v75, s[28:31], s64 offen sc1 lds\n\t" \
  "ds_read_b128 v[44:47], v70 offset:0\n\t" \
  "ds_read_b128 v[48:51], v70 offset:1024\n\t" \
  "ds_read_b128 v[60:63], v70 offset:2048\n\t" \
  "ds_read_b128 v[64:67], v70 offset:3072\n\t" \
  "v_mov_b64 v[36:37], 0\n\t" \
  "v_mov_b64 v[38:39], 0\n\t" \
  "v_mov_b64 v[40:41], 0\n\t" \
  "v_mov_b64 v[42:43], 0\n\t" \
  "v_lshrrev_b32 v71, 1, v69\n\t" \
  "v_add_u32 v71, 4, v71\n\t" \
  "v_lshrrev_b32 v72, 3, v69\n\t" \
  "v_and_b32 v72, 0x78, v72\n\t" \
  "v_and_b32 v74, 0x30, v69\n\t" \
  "v_lshl_add_u32 v72, v74, 3, v72\n\t" \
  "s_add_u32 s54, s53, s36\n\t" \
  "s_add_u32 s55, s54, 540672\n\t" \
  "s_add_u32 s54, s54, 4096\n\t" \
  "s_add_u32 s75, s54, 262144\n\t" \
  "s_add_u32 s80, s53, s26\n\t" \
  "s_barrier\n\t" \
  CH_DP0 \
  "ds_read_b128 v[0:3], v69 offset:0\n\t" \
  "ds_read_b128 v[4:7], v69 offset:1024\n\t" \
  "ds_read_b128 v[8:11], v69 offset:2048\n\t" \
  "ds_read_b128 v[12:15], v69 offset:3072\n\t" \
  "s_waitcnt lgkmcnt(3)\n\t" \
  "v_smfmac_f32_16x16x64_bf16 v[36:39], v[0:3], v[44:51], v68\n\t" \
  "ds_read_b128 v[44:47], v70 offset:4096\n\t" \
  "v_smfmac_f32_16x16x64_bf16 v[40:43], v[0:3], v[60:67], v68\n\t" \
  "ds_read_b128 v[48:51], v70 offset:5120\n\t" \
  "s_waitcnt lgkmcnt(4)\n\t" \
  "v_smfmac_f32_16x16x64_bf16 v[36:39], v[4:7], a[32:39], v68\n\t" \
  "ds_read_b128 v[60:63], v70 offset:6144\n\t" \
  "v_smfmac_f32_16x16x64_bf16 v[40:43], v[4:7], a[40:47], v68\n\t" \
  "ds_read_b128 v[64:67], v70 offset:7168\n\t" \
  "s_waitcnt lgkmcnt(5)\n\t" \
  "v_smfmac_f32_16x16x64_bf16 v[36:39], v[8:11], a[80:87], v68\n\t" \
  "s_barrier\n\t" \
  CH_DP1 \
  "ds_read_b128 v[16:19], v69 offset:4096\n\t" \
  "v_smfmac_f32_16x16x64_bf16 v[40:43], v[8:11], a[88:95], v68\n\t" \
  "ds_read_b128 v[20:23], v69 offset:5120\n\t" \
  "s_waitcnt lgkmcnt(6)\n\t" \
  "v_smfmac_f32_16x16x64_bf16 v[36:39], v[12:15], a[120:127], v68\n\t" \
  "ds_read_b128 v[24:27], v69 offset:6144\n\t" \
  "s_waitcnt lgkmcnt(5)\n\t" \
  "v_smfmac_f32_16x16x64_bf16 v[40:43], v[12:15], v[44:51], v68\n\t" \
  "ds_read_b128 v[28:31], v69 offset:7168\n\t" \
  "s_waitcnt lgkmcnt(3)\n\t" \
  "v_smfmac_f32_16x16x64_bf16 v[36:39], v[16:19], a[160:167], v68\n\t" \
  "ds_read_b128 v[32:35], v69 offset:8192\n\t" \
  "v_smfmac_f32_16x16x64_bf16 v[40:43], v[16:19], a[168:175], v68\n\t" \
  "s_waitcnt lgkmcnt(3)\n\t" \
  "v_mov_b64 v[44:45], 0\n\t" \
  "v_smfmac_f32_16x16x64_bf16 v[36:39], v[20:23], a[200:207], v68\n\t" \
  "s_waitcnt lgkmcnt(2)\n\t" \
  "v_mov_b64 v[46:47], 0\n\t" \
  "v_smfmac_f32_16x16x64_bf16 v[36:39], v[24:27], a[240:247], v68\n\t" \
  "s_waitcnt lgkmcnt(1)\n\t" \
  "v_mov_b64 v[48:49], 0\n\t" \
  "v_smfmac_f32_16x16x64_bf16 v[36:39], v[28:31], v[112:119], v68\n\t" \
  "s_waitcnt lgkmcnt(0)\n\t" \
  "v_mov_b64 v[50:51], 0\n\t" \
  "v_smfmac_f32_16x16x64_bf16 v[36:39], v[32:35], v[160:167], v68\n\t" \
  "v_smfmac_f32_16x16x64_bf16 v[40:43], v[20:23], v[60:67], v68\n\t" \
  "ds_read_b128 v[60:63], v70 offset:8192\n\t" \
  "v_smfmac_f32_16x16x64_bf16 v[40:43], v[24:27], a[248:255], v68\n\t" \
  "ds_read_b128 v[64:67], v70 offset:9216\n\t" \
  "v_add_f32 v74, v36, v38\n\t" \
  "v_smfmac_f32_16x16x64_bf16 v[40:43], v[28:31], v[120:127], v68\n\t" \
  "v_add_f32 v37, v37, v39\n\t" \
  "v_smfmac_f32_16x16x64_bf16 v[40:43], v[32:35], v[168:175], v68\n\t" \
  "v_cmp_u_f32 vcc, v74, v37\n\t" \
  CH_DP2 \
  "v_smfmac_f32_16x16x64_bf16 v[44:47], v[0:3], a[0:7], v68\n\t" \
  "s_cbranch_vccnz LFAIL1_%=\n\t" \
  "LGO1_%=:\n\t" \
  "v_cvt_pk_bf16_f32 v74, v74, v37\n\t" \
  "v_smfmac_f32_16x16x64_bf16 v[44:47], v[4:7], a[48:55], v68\n\t" \
  "s_waitcnt lgkmcnt(0)\n\t" \
  "v_add_f32 v75, v40, v42\n\t" \
  "v_smfmac_f32_16x16x64_bf16 v[44:47], v[8:11], v[60:67], v68\n\t" \
  "ds_read_b128 v[60:63], v70 offset:10240\n\t" \
  "v_add_f32 v41, v41, v43\n\t" \
  "v_cvt_pk_bf16_f32 v75, v75, v41\n\t" \
  "v_smfmac_f32_16x16x64_bf16 v[44:47], v[12:15], a[128:135], v68\n\t" \
  "ds_write2st64_b32 v71, v74, v75 offset0:36 offset1:38\n\t" \
  "v_smfmac_f32_16x16x64_bf16 v[44:47], v[16:19], a[176:183], v68\n\t" \
  "ds_read_b128 v[64:67], v70 offset:11264\n\t" \
  "v_smfmac_f32_16x16x64_bf16 v[44:47], v[20:23], a[208:215], v68\n\t" \
  "v_smfmac_f32_16x16x64_bf16 v[44:47], v[24:27], v[80:87], v68\n\t" \
  "v_smfmac_f32_16x16x64_bf16 v[44:47], v[28:31], v[128:135], v68\n\t" \
  "s_waitcnt lgkmcnt(0)\n\t" \
  "v_smfmac_f32_16x16x64_bf16 v[44:47], v[32:35], v[60:67], v68\n\t" \
  "s_barrier\n\t" \
  "ds_read_b64_tr_b16 v[76:77], v72 offset:9216\n\t" \
  CH_DP3 \
  "v_smfmac_f32_16x16x64_bf16 v[48:51], v[0:3], a[8:15], v68\n\t" \
  "buffer_load_dwordx4 v[60:63], v69, s[28:31], s64 offen sc1\n\t" \
  "ds_read_b64_tr_b16 v[78:79], v72 offset:9728\n\t" \
  "v_smfmac_f32_16x16x64_bf16 v[48:51], v[4:7], a[56:63], v68\n\t" \
  "v_add_f32 v74, v44, v46\n\t" \
  "v_smfmac_f32_16x16x64_bf16 v[48:51], v[8:11], a[96:103], v68\n\t" \
  "v_add_f32 v45, v45, v47\n\t" \
  "v_smfmac_f32_16x16x64_bf16 v[48:51], v[12:15], a[136:143], v68\n\t" \
  "s_waitcnt vmcnt(3)\n\t" \
  "s_waitcnt lgkmcnt(0)\n\t" \
  "v_cvt_pk_bf16_f32 v74, v74, v45\n\t" \
  "v_mfma_f32_16x16x32_bf16 v[36:39], v[52:55], v[76:79], 0\n\t" \
  "s_waitcnt vmcnt(2)\n\t" \
  "v_mfma_f32_16x16x32_bf16 v[40:43], v[56:59], v[76:79], 0\n\t" \
  "buffer_load_dwordx4 v[64:67], v69, s[28:31], s64 offen offset:1024 sc1\n\t" \
  "v_mov_b64 v[52:53], 0\n\t" \
  "v_mov_b64 v[54:55], 0\n\t" \
  "v_smfmac_f32_16x16x64_bf16 v[48:51], v[16:19], a[184:191], v68\n\t" \
  "v_mov_b64 v[56:57], 0\n\t" \
  "v_mov_b64 v[58:59], 0\n\t" \
  "v_smfmac_f32_16x16x64_bf16 v[48:51], v[20:23], a[216:223], v68\n\t" \
  "ds_write_b32 v71, v74 offset:0\n\t" \
  "v_add_f32_dpp v36, v40, v36 quad_perm:[2,3,0,1] row_mask:0xf bank_mask:0xf\n\t" \
  "v_smfmac_f32_16x16x64_bf16 v[48:51], v[24:27], v[88:95], v68\n\t" \
  "v_add_f32_dpp v37, v41, v37 quad_perm:[2,3,0,1] row_mask:0xf bank_mask:0xf\n\t" \
  "v_smfmac_f32_16x16x64_bf16 v[48:51], v[28:31], v[136:143], v68\n\t" \
  "v_add_f32_dpp v38, v42, v38 quad_perm:[2,3,0,1] row_mask:0xf bank_mask:0xf\n\t" \
  "v_smfmac_f32_16x16x64_bf16 v[48:51], v[32:35], v[176:183], v68\n\t" \
  "v_add_f32_dpp v39, v43, v39 quad_perm:[2,3,0,1] row_mask:0xf bank_mask:0xf\n\t" \
  CH_DP4 \
  "v_smfmac_f32_16x16x64_bf16 v[52:55], v[0:3], a[16:23], v68\n\t" \
  "s_mov_b64 exec, s[58:59]\n\t" \
  "ds_write_b128 v69, v[36:39] offset:2080\n\t" \
  "s_mov_b64 exec, -1\n\t" \
  "v_smfmac_f32_16x16x64_bf16 v[52:55], v[4:7], a[64:71], v68\n\t" \
  "v_add_f32 v75, v48, v50\n\t" \
  "v_add_f32 v49, v49, v51\n\t" \
  "v_cvt_pk_bf16_f32 v75, v75, v49\n\t" \
  "v_smfmac_f32_16x16x64_bf16 v[56:59], v[0:3], a[24:31], v68\n\t" \
  "ds_write_b32 v71, v75 offset:512\n\t" \
  "v_smfmac_f32_16x16x64_bf16 v[56:59], v[4:7], a[72:79], v68\n\t" \
  "ds_read_b64_tr_b16 v[0:1], v72 offset:10240\n\t" \
  "ds_read_b64_tr_b16 v[2:3], v72 offset:10752\n\t" \
  "v_smfmac_f32_16x16x64_bf16 v[52:55], v[8:11], a[104:111], v68\n\t" \
  "ds_read_b128 v[36:39], v70 offset:12288\n\t" \
  "v_smfmac_f32_16x16x64_bf16 v[56:59], v[8:11], a[112:119], v68\n\t" \
  "ds_read_b128 v[40:43], v70 offset:13312\n\t" \
  "v_smfmac_f32_16x16x64_bf16 v[52:55], v[12:15], a[144:151], v68\n\t" \
  "s_waitcnt lgkmcnt(4)\n\t" \
  "s_barrier\n\t" \
  CH_DP5 \
  "ds_read_b64_tr_b16 v[44:45], v72 offset:0\n\t" \
  "ds_read_b64_tr_b16 v[46:47], v72 offset:512\n\t" \
  "v_smfmac_f32_16x16x64_bf16 v[56:59], v[12:15], a[152:159], v68\n\t" \
  "ds_read_b64_tr_b16 v[48:49], v72 offset:1024\n\t" \
  "ds_read_b64_tr_b16 v[50:51], v72 offset:1536\n\t" \
  "v_smfmac_f32_16x16x64_bf16 v[52:55], v[16:19], a[192:199], v68\n\t" \
  "ds_read_b128 v[4:7], v69 offset:2048\n\t" \
  "s_waitcnt lgkmcnt(5)\n\t" \
  "v_smfmac_f32_16x16x64_bf16 v[56:59], v[16:19], v[36:43], v68\n\t" \
  "ds_read_b128 v[8:11], v69 offset:3072\n\t" \
  "s_waitcnt lgkmcnt(4)\n\t" \
  "v_mfma_f32_16x16x32_bf16 v[12:15], v[44:47], v[76:79], 0\n\t" \
  "ds_read_b128 v[36:39], v70 offset:14336\n\t" \
  "s_waitcnt lgkmcnt(3)\n\t" \
  "v_mfma_f32_16x16x32_bf16 v[12:15], v[48:51], v[0:3], v[12:15]\n\t" \
  "buffer_store_dwordx4 v[44:47], v69, s[28:31], s69 offen offset:0 sc1\n\t" \
  "ds_read_b128 v[40:43], v70 offset:15360\n\t" \
  "v_smfmac_f32_16x16x64_bf16 v[52:55], v[20:23], a[224:231], v68\n\t" \
  "s_waitcnt lgkmcnt(2)\n\t" \
  "v_add_f32 v4, v4, v8\n\t" \
  "v_add_f32 v5, v5, v9\n\t" \
  "v_add_f32 v6, v6, v10\n\t" \
  CH_DP6 \
  "v_cndmask_b32 v78, v13, v14, s[56:57]\n\t" \
  "v_cndmask_b32 v76, v12, v15, s[56:57]\n\t" \
  "v_add_f32 v7, v7, v11\n\t" \
  "v_max3_f32 v77, v4, v5, v6\n\t" \
  "v_add_f32_dpp v76, v78, v76 quad_perm:[2,3,0,1] row_mask:0xf bank_mask:0xf\n\t" \
  "v_max3_f32 v77, v77, v7, v76\n\t" \
  "v_smfmac_f32_16x16x64_bf16 v[56:59], v[20:23], a[232:239], v68\n\t" \
  "ds_read_b128 v[44:47], v70 offset:16384\n\t" \
  "v_max_f32_dpp v77, v77, v77 quad_perm:[2,3,0,1] row_mask:0xf bank_mask:0xf\n\t" \
  "v_mov_b32 v78, v77\n\t" \
  "v_smfmac_f32_16x16x64_bf16 v[52:55], v[24:27], v[96:103], v68\n\t" \
  "ds_read_b128 v[48:51], v70 offset:17408\n\t" \
  "v_permlane16_swap_b32 v77, v78\n\t" \
  "v_max_f32 v77, v77, v78\n\t" \
  "v_mov_b32 v78, v77\n\t" \
  "v_smfmac_f32_16x16x64_bf16 v[52:55], v[28:31], v[144:151], v68\n\t" \
  "ds_read_b128 v[16:19], v70 offset:18432\n\t" \
  "v_permlane32_swap_b32 v77, v78\n\t" \
  "v_max_f32 v77, v77, v78\n\t" \
  "v_smfmac_f32_16x16x64_bf16 v[52:55], v[32:35], v[184:191], v68\n\t" \
  "ds_read_b128 v[20:23], v70 offset:19456\n\t" \
  "v_mul_f32 v77, 0xbe0293ee, v77\n\t" \
  CH_DP7 \
  "v_smfmac_f32_16x16x64_bf16 v[56:59], v[24:27], v[104:111], v68\n\t" \
  "v_fmamk_f32 v4, v4, 0x3e0293ee, v77\n\t" \
  "v_smfmac_f32_16x16x64_bf16 v[56:59], v[28:31], v[152:159], v68\n\t" \
  "ds_read_b128 v[24:27], v70 offset:22528\n\t" \
  "s_waitcnt lgkmcnt(4)\n\t" \
  "v_fmamk_f32 v5, v5, 0x3e0293ee, v77\n\t" \
  "v_smfmac_f32_16x16x64_bf16 v[56:59], v[32:35], v[36:43], v68\n\t" \
  "ds_read_b128 v[28:31], v70 offset:23552\n\t" \
  "v_fmamk_f32 v6, v6, 0x3e0293ee, v77\n\t" \
  "v_fmamk_f32 v7, v7, 0x3e0293ee, v77\n\t" \
  "v_fmamk_f32 v76, v76, 0x3e0293ee, v77\n\t" \
  "v_exp_f32 v4, v4\n\t" \
  "v_exp_f32 v5, v5\n\t" \
  "v_exp_f32 v6, v6\n\t" \
  "v_exp_f32 v7, v7\n\t" \
  "v_cvt_pk_bf16_f32 v4, v4, v5\n\t" \
  "v_exp_f32 v76, v76\n\t" \
  "v_cvt_pk_bf16_f32 v5, v6, v7\n\t" \
  "v_mov_b32_dpp v12, v4 quad_perm:[0,1,0,1] row_mask:0xf bank_mask:0xf\n\t" \
  "v_mov_b32_dpp v14, v4 quad_perm:[2,3,2,3] row_mask:0xf bank_mask:0xf\n\t" \
  "v_mov_b32_dpp v13, v5 quad_perm:[0,1,0,1] row_mask:0xf bank_mask:0xf\n\t" \
  "v_mov_b32_dpp v15, v5 quad_perm:[2,3,2,3] row_mask:0xf bank_mask:0xf\n\t" \
  "v_add_f32 v52, v52, v54\n\t" \
  "v_add_f32 v53, v53, v55\n\t" \
  "s_waitcnt vmcnt(2)\n\t" \
  CH_DP8 \
  "v_mfma_f32_16x16x32_bf16 v[0:3], v[60:63], v[12:15], 0\n\t" \
  "ds_read_b128 v[36:39], v70 offset:20480\n\t" \
  "v_add_f32 v56, v56, v58\n\t" \
  "v_add_f32 v57, v57, v59\n\t" \
  "v_cvt_pk_bf16_f32 v52, v52, v53\n\t" \
  "v_cvt_pk_bf16_f32 v53, v56, v57\n\t" \
  "s_waitcnt vmcnt(1)\n\t" \
  "v_mfma_f32_16x16x32_bf16 v[4:7], v[64:67], v[12:15], 0\n\t" \
  "ds_read_b128 v[40:43], v70 offset:21504\n\t" \
  "v_mov_b64 v[56:57], s[88:89]\n\t" \
  "v_mov_b64 v[58:59], s[88:89]\n\t" \
  "v_mov_b32_dpp v78, v76 quad_perm:[0,1,0,1] row_mask:0xf bank_mask:0xf\n\t" \
  "v_mov_b32_dpp v79, v76 quad_perm:[2,3,2,3] row_mask:0xf bank_mask:0xf\n\t" \
  "v_mfma_f32_16x16x32_bf16 v[32:35], v[56:59], v[12:15], 0\n\t" \
  "buffer_store_dwordx2 v[52:53], v71, s[28:31], s69 offen offset:2556 sc1\n\t" \
  "ds_read_b128 v[60:63], v70 offset:24576\n\t" \
  "v_cvt_pk_bf16_f32 v11, v78, v79\n\t" \
  "v_cvt_pk_bf16_f32 v8, v78, v79\n\t" \
  "v_mov_b64 v[54:55], 0\n\t" \
  "v_mov_b32 v9, 0\n\t" \
  "v_mov_b32 v10, 0\n\t" \
  "v_mov_b64 v[14:15], 0\n\t" \
  "s_nop 0\n\t" \
  "v_mfma_f32_16x16x32_bf16 v[32:35], v[54:57], v[8:11], v[32:35]\n\t" \
  "s_cmp_eq_u32 s48, 63\n\t" \
  "v_mfma_f32_16x16x32_bf16 v[0:3], v[52:55], v[8:11], v[0:3]\n\t" \
  "ds_read_b128 v[64:67], v70 offset:25600\n\t" \
  "v_mov_b64 v[56:57], 0\n\t" \
  "v_mov_b64 v[58:59], 0\n\t" \
  "v_mfma_f32_16x16x32_bf16 v[4:7], v[52:55], v[10:13], v[4:7]\n\t" \
  "s_cbranch_scc1 LLAST1_%=\n\t" \
  "v_rcp_f32 v74, v32\n\t" \
  "v_mov_b64 v[12:13], 0\n\t" \
  "v_mov_b32 v8, 0\n\t" \
  "v_pk_mul_f32 v[0:1], v[0:1], v[74:75] op_sel_hi:[1,0]\n\t" \
  "v_pk_mul_f32 v[2:3], v[2:3], v[74:75] op_sel_hi:[1,0]\n\t" \
  "v_cvt_pk_bf16_f32 v76, v0, v1\n\t" \
  "v_cvt_pk_bf16_f32 v77, v2, v3\n\t" \
  "v_pk_mul_f32 v[4:5], v[4:5], v[74:75] op_sel_hi:[1,0]\n\t" \
  "v_pk_mul_f32 v[6:7], v[6:7], v[74:75] op_sel_hi:[1,0]\n\t" \
  "v_cvt_pk_bf16_f32 v78, v4, v5\n\t" \
  "v_cvt_pk_bf16_f32 v79, v6, v7\n\t" \
  "ds_write_b128 v69, v[76:79] offset:5120\n\t" \
  CH_DP9 \
  "s_waitcnt lgkmcnt(2)\n\t" \
  "v_smfmac_f32_16x16x64_bf16 v[12:15], v[76:79], v[44:51], v68\n\t" \
  "ds_read_b128 v[44:47], v70 offset:26624\n\t" \
  "v_mov_b32 v11, 0\n\t" \
  "v_smfmac_f32_16x16x64_bf16 v[56:59], v[76:79], v[16:23], v68\n\t" \
  "ds_read_b128 v[48:51], v70 offset:27648\n\t" \
  "v_mov_b64 v[52:53], 0\n\t" \
  "v_smfmac_f32_16x16x64_bf16 v[8:11], v[76:79], v[36:43], v68\n\t" \
  "ds_read_b128 v[16:19], v70 offset:28672\n\t" \
  "v_smfmac_f32_16x16x64_bf16 v[52:55], v[76:79], v[24:31], v68\n\t" \
  "s_waitcnt lgkmcnt(3)\n\t" \
  "s_barrier\n\t" \
  CH_DP10 \
  "ds_read_b128 v[0:3], v69 offset:4096\n\t" \
  "ds_read_b128 v[4:7], v69 offset:7168\n\t" \
  "v_lshrrev_b32 v71, 7, v69\n\t" \
  "v_and_b32 v74, 0x70, v69\n\t" \
  "v_lshl_add_u32 v71, v71, 14, v74\n\t" \
  "v_bfe_u32 v72, v69, 2, 7\n\t" \
  "v_and_b32 v74, 0x200, v69\n\t" \
  "s_waitcnt lgkmcnt(1)\n\t" \
  "v_smfmac_f32_16x16x64_bf16 v[8:11], v[0:3], v[60:67], v68\n\t" \
  "buffer_load_dword v75, off, s[28:31], s80 sc1\n\t" \
  "ds_read_b128 v[20:23], v70 offset:29696\n\t" \
  "v_mov_b64 v[76:77], 0\n\t" \
  "v_smfmac_f32_16x16x64_bf16 v[12:15], v[0:3], v[200:207], v68\n\t" \
  "ds_read_b128 v[32:35], v69 offset:6144\n\t" \
  "v_mov_b64 v[78:79], 0\n\t" \
  "v_smfmac_f32_16x16x64_bf16 v[52:55], v[0:3], v[44:51], v68\n\t" \
  "ds_read_b128 v[24:27], v70 offset:30720\n\t" \
  "v_lshl_add_u32 v72, v74, 3, v72\n\t" \
  "v_smfmac_f32_16x16x64_bf16 v[56:59], v[0:3], v[232:239], v68\n\t" \
  "ds_read_b128 v[28:31], v70 offset:31744\n\t" \
  "s_waitcnt lgkmcnt(4)\n\t" \
  "v_smfmac_f32_16x16x64_bf16 v[12:15], v[4:7], v[208:215], v68\n\t" \
  "ds_read_b128 v[60:63], v70 offset:32768\n\t" \
  "s_waitcnt lgkmcnt(4)\n\t" \
  "v_smfmac_f32_16x16x64_bf16 v[8:11], v[4:7], v[16:23], v68\n\t" \
  "ds_read_b128 v[64:67], v70 offset:33792\n\t" \
  "s_waitcnt lgkmcnt(4)\n\t" \
  "v_smfmac_f32_16x16x64_bf16 v[8:11], v[32:35], v[192:199], v68\n\t" \
  "ds_read_b128 v[36:39], v73 offset:4096\n\t" \
  "v_smfmac_f32_16x16x64_bf16 v[12:15], v[32:35], v[216:223], v68\n\t" \
  "ds_read_b128 v[40:43], v73 offset:6144\n\t" \
  "s_waitcnt lgkmcnt(4)\n\t" \
  "v_smfmac_f32_16x16x64_bf16 v[52:55], v[4:7], v[24:31], v68\n\t" \
  "ds_read_b128 v[44:47], v70 offset:34816\n\t" \
  "v_cvt_pk_bf16_f32 v8, v8, v9\n\t" \
  "v_smfmac_f32_16x16x64_bf16 v[52:55], v[32:35], v[224:231], v68\n\t" \
  "ds_read_b128 v[48:51], v70 offset:35840\n\t" \
  "v_cvt_pk_bf16_f32 v9, v10, v11\n\t" \
  "v_smfmac_f32_16x16x64_bf16 v[56:59], v[4:7], v[240:247], v68\n\t" \
  "s_waitcnt lgkmcnt(4)\n\t" \
  "v_cvt_pk_bf16_f32 v10, v12, v13\n\t" \
  "v_smfmac_f32_16x16x64_bf16 v[56:59], v[32:35], v[60:67], v68\n\t" \
  "s_waitcnt lgkmcnt(3)\n\t" \
  "v_cvt_pk_bf16_f32 v11, v14, v15\n\t" \
  "v_smfmac_f32_16x16x64_bf16 v[76:79], v[36:39], v[248:255], v68\n\t" \
  "buffer_store_dwordx4 v[8:11], v71, s[28:31], s54 offen sc1\n\t" \
  "s_waitcnt lgkmcnt(0)\n\t" \
  CH_DP11 \
  "v_cvt_pk_bf16_f32 v52, v52, v53\n\t" \
  "v_smfmac_f32_16x16x64_bf16 v[76:79], v[40:43], v[44:51], v68\n\t" \
  "v_cvt_pk_bf16_f32 v53, v54, v55\n\t" \
  "v_cvt_pk_bf16_f32 v54, v56, v57\n\t" \
  "v_cvt_pk_bf16_f32 v55, v58, v59\n\t" \
  "buffer_store_dwordx4 v[52:55], v71, s[28:31], s75 offen sc1\n\t" \
  "s_nop 2\n\t" \
  "v_add_f32 v76, v76, v78\n\t" \
  "v_add_f32 v77, v77, v79\n\t" \
  "v_cvt_pk_bf16_f32 v76, v76, v77\n\t" \
  "buffer_store_dword v76, v72, s[28:31], s55 offen sc1\n\t" \
  CH_DP12 \
  "s_memrealtime s[70:71]\n\t" \
  "s_waitcnt vmcnt(3)\n\t" \
  "v_readfirstlane_b32 s65, v75\n\t" \
  "s_barrier\n\t" \
  "s_waitcnt lgkmcnt(0)\n\t" \
  "s_not_b32 s65, s65\n\t" \
  "s_lshl_b32 s61, s70, 8\n\t" \
  "s_andn2_b32 s64, s65, 0xff\n\t" \
  "s_sub_u32 s64, s64, s61\n\t" \
  "s_ashr_i32 s64, s64, 8\n\t" \
  "s_cmp_eq_u32 s65, 0\n\t" \
  "s_cselect_b32 s64, " CH_DLF ", s64\n\t" \
  "s_sub_i32 s64, s64, " CH_RO1 "\n\t" \
  "s_min_i32 s64, s64, s34\n\t" \
  "s_max_i32 s64, s64, " CH_DLL1 "\n\t" \
  "s_add_u32 s64, s64, s70\n\t" \
  CH_ST2 \
  "s_mov_b32 s74, 0\n\t" \
  "s_movk_i32 s60, 8192\n\t" \
  "RWT1_%=:\n\t" \
  "s_memrealtime s[72:73]\n\t" \
  "s_sub_u32 s60, s60, 1\n\t" \
  "s_cselect_b32 s61, 0, -1\n\t" \
  "s_waitcnt lgkmcnt(0)\n\t" \
  "s_sub_i32 s73, s72, s64\n\t" \
  "s_and_b32 s73, s73, s61\n\t" \
  "s_cmp_lt_i32 s73, " CH_FIN "\n\t" \
  "s_cbranch_scc1 RWT1_%=\n\t" \
  "s_cmp_lt_i32 s73, 0\n\t" \
  "s_cbranch_scc1 RWT1_%=f\n\t" \
  "RWT1_%=g:\n\t" \
  "RRETRY1_%=:\n\t" \
  CH_DP13 \
  "buffer_load_dwordx4 v[0:3], v69, s[28:31], s97 offen offset:0 sc1\n\t" \
  "buffer_load_dwordx4 v[4:7], v69, s[28:31], s97 offen offset:1024 sc1\n\t" \
  "buffer_load_dwordx4 v[8:11], v69, s[28:31], s97 offen offset:2048 sc1\n\t" \
  "buffer_load_dwordx4 v[12:15], v69, s[28:31], s97 offen offset:3072 sc1\n\t" \
  "v_lshrrev_b32 v20, 2, v69\n\t" \
  "v_and_b32 v20, 16, v20\n\t" \
  "v_mov_b32 v21, 0x3f80\n\t" \
  "v_lshlrev_b32 v21, v20, v21\n\t" \
  "v_bfe_u32 v20, v69, 4, 2\n\t" \
  "v_cmp_eq_u32 vcc, 0, v20\n\t" \
  "s_nop 1\n\t" \
  "v_cndmask_b32 v32, 0, v21, vcc\n\t" \
  "v_cmp_eq_u32 vcc, 1, v20\n\t" \
  "s_nop 1\n\t" \
  "v_cndmask_b32 v33, 0, v21, vcc\n\t" \
  "v_cmp_eq_u32 vcc, 2, v20\n\t" \
  "s_nop 1\n\t" \
  "v_cndmask_b32 v34, 0, v21, vcc\n\t" \
  "v_cmp_eq_u32 vcc, 3, v20\n\t" \
  "s_nop 1\n\t" \
  "v_cndmask_b32 v35, 0, v21, vcc\n\t" \
  "v_bfe_u32 v38, v69, 7, 1\n\t" \
  "v_bfe_u32 v20, v69, 9, 1\n\t" \
  "v_lshl_add_u32 v38, v20, 2, v38\n\t" \
  "v_lshlrev_b32 v38, 12, v38\n\t" \
  "v_and_b32 v20, 0x60, v69\n\t" \
  "v_add_u32 v38, v38, v20\n\t" \
  "v_bfe_u32 v20, v69, 8, 1\n\t" \
  "v_lshl_add_u32 v38, v20, 4, v38\n\t" \
  "v_bfe_u32 v20, v69, 4, 1\n\t" \
  "v_lshl_add_u32 v38, v20, 3, v38\n\t" \
  "v_mov_b32 v44, 0\n\t" \
  CH_DP17 \
  "s_waitcnt vmcnt(3)\n\t" \
  CH_DP14 \
  "v_mfma_f32_16x16x32_bf16 v[40:43], v[32:35], v[0:3], 0\n\t" \
  "v_max3_u32 v44, v44, v0, v1\n\t" \
  "v_max3_u32 v44, v44, v2, v3\n\t" \
  "s_waitcnt vmcnt(2)\n\t" \
  "v_mfma_f32_16x16x32_bf16 v[40:43], v[32:35], v[4:7], v[40:43]\n\t" \
  "v_max3_u32 v44, v44, v4, v5\n\t" \
  "v_max3_u32 v44, v44, v6, v7\n\t" \
  "s_waitcnt vmcnt(1)\n\t" \
  "v_mfma_f32_16x16x32_bf16 v[40:43], v[32:35], v[8:11], v[40:43]\n\t" \
  "v_max3_u32 v44, v44, v8, v9\n\t" \
  "v_max3_u32 v44, v44, v10, v11\n\t" \
  "s_waitcnt vmcnt(0)\n\t" \
  CH_DP15 \
  "v_mfma_f32_16x16x32_bf16 v[40:43], v[32:35], v[12:15], v[40:43]\n\t" \
  "v_max3_u32 v44, v44, v12, v13\n\t" \
  "v_max3_u32 v44, v44, v14, v15\n\t" \
  "v_cmp_eq_u32 vcc, -1, v44\n\t" \
  "s_cbranch_vccnz RFAIL1_%=\n\t" \
  "s_nop 3\n\t" \
  "v_add_f32_dpp v40, v40, v40 row_ror:8 row_mask:0xf bank_mask:0xf\n\t" \
  "v_add_f32_dpp v41, v41, v41 row_ror:8 row_mask:0xf bank_mask:0xf\n\t" \
  "v_add_f32_dpp v42, v42, v42 row_ror:8 row_mask:0xf bank_mask:0xf\n\t" \
  "v_add_f32_dpp v43, v43, v43 row_ror:8 row_mask:0xf bank_mask:0xf\n\t" \
  "v_cvt_pk_bf16_f32 v40, v40, v41\n\t" \
  "v_cvt_pk_bf16_f32 v41, v42, v43\n\t" \
  "buffer_store_dwordx2 v[40:41], v38, s[28:31], s98 offen sc1\n\t" \
  "RDONE1_%=:\n\t" \
  CH_DP16 \
  CH_ST4 \
  "s_barrier\n\t" \
  CH_ST3 \
  CH_TRC \
  "s_add_u32 s48, s48, 1\n\t" \
  "s_mov_b32 s49, s53\n\t" \
  "s_add_u32 s53, s53, " CH_SLOT "\n\t" \
  "s_add_u32 s60, s53, " CH_SKIP "\n\t" \
  "s_lshr_b32 s61, s96, 1\n\t" \
  "s_add_u32 s86, s86, 1\n\t" \
  "s_cmp_eq_u32 s86, 3\n\t" \
  "s_cselect_b32 s53, s60, s53\n\t" \
  "s_cselect_b32 s96, s61, s96\n\t" \
  "s_cselect_b32 s86, 0, s86\n\t" \
  "s_andn2_b32 s53, s53, 0x2000\n\t" \
  "s_and_b32 s60, s96, 1\n\t" \
  "s_lshl_b32 s60, s60, 13\n\t" \
  "s_or_b32 s53, s53, s60\n\t" \
  "s_mov_b32 s68, 0\n\t" \
  "s_add_u32 s97, s41, s53\n\t" \
  "s_add_u32 s98, s37, s53\n\t" \
  "s_add_u32 s82, s48, 0\n\t" \
  CH_SEGC \
  "s_lshr_b64 s[60:61], s[90:91], s82\n\t" \
  "s_and_b32 s60, s60, 1\n\t" \
  "s_lshl_b32 s60, s60, 13\n\t" \
  "s_add_u32 s82, s82, " CH_SG0 "\n\t" \
  "s_lshl_b32 s82, s82, 21\n\t" \
  "s_or_b32 s82, s82, s60\n\t" \
  "s_add_u32 s50, s82, s38\n\t" \
  "s_add_u32 s69, s82, s39\n\t" \
  "s_cmp_eq_u32 s34, 0\n\t" \
  "s_cbranch_scc1 LINI_%=\n\t" \
  "s_branch LG1_%=\n\t" \
  "RFAIL1_%=:\n\t" \
  "s_add_u32 s74, s74, 1\n\t" \
  "s_sleep " CH_RSL "\n\t" \
  "s_waitcnt vmcnt(0)\n\t" \
  "s_cmp_gt_u32 s74, s84\n\t" \
  "s_cbranch_scc1 RG1_%=\n\t" \
  "s_and_b32 s60, s74, 1023\n\t" \
  "s_cmp_lg_u32 s60, 0\n\t" \
  "s_cbranch_scc1 RRETRY1_%=\n\t" \
  "buffer_load_dword v46, off, s[28:31], s92 sc1\n\t" \
  "s_waitcnt vmcnt(0)\n\t" \
  "v_readfirstlane_b32 s60, v46\n\t" \
  "s_nop 0\n\t" \
  "s_cmp_eq_u32 s60, 0\n\t" \
  "s_cbranch_scc1 RRETRY1_%=\n\t" \
  "RG1_%=:\n\t" \
  "s_mov_b32 s84, 0\n\t" \
  "s_lshl_b32 s60, s36, 4\n\t" \
  "s_or_b32 s60, s60, s48\n\t" \
  "s_or_b32 s60, s60, 536870912\n\t" \
  "v_mov_b32 v76, s60\n\t" \
  "v_mov_b32 v77, 0\n\t" \
  "s_mov_b64 exec, 1\n\t" \
  "buffer_atomic_cmpswap v[76:77], off, s[28:31], s92 sc1\n\t" \
  "s_mov_b64 exec, -1\n\t" \
  "s_branch RDONE1_%=\n\t" \
  "LFAIL1_%=:\n\t" \
  "v_max_u32 v75, v0, v1\n\t" \
  "v_max3_u32 v75, v75, v2, v3\n\t" \
  "v_max3_u32 v75, v75, v4, v5\n\t" \
  "v_max3_u32 v75, v75, v6, v7\n\t" \
  "v_max3_u32 v75, v75, v8, v9\n\t" \
  "v_max3_u32 v75, v75, v10, v11\n\t" \
  "v_max3_u32 v75, v75, v12, v13\n\t" \
  "v_max3_u32 v75, v75, v14, v15\n\t" \
  "v_max3_u32 v75, v75, v16, v17\n\t" \
  "v_max3_u32 v75, v75, v18, v19\n\t" \
  "v_max3_u32 v75, v75, v20, v21\n\t" \
  "v_max3_u32 v75, v75, v22, v23\n\t" \
  "v_max3_u32 v75, v75, v24, v25\n\t" \
  "v_max3_u32 v75, v75, v26, v27\n\t" \
  "v_max3_u32 v75, v75, v28, v29\n\t" \
  "v_max3_u32 v75, v75, v30, v31\n\t" \
  "v_max3_u32 v75, v75, v32, v33\n\t" \
  "v_max3_u32 v75, v75, v34, v35\n\t" \
  "v_cmp_eq_u32 vcc, -1, v75\n\t" \
  "s_cbranch_vccz LGO1_%=\n\t" \
  "s_add_u32 s68, s68, 1\n\t" \
  "s_waitcnt lgkmcnt(0)\n\t" \
  "s_cmp_gt_u32 s68, s83\n\t" \
  "s_cbranch_scc1 LGV1_%=\n\t" \
  "s_barrier\n\t" \
  "s_waitcnt vmcnt(8)\n\t" \
  "s_branch LG1_%=\n\t" \
  "RWT1_%=f:\n\t" \
  "s_cmp_lt_i32 s73, -2\n\t" \
  "s_cbranch_scc0 RWT1_%=g\n\t" \
  "s_nop 4\n\t" \
  "s_branch RWT1_%=g\n\t" \
  "LGV1_%=:\n\t" \
  "s_mov_b32 s83, 0\n\t" \
  "s_lshl_b32 s60, s36, 4\n\t" \
  "s_or_b32 s60, s60, s48\n\t" \
  "s_or_b32 s60, s60, 805306368\n\t" \
  "v_mov_b32 v76, s60\n\t" \
  "v_mov_b32 v77, 0\n\t" \
  "s_mov_b64 exec, 1\n\t" \
  "buffer_atomic_cmpswap v[76:77], off, s[28:31], s92 sc1\n\t" \
  "s_mov_b64 exec, -1\n\t" \
  "s_branch LGO1_%=\n\t" \
  "LLAST1_%=:\n\t" \
  "s_waitcnt vmcnt(0) lgkmcnt(0)\n\t" \
  "s_barrier\n\t" \
  "s_branch LEND_%=\n\t" \
  ".p2align 6\n\t" \
  "LG2_%=:\n\t" \
  "s_barrier\n\t" \
  "buffer_load_dwordx4 v[52:55], v69, s[28:31], s50 offen sc1\n\t" \
  "buffer_load_dwordx4 v[56:59], v69, s[28:31], s50 offen offset:1024 sc1\n\t" \
  "s_add_u32 s64, s50, 18432\n\t" \
  "s_movk_i32 m0, 11264\n\t" \
  "v_lshlrev_b32 v75, 1, v69\n\t" \
  "buffer_load_dword v75, s[28:31], s64 offen sc1 lds\n\t" \
  "ds_read_b128 v[44:47], v70 offset:0\n\t" \
  "ds_read_b128 v[48:51], v70 offset:1024\n\t" \
  "ds_read_b128 v[60:63], v70 offset:2048\n\t" \
  "ds_read_b128 v[64:67], v70 offset:3072\n\t" \
  "v_mov_b64 v[36:37], 0\n\t" \
  "v_mov_b64 v[38:39], 0\n\t" \
  "v_mov_b64 v[40:41], 0\n\t" \
  "v_mov_b64 v[42:43], 0\n\t" \
  "v_lshrrev_b32 v71, 1, v69\n\t" \
  "v_lshrrev_b32 v72, 3, v69\n\t" \
  "v_and_b32 v72, 0x78, v72\n\t" \
  "v_and_b32 v74, 0x30, v69\n\t" \
  "v_lshl_add_u32 v72, v74, 3, v72\n\t" \
  "s_add_u32 s54, s53, s36\n\t" \
  "s_add_u32 s55, s54, 557056\n\t" \
  "s_add_u32 s54, s54, 131072\n\t" \
  "s_add_u32 s75, s54, 262144\n\t" \
  "s_add_u32 s80, s53, s26\n\t" \
  "s_barrier\n\t" \
  CH_DP0 \
  "ds_read_b128 v[0:3], v69 offset:0\n\t" \
  "ds_read_b128 v[4:7], v69 offset:1024\n\t" \
  "ds_read_b128 v[8:11], v69 offset:2048\n\t" \
  "ds_read_b128 v[12:15], v69 offset:3072\n\t" \
  "s_waitcnt lgkmcnt(3)\n\t" \
  "v_smfmac_f32_16x16x64_bf16 v[36:39], v[0:3], v[44:51], v68\n\t" \
  "ds_read_b128 v[44:47], v70 offset:4096\n\t" \
  "v_smfmac_f32_16x16x64_bf16 v[40:43], v[0:3], v[60:67], v68\n\t" \
  "ds_read_b128 v[48:51], v70 offset:5120\n\t" \
  "s_waitcnt lgkmcnt(4)\n\t" \
  "v_smfmac_f32_16x16x64_bf16 v[36:39], v[4:7], a[32:39], v68\n\t" \
  "ds_read_b128 v[60:63], v70 offset:6144\n\t" \
  "v_smfmac_f32_16x16x64_bf16 v[40:43], v[4:7], a[40:47], v68\n\t" \
  "ds_read_b128 v[64:67], v70 offset:7168\n\t" \
  "s_waitcnt lgkmcnt(5)\n\t" \
  "v_smfmac_f32_16x16x64_bf16 v[36:39], v[8:11], a[80:87], v68\n\t" \
  "s_barrier\n\t" \
  CH_DP1 \
  "ds_read_b128 v[16:19], v69 offset:4096\n\t" \
  "v_smfmac_f32_16x16x64_bf16 v[40:43], v[8:11], a[88:95], v68\n\t" \
  "ds_read_b128 v[20:23], v69 offset:5120\n\t" \
  "s_waitcnt lgkmcnt(6)\n\t" \
  "v_smfmac_f32_16x16x64_bf16 v[36:39], v[12:15], a[120:127], v68\n\t" \
  "ds_read_b128 v[24:27], v69 offset:6144\n\t" \
  "s_waitcnt lgkmcnt(5)\n\t" \
  "v_smfmac_f32_16x16x64_bf16 v[40:43], v[12:15], v[44:51], v68\n\t" \
  "ds_read_b128 v[28:31], v69 offset:7168\n\t" \
  "s_waitcnt lgkmcnt(3)\n\t" \
  "v_smfmac_f32_16x16x64_bf16 v[36:39], v[16:19], a[160:167], v68\n\t" \
  "ds_read_b128 v[32:35], v69 offset:8192\n\t" \
  "v_smfmac_f32_16x16x64_bf16 v[40:43], v[16:19], a[168:175], v68\n\t" \
  "s_waitcnt lgkmcnt(3)\n\t" \
  "v_mov_b64 v[44:45], 0\n\t" \
  "v_smfmac_f32_16x16x64_bf16 v[36:39], v[20:23], a[200:207], v68\n\t" \
  "s_waitcnt lgkmcnt(2)\n\t" \
  "v_mov_b64 v[46:47], 0\n\t" \
  "v_smfmac_f32_16x16x64_bf16 v[36:39], v[24:27], a[240:247], v68\n\t" \
  "s_waitcnt lgkmcnt(1)\n\t" \
  "v_mov_b64 v[48:49], 0\n\t" \
  "v_smfmac_f32_16x16x64_bf16 v[36:39], v[28:31], v[112:119], v68\n\t" \
  "s_waitcnt lgkmcnt(0)\n\t" \
  "v_mov_b64 v[50:51], 0\n\t" \
  "v_smfmac_f32_16x16x64_bf16 v[36:39], v[32:35], v[160:167], v68\n\t" \
  "v_smfmac_f32_16x16x64_bf16 v[40:43], v[20:23], v[60:67], v68\n\t" \
  "ds_read_b128 v[60:63], v70 offset:8192\n\t" \
  "v_smfmac_f32_16x16x64_bf16 v[40:43], v[24:27], a[248:255], v68\n\t" \
  "ds_read_b128 v[64:67], v70 offset:9216\n\t" \
  "v_add_f32 v74, v36, v38\n\t" \
  "v_smfmac_f32_16x16x64_bf16 v[40:43], v[28:31], v[120:127], v68\n\t" \
  "v_add_f32 v37, v37, v39\n\t" \
  "v_smfmac_f32_16x16x64_bf16 v[40:43], v[32:35], v[168:175], v68\n\t" \
  "v_cmp_u_f32 vcc, v74, v37\n\t" \
  CH_DP2 \
  "v_smfmac_f32_16x16x64_bf16 v[44:47], v[0:3], a[0:7], v68\n\t" \
  "s_cbranch_vccnz LFAIL2_%=\n\t" \
  "LGO2_%=:\n\t" \
  "v_cvt_pk_bf16_f32 v74, v74, v37\n\t" \
  "v_smfmac_f32_16x16x64_bf16 v[44:47], v[4:7], a[48:55], v68\n\t" \
  "s_waitcnt lgkmcnt(0)\n\t" \
  "v_add_f32 v75, v40, v42\n\t" \
  "v_smfmac_f32_16x16x64_bf16 v[44:47], v[8:11], v[60:67], v68\n\t" \
  "ds_read_b128 v[60:63], v70 offset:10240\n\t" \
  "v_add_f32 v41, v41, v43\n\t" \
  "v_cvt_pk_bf16_f32 v75, v75, v41\n\t" \
  "v_smfmac_f32_16x16x64_bf16 v[44:47], v[12:15], a[128:135], v68\n\t" \
  "ds_write2st64_b32 v71, v74, v75 offset0:40 offset1:42\n\t" \
  "v_smfmac_f32_16x16x64_bf16 v[44:47], v[16:19], a[176:183], v68\n\t" \
  "ds_read_b128 v[64:67], v70 offset:11264\n\t" \
  "v_smfmac_f32_16x16x64_bf16 v[44:47], v[20:23], a[208:215], v68\n\t" \
  "v_smfmac_f32_16x16x64_bf16 v[44:47], v[24:27], v[80:87], v68\n\t" \
  "v_smfmac_f32_16x16x64_bf16 v[44:47], v[28:31], v[128:135], v68\n\t" \
  "s_waitcnt lgkmcnt(0)\n\t" \
  "v_smfmac_f32_16x16x64_bf16 v[44:47], v[32:35], v[60:67], v68\n\t" \
  "s_barrier\n\t" \
  "ds_read_b64_tr_b16 v[76:77], v72 offset:10240\n\t" \
  CH_DP3 \
  "v_smfmac_f32_16x16x64_bf16 v[48:51], v[0:3], a[8:15], v68\n\t" \
  "ds_read_b64_tr_b16 v[78:79], v72 offset:10752\n\t" \
  "v_smfmac_f32_16x16x64_bf16 v[48:51], v[4:7], a[56:63], v68\n\t" \
  "buffer_load_dwordx4 v[60:63], v69, s[28:31], s64 offen sc1\n\t" \
  "v_add_f32 v74, v44, v46\n\t" \
  "v_smfmac_f32_16x16x64_bf16 v[48:51], v[8:11], a[96:103], v68\n\t" \
  "v_add_f32 v45, v45, v47\n\t" \
  "v_smfmac_f32_16x16x64_bf16 v[48:51], v[12:15], a[136:143], v68\n\t" \
  "buffer_load_dwordx4 v[64:67], v69, s[28:31], s64 offen offset:1024 sc1\n\t" \
  "s_waitcnt vmcnt(4)\n\t" \
  "s_waitcnt lgkmcnt(0)\n\t" \
  "v_cvt_pk_bf16_f32 v74, v74, v45\n\t" \
  "v_mfma_f32_16x16x32_bf16 v[36:39], v[52:55], v[76:79], 0\n\t" \
  "s_waitcnt vmcnt(3)\n\t" \
  "v_mfma_f32_16x16x32_bf16 v[40:43], v[56:59], v[76:79], 0\n\t" \
  "v_mov_b64 v[52:53], 0\n\t" \
  "v_mov_b64 v[54:55], 0\n\t" \
  "v_smfmac_f32_16x16x64_bf16 v[48:51], v[16:19], a[184:191], v68\n\t" \
  "v_mov_b64 v[56:57], 0\n\t" \
  "v_mov_b64 v[58:59], 0\n\t" \
  "v_smfmac_f32_16x16x64_bf16 v[48:51], v[20:23], a[216:223], v68\n\t" \
  "ds_write_b32 v71, v74 offset:1024\n\t" \
  "v_add_f32_dpp v36, v40, v36 quad_perm:[2,3,0,1] row_mask:0xf bank_mask:0xf\n\t" \
  "v_smfmac_f32_16x16x64_bf16 v[48:51], v[24:27], v[88:95], v68\n\t" \
  "v_add_f32_dpp v37, v41, v37 quad_perm:[2,3,0,1] row_mask:0xf bank_mask:0xf\n\t" \
  "v_smfmac_f32_16x16x64_bf16 v[48:51], v[28:31], v[136:143], v68\n\t" \
  "v_add_f32_dpp v38, v42, v38 quad_perm:[2,3,0,1] row_mask:0xf bank_mask:0xf\n\t" \
  "v_smfmac_f32_16x16x64_bf16 v[48:51], v[32:35], v[176:183], v68\n\t" \
  "v_add_f32_dpp v39, v43, v39 quad_perm:[2,3,0,1] row_mask:0xf bank_mask:0xf\n\t" \
  CH_DP4 \
  "v_smfmac_f32_16x16x64_bf16 v[52:55], v[0:3], a[16:23], v68\n\t" \
  "s_mov_b64 exec, s[58:59]\n\t" \
  "ds_write_b128 v69, v[36:39] offset:3072\n\t" \
  "s_mov_b64 exec, -1\n\t" \
  "v_smfmac_f32_16x16x64_bf16 v[52:55], v[4:7], a[64:71], v68\n\t" \
  "v_add_f32 v75, v48, v50\n\t" \
  "v_add_f32 v49, v49, v51\n\t" \
  "v_cvt_pk_bf16_f32 v75, v75, v49\n\t" \
  "v_smfmac_f32_16x16x64_bf16 v[56:59], v[0:3], a[24:31], v68\n\t" \
  "ds_write_b32 v71, v75 offset:1536\n\t" \
  "v_smfmac_f32_16x16x64_bf16 v[56:59], v[4:7], a[72:79], v68\n\t" \
  "ds_read_b64_tr_b16 v[0:1], v72 offset:9216\n\t" \
  "ds_read_b64_tr_b16 v[2:3], v72 offset:9728\n\t" \
  "v_smfmac_f32_16x16x64_bf16 v[52:55], v[8:11], a[104:111], v68\n\t" \
  "ds_read_b128 v[36:39], v70 offset:12288\n\t" \
  "v_smfmac_f32_16x16x64_bf16 v[56:59], v[8:11], a[112:119], v68\n\t" \
  "ds_read_b128 v[40:43], v70 offset:13312\n\t" \
  "v_smfmac_f32_16x16x64_bf16 v[52:55], v[12:15], a[144:151], v68\n\t" \
  "s_waitcnt lgkmcnt(4)\n\t" \
  "s_barrier\n\t" \
  CH_DP5 \
  "ds_read_b64_tr_b16 v[44:45], v72 offset:1024\n\t" \
  "ds_read_b64_tr_b16 v[46:47], v72 offset:1536\n\t" \
  "v_smfmac_f32_16x16x64_bf16 v[56:59], v[12:15], a[152:159], v68\n\t" \
  "ds_read_b64_tr_b16 v[48:49], v72 offset:0\n\t" \
  "ds_read_b64_tr_b16 v[50:51], v72 offset:512\n\t" \
  "v_smfmac_f32_16x16x64_bf16 v[52:55], v[16:19], a[192:199], v68\n\t" \
  "ds_read_b128 v[4:7], v69 offset:2048\n\t" \
  "s_waitcnt lgkmcnt(5)\n\t" \
  "v_smfmac_f32_16x16x64_bf16 v[56:59], v[16:19], v[36:43], v68\n\t" \
  "ds_read_b128 v[8:11], v69 offset:3072\n\t" \
  "s_waitcnt lgkmcnt(4)\n\t" \
  "v_mfma_f32_16x16x32_bf16 v[12:15], v[44:47], v[76:79], 0\n\t" \
  "ds_read_b128 v[36:39], v70 offset:14336\n\t" \
  "s_waitcnt lgkmcnt(3)\n\t" \
  "v_mfma_f32_16x16x32_bf16 v[12:15], v[48:51], v[0:3], v[12:15]\n\t" \
  "ds_read_b128 v[40:43], v70 offset:15360\n\t" \
  "v_smfmac_f32_16x16x64_bf16 v[52:55], v[20:23], a[224:231], v68\n\t" \
  "buffer_store_dwordx4 v[44:47], v69, s[28:31], s69 offen offset:1024 sc1\n\t" \
  "s_waitcnt lgkmcnt(2)\n\t" \
  "v_add_f32 v4, v4, v8\n\t" \
  "v_add_f32 v5, v5, v9\n\t" \
  "v_add_f32 v6, v6, v10\n\t" \
  CH_DP6 \
  "v_cndmask_b32 v78, v13, v14, s[56:57]\n\t" \
  "v_cndmask_b32 v76, v12, v15, s[56:57]\n\t" \
  "v_add_f32 v7, v7, v11\n\t" \
  "v_max3_f32 v77, v4, v5, v6\n\t" \
  "v_add_f32_dpp v76, v78, v76 quad_perm:[2,3,0,1] row_mask:0xf bank_mask:0xf\n\t" \
  "v_max3_f32 v77, v77, v7, v76\n\t" \
  "v_smfmac_f32_16x16x64_bf16 v[56:59], v[20:23], a[232:239], v68\n\t" \
  "ds_read_b128 v[44:47], v70 offset:16384\n\t" \
  "v_max_f32_dpp v77, v77, v77 quad_perm:[2,3,0,1] row_mask:0xf bank_mask:0xf\n\t" \
  "v_mov_b32 v78, v77\n\t" \
  "v_smfmac_f32_16x16x64_bf16 v[52:55], v[24:27], v[96:103], v68\n\t" \
  "ds_read_b128 v[48:51], v70 offset:17408\n\t" \
  "v_permlane16_swap_b32 v77, v78\n\t" \
  "v_max_f32 v77, v77, v78\n\t" \
  "v_mov_b32 v78, v77\n\t" \
  "v_smfmac_f32_16x16x64_bf16 v[52:55], v[28:31], v[144:151], v68\n\t" \
  "ds_read_b128 v[16:19], v70 offset:18432\n\t" \
  "v_permlane32_swap_b32 v77, v78\n\t" \
  "v_max_f32 v77, v77, v78\n\t" \
  "v_smfmac_f32_16x16x64_bf16 v[52:55], v[32:35], v[184:191], v68\n\t" \
  "ds_read_b128 v[20:23], v70 offset:19456\n\t" \
  "v_mul_f32 v77, 0xbe0293ee, v77\n\t" \
  CH_DP7 \
  "v_smfmac_f32_16x16x64_bf16 v[56:59], v[24:27], v[104:111], v68\n\t" \
  "v_fmamk_f32 v4, v4, 0x3e0293ee, v77\n\t" \
  "v_smfmac_f32_16x16x64_bf16 v[56:59], v[28:31], v[152:159], v68\n\t" \
  "ds_read_b128 v[24:27], v70 offset:22528\n\t" \
  "s_waitcnt lgkmcnt(4)\n\t" \
  "v_fmamk_f32 v5, v5, 0x3e0293ee, v77\n\t" \
  "v_smfmac_f32_16x16x64_bf16 v[56:59], v[32:35], v[36:43], v68\n\t" \
  "ds_read_b128 v[28:31], v70 offset:23552\n\t" \
  "v_fmamk_f32 v6, v6, 0x3e0293ee, v77\n\t" \
  "v_fmamk_f32 v7, v7, 0x3e0293ee, v77\n\t" \
  "v_fmamk_f32 v76, v76, 0x3e0293ee, v77\n\t" \
  "v_exp_f32 v4, v4\n\t" \
  "v_exp_f32 v5, v5\n\t" \
  "v_exp_f32 v6, v6\n\t" \
  "v_exp_f32 v7, v7\n\t" \
  "v_cvt_pk_bf16_f32 v4, v4, v5\n\t" \
  "v_exp_f32 v76, v76\n\t" \
  "v_cvt_pk_bf16_f32 v5, v6, v7\n\t" \
  "v_mov_b32_dpp v12, v4 quad_perm:[0,1,0,1] row_mask:0xf bank_mask:0xf\n\t" \
  "v_mov_b32_dpp v14, v4 quad_perm:[2,3,2,3] row_mask:0xf bank_mask:0xf\n\t" \
  "v_mov_b32_dpp v13, v5 quad_perm:[0,1,0,1] row_mask:0xf bank_mask:0xf\n\t" \
  "v_mov_b32_dpp v15, v5 quad_perm:[2,3,2,3] row_mask:0xf bank_mask:0xf\n\t" \
  "v_add_f32 v52, v52, v54\n\t" \
  "v_add_f32 v53, v53, v55\n\t" \
  "s_waitcnt vmcnt(2)\n\t" \
  CH_DP8 \
  "v_mfma_f32_16x16x32_bf16 v[0:3], v[60:63], v[12:15], 0\n\t" \
  "ds_read_b128 v[36:39], v70 offset:20480\n\t" \
  "v_add_f32 v56, v56, v58\n\t" \
  "v_add_f32 v57, v57, v59\n\t" \
  "v_cvt_pk_bf16_f32 v52, v52, v53\n\t" \
  "v_cvt_pk_bf16_f32 v53, v56, v57\n\t" \
  "s_waitcnt vmcnt(1)\n\t" \
  "v_mfma_f32_16x16x32_bf16 v[4:7], v[64:67], v[12:15], 0\n\t" \
  "ds_read_b128 v[40:43], v70 offset:21504\n\t" \
  "v_mov_b64 v[56:57], s[88:89]\n\t" \
  "v_mov_b64 v[58:59], s[88:89]\n\t" \
  "v_mov_b32_dpp v78, v76 quad_perm:[0,1,0,1] row_mask:0xf bank_mask:0xf\n\t" \
  "v_mov_b32_dpp v79, v76 quad_perm:[2,3,2,3] row_mask:0xf bank_mask:0xf\n\t" \
  "v_mfma_f32_16x16x32_bf16 v[32:35], v[56:59], v[12:15], 0\n\t" \
  "ds_read_b128 v[60:63], v70 offset:24576\n\t" \
  "v_cvt_pk_bf16_f32 v11, v78, v79\n\t" \
  "v_cvt_pk_bf16_f32 v8, v78, v79\n\t" \
  "v_mov_b64 v[54:55], 0\n\t" \
  "v_mov_b32 v9, 0\n\t" \
  "v_mov_b32 v10, 0\n\t" \
  "v_mov_b64 v[14:15], 0\n\t" \
  "s_nop 0\n\t" \
  "v_mfma_f32_16x16x32_bf16 v[32:35], v[54:57], v[8:11], v[32:35]\n\t" \
  "buffer_store_dwordx2 v[52:53], v71, s[28:31], s69 offen offset:3072 sc1\n\t" \
  "s_cmp_eq_u32 s48, 63\n\t" \
  "v_mfma_f32_16x16x32_bf16 v[0:3], v[52:55], v[8:11], v[0:3]\n\t" \
  "ds_read_b128 v[64:67], v70 offset:25600\n\t" \
  "v_mov_b64 v[56:57], 0\n\t" \
  "v_mov_b64 v[58:59], 0\n\t" \
  "v_mfma_f32_16x16x32_bf16 v[4:7], v[52:55], v[10:13], v[4:7]\n\t" \
  "s_cbranch_scc1 LLAST2_%=\n\t" \
  "v_rcp_f32 v74, v32\n\t" \
  "v_mov_b64 v[12:13], 0\n\t" \
  "v_mov_b32 v8, 0\n\t" \
  "v_pk_mul_f32 v[0:1], v[0:1], v[74:75] op_sel_hi:[1,0]\n\t" \
  "v_pk_mul_f32 v[2:3], v[2:3], v[74:75] op_sel_hi:[1,0]\n\t" \
  "v_cvt_pk_bf16_f32 v76, v0, v1\n\t" \
  "v_cvt_pk_bf16_f32 v77, v2, v3\n\t" \
  "v_pk_mul_f32 v[4:5], v[4:5], v[74:75] op_sel_hi:[1,0]\n\t" \
  "v_pk_mul_f32 v[6:7], v[6:7], v[74:75] op_sel_hi:[1,0]\n\t" \
  "v_cvt_pk_bf16_f32 v78, v4, v5\n\t" \
  "v_cvt_pk_bf16_f32 v79, v6, v7\n\t" \
  "ds_write_b128 v69, v[76:79] offset:6144\n\t" \
  CH_DP9 \
  "s_waitcnt lgkmcnt(2)\n\t" \
  "v_smfmac_f32_16x16x64_bf16 v[12:15], v[76:79], v[44:51], v68\n\t" \
  "ds_read_b128 v[44:47], v70 offset:26624\n\t" \
  "v_mov_b32 v11, 0\n\t" \
  "v_smfmac_f32_16x16x64_bf16 v[56:59], v[76:79], v[16:23], v68\n\t" \
  "ds_read_b128 v[48:51], v70 offset:27648\n\t" \
  "v_mov_b64 v[52:53], 0\n\t" \
  "v_smfmac_f32_16x16x64_bf16 v[8:11], v[76:79], v[36:43], v68\n\t" \
  "ds_read_b128 v[16:19], v70 offset:28672\n\t" \
  "v_smfmac_f32_16x16x64_bf16 v[52:55], v[76:79], v[24:31], v68\n\t" \
  "s_waitcnt lgkmcnt(3)\n\t" \
  "s_barrier\n\t" \
  CH_DP10 \
  "ds_read_b128 v[0:3], v69 offset:7168\n\t" \
  "ds_read_b128 v[4:7], v69 offset:4096\n\t" \
  "v_lshrrev_b32 v71, 7, v69\n\t" \
  "v_and_b32 v74, 0x70, v69\n\t" \
  "v_lshl_add_u32 v71, v71, 14, v74\n\t" \
  "v_bfe_u32 v72, v69, 2, 7\n\t" \
  "v_and_b32 v74, 0x200, v69\n\t" \
  "s_waitcnt lgkmcnt(1)\n\t" \
  "v_smfmac_f32_16x16x64_bf16 v[8:11], v[0:3], v[60:67], v68\n\t" \
  "ds_read_b128 v[20:23], v70 offset:29696\n\t" \
  "v_mov_b64 v[76:77], 0\n\t" \
  "v_smfmac_f32_16x16x64_bf16 v[12:15], v[0:3], v[200:207], v68\n\t" \
  "buffer_load_dword v75, off, s[28:31], s80 sc1\n\t" \
  "ds_read_b128 v[32:35], v69 offset:5120\n\t" \
  "v_mov_b64 v[78:79], 0\n\t" \
  "v_smfmac_f32_16x16x64_bf16 v[52:55], v[0:3], v[44:51], v68\n\t" \
  "ds_read_b128 v[24:27], v70 offset:30720\n\t" \
  "v_lshl_add_u32 v72, v74, 3, v72\n\t" \
  "v_smfmac_f32_16x16x64_bf16 v[56:59], v[0:3], v[232:239], v68\n\t" \
  "ds_read_b128 v[28:31], v70 offset:31744\n\t" \
  "s_waitcnt lgkmcnt(4)\n\t" \
  "v_smfmac_f32_16x16x64_bf16 v[12:15], v[4:7], v[208:215], v68\n\t" \
  "ds_read_b128 v[60:63], v70 offset:32768\n\t" \
  "s_waitcnt lgkmcnt(4)\n\t" \
  "v_smfmac_f32_16x16x64_bf16 v[8:11], v[4:7], v[16:23], v68\n\t" \
  "ds_read_b128 v[64:67], v70 offset:33792\n\t" \
  "s_waitcnt lgkmcnt(4)\n\t" \
  "v_smfmac_f32_16x16x64_bf16 v[8:11], v[32:35], v[192:199], v68\n\t" \
  "ds_read_b128 v[36:39], v73 offset:4096\n\t" \
  "v_smfmac_f32_16x16x64_bf16 v[12:15], v[32:35], v[216:223], v68\n\t" \
  "ds_read_b128 v[40:43], v73 offset:6144\n\t" \
  "s_waitcnt lgkmcnt(4)\n\t" \
  "v_smfmac_f32_16x16x64_bf16 v[52:55], v[4:7], v[24:31], v68\n\t" \
  "ds_read_b128 v[44:47], v70 offset:34816\n\t" \
  "v_cvt_pk_bf16_f32 v8, v8, v9\n\t" \
  "v_smfmac_f32_16x16x64_bf16 v[52:55], v[32:35], v[224:231], v68\n\t" \
  "ds_read_b128 v[48:51], v70 offset:35840\n\t" \
  "v_cvt_pk_bf16_f32 v9, v10, v11\n\t" \
  "v_smfmac_f32_16x16x64_bf16 v[56:59], v[4:7], v[240:247], v68\n\t" \
  "s_waitcnt lgkmcnt(4)\n\t" \
  "v_cvt_pk_bf16_f32 v10, v12, v13\n\t" \
  "v_smfmac_f32_16x16x64_bf16 v[56:59], v[32:35], v[60:67], v68\n\t" \
  "s_waitcnt lgkmcnt(3)\n\t" \
  "v_cvt_pk_bf16_f32 v11, v14, v15\n\t" \
  "v_smfmac_f32_16x16x64_bf16 v[76:79], v[36:39], v[248:255], v68\n\t" \
  "buffer_store_dwordx4 v[8:11], v71, s[28:31], s54 offen sc1\n\t" \
  "s_waitcnt lgkmcnt(0)\n\t" \
  CH_DP11 \
  "v_cvt_pk_bf16_f32 v52, v52, v53\n\t" \
  "v_smfmac_f32_16x16x64_bf16 v[76:79], v[40:43], v[44:51], v68\n\t" \
  "v_cvt_pk_bf16_f32 v53, v54, v55\n\t" \
  "v_cvt_pk_bf16_f32 v54, v56, v57\n\t" \
  "v_cvt_pk_bf16_f32 v55, v58, v59\n\t" \
  "buffer_store_dwordx4 v[52:55], v71, s[28:31], s75 offen sc1\n\t" \
  "s_nop 2\n\t" \
  "v_add_f32 v76, v76, v78\n\t" \
  "v_add_f32 v77, v77, v79\n\t" \
  "v_cvt_pk_bf16_f32 v76, v76, v77\n\t" \
  "buffer_store_dword v76, v72, s[28:31], s55 offen sc1\n\t" \
  CH_DP12 \
  "s_memrealtime s[70:71]\n\t" \
  "s_waitcnt vmcnt(3)\n\t" \
  "v_readfirstlane_b32 s65, v75\n\t" \
  "s_barrier\n\t" \
  "s_waitcnt lgkmcnt(0)\n\t" \
  "s_not_b32 s65, s65\n\t" \
  "s_lshl_b32 s61, s70, 8\n\t" \
  "s_andn2_b32 s64, s65, 0xff\n\t" \
  "s_sub_u32 s64, s64, s61\n\t" \
  "s_ashr_i32 s64, s64, 8\n\t" \
  "s_cmp_eq_u32 s65, 0\n\t" \
  "s_cselect_b32 s64, " CH_DLF ", s64\n\t" \
  "s_min_i32 s64, s64, s34\n\t" \
  "s_max_i32 s64, s64, " CH_DLL1 "\n\t" \
  "s_add_u32 s64, s64, s70\n\t" \
  CH_ST2 \
  "s_mov_b32 s74, 0\n\t" \
  "s_movk_i32 s60, 8192\n\t" \
  "RWT2_%=:\n\t" \
  "s_memrealtime s[72:73]\n\t" \
  "s_sub_u32 s60, s60, 1\n\t" \
  "s_cselect_b32 s61, 0, -1\n\t" \
  "s_waitcnt lgkmcnt(0)\n\t" \
  "s_sub_i32 s73, s72, s64\n\t" \
  "s_and_b32 s73, s73, s61\n\t" \
  "s_cmp_lt_i32 s73, " CH_FIN "\n\t" \
  "s_cbranch_scc1 RWT2_%=\n\t" \
  "s_cmp_lt_i32 s73, 0\n\t" \
  "s_cbranch_scc1 RWT2_%=f\n\t" \
  "RWT2_%=g:\n\t" \
  "RRETRY2_%=:\n\t" \
  CH_DP13 \
  "buffer_load_dwordx4 v[0:3], v69, s[28:31], s97 offen offset:0 sc1\n\t" \
  "buffer_load_dwordx4 v[4:7], v69, s[28:31], s97 offen offset:1024 sc1\n\t" \
  "buffer_load_dwordx4 v[8:11], v69, s[28:31], s97 offen offset:2048 sc1\n\t" \
  "buffer_load_dwordx4 v[12:15], v69, s[28:31], s97 offen offset:3072 sc1\n\t" \
  "v_lshrrev_b32 v20, 2, v69\n\t" \
  "v_and_b32 v20, 16, v20\n\t" \
  "v_mov_b32 v21, 0x3f80\n\t" \
  "v_lshlrev_b32 v21, v20, v21\n\t" \
  "v_bfe_u32 v20, v69, 4, 2\n\t" \
  "v_cmp_eq_u32 vcc, 0, v20\n\t" \
  "s_nop 1\n\t" \
  "v_cndmask_b32 v32, 0, v21, vcc\n\t" \
  "v_cmp_eq_u32 vcc, 1, v20\n\t" \
  "s_nop 1\n\t" \
  "v_cndmask_b32 v33, 0, v21, vcc\n\t" \
  "v_cmp_eq_u32 vcc, 2, v20\n\t" \
  "s_nop 1\n\t" \
  "v_cndmask_b32 v34, 0, v21, vcc\n\t" \
  "v_cmp_eq_u32 vcc, 3, v20\n\t" \
  "s_nop 1\n\t" \
  "v_cndmask_b32 v35, 0, v21, vcc\n\t" \
  "v_bfe_u32 v38, v69, 7, 1\n\t" \
  "v_bfe_u32 v20, v69, 9, 1\n\t" \
  "v_lshl_add_u32 v38, v20, 2, v38\n\t" \
  "v_lshlrev_b32 v38, 12, v38\n\t" \
  "v_and_b32 v20, 0x60, v69\n\t" \
  "v_add_u32 v38, v38, v20\n\t" \
  "v_bfe_u32 v20, v69, 8, 1\n\t" \
  "v_lshl_add_u32 v38, v20, 4, v38\n\t" \
  "v_bfe_u32 v20, v69, 4, 1\n\t" \
  "v_lshl_add_u32 v38, v20, 3, v38\n\t" \
  "v_mov_b32 v44, 0\n\t" \
  CH_DP17 \
  "s_waitcnt vmcnt(3)\n\t" \
  CH_DP14 \
  "v_mfma_f32_16x16x32_bf16 v[40:43], v[32:35], v[0:3], 0\n\t" \
  "v_max3_u32 v44, v44, v0, v1\n\t" \
  "v_max3_u32 v44, v44, v2, v3\n\t" \
  "s_waitcnt vmcnt(2)\n\t" \
  "v_mfma_f32_16x16x32_bf16 v[40:43], v[32:35], v[4:7], v[40:43]\n\t" \
  "v_max3_u32 v44, v44, v4, v5\n\t" \
  "v_max3_u32 v44, v44, v6, v7\n\t" \
  "s_waitcnt vmcnt(1)\n\t" \
  "v_mfma_f32_16x16x32_bf16 v[40:43], v[32:35], v[8:11], v[40:43]\n\t" \
  "v_max3_u32 v44, v44, v8, v9\n\t" \
  "v_max3_u32 v44, v44, v10, v11\n\t" \
  "s_waitcnt vmcnt(0)\n\t" \
  CH_DP15 \
  "v_mfma_f32_16x16x32_bf16 v[40:43], v[32:35], v[12:15], v[40:43]\n\t" \
  "v_max3_u32 v44, v44, v12, v13\n\t" \
  "v_max3_u32 v44, v44, v14, v15\n\t" \
  "v_cmp_eq_u32 vcc, -1, v44\n\t" \
  "s_cbranch_vccnz RFAIL2_%=\n\t" \
  "s_nop 3\n\t" \
  "v_add_f32_dpp v40, v40, v40 row_ror:8 row_mask:0xf bank_mask:0xf\n\t" \
  "v_add_f32_dpp v41, v41, v41 row_ror:8 row_mask:0xf bank_mask:0xf\n\t" \
  "v_add_f32_dpp v42, v42, v42 row_ror:8 row_mask:0xf bank_mask:0xf\n\t" \
  "v_add_f32_dpp v43, v43, v43 row_ror:8 row_mask:0xf bank_mask:0xf\n\t" \
  "v_cvt_pk_bf16_f32 v40, v40, v41\n\t" \
  "v_cvt_pk_bf16_f32 v41, v42, v43\n\t" \
  "buffer_store_dwordx2 v[40:41], v38, s[28:31], s98 offen sc1\n\t" \
  "RDONE2_%=:\n\t" \
  CH_DP16 \
  CH_ST4 \
  "s_barrier\n\t" \
  CH_ST3 \
  CH_TRC \
  "s_add_u32 s48, s48, 1\n\t" \
  "s_mov_b32 s49, s53\n\t" \
  "s_add_u32 s53, s53, " CH_SLOT "\n\t" \
  "s_add_u32 s60, s53, " CH_SKIP "\n\t" \
  "s_lshr_b32 s61, s96, 1\n\t" \
  "s_add_u32 s86, s86, 1\n\t" \
  "s_cmp_eq_u32 s86, 3\n\t" \
  "s_cselect_b32 s53, s60, s53\n\t" \
  "s_cselect_b32 s96, s61, s96\n\t" \
  "s_cselect_b32 s86, 0, s86\n\t" \
  "s_andn2_b32 s53, s53, 0x2000\n\t" \
  "s_and_b32 s60, s96, 1\n\t" \
  "s_lshl_b32 s60, s60, 13\n\t" \
  "s_or_b32 s53, s53, s60\n\t" \
  "s_mov_b32 s68, 0\n\t" \
  "s_add_u32 s97, s41, s53\n\t" \
  "s_add_u32 s98, s37, s53\n\t" \
  "s_add_u32 s82, s48, 0\n\t" \
  CH_SEGC \
  "s_lshr_b64 s[60:61], s[90:91], s82\n\t" \
  "s_and_b32 s60, s60, 1\n\t" \
  "s_lshl_b32 s60, s60, 13\n\t" \
  "s_add_u32 s82, s82, " CH_SG0 "\n\t" \
  "s_lshl_b32 s82, s82, 21\n\t" \
  "s_or_b32 s82, s82, s60\n\t" \
  "s_add_u32 s50, s82, s38\n\t" \
  "s_add_u32 s69, s82, s39\n\t" \
  "s_cmp_eq_u32 s34, 0\n\t" \
  "s_cbranch_scc1 LINI_%=\n\t" \
  "s_branch LG2_%=\n\t" \
  "RFAIL2_%=:\n\t" \
  "s_add_u32 s74, s74, 1\n\t" \
  "s_sleep " CH_RSL "\n\t" \
  "s_waitcnt vmcnt(0)\n\t" \
  "s_cmp_gt_u32 s74, s84\n\t" \
  "s_cbranch_scc1 RG2_%=\n\t" \
  "s_and_b32 s60, s74, 1023\n\t" \
  "s_cmp_lg_u32 s60, 0\n\t" \
  "s_cbranch_scc1 RRETRY2_%=\n\t" \
  "buffer_load_dword v46, off, s[28:31], s92 sc1\n\t" \
  "s_waitcnt vmcnt(0)\n\t" \
  "v_readfirstlane_b32 s60, v46\n\t" \
  "s_nop 0\n\t" \
  "s_cmp_eq_u32 s60, 0\n\t" \
  "s_cbranch_scc1 RRETRY2_%=\n\t" \
  "RG2_%=:\n\t" \
  "s_mov_b32 s84, 0\n\t" \
  "s_lshl_b32 s60, s36, 4\n\t" \
  "s_or_b32 s60, s60, s48\n\t" \
  "s_or_b32 s60, s60, 536870912\n\t" \
  "v_mov_b32 v76, s60\n\t" \
  "v_mov_b32 v77, 0\n\t" \
  "s_mov_b64 exec, 1\n\t" \
  "buffer_atomic_cmpswap v[76:77], off, s[28:31], s92 sc1\n\t" \
  "s_mov_b64 exec, -1\n\t" \
  "s_branch RDONE2_%=\n\t" \
  "LFAIL2_%=:\n\t" \
  "v_max_u32 v75, v0, v1\n\t" \
  "v_max3_u32 v75, v75, v2, v3\n\t" \
  "v_max3_u32 v75, v75, v4, v5\n\t" \
  "v_max3_u32 v75, v75, v6, v7\n\t" \
  "v_max3_u32 v75, v75, v8, v9\n\t" \
  "v_max3_u32 v75, v75, v10, v11\n\t" \
  "v_max3_u32 v75, v75, v12, v13\n\t" \
  "v_max3_u32 v75, v75, v14, v15\n\t" \
  "v_max3_u32 v75, v75, v16, v17\n\t" \
  "v_max3_u32 v75, v75, v18, v19\n\t" \
  "v_max3_u32 v75, v75, v20, v21\n\t" \
  "v_max3_u32 v75, v75, v22, v23\n\t" \
  "v_max3_u32 v75, v75, v24, v25\n\t" \
  "v_max3_u32 v75, v75, v26, v27\n\t" \
  "v_max3_u32 v75, v75, v28, v29\n\t" \
  "v_max3_u32 v75, v75, v30, v31\n\t" \
  "v_max3_u32 v75, v75, v32, v33\n\t" \
  "v_max3_u32 v75, v75, v34, v35\n\t" \
  "v_cmp_eq_u32 vcc, -1, v75\n\t" \
  "s_cbranch_vccz LGO2_%=\n\t" \
  "s_add_u32 s68, s68, 1\n\t" \
  "s_waitcnt lgkmcnt(0)\n\t" \
  "s_cmp_gt_u32 s68, s83\n\t" \
  "s_cbranch_scc1 LGV2_%=\n\t" \
  "s_barrier\n\t" \
  "s_waitcnt vmcnt(8)\n\t" \
  "s_branch LG2_%=\n\t" \
  "RWT2_%=f:\n\t" \
  "s_cmp_lt_i32 s73, -2\n\t" \
  "s_cbranch_scc0 RWT2_%=g\n\t" \
  "s_nop 4\n\t" \
  "s_branch RWT2_%=g\n\t" \
  "LGV2_%=:\n\t" \
  "s_mov_b32 s83, 0\n\t" \
  "s_lshl_b32 s60, s36, 4\n\t" \
  "s_or_b32 s60, s60, s48\n\t" \
  "s_or_b32 s60, s60, 805306368\n\t" \
  "v_mov_b32 v76, s60\n\t" \
  "v_mov_b32 v77, 0\n\t" \
  "s_mov_b64 exec, 1\n\t" \
  "buffer_atomic_cmpswap v[76:77], off, s[28:31], s92 sc1\n\t" \
  "s_mov_b64 exec, -1\n\t" \
  "s_branch LGO2_%=\n\t" \
  "LLAST2_%=:\n\t" \
  "s_waitcnt vmcnt(0) lgkmcnt(0)\n\t" \
  "s_barrier\n\t" \
  "s_branch LEND_%=\n\t" \
  ".p2align 6\n\t" \
  "LG3_%=:\n\t" \
  "s_barrier\n\t" \
  "buffer_load_dwordx4 v[52:55], v69, s[28:31], s50 offen sc1\n\t" \
  "buffer_load_dwordx4 v[56:59], v69, s[28:31], s50 offen offset:1024 sc1\n\t" \
  "s_add_u32 s64, s50, 16384\n\t" \
  "s_movk_i32 m0, 11264\n\t" \
  "v_lshlrev_b32 v75, 1, v69\n\t" \
  "buffer_load_dword v75, s[28:31], s64 offen sc1 lds\n\t" \
  "s_add_u32 s47, s81, " CH_DL3 "\n\t" \
  "s_lshl_b32 s47, s47, 8\n\t" \
  "s_or_b32 s47, s47, " CH_B0 "\n\t" \
  "s_cmp_lt_u32 s48, " CH_ES "\n\t" \
  "s_cselect_b32 s60, " CH_EX ", 0\n\t" \
  "s_add_u32 s47, s47, s60\n\t" \
  "s_not_b32 s47, s47\n\t" \
  "s_add_u32 s71, s53, s46\n\t" \
  "ds_read_b128 v[44:47], v70 offset:0\n\t" \
  "ds_read_b128 v[48:51], v70 offset:1024\n\t" \
  "ds_read_b128 v[60:63], v70 offset:2048\n\t" \
  "ds_read_b128 v[64:67], v70 offset:3072\n\t" \
  "v_mov_b64 v[36:37], 0\n\t" \
  "v_mov_b64 v[38:39], 0\n\t" \
  "v_mov_b64 v[40:41], 0\n\t" \
  "v_mov_b64 v[42:43], 0\n\t" \
  "v_lshrrev_b32 v71, 1, v69\n\t" \
  "v_add_u32 v71, 4, v71\n\t" \
  "v_lshrrev_b32 v72, 3, v69\n\t" \
  "v_and_b32 v72, 0x78, v72\n\t" \
  "v_and_b32 v74, 0x30, v69\n\t" \
  "v_lshl_add_u32 v72, v74, 3, v72\n\t" \
  "s_add_u32 s54, s53, s36\n\t" \
  "s_add_u32 s55, s54, 573440\n\t" \
  "s_add_u32 s54, s54, 135168\n\t" \
  "s_add_u32 s75, s54, 262144\n\t" \
  "s_add_u32 s80, s53, s26\n\t" \
  "s_or_b32 s87, s50, 0x1fff80\n\t" \
  "s_add_u32 s87, s87, 4194304\n\t" \
  "s_barrier\n\t" \
  CH_DP0 \
  "ds_read_b128 v[0:3], v69 offset:0\n\t" \
  "ds_read_b128 v[4:7], v69 offset:1024\n\t" \
  "ds_read_b128 v[8:11], v69 offset:2048\n\t" \
  "ds_read_b128 v[12:15], v69 offset:3072\n\t" \
  "s_waitcnt lgkmcnt(3)\n\t" \
  "v_smfmac_f32_16x16x64_bf16 v[36:39], v[0:3], v[44:51], v68\n\t" \
  "ds_read_b128 v[44:47], v70 offset:4096\n\t" \
  "v_smfmac_f32_16x16x64_bf16 v[40:43], v[0:3], v[60:67], v68\n\t" \
  "ds_read_b128 v[48:51], v70 offset:5120\n\t" \
  "s_waitcnt lgkmcnt(4)\n\t" \
  "v_smfmac_f32_16x16x64_bf16 v[36:39], v[4:7], a[32:39], v68\n\t" \
  "ds_read_b128 v[60:63], v70 offset:6144\n\t" \
  "v_smfmac_f32_16x16x64_bf16 v[40:43], v[4:7], a[40:47], v68\n\t" \
  "ds_read_b128 v[64:67], v70 offset:7168\n\t" \
  "s_waitcnt lgkmcnt(5)\n\t" \
  "v_smfmac_f32_16x16x64_bf16 v[36:39], v[8:11], a[80:87], v68\n\t" \
  "s_barrier\n\t" \
  CH_DP1 \
  "ds_read_b128 v[16:19], v69 offset:4096\n\t" \
  "v_smfmac_f32_16x16x64_bf16 v[40:43], v[8:11], a[88:95], v68\n\t" \
  "ds_read_b128 v[20:23], v69 offset:5120\n\t" \
  "s_waitcnt lgkmcnt(6)\n\t" \
  "v_smfmac_f32_16x16x64_bf16 v[36:39], v[12:15], a[120:127], v68\n\t" \
  "ds_read_b128 v[24:27], v69 offset:6144\n\t" \
  "s_waitcnt lgkmcnt(5)\n\t" \
  "v_smfmac_f32_16x16x64_bf16 v[40:43], v[12:15], v[44:51], v68\n\t" \
  "ds_read_b128 v[28:31], v69 offset:7168\n\t" \
  "s_waitcnt lgkmcnt(3)\n\t" \
  "v_smfmac_f32_16x16x64_bf16 v[36:39], v[16:19], a[160:167], v68\n\t" \
  "ds_read_b128 v[32:35], v69 offset:8192\n\t" \
  "v_smfmac_f32_16x16x64_bf16 v[40:43], v[16:19], a[168:175], v68\n\t" \
  "s_waitcnt lgkmcnt(3)\n\t" \
  "v_mov_b64 v[44:45], 0\n\t" \
  "v_smfmac_f32_16x16x64_bf16 v[36:39], v[20:23], a[200:207], v68\n\t" \
  "s_waitcnt lgkmcnt(2)\n\t" \
  "v_mov_b64 v[46:47], 0\n\t" \
  "v_smfmac_f32_16x16x64_bf16 v[36:39], v[24:27], a[240:247], v68\n\t" \
  "s_waitcnt lgkmcnt(1)\n\t" \
  "v_mov_b64 v[48:49], 0\n\t" \
  "v_smfmac_f32_16x16x64_bf16 v[36:39], v[28:31], v[112:119], v68\n\t" \
  "s_waitcnt lgkmcnt(0)\n\t" \
  "v_mov_b64 v[50:51], 0\n\t" \
  "v_smfmac_f32_16x16x64_bf16 v[36:39], v[32:35], v[160:167], v68\n\t" \
  "v_smfmac_f32_16x16x64_bf16 v[40:43], v[20:23], v[60:67], v68\n\t" \
  "ds_read_b128 v[60:63], v70 offset:8192\n\t" \
  "v_smfmac_f32_16x16x64_bf16 v[40:43], v[24:27], a[248:255], v68\n\t" \
  "ds_read_b128 v[64:67], v70 offset:9216\n\t" \
  "v_add_f32 v74, v36, v38\n\t" \
  "v_smfmac_f32_16x16x64_bf16 v[40:43], v[28:31], v[120:127], v68\n\t" \
  "v_add_f32 v37, v37, v39\n\t" \
  "v_smfmac_f32_16x16x64_bf16 v[40:43], v[32:35], v[168:175], v68\n\t" \
  "v_cmp_u_f32 vcc, v74, v37\n\t" \
  CH_DP2 \
  "v_smfmac_f32_16x16x64_bf16 v[44:47], v[0:3], a[0:7], v68\n\t" \
  "s_cbranch_vccnz LFAIL3_%=\n\t" \
  "LGO3_%=:\n\t" \
  "v_cvt_pk_bf16_f32 v74, v74, v37\n\t" \
  "v_smfmac_f32_16x16x64_bf16 v[44:47], v[4:7], a[48:55], v68\n\t" \
  "s_waitcnt lgkmcnt(0)\n\t" \
  "v_add_f32 v75, v40, v42\n\t" \
  "v_smfmac_f32_16x16x64_bf16 v[44:47], v[8:11], v[60:67], v68\n\t" \
  "ds_read_b128 v[60:63], v70 offset:10240\n\t" \
  "v_add_f32 v41, v41, v43\n\t" \
  "v_cvt_pk_bf16_f32 v75, v75, v41\n\t" \
  "v_smfmac_f32_16x16x64_bf16 v[44:47], v[12:15], a[128:135], v68\n\t" \
  "ds_write2st64_b32 v71, v74, v75 offset0:40 offset1:42\n\t" \
  "v_smfmac_f32_16x16x64_bf16 v[44:47], v[16:19], a[176:183], v68\n\t" \
  "ds_read_b128 v[64:67], v70 offset:11264\n\t" \
  "s_memrealtime s[66:67]\n\t" \
  "v_smfmac_f32_16x16x64_bf16 v[44:47], v[20:23], a[208:215], v68\n\t" \
  "v_smfmac_f32_16x16x64_bf16 v[44:47], v[24:27], v[80:87], v68\n\t" \
  "v_smfmac_f32_16x16x64_bf16 v[44:47], v[28:31], v[128:135], v68\n\t" \
  "s_waitcnt lgkmcnt(0)\n\t" \
  "v_smfmac_f32_16x16x64_bf16 v[44:47], v[32:35], v[60:67], v68\n\t" \
  "s_barrier\n\t" \
  "ds_read_b64_tr_b16 v[76:77], v72 offset:10240\n\t" \
  CH_DP3 \
  "v_smfmac_f32_16x16x64_bf16 v[48:51], v[0:3], a[8:15], v68\n\t" \
  "s_lshl_b32 s60, s66, 8\n\t" \
  "s_sub_u32 s60, s47, s60\n\t" \
  "ds_read_b64_tr_b16 v[78:79], v72 offset:10752\n\t" \
  "v_mov_b32 v75, s60\n\t" \
  "v_smfmac_f32_16x16x64_bf16 v[48:51], v[4:7], a[56:63], v68\n\t" \
  "s_mov_b64 exec, s[24:25]\n\t" \
  "buffer_atomic_umin v75, v69, s[28:31], s71 offen sc1\n\t" \
  "s_mov_b64 exec, -1\n\t" \
  "v_add_f32 v74, v44, v46\n\t" \
  "v_smfmac_f32_16x16x64_bf16 v[48:51], v[8:11], a[96:103], v68\n\t" \
  "buffer_load_dwordx4 v[60:63], v69, s[28:31], s64 offen sc1\n\t" \
  "v_add_f32 v45, v45, v47\n\t" \
  "v_smfmac_f32_16x16x64_bf16 v[48:51], v[12:15], a[136:143], v68\n\t" \
  "s_waitcnt vmcnt(4)\n\t" \
  "s_waitcnt lgkmcnt(0)\n\t" \
  "v_cvt_pk_bf16_f32 v74, v74, v45\n\t" \
  "v_mfma_f32_16x16x32_bf16 v[36:39], v[52:55], v[76:79], 0\n\t" \
  "s_waitcnt vmcnt(3)\n\t" \
  "v_mfma_f32_16x16x32_bf16 v[40:43], v[56:59], v[76:79], 0\n\t" \
  "v_mov_b64 v[52:53], 0\n\t" \
  "v_mov_b64 v[54:55], 0\n\t" \
  "v_smfmac_f32_16x16x64_bf16 v[48:51], v[16:19], a[184:191], v68\n\t" \
  "buffer_load_dwordx4 v[64:67], v69, s[28:31], s64 offen offset:1024 sc1\n\t" \
  "v_mov_b64 v[56:57], 0\n\t" \
  "v_mov_b64 v[58:59], 0\n\t" \
  "v_smfmac_f32_16x16x64_bf16 v[48:51], v[20:23], a[216:223], v68\n\t" \
  "ds_write_b32 v71, v74 offset:1024\n\t" \
  "v_add_f32_dpp v36, v40, v36 quad_perm:[2,3,0,1] row_mask:0xf bank_mask:0xf\n\t" \
  "v_smfmac_f32_16x16x64_bf16 v[48:51], v[24:27], v[88:95], v68\n\t" \
  "v_add_f32_dpp v37, v41, v37 quad_perm:[2,3,0,1] row_mask:0xf bank_mask:0xf\n\t" \
  "v_smfmac_f32_16x16x64_bf16 v[48:51], v[28:31], v[136:143], v68\n\t" \
  "v_add_f32_dpp v38, v42, v38 quad_perm:[2,3,0,1] row_mask:0xf bank_mask:0xf\n\t" \
  "v_smfmac_f32_16x16x64_bf16 v[48:51], v[32:35], v[176:183], v68\n\t" \
  "v_add_f32_dpp v39, v43, v39 quad_perm:[2,3,0,1] row_mask:0xf bank_mask:0xf\n\t" \
  CH_DP4 \
  "v_smfmac_f32_16x16x64_bf16 v[52:55], v[0:3], a[16:23], v68\n\t" \
  "s_mov_b64 exec, s[58:59]\n\t" \
  "ds_write_b128 v69, v[36:39] offset:3104\n\t" \
  "s_mov_b64 exec, -1\n\t" \
  "v_smfmac_f32_16x16x64_bf16 v[52:55], v[4:7], a[64:71], v68\n\t" \
  "v_add_f32 v75, v48, v50\n\t" \
  "v_add_f32 v49, v49, v51\n\t" \
  "v_cvt_pk_bf16_f32 v75, v75, v49\n\t" \
  "v_smfmac_f32_16x16x64_bf16 v[56:59], v[0:3], a[24:31], v68\n\t" \
  "ds_write_b32 v71, v75 offset:1536\n\t" \
  "v_smfmac_f32_16x16x64_bf16 v[56:59], v[4:7], a[72:79], v68\n\t" \
  "ds_read_b64_tr_b16 v[0:1], v72 offset:9216\n\t" \
  "ds_read_b64_tr_b16 v[2:3], v72 offset:9728\n\t" \
  "v_smfmac_f32_16x16x64_bf16 v[52:55], v[8:11], a[104:111], v68\n\t" \
  "ds_read_b128 v[36:39], v70 offset:12288\n\t" \
  "v_smfmac_f32_16x16x64_bf16 v[56:59], v[8:11], a[112:119], v68\n\t" \
  "buffer_load_dword off, s[28:31], s87 sc1 lds\n\t" \
  "ds_read_b128 v[40:43], v70 offset:13312\n\t" \
  "v_smfmac_f32_16x16x64_bf16 v[52:55], v[12:15], a[144:151], v68\n\t" \
  "s_waitcnt lgkmcnt(4)\n\t" \
  "s_barrier\n\t" \
  CH_DP5 \
  "ds_read_b64_tr_b16 v[44:45], v72 offset:1024\n\t" \
  "ds_read_b64_tr_b16 v[46:47], v72 offset:1536\n\t" \
  "v_smfmac_f32_16x16x64_bf16 v[56:59], v[12:15], a[152:159], v68\n\t" \
  "ds_read_b64_tr_b16 v[48:49], v72 offset:0\n\t" \
  "ds_read_b64_tr_b16 v[50:51], v72 offset:512\n\t" \
  "v_smfmac_f32_16x16x64_bf16 v[52:55], v[16:19], a[192:199], v68\n\t" \
  "ds_read_b128 v[4:7], v69 offset:2048\n\t" \
  "s_waitcnt lgkmcnt(5)\n\t" \
  "v_smfmac_f32_16x16x64_bf16 v[56:59], v[16:19], v[36:43], v68\n\t" \
  "ds_read_b128 v[8:11], v69 offset:3072\n\t" \
  "s_waitcnt lgkmcnt(4)\n\t" \
  "v_mfma_f32_16x16x32_bf16 v[12:15], v[44:47], v[76:79], 0\n\t" \
  "ds_read_b128 v[36:39], v70 offset:14336\n\t" \
  "s_waitcnt lgkmcnt(3)\n\t" \
  "v_mfma_f32_16x16x32_bf16 v[12:15], v[48:51], v[0:3], v[12:15]\n\t" \
  "ds_read_b128 v[40:43], v70 offset:15360\n\t" \
  "v_smfmac_f32_16x16x64_bf16 v[52:55], v[20:23], a[224:231], v68\n\t" \
  "s_waitcnt lgkmcnt(2)\n\t" \
  "v_add_f32 v4, v4, v8\n\t" \
  "v_add_f32 v5, v5, v9\n\t" \
  "v_add_f32 v6, v6, v10\n\t" \
  CH_DP6 \
  "v_cndmask_b32 v78, v13, v14, s[56:57]\n\t" \
  "v_cndmask_b32 v76, v12, v15, s[56:57]\n\t" \
  "v_add_f32 v7, v7, v11\n\t" \
  "v_max3_f32 v77, v4, v5, v6\n\t" \
  "v_add_f32_dpp v76, v78, v76 quad_perm:[2,3,0,1] row_mask:0xf bank_mask:0xf\n\t" \
  "v_max3_f32 v77, v77, v7, v76\n\t" \
  "v_smfmac_f32_16x16x64_bf16 v[56:59], v[20:23], a[232:239], v68\n\t" \
  "ds_read_b128 v[44:47], v70 offset:16384\n\t" \
  "v_max_f32_dpp v77, v77, v77 quad_perm:[2,3,0,1] row_mask:0xf bank_mask:0xf\n\t" \
  "v_mov_b32 v78, v77\n\t" \
  "v_smfmac_f32_16x16x64_bf16 v[52:55], v[24:27], v[96:103], v68\n\t" \
  "ds_read_b128 v[48:51], v70 offset:17408\n\t" \
  "v_permlane16_swap_b32 v77, v78\n\t" \
  "v_max_f32 v77, v77, v78\n\t" \
  "v_mov_b32 v78, v77\n\t" \
  "v_smfmac_f32_16x16x64_bf16 v[52:55], v[28:31], v[144:151], v68\n\t" \
  "ds_read_b128 v[16:19], v70 offset:18432\n\t" \
  "v_permlane32_swap_b32 v77, v78\n\t" \
  "v_max_f32 v77, v77, v78\n\t" \
  "v_smfmac_f32_16x16x64_bf16 v[52:55], v[32:35], v[184:191], v68\n\t" \
  "ds_read_b128 v[20:23], v70 offset:19456\n\t" \
  "v_mul_f32 v77, 0xbe0293ee, v77\n\t" \
  CH_DP7 \
  "v_smfmac_f32_16x16x64_bf16 v[56:59], v[24:27], v[104:111], v68\n\t" \
  "v_fmamk_f32 v4, v4, 0x3e0293ee, v77\n\t" \
  "v_smfmac_f32_16x16x64_bf16 v[56:59], v[28:31], v[152:159], v68\n\t" \
  "ds_read_b128 v[24:27], v70 offset:22528\n\t" \
  "s_waitcnt lgkmcnt(4)\n\t" \
  "v_fmamk_f32 v5, v5, 0x3e0293ee, v77\n\t" \
  "v_smfmac_f32_16x16x64_bf16 v[56:59], v[32:35], v[36:43], v68\n\t" \
  "ds_read_b128 v[28:31], v70 offset:23552\n\t" \
  "v_fmamk_f32 v6, v6, 0x3e0293ee, v77\n\t" \
  "v_fmamk_f32 v7, v7, 0x3e0293ee, v77\n\t" \
  "v_fmamk_f32 v76, v76, 0x3e0293ee, v77\n\t" \
  "v_exp_f32 v4, v4\n\t" \
  "v_exp_f32 v5, v5\n\t" \
  "v_exp_f32 v6, v6\n\t" \
  "v_exp_f32 v7, v7\n\t" \
  "v_cvt_pk_bf16_f32 v4, v4, v5\n\t" \
  "v_exp_f32 v76, v76\n\t" \
  "v_cvt_pk_bf16_f32 v5, v6, v7\n\t" \
  "v_mov_b32_dpp v12, v4 quad_perm:[0,1,0,1] row_mask:0xf bank_mask:0xf\n\t" \
  "v_mov_b32_dpp v14, v4 quad_perm:[2,3,2,3] row_mask:0xf bank_mask:0xf\n\t" \
  "v_mov_b32_dpp v13, v5 quad_perm:[0,1,0,1] row_mask:0xf bank_mask:0xf\n\t" \
  "v_mov_b32_dpp v15, v5 quad_perm:[2,3,2,3] row_mask:0xf bank_mask:0xf\n\t" \
  "v_add_f32 v52, v52, v54\n\t" \
  "v_add_f32 v53, v53, v55\n\t" \
  "s_waitcnt vmcnt(2)\n\t" \
  CH_DP8 \
  "v_mfma_f32_16x16x32_bf16 v[0:3], v[60:63], v[12:15], 0\n\t" \
  "ds_read_b128 v[36:39], v70 offset:20480\n\t" \
  "v_add_f32 v56, v56, v58\n\t" \
  "v_add_f32 v57, v57, v59\n\t" \
  "v_cvt_pk_bf16_f32 v52, v52, v53\n\t" \
  "v_cvt_pk_bf16_f32 v53, v56, v57\n\t" \
  "s_waitcnt vmcnt(1)\n\t" \
  "v_mfma_f32_16x16x32_bf16 v[4:7], v[64:67], v[12:15], 0\n\t" \
  "ds_read_b128 v[40:43], v70 offset:21504\n\t" \
  "v_mov_b64 v[56:57], s[88:89]\n\t" \
  "v_mov_b64 v[58:59], s[88:89]\n\t" \
  "v_mov_b32_dpp v78, v76 quad_perm:[0,1,0,1] row_mask:0xf bank_mask:0xf\n\t" \
  "v_mov_b32_dpp v79, v76 quad_perm:[2,3,2,3] row_mask:0xf bank_mask:0xf\n\t" \
  "v_mfma_f32_16x16x32_bf16 v[32:35], v[56:59], v[12:15], 0\n\t" \
  "ds_read_b128 v[60:63], v70 offset:24576\n\t" \
  "v_cvt_pk_bf16_f32 v11, v78, v79\n\t" \
  "v_cvt_pk_bf16_f32 v8, v78, v79\n\t" \
  "v_mov_b64 v[54:55], 0\n\t" \
  "v_mov_b32 v9, 0\n\t" \
  "v_mov_b32 v10, 0\n\t" \
  "v_mov_b64 v[14:15], 0\n\t" \
  "s_nop 0\n\t" \
  "v_mfma_f32_16x16x32_bf16 v[32:35], v[54:57], v[8:11], v[32:35]\n\t" \
  "s_cmp_eq_u32 s48, 63\n\t" \
  "v_mfma_f32_16x16x32_bf16 v[0:3], v[52:55], v[8:11], v[0:3]\n\t" \
  "buffer_store_dwordx2 v[52:53], v71, s[28:31], s69 offen offset:3580 sc1\n\t" \
  "ds_read_b128 v[64:67], v70 offset:25600\n\t" \
  "v_mov_b64 v[56:57], 0\n\t" \
  "v_mov_b64 v[58:59], 0\n\t" \
  "v_mfma_f32_16x16x32_bf16 v[4:7], v[52:55], v[10:13], v[4:7]\n\t" \
  "s_cbranch_scc1 LLAST3_%=\n\t" \
  "v_rcp_f32 v74, v32\n\t" \
  "v_mov_b64 v[12:13], 0\n\t" \
  "v_mov_b32 v8, 0\n\t" \
  "v_pk_mul_f32 v[0:1], v[0:1], v[74:75] op_sel_hi:[1,0]\n\t" \
  "v_pk_mul_f32 v[2:3], v[2:3], v[74:75] op_sel_hi:[1,0]\n\t" \
  "v_cvt_pk_bf16_f32 v76, v0, v1\n\t" \
  "v_cvt_pk_bf16_f32 v77, v2, v3\n\t" \
  "v_pk_mul_f32 v[4:5], v[4:5], v[74:75] op_sel_hi:[1,0]\n\t" \
  "v_pk_mul_f32 v[6:7], v[6:7], v[74:75] op_sel_hi:[1,0]\n\t" \
  "v_cvt_pk_bf16_f32 v78, v4, v5\n\t" \
  "v_cvt_pk_bf16_f32 v79, v6, v7\n\t" \
  "ds_write_b128 v69, v[76:79] offset:7168\n\t" \
  CH_DP9 \
  "s_waitcnt lgkmcnt(2)\n\t" \
  "v_smfmac_f32_16x16x64_bf16 v[12:15], v[76:79], v[44:51], v68\n\t" \
  "ds_read_b128 v[44:47], v70 offset:26624\n\t" \
  "v_mov_b32 v11, 0\n\t" \
  "v_smfmac_f32_16x16x64_bf16 v[56:59], v[76:79], v[16:23], v68\n\t" \
  "ds_read_b128 v[48:51], v70 offset:27648\n\t" \
  "v_mov_b64 v[52:53], 0\n\t" \
  "v_smfmac_f32_16x16x64_bf16 v[8:11], v[76:79], v[36:43], v68\n\t" \
  "ds_read_b128 v[16:19], v70 offset:28672\n\t" \
  "v_smfmac_f32_16x16x64_bf16 v[52:55], v[76:79], v[24:31], v68\n\t" \
  "s_waitcnt lgkmcnt(3)\n\t" \
  "s_barrier\n\t" \
  CH_DP10 \
  "ds_read_b128 v[0:3], v69 offset:6144\n\t" \
  "ds_read_b128 v[4:7], v69 offset:5120\n\t" \
  "v_lshrrev_b32 v71, 7, v69\n\t" \
  "v_and_b32 v74, 0x70, v69\n\t" \
  "v_lshl_add_u32 v71, v71, 14, v74\n\t" \
  "v_bfe_u32 v72, v69, 2, 7\n\t" \
  "v_and_b32 v74, 0x200, v69\n\t" \
  "s_waitcnt lgkmcnt(1)\n\t" \
  "v_smfmac_f32_16x16x64_bf16 v[8:11], v[0:3], v[60:67], v68\n\t" \
  "ds_read_b128 v[20:23], v70 offset:29696\n\t" \
  "v_mov_b64 v[76:77], 0\n\t" \
  "v_smfmac_f32_16x16x64_bf16 v[12:15], v[0:3], v[200:207], v68\n\t" \
  "ds_read_b128 v[32:35], v69 offset:4096\n\t" \
  "v_mov_b64 v[78:79], 0\n\t" \
  "v_smfmac_f32_16x16x64_bf16 v[52:55], v[0:3], v[44:51], v68\n\t" \
  "buffer_load_dword v75, off, s[28:31], s80 sc1\n\t" \
  "ds_read_b128 v[24:27], v70 offset:30720\n\t" \
  "v_lshl_add_u32 v72, v74, 3, v72\n\t" \
  "v_smfmac_f32_16x16x64_bf16 v[56:59], v[0:3], v[232:239], v68\n\t" \
  "ds_read_b128 v[28:31], v70 offset:31744\n\t" \
  "s_waitcnt lgkmcnt(4)\n\t" \
  "v_smfmac_f32_16x16x64_bf16 v[12:15], v[4:7], v[208:215], v68\n\t" \
  "ds_read_b128 v[60:63], v70 offset:32768\n\t" \
  "s_waitcnt lgkmcnt(4)\n\t" \
  "v_smfmac_f32_16x16x64_bf16 v[8:11], v[4:7], v[16:23], v68\n\t" \
  "ds_read_b128 v[64:67], v70 offset:33792\n\t" \
  "s_waitcnt lgkmcnt(4)\n\t" \
  "v_smfmac_f32_16x16x64_bf16 v[8:11], v[32:35], v[192:199], v68\n\t" \
  "ds_read_b128 v[36:39], v73 offset:4096\n\t" \
  "v_smfmac_f32_16x16x64_bf16 v[12:15], v[32:35], v[216:223], v68\n\t" \
  "ds_read_b128 v[40:43], v73 offset:6144\n\t" \
  "s_waitcnt lgkmcnt(4)\n\t" \
  "v_smfmac_f32_16x16x64_bf16 v[52:55], v[4:7], v[24:31], v68\n\t" \
  "ds_read_b128 v[44:47], v70 offset:34816\n\t" \
  "v_cvt_pk_bf16_f32 v8, v8, v9\n\t" \
  "v_smfmac_f32_16x16x64_bf16 v[52:55], v[32:35], v[224:231], v68\n\t" \
  "ds_read_b128 v[48:51], v70 offset:35840\n\t" \
  "v_cvt_pk_bf16_f32 v9, v10, v11\n\t" \
  "v_smfmac_f32_16x16x64_bf16 v[56:59], v[4:7], v[240:247], v68\n\t" \
  "s_waitcnt lgkmcnt(4)\n\t" \
  "v_cvt_pk_bf16_f32 v10, v12, v13\n\t" \
  "v_smfmac_f32_16x16x64_bf16 v[56:59], v[32:35], v[60:67], v68\n\t" \
  "s_waitcnt lgkmcnt(3)\n\t" \
  "v_cvt_pk_bf16_f32 v11, v14, v15\n\t" \
  "v_smfmac_f32_16x16x64_bf16 v[76:79], v[36:39], v[248:255], v68\n\t" \
  "buffer_store_dwordx4 v[8:11], v71, s[28:31], s54 offen sc1\n\t" \
  "s_waitcnt lgkmcnt(0)\n\t" \
  CH_DP11 \
  "v_cvt_pk_bf16_f32 v52, v52, v53\n\t" \
  "v_smfmac_f32_16x16x64_bf16 v[76:79], v[40:43], v[44:51], v68\n\t" \
  "v_cvt_pk_bf16_f32 v53, v54, v55\n\t" \
  "v_cvt_pk_bf16_f32 v54, v56, v57\n\t" \
  "v_cvt_pk_bf16_f32 v55, v58, v59\n\t" \
  "buffer_store_dwordx4 v[52:55], v71, s[28:31], s75 offen sc1\n\t" \
  "s_nop 2\n\t" \
  "v_add_f32 v76, v76, v78\n\t" \
  "v_add_f32 v77, v77, v79\n\t" \
  "v_cvt_pk_bf16_f32 v76, v76, v77\n\t" \
  "buffer_store_dword v76, v72, s[28:31], s55 offen sc1\n\t" \
  CH_DP12 \
  "s_memrealtime s[70:71]\n\t" \
  "s_waitcnt vmcnt(3)\n\t" \
  "v_readfirstlane_b32 s65, v75\n\t" \
  "s_barrier\n\t" \
  "s_waitcnt lgkmcnt(0)\n\t" \
  "s_not_b32 s65, s65\n\t" \
  "s_lshl_b32 s61, s70, 8\n\t" \
  "s_andn2_b32 s64, s65, 0xff\n\t" \
  "s_sub_u32 s64, s64, s61\n\t" \
  "s_ashr_i32 s64, s64, 8\n\t" \
  "s_cmp_eq_u32 s65, 0\n\t" \
  "s_cselect_b32 s64, " CH_DLF ", s64\n\t" \
  "s_min_i32 s64, s64, s34\n\t" \
  "s_max_i32 s64, s64, " CH_DLL1 "\n\t" \
  "s_add_u32 s64, s64, s70\n\t" \
  CH_ST2 \
  "s_mov_b32 s74, 0\n\t" \
  "v_and_b32 v37, 0x30, v69\n\t" \
  "v_add_u32 v37, s52, v37\n\t" \
  "s_mov_b32 exec_lo, 0\n\t" \
  "v_mov_b32 v37, s85\n\t" \
  "s_mov_b32 exec_lo, -1\n\t" \
  "s_cmp_lt_i32 s41, 0\n\t" \
  "s_cbranch_scc1 LIDLE_%=\n\t" \
  "s_movk_i32 s60, 8192\n\t" \
  "RWT3_%=:\n\t" \
  "s_memrealtime s[72:73]\n\t" \
  "s_sub_u32 s60, s60, 1\n\t" \
  "s_cselect_b32 s61, 0, -1\n\t" \
  "s_waitcnt lgkmcnt(0)\n\t" \
  "s_sub_i32 s73, s72, s64\n\t" \
  "s_and_b32 s73, s73, s61\n\t" \
  "s_cmp_lt_i32 s73, " CH_FIN "\n\t" \
  "s_cbranch_scc1 RWT3_%=\n\t" \
  "s_cmp_lt_i32 s73, 0\n\t" \
  "s_cbranch_scc1 RWT3_%=f\n\t" \
  "RWT3_%=g:\n\t" \
  "RRETRY3_%=:\n\t" \
  CH_DP13 \
  "buffer_load_dwordx4 v[0:3], v69, s[28:31], s97 offen offset:0 sc1\n\t" \
  "buffer_load_dwordx4 v[4:7], v69, s[28:31], s97 offen offset:1024 sc1\n\t" \
  "buffer_load_dwordx4 v[8:11], v69, s[28:31], s97 offen offset:2048 sc1\n\t" \
  "buffer_load_dwordx4 v[12:15], v69, s[28:31], s97 offen offset:3072 sc1\n\t" \
  "v_lshrrev_b32 v20, 2, v69\n\t" \
  "v_and_b32 v20, 16, v20\n\t" \
  "v_mov_b32 v21, 0x3f80\n\t" \
  "v_lshlrev_b32 v21, v20, v21\n\t" \
  "v_bfe_u32 v20, v69, 4, 2\n\t" \
  "v_cmp_eq_u32 vcc, 0, v20\n\t" \
  "s_nop 1\n\t" \
  "v_cndmask_b32 v32, 0, v21, vcc\n\t" \
  "v_cmp_eq_u32 vcc, 1, v20\n\t" \
  "s_nop 1\n\t" \
  "v_cndmask_b32 v33, 0, v21, vcc\n\t" \
  "v_cmp_eq_u32 vcc, 2, v20\n\t" \
  "s_nop 1\n\t" \
  "v_cndmask_b32 v34, 0, v21, vcc\n\t" \
  "v_cmp_eq_u32 vcc, 3, v20\n\t" \
  "s_nop 1\n\t" \
  "v_cndmask_b32 v35, 0, v21, vcc\n\t" \
  "v_bfe_u32 v38, v69, 7, 1\n\t" \
  "v_bfe_u32 v20, v69, 9, 1\n\t" \
  "v_lshl_add_u32 v38, v20, 2, v38\n\t" \
  "v_lshlrev_b32 v38, 12, v38\n\t" \
  "v_and_b32 v20, 0x60, v69\n\t" \
  "v_add_u32 v38, v38, v20\n\t" \
  "v_bfe_u32 v20, v69, 8, 1\n\t" \
  "v_lshl_add_u32 v38, v20, 4, v38\n\t" \
  "v_bfe_u32 v20, v69, 4, 1\n\t" \
  "v_lshl_add_u32 v38, v20, 3, v38\n\t" \
  "v_mov_b32 v44, 0\n\t" \
  "buffer_load_dword v39, v37, s[28:31], 0 offen sc1\n\t" \
  CH_DP17 \
  "s_waitcnt vmcnt(4)\n\t" \
  CH_DP14 \
  "v_mfma_f32_16x16x32_bf16 v[40:43], v[32:35], v[0:3], 0\n\t" \
  "v_max3_u32 v44, v44, v0, v1\n\t" \
  "v_max3_u32 v44, v44, v2, v3\n\t" \
  "s_waitcnt vmcnt(3)\n\t" \
  "v_mfma_f32_16x16x32_bf16 v[40:43], v[32:35], v[4:7], v[40:43]\n\t" \
  "v_max3_u32 v44, v44, v4, v5\n\t" \
  "v_max3_u32 v44, v44, v6, v7\n\t" \
  "s_waitcnt vmcnt(2)\n\t" \
  "v_mfma_f32_16x16x32_bf16 v[40:43], v[32:35], v[8:11], v[40:43]\n\t" \
  "v_max3_u32 v44, v44, v8, v9\n\t" \
  "v_max3_u32 v44, v44, v10, v11\n\t" \
  "s_waitcnt vmcnt(1)\n\t" \
  CH_DP15 \
  "v_mfma_f32_16x16x32_bf16 v[40:43], v[32:35], v[12:15], v[40:43]\n\t" \
  "v_max3_u32 v44, v44, v12, v13\n\t" \
  "v_max3_u32 v44, v44, v14, v15\n\t" \
  "v_cmp_eq_u32 vcc, -1, v44\n\t" \
  "s_cbranch_vccnz RFAIL3_%=\n\t" \
  "s_nop 3\n\t" \
  "v_add_f32_dpp v40, v40, v40 row_ror:8 row_mask:0xf bank_mask:0xf\n\t" \
  "v_add_f32_dpp v41, v41, v41 row_ror:8 row_mask:0xf bank_mask:0xf\n\t" \
  "v_add_f32_dpp v42, v42, v42 row_ror:8 row_mask:0xf bank_mask:0xf\n\t" \
  "v_add_f32_dpp v43, v43, v43 row_ror:8 row_mask:0xf bank_mask:0xf\n\t" \
  "v_cvt_pk_bf16_f32 v40, v40, v41\n\t" \
  "v_cvt_pk_bf16_f32 v41, v42, v43\n\t" \
  "buffer_store_dwordx2 v[40:41], v38, s[28:31], s98 offen sc1\n\t" \
  "RDONE3_%=:\n\t" \
  CH_DP16 \
  CH_ST4 \
  "s_barrier\n\t" \
  CH_ST3 \
  "s_waitcnt vmcnt(1)\n\t" \
  "v_cmp_eq_u32 vcc, 0, v39\n\t" \
  "s_sub_u32 s60, s70, s66\n\t" \
  "s_add_u32 s81, s81, 1\n\t" \
  "s_min_u32 s81, s81, s60\n\t" \
  CH_TRC \
  "s_add_u32 s48, s48, 1\n\t" \
  "s_mov_b32 s49, s53\n\t" \
  "s_add_u32 s53, s53, " CH_SLOT "\n\t" \
  "s_add_u32 s60, s53, " CH_SKIP "\n\t" \
  "s_lshr_b32 s61, s96, 1\n\t" \
  "s_add_u32 s86, s86, 1\n\t" \
  "s_cmp_eq_u32 s86, 3\n\t" \
  "s_cselect_b32 s53, s60, s53\n\t" \
  "s_cselect_b32 s96, s61, s96\n\t" \
  "s_cselect_b32 s86, 0, s86\n\t" \
  "s_andn2_b32 s53, s53, 0x2000\n\t" \
  "s_and_b32 s60, s96, 1\n\t" \
  "s_lshl_b32 s60, s60, 13\n\t" \
  "s_or_b32 s53, s53, s60\n\t" \
  "s_mov_b32 s68, 0\n\t" \
  "s_add_u32 s97, s41, s53\n\t" \
  "s_add_u32 s98, s37, s53\n\t" \
  "s_add_u32 s82, s48, 0\n\t" \
  CH_SEGC \
  "s_lshr_b64 s[60:61], s[90:91], s82\n\t" \
  "s_and_b32 s60, s60, 1\n\t" \
  "s_lshl_b32 s60, s60, 13\n\t" \
  "s_add_u32 s82, s82, " CH_SG0 "\n\t" \
  "s_lshl_b32 s82, s82, 21\n\t" \
  "s_or_b32 s82, s82, s60\n\t" \
  "s_add_u32 s50, s82, s38\n\t" \
  "s_add_u32 s69, s82, s39\n\t" \
  "s_add_u32 s82, s48, 1\n\t" \
  CH_SEGC \
  "s_lshr_b64 s[60:61], s[90:91], s82\n\t" \
  "s_and_b32 s60, s60, 1\n\t" \
  "s_lshl_b32 s60, s60, 13\n\t" \
  "s_add_u32 s82, s82, " CH_SG0 "\n\t" \
  "s_lshl_b32 s82, s82, 21\n\t" \
  "s_or_b32 s82, s82, s60\n\t" \
  "s_add_u32 s52, s82, s43\n\t" \
  "s_add_u32 s82, s48, " CH_ARMD "\n\t" \
  CH_SEGC \
  "s_lshr_b64 s[60:61], s[90:91], s82\n\t" \
  "s_and_b32 s60, s60, 1\n\t" \
  "s_lshl_b32 s60, s60, 13\n\t" \
  "s_add_u32 s82, s82, " CH_SG0 "\n\t" \
  "s_lshl_b32 s82, s82, 21\n\t" \
  "s_or_b32 s82, s82, s60\n\t" \
  "s_add_u32 s85, s82, s45\n\t" \
  "s_cmp_lt_i32 s48, " CH_ARMS "\n\t" \
  "s_cselect_b32 s85, s27, s85\n\t" \
  "s_cmp_eq_u32 s34, 0\n\t" \
  "s_cbranch_scc1 LINI_%=\n\t" \
  "LTK_%=:\n\t" \
  CH_TOKCHK \
  "s_mov_b32 s61, 0\n\t" \
  "LTW_%=:\n\t" \
  "buffer_load_dword v1, v37, s[28:31], 0 offen sc1\n\t" \
  "s_waitcnt vmcnt(0)\n\t" \
  "v_cmp_eq_u32 vcc, 0, v1\n\t" \
  "s_cbranch_vccz LG3_%=\n\t" \
  "s_add_u32 s61, s61, 1\n\t" \
  "s_sleep 2\n\t" \
  "s_cmp_gt_u32 s61, s84\n\t" \
  "s_cbranch_scc1 LTG_%=\n\t" \
  "s_and_b32 s60, s61, 1023\n\t" \
  "s_cmp_lg_u32 s60, 0\n\t" \
  "s_cbranch_scc1 LTW_%=\n\t" \
  "buffer_load_dword v46, off, s[28:31], s92 sc1\n\t" \
  "s_waitcnt vmcnt(0)\n\t" \
  "v_readfirstlane_b32 s60, v46\n\t" \
  "s_nop 0\n\t" \
  "s_cmp_eq_u32 s60, 0\n\t" \
  "s_cbranch_scc1 LTW_%=\n\t" \
  "LTG_%=:\n\t" \
  "s_mov_b32 s84, 0\n\t" \
  "s_lshl_b32 s60, s36, 4\n\t" \
  "s_or_b32 s60, s60, s48\n\t" \
  "s_or_b32 s60, s60, 268435456\n\t" \
  "v_mov_b32 v76, s60\n\t" \
  "v_mov_b32 v77, 0\n\t" \
  "s_mov_b64 exec, 1\n\t" \
  "buffer_atomic_cmpswap v[76:77], off, s[28:31], s92 sc1\n\t" \
  "s_mov_b64 exec, -1\n\t" \
  "s_branch LG3_%=\n\t" \
  "LIDLE_%=:\n\t" \
  "s_add_u32 s64, s64, 8\n\t" \
  "s_movk_i32 s60, 8192\n\t" \
  "IWT_%=:\n\t" \
  "s_memrealtime s[72:73]\n\t" \
  "s_sub_u32 s60, s60, 1\n\t" \
  "s_cselect_b32 s61, 0, -1\n\t" \
  "s_waitcnt lgkmcnt(0)\n\t" \
  "s_sub_i32 s73, s72, s64\n\t" \
  "s_and_b32 s73, s73, s61\n\t" \
  "s_cmp_lt_i32 s73, 0\n\t" \
  "s_cbranch_scc1 IWT_%=\n\t" \
  "buffer_load_dword v39, v37, s[28:31], 0 offen sc1\n\t" \
  "buffer_store_dword v37, off, s[28:31], s41\n\t" \
  "s_branch RDONE3_%=\n\t" \
  "RFAIL3_%=:\n\t" \
  "s_add_u32 s74, s74, 1\n\t" \
  "s_sleep " CH_RSL "\n\t" \
  "s_waitcnt vmcnt(0)\n\t" \
  "s_cmp_gt_u32 s74, s84\n\t" \
  "s_cbranch_scc1 RG3_%=\n\t" \
  "s_and_b32 s60, s74, 1023\n\t" \
  "s_cmp_lg_u32 s60, 0\n\t" \
  "s_cbranch_scc1 RRETRY3_%=\n\t" \
  "buffer_load_dword v46, off, s[28:31], s92 sc1\n\t" \
  "s_waitcnt vmcnt(0)\n\t" \
  "v_readfirstlane_b32 s60, v46\n\t" \
  "s_nop 0\n\t" \
  "s_cmp_eq_u32 s60, 0\n\t" \
  "s_cbranch_scc1 RRETRY3_%=\n\t" \
  "RG3_%=:\n\t" \
  "s_mov_b32 s84, 0\n\t" \
  "s_lshl_b32 s60, s36, 4\n\t" \
  "s_or_b32 s60, s60, s48\n\t" \
  "s_or_b32 s60, s60, 536870912\n\t" \
  "v_mov_b32 v76, s60\n\t" \
  "v_mov_b32 v77, 0\n\t" \
  "s_mov_b64 exec, 1\n\t" \
  "buffer_atomic_cmpswap v[76:77], off, s[28:31], s92 sc1\n\t" \
  "s_mov_b64 exec, -1\n\t" \
  "s_branch RDONE3_%=\n\t" \
  "LFAIL3_%=:\n\t" \
  "v_max_u32 v75, v0, v1\n\t" \
  "v_max3_u32 v75, v75, v2, v3\n\t" \
  "v_max3_u32 v75, v75, v4, v5\n\t" \
  "v_max3_u32 v75, v75, v6, v7\n\t" \
  "v_max3_u32 v75, v75, v8, v9\n\t" \
  "v_max3_u32 v75, v75, v10, v11\n\t" \
  "v_max3_u32 v75, v75, v12, v13\n\t" \
  "v_max3_u32 v75, v75, v14, v15\n\t" \
  "v_max3_u32 v75, v75, v16, v17\n\t" \
  "v_max3_u32 v75, v75, v18, v19\n\t" \
  "v_max3_u32 v75, v75, v20, v21\n\t" \
  "v_max3_u32 v75, v75, v22, v23\n\t" \
  "v_max3_u32 v75, v75, v24, v25\n\t" \
  "v_max3_u32 v75, v75, v26, v27\n\t" \
  "v_max3_u32 v75, v75, v28, v29\n\t" \
  "v_max3_u32 v75, v75, v30, v31\n\t" \
  "v_max3_u32 v75, v75, v32, v33\n\t" \
  "v_max3_u32 v75, v75, v34, v35\n\t" \
  "v_cmp_eq_u32 vcc, -1, v75\n\t" \
  "s_cbranch_vccz LGO3_%=\n\t" \
  "s_add_u32 s68, s68, 1\n\t" \
  "s_waitcnt lgkmcnt(0)\n\t" \
  "s_cmp_gt_u32 s68, s83\n\t" \
  "s_cbranch_scc1 LGV3_%=\n\t" \
  "s_barrier\n\t" \
  "s_waitcnt vmcnt(8)\n\t" \
  "s_branch LG3_%=\n\t" \
  "RWT3_%=f:\n\t" \
  "s_cmp_lt_i32 s73, -2\n\t" \
  "s_cbranch_scc0 RWT3_%=g\n\t" \
  "s_nop 4\n\t" \
  "s_branch RWT3_%=g\n\t" \
  "LGV3_%=:\n\t" \
  "s_mov_b32 s83, 0\n\t" \
  "s_lshl_b32 s60, s36, 4\n\t" \
  "s_or_b32 s60, s60, s48\n\t" \
  "s_or_b32 s60, s60, 805306368\n\t" \
  "v_mov_b32 v76, s60\n\t" \
  "v_mov_b32 v77, 0\n\t" \
  "s_mov_b64 exec, 1\n\t" \
  "buffer_atomic_cmpswap v[76:77], off, s[28:31], s92 sc1\n\t" \
  "s_mov_b64 exec, -1\n\t" \
  "s_branch LGO3_%=\n\t" \
  "LLAST3_%=:\n\t" \
  "s_waitcnt vmcnt(0) lgkmcnt(0)\n\t" \
  "s_barrier\n\t" \
  "LEND_%=:\n\t" \
  ""
typedef unsigned v16u __attribute__((ext_vector_type(16)));
#define CH_WCLOB "a0", "a1", "a2", "a3", "a4", "a5", "a6", "a7", "a8", "a9", "a10", "a11", "a12", "a13", "a14", "a15", "a16", "a17", "a18", "a19", "a20", "a21", "a22", "a23", "a24", "a25", "a26", "a27", "a28", "a29", "a30", "a31", "a32", "a33", "a34", "a35", "a36", "a37", "a38", "a39", "a40", "a41", "a42", "a43", "a44", "a45", "a46", "a47", "a48", "a49", "a50", "a51", "a52", "a53", "a54", "a55", "a56", "a57", "a58", "a59", "a60", "a61", "a62", "a63", "a64", "a65", "a66", "a67", "a68", "a69", "a70", "a71", "a72", "a73", "a74", "a75", "a76", "a77", "a78", "a79", "a80", "a81", "a82", "a83", "a84", "a85", "a86", "a87", "a88", "a89", "a90", "a91", "a92", "a93", "a94", "a95", "a96", "a97", "a98", "a99", "a100", "a101", "a102", "a103", "a104", "a105", "a106", "a107", "a108", "a109", "a110", "a111", "a112", "a113", "a114", "a115", "a116", "a117", "a118", "a119", "a120", "a121", "a122", "a123", "a124", "a125", "a126", "a127", "a128", "a129", "a130", "a131", "a132", "a133", "a134", "a135", "a136", "a137", "a138", "a139", "a140", "a141", "a142", "a143", "a144", "a145", "a146", "a147", "a148", "a149", "a150", "a151", "a152", "a153", "a154", "a155", "a156", "a157", "a158", "a159", "a160", "a161", "a162", "a163", "a164", "a165", "a166", "a167", "a168", "a169", "a170", "a171", "a172", "a173", "a174", "a175", "a176", "a177", "a178", "a179", "a180", "a181", "a182", "a183", "a184", "a185", "a186", "a187", "a188", "a189", "a190", "a191", "a192", "a193", "a194", "a195", "a196", "a197", "a198", "a199", "a200", "a201", "a202", "a203", "a204", "a205", "a206", "a207", "a208", "a209", "a210", "a211", "a212", "a213", "a214", "a215", "a216", "a217", "a218", "a219", "a220", "a221", "a222", "a223", "a224", "a225", "a226", "a227", "a228", "a229", "a230", "a231", "a232", "a233", "a234", "a235", "a236", "a237", "a238", "a239", "a240", "a241", "a242", "a243", "a244", "a245", "a246", "a247", "a248", "a249", "a250", "a251", "a252", "a253", "a254", "a255", "v80", "v81", "v82", "v83", "v84", "v85", "v86", "v87", "v88", "v89", "v90", "v91", "v92", "v93", "v94", "v95", "v96", "v97", "v98", "v99", "v100", "v101", "v102", "v103", "v104", "v105", "v106", "v107", "v108", "v109", "v110", "v111", "v112", "v113", "v114", "v115", "v116", "v117", "v118", "v119", "v120", "v121", "v122", "v123", "v124", "v125", "v126", "v127", "v128", "v129", "v130", "v131", "v132", "v133", "v134", "v135", "v136", "v137", "v138", "v139", "v140", "v141", "v142", "v143", "v144", "v145", "v146", "v147", "v148", "v149", "v150", "v151", "v152", "v153", "v154", "v155", "v156", "v157", "v158", "v159", "v160", "v161", "v162", "v163", "v164", "v165", "v166", "v167", "v168", "v169", "v170", "v171", "v172", "v173", "v174", "v175", "v176", "v177", "v178", "v179", "v180", "v181", "v182", "v183", "v184", "v185", "v186", "v187", "v188", "v189", "v190", "v191", "v192", "v193", "v194", "v195", "v196", "v197", "v198", "v199", "v200", "v201", "v202", "v203", "v204", "v205", "v206", "v207", "v208", "v209", "v210", "v211", "v212", "v213", "v214", "v215", "v216", "v217", "v218", "v219", "v220", "v221", "v222", "v223", "v224", "v225", "v226", "v227", "v228", "v229", "v230", "v231", "v232", "v233", "v234", "v235", "v236", "v237", "v238", "v239", "v240", "v241", "v242", "v243", "v244", "v245", "v246", "v247", "v248", "v249", "v250", "v251", "v252", "v253", "v254", "v255"
#define CH_H(b) (8 * ((b) & 3) + ((b) >> 3))
#define CH_L(b) (((b) >> 2) & 1)
__device__ __forceinline__ void start_work(char* smem, int wk);
__device__ __forceinline__ void chain_wg(char* smem) {
  if (CH_L((int)blockIdx.x)) start_work(smem, CH_H((int)blockIdx.x));
  const MP p = getp();
  const int b = (int)blockIdx.x, h = CH_H(b), L = CH_L(b);
  int w = __builtin_amdgcn_readfirstlane(threadIdx.x >> 6);
  {
    unsigned id; asm volatile("s_getreg_b32 %0, hwreg(HW_REG_HW_ID, 4, 2)" : "=s"(id));
    volatile __attribute__((address_space(3))) unsigned* lw = (volatile __attribute__((address_space(3))) unsigned*)smem;
    lw[w] = 1u << id;
    BAR();
    if (__builtin_amdgcn_readfirstlane(lw[0] | lw[1] | lw[2] | lw[3]) == 15u) w = (int)((SMAP >> (4u * id)) & 3u);
  }
  unsigned lp16 = (threadIdx.x & 63u) * 16u;
  rsrc_t X = mkr(p.xb, XB_BYTES);
  if ((unsigned)(size_t)(__attribute__((address_space(3))) char*)smem != 0u) return;
  const unsigned wsz = DM * HD * 2u - (unsigned)(h * DH) * 2u;
  const rsrc_t RQ = mkr(p.Wq + h * DH, wsz), RK = mkr(p.Wk + h * DH, wsz), RV = mkr(p.Wv + h * DH, wsz), RO = mkr(p.Wo + (long)h * DH * DM, DH * DM * 2u);
  unsigned xcc; asm volatile("s_getreg_b32 %0, hwreg(HW_REG_XCC_ID)" : "=s"(xcc)); xcc &= 3u;
  unsigned d96 = 0u;
  const unsigned c43 = P_FA * PAIR + (unsigned)h * 128u + 32u;
  const unsigned h128 = (unsigned)h * 128u, uw = (unsigned)w, t = uw >> 1, T = uw & 1u, NONE = 0xF0000000u;
  const unsigned u0 = w == 3 ? (unsigned)h >> 2 : (unsigned)((h & 1) + (h >> 3) * 2 + ((h >> 1) & 3) * 8 + (w == 2 ? 32 : 0));
  const bool idle = w == 3 && (h & 3);
  const unsigned c37 = idle ? NONE : (w == 3 ? AGT + u0 * 128u : AGO + (u0 >> 5) * 32768u + (u0 & 31u) * 128u);
  const unsigned c41 = idle ? NONE : (w == 3 ? 524288u + (u0 >> 1) * 16384u + (u0 & 1u) * 4096u : (u0 & 7u) * 16384u + ((u0 >> 3) & 1u) * 4096u + (u0 >> 4) * 131072u);
  const unsigned c42 = AGO + (xcc & 1u) * 16384u + (xcc >> 1) * 4096u;
  const unsigned c46 = (w == 3 ? (h & 7) == 5 && DL3 > -50 : h == 30) ? AGT + 1024u : NONE;
  const unsigned c38 = (unsigned)(3 * h + 1) * PAIR + T * 4096u + t * 2048u, c39 = (P_KV + (unsigned)(h >> 1)) * PAIR + (unsigned)(h & 1) * 4096u;
  const unsigned c45 = P_FB * PAIR + 4096u + (unsigned)(16 + (h & 15)) * 128u;
  unsigned d48 = 0u, d49 = 0u, d53 = SLOT, d86 = 1u, d65 = rtc();
  unsigned mn = 120u;
  v4u stt;
  asm volatile("s_waitcnt vmcnt(0) lgkmcnt(0)\n\t" CH_ASM "LCHEND:"
               : "+{s48}"(d48), "+{s49}"(d49), "+{s53}"(d53), "+{s65}"(d65), "+{s81}"(mn), "+{s86}"(d86), "+{s96}"(d96), "=&{s[76:79]}"(stt), "+{v69}"(lp16), "+{s[28:31]}"(X)
               : [Wq] "s"(RQ), [Wk] "s"(RK), [Wv] "s"(RV), [Wo] "s"(RO), "{s33}"(HT_OFF + 8192u + 256u + (unsigned)b * 4u), "{s36}"(h128), "{s37}"(c37), "{s40}"(w), "{s41}"(c41), "{s42}"(c42), "{s46}"(c46), "{s92}"(TMO_OFF), "{s38}"(c38), "{s39}"(c39), "{s43}"(c43), "{s45}"(c45), "{s32}"(L), "{s26}"(AGT + 1024u + xcc * 128u), "{s27}"(RDY_OFF(0) + (unsigned)((h % 9) * 1024 + (b & 7) * 128)), [ctab] "i"(CTAB_OFF), [ex] "i"(ED * 256 + EB), [dl1l] "i"(DL1 - LATE), [armd] "i"(3 - ARMK), [arms] "i"(ARMK - 3),
                 [cap] "i"(SPIN_CAP)
               : "v0", "v1", "v2", "v3", "v4", "v5", "v6", "v7", "v8", "v9", "v10", "v11", "v12", "v13", "v14", "v15", "v16", "v17", "v18", "v19",
                 "v20", "v21", "v22", "v23", "v24", "v25", "v26", "v27", "v28", "v29", "v30", "v31", "v32", "v33", "v34", "v35", "v36", "v37", "v38", "v39",
                 "v40", "v41", "v42", "v43", "v44", "v45", "v46", "v47", "v48", "v49", "v50", "v51", "v52", "v53", "v54", "v55", "v56", "v57", "v58", "v59",
                 "v60", "v61", "v62", "v63", "v64", "v65", "v66", "v67", "v68", "v70", "v71", "v72", "v73", "v74", "v75", "v76", "v77", "v78", "v79",
                 CH_WCLOB, "s34", "s35", "s24", "s25", "s90", "s91", "s47", "s87", "s50", "s51", "s52", "s69", "s82", "s85", "s54", "s75", "s80", "s55", "s56", "s57", "s58", "s59", "s60", "s61", "s62", "s63", "s64", "s66", "s67", "s68", "s70", "s71", "s72", "s73", "s74", "s88", "s89",
                 "s83", "s84", "s97", "s98", "s99", "s93", "s94", "s95", "m0", "vcc", "scc", "memory");
}
#define XQ 0u
#define XQL 0u
#define XK 0u
#define XT 0u
#define XA 0u
#define XAL 0u
#define XRL 0u
#define QLEAD 4
#define RONE 2
#define OAUX 0
#define PSP 4
#define PSR 8
#define RDLY 40
#define QLEAD1 2
#define QXW -18
#define QXC QXW
#define TK_QXW "%[qxw]"
#define TK_QXC "%[qxc]"
#define PSG 16
#define TK_PSG CH_S(PSG)
#define AX 30
#define AX1 45
#define AM 22
#define TK_AM CH_S(AM)
#define PSK 2
#define PSH 2
#define TK_CAP "%[cap]"
#define TK_CTAB "%[ctab]"
#define TK_SG0 CH_S(SG0)
#define TK_PSK CH_S(PSK)
#define TK_PSH CH_S(PSH)
#define TK_PA0
#define TK_PA1
#define TK_PA2
#define TK_PQ0
#define TK_PQ1
#define TK_PQ2
#define TT(s, i)
#define TK_AC(n)
#define TK_ACE
#define TK_AC0 TK_AC(212)
#define TK_AC1 TK_AC(213)
#define TK_AC2 TK_AC(214)
#define TK_AC3 TK_AC(215)
#define TK_AC4 TK_AC(216)
#define TK_AC5 TK_AC(217)
#define TK_AC6 TK_AC(218)
#define TK_AC7 TK_AC(219)
#define TK_AC8 TK_AC(220)
#define AP_ASM \
  "v_lshrrev_b32 v40, 8, v0\n\t" \
  "v_mul_u32_u24 v40, 4608, v40\n\t" \
  "v_and_b32 v41, 0xf0, v0\n\t" \
  "v_lshrrev_b32 v42, 3, v41\n\t" \
  "v_add3_u32 v41, v41, v40, s64\n\t" \
  "v_add3_u32 v42, v42, v40, s65\n\t" \
  "s_mov_b32 s62, 0x05040100\n\t" \
  "s_mov_b32 s63, 0x07060302\n\t" \
  "s_xor_b32 s60, s66, 0\n\t" \
  "s_add_u32 s60, s60, 0\n\t" \
  "s_add_u32 s61, s60, 18432\n\t" \
  "buffer_load_dwordx4 v[48:51], v41, %[W], s60 offen offset:0\n\t" \
  "buffer_load_dwordx4 v[52:55], v41, %[W], s60 offen offset:1152\n\t" \
  "buffer_load_dwordx4 v[56:59], v41, %[W], s60 offen offset:2304\n\t" \
  "buffer_load_dwordx4 v[60:63], v41, %[W], s60 offen offset:3456\n\t" \
  "buffer_load_dwordx4 v[64:67], v41, %[W], s61 offen offset:0\n\t" \
  "buffer_load_dwordx4 v[68:71], v41, %[W], s61 offen offset:1152\n\t" \
  "buffer_load_dwordx4 v[72:75], v41, %[W], s61 offen offset:2304\n\t" \
  "buffer_load_dwordx4 v[76:79], v41, %[W], s61 offen offset:3456\n\t" \
  "s_xor_b32 s60, s66, 0\n\t" \
  "s_add_u32 s60, s60, 36864\n\t" \
  "s_add_u32 s61, s60, 18432\n\t" \
  "buffer_load_dwordx4 v[80:83], v41, %[W], s60 offen offset:0\n\t" \
  "buffer_load_dwordx4 v[84:87], v41, %[W], s60 offen offset:1152\n\t" \
  "buffer_load_dwordx4 v[88:91], v41, %[W], s60 offen offset:2304\n\t" \
  "buffer_load_dwordx4 v[92:95], v41, %[W], s60 offen offset:3456\n\t" \
  "buffer_load_dwordx4 v[96:99], v41, %[W], s61 offen offset:0\n\t" \
  "buffer_load_dwordx4 v[100:103], v41, %[W], s61 offen offset:1152\n\t" \
  "buffer_load_dwordx4 v[104:107], v41, %[W], s61 offen offset:2304\n\t" \
  "buffer_load_dwordx4 v[108:111], v41, %[W], s61 offen offset:3456\n\t" \
  "s_xor_b32 s60, s66, 0\n\t" \
  "s_add_u32 s60, s60, 73728\n\t" \
  "s_add_u32 s61, s60, 18432\n\t" \
  "buffer_load_dwordx4 v[112:115], v41, %[W], s60 offen offset:0\n\t" \
  "buffer_load_dwordx4 v[116:119], v41, %[W], s60 offen offset:1152\n\t" \
  "buffer_load_dwordx4 v[120:123], v41, %[W], s60 offen offset:2304\n\t" \
  "buffer_load_dwordx4 v[124:127], v41, %[W], s60 offen offset:3456\n\t" \
  "buffer_load_dwordx4 v[128:131], v41, %[W], s61 offen offset:0\n\t" \
  "buffer_load_dwordx4 v[132:135], v41, %[W], s61 offen offset:1152\n\t" \
  "buffer_load_dwordx4 v[136:139], v41, %[W], s61 offen offset:2304\n\t" \
  "buffer_load_dwordx4 v[140:143], v41, %[W], s61 offen offset:3456\n\t" \
  "s_xor_b32 s60, s66, 0\n\t" \
  "s_add_u32 s60, s60, 110592\n\t" \
  "s_add_u32 s61, s60, 18432\n\t" \
  "buffer_load_dwordx4 v[144:147], v41, %[W], s60 offen offset:0\n\t" \
  "buffer_load_dwordx4 v[148:151], v41, %[W], s60 offen offset:1152\n\t" \
  "buffer_load_dwordx4 v[152:155], v41, %[W], s60 offen offset:2304\n\t" \
  "buffer_load_dwordx4 v[156:159], v41, %[W], s60 offen offset:3456\n\t" \
  "buffer_load_dwordx4 v[160:163], v41, %[W], s61 offen offset:0\n\t" \
  "buffer_load_dwordx4 v[164:167], v41, %[W], s61 offen offset:1152\n\t" \
  "buffer_load_dwordx4 v[168:171], v41, %[W], s61 offen offset:2304\n\t" \
  "buffer_load_dwordx4 v[172:175], v41, %[W], s61 offen offset:3456\n\t" \
  "s_xor_b32 s60, s66, 147456\n\t" \
  "s_add_u32 s60, s60, 0\n\t" \
  "s_add_u32 s61, s60, 18432\n\t" \
  "buffer_load_dwordx4 v[176:179], v41, %[W], s60 offen offset:0\n\t" \
  "buffer_load_dwordx4 v[180:183], v41, %[W], s60 offen offset:1152\n\t" \
  "buffer_load_dwordx4 v[184:187], v41, %[W], s60 offen offset:2304\n\t" \
  "buffer_load_dwordx4 v[188:191], v41, %[W], s60 offen offset:3456\n\t" \
  "buffer_load_dwordx4 v[192:195], v41, %[W], s61 offen offset:0\n\t" \
  "buffer_load_dwordx4 v[196:199], v41, %[W], s61 offen offset:1152\n\t" \
  "buffer_load_dwordx4 v[200:203], v41, %[W], s61 offen offset:2304\n\t" \
  "buffer_load_dwordx4 v[204:207], v41, %[W], s61 offen offset:3456\n\t" \
  "s_waitcnt vmcnt(38)\n\t" \
  "v_perm_b32 v44, v52, v48, s62\n\t" \
  "s_waitcnt vmcnt(36)\n\t" \
  "v_perm_b32 v45, v60, v56, s62\n\t" \
  "s_waitcnt vmcnt(34)\n\t" \
  "v_perm_b32 v46, v68, v64, s62\n\t" \
  "s_waitcnt vmcnt(32)\n\t" \
  "v_perm_b32 v47, v76, v72, s62\n\t" \
  "v_accvgpr_write_b32 a0, v44\n\t" \
  "v_accvgpr_write_b32 a1, v45\n\t" \
  "v_accvgpr_write_b32 a2, v46\n\t" \
  "v_accvgpr_write_b32 a3, v47\n\t" \
  "v_perm_b32 v44, v52, v48, s63\n\t" \
  "v_perm_b32 v45, v60, v56, s63\n\t" \
  "v_perm_b32 v46, v68, v64, s63\n\t" \
  "v_perm_b32 v47, v76, v72, s63\n\t" \
  "v_accvgpr_write_b32 a4, v44\n\t" \
  "v_accvgpr_write_b32 a5, v45\n\t" \
  "v_accvgpr_write_b32 a6, v46\n\t" \
  "v_accvgpr_write_b32 a7, v47\n\t" \
  "v_perm_b32 v44, v53, v49, s62\n\t" \
  "v_perm_b32 v45, v61, v57, s62\n\t" \
  "v_perm_b32 v46, v69, v65, s62\n\t" \
  "v_perm_b32 v47, v77, v73, s62\n\t" \
  "v_accvgpr_write_b32 a8, v44\n\t" \
  "v_accvgpr_write_b32 a9, v45\n\t" \
  "v_accvgpr_write_b32 a10, v46\n\t" \
  "v_accvgpr_write_b32 a11, v47\n\t" \
  "v_perm_b32 v44, v53, v49, s63\n\t" \
  "v_perm_b32 v45, v61, v57, s63\n\t" \
  "v_perm_b32 v46, v69, v65, s63\n\t" \
  "v_perm_b32 v47, v77, v73, s63\n\t" \
  "v_accvgpr_write_b32 a12, v44\n\t" \
  "v_accvgpr_write_b32 a13, v45\n\t" \
  "v_accvgpr_write_b32 a14, v46\n\t" \
  "v_accvgpr_write_b32 a15, v47\n\t" \
  "v_perm_b32 v44, v54, v50, s62\n\t" \
  "v_perm_b32 v45, v62, v58, s62\n\t" \
  "v_perm_b32 v46, v70, v66, s62\n\t" \
  "v_perm_b32 v47, v78, v74, s62\n\t" \
  "v_accvgpr_write_b32 a16, v44\n\t" \
  "v_accvgpr_write_b32 a17, v45\n\t" \
  "v_accvgpr_write_b32 a18, v46\n\t" \
  "v_accvgpr_write_b32 a19, v47\n\t" \
  "v_perm_b32 v44, v54, v50, s63\n\t" \
  "v_perm_b32 v45, v62, v58, s63\n\t" \
  "v_perm_b32 v46, v70, v66, s63\n\t" \
  "v_perm_b32 v47, v78, v74, s63\n\t" \
  "v_accvgpr_write_b32 a20, v44\n\t" \
  "v_accvgpr_write_b32 a21, v45\n\t" \
  "v_accvgpr_write_b32 a22, v46\n\t" \
  "v_accvgpr_write_b32 a23, v47\n\t" \
  "v_perm_b32 v44, v55, v51, s62\n\t" \
  "v_perm_b32 v45, v63, v59, s62\n\t" \
  "v_perm_b32 v46, v71, v67, s62\n\t" \
  "v_perm_b32 v47, v79, v75, s62\n\t" \
  "v_accvgpr_write_b32 a24, v44\n\t" \
  "v_accvgpr_write_b32 a25, v45\n\t" \
  "v_accvgpr_write_b32 a26, v46\n\t" \
  "v_accvgpr_write_b32 a27, v47\n\t" \
  "v_perm_b32 v44, v55, v51, s63\n\t" \
  "v_perm_b32 v45, v63, v59, s63\n\t" \
  "v_perm_b32 v46, v71, v67, s63\n\t" \
  "v_perm_b32 v47, v79, v75, s63\n\t" \
  "v_accvgpr_write_b32 a28, v44\n\t" \
  "v_accvgpr_write_b32 a29, v45\n\t" \
  "v_accvgpr_write_b32 a30, v46\n\t" \
  "v_accvgpr_write_b32 a31, v47\n\t" \
  "s_xor_b32 s60, s66, 147456\n\t" \
  "s_add_u32 s60, s60, 36864\n\t" \
  "s_add_u32 s61, s60, 18432\n\t" \
  "buffer_load_dwordx4 v[48:51], v41, %[W], s60 offen offset:0\n\t" \
  "buffer_load_dwordx4 v[52:55], v41, %[W], s60 offen offset:1152\n\t" \
  "buffer_load_dwordx4 v[56:59], v41, %[W], s60 offen offset:2304\n\t" \
  "buffer_load_dwordx4 v[60:63], v41, %[W], s60 offen offset:3456\n\t" \
  "buffer_load_dwordx4 v[64:67], v41, %[W], s61 offen offset:0\n\t" \
  "buffer_load_dwordx4 v[68:71], v41, %[W], s61 offen offset:1152\n\t" \
  "buffer_load_dwordx4 v[72:75], v41, %[W], s61 offen offset:2304\n\t" \
  "buffer_load_dwordx4 v[76:79], v41, %[W], s61 offen offset:3456\n\t" \
  "s_waitcnt vmcnt(38)\n\t" \
  "v_perm_b32 v44, v84, v80, s62\n\t" \
  "s_waitcnt vmcnt(36)\n\t" \
  "v_perm_b32 v45, v92, v88, s62\n\t" \
  "s_waitcnt vmcnt(34)\n\t" \
  "v_perm_b32 v46, v100, v96, s62\n\t" \
  "s_waitcnt vmcnt(32)\n\t" \
  "v_perm_b32 v47, v108, v104, s62\n\t" \
  "v_accvgpr_write_b32 a32, v44\n\t" \
  "v_accvgpr_write_b32 a33, v45\n\t" \
  "v_accvgpr_write_b32 a34, v46\n\t" \
  "v_accvgpr_write_b32 a35, v47\n\t" \
  "v_perm_b32 v44, v84, v80, s63\n\t" \
  "v_perm_b32 v45, v92, v88, s63\n\t" \
  "v_perm_b32 v46, v100, v96, s63\n\t" \
  "v_perm_b32 v47, v108, v104, s63\n\t" \
  "v_accvgpr_write_b32 a36, v44\n\t" \
  "v_accvgpr_write_b32 a37, v45\n\t" \
  "v_accvgpr_write_b32 a38, v46\n\t" \
  "v_accvgpr_write_b32 a39, v47\n\t" \
  "v_perm_b32 v44, v85, v81, s62\n\t" \
  "v_perm_b32 v45, v93, v89, s62\n\t" \
  "v_perm_b32 v46, v101, v97, s62\n\t" \
  "v_perm_b32 v47, v109, v105, s62\n\t" \
  "v_accvgpr_write_b32 a40, v44\n\t" \
  "v_accvgpr_write_b32 a41, v45\n\t" \
  "v_accvgpr_write_b32 a42, v46\n\t" \
  "v_accvgpr_write_b32 a43, v47\n\t" \
  "v_perm_b32 v44, v85, v81, s63\n\t" \
  "v_perm_b32 v45, v93, v89, s63\n\t" \
  "v_perm_b32 v46, v101, v97, s63\n\t" \
  "v_perm_b32 v47, v109, v105, s63\n\t" \
  "v_accvgpr_write_b32 a44, v44\n\t" \
  "v_accvgpr_write_b32 a45, v45\n\t" \
  "v_accvgpr_write_b32 a46, v46\n\t" \
  "v_accvgpr_write_b32 a47, v47\n\t" \
  "v_perm_b32 v44, v86, v82, s62\n\t" \
  "v_perm_b32 v45, v94, v90, s62\n\t" \
  "v_perm_b32 v46, v102, v98, s62\n\t" \
  "v_perm_b32 v47, v110, v106, s62\n\t" \
  "v_accvgpr_write_b32 a48, v44\n\t" \
  "v_accvgpr_write_b32 a49, v45\n\t" \
  "v_accvgpr_write_b32 a50, v46\n\t" \
  "v_accvgpr_write_b32 a51, v47\n\t" \
  "v_perm_b32 v44, v86, v82, s63\n\t" \
  "v_perm_b32 v45, v94, v90, s63\n\t" \
  "v_perm_b32 v46, v102, v98, s63\n\t" \
  "v_perm_b32 v47, v110, v106, s63\n\t" \
  "v_accvgpr_write_b32 a52, v44\n\t" \
  "v_accvgpr_write_b32 a53, v45\n\t" \
  "v_accvgpr_write_b32 a54, v46\n\t" \
  "v_accvgpr_write_b32 a55, v47\n\t" \
  "v_perm_b32 v44, v87, v83, s62\n\t" \
  "v_perm_b32 v45, v95, v91, s62\n\t" \
  "v_perm_b32 v46, v103, v99, s62\n\t" \
  "v_perm_b32 v47, v111, v107, s62\n\t" \
  "v_accvgpr_write_b32 a56, v44\n\t" \
  "v_accvgpr_write_b32 a57, v45\n\t" \
  "v_accvgpr_write_b32 a58, v46\n\t" \
  "v_accvgpr_write_b32 a59, v47\n\t" \
  "v_perm_b32 v44, v87, v83, s63\n\t" \
  "v_perm_b32 v45, v95, v91, s63\n\t" \
  "v_perm_b32 v46, v103, v99, s63\n\t" \
  "v_perm_b32 v47, v111, v107, s63\n\t" \
  "v_accvgpr_write_b32 a60, v44\n\t" \
  "v_accvgpr_write_b32 a61, v45\n\t" \
  "v_accvgpr_write_b32 a62, v46\n\t" \
  "v_accvgpr_write_b32 a63, v47\n\t" \
  "s_xor_b32 s60, s66, 147456\n\t" \
  "s_add_u32 s60, s60, 73728\n\t" \
  "s_add_u32 s61, s60, 18432\n\t" \
  "buffer_load_dwordx4 v[80:83], v41, %[W], s60 offen offset:0\n\t" \
  "buffer_load_dwordx4 v[84:87], v41, %[W], s60 offen offset:1152\n\t" \
  "buffer_load_dwordx4 v[88:91], v41, %[W], s60 offen offset:2304\n\t" \
  "buffer_load_dwordx4 v[92:95], v41, %[W], s60 offen offset:3456\n\t" \
  "buffer_load_dwordx4 v[96:99], v41, %[W], s61 offen offset:0\n\t" \
  "buffer_load_dwordx4 v[100:103], v41, %[W], s61 offen offset:1152\n\t" \
  "buffer_load_dwordx4 v[104:107], v41, %[W], s61 offen offset:2304\n\t" \
  "buffer_load_dwordx4 v[108:111], v41, %[W], s61 offen offset:3456\n\t" \
  "s_waitcnt vmcnt(38)\n\t" \
  "v_perm_b32 v44, v116, v112, s62\n\t" \
  "s_waitcnt vmcnt(36)\n\t" \
  "v_perm_b32 v45, v124, v120, s62\n\t" \
  "s_waitcnt vmcnt(34)\n\t" \
  "v_perm_b32 v46, v132, v128, s62\n\t" \
  "s_waitcnt vmcnt(32)\n\t" \
  "v_perm_b32 v47, v140, v136, s62\n\t" \
  "v_accvgpr_write_b32 a64, v44\n\t" \
  "v_accvgpr_write_b32 a65, v45\n\t" \
  "v_accvgpr_write_b32 a66, v46\n\t" \
  "v_accvgpr_write_b32 a67, v47\n\t" \
  "v_perm_b32 v44, v116, v112, s63\n\t" \
  "v_perm_b32 v45, v124, v120, s63\n\t" \
  "v_perm_b32 v46, v132, v128, s63\n\t" \
  "v_perm_b32 v47, v140, v136, s63\n\t" \
  "v_accvgpr_write_b32 a68, v44\n\t" \
  "v_accvgpr_write_b32 a69, v45\n\t" \
  "v_accvgpr_write_b32 a70, v46\n\t" \
  "v_accvgpr_write_b32 a71, v47\n\t" \
  "v_perm_b32 v44, v117, v113, s62\n\t" \
  "v_perm_b32 v45, v125, v121, s62\n\t" \
  "v_perm_b32 v46, v133, v129, s62\n\t" \
  "v_perm_b32 v47, v141, v137, s62\n\t" \
  "v_accvgpr_write_b32 a72, v44\n\t" \
  "v_accvgpr_write_b32 a73, v45\n\t" \
  "v_accvgpr_write_b32 a74, v46\n\t" \
  "v_accvgpr_write_b32 a75, v47\n\t" \
  "v_perm_b32 v44, v117, v113, s63\n\t" \
  "v_perm_b32 v45, v125, v121, s63\n\t" \
  "v_perm_b32 v46, v133, v129, s63\n\t" \
  "v_perm_b32 v47, v141, v137, s63\n\t" \
  "v_accvgpr_write_b32 a76, v44\n\t" \
  "v_accvgpr_write_b32 a77, v45\n\t" \
  "v_accvgpr_write_b32 a78, v46\n\t" \
  "v_accvgpr_write_b32 a79, v47\n\t" \
  "v_perm_b32 v44, v118, v114, s62\n\t" \
  "v_perm_b32 v45, v126, v122, s62\n\t" \
  "v_perm_b32 v46, v134, v130, s62\n\t" \
  "v_perm_b32 v47, v142, v138, s62\n\t" \
  "v_accvgpr_write_b32 a80, v44\n\t" \
  "v_accvgpr_write_b32 a81, v45\n\t" \
  "v_accvgpr_write_b32 a82, v46\n\t" \
  "v_accvgpr_write_b32 a83, v47\n\t" \
  "v_perm_b32 v44, v118, v114, s63\n\t" \
  "v_perm_b32 v45, v126, v122, s63\n\t" \
  "v_perm_b32 v46, v134, v130, s63\n\t" \
  "v_perm_b32 v47, v142, v138, s63\n\t" \
  "v_accvgpr_write_b32 a84, v44\n\t" \
  "v_accvgpr_write_b32 a85, v45\n\t" \
  "v_accvgpr_write_b32 a86, v46\n\t" \
  "v_accvgpr_write_b32 a87, v47\n\t" \
  "v_perm_b32 v44, v119, v115, s62\n\t" \
  "v_perm_b32 v45, v127, v123, s62\n\t" \
  "v_perm_b32 v46, v135, v131, s62\n\t" \
  "v_perm_b32 v47, v143, v139, s62\n\t" \
  "v_accvgpr_write_b32 a88, v44\n\t" \
  "v_accvgpr_write_b32 a89, v45\n\t" \
  "v_accvgpr_write_b32 a90, v46\n\t" \
  "v_accvgpr_write_b32 a91, v47\n\t" \
  "v_perm_b32 v44, v119, v115, s63\n\t" \
  "v_perm_b32 v45, v127, v123, s63\n\t" \
  "v_perm_b32 v46, v135, v131, s63\n\t" \
  "v_perm_b32 v47, v143, v139, s63\n\t" \
  "v_accvgpr_write_b32 a92, v44\n\t" \
  "v_accvgpr_write_b32 a93, v45\n\t" \
  "v_accvgpr_write_b32 a94, v46\n\t" \
  "v_accvgpr_write_b32 a95, v47\n\t" \
  "s_xor_b32 s60, s66, 147456\n\t" \
  "s_add_u32 s60, s60, 110592\n\t" \
  "s_add_u32 s61, s60, 18432\n\t" \
  "buffer_load_dwordx4 v[112:115], v41, %[W], s60 offen offset:0\n\t" \
  "buffer_load_dwordx4 v[116:119], v41, %[W], s60 offen offset:1152\n\t" \
  "buffer_load_dwordx4 v[120:123], v41, %[W], s60 offen offset:2304\n\t" \
  "buffer_load_dwordx4 v[124:127], v41, %[W], s60 offen offset:3456\n\t" \
  "buffer_load_dwordx4 v[128:131], v41, %[W], s61 offen offset:0\n\t" \
  "buffer_load_dwordx4 v[132:135], v41, %[W], s61 offen offset:1152\n\t" \
  "buffer_load_dwordx4 v[136:139], v41, %[W], s61 offen offset:2304\n\t" \
  "buffer_load_dwordx4 v[140:143], v41, %[W], s61 offen offset:3456\n\t" \
  "s_waitcnt vmcnt(38)\n\t" \
  "v_perm_b32 v44, v148, v144, s62\n\t" \
  "s_waitcnt vmcnt(36)\n\t" \
  "v_perm_b32 v45, v156, v152, s62\n\t" \
  "s_waitcnt vmcnt(34)\n\t" \
  "v_perm_b32 v46, v164, v160, s62\n\t" \
  "s_waitcnt vmcnt(32)\n\t" \
  "v_perm_b32 v47, v172, v168, s62\n\t" \
  "v_accvgpr_write_b32 a96, v44\n\t" \
  "v_accvgpr_write_b32 a97, v45\n\t" \
  "v_accvgpr_write_b32 a98, v46\n\t" \
  "v_accvgpr_write_b32 a99, v47\n\t" \
  "v_perm_b32 v44, v148, v144, s63\n\t" \
  "v_perm_b32 v45, v156, v152, s63\n\t" \
  "v_perm_b32 v46, v164, v160, s63\n\t" \
  "v_perm_b32 v47, v172, v168, s63\n\t" \
  "v_accvgpr_write_b32 a100, v44\n\t" \
  "v_accvgpr_write_b32 a101, v45\n\t" \
  "v_accvgpr_write_b32 a102, v46\n\t" \
  "v_accvgpr_write_b32 a103, v47\n\t" \
  "v_perm_b32 v44, v149, v145, s62\n\t" \
  "v_perm_b32 v45, v157, v153, s62\n\t" \
  "v_perm_b32 v46, v165, v161, s62\n\t" \
  "v_perm_b32 v47, v173, v169, s62\n\t" \
  "v_accvgpr_write_b32 a104, v44\n\t" \
  "v_accvgpr_write_b32 a105, v45\n\t" \
  "v_accvgpr_write_b32 a106, v46\n\t" \
  "v_accvgpr_write_b32 a107, v47\n\t" \
  "v_perm_b32 v44, v149, v145, s63\n\t" \
  "v_perm_b32 v45, v157, v153, s63\n\t" \
  "v_perm_b32 v46, v165, v161, s63\n\t" \
  "v_perm_b32 v47, v173, v169, s63\n\t" \
  "v_accvgpr_write_b32 a108, v44\n\t" \
  "v_accvgpr_write_b32 a109, v45\n\t" \
  "v_accvgpr_write_b32 a110, v46\n\t" \
  "v_accvgpr_write_b32 a111, v47\n\t" \
  "v_perm_b32 v44, v150, v146, s62\n\t" \
  "v_perm_b32 v45, v158, v154, s62\n\t" \
  "v_perm_b32 v46, v166, v162, s62\n\t" \
  "v_perm_b32 v47, v174, v170, s62\n\t" \
  "v_accvgpr_write_b32 a112, v44\n\t" \
  "v_accvgpr_write_b32 a113, v45\n\t" \
  "v_accvgpr_write_b32 a114, v46\n\t" \
  "v_accvgpr_write_b32 a115, v47\n\t" \
  "v_perm_b32 v44, v150, v146, s63\n\t" \
  "v_perm_b32 v45, v158, v154, s63\n\t" \
  "v_perm_b32 v46, v166, v162, s63\n\t" \
  "v_perm_b32 v47, v174, v170, s63\n\t" \
  "v_accvgpr_write_b32 a116, v44\n\t" \
  "v_accvgpr_write_b32 a117, v45\n\t" \
  "v_accvgpr_write_b32 a118, v46\n\t" \
  "v_accvgpr_write_b32 a119, v47\n\t" \
  "v_perm_b32 v44, v151, v147, s62\n\t" \
  "v_perm_b32 v45, v159, v155, s62\n\t" \
  "v_perm_b32 v46, v167, v163, s62\n\t" \
  "v_perm_b32 v47, v175, v171, s62\n\t" \
  "v_accvgpr_write_b32 a120, v44\n\t" \
  "v_accvgpr_write_b32 a121, v45\n\t" \
  "v_accvgpr_write_b32 a122, v46\n\t" \
  "v_accvgpr_write_b32 a123, v47\n\t" \
  "v_perm_b32 v44, v151, v147, s63\n\t" \
  "v_perm_b32 v45, v159, v155, s63\n\t" \
  "v_perm_b32 v46, v167, v163, s63\n\t" \
  "v_perm_b32 v47, v175, v171, s63\n\t" \
  "v_accvgpr_write_b32 a124, v44\n\t" \
  "v_accvgpr_write_b32 a125, v45\n\t" \
  "v_accvgpr_write_b32 a126, v46\n\t" \
  "v_accvgpr_write_b32 a127, v47\n\t" \
  "s_xor_b32 s60, s66, 0\n\t" \
  "s_add_u32 s60, s60, 0\n\t" \
  "s_add_u32 s61, s60, 18432\n\t" \
  "buffer_load_ushort v224, v42, %[W], s60 offen offset:0\n\t" \
  "buffer_load_ushort v144, v42, %[W], s60 offen offset:1152\n\t" \
  "buffer_load_ushort v225, v42, %[W], s60 offen offset:2304\n\t" \
  "buffer_load_ushort v145, v42, %[W], s60 offen offset:3456\n\t" \
  "buffer_load_ushort v226, v42, %[W], s61 offen offset:0\n\t" \
  "buffer_load_ushort v146, v42, %[W], s61 offen offset:1152\n\t" \
  "buffer_load_ushort v227, v42, %[W], s61 offen offset:2304\n\t" \
  "buffer_load_ushort v147, v42, %[W], s61 offen offset:3456\n\t" \
  "s_xor_b32 s60, s66, 0\n\t" \
  "s_add_u32 s60, s60, 36864\n\t" \
  "s_add_u32 s61, s60, 18432\n\t" \
  "buffer_load_ushort v228, v42, %[W], s60 offen offset:0\n\t" \
  "buffer_load_ushort v148, v42, %[W], s60 offen offset:1152\n\t" \
  "buffer_load_ushort v229, v42, %[W], s60 offen offset:2304\n\t" \
  "buffer_load_ushort v149, v42, %[W], s60 offen offset:3456\n\t" \
  "buffer_load_ushort v230, v42, %[W], s61 offen offset:0\n\t" \
  "buffer_load_ushort v150, v42, %[W], s61 offen offset:1152\n\t" \
  "buffer_load_ushort v231, v42, %[W], s61 offen offset:2304\n\t" \
  "buffer_load_ushort v151, v42, %[W], s61 offen offset:3456\n\t" \
  "s_waitcnt vmcnt(46)\n\t" \
  "v_perm_b32 v44, v180, v176, s62\n\t" \
  "s_waitcnt vmcnt(44)\n\t" \
  "v_perm_b32 v45, v188, v184, s62\n\t" \
  "s_waitcnt vmcnt(42)\n\t" \
  "v_perm_b32 v46, v196, v192, s62\n\t" \
  "s_waitcnt vmcnt(40)\n\t" \
  "v_perm_b32 v47, v204, v200, s62\n\t" \
  "v_accvgpr_write_b32 a128, v44\n\t" \
  "v_accvgpr_write_b32 a129, v45\n\t" \
  "v_accvgpr_write_b32 a130, v46\n\t" \
  "v_accvgpr_write_b32 a131, v47\n\t" \
  "v_perm_b32 v44, v180, v176, s63\n\t" \
  "v_perm_b32 v45, v188, v184, s63\n\t" \
  "v_perm_b32 v46, v196, v192, s63\n\t" \
  "v_perm_b32 v47, v204, v200, s63\n\t" \
  "v_accvgpr_write_b32 a132, v44\n\t" \
  "v_accvgpr_write_b32 a133, v45\n\t" \
  "v_accvgpr_write_b32 a134, v46\n\t" \
  "v_accvgpr_write_b32 a135, v47\n\t" \
  "v_perm_b32 v44, v181, v177, s62\n\t" \
  "v_perm_b32 v45, v189, v185, s62\n\t" \
  "v_perm_b32 v46, v197, v193, s62\n\t" \
  "v_perm_b32 v47, v205, v201, s62\n\t" \
  "v_accvgpr_write_b32 a136, v44\n\t" \
  "v_accvgpr_write_b32 a137, v45\n\t" \
  "v_accvgpr_write_b32 a138, v46\n\t" \
  "v_accvgpr_write_b32 a139, v47\n\t" \
  "v_perm_b32 v44, v181, v177, s63\n\t" \
  "v_perm_b32 v45, v189, v185, s63\n\t" \
  "v_perm_b32 v46, v197, v193, s63\n\t" \
  "v_perm_b32 v47, v205, v201, s63\n\t" \
  "v_accvgpr_write_b32 a140, v44\n\t" \
  "v_accvgpr_write_b32 a141, v45\n\t" \
  "v_accvgpr_write_b32 a142, v46\n\t" \
  "v_accvgpr_write_b32 a143, v47\n\t" \
  "v_perm_b32 v44, v182, v178, s62\n\t" \
  "v_perm_b32 v45, v190, v186, s62\n\t" \
  "v_perm_b32 v46, v198, v194, s62\n\t" \
  "v_perm_b32 v47, v206, v202, s62\n\t" \
  "v_accvgpr_write_b32 a144, v44\n\t" \
  "v_accvgpr_write_b32 a145, v45\n\t" \
  "v_accvgpr_write_b32 a146, v46\n\t" \
  "v_accvgpr_write_b32 a147, v47\n\t" \
  "v_perm_b32 v44, v182, v178, s63\n\t" \
  "v_perm_b32 v45, v190, v186, s63\n\t" \
  "v_perm_b32 v46, v198, v194, s63\n\t" \
  "v_perm_b32 v47, v206, v202, s63\n\t" \
  "v_accvgpr_write_b32 a148, v44\n\t" \
  "v_accvgpr_write_b32 a149, v45\n\t" \
  "v_accvgpr_write_b32 a150, v46\n\t" \
  "v_accvgpr_write_b32 a151, v47\n\t" \
  "v_perm_b32 v44, v183, v179, s62\n\t" \
  "v_perm_b32 v45, v191, v187, s62\n\t" \
  "v_perm_b32 v46, v199, v195, s62\n\t" \
  "v_perm_b32 v47, v207, v203, s62\n\t" \
  "v_accvgpr_write_b32 a152, v44\n\t" \
  "v_accvgpr_write_b32 a153, v45\n\t" \
  "v_accvgpr_write_b32 a154, v46\n\t" \
  "v_accvgpr_write_b32 a155, v47\n\t" \
  "v_perm_b32 v44, v183, v179, s63\n\t" \
  "v_perm_b32 v45, v191, v187, s63\n\t" \
  "v_perm_b32 v46, v199, v195, s63\n\t" \
  "v_perm_b32 v47, v207, v203, s63\n\t" \
  "v_accvgpr_write_b32 a156, v44\n\t" \
  "v_accvgpr_write_b32 a157, v45\n\t" \
  "v_accvgpr_write_b32 a158, v46\n\t" \
  "v_accvgpr_write_b32 a159, v47\n\t" \
  "s_xor_b32 s60, s66, 0\n\t" \
  "s_add_u32 s60, s60, 73728\n\t" \
  "s_add_u32 s61, s60, 18432\n\t" \
  "buffer_load_ushort v232, v42, %[W], s60 offen offset:0\n\t" \
  "buffer_load_ushort v152, v42, %[W], s60 offen offset:1152\n\t" \
  "buffer_load_ushort v233, v42, %[W], s60 offen offset:2304\n\t" \
  "buffer_load_ushort v153, v42, %[W], s60 offen offset:3456\n\t" \
  "buffer_load_ushort v234, v42, %[W], s61 offen offset:0\n\t" \
  "buffer_load_ushort v154, v42, %[W], s61 offen offset:1152\n\t" \
  "buffer_load_ushort v235, v42, %[W], s61 offen offset:2304\n\t" \
  "buffer_load_ushort v155, v42, %[W], s61 offen offset:3456\n\t" \
  "s_xor_b32 s60, s66, 0\n\t" \
  "s_add_u32 s60, s60, 110592\n\t" \
  "s_add_u32 s61, s60, 18432\n\t" \
  "buffer_load_ushort v236, v42, %[W], s60 offen offset:0\n\t" \
  "buffer_load_ushort v156, v42, %[W], s60 offen offset:1152\n\t" \
  "buffer_load_ushort v237, v42, %[W], s60 offen offset:2304\n\t" \
  "buffer_load_ushort v157, v42, %[W], s60 offen offset:3456\n\t" \
  "buffer_load_ushort v238, v42, %[W], s61 offen offset:0\n\t" \
  "buffer_load_ushort v158, v42, %[W], s61 offen offset:1152\n\t" \
  "buffer_load_ushort v239, v42, %[W], s61 offen offset:2304\n\t" \
  "buffer_load_ushort v159, v42, %[W], s61 offen offset:3456\n\t" \
  "s_waitcnt vmcnt(54)\n\t" \
  "v_perm_b32 v44, v52, v48, s62\n\t" \
  "s_waitcnt vmcnt(52)\n\t" \
  "v_perm_b32 v45, v60, v56, s62\n\t" \
  "s_waitcnt vmcnt(50)\n\t" \
  "v_perm_b32 v46, v68, v64, s62\n\t" \
  "s_waitcnt vmcnt(48)\n\t" \
  "v_perm_b32 v47, v76, v72, s62\n\t" \
  "v_accvgpr_write_b32 a160, v44\n\t" \
  "v_accvgpr_write_b32 a161, v45\n\t" \
  "v_accvgpr_write_b32 a162, v46\n\t" \
  "v_accvgpr_write_b32 a163, v47\n\t" \
  "v_perm_b32 v44, v52, v48, s63\n\t" \
  "v_perm_b32 v45, v60, v56, s63\n\t" \
  "v_perm_b32 v46, v68, v64, s63\n\t" \
  "v_perm_b32 v47, v76, v72, s63\n\t" \
  "v_accvgpr_write_b32 a164, v44\n\t" \
  "v_accvgpr_write_b32 a165, v45\n\t" \
  "v_accvgpr_write_b32 a166, v46\n\t" \
  "v_accvgpr_write_b32 a167, v47\n\t" \
  "v_perm_b32 v44, v53, v49, s62\n\t" \
  "v_perm_b32 v45, v61, v57, s62\n\t" \
  "v_perm_b32 v46, v69, v65, s62\n\t" \
  "v_perm_b32 v47, v77, v73, s62\n\t" \
  "v_accvgpr_write_b32 a168, v44\n\t" \
  "v_accvgpr_write_b32 a169, v45\n\t" \
  "v_accvgpr_write_b32 a170, v46\n\t" \
  "v_accvgpr_write_b32 a171, v47\n\t" \
  "v_perm_b32 v44, v53, v49, s63\n\t" \
  "v_perm_b32 v45, v61, v57, s63\n\t" \
  "v_perm_b32 v46, v69, v65, s63\n\t" \
  "v_perm_b32 v47, v77, v73, s63\n\t" \
  "v_accvgpr_write_b32 a172, v44\n\t" \
  "v_accvgpr_write_b32 a173, v45\n\t" \
  "v_accvgpr_write_b32 a174, v46\n\t" \
  "v_accvgpr_write_b32 a175, v47\n\t" \
  "v_perm_b32 v44, v54, v50, s62\n\t" \
  "v_perm_b32 v45, v62, v58, s62\n\t" \
  "v_perm_b32 v46, v70, v66, s62\n\t" \
  "v_perm_b32 v47, v78, v74, s62\n\t" \
  "v_accvgpr_write_b32 a176, v44\n\t" \
  "v_accvgpr_write_b32 a177, v45\n\t" \
  "v_accvgpr_write_b32 a178, v46\n\t" \
  "v_accvgpr_write_b32 a179, v47\n\t" \
  "v_perm_b32 v44, v54, v50, s63\n\t" \
  "v_perm_b32 v45, v62, v58, s63\n\t" \
  "v_perm_b32 v46, v70, v66, s63\n\t" \
  "v_perm_b32 v47, v78, v74, s63\n\t" \
  "v_accvgpr_write_b32 a180, v44\n\t" \
  "v_accvgpr_write_b32 a181, v45\n\t" \
  "v_accvgpr_write_b32 a182, v46\n\t" \
  "v_accvgpr_write_b32 a183, v47\n\t" \
  "v_perm_b32 v44, v55, v51, s62\n\t" \
  "v_perm_b32 v45, v63, v59, s62\n\t" \
  "v_perm_b32 v46, v71, v67, s62\n\t" \
  "v_perm_b32 v47, v79, v75, s62\n\t" \
  "v_accvgpr_write_b32 a184, v44\n\t" \
  "v_accvgpr_write_b32 a185, v45\n\t" \
  "v_accvgpr_write_b32 a186, v46\n\t" \
  "v_accvgpr_write_b32 a187, v47\n\t" \
  "v_perm_b32 v44, v55, v51, s63\n\t" \
  "v_perm_b32 v45, v63, v59, s63\n\t" \
  "v_perm_b32 v46, v71, v67, s63\n\t" \
  "v_perm_b32 v47, v79, v75, s63\n\t" \
  "v_accvgpr_write_b32 a188, v44\n\t" \
  "v_accvgpr_write_b32 a189, v45\n\t" \
  "v_accvgpr_write_b32 a190, v46\n\t" \
  "v_accvgpr_write_b32 a191, v47\n\t" \
  "s_xor_b32 s60, s66, 147456\n\t" \
  "s_add_u32 s60, s60, 0\n\t" \
  "s_add_u32 s61, s60, 18432\n\t" \
  "buffer_load_ushort v240, v42, %[W], s60 offen offset:0\n\t" \
  "buffer_load_ushort v160, v42, %[W], s60 offen offset:1152\n\t" \
  "buffer_load_ushort v241, v42, %[W], s60 offen offset:2304\n\t" \
  "buffer_load_ushort v161, v42, %[W], s60 offen offset:3456\n\t" \
  "buffer_load_ushort v242, v42, %[W], s61 offen offset:0\n\t" \
  "buffer_load_ushort v162, v42, %[W], s61 offen offset:1152\n\t" \
  "buffer_load_ushort v243, v42, %[W], s61 offen offset:2304\n\t" \
  "buffer_load_ushort v163, v42, %[W], s61 offen offset:3456\n\t" \
  "s_waitcnt vmcnt(54)\n\t" \
  "v_perm_b32 v44, v84, v80, s62\n\t" \
  "s_waitcnt vmcnt(52)\n\t" \
  "v_perm_b32 v45, v92, v88, s62\n\t" \
  "s_waitcnt vmcnt(50)\n\t" \
  "v_perm_b32 v46, v100, v96, s62\n\t" \
  "s_waitcnt vmcnt(48)\n\t" \
  "v_perm_b32 v47, v108, v104, s62\n\t" \
  "v_accvgpr_write_b32 a192, v44\n\t" \
  "v_accvgpr_write_b32 a193, v45\n\t" \
  "v_accvgpr_write_b32 a194, v46\n\t" \
  "v_accvgpr_write_b32 a195, v47\n\t" \
  "v_perm_b32 v44, v84, v80, s63\n\t" \
  "v_perm_b32 v45, v92, v88, s63\n\t" \
  "v_perm_b32 v46, v100, v96, s63\n\t" \
  "v_perm_b32 v47, v108, v104, s63\n\t" \
  "v_accvgpr_write_b32 a196, v44\n\t" \
  "v_accvgpr_write_b32 a197, v45\n\t" \
  "v_accvgpr_write_b32 a198, v46\n\t" \
  "v_accvgpr_write_b32 a199, v47\n\t" \
  "v_perm_b32 v44, v85, v81, s62\n\t" \
  "v_perm_b32 v45, v93, v89, s62\n\t" \
  "v_perm_b32 v46, v101, v97, s62\n\t" \
  "v_perm_b32 v47, v109, v105, s62\n\t" \
  "v_accvgpr_write_b32 a200, v44\n\t" \
  "v_accvgpr_write_b32 a201, v45\n\t" \
  "v_accvgpr_write_b32 a202, v46\n\t" \
  "v_accvgpr_write_b32 a203, v47\n\t" \
  "v_perm_b32 v44, v85, v81, s63\n\t" \
  "v_perm_b32 v45, v93, v89, s63\n\t" \
  "v_perm_b32 v46, v101, v97, s63\n\t" \
  "v_perm_b32 v47, v109, v105, s63\n\t" \
  "v_accvgpr_write_b32 a204, v44\n\t" \
  "v_accvgpr_write_b32 a205, v45\n\t" \
  "v_accvgpr_write_b32 a206, v46\n\t" \
  "v_accvgpr_write_b32 a207, v47\n\t" \
  "v_perm_b32 v44, v86, v82, s62\n\t" \
  "v_perm_b32 v45, v94, v90, s62\n\t" \
  "v_perm_b32 v46, v102, v98, s62\n\t" \
  "v_perm_b32 v47, v110, v106, s62\n\t" \
  "v_accvgpr_write_b32 a208, v44\n\t" \
  "v_accvgpr_write_b32 a209, v45\n\t" \
  "v_accvgpr_write_b32 a210, v46\n\t" \
  "v_accvgpr_write_b32 a211, v47\n\t" \
  "v_perm_b32 v44, v86, v82, s63\n\t" \
  "v_perm_b32 v45, v94, v90, s63\n\t" \
  "v_perm_b32 v46, v102, v98, s63\n\t" \
  "v_perm_b32 v47, v110, v106, s63\n\t" \
  "v_accvgpr_write_b32 a212, v44\n\t" \
  "v_accvgpr_write_b32 a213, v45\n\t" \
  "v_accvgpr_write_b32 a214, v46\n\t" \
  "v_accvgpr_write_b32 a215, v47\n\t" \
  "v_perm_b32 v44, v87, v83, s62\n\t" \
  "v_perm_b32 v45, v95, v91, s62\n\t" \
  "v_perm_b32 v46, v103, v99, s62\n\t" \
  "v_perm_b32 v47, v111, v107, s62\n\t" \
  "v_accvgpr_write_b32 a216, v44\n\t" \
  "v_accvgpr_write_b32 a217, v45\n\t" \
  "v_accvgpr_write_b32 a218, v46\n\t" \
  "v_accvgpr_write_b32 a219, v47\n\t" \
  "v_perm_b32 v44, v87, v83, s63\n\t" \
  "v_perm_b32 v45, v95, v91, s63\n\t" \
  "v_perm_b32 v46, v103, v99, s63\n\t" \
  "v_perm_b32 v47, v111, v107, s63\n\t" \
  "v_accvgpr_write_b32 a220, v44\n\t" \
  "v_accvgpr_write_b32 a221, v45\n\t" \
  "v_accvgpr_write_b32 a222, v46\n\t" \
  "v_accvgpr_write_b32 a223, v47\n\t" \
  "s_xor_b32 s60, s66, 147456\n\t" \
  "s_add_u32 s60, s60, 36864\n\t" \
  "s_add_u32 s61, s60, 18432\n\t" \
  "buffer_load_ushort v244, v42, %[W], s60 offen offset:0\n\t" \
  "buffer_load_ushort v164, v42, %[W], s60 offen offset:1152\n\t" \
  "buffer_load_ushort v245, v42, %[W], s60 offen offset:2304\n\t" \
  "buffer_load_ushort v165, v42, %[W], s60 offen offset:3456\n\t" \
  "buffer_load_ushort v246, v42, %[W], s61 offen offset:0\n\t" \
  "buffer_load_ushort v166, v42, %[W], s61 offen offset:1152\n\t" \
  "buffer_load_ushort v247, v42, %[W], s61 offen offset:2304\n\t" \
  "buffer_load_ushort v167, v42, %[W], s61 offen offset:3456\n\t" \
  "s_waitcnt vmcnt(54)\n\t" \
  "v_perm_b32 v44, v116, v112, s62\n\t" \
  "s_waitcnt vmcnt(52)\n\t" \
  "v_perm_b32 v45, v124, v120, s62\n\t" \
  "s_waitcnt vmcnt(50)\n\t" \
  "v_perm_b32 v46, v132, v128, s62\n\t" \
  "s_waitcnt vmcnt(48)\n\t" \
  "v_perm_b32 v47, v140, v136, s62\n\t" \
  "v_accvgpr_write_b32 a224, v44\n\t" \
  "v_accvgpr_write_b32 a225, v45\n\t" \
  "v_accvgpr_write_b32 a226, v46\n\t" \
  "v_accvgpr_write_b32 a227, v47\n\t" \
  "v_perm_b32 v44, v116, v112, s63\n\t" \
  "v_perm_b32 v45, v124, v120, s63\n\t" \
  "v_perm_b32 v46, v132, v128, s63\n\t" \
  "v_perm_b32 v47, v140, v136, s63\n\t" \
  "v_accvgpr_write_b32 a228, v44\n\t" \
  "v_accvgpr_write_b32 a229, v45\n\t" \
  "v_accvgpr_write_b32 a230, v46\n\t" \
  "v_accvgpr_write_b32 a231, v47\n\t" \
  "v_perm_b32 v44, v117, v113, s62\n\t" \
  "v_perm_b32 v45, v125, v121, s62\n\t" \
  "v_perm_b32 v46, v133, v129, s62\n\t" \
  "v_perm_b32 v47, v141, v137, s62\n\t" \
  "v_accvgpr_write_b32 a232, v44\n\t" \
  "v_accvgpr_write_b32 a233, v45\n\t" \
  "v_accvgpr_write_b32 a234, v46\n\t" \
  "v_accvgpr_write_b32 a235, v47\n\t" \
  "v_perm_b32 v44, v117, v113, s63\n\t" \
  "v_perm_b32 v45, v125, v121, s63\n\t" \
  "v_perm_b32 v46, v133, v129, s63\n\t" \
  "v_perm_b32 v47, v141, v137, s63\n\t" \
  "v_accvgpr_write_b32 a236, v44\n\t" \
  "v_accvgpr_write_b32 a237, v45\n\t" \
  "v_accvgpr_write_b32 a238, v46\n\t" \
  "v_accvgpr_write_b32 a239, v47\n\t" \
  "v_perm_b32 v44, v118, v114, s62\n\t" \
  "v_perm_b32 v45, v126, v122, s62\n\t" \
  "v_perm_b32 v46, v134, v130, s62\n\t" \
  "v_perm_b32 v47, v142, v138, s62\n\t" \
  "v_accvgpr_write_b32 a240, v44\n\t" \
  "v_accvgpr_write_b32 a241, v45\n\t" \
  "v_accvgpr_write_b32 a242, v46\n\t" \
  "v_accvgpr_write_b32 a243, v47\n\t" \
  "v_perm_b32 v44, v118, v114, s63\n\t" \
  "v_perm_b32 v45, v126, v122, s63\n\t" \
  "v_perm_b32 v46, v134, v130, s63\n\t" \
  "v_perm_b32 v47, v142, v138, s63\n\t" \
  "v_accvgpr_write_b32 a244, v44\n\t" \
  "v_accvgpr_write_b32 a245, v45\n\t" \
  "v_accvgpr_write_b32 a246, v46\n\t" \
  "v_accvgpr_write_b32 a247, v47\n\t" \
  "v_perm_b32 v44, v119, v115, s62\n\t" \
  "v_perm_b32 v45, v127, v123, s62\n\t" \
  "v_perm_b32 v46, v135, v131, s62\n\t" \
  "v_perm_b32 v47, v143, v139, s62\n\t" \
  "v_accvgpr_write_b32 a248, v44\n\t" \
  "v_accvgpr_write_b32 a249, v45\n\t" \
  "v_accvgpr_write_b32 a250, v46\n\t" \
  "v_accvgpr_write_b32 a251, v47\n\t" \
  "v_perm_b32 v44, v119, v115, s63\n\t" \
  "v_perm_b32 v45, v127, v123, s63\n\t" \
  "v_perm_b32 v46, v135, v131, s63\n\t" \
  "v_perm_b32 v47, v143, v139, s63\n\t" \
  "v_accvgpr_write_b32 a252, v44\n\t" \
  "v_accvgpr_write_b32 a253, v45\n\t" \
  "v_accvgpr_write_b32 a254, v46\n\t" \
  "v_accvgpr_write_b32 a255, v47\n\t" \
  "s_xor_b32 s60, s66, 147456\n\t" \
  "s_add_u32 s60, s60, 73728\n\t" \
  "s_add_u32 s61, s60, 18432\n\t" \
  "buffer_load_ushort v248, v42, %[W], s60 offen offset:0\n\t" \
  "buffer_load_ushort v168, v42, %[W], s60 offen offset:1152\n\t" \
  "buffer_load_ushort v249, v42, %[W], s60 offen offset:2304\n\t" \
  "buffer_load_ushort v169, v42, %[W], s60 offen offset:3456\n\t" \
  "buffer_load_ushort v250, v42, %[W], s61 offen offset:0\n\t" \
  "buffer_load_ushort v170, v42, %[W], s61 offen offset:1152\n\t" \
  "buffer_load_ushort v251, v42, %[W], s61 offen offset:2304\n\t" \
  "buffer_load_ushort v171, v42, %[W], s61 offen offset:3456\n\t" \
  "s_xor_b32 s60, s66, 147456\n\t" \
  "s_add_u32 s60, s60, 110592\n\t" \
  "s_add_u32 s61, s60, 18432\n\t" \
  "s_waitcnt vmcnt(40)\n\t" \
  "buffer_load_ushort v252, v42, %[W], s60 offen offset:0\n\t" \
  "buffer_load_ushort v172, v42, %[W], s60 offen offset:1152\n\t" \
  "buffer_load_ushort v253, v42, %[W], s60 offen offset:2304\n\t" \
  "buffer_load_ushort v173, v42, %[W], s60 offen offset:3456\n\t" \
  "buffer_load_ushort v254, v42, %[W], s61 offen offset:0\n\t" \
  "buffer_load_ushort v174, v42, %[W], s61 offen offset:1152\n\t" \
  "buffer_load_ushort v255, v42, %[W], s61 offen offset:2304\n\t" \
  "buffer_load_ushort v175, v42, %[W], s61 offen offset:3456\n\t" \
  "v_lshl_or_b32 v224, v144, 16, v224\n\t" \
  "v_lshl_or_b32 v225, v145, 16, v225\n\t" \
  "v_lshl_or_b32 v226, v146, 16, v226\n\t" \
  "v_lshl_or_b32 v227, v147, 16, v227\n\t" \
  "v_lshl_or_b32 v228, v148, 16, v228\n\t" \
  "v_lshl_or_b32 v229, v149, 16, v229\n\t" \
  "v_lshl_or_b32 v230, v150, 16, v230\n\t" \
  "v_lshl_or_b32 v231, v151, 16, v231\n\t" \
  "s_waitcnt vmcnt(46)\n\t" \
  "v_lshl_or_b32 v232, v152, 16, v232\n\t" \
  "s_waitcnt vmcnt(44)\n\t" \
  "v_lshl_or_b32 v233, v153, 16, v233\n\t" \
  "s_waitcnt vmcnt(42)\n\t" \
  "v_lshl_or_b32 v234, v154, 16, v234\n\t" \
  "s_waitcnt vmcnt(40)\n\t" \
  "v_lshl_or_b32 v235, v155, 16, v235\n\t" \
  "s_waitcnt vmcnt(38)\n\t" \
  "v_lshl_or_b32 v236, v156, 16, v236\n\t" \
  "s_waitcnt vmcnt(36)\n\t" \
  "v_lshl_or_b32 v237, v157, 16, v237\n\t" \
  "s_waitcnt vmcnt(34)\n\t" \
  "v_lshl_or_b32 v238, v158, 16, v238\n\t" \
  "s_waitcnt vmcnt(32)\n\t" \
  "v_lshl_or_b32 v239, v159, 16, v239\n\t" \
  "s_waitcnt vmcnt(30)\n\t" \
  "v_lshl_or_b32 v240, v160, 16, v240\n\t" \
  "s_waitcnt vmcnt(28)\n\t" \
  "v_lshl_or_b32 v241, v161, 16, v241\n\t" \
  "s_waitcnt vmcnt(26)\n\t" \
  "v_lshl_or_b32 v242, v162, 16, v242\n\t" \
  "s_waitcnt vmcnt(24)\n\t" \
  "v_lshl_or_b32 v243, v163, 16, v243\n\t" \
  "s_waitcnt vmcnt(22)\n\t" \
  "v_lshl_or_b32 v244, v164, 16, v244\n\t" \
  "s_waitcnt vmcnt(20)\n\t" \
  "v_lshl_or_b32 v245, v165, 16, v245\n\t" \
  "s_waitcnt vmcnt(18)\n\t" \
  "v_lshl_or_b32 v246, v166, 16, v246\n\t" \
  "s_waitcnt vmcnt(16)\n\t" \
  "v_lshl_or_b32 v247, v167, 16, v247\n\t" \
  "s_waitcnt vmcnt(14)\n\t" \
  "v_lshl_or_b32 v248, v168, 16, v248\n\t" \
  "s_waitcnt vmcnt(12)\n\t" \
  "v_lshl_or_b32 v249, v169, 16, v249\n\t" \
  "s_waitcnt vmcnt(10)\n\t" \
  "v_lshl_or_b32 v250, v170, 16, v250\n\t" \
  "s_waitcnt vmcnt(8)\n\t" \
  "v_lshl_or_b32 v251, v171, 16, v251\n\t" \
  "s_waitcnt vmcnt(6)\n\t" \
  "v_lshl_or_b32 v252, v172, 16, v252\n\t" \
  "s_waitcnt vmcnt(4)\n\t" \
  "v_lshl_or_b32 v253, v173, 16, v253\n\t" \
  "s_waitcnt vmcnt(2)\n\t" \
  "v_lshl_or_b32 v254, v174, 16, v254\n\t" \
  "s_waitcnt vmcnt(0)\n\t" \
  "v_lshl_or_b32 v255, v175, 16, v255\n\t" \
  "s_mov_b32 s84, " TK_CAP "\n\t" \
  "s_mov_b32 s48, 0\n\t" \
  "s_mov_b32 s49, 0xf0000000\n\t" \
  "s_mov_b32 s46, s45\n\t" \
  "v_mov_b32 v5, -1\n\t" \
  "v_lshrrev_b32 v13, 2, v0\n\t" \
  "s_mov_b32 s60, " TK_CTAB "\n\t" \
  "buffer_load_dword v10, v13, %[X], s60 offen offset:88 sc1\n\t" \
  "buffer_load_dword v13, v13, %[X], s60 offen offset:0 sc1\n\t" \
  "s_waitcnt vmcnt(0)\n\t" \
  "v_and_b32 v10, 1, v10\n\t" \
  "v_and_b32 v13, 1, v13\n\t" \
  "v_cmp_ne_u32 s[90:91], s67, v10\n\t" \
  "v_cmp_ne_u32 s[88:89], s67, v13\n\t" \
  "s_nop 1\n\t" \
  "s_mov_b32 s86, s88\n\t" \
  "v_lshrrev_b32 v13, 2, v0\n\t" \
  "s_mov_b32 s60, " TK_CTAB "\n\t" \
  "buffer_load_dword v10, v13, %[X], s60 offen offset:88 sc1\n\t" \
  "buffer_load_dword v13, v13, %[X], s60 offen offset:344 sc1\n\t" \
  "s_waitcnt vmcnt(0)\n\t" \
  "v_and_b32 v10, 1, v10\n\t" \
  "v_and_b32 v13, 1, v13\n\t" \
  "v_cmp_ne_u32 s[90:91], s67, v10\n\t" \
  "v_cmp_ne_u32 s[88:89], s67, v13\n\t" \
  "s_nop 1\n\t" \
  "s_and_b32 s60, s88, 256\n\t" \
  "s_lshl_b32 s60, s60, 5\n\t" \
  "s_add_u32 s44, s44, s60\n\t" \
  "s_add_u32 s83, s48, 0\n\t" \
  "s_lshr_b64 s[60:61], s[90:91], s83\n\t" \
  "s_and_b32 s60, s60, 1\n\t" \
  "s_lshl_b32 s60, s60, 13\n\t" \
  "s_add_u32 s83, s83, " TK_SG0 "\n\t" \
  "s_lshl_b32 s83, s83, 21\n\t" \
  "s_or_b32 s83, s83, s60\n\t" \
  "LCOLD_%=:\n\t" \
  "s_add_u32 s62, s83, s41\n\t" \
  "s_mov_b32 s61, 0\n\t" \
  "LPQ_%=:\n\t" \
  "buffer_load_dword v10, v9, %[X], s62 offen sc1\n\t" \
  "s_waitcnt vmcnt(0)\n\t" \
  "v_cmp_eq_u32 vcc, 0, v10\n\t" \
  "s_cbranch_vccz LPQ_%=x\n\t" \
  "s_add_u32 s61, s61, 1\n\t" \
  "s_sleep 4\n\t" \
  "s_cmp_gt_u32 s61, s84\n\t" \
  "s_cbranch_scc1 LPQ_%=g\n\t" \
  "s_and_b32 s60, s61, 1023\n\t" \
  "s_cmp_lg_u32 s60, 0\n\t" \
  "s_cbranch_scc1 LPQ_%=\n\t" \
  "buffer_load_dword v10, off, %[X], s92 sc1\n\t" \
  "s_waitcnt vmcnt(0)\n\t" \
  "v_readfirstlane_b32 s60, v10\n\t" \
  "s_nop 0\n\t" \
  "s_cmp_eq_u32 s60, 0\n\t" \
  "s_cbranch_scc1 LPQ_%=\n\t" \
  "LPQ_%=g:\n\t" \
  "s_mov_b32 s84, 0\n\t" \
  "s_or_b32 s60, s48, 1073741824\n\t" \
  "v_mov_b32 v10, s60\n\t" \
  "s_mov_b64 exec, 1\n\t" \
  "buffer_store_dword v10, off, %[X], s92 sc1\n\t" \
  "s_mov_b64 exec, -1\n\t" \
  "LPQ_%=x:\n\t" \
  "s_add_u32 m0, s46, 0\n\t" \
  "s_add_u32 s47, s83, s36\n\t" \
  "buffer_load_dwordx4 v0, %[X], s47 offen offset:0 sc1 lds\n\t" \
  "buffer_load_dwordx4 v0, %[X], s47 offen offset:1024 sc1 lds\n\t" \
  "buffer_load_dwordx4 v0, %[X], s47 offen offset:2048 sc1 lds\n\t" \
  "buffer_load_dwordx4 v0, %[X], s47 offen offset:3072 sc1 lds\n\t" \
  "s_add_u32 m0, s46, 4096\n\t" \
  "s_add_u32 s47, s83, s37\n\t" \
  "buffer_load_dwordx4 v0, %[X], s47 offen offset:0 sc1 lds\n\t" \
  "buffer_load_dwordx4 v0, %[X], s47 offen offset:1024 sc1 lds\n\t" \
  "buffer_load_dwordx4 v0, %[X], s47 offen offset:2048 sc1 lds\n\t" \
  "buffer_load_dwordx4 v0, %[X], s47 offen offset:3072 sc1 lds\n\t" \
  "s_add_u32 m0, s46, 8192\n\t" \
  "s_add_u32 s47, s83, s38\n\t" \
  "buffer_load_dwordx4 v0, %[X], s47 offen offset:0 sc1 lds\n\t" \
  "buffer_load_dwordx4 v0, %[X], s47 offen offset:1024 sc1 lds\n\t" \
  "buffer_load_dwordx4 v0, %[X], s47 offen offset:2048 sc1 lds\n\t" \
  "buffer_load_dwordx4 v0, %[X], s47 offen offset:3072 sc1 lds\n\t" \
  "s_waitcnt vmcnt(0)\n\t" \
  "LTOP_%=:\n\t" \
  "s_memrealtime s[78:79]\n\t" \
  "v_readfirstlane_b32 s76, v5\n\t" \
  "s_waitcnt lgkmcnt(0)\n\t" \
  "s_add_u32 s60, s76, 1\n\t" \
  "s_not_b32 s76, s76\n\t" \
  "s_cmp_gt_u32 s60, 1\n\t" \
  "s_cselect_b32 s61, -1, 0\n\t" \
  "s_lshl_b32 s60, s78, 8\n\t" \
  "s_sub_u32 s76, s76, s60\n\t" \
  "s_ashr_i32 s76, s76, 8\n\t" \
  "s_add_i32 s76, s76, s77\n\t" \
  "s_min_i32 s76, s76, 400\n\t" \
  "s_and_b32 s76, s76, s61\n\t" \
  "s_add_u32 s57, s78, s76\n\t" \
  TK_AC0 \
  "s_barrier\n\t" \
  TK_AC1 \
  "ds_read_b128 v[48:51], v2 offset:0\n\t" \
  "ds_read_b128 v[64:67], v1 offset:8192\n\t" \
  "ds_read_b128 v[80:83], v1 offset:12288\n\t" \
  "ds_read_b128 v[52:55], v2 offset:1024\n\t" \
  "ds_read_b128 v[68:71], v1 offset:9216\n\t" \
  "ds_read_b128 v[84:87], v1 offset:13312\n\t" \
  "ds_read_b128 v[56:59], v2 offset:2048\n\t" \
  "ds_read_b128 v[72:75], v1 offset:10240\n\t" \
  "ds_read_b128 v[88:91], v1 offset:14336\n\t" \
  "ds_read_b128 v[60:63], v2 offset:3072\n\t" \
  "ds_read_b128 v[76:79], v1 offset:11264\n\t" \
  "ds_read_b128 v[92:95], v1 offset:15360\n\t" \
  "s_mov_b32 s82, s83\n\t" \
  "s_xor_b32 s46, s46, 0x10000\n\t" \
  "s_add_u32 s58, s82, s41\n\t" \
  "s_add_u32 s58, s58, 4096\n\t" \
  "s_add_u32 s59, s82, s39\n\t" \
  "s_cmp_gt_i32 s76, 0\n\t" \
  "s_cselect_b32 s62, 0xf0000000, s58\n\t" \
  "buffer_load_dword v5, off, %[X], s62 sc1\n\t" \
  "s_cmp_gt_i32 s76, " TK_AM "\n\t" \
  "s_cselect_b32 s79, 0xf0000000, s58\n\t" \
  "s_waitcnt lgkmcnt(10)\n\t" \
  "v_mfma_f32_16x16x32_bf16 v[36:39], v[64:67], v[48:51], v[16:19]\n\t" \
  "ds_read_b128 v[96:99], v1 offset:16384\n\t" \
  "s_waitcnt lgkmcnt(10)\n\t" \
  "v_mfma_f32_16x16x32_bf16 v[40:43], v[80:83], v[48:51], v[20:23]\n\t" \
  "ds_read_b128 v[100:103], v1 offset:17408\n\t" \
  "s_waitcnt lgkmcnt(9)\n\t" \
  "v_mfma_f32_16x16x32_bf16 v[36:39], v[68:71], v[52:55], v[36:39]\n\t" \
  "ds_read_b128 v[104:107], v1 offset:18432\n\t" \
  "s_waitcnt lgkmcnt(9)\n\t" \
  "v_mfma_f32_16x16x32_bf16 v[40:43], v[84:87], v[52:55], v[40:43]\n\t" \
  "ds_read_b128 v[108:111], v1 offset:19456\n\t" \
  "s_waitcnt lgkmcnt(8)\n\t" \
  "v_mfma_f32_16x16x32_bf16 v[36:39], v[72:75], v[56:59], v[36:39]\n\t" \
  "ds_read_b128 v[112:115], v1 offset:20480\n\t" \
  "s_waitcnt lgkmcnt(8)\n\t" \
  "v_mfma_f32_16x16x32_bf16 v[40:43], v[88:91], v[56:59], v[40:43]\n\t" \
  "ds_read_b128 v[116:119], v1 offset:21504\n\t" \
  "s_waitcnt lgkmcnt(7)\n\t" \
  "v_mfma_f32_16x16x32_bf16 v[36:39], v[76:79], v[60:63], v[36:39]\n\t" \
  "ds_read_b128 v[120:123], v1 offset:22528\n\t" \
  "s_waitcnt lgkmcnt(7)\n\t" \
  "v_mfma_f32_16x16x32_bf16 v[40:43], v[92:95], v[60:63], v[40:43]\n\t" \
  "ds_read_b128 v[124:127], v1 offset:23552\n\t" \
  "s_add_u32 s83, s48, 1\n\t" \
  "s_lshr_b64 s[60:61], s[90:91], s83\n\t" \
  "s_and_b32 s60, s60, 1\n\t" \
  "s_lshl_b32 s60, s60, 13\n\t" \
  "s_add_u32 s83, s83, " TK_SG0 "\n\t" \
  "s_lshl_b32 s83, s83, 21\n\t" \
  "s_or_b32 s83, s83, s60\n\t" \
  "v_xor_b32 v1, 0x10000, v1\n\t" \
  "v_xor_b32 v2, 0x10000, v2\n\t" \
  "v_max3_f32 v11, v36, v37, v38\n\t" \
  "v_max3_f32 v11, v11, v39, v40\n\t" \
  "v_max3_f32 v13, v41, v42, v43\n\t" \
  "v_max_f32 v11, v11, v13\n\t" \
  "v_mov_b32 v13, v11\n\t" \
  "s_nop 1\n\t" \
  "v_permlane16_swap_b32 v11, v13\n\t" \
  "v_max_f32 v11, v11, v13\n\t" \
  "v_mov_b32 v13, v11\n\t" \
  "s_nop 1\n\t" \
  "v_permlane32_swap_b32 v11, v13\n\t" \
  "v_max_f32 v11, v11, v13\n\t" \
  "v_mul_f32 v13, 0xbe0293ee, v11\n\t" \
  "v_fmamk_f32 v36, v36, 0x3e0293ee, v13\n\t" \
  "v_fmamk_f32 v37, v37, 0x3e0293ee, v13\n\t" \
  "v_fmamk_f32 v38, v38, 0x3e0293ee, v13\n\t" \
  "v_fmamk_f32 v39, v39, 0x3e0293ee, v13\n\t" \
  "v_fmamk_f32 v40, v40, 0x3e0293ee, v13\n\t" \
  "v_fmamk_f32 v41, v41, 0x3e0293ee, v13\n\t" \
  "v_fmamk_f32 v42, v42, 0x3e0293ee, v13\n\t" \
  "v_fmamk_f32 v43, v43, 0x3e0293ee, v13\n\t" \
  "s_waitcnt vmcnt(0)\n\t" \
  "v_readfirstlane_b32 s76, v5\n\t" \
  "s_barrier\n\t" \
  "s_cmp_eq_u32 s76, 0\n\t" \
  "s_cbranch_scc1 LNG_%=\n\t" \
  "buffer_load_dwordx4 v[196:199], v0, %[X], s59 offen offset:0 sc1\n\t" \
  "buffer_load_dwordx4 v[200:203], v0, %[X], s59 offen offset:1024 sc1\n\t" \
  "buffer_load_dwordx2 v[204:205], v6, %[X], s59 offen offset:2048 sc1\n\t" \
  "buffer_load_dwordx2 v[206:207], v6, %[X], s59 offen offset:2560 sc1\n\t" \
  "buffer_load_dwordx2 v[208:209], v6, %[X], s59 offen offset:3072 sc1\n\t" \
  "buffer_load_dwordx2 v[210:211], v6, %[X], s59 offen offset:3584 sc1\n\t" \
  "LNG_%=:\n\t" \
  "buffer_load_dword v5, off, %[X], s79 sc1\n\t" \
  "s_mov_b64 exec, 1\n\t" \
  "buffer_store_dword v3, off, %[X], s49 sc1\n\t" \
  "s_mov_b64 exec, -1\n\t" \
  "s_mov_b32 s49, 0xf0000000\n\t" \
  "v_exp_f32 v36, v36\n\t" \
  "v_exp_f32 v37, v37\n\t" \
  "v_exp_f32 v38, v38\n\t" \
  "v_exp_f32 v39, v39\n\t" \
  "v_exp_f32 v40, v40\n\t" \
  "v_exp_f32 v41, v41\n\t" \
  "v_exp_f32 v42, v42\n\t" \
  "v_exp_f32 v43, v43\n\t" \
  "v_cvt_pk_bf16_f32 v44, v36, v37\n\t" \
  "v_cvt_pk_bf16_f32 v45, v38, v39\n\t" \
  "v_cvt_pk_bf16_f32 v46, v40, v41\n\t" \
  "v_cvt_pk_bf16_f32 v47, v42, v43\n\t" \
  "v_add_f32 v36, v36, v37\n\t" \
  "v_add_f32 v38, v38, v39\n\t" \
  "s_waitcnt lgkmcnt(7)\n\t" \
  "v_mfma_f32_16x16x32_bf16 v[128:131], v[96:99], v[44:47], 0\n\t" \
  "v_add_f32 v40, v40, v41\n\t" \
  "s_and_b32 s62, s48, 15\n\t" \
  "s_lshr_b32 s63, s62, 1\n\t" \
  "s_waitcnt lgkmcnt(6)\n\t" \
  "v_mfma_f32_16x16x32_bf16 v[132:135], v[100:103], v[44:47], 0\n\t" \
  "v_add_f32 v42, v42, v43\n\t" \
  "s_lshr_b32 s60, s88, s63\n\t" \
  "s_and_b32 s60, s60, 1\n\t" \
  "s_waitcnt lgkmcnt(5)\n\t" \
  "v_mfma_f32_16x16x32_bf16 v[136:139], v[104:107], v[44:47], 0\n\t" \
  "v_add_f32 v36, v36, v38\n\t" \
  "s_lshl_b32 s60, s60, 13\n\t" \
  "s_lshl_b32 s61, s62, 20\n\t" \
  "s_waitcnt lgkmcnt(4)\n\t" \
  "v_mfma_f32_16x16x32_bf16 v[140:143], v[108:111], v[44:47], 0\n\t" \
  "v_add_f32 v40, v40, v42\n\t" \
  "s_add_u32 s50, s61, s60\n\t" \
  "s_add_u32 s50, s50, s43\n\t" \
  "s_waitcnt lgkmcnt(3)\n\t" \
  "v_mfma_f32_16x16x32_bf16 v[144:147], v[112:115], v[44:47], 0\n\t" \
  "v_add_f32 v12, v36, v40\n\t" \
  "s_add_u32 s51, s50, 32768\n\t" \
  "s_add_u32 s52, s51, 32768\n\t" \
  "s_waitcnt lgkmcnt(2)\n\t" \
  "v_mfma_f32_16x16x32_bf16 v[148:151], v[116:119], v[44:47], 0\n\t" \
  "s_add_u32 s53, s52, 32768\n\t" \
  "s_lshl_b32 s61, s62, 17\n\t" \
  "s_waitcnt lgkmcnt(1)\n\t" \
  "v_mfma_f32_16x16x32_bf16 v[152:155], v[120:123], v[44:47], 0\n\t" \
  "s_add_u32 s54, s61, s44\n\t" \
  "s_add_u32 s63, s83, s41\n\t" \
  "s_waitcnt lgkmcnt(0)\n\t" \
  "v_mfma_f32_16x16x32_bf16 v[156:159], v[124:127], v[44:47], 0\n\t" \
  TK_AC2 \
  TK_AC3 \
  "s_cmp_lg_u32 s76, 0\n\t" \
  "s_cbranch_scc1 LGOT_%=\n\t" \
  "s_waitcnt vmcnt(1)\n\t" \
  "v_readfirstlane_b32 s60, v5\n\t" \
  "s_nop 0\n\t" \
  "s_cmp_lg_u32 s60, 0\n\t" \
  "s_cbranch_scc1 LPK_%=x\n\t" \
  "LKW_%=:\n\t" \
  "s_memrealtime s[78:79]\n\t" \
  "s_waitcnt lgkmcnt(0)\n\t" \
  "s_sub_i32 s79, s78, s57\n\t" \
  "s_cmp_lt_i32 s79, 0\n\t" \
  "s_cbranch_scc1 LKW_%=\n\t" \
  "s_mov_b32 s61, 0\n\t" \
  "LPK_%=:\n\t" \
  "buffer_load_dword v5, off, %[X], s58 sc1\n\t" \
  "s_waitcnt vmcnt(0)\n\t" \
  "v_cmp_eq_u32 vcc, 0, v5\n\t" \
  "s_cbranch_vccz LPK_%=x\n\t" \
  "s_add_u32 s61, s61, 1\n\t" \
  "s_sleep " TK_PSK "\n\t" \
  "s_cmp_gt_u32 s61, s84\n\t" \
  "s_cbranch_scc1 LPK_%=g\n\t" \
  "s_and_b32 s60, s61, 1023\n\t" \
  "s_cmp_lg_u32 s60, 0\n\t" \
  "s_cbranch_scc1 LPK_%=\n\t" \
  "buffer_load_dword v5, off, %[X], s92 sc1\n\t" \
  "s_waitcnt vmcnt(0)\n\t" \
  "v_readfirstlane_b32 s60, v5\n\t" \
  "s_nop 0\n\t" \
  "s_cmp_eq_u32 s60, 0\n\t" \
  "s_cbranch_scc1 LPK_%=\n\t" \
  "LPK_%=g:\n\t" \
  "s_mov_b32 s84, 0\n\t" \
  "s_or_b32 s60, s48, 1342177280\n\t" \
  "v_mov_b32 v5, s60\n\t" \
  "s_mov_b64 exec, 1\n\t" \
  "buffer_store_dword v5, off, %[X], s92 sc1\n\t" \
  "s_mov_b64 exec, -1\n\t" \
  "LPK_%=x:\n\t" \
  "buffer_load_dwordx4 v[196:199], v0, %[X], s59 offen offset:0 sc1\n\t" \
  "buffer_load_dwordx4 v[200:203], v0, %[X], s59 offen offset:1024 sc1\n\t" \
  "buffer_load_dwordx2 v[204:205], v6, %[X], s59 offen offset:2048 sc1\n\t" \
  "buffer_load_dwordx2 v[206:207], v6, %[X], s59 offen offset:2560 sc1\n\t" \
  "buffer_load_dwordx2 v[208:209], v6, %[X], s59 offen offset:3072 sc1\n\t" \
  "buffer_load_dwordx2 v[210:211], v6, %[X], s59 offen offset:3584 sc1\n\t" \
  "LGOT_%=:\n\t" \
  TK_PA0 \
  TK_PA1 \
  TK_AC4 \
  "buffer_load_dword v10, v9, %[X], s63 offen sc1\n\t" \
  "s_add_u32 s49, s82, s42\n\t" \
  "s_waitcnt vmcnt(6)\n\t" \
  "v_mfma_f32_16x16x32_bf16 v[36:39], v[196:199], v[48:51], 0\n\t" \
  "v_mfma_f32_16x16x32_bf16 v[40:43], v[196:199], v[52:55], 0\n\t" \
  "s_waitcnt vmcnt(5)\n\t" \
  "v_mfma_f32_16x16x32_bf16 v[36:39], v[200:203], v[56:59], v[36:39]\n\t" \
  "v_mfma_f32_16x16x32_bf16 v[40:43], v[200:203], v[60:63], v[40:43]\n\t" \
  TK_AC5 \
  "s_nop 8\n\t" \
  "v_add_f32 v36, v36, v42\n\t" \
  "v_add_f32 v37, v37, v43\n\t" \
  "v_max3_f32 v14, v36, v37, v11\n\t" \
  "v_mov_b32 v13, v14\n\t" \
  "s_nop 1\n\t" \
  "v_permlane16_swap_b32 v14, v13\n\t" \
  "v_max_f32 v14, v14, v13\n\t" \
  "v_mov_b32 v13, v14\n\t" \
  "s_nop 1\n\t" \
  "v_permlane32_swap_b32 v14, v13\n\t" \
  "v_max_f32 v14, v14, v13\n\t" \
  "v_mul_f32 v14, 0xbe0293ee, v14\n\t" \
  "v_fmamk_f32 v11, v11, 0x3e0293ee, v14\n\t" \
  "v_fmamk_f32 v36, v36, 0x3e0293ee, v14\n\t" \
  "v_fmamk_f32 v37, v37, 0x3e0293ee, v14\n\t" \
  "v_exp_f32 v11, v11\n\t" \
  "v_exp_f32 v36, v36\n\t" \
  "v_exp_f32 v37, v37\n\t" \
  "v_fma_f32 v12, v12, v11, v36\n\t" \
  "v_add_f32 v12, v12, v37\n\t" \
  "v_mov_b32 v13, v12\n\t" \
  "s_nop 1\n\t" \
  "v_permlane16_swap_b32 v12, v13\n\t" \
  "v_add_f32 v12, v12, v13\n\t" \
  "v_mov_b32 v13, v12\n\t" \
  "s_nop 1\n\t" \
  "v_permlane32_swap_b32 v12, v13\n\t" \
  "v_add_f32 v12, v12, v13\n\t" \
  "v_rcp_f32 v12, v12\n\t" \
  "s_nop 0\n\t" \
  "v_mul_f32 v14, v11, v12\n\t" \
  "v_mul_f32 v36, v36, v12\n\t" \
  "v_mul_f32 v37, v37, v12\n\t" \
  "v_cvt_pk_bf16_f32 v26, v36, v37\n\t" \
  "v_cvt_pk_bf16_f32 v33, v36, v37\n\t" \
  "v_pk_mul_f32 v[128:129], v[128:129], v[14:15] op_sel_hi:[1,0]\n\t" \
  "v_pk_mul_f32 v[130:131], v[130:131], v[14:15] op_sel_hi:[1,0]\n\t" \
  "v_pk_mul_f32 v[132:133], v[132:133], v[14:15] op_sel_hi:[1,0]\n\t" \
  "v_pk_mul_f32 v[134:135], v[134:135], v[14:15] op_sel_hi:[1,0]\n\t" \
  "v_pk_mul_f32 v[136:137], v[136:137], v[14:15] op_sel_hi:[1,0]\n\t" \
  "v_pk_mul_f32 v[138:139], v[138:139], v[14:15] op_sel_hi:[1,0]\n\t" \
  "v_pk_mul_f32 v[140:141], v[140:141], v[14:15] op_sel_hi:[1,0]\n\t" \
  "v_pk_mul_f32 v[142:143], v[142:143], v[14:15] op_sel_hi:[1,0]\n\t" \
  "s_waitcnt vmcnt(3)\n\t" \
  "v_mfma_f32_16x16x32_bf16 v[128:131], v[204:207], v[26:29], v[128:131]\n\t" \
  "v_mul_f32 v144, v144, v14\n\t" \
  "v_mul_f32 v145, v145, v14\n\t" \
  "v_mfma_f32_16x16x32_bf16 v[132:135], v[204:207], v[32:35], v[132:135]\n\t" \
  "v_mul_f32 v146, v146, v14\n\t" \
  "v_mul_f32 v147, v147, v14\n\t" \
  "v_mfma_f32_16x16x32_bf16 v[136:139], v[204:207], v[24:27], v[136:139]\n\t" \
  "v_mul_f32 v148, v148, v14\n\t" \
  "v_mul_f32 v149, v149, v14\n\t" \
  "v_mfma_f32_16x16x32_bf16 v[140:143], v[204:207], v[30:33], v[140:143]\n\t" \
  "v_cvt_pk_bf16_f32 v64, v128, v129\n\t" \
  "v_cvt_pk_bf16_f32 v65, v130, v131\n\t" \
  "v_cvt_pk_bf16_f32 v66, v132, v133\n\t" \
  "v_cvt_pk_bf16_f32 v67, v134, v135\n\t" \
  "v_mul_f32 v150, v150, v14\n\t" \
  "v_mul_f32 v151, v151, v14\n\t" \
  TK_AC6 \
  "v_mfma_f32_16x16x32_bf16 v[192:195], v[224:227], v[64:67], 0\n\t" \
  "ds_write_b128 v3, v[64:67] offset:0\n\t" \
  "v_cvt_pk_bf16_f32 v68, v136, v137\n\t" \
  "v_cvt_pk_bf16_f32 v69, v138, v139\n\t" \
  "v_mfma_f32_16x16x32_bf16 v[160:163], v[64:67], a[0:3], 0\n\t" \
  "v_cvt_pk_bf16_f32 v70, v140, v141\n\t" \
  "v_cvt_pk_bf16_f32 v71, v142, v143\n\t" \
  "s_waitcnt vmcnt(1)\n\t" \
  "v_mfma_f32_16x16x32_bf16 v[144:147], v[208:211], v[26:29], v[144:147]\n\t" \
  "v_mul_f32 v152, v152, v14\n\t" \
  "v_mul_f32 v153, v153, v14\n\t" \
  "v_mfma_f32_16x16x32_bf16 v[148:151], v[208:211], v[32:35], v[148:151]\n\t" \
  "v_mul_f32 v154, v154, v14\n\t" \
  "v_mul_f32 v155, v155, v14\n\t" \
  "v_mfma_f32_16x16x32_bf16 v[164:167], v[64:67], a[4:7], 0\n\t" \
  "ds_write_b128 v3, v[68:71] offset:1024\n\t" \
  "v_mul_f32 v156, v156, v14\n\t" \
  "v_mul_f32 v157, v157, v14\n\t" \
  "v_mfma_f32_16x16x32_bf16 v[168:171], v[64:67], a[8:11], 0\n\t" \
  "v_mul_f32 v158, v158, v14\n\t" \
  "v_mul_f32 v159, v159, v14\n\t" \
  "v_mfma_f32_16x16x32_bf16 v[172:175], v[64:67], a[12:15], 0\n\t" \
  "v_cvt_pk_bf16_f32 v72, v144, v145\n\t" \
  "v_cvt_pk_bf16_f32 v73, v146, v147\n\t" \
  "v_mfma_f32_16x16x32_bf16 v[152:155], v[208:211], v[24:27], v[152:155]\n\t" \
  "v_cvt_pk_bf16_f32 v74, v148, v149\n\t" \
  "v_cvt_pk_bf16_f32 v75, v150, v151\n\t" \
  "v_mfma_f32_16x16x32_bf16 v[156:159], v[208:211], v[30:33], v[156:159]\n\t" \
  "v_mfma_f32_16x16x32_bf16 v[176:179], v[64:67], a[16:19], 0\n\t" \
  "ds_write_b128 v3, v[72:75] offset:2048\n\t" \
  "v_mfma_f32_16x16x32_bf16 v[180:183], v[64:67], a[20:23], 0\n\t" \
  "v_mfma_f32_16x16x32_bf16 v[184:187], v[64:67], a[24:27], 0\n\t" \
  "v_cvt_pk_bf16_f32 v76, v152, v153\n\t" \
  "v_cvt_pk_bf16_f32 v77, v154, v155\n\t" \
  "v_mfma_f32_16x16x32_bf16 v[188:191], v[64:67], a[28:31], 0\n\t" \
  "v_cvt_pk_bf16_f32 v78, v156, v157\n\t" \
  "v_cvt_pk_bf16_f32 v79, v158, v159\n\t" \
  "v_mfma_f32_16x16x32_bf16 v[192:195], v[228:231], v[68:71], v[192:195]\n\t" \
  "ds_write_b128 v3, v[76:79] offset:3072\n\t" \
  "v_mfma_f32_16x16x32_bf16 v[160:163], v[68:71], a[32:35], v[160:163]\n\t" \
  "s_waitcnt vmcnt(0)\n\t" \
  "v_cmp_eq_u32 vcc, 0, v10\n\t" \
  "v_mfma_f32_16x16x32_bf16 v[164:167], v[68:71], a[36:39], v[164:167]\n\t" \
  "s_cmp_eq_u64 vcc, 0\n\t" \
  "s_cselect_b32 s55, s83, 0xf0000000\n\t" \
  "s_cmp_eq_u32 s48, 63\n\t" \
  "v_mfma_f32_16x16x32_bf16 v[168:171], v[68:71], a[40:43], v[168:171]\n\t" \
  "s_cselect_b32 s55, 0xf0000000, s55\n\t" \
  "s_mov_b32 s56, s83\n\t" \
  "s_mov_b32 s83, s55\n\t" \
  "v_mfma_f32_16x16x32_bf16 v[172:175], v[68:71], a[44:47], v[172:175]\n\t" \
  "v_mfma_f32_16x16x32_bf16 v[176:179], v[68:71], a[48:51], v[176:179]\n\t" \
  "s_add_u32 m0, s46, 0\n\t" \
  "s_add_u32 s47, s83, s36\n\t" \
  "buffer_load_dwordx4 v0, %[X], s47 offen offset:0 sc1 lds\n\t" \
  "v_mfma_f32_16x16x32_bf16 v[180:183], v[68:71], a[52:55], v[180:183]\n\t" \
  "v_mfma_f32_16x16x32_bf16 v[184:187], v[68:71], a[56:59], v[184:187]\n\t" \
  "s_waitcnt lgkmcnt(0)\n\t" \
  "s_barrier\n\t" \
  "v_mfma_f32_16x16x32_bf16 v[188:191], v[68:71], a[60:63], v[188:191]\n\t" \
  "ds_read_b128 v[80:83], v4 offset:0\n\t" \
  "v_mfma_f32_16x16x32_bf16 v[192:195], v[232:235], v[72:75], v[192:195]\n\t" \
  "buffer_load_dwordx4 v0, %[X], s47 offen offset:1024 sc1 lds\n\t" \
  "v_mfma_f32_16x16x32_bf16 v[160:163], v[72:75], a[64:67], v[160:163]\n\t" \
  "ds_read_b128 v[84:87], v4 offset:1024\n\t" \
  "v_mfma_f32_16x16x32_bf16 v[164:167], v[72:75], a[68:71], v[164:167]\n\t" \
  "v_mfma_f32_16x16x32_bf16 v[168:171], v[72:75], a[72:75], v[168:171]\n\t" \
  "ds_read_b128 v[88:91], v4 offset:2048\n\t" \
  "v_mfma_f32_16x16x32_bf16 v[172:175], v[72:75], a[76:79], v[172:175]\n\t" \
  "buffer_load_dwordx4 v0, %[X], s47 offen offset:2048 sc1 lds\n\t" \
  "v_mfma_f32_16x16x32_bf16 v[176:179], v[72:75], a[80:83], v[176:179]\n\t" \
  "ds_read_b128 v[92:95], v4 offset:3072\n\t" \
  "v_mfma_f32_16x16x32_bf16 v[180:183], v[72:75], a[84:87], v[180:183]\n\t" \
  "v_mfma_f32_16x16x32_bf16 v[184:187], v[72:75], a[88:91], v[184:187]\n\t" \
  "v_mfma_f32_16x16x32_bf16 v[188:191], v[72:75], a[92:95], v[188:191]\n\t" \
  "buffer_load_dwordx4 v0, %[X], s47 offen offset:3072 sc1 lds\n\t" \
  "v_mfma_f32_16x16x32_bf16 v[192:195], v[236:239], v[76:79], v[192:195]\n\t" \
  "s_add_u32 s62, s48, 2\n\t" \
  "v_mfma_f32_16x16x32_bf16 v[160:163], v[76:79], a[96:99], v[160:163]\n\t" \
  "v_mfma_f32_16x16x32_bf16 v[164:167], v[76:79], a[100:103], v[164:167]\n\t" \
  "s_mul_i32 s63, s62, 43\n\t" \
  "s_lshr_b32 s63, s63, 7\n\t" \
  "v_mfma_f32_16x16x32_bf16 v[168:171], v[76:79], a[104:107], v[168:171]\n\t" \
  "s_add_u32 m0, s46, 4096\n\t" \
  "s_add_u32 s47, s83, s37\n\t" \
  "buffer_load_dwordx4 v0, %[X], s47 offen offset:0 sc1 lds\n\t" \
  "v_mfma_f32_16x16x32_bf16 v[172:175], v[76:79], a[108:111], v[172:175]\n\t" \
  "s_mul_i32 s62, s62, 688128\n\t" \
  "s_lshl_b32 s60, s63, 15\n\t" \
  "v_mfma_f32_16x16x32_bf16 v[176:179], v[76:79], a[112:115], v[176:179]\n\t" \
  "v_mfma_f32_16x16x32_bf16 v[180:183], v[76:79], a[116:119], v[180:183]\n\t" \
  "s_add_u32 s62, s62, s60\n\t" \
  "s_lshr_b32 s60, s86, s63\n\t" \
  "v_mfma_f32_16x16x32_bf16 v[184:187], v[76:79], a[120:123], v[184:187]\n\t" \
  "buffer_load_dwordx4 v0, %[X], s47 offen offset:1024 sc1 lds\n\t" \
  "v_mfma_f32_16x16x32_bf16 v[188:191], v[76:79], a[124:127], v[188:191]\n\t" \
  "s_and_b32 s60, s60, 1\n\t" \
  "s_lshl_b32 s60, s60, 13\n\t" \
  "s_waitcnt lgkmcnt(3)\n\t" \
  "v_mfma_f32_16x16x32_bf16 v[192:195], v[240:243], v[80:83], v[192:195]\n\t" \
  "v_mfma_f32_16x16x32_bf16 v[160:163], v[80:83], a[128:131], v[160:163]\n\t" \
  "s_add_u32 s62, s62, s60\n\t" \
  "s_add_u32 s62, s62, 656896\n\t" \
  "v_mfma_f32_16x16x32_bf16 v[164:167], v[80:83], a[132:135], v[164:167]\n\t" \
  "buffer_load_dwordx4 v0, %[X], s47 offen offset:2048 sc1 lds\n\t" \
  "v_mfma_f32_16x16x32_bf16 v[168:171], v[80:83], a[136:139], v[168:171]\n\t" \
  "s_cmp_gt_u32 s48, 61\n\t" \
  "s_cselect_b32 s62, 0xf0000000, s62\n\t" \
  "v_mfma_f32_16x16x32_bf16 v[172:175], v[80:83], a[140:143], v[172:175]\n\t" \
  "v_mfma_f32_16x16x32_bf16 v[176:179], v[80:83], a[144:147], v[176:179]\n\t" \
  "v_mfma_f32_16x16x32_bf16 v[180:183], v[80:83], a[148:151], v[180:183]\n\t" \
  "buffer_load_dwordx4 v0, %[X], s47 offen offset:3072 sc1 lds\n\t" \
  "v_mfma_f32_16x16x32_bf16 v[184:187], v[80:83], a[152:155], v[184:187]\n\t" \
  "v_mfma_f32_16x16x32_bf16 v[188:191], v[80:83], a[156:159], v[188:191]\n\t" \
  "s_waitcnt lgkmcnt(2)\n\t" \
  "v_mfma_f32_16x16x32_bf16 v[192:195], v[244:247], v[84:87], v[192:195]\n\t" \
  "v_mfma_f32_16x16x32_bf16 v[160:163], v[84:87], a[160:163], v[160:163]\n\t" \
  "s_add_u32 m0, s46, 8192\n\t" \
  "s_add_u32 s47, s83, s38\n\t" \
  "buffer_load_dwordx4 v0, %[X], s47 offen offset:0 sc1 lds\n\t" \
  "v_mfma_f32_16x16x32_bf16 v[164:167], v[84:87], a[164:167], v[164:167]\n\t" \
  "v_mfma_f32_16x16x32_bf16 v[168:171], v[84:87], a[168:171], v[168:171]\n\t" \
  "v_mfma_f32_16x16x32_bf16 v[172:175], v[84:87], a[172:175], v[172:175]\n\t" \
  "v_mfma_f32_16x16x32_bf16 v[176:179], v[84:87], a[176:179], v[176:179]\n\t" \
  "buffer_load_dwordx4 v0, %[X], s47 offen offset:1024 sc1 lds\n\t" \
  "v_mfma_f32_16x16x32_bf16 v[180:183], v[84:87], a[180:183], v[180:183]\n\t" \
  "v_mfma_f32_16x16x32_bf16 v[184:187], v[84:87], a[184:187], v[184:187]\n\t" \
  "v_mfma_f32_16x16x32_bf16 v[188:191], v[84:87], a[188:191], v[188:191]\n\t" \
  "s_waitcnt lgkmcnt(1)\n\t" \
  "v_mfma_f32_16x16x32_bf16 v[192:195], v[248:251], v[88:91], v[192:195]\n\t" \
  "buffer_load_dwordx4 v0, %[X], s47 offen offset:2048 sc1 lds\n\t" \
  "v_mfma_f32_16x16x32_bf16 v[160:163], v[88:91], a[192:195], v[160:163]\n\t" \
  "v_mfma_f32_16x16x32_bf16 v[164:167], v[88:91], a[196:199], v[164:167]\n\t" \
  "v_mfma_f32_16x16x32_bf16 v[168:171], v[88:91], a[200:203], v[168:171]\n\t" \
  "v_mfma_f32_16x16x32_bf16 v[172:175], v[88:91], a[204:207], v[172:175]\n\t" \
  "buffer_load_dwordx4 v0, %[X], s47 offen offset:3072 sc1 lds\n\t" \
  "v_mfma_f32_16x16x32_bf16 v[176:179], v[88:91], a[208:211], v[176:179]\n\t" \
  "v_mfma_f32_16x16x32_bf16 v[180:183], v[88:91], a[212:215], v[180:183]\n\t" \
  "buffer_load_dword v5, off, %[X], s62 sc1\n\t" \
  "v_mfma_f32_16x16x32_bf16 v[184:187], v[88:91], a[216:219], v[184:187]\n\t" \
  "v_mfma_f32_16x16x32_bf16 v[188:191], v[88:91], a[220:223], v[188:191]\n\t" \
  "s_waitcnt lgkmcnt(0)\n\t" \
  "v_mfma_f32_16x16x32_bf16 v[192:195], v[252:255], v[92:95], v[192:195]\n\t" \
  "v_mfma_f32_16x16x32_bf16 v[160:163], v[92:95], a[224:227], v[160:163]\n\t" \
  "v_mfma_f32_16x16x32_bf16 v[164:167], v[92:95], a[228:231], v[164:167]\n\t" \
  "v_mfma_f32_16x16x32_bf16 v[168:171], v[92:95], a[232:235], v[168:171]\n\t" \
  "v_cvt_pk_bf16_f32 v112, v192, v193\n\t" \
  "v_cvt_pk_bf16_f32 v113, v194, v195\n\t" \
  "v_mfma_f32_16x16x32_bf16 v[172:175], v[92:95], a[236:239], v[172:175]\n\t" \
  "buffer_store_dwordx2 v[112:113], v8, %[X], s54 offen sc1\n\t" \
  "v_mfma_f32_16x16x32_bf16 v[176:179], v[92:95], a[240:243], v[176:179]\n\t" \
  "v_cvt_pk_bf16_f32 v96, v160, v164\n\t" \
  "v_cvt_pk_bf16_f32 v100, v161, v165\n\t" \
  "v_mfma_f32_16x16x32_bf16 v[180:183], v[92:95], a[244:247], v[180:183]\n\t" \
  "v_cvt_pk_bf16_f32 v104, v162, v166\n\t" \
  "v_cvt_pk_bf16_f32 v108, v163, v167\n\t" \
  "v_mfma_f32_16x16x32_bf16 v[184:187], v[92:95], a[248:251], v[184:187]\n\t" \
  "v_cvt_pk_bf16_f32 v97, v168, v172\n\t" \
  "v_cvt_pk_bf16_f32 v101, v169, v173\n\t" \
  "v_mfma_f32_16x16x32_bf16 v[188:191], v[92:95], a[252:255], v[188:191]\n\t" \
  "v_cvt_pk_bf16_f32 v105, v170, v174\n\t" \
  "v_cvt_pk_bf16_f32 v109, v171, v175\n\t" \
  "v_cvt_pk_bf16_f32 v98, v176, v180\n\t" \
  "v_cvt_pk_bf16_f32 v102, v177, v181\n\t" \
  "v_cvt_pk_bf16_f32 v106, v178, v182\n\t" \
  "v_cvt_pk_bf16_f32 v110, v179, v183\n\t" \
  TK_AC7 \
  "s_nop 1\n\t" \
  "v_cvt_pk_bf16_f32 v99, v184, v188\n\t" \
  "buffer_store_dwordx4 v[96:99], v7, %[X], s50 offen sc1\n\t" \
  "v_cvt_pk_bf16_f32 v103, v185, v189\n\t" \
  "buffer_store_dwordx4 v[100:103], v7, %[X], s51 offen sc1\n\t" \
  "v_cvt_pk_bf16_f32 v107, v186, v190\n\t" \
  "buffer_store_dwordx4 v[104:107], v7, %[X], s52 offen sc1\n\t" \
  "v_cvt_pk_bf16_f32 v111, v187, v191\n\t" \
  "buffer_store_dwordx4 v[108:111], v7, %[X], s53 offen sc1\n\t" \
  TK_PA2 \
  TK_AC8 \
  "s_cmp_eq_u32 s48, 63\n\t" \
  "s_cbranch_scc1 LEND_%=\n\t" \
  "s_add_u32 s48, s48, 1\n\t" \
  "s_cmp_eq_u32 s83, s56\n\t" \
  "s_mov_b32 s83, s56\n\t" \
  "s_cbranch_scc0 LCOLD_%=\n\t" \
  "s_waitcnt vmcnt(5)\n\t" \
  "s_branch LTOP_%=\n\t" \
  "LEND_%=:\n\t" \
  "s_waitcnt vmcnt(0)\n\t" \
  "s_barrier\n\t" \
  "s_mov_b64 exec, 1\n\t" \
  "buffer_store_dword v3, off, %[X], s49 sc1\n\t" \
  "s_mov_b64 exec, -1\n\t" \
  "s_mov_b32 s49, 0xf0000000\n\t" \
  TK_PA0 \
  TK_ACE \
  ""
#define QK_ASM \
  "v_lshrrev_b32 v16, 7, v0\n\t" \
  "v_lshlrev_b32 v16, 13, v16\n\t" \
  "v_lshrrev_b32 v19, 2, v0\n\t" \
  "v_and_b32 v19, 64, v19\n\t" \
  "v_xor_b32 v19, v19, v0\n\t" \
  "v_and_b32 v19, 0x70, v19\n\t" \
  "v_add_u32 v16, v16, v19\n\t" \
  "s_mul_i32 s63, s40, 40960\n\t" \
  "v_and_b32 v19, 0x300, v0\n\t" \
  "v_lshlrev_b32 v17, 2, v19\n\t" \
  "v_and_b32 v19, 0xc0, v0\n\t" \
  "v_lshl_add_u32 v17, v19, 1, v17\n\t" \
  "v_and_b32 v19, 0x30, v0\n\t" \
  "v_add3_u32 v17, v17, v19, s63\n\t" \
  "v_lshrrev_b32 v19, 1, v0\n\t" \
  "v_and_b32 v19, 64, v19\n\t" \
  "v_add_u32 v17, v17, v19\n\t" \
  "v_xor_b32 v18, 64, v17\n\t" \
  "s_add_u32 m0, s63, 0\n\t" \
  "s_mov_b32 s60, 0\n\t" \
  "buffer_load_dwordx4 v16, %[W], s60 offen lds\n\t" \
  "s_add_u32 m0, s63, 1024\n\t" \
  "s_mov_b32 s60, 65536\n\t" \
  "buffer_load_dwordx4 v16, %[W], s60 offen lds\n\t" \
  "s_add_u32 m0, s63, 2048\n\t" \
  "s_mov_b32 s60, 131072\n\t" \
  "buffer_load_dwordx4 v16, %[W], s60 offen lds\n\t" \
  "s_add_u32 m0, s63, 3072\n\t" \
  "s_mov_b32 s60, 196608\n\t" \
  "buffer_load_dwordx4 v16, %[W], s60 offen lds\n\t" \
  "s_add_u32 m0, s63, 4096\n\t" \
  "s_mov_b32 s60, 262144\n\t" \
  "buffer_load_dwordx4 v16, %[W], s60 offen lds\n\t" \
  "s_add_u32 m0, s63, 5120\n\t" \
  "s_mov_b32 s60, 327680\n\t" \
  "buffer_load_dwordx4 v16, %[W], s60 offen lds\n\t" \
  "s_add_u32 m0, s63, 6144\n\t" \
  "s_mov_b32 s60, 393216\n\t" \
  "buffer_load_dwordx4 v16, %[W], s60 offen lds\n\t" \
  "s_add_u32 m0, s63, 7168\n\t" \
  "s_mov_b32 s60, 458752\n\t" \
  "buffer_load_dwordx4 v16, %[W], s60 offen lds\n\t" \
  "s_add_u32 m0, s63, 8192\n\t" \
  "s_mov_b32 s60, 524288\n\t" \
  "buffer_load_dwordx4 v16, %[W], s60 offen lds\n\t" \
  "s_add_u32 m0, s63, 9216\n\t" \
  "s_mov_b32 s60, 589824\n\t" \
  "buffer_load_dwordx4 v16, %[W], s60 offen lds\n\t" \
  "s_add_u32 m0, s63, 10240\n\t" \
  "s_mov_b32 s60, 655360\n\t" \
  "buffer_load_dwordx4 v16, %[W], s60 offen lds\n\t" \
  "s_add_u32 m0, s63, 11264\n\t" \
  "s_mov_b32 s60, 720896\n\t" \
  "buffer_load_dwordx4 v16, %[W], s60 offen lds\n\t" \
  "s_add_u32 m0, s63, 12288\n\t" \
  "s_mov_b32 s60, 786432\n\t" \
  "buffer_load_dwordx4 v16, %[W], s60 offen lds\n\t" \
  "s_add_u32 m0, s63, 13312\n\t" \
  "s_mov_b32 s60, 851968\n\t" \
  "buffer_load_dwordx4 v16, %[W], s60 offen lds\n\t" \
  "s_add_u32 m0, s63, 14336\n\t" \
  "s_mov_b32 s60, 917504\n\t" \
  "buffer_load_dwordx4 v16, %[W], s60 offen lds\n\t" \
  "s_add_u32 m0, s63, 15360\n\t" \
  "s_mov_b32 s60, 983040\n\t" \
  "buffer_load_dwordx4 v16, %[W], s60 offen lds\n\t" \
  "s_add_u32 m0, s63, 16384\n\t" \
  "s_mov_b32 s60, 1048576\n\t" \
  "buffer_load_dwordx4 v16, %[W], s60 offen lds\n\t" \
  "s_add_u32 m0, s63, 17408\n\t" \
  "s_mov_b32 s60, 1114112\n\t" \
  "buffer_load_dwordx4 v16, %[W], s60 offen lds\n\t" \
  "s_add_u32 m0, s63, 18432\n\t" \
  "s_mov_b32 s60, 1179648\n\t" \
  "buffer_load_dwordx4 v16, %[W], s60 offen lds\n\t" \
  "s_add_u32 m0, s63, 19456\n\t" \
  "s_mov_b32 s60, 1245184\n\t" \
  "buffer_load_dwordx4 v16, %[W], s60 offen lds\n\t" \
  "s_add_u32 m0, s63, 20480\n\t" \
  "s_mov_b32 s60, 1310720\n\t" \
  "buffer_load_dwordx4 v16, %[W], s60 offen lds\n\t" \
  "s_add_u32 m0, s63, 21504\n\t" \
  "s_mov_b32 s60, 1376256\n\t" \
  "buffer_load_dwordx4 v16, %[W], s60 offen lds\n\t" \
  "s_add_u32 m0, s63, 22528\n\t" \
  "s_mov_b32 s60, 1441792\n\t" \
  "buffer_load_dwordx4 v16, %[W], s60 offen lds\n\t" \
  "s_add_u32 m0, s63, 23552\n\t" \
  "s_mov_b32 s60, 1507328\n\t" \
  "buffer_load_dwordx4 v16, %[W], s60 offen lds\n\t" \
  "s_add_u32 m0, s63, 24576\n\t" \
  "s_mov_b32 s60, 1572864\n\t" \
  "buffer_load_dwordx4 v16, %[W], s60 offen lds\n\t" \
  "s_add_u32 m0, s63, 25600\n\t" \
  "s_mov_b32 s60, 1638400\n\t" \
  "buffer_load_dwordx4 v16, %[W], s60 offen lds\n\t" \
  "s_add_u32 m0, s63, 26624\n\t" \
  "s_mov_b32 s60, 1703936\n\t" \
  "buffer_load_dwordx4 v16, %[W], s60 offen lds\n\t" \
  "s_add_u32 m0, s63, 27648\n\t" \
  "s_mov_b32 s60, 1769472\n\t" \
  "buffer_load_dwordx4 v16, %[W], s60 offen lds\n\t" \
  "s_add_u32 m0, s63, 28672\n\t" \
  "s_mov_b32 s60, 1835008\n\t" \
  "buffer_load_dwordx4 v16, %[W], s60 offen lds\n\t" \
  "s_add_u32 m0, s63, 29696\n\t" \
  "s_mov_b32 s60, 1900544\n\t" \
  "buffer_load_dwordx4 v16, %[W], s60 offen lds\n\t" \
  "s_add_u32 m0, s63, 30720\n\t" \
  "s_mov_b32 s60, 1966080\n\t" \
  "buffer_load_dwordx4 v16, %[W], s60 offen lds\n\t" \
  "s_add_u32 m0, s63, 31744\n\t" \
  "s_mov_b32 s60, 2031616\n\t" \
  "buffer_load_dwordx4 v16, %[W], s60 offen lds\n\t" \
  "s_add_u32 m0, s63, 32768\n\t" \
  "s_mov_b32 s60, 2097152\n\t" \
  "buffer_load_dwordx4 v16, %[W], s60 offen lds\n\t" \
  "s_add_u32 m0, s63, 33792\n\t" \
  "s_mov_b32 s60, 2162688\n\t" \
  "buffer_load_dwordx4 v16, %[W], s60 offen lds\n\t" \
  "s_add_u32 m0, s63, 34816\n\t" \
  "s_mov_b32 s60, 2228224\n\t" \
  "buffer_load_dwordx4 v16, %[W], s60 offen lds\n\t" \
  "s_add_u32 m0, s63, 35840\n\t" \
  "s_mov_b32 s60, 2293760\n\t" \
  "buffer_load_dwordx4 v16, %[W], s60 offen lds\n\t" \
  "s_add_u32 m0, s63, 36864\n\t" \
  "s_mov_b32 s60, 2359296\n\t" \
  "buffer_load_dwordx4 v16, %[W], s60 offen lds\n\t" \
  "s_add_u32 m0, s63, 37888\n\t" \
  "s_mov_b32 s60, 2424832\n\t" \
  "buffer_load_dwordx4 v16, %[W], s60 offen lds\n\t" \
  "s_add_u32 m0, s63, 38912\n\t" \
  "s_mov_b32 s60, 2490368\n\t" \
  "buffer_load_dwordx4 v16, %[W], s60 offen lds\n\t" \
  "s_add_u32 m0, s63, 39936\n\t" \
  "s_mov_b32 s60, 2555904\n\t" \
  "buffer_load_dwordx4 v16, %[W], s60 offen lds\n\t" \
  "s_waitcnt vmcnt(32)\n\t" \
  "ds_read_b64_tr_b16 a[0:1], v17 offset:0\n\t" \
  "ds_read_b64_tr_b16 a[2:3], v17 offset:512\n\t" \
  "ds_read_b64_tr_b16 a[4:5], v17 offset:4096\n\t" \
  "ds_read_b64_tr_b16 a[6:7], v17 offset:4608\n\t" \
  "ds_read_b64_tr_b16 a[72:73], v17 offset:8\n\t" \
  "ds_read_b64_tr_b16 a[74:75], v17 offset:520\n\t" \
  "ds_read_b64_tr_b16 a[76:77], v17 offset:4104\n\t" \
  "ds_read_b64_tr_b16 a[78:79], v17 offset:4616\n\t" \
  "ds_read_b64_tr_b16 a[144:145], v18 offset:0\n\t" \
  "ds_read_b64_tr_b16 a[146:147], v18 offset:512\n\t" \
  "ds_read_b64_tr_b16 a[148:149], v18 offset:4096\n\t" \
  "ds_read_b64_tr_b16 a[150:151], v18 offset:4608\n\t" \
  "ds_read_b64_tr_b16 a[216:217], v18 offset:8\n\t" \
  "ds_read_b64_tr_b16 a[218:219], v18 offset:520\n\t" \
  "ds_read_b64_tr_b16 a[220:221], v18 offset:4104\n\t" \
  "ds_read_b64_tr_b16 a[222:223], v18 offset:4616\n\t" \
  "s_waitcnt lgkmcnt(0)\n\t" \
  "s_add_u32 m0, s63, 0\n\t" \
  "s_mov_b32 s60, 2621440\n\t" \
  "buffer_load_dwordx4 v16, %[W], s60 offen lds\n\t" \
  "s_add_u32 m0, s63, 1024\n\t" \
  "s_mov_b32 s60, 2686976\n\t" \
  "buffer_load_dwordx4 v16, %[W], s60 offen lds\n\t" \
  "s_add_u32 m0, s63, 2048\n\t" \
  "s_mov_b32 s60, 2752512\n\t" \
  "buffer_load_dwordx4 v16, %[W], s60 offen lds\n\t" \
  "s_add_u32 m0, s63, 3072\n\t" \
  "s_mov_b32 s60, 2818048\n\t" \
  "buffer_load_dwordx4 v16, %[W], s60 offen lds\n\t" \
  "s_add_u32 m0, s63, 4096\n\t" \
  "s_mov_b32 s60, 2883584\n\t" \
  "buffer_load_dwordx4 v16, %[W], s60 offen lds\n\t" \
  "s_add_u32 m0, s63, 5120\n\t" \
  "s_mov_b32 s60, 2949120\n\t" \
  "buffer_load_dwordx4 v16, %[W], s60 offen lds\n\t" \
  "s_add_u32 m0, s63, 6144\n\t" \
  "s_mov_b32 s60, 3014656\n\t" \
  "buffer_load_dwordx4 v16, %[W], s60 offen lds\n\t" \
  "s_add_u32 m0, s63, 7168\n\t" \
  "s_mov_b32 s60, 3080192\n\t" \
  "buffer_load_dwordx4 v16, %[W], s60 offen lds\n\t" \
  "s_waitcnt vmcnt(32)\n\t" \
  "ds_read_b64_tr_b16 a[8:9], v17 offset:8192\n\t" \
  "ds_read_b64_tr_b16 a[10:11], v17 offset:8704\n\t" \
  "ds_read_b64_tr_b16 a[12:13], v17 offset:12288\n\t" \
  "ds_read_b64_tr_b16 a[14:15], v17 offset:12800\n\t" \
  "ds_read_b64_tr_b16 a[80:81], v17 offset:8200\n\t" \
  "ds_read_b64_tr_b16 a[82:83], v17 offset:8712\n\t" \
  "ds_read_b64_tr_b16 a[84:85], v17 offset:12296\n\t" \
  "ds_read_b64_tr_b16 a[86:87], v17 offset:12808\n\t" \
  "ds_read_b64_tr_b16 a[152:153], v18 offset:8192\n\t" \
  "ds_read_b64_tr_b16 a[154:155], v18 offset:8704\n\t" \
  "ds_read_b64_tr_b16 a[156:157], v18 offset:12288\n\t" \
  "ds_read_b64_tr_b16 a[158:159], v18 offset:12800\n\t" \
  "ds_read_b64_tr_b16 a[224:225], v18 offset:8200\n\t" \
  "ds_read_b64_tr_b16 a[226:227], v18 offset:8712\n\t" \
  "ds_read_b64_tr_b16 a[228:229], v18 offset:12296\n\t" \
  "ds_read_b64_tr_b16 a[230:231], v18 offset:12808\n\t" \
  "s_waitcnt lgkmcnt(0)\n\t" \
  "s_add_u32 m0, s63, 8192\n\t" \
  "s_mov_b32 s60, 3145728\n\t" \
  "buffer_load_dwordx4 v16, %[W], s60 offen lds\n\t" \
  "s_add_u32 m0, s63, 9216\n\t" \
  "s_mov_b32 s60, 3211264\n\t" \
  "buffer_load_dwordx4 v16, %[W], s60 offen lds\n\t" \
  "s_add_u32 m0, s63, 10240\n\t" \
  "s_mov_b32 s60, 3276800\n\t" \
  "buffer_load_dwordx4 v16, %[W], s60 offen lds\n\t" \
  "s_add_u32 m0, s63, 11264\n\t" \
  "s_mov_b32 s60, 3342336\n\t" \
  "buffer_load_dwordx4 v16, %[W], s60 offen lds\n\t" \
  "s_add_u32 m0, s63, 12288\n\t" \
  "s_mov_b32 s60, 3407872\n\t" \
  "buffer_load_dwordx4 v16, %[W], s60 offen lds\n\t" \
  "s_add_u32 m0, s63, 13312\n\t" \
  "s_mov_b32 s60, 3473408\n\t" \
  "buffer_load_dwordx4 v16, %[W], s60 offen lds\n\t" \
  "s_add_u32 m0, s63, 14336\n\t" \
  "s_mov_b32 s60, 3538944\n\t" \
  "buffer_load_dwordx4 v16, %[W], s60 offen lds\n\t" \
  "s_add_u32 m0, s63, 15360\n\t" \
  "s_mov_b32 s60, 3604480\n\t" \
  "buffer_load_dwordx4 v16, %[W], s60 offen lds\n\t" \
  "s_waitcnt vmcnt(32)\n\t" \
  "ds_read_b64_tr_b16 a[16:17], v17 offset:16384\n\t" \
  "ds_read_b64_tr_b16 a[18:19], v17 offset:16896\n\t" \
  "ds_read_b64_tr_b16 a[20:21], v17 offset:20480\n\t" \
  "ds_read_b64_tr_b16 a[22:23], v17 offset:20992\n\t" \
  "ds_read_b64_tr_b16 a[88:89], v17 offset:16392\n\t" \
  "ds_read_b64_tr_b16 a[90:91], v17 offset:16904\n\t" \
  "ds_read_b64_tr_b16 a[92:93], v17 offset:20488\n\t" \
  "ds_read_b64_tr_b16 a[94:95], v17 offset:21000\n\t" \
  "ds_read_b64_tr_b16 a[160:161], v18 offset:16384\n\t" \
  "ds_read_b64_tr_b16 a[162:163], v18 offset:16896\n\t" \
  "ds_read_b64_tr_b16 a[164:165], v18 offset:20480\n\t" \
  "ds_read_b64_tr_b16 a[166:167], v18 offset:20992\n\t" \
  "ds_read_b64_tr_b16 a[232:233], v18 offset:16392\n\t" \
  "ds_read_b64_tr_b16 a[234:235], v18 offset:16904\n\t" \
  "ds_read_b64_tr_b16 a[236:237], v18 offset:20488\n\t" \
  "ds_read_b64_tr_b16 a[238:239], v18 offset:21000\n\t" \
  "s_waitcnt lgkmcnt(0)\n\t" \
  "s_add_u32 m0, s63, 16384\n\t" \
  "s_mov_b32 s60, 3670016\n\t" \
  "buffer_load_dwordx4 v16, %[W], s60 offen lds\n\t" \
  "s_add_u32 m0, s63, 17408\n\t" \
  "s_mov_b32 s60, 3735552\n\t" \
  "buffer_load_dwordx4 v16, %[W], s60 offen lds\n\t" \
  "s_add_u32 m0, s63, 18432\n\t" \
  "s_mov_b32 s60, 3801088\n\t" \
  "buffer_load_dwordx4 v16, %[W], s60 offen lds\n\t" \
  "s_add_u32 m0, s63, 19456\n\t" \
  "s_mov_b32 s60, 3866624\n\t" \
  "buffer_load_dwordx4 v16, %[W], s60 offen lds\n\t" \
  "s_add_u32 m0, s63, 20480\n\t" \
  "s_mov_b32 s60, 3932160\n\t" \
  "buffer_load_dwordx4 v16, %[W], s60 offen lds\n\t" \
  "s_add_u32 m0, s63, 21504\n\t" \
  "s_mov_b32 s60, 3997696\n\t" \
  "buffer_load_dwordx4 v16, %[W], s60 offen lds\n\t" \
  "s_add_u32 m0, s63, 22528\n\t" \
  "s_mov_b32 s60, 4063232\n\t" \
  "buffer_load_dwordx4 v16, %[W], s60 offen lds\n\t" \
  "s_add_u32 m0, s63, 23552\n\t" \
  "s_mov_b32 s60, 4128768\n\t" \
  "buffer_load_dwordx4 v16, %[W], s60 offen lds\n\t" \
  "s_waitcnt vmcnt(32)\n\t" \
  "ds_read_b64_tr_b16 a[24:25], v17 offset:24576\n\t" \
  "ds_read_b64_tr_b16 a[26:27], v17 offset:25088\n\t" \
  "ds_read_b64_tr_b16 a[28:29], v17 offset:28672\n\t" \
  "ds_read_b64_tr_b16 a[30:31], v17 offset:29184\n\t" \
  "ds_read_b64_tr_b16 a[96:97], v17 offset:24584\n\t" \
  "ds_read_b64_tr_b16 a[98:99], v17 offset:25096\n\t" \
  "ds_read_b64_tr_b16 a[100:101], v17 offset:28680\n\t" \
  "ds_read_b64_tr_b16 a[102:103], v17 offset:29192\n\t" \
  "ds_read_b64_tr_b16 a[168:169], v18 offset:24576\n\t" \
  "ds_read_b64_tr_b16 a[170:171], v18 offset:25088\n\t" \
  "ds_read_b64_tr_b16 a[172:173], v18 offset:28672\n\t" \
  "ds_read_b64_tr_b16 a[174:175], v18 offset:29184\n\t" \
  "ds_read_b64_tr_b16 a[240:241], v18 offset:24584\n\t" \
  "ds_read_b64_tr_b16 a[242:243], v18 offset:25096\n\t" \
  "ds_read_b64_tr_b16 a[244:245], v18 offset:28680\n\t" \
  "ds_read_b64_tr_b16 a[246:247], v18 offset:29192\n\t" \
  "s_waitcnt lgkmcnt(0)\n\t" \
  "s_add_u32 m0, s63, 24576\n\t" \
  "s_mov_b32 s60, 4194304\n\t" \
  "buffer_load_dwordx4 v16, %[W], s60 offen lds\n\t" \
  "s_add_u32 m0, s63, 25600\n\t" \
  "s_mov_b32 s60, 4259840\n\t" \
  "buffer_load_dwordx4 v16, %[W], s60 offen lds\n\t" \
  "s_add_u32 m0, s63, 26624\n\t" \
  "s_mov_b32 s60, 4325376\n\t" \
  "buffer_load_dwordx4 v16, %[W], s60 offen lds\n\t" \
  "s_add_u32 m0, s63, 27648\n\t" \
  "s_mov_b32 s60, 4390912\n\t" \
  "buffer_load_dwordx4 v16, %[W], s60 offen lds\n\t" \
  "s_add_u32 m0, s63, 28672\n\t" \
  "s_mov_b32 s60, 4456448\n\t" \
  "buffer_load_dwordx4 v16, %[W], s60 offen lds\n\t" \
  "s_add_u32 m0, s63, 29696\n\t" \
  "s_mov_b32 s60, 4521984\n\t" \
  "buffer_load_dwordx4 v16, %[W], s60 offen lds\n\t" \
  "s_add_u32 m0, s63, 30720\n\t" \
  "s_mov_b32 s60, 4587520\n\t" \
  "buffer_load_dwordx4 v16, %[W], s60 offen lds\n\t" \
  "s_add_u32 m0, s63, 31744\n\t" \
  "s_mov_b32 s60, 4653056\n\t" \
  "buffer_load_dwordx4 v16, %[W], s60 offen lds\n\t" \
  "s_waitcnt vmcnt(32)\n\t" \
  "ds_read_b64_tr_b16 a[32:33], v17 offset:32768\n\t" \
  "ds_read_b64_tr_b16 a[34:35], v17 offset:33280\n\t" \
  "ds_read_b64_tr_b16 a[36:37], v17 offset:36864\n\t" \
  "ds_read_b64_tr_b16 a[38:39], v17 offset:37376\n\t" \
  "ds_read_b64_tr_b16 a[104:105], v17 offset:32776\n\t" \
  "ds_read_b64_tr_b16 a[106:107], v17 offset:33288\n\t" \
  "ds_read_b64_tr_b16 a[108:109], v17 offset:36872\n\t" \
  "ds_read_b64_tr_b16 a[110:111], v17 offset:37384\n\t" \
  "ds_read_b64_tr_b16 a[176:177], v18 offset:32768\n\t" \
  "ds_read_b64_tr_b16 a[178:179], v18 offset:33280\n\t" \
  "ds_read_b64_tr_b16 a[180:181], v18 offset:36864\n\t" \
  "ds_read_b64_tr_b16 a[182:183], v18 offset:37376\n\t" \
  "ds_read_b64_tr_b16 a[248:249], v18 offset:32776\n\t" \
  "ds_read_b64_tr_b16 a[250:251], v18 offset:33288\n\t" \
  "ds_read_b64_tr_b16 a[252:253], v18 offset:36872\n\t" \
  "ds_read_b64_tr_b16 a[254:255], v18 offset:37384\n\t" \
  "s_waitcnt lgkmcnt(0)\n\t" \
  "s_waitcnt vmcnt(24)\n\t" \
  "ds_read_b64_tr_b16 a[40:41], v17 offset:0\n\t" \
  "ds_read_b64_tr_b16 a[42:43], v17 offset:512\n\t" \
  "ds_read_b64_tr_b16 a[44:45], v17 offset:4096\n\t" \
  "ds_read_b64_tr_b16 a[46:47], v17 offset:4608\n\t" \
  "ds_read_b64_tr_b16 a[112:113], v17 offset:8\n\t" \
  "ds_read_b64_tr_b16 a[114:115], v17 offset:520\n\t" \
  "ds_read_b64_tr_b16 a[116:117], v17 offset:4104\n\t" \
  "ds_read_b64_tr_b16 a[118:119], v17 offset:4616\n\t" \
  "ds_read_b64_tr_b16 a[184:185], v18 offset:0\n\t" \
  "ds_read_b64_tr_b16 a[186:187], v18 offset:512\n\t" \
  "ds_read_b64_tr_b16 a[188:189], v18 offset:4096\n\t" \
  "ds_read_b64_tr_b16 a[190:191], v18 offset:4608\n\t" \
  "ds_read_b64_tr_b16 v[224:225], v18 offset:8\n\t" \
  "ds_read_b64_tr_b16 v[226:227], v18 offset:520\n\t" \
  "ds_read_b64_tr_b16 v[228:229], v18 offset:4104\n\t" \
  "ds_read_b64_tr_b16 v[230:231], v18 offset:4616\n\t" \
  "s_waitcnt lgkmcnt(0)\n\t" \
  "s_waitcnt vmcnt(16)\n\t" \
  "ds_read_b64_tr_b16 a[48:49], v17 offset:8192\n\t" \
  "ds_read_b64_tr_b16 a[50:51], v17 offset:8704\n\t" \
  "ds_read_b64_tr_b16 a[52:53], v17 offset:12288\n\t" \
  "ds_read_b64_tr_b16 a[54:55], v17 offset:12800\n\t" \
  "ds_read_b64_tr_b16 a[120:121], v17 offset:8200\n\t" \
  "ds_read_b64_tr_b16 a[122:123], v17 offset:8712\n\t" \
  "ds_read_b64_tr_b16 a[124:125], v17 offset:12296\n\t" \
  "ds_read_b64_tr_b16 a[126:127], v17 offset:12808\n\t" \
  "ds_read_b64_tr_b16 a[192:193], v18 offset:8192\n\t" \
  "ds_read_b64_tr_b16 a[194:195], v18 offset:8704\n\t" \
  "ds_read_b64_tr_b16 a[196:197], v18 offset:12288\n\t" \
  "ds_read_b64_tr_b16 a[198:199], v18 offset:12800\n\t" \
  "ds_read_b64_tr_b16 v[232:233], v18 offset:8200\n\t" \
  "ds_read_b64_tr_b16 v[234:235], v18 offset:8712\n\t" \
  "ds_read_b64_tr_b16 v[236:237], v18 offset:12296\n\t" \
  "ds_read_b64_tr_b16 v[238:239], v18 offset:12808\n\t" \
  "s_waitcnt lgkmcnt(0)\n\t" \
  "s_waitcnt vmcnt(8)\n\t" \
  "ds_read_b64_tr_b16 a[56:57], v17 offset:16384\n\t" \
  "ds_read_b64_tr_b16 a[58:59], v17 offset:16896\n\t" \
  "ds_read_b64_tr_b16 a[60:61], v17 offset:20480\n\t" \
  "ds_read_b64_tr_b16 a[62:63], v17 offset:20992\n\t" \
  "ds_read_b64_tr_b16 a[128:129], v17 offset:16392\n\t" \
  "ds_read_b64_tr_b16 a[130:131], v17 offset:16904\n\t" \
  "ds_read_b64_tr_b16 a[132:133], v17 offset:20488\n\t" \
  "ds_read_b64_tr_b16 a[134:135], v17 offset:21000\n\t" \
  "ds_read_b64_tr_b16 a[200:201], v18 offset:16384\n\t" \
  "ds_read_b64_tr_b16 a[202:203], v18 offset:16896\n\t" \
  "ds_read_b64_tr_b16 a[204:205], v18 offset:20480\n\t" \
  "ds_read_b64_tr_b16 a[206:207], v18 offset:20992\n\t" \
  "ds_read_b64_tr_b16 v[240:241], v18 offset:16392\n\t" \
  "ds_read_b64_tr_b16 v[242:243], v18 offset:16904\n\t" \
  "ds_read_b64_tr_b16 v[244:245], v18 offset:20488\n\t" \
  "ds_read_b64_tr_b16 v[246:247], v18 offset:21000\n\t" \
  "s_waitcnt lgkmcnt(0)\n\t" \
  "s_waitcnt vmcnt(0)\n\t" \
  "ds_read_b64_tr_b16 a[64:65], v17 offset:24576\n\t" \
  "ds_read_b64_tr_b16 a[66:67], v17 offset:25088\n\t" \
  "ds_read_b64_tr_b16 a[68:69], v17 offset:28672\n\t" \
  "ds_read_b64_tr_b16 a[70:71], v17 offset:29184\n\t" \
  "ds_read_b64_tr_b16 a[136:137], v17 offset:24584\n\t" \
  "ds_read_b64_tr_b16 a[138:139], v17 offset:25096\n\t" \
  "ds_read_b64_tr_b16 a[140:141], v17 offset:28680\n\t" \
  "ds_read_b64_tr_b16 a[142:143], v17 offset:29192\n\t" \
  "ds_read_b64_tr_b16 a[208:209], v18 offset:24576\n\t" \
  "ds_read_b64_tr_b16 a[210:211], v18 offset:25088\n\t" \
  "ds_read_b64_tr_b16 a[212:213], v18 offset:28672\n\t" \
  "ds_read_b64_tr_b16 a[214:215], v18 offset:29184\n\t" \
  "ds_read_b64_tr_b16 v[248:249], v18 offset:24584\n\t" \
  "ds_read_b64_tr_b16 v[250:251], v18 offset:25096\n\t" \
  "ds_read_b64_tr_b16 v[252:253], v18 offset:28680\n\t" \
  "ds_read_b64_tr_b16 v[254:255], v18 offset:29192\n\t" \
  "s_waitcnt lgkmcnt(0)\n\t" \
  "s_mov_b32 s84, " TK_CAP "\n\t" \
  "s_mov_b32 s48, 0\n\t" \
  "s_mov_b32 s49, 0xf0000000\n\t" \
  "s_mov_b32 s46, s41\n\t" \
  "s_mov_b32 s56, 0\n\t" \
  "v_lshrrev_b32 v7, 2, v0\n\t" \
  "s_mov_b32 s60, " TK_CTAB "\n\t" \
  "buffer_load_dword v4, v7, %[X], s60 offen offset:88 sc1\n\t" \
  "buffer_load_dword v7, v7, %[X], s60 offen offset:0 sc1\n\t" \
  "s_waitcnt vmcnt(0)\n\t" \
  "v_and_b32 v4, 1, v4\n\t" \
  "v_and_b32 v7, 1, v7\n\t" \
  "v_cmp_ne_u32 s[90:91], s67, v4\n\t" \
  "v_cmp_ne_u32 s[88:89], s67, v7\n\t" \
  "s_nop 1\n\t" \
  "s_mov_b32 s57, 0xf0000000\n\t" \
  "s_add_u32 s83, s48, 0\n\t" \
  "s_lshr_b64 s[60:61], s[90:91], s83\n\t" \
  "s_and_b32 s60, s60, 1\n\t" \
  "s_lshl_b32 s60, s60, 13\n\t" \
  "s_add_u32 s83, s83, " TK_SG0 "\n\t" \
  "s_lshl_b32 s83, s83, 21\n\t" \
  "s_or_b32 s83, s83, s60\n\t" \
  "s_add_u32 s62, s83, s39\n\t" \
  "v_add_u32 v7, s62, v3\n\t" \
  "LCOLD_%=:\n\t" \
  "s_waitcnt vmcnt(0)\n\t" \
  "s_mov_b64 exec, 1\n\t" \
  "buffer_store_dword v6, off, %[X], s49 sc1\n\t" \
  "s_mov_b64 exec, -1\n\t" \
  "s_mov_b32 s49, 0xf0000000\n\t" \
  "s_add_u32 s55, s83, s36\n\t" \
  "s_cmp_lg_u32 s40, 0\n\t" \
  "s_cbranch_scc1 LGO_%=\n\t" \
  "s_and_b32 s62, s45, 3\n\t" \
  "s_lshl_b32 s62, s62, 7\n\t" \
  "s_add_u32 s62, s62, s83\n\t" \
  "s_add_u32 s62, s62, s39\n\t" \
  "s_mov_b32 s61, 0\n\t" \
  "LPO_%=:\n\t" \
  "buffer_load_dword v4, off, %[X], s62 sc1\n\t" \
  "s_waitcnt vmcnt(0)\n\t" \
  "v_cmp_eq_u32 vcc, 0, v4\n\t" \
  "s_cbranch_vccz LPO_%=x\n\t" \
  "s_add_u32 s61, s61, 1\n\t" \
  "s_sleep " TK_PSH "\n\t" \
  "s_cmp_gt_u32 s61, s84\n\t" \
  "s_cbranch_scc1 LPO_%=g\n\t" \
  "s_and_b32 s60, s61, 1023\n\t" \
  "s_cmp_lg_u32 s60, 0\n\t" \
  "s_cbranch_scc1 LPO_%=\n\t" \
  "buffer_load_dword v4, off, %[X], s92 sc1\n\t" \
  "s_waitcnt vmcnt(0)\n\t" \
  "v_readfirstlane_b32 s60, v4\n\t" \
  "s_nop 0\n\t" \
  "s_cmp_eq_u32 s60, 0\n\t" \
  "s_cbranch_scc1 LPO_%=\n\t" \
  "LPO_%=g:\n\t" \
  "s_mov_b32 s84, 0\n\t" \
  "s_or_b32 s60, s48, 1610612736\n\t" \
  "v_mov_b32 v4, s60\n\t" \
  "s_mov_b64 exec, 1\n\t" \
  "buffer_store_dword v4, off, %[X], s92 sc1\n\t" \
  "s_mov_b64 exec, -1\n\t" \
  "LPO_%=x:\n\t" \
  "s_mov_b32 s61, 0\n\t" \
  "LPH_%=:\n\t" \
  "buffer_load_dword v4, v7, %[X], 0 offen sc1\n\t" \
  "s_waitcnt vmcnt(0)\n\t" \
  "v_cmp_eq_u32 vcc, 0, v4\n\t" \
  "s_cbranch_vccz LPH_%=x\n\t" \
  "s_add_u32 s61, s61, 1\n\t" \
  "s_sleep " TK_PSH "\n\t" \
  "s_cmp_gt_u32 s61, s84\n\t" \
  "s_cbranch_scc1 LPH_%=g\n\t" \
  "s_and_b32 s60, s61, 1023\n\t" \
  "s_cmp_lg_u32 s60, 0\n\t" \
  "s_cbranch_scc1 LPH_%=\n\t" \
  "buffer_load_dword v4, off, %[X], s92 sc1\n\t" \
  "s_waitcnt vmcnt(0)\n\t" \
  "v_readfirstlane_b32 s60, v4\n\t" \
  "s_nop 0\n\t" \
  "s_cmp_eq_u32 s60, 0\n\t" \
  "s_cbranch_scc1 LPH_%=\n\t" \
  "LPH_%=g:\n\t" \
  "s_mov_b32 s84, 0\n\t" \
  "s_or_b32 s60, s48, 1610612736\n\t" \
  "v_mov_b32 v4, s60\n\t" \
  "s_mov_b64 exec, 1\n\t" \
  "buffer_store_dword v4, off, %[X], s92 sc1\n\t" \
  "s_mov_b64 exec, -1\n\t" \
  "LPH_%=x:\n\t" \
  "s_cmp_lt_i32 s57, 0\n\t" \
  "s_cbranch_scc1 LGO_%=\n\t" \
  "s_movk_i32 s63, 4000\n\t" \
  "s_branch LGC_%=u\n\t" \
  "LGC_%=:\n\t" \
  "s_sleep " TK_PSG "\n\t" \
  "buffer_load_dword v8, off, %[X], s57 sc1\n\t" \
  "s_waitcnt vmcnt(0)\n\t" \
  "LGC_%=u:\n\t" \
  "s_memrealtime s[52:53]\n\t" \
  "v_readfirstlane_b32 s60, v8\n\t" \
  "s_waitcnt lgkmcnt(0)\n\t" \
  "s_sub_u32 s63, s63, 1\n\t" \
  "s_cbranch_scc1 LGO_%=\n\t" \
  "s_cmp_eq_u32 s60, -1\n\t" \
  "s_cbranch_scc1 LGC_%=\n\t" \
  "s_not_b32 s60, s60\n\t" \
  "s_lshl_b32 s61, s52, 8\n\t" \
  "s_sub_u32 s60, s60, s61\n\t" \
  "s_ashr_i32 s60, s60, 8\n\t" \
  "s_sub_i32 s60, s60, " TK_QXC "\n\t" \
  "s_min_i32 s60, s60, 400\n\t" \
  "s_add_u32 s54, s52, s60\n\t" \
  "LGC_%=w:\n\t" \
  "s_sleep 1\n\t" \
  "s_memrealtime s[52:53]\n\t" \
  "s_waitcnt lgkmcnt(0)\n\t" \
  "s_sub_i32 s53, s52, s54\n\t" \
  "s_cmp_lt_i32 s53, 0\n\t" \
  "s_cbranch_scc1 LGC_%=w\n\t" \
  "LGO_%=:\n\t" \
  "s_barrier\n\t" \
  "s_add_u32 m0, s46, 0\n\t" \
  "s_add_u32 s47, s55, 0\n\t" \
  "buffer_load_dwordx4 v0, %[X], s47 offen sc1 lds\n\t" \
  "s_add_u32 m0, s46, 4096\n\t" \
  "s_add_u32 s47, s55, 4096\n\t" \
  "buffer_load_dwordx4 v0, %[X], s47 offen sc1 lds\n\t" \
  "s_add_u32 m0, s46, 8192\n\t" \
  "s_add_u32 s47, s55, 16384\n\t" \
  "buffer_load_dwordx4 v0, %[X], s47 offen sc1 lds\n\t" \
  "s_add_u32 m0, s46, 12288\n\t" \
  "s_add_u32 s47, s55, 20480\n\t" \
  "buffer_load_dwordx4 v0, %[X], s47 offen sc1 lds\n\t" \
  "s_add_u32 m0, s46, 16384\n\t" \
  "s_add_u32 s47, s55, 32768\n\t" \
  "buffer_load_dwordx4 v0, %[X], s47 offen sc1 lds\n\t" \
  "s_add_u32 m0, s46, 20480\n\t" \
  "s_add_u32 s47, s55, 36864\n\t" \
  "buffer_load_dwordx4 v0, %[X], s47 offen sc1 lds\n\t" \
  "s_add_u32 m0, s46, 24576\n\t" \
  "s_add_u32 s47, s55, 49152\n\t" \
  "buffer_load_dwordx4 v0, %[X], s47 offen sc1 lds\n\t" \
  "s_add_u32 m0, s46, 28672\n\t" \
  "s_add_u32 s47, s55, 53248\n\t" \
  "buffer_load_dwordx4 v0, %[X], s47 offen sc1 lds\n\t" \
  "s_add_u32 m0, s46, 32768\n\t" \
  "s_add_u32 s47, s55, 65536\n\t" \
  "buffer_load_dwordx4 v0, %[X], s47 offen sc1 lds\n\t" \
  "s_waitcnt vmcnt(5)\n\t" \
  "LTOP_%=:\n\t" \
  "s_barrier\n\t" \
  TK_PQ1 \
  "ds_read_b128 v[64:67], v1 offset:0\n\t" \
  "ds_read_b128 v[68:71], v1 offset:2048\n\t" \
  "ds_read_b128 v[72:75], v2 offset:0\n\t" \
  "ds_read_b128 v[76:79], v2 offset:2048\n\t" \
  "ds_read_b128 v[80:83], v1 offset:4096\n\t" \
  "ds_read_b128 v[84:87], v1 offset:6144\n\t" \
  "s_mov_b32 s82, s83\n\t" \
  "s_xor_b32 s46, s46, 0x10000\n\t" \
  "s_waitcnt lgkmcnt(5)\n\t" \
  "v_mfma_f32_16x16x32_bf16 v[16:19], a[0:3], v[64:67], 0\n\t" \
  "ds_read_b128 v[88:91], v2 offset:4096\n\t" \
  "v_mfma_f32_16x16x32_bf16 v[20:23], a[72:75], v[64:67], 0\n\t" \
  "s_add_u32 s83, s48, 1\n\t" \
  "s_lshr_b64 s[60:61], s[90:91], s83\n\t" \
  "v_mfma_f32_16x16x32_bf16 v[24:27], a[144:147], v[64:67], 0\n\t" \
  "ds_read_b128 v[92:95], v2 offset:6144\n\t" \
  "v_mfma_f32_16x16x32_bf16 v[28:31], a[216:219], v[64:67], 0\n\t" \
  "s_and_b32 s60, s60, 1\n\t" \
  "s_lshl_b32 s60, s60, 13\n\t" \
  "s_waitcnt lgkmcnt(5)\n\t" \
  "v_mfma_f32_16x16x32_bf16 v[16:19], a[4:7], v[72:75], v[16:19]\n\t" \
  "ds_read_b128 v[96:99], v1 offset:8192\n\t" \
  "v_mfma_f32_16x16x32_bf16 v[20:23], a[76:79], v[72:75], v[20:23]\n\t" \
  "s_add_u32 s83, s83, " TK_SG0 "\n\t" \
  "s_lshl_b32 s83, s83, 21\n\t" \
  "v_mfma_f32_16x16x32_bf16 v[24:27], a[148:151], v[72:75], v[24:27]\n\t" \
  "ds_read_b128 v[100:103], v1 offset:10240\n\t" \
  "v_mfma_f32_16x16x32_bf16 v[28:31], a[220:223], v[72:75], v[28:31]\n\t" \
  "s_or_b32 s83, s83, s60\n\t" \
  "s_add_u32 s62, s83, s39\n\t" \
  "s_waitcnt lgkmcnt(5)\n\t" \
  "v_mfma_f32_16x16x32_bf16 v[16:19], a[8:11], v[80:83], v[16:19]\n\t" \
  "ds_read_b128 v[104:107], v2 offset:8192\n\t" \
  "v_mfma_f32_16x16x32_bf16 v[20:23], a[80:83], v[80:83], v[20:23]\n\t" \
  "v_add_u32 v7, s62, v3\n\t" \
  "s_add_u32 s50, s82, s37\n\t" \
  "v_mfma_f32_16x16x32_bf16 v[24:27], a[152:155], v[80:83], v[24:27]\n\t" \
  "ds_read_b128 v[108:111], v2 offset:10240\n\t" \
  "v_mfma_f32_16x16x32_bf16 v[28:31], a[224:227], v[80:83], v[28:31]\n\t" \
  "s_add_u32 s51, s50, 4096\n\t" \
  "s_waitcnt lgkmcnt(5)\n\t" \
  "v_mfma_f32_16x16x32_bf16 v[16:19], a[12:15], v[88:91], v[16:19]\n\t" \
  "ds_read_b128 v[112:115], v1 offset:12288\n\t" \
  "v_mfma_f32_16x16x32_bf16 v[20:23], a[84:87], v[88:91], v[20:23]\n\t" \
  "v_mfma_f32_16x16x32_bf16 v[24:27], a[156:159], v[88:91], v[24:27]\n\t" \
  "ds_read_b128 v[116:119], v1 offset:14336\n\t" \
  "v_mfma_f32_16x16x32_bf16 v[28:31], a[228:231], v[88:91], v[28:31]\n\t" \
  "s_waitcnt lgkmcnt(5)\n\t" \
  "v_mfma_f32_16x16x32_bf16 v[16:19], a[16:19], v[96:99], v[16:19]\n\t" \
  "ds_read_b128 v[120:123], v2 offset:12288\n\t" \
  "v_mfma_f32_16x16x32_bf16 v[20:23], a[88:91], v[96:99], v[20:23]\n\t" \
  "s_cmp_lg_u32 s56, 0\n\t" \
  "v_mfma_f32_16x16x32_bf16 v[24:27], a[160:163], v[96:99], v[24:27]\n\t" \
  "ds_read_b128 v[124:127], v2 offset:14336\n\t" \
  "v_mfma_f32_16x16x32_bf16 v[28:31], a[232:235], v[96:99], v[28:31]\n\t" \
  "s_cbranch_scc1 LWARM_%=\n\t" \
  "s_waitcnt vmcnt(0)\n\t" \
  "LWARM_%=:\n\t" \
  "s_barrier\n\t" \
  "s_waitcnt lgkmcnt(5)\n\t" \
  "v_mfma_f32_16x16x32_bf16 v[16:19], a[20:23], v[104:107], v[16:19]\n\t" \
  "ds_read_b128 v[128:131], v1 offset:16384\n\t" \
  "v_mfma_f32_16x16x32_bf16 v[20:23], a[92:95], v[104:107], v[20:23]\n\t" \
  "v_mfma_f32_16x16x32_bf16 v[24:27], a[164:167], v[104:107], v[24:27]\n\t" \
  "ds_read_b128 v[132:135], v1 offset:18432\n\t" \
  "v_mfma_f32_16x16x32_bf16 v[28:31], a[236:239], v[104:107], v[28:31]\n\t" \
  "s_add_i32 s62, s48, s42\n\t" \
  "s_add_i32 s62, s62, 1\n\t" \
  "s_waitcnt lgkmcnt(5)\n\t" \
  "v_mfma_f32_16x16x32_bf16 v[16:19], a[24:27], v[112:115], v[16:19]\n\t" \
  "ds_read_b128 v[136:139], v2 offset:16384\n\t" \
  "v_mfma_f32_16x16x32_bf16 v[20:23], a[96:99], v[112:115], v[20:23]\n\t" \
  "s_mul_i32 s63, s62, 43\n\t" \
  "s_lshr_b32 s63, s63, 7\n\t" \
  "v_mfma_f32_16x16x32_bf16 v[24:27], a[168:171], v[112:115], v[24:27]\n\t" \
  "ds_read_b128 v[140:143], v2 offset:18432\n\t" \
  "v_mfma_f32_16x16x32_bf16 v[28:31], a[240:243], v[112:115], v[28:31]\n\t" \
  "s_mul_i32 s57, s62, 688128\n\t" \
  "s_lshl_b32 s60, s63, 15\n\t" \
  "s_waitcnt lgkmcnt(5)\n\t" \
  "v_mfma_f32_16x16x32_bf16 v[16:19], a[28:31], v[120:123], v[16:19]\n\t" \
  "ds_read_b128 v[144:147], v1 offset:20480\n\t" \
  "v_mfma_f32_16x16x32_bf16 v[20:23], a[100:103], v[120:123], v[20:23]\n\t" \
  "s_add_u32 s57, s57, s60\n\t" \
  "s_lshr_b32 s60, s88, s63\n\t" \
  "v_mfma_f32_16x16x32_bf16 v[24:27], a[172:175], v[120:123], v[24:27]\n\t" \
  "ds_read_b128 v[148:151], v1 offset:22528\n\t" \
  "v_mfma_f32_16x16x32_bf16 v[28:31], a[244:247], v[120:123], v[28:31]\n\t" \
  "s_and_b32 s60, s60, 1\n\t" \
  "s_lshl_b32 s60, s60, 13\n\t" \
  "s_waitcnt lgkmcnt(5)\n\t" \
  "v_mfma_f32_16x16x32_bf16 v[16:19], a[32:35], v[128:131], v[16:19]\n\t" \
  "ds_read_b128 v[152:155], v2 offset:20480\n\t" \
  "v_mfma_f32_16x16x32_bf16 v[20:23], a[104:107], v[128:131], v[20:23]\n\t" \
  "s_add_u32 s57, s57, s60\n\t" \
  "s_add_u32 s57, s57, 656896\n\t" \
  "v_mfma_f32_16x16x32_bf16 v[24:27], a[176:179], v[128:131], v[24:27]\n\t" \
  "ds_read_b128 v[156:159], v2 offset:22528\n\t" \
  "v_mfma_f32_16x16x32_bf16 v[28:31], a[248:251], v[128:131], v[28:31]\n\t" \
  "s_cmp_lt_i32 s62, 1\n\t" \
  "s_cselect_b32 s57, 0xf0000000, s57\n\t" \
  "s_waitcnt lgkmcnt(5)\n\t" \
  "v_mfma_f32_16x16x32_bf16 v[16:19], a[36:39], v[136:139], v[16:19]\n\t" \
  "ds_read_b128 v[160:163], v1 offset:24576\n\t" \
  "v_mfma_f32_16x16x32_bf16 v[20:23], a[108:111], v[136:139], v[20:23]\n\t" \
  "s_cmp_lg_u32 s40, 0\n\t" \
  "s_cselect_b32 s57, 0xf0000000, s57\n\t" \
  "v_mfma_f32_16x16x32_bf16 v[24:27], a[180:183], v[136:139], v[24:27]\n\t" \
  "ds_read_b128 v[164:167], v1 offset:26624\n\t" \
  "v_mfma_f32_16x16x32_bf16 v[28:31], a[252:255], v[136:139], v[28:31]\n\t" \
  "s_waitcnt lgkmcnt(5)\n\t" \
  "v_mfma_f32_16x16x32_bf16 v[16:19], a[40:43], v[144:147], v[16:19]\n\t" \
  "ds_read_b128 v[168:171], v2 offset:24576\n\t" \
  "v_mfma_f32_16x16x32_bf16 v[20:23], a[112:115], v[144:147], v[20:23]\n\t" \
  "v_mfma_f32_16x16x32_bf16 v[24:27], a[184:187], v[144:147], v[24:27]\n\t" \
  "ds_read_b128 v[172:175], v2 offset:26624\n\t" \
  "v_mfma_f32_16x16x32_bf16 v[28:31], v[224:227], v[144:147], v[28:31]\n\t" \
  "s_waitcnt lgkmcnt(5)\n\t" \
  "v_mfma_f32_16x16x32_bf16 v[16:19], a[44:47], v[152:155], v[16:19]\n\t" \
  "ds_read_b128 v[176:179], v1 offset:28672\n\t" \
  "v_mfma_f32_16x16x32_bf16 v[20:23], a[116:119], v[152:155], v[20:23]\n\t" \
  "v_mfma_f32_16x16x32_bf16 v[24:27], a[188:191], v[152:155], v[24:27]\n\t" \
  "ds_read_b128 v[180:183], v1 offset:30720\n\t" \
  "v_mfma_f32_16x16x32_bf16 v[28:31], v[228:231], v[152:155], v[28:31]\n\t" \
  "s_waitcnt lgkmcnt(5)\n\t" \
  "v_mfma_f32_16x16x32_bf16 v[16:19], a[48:51], v[160:163], v[16:19]\n\t" \
  "ds_read_b128 v[184:187], v2 offset:28672\n\t" \
  "v_mfma_f32_16x16x32_bf16 v[20:23], a[120:123], v[160:163], v[20:23]\n\t" \
  TK_PQ0 \
  "v_mfma_f32_16x16x32_bf16 v[24:27], a[192:195], v[160:163], v[24:27]\n\t" \
  "ds_read_b128 v[188:191], v2 offset:30720\n\t" \
  "v_mfma_f32_16x16x32_bf16 v[28:31], v[232:235], v[160:163], v[28:31]\n\t" \
  "s_waitcnt lgkmcnt(5)\n\t" \
  "v_mfma_f32_16x16x32_bf16 v[16:19], a[52:55], v[168:171], v[16:19]\n\t" \
  "ds_read_b128 v[192:195], v1 offset:32768\n\t" \
  "v_mfma_f32_16x16x32_bf16 v[20:23], a[124:127], v[168:171], v[20:23]\n\t" \
  "v_mfma_f32_16x16x32_bf16 v[24:27], a[196:199], v[168:171], v[24:27]\n\t" \
  "ds_read_b128 v[196:199], v1 offset:34816\n\t" \
  "v_mfma_f32_16x16x32_bf16 v[28:31], v[236:239], v[168:171], v[28:31]\n\t" \
  "s_waitcnt lgkmcnt(5)\n\t" \
  "v_mfma_f32_16x16x32_bf16 v[16:19], a[56:59], v[176:179], v[16:19]\n\t" \
  "ds_read_b128 v[200:203], v2 offset:32768\n\t" \
  "v_mfma_f32_16x16x32_bf16 v[20:23], a[128:131], v[176:179], v[20:23]\n\t" \
  "v_mfma_f32_16x16x32_bf16 v[24:27], a[200:203], v[176:179], v[24:27]\n\t" \
  "ds_read_b128 v[204:207], v2 offset:34816\n\t" \
  "v_mfma_f32_16x16x32_bf16 v[28:31], v[240:243], v[176:179], v[28:31]\n\t" \
  "s_waitcnt lgkmcnt(5)\n\t" \
  "v_mfma_f32_16x16x32_bf16 v[16:19], a[60:63], v[184:187], v[16:19]\n\t" \
  "v_mfma_f32_16x16x32_bf16 v[20:23], a[132:135], v[184:187], v[20:23]\n\t" \
  "buffer_load_dword v4, v7, %[X], 0 offen sc1\n\t" \
  "v_mfma_f32_16x16x32_bf16 v[24:27], a[204:207], v[184:187], v[24:27]\n\t" \
  "v_mfma_f32_16x16x32_bf16 v[28:31], v[244:247], v[184:187], v[28:31]\n\t" \
  "s_waitcnt lgkmcnt(3)\n\t" \
  "v_mfma_f32_16x16x32_bf16 v[16:19], a[64:67], v[192:195], v[16:19]\n\t" \
  "v_xor_b32 v1, 0x10000, v1\n\t" \
  "v_xor_b32 v2, 0x10000, v2\n\t" \
  "v_mfma_f32_16x16x32_bf16 v[20:23], a[136:139], v[192:195], v[20:23]\n\t" \
  "v_mfma_f32_16x16x32_bf16 v[24:27], a[208:211], v[192:195], v[24:27]\n\t" \
  "v_mfma_f32_16x16x32_bf16 v[28:31], v[248:251], v[192:195], v[28:31]\n\t" \
  "s_waitcnt lgkmcnt(1)\n\t" \
  "v_mfma_f32_16x16x32_bf16 v[16:19], a[68:71], v[200:203], v[16:19]\n\t" \
  "v_mfma_f32_16x16x32_bf16 v[20:23], a[140:143], v[200:203], v[20:23]\n\t" \
  "v_mfma_f32_16x16x32_bf16 v[24:27], a[212:215], v[200:203], v[24:27]\n\t" \
  "v_mfma_f32_16x16x32_bf16 v[28:31], v[252:255], v[200:203], v[28:31]\n\t" \
  "v_mfma_f32_16x16x32_bf16 v[32:35], a[0:3], v[68:71], 0\n\t" \
  "v_cvt_pk_bf16_f32 v48, v16, v17\n\t" \
  "v_cvt_pk_bf16_f32 v49, v18, v19\n\t" \
  "v_mfma_f32_16x16x32_bf16 v[36:39], a[72:75], v[68:71], 0\n\t" \
  "v_cvt_pk_bf16_f32 v50, v20, v21\n\t" \
  "v_cvt_pk_bf16_f32 v51, v22, v23\n\t" \
  "v_mfma_f32_16x16x32_bf16 v[32:35], a[4:7], v[76:79], v[32:35]\n\t" \
  "v_cvt_pk_bf16_f32 v52, v24, v25\n\t" \
  "v_cvt_pk_bf16_f32 v53, v26, v27\n\t" \
  "v_mfma_f32_16x16x32_bf16 v[36:39], a[76:79], v[76:79], v[36:39]\n\t" \
  "v_cvt_pk_bf16_f32 v54, v28, v29\n\t" \
  "v_cvt_pk_bf16_f32 v55, v30, v31\n\t" \
  "v_mfma_f32_16x16x32_bf16 v[32:35], a[8:11], v[84:87], v[32:35]\n\t" \
  "buffer_store_dwordx4 v[48:51], v0, %[X], s50 offen offset:0 sc1\n\t" \
  "v_mfma_f32_16x16x32_bf16 v[36:39], a[80:83], v[84:87], v[36:39]\n\t" \
  "v_mfma_f32_16x16x32_bf16 v[32:35], a[12:15], v[92:95], v[32:35]\n\t" \
  "v_mfma_f32_16x16x32_bf16 v[36:39], a[84:87], v[92:95], v[36:39]\n\t" \
  "v_mfma_f32_16x16x32_bf16 v[32:35], a[16:19], v[100:103], v[32:35]\n\t" \
  "v_mfma_f32_16x16x32_bf16 v[36:39], a[88:91], v[100:103], v[36:39]\n\t" \
  "v_mfma_f32_16x16x32_bf16 v[32:35], a[20:23], v[108:111], v[32:35]\n\t" \
  "v_mfma_f32_16x16x32_bf16 v[36:39], a[92:95], v[108:111], v[36:39]\n\t" \
  "v_mfma_f32_16x16x32_bf16 v[32:35], a[24:27], v[116:119], v[32:35]\n\t" \
  "v_mfma_f32_16x16x32_bf16 v[36:39], a[96:99], v[116:119], v[36:39]\n\t" \
  "v_mfma_f32_16x16x32_bf16 v[32:35], a[28:31], v[124:127], v[32:35]\n\t" \
  "v_mfma_f32_16x16x32_bf16 v[36:39], a[100:103], v[124:127], v[36:39]\n\t" \
  "s_waitcnt vmcnt(1)\n\t" \
  "v_cmp_eq_u32 vcc, 0, v4\n\t" \
  "v_mfma_f32_16x16x32_bf16 v[32:35], a[32:35], v[132:135], v[32:35]\n\t" \
  "v_mfma_f32_16x16x32_bf16 v[36:39], a[104:107], v[132:135], v[36:39]\n\t" \
  "s_cmp_eq_u64 vcc, 0\n\t" \
  "s_cselect_b32 s60, 1, 0\n\t" \
  "v_mov_b32 v4, s60\n\t" \
  "v_mfma_f32_16x16x32_bf16 v[32:35], a[36:39], v[140:143], v[32:35]\n\t" \
  "ds_write_b32 v5, v4\n\t" \
  "v_mfma_f32_16x16x32_bf16 v[36:39], a[108:111], v[140:143], v[36:39]\n\t" \
  "v_mfma_f32_16x16x32_bf16 v[32:35], a[40:43], v[148:151], v[32:35]\n\t" \
  "v_mfma_f32_16x16x32_bf16 v[36:39], a[112:115], v[148:151], v[36:39]\n\t" \
  "buffer_store_dwordx4 v[52:55], v0, %[X], s50 offen offset:1024 sc1\n\t" \
  "v_mfma_f32_16x16x32_bf16 v[32:35], a[44:47], v[156:159], v[32:35]\n\t" \
  "v_mfma_f32_16x16x32_bf16 v[36:39], a[116:119], v[156:159], v[36:39]\n\t" \
  "s_waitcnt lgkmcnt(0)\n\t" \
  "s_barrier\n\t" \
  "v_mfma_f32_16x16x32_bf16 v[32:35], a[48:51], v[164:167], v[32:35]\n\t" \
  "ds_read_b32 v4, v6 offset:40959\n\t" \
  "v_mfma_f32_16x16x32_bf16 v[36:39], a[120:123], v[164:167], v[36:39]\n\t" \
  "v_mfma_f32_16x16x32_bf16 v[32:35], a[52:55], v[172:175], v[32:35]\n\t" \
  "v_mfma_f32_16x16x32_bf16 v[36:39], a[124:127], v[172:175], v[36:39]\n\t" \
  "v_mfma_f32_16x16x32_bf16 v[32:35], a[56:59], v[180:183], v[32:35]\n\t" \
  "v_mfma_f32_16x16x32_bf16 v[36:39], a[128:131], v[180:183], v[36:39]\n\t" \
  "s_waitcnt lgkmcnt(0)\n\t" \
  "v_readfirstlane_b32 s60, v4\n\t" \
  "s_add_u32 s55, s83, s36\n\t" \
  "v_mfma_f32_16x16x32_bf16 v[32:35], a[60:63], v[188:191], v[32:35]\n\t" \
  "s_cmp_eq_u32 s48, 63\n\t" \
  "s_cselect_b32 s56, 0, s60\n\t" \
  "v_mfma_f32_16x16x32_bf16 v[36:39], a[132:135], v[188:191], v[36:39]\n\t" \
  "s_cmp_eq_u32 s56, 0\n\t" \
  "s_cselect_b32 s55, 0xf0000000, s55\n\t" \
  "v_mfma_f32_16x16x32_bf16 v[32:35], a[64:67], v[196:199], v[32:35]\n\t" \
  "v_mfma_f32_16x16x32_bf16 v[36:39], a[136:139], v[196:199], v[36:39]\n\t" \
  "v_mfma_f32_16x16x32_bf16 v[32:35], a[68:71], v[204:207], v[32:35]\n\t" \
  "s_add_u32 m0, s46, 0\n\t" \
  "s_add_u32 s47, s55, 0\n\t" \
  "buffer_load_dwordx4 v0, %[X], s47 offen sc1 lds\n\t" \
  "v_mfma_f32_16x16x32_bf16 v[36:39], a[140:143], v[204:207], v[36:39]\n\t" \
  "v_mfma_f32_16x16x32_bf16 v[40:43], a[144:147], v[68:71], 0\n\t" \
  "v_mfma_f32_16x16x32_bf16 v[44:47], a[216:219], v[68:71], 0\n\t" \
  "v_mfma_f32_16x16x32_bf16 v[40:43], a[148:151], v[76:79], v[40:43]\n\t" \
  "v_cvt_pk_bf16_f32 v56, v32, v33\n\t" \
  "v_cvt_pk_bf16_f32 v57, v34, v35\n\t" \
  "s_add_u32 m0, s46, 4096\n\t" \
  "s_add_u32 s47, s55, 4096\n\t" \
  "buffer_load_dwordx4 v0, %[X], s47 offen sc1 lds\n\t" \
  "v_mfma_f32_16x16x32_bf16 v[44:47], a[220:223], v[76:79], v[44:47]\n\t" \
  "v_cvt_pk_bf16_f32 v58, v36, v37\n\t" \
  "v_cvt_pk_bf16_f32 v59, v38, v39\n\t" \
  "v_mfma_f32_16x16x32_bf16 v[40:43], a[152:155], v[84:87], v[40:43]\n\t" \
  "v_mfma_f32_16x16x32_bf16 v[44:47], a[224:227], v[84:87], v[44:47]\n\t" \
  "buffer_store_dwordx4 v[56:59], v0, %[X], s51 offen offset:0 sc1\n\t" \
  "v_mfma_f32_16x16x32_bf16 v[40:43], a[156:159], v[92:95], v[40:43]\n\t" \
  "s_add_u32 m0, s46, 8192\n\t" \
  "s_add_u32 s47, s55, 16384\n\t" \
  "buffer_load_dwordx4 v0, %[X], s47 offen sc1 lds\n\t" \
  "v_mfma_f32_16x16x32_bf16 v[44:47], a[228:231], v[92:95], v[44:47]\n\t" \
  "v_mfma_f32_16x16x32_bf16 v[40:43], a[160:163], v[100:103], v[40:43]\n\t" \
  "v_mfma_f32_16x16x32_bf16 v[44:47], a[232:235], v[100:103], v[44:47]\n\t" \
  "v_mfma_f32_16x16x32_bf16 v[40:43], a[164:167], v[108:111], v[40:43]\n\t" \
  "s_add_u32 m0, s46, 12288\n\t" \
  "s_add_u32 s47, s55, 20480\n\t" \
  "buffer_load_dwordx4 v0, %[X], s47 offen sc1 lds\n\t" \
  "v_mfma_f32_16x16x32_bf16 v[44:47], a[236:239], v[108:111], v[44:47]\n\t" \
  "v_mfma_f32_16x16x32_bf16 v[40:43], a[168:171], v[116:119], v[40:43]\n\t" \
  "v_mfma_f32_16x16x32_bf16 v[44:47], a[240:243], v[116:119], v[44:47]\n\t" \
  "v_mfma_f32_16x16x32_bf16 v[40:43], a[172:175], v[124:127], v[40:43]\n\t" \
  "s_add_u32 m0, s46, 16384\n\t" \
  "s_add_u32 s47, s55, 32768\n\t" \
  "buffer_load_dwordx4 v0, %[X], s47 offen sc1 lds\n\t" \
  "v_mfma_f32_16x16x32_bf16 v[44:47], a[244:247], v[124:127], v[44:47]\n\t" \
  "v_mfma_f32_16x16x32_bf16 v[40:43], a[176:179], v[132:135], v[40:43]\n\t" \
  "v_mfma_f32_16x16x32_bf16 v[44:47], a[248:251], v[132:135], v[44:47]\n\t" \
  "v_mfma_f32_16x16x32_bf16 v[40:43], a[180:183], v[140:143], v[40:43]\n\t" \
  "s_add_u32 m0, s46, 20480\n\t" \
  "s_add_u32 s47, s55, 36864\n\t" \
  "buffer_load_dwordx4 v0, %[X], s47 offen sc1 lds\n\t" \
  "v_mfma_f32_16x16x32_bf16 v[44:47], a[252:255], v[140:143], v[44:47]\n\t" \
  "v_mfma_f32_16x16x32_bf16 v[40:43], a[184:187], v[148:151], v[40:43]\n\t" \
  "v_mfma_f32_16x16x32_bf16 v[44:47], v[224:227], v[148:151], v[44:47]\n\t" \
  "v_mfma_f32_16x16x32_bf16 v[40:43], a[188:191], v[156:159], v[40:43]\n\t" \
  "s_add_u32 m0, s46, 24576\n\t" \
  "s_add_u32 s47, s55, 49152\n\t" \
  "buffer_load_dwordx4 v0, %[X], s47 offen sc1 lds\n\t" \
  "v_mfma_f32_16x16x32_bf16 v[44:47], v[228:231], v[156:159], v[44:47]\n\t" \
  "v_mfma_f32_16x16x32_bf16 v[40:43], a[192:195], v[164:167], v[40:43]\n\t" \
  "v_mfma_f32_16x16x32_bf16 v[44:47], v[232:235], v[164:167], v[44:47]\n\t" \
  "v_mfma_f32_16x16x32_bf16 v[40:43], a[196:199], v[172:175], v[40:43]\n\t" \
  "s_add_u32 m0, s46, 28672\n\t" \
  "s_add_u32 s47, s55, 53248\n\t" \
  "buffer_load_dwordx4 v0, %[X], s47 offen sc1 lds\n\t" \
  "v_mfma_f32_16x16x32_bf16 v[44:47], v[236:239], v[172:175], v[44:47]\n\t" \
  "v_mfma_f32_16x16x32_bf16 v[40:43], a[200:203], v[180:183], v[40:43]\n\t" \
  "v_mfma_f32_16x16x32_bf16 v[44:47], v[240:243], v[180:183], v[44:47]\n\t" \
  "v_mfma_f32_16x16x32_bf16 v[40:43], a[204:207], v[188:191], v[40:43]\n\t" \
  "s_add_u32 m0, s46, 32768\n\t" \
  "s_add_u32 s47, s55, 65536\n\t" \
  "buffer_load_dwordx4 v0, %[X], s47 offen sc1 lds\n\t" \
  "v_mfma_f32_16x16x32_bf16 v[44:47], v[244:247], v[188:191], v[44:47]\n\t" \
  "v_mfma_f32_16x16x32_bf16 v[40:43], a[208:211], v[196:199], v[40:43]\n\t" \
  "v_mfma_f32_16x16x32_bf16 v[44:47], v[248:251], v[196:199], v[44:47]\n\t" \
  "buffer_load_dword v8, off, %[X], s57 sc1\n\t" \
  "v_mfma_f32_16x16x32_bf16 v[40:43], a[212:215], v[204:207], v[40:43]\n\t" \
  "v_mfma_f32_16x16x32_bf16 v[44:47], v[252:255], v[204:207], v[44:47]\n\t" \
  "s_nop 4\n\t" \
  "v_cvt_pk_bf16_f32 v60, v40, v41\n\t" \
  "v_cvt_pk_bf16_f32 v61, v42, v43\n\t" \
  "s_nop 1\n\t" \
  "v_cvt_pk_bf16_f32 v62, v44, v45\n\t" \
  "v_cvt_pk_bf16_f32 v63, v46, v47\n\t" \
  "buffer_store_dwordx4 v[60:63], v0, %[X], s51 offen offset:1024 sc1\n\t" \
  TK_PQ2 \
  "s_add_u32 s49, s82, s38\n\t" \
  "s_cmp_eq_u32 s48, 63\n\t" \
  "s_cbranch_scc1 LEND_%=\n\t" \
  "s_add_u32 s48, s48, 1\n\t" \
  "s_cmp_eq_u32 s56, 0\n\t" \
  "s_cbranch_scc1 LCOLD_%=\n\t" \
  "s_waitcnt vmcnt(0)\n\t" \
  "s_mov_b64 exec, 1\n\t" \
  "buffer_store_dword v6, off, %[X], s49 sc1\n\t" \
  "s_mov_b64 exec, -1\n\t" \
  "s_mov_b32 s49, 0xf0000000\n\t" \
  "s_cmp_lt_i32 s57, 0\n\t" \
  "s_cbranch_scc1 LTOP_%=\n\t" \
  "s_movk_i32 s63, 4000\n\t" \
  "s_branch LGW_%=u\n\t" \
  "LGW_%=:\n\t" \
  "s_sleep " TK_PSG "\n\t" \
  "buffer_load_dword v8, off, %[X], s57 sc1\n\t" \
  "s_waitcnt vmcnt(0)\n\t" \
  "LGW_%=u:\n\t" \
  "s_memrealtime s[52:53]\n\t" \
  "v_readfirstlane_b32 s60, v8\n\t" \
  "s_waitcnt lgkmcnt(0)\n\t" \
  "s_sub_u32 s63, s63, 1\n\t" \
  "s_cbranch_scc1 LTOP_%=\n\t" \
  "s_cmp_eq_u32 s60, -1\n\t" \
  "s_cbranch_scc1 LGW_%=\n\t" \
  "s_not_b32 s60, s60\n\t" \
  "s_lshl_b32 s61, s52, 8\n\t" \
  "s_sub_u32 s60, s60, s61\n\t" \
  "s_ashr_i32 s60, s60, 8\n\t" \
  "s_sub_i32 s60, s60, " TK_QXW "\n\t" \
  "s_min_i32 s60, s60, 400\n\t" \
  "s_add_u32 s54, s52, s60\n\t" \
  "LGW_%=w:\n\t" \
  "s_sleep 1\n\t" \
  "s_memrealtime s[52:53]\n\t" \
  "s_waitcnt lgkmcnt(0)\n\t" \
  "s_sub_i32 s53, s52, s54\n\t" \
  "s_cmp_lt_i32 s53, 0\n\t" \
  "s_cbranch_scc1 LGW_%=w\n\t" \
  "s_branch LTOP_%=\n\t" \
  "LEND_%=:\n\t" \
  "s_waitcnt vmcnt(0)\n\t" \
  "s_mov_b64 exec, 1\n\t" \
  "buffer_store_dword v6, off, %[X], s49 sc1\n\t" \
  "s_mov_b64 exec, -1\n\t" \
  "s_mov_b32 s49, 0xf0000000\n\t" \
  TK_PQ0 \
  ""
#define QV_ASM \
  "v_lshrrev_b32 v16, 7, v0\n\t" \
  "v_lshlrev_b32 v16, 13, v16\n\t" \
  "v_lshrrev_b32 v19, 2, v0\n\t" \
  "v_and_b32 v19, 64, v19\n\t" \
  "v_xor_b32 v19, v19, v0\n\t" \
  "v_and_b32 v19, 0x70, v19\n\t" \
  "v_add_u32 v16, v16, v19\n\t" \
  "s_mul_i32 s63, s40, 40960\n\t" \
  "v_and_b32 v19, 0x300, v0\n\t" \
  "v_lshlrev_b32 v17, 2, v19\n\t" \
  "v_and_b32 v19, 0xc0, v0\n\t" \
  "v_lshl_add_u32 v17, v19, 1, v17\n\t" \
  "v_and_b32 v19, 0x30, v0\n\t" \
  "v_lshrrev_b32 v19, 1, v19\n\t" \
  "v_add3_u32 v17, v17, v19, s63\n\t" \
  "v_lshrrev_b32 v19, 1, v0\n\t" \
  "v_and_b32 v19, 64, v19\n\t" \
  "v_add_u32 v17, v17, v19\n\t" \
  "v_xor_b32 v18, 64, v17\n\t" \
  "s_add_u32 m0, s63, 0\n\t" \
  "s_mov_b32 s60, 0\n\t" \
  "buffer_load_dwordx4 v16, %[W], s60 offen lds\n\t" \
  "s_add_u32 m0, s63, 1024\n\t" \
  "s_mov_b32 s60, 65536\n\t" \
  "buffer_load_dwordx4 v16, %[W], s60 offen lds\n\t" \
  "s_add_u32 m0, s63, 2048\n\t" \
  "s_mov_b32 s60, 131072\n\t" \
  "buffer_load_dwordx4 v16, %[W], s60 offen lds\n\t" \
  "s_add_u32 m0, s63, 3072\n\t" \
  "s_mov_b32 s60, 196608\n\t" \
  "buffer_load_dwordx4 v16, %[W], s60 offen lds\n\t" \
  "s_add_u32 m0, s63, 4096\n\t" \
  "s_mov_b32 s60, 262144\n\t" \
  "buffer_load_dwordx4 v16, %[W], s60 offen lds\n\t" \
  "s_add_u32 m0, s63, 5120\n\t" \
  "s_mov_b32 s60, 327680\n\t" \
  "buffer_load_dwordx4 v16, %[W], s60 offen lds\n\t" \
  "s_add_u32 m0, s63, 6144\n\t" \
  "s_mov_b32 s60, 393216\n\t" \
  "buffer_load_dwordx4 v16, %[W], s60 offen lds\n\t" \
  "s_add_u32 m0, s63, 7168\n\t" \
  "s_mov_b32 s60, 458752\n\t" \
  "buffer_load_dwordx4 v16, %[W], s60 offen lds\n\t" \
  "s_add_u32 m0, s63, 8192\n\t" \
  "s_mov_b32 s60, 524288\n\t" \
  "buffer_load_dwordx4 v16, %[W], s60 offen lds\n\t" \
  "s_add_u32 m0, s63, 9216\n\t" \
  "s_mov_b32 s60, 589824\n\t" \
  "buffer_load_dwordx4 v16, %[W], s60 offen lds\n\t" \
  "s_add_u32 m0, s63, 10240\n\t" \
  "s_mov_b32 s60, 655360\n\t" \
  "buffer_load_dwordx4 v16, %[W], s60 offen lds\n\t" \
  "s_add_u32 m0, s63, 11264\n\t" \
  "s_mov_b32 s60, 720896\n\t" \
  "buffer_load_dwordx4 v16, %[W], s60 offen lds\n\t" \
  "s_add_u32 m0, s63, 12288\n\t" \
  "s_mov_b32 s60, 786432\n\t" \
  "buffer_load_dwordx4 v16, %[W], s60 offen lds\n\t" \
  "s_add_u32 m0, s63, 13312\n\t" \
  "s_mov_b32 s60, 851968\n\t" \
  "buffer_load_dwordx4 v16, %[W], s60 offen lds\n\t" \
  "s_add_u32 m0, s63, 14336\n\t" \
  "s_mov_b32 s60, 917504\n\t" \
  "buffer_load_dwordx4 v16, %[W], s60 offen lds\n\t" \
  "s_add_u32 m0, s63, 15360\n\t" \
  "s_mov_b32 s60, 983040\n\t" \
  "buffer_load_dwordx4 v16, %[W], s60 offen lds\n\t" \
  "s_add_u32 m0, s63, 16384\n\t" \
  "s_mov_b32 s60, 1048576\n\t" \
  "buffer_load_dwordx4 v16, %[W], s60 offen lds\n\t" \
  "s_add_u32 m0, s63, 17408\n\t" \
  "s_mov_b32 s60, 1114112\n\t" \
  "buffer_load_dwordx4 v16, %[W], s60 offen lds\n\t" \
  "s_add_u32 m0, s63, 18432\n\t" \
  "s_mov_b32 s60, 1179648\n\t" \
  "buffer_load_dwordx4 v16, %[W], s60 offen lds\n\t" \
  "s_add_u32 m0, s63, 19456\n\t" \
  "s_mov_b32 s60, 1245184\n\t" \
  "buffer_load_dwordx4 v16, %[W], s60 offen lds\n\t" \
  "s_add_u32 m0, s63, 20480\n\t" \
  "s_mov_b32 s60, 1310720\n\t" \
  "buffer_load_dwordx4 v16, %[W], s60 offen lds\n\t" \
  "s_add_u32 m0, s63, 21504\n\t" \
  "s_mov_b32 s60, 1376256\n\t" \
  "buffer_load_dwordx4 v16, %[W], s60 offen lds\n\t" \
  "s_add_u32 m0, s63, 22528\n\t" \
  "s_mov_b32 s60, 1441792\n\t" \
  "buffer_load_dwordx4 v16, %[W], s60 offen lds\n\t" \
  "s_add_u32 m0, s63, 23552\n\t" \
  "s_mov_b32 s60, 1507328\n\t" \
  "buffer_load_dwordx4 v16, %[W], s60 offen lds\n\t" \
  "s_add_u32 m0, s63, 24576\n\t" \
  "s_mov_b32 s60, 1572864\n\t" \
  "buffer_load_dwordx4 v16, %[W], s60 offen lds\n\t" \
  "s_add_u32 m0, s63, 25600\n\t" \
  "s_mov_b32 s60, 1638400\n\t" \
  "buffer_load_dwordx4 v16, %[W], s60 offen lds\n\t" \
  "s_add_u32 m0, s63, 26624\n\t" \
  "s_mov_b32 s60, 1703936\n\t" \
  "buffer_load_dwordx4 v16, %[W], s60 offen lds\n\t" \
  "s_add_u32 m0, s63, 27648\n\t" \
  "s_mov_b32 s60, 1769472\n\t" \
  "buffer_load_dwordx4 v16, %[W], s60 offen lds\n\t" \
  "s_add_u32 m0, s63, 28672\n\t" \
  "s_mov_b32 s60, 1835008\n\t" \
  "buffer_load_dwordx4 v16, %[W], s60 offen lds\n\t" \
  "s_add_u32 m0, s63, 29696\n\t" \
  "s_mov_b32 s60, 1900544\n\t" \
  "buffer_load_dwordx4 v16, %[W], s60 offen lds\n\t" \
  "s_add_u32 m0, s63, 30720\n\t" \
  "s_mov_b32 s60, 1966080\n\t" \
  "buffer_load_dwordx4 v16, %[W], s60 offen lds\n\t" \
  "s_add_u32 m0, s63, 31744\n\t" \
  "s_mov_b32 s60, 2031616\n\t" \
  "buffer_load_dwordx4 v16, %[W], s60 offen lds\n\t" \
  "s_add_u32 m0, s63, 32768\n\t" \
  "s_mov_b32 s60, 2097152\n\t" \
  "buffer_load_dwordx4 v16, %[W], s60 offen lds\n\t" \
  "s_add_u32 m0, s63, 33792\n\t" \
  "s_mov_b32 s60, 2162688\n\t" \
  "buffer_load_dwordx4 v16, %[W], s60 offen lds\n\t" \
  "s_add_u32 m0, s63, 34816\n\t" \
  "s_mov_b32 s60, 2228224\n\t" \
  "buffer_load_dwordx4 v16, %[W], s60 offen lds\n\t" \
  "s_add_u32 m0, s63, 35840\n\t" \
  "s_mov_b32 s60, 2293760\n\t" \
  "buffer_load_dwordx4 v16, %[W], s60 offen lds\n\t" \
  "s_add_u32 m0, s63, 36864\n\t" \
  "s_mov_b32 s60, 2359296\n\t" \
  "buffer_load_dwordx4 v16, %[W], s60 offen lds\n\t" \
  "s_add_u32 m0, s63, 37888\n\t" \
  "s_mov_b32 s60, 2424832\n\t" \
  "buffer_load_dwordx4 v16, %[W], s60 offen lds\n\t" \
  "s_add_u32 m0, s63, 38912\n\t" \
  "s_mov_b32 s60, 2490368\n\t" \
  "buffer_load_dwordx4 v16, %[W], s60 offen lds\n\t" \
  "s_add_u32 m0, s63, 39936\n\t" \
  "s_mov_b32 s60, 2555904\n\t" \
  "buffer_load_dwordx4 v16, %[W], s60 offen lds\n\t" \
  "s_waitcnt vmcnt(32)\n\t" \
  "ds_read_b64_tr_b16 a[0:1], v17 offset:0\n\t" \
  "ds_read_b64_tr_b16 a[2:3], v17 offset:512\n\t" \
  "ds_read_b64_tr_b16 a[4:5], v17 offset:4096\n\t" \
  "ds_read_b64_tr_b16 a[6:7], v17 offset:4608\n\t" \
  "ds_read_b64_tr_b16 a[72:73], v17 offset:32\n\t" \
  "ds_read_b64_tr_b16 a[74:75], v17 offset:544\n\t" \
  "ds_read_b64_tr_b16 a[76:77], v17 offset:4128\n\t" \
  "ds_read_b64_tr_b16 a[78:79], v17 offset:4640\n\t" \
  "ds_read_b64_tr_b16 a[144:145], v18 offset:0\n\t" \
  "ds_read_b64_tr_b16 a[146:147], v18 offset:512\n\t" \
  "ds_read_b64_tr_b16 a[148:149], v18 offset:4096\n\t" \
  "ds_read_b64_tr_b16 a[150:151], v18 offset:4608\n\t" \
  "ds_read_b64_tr_b16 a[216:217], v18 offset:32\n\t" \
  "ds_read_b64_tr_b16 a[218:219], v18 offset:544\n\t" \
  "ds_read_b64_tr_b16 a[220:221], v18 offset:4128\n\t" \
  "ds_read_b64_tr_b16 a[222:223], v18 offset:4640\n\t" \
  "s_waitcnt lgkmcnt(0)\n\t" \
  "s_add_u32 m0, s63, 0\n\t" \
  "s_mov_b32 s60, 2621440\n\t" \
  "buffer_load_dwordx4 v16, %[W], s60 offen lds\n\t" \
  "s_add_u32 m0, s63, 1024\n\t" \
  "s_mov_b32 s60, 2686976\n\t" \
  "buffer_load_dwordx4 v16, %[W], s60 offen lds\n\t" \
  "s_add_u32 m0, s63, 2048\n\t" \
  "s_mov_b32 s60, 2752512\n\t" \
  "buffer_load_dwordx4 v16, %[W], s60 offen lds\n\t" \
  "s_add_u32 m0, s63, 3072\n\t" \
  "s_mov_b32 s60, 2818048\n\t" \
  "buffer_load_dwordx4 v16, %[W], s60 offen lds\n\t" \
  "s_add_u32 m0, s63, 4096\n\t" \
  "s_mov_b32 s60, 2883584\n\t" \
  "buffer_load_dwordx4 v16, %[W], s60 offen lds\n\t" \
  "s_add_u32 m0, s63, 5120\n\t" \
  "s_mov_b32 s60, 2949120\n\t" \
  "buffer_load_dwordx4 v16, %[W], s60 offen lds\n\t" \
  "s_add_u32 m0, s63, 6144\n\t" \
  "s_mov_b32 s60, 3014656\n\t" \
  "buffer_load_dwordx4 v16, %[W], s60 offen lds\n\t" \
  "s_add_u32 m0, s63, 7168\n\t" \
  "s_mov_b32 s60, 3080192\n\t" \
  "buffer_load_dwordx4 v16, %[W], s60 offen lds\n\t" \
  "s_waitcnt vmcnt(32)\n\t" \
  "ds_read_b64_tr_b16 a[8:9], v17 offset:8192\n\t" \
  "ds_read_b64_tr_b16 a[10:11], v17 offset:8704\n\t" \
  "ds_read_b64_tr_b16 a[12:13], v17 offset:12288\n\t" \
  "ds_read_b64_tr_b16 a[14:15], v17 offset:12800\n\t" \
  "ds_read_b64_tr_b16 a[80:81], v17 offset:8224\n\t" \
  "ds_read_b64_tr_b16 a[82:83], v17 offset:8736\n\t" \
  "ds_read_b64_tr_b16 a[84:85], v17 offset:12320\n\t" \
  "ds_read_b64_tr_b16 a[86:87], v17 offset:12832\n\t" \
  "ds_read_b64_tr_b16 a[152:153], v18 offset:8192\n\t" \
  "ds_read_b64_tr_b16 a[154:155], v18 offset:8704\n\t" \
  "ds_read_b64_tr_b16 a[156:157], v18 offset:12288\n\t" \
  "ds_read_b64_tr_b16 a[158:159], v18 offset:12800\n\t" \
  "ds_read_b64_tr_b16 a[224:225], v18 offset:8224\n\t" \
  "ds_read_b64_tr_b16 a[226:227], v18 offset:8736\n\t" \
  "ds_read_b64_tr_b16 a[228:229], v18 offset:12320\n\t" \
  "ds_read_b64_tr_b16 a[230:231], v18 offset:12832\n\t" \
  "s_waitcnt lgkmcnt(0)\n\t" \
  "s_add_u32 m0, s63, 8192\n\t" \
  "s_mov_b32 s60, 3145728\n\t" \
  "buffer_load_dwordx4 v16, %[W], s60 offen lds\n\t" \
  "s_add_u32 m0, s63, 9216\n\t" \
  "s_mov_b32 s60, 3211264\n\t" \
  "buffer_load_dwordx4 v16, %[W], s60 offen lds\n\t" \
  "s_add_u32 m0, s63, 10240\n\t" \
  "s_mov_b32 s60, 3276800\n\t" \
  "buffer_load_dwordx4 v16, %[W], s60 offen lds\n\t" \
  "s_add_u32 m0, s63, 11264\n\t" \
  "s_mov_b32 s60, 3342336\n\t" \
  "buffer_load_dwordx4 v16, %[W], s60 offen lds\n\t" \
  "s_add_u32 m0, s63, 12288\n\t" \
  "s_mov_b32 s60, 3407872\n\t" \
  "buffer_load_dwordx4 v16, %[W], s60 offen lds\n\t" \
  "s_add_u32 m0, s63, 13312\n\t" \
  "s_mov_b32 s60, 3473408\n\t" \
  "buffer_load_dwordx4 v16, %[W], s60 offen lds\n\t" \
  "s_add_u32 m0, s63, 14336\n\t" \
  "s_mov_b32 s60, 3538944\n\t" \
  "buffer_load_dwordx4 v16, %[W], s60 offen lds\n\t" \
  "s_add_u32 m0, s63, 15360\n\t" \
  "s_mov_b32 s60, 3604480\n\t" \
  "buffer_load_dwordx4 v16, %[W], s60 offen lds\n\t" \
  "s_waitcnt vmcnt(32)\n\t" \
  "ds_read_b64_tr_b16 a[16:17], v17 offset:16384\n\t" \
  "ds_read_b64_tr_b16 a[18:19], v17 offset:16896\n\t" \
  "ds_read_b64_tr_b16 a[20:21], v17 offset:20480\n\t" \
  "ds_read_b64_tr_b16 a[22:23], v17 offset:20992\n\t" \
  "ds_read_b64_tr_b16 a[88:89], v17 offset:16416\n\t" \
  "ds_read_b64_tr_b16 a[90:91], v17 offset:16928\n\t" \
  "ds_read_b64_tr_b16 a[92:93], v17 offset:20512\n\t" \
  "ds_read_b64_tr_b16 a[94:95], v17 offset:21024\n\t" \
  "ds_read_b64_tr_b16 a[160:161], v18 offset:16384\n\t" \
  "ds_read_b64_tr_b16 a[162:163], v18 offset:16896\n\t" \
  "ds_read_b64_tr_b16 a[164:165], v18 offset:20480\n\t" \
  "ds_read_b64_tr_b16 a[166:167], v18 offset:20992\n\t" \
  "ds_read_b64_tr_b16 a[232:233], v18 offset:16416\n\t" \
  "ds_read_b64_tr_b16 a[234:235], v18 offset:16928\n\t" \
  "ds_read_b64_tr_b16 a[236:237], v18 offset:20512\n\t" \
  "ds_read_b64_tr_b16 a[238:239], v18 offset:21024\n\t" \
  "s_waitcnt lgkmcnt(0)\n\t" \
  "s_add_u32 m0, s63, 16384\n\t" \
  "s_mov_b32 s60, 3670016\n\t" \
  "buffer_load_dwordx4 v16, %[W], s60 offen lds\n\t" \
  "s_add_u32 m0, s63, 17408\n\t" \
  "s_mov_b32 s60, 3735552\n\t" \
  "buffer_load_dwordx4 v16, %[W], s60 offen lds\n\t" \
  "s_add_u32 m0, s63, 18432\n\t" \
  "s_mov_b32 s60, 3801088\n\t" \
  "buffer_load_dwordx4 v16, %[W], s60 offen lds\n\t" \
  "s_add_u32 m0, s63, 19456\n\t" \
  "s_mov_b32 s60, 3866624\n\t" \
  "buffer_load_dwordx4 v16, %[W], s60 offen lds\n\t" \
  "s_add_u32 m0, s63, 20480\n\t" \
  "s_mov_b32 s60, 3932160\n\t" \
  "buffer_load_dwordx4 v16, %[W], s60 offen lds\n\t" \
  "s_add_u32 m0, s63, 21504\n\t" \
  "s_mov_b32 s60, 3997696\n\t" \
  "buffer_load_dwordx4 v16, %[W], s60 offen lds\n\t" \
  "s_add_u32 m0, s63, 22528\n\t" \
  "s_mov_b32 s60, 4063232\n\t" \
  "buffer_load_dwordx4 v16, %[W], s60 offen lds\n\t" \
  "s_add_u32 m0, s63, 23552\n\t" \
  "s_mov_b32 s60, 4128768\n\t" \
  "buffer_load_dwordx4 v16, %[W], s60 offen lds\n\t" \
  "s_waitcnt vmcnt(32)\n\t" \
  "ds_read_b64_tr_b16 a[24:25], v17 offset:24576\n\t" \
  "ds_read_b64_tr_b16 a[26:27], v17 offset:25088\n\t" \
  "ds_read_b64_tr_b16 a[28:29], v17 offset:28672\n\t" \
  "ds_read_b64_tr_b16 a[30:31], v17 offset:29184\n\t" \
  "ds_read_b64_tr_b16 a[96:97], v17 offset:24608\n\t" \
  "ds_read_b64_tr_b16 a[98:99], v17 offset:25120\n\t" \
  "ds_read_b64_tr_b16 a[100:101], v17 offset:28704\n\t" \
  "ds_read_b64_tr_b16 a[102:103], v17 offset:29216\n\t" \
  "ds_read_b64_tr_b16 a[168:169], v18 offset:24576\n\t" \
  "ds_read_b64_tr_b16 a[170:171], v18 offset:25088\n\t" \
  "ds_read_b64_tr_b16 a[172:173], v18 offset:28672\n\t" \
  "ds_read_b64_tr_b16 a[174:175], v18 offset:29184\n\t" \
  "ds_read_b64_tr_b16 a[240:241], v18 offset:24608\n\t" \
  "ds_read_b64_tr_b16 a[242:243], v18 offset:25120\n\t" \
  "ds_read_b64_tr_b16 a[244:245], v18 offset:28704\n\t" \
  "ds_read_b64_tr_b16 a[246:247], v18 offset:29216\n\t" \
  "s_waitcnt lgkmcnt(0)\n\t" \
  "s_add_u32 m0, s63, 24576\n\t" \
  "s_mov_b32 s60, 4194304\n\t" \
  "buffer_load_dwordx4 v16, %[W], s60 offen lds\n\t" \
  "s_add_u32 m0, s63, 25600\n\t" \
  "s_mov_b32 s60, 4259840\n\t" \
  "buffer_load_dwordx4 v16, %[W], s60 offen lds\n\t" \
  "s_add_u32 m0, s63, 26624\n\t" \
  "s_mov_b32 s60, 4325376\n\t" \
  "buffer_load_dwordx4 v16, %[W], s60 offen lds\n\t" \
  "s_add_u32 m0, s63, 27648\n\t" \
  "s_mov_b32 s60, 4390912\n\t" \
  "buffer_load_dwordx4 v16, %[W], s60 offen lds\n\t" \
  "s_add_u32 m0, s63, 28672\n\t" \
  "s_mov_b32 s60, 4456448\n\t" \
  "buffer_load_dwordx4 v16, %[W], s60 offen lds\n\t" \
  "s_add_u32 m0, s63, 29696\n\t" \
  "s_mov_b32 s60, 4521984\n\t" \
  "buffer_load_dwordx4 v16, %[W], s60 offen lds\n\t" \
  "s_add_u32 m0, s63, 30720\n\t" \
  "s_mov_b32 s60, 4587520\n\t" \
  "buffer_load_dwordx4 v16, %[W], s60 offen lds\n\t" \
  "s_add_u32 m0, s63, 31744\n\t" \
  "s_mov_b32 s60, 4653056\n\t" \
  "buffer_load_dwordx4 v16, %[W], s60 offen lds\n\t" \
  "s_waitcnt vmcnt(32)\n\t" \
  "ds_read_b64_tr_b16 a[32:33], v17 offset:32768\n\t" \
  "ds_read_b64_tr_b16 a[34:35], v17 offset:33280\n\t" \
  "ds_read_b64_tr_b16 a[36:37], v17 offset:36864\n\t" \
  "ds_read_b64_tr_b16 a[38:39], v17 offset:37376\n\t" \
  "ds_read_b64_tr_b16 a[104:105], v17 offset:32800\n\t" \
  "ds_read_b64_tr_b16 a[106:107], v17 offset:33312\n\t" \
  "ds_read_b64_tr_b16 a[108:109], v17 offset:36896\n\t" \
  "ds_read_b64_tr_b16 a[110:111], v17 offset:37408\n\t" \
  "ds_read_b64_tr_b16 a[176:177], v18 offset:32768\n\t" \
  "ds_read_b64_tr_b16 a[178:179], v18 offset:33280\n\t" \
  "ds_read_b64_tr_b16 a[180:181], v18 offset:36864\n\t" \
  "ds_read_b64_tr_b16 a[182:183], v18 offset:37376\n\t" \
  "ds_read_b64_tr_b16 a[248:249], v18 offset:32800\n\t" \
  "ds_read_b64_tr_b16 a[250:251], v18 offset:33312\n\t" \
  "ds_read_b64_tr_b16 a[252:253], v18 offset:36896\n\t" \
  "ds_read_b64_tr_b16 a[254:255], v18 offset:37408\n\t" \
  "s_waitcnt lgkmcnt(0)\n\t" \
  "s_waitcnt vmcnt(24)\n\t" \
  "ds_read_b64_tr_b16 a[40:41], v17 offset:0\n\t" \
  "ds_read_b64_tr_b16 a[42:43], v17 offset:512\n\t" \
  "ds_read_b64_tr_b16 a[44:45], v17 offset:4096\n\t" \
  "ds_read_b64_tr_b16 a[46:47], v17 offset:4608\n\t" \
  "ds_read_b64_tr_b16 a[112:113], v17 offset:32\n\t" \
  "ds_read_b64_tr_b16 a[114:115], v17 offset:544\n\t" \
  "ds_read_b64_tr_b16 a[116:117], v17 offset:4128\n\t" \
  "ds_read_b64_tr_b16 a[118:119], v17 offset:4640\n\t" \
  "ds_read_b64_tr_b16 a[184:185], v18 offset:0\n\t" \
  "ds_read_b64_tr_b16 a[186:187], v18 offset:512\n\t" \
  "ds_read_b64_tr_b16 a[188:189], v18 offset:4096\n\t" \
  "ds_read_b64_tr_b16 a[190:191], v18 offset:4608\n\t" \
  "ds_read_b64_tr_b16 v[224:225], v18 offset:32\n\t" \
  "ds_read_b64_tr_b16 v[226:227], v18 offset:544\n\t" \
  "ds_read_b64_tr_b16 v[228:229], v18 offset:4128\n\t" \
  "ds_read_b64_tr_b16 v[230:231], v18 offset:4640\n\t" \
  "s_waitcnt lgkmcnt(0)\n\t" \
  "s_waitcnt vmcnt(16)\n\t" \
  "ds_read_b64_tr_b16 a[48:49], v17 offset:8192\n\t" \
  "ds_read_b64_tr_b16 a[50:51], v17 offset:8704\n\t" \
  "ds_read_b64_tr_b16 a[52:53], v17 offset:12288\n\t" \
  "ds_read_b64_tr_b16 a[54:55], v17 offset:12800\n\t" \
  "ds_read_b64_tr_b16 a[120:121], v17 offset:8224\n\t" \
  "ds_read_b64_tr_b16 a[122:123], v17 offset:8736\n\t" \
  "ds_read_b64_tr_b16 a[124:125], v17 offset:12320\n\t" \
  "ds_read_b64_tr_b16 a[126:127], v17 offset:12832\n\t" \
  "ds_read_b64_tr_b16 a[192:193], v18 offset:8192\n\t" \
  "ds_read_b64_tr_b16 a[194:195], v18 offset:8704\n\t" \
  "ds_read_b64_tr_b16 a[196:197], v18 offset:12288\n\t" \
  "ds_read_b64_tr_b16 a[198:199], v18 offset:12800\n\t" \
  "ds_read_b64_tr_b16 v[232:233], v18 offset:8224\n\t" \
  "ds_read_b64_tr_b16 v[234:235], v18 offset:8736\n\t" \
  "ds_read_b64_tr_b16 v[236:237], v18 offset:12320\n\t" \
  "ds_read_b64_tr_b16 v[238:239], v18 offset:12832\n\t" \
  "s_waitcnt lgkmcnt(0)\n\t" \
  "s_waitcnt vmcnt(8)\n\t" \
  "ds_read_b64_tr_b16 a[56:57], v17 offset:16384\n\t" \
  "ds_read_b64_tr_b16 a[58:59], v17 offset:16896\n\t" \
  "ds_read_b64_tr_b16 a[60:61], v17 offset:20480\n\t" \
  "ds_read_b64_tr_b16 a[62:63], v17 offset:20992\n\t" \
  "ds_read_b64_tr_b16 a[128:129], v17 offset:16416\n\t" \
  "ds_read_b64_tr_b16 a[130:131], v17 offset:16928\n\t" \
  "ds_read_b64_tr_b16 a[132:133], v17 offset:20512\n\t" \
  "ds_read_b64_tr_b16 a[134:135], v17 offset:21024\n\t" \
  "ds_read_b64_tr_b16 a[200:201], v18 offset:16384\n\t" \
  "ds_read_b64_tr_b16 a[202:203], v18 offset:16896\n\t" \
  "ds_read_b64_tr_b16 a[204:205], v18 offset:20480\n\t" \
  "ds_read_b64_tr_b16 a[206:207], v18 offset:20992\n\t" \
  "ds_read_b64_tr_b16 v[240:241], v18 offset:16416\n\t" \
  "ds_read_b64_tr_b16 v[242:243], v18 offset:16928\n\t" \
  "ds_read_b64_tr_b16 v[244:245], v18 offset:20512\n\t" \
  "ds_read_b64_tr_b16 v[246:247], v18 offset:21024\n\t" \
  "s_waitcnt lgkmcnt(0)\n\t" \
  "s_waitcnt vmcnt(0)\n\t" \
  "ds_read_b64_tr_b16 a[64:65], v17 offset:24576\n\t" \
  "ds_read_b64_tr_b16 a[66:67], v17 offset:25088\n\t" \
  "ds_read_b64_tr_b16 a[68:69], v17 offset:28672\n\t" \
  "ds_read_b64_tr_b16 a[70:71], v17 offset:29184\n\t" \
  "ds_read_b64_tr_b16 a[136:137], v17 offset:24608\n\t" \
  "ds_read_b64_tr_b16 a[138:139], v17 offset:25120\n\t" \
  "ds_read_b64_tr_b16 a[140:141], v17 offset:28704\n\t" \
  "ds_read_b64_tr_b16 a[142:143], v17 offset:29216\n\t" \
  "ds_read_b64_tr_b16 a[208:209], v18 offset:24576\n\t" \
  "ds_read_b64_tr_b16 a[210:211], v18 offset:25088\n\t" \
  "ds_read_b64_tr_b16 a[212:213], v18 offset:28672\n\t" \
  "ds_read_b64_tr_b16 a[214:215], v18 offset:29184\n\t" \
  "ds_read_b64_tr_b16 v[248:249], v18 offset:24608\n\t" \
  "ds_read_b64_tr_b16 v[250:251], v18 offset:25120\n\t" \
  "ds_read_b64_tr_b16 v[252:253], v18 offset:28704\n\t" \
  "ds_read_b64_tr_b16 v[254:255], v18 offset:29216\n\t" \
  "s_waitcnt lgkmcnt(0)\n\t" \
  "s_mov_b32 s84, " TK_CAP "\n\t" \
  "s_mov_b32 s48, 0\n\t" \
  "s_mov_b32 s49, 0xf0000000\n\t" \
  "s_mov_b32 s46, s41\n\t" \
  "s_mov_b32 s56, 0\n\t" \
  "v_lshrrev_b32 v7, 2, v0\n\t" \
  "s_mov_b32 s60, " TK_CTAB "\n\t" \
  "buffer_load_dword v4, v7, %[X], s60 offen offset:88 sc1\n\t" \
  "buffer_load_dword v7, v7, %[X], s60 offen offset:0 sc1\n\t" \
  "s_waitcnt vmcnt(0)\n\t" \
  "v_and_b32 v4, 1, v4\n\t" \
  "v_and_b32 v7, 1, v7\n\t" \
  "v_cmp_ne_u32 s[90:91], s67, v4\n\t" \
  "v_cmp_ne_u32 s[88:89], s67, v7\n\t" \
  "s_nop 1\n\t" \
  "s_mov_b32 s57, 0xf0000000\n\t" \
  "s_add_u32 s83, s48, 0\n\t" \
  "s_lshr_b64 s[60:61], s[90:91], s83\n\t" \
  "s_and_b32 s60, s60, 1\n\t" \
  "s_lshl_b32 s60, s60, 13\n\t" \
  "s_add_u32 s83, s83, " TK_SG0 "\n\t" \
  "s_lshl_b32 s83, s83, 21\n\t" \
  "s_or_b32 s83, s83, s60\n\t" \
  "s_add_u32 s62, s83, s39\n\t" \
  "v_add_u32 v7, s62, v3\n\t" \
  "LCOLD_%=:\n\t" \
  "s_waitcnt vmcnt(0)\n\t" \
  "s_mov_b64 exec, 1\n\t" \
  "buffer_store_dword v6, off, %[X], s49 sc1\n\t" \
  "s_mov_b64 exec, -1\n\t" \
  "s_mov_b32 s49, 0xf0000000\n\t" \
  "s_add_u32 s55, s83, s36\n\t" \
  "s_cmp_lg_u32 s40, 0\n\t" \
  "s_cbranch_scc1 LGO_%=\n\t" \
  "s_and_b32 s62, s45, 3\n\t" \
  "s_lshl_b32 s62, s62, 7\n\t" \
  "s_add_u32 s62, s62, s83\n\t" \
  "s_add_u32 s62, s62, s39\n\t" \
  "s_mov_b32 s61, 0\n\t" \
  "LPO_%=:\n\t" \
  "buffer_load_dword v4, off, %[X], s62 sc1\n\t" \
  "s_waitcnt vmcnt(0)\n\t" \
  "v_cmp_eq_u32 vcc, 0, v4\n\t" \
  "s_cbranch_vccz LPO_%=x\n\t" \
  "s_add_u32 s61, s61, 1\n\t" \
  "s_sleep " TK_PSH "\n\t" \
  "s_cmp_gt_u32 s61, s84\n\t" \
  "s_cbranch_scc1 LPO_%=g\n\t" \
  "s_and_b32 s60, s61, 1023\n\t" \
  "s_cmp_lg_u32 s60, 0\n\t" \
  "s_cbranch_scc1 LPO_%=\n\t" \
  "buffer_load_dword v4, off, %[X], s92 sc1\n\t" \
  "s_waitcnt vmcnt(0)\n\t" \
  "v_readfirstlane_b32 s60, v4\n\t" \
  "s_nop 0\n\t" \
  "s_cmp_eq_u32 s60, 0\n\t" \
  "s_cbranch_scc1 LPO_%=\n\t" \
  "LPO_%=g:\n\t" \
  "s_mov_b32 s84, 0\n\t" \
  "s_or_b32 s60, s48, 1610612736\n\t" \
  "v_mov_b32 v4, s60\n\t" \
  "s_mov_b64 exec, 1\n\t" \
  "buffer_store_dword v4, off, %[X], s92 sc1\n\t" \
  "s_mov_b64 exec, -1\n\t" \
  "LPO_%=x:\n\t" \
  "s_mov_b32 s61, 0\n\t" \
  "LPH_%=:\n\t" \
  "buffer_load_dword v4, v7, %[X], 0 offen sc1\n\t" \
  "s_waitcnt vmcnt(0)\n\t" \
  "v_cmp_eq_u32 vcc, 0, v4\n\t" \
  "s_cbranch_vccz LPH_%=x\n\t" \
  "s_add_u32 s61, s61, 1\n\t" \
  "s_sleep " TK_PSH "\n\t" \
  "s_cmp_gt_u32 s61, s84\n\t" \
  "s_cbranch_scc1 LPH_%=g\n\t" \
  "s_and_b32 s60, s61, 1023\n\t" \
  "s_cmp_lg_u32 s60, 0\n\t" \
  "s_cbranch_scc1 LPH_%=\n\t" \
  "buffer_load_dword v4, off, %[X], s92 sc1\n\t" \
  "s_waitcnt vmcnt(0)\n\t" \
  "v_readfirstlane_b32 s60, v4\n\t" \
  "s_nop 0\n\t" \
  "s_cmp_eq_u32 s60, 0\n\t" \
  "s_cbranch_scc1 LPH_%=\n\t" \
  "LPH_%=g:\n\t" \
  "s_mov_b32 s84, 0\n\t" \
  "s_or_b32 s60, s48, 1610612736\n\t" \
  "v_mov_b32 v4, s60\n\t" \
  "s_mov_b64 exec, 1\n\t" \
  "buffer_store_dword v4, off, %[X], s92 sc1\n\t" \
  "s_mov_b64 exec, -1\n\t" \
  "LPH_%=x:\n\t" \
  "s_cmp_lt_i32 s57, 0\n\t" \
  "s_cbranch_scc1 LGO_%=\n\t" \
  "s_movk_i32 s63, 4000\n\t" \
  "s_branch LGC_%=u\n\t" \
  "LGC_%=:\n\t" \
  "s_sleep " TK_PSG "\n\t" \
  "buffer_load_dword v8, off, %[X], s57 sc1\n\t" \
  "s_waitcnt vmcnt(0)\n\t" \
  "LGC_%=u:\n\t" \
  "s_memrealtime s[52:53]\n\t" \
  "v_readfirstlane_b32 s60, v8\n\t" \
  "s_waitcnt lgkmcnt(0)\n\t" \
  "s_sub_u32 s63, s63, 1\n\t" \
  "s_cbranch_scc1 LGO_%=\n\t" \
  "s_cmp_eq_u32 s60, -1\n\t" \
  "s_cbranch_scc1 LGC_%=\n\t" \
  "s_not_b32 s60, s60\n\t" \
  "s_lshl_b32 s61, s52, 8\n\t" \
  "s_sub_u32 s60, s60, s61\n\t" \
  "s_ashr_i32 s60, s60, 8\n\t" \
  "s_sub_i32 s60, s60, " TK_QXC "\n\t" \
  "s_min_i32 s60, s60, 400\n\t" \
  "s_add_u32 s54, s52, s60\n\t" \
  "LGC_%=w:\n\t" \
  "s_sleep 1\n\t" \
  "s_memrealtime s[52:53]\n\t" \
  "s_waitcnt lgkmcnt(0)\n\t" \
  "s_sub_i32 s53, s52, s54\n\t" \
  "s_cmp_lt_i32 s53, 0\n\t" \
  "s_cbranch_scc1 LGC_%=w\n\t" \
  "LGO_%=:\n\t" \
  "s_barrier\n\t" \
  "s_add_u32 m0, s46, 0\n\t" \
  "s_add_u32 s47, s55, 0\n\t" \
  "buffer_load_dwordx4 v0, %[X], s47 offen sc1 lds\n\t" \
  "s_add_u32 m0, s46, 4096\n\t" \
  "s_add_u32 s47, s55, 4096\n\t" \
  "buffer_load_dwordx4 v0, %[X], s47 offen sc1 lds\n\t" \
  "s_add_u32 m0, s46, 8192\n\t" \
  "s_add_u32 s47, s55, 16384\n\t" \
  "buffer_load_dwordx4 v0, %[X], s47 offen sc1 lds\n\t" \
  "s_add_u32 m0, s46, 12288\n\t" \
  "s_add_u32 s47, s55, 20480\n\t" \
  "buffer_load_dwordx4 v0, %[X], s47 offen sc1 lds\n\t" \
  "s_add_u32 m0, s46, 16384\n\t" \
  "s_add_u32 s47, s55, 32768\n\t" \
  "buffer_load_dwordx4 v0, %[X], s47 offen sc1 lds\n\t" \
  "s_add_u32 m0, s46, 20480\n\t" \
  "s_add_u32 s47, s55, 36864\n\t" \
  "buffer_load_dwordx4 v0, %[X], s47 offen sc1 lds\n\t" \
  "s_add_u32 m0, s46, 24576\n\t" \
  "s_add_u32 s47, s55, 49152\n\t" \
  "buffer_load_dwordx4 v0, %[X], s47 offen sc1 lds\n\t" \
  "s_add_u32 m0, s46, 28672\n\t" \
  "s_add_u32 s47, s55, 53248\n\t" \
  "buffer_load_dwordx4 v0, %[X], s47 offen sc1 lds\n\t" \
  "s_add_u32 m0, s46, 32768\n\t" \
  "s_add_u32 s47, s55, 65536\n\t" \
  "buffer_load_dwordx4 v0, %[X], s47 offen sc1 lds\n\t" \
  "s_waitcnt vmcnt(5)\n\t" \
  "LTOP_%=:\n\t" \
  "s_barrier\n\t" \
  TK_PQ1 \
  "ds_read_b128 v[64:67], v1 offset:0\n\t" \
  "ds_read_b128 v[68:71], v1 offset:2048\n\t" \
  "ds_read_b128 v[72:75], v2 offset:0\n\t" \
  "ds_read_b128 v[76:79], v2 offset:2048\n\t" \
  "ds_read_b128 v[80:83], v1 offset:4096\n\t" \
  "ds_read_b128 v[84:87], v1 offset:6144\n\t" \
  "s_mov_b32 s82, s83\n\t" \
  "s_xor_b32 s46, s46, 0x10000\n\t" \
  "s_waitcnt lgkmcnt(5)\n\t" \
  "v_mfma_f32_16x16x32_bf16 v[16:19], v[64:67], a[0:3], 0\n\t" \
  "ds_read_b128 v[88:91], v2 offset:4096\n\t" \
  "s_waitcnt lgkmcnt(5)\n\t" \
  "v_mfma_f32_16x16x32_bf16 v[20:23], v[68:71], a[0:3], 0\n\t" \
  "s_add_u32 s83, s48, 1\n\t" \
  "s_lshr_b64 s[60:61], s[90:91], s83\n\t" \
  "v_mfma_f32_16x16x32_bf16 v[24:27], v[64:67], a[72:75], 0\n\t" \
  "ds_read_b128 v[92:95], v2 offset:6144\n\t" \
  "v_mfma_f32_16x16x32_bf16 v[28:31], v[68:71], a[72:75], 0\n\t" \
  "s_and_b32 s60, s60, 1\n\t" \
  "s_lshl_b32 s60, s60, 13\n\t" \
  "s_waitcnt lgkmcnt(5)\n\t" \
  "v_mfma_f32_16x16x32_bf16 v[16:19], v[72:75], a[4:7], v[16:19]\n\t" \
  "ds_read_b128 v[96:99], v1 offset:8192\n\t" \
  "s_waitcnt lgkmcnt(5)\n\t" \
  "v_mfma_f32_16x16x32_bf16 v[20:23], v[76:79], a[4:7], v[20:23]\n\t" \
  "s_add_u32 s83, s83, " TK_SG0 "\n\t" \
  "s_lshl_b32 s83, s83, 21\n\t" \
  "v_mfma_f32_16x16x32_bf16 v[24:27], v[72:75], a[76:79], v[24:27]\n\t" \
  "ds_read_b128 v[100:103], v1 offset:10240\n\t" \
  "v_mfma_f32_16x16x32_bf16 v[28:31], v[76:79], a[76:79], v[28:31]\n\t" \
  "s_or_b32 s83, s83, s60\n\t" \
  "s_add_u32 s62, s83, s39\n\t" \
  "s_waitcnt lgkmcnt(5)\n\t" \
  "v_mfma_f32_16x16x32_bf16 v[16:19], v[80:83], a[8:11], v[16:19]\n\t" \
  "ds_read_b128 v[104:107], v2 offset:8192\n\t" \
  "s_waitcnt lgkmcnt(5)\n\t" \
  "v_mfma_f32_16x16x32_bf16 v[20:23], v[84:87], a[8:11], v[20:23]\n\t" \
  "v_add_u32 v7, s62, v3\n\t" \
  "s_add_u32 s50, s82, s37\n\t" \
  "v_mfma_f32_16x16x32_bf16 v[24:27], v[80:83], a[80:83], v[24:27]\n\t" \
  "ds_read_b128 v[108:111], v2 offset:10240\n\t" \
  "v_mfma_f32_16x16x32_bf16 v[28:31], v[84:87], a[80:83], v[28:31]\n\t" \
  "s_add_u32 s51, s50, 4096\n\t" \
  "s_waitcnt lgkmcnt(5)\n\t" \
  "v_mfma_f32_16x16x32_bf16 v[16:19], v[88:91], a[12:15], v[16:19]\n\t" \
  "ds_read_b128 v[112:115], v1 offset:12288\n\t" \
  "s_waitcnt lgkmcnt(5)\n\t" \
  "v_mfma_f32_16x16x32_bf16 v[20:23], v[92:95], a[12:15], v[20:23]\n\t" \
  "v_mfma_f32_16x16x32_bf16 v[24:27], v[88:91], a[84:87], v[24:27]\n\t" \
  "ds_read_b128 v[116:119], v1 offset:14336\n\t" \
  "v_mfma_f32_16x16x32_bf16 v[28:31], v[92:95], a[84:87], v[28:31]\n\t" \
  "s_waitcnt lgkmcnt(5)\n\t" \
  "v_mfma_f32_16x16x32_bf16 v[16:19], v[96:99], a[16:19], v[16:19]\n\t" \
  "ds_read_b128 v[120:123], v2 offset:12288\n\t" \
  "s_waitcnt lgkmcnt(5)\n\t" \
  "v_mfma_f32_16x16x32_bf16 v[20:23], v[100:103], a[16:19], v[20:23]\n\t" \
  "s_cmp_lg_u32 s56, 0\n\t" \
  "v_mfma_f32_16x16x32_bf16 v[24:27], v[96:99], a[88:91], v[24:27]\n\t" \
  "ds_read_b128 v[124:127], v2 offset:14336\n\t" \
  "v_mfma_f32_16x16x32_bf16 v[28:31], v[100:103], a[88:91], v[28:31]\n\t" \
  "s_cbranch_scc1 LWARM_%=\n\t" \
  "s_waitcnt vmcnt(0)\n\t" \
  "LWARM_%=:\n\t" \
  "s_barrier\n\t" \
  "s_waitcnt lgkmcnt(5)\n\t" \
  "v_mfma_f32_16x16x32_bf16 v[16:19], v[104:107], a[20:23], v[16:19]\n\t" \
  "ds_read_b128 v[128:131], v1 offset:16384\n\t" \
  "s_waitcnt lgkmcnt(5)\n\t" \
  "v_mfma_f32_16x16x32_bf16 v[20:23], v[108:111], a[20:23], v[20:23]\n\t" \
  "v_mfma_f32_16x16x32_bf16 v[24:27], v[104:107], a[92:95], v[24:27]\n\t" \
  "ds_read_b128 v[132:135], v1 offset:18432\n\t" \
  "v_mfma_f32_16x16x32_bf16 v[28:31], v[108:111], a[92:95], v[28:31]\n\t" \
  "s_add_i32 s62, s48, s42\n\t" \
  "s_add_i32 s62, s62, 1\n\t" \
  "s_waitcnt lgkmcnt(5)\n\t" \
  "v_mfma_f32_16x16x32_bf16 v[16:19], v[112:115], a[24:27], v[16:19]\n\t" \
  "ds_read_b128 v[136:139], v2 offset:16384\n\t" \
  "s_waitcnt lgkmcnt(5)\n\t" \
  "v_mfma_f32_16x16x32_bf16 v[20:23], v[116:119], a[24:27], v[20:23]\n\t" \
  "s_mul_i32 s63, s62, 43\n\t" \
  "s_lshr_b32 s63, s63, 7\n\t" \
  "v_mfma_f32_16x16x32_bf16 v[24:27], v[112:115], a[96:99], v[24:27]\n\t" \
  "ds_read_b128 v[140:143], v2 offset:18432\n\t" \
  "v_mfma_f32_16x16x32_bf16 v[28:31], v[116:119], a[96:99], v[28:31]\n\t" \
  "s_mul_i32 s57, s62, 688128\n\t" \
  "s_lshl_b32 s60, s63, 15\n\t" \
  "s_waitcnt lgkmcnt(5)\n\t" \
  "v_mfma_f32_16x16x32_bf16 v[16:19], v[120:123], a[28:31], v[16:19]\n\t" \
  "ds_read_b128 v[144:147], v1 offset:20480\n\t" \
  "s_waitcnt lgkmcnt(5)\n\t" \
  "v_mfma_f32_16x16x32_bf16 v[20:23], v[124:127], a[28:31], v[20:23]\n\t" \
  "s_add_u32 s57, s57, s60\n\t" \
  "s_lshr_b32 s60, s88, s63\n\t" \
  "v_mfma_f32_16x16x32_bf16 v[24:27], v[120:123], a[100:103], v[24:27]\n\t" \
  "ds_read_b128 v[148:151], v1 offset:22528\n\t" \
  "v_mfma_f32_16x16x32_bf16 v[28:31], v[124:127], a[100:103], v[28:31]\n\t" \
  "s_and_b32 s60, s60, 1\n\t" \
  "s_lshl_b32 s60, s60, 13\n\t" \
  "s_waitcnt lgkmcnt(5)\n\t" \
  "v_mfma_f32_16x16x32_bf16 v[16:19], v[128:131], a[32:35], v[16:19]\n\t" \
  "ds_read_b128 v[152:155], v2 offset:20480\n\t" \
  "s_waitcnt lgkmcnt(5)\n\t" \
  "v_mfma_f32_16x16x32_bf16 v[20:23], v[132:135], a[32:35], v[20:23]\n\t" \
  "s_add_u32 s57, s57, s60\n\t" \
  "s_add_u32 s57, s57, 656896\n\t" \
  "v_mfma_f32_16x16x32_bf16 v[24:27], v[128:131], a[104:107], v[24:27]\n\t" \
  "ds_read_b128 v[156:159], v2 offset:22528\n\t" \
  "v_mfma_f32_16x16x32_bf16 v[28:31], v[132:135], a[104:107], v[28:31]\n\t" \
  "s_cmp_lt_i32 s62, 1\n\t" \
  "s_cselect_b32 s57, 0xf0000000, s57\n\t" \
  "s_waitcnt lgkmcnt(5)\n\t" \
  "v_mfma_f32_16x16x32_bf16 v[16:19], v[136:139], a[36:39], v[16:19]\n\t" \
  "ds_read_b128 v[160:163], v1 offset:24576\n\t" \
  "s_waitcnt lgkmcnt(5)\n\t" \
  "v_mfma_f32_16x16x32_bf16 v[20:23], v[140:143], a[36:39], v[20:23]\n\t" \
  "s_cmp_lg_u32 s40, 0\n\t" \
  "s_cselect_b32 s57, 0xf0000000, s57\n\t" \
  "v_mfma_f32_16x16x32_bf16 v[24:27], v[136:139], a[108:111], v[24:27]\n\t" \
  "ds_read_b128 v[164:167], v1 offset:26624\n\t" \
  "v_mfma_f32_16x16x32_bf16 v[28:31], v[140:143], a[108:111], v[28:31]\n\t" \
  "s_waitcnt lgkmcnt(5)\n\t" \
  "v_mfma_f32_16x16x32_bf16 v[16:19], v[144:147], a[40:43], v[16:19]\n\t" \
  "ds_read_b128 v[168:171], v2 offset:24576\n\t" \
  "s_waitcnt lgkmcnt(5)\n\t" \
  "v_mfma_f32_16x16x32_bf16 v[20:23], v[148:151], a[40:43], v[20:23]\n\t" \
  "v_mfma_f32_16x16x32_bf16 v[24:27], v[144:147], a[112:115], v[24:27]\n\t" \
  "ds_read_b128 v[172:175], v2 offset:26624\n\t" \
  "v_mfma_f32_16x16x32_bf16 v[28:31], v[148:151], a[112:115], v[28:31]\n\t" \
  "s_waitcnt lgkmcnt(5)\n\t" \
  "v_mfma_f32_16x16x32_bf16 v[16:19], v[152:155], a[44:47], v[16:19]\n\t" \
  "ds_read_b128 v[176:179], v1 offset:28672\n\t" \
  "s_waitcnt lgkmcnt(5)\n\t" \
  "v_mfma_f32_16x16x32_bf16 v[20:23], v[156:159], a[44:47], v[20:23]\n\t" \
  "v_mfma_f32_16x16x32_bf16 v[24:27], v[152:155], a[116:119], v[24:27]\n\t" \
  "ds_read_b128 v[180:183], v1 offset:30720\n\t" \
  "v_mfma_f32_16x16x32_bf16 v[28:31], v[156:159], a[116:119], v[28:31]\n\t" \
  "s_waitcnt lgkmcnt(5)\n\t" \
  "v_mfma_f32_16x16x32_bf16 v[16:19], v[160:163], a[48:51], v[16:19]\n\t" \
  "ds_read_b128 v[184:187], v2 offset:28672\n\t" \
  "s_waitcnt lgkmcnt(5)\n\t" \
  "v_mfma_f32_16x16x32_bf16 v[20:23], v[164:167], a[48:51], v[20:23]\n\t" \
  TK_PQ0 \
  "v_mfma_f32_16x16x32_bf16 v[24:27], v[160:163], a[120:123], v[24:27]\n\t" \
  "ds_read_b128 v[188:191], v2 offset:30720\n\t" \
  "v_mfma_f32_16x16x32_bf16 v[28:31], v[164:167], a[120:123], v[28:31]\n\t" \
  "s_waitcnt lgkmcnt(5)\n\t" \
  "v_mfma_f32_16x16x32_bf16 v[16:19], v[168:171], a[52:55], v[16:19]\n\t" \
  "ds_read_b128 v[192:195], v1 offset:32768\n\t" \
  "s_waitcnt lgkmcnt(5)\n\t" \
  "v_mfma_f32_16x16x32_bf16 v[20:23], v[172:175], a[52:55], v[20:23]\n\t" \
  "v_mfma_f32_16x16x32_bf16 v[24:27], v[168:171], a[124:127], v[24:27]\n\t" \
  "ds_read_b128 v[196:199], v1 offset:34816\n\t" \
  "v_mfma_f32_16x16x32_bf16 v[28:31], v[172:175], a[124:127], v[28:31]\n\t" \
  "s_waitcnt lgkmcnt(5)\n\t" \
  "v_mfma_f32_16x16x32_bf16 v[16:19], v[176:179], a[56:59], v[16:19]\n\t" \
  "ds_read_b128 v[200:203], v2 offset:32768\n\t" \
  "s_waitcnt lgkmcnt(5)\n\t" \
  "v_mfma_f32_16x16x32_bf16 v[20:23], v[180:183], a[56:59], v[20:23]\n\t" \
  "v_mfma_f32_16x16x32_bf16 v[24:27], v[176:179], a[128:131], v[24:27]\n\t" \
  "ds_read_b128 v[204:207], v2 offset:34816\n\t" \
  "v_mfma_f32_16x16x32_bf16 v[28:31], v[180:183], a[128:131], v[28:31]\n\t" \
  "s_waitcnt lgkmcnt(5)\n\t" \
  "v_mfma_f32_16x16x32_bf16 v[16:19], v[184:187], a[60:63], v[16:19]\n\t" \
  "s_waitcnt lgkmcnt(4)\n\t" \
  "v_mfma_f32_16x16x32_bf16 v[20:23], v[188:191], a[60:63], v[20:23]\n\t" \
  "buffer_load_dword v4, v7, %[X], 0 offen sc1\n\t" \
  "v_mfma_f32_16x16x32_bf16 v[24:27], v[184:187], a[132:135], v[24:27]\n\t" \
  "v_mfma_f32_16x16x32_bf16 v[28:31], v[188:191], a[132:135], v[28:31]\n\t" \
  "s_waitcnt lgkmcnt(3)\n\t" \
  "v_mfma_f32_16x16x32_bf16 v[16:19], v[192:195], a[64:67], v[16:19]\n\t" \
  "v_xor_b32 v1, 0x10000, v1\n\t" \
  "v_xor_b32 v2, 0x10000, v2\n\t" \
  "s_waitcnt lgkmcnt(2)\n\t" \
  "v_mfma_f32_16x16x32_bf16 v[20:23], v[196:199], a[64:67], v[20:23]\n\t" \
  "v_mfma_f32_16x16x32_bf16 v[24:27], v[192:195], a[136:139], v[24:27]\n\t" \
  "v_mfma_f32_16x16x32_bf16 v[28:31], v[196:199], a[136:139], v[28:31]\n\t" \
  "s_waitcnt lgkmcnt(1)\n\t" \
  "v_mfma_f32_16x16x32_bf16 v[16:19], v[200:203], a[68:71], v[16:19]\n\t" \
  "s_waitcnt lgkmcnt(0)\n\t" \
  "v_mfma_f32_16x16x32_bf16 v[20:23], v[204:207], a[68:71], v[20:23]\n\t" \
  "v_mfma_f32_16x16x32_bf16 v[24:27], v[200:203], a[140:143], v[24:27]\n\t" \
  "v_mfma_f32_16x16x32_bf16 v[28:31], v[204:207], a[140:143], v[28:31]\n\t" \
  "v_mfma_f32_16x16x32_bf16 v[32:35], v[64:67], a[144:147], 0\n\t" \
  "v_cvt_pk_bf16_f32 v48, v16, v17\n\t" \
  "v_cvt_pk_bf16_f32 v49, v18, v19\n\t" \
  "v_mfma_f32_16x16x32_bf16 v[36:39], v[68:71], a[144:147], 0\n\t" \
  "v_cvt_pk_bf16_f32 v50, v20, v21\n\t" \
  "v_cvt_pk_bf16_f32 v51, v22, v23\n\t" \
  "v_mfma_f32_16x16x32_bf16 v[32:35], v[72:75], a[148:151], v[32:35]\n\t" \
  "v_cvt_pk_bf16_f32 v52, v24, v25\n\t" \
  "v_cvt_pk_bf16_f32 v53, v26, v27\n\t" \
  "v_mfma_f32_16x16x32_bf16 v[36:39], v[76:79], a[148:151], v[36:39]\n\t" \
  "v_cvt_pk_bf16_f32 v54, v28, v29\n\t" \
  "v_cvt_pk_bf16_f32 v55, v30, v31\n\t" \
  "v_mfma_f32_16x16x32_bf16 v[32:35], v[80:83], a[152:155], v[32:35]\n\t" \
  "buffer_store_dwordx4 v[48:51], v0, %[X], s50 offen offset:0 sc1\n\t" \
  "v_mfma_f32_16x16x32_bf16 v[36:39], v[84:87], a[152:155], v[36:39]\n\t" \
  "v_mfma_f32_16x16x32_bf16 v[32:35], v[88:91], a[156:159], v[32:35]\n\t" \
  "v_mfma_f32_16x16x32_bf16 v[36:39], v[92:95], a[156:159], v[36:39]\n\t" \
  "v_mfma_f32_16x16x32_bf16 v[32:35], v[96:99], a[160:163], v[32:35]\n\t" \
  "v_mfma_f32_16x16x32_bf16 v[36:39], v[100:103], a[160:163], v[36:39]\n\t" \
  "v_mfma_f32_16x16x32_bf16 v[32:35], v[104:107], a[164:167], v[32:35]\n\t" \
  "v_mfma_f32_16x16x32_bf16 v[36:39], v[108:111], a[164:167], v[36:39]\n\t" \
  "v_mfma_f32_16x16x32_bf16 v[32:35], v[112:115], a[168:171], v[32:35]\n\t" \
  "v_mfma_f32_16x16x32_bf16 v[36:39], v[116:119], a[168:171], v[36:39]\n\t" \
  "v_mfma_f32_16x16x32_bf16 v[32:35], v[120:123], a[172:175], v[32:35]\n\t" \
  "v_mfma_f32_16x16x32_bf16 v[36:39], v[124:127], a[172:175], v[36:39]\n\t" \
  "s_waitcnt vmcnt(1)\n\t" \
  "v_cmp_eq_u32 vcc, 0, v4\n\t" \
  "v_mfma_f32_16x16x32_bf16 v[32:35], v[128:131], a[176:179], v[32:35]\n\t" \
  "v_mfma_f32_16x16x32_bf16 v[36:39], v[132:135], a[176:179], v[36:39]\n\t" \
  "s_cmp_eq_u64 vcc, 0\n\t" \
  "s_cselect_b32 s60, 1, 0\n\t" \
  "v_mov_b32 v4, s60\n\t" \
  "v_mfma_f32_16x16x32_bf16 v[32:35], v[136:139], a[180:183], v[32:35]\n\t" \
  "ds_write_b32 v5, v4\n\t" \
  "v_mfma_f32_16x16x32_bf16 v[36:39], v[140:143], a[180:183], v[36:39]\n\t" \
  "v_mfma_f32_16x16x32_bf16 v[32:35], v[144:147], a[184:187], v[32:35]\n\t" \
  "v_mfma_f32_16x16x32_bf16 v[36:39], v[148:151], a[184:187], v[36:39]\n\t" \
  "buffer_store_dwordx4 v[52:55], v0, %[X], s50 offen offset:1024 sc1\n\t" \
  "v_mfma_f32_16x16x32_bf16 v[32:35], v[152:155], a[188:191], v[32:35]\n\t" \
  "v_mfma_f32_16x16x32_bf16 v[36:39], v[156:159], a[188:191], v[36:39]\n\t" \
  "s_waitcnt lgkmcnt(0)\n\t" \
  "s_barrier\n\t" \
  "v_mfma_f32_16x16x32_bf16 v[32:35], v[160:163], a[192:195], v[32:35]\n\t" \
  "ds_read_b32 v4, v6 offset:40959\n\t" \
  "v_mfma_f32_16x16x32_bf16 v[36:39], v[164:167], a[192:195], v[36:39]\n\t" \
  "v_mfma_f32_16x16x32_bf16 v[32:35], v[168:171], a[196:199], v[32:35]\n\t" \
  "v_mfma_f32_16x16x32_bf16 v[36:39], v[172:175], a[196:199], v[36:39]\n\t" \
  "v_mfma_f32_16x16x32_bf16 v[32:35], v[176:179], a[200:203], v[32:35]\n\t" \
  "v_mfma_f32_16x16x32_bf16 v[36:39], v[180:183], a[200:203], v[36:39]\n\t" \
  "s_waitcnt lgkmcnt(0)\n\t" \
  "v_readfirstlane_b32 s60, v4\n\t" \
  "s_add_u32 s55, s83, s36\n\t" \
  "v_mfma_f32_16x16x32_bf16 v[32:35], v[184:187], a[204:207], v[32:35]\n\t" \
  "s_cmp_eq_u32 s48, 63\n\t" \
  "s_cselect_b32 s56, 0, s60\n\t" \
  "v_mfma_f32_16x16x32_bf16 v[36:39], v[188:191], a[204:207], v[36:39]\n\t" \
  "s_cmp_eq_u32 s56, 0\n\t" \
  "s_cselect_b32 s55, 0xf0000000, s55\n\t" \
  "v_mfma_f32_16x16x32_bf16 v[32:35], v[192:195], a[208:211], v[32:35]\n\t" \
  "v_mfma_f32_16x16x32_bf16 v[36:39], v[196:199], a[208:211], v[36:39]\n\t" \
  "v_mfma_f32_16x16x32_bf16 v[32:35], v[200:203], a[212:215], v[32:35]\n\t" \
  "s_add_u32 m0, s46, 0\n\t" \
  "s_add_u32 s47, s55, 0\n\t" \
  "buffer_load_dwordx4 v0, %[X], s47 offen sc1 lds\n\t" \
  "v_mfma_f32_16x16x32_bf16 v[36:39], v[204:207], a[212:215], v[36:39]\n\t" \
  "v_mfma_f32_16x16x32_bf16 v[40:43], v[64:67], a[216:219], 0\n\t" \
  "v_mfma_f32_16x16x32_bf16 v[44:47], v[68:71], a[216:219], 0\n\t" \
  "v_mfma_f32_16x16x32_bf16 v[40:43], v[72:75], a[220:223], v[40:43]\n\t" \
  "v_cvt_pk_bf16_f32 v56, v32, v33\n\t" \
  "v_cvt_pk_bf16_f32 v57, v34, v35\n\t" \
  "s_add_u32 m0, s46, 4096\n\t" \
  "s_add_u32 s47, s55, 4096\n\t" \
  "buffer_load_dwordx4 v0, %[X], s47 offen sc1 lds\n\t" \
  "v_mfma_f32_16x16x32_bf16 v[44:47], v[76:79], a[220:223], v[44:47]\n\t" \
  "v_cvt_pk_bf16_f32 v58, v36, v37\n\t" \
  "v_cvt_pk_bf16_f32 v59, v38, v39\n\t" \
  "v_mfma_f32_16x16x32_bf16 v[40:43], v[80:83], a[224:227], v[40:43]\n\t" \
  "v_mfma_f32_16x16x32_bf16 v[44:47], v[84:87], a[224:227], v[44:47]\n\t" \
  "buffer_store_dwordx4 v[56:59], v0, %[X], s50 offen offset:2048 sc1\n\t" \
  "v_mfma_f32_16x16x32_bf16 v[40:43], v[88:91], a[228:231], v[40:43]\n\t" \
  "s_add_u32 m0, s46, 8192\n\t" \
  "s_add_u32 s47, s55, 16384\n\t" \
  "buffer_load_dwordx4 v0, %[X], s47 offen sc1 lds\n\t" \
  "v_mfma_f32_16x16x32_bf16 v[44:47], v[92:95], a[228:231], v[44:47]\n\t" \
  "v_mfma_f32_16x16x32_bf16 v[40:43], v[96:99], a[232:235], v[40:43]\n\t" \
  "v_mfma_f32_16x16x32_bf16 v[44:47], v[100:103], a[232:235], v[44:47]\n\t" \
  "v_mfma_f32_16x16x32_bf16 v[40:43], v[104:107], a[236:239], v[40:43]\n\t" \
  "s_add_u32 m0, s46, 12288\n\t" \
  "s_add_u32 s47, s55, 20480\n\t" \
  "buffer_load_dwordx4 v0, %[X], s47 offen sc1 lds\n\t" \
  "v_mfma_f32_16x16x32_bf16 v[44:47], v[108:111], a[236:239], v[44:47]\n\t" \
  "v_mfma_f32_16x16x32_bf16 v[40:43], v[112:115], a[240:243], v[40:43]\n\t" \
  "v_mfma_f32_16x16x32_bf16 v[44:47], v[116:119], a[240:243], v[44:47]\n\t" \
  "v_mfma_f32_16x16x32_bf16 v[40:43], v[120:123], a[244:247], v[40:43]\n\t" \
  "s_add_u32 m0, s46, 16384\n\t" \
  "s_add_u32 s47, s55, 32768\n\t" \
  "buffer_load_dwordx4 v0, %[X], s47 offen sc1 lds\n\t" \
  "v_mfma_f32_16x16x32_bf16 v[44:47], v[124:127], a[244:247], v[44:47]\n\t" \
  "v_mfma_f32_16x16x32_bf16 v[40:43], v[128:131], a[248:251], v[40:43]\n\t" \
  "v_mfma_f32_16x16x32_bf16 v[44:47], v[132:135], a[248:251], v[44:47]\n\t" \
  "v_mfma_f32_16x16x32_bf16 v[40:43], v[136:139], a[252:255], v[40:43]\n\t" \
  "s_add_u32 m0, s46, 20480\n\t" \
  "s_add_u32 s47, s55, 36864\n\t" \
  "buffer_load_dwordx4 v0, %[X], s47 offen sc1 lds\n\t" \
  "v_mfma_f32_16x16x32_bf16 v[44:47], v[140:143], a[252:255], v[44:47]\n\t" \
  "v_mfma_f32_16x16x32_bf16 v[40:43], v[144:147], v[224:227], v[40:43]\n\t" \
  "v_mfma_f32_16x16x32_bf16 v[44:47], v[148:151], v[224:227], v[44:47]\n\t" \
  "v_mfma_f32_16x16x32_bf16 v[40:43], v[152:155], v[228:231], v[40:43]\n\t" \
  "s_add_u32 m0, s46, 24576\n\t" \
  "s_add_u32 s47, s55, 49152\n\t" \
  "buffer_load_dwordx4 v0, %[X], s47 offen sc1 lds\n\t" \
  "v_mfma_f32_16x16x32_bf16 v[44:47], v[156:159], v[228:231], v[44:47]\n\t" \
  "v_mfma_f32_16x16x32_bf16 v[40:43], v[160:163], v[232:235], v[40:43]\n\t" \
  "v_mfma_f32_16x16x32_bf16 v[44:47], v[164:167], v[232:235], v[44:47]\n\t" \
  "v_mfma_f32_16x16x32_bf16 v[40:43], v[168:171], v[236:239], v[40:43]\n\t" \
  "s_add_u32 m0, s46, 28672\n\t" \
  "s_add_u32 s47, s55, 53248\n\t" \
  "buffer_load_dwordx4 v0, %[X], s47 offen sc1 lds\n\t" \
  "v_mfma_f32_16x16x32_bf16 v[44:47], v[172:175], v[236:239], v[44:47]\n\t" \
  "v_mfma_f32_16x16x32_bf16 v[40:43], v[176:179], v[240:243], v[40:43]\n\t" \
  "v_mfma_f32_16x16x32_bf16 v[44:47], v[180:183], v[240:243], v[44:47]\n\t" \
  "v_mfma_f32_16x16x32_bf16 v[40:43], v[184:187], v[244:247], v[40:43]\n\t" \
  "s_add_u32 m0, s46, 32768\n\t" \
  "s_add_u32 s47, s55, 65536\n\t" \
  "buffer_load_dwordx4 v0, %[X], s47 offen sc1 lds\n\t" \
  "v_mfma_f32_16x16x32_bf16 v[44:47], v[188:191], v[244:247], v[44:47]\n\t" \
  "v_mfma_f32_16x16x32_bf16 v[40:43], v[192:195], v[248:251], v[40:43]\n\t" \
  "v_mfma_f32_16x16x32_bf16 v[44:47], v[196:199], v[248:251], v[44:47]\n\t" \
  "buffer_load_dword v8, off, %[X], s57 sc1\n\t" \
  "v_mfma_f32_16x16x32_bf16 v[40:43], v[200:203], v[252:255], v[40:43]\n\t" \
  "v_mfma_f32_16x16x32_bf16 v[44:47], v[204:207], v[252:255], v[44:47]\n\t" \
  "s_nop 4\n\t" \
  "v_cvt_pk_bf16_f32 v60, v40, v41\n\t" \
  "v_cvt_pk_bf16_f32 v61, v42, v43\n\t" \
  "s_nop 1\n\t" \
  "v_cvt_pk_bf16_f32 v62, v44, v45\n\t" \
  "v_cvt_pk_bf16_f32 v63, v46, v47\n\t" \
  "buffer_store_dwordx4 v[60:63], v0, %[X], s50 offen offset:3072 sc1\n\t" \
  TK_PQ2 \
  "s_add_u32 s49, s82, s38\n\t" \
  "s_cmp_eq_u32 s48, 63\n\t" \
  "s_cbranch_scc1 LEND_%=\n\t" \
  "s_add_u32 s48, s48, 1\n\t" \
  "s_cmp_eq_u32 s56, 0\n\t" \
  "s_cbranch_scc1 LCOLD_%=\n\t" \
  "s_waitcnt vmcnt(0)\n\t" \
  "s_mov_b64 exec, 1\n\t" \
  "buffer_store_dword v6, off, %[X], s49 sc1\n\t" \
  "s_mov_b64 exec, -1\n\t" \
  "s_mov_b32 s49, 0xf0000000\n\t" \
  "s_cmp_lt_i32 s57, 0\n\t" \
  "s_cbranch_scc1 LTOP_%=\n\t" \
  "s_movk_i32 s63, 4000\n\t" \
  "s_branch LGW_%=u\n\t" \
  "LGW_%=:\n\t" \
  "s_sleep " TK_PSG "\n\t" \
  "buffer_load_dword v8, off, %[X], s57 sc1\n\t" \
  "s_waitcnt vmcnt(0)\n\t" \
  "LGW_%=u:\n\t" \
  "s_memrealtime s[52:53]\n\t" \
  "v_readfirstlane_b32 s60, v8\n\t" \
  "s_waitcnt lgkmcnt(0)\n\t" \
  "s_sub_u32 s63, s63, 1\n\t" \
  "s_cbranch_scc1 LTOP_%=\n\t" \
  "s_cmp_eq_u32 s60, -1\n\t" \
  "s_cbranch_scc1 LGW_%=\n\t" \
  "s_not_b32 s60, s60\n\t" \
  "s_lshl_b32 s61, s52, 8\n\t" \
  "s_sub_u32 s60, s60, s61\n\t" \
  "s_ashr_i32 s60, s60, 8\n\t" \
  "s_sub_i32 s60, s60, " TK_QXW "\n\t" \
  "s_min_i32 s60, s60, 400\n\t" \
  "s_add_u32 s54, s52, s60\n\t" \
  "LGW_%=w:\n\t" \
  "s_sleep 1\n\t" \
  "s_memrealtime s[52:53]\n\t" \
  "s_waitcnt lgkmcnt(0)\n\t" \
  "s_sub_i32 s53, s52, s54\n\t" \
  "s_cmp_lt_i32 s53, 0\n\t" \
  "s_cbranch_scc1 LGW_%=w\n\t" \
  "s_branch LTOP_%=\n\t" \
  "LEND_%=:\n\t" \
  "s_waitcnt vmcnt(0)\n\t" \
  "s_mov_b64 exec, 1\n\t" \
  "buffer_store_dword v6, off, %[X], s49 sc1\n\t" \
  "s_mov_b64 exec, -1\n\t" \
  "s_mov_b32 s49, 0xf0000000\n\t" \
  TK_PQ0 \
  ""
__device__ __forceinline__ unsigned hoff(int r, int piece) {
  const int m = 4 * (r >> 3) + (r & 3), kp = piece >> 3;
  return (P_H + (unsigned)(kp >> 1)) * PAIR + (unsigned)((kp & 1) * 4096 + (16 * ((r >> 2) & 1) + m) * 128 + (((piece ^ m) & 7) << 4));
}
template <int SLEEP>
__device__ __forceinline__ void wait_all(rsrc_t X, unsigned vo, unsigned so) {
  unsigned spins = 0;
  while (true) {
    asm volatile("" ::: "memory");
    if (__all(ld4(X, vo, so) != 0u)) break;
    if (spin_fail(spins, X)) break;
    __builtin_amdgcn_s_sleep(SLEEP);
  }
  asm volatile("" ::: "memory");
}
#define VQ_CLOB "v8", "v16", "v17", "v18", "v19", "v20", "v21", "v22", "v23", "v24", "v25", "v26", "v27", "v28", "v29", "v30", "v31", "v32", "v33", "v34", "v35", "v36", "v37", "v38", "v39", "v40", "v41", "v42", "v43", "v44", "v45", "v46", "v47", "v48", "v49", "v50", "v51", "v52", "v53", "v54", "v55", "v56", "v57", "v58", "v59", "v60", "v61", "v62", "v63", "v64", "v65", "v66", "v67", "v68", "v69", "v70", "v71", "v72", "v73", "v74", "v75", "v76", "v77", "v78", "v79", "v80", "v81", "v82", "v83", "v84", "v85", "v86", "v87", "v88", "v89", "v90", "v91", "v92", "v93", "v94", "v95", "v96", "v97", "v98", "v99", "v100", "v101", "v102", "v103", "v104", "v105", "v106", "v107", "v108", "v109", "v110", "v111", "v112", "v113", "v114", "v115", "v116", "v117", "v118", "v119", "v120", "v121", "v122", "v123", "v124", "v125", "v126", "v127", "v128", "v129", "v130", "v131", "v132", "v133", "v134", "v135", "v136", "v137", "v138", "v139", "v140", "v141", "v142", "v143", "v144", "v145", "v146", "v147", "v148", "v149", "v150", "v151", "v152", "v153", "v154", "v155", "v156", "v157", "v158", "v159", "v160", "v161", "v162", "v163", "v164", "v165", "v166", "v167", "v168", "v169", "v170", "v171", "v172", "v173", "v174", "v175", "v176", "v177", "v178", "v179", "v180", "v181", "v182", "v183", "v184", "v185", "v186", "v187", "v188", "v189", "v190", "v191", "v192", "v193", "v194", "v195", "v196", "v197", "v198", "v199", "v200", "v201", "v202", "v203", "v204", "v205", "v206", "v207", "v220", "v221"
#define VA_CLOB "v40", "v41", "v42", "v43", "v44", "v45", "v46", "v47", "v48", "v49", "v50", "v51", "v52", "v53", "v54", "v55", "v56", "v57", "v58", "v59", "v60", "v61", "v62", "v63", "v64", "v65", "v66", "v67", "v68", "v69", "v70", "v71", "v72", "v73", "v74", "v75", "v76", "v77", "v78", "v79", "v80", "v81", "v82", "v83", "v84", "v85", "v86", "v87", "v88", "v89", "v90", "v91", "v92", "v93", "v94", "v95", "v96", "v97", "v98", "v99", "v100", "v101", "v102", "v103", "v104", "v105", "v106", "v107", "v108", "v109", "v110", "v111", "v112", "v113", "v114", "v115", "v116", "v117", "v118", "v119", "v120", "v121", "v122", "v123", "v124", "v125", "v126", "v127", "v128", "v129", "v130", "v131", "v132", "v133", "v134", "v135", "v136", "v137", "v138", "v139", "v140", "v141", "v142", "v143", "v144", "v145", "v146", "v147", "v148", "v149", "v150", "v151", "v152", "v153", "v154", "v155", "v156", "v157", "v158", "v159", "v160", "v161", "v162", "v163", "v164", "v165", "v166", "v167", "v168", "v169", "v170", "v171", "v172", "v173", "v174", "v175", "v176", "v177", "v178", "v179", "v180", "v181", "v182", "v183", "v184", "v185", "v186", "v187", "v188", "v189", "v190", "v191", "v192", "v193", "v194", "v195", "v196", "v197", "v198", "v199", "v200", "v201", "v202", "v203", "v204", "v205", "v206", "v207", "v208", "v209", "v210", "v211", "v220", "v221"
#define TOK_COMMON() \
  const MP p = getp(); \
  const int c = (int)blockIdx.x - NCHW, xq = c & 3, L = (c >> 2) & 1, i = c >> 3; \
  const int tid = threadIdx.x, lane = tid & 63, w = __builtin_amdgcn_readfirstlane(tid >> 6); \
  const rsrc_t X = mkr(p.xb, XB_BYTES)
#define W_IN [W] "s"(WR)
#define W_CLOB "a0", "a1", "a2", "a3", "a4", "a5", "a6", "a7", "a8", "a9", "a10", "a11", "a12", "a13", "a14", "a15", "a16", "a17", "a18", "a19", "a20", "a21", "a22", "a23", "a24", "a25", "a26", "a27", "a28", "a29", "a30", "a31", "a32", "a33", "a34", "a35", "a36", "a37", "a38", "a39", "a40", "a41", "a42", "a43", "a44", "a45", "a46", "a47", "a48", "a49", "a50", "a51", "a52", "a53", "a54", "a55", "a56", "a57", "a58", "a59", "a60", "a61", "a62", "a63", "a64", "a65", "a66", "a67", "a68", "a69", "a70", "a71", "a72", "a73", "a74", "a75", "a76", "a77", "a78", "a79", "a80", "a81", "a82", "a83", "a84", "a85", "a86", "a87", "a88", "a89", "a90", "a91", "a92", "a93", "a94", "a95", "a96", "a97", "a98", "a99", "a100", "a101", "a102", "a103", "a104", "a105", "a106", "a107", "a108", "a109", "a110", "a111", "a112", "a113", "a114", "a115", "a116", "a117", "a118", "a119", "a120", "a121", "a122", "a123", "a124", "a125", "a126", "a127", "a128", "a129", "a130", "a131", "a132", "a133", "a134", "a135", "a136", "a137", "a138", "a139", "a140", "a141", "a142", "a143", "a144", "a145", "a146", "a147", "a148", "a149", "a150", "a151", "a152", "a153", "a154", "a155", "a156", "a157", "a158", "a159", "a160", "a161", "a162", "a163", "a164", "a165", "a166", "a167", "a168", "a169", "a170", "a171", "a172", "a173", "a174", "a175", "a176", "a177", "a178", "a179", "a180", "a181", "a182", "a183", "a184", "a185", "a186", "a187", "a188", "a189", "a190", "a191", "a192", "a193", "a194", "a195", "a196", "a197", "a198", "a199", "a200", "a201", "a202", "a203", "a204", "a205", "a206", "a207", "a208", "a209", "a210", "a211", "a212", "a213", "a214", "a215", "a216", "a217", "a218", "a219", "a220", "a221", "a222", "a223", "a224", "a225", "a226", "a227", "a228", "a229", "a230", "a231", "a232", "a233", "a234", "a235", "a236", "a237", "a238", "a239", "a240", "a241", "a242", "a243", "a244", "a245", "a246", "a247", "a248", "a249", "a250", "a251", "a252", "a253", "a254", "a255", "v224", "v225", "v226", "v227", "v228", "v229", "v230", "v231", "v232", "v233", "v234", "v235", "v236", "v237", "v238", "v239", "v240", "v241", "v242", "v243", "v244", "v245", "v246", "v247", "v248", "v249", "v250", "v251", "v252", "v253", "v254", "v255"
#define BOOT_IN "{s67}"(L), [ctab] "i"(CTAB_OFF)
#define S_CLOB "s76", "s78", "s79", "s86", "s88", "s89", "s90", "s91", "s46", "s47", "s48", "s49", "s50", "s51", "s52", "s53", "s54", "s55", "s56", "s57", "s58", "s59", "s60", "s61", "s62", "s63", "s68", "s69", "s70", "s71", "s72", "s73", "s82", "s83", "s84", "m0", "vcc", "scc", "memory"
typedef unsigned v8w __attribute__((ext_vector_type(8)));

__device__ __forceinline__ void qkv_role() {
  TOK_COMMON();
  const int role = i * 4 + w, hh = 8 * xq + role / 6, part = role % 6, which = part >> 1, half = part & 1;
  const rsrc_t WR = mkr((which == 0 ? p.Wq : (which == 1 ? p.Wk : p.Wv)) + hh * DH + 64 * half, DM * HD * 2u - (unsigned)(hh * DH + 64 * half) * 2u);
  const int m = lane & 15, g = lane >> 4;
  const unsigned a0 = (unsigned)(m * 128 + ((g ^ (m & 7)) << 4));
  v8w lv = {(unsigned)lane * 16u, a0, a0 ^ 64u, (unsigned)(lane & (L ? 15 : 3)) * 128u, 0u, 40960u + 4u * (unsigned)w, 1u, 0u};
  const unsigned c36 = XQL + P_H * PAIR + (unsigned)w * 1024u, c37 = XQ + (unsigned)(3 * hh + which) * PAIR + (unsigned)half * (which == 2 ? 4096u : 2048u);
  const unsigned c38 = P_FA * PAIR + (unsigned)(hh * 128 + part * 16), c39 = w == 0 ? P_FB * PAIR + 4096u : 0xF0000000u, c41 = (unsigned)w * 1024u;
  const int c42 = L ? 1 - QLEAD1 : 1 - QLEAD;
  unsigned d74 = TRC_OFF + 262144u + (unsigned)((c * 4 + w) * 66) * 8u;
  TT(64, 0);
#define Q_CALL(A) asm volatile("s_waitcnt vmcnt(0) lgkmcnt(0)\n\t" A : "+{v[0:7]}"(lv), "+{s74}"(d74) \
               : [X] "s"(X), "{s36}"(c36), "{s37}"(c37), "{s38}"(c38), "{s39}"(c39), "{s40}"(w), "{s41}"(c41), "{s42}"(c42), "{s45}"(i), "{s92}"(TMO_OFF), BOOT_IN, [cap] "i"(SPIN_CAP), [qxw] "i"(QXW), [qxc] "i"(QXC), W_IN : VQ_CLOB, W_CLOB, S_CLOB)
  if (which == 2) { Q_CALL(QV_ASM "LQVEND:"); __builtin_amdgcn_endpgm(); }
  Q_CALL(QK_ASM "LQKEND:");
  __builtin_amdgcn_endpgm();
}

__device__ __forceinline__ void ap_role() {
  TOK_COMMON();
  const int hp = 4 * xq + ((i - 12) >> 1), ch = i & 1, a = w >> 1, rt = w & 1, h = 2 * hp + a;
  const int n = lane & 15, g = lane >> 4;
  const rsrc_t WR = mkr(p.Wo + (long)hp * 2 * DH * DM, 2u * DH * DM * 2u);
  const unsigned l16 = (unsigned)lane * 16u, ab = (unsigned)a * 24576u + l16;
  v16u lv = {l16, ab, ab + (unsigned)rt * 4096u, 49152u + (unsigned)w * 4096u + l16, 49152u + (unsigned)(w ^ 2) * 4096u + l16, l16 & ~32u, l16 >> 1,
             (unsigned)(g * 262144 + n * 16), (unsigned)(n * 32 + g * 8), (unsigned)((lane & 7) % 6) * 16u, 0u, 0u, 0u, 0u, 0u, 0u};
  v8w mk;
  const int qi = 8 * (n >> 2) + (n & 3) + 4 * rt;
#pragma unroll
  for (int k = 0; k < 8; ++k) mk[k] = 8 * g + k > qi ? 0xFF800000u : 0u;
  v16u zz = {0u, 0u, 0u, 0u, 0u, 0u, 0u, 0u, 0u, 0u, 0u, 0u, 0u, 0u, 0u, 0u};
  const unsigned tb = XAL + (unsigned)(3 * h) * PAIR;
  const unsigned c36 = tb + (rt ? PAIR + 4096u : 0u), c37 = tb + (rt ? 2u * PAIR : 4096u), c38 = tb + (rt ? 2u * PAIR + 4096u : PAIR);
  const unsigned c39 = XK + (P_KV + (unsigned)(h >> 1)) * PAIR + (unsigned)(h & 1) * 4096u, c41 = P_FA * PAIR + (unsigned)h * 128u;
  const unsigned c42 = w == 0 ? P_FB * PAIR + (unsigned)(2 * hp + ch) * 128u : 0xF0000000u;
  const unsigned c43 = XA + TP0 * XBLK + (unsigned)(8 * rt + (hp >> 3)) * PAIR + (unsigned)((hp & 7) * 1024 + ch * 512 + a * 256);
  unsigned d44 = XT + (TP0 + 8u) * XBLK + (unsigned)(hp >> 1) * PAIR + (unsigned)((((hp & 1) * 4 + 2 * ch + a) * 2 + rt) * 512);
  const unsigned c45 = (unsigned)(a * 24576 + rt * 12288);
  unsigned d74 = TRC_OFF + 262144u + (unsigned)((c * 4 + w) * 66) * 8u;
  asm volatile("s_waitcnt vmcnt(0) lgkmcnt(0)\n\t" AP_ASM "LAPEND:" : "+{v[0:15]}"(lv), "+{v[24:39]}"(zz), "+{s74}"(d74), "+{s44}"(d44)
               : [X] "s"(X), "{s36}"(c36), "{s37}"(c37), "{s38}"(c38), "{s39}"(c39), "{s41}"(c41), "{s42}"(c42), "{s43}"(c43), "{s45}"(c45), "{s64}"(512 * ch + 256 * a), "{s65}"(1024 + 64 * ch + 32 * a), "{s66}"(147456 * a), "{s77}"(L ? AX1 : AX), BOOT_IN,
                 "{s92}"(TMO_OFF), [cap] "i"(SPIN_CAP), "{v[16:23]}"(mk), W_IN : VA_CLOB, W_CLOB, S_CLOB);
  __builtin_amdgcn_endpgm();
}

template <int N> __device__ __forceinline__ float rorN(float v) { return __uint_as_float((unsigned)__builtin_amdgcn_update_dpp(0, (int)__float_as_uint(v), 0x120 + N, 0xf, 0xf, false)); }
__device__ __forceinline__ void up8(v4u v, float* x) {
  x[0] = lo2f(v.x); x[1] = hi2f(v.x); x[2] = lo2f(v.y); x[3] = hi2f(v.y); x[4] = lo2f(v.z); x[5] = hi2f(v.z); x[6] = lo2f(v.w); x[7] = hi2f(v.w);
}
__device__ __forceinline__ v4u pk8(const float* x) { return (v4u){pk2(x[0], x[1]), pk2(x[2], x[3]), pk2(x[4], x[5]), pk2(x[6], x[7])}; }

__device__ __forceinline__ void rb9(float (&x)[9]) {
#pragma unroll
  for (int i = 0; i < 8; i += 2) { const unsigned u = pk2(x[i], x[i + 1]); x[i] = lo2f(u); x[i + 1] = hi2f(u); }
  x[8] = lo2f(pk2(x[8], x[8]));
}
template <int R>
__device__ __forceinline__ void lnR(const float (&x)[R][9], float (&o)[R][9]) {
  float s[2 * R], pv[R];
#pragma unroll
  for (int r = 0; r < R; ++r) {
    pv[r] = __uint_as_float(__builtin_amdgcn_readfirstlane(__float_as_uint(x[r][0])));
    const f32x2 p2 = {pv[r], pv[r]};
    f32x2 d[4];
#pragma unroll
    for (int i = 0; i < 4; ++i) d[i] = (f32x2){x[r][2 * i], x[r][2 * i + 1]} - p2;
    const float d8 = x[r][8] - pv[r];
    const f32x2 a = (d[0] + d[1]) + (d[2] + d[3]);
    const f32x2 q = __builtin_elementwise_fma(d[3], d[3], __builtin_elementwise_fma(d[2], d[2], __builtin_elementwise_fma(d[1], d[1], d[0] * d[0])));
    s[2 * r] = (a[0] + d8) + a[1];
    s[2 * r + 1] = __builtin_fmaf(d8, d8, q[0]) + q[1];
  }
#pragma unroll
  for (int k = 0; k < 2 * R; ++k) s[k] += rorN<8>(s[k]);
#pragma unroll
  for (int k = 0; k < 2 * R; ++k) s[k] += rorN<4>(s[k]);
#pragma unroll
  for (int k = 0; k < 2 * R; ++k) s[k] += rorN<2>(s[k]);
#pragma unroll
  for (int k = 0; k < 2 * R; ++k) s[k] += rorN<1>(s[k]);
#pragma unroll
  for (int r = 0; r < R; ++r) {
    auto u = __builtin_amdgcn_permlane16_swap(__float_as_uint(s[2 * r]), __float_as_uint(s[2 * r + 1]), false, false);
    s[2 * r] = __uint_as_float(u[0]) + __uint_as_float(u[1]);
  }
#pragma unroll
  for (int r = 0; r < R; ++r) {
    const unsigned u = __float_as_uint(s[2 * r]);
    const float md = (__uint_as_float(__builtin_amdgcn_readlane(u, 0)) + __uint_as_float(__builtin_amdgcn_readlane(u, 32))) * (1.0f / DM);
    const float sq = (__uint_as_float(__builtin_amdgcn_readlane(u, 16)) + __uint_as_float(__builtin_amdgcn_readlane(u, 48))) * (1.0f / DM);
    const unsigned mv = pk2(pv[r] + md, __builtin_fmaxf(__builtin_fmaf(-md, md, sq), 0.f));
    const float m = lo2f(mv), var = hi2f(mv);
    const float ve = lo2f(pk2(var + 1e-5f, 0.f));
    const float den = lo2f(pk2(__builtin_amdgcn_sqrtf(ve), 0.f));
    const float inv = __builtin_amdgcn_rcpf(den);
#pragma unroll
    for (int i = 0; i < 9; ++i) o[r][i] = (x[r][i] - m) * inv;
  }
}
__device__ __forceinline__ void ln9(const float (&x)[9], float (&o)[9]) { lnR<1>((const float (&)[1][9])x, (float (&)[1][9])o); }
__device__ __forceinline__ v4u gat8(float v) {
  const unsigned d = pk2(v, __uint_as_float((unsigned)__builtin_amdgcn_mov_dpp((int)__float_as_uint(v), 0xB1, 0xf, 0xf, true)));
  return (v4u){d, (unsigned)__builtin_amdgcn_mov_dpp((int)d, 0x102, 0xf, 0xf, true), (unsigned)__builtin_amdgcn_mov_dpp((int)d, 0x104, 0xf, 0xf, true),
               (unsigned)__builtin_amdgcn_mov_dpp((int)d, 0x106, 0xf, 0xf, true)};
}
__device__ __forceinline__ void tok_row(const MP& p, const rsrc_t X, unsigned sb1, int L, int s, int r, int lane, const v4u (&v)[18], v4u px, v4u py, unsigned px9, unsigned py9) {
  const long tok = (long)s * SEG + r;
  const int m = lane & 15, g = lane >> 4;
  const int xp = 4 * (g & 1) + 2 * (g >> 1) + (m >> 3);
  const unsigned zr = opq(0u);
  const unsigned one = (g == (m >> 2)) ? (opq(0x3F80u) << (16 * (m & 1))) : zr;
  const unsigned ax = (m & 2) ? zr : one, ay = (m & 2) ? one : zr;
  const v4u A1 = {ax, ay, zr, zr}, A2 = {zr, zr, ax, ay};
  const unsigned on2 = opq(0x3F80u) << (16 * (m & 1));
  const int q2 = (m >> 1) & 3;
  const v4u sel = {q2 == 0 ? on2 : zr, q2 == 1 ? on2 : zr, q2 == 2 ? on2 : zr, q2 == 3 ? on2 : zr};
  f32x4 d1, d2, e1;
  asm volatile("s_nop 1\n\t" MF "%0, %1, %3, 0\n\t" MF "%0, %2, %3, %0" : "=&v"(e1) : "v"(v[0]), "v"(v[1]), "v"(sel));
#pragma unroll
  for (int i = 0; i < 16; ++i) {
    if (i == 0) asm volatile("s_nop 1\n\t" MF "%0, %2, %4, 0\n\t" MF "%1, %3, %4, 0" : "=&v"(d1), "=&v"(d2) : "v"(A1), "v"(A2), "v"(v[2]));
    else asm volatile(MF "%0, %2, %4, %0\n\t" MF "%1, %3, %4, %1" : "+v"(d1), "+v"(d2) : "v"(A1), "v"(A2), "v"(v[2 + i]));
  }
  asm volatile("s_nop 7" : "+v"(d1), "+v"(d2), "+v"(e1));
  float o[9];
#pragma unroll
  for (int i = 0; i < 4; ++i) { o[i] = d1[i]; o[4 + i] = d2[i]; }
  {
    auto a02 = __builtin_amdgcn_permlane32_swap(__float_as_uint(e1[0]), __float_as_uint(e1[2]), false, false);
    auto a13 = __builtin_amdgcn_permlane32_swap(__float_as_uint(e1[1]), __float_as_uint(e1[3]), false, false);
    o[8] = m >= 8 ? __uint_as_float(a13[0]) + __uint_as_float(a13[1]) : __uint_as_float(a02[0]) + __uint_as_float(a02[1]);
  }
  float x[9];
  up8(px, x); x[8] = lo2f(px9);
#pragma unroll
  for (int i = 0; i < 9; ++i) x[i] += o[i];
  if (L == 0) {
    float y[9], ln[9];
    up8(py, y); y[8] = lo2f(py9);
    rb9(x);
    ln9(x, ln);
#pragma unroll
    for (int i = 0; i < 9; ++i) x[i] = __builtin_fmaf(ln[i], y[i], x[i]);
    rb9(x);
    st16(X, (unsigned)lane * 16u, sb1 + (P_X3 + (unsigned)(r >> 3)) * PAIR + (unsigned)(r & 7) * 1024u, pk8(x));
    const v4u x9 = gat8(x[8]);
    if ((lane & 7) == 0) st16(X, (unsigned)xp * 16u, sb1 + (P_H + 4u) * PAIR + 4096u + (unsigned)r * 128u, x9);
    ln9(x, ln);
    st16(X, hoff(r, lane), sb1, pk8(ln));
    const v4u h9 = gat8(ln[8]);
    if ((lane & 7) == 0) st16(X, hoff(r, 64 + xp), sb1, h9);
  } else {
    const rsrc_t O = mkr(p.out + tok * DM, DM * 2u);
    __builtin_amdgcn_raw_buffer_store_b128(pk8(x), O, (unsigned)lane * 16u, 0u, OAUX);
    __builtin_amdgcn_raw_buffer_store_b128(gat8(x[8]), O, (lane & 7) == 0 ? 1024u + (unsigned)xp * 16u : 0xF0000000u, 0u, OAUX);
  }
}

__device__ __forceinline__ void row_role(char* smem) {
  TOK_COMMON();
  const int rw = 4 * xq + (i - 20), r = 2 * rw + (w >> 1);
  const unsigned long long sm = die_mask(X, SG0, L, lane);
  const unsigned tm = (unsigned)die_mask(X, TP0, L, lane), dm = (unsigned)die_mask(X, 0u, L, lane);
  __attribute__((address_space(3))) unsigned* cnt = (__attribute__((address_space(3))) unsigned*)smem;
  BAR();
  if (tid < 64) cnt[tid] = 0u;
  BAR();
#define ANCT 25000
  for (int s = w & 1; s < NSEG; s += 2) {
    int ln_ = lane; asm volatile("" : "+v"(ln_));
    const int m = ln_ & 15, g = ln_ >> 4;
    const unsigned l16 = (unsigned)ln_ * 16u, xo = (unsigned)(8 * (4 * (g & 1) + 2 * (g >> 1) + (m >> 3)) + (m & 7)) * 2u;
    const unsigned sb = SEGB(sm, s), sb1 = sb ^ (L ? 0u : 8192u);
    if (s + ARMK < NSEG) {
      const unsigned sv = opq(SENT), sl = SLOT_AT(s + ARMK) + ((dm >> ((s + ARMK) / 3)) & 1u) * 8192u;
      for (int j = r; j < 84; j += 32) {
#pragma unroll
        for (int k = 0; k < 4; ++k) st16(X, l16 + (unsigned)k * 1024u, sl + (unsigned)(j >> 1) * PAIR + (unsigned)(j & 1) * 4096u, (v4u){sv, sv, sv, sv});
      }
    }
    wait_all<PSL>(X, 0u, sb + P_FB * PAIR + 4096u + (unsigned)(L ? rw : r >> 3) * 128u);
    const v4u px = ld16(X, l16, sb + (P_X3 + (unsigned)(r >> 3)) * PAIR + (unsigned)(r & 7) * 1024u);
    const unsigned px9 = __builtin_amdgcn_raw_buffer_load_b16(X, xo, sb + (P_H + 4u) * PAIR + 4096u + (unsigned)r * 128u, 16);
    const rsrc_t YX = mkr(p.yT + (2 * s + (r >> 4)) * 9216, L ? 0u : 18432u);
    const v2u ya = ld8(YX, (unsigned)(ln_ * 256 + ((r ^ m) & 15) * 8), 0u), yb = ld8(YX, (unsigned)(ln_ * 256 + 128 + ((r ^ m) & 15) * 8), 0u);
    const v4u py = {ya.x, ya.y, yb.x, yb.y};
    const unsigned py9 = __builtin_amdgcn_raw_buffer_load_b16(YX, 16384u + (xo >> 3) * 128u + (((unsigned)r ^ (xo >> 4)) & 15u) * 8u + (xo & 6u), 0u, 16);
    wait_all<PSP>(X, 0u, sb + P_FB * PAIR + (unsigned)r * 128u);
    __builtin_amdgcn_s_sleep(RONE);
    wait_all<PSP>(X, (unsigned)(ln_ & 31) * 128u, sb + P_FB * PAIR);
    TT(s, 0);
    const unsigned j = (unsigned)s & 15u, to = XRL + TP0 * XBLK + (j << 20) + ((tm >> (j >> 1)) & 1u) * 8192u + (unsigned)r * 2u * PAIR;
    const unsigned t9 = (TP0 + 8u) * XBLK + ((tm >> 8) & 1u) * 8192u + (j << 17) + (unsigned)(((r >> 2) & 1) * 512 + (4 * (r >> 3) + (r & 3)) * 32);
    const unsigned v9 = (unsigned)(g * (int)PAIR + (m >> 1) * 1024 + (m & 1) * 16);
    v4u v[18];
#pragma unroll
    for (int k = 0; k < 2; ++k) v[k] = ld16(X, v9, t9 + (unsigned)k * 4u * PAIR);
#pragma unroll
    for (int k = 0; k < 16; ++k) v[2 + k] = ld16(X, l16 + (unsigned)(k & 3) * 1024u, to + (unsigned)(k >> 3) * PAIR + (unsigned)(k & 4) * 1024u);
    SB();
    tok_row(p, X, sb1, L, s, r, ln_, v, px, py, px9, py9);
    VM0();
    unsigned o1 = 0u;
    if (ln_ == 0) o1 = __hip_atomic_fetch_add(cnt + s, 1u, __ATOMIC_RELAXED, __HIP_MEMORY_SCOPE_WORKGROUP);
    if (__builtin_amdgcn_readfirstlane(o1) == 1u && ln_ < 2 - L) st4(X, ln_ ? sb1 : sb + 2048u, P_FB * PAIR + 4096u + (unsigned)rw * 128u, opq(1u));
    TT(s, 1);
  }
}

__device__ __forceinline__ void rows_task(const MP& p, const rsrc_t X, unsigned long long sm, u16* ys, int grp, int ng) {
  const int tid = threadIdx.x, lane = tid & 63, w = __builtin_amdgcn_readfirstlane(tid >> 6);
  const int m = lane & 15, g = lane >> 4;
  const int xp = 4 * (g & 1) + 2 * (g >> 1) + (m >> 3), c9 = 512 + 8 * xp + (m & 7);
  const unsigned sb = SEGB(sm, grp >> 2);
  v4u yv[5], xa[4];
  unsigned x9[4];
#pragma unroll
  for (int i = 0; i < 5; ++i) yv[i] = ld16(mkr(p.yT + (grp >> 1) * 9216, 18432u), (unsigned)(tid + 256 * i) * 16u, 0u);
#pragma unroll
  for (int rr = 0; rr < 4; ++rr) {
    const long t = grp * 8 + w + 4 * (rr & (2 * ng - 1));
    xa[rr] = *(const v4u*)&p.xin[t * DM + 8 * lane]; x9[rr] = p.xin[t * DM + c9];
  }
#pragma unroll
  for (int i = 0; i < 5; ++i) ((v4u*)ys)[tid + 256 * i] = yv[i];
  BAR();
  SP(13 - 3 * ng);
  const unsigned o9 = (lane & 7) == 0 ? 0u : 0xF0000000u;
#pragma unroll 1
  for (int k = 0; k < ng; ++k) {
    float x[2][9], y[2][9], ln[2][9];
    const int t0 = (grp + k) * 8 + w;
#pragma unroll
    for (int rr = 0; rr < 2; ++rr) {
      const int j = (t0 + 4 * rr) & 15;
      const v2u ya = *(const v2u*)&ys[lane * 128 + (j ^ m) * 4], yb = *(const v2u*)&ys[lane * 128 + 64 + (j ^ m) * 4];
      up8(xa[rr], x[rr]); x[rr][8] = lo2f(x9[rr]); up8((v4u){ya.x, ya.y, yb.x, yb.y}, y[rr]); y[rr][8] = bf2f(ys[(c9 >> 2) * 64 + (j ^ xp) * 4 + (m & 3)]);
      xa[rr] = xa[2 + rr]; x9[rr] = x9[2 + rr];
    }
    lnR<2>(x, ln);
#pragma unroll
    for (int rr = 0; rr < 2; ++rr) {
#pragma unroll
      for (int i = 0; i < 9; ++i) x[rr][i] = __builtin_fmaf(ln[rr][i], y[rr][i], x[rr][i]);
      rb9(x[rr]);
      const int r = (t0 + 4 * rr) & 31;
      st16(X, (unsigned)lane * 16u, sb + (P_X3 + (unsigned)(r >> 3)) * PAIR + (unsigned)(r & 7) * 1024u, pk8(x[rr]));
      st16(X, (unsigned)xp * 16u + o9, sb + (P_H + 4u) * PAIR + 4096u + (unsigned)r * 128u, gat8(x[rr][8]));
    }
    lnR<2>(x, ln);
#pragma unroll
    for (int rr = 0; rr < 2; ++rr) {
      const int r = (t0 + 4 * rr) & 31;
      st16(X, hoff(r, lane), sb, pk8(ln[rr]));
      st16(X, hoff(r, 64 + xp) + o9, sb, gat8(ln[rr][8]));
    }
  }
  SP(14 - 3 * ng);
  VM0();
  BAR();
  SP(15 - 3 * ng);
  if (tid < ng) st4(X, (unsigned)tid * 128u, sb + P_FB * PAIR + 4096u + (unsigned)(grp & 3) * 128u, opq(1u));
}

__device__ __forceinline__ void arrive(const MP& p, int k, int grp) {
  VM0();
  BAR();
  if (threadIdx.x < 64) {
    unsigned old = 0u;
    if (threadIdx.x == 0) old = __hip_atomic_fetch_add(p.xb + MF_OFF / 4u + (k * 16 + grp) * 32, 1u, __ATOMIC_RELAXED, __HIP_MEMORY_SCOPE_AGENT);
    if (__builtin_amdgcn_readfirstlane(old) == 15u && threadIdx.x < 8) st4(mkr(p.xb, XB_BYTES), threadIdx.x * 128u, RDY_OFF(k) + (unsigned)grp * 1024u, opq(1u));
  }
}
__device__ __forceinline__ void wait_ready(const MP& p, int k) {
  if (threadIdx.x < 64) wait_all<PSL>(mkr(p.xb, XB_BYTES), (threadIdx.x % 9u) * 1024u, RDY_OFF(k) + (blockIdx.x & 7u) * 128u);
  BAR();
}
__device__ __forceinline__ void start_work(char* smem, int wk) {
  const MP p = getp();
  const int w = __builtin_amdgcn_readfirstlane(threadIdx.x >> 6);
#define NARM (ARMK - 3u)
  conv_task(p.kvT, p.ssm_k, p.ssm_D, p.yT, (u16*)(smem + w * 32768), 4 * wk + w, [&]() {
    const unsigned sv = opq(SENT);
    constexpr unsigned S16 = SLOT / 16u;
#pragma unroll
    for (unsigned k = 0; k < (NARM * S16 + 36863u) / 36864u; ++k) {
      const unsigned i = k * 36864u + wk * 256 + threadIdx.x;
      st16(mkr(p.xb, XB_BYTES), i < NARM * S16 ? SLOT_AT(3u + i / S16) + (i % S16) * 16u : 0xF0000000u, 0u, (v4u){sv, sv, sv, sv});
    }
  });
  SP(3);
  arrive(p, 0, wk >> 4);
  SP(4);
}
__device__ __forceinline__ void token_start(char* smem) {
  TOK_COMMON();
  (void)w;
  const int k = 4 * i + xq;
  if (L || (i >= 12 && i < 16)) start_work(smem, L ? 32 + k : 80 + k);
  if (i >= (L ? 12 : 20)) {
    const unsigned long long sm = die_mask(X, SG0, 0, lane);
    int g = L ? 111 - k : k - 96;
    while (g < 120) {
      if (g >= 0 && g < 64) {
        SP(5);
        wait_ready(p, 0);
        SP(6);
      }
      rows_task(p, X, sm, (u16*)smem, g < 0 ? g + 16 : 16 + 2 * g, g < 0 ? 1 : 2);
      g += g < 0 ? 16 : (L ? 48 : 120);
    }
  }
}
__device__ __forceinline__ void token_wg(char* smem) {
  if ((unsigned)(size_t)(__attribute__((address_space(3))) char*)smem != 0u) return;
  token_start(smem);
  const int i = ((int)blockIdx.x - NCHW) >> 3;
  if (i < 12) qkv_role();
  else if (i < 20) ap_role();
  else row_role(smem);
}

__global__ __launch_bounds__(256, 1) void k_main(MP) {
  extern __shared__ __attribute__((aligned(16))) char smem[];
  asm volatile("LKBEG:");
  SP(13);
  if ((int)blockIdx.x < NCHW) chain_wg(smem);
  else token_wg(smem);
}

extern "C" void kernel_launch(void* const* d_in, const int* in_sizes, int n_in, void* d_out, int out_size, void* d_ws,
                              size_t ws_size, hipStream_t stream) {
  const u16* A_q = (const u16*)d_in[0];
  const u16* A_k = (const u16*)d_in[1];
  const u16* A_v = (const u16*)d_in[2];
  const u16* A_shift = (const u16*)d_in[3];
  const u16* A_ssmk = (const u16*)d_in[4];
  const u16* A_ssmD = (const u16*)d_in[5];
  const u16* B_mem0 = (const u16*)d_in[7];
  const u16* B_Wq = (const u16*)d_in[8];
  const u16* B_Wk = (const u16*)d_in[9];
  const u16* B_Wv = (const u16*)d_in[10];
  const u16* B_Wo = (const u16*)d_in[11];

  char* ws = (char*)d_ws;
  size_t off = (size_t)(-(uintptr_t)ws) & (XBLK - 1u);
  auto alloc = [&](size_t bytes) { char* q = ws + off; off += (bytes + 4095) & ~(size_t)4095; return q; };
  unsigned* xb = (unsigned*)alloc((size_t)XB_BYTES);
  u16* kvT = (u16*)alloc((size_t)DM * S_LEN * 2);
  u16* yT = (u16*)alloc((size_t)DM * S_LEN * 2);

  PreP pp;
  pp.kk = A_k; pp.vv = A_v; pp.shift_k = A_shift; pp.ssm_k = A_ssmk; pp.ssm_D = A_ssmD; pp.yT = yT; pp.kvT = kvT;
  pp.Wq = B_Wq; pp.Wk = B_Wk; pp.Wv = B_Wv; pp.Wo = B_Wo; pp.xb = xb; pp.mem0 = B_mem0;
  k_pre<<<240 + PRE_FILL + 256 + 288, 256, 0, stream>>>(pp);
  MP mp;
  mp.out = (u16*)d_out; mp.xb = xb;
  mp.xin = A_q; mp.yT = yT; mp.kvT = kvT; mp.ssm_k = A_ssmk; mp.ssm_D = A_ssmD;
  mp.Wq = B_Wq; mp.Wk = B_Wk; mp.Wv = B_Wv; mp.Wo = B_Wo;
  const int smem_bytes = 160 * 1024;
  (void)hipFuncSetAttribute((const void*)k_main, hipFuncAttributeMaxDynamicSharedMemorySize, smem_bytes);
  k_main<<<NCHW + NTOKW, 256, smem_bytes, stream>>>(mp);
}
```

```cpp
#include <hip/hip_runtime.h>
#include <stdint.h>

typedef unsigned short u16;
using bf16x8 = __attribute__((ext_vector_type(8))) short;
using f32x4 = __attribute__((ext_vector_type(4))) float;
using f32x2 = __attribute__((ext_vector_type(2))) float;
using bfx8 = __attribute__((ext_vector_type(8))) __bf16;
using bfx2 = __attribute__((ext_vector_type(2))) __bf16;
typedef unsigned v4u __attribute__((ext_vector_type(4)));
typedef unsigned v2u __attribute__((ext_vector_type(2)));
typedef short v4s __attribute__((ext_vector_type(4)));

#define S_LEN 2048
#define DM 576
#define NH 32
#define DH 128
#define HD (NH * DH)
#define SEG 32
#define NSEG 64

__device__ __forceinline__ float bf2f(u16 x) { return __uint_as_float(((unsigned)x) << 16); }
__device__ __forceinline__ unsigned pk2(float a, float b) { f32x2 v = {a, b}; bfx2 r = __builtin_convertvector(v, bfx2); return __builtin_bit_cast(unsigned, r); }
__device__ __forceinline__ float lo2f(unsigned u) { return __uint_as_float(u << 16); }
__device__ __forceinline__ float hi2f(unsigned u) { return __uint_as_float(u & 0xFFFF0000u); }

#define NFC 128
#define XBLK 2097152u
#define PAIR 16384u
#define SLOT 688128u
#define AGO 589824u
#define AGT 655360u
#define SG0 22
#define TP0 86
#define NXB 95
#define XB_BYTES (NXB * XBLK)
#define SLOT_AT(i) ((unsigned)((i) / 3) * XBLK + (unsigned)((i) % 3) * SLOT)
#define HT_OFF SLOT_AT(65)
#define CTAB_OFF (HT_OFF + 16384u)
#define MF_OFF (HT_OFF + 20480u)
#define TMO_OFF (MF_OFF + 15u * 128u)
#define RDY_OFF(k) (MF_OFF + 4096u + (k) * 10240u)
#define MF_BYTES 16384u
#define TRC_OFF SLOT_AT(64)
#define CAL_OFF (XBLK - 16384u)
#define P_KV 96u
#define P_H 112u
#define P_X3 117u
#define P_FA 121u
#define P_FB 122u
#define ARMK 8
#define NCAL 8
#define CALT 560u
#define CALM 1
#define SRING 12
#define TRING 64
__device__ __forceinline__ unsigned kv2(const unsigned (&u)[4], unsigned v, v2u w0, v2u w1) {
  const float a[4] = {hi2f(w0.y), lo2f(w0.y), hi2f(w0.x), lo2f(w0.x)}, b[4] = {hi2f(w1.y), lo2f(w1.y), hi2f(w1.x), lo2f(w1.x)};
  unsigned r = pk2(lo2f(u[0]) * a[0], hi2f(u[0]) * b[0]);
#pragma unroll
  for (int i = 1; i < 4; ++i) { const unsigned q = pk2(lo2f(u[i]) * a[i], hi2f(u[i]) * b[i]); r = pk2(lo2f(r) + lo2f(q), hi2f(r) + hi2f(q)); }
  return pk2(lo2f(r) * lo2f(v), hi2f(r) * hi2f(v));
}
struct PreP {
  const u16* kk; const u16* vv; const u16* shift_k; const u16* ssm_k; const u16* ssm_D; u16* yT; u16* kvT;
  const u16* Wq; const u16* Wk; const u16* Wv; const u16* Wo; unsigned* xb; const u16* mem0;
};
#define PRE_FILL 112
#define PFN 2
__global__ __launch_bounds__(256, 4) void k_pre(PreP p) {
  __shared__ __attribute__((aligned(16))) float sh[64 * 36];
  const int tid = threadIdx.x, b0 = blockIdx.x, b = b0 < 240 ? b0 : (b0 < 528 ? b0 + PRE_FILL + 256 : b0 - 288);
  const __amdgpu_buffer_rsrc_t rx = __builtin_amdgcn_make_buffer_rsrc((void*)p.xb, 0, XB_BYTES, 0x00020000);
  const v4u ones = {0xFFFFFFFFu, 0xFFFFFFFFu, 0xFFFFFFFFu, 0xFFFFFFFFu}, zero = {0, 0, 0, 0};
  if (b < 96) {
    if (tid >= 64) return;
    const unsigned cb = (unsigned)b * XBLK + CAL_OFF;
    __builtin_amdgcn_raw_buffer_store_b128(zero, rx, (unsigned)((tid >> 3) & 1) * 8192u + (unsigned)(tid >> 4) * 256u + (unsigned)(tid & 7) * 16u, cb, 16);
    asm volatile("s_waitcnt vmcnt(0)" ::: "memory");
    unsigned mn[2] = {~0u, ~0u};
    for (int i = 0; i < NCAL; ++i) {
      const unsigned long long c0 = __builtin_amdgcn_s_memtime();
      asm volatile("s_waitcnt lgkmcnt(0)" ::: "memory");
      const unsigned v = __builtin_amdgcn_raw_buffer_load_b32(rx, 0, cb + (unsigned)(i & 1) * 8192u + (unsigned)(i >> 1) * 256u, 16);
      asm volatile("s_waitcnt vmcnt(0)" :: "v"(v) : "memory");
      const unsigned d = (unsigned)(__builtin_amdgcn_s_memtime() - c0);
      if (i & 1) mn[1] = d < mn[1] ? d : mn[1]; else mn[0] = d < mn[0] ? d : mn[0];
      const unsigned lo = mn[0] < mn[1] ? mn[0] : mn[1], hi = mn[0] < mn[1] ? mn[1] : mn[0];
      if (i >= CALM && lo < CALT && hi > lo + 100u) break;
    }
    unsigned xcc; asm volatile("s_getreg_b32 %0, hwreg(HW_REG_XCC_ID)" : "=s"(xcc));
    const unsigned res = mn[1] < mn[0] ? 1u : 0u;
    if (tid == 0) __builtin_amdgcn_raw_buffer_store_b32(res ^ ((xcc >> 2) & 1u), rx, 0, CTAB_OFF + (unsigned)b * 4u, 16);
  } else if (b < 240) {
    const __amdgpu_buffer_rsrc_t rk = __builtin_amdgcn_make_buffer_rsrc((void*)p.kk, 0, S_LEN * DM * 2, 0x00020000);
    float (*kx)[132] = (float (*)[132])sh;
    float (*cf)[140] = (float (*)[140])(sh + 4 * 132);
    const int d0 = (b - 96) * 4, q = tid & 3, dd = (tid >> 2) & 3, t0 = (tid >> 4) * 8;
    const float Dv = bf2f(p.ssm_D[d0 + dd]);
    {
      const int s = tid >> 1, d = d0 + (tid & 1) * 2;
      unsigned u[4];
#pragma unroll
      for (int a = 0; a < 4; ++a) u[a] = __builtin_amdgcn_raw_buffer_load_b32(rk, (unsigned)((s - 3 + a) * DM + d) * 2u, 0, 0);
      const unsigned vr = *(const unsigned*)&p.vv[s * DM + d], c0 = *(const unsigned*)&p.ssm_k[(long)(d0 + (tid >> 6)) * S_LEN + (tid & 63) * 2];
      const v4u w = *(const v4u*)&p.shift_k[d * 4];
      const unsigned r = kv2(u, vr, (v2u){w.x, w.y}, (v2u){w.z, w.w});
      kx[(tid & 1) * 2][s] = lo2f(r); kx[(tid & 1) * 2 + 1][s] = hi2f(r);
      *(f32x2*)&cf[tid >> 6][8 + (tid & 63) * 2] = (f32x2){lo2f(c0), hi2f(c0)};
      if (tid < 32) cf[tid >> 3][tid & 7] = 0.f;
    }
    __syncthreads();
    float y[8];
#pragma unroll
    for (int i = 0; i < 8; ++i) y[i] = 0.f;
#pragma unroll 1
    for (int j = 8 * q; j <= t0; j += 32) {
      f32x4 kk[2], cw[4];
#pragma unroll
      for (int x = 0; x < 2; ++x) kk[x] = *(const f32x4*)&kx[dd][j + 4 * x];
#pragma unroll
      for (int x = 0; x < 4; ++x) cw[x] = *(const f32x4*)&cf[dd][t0 - j + 4 * x];
#pragma unroll
      for (int jj = 0; jj < 8; ++jj)
#pragma unroll
        for (int i = 0; i < 8; ++i) y[i] = __builtin_fmaf(cw[(8 + i - jj) >> 2][(8 + i - jj) & 3], kk[jj >> 2][jj & 3], y[i]);
    }
#pragma unroll
    for (int i = 0; i < 8; ++i) {
      y[i] += __uint_as_float((unsigned)__builtin_amdgcn_mov_dpp((int)__float_as_uint(y[i]), 0xB1, 0xf, 0xf, true));
      y[i] += __uint_as_float((unsigned)__builtin_amdgcn_mov_dpp((int)__float_as_uint(y[i]), 0x4E, 0xf, 0xf, true));
    }
    const float ya = q == 0 ? y[0] : (q == 1 ? y[2] : (q == 2 ? y[4] : y[6])), yb = q == 0 ? y[1] : (q == 1 ? y[3] : (q == 2 ? y[5] : y[7]));
    const int t = t0 + 2 * q, f = (d0 >> 3) & 15;
    const unsigned r0 = pk2(ya, yb), r1 = pk2(kx[dd][t] * Dv, kx[dd][t + 1] * Dv), o = pk2(lo2f(r0) + lo2f(r1), hi2f(r0) + hi2f(r1));
    u16* dst = p.yT + (t >> 4) * 9216 + (d0 >> 2) * 64 + dd;
    dst[((t & 15) ^ f) << 2] = (u16)(o & 0xFFFFu); dst[(((t + 1) & 15) ^ f) << 2] = (u16)(o >> 16);
  } else if (b < 240 + PRE_FILL) {
    const unsigned i0 = (unsigned)(b - 240) * 256u + tid;
    if (i0 < (SLOT - AGO) / 16u) {
      const unsigned o = AGO + i0 * 16u, s = o < AGT ? (i0 >> 11) * 4u + ((o >> 10) & 3u) : 8u, r = o & 1023u;
      const unsigned gm = s < 8u ? r >> 8 : (r >> 6) & 3u, m3 = (r >> 4) & 3u, ga = s < 8u ? (r >> 6) & 3u : r >> 8, row = 4u * (gm & 1u) + 2u * (gm >> 1) + (m3 & 1u), half = m3 >> 1;
      const unsigned* src = (const unsigned*)(p.mem0 + row * DM + 64u * s + 16u * ga + 2u * half);
      __builtin_amdgcn_raw_buffer_store_b128((v4u){src[0], src[2], src[4], src[6]}, rx, o, 0, 16);
    }
    for (unsigned i = i0; i < 131072u + MF_BYTES / 16u; i += PRE_FILL * 256)
      __builtin_amdgcn_raw_buffer_store_b128(zero, rx, i < 131072u ? (SG0 + (i >> 11)) * XBLK + P_FA * PAIR + (i & 2047u) * 16u : MF_OFF + (i & 2047u) * 16u, 0, 16);
    for (unsigned i = i0; i < 2u * SLOT / 16u; i += PRE_FILL * 256) __builtin_amdgcn_raw_buffer_store_b128(ones, rx, SLOT + i * 16u, 0, 16);
  } else if (b >= 240 + PRE_FILL + 256) {
    const __amdgpu_buffer_rsrc_t rk = __builtin_amdgcn_make_buffer_rsrc((void*)p.kk, 0, S_LEN * DM * 2, 0x00020000);
    u16 (*tile)[72] = (u16 (*)[72])sh;
    const int bid = b - 240 - PRE_FILL - 256, d0 = (bid % 9) * 64, s0 = (bid / 9) * 64;
    const int sr = (tid >> 3) * 2, d = d0 + (tid & 7) * 8;
    v4u kr[5], vr[2], w[4];
#pragma unroll
    for (int a = 0; a < 5; ++a) kr[a] = __builtin_amdgcn_raw_buffer_load_b128(rk, (unsigned)((s0 + sr - 3 + a) * DM + d) * 2u, 0, 0);
#pragma unroll
    for (int a = 0; a < 2; ++a) vr[a] = *(const v4u*)&p.vv[(long)(s0 + sr + a) * DM + d];
#pragma unroll
    for (int a = 0; a < 4; ++a) w[a] = *(const v4u*)&p.shift_k[(d + 2 * a) * 4];
    __builtin_amdgcn_sched_barrier(0);
#pragma unroll
    for (int it = 0; it < 2; ++it) {
      v4u o;
#pragma unroll
      for (int j = 0; j < 4; ++j) { const unsigned u[4] = {kr[it][j], kr[it + 1][j], kr[it + 2][j], kr[it + 3][j]}; o[j] = kv2(u, vr[it][j], (v2u){w[j].x, w[j].y}, (v2u){w[j].z, w[j].w}); }
      *(v4u*)&tile[sr + it][(tid & 7) * 8] = o;
    }
    __syncthreads();
    const int lane = tid & 63, wv = tid >> 6, g = lane >> 4, r = (lane >> 2) & 3, c = lane & 3;
    v2u o[4];
#pragma unroll
    for (int i = 0; i < 4; ++i) o[i] = __builtin_bit_cast(v2u, __builtin_amdgcn_ds_read_tr16_b64_v4i16((__attribute__((address_space(3))) v4s*)&tile[16 * g + 4 * i + r][16 * wv + 4 * c]));
    u16* dst = p.kvT + (long)(d0 + 16 * wv + (lane & 15)) * S_LEN + s0 + 16 * g;
    *(v4u*)dst = (v4u){o[0].x, o[0].y, o[1].x, o[1].y};
    *(v4u*)(dst + 8) = (v4u){o[2].x, o[2].y, o[3].x, o[3].y};
  } else {
    const int x = b & 7, k = (b - 240 - PRE_FILL) >> 3;
    unsigned long long pc; unsigned l0, l1, l2, l3; int rel;
    asm volatile("s_getpc_b64 %0\nLPRE:\n\ts_mov_b32 %1, LKBEG-LPRE\n\ts_mov_b32 %2, LCHEND-LKBEG\n\ts_mov_b32 %3, LQVEND-LKBEG\n\ts_mov_b32 %4, LQKEND-LKBEG\n\ts_mov_b32 %5, LAPEND-LKBEG"
                 : "=s"(pc), "=s"(rel), "=s"(l0), "=s"(l1), "=s"(l2), "=s"(l3));
    l0 = l0 > l1 ? l0 : l1; l2 = l2 > l3 ? l2 : l3; l0 = l0 > l2 ? l0 : l2;
    const unsigned o = (unsigned)(k * 64 + (tid & 63)) * 64u;
    unsigned a[PFN + 2];
    a[PFN] = *(const __attribute__((address_space(1))) unsigned*)(pc + rel + (o < l0 ? o : 0u));
    a[PFN + 1] = __builtin_amdgcn_raw_buffer_load_b32(rx, k == 31 ? tid * XBLK + CAL_OFF + 8064u : 0xF0000000u, 0, 16);
#pragma unroll
    for (int i = 0; i < PFN; ++i) {
      const int q = (i * 32 + k) * 256 + tid, r2 = q >> 5, gr = r2 >> 5, row = (gr / 3) * 64 + (x >> 2) * 32 + (r2 & 31), off = (q & 31) * 32, ty = gr % 3;
      a[i] = *(const unsigned*)(gr < 27 ? (ty == 0 ? p.Wq : (ty == 1 ? p.Wk : p.Wv)) + (long)row * HD + (x & 3) * 1024 + off
                                        : p.Wo + ((long)(x & 3) * DM + (gr < 36 ? gr - 27 : 0) * 64 + (x >> 2) * 32 + (r2 & 31)) * 1024 + off);
    }
#pragma unroll
    for (int i = 0; i < PFN + 2; ++i) asm volatile("" :: "v"(a[i]));
  }
}

#define CREV_STRIDE 2208
#define LWAIT() asm volatile("s_waitcnt lgkmcnt(0)" ::: "memory")
template <class E, class F>
__device__ __forceinline__ void conv_task(const u16* __restrict__ kvT, const u16* __restrict__ ssm_k, const u16* __restrict__ ssm_D, u16* yT, u16* shb, int d, E early, F fill) {
  const int lane = threadIdx.x & 63;
  u16* crev = shb + 64;
  u16* kvs = crev + 4 * CREV_STRIDE;
  const int X0 = 2048 + 63;
  v4u cv[4], kq[4];
#pragma unroll
  for (int j = 0; j < 4; ++j) { cv[j] = *(const v4u*)&ssm_k[(long)d * S_LEN + lane * 32 + 8 * j]; kq[j] = *(const v4u*)&kvT[(long)d * S_LEN + (j * 64 + lane) * 8]; }
  const float Dv = bf2f(ssm_D[d]);
  __builtin_amdgcn_sched_barrier(0);
  early();
  __builtin_amdgcn_sched_barrier(0);
  const v4u z4 = {0u, 0u, 0u, 0u};
  *(v4u*)&kvs[(lane >> 2) * 40 + (lane & 3) * 8] = z4;
  if (lane < 32) *(v4u*)&crev[(lane >> 3) * CREV_STRIDE + (lane & 7) * 8] = z4;
  if (lane < 36) *(v4u*)&crev[(lane / 9) * CREV_STRIDE + 2112 + (lane % 9) * 8] = z4;
  {
    unsigned r[18];
#pragma unroll
    for (int k = 0; k < 16; ++k) { const unsigned u = cv[3 - (k >> 2)][3 - (k & 3)]; r[k] = __builtin_amdgcn_alignbyte(u, u, 2); }
#pragma unroll
    for (int k = 0; k < 2; ++k) r[16 + k] = (unsigned)__builtin_amdgcn_update_dpp(0, (int)r[k], 0x138, 0xf, 0xf, true);
    LWAIT();
#pragma unroll
    for (int rho = 0; rho < 4; ++rho)
#pragma unroll
      for (int j = 0; j < 4; ++j) {
        v4u o;
#pragma unroll
        for (int k = 0; k < 4; ++k) { const int q = 4 * j + k + (rho >> 1); o[k] = (rho & 1) ? __builtin_amdgcn_alignbyte(r[q + 1], r[q], 2) : r[q]; }
        *(v4u*)&crev[rho * CREV_STRIDE + 2080 - 32 * lane + 8 * j] = o;
      }
    if (lane == 63) {
      *(v2u*)&crev[1 * CREV_STRIDE + 60] = (v2u){0u, r[0] << 16};
      *(v2u*)&crev[2 * CREV_STRIDE + 60] = (v2u){0u, r[0]};
      *(v2u*)&crev[3 * CREV_STRIDE + 60] = (v2u){r[0] << 16, __builtin_amdgcn_alignbyte(r[1], r[0], 2)};
    }
#pragma unroll
    for (int j = 0; j < 4; ++j) *(v4u*)&kvs[(16 + j * 16 + (lane >> 2)) * 40 + (lane & 3) * 8] = kq[j];
  }
  fill();
  LWAIT();
  const int n = lane & 15, g = lane >> 4;
  const int rho = (X0 - n) & 3;
  f32x4 acc[4][2];
#pragma unroll
  for (int q = 0; q < 8; ++q) acc[q >> 1][q & 1] = (f32x4){0.f, 0.f, 0.f, 0.f};
  {
    typedef __attribute__((address_space(3))) const v2u* l2p;
    typedef __attribute__((address_space(3))) const bf16x8* l4p;
    unsigned a0 = (unsigned)(size_t)(__attribute__((address_space(3))) u16*)&crev[rho * CREV_STRIDE + (X0 - (n - 8 * g) - rho)] - 3232u, a1 = a0 + 8u;
    unsigned b0 = (unsigned)(size_t)(__attribute__((address_space(3))) u16*)&kvs[(14 + n) * 40 + 8 * g];
    asm volatile("" : "+v"(a1));
#define AFR(m, f) ({ const unsigned o_ = 3232u - 64u * (m) - 1024u * ((f) >> 1) - 32u * ((f) & 1); const v2u lo_ = *(l2p)(size_t)(a0 + o_), hi_ = *(l2p)(size_t)(a1 + o_); \
                     __builtin_bit_cast(bf16x8, (v4u){lo_.x, lo_.y, hi_.x, hi_.y}); })
#define BFR(m, j) (*(l4p)(size_t)(b0 + 80u * (2 - (m)) + 1280u * (j)))
    bf16x8 af[2][8], bf[2][4];
#pragma unroll
    for (int f = 0; f < 8; ++f) { af[0][f] = AFR(0, f); af[1][f] = af[0][f]; __builtin_amdgcn_sched_barrier(0); }
#pragma unroll
    for (int j = 0; j < 4; ++j) { bf[0][j] = BFR(0, j); bf[1][j] = bf[0][j]; }
#pragma unroll 1
    for (int mo = 0; mo < 8; ++mo) {
#pragma unroll
      for (int m = 0; m < 2; ++m) {
        int k = 0;
#pragma unroll
        for (int a = 0; a < 4; ++a)
#pragma unroll
          for (int ct = a; ct < 4; ++ct)
#pragma unroll
            for (int ti = 0; ti < 2; ++ti) {
              acc[ct][ti] = __builtin_amdgcn_mfma_f32_16x16x32_bf16(__builtin_bit_cast(bfx8, af[m][2 * a + ti]), __builtin_bit_cast(bfx8, bf[m][ct - a]), acc[ct][ti], 0, 0, 0);
              if (k < 2) af[m ^ 1][k] = AFR(m + 1, k);
              else if (k < 6) bf[m ^ 1][k - 2] = BFR(m + 1, k - 2);
              else if (k < 12) af[m ^ 1][k - 4] = AFR(m + 1, k - 4);
              __builtin_amdgcn_sched_barrier(0);
              ++k;
            }
      }
      a0 -= 128u; a1 -= 128u; b0 -= 160u;
    }
  }
  u16* yo = crev;
  LWAIT();
#pragma unroll
  for (int q = 0; q < 8; ++q) {
    const int o = (16 * (q >> 1) + n) * 40 + 16 * (q & 1) + 4 * g;
    const v2u kx = *(const v2u*)&kvs[16 * 40 + o];
    const f32x4 y = acc[q >> 1][q & 1];
    const unsigned y0 = pk2(y[0], y[1]), y1 = pk2(y[2], y[3]), r0 = pk2(lo2f(kx.x) * Dv, hi2f(kx.x) * Dv), r1 = pk2(lo2f(kx.y) * Dv, hi2f(kx.y) * Dv);
    *(v2u*)&yo[o] = (v2u){pk2(lo2f(y0) + lo2f(r0), hi2f(y0) + hi2f(r0)), pk2(lo2f(y1) + lo2f(r1), hi2f(y1) + hi2f(r1))};
  }
  LWAIT();
  __builtin_amdgcn_s_barrier();
  asm volatile("" ::: "memory");
  {
    const int tid = threadIdx.x, f = (d >> 3) & 15;
    const u16* y0 = shb - (tid >> 6) * 16384 + 64;
#pragma unroll
    for (int k = 0; k < 4; ++k) {
      const int tile = 32 * k + (tid >> 3), t = 16 * tile + 2 * ((tid & 7) ^ (f >> 1));
      unsigned u[4];
#pragma unroll
      for (int c = 0; c < 4; ++c) u[c] = *(const unsigned*)&y0[c * 16384 + (t >> 5) * 40 + (t & 31)];
      const v2u e = {__builtin_amdgcn_perm(u[1], u[0], 0x05040100u), __builtin_amdgcn_perm(u[3], u[2], 0x05040100u)}, o = {__builtin_amdgcn_perm(u[1], u[0], 0x07060302u), __builtin_amdgcn_perm(u[3], u[2], 0x07060302u)};
      if (tile >= NFC / 16) __builtin_amdgcn_raw_buffer_store_b128((f & 1) ? (v4u){o.x, o.y, e.x, e.y} : (v4u){e.x, e.y, o.x, o.y}, __builtin_amdgcn_make_buffer_rsrc((void*)yT, 0, DM * S_LEN * 2, 0x00020000),
                                                                   tile * 18432 + (tid & 7) * 16, (d >> 2) * 128, 16);
    }
  }
}

#define NCHW 64
#define NTOKW 192
#define PSL 4
#define SENT 0xFFFFFFFFu
#define SPIN_CAP 300000u
struct MP {
  const u16* Wq; const u16* Wk; const u16* Wv; const u16* Wo; u16* out; unsigned* xb;
  const u16* xin; u16* yT; u16* kvT; const u16* ssm_k; const u16* ssm_D;
};

__device__ __forceinline__ MP getp() {
  MP r;
#if defined(__HIP_DEVICE_COMPILE__)
  const __attribute__((address_space(4))) unsigned long long* k = (const __attribute__((address_space(4))) unsigned long long*)__builtin_amdgcn_kernarg_segment_ptr();
  asm volatile("" : "+s"(k));
#define GP(T, i) ((T*)(__attribute__((address_space(1))) T*)k[i])
  r.Wq = GP(const u16, 0);
  r.Wk = GP(const u16, 1);
  r.Wv = GP(const u16, 2);
  r.Wo = GP(const u16, 3);
  r.out = GP(u16, 4);
  r.xb = GP(unsigned, 5);
  r.xin = GP(const u16, 6);
  r.yT = GP(u16, 7);
  r.kvT = GP(u16, 8);
  r.ssm_k = GP(const u16, 9);
  r.ssm_D = GP(const u16, 10);
#endif
  return r;
}
typedef __amdgpu_buffer_rsrc_t rsrc_t;
__device__ __forceinline__ rsrc_t mkr(const void* p, unsigned bytes) { return __builtin_amdgcn_make_buffer_rsrc((void*)p, 0, bytes, 0x00020000); }
__device__ __forceinline__ void st16(rsrc_t r, unsigned vo, unsigned so, v4u v) { __builtin_amdgcn_raw_buffer_store_b128(v, r, vo, so, 16); }
__device__ __forceinline__ void st4(rsrc_t r, unsigned vo, unsigned so, unsigned v) { __builtin_amdgcn_raw_buffer_store_b32(v, r, vo, so, 16); }
__device__ __forceinline__ v4u ld16(rsrc_t r, unsigned vo, unsigned so) { return __builtin_amdgcn_raw_buffer_load_b128(r, vo, so, 16); }
__device__ __forceinline__ v2u ld8(rsrc_t r, unsigned vo, unsigned so) { return __builtin_amdgcn_raw_buffer_load_b64(r, vo, so, 16); }
__device__ __forceinline__ unsigned ld4(rsrc_t r, unsigned vo, unsigned so) { return __builtin_amdgcn_raw_buffer_load_b32(r, vo, so, 16); }
__device__ __forceinline__ unsigned opq(unsigned x) { asm volatile("" : "+v"(x)); return x; }

__device__ __forceinline__ unsigned long long die_mask(rsrc_t X, unsigned first, int L, int lane) { return __ballot((ld4(X, (unsigned)lane * 4u, CTAB_OFF + first * 4u) & 1u) != (unsigned)L); }
#define SEGB(m, s) ((unsigned)(SG0 + (s)) * XBLK + (unsigned)(((m) >> (s)) & 1ull) * 8192u)
__device__ __forceinline__ bool spin_fail(unsigned& spins, rsrc_t F) {
  ++spins;
  if (spins > SPIN_CAP) { unsigned one = 0x40000000u | (blockIdx.x << 8) | threadIdx.x; asm volatile("" : "+v"(one)); if (__builtin_amdgcn_readfirstlane(ld4(F, 0, TMO_OFF)) == 0u) st4(F, 0, TMO_OFF, one); return true; }
  if ((spins & 1023u) == 0u) return __builtin_amdgcn_readfirstlane(ld4(F, 0, TMO_OFF)) != 0;
  return false;
}
__device__ __forceinline__ unsigned rtc() { return (unsigned)__builtin_amdgcn_s_memrealtime(); }
#define BAR() do { asm volatile("s_waitcnt lgkmcnt(0)" ::: "memory"); __builtin_amdgcn_s_barrier(); asm volatile("" ::: "memory"); } while (0)
#define SB() __builtin_amdgcn_sched_barrier(0)
#define MF "v_mfma_f32_16x16x32_bf16 "
#define VM0() __builtin_amdgcn_s_waitcnt(0x0F70)
#define DL1 12
#define SCH_B0 48
#define DLL1 6
#define DLL2 0
#define DLF 24
#define SCH_PEN 0
#define DLX 4
#define LATE 2
#define TKL 28
#define UP 1
#define DN 100
#define DL3 16
#define FIN -3
#define CH_FIN CH_S(FIN)
#define DLT 16
#define DLY 20
#define CH_DLT CH_S(DLT)
#define CH_DLY CH_S(DLY)
#define ES 6
#define ED 4
#define EB 8
#define CH_ES CH_S(ES)
#define CH_EX "%[ex]"
#define DRYL 1
#define CH_DRYL CH_S(DRYL)
#define SMAP 0x1023u
#define RO1 4
#define CH_RO1 CH_S(RO1)
#define RSL 2
#define CH_S2(x) #x
#define CH_S(x) CH_S2(x)
#define CH_TOKCHK "s_cbranch_vccz LG3_%=\n\t"
#define CH_SG0 CH_S(SG0)
#define CH_ARMD "%[armd]"
#define CH_ARMS "%[arms]"
#define CH_SLOT "688128"
#define CH_SKIP "32768"
#define CH_DLL1 CH_S(DLL1)
#define CH_DLL2 CH_S(DLL2)
#define CH_DLF CH_S(DLF)
#define CH_B0 CH_S(SCH_B0)
#define CH_PEN CH_S(SCH_PEN)
#define CH_RSL CH_S(RSL)
#define CH_DL3 CH_S(DL3)
#define CH_UP CH_S(UP)
#define CH_DN CH_S(DN)
#define CH_DLX CH_S(DLX)
#define CH_LATE CH_S(LATE)
#define CH_DL1L "%[dl1l]"
#define CH_TKL CH_S(TKL)
#define CH_SCHW "656384"
#define CH_CAP "%[cap]"
#define CH_CTAB "%[ctab]"
#define CH_RING "%[ring]"
#define CH_TRM "%[trm]"
#define FDL 6
#define CH_FDL CH_S(FDL)
#define CH_ASM \
  "s_mov_b32 s84, " CH_CAP "\n\t" \
  "s_mov_b32 s83, " CH_CAP "\n\t" \
  "s_mov_b32 s68, 0\n\t" \
  "s_mov_b32 s74, 0\n\t" \
  "s_mov_b32 s76, 0\n\t" \
  "s_mov_b32 s77, 0\n\t" \
  "s_mov_b32 s78, 0\n\t" \
  "s_mov_b32 s79, 0\n\t" \
  "s_mov_b32 s56, 0xcccccccc\n\t" \
  "s_mov_b32 s57, 0xcccccccc\n\t" \
  "s_mov_b32 s58, 0x33333333\n\t" \
  "s_mov_b32 s59, 0x33333333\n\t" \
  "s_mov_b32 s24, 0x01010101\n\t" \
  "s_mov_b32 s25, 0\n\t" \
  "s_mov_b32 s88, 0x3f803f80\n\t" \
  "s_mov_b32 s89, 0x3f803f80\n\t" \
  "s_mov_b32 s66, s65\n\t" \
  "s_mov_b32 s47, s65\n\t" \
  "s_mov_b32 s72, 0\n\t" \
  "s_mov_b32 s73, 0\n\t" \
  "s_mov_b32 s67, 0\n\t" \
  "s_mul_i32 s60, s40, 36864\n\t" \
  "s_add_u32 s60, s60, 16384\n\t" \
  "v_add_u32 v70, s60, v69\n\t" \
  "v_bfe_i32 v68, v69, 5, 1\n\t" \
  "v_and_b32 v68, 0xaaaaaaaa, v68\n\t" \
  "v_or_b32 v68, 0x44444444, v68\n\t" \
  "v_and_b32 v73, 32, v69\n\t" \
  "v_lshl_add_u32 v73, v73, 5, v69\n\t" \
  "LLOAD_%=:\n\t" \
  "v_lshrrev_b32 v74, 2, v69\n\t" \
  "s_mov_b32 s60, " CH_CTAB "\n\t" \
  "buffer_load_dword v72, v74, s[28:31], s60 offen offset:88 sc1\n\t" \
  "buffer_load_dword v74, v74, s[28:31], s60 offen sc1\n\t" \
  "v_lshrrev_b32 v71, 8, v69\n\t" \
  "v_lshlrev_b32 v1, 6, v71\n\t" \
  "v_and_b32 v2, 0xf0, v69\n\t" \
  "v_xor_b32 v1, v1, v2\n\t" \
  "v_lshl_add_u32 v71, v71, 13, v1\n\t" \
  "s_lshl_b32 s60, s40, 17\n\t" \
  "v_add_u32 v71, s60, v71\n\t" \
  "v_lshrrev_b32 v1, 7, v69\n\t" \
  "v_and_b32 v2, 0x70, v69\n\t" \
  "s_movk_i32 s60, 1152\n\t" \
  "v_mad_u32_u24 v75, v1, s60, v2\n\t" \
  "s_lshl_b32 s54, s40, 12\n\t" \
  "s_lshl_b32 s55, s40, 7\n\t" \
  "s_mul_i32 s75, s40, 36864\n\t" \
  "s_add_u32 s75, s75, 1024\n\t" \
  "s_mov_b32 s62, 0x05040100\n\t" \
  "s_mov_b32 s63, 0x07060302\n\t" \
  "s_barrier\n\t" \
  "s_xor_b32 s61, s40, 0\n\t" \
  "s_mul_i32 s61, s61, 36864\n\t" \
  "s_add_u32 s61, s61, s55\n\t" \
  "s_add_u32 m0, s54, 0\n\t" \
  "s_add_u32 s60, s61, 0\n\t" \
  "buffer_load_dwordx4 v75, %[Wo], s60 offen lds\n\t" \
  "s_add_u32 m0, s54, 1024\n\t" \
  "s_add_u32 s60, s61, 9216\n\t" \
  "buffer_load_dwordx4 v75, %[Wo], s60 offen lds\n\t" \
  "s_add_u32 m0, s54, 2048\n\t" \
  "s_add_u32 s60, s61, 18432\n\t" \
  "buffer_load_dwordx4 v75, %[Wo], s60 offen lds\n\t" \
  "s_add_u32 m0, s54, 3072\n\t" \
  "s_add_u32 s60, s61, 27648\n\t" \
  "buffer_load_dwordx4 v75, %[Wo], s60 offen lds\n\t" \
  "s_add_u32 m0, s54, 16384\n\t" \
  "s_mov_b32 s60, 0\n\t" \
  "buffer_load_dwordx4 v71, %[Wq], s60 offen lds\n\t" \
  "s_add_u32 m0, s54, 17408\n\t" \
  "s_mov_b32 s60, 32768\n\t" \
  "buffer_load_dwordx4 v71, %[Wq], s60 offen lds\n\t" \
  "s_add_u32 m0, s54, 18432\n\t" \
  "s_mov_b32 s60, 65536\n\t" \
  "buffer_load_dwordx4 v71, %[Wq], s60 offen lds\n\t" \
  "s_add_u32 m0, s54, 19456\n\t" \
  "s_mov_b32 s60, 98304\n\t" \
  "buffer_load_dwordx4 v71, %[Wq], s60 offen lds\n\t" \
  "s_add_u32 m0, s54, 53248\n\t" \
  "s_mov_b32 s60, 0\n\t" \
  "buffer_load_dwordx4 v71, %[Wk], s60 offen lds\n\t" \
  "s_add_u32 m0, s54, 54272\n\t" \
  "s_mov_b32 s60, 32768\n\t" \
  "buffer_load_dwordx4 v71, %[Wk], s60 offen lds\n\t" \
  "s_add_u32 m0, s54, 55296\n\t" \
  "s_mov_b32 s60, 65536\n\t" \
  "buffer_load_dwordx4 v71, %[Wk], s60 offen lds\n\t" \
  "s_add_u32 m0, s54, 56320\n\t" \
  "s_mov_b32 s60, 98304\n\t" \
  "buffer_load_dwordx4 v71, %[Wk], s60 offen lds\n\t" \
  "s_xor_b32 s61, s40, 0\n\t" \
  "s_mul_i32 s61, s61, 36864\n\t" \
  "s_add_u32 s61, s61, s55\n\t" \
  "s_add_u32 m0, s54, 90112\n\t" \
  "s_add_u32 s60, s61, 512\n\t" \
  "buffer_load_dwordx4 v75, %[Wo], s60 offen lds\n\t" \
  "s_add_u32 m0, s54, 91136\n\t" \
  "s_add_u32 s60, s61, 9728\n\t" \
  "buffer_load_dwordx4 v75, %[Wo], s60 offen lds\n\t" \
  "s_add_u32 m0, s54, 92160\n\t" \
  "s_add_u32 s60, s61, 18944\n\t" \
  "buffer_load_dwordx4 v75, %[Wo], s60 offen lds\n\t" \
  "s_add_u32 m0, s54, 93184\n\t" \
  "s_add_u32 s60, s61, 28160\n\t" \
  "buffer_load_dwordx4 v75, %[Wo], s60 offen lds\n\t" \
  "s_add_u32 m0, s54, 126976\n\t" \
  "s_mov_b32 s60, 0\n\t" \
  "buffer_load_dwordx4 v71, %[Wv], s60 offen lds\n\t" \
  "s_add_u32 m0, s54, 128000\n\t" \
  "s_mov_b32 s60, 32768\n\t" \
  "buffer_load_dwordx4 v71, %[Wv], s60 offen lds\n\t" \
  "s_add_u32 m0, s54, 129024\n\t" \
  "s_mov_b32 s60, 65536\n\t" \
  "buffer_load_dwordx4 v71, %[Wv], s60 offen lds\n\t" \
  "s_add_u32 m0, s54, 130048\n\t" \
  "s_mov_b32 s60, 98304\n\t" \
  "buffer_load_dwordx4 v71, %[Wv], s60 offen lds\n\t" \
  "v_and_b32 v1, 0x300, v69\n\t" \
  "v_lshlrev_b32 v65, 3, v1\n\t" \
  "v_and_b32 v1, 0xc0, v69\n\t" \
  "v_lshl_add_u32 v65, v1, 2, v65\n\t" \
  "s_lshl_b32 s60, s40, 6\n\t" \
  "v_xor_b32 v1, s60, v1\n\t" \
  "v_and_b32 v2, 48, v69\n\t" \
  "v_add3_u32 v65, v65, v1, v2\n\t" \
  "v_lshrrev_b32 v2, 1, v2\n\t" \
  "v_sub_u32 v66, v65, v2\n\t" \
  "v_bfe_u32 v0, v69, 8, 1\n\t" \
  "v_bfe_u32 v1, v69, 9, 1\n\t" \
  "v_lshlrev_b32 v0, 2, v0\n\t" \
  "v_add_u32 v0, v0, v1\n\t" \
  "v_lshlrev_b32 v0, 9, v0\n\t" \
  "v_lshrrev_b32 v1, 1, v69\n\t" \
  "v_and_b32 v1, 0x60, v1\n\t" \
  "v_and_b32 v2, 16, v69\n\t" \
  "v_bfe_u32 v3, v69, 5, 1\n\t" \
  "v_add3_u32 v64, v0, v1, v2\n\t" \
  "v_lshl_add_u32 v64, v3, 2, v64\n\t" \
  "v_add_u32 v64, s54, v64\n\t" \
  "v_lshl_add_u32 v67, v3, 3, v0\n\t" \
  "v_bfe_u32 v1, v69, 7, 1\n\t" \
  "v_lshl_add_u32 v67, v1, 2, v67\n\t" \
  "v_bfe_u32 v1, v69, 6, 1\n\t" \
  "v_lshl_add_u32 v67, v1, 4, v67\n\t" \
  "v_bfe_u32 v1, v69, 4, 1\n\t" \
  "v_lshl_add_u32 v67, v1, 1, v67\n\t" \
  "s_lshl_b32 s60, s40, 5\n\t" \
  "v_add_u32 v67, s60, v67\n\t" \
  "v_add_u32 v78, 0, v64\n\t" \
  "v_add_u32 v79, 1024, v64\n\t" \
  "s_waitcnt vmcnt(16) lgkmcnt(0)\n\t" \
  "s_barrier\n\t" \
  "ds_read2_b32 v[80:81], v78 offset0:0 offset1:2\n\t" \
  "ds_read2_b32 v[82:83], v78 offset0:32 offset1:34\n\t" \
  "ds_read2_b32 v[84:85], v78 offset0:64 offset1:66\n\t" \
  "ds_read2_b32 v[86:87], v78 offset0:96 offset1:98\n\t" \
  "ds_read2_b32 v[88:89], v79 offset0:0 offset1:2\n\t" \
  "ds_read2_b32 v[90:91], v79 offset0:32 offset1:34\n\t" \
  "ds_read2_b32 v[92:93], v79 offset0:64 offset1:66\n\t" \
  "ds_read2_b32 v[94:95], v79 offset0:96 offset1:98\n\t" \
  "s_waitcnt lgkmcnt(6)\n\t" \
  "v_perm_b32 v112, v82, v80, s62\n\t" \
  "v_perm_b32 v113, v82, v80, s63\n\t" \
  "s_waitcnt lgkmcnt(4)\n\t" \
  "v_perm_b32 v114, v86, v84, s62\n\t" \
  "v_perm_b32 v115, v86, v84, s63\n\t" \
  "s_waitcnt lgkmcnt(2)\n\t" \
  "v_perm_b32 v116, v90, v88, s62\n\t" \
  "v_perm_b32 v117, v90, v88, s63\n\t" \
  "s_waitcnt lgkmcnt(0)\n\t" \
  "v_perm_b32 v118, v94, v92, s62\n\t" \
  "v_perm_b32 v119, v94, v92, s63\n\t" \
  "ds_write_b128 v70, v[112:115] offset:20480\n\t" \
  "ds_write_b128 v70, v[116:119] offset:21504\n\t" \
  "v_perm_b32 v120, v83, v81, s62\n\t" \
  "v_perm_b32 v121, v83, v81, s63\n\t" \
  "v_perm_b32 v122, v87, v85, s62\n\t" \
  "v_perm_b32 v123, v87, v85, s63\n\t" \
  "v_perm_b32 v124, v91, v89, s62\n\t" \
  "v_perm_b32 v125, v91, v89, s63\n\t" \
  "v_perm_b32 v126, v95, v93, s62\n\t" \
  "v_perm_b32 v127, v95, v93, s63\n\t" \
  "ds_write_b128 v70, v[120:123] offset:16384\n\t" \
  "ds_write_b128 v70, v[124:127] offset:17408\n\t" \
  "v_add_u32 v78, 16384, v65\n\t" \
  "s_waitcnt vmcnt(12) lgkmcnt(0)\n\t" \
  "s_barrier\n\t" \
  "ds_read_b64_tr_b16 v[0:1], v78 offset:0\n\t" \
  "ds_read_b64_tr_b16 v[2:3], v78 offset:1024\n\t" \
  "ds_read_b64_tr_b16 v[4:5], v78 offset:8192\n\t" \
  "ds_read_b64_tr_b16 v[6:7], v78 offset:9216\n\t" \
  "ds_read_b64_tr_b16 v[8:9], v78 offset:8\n\t" \
  "ds_read_b64_tr_b16 v[10:11], v78 offset:1032\n\t" \
  "ds_read_b64_tr_b16 v[12:13], v78 offset:8200\n\t" \
  "ds_read_b64_tr_b16 v[14:15], v78 offset:9224\n\t" \
  "s_add_u32 m0, s54, 0\n\t" \
  "s_mov_b32 s60, 524288\n\t" \
  "buffer_load_dwordx4 v71, %[Wq], s60 offen lds\n\t" \
  "s_add_u32 m0, s54, 1024\n\t" \
  "s_mov_b32 s60, 557056\n\t" \
  "buffer_load_dwordx4 v71, %[Wq], s60 offen lds\n\t" \
  "s_add_u32 m0, s54, 2048\n\t" \
  "s_mov_b32 s60, 589824\n\t" \
  "buffer_load_dwordx4 v71, %[Wq], s60 offen lds\n\t" \
  "s_add_u32 m0, s54, 3072\n\t" \
  "s_mov_b32 s60, 622592\n\t" \
  "buffer_load_dwordx4 v71, %[Wq], s60 offen lds\n\t" \
  "v_add_u32 v78, 53248, v65\n\t" \
  "s_waitcnt vmcnt(12) lgkmcnt(0)\n\t" \
  "s_barrier\n\t" \
  "ds_read_b64_tr_b16 a[0:1], v78 offset:0\n\t" \
  "ds_read_b64_tr_b16 a[2:3], v78 offset:1024\n\t" \
  "ds_read_b64_tr_b16 a[4:5], v78 offset:8192\n\t" \
  "ds_read_b64_tr_b16 a[6:7], v78 offset:9216\n\t" \
  "ds_read_b64_tr_b16 a[8:9], v78 offset:8\n\t" \
  "ds_read_b64_tr_b16 a[10:11], v78 offset:1032\n\t" \
  "ds_read_b64_tr_b16 a[12:13], v78 offset:8200\n\t" \
  "ds_read_b64_tr_b16 a[14:15], v78 offset:9224\n\t" \
  "s_xor_b32 s61, s40, 1\n\t" \
  "s_mul_i32 s61, s61, 36864\n\t" \
  "s_add_u32 s61, s61, s55\n\t" \
  "s_add_u32 m0, s54, 16384\n\t" \
  "s_add_u32 s60, s61, 0\n\t" \
  "buffer_load_dwordx4 v75, %[Wo], s60 offen lds\n\t" \
  "s_add_u32 m0, s54, 17408\n\t" \
  "s_add_u32 s60, s61, 9216\n\t" \
  "buffer_load_dwordx4 v75, %[Wo], s60 offen lds\n\t" \
  "s_add_u32 m0, s54, 18432\n\t" \
  "s_add_u32 s60, s61, 18432\n\t" \
  "buffer_load_dwordx4 v75, %[Wo], s60 offen lds\n\t" \
  "s_add_u32 m0, s54, 19456\n\t" \
  "s_add_u32 s60, s61, 27648\n\t" \
  "buffer_load_dwordx4 v75, %[Wo], s60 offen lds\n\t" \
  "v_add_u32 v78, 90112, v64\n\t" \
  "v_add_u32 v79, 91136, v64\n\t" \
  "s_waitcnt vmcnt(12) lgkmcnt(0)\n\t" \
  "s_barrier\n\t" \
  "ds_read2_b32 v[80:81], v78 offset0:0 offset1:2\n\t" \
  "ds_read2_b32 v[82:83], v78 offset0:32 offset1:34\n\t" \
  "ds_read2_b32 v[84:85], v78 offset0:64 offset1:66\n\t" \
  "ds_read2_b32 v[86:87], v78 offset0:96 offset1:98\n\t" \
  "ds_read2_b32 v[88:89], v79 offset0:0 offset1:2\n\t" \
  "ds_read2_b32 v[90:91], v79 offset0:32 offset1:34\n\t" \
  "ds_read2_b32 v[92:93], v79 offset0:64 offset1:66\n\t" \
  "ds_read2_b32 v[94:95], v79 offset0:96 offset1:98\n\t" \
  "s_add_u32 m0, s54, 53248\n\t" \
  "s_mov_b32 s60, 524288\n\t" \
  "buffer_load_dwordx4 v71, %[Wk], s60 offen lds\n\t" \
  "s_add_u32 m0, s54, 54272\n\t" \
  "s_mov_b32 s60, 557056\n\t" \
  "buffer_load_dwordx4 v71, %[Wk], s60 offen lds\n\t" \
  "s_add_u32 m0, s54, 55296\n\t" \
  "s_mov_b32 s60, 589824\n\t" \
  "buffer_load_dwordx4 v71, %[Wk], s60 offen lds\n\t" \
  "s_add_u32 m0, s54, 56320\n\t" \
  "s_mov_b32 s60, 622592\n\t" \
  "buffer_load_dwordx4 v71, %[Wk], s60 offen lds\n\t" \
  "s_waitcnt lgkmcnt(6)\n\t" \
  "v_perm_b32 v112, v82, v80, s62\n\t" \
  "v_perm_b32 v113, v82, v80, s63\n\t" \
  "s_waitcnt lgkmcnt(4)\n\t" \
  "v_perm_b32 v114, v86, v84, s62\n\t" \
  "v_perm_b32 v115, v86, v84, s63\n\t" \
  "s_waitcnt lgkmcnt(2)\n\t" \
  "v_perm_b32 v116, v90, v88, s62\n\t" \
  "v_perm_b32 v117, v90, v88, s63\n\t" \
  "s_waitcnt lgkmcnt(0)\n\t" \
  "v_perm_b32 v118, v94, v92, s62\n\t" \
  "v_perm_b32 v119, v94, v92, s63\n\t" \
  "ds_write_b128 v70, v[112:115] offset:22528\n\t" \
  "ds_write_b128 v70, v[116:119] offset:23552\n\t" \
  "v_perm_b32 v120, v83, v81, s62\n\t" \
  "v_perm_b32 v121, v83, v81, s63\n\t" \
  "v_perm_b32 v122, v87, v85, s62\n\t" \
  "v_perm_b32 v123, v87, v85, s63\n\t" \
  "v_perm_b32 v124, v91, v89, s62\n\t" \
  "v_perm_b32 v125, v91, v89, s63\n\t" \
  "v_perm_b32 v126, v95, v93, s62\n\t" \
  "v_perm_b32 v127, v95, v93, s63\n\t" \
  "ds_write_b128 v70, v[120:123] offset:18432\n\t" \
  "ds_write_b128 v70, v[124:127] offset:19456\n\t" \
  "v_add_u32 v78, 126976, v66\n\t" \
  "s_waitcnt vmcnt(12) lgkmcnt(0)\n\t" \
  "s_barrier\n\t" \
  "ds_read_b64_tr_b16 a[16:17], v78 offset:0\n\t" \
  "ds_read_b64_tr_b16 a[18:19], v78 offset:1024\n\t" \
  "ds_read_b64_tr_b16 a[20:21], v78 offset:8192\n\t" \
  "ds_read_b64_tr_b16 a[22:23], v78 offset:9216\n\t" \
  "ds_read_b64_tr_b16 a[24:25], v78 offset:32\n\t" \
  "ds_read_b64_tr_b16 a[26:27], v78 offset:1056\n\t" \
  "ds_read_b64_tr_b16 a[28:29], v78 offset:8224\n\t" \
  "ds_read_b64_tr_b16 a[30:31], v78 offset:9248\n\t" \
  "s_add_u32 m0, s54, 90112\n\t" \
  "s_mov_b32 s60, 524288\n\t" \
  "buffer_load_dwordx4 v71, %[Wv], s60 offen lds\n\t" \
  "s_add_u32 m0, s54, 91136\n\t" \
  "s_mov_b32 s60, 557056\n\t" \
  "buffer_load_dwordx4 v71, %[Wv], s60 offen lds\n\t" \
  "s_add_u32 m0, s54, 92160\n\t" \
  "s_mov_b32 s60, 589824\n\t" \
  "buffer_load_dwordx4 v71, %[Wv], s60 offen lds\n\t" \
  "s_add_u32 m0, s54, 93184\n\t" \
  "s_mov_b32 s60, 622592\n\t" \
  "buffer_load_dwordx4 v71, %[Wv], s60 offen lds\n\t" \
  "v_add_u32 v78, 0, v65\n\t" \
  "s_waitcnt vmcnt(12) lgkmcnt(0)\n\t" \
  "s_barrier\n\t" \
  "ds_read_b64_tr_b16 a[32:33], v78 offset:0\n\t" \
  "ds_read_b64_tr_b16 a[34:35], v78 offset:1024\n\t" \
  "ds_read_b64_tr_b16 a[36:37], v78 offset:8192\n\t" \
  "ds_read_b64_tr_b16 a[38:39], v78 offset:9216\n\t" \
  "ds_read_b64_tr_b16 a[40:41], v78 offset:8\n\t" \
  "ds_read_b64_tr_b16 a[42:43], v78 offset:1032\n\t" \
  "ds_read_b64_tr_b16 a[44:45], v78 offset:8200\n\t" \
  "ds_read_b64_tr_b16 a[46:47], v78 offset:9224\n\t" \
  "s_xor_b32 s61, s40, 1\n\t" \
  "s_mul_i32 s61, s61, 36864\n\t" \
  "s_add_u32 s61, s61, s55\n\t" \
  "s_add_u32 m0, s54, 126976\n\t" \
  "s_add_u32 s60, s61, 512\n\t" \
  "buffer_load_dwordx4 v75, %[Wo], s60 offen lds\n\t" \
  "s_add_u32 m0, s54, 128000\n\t" \
  "s_add_u32 s60, s61, 9728\n\t" \
  "buffer_load_dwordx4 v75, %[Wo], s60 offen lds\n\t" \
  "s_add_u32 m0, s54, 129024\n\t" \
  "s_add_u32 s60, s61, 18944\n\t" \
  "buffer_load_dwordx4 v75, %[Wo], s60 offen lds\n\t" \
  "s_add_u32 m0, s54, 130048\n\t" \
  "s_add_u32 s60, s61, 28160\n\t" \
  "buffer_load_dwordx4 v75, %[Wo], s60 offen lds\n\t" \
  "v_add_u32 v78, 16384, v64\n\t" \
  "v_add_u32 v79, 17408, v64\n\t" \
  "s_waitcnt vmcnt(12) lgkmcnt(0)\n\t" \
  "s_barrier\n\t" \
  "ds_read2_b32 v[80:81], v78 offset0:0 offset1:2\n\t" \
  "ds_read2_b32 v[82:83], v78 offset0:32 offset1:34\n\t" \
  "ds_read2_b32 v[84:85], v78 offset0:64 offset1:66\n\t" \
  "ds_read2_b32 v[86:87], v78 offset0:96 offset1:98\n\t" \
  "ds_read2_b32 v[88:89], v79 offset0:0 offset1:2\n\t" \
  "ds_read2_b32 v[90:91], v79 offset0:32 offset1:34\n\t" \
  "ds_read2_b32 v[92:93], v79 offset0:64 offset1:66\n\t" \
  "ds_read2_b32 v[94:95], v79 offset0:96 offset1:98\n\t" \
  "s_add_u32 m0, s54, 0\n\t" \
  "s_mov_b32 s60, 1048576\n\t" \
  "buffer_load_dwordx4 v71, %[Wq], s60 offen lds\n\t" \
  "s_add_u32 m0, s54, 1024\n\t" \
  "s_mov_b32 s60, 1081344\n\t" \
  "buffer_load_dwordx4 v71, %[Wq], s60 offen lds\n\t" \
  "s_add_u32 m0, s54, 2048\n\t" \
  "s_mov_b32 s60, 1114112\n\t" \
  "buffer_load_dwordx4 v71, %[Wq], s60 offen lds\n\t" \
  "s_add_u32 m0, s54, 3072\n\t" \
  "s_mov_b32 s60, 1146880\n\t" \
  "buffer_load_dwordx4 v71, %[Wq], s60 offen lds\n\t" \
  "s_waitcnt lgkmcnt(6)\n\t" \
  "v_perm_b32 v112, v82, v80, s62\n\t" \
  "v_perm_b32 v113, v82, v80, s63\n\t" \
  "s_waitcnt lgkmcnt(4)\n\t" \
  "v_perm_b32 v114, v86, v84, s62\n\t" \
  "v_perm_b32 v115, v86, v84, s63\n\t" \
  "s_waitcnt lgkmcnt(2)\n\t" \
  "v_perm_b32 v116, v90, v88, s62\n\t" \
  "v_perm_b32 v117, v90, v88, s63\n\t" \
  "s_waitcnt lgkmcnt(0)\n\t" \
  "v_perm_b32 v118, v94, v92, s62\n\t" \
  "v_perm_b32 v119, v94, v92, s63\n\t" \
  "ds_write_b128 v70, v[112:115] offset:24576\n\t" \
  "ds_write_b128 v70, v[116:119] offset:25600\n\t" \
  "v_perm_b32 v200, v83, v81, s62\n\t" \
  "v_perm_b32 v201, v83, v81, s63\n\t" \
  "v_perm_b32 v202, v87, v85, s62\n\t" \
  "v_perm_b32 v203, v87, v85, s63\n\t" \
  "v_perm_b32 v204, v91, v89, s62\n\t" \
  "v_perm_b32 v205, v91, v89, s63\n\t" \
  "v_perm_b32 v206, v95, v93, s62\n\t" \
  "v_perm_b32 v207, v95, v93, s63\n\t" \
  "v_add_u32 v78, 53248, v65\n\t" \
  "s_waitcnt vmcnt(12) lgkmcnt(0)\n\t" \
  "s_barrier\n\t" \
  "ds_read_b64_tr_b16 a[48:49], v78 offset:0\n\t" \
  "ds_read_b64_tr_b16 a[50:51], v78 offset:1024\n\t" \
  "ds_read_b64_tr_b16 a[52:53], v78 offset:8192\n\t" \
  "ds_read_b64_tr_b16 a[54:55], v78 offset:9216\n\t" \
  "ds_read_b64_tr_b16 a[56:57], v78 offset:8\n\t" \
  "ds_read_b64_tr_b16 a[58:59], v78 offset:1032\n\t" \
  "ds_read_b64_tr_b16 a[60:61], v78 offset:8200\n\t" \
  "ds_read_b64_tr_b16 a[62:63], v78 offset:9224\n\t" \
  "s_add_u32 m0, s54, 16384\n\t" \
  "s_mov_b32 s60, 1048576\n\t" \
  "buffer_load_dwordx4 v71, %[Wk], s60 offen lds\n\t" \
  "s_add_u32 m0, s54, 17408\n\t" \
  "s_mov_b32 s60, 1081344\n\t" \
  "buffer_load_dwordx4 v71, %[Wk], s60 offen lds\n\t" \
  "s_add_u32 m0, s54, 18432\n\t" \
  "s_mov_b32 s60, 1114112\n\t" \
  "buffer_load_dwordx4 v71, %[Wk], s60 offen lds\n\t" \
  "s_add_u32 m0, s54, 19456\n\t" \
  "s_mov_b32 s60, 1146880\n\t" \
  "buffer_load_dwordx4 v71, %[Wk], s60 offen lds\n\t" \
  "v_add_u32 v78, 90112, v66\n\t" \
  "s_waitcnt vmcnt(12) lgkmcnt(0)\n\t" \
  "s_barrier\n\t" \
  "ds_read_b64_tr_b16 a[64:65], v78 offset:0\n\t" \
  "ds_read_b64_tr_b16 a[66:67], v78 offset:1024\n\t" \
  "ds_read_b64_tr_b16 a[68:69], v78 offset:8192\n\t" \
  "ds_read_b64_tr_b16 a[70:71], v78 offset:9216\n\t" \
  "ds_read_b64_tr_b16 a[72:73], v78 offset:32\n\t" \
  "ds_read_b64_tr_b16 a[74:75], v78 offset:1056\n\t" \
  "ds_read_b64_tr_b16 a[76:77], v78 offset:8224\n\t" \
  "ds_read_b64_tr_b16 a[78:79], v78 offset:9248\n\t" \
  "s_xor_b32 s61, s40, 2\n\t" \
  "s_mul_i32 s61, s61, 36864\n\t" \
  "s_add_u32 s61, s61, s55\n\t" \
  "s_add_u32 m0, s54, 53248\n\t" \
  "s_add_u32 s60, s61, 0\n\t" \
  "buffer_load_dwordx4 v75, %[Wo], s60 offen lds\n\t" \
  "s_add_u32 m0, s54, 54272\n\t" \
  "s_add_u32 s60, s61, 9216\n\t" \
  "buffer_load_dwordx4 v75, %[Wo], s60 offen lds\n\t" \
  "s_add_u32 m0, s54, 55296\n\t" \
  "s_add_u32 s60, s61, 18432\n\t" \
  "buffer_load_dwordx4 v75, %[Wo], s60 offen lds\n\t" \
  "s_add_u32 m0, s54, 56320\n\t" \
  "s_add_u32 s60, s61, 27648\n\t" \
  "buffer_load_dwordx4 v75, %[Wo], s60 offen lds\n\t" \
  "v_add_u32 v78, 126976, v64\n\t" \
  "v_add_u32 v79, 128000, v64\n\t" \
  "s_waitcnt vmcnt(12) lgkmcnt(0)\n\t" \
  "s_barrier\n\t" \
  "ds_read2_b32 v[80:81], v78 offset0:0 offset1:2\n\t" \
  "ds_read2_b32 v[82:83], v78 offset0:32 offset1:34\n\t" \
  "ds_read2_b32 v[84:85], v78 offset0:64 offset1:66\n\t" \
  "ds_read2_b32 v[86:87], v78 offset0:96 offset1:98\n\t" \
  "ds_read2_b32 v[88:89], v79 offset0:0 offset1:2\n\t" \
  "ds_read2_b32 v[90:91], v79 offset0:32 offset1:34\n\t" \
  "ds_read2_b32 v[92:93], v79 offset0:64 offset1:66\n\t" \
  "ds_read2_b32 v[94:95], v79 offset0:96 offset1:98\n\t" \
  "s_add_u32 m0, s54, 90112\n\t" \
  "s_mov_b32 s60, 1048576\n\t" \
  "buffer_load_dwordx4 v71, %[Wv], s60 offen lds\n\t" \
  "s_add_u32 m0, s54, 91136\n\t" \
  "s_mov_b32 s60, 1081344\n\t" \
  "buffer_load_dwordx4 v71, %[Wv], s60 offen lds\n\t" \
  "s_add_u32 m0, s54, 92160\n\t" \
  "s_mov_b32 s60, 1114112\n\t" \
  "buffer_load_dwordx4 v71, %[Wv], s60 offen lds\n\t" \
  "s_add_u32 m0, s54, 93184\n\t" \
  "s_mov_b32 s60, 1146880\n\t" \
  "buffer_load_dwordx4 v71, %[Wv], s60 offen lds\n\t" \
  "s_waitcnt lgkmcnt(6)\n\t" \
  "v_perm_b32 v112, v82, v80, s62\n\t" \
  "v_perm_b32 v113, v82, v80, s63\n\t" \
  "s_waitcnt lgkmcnt(4)\n\t" \
  "v_perm_b32 v114, v86, v84, s62\n\t" \
  "v_perm_b32 v115, v86, v84, s63\n\t" \
  "s_waitcnt lgkmcnt(2)\n\t" \
  "v_perm_b32 v116, v90, v88, s62\n\t" \
  "v_perm_b32 v117, v90, v88, s63\n\t" \
  "s_waitcnt lgkmcnt(0)\n\t" \
  "v_perm_b32 v118, v94, v92, s62\n\t" \
  "v_perm_b32 v119, v94, v92, s63\n\t" \
  "ds_write_b128 v70, v[112:115] offset:26624\n\t" \
  "ds_write_b128 v70, v[116:119] offset:27648\n\t" \
  "v_perm_b32 v232, v83, v81, s62\n\t" \
  "v_perm_b32 v233, v83, v81, s63\n\t" \
  "v_perm_b32 v234, v87, v85, s62\n\t" \
  "v_perm_b32 v235, v87, v85, s63\n\t" \
  "v_perm_b32 v236, v91, v89, s62\n\t" \
  "v_perm_b32 v237, v91, v89, s63\n\t" \
  "v_perm_b32 v238, v95, v93, s62\n\t" \
  "v_perm_b32 v239, v95, v93, s63\n\t" \
  "v_add_u32 v78, 0, v65\n\t" \
  "s_waitcnt vmcnt(12) lgkmcnt(0)\n\t" \
  "s_barrier\n\t" \
  "ds_read_b64_tr_b16 a[80:81], v78 offset:0\n\t" \
  "ds_read_b64_tr_b16 a[82:83], v78 offset:1024\n\t" \
  "ds_read_b64_tr_b16 a[84:85], v78 offset:8192\n\t" \
  "ds_read_b64_tr_b16 a[86:87], v78 offset:9216\n\t" \
  "ds_read_b64_tr_b16 a[88:89], v78 offset:8\n\t" \
  "ds_read_b64_tr_b16 a[90:91], v78 offset:1032\n\t" \
  "ds_read_b64_tr_b16 a[92:93], v78 offset:8200\n\t" \
  "ds_read_b64_tr_b16 a[94:95], v78 offset:9224\n\t" \
  "s_add_u32 m0, s54, 126976\n\t" \
  "s_mov_b32 s60, 1572864\n\t" \
  "buffer_load_dwordx4 v71, %[Wq], s60 offen lds\n\t" \
  "s_add_u32 m0, s54, 128000\n\t" \
  "s_mov_b32 s60, 1605632\n\t" \
  "buffer_load_dwordx4 v71, %[Wq], s60 offen lds\n\t" \
  "s_add_u32 m0, s54, 129024\n\t" \
  "s_mov_b32 s60, 1638400\n\t" \
  "buffer_load_dwordx4 v71, %[Wq], s60 offen lds\n\t" \
  "s_add_u32 m0, s54, 130048\n\t" \
  "s_mov_b32 s60, 1671168\n\t" \
  "buffer_load_dwordx4 v71, %[Wq], s60 offen lds\n\t" \
  "v_add_u32 v78, 16384, v65\n\t" \
  "s_waitcnt vmcnt(12) lgkmcnt(0)\n\t" \
  "s_barrier\n\t" \
  "ds_read_b64_tr_b16 v[32:33], v78 offset:0\n\t" \
  "ds_read_b64_tr_b16 v[34:35], v78 offset:1024\n\t" \
  "ds_read_b64_tr_b16 v[36:37], v78 offset:8192\n\t" \
  "ds_read_b64_tr_b16 v[38:39], v78 offset:9216\n\t" \
  "ds_read_b64_tr_b16 a[96:97], v78 offset:8\n\t" \
  "ds_read_b64_tr_b16 a[98:99], v78 offset:1032\n\t" \
  "ds_read_b64_tr_b16 a[100:101], v78 offset:8200\n\t" \
  "ds_read_b64_tr_b16 a[102:103], v78 offset:9224\n\t" \
  "s_xor_b32 s61, s40, 2\n\t" \
  "s_mul_i32 s61, s61, 36864\n\t" \
  "s_add_u32 s61, s61, s55\n\t" \
  "s_add_u32 m0, s54, 0\n\t" \
  "s_add_u32 s60, s61, 512\n\t" \
  "buffer_load_dwordx4 v75, %[Wo], s60 offen lds\n\t" \
  "s_add_u32 m0, s54, 1024\n\t" \
  "s_add_u32 s60, s61, 9728\n\t" \
  "buffer_load_dwordx4 v75, %[Wo], s60 offen lds\n\t" \
  "s_add_u32 m0, s54, 2048\n\t" \
  "s_add_u32 s60, s61, 18944\n\t" \
  "buffer_load_dwordx4 v75, %[Wo], s60 offen lds\n\t" \
  "s_add_u32 m0, s54, 3072\n\t" \
  "s_add_u32 s60, s61, 28160\n\t" \
  "buffer_load_dwordx4 v75, %[Wo], s60 offen lds\n\t" \
  "v_add_u32 v78, 53248, v64\n\t" \
  "v_add_u32 v79, 54272, v64\n\t" \
  "s_waitcnt vmcnt(12) lgkmcnt(0)\n\t" \
  "s_barrier\n\t" \
  "ds_read2_b32 v[80:81], v78 offset0:0 offset1:2\n\t" \
  "ds_read2_b32 v[82:83], v78 offset0:32 offset1:34\n\t" \
  "ds_read2_b32 v[84:85], v78 offset0:64 offset1:66\n\t" \
  "ds_read2_b32 v[86:87], v78 offset0:96 offset1:98\n\t" \
  "ds_read2_b32 v[88:89], v79 offset0:0 offset1:2\n\t" \
  "ds_read2_b32 v[90:91], v79 offset0:32 offset1:34\n\t" \
  "ds_read2_b32 v[92:93], v79 offset0:64 offset1:66\n\t" \
  "ds_read2_b32 v[94:95], v79 offset0:96 offset1:98\n\t" \
  "s_add_u32 m0, s54, 16384\n\t" \
  "s_mov_b32 s60, 1572864\n\t" \
  "buffer_load_dwordx4 v71, %[Wk], s60 offen lds\n\t" \
  "s_add_u32 m0, s54, 17408\n\t" \
  "s_mov_b32 s60, 1605632\n\t" \
  "buffer_load_dwordx4 v71, %[Wk], s60 offen lds\n\t" \
  "s_add_u32 m0, s54, 18432\n\t" \
  "s_mov_b32 s60, 1638400\n\t" \
  "buffer_load_dwordx4 v71, %[Wk], s60 offen lds\n\t" \
  "s_add_u32 m0, s54, 19456\n\t" \
  "s_mov_b32 s60, 1671168\n\t" \
  "buffer_load_dwordx4 v71, %[Wk], s60 offen lds\n\t" \
  "s_waitcnt lgkmcnt(6)\n\t" \
  "v_perm_b32 v112, v82, v80, s62\n\t" \
  "v_perm_b32 v113, v82, v80, s63\n\t" \
  "s_waitcnt lgkmcnt(4)\n\t" \
  "v_perm_b32 v114, v86, v84, s62\n\t" \
  "v_perm_b32 v115, v86, v84, s63\n\t" \
  "s_waitcnt lgkmcnt(2)\n\t" \
  "v_perm_b32 v116, v90, v88, s62\n\t" \
  "v_perm_b32 v117, v90, v88, s63\n\t" \
  "s_waitcnt lgkmcnt(0)\n\t" \
  "v_perm_b32 v118, v94, v92, s62\n\t" \
  "v_perm_b32 v119, v94, v92, s63\n\t" \
  "ds_write_b128 v70, v[112:115] offset:28672\n\t" \
  "ds_write_b128 v70, v[116:119] offset:29696\n\t" \
  "v_perm_b32 v208, v83, v81, s62\n\t" \
  "v_perm_b32 v209, v83, v81, s63\n\t" \
  "v_perm_b32 v210, v87, v85, s62\n\t" \
  "v_perm_b32 v211, v87, v85, s63\n\t" \
  "v_perm_b32 v212, v91, v89, s62\n\t" \
  "v_perm_b32 v213, v91, v89, s63\n\t" \
  "v_perm_b32 v214, v95, v93, s62\n\t" \
  "v_perm_b32 v215, v95, v93, s63\n\t" \
  "v_add_u32 v78, 90112, v66\n\t" \
  "s_waitcnt vmcnt(12) lgkmcnt(0)\n\t" \
  "s_barrier\n\t" \
  "ds_read_b64_tr_b16 a[104:105], v78 offset:0\n\t" \
  "ds_read_b64_tr_b16 a[106:107], v78 offset:1024\n\t" \
  "ds_read_b64_tr_b16 a[108:109], v78 offset:8192\n\t" \
  "ds_read_b64_tr_b16 a[110:111], v78 offset:9216\n\t" \
  "ds_read_b64_tr_b16 a[112:113], v78 offset:32\n\t" \
  "ds_read_b64_tr_b16 a[114:115], v78 offset:1056\n\t" \
  "ds_read_b64_tr_b16 a[116:117], v78 offset:8224\n\t" \
  "ds_read_b64_tr_b16 a[118:119], v78 offset:9248\n\t" \
  "s_add_u32 m0, s54, 53248\n\t" \
  "s_mov_b32 s60, 1572864\n\t" \
  "buffer_load_dwordx4 v71, %[Wv], s60 offen lds\n\t" \
  "s_add_u32 m0, s54, 54272\n\t" \
  "s_mov_b32 s60, 1605632\n\t" \
  "buffer_load_dwordx4 v71, %[Wv], s60 offen lds\n\t" \
  "s_add_u32 m0, s54, 55296\n\t" \
  "s_mov_b32 s60, 1638400\n\t" \
  "buffer_load_dwordx4 v71, %[Wv], s60 offen lds\n\t" \
  "s_add_u32 m0, s54, 56320\n\t" \
  "s_mov_b32 s60, 1671168\n\t" \
  "buffer_load_dwordx4 v71, %[Wv], s60 offen lds\n\t" \
  "v_add_u32 v78, 126976, v65\n\t" \
  "s_waitcnt vmcnt(12) lgkmcnt(0)\n\t" \
  "s_barrier\n\t" \
  "ds_read_b64_tr_b16 a[120:121], v78 offset:0\n\t" \
  "ds_read_b64_tr_b16 a[122:123], v78 offset:1024\n\t" \
  "ds_read_b64_tr_b16 a[124:125], v78 offset:8192\n\t" \
  "ds_read_b64_tr_b16 a[126:127], v78 offset:9216\n\t" \
  "ds_read_b64_tr_b16 v[16:17], v78 offset:8\n\t" \
  "ds_read_b64_tr_b16 v[18:19], v78 offset:1032\n\t" \
  "ds_read_b64_tr_b16 v[20:21], v78 offset:8200\n\t" \
  "ds_read_b64_tr_b16 v[22:23], v78 offset:9224\n\t" \
  "s_xor_b32 s61, s40, 3\n\t" \
  "s_mul_i32 s61, s61, 36864\n\t" \
  "s_add_u32 s61, s61, s55\n\t" \
  "s_add_u32 m0, s54, 90112\n\t" \
  "s_add_u32 s60, s61, 0\n\t" \
  "buffer_load_dwordx4 v75, %[Wo], s60 offen lds\n\t" \
  "s_add_u32 m0, s54, 91136\n\t" \
  "s_add_u32 s60, s61, 9216\n\t" \
  "buffer_load_dwordx4 v75, %[Wo], s60 offen lds\n\t" \
  "s_add_u32 m0, s54, 92160\n\t" \
  "s_add_u32 s60, s61, 18432\n\t" \
  "buffer_load_dwordx4 v75, %[Wo], s60 offen lds\n\t" \
  "s_add_u32 m0, s54, 93184\n\t" \
  "s_add_u32 s60, s61, 27648\n\t" \
  "buffer_load_dwordx4 v75, %[Wo], s60 offen lds\n\t" \
  "v_add_u32 v78, 0, v64\n\t" \
  "v_add_u32 v79, 1024, v64\n\t" \
  "s_waitcnt vmcnt(12) lgkmcnt(0)\n\t" \
  "s_barrier\n\t" \
  "ds_read2_b32 v[80:81], v78 offset0:0 offset1:2\n\t" \
  "ds_read2_b32 v[82:83], v78 offset0:32 offset1:34\n\t" \
  "ds_read2_b32 v[84:85], v78 offset0:64 offset1:66\n\t" \
  "ds_read2_b32 v[86:87], v78 offset0:96 offset1:98\n\t" \
  "ds_read2_b32 v[88:89], v79 offset0:0 offset1:2\n\t" \
  "ds_read2_b32 v[90:91], v79 offset0:32 offset1:34\n\t" \
  "ds_read2_b32 v[92:93], v79 offset0:64 offset1:66\n\t" \
  "ds_read2_b32 v[94:95], v79 offset0:96 offset1:98\n\t" \
  "s_add_u32 m0, s54, 126976\n\t" \
  "s_mov_b32 s60, 2097152\n\t" \
  "buffer_load_dwordx4 v71, %[Wq], s60 offen lds\n\t" \
  "s_add_u32 m0, s54, 128000\n\t" \
  "s_mov_b32 s60, 2129920\n\t" \
  "buffer_load_dwordx4 v71, %[Wq], s60 offen lds\n\t" \
  "s_add_u32 m0, s54, 129024\n\t" \
  "s_mov_b32 s60, 2162688\n\t" \
  "buffer_load_dwordx4 v71, %[Wq], s60 offen lds\n\t" \
  "s_add_u32 m0, s54, 130048\n\t" \
  "s_mov_b32 s60, 2195456\n\t" \
  "buffer_load_dwordx4 v71, %[Wq], s60 offen lds\n\t" \
  "s_waitcnt lgkmcnt(6)\n\t" \
  "v_perm_b32 v112, v82, v80, s62\n\t" \
  "v_perm_b32 v113, v82, v80, s63\n\t" \
  "s_waitcnt lgkmcnt(4)\n\t" \
  "v_perm_b32 v114, v86, v84, s62\n\t" \
  "v_perm_b32 v115, v86, v84, s63\n\t" \
  "s_waitcnt lgkmcnt(2)\n\t" \
  "v_perm_b32 v116, v90, v88, s62\n\t" \
  "v_perm_b32 v117, v90, v88, s63\n\t" \
  "s_waitcnt lgkmcnt(0)\n\t" \
  "v_perm_b32 v118, v94, v92, s62\n\t" \
  "v_perm_b32 v119, v94, v92, s63\n\t" \
  "ds_write_b128 v70, v[112:115] offset:30720\n\t" \
  "ds_write_b128 v70, v[116:119] offset:31744\n\t" \
  "v_perm_b32 v240, v83, v81, s62\n\t" \
  "v_perm_b32 v241, v83, v81, s63\n\t" \
  "v_perm_b32 v242, v87, v85, s62\n\t" \
  "v_perm_b32 v243, v87, v85, s63\n\t" \
  "v_perm_b32 v244, v91, v89, s62\n\t" \
  "v_perm_b32 v245, v91, v89, s63\n\t" \
  "v_perm_b32 v246, v95, v93, s62\n\t" \
  "v_perm_b32 v247, v95, v93, s63\n\t" \
  "v_add_u32 v78, 16384, v65\n\t" \
  "s_waitcnt vmcnt(12) lgkmcnt(0)\n\t" \
  "s_barrier\n\t" \
  "ds_read_b64_tr_b16 a[128:129], v78 offset:0\n\t" \
  "ds_read_b64_tr_b16 a[130:131], v78 offset:1024\n\t" \
  "ds_read_b64_tr_b16 a[132:133], v78 offset:8192\n\t" \
  "ds_read_b64_tr_b16 a[134:135], v78 offset:9216\n\t" \
  "ds_read_b64_tr_b16 a[136:137], v78 offset:8\n\t" \
  "ds_read_b64_tr_b16 a[138:139], v78 offset:1032\n\t" \
  "ds_read_b64_tr_b16 a[140:141], v78 offset:8200\n\t" \
  "ds_read_b64_tr_b16 a[142:143], v78 offset:9224\n\t" \
  "s_add_u32 m0, s54, 0\n\t" \
  "s_mov_b32 s60, 2097152\n\t" \
  "buffer_load_dwordx4 v71, %[Wk], s60 offen lds\n\t" \
  "s_add_u32 m0, s54, 1024\n\t" \
  "s_mov_b32 s60, 2129920\n\t" \
  "buffer_load_dwordx4 v71, %[Wk], s60 offen lds\n\t" \
  "s_add_u32 m0, s54, 2048\n\t" \
  "s_mov_b32 s60, 2162688\n\t" \
  "buffer_load_dwordx4 v71, %[Wk], s60 offen lds\n\t" \
  "s_add_u32 m0, s54, 3072\n\t" \
  "s_mov_b32 s60, 2195456\n\t" \
  "buffer_load_dwordx4 v71, %[Wk], s60 offen lds\n\t" \
  "v_add_u32 v78, 53248, v66\n\t" \
  "s_waitcnt vmcnt(12) lgkmcnt(0)\n\t" \
  "s_barrier\n\t" \
  "ds_read_b64_tr_b16 a[144:145], v78 offset:0\n\t" \
  "ds_read_b64_tr_b16 a[146:147], v78 offset:1024\n\t" \
  "ds_read_b64_tr_b16 a[148:149], v78 offset:8192\n\t" \
  "ds_read_b64_tr_b16 a[150:151], v78 offset:9216\n\t" \
  "ds_read_b64_tr_b16 a[152:153], v78 offset:32\n\t" \
  "ds_read_b64_tr_b16 a[154:155], v78 offset:1056\n\t" \
  "ds_read_b64_tr_b16 a[156:157], v78 offset:8224\n\t" \
  "ds_read_b64_tr_b16 a[158:159], v78 offset:9248\n\t" \
  "s_xor_b32 s61, s40, 3\n\t" \
  "s_mul_i32 s61, s61, 36864\n\t" \
  "s_add_u32 s61, s61, s55\n\t" \
  "s_add_u32 m0, s54, 16384\n\t" \
  "s_add_u32 s60, s61, 512\n\t" \
  "buffer_load_dwordx4 v75, %[Wo], s60 offen lds\n\t" \
  "s_add_u32 m0, s54, 17408\n\t" \
  "s_add_u32 s60, s61, 9728\n\t" \
  "buffer_load_dwordx4 v75, %[Wo], s60 offen lds\n\t" \
  "s_add_u32 m0, s54, 18432\n\t" \
  "s_add_u32 s60, s61, 18944\n\t" \
  "buffer_load_dwordx4 v75, %[Wo], s60 offen lds\n\t" \
  "s_add_u32 m0, s54, 19456\n\t" \
  "s_add_u32 s60, s61, 28160\n\t" \
  "buffer_load_dwordx4 v75, %[Wo], s60 offen lds\n\t" \
  "v_add_u32 v78, 90112, v64\n\t" \
  "v_add_u32 v79, 91136, v64\n\t" \
  "s_waitcnt vmcnt(12) lgkmcnt(0)\n\t" \
  "s_barrier\n\t" \
  "ds_read2_b32 v[80:81], v78 offset0:0 offset1:2\n\t" \
  "ds_read2_b32 v[82:83], v78 offset0:32 offset1:34\n\t" \
  "ds_read2_b32 v[84:85], v78 offset0:64 offset1:66\n\t" \
  "ds_read2_b32 v[86:87], v78 offset0:96 offset1:98\n\t" \
  "ds_read2_b32 v[88:89], v79 offset0:0 offset1:2\n\t" \
  "ds_read2_b32 v[90:91], v79 offset0:32 offset1:34\n\t" \
  "ds_read2_b32 v[92:93], v79 offset0:64 offset1:66\n\t" \
  "ds_read2_b32 v[94:95], v79 offset0:96 offset1:98\n\t" \
  "s_add_u32 m0, s54, 53248\n\t" \
  "s_mov_b32 s60, 2097152\n\t" \
  "buffer_load_dwordx4 v71, %[Wv], s60 offen lds\n\t" \
  "s_add_u32 m0, s54, 54272\n\t" \
  "s_mov_b32 s60, 2129920\n\t" \
  "buffer_load_dwordx4 v71, %[Wv], s60 offen lds\n\t" \
  "s_add_u32 m0, s54, 55296\n\t" \
  "s_mov_b32 s60, 2162688\n\t" \
  "buffer_load_dwordx4 v71, %[Wv], s60 offen lds\n\t" \
  "s_add_u32 m0, s54, 56320\n\t" \
  "s_mov_b32 s60, 2195456\n\t" \
  "buffer_load_dwordx4 v71, %[Wv], s60 offen lds\n\t" \
  "s_waitcnt lgkmcnt(6)\n\t" \
  "v_perm_b32 v192, v82, v80, s62\n\t" \
  "v_perm_b32 v193, v82, v80, s63\n\t" \
  "s_waitcnt lgkmcnt(4)\n\t" \
  "v_perm_b32 v194, v86, v84, s62\n\t" \
  "v_perm_b32 v195, v86, v84, s63\n\t" \
  "s_waitcnt lgkmcnt(2)\n\t" \
  "v_perm_b32 v196, v90, v88, s62\n\t" \
  "v_perm_b32 v197, v90, v88, s63\n\t" \
  "s_waitcnt lgkmcnt(0)\n\t" \
  "v_perm_b32 v198, v94, v92, s62\n\t" \
  "v_perm_b32 v199, v94, v92, s63\n\t" \
  "v_perm_b32 v216, v83, v81, s62\n\t" \
  "v_perm_b32 v217, v83, v81, s63\n\t" \
  "v_perm_b32 v218, v87, v85, s62\n\t" \
  "v_perm_b32 v219, v87, v85, s63\n\t" \
  "v_perm_b32 v220, v91, v89, s62\n\t" \
  "v_perm_b32 v221, v91, v89, s63\n\t" \
  "v_perm_b32 v222, v95, v93, s62\n\t" \
  "v_perm_b32 v223, v95, v93, s63\n\t" \
  "v_add_u32 v78, 126976, v65\n\t" \
  "s_waitcnt vmcnt(12) lgkmcnt(0)\n\t" \
  "s_barrier\n\t" \
  "ds_read_b64_tr_b16 a[160:161], v78 offset:0\n\t" \
  "ds_read_b64_tr_b16 a[162:163], v78 offset:1024\n\t" \
  "ds_read_b64_tr_b16 a[164:165], v78 offset:8192\n\t" \
  "ds_read_b64_tr_b16 a[166:167], v78 offset:9216\n\t" \
  "ds_read_b64_tr_b16 a[168:169], v78 offset:8\n\t" \
  "ds_read_b64_tr_b16 a[170:171], v78 offset:1032\n\t" \
  "ds_read_b64_tr_b16 a[172:173], v78 offset:8200\n\t" \
  "ds_read_b64_tr_b16 a[174:175], v78 offset:9224\n\t" \
  "s_add_u32 m0, s54, 90112\n\t" \
  "s_mov_b32 s60, 2621440\n\t" \
  "buffer_load_dwordx4 v71, %[Wq], s60 offen lds\n\t" \
  "s_add_u32 m0, s54, 91136\n\t" \
  "s_mov_b32 s60, 2654208\n\t" \
  "buffer_load_dwordx4 v71, %[Wq], s60 offen lds\n\t" \
  "s_add_u32 m0, s54, 92160\n\t" \
  "s_mov_b32 s60, 2686976\n\t" \
  "buffer_load_dwordx4 v71, %[Wq], s60 offen lds\n\t" \
  "s_add_u32 m0, s54, 93184\n\t" \
  "s_mov_b32 s60, 2719744\n\t" \
  "buffer_load_dwordx4 v71, %[Wq], s60 offen lds\n\t" \
  "v_add_u32 v78, 0, v65\n\t" \
  "s_waitcnt vmcnt(12) lgkmcnt(0)\n\t" \
  "s_barrier\n\t" \
  "ds_read_b64_tr_b16 a[176:177], v78 offset:0\n\t" \
  "ds_read_b64_tr_b16 a[178:179], v78 offset:1024\n\t" \
  "ds_read_b64_tr_b16 a[180:181], v78 offset:8192\n\t" \
  "ds_read_b64_tr_b16 a[182:183], v78 offset:9216\n\t" \
  "ds_read_b64_tr_b16 a[184:185], v78 offset:8\n\t" \
  "ds_read_b64_tr_b16 a[186:187], v78 offset:1032\n\t" \
  "ds_read_b64_tr_b16 a[188:189], v78 offset:8200\n\t" \
  "ds_read_b64_tr_b16 a[190:191], v78 offset:9224\n\t" \
  "s_add_u32 m0, s54, 126976\n\t" \
  "s_add_u32 s60, s75, 0\n\t" \
  "buffer_load_dwordx4 v75, %[Wo], s60 offen lds\n\t" \
  "s_add_u32 m0, s54, 128000\n\t" \
  "s_add_u32 s60, s75, 9216\n\t" \
  "buffer_load_dwordx4 v75, %[Wo], s60 offen lds\n\t" \
  "s_add_u32 m0, s54, 129024\n\t" \
  "s_add_u32 s60, s75, 18432\n\t" \
  "buffer_load_dwordx4 v75, %[Wo], s60 offen lds\n\t" \
  "s_add_u32 m0, s54, 130048\n\t" \
  "s_add_u32 s60, s75, 27648\n\t" \
  "buffer_load_dwordx4 v75, %[Wo], s60 offen lds\n\t" \
  "v_add_u32 v78, 16384, v64\n\t" \
  "v_add_u32 v79, 17408, v64\n\t" \
  "s_waitcnt vmcnt(12) lgkmcnt(0)\n\t" \
  "s_barrier\n\t" \
  "ds_read2_b32 v[80:81], v78 offset0:0 offset1:2\n\t" \
  "ds_read2_b32 v[82:83], v78 offset0:32 offset1:34\n\t" \
  "ds_read2_b32 v[84:85], v78 offset0:64 offset1:66\n\t" \
  "ds_read2_b32 v[86:87], v78 offset0:96 offset1:98\n\t" \
  "ds_read2_b32 v[88:89], v79 offset0:0 offset1:2\n\t" \
  "ds_read2_b32 v[90:91], v79 offset0:32 offset1:34\n\t" \
  "ds_read2_b32 v[92:93], v79 offset0:64 offset1:66\n\t" \
  "ds_read2_b32 v[94:95], v79 offset0:96 offset1:98\n\t" \
  "s_add_u32 m0, s54, 0\n\t" \
  "s_mov_b32 s60, 2621440\n\t" \
  "buffer_load_dwordx4 v71, %[Wk], s60 offen lds\n\t" \
  "s_add_u32 m0, s54, 1024\n\t" \
  "s_mov_b32 s60, 2654208\n\t" \
  "buffer_load_dwordx4 v71, %[Wk], s60 offen lds\n\t" \
  "s_add_u32 m0, s54, 2048\n\t" \
  "s_mov_b32 s60, 2686976\n\t" \
  "buffer_load_dwordx4 v71, %[Wk], s60 offen lds\n\t" \
  "s_add_u32 m0, s54, 3072\n\t" \
  "s_mov_b32 s60, 2719744\n\t" \
  "buffer_load_dwordx4 v71, %[Wk], s60 offen lds\n\t" \
  "s_waitcnt lgkmcnt(6)\n\t" \
  "v_perm_b32 v224, v82, v80, s62\n\t" \
  "v_perm_b32 v225, v82, v80, s63\n\t" \
  "s_waitcnt lgkmcnt(4)\n\t" \
  "v_perm_b32 v226, v86, v84, s62\n\t" \
  "v_perm_b32 v227, v86, v84, s63\n\t" \
  "s_waitcnt lgkmcnt(2)\n\t" \
  "v_perm_b32 v228, v90, v88, s62\n\t" \
  "v_perm_b32 v229, v90, v88, s63\n\t" \
  "s_waitcnt lgkmcnt(0)\n\t" \
  "v_perm_b32 v230, v94, v92, s62\n\t" \
  "v_perm_b32 v231, v94, v92, s63\n\t" \
  "v_perm_b32 v120, v83, v81, s62\n\t" \
  "v_perm_b32 v121, v83, v81, s63\n\t" \
  "v_perm_b32 v122, v87, v85, s62\n\t" \
  "v_perm_b32 v123, v87, v85, s63\n\t" \
  "v_perm_b32 v124, v91, v89, s62\n\t" \
  "v_perm_b32 v125, v91, v89, s63\n\t" \
  "v_perm_b32 v126, v95, v93, s62\n\t" \
  "v_perm_b32 v127, v95, v93, s63\n\t" \
  "ds_write_b128 v70, v[120:123] offset:32768\n\t" \
  "ds_write_b128 v70, v[124:127] offset:33792\n\t" \
  "v_add_u32 v78, 53248, v66\n\t" \
  "s_waitcnt vmcnt(12) lgkmcnt(0)\n\t" \
  "s_barrier\n\t" \
  "ds_read_b64_tr_b16 a[192:193], v78 offset:0\n\t" \
  "ds_read_b64_tr_b16 a[194:195], v78 offset:1024\n\t" \
  "ds_read_b64_tr_b16 a[196:197], v78 offset:8192\n\t" \
  "ds_read_b64_tr_b16 a[198:199], v78 offset:9216\n\t" \
  "ds_read_b64_tr_b16 v[48:49], v78 offset:32\n\t" \
  "ds_read_b64_tr_b16 v[50:51], v78 offset:1056\n\t" \
  "ds_read_b64_tr_b16 v[52:53], v78 offset:8224\n\t" \
  "ds_read_b64_tr_b16 v[54:55], v78 offset:9248\n\t" \
  "s_add_u32 m0, s54, 16384\n\t" \
  "s_mov_b32 s60, 2621440\n\t" \
  "buffer_load_dwordx4 v71, %[Wv], s60 offen lds\n\t" \
  "s_add_u32 m0, s54, 17408\n\t" \
  "s_mov_b32 s60, 2654208\n\t" \
  "buffer_load_dwordx4 v71, %[Wv], s60 offen lds\n\t" \
  "s_add_u32 m0, s54, 18432\n\t" \
  "s_mov_b32 s60, 2686976\n\t" \
  "buffer_load_dwordx4 v71, %[Wv], s60 offen lds\n\t" \
  "s_add_u32 m0, s54, 19456\n\t" \
  "s_mov_b32 s60, 2719744\n\t" \
  "buffer_load_dwordx4 v71, %[Wv], s60 offen lds\n\t" \
  "v_add_u32 v78, 90112, v65\n\t" \
  "s_waitcnt vmcnt(12) lgkmcnt(0)\n\t" \
  "s_barrier\n\t" \
  "ds_read_b64_tr_b16 a[200:201], v78 offset:0\n\t" \
  "ds_read_b64_tr_b16 a[202:203], v78 offset:1024\n\t" \
  "ds_read_b64_tr_b16 a[204:205], v78 offset:8192\n\t" \
  "ds_read_b64_tr_b16 a[206:207], v78 offset:9216\n\t" \
  "ds_read_b64_tr_b16 v[24:25], v78 offset:8\n\t" \
  "ds_read_b64_tr_b16 v[26:27], v78 offset:1032\n\t" \
  "ds_read_b64_tr_b16 v[28:29], v78 offset:8200\n\t" \
  "ds_read_b64_tr_b16 v[30:31], v78 offset:9224\n\t" \
  "s_add_u32 m0, s54, 53248\n\t" \
  "s_mov_b32 s60, 3145728\n\t" \
  "buffer_load_dwordx4 v71, %[Wq], s60 offen lds\n\t" \
  "s_add_u32 m0, s54, 54272\n\t" \
  "s_mov_b32 s60, 3178496\n\t" \
  "buffer_load_dwordx4 v71, %[Wq], s60 offen lds\n\t" \
  "s_add_u32 m0, s54, 55296\n\t" \
  "s_mov_b32 s60, 3211264\n\t" \
  "buffer_load_dwordx4 v71, %[Wq], s60 offen lds\n\t" \
  "s_add_u32 m0, s54, 56320\n\t" \
  "s_mov_b32 s60, 3244032\n\t" \
  "buffer_load_dwordx4 v71, %[Wq], s60 offen lds\n\t" \
  "v_add_u32 v78, 126976, v67\n\t" \
  "s_waitcnt vmcnt(12) lgkmcnt(0)\n\t" \
  "s_barrier\n\t" \
  "ds_read_u16 v80, v78 offset:0\n\t" \
  "ds_read_u16 v81, v78 offset:128\n\t" \
  "ds_read_u16 v82, v78 offset:4096\n\t" \
  "ds_read_u16 v83, v78 offset:4224\n\t" \
  "ds_read_u16 v84, v78 offset:256\n\t" \
  "ds_read_u16 v85, v78 offset:384\n\t" \
  "ds_read_u16 v86, v78 offset:4352\n\t" \
  "ds_read_u16 v87, v78 offset:4480\n\t" \
  "ds_read_u16 v88, v78 offset:1024\n\t" \
  "ds_read_u16 v89, v78 offset:1152\n\t" \
  "ds_read_u16 v90, v78 offset:5120\n\t" \
  "ds_read_u16 v91, v78 offset:5248\n\t" \
  "ds_read_u16 v92, v78 offset:1280\n\t" \
  "ds_read_u16 v93, v78 offset:1408\n\t" \
  "ds_read_u16 v94, v78 offset:5376\n\t" \
  "ds_read_u16 v95, v78 offset:5504\n\t" \
  "ds_read_u16 v96, v78 offset:8192\n\t" \
  "ds_read_u16 v97, v78 offset:8320\n\t" \
  "ds_read_u16 v98, v78 offset:12288\n\t" \
  "ds_read_u16 v99, v78 offset:12416\n\t" \
  "ds_read_u16 v100, v78 offset:8448\n\t" \
  "ds_read_u16 v101, v78 offset:8576\n\t" \
  "ds_read_u16 v102, v78 offset:12544\n\t" \
  "ds_read_u16 v103, v78 offset:12672\n\t" \
  "ds_read_u16 v104, v78 offset:9216\n\t" \
  "ds_read_u16 v105, v78 offset:9344\n\t" \
  "ds_read_u16 v106, v78 offset:13312\n\t" \
  "ds_read_u16 v107, v78 offset:13440\n\t" \
  "ds_read_u16 v108, v78 offset:9472\n\t" \
  "ds_read_u16 v109, v78 offset:9600\n\t" \
  "ds_read_u16 v110, v78 offset:13568\n\t" \
  "ds_read_u16 v111, v78 offset:13696\n\t" \
  "s_add_u32 m0, s54, 90112\n\t" \
  "s_mov_b32 s60, 3145728\n\t" \
  "buffer_load_dwordx4 v71, %[Wk], s60 offen lds\n\t" \
  "s_add_u32 m0, s54, 91136\n\t" \
  "s_mov_b32 s60, 3178496\n\t" \
  "buffer_load_dwordx4 v71, %[Wk], s60 offen lds\n\t" \
  "s_add_u32 m0, s54, 92160\n\t" \
  "s_mov_b32 s60, 3211264\n\t" \
  "buffer_load_dwordx4 v71, %[Wk], s60 offen lds\n\t" \
  "s_add_u32 m0, s54, 93184\n\t" \
  "s_mov_b32 s60, 3244032\n\t" \
  "buffer_load_dwordx4 v71, %[Wk], s60 offen lds\n\t" \
  "s_waitcnt lgkmcnt(0)\n\t" \
  "v_lshl_or_b32 v248, v81, 16, v80\n\t" \
  "v_lshl_or_b32 v249, v83, 16, v82\n\t" \
  "v_lshl_or_b32 v250, v85, 16, v84\n\t" \
  "v_lshl_or_b32 v251, v87, 16, v86\n\t" \
  "v_lshl_or_b32 v252, v89, 16, v88\n\t" \
  "v_lshl_or_b32 v253, v91, 16, v90\n\t" \
  "v_lshl_or_b32 v254, v93, 16, v92\n\t" \
  "v_lshl_or_b32 v255, v95, 16, v94\n\t" \
  "v_lshl_or_b32 v112, v97, 16, v96\n\t" \
  "v_lshl_or_b32 v113, v99, 16, v98\n\t" \
  "v_lshl_or_b32 v114, v101, 16, v100\n\t" \
  "v_lshl_or_b32 v115, v103, 16, v102\n\t" \
  "v_lshl_or_b32 v116, v105, 16, v104\n\t" \
  "v_lshl_or_b32 v117, v107, 16, v106\n\t" \
  "v_lshl_or_b32 v118, v109, 16, v108\n\t" \
  "v_lshl_or_b32 v119, v111, 16, v110\n\t" \
  "ds_write_b128 v70, v[112:115] offset:34816\n\t" \
  "ds_write_b128 v70, v[116:119] offset:35840\n\t" \
  "v_add_u32 v78, 0, v65\n\t" \
  "s_waitcnt vmcnt(12) lgkmcnt(0)\n\t" \
  "s_barrier\n\t" \
  "ds_read_b64_tr_b16 a[208:209], v78 offset:0\n\t" \
  "ds_read_b64_tr_b16 a[210:211], v78 offset:1024\n\t" \
  "ds_read_b64_tr_b16 a[212:213], v78 offset:8192\n\t" \
  "ds_read_b64_tr_b16 a[214:215], v78 offset:9216\n\t" \
  "ds_read_b64_tr_b16 a[216:217], v78 offset:8\n\t" \
  "ds_read_b64_tr_b16 a[218:219], v78 offset:1032\n\t" \
  "ds_read_b64_tr_b16 a[220:221], v78 offset:8200\n\t" \
  "ds_read_b64_tr_b16 a[222:223], v78 offset:9224\n\t" \
  "s_add_u32 m0, s54, 126976\n\t" \
  "s_mov_b32 s60, 3145728\n\t" \
  "buffer_load_dwordx4 v71, %[Wv], s60 offen lds\n\t" \
  "s_add_u32 m0, s54, 128000\n\t" \
  "s_mov_b32 s60, 3178496\n\t" \
  "buffer_load_dwordx4 v71, %[Wv], s60 offen lds\n\t" \
  "s_add_u32 m0, s54, 129024\n\t" \
  "s_mov_b32 s60, 3211264\n\t" \
  "buffer_load_dwordx4 v71, %[Wv], s60 offen lds\n\t" \
  "s_add_u32 m0, s54, 130048\n\t" \
  "s_mov_b32 s60, 3244032\n\t" \
  "buffer_load_dwordx4 v71, %[Wv], s60 offen lds\n\t" \
  "v_add_u32 v78, 16384, v66\n\t" \
  "s_waitcnt vmcnt(12) lgkmcnt(0)\n\t" \
  "s_barrier\n\t" \
  "ds_read_b64_tr_b16 a[224:225], v78 offset:0\n\t" \
  "ds_read_b64_tr_b16 a[226:227], v78 offset:1024\n\t" \
  "ds_read_b64_tr_b16 a[228:229], v78 offset:8192\n\t" \
  "ds_read_b64_tr_b16 a[230:231], v78 offset:9216\n\t" \
  "ds_read_b64_tr_b16 a[232:233], v78 offset:32\n\t" \
  "ds_read_b64_tr_b16 a[234:235], v78 offset:1056\n\t" \
  "ds_read_b64_tr_b16 a[236:237], v78 offset:8224\n\t" \
  "ds_read_b64_tr_b16 a[238:239], v78 offset:9248\n\t" \
  "s_add_u32 m0, s54, 0\n\t" \
  "s_mov_b32 s60, 3670016\n\t" \
  "buffer_load_dwordx4 v71, %[Wq], s60 offen lds\n\t" \
  "s_add_u32 m0, s54, 1024\n\t" \
  "s_mov_b32 s60, 3702784\n\t" \
  "buffer_load_dwordx4 v71, %[Wq], s60 offen lds\n\t" \
  "s_add_u32 m0, s54, 2048\n\t" \
  "s_mov_b32 s60, 3735552\n\t" \
  "buffer_load_dwordx4 v71, %[Wq], s60 offen lds\n\t" \
  "s_add_u32 m0, s54, 3072\n\t" \
  "s_mov_b32 s60, 3768320\n\t" \
  "buffer_load_dwordx4 v71, %[Wq], s60 offen lds\n\t" \
  "v_add_u32 v78, 53248, v65\n\t" \
  "s_waitcnt vmcnt(12) lgkmcnt(0)\n\t" \
  "s_barrier\n\t" \
  "ds_read_b64_tr_b16 a[240:241], v78 offset:0\n\t" \
  "ds_read_b64_tr_b16 a[242:243], v78 offset:1024\n\t" \
  "ds_read_b64_tr_b16 a[244:245], v78 offset:8192\n\t" \
  "ds_read_b64_tr_b16 a[246:247], v78 offset:9216\n\t" \
  "ds_read_b64_tr_b16 a[248:249], v78 offset:8\n\t" \
  "ds_read_b64_tr_b16 a[250:251], v78 offset:1032\n\t" \
  "ds_read_b64_tr_b16 a[252:253], v78 offset:8200\n\t" \
  "ds_read_b64_tr_b16 a[254:255], v78 offset:9224\n\t" \
  "s_add_u32 m0, s54, 16384\n\t" \
  "s_mov_b32 s60, 3670016\n\t" \
  "buffer_load_dwordx4 v71, %[Wk], s60 offen lds\n\t" \
  "s_add_u32 m0, s54, 17408\n\t" \
  "s_mov_b32 s60, 3702784\n\t" \
  "buffer_load_dwordx4 v71, %[Wk], s60 offen lds\n\t" \
  "s_add_u32 m0, s54, 18432\n\t" \
  "s_mov_b32 s60, 3735552\n\t" \
  "buffer_load_dwordx4 v71, %[Wk], s60 offen lds\n\t" \
  "s_add_u32 m0, s54, 19456\n\t" \
  "s_mov_b32 s60, 3768320\n\t" \
  "buffer_load_dwordx4 v71, %[Wk], s60 offen lds\n\t" \
  "v_add_u32 v78, 90112, v65\n\t" \
  "s_waitcnt vmcnt(12) lgkmcnt(0)\n\t" \
  "s_barrier\n\t" \
  "ds_read_b64_tr_b16 v[80:81], v78 offset:0\n\t" \
  "ds_read_b64_tr_b16 v[82:83], v78 offset:1024\n\t" \
  "ds_read_b64_tr_b16 v[84:85], v78 offset:8192\n\t" \
  "ds_read_b64_tr_b16 v[86:87], v78 offset:9216\n\t" \
  "ds_read_b64_tr_b16 v[88:89], v78 offset:8\n\t" \
  "ds_read_b64_tr_b16 v[90:91], v78 offset:1032\n\t" \
  "ds_read_b64_tr_b16 v[92:93], v78 offset:8200\n\t" \
  "ds_read_b64_tr_b16 v[94:95], v78 offset:9224\n\t" \
  "s_add_u32 m0, s54, 53248\n\t" \
  "s_mov_b32 s60, 3670016\n\t" \
  "buffer_load_dwordx4 v71, %[Wv], s60 offen lds\n\t" \
  "s_add_u32 m0, s54, 54272\n\t" \
  "s_mov_b32 s60, 3702784\n\t" \
  "buffer_load_dwordx4 v71, %[Wv], s60 offen lds\n\t" \
  "s_add_u32 m0, s54, 55296\n\t" \
  "s_mov_b32 s60, 3735552\n\t" \
  "buffer_load_dwordx4 v71, %[Wv], s60 offen lds\n\t" \
  "s_add_u32 m0, s54, 56320\n\t" \
  "s_mov_b32 s60, 3768320\n\t" \
  "buffer_load_dwordx4 v71, %[Wv], s60 offen lds\n\t" \
  "v_add_u32 v78, 126976, v66\n\t" \
  "s_waitcnt vmcnt(12) lgkmcnt(0)\n\t" \
  "s_barrier\n\t" \
  "ds_read_b64_tr_b16 v[96:97], v78 offset:0\n\t" \
  "ds_read_b64_tr_b16 v[98:99], v78 offset:1024\n\t" \
  "ds_read_b64_tr_b16 v[100:101], v78 offset:8192\n\t" \
  "ds_read_b64_tr_b16 v[102:103], v78 offset:9216\n\t" \
  "ds_read_b64_tr_b16 v[104:105], v78 offset:32\n\t" \
  "ds_read_b64_tr_b16 v[106:107], v78 offset:1056\n\t" \
  "ds_read_b64_tr_b16 v[108:109], v78 offset:8224\n\t" \
  "ds_read_b64_tr_b16 v[110:111], v78 offset:9248\n\t" \
  "s_add_u32 m0, s54, 90112\n\t" \
  "s_mov_b32 s60, 4194304\n\t" \
  "buffer_load_dwordx4 v71, %[Wq], s60 offen lds\n\t" \
  "s_add_u32 m0, s54, 91136\n\t" \
  "s_mov_b32 s60, 4227072\n\t" \
  "buffer_load_dwordx4 v71, %[Wq], s60 offen lds\n\t" \
  "s_add_u32 m0, s54, 92160\n\t" \
  "s_mov_b32 s60, 4259840\n\t" \
  "buffer_load_dwordx4 v71, %[Wq], s60 offen lds\n\t" \
  "s_add_u32 m0, s54, 93184\n\t" \
  "s_mov_b32 s60, 4292608\n\t" \
  "buffer_load_dwordx4 v71, %[Wq], s60 offen lds\n\t" \
  "v_add_u32 v78, 0, v65\n\t" \
  "s_waitcnt vmcnt(12) lgkmcnt(0)\n\t" \
  "s_barrier\n\t" \
  "ds_read_b64_tr_b16 v[112:113], v78 offset:0\n\t" \
  "ds_read_b64_tr_b16 v[114:115], v78 offset:1024\n\t" \
  "ds_read_b64_tr_b16 v[116:117], v78 offset:8192\n\t" \
  "ds_read_b64_tr_b16 v[118:119], v78 offset:9216\n\t" \
  "ds_read_b64_tr_b16 v[120:121], v78 offset:8\n\t" \
  "ds_read_b64_tr_b16 v[122:123], v78 offset:1032\n\t" \
  "ds_read_b64_tr_b16 v[124:125], v78 offset:8200\n\t" \
  "ds_read_b64_tr_b16 v[126:127], v78 offset:9224\n\t" \
  "s_add_u32 m0, s54, 126976\n\t" \
  "s_mov_b32 s60, 4194304\n\t" \
  "buffer_load_dwordx4 v71, %[Wk], s60 offen lds\n\t" \
  "s_add_u32 m0, s54, 128000\n\t" \
  "s_mov_b32 s60, 4227072\n\t" \
  "buffer_load_dwordx4 v71, %[Wk], s60 offen lds\n\t" \
  "s_add_u32 m0, s54, 129024\n\t" \
  "s_mov_b32 s60, 4259840\n\t" \
  "buffer_load_dwordx4 v71, %[Wk], s60 offen lds\n\t" \
  "s_add_u32 m0, s54, 130048\n\t" \
  "s_mov_b32 s60, 4292608\n\t" \
  "buffer_load_dwordx4 v71, %[Wk], s60 offen lds\n\t" \
  "v_add_u32 v78, 16384, v65\n\t" \
  "s_waitcnt vmcnt(12) lgkmcnt(0)\n\t" \
  "s_barrier\n\t" \
  "ds_read_b64_tr_b16 v[128:129], v78 offset:0\n\t" \
  "ds_read_b64_tr_b16 v[130:131], v78 offset:1024\n\t" \
  "ds_read_b64_tr_b16 v[132:133], v78 offset:8192\n\t" \
  "ds_read_b64_tr_b16 v[134:135], v78 offset:9216\n\t" \
  "ds_read_b64_tr_b16 v[136:137], v78 offset:8\n\t" \
  "ds_read_b64_tr_b16 v[138:139], v78 offset:1032\n\t" \
  "ds_read_b64_tr_b16 v[140:141], v78 offset:8200\n\t" \
  "ds_read_b64_tr_b16 v[142:143], v78 offset:9224\n\t" \
  "s_add_u32 m0, s54, 0\n\t" \
  "s_mov_b32 s60, 4194304\n\t" \
  "buffer_load_dwordx4 v71, %[Wv], s60 offen lds\n\t" \
  "s_add_u32 m0, s54, 1024\n\t" \
  "s_mov_b32 s60, 4227072\n\t" \
  "buffer_load_dwordx4 v71, %[Wv], s60 offen lds\n\t" \
  "s_add_u32 m0, s54, 2048\n\t" \
  "s_mov_b32 s60, 4259840\n\t" \
  "buffer_load_dwordx4 v71, %[Wv], s60 offen lds\n\t" \
  "s_add_u32 m0, s54, 3072\n\t" \
  "s_mov_b32 s60, 4292608\n\t" \
  "buffer_load_dwordx4 v71, %[Wv], s60 offen lds\n\t" \
  "v_add_u32 v78, 53248, v66\n\t" \
  "s_waitcnt vmcnt(12) lgkmcnt(0)\n\t" \
  "s_barrier\n\t" \
  "ds_read_b64_tr_b16 v[144:145], v78 offset:0\n\t" \
  "ds_read_b64_tr_b16 v[146:147], v78 offset:1024\n\t" \
  "ds_read_b64_tr_b16 v[148:149], v78 offset:8192\n\t" \
  "ds_read_b64_tr_b16 v[150:151], v78 offset:9216\n\t" \
  "ds_read_b64_tr_b16 v[152:153], v78 offset:32\n\t" \
  "ds_read_b64_tr_b16 v[154:155], v78 offset:1056\n\t" \
  "ds_read_b64_tr_b16 v[156:157], v78 offset:8224\n\t" \
  "ds_read_b64_tr_b16 v[158:159], v78 offset:9248\n\t" \
  "v_add_u32 v78, 90112, v65\n\t" \
  "s_waitcnt vmcnt(8) lgkmcnt(0)\n\t" \
  "s_barrier\n\t" \
  "ds_read_b64_tr_b16 v[160:161], v78 offset:0\n\t" \
  "ds_read_b64_tr_b16 v[162:163], v78 offset:1024\n\t" \
  "ds_read_b64_tr_b16 v[164:165], v78 offset:8192\n\t" \
  "ds_read_b64_tr_b16 v[166:167], v78 offset:9216\n\t" \
  "ds_read_b64_tr_b16 v[168:169], v78 offset:8\n\t" \
  "ds_read_b64_tr_b16 v[170:171], v78 offset:1032\n\t" \
  "ds_read_b64_tr_b16 v[172:173], v78 offset:8200\n\t" \
  "ds_read_b64_tr_b16 v[174:175], v78 offset:9224\n\t" \
  "v_add_u32 v78, 126976, v65\n\t" \
  "s_waitcnt vmcnt(4) lgkmcnt(0)\n\t" \
  "s_barrier\n\t" \
  "ds_read_b64_tr_b16 v[40:41], v78 offset:0\n\t" \
  "ds_read_b64_tr_b16 v[42:43], v78 offset:1024\n\t" \
  "ds_read_b64_tr_b16 v[44:45], v78 offset:8192\n\t" \
  "ds_read_b64_tr_b16 v[46:47], v78 offset:9216\n\t" \
  "ds_read_b64_tr_b16 v[176:177], v78 offset:8\n\t" \
  "ds_read_b64_tr_b16 v[178:179], v78 offset:1032\n\t" \
  "ds_read_b64_tr_b16 v[180:181], v78 offset:8200\n\t" \
  "ds_read_b64_tr_b16 v[182:183], v78 offset:9224\n\t" \
  "v_add_u32 v78, 0, v66\n\t" \
  "s_waitcnt vmcnt(0) lgkmcnt(0)\n\t" \
  "s_barrier\n\t" \
  "ds_read_b64_tr_b16 v[184:185], v78 offset:0\n\t" \
  "ds_read_b64_tr_b16 v[186:187], v78 offset:1024\n\t" \
  "ds_read_b64_tr_b16 v[188:189], v78 offset:8192\n\t" \
  "ds_read_b64_tr_b16 v[190:191], v78 offset:9216\n\t" \
  "ds_read_b64_tr_b16 v[56:57], v78 offset:32\n\t" \
  "ds_read_b64_tr_b16 v[58:59], v78 offset:1056\n\t" \
  "ds_read_b64_tr_b16 v[60:61], v78 offset:8224\n\t" \
  "ds_read_b64_tr_b16 v[62:63], v78 offset:9248\n\t" \
  "s_waitcnt lgkmcnt(0)\n\t" \
  "s_barrier\n\t" \
  "ds_write_b128 v70, v[0:3] offset:0\n\t" \
  "ds_write_b128 v70, v[4:7] offset:1024\n\t" \
  "ds_write_b128 v70, v[8:11] offset:2048\n\t" \
  "ds_write_b128 v70, v[12:15] offset:3072\n\t" \
  "ds_write_b128 v70, v[16:19] offset:4096\n\t" \
  "ds_write_b128 v70, v[20:23] offset:5120\n\t" \
  "ds_write_b128 v70, v[24:27] offset:6144\n\t" \
  "ds_write_b128 v70, v[28:31] offset:7168\n\t" \
  "ds_write_b128 v70, v[32:35] offset:8192\n\t" \
  "ds_write_b128 v70, v[36:39] offset:9216\n\t" \
  "ds_write_b128 v70, v[40:43] offset:10240\n\t" \
  "ds_write_b128 v70, v[44:47] offset:11264\n\t" \
  "ds_write_b128 v70, v[48:51] offset:12288\n\t" \
  "ds_write_b128 v70, v[52:55] offset:13312\n\t" \
  "ds_write_b128 v70, v[56:59] offset:14336\n\t" \
  "ds_write_b128 v70, v[60:63] offset:15360\n\t" \
  "s_waitcnt lgkmcnt(0)\n\t" \
  "v_and_b32 v72, 1, v72\n\t" \
  "v_and_b32 v74, 1, v74\n\t" \
  "v_cmp_ne_u32 s[90:91], s32, v72\n\t" \
  "v_cmp_ne_u32 vcc, s32, v74\n\t" \
  "s_nop 1\n\t" \
  "s_mov_b32 s96, vcc_lo\n\t" \
  "s_mov_b32 s21, vcc_lo\n\t" \
  "v_and_b32 v73, 32, v69\n\t" \
  "v_lshl_add_u32 v73, v73, 5, v69\n\t" \
  "s_mov_b32 s35, s30\n\t" \
  "s_cmp_lt_u32 s32, " CH_DRYL "\n\t" \
  "s_cbranch_scc1 LINI_%=\n\t" \
  "s_mov_b32 s30, 0\n\t" \
  "s_mov_b32 s34, 0\n\t" \
  "s_cmp_eq_u32 s40, 0\n\t" \
  "s_cbranch_scc1 LW0_%=\n\t" \
  "s_barrier\n\t" \
  "s_cmp_eq_u32 s40, 1\n\t" \
  "s_cbranch_scc1 LG1_%=\n\t" \
  "s_cmp_eq_u32 s40, 2\n\t" \
  "s_cbranch_scc1 LG2_%=\n\t" \
  "s_branch LG3_%=\n\t" \
  "LINI_%=:\n\t" \
  "s_waitcnt vmcnt(0) lgkmcnt(0)\n\t" \
  "s_mov_b32 s30, s35\n\t" \
  "s_movk_i32 s34, 1000\n\t" \
  "s_mov_b32 s48, -1\n\t" \
  "s_add_u32 s97, s41, s53\n\t" \
  "s_add_u32 s98, s37, s53\n\t" \
  "s_add_u32 s82, s48, 0\n\t" \
  "s_and_b32 s82, s82, " CH_TRM "\n\t" \
  "s_lshr_b64 s[60:61], s[90:91], s82\n\t" \
  "s_and_b32 s60, s60, 1\n\t" \
  "s_lshl_b32 s60, s60, 13\n\t" \
  "s_add_u32 s82, s82, " CH_SG0 "\n\t" \
  "s_lshl_b32 s82, s82, 21\n\t" \
  "s_or_b32 s82, s82, s60\n\t" \
  "s_add_u32 s50, s82, s38\n\t" \
  "s_add_u32 s69, s82, s39\n\t" \
  "s_add_u32 s82, s48, 1\n\t" \
  "s_lshr_b64 s[60:61], s[90:91], s82\n\t" \
  "s_and_b32 s60, s60, 1\n\t" \
  "s_lshl_b32 s60, s60, 13\n\t" \
  "s_add_u32 s82, s82, " CH_SG0 "\n\t" \
  "s_lshl_b32 s82, s82, 21\n\t" \
  "s_or_b32 s82, s82, s60\n\t" \
  "s_add_u32 s52, s82, s43\n\t" \
  "s_add_u32 s82, s48, " CH_ARMD "\n\t" \
  "s_lshr_b64 s[60:61], s[90:91], s82\n\t" \
  "s_and_b32 s60, s60, 1\n\t" \
  "s_lshl_b32 s60, s60, 13\n\t" \
  "s_add_u32 s82, s82, " CH_SG0 "\n\t" \
  "s_lshl_b32 s82, s82, 21\n\t" \
  "s_or_b32 s82, s82, s60\n\t" \
  "s_add_u32 s85, s82, s45\n\t" \
  "s_cmp_lt_i32 s48, " CH_ARMS "\n\t" \
  "s_cselect_b32 s85, s27, s85\n\t" \
  "v_and_b32 v37, 0x30, v69\n\t" \
  "v_add_u32 v37, s52, v37\n\t" \
  "s_mov_b32 s48, 0\n\t" \
  "s_mov_b32 s86, 1\n\t" \
  "s_and_b32 s49, s96, 1\n\t" \
  "s_lshl_b32 s49, s49, 13\n\t" \
  "s_add_u32 s53, s49, " CH_SLOT "\n\t" \
  "s_movk_i32 s60, 124\n\t" \
  "s_cmp_eq_u32 s40, 3\n\t" \
  "s_cselect_b32 s81, 76, s60\n\t" \
  "s_add_u32 s82, s48, 0\n\t" \
  "s_and_b32 s82, s82, " CH_TRM "\n\t" \
  "s_lshr_b64 s[60:61], s[90:91], s82\n\t" \
  "s_and_b32 s60, s60, 1\n\t" \
  "s_lshl_b32 s60, s60, 13\n\t" \
  "s_add_u32 s82, s82, " CH_SG0 "\n\t" \
  "s_lshl_b32 s82, s82, 21\n\t" \
  "s_or_b32 s82, s82, s60\n\t" \
  "s_add_u32 s50, s82, s38\n\t" \
  "s_add_u32 s69, s82, s39\n\t" \
  "s_add_u32 s82, s48, 0\n\t" \
  "s_lshr_b64 s[60:61], s[90:91], s82\n\t" \
  "s_and_b32 s60, s60, 1\n\t" \
  "s_lshl_b32 s60, s60, 13\n\t" \
  "s_add_u32 s82, s82, " CH_SG0 "\n\t" \
  "s_lshl_b32 s82, s82, 21\n\t" \
  "s_or_b32 s82, s82, s60\n\t" \
  "s_add_u32 s51, s82, s43\n\t" \
  "s_add_u32 s97, s41, s53\n\t" \
  "s_add_u32 s98, s37, s53\n\t" \
  "s_add_u32 s82, s48, 0\n\t" \
  "s_and_b32 s82, s82, " CH_TRM "\n\t" \
  "s_lshr_b64 s[60:61], s[90:91], s82\n\t" \
  "s_and_b32 s60, s60, 1\n\t" \
  "s_lshl_b32 s60, s60, 13\n\t" \
  "s_add_u32 s82, s82, " CH_SG0 "\n\t" \
  "s_lshl_b32 s82, s82, 21\n\t" \
  "s_or_b32 s82, s82, s60\n\t" \
  "s_add_u32 s50, s82, s38\n\t" \
  "s_add_u32 s69, s82, s39\n\t" \
  "s_add_u32 s82, s48, 1\n\t" \
  "s_lshr_b64 s[60:61], s[90:91], s82\n\t" \
  "s_and_b32 s60, s60, 1\n\t" \
  "s_lshl_b32 s60, s60, 13\n\t" \
  "s_add_u32 s82, s82, " CH_SG0 "\n\t" \
  "s_lshl_b32 s82, s82, 21\n\t" \
  "s_or_b32 s82, s82, s60\n\t" \
  "s_add_u32 s52, s82, s43\n\t" \
  "s_add_u32 s82, s48, " CH_ARMD "\n\t" \
  "s_lshr_b64 s[60:61], s[90:91], s82\n\t" \
  "s_and_b32 s60, s60, 1\n\t" \
  "s_lshl_b32 s60, s60, 13\n\t" \
  "s_add_u32 s82, s82, " CH_SG0 "\n\t" \
  "s_lshl_b32 s82, s82, 21\n\t" \
  "s_or_b32 s82, s82, s60\n\t" \
  "s_add_u32 s85, s82, s45\n\t" \
  "s_cmp_lt_i32 s48, " CH_ARMS "\n\t" \
  "s_cselect_b32 s85, s27, s85\n\t" \
  "s_memrealtime s[62:63]\n\t" \
  "s_waitcnt lgkmcnt(0)\n\t" \
  "s_mov_b32 s65, s62\n\t" \
  "s_sub_u32 s47, s62, 1000\n\t" \
  "s_mov_b64 vcc, -1\n\t" \
  "s_cmp_eq_u32 s40, 0\n\t" \
  "s_cbranch_scc1 LW0_%=\n\t" \
  "s_barrier\n\t" \
  "s_cmp_eq_u32 s40, 1\n\t" \
  "s_cbranch_scc1 LG1_%=\n\t" \
  "s_cmp_eq_u32 s40, 2\n\t" \
  "s_cbranch_scc1 LG2_%=\n\t" \
  "s_branch LTK_%=\n\t" \
  ".p2align 6\n\t" \
  "LW0_%=:\n\t" \
  "v_lshlrev_b32 v78, 2, v69\n\t" \
  "v_and_b32 v78, 0x300, v78\n\t" \
  "v_lshrrev_b32 v79, 8, v69\n\t" \
  "v_lshl_add_u32 v78, v79, 6, v78\n\t" \
  "v_and_b32 v79, 48, v69\n\t" \
  "v_add_u32 v78, v78, v79\n\t" \
  "s_add_u32 s98, s49, s42\n\t" \
  "s_add_u32 s97, s98, 32768\n\t" \
  "s_add_u32 s99, s98, 65536\n\t" \
  "s_barrier\n\t" \
  "s_memrealtime s[62:63]\n\t" \
  "s_waitcnt lgkmcnt(0)\n\t" \
  "s_add_u32 s62, s62, " CH_DLL2 "\n\t" \
  "s_sub_i32 s63, s62, s65\n\t" \
  "s_cmp_gt_i32 s63, " CH_DLT "\n\t" \
  "s_cselect_b32 s60, " CH_DLY ", " CH_DLX "\n\t" \
  "s_add_u32 s62, s62, s60\n\t" \
  "s_cmp_gt_i32 s63, 0\n\t" \
  "s_cselect_b32 s65, s62, s65\n\t" \
  "s_mov_b32 m0, 0\n\t" \
  "s_movk_i32 s60, 8192\n\t" \
  "LWT_%=:\n\t" \
  "s_memrealtime s[62:63]\n\t" \
  "s_sub_u32 s60, s60, 1\n\t" \
  "s_cselect_b32 s61, 0, -1\n\t" \
  "s_waitcnt lgkmcnt(0)\n\t" \
  "s_sub_i32 s63, s62, s65\n\t" \
  "s_and_b32 s63, s63, s61\n\t" \
  "s_cmp_lt_i32 s63, " CH_FIN "\n\t" \
  "s_cbranch_scc1 LWT_%=\n\t" \
  "s_cmp_lt_i32 s63, 0\n\t" \
  "s_cbranch_scc1 LWT_%=f\n\t" \
  "LWT_%=g:\n\t" \
  "LDMA_%=:\n\t" \
  "buffer_load_dwordx4 v78, s[28:31], s98 offen offset:0 sc1 lds\n\t" \
  "buffer_load_dwordx4 v78, s[28:31], s98 offen offset:1024 sc1 lds\n\t" \
  "buffer_load_dwordx4 v78, s[28:31], s98 offen offset:2048 sc1 lds\n\t" \
  "buffer_load_dwordx4 v78, s[28:31], s98 offen offset:3072 sc1 lds\n\t" \
  "s_movk_i32 m0, 4096\n\t" \
  "s_mov_b32 s66, s62\n\t" \
  "buffer_load_dwordx4 v78, s[28:31], s97 offen offset:0 sc1 lds\n\t" \
  "buffer_load_dwordx4 v78, s[28:31], s97 offen offset:1024 sc1 lds\n\t" \
  "buffer_load_dwordx4 v78, s[28:31], s97 offen offset:2048 sc1 lds\n\t" \
  "buffer_load_dwordx4 v78, s[28:31], s97 offen offset:3072 sc1 lds\n\t" \
  "s_movk_i32 m0, 8192\n\t" \
  "s_nop 0\n\t" \
  "buffer_load_dwordx4 v69, s[28:31], s99 offen sc1 lds\n\t" \
  ".p2align 6\n\t" \
  "LG0_%=:\n\t" \
  "s_barrier\n\t" \
  "buffer_load_dwordx4 v[52:55], v69, s[28:31], s50 offen sc1\n\t" \
  "buffer_load_dwordx4 v[56:59], v69, s[28:31], s50 offen offset:1024 sc1\n\t" \
  "s_add_u32 s64, s50, 16384\n\t" \
  "s_movk_i32 m0, 11264\n\t" \
  "v_lshlrev_b32 v75, 1, v69\n\t" \
  "buffer_load_dword v75, s[28:31], s64 offen sc1 lds\n\t" \
  "s_memrealtime s[62:63]\n\t" \
  "s_waitcnt lgkmcnt(0)\n\t" \
  "s_sub_u32 s62, s62, " CH_TKL "\n\t" \
  "s_sub_i32 s63, s62, s65\n\t" \
  "s_cmp_gt_i32 s63, 0\n\t" \
  "s_cselect_b32 s62, s62, s65\n\t" \
  "s_sub_i32 s63, s62, s47\n\t" \
  "s_cmp_gt_i32 s63, " CH_LATE "\n\t" \
  "s_cselect_b32 s71, " CH_SCHW ", s46\n\t" \
  "s_add_u32 s61, s47, " CH_LATE "\n\t" \
  "s_cmp_gt_i32 s63, " CH_LATE "\n\t" \
  "s_cselect_b32 s62, s62, s61\n\t" \
  "s_add_u32 s71, s71, s53\n\t" \
  "s_add_u32 s60, s62, s81\n\t" \
  "s_add_u32 s60, s60, " CH_DL1L "\n\t" \
  "s_lshl_b32 s60, s60, 8\n\t" \
  "s_or_b32 s60, s60, " CH_B0 "\n\t" \
  "s_cmp_lt_u32 s48, " CH_ES "\n\t" \
  "s_cselect_b32 s61, " CH_EX ", 0\n\t" \
  "s_add_u32 s60, s60, s61\n\t" \
  "s_not_b32 s60, s60\n\t" \
  "v_mov_b32 v76, s60\n\t" \
  "s_lshr_b32 s60, s46, 31\n\t" \
  "s_or_b32 s60, s60, s68\n\t" \
  "s_cselect_b32 s25, 0, 1\n\t" \
  "s_mov_b64 exec, s[24:25]\n\t" \
  "buffer_atomic_umin v76, v69, s[28:31], s71 offen sc1\n\t" \
  "s_mov_b64 exec, -1\n\t" \
  "ds_read_b128 v[44:47], v70 offset:0\n\t" \
  "ds_read_b128 v[48:51], v70 offset:1024\n\t" \
  "ds_read_b128 v[60:63], v70 offset:2048\n\t" \
  "ds_read_b128 v[64:67], v70 offset:3072\n\t" \
  "v_mov_b64 v[36:37], 0\n\t" \
  "v_mov_b64 v[38:39], 0\n\t" \
  "v_mov_b64 v[40:41], 0\n\t" \
  "v_mov_b64 v[42:43], 0\n\t" \
  "v_lshrrev_b32 v71, 1, v69\n\t" \
  "v_lshrrev_b32 v72, 3, v69\n\t" \
  "v_and_b32 v72, 0x78, v72\n\t" \
  "v_and_b32 v74, 0x30, v69\n\t" \
  "v_lshl_add_u32 v72, v74, 3, v72\n\t" \
  "s_add_u32 s54, s53, s36\n\t" \
  "s_add_u32 s55, s54, 524288\n\t" \
  "s_add_u32 s54, s54, 0\n\t" \
  "s_add_u32 s75, s54, 262144\n\t" \
  "s_add_u32 s80, s53, s26\n\t" \
  "s_or_b32 s87, s53, 0x1fff80\n\t" \
  "s_and_b32 s87, s87, -128\n\t" \
  "s_add_u32 s87, s87, 0x200000\n\t" \
  "s_waitcnt vmcnt(9)\n\t" \
  "s_barrier\n\t" \
  "ds_read_b128 v[0:3], v69 offset:0\n\t" \
  "ds_read_b128 v[4:7], v69 offset:1024\n\t" \
  "ds_read_b128 v[8:11], v69 offset:2048\n\t" \
  "ds_read_b128 v[12:15], v69 offset:3072\n\t" \
  "s_waitcnt lgkmcnt(3)\n\t" \
  "v_smfmac_f32_16x16x64_bf16 v[36:39], v[0:3], v[44:51], v68\n\t" \
  "ds_read_b128 v[44:47], v70 offset:4096\n\t" \
  "v_smfmac_f32_16x16x64_bf16 v[40:43], v[0:3], v[60:67], v68\n\t" \
  "ds_read_b128 v[48:51], v70 offset:5120\n\t" \
  "s_waitcnt lgkmcnt(4)\n\t" \
  "v_smfmac_f32_16x16x64_bf16 v[36:39], v[4:7], a[32:39], v68\n\t" \
  "ds_read_b128 v[60:63], v70 offset:6144\n\t" \
  "v_smfmac_f32_16x16x64_bf16 v[40:43], v[4:7], a[40:47], v68\n\t" \
  "ds_read_b128 v[64:67], v70 offset:7168\n\t" \
  "s_waitcnt lgkmcnt(5)\n\t" \
  "v_smfmac_f32_16x16x64_bf16 v[36:39], v[8:11], a[80:87], v68\n\t" \
  "s_waitcnt vmcnt(4)\n\t" \
  "s_barrier\n\t" \
  "ds_read_b128 v[16:19], v69 offset:4096\n\t" \
  "v_smfmac_f32_16x16x64_bf16 v[40:43], v[8:11], a[88:95], v68\n\t" \
  "ds_read_b128 v[20:23], v69 offset:5120\n\t" \
  "s_waitcnt lgkmcnt(6)\n\t" \
  "v_smfmac_f32_16x16x64_bf16 v[36:39], v[12:15], a[120:127], v68\n\t" \
  "ds_read_b128 v[24:27], v69 offset:6144\n\t" \
  "s_waitcnt lgkmcnt(5)\n\t" \
  "v_smfmac_f32_16x16x64_bf16 v[40:43], v[12:15], v[44:51], v68\n\t" \
  "ds_read_b128 v[28:31], v69 offset:7168\n\t" \
  "s_waitcnt lgkmcnt(3)\n\t" \
  "v_smfmac_f32_16x16x64_bf16 v[36:39], v[16:19], a[160:167], v68\n\t" \
  "ds_read_b128 v[32:35], v69 offset:8192\n\t" \
  "v_smfmac_f32_16x16x64_bf16 v[40:43], v[16:19], a[168:175], v68\n\t" \
  "s_waitcnt lgkmcnt(3)\n\t" \
  "v_mov_b64 v[44:45], 0\n\t" \
  "v_smfmac_f32_16x16x64_bf16 v[36:39], v[20:23], a[200:207], v68\n\t" \
  "s_waitcnt lgkmcnt(2)\n\t" \
  "v_mov_b64 v[46:47], 0\n\t" \
  "v_smfmac_f32_16x16x64_bf16 v[36:39], v[24:27], a[240:247], v68\n\t" \
  "s_waitcnt lgkmcnt(1)\n\t" \
  "v_mov_b64 v[48:49], 0\n\t" \
  "v_smfmac_f32_16x16x64_bf16 v[36:39], v[28:31], v[112:119], v68\n\t" \
  "s_waitcnt lgkmcnt(0)\n\t" \
  "v_mov_b64 v[50:51], 0\n\t" \
  "v_smfmac_f32_16x16x64_bf16 v[36:39], v[32:35], v[160:167], v68\n\t" \
  "v_smfmac_f32_16x16x64_bf16 v[40:43], v[20:23], v[60:67], v68\n\t" \
  "ds_read_b128 v[60:63], v70 offset:8192\n\t" \
  "v_smfmac_f32_16x16x64_bf16 v[40:43], v[24:27], a[248:255], v68\n\t" \
  "ds_read_b128 v[64:67], v70 offset:9216\n\t" \
  "v_add_f32 v74, v36, v38\n\t" \
  "v_smfmac_f32_16x16x64_bf16 v[40:43], v[28:31], v[120:127], v68\n\t" \
  "v_add_f32 v37, v37, v39\n\t" \
  "v_smfmac_f32_16x16x64_bf16 v[40:43], v[32:35], v[168:175], v68\n\t" \
  "v_cmp_u_f32 vcc, v74, v37\n\t" \
  "v_smfmac_f32_16x16x64_bf16 v[44:47], v[0:3], a[0:7], v68\n\t" \
  "s_cbranch_vccnz LFAIL0_%=\n\t" \
  "LGO0_%=:\n\t" \
  "v_cvt_pk_bf16_f32 v74, v74, v37\n\t" \
  "v_smfmac_f32_16x16x64_bf16 v[44:47], v[4:7], a[48:55], v68\n\t" \
  "s_waitcnt lgkmcnt(0)\n\t" \
  "v_add_f32 v75, v40, v42\n\t" \
  "v_smfmac_f32_16x16x64_bf16 v[44:47], v[8:11], v[60:67], v68\n\t" \
  "ds_read_b128 v[60:63], v70 offset:10240\n\t" \
  "v_add_f32 v41, v41, v43\n\t" \
  "v_cvt_pk_bf16_f32 v75, v75, v41\n\t" \
  "v_smfmac_f32_16x16x64_bf16 v[44:47], v[12:15], a[128:135], v68\n\t" \
  "ds_write2st64_b32 v71, v74, v75 offset0:36 offset1:38\n\t" \
  "v_smfmac_f32_16x16x64_bf16 v[44:47], v[16:19], a[176:183], v68\n\t" \
  "ds_read_b128 v[64:67], v70 offset:11264\n\t" \
  "v_smfmac_f32_16x16x64_bf16 v[44:47], v[20:23], a[208:215], v68\n\t" \
  "v_smfmac_f32_16x16x64_bf16 v[44:47], v[24:27], v[80:87], v68\n\t" \
  "v_smfmac_f32_16x16x64_bf16 v[44:47], v[28:31], v[128:135], v68\n\t" \
  "s_waitcnt lgkmcnt(0)\n\t" \
  "v_smfmac_f32_16x16x64_bf16 v[44:47], v[32:35], v[60:67], v68\n\t" \
  "buffer_load_dwordx4 v[60:63], v69, s[28:31], s64 offen sc1\n\t" \
  "s_barrier\n\t" \
  "ds_read_b64_tr_b16 v[76:77], v72 offset:9216\n\t" \
  "v_smfmac_f32_16x16x64_bf16 v[48:51], v[0:3], a[8:15], v68\n\t" \
  "ds_read_b64_tr_b16 v[78:79], v72 offset:9728\n\t" \
  "v_smfmac_f32_16x16x64_bf16 v[48:51], v[4:7], a[56:63], v68\n\t" \
  "v_add_f32 v74, v44, v46\n\t" \
  "v_smfmac_f32_16x16x64_bf16 v[48:51], v[8:11], a[96:103], v68\n\t" \
  "v_add_f32 v45, v45, v47\n\t" \
  "v_smfmac_f32_16x16x64_bf16 v[48:51], v[12:15], a[136:143], v68\n\t" \
  "s_waitcnt vmcnt(4)\n\t" \
  "s_waitcnt lgkmcnt(0)\n\t" \
  "v_cvt_pk_bf16_f32 v74, v74, v45\n\t" \
  "v_mfma_f32_16x16x32_bf16 v[36:39], v[52:55], v[76:79], 0\n\t" \
  "buffer_load_dwordx4 v[64:67], v69, s[28:31], s64 offen offset:1024 sc1\n\t" \
  "s_waitcnt vmcnt(4)\n\t" \
  "v_mfma_f32_16x16x32_bf16 v[40:43], v[56:59], v[76:79], 0\n\t" \
  "v_mov_b64 v[52:53], 0\n\t" \
  "v_mov_b64 v[54:55], 0\n\t" \
  "v_smfmac_f32_16x16x64_bf16 v[48:51], v[16:19], a[184:191], v68\n\t" \
  "v_mov_b64 v[56:57], 0\n\t" \
  "v_mov_b64 v[58:59], 0\n\t" \
  "v_smfmac_f32_16x16x64_bf16 v[48:51], v[20:23], a[216:223], v68\n\t" \
  "ds_write_b32 v71, v74 offset:0\n\t" \
  "v_add_f32_dpp v36, v40, v36 quad_perm:[2,3,0,1] row_mask:0xf bank_mask:0xf\n\t" \
  "v_smfmac_f32_16x16x64_bf16 v[48:51], v[24:27], v[88:95], v68\n\t" \
  "v_add_f32_dpp v37, v41, v37 quad_perm:[2,3,0,1] row_mask:0xf bank_mask:0xf\n\t" \
  "v_smfmac_f32_16x16x64_bf16 v[48:51], v[28:31], v[136:143], v68\n\t" \
  "v_add_f32_dpp v38, v42, v38 quad_perm:[2,3,0,1] row_mask:0xf bank_mask:0xf\n\t" \
  "v_smfmac_f32_16x16x64_bf16 v[48:51], v[32:35], v[176:183], v68\n\t" \
  "v_add_f32_dpp v39, v43, v39 quad_perm:[2,3,0,1] row_mask:0xf bank_mask:0xf\n\t" \
  "v_smfmac_f32_16x16x64_bf16 v[52:55], v[0:3], a[16:23], v68\n\t" \
  "s_mov_b64 exec, s[58:59]\n\t" \
  "ds_write_b128 v69, v[36:39] offset:2048\n\t" \
  "s_mov_b64 exec, -1\n\t" \
  "v_smfmac_f32_16x16x64_bf16 v[52:55], v[4:7], a[64:71], v68\n\t" \
  "v_add_f32 v75, v48, v50\n\t" \
  "v_add_f32 v49, v49, v51\n\t" \
  "v_cvt_pk_bf16_f32 v75, v75, v49\n\t" \
  "v_smfmac_f32_16x16x64_bf16 v[56:59], v[0:3], a[24:31], v68\n\t" \
  "ds_write_b32 v71, v75 offset:512\n\t" \
  "v_smfmac_f32_16x16x64_bf16 v[56:59], v[4:7], a[72:79], v68\n\t" \
  "ds_read_b64_tr_b16 v[0:1], v72 offset:10240\n\t" \
  "ds_read_b64_tr_b16 v[2:3], v72 offset:10752\n\t" \
  "v_smfmac_f32_16x16x64_bf16 v[52:55], v[8:11], a[104:111], v68\n\t" \
  "ds_read_b128 v[36:39], v70 offset:12288\n\t" \
  "v_smfmac_f32_16x16x64_bf16 v[56:59], v[8:11], a[112:119], v68\n\t" \
  "buffer_load_dword off, s[28:31], s87 sc1 lds\n\t" \
  "ds_read_b128 v[40:43], v70 offset:13312\n\t" \
  "v_smfmac_f32_16x16x64_bf16 v[52:55], v[12:15], a[144:151], v68\n\t" \
  "s_waitcnt lgkmcnt(4)\n\t" \
  "s_barrier\n\t" \
  "ds_read_b64_tr_b16 v[44:45], v72 offset:0\n\t" \
  "ds_read_b64_tr_b16 v[46:47], v72 offset:512\n\t" \
  "v_smfmac_f32_16x16x64_bf16 v[56:59], v[12:15], a[152:159], v68\n\t" \
  "ds_read_b64_tr_b16 v[48:49], v72 offset:1024\n\t" \
  "ds_read_b64_tr_b16 v[50:51], v72 offset:1536\n\t" \
  "v_smfmac_f32_16x16x64_bf16 v[52:55], v[16:19], a[192:199], v68\n\t" \
  "ds_read_b128 v[4:7], v69 offset:2048\n\t" \
  "s_waitcnt lgkmcnt(5)\n\t" \
  "v_smfmac_f32_16x16x64_bf16 v[56:59], v[16:19], v[36:43], v68\n\t" \
  "ds_read_b128 v[8:11], v69 offset:3072\n\t" \
  "s_waitcnt lgkmcnt(4)\n\t" \
  "v_mfma_f32_16x16x32_bf16 v[12:15], v[44:47], v[76:79], 0\n\t" \
  "ds_read_b128 v[36:39], v70 offset:14336\n\t" \
  "s_waitcnt lgkmcnt(3)\n\t" \
  "v_mfma_f32_16x16x32_bf16 v[12:15], v[48:51], v[0:3], v[12:15]\n\t" \
  "ds_read_b128 v[40:43], v70 offset:15360\n\t" \
  "v_smfmac_f32_16x16x64_bf16 v[52:55], v[20:23], a[224:231], v68\n\t" \
  "s_waitcnt lgkmcnt(2)\n\t" \
  "v_add_f32 v4, v4, v8\n\t" \
  "v_add_f32 v5, v5, v9\n\t" \
  "v_add_f32 v6, v6, v10\n\t" \
  "v_cndmask_b32 v78, v13, v14, s[56:57]\n\t" \
  "v_cndmask_b32 v76, v12, v15, s[56:57]\n\t" \
  "v_add_f32 v7, v7, v11\n\t" \
  "v_max3_f32 v77, v4, v5, v6\n\t" \
  "v_add_f32_dpp v76, v78, v76 quad_perm:[2,3,0,1] row_mask:0xf bank_mask:0xf\n\t" \
  "v_max3_f32 v77, v77, v7, v76\n\t" \
  "v_smfmac_f32_16x16x64_bf16 v[56:59], v[20:23], a[232:239], v68\n\t" \
  "ds_read_b128 v[44:47], v70 offset:16384\n\t" \
  "v_max_f32_dpp v77, v77, v77 quad_perm:[2,3,0,1] row_mask:0xf bank_mask:0xf\n\t" \
  "v_mov_b32 v78, v77\n\t" \
  "v_smfmac_f32_16x16x64_bf16 v[52:55], v[24:27], v[96:103], v68\n\t" \
  "ds_read_b128 v[48:51], v70 offset:17408\n\t" \
  "v_permlane16_swap_b32 v77, v78\n\t" \
  "v_max_f32 v77, v77, v78\n\t" \
  "v_mov_b32 v78, v77\n\t" \
  "v_smfmac_f32_16x16x64_bf16 v[52:55], v[28:31], v[144:151], v68\n\t" \
  "ds_read_b128 v[16:19], v70 offset:18432\n\t" \
  "v_permlane32_swap_b32 v77, v78\n\t" \
  "v_max_f32 v77, v77, v78\n\t" \
  "v_smfmac_f32_16x16x64_bf16 v[52:55], v[32:35], v[184:191], v68\n\t" \
  "ds_read_b128 v[20:23], v70 offset:19456\n\t" \
  "v_mul_f32 v77, 0xbe0293ee, v77\n\t" \
  "v_smfmac_f32_16x16x64_bf16 v[56:59], v[24:27], v[104:111], v68\n\t" \
  "v_fmamk_f32 v4, v4, 0x3e0293ee, v77\n\t" \
  "v_smfmac_f32_16x16x64_bf16 v[56:59], v[28:31], v[152:159], v68\n\t" \
  "ds_read_b128 v[24:27], v70 offset:22528\n\t" \
  "s_waitcnt lgkmcnt(4)\n\t" \
  "v_fmamk_f32 v5, v5, 0x3e0293ee, v77\n\t" \
  "v_smfmac_f32_16x16x64_bf16 v[56:59], v[32:35], v[36:43], v68\n\t" \
  "ds_read_b128 v[28:31], v70 offset:23552\n\t" \
  "v_fmamk_f32 v6, v6, 0x3e0293ee, v77\n\t" \
  "v_fmamk_f32 v7, v7, 0x3e0293ee, v77\n\t" \
  "v_fmamk_f32 v76, v76, 0x3e0293ee, v77\n\t" \
  "v_exp_f32 v4, v4\n\t" \
  "v_exp_f32 v5, v5\n\t" \
  "v_exp_f32 v6, v6\n\t" \
  "v_exp_f32 v7, v7\n\t" \
  "v_cvt_pk_bf16_f32 v4, v4, v5\n\t" \
  "v_exp_f32 v76, v76\n\t" \
  "v_cvt_pk_bf16_f32 v5, v6, v7\n\t" \
  "v_mov_b32_dpp v12, v4 quad_perm:[0,1,0,1] row_mask:0xf bank_mask:0xf\n\t" \
  "v_mov_b32_dpp v14, v4 quad_perm:[2,3,2,3] row_mask:0xf bank_mask:0xf\n\t" \
  "v_mov_b32_dpp v13, v5 quad_perm:[0,1,0,1] row_mask:0xf bank_mask:0xf\n\t" \
  "v_mov_b32_dpp v15, v5 quad_perm:[2,3,2,3] row_mask:0xf bank_mask:0xf\n\t" \
  "v_add_f32 v52, v52, v54\n\t" \
  "v_add_f32 v53, v53, v55\n\t" \
  "s_waitcnt vmcnt(2)\n\t" \
  "v_mfma_f32_16x16x32_bf16 v[0:3], v[60:63], v[12:15], 0\n\t" \
  "ds_read_b128 v[36:39], v70 offset:20480\n\t" \
  "v_add_f32 v56, v56, v58\n\t" \
  "v_add_f32 v57, v57, v59\n\t" \
  "v_cvt_pk_bf16_f32 v52, v52, v53\n\t" \
  "v_cvt_pk_bf16_f32 v53, v56, v57\n\t" \
  "s_waitcnt vmcnt(1)\n\t" \
  "v_mfma_f32_16x16x32_bf16 v[4:7], v[64:67], v[12:15], 0\n\t" \
  "buffer_store_dwordx2 v[52:53], v71, s[28:31], s69 offen offset:2048 sc1\n\t" \
  "ds_read_b128 v[40:43], v70 offset:21504\n\t" \
  "v_mov_b64 v[56:57], s[88:89]\n\t" \
  "v_mov_b64 v[58:59], s[88:89]\n\t" \
  "v_mov_b32_dpp v78, v76 quad_perm:[0,1,0,1] row_mask:0xf bank_mask:0xf\n\t" \
  "v_mov_b32_dpp v79, v76 quad_perm:[2,3,2,3] row_mask:0xf bank_mask:0xf\n\t" \
  "v_mfma_f32_16x16x32_bf16 v[32:35], v[56:59], v[12:15], 0\n\t" \
  "ds_read_b128 v[60:63], v70 offset:24576\n\t" \
  "v_cvt_pk_bf16_f32 v11, v78, v79\n\t" \
  "v_cvt_pk_bf16_f32 v8, v78, v79\n\t" \
  "v_mov_b64 v[54:55], 0\n\t" \
  "v_mov_b32 v9, 0\n\t" \
  "v_mov_b32 v10, 0\n\t" \
  "v_mov_b64 v[14:15], 0\n\t" \
  "s_nop 0\n\t" \
  "v_mfma_f32_16x16x32_bf16 v[32:35], v[54:57], v[8:11], v[32:35]\n\t" \
  "s_cmp_eq_u32 s48, 63\n\t" \
  "v_mfma_f32_16x16x32_bf16 v[0:3], v[52:55], v[8:11], v[0:3]\n\t" \
  "ds_read_b128 v[64:67], v70 offset:25600\n\t" \
  "v_mov_b64 v[56:57], 0\n\t" \
  "v_mov_b64 v[58:59], 0\n\t" \
  "v_mfma_f32_16x16x32_bf16 v[4:7], v[52:55], v[10:13], v[4:7]\n\t" \
  "s_cbranch_scc1 LLAST0_%=\n\t" \
  "v_rcp_f32 v74, v32\n\t" \
  "v_mov_b64 v[12:13], 0\n\t" \
  "v_mov_b32 v8, 0\n\t" \
  "v_pk_mul_f32 v[0:1], v[0:1], v[74:75] op_sel_hi:[1,0]\n\t" \
  "v_pk_mul_f32 v[2:3], v[2:3], v[74:75] op_sel_hi:[1,0]\n\t" \
  "v_cvt_pk_bf16_f32 v76, v0, v1\n\t" \
  "v_cvt_pk_bf16_f32 v77, v2, v3\n\t" \
  "v_pk_mul_f32 v[4:5], v[4:5], v[74:75] op_sel_hi:[1,0]\n\t" \
  "v_pk_mul_f32 v[6:7], v[6:7], v[74:75] op_sel_hi:[1,0]\n\t" \
  "v_cvt_pk_bf16_f32 v78, v4, v5\n\t" \
  "v_cvt_pk_bf16_f32 v79, v6, v7\n\t" \
  "ds_write_b128 v69, v[76:79] offset:4096\n\t" \
  "s_waitcnt lgkmcnt(2)\n\t" \
  "v_smfmac_f32_16x16x64_bf16 v[12:15], v[76:79], v[44:51], v68\n\t" \
  "ds_read_b128 v[44:47], v70 offset:26624\n\t" \
  "v_mov_b32 v11, 0\n\t" \
  "v_smfmac_f32_16x16x64_bf16 v[56:59], v[76:79], v[16:23], v68\n\t" \
  "ds_read_b128 v[48:51], v70 offset:27648\n\t" \
  "v_mov_b64 v[52:53], 0\n\t" \
  "v_smfmac_f32_16x16x64_bf16 v[8:11], v[76:79], v[36:43], v68\n\t" \
  "ds_read_b128 v[16:19], v70 offset:28672\n\t" \
  "v_smfmac_f32_16x16x64_bf16 v[52:55], v[76:79], v[24:31], v68\n\t" \
  "s_waitcnt lgkmcnt(3)\n\t" \
  "s_barrier\n\t" \
  "ds_read_b128 v[0:3], v69 offset:5120\n\t" \
  "ds_read_b128 v[4:7], v69 offset:6144\n\t" \
  "v_lshrrev_b32 v71, 7, v69\n\t" \
  "v_and_b32 v74, 0x70, v69\n\t" \
  "v_lshl_add_u32 v71, v71, 14, v74\n\t" \
  "v_bfe_u32 v72, v69, 2, 7\n\t" \
  "v_and_b32 v74, 0x200, v69\n\t" \
  "s_waitcnt lgkmcnt(1)\n\t" \
  "v_smfmac_f32_16x16x64_bf16 v[8:11], v[0:3], v[60:67], v68\n\t" \
  "ds_read_b128 v[20:23], v70 offset:29696\n\t" \
  "v_mov_b64 v[76:77], 0\n\t" \
  "v_smfmac_f32_16x16x64_bf16 v[12:15], v[0:3], v[200:207], v68\n\t" \
  "ds_read_b128 v[32:35], v69 offset:7168\n\t" \
  "v_mov_b64 v[78:79], 0\n\t" \
  "v_smfmac_f32_16x16x64_bf16 v[52:55], v[0:3], v[44:51], v68\n\t" \
  "ds_read_b128 v[24:27], v70 offset:30720\n\t" \
  "v_lshl_add_u32 v72, v74, 3, v72\n\t" \
  "v_smfmac_f32_16x16x64_bf16 v[56:59], v[0:3], v[232:239], v68\n\t" \
  "buffer_load_dword v75, off, s[28:31], s80 sc1\n\t" \
  "ds_read_b128 v[28:31], v70 offset:31744\n\t" \
  "s_waitcnt lgkmcnt(4)\n\t" \
  "v_smfmac_f32_16x16x64_bf16 v[12:15], v[4:7], v[208:215], v68\n\t" \
  "ds_read_b128 v[60:63], v70 offset:32768\n\t" \
  "s_waitcnt lgkmcnt(4)\n\t" \
  "v_smfmac_f32_16x16x64_bf16 v[8:11], v[4:7], v[16:23], v68\n\t" \
  "ds_read_b128 v[64:67], v70 offset:33792\n\t" \
  "s_waitcnt lgkmcnt(4)\n\t" \
  "v_smfmac_f32_16x16x64_bf16 v[8:11], v[32:35], v[192:199], v68\n\t" \
  "ds_read_b128 v[36:39], v73 offset:4096\n\t" \
  "v_smfmac_f32_16x16x64_bf16 v[12:15], v[32:35], v[216:223], v68\n\t" \
  "ds_read_b128 v[40:43], v73 offset:6144\n\t" \
  "s_waitcnt lgkmcnt(4)\n\t" \
  "v_smfmac_f32_16x16x64_bf16 v[52:55], v[4:7], v[24:31], v68\n\t" \
  "ds_read_b128 v[44:47], v70 offset:34816\n\t" \
  "v_cvt_pk_bf16_f32 v8, v8, v9\n\t" \
  "v_smfmac_f32_16x16x64_bf16 v[52:55], v[32:35], v[224:231], v68\n\t" \
  "ds_read_b128 v[48:51], v70 offset:35840\n\t" \
  "v_cvt_pk_bf16_f32 v9, v10, v11\n\t" \
  "v_smfmac_f32_16x16x64_bf16 v[56:59], v[4:7], v[240:247], v68\n\t" \
  "s_waitcnt lgkmcnt(4)\n\t" \
  "v_cvt_pk_bf16_f32 v10, v12, v13\n\t" \
  "v_smfmac_f32_16x16x64_bf16 v[56:59], v[32:35], v[60:67], v68\n\t" \
  "s_waitcnt lgkmcnt(3)\n\t" \
  "v_cvt_pk_bf16_f32 v11, v14, v15\n\t" \
  "v_smfmac_f32_16x16x64_bf16 v[76:79], v[36:39], v[248:255], v68\n\t" \
  "buffer_store_dwordx4 v[8:11], v71, s[28:31], s54 offen sc1\n\t" \
  "s_waitcnt lgkmcnt(0)\n\t" \
  "v_cvt_pk_bf16_f32 v52, v52, v53\n\t" \
  "v_smfmac_f32_16x16x64_bf16 v[76:79], v[40:43], v[44:51], v68\n\t" \
  "v_cvt_pk_bf16_f32 v53, v54, v55\n\t" \
  "v_cvt_pk_bf16_f32 v54, v56, v57\n\t" \
  "v_cvt_pk_bf16_f32 v55, v58, v59\n\t" \
  "buffer_store_dwordx4 v[52:55], v71, s[28:31], s75 offen sc1\n\t" \
  "s_nop 2\n\t" \
  "v_add_f32 v76, v76, v78\n\t" \
  "v_add_f32 v77, v77, v79\n\t" \
  "v_cvt_pk_bf16_f32 v76, v76, v77\n\t" \
  "buffer_store_dword v76, v72, s[28:31], s55 offen sc1\n\t" \
  "s_memrealtime s[70:71]\n\t" \
  "s_waitcnt vmcnt(3)\n\t" \
  "v_readfirstlane_b32 s65, v75\n\t" \
  "s_barrier\n\t" \
  "s_waitcnt lgkmcnt(0)\n\t" \
  "s_not_b32 s65, s65\n\t" \
  "s_lshl_b32 s61, s70, 8\n\t" \
  "s_andn2_b32 s64, s65, 0xff\n\t" \
  "s_sub_u32 s64, s64, s61\n\t" \
  "s_ashr_i32 s64, s64, 8\n\t" \
  "s_cmp_eq_u32 s65, 0\n\t" \
  "s_cselect_b32 s64, " CH_DLF ", s64\n\t" \
  "s_cselect_b32 s65, " CH_B0 ", s65\n\t" \
  "s_add_u32 s47, s64, s70\n\t" \
  "s_min_i32 s64, s64, s34\n\t" \
  "s_max_i32 s64, s64, " CH_DLL1 "\n\t" \
  "s_add_u32 s64, s64, s70\n\t" \
  "s_add_u32 s55, s64, " CH_FDL "\n\t" \
  "s_movk_i32 s60, 8192\n\t" \
  "FWT_%=:\n\t" \
  "s_memrealtime s[62:63]\n\t" \
  "s_sub_u32 s60, s60, 1\n\t" \
  "s_cselect_b32 s61, 0, -1\n\t" \
  "s_waitcnt lgkmcnt(0)\n\t" \
  "s_sub_i32 s63, s62, s55\n\t" \
  "s_and_b32 s63, s63, s61\n\t" \
  "s_cmp_lt_i32 s63, 0\n\t" \
  "s_cbranch_scc1 FWT_%=\n\t" \
  "s_mov_b64 exec, 1\n\t" \
  "buffer_store_dword v68, off, s[28:31], s51 offset:4064 sc1\n\t" \
  "s_mov_b64 exec, -1\n\t" \
  "s_and_b32 s65, s65, 0xff\n\t" \
  "s_add_u32 s47, s47, s65\n\t" \
  "s_add_u32 s65, s65, s64\n\t" \
  "s_sub_u32 s60, s70, s66\n\t" \
  "s_add_u32 s61, s81, " CH_UP "\n\t" \
  "s_min_u32 s60, s60, s61\n\t" \
  "s_sub_u32 s61, s81, " CH_DN "\n\t" \
  "s_max_u32 s60, s60, s61\n\t" \
  "s_cmp_eq_u32 s68, 0\n\t" \
  "s_cselect_b32 s81, s60, s81\n\t" \
  "s_add_u32 s48, s48, 1\n\t" \
  "s_mov_b32 s49, s53\n\t" \
  "s_add_u32 s53, s53, " CH_SLOT "\n\t" \
  "s_add_u32 s60, s53, " CH_SKIP "\n\t" \
  "s_lshr_b32 s61, s96, 1\n\t" \
  "s_add_u32 s86, s86, 1\n\t" \
  "s_cmp_eq_u32 s86, 3\n\t" \
  "s_cselect_b32 s53, s60, s53\n\t" \
  "s_cselect_b32 s96, s61, s96\n\t" \
  "s_cselect_b32 s86, 0, s86\n\t" \
  "s_cmp_ge_u32 s53, " CH_RING "\n\t" \
  "s_cselect_b32 s53, 0, s53\n\t" \
  "s_cselect_b32 s96, s21, s96\n\t" \
  "s_andn2_b32 s53, s53, 0x2000\n\t" \
  "s_and_b32 s60, s96, 1\n\t" \
  "s_lshl_b32 s60, s60, 13\n\t" \
  "s_or_b32 s53, s53, s60\n\t" \
  "s_mov_b32 s68, 0\n\t" \
  "s_add_u32 s82, s48, 0\n\t" \
  "s_and_b32 s82, s82, " CH_TRM "\n\t" \
  "s_lshr_b64 s[60:61], s[90:91], s82\n\t" \
  "s_and_b32 s60, s60, 1\n\t" \
  "s_lshl_b32 s60, s60, 13\n\t" \
  "s_add_u32 s82, s82, " CH_SG0 "\n\t" \
  "s_lshl_b32 s82, s82, 21\n\t" \
  "s_or_b32 s82, s82, s60\n\t" \
  "s_add_u32 s50, s82, s38\n\t" \
  "s_add_u32 s69, s82, s39\n\t" \
  "s_add_u32 s82, s48, 0\n\t" \
  "s_lshr_b64 s[60:61], s[90:91], s82\n\t" \
  "s_and_b32 s60, s60, 1\n\t" \
  "s_lshl_b32 s60, s60, 13\n\t" \
  "s_add_u32 s82, s82, " CH_SG0 "\n\t" \
  "s_lshl_b32 s82, s82, 21\n\t" \
  "s_or_b32 s82, s82, s60\n\t" \
  "s_add_u32 s51, s82, s43\n\t" \
  "s_cmp_lg_u32 s34, 0\n\t" \
  "s_cbranch_scc1 LW0_%=\n\t" \
  "s_barrier\n\t" \
  "s_branch LINI_%=\n\t" \
  "LFAIL0_%=:\n\t" \
  "v_max_u32 v75, v0, v1\n\t" \
  "v_max3_u32 v75, v75, v2, v3\n\t" \
  "v_max3_u32 v75, v75, v4, v5\n\t" \
  "v_max3_u32 v75, v75, v6, v7\n\t" \
  "v_max3_u32 v75, v75, v8, v9\n\t" \
  "v_max3_u32 v75, v75, v10, v11\n\t" \
  "v_max3_u32 v75, v75, v12, v13\n\t" \
  "v_max3_u32 v75, v75, v14, v15\n\t" \
  "v_max3_u32 v75, v75, v16, v17\n\t" \
  "v_max3_u32 v75, v75, v18, v19\n\t" \
  "v_max3_u32 v75, v75, v20, v21\n\t" \
  "v_max3_u32 v75, v75, v22, v23\n\t" \
  "v_max3_u32 v75, v75, v24, v25\n\t" \
  "v_max3_u32 v75, v75, v26, v27\n\t" \
  "v_max3_u32 v75, v75, v28, v29\n\t" \
  "v_max3_u32 v75, v75, v30, v31\n\t" \
  "v_max3_u32 v75, v75, v32, v33\n\t" \
  "v_max3_u32 v75, v75, v34, v35\n\t" \
  "v_cmp_eq_u32 vcc, -1, v75\n\t" \
  "s_cbranch_vccz LGO0_%=\n\t" \
  "s_add_u32 s68, s68, 1\n\t" \
  "s_waitcnt lgkmcnt(0)\n\t" \
  "s_cmp_gt_u32 s68, s83\n\t" \
  "s_cbranch_scc1 LGV0_%=\n\t" \
  "s_barrier\n\t" \
  "s_memrealtime s[62:63]\n\t" \
  "s_mov_b32 m0, 0\n\t" \
  "s_waitcnt lgkmcnt(0)\n\t" \
  "s_add_u32 s65, s62, " CH_PEN "\n\t" \
  "s_branch LDMA_%=\n\t" \
  "LWT_%=f:\n\t" \
  "s_cmp_lt_i32 s63, -2\n\t" \
  "s_cbranch_scc0 LWT_%=g\n\t" \
  "s_nop 4\n\t" \
  "s_branch LWT_%=g\n\t" \
  "LGV0_%=:\n\t" \
  "s_mov_b32 s83, 0\n\t" \
  "s_lshl_b32 s60, s36, 4\n\t" \
  "s_or_b32 s60, s60, s48\n\t" \
  "s_or_b32 s60, s60, 805306368\n\t" \
  "v_mov_b32 v76, s60\n\t" \
  "v_mov_b32 v77, 0\n\t" \
  "s_mov_b64 exec, 1\n\t" \
  "buffer_atomic_cmpswap v[76:77], off, s[28:31], s92 sc1\n\t" \
  "s_mov_b64 exec, -1\n\t" \
  "s_branch LGO0_%=\n\t" \
  "LLAST0_%=:\n\t" \
  "s_waitcnt vmcnt(0) lgkmcnt(0)\n\t" \
  "s_barrier\n\t" \
  "s_mov_b64 exec, 1\n\t" \
  "buffer_store_dword v68, off, s[28:31], s51 offset:4064 sc1\n\t" \
  "s_mov_b64 exec, -1\n\t" \
  "s_branch LEND_%=\n\t" \
  ".p2align 6\n\t" \
  "LG1_%=:\n\t" \
  "s_barrier\n\t" \
  "buffer_load_dwordx4 v[52:55], v69, s[28:31], s50 offen sc1\n\t" \
  "buffer_load_dwordx4 v[56:59], v69, s[28:31], s50 offen offset:1024 sc1\n\t" \
  "s_add_u32 s64, s50, 14336\n\t" \
  "s_movk_i32 m0, 11264\n\t" \
  "v_lshlrev_b32 v75, 1, v69\n\t" \
  "buffer_load_dword v75, s[28:31], s64 offen sc1 lds\n\t" \
  "ds_read_b128 v[44:47], v70 offset:0\n\t" \
  "ds_read_b128 v[48:51], v70 offset:1024\n\t" \
  "ds_read_b128 v[60:63], v70 offset:2048\n\t" \
  "ds_read_b128 v[64:67], v70 offset:3072\n\t" \
  "v_mov_b64 v[36:37], 0\n\t" \
  "v_mov_b64 v[38:39], 0\n\t" \
  "v_mov_b64 v[40:41], 0\n\t" \
  "v_mov_b64 v[42:43], 0\n\t" \
  "v_lshrrev_b32 v71, 1, v69\n\t" \
  "v_add_u32 v71, 4, v71\n\t" \
  "v_lshrrev_b32 v72, 3, v69\n\t" \
  "v_and_b32 v72, 0x78, v72\n\t" \
  "v_and_b32 v74, 0x30, v69\n\t" \
  "v_lshl_add_u32 v72, v74, 3, v72\n\t" \
  "s_add_u32 s54, s53, s36\n\t" \
  "s_add_u32 s55, s54, 540672\n\t" \
  "s_add_u32 s54, s54, 4096\n\t" \
  "s_add_u32 s75, s54, 262144\n\t" \
  "s_add_u32 s80, s53, s26\n\t" \
  "s_barrier\n\t" \
  "ds_read_b128 v[0:3], v69 offset:0\n\t" \
  "ds_read_b128 v[4:7], v69 offset:1024\n\t" \
  "ds_read_b128 v[8:11], v69 offset:2048\n\t" \
  "ds_read_b128 v[12:15], v69 offset:3072\n\t" \
  "s_waitcnt lgkmcnt(3)\n\t" \
  "v_smfmac_f32_16x16x64_bf16 v[36:39], v[0:3], v[44:51], v68\n\t" \
  "ds_read_b128 v[44:47], v70 offset:4096\n\t" \
  "v_smfmac_f32_16x16x64_bf16 v[40:43], v[0:3], v[60:67], v68\n\t" \
  "ds_read_b128 v[48:51], v70 offset:5120\n\t" \
  "s_waitcnt lgkmcnt(4)\n\t" \
  "v_smfmac_f32_16x16x64_bf16 v[36:39], v[4:7], a[32:39], v68\n\t" \
  "ds_read_b128 v[60:63], v70 offset:6144\n\t" \
  "v_smfmac_f32_16x16x64_bf16 v[40:43], v[4:7], a[40:47], v68\n\t" \
  "ds_read_b128 v[64:67], v70 offset:7168\n\t" \
  "s_waitcnt lgkmcnt(5)\n\t" \
  "v_smfmac_f32_16x16x64_bf16 v[36:39], v[8:11], a[80:87], v68\n\t" \
  "s_barrier\n\t" \
  "ds_read_b128 v[16:19], v69 offset:4096\n\t" \
  "v_smfmac_f32_16x16x64_bf16 v[40:43], v[8:11], a[88:95], v68\n\t" \
  "ds_read_b128 v[20:23], v69 offset:5120\n\t" \
  "s_waitcnt lgkmcnt(6)\n\t" \
  "v_smfmac_f32_16x16x64_bf16 v[36:39], v[12:15], a[120:127], v68\n\t" \
  "ds_read_b128 v[24:27], v69 offset:6144\n\t" \
  "s_waitcnt lgkmcnt(5)\n\t" \
  "v_smfmac_f32_16x16x64_bf16 v[40:43], v[12:15], v[44:51], v68\n\t" \
  "ds_read_b128 v[28:31], v69 offset:7168\n\t" \
  "s_waitcnt lgkmcnt(3)\n\t" \
  "v_smfmac_f32_16x16x64_bf16 v[36:39], v[16:19], a[160:167], v68\n\t" \
  "ds_read_b128 v[32:35], v69 offset:8192\n\t" \
  "v_smfmac_f32_16x16x64_bf16 v[40:43], v[16:19], a[168:175], v68\n\t" \
  "s_waitcnt lgkmcnt(3)\n\t" \
  "v_mov_b64 v[44:45], 0\n\t" \
  "v_smfmac_f32_16x16x64_bf16 v[36:39], v[20:23], a[200:207], v68\n\t" \
  "s_waitcnt lgkmcnt(2)\n\t" \
  "v_mov_b64 v[46:47], 0\n\t" \
  "v_smfmac_f32_16x16x64_bf16 v[36:39], v[24:27], a[240:247], v68\n\t" \
  "s_waitcnt lgkmcnt(1)\n\t" \
  "v_mov_b64 v[48:49], 0\n\t" \
  "v_smfmac_f32_16x16x64_bf16 v[36:39], v[28:31], v[112:119], v68\n\t" \
  "s_waitcnt lgkmcnt(0)\n\t" \
  "v_mov_b64 v[50:51], 0\n\t" \
  "v_smfmac_f32_16x16x64_bf16 v[36:39], v[32:35], v[160:167], v68\n\t" \
  "v_smfmac_f32_16x16x64_bf16 v[40:43], v[20:23], v[60:67], v68\n\t" \
  "ds_read_b128 v[60:63], v70 offset:8192\n\t" \
  "v_smfmac_f32_16x16x64_bf16 v[40:43], v[24:27], a[248:255], v68\n\t" \
  "ds_read_b128 v[64:67], v70 offset:9216\n\t" \
  "v_add_f32 v74, v36, v38\n\t" \
  "v_smfmac_f32_16x16x64_bf16 v[40:43], v[28:31], v[120:127], v68\n\t" \
  "v_add_f32 v37, v37, v39\n\t" \
  "v_smfmac_f32_16x16x64_bf16 v[40:43], v[32:35], v[168:175], v68\n\t" \
  "v_cmp_u_f32 vcc, v74, v37\n\t" \
  "v_smfmac_f32_16x16x64_bf16 v[44:47], v[0:3], a[0:7], v68\n\t" \
  "s_cbranch_vccnz LFAIL1_%=\n\t" \
  "LGO1_%=:\n\t" \
  "v_cvt_pk_bf16_f32 v74, v74, v37\n\t" \
  "v_smfmac_f32_16x16x64_bf16 v[44:47], v[4:7], a[48:55], v68\n\t" \
  "s_waitcnt lgkmcnt(0)\n\t" \
  "v_add_f32 v75, v40, v42\n\t" \
  "v_smfmac_f32_16x16x64_bf16 v[44:47], v[8:11], v[60:67], v68\n\t" \
  "ds_read_b128 v[60:63], v70 offset:10240\n\t" \
  "v_add_f32 v41, v41, v43\n\t" \
  "v_cvt_pk_bf16_f32 v75, v75, v41\n\t" \
  "v_smfmac_f32_16x16x64_bf16 v[44:47], v[12:15], a[128:135], v68\n\t" \
  "ds_write2st64_b32 v71, v74, v75 offset0:36 offset1:38\n\t" \
  "v_smfmac_f32_16x16x64_bf16 v[44:47], v[16:19], a[176:183], v68\n\t" \
  "ds_read_b128 v[64:67], v70 offset:11264\n\t" \
  "v_smfmac_f32_16x16x64_bf16 v[44:47], v[20:23], a[208:215], v68\n\t" \
  "v_smfmac_f32_16x16x64_bf16 v[44:47], v[24:27], v[80:87], v68\n\t" \
  "v_smfmac_f32_16x16x64_bf16 v[44:47], v[28:31], v[128:135], v68\n\t" \
  "s_waitcnt lgkmcnt(0)\n\t" \
  "v_smfmac_f32_16x16x64_bf16 v[44:47], v[32:35], v[60:67], v68\n\t" \
  "s_barrier\n\t" \
  "ds_read_b64_tr_b16 v[76:77], v72 offset:9216\n\t" \
  "v_smfmac_f32_16x16x64_bf16 v[48:51], v[0:3], a[8:15], v68\n\t" \
  "buffer_load_dwordx4 v[60:63], v69, s[28:31], s64 offen sc1\n\t" \
  "ds_read_b64_tr_b16 v[78:79], v72 offset:9728\n\t" \
  "v_smfmac_f32_16x16x64_bf16 v[48:51], v[4:7], a[56:63], v68\n\t" \
  "v_add_f32 v74, v44, v46\n\t" \
  "v_smfmac_f32_16x16x64_bf16 v[48:51], v[8:11], a[96:103], v68\n\t" \
  "v_add_f32 v45, v45, v47\n\t" \
  "v_smfmac_f32_16x16x64_bf16 v[48:51], v[12:15], a[136:143], v68\n\t" \
  "s_waitcnt vmcnt(3)\n\t" \
  "s_waitcnt lgkmcnt(0)\n\t" \
  "v_cvt_pk_bf16_f32 v74, v74, v45\n\t" \
  "v_mfma_f32_16x16x32_bf16 v[36:39], v[52:55], v[76:79], 0\n\t" \
  "s_waitcnt vmcnt(2)\n\t" \
  "v_mfma_f32_16x16x32_bf16 v[40:43], v[56:59], v[76:79], 0\n\t" \
  "buffer_load_dwordx4 v[64:67], v69, s[28:31], s64 offen offset:1024 sc1\n\t" \
  "v_mov_b64 v[52:53], 0\n\t" \
  "v_mov_b64 v[54:55], 0\n\t" \
  "v_smfmac_f32_16x16x64_bf16 v[48:51], v[16:19], a[184:191], v68\n\t" \
  "v_mov_b64 v[56:57], 0\n\t" \
  "v_mov_b64 v[58:59], 0\n\t" \
  "v_smfmac_f32_16x16x64_bf16 v[48:51], v[20:23], a[216:223], v68\n\t" \
  "ds_write_b32 v71, v74 offset:0\n\t" \
  "v_add_f32_dpp v36, v40, v36 quad_perm:[2,3,0,1] row_mask:0xf bank_mask:0xf\n\t" \
  "v_smfmac_f32_16x16x64_bf16 v[48:51], v[24:27], v[88:95], v68\n\t" \
  "v_add_f32_dpp v37, v41, v37 quad_perm:[2,3,0,1] row_mask:0xf bank_mask:0xf\n\t" \
  "v_smfmac_f32_16x16x64_bf16 v[48:51], v[28:31], v[136:143], v68\n\t" \
  "v_add_f32_dpp v38, v42, v38 quad_perm:[2,3,0,1] row_mask:0xf bank_mask:0xf\n\t" \
  "v_smfmac_f32_16x16x64_bf16 v[48:51], v[32:35], v[176:183], v68\n\t" \
  "v_add_f32_dpp v39, v43, v39 quad_perm:[2,3,0,1] row_mask:0xf bank_mask:0xf\n\t" \
  "v_smfmac_f32_16x16x64_bf16 v[52:55], v[0:3], a[16:23], v68\n\t" \
  "s_mov_b64 exec, s[58:59]\n\t" \
  "ds_write_b128 v69, v[36:39] offset:2080\n\t" \
  "s_mov_b64 exec, -1\n\t" \
  "v_smfmac_f32_16x16x64_bf16 v[52:55], v[4:7], a[64:71], v68\n\t" \
  "v_add_f32 v75, v48, v50\n\t" \
  "v_add_f32 v49, v49, v51\n\t" \
  "v_cvt_pk_bf16_f32 v75, v75, v49\n\t" \
  "v_smfmac_f32_16x16x64_bf16 v[56:59], v[0:3], a[24:31], v68\n\t" \
  "ds_write_b32 v71, v75 offset:512\n\t" \
  "v_smfmac_f32_16x16x64_bf16 v[56:59], v[4:7], a[72:79], v68\n\t" \
  "ds_read_b64_tr_b16 v[0:1], v72 offset:10240\n\t" \
  "ds_read_b64_tr_b16 v[2:3], v72 offset:10752\n\t" \
  "v_smfmac_f32_16x16x64_bf16 v[52:55], v[8:11], a[104:111], v68\n\t" \
  "ds_read_b128 v[36:39], v70 offset:12288\n\t" \
  "v_smfmac_f32_16x16x64_bf16 v[56:59], v[8:11], a[112:119], v68\n\t" \
  "ds_read_b128 v[40:43], v70 offset:13312\n\t" \
  "v_smfmac_f32_16x16x64_bf16 v[52:55], v[12:15], a[144:151], v68\n\t" \
  "s_waitcnt lgkmcnt(4)\n\t" \
  "s_barrier\n\t" \
  "ds_read_b64_tr_b16 v[44:45], v72 offset:0\n\t" \
  "ds_read_b64_tr_b16 v[46:47], v72 offset:512\n\t" \
  "v_smfmac_f32_16x16x64_bf16 v[56:59], v[12:15], a[152:159], v68\n\t" \
  "ds_read_b64_tr_b16 v[48:49], v72 offset:1024\n\t" \
  "ds_read_b64_tr_b16 v[50:51], v72 offset:1536\n\t" \
  "v_smfmac_f32_16x16x64_bf16 v[52:55], v[16:19], a[192:199], v68\n\t" \
  "ds_read_b128 v[4:7], v69 offset:2048\n\t" \
  "s_waitcnt lgkmcnt(5)\n\t" \
  "v_smfmac_f32_16x16x64_bf16 v[56:59], v[16:19], v[36:43], v68\n\t" \
  "ds_read_b128 v[8:11], v69 offset:3072\n\t" \
  "s_waitcnt lgkmcnt(4)\n\t" \
  "v_mfma_f32_16x16x32_bf16 v[12:15], v[44:47], v[76:79], 0\n\t" \
  "ds_read_b128 v[36:39], v70 offset:14336\n\t" \
  "s_waitcnt lgkmcnt(3)\n\t" \
  "v_mfma_f32_16x16x32_bf16 v[12:15], v[48:51], v[0:3], v[12:15]\n\t" \
  "buffer_store_dwordx4 v[44:47], v69, s[28:31], s69 offen offset:0 sc1\n\t" \
  "ds_read_b128 v[40:43], v70 offset:15360\n\t" \
  "v_smfmac_f32_16x16x64_bf16 v[52:55], v[20:23], a[224:231], v68\n\t" \
  "s_waitcnt lgkmcnt(2)\n\t" \
  "v_add_f32 v4, v4, v8\n\t" \
  "v_add_f32 v5, v5, v9\n\t" \
  "v_add_f32 v6, v6, v10\n\t" \
  "v_cndmask_b32 v78, v13, v14, s[56:57]\n\t" \
  "v_cndmask_b32 v76, v12, v15, s[56:57]\n\t" \
  "v_add_f32 v7, v7, v11\n\t" \
  "v_max3_f32 v77, v4, v5, v6\n\t" \
  "v_add_f32_dpp v76, v78, v76 quad_perm:[2,3,0,1] row_mask:0xf bank_mask:0xf\n\t" \
  "v_max3_f32 v77, v77, v7, v76\n\t" \
  "v_smfmac_f32_16x16x64_bf16 v[56:59], v[20:23], a[232:239], v68\n\t" \
  "ds_read_b128 v[44:47], v70 offset:16384\n\t" \
  "v_max_f32_dpp v77, v77, v77 quad_perm:[2,3,0,1] row_mask:0xf bank_mask:0xf\n\t" \
  "v_mov_b32 v78, v77\n\t" \
  "v_smfmac_f32_16x16x64_bf16 v[52:55], v[24:27], v[96:103], v68\n\t" \
  "ds_read_b128 v[48:51], v70 offset:17408\n\t" \
  "v_permlane16_swap_b32 v77, v78\n\t" \
  "v_max_f32 v77, v77, v78\n\t" \
  "v_mov_b32 v78, v77\n\t" \
  "v_smfmac_f32_16x16x64_bf16 v[52:55], v[28:31], v[144:151], v68\n\t" \
  "ds_read_b128 v[16:19], v70 offset:18432\n\t" \
  "v_permlane32_swap_b32 v77, v78\n\t" \
  "v_max_f32 v77, v77, v78\n\t" \
  "v_smfmac_f32_16x16x64_bf16 v[52:55], v[32:35], v[184:191], v68\n\t" \
  "ds_read_b128 v[20:23], v70 offset:19456\n\t" \
  "v_mul_f32 v77, 0xbe0293ee, v77\n\t" \
  "v_smfmac_f32_16x16x64_bf16 v[56:59], v[24:27], v[104:111], v68\n\t" \
  "v_fmamk_f32 v4, v4, 0x3e0293ee, v77\n\t" \
  "v_smfmac_f32_16x16x64_bf16 v[56:59], v[28:31], v[152:159], v68\n\t" \
  "ds_read_b128 v[24:27], v70 offset:22528\n\t" \
  "s_waitcnt lgkmcnt(4)\n\t" \
  "v_fmamk_f32 v5, v5, 0x3e0293ee, v77\n\t" \
  "v_smfmac_f32_16x16x64_bf16 v[56:59], v[32:35], v[36:43], v68\n\t" \
  "ds_read_b128 v[28:31], v70 offset:23552\n\t" \
  "v_fmamk_f32 v6, v6, 0x3e0293ee, v77\n\t" \
  "v_fmamk_f32 v7, v7, 0x3e0293ee, v77\n\t" \
  "v_fmamk_f32 v76, v76, 0x3e0293ee, v77\n\t" \
  "v_exp_f32 v4, v4\n\t" \
  "v_exp_f32 v5, v5\n\t" \
  "v_exp_f32 v6, v6\n\t" \
  "v_exp_f32 v7, v7\n\t" \
  "v_cvt_pk_bf16_f32 v4, v4, v5\n\t" \
  "v_exp_f32 v76, v76\n\t" \
  "v_cvt_pk_bf16_f32 v5, v6, v7\n\t" \
  "v_mov_b32_dpp v12, v4 quad_perm:[0,1,0,1] row_mask:0xf bank_mask:0xf\n\t" \
  "v_mov_b32_dpp v14, v4 quad_perm:[2,3,2,3] row_mask:0xf bank_mask:0xf\n\t" \
  "v_mov_b32_dpp v13, v5 quad_perm:[0,1,0,1] row_mask:0xf bank_mask:0xf\n\t" \
  "v_mov_b32_dpp v15, v5 quad_perm:[2,3,2,3] row_mask:0xf bank_mask:0xf\n\t" \
  "v_add_f32 v52, v52, v54\n\t" \
  "v_add_f32 v53, v53, v55\n\t" \
  "s_waitcnt vmcnt(2)\n\t" \
  "v_mfma_f32_16x16x32_bf16 v[0:3], v[60:63], v[12:15], 0\n\t" \
  "ds_read_b128 v[36:39], v70 offset:20480\n\t" \
  "v_add_f32 v56, v56, v58\n\t" \
  "v_add_f32 v57, v57, v59\n\t" \
  "v_cvt_pk_bf16_f32 v52, v52, v53\n\t" \
  "v_cvt_pk_bf16_f32 v53, v56, v57\n\t" \
  "s_waitcnt vmcnt(1)\n\t" \
  "v_mfma_f32_16x16x32_bf16 v[4:7], v[64:67], v[12:15], 0\n\t" \
  "ds_read_b128 v[40:43], v70 offset:21504\n\t" \
  "v_mov_b64 v[56:57], s[88:89]\n\t" \
  "v_mov_b64 v[58:59], s[88:89]\n\t" \
  "v_mov_b32_dpp v78, v76 quad_perm:[0,1,0,1] row_mask:0xf bank_mask:0xf\n\t" \
  "v_mov_b32_dpp v79, v76 quad_perm:[2,3,2,3] row_mask:0xf bank_mask:0xf\n\t" \
  "v_mfma_f32_16x16x32_bf16 v[32:35], v[56:59], v[12:15], 0\n\t" \
  "buffer_store_dwordx2 v[52:53], v71, s[28:31], s69 offen offset:2556 sc1\n\t" \
  "ds_read_b128 v[60:63], v70 offset:24576\n\t" \
  "v_cvt_pk_bf16_f32 v11, v78, v79\n\t" \
  "v_cvt_pk_bf16_f32 v8, v78, v79\n\t" \
  "v_mov_b64 v[54:55], 0\n\t" \
  "v_mov_b32 v9, 0\n\t" \
  "v_mov_b32 v10, 0\n\t" \
  "v_mov_b64 v[14:15], 0\n\t" \
  "s_nop 0\n\t" \
  "v_mfma_f32_16x16x32_bf16 v[32:35], v[54:57], v[8:11], v[32:35]\n\t" \
  "s_cmp_eq_u32 s48, 63\n\t" \
  "v_mfma_f32_16x16x32_bf16 v[0:3], v[52:55], v[8:11], v[0:3]\n\t" \
  "ds_read_b128 v[64:67], v70 offset:25600\n\t" \
  "v_mov_b64 v[56:57], 0\n\t" \
  "v_mov_b64 v[58:59], 0\n\t" \
  "v_mfma_f32_16x16x32_bf16 v[4:7], v[52:55], v[10:13], v[4:7]\n\t" \
  "s_cbranch_scc1 LLAST1_%=\n\t" \
  "v_rcp_f32 v74, v32\n\t" \
  "v_mov_b64 v[12:13], 0\n\t" \
  "v_mov_b32 v8, 0\n\t" \
  "v_pk_mul_f32 v[0:1], v[0:1], v[74:75] op_sel_hi:[1,0]\n\t" \
  "v_pk_mul_f32 v[2:3], v[2:3], v[74:75] op_sel_hi:[1,0]\n\t" \
  "v_cvt_pk_bf16_f32 v76, v0, v1\n\t" \
  "v_cvt_pk_bf16_f32 v77, v2, v3\n\t" \
  "v_pk_mul_f32 v[4:5], v[4:5], v[74:75] op_sel_hi:[1,0]\n\t" \
  "v_pk_mul_f32 v[6:7], v[6:7], v[74:75] op_sel_hi:[1,0]\n\t" \
  "v_cvt_pk_bf16_f32 v78, v4, v5\n\t" \
  "v_cvt_pk_bf16_f32 v79, v6, v7\n\t" \
  "ds_write_b128 v69, v[76:79] offset:5120\n\t" \
  "s_waitcnt lgkmcnt(2)\n\t" \
  "v_smfmac_f32_16x16x64_bf16 v[12:15], v[76:79], v[44:51], v68\n\t" \
  "ds_read_b128 v[44:47], v70 offset:26624\n\t" \
  "v_mov_b32 v11, 0\n\t" \
  "v_smfmac_f32_16x16x64_bf16 v[56:59], v[76:79], v[16:23], v68\n\t" \
  "ds_read_b128 v[48:51], v70 offset:27648\n\t" \
  "v_mov_b64 v[52:53], 0\n\t" \
  "v_smfmac_f32_16x16x64_bf16 v[8:11], v[76:79], v[36:43], v68\n\t" \
  "ds_read_b128 v[16:19], v70 offset:28672\n\t" \
  "v_smfmac_f32_16x16x64_bf16 v[52:55], v[76:79], v[24:31], v68\n\t" \
  "s_waitcnt lgkmcnt(3)\n\t" \
  "s_barrier\n\t" \
  "ds_read_b128 v[0:3], v69 offset:4096\n\t" \
  "ds_read_b128 v[4:7], v69 offset:7168\n\t" \
  "v_lshrrev_b32 v71, 7, v69\n\t" \
  "v_and_b32 v74, 0x70, v69\n\t" \
  "v_lshl_add_u32 v71, v71, 14, v74\n\t" \
  "v_bfe_u32 v72, v69, 2, 7\n\t" \
  "v_and_b32 v74, 0x200, v69\n\t" \
  "s_waitcnt lgkmcnt(1)\n\t" \
  "v_smfmac_f32_16x16x64_bf16 v[8:11], v[0:3], v[60:67], v68\n\t" \
  "buffer_load_dword v75, off, s[28:31], s80 sc1\n\t" \
  "ds_read_b128 v[20:23], v70 offset:29696\n\t" \
  "v_mov_b64 v[76:77], 0\n\t" \
  "v_smfmac_f32_16x16x64_bf16 v[12:15], v[0:3], v[200:207], v68\n\t" \
  "ds_read_b128 v[32:35], v69 offset:6144\n\t" \
  "v_mov_b64 v[78:79], 0\n\t" \
  "v_smfmac_f32_16x16x64_bf16 v[52:55], v[0:3], v[44:51], v68\n\t" \
  "ds_read_b128 v[24:27], v70 offset:30720\n\t" \
  "v_lshl_add_u32 v72, v74, 3, v72\n\t" \
  "v_smfmac_f32_16x16x64_bf16 v[56:59], v[0:3], v[232:239], v68\n\t" \
  "ds_read_b128 v[28:31], v70 offset:31744\n\t" \
  "s_waitcnt lgkmcnt(4)\n\t" \
  "v_smfmac_f32_16x16x64_bf16 v[12:15], v[4:7], v[208:215], v68\n\t" \
  "ds_read_b128 v[60:63], v70 offset:32768\n\t" \
  "s_waitcnt lgkmcnt(4)\n\t" \
  "v_smfmac_f32_16x16x64_bf16 v[8:11], v[4:7], v[16:23], v68\n\t" \
  "ds_read_b128 v[64:67], v70 offset:33792\n\t" \
  "s_waitcnt lgkmcnt(4)\n\t" \
  "v_smfmac_f32_16x16x64_bf16 v[8:11], v[32:35], v[192:199], v68\n\t" \
  "ds_read_b128 v[36:39], v73 offset:4096\n\t" \
  "v_smfmac_f32_16x16x64_bf16 v[12:15], v[32:35], v[216:223], v68\n\t" \
  "ds_read_b128 v[40:43], v73 offset:6144\n\t" \
  "s_waitcnt lgkmcnt(4)\n\t" \
  "v_smfmac_f32_16x16x64_bf16 v[52:55], v[4:7], v[24:31], v68\n\t" \
  "ds_read_b128 v[44:47], v70 offset:34816\n\t" \
  "v_cvt_pk_bf16_f32 v8, v8, v9\n\t" \
  "v_smfmac_f32_16x16x64_bf16 v[52:55], v[32:35], v[224:231], v68\n\t" \
  "ds_read_b128 v[48:51], v70 offset:35840\n\t" \
  "v_cvt_pk_bf16_f32 v9, v10, v11\n\t" \
  "v_smfmac_f32_16x16x64_bf16 v[56:59], v[4:7], v[240:247], v68\n\t" \
  "s_waitcnt lgkmcnt(4)\n\t" \
  "v_cvt_pk_bf16_f32 v10, v12, v13\n\t" \
  "v_smfmac_f32_16x16x64_bf16 v[56:59], v[32:35], v[60:67], v68\n\t" \
  "s_waitcnt lgkmcnt(3)\n\t" \
  "v_cvt_pk_bf16_f32 v11, v14, v15\n\t" \
  "v_smfmac_f32_16x16x64_bf16 v[76:79], v[36:39], v[248:255], v68\n\t" \
  "buffer_store_dwordx4 v[8:11], v71, s[28:31], s54 offen sc1\n\t" \
  "s_waitcnt lgkmcnt(0)\n\t" \
  "v_cvt_pk_bf16_f32 v52, v52, v53\n\t" \
  "v_smfmac_f32_16x16x64_bf16 v[76:79], v[40:43], v[44:51], v68\n\t" \
  "v_cvt_pk_bf16_f32 v53, v54, v55\n\t" \
  "v_cvt_pk_bf16_f32 v54, v56, v57\n\t" \
  "v_cvt_pk_bf16_f32 v55, v58, v59\n\t" \
  "buffer_store_dwordx4 v[52:55], v71, s[28:31], s75 offen sc1\n\t" \
  "s_nop 2\n\t" \
  "v_add_f32 v76, v76, v78\n\t" \
  "v_add_f32 v77, v77, v79\n\t" \
  "v_cvt_pk_bf16_f32 v76, v76, v77\n\t" \
  "buffer_store_dword v76, v72, s[28:31], s55 offen sc1\n\t" \
  "s_memrealtime s[70:71]\n\t" \
  "s_waitcnt vmcnt(3)\n\t" \
  "v_readfirstlane_b32 s65, v75\n\t" \
  "s_barrier\n\t" \
  "s_waitcnt lgkmcnt(0)\n\t" \
  "s_not_b32 s65, s65\n\t" \
  "s_lshl_b32 s61, s70, 8\n\t" \
  "s_andn2_b32 s64, s65, 0xff\n\t" \
  "s_sub_u32 s64, s64, s61\n\t" \
  "s_ashr_i32 s64, s64, 8\n\t" \
  "s_cmp_eq_u32 s65, 0\n\t" \
  "s_cselect_b32 s64, " CH_DLF ", s64\n\t" \
  "s_sub_i32 s64, s64, " CH_RO1 "\n\t" \
  "s_min_i32 s64, s64, s34\n\t" \
  "s_max_i32 s64, s64, " CH_DLL1 "\n\t" \
  "s_add_u32 s64, s64, s70\n\t" \
  "s_mov_b32 s74, 0\n\t" \
  "s_movk_i32 s60, 8192\n\t" \
  "RWT1_%=:\n\t" \
  "s_memrealtime s[72:73]\n\t" \
  "s_sub_u32 s60, s60, 1\n\t" \
  "s_cselect_b32 s61, 0, -1\n\t" \
  "s_waitcnt lgkmcnt(0)\n\t" \
  "s_sub_i32 s73, s72, s64\n\t" \
  "s_and_b32 s73, s73, s61\n\t" \
  "s_cmp_lt_i32 s73, " CH_FIN "\n\t" \
  "s_cbranch_scc1 RWT1_%=\n\t" \
  "s_cmp_lt_i32 s73, 0\n\t" \
  "s_cbranch_scc1 RWT1_%=f\n\t" \
  "RWT1_%=g:\n\t" \
  "RRETRY1_%=:\n\t" \
  "buffer_load_dwordx4 v[0:3], v69, s[28:31], s97 offen offset:0 sc1\n\t" \
  "buffer_load_dwordx4 v[4:7], v69, s[28:31], s97 offen offset:1024 sc1\n\t" \
  "buffer_load_dwordx4 v[8:11], v69, s[28:31], s97 offen offset:2048 sc1\n\t" \
  "buffer_load_dwordx4 v[12:15], v69, s[28:31], s97 offen offset:3072 sc1\n\t" \
  "v_lshrrev_b32 v20, 2, v69\n\t" \
  "v_and_b32 v20, 16, v20\n\t" \
  "v_mov_b32 v21, 0x3f80\n\t" \
  "v_lshlrev_b32 v21, v20, v21\n\t" \
  "v_bfe_u32 v20, v69, 4, 2\n\t" \
  "v_cmp_eq_u32 vcc, 0, v20\n\t" \
  "s_nop 1\n\t" \
  "v_cndmask_b32 v32, 0, v21, vcc\n\t" \
  "v_cmp_eq_u32 vcc, 1, v20\n\t" \
  "s_nop 1\n\t" \
  "v_cndmask_b32 v33, 0, v21, vcc\n\t" \
  "v_cmp_eq_u32 vcc, 2, v20\n\t" \
  "s_nop 1\n\t" \
  "v_cndmask_b32 v34, 0, v21, vcc\n\t" \
  "v_cmp_eq_u32 vcc, 3, v20\n\t" \
  "s_nop 1\n\t" \
  "v_cndmask_b32 v35, 0, v21, vcc\n\t" \
  "v_bfe_u32 v38, v69, 7, 1\n\t" \
  "v_bfe_u32 v20, v69, 9, 1\n\t" \
  "v_lshl_add_u32 v38, v20, 2, v38\n\t" \
  "v_lshlrev_b32 v38, 12, v38\n\t" \
  "v_and_b32 v20, 0x60, v69\n\t" \
  "v_add_u32 v38, v38, v20\n\t" \
  "v_bfe_u32 v20, v69, 8, 1\n\t" \
  "v_lshl_add_u32 v38, v20, 4, v38\n\t" \
  "v_bfe_u32 v20, v69, 4, 1\n\t" \
  "v_lshl_add_u32 v38, v20, 3, v38\n\t" \
  "v_mov_b32 v44, 0\n\t" \
  "s_waitcnt vmcnt(3)\n\t" \
  "v_mfma_f32_16x16x32_bf16 v[40:43], v[32:35], v[0:3], 0\n\t" \
  "v_max3_u32 v44, v44, v0, v1\n\t" \
  "v_max3_u32 v44, v44, v2, v3\n\t" \
  "s_waitcnt vmcnt(2)\n\t" \
  "v_mfma_f32_16x16x32_bf16 v[40:43], v[32:35], v[4:7], v[40:43]\n\t" \
  "v_max3_u32 v44, v44, v4, v5\n\t" \
  "v_max3_u32 v44, v44, v6, v7\n\t" \
  "s_waitcnt vmcnt(1)\n\t" \
  "v_mfma_f32_16x16x32_bf16 v[40:43], v[32:35], v[8:11], v[40:43]\n\t" \
  "v_max3_u32 v44, v44, v8, v9\n\t" \
  "v_max3_u32 v44, v44, v10, v11\n\t" \
  "s_waitcnt vmcnt(0)\n\t" \
  "v_mfma_f32_16x16x32_bf16 v[40:43], v[32:35], v[12:15], v[40:43]\n\t" \
  "v_max3_u32 v44, v44, v12, v13\n\t" \
  "v_max3_u32 v44, v44, v14, v15\n\t" \
  "v_cmp_eq_u32 vcc, -1, v44\n\t" \
  "s_cbranch_vccnz RFAIL1_%=\n\t" \
  "s_nop 3\n\t" \
  "v_add_f32_dpp v40, v40, v40 row_ror:8 row_mask:0xf bank_mask:0xf\n\t" \
  "v_add_f32_dpp v41, v41, v41 row_ror:8 row_mask:0xf bank_mask:0xf\n\t" \
  "v_add_f32_dpp v42, v42, v42 row_ror:8 row_mask:0xf bank_mask:0xf\n\t" \
  "v_add_f32_dpp v43, v43, v43 row_ror:8 row_mask:0xf bank_mask:0xf\n\t" \
  "v_cvt_pk_bf16_f32 v40, v40, v41\n\t" \
  "v_cvt_pk_bf16_f32 v41, v42, v43\n\t" \
  "buffer_store_dwordx2 v[40:41], v38, s[28:31], s98 offen sc1\n\t" \
  "RDONE1_%=:\n\t" \
  "s_barrier\n\t" \
  "s_add_u32 s48, s48, 1\n\t" \
  "s_mov_b32 s49, s53\n\t" \
  "s_add_u32 s53, s53, " CH_SLOT "\n\t" \
  "s_add_u32 s60, s53, " CH_SKIP "\n\t" \
  "s_lshr_b32 s61, s96, 1\n\t" \
  "s_add_u32 s86, s86, 1\n\t" \
  "s_cmp_eq_u32 s86, 3\n\t" \
  "s_cselect_b32 s53, s60, s53\n\t" \
  "s_cselect_b32 s96, s61, s96\n\t" \
  "s_cselect_b32 s86, 0, s86\n\t" \
  "s_cmp_ge_u32 s53, " CH_RING "\n\t" \
  "s_cselect_b32 s53, 0, s53\n\t" \
  "s_cselect_b32 s96, s21, s96\n\t" \
  "s_andn2_b32 s53, s53, 0x2000\n\t" \
  "s_and_b32 s60, s96, 1\n\t" \
  "s_lshl_b32 s60, s60, 13\n\t" \
  "s_or_b32 s53, s53, s60\n\t" \
  "s_mov_b32 s68, 0\n\t" \
  "s_add_u32 s97, s41, s53\n\t" \
  "s_add_u32 s98, s37, s53\n\t" \
  "s_add_u32 s82, s48, 0\n\t" \
  "s_and_b32 s82, s82, " CH_TRM "\n\t" \
  "s_lshr_b64 s[60:61], s[90:91], s82\n\t" \
  "s_and_b32 s60, s60, 1\n\t" \
  "s_lshl_b32 s60, s60, 13\n\t" \
  "s_add_u32 s82, s82, " CH_SG0 "\n\t" \
  "s_lshl_b32 s82, s82, 21\n\t" \
  "s_or_b32 s82, s82, s60\n\t" \
  "s_add_u32 s50, s82, s38\n\t" \
  "s_add_u32 s69, s82, s39\n\t" \
  "s_cmp_eq_u32 s34, 0\n\t" \
  "s_cbranch_scc1 LINI_%=\n\t" \
  "s_branch LG1_%=\n\t" \
  "RFAIL1_%=:\n\t" \
  "s_add_u32 s74, s74, 1\n\t" \
  "s_sleep " CH_RSL "\n\t" \
  "s_waitcnt vmcnt(0)\n\t" \
  "s_cmp_gt_u32 s74, s84\n\t" \
  "s_cbranch_scc1 RG1_%=\n\t" \
  "s_and_b32 s60, s74, 1023\n\t" \
  "s_cmp_lg_u32 s60, 0\n\t" \
  "s_cbranch_scc1 RRETRY1_%=\n\t" \
  "buffer_load_dword v46, off, s[28:31], s92 sc1\n\t" \
  "s_waitcnt vmcnt(0)\n\t" \
  "v_readfirstlane_b32 s60, v46\n\t" \
  "s_nop 0\n\t" \
  "s_cmp_eq_u32 s60, 0\n\t" \
  "s_cbranch_scc1 RRETRY1_%=\n\t" \
  "RG1_%=:\n\t" \
  "s_mov_b32 s84, 0\n\t" \
  "s_lshl_b32 s60, s36, 4\n\t" \
  "s_or_b32 s60, s60, s48\n\t" \
  "s_or_b32 s60, s60, 536870912\n\t" \
  "v_mov_b32 v76, s60\n\t" \
  "v_mov_b32 v77, 0\n\t" \
  "s_mov_b64 exec, 1\n\t" \
  "buffer_atomic_cmpswap v[76:77], off, s[28:31], s92 sc1\n\t" \
  "s_mov_b64 exec, -1\n\t" \
  "s_branch RDONE1_%=\n\t" \
  "LFAIL1_%=:\n\t" \
  "v_max_u32 v75, v0, v1\n\t" \
  "v_max3_u32 v75, v75, v2, v3\n\t" \
  "v_max3_u32 v75, v75, v4, v5\n\t" \
  "v_max3_u32 v75, v75, v6, v7\n\t" \
  "v_max3_u32 v75, v75, v8, v9\n\t" \
  "v_max3_u32 v75, v75, v10, v11\n\t" \
  "v_max3_u32 v75, v75, v12, v13\n\t" \
  "v_max3_u32 v75, v75, v14, v15\n\t" \
  "v_max3_u32 v75, v75, v16, v17\n\t" \
  "v_max3_u32 v75, v75, v18, v19\n\t" \
  "v_max3_u32 v75, v75, v20, v21\n\t" \
  "v_max3_u32 v75, v75, v22, v23\n\t" \
  "v_max3_u32 v75, v75, v24, v25\n\t" \
  "v_max3_u32 v75, v75, v26, v27\n\t" \
  "v_max3_u32 v75, v75, v28, v29\n\t" \
  "v_max3_u32 v75, v75, v30, v31\n\t" \
  "v_max3_u32 v75, v75, v32, v33\n\t" \
  "v_max3_u32 v75, v75, v34, v35\n\t" \
  "v_cmp_eq_u32 vcc, -1, v75\n\t" \
  "s_cbranch_vccz LGO1_%=\n\t" \
  "s_add_u32 s68, s68, 1\n\t" \
  "s_waitcnt lgkmcnt(0)\n\t" \
  "s_cmp_gt_u32 s68, s83\n\t" \
  "s_cbranch_scc1 LGV1_%=\n\t" \
  "s_barrier\n\t" \
  "s_waitcnt vmcnt(8)\n\t" \
  "s_branch LG1_%=\n\t" \
  "RWT1_%=f:\n\t" \
  "s_cmp_lt_i32 s73, -2\n\t" \
  "s_cbranch_scc0 RWT1_%=g\n\t" \
  "s_nop 4\n\t" \
  "s_branch RWT1_%=g\n\t" \
  "LGV1_%=:\n\t" \
  "s_mov_b32 s83, 0\n\t" \
  "s_lshl_b32 s60, s36, 4\n\t" \
  "s_or_b32 s60, s60, s48\n\t" \
  "s_or_b32 s60, s60, 805306368\n\t" \
  "v_mov_b32 v76, s60\n\t" \
  "v_mov_b32 v77, 0\n\t" \
  "s_mov_b64 exec, 1\n\t" \
  "buffer_atomic_cmpswap v[76:77], off, s[28:31], s92 sc1\n\t" \
  "s_mov_b64 exec, -1\n\t" \
  "s_branch LGO1_%=\n\t" \
  "LLAST1_%=:\n\t" \
  "s_waitcnt vmcnt(0) lgkmcnt(0)\n\t" \
  "s_barrier\n\t" \
  "s_branch LEND_%=\n\t" \
  ".p2align 6\n\t" \
  "LG2_%=:\n\t" \
  "s_barrier\n\t" \
  "buffer_load_dwordx4 v[52:55], v69, s[28:31], s50 offen sc1\n\t" \
  "buffer_load_dwordx4 v[56:59], v69, s[28:31], s50 offen offset:1024 sc1\n\t" \
  "s_add_u32 s64, s50, 18432\n\t" \
  "s_movk_i32 m0, 11264\n\t" \
  "v_lshlrev_b32 v75, 1, v69\n\t" \
  "buffer_load_dword v75, s[28:31], s64 offen sc1 lds\n\t" \
  "ds_read_b128 v[44:47], v70 offset:0\n\t" \
  "ds_read_b128 v[48:51], v70 offset:1024\n\t" \
  "ds_read_b128 v[60:63], v70 offset:2048\n\t" \
  "ds_read_b128 v[64:67], v70 offset:3072\n\t" \
  "v_mov_b64 v[36:37], 0\n\t" \
  "v_mov_b64 v[38:39], 0\n\t" \
  "v_mov_b64 v[40:41], 0\n\t" \
  "v_mov_b64 v[42:43], 0\n\t" \
  "v_lshrrev_b32 v71, 1, v69\n\t" \
  "v_lshrrev_b32 v72, 3, v69\n\t" \
  "v_and_b32 v72, 0x78, v72\n\t" \
  "v_and_b32 v74, 0x30, v69\n\t" \
  "v_lshl_add_u32 v72, v74, 3, v72\n\t" \
  "s_add_u32 s54, s53, s36\n\t" \
  "s_add_u32 s55, s54, 557056\n\t" \
  "s_add_u32 s54, s54, 131072\n\t" \
  "s_add_u32 s75, s54, 262144\n\t" \
  "s_add_u32 s80, s53, s26\n\t" \
  "s_barrier\n\t" \
  "ds_read_b128 v[0:3], v69 offset:0\n\t" \
  "ds_read_b128 v[4:7], v69 offset:1024\n\t" \
  "ds_read_b128 v[8:11], v69 offset:2048\n\t" \
  "ds_read_b128 v[12:15], v69 offset:3072\n\t" \
  "s_waitcnt lgkmcnt(3)\n\t" \
  "v_smfmac_f32_16x16x64_bf16 v[36:39], v[0:3], v[44:51], v68\n\t" \
  "ds_read_b128 v[44:47], v70 offset:4096\n\t" \
  "v_smfmac_f32_16x16x64_bf16 v[40:43], v[0:3], v[60:67], v68\n\t" \
  "ds_read_b128 v[48:51], v70 offset:5120\n\t" \
  "s_waitcnt lgkmcnt(4)\n\t" \
  "v_smfmac_f32_16x16x64_bf16 v[36:39], v[4:7], a[32:39], v68\n\t" \
  "ds_read_b128 v[60:63], v70 offset:6144\n\t" \
  "v_smfmac_f32_16x16x64_bf16 v[40:43], v[4:7], a[40:47], v68\n\t" \
  "ds_read_b128 v[64:67], v70 offset:7168\n\t" \
  "s_waitcnt lgkmcnt(5)\n\t" \
  "v_smfmac_f32_16x16x64_bf16 v[36:39], v[8:11], a[80:87], v68\n\t" \
  "s_barrier\n\t" \
  "ds_read_b128 v[16:19], v69 offset:4096\n\t" \
  "v_smfmac_f32_16x16x64_bf16 v[40:43], v[8:11], a[88:95], v68\n\t" \
  "ds_read_b128 v[20:23], v69 offset:5120\n\t" \
  "s_waitcnt lgkmcnt(6)\n\t" \
  "v_smfmac_f32_16x16x64_bf16 v[36:39], v[12:15], a[120:127], v68\n\t" \
  "ds_read_b128 v[24:27], v69 offset:6144\n\t" \
  "s_waitcnt lgkmcnt(5)\n\t" \
  "v_smfmac_f32_16x16x64_bf16 v[40:43], v[12:15], v[44:51], v68\n\t" \
  "ds_read_b128 v[28:31], v69 offset:7168\n\t" \
  "s_waitcnt lgkmcnt(3)\n\t" \
  "v_smfmac_f32_16x16x64_bf16 v[36:39], v[16:19], a[160:167], v68\n\t" \
  "ds_read_b128 v[32:35], v69 offset:8192\n\t" \
  "v_smfmac_f32_16x16x64_bf16 v[40:43], v[16:19], a[168:175], v68\n\t" \
  "s_waitcnt lgkmcnt(3)\n\t" \
  "v_mov_b64 v[44:45], 0\n\t" \
  "v_smfmac_f32_16x16x64_bf16 v[36:39], v[20:23], a[200:207], v68\n\t" \
  "s_waitcnt lgkmcnt(2)\n\t" \
  "v_mov_b64 v[46:47], 0\n\t" \
  "v_smfmac_f32_16x16x64_bf16 v[36:39], v[24:27], a[240:247], v68\n\t" \
  "s_waitcnt lgkmcnt(1)\n\t" \
  "v_mov_b64 v[48:49], 0\n\t" \
  "v_smfmac_f32_16x16x64_bf16 v[36:39], v[28:31], v[112:119], v68\n\t" \
  "s_waitcnt lgkmcnt(0)\n\t" \
  "v_mov_b64 v[50:51], 0\n\t" \
  "v_smfmac_f32_16x16x64_bf16 v[36:39], v[32:35], v[160:167], v68\n\t" \
  "v_smfmac_f32_16x16x64_bf16 v[40:43], v[20:23], v[60:67], v68\n\t" \
  "ds_read_b128 v[60:63], v70 offset:8192\n\t" \
  "v_smfmac_f32_16x16x64_bf16 v[40:43], v[24:27], a[248:255], v68\n\t" \
  "ds_read_b128 v[64:67], v70 offset:9216\n\t" \
  "v_add_f32 v74, v36, v38\n\t" \
  "v_smfmac_f32_16x16x64_bf16 v[40:43], v[28:31], v[120:127], v68\n\t" \
  "v_add_f32 v37, v37, v39\n\t" \
  "v_smfmac_f32_16x16x64_bf16 v[40:43], v[32:35], v[168:175], v68\n\t" \
  "v_cmp_u_f32 vcc, v74, v37\n\t" \
  "v_smfmac_f32_16x16x64_bf16 v[44:47], v[0:3], a[0:7], v68\n\t" \
  "s_cbranch_vccnz LFAIL2_%=\n\t" \
  "LGO2_%=:\n\t" \
  "v_cvt_pk_bf16_f32 v74, v74, v37\n\t" \
  "v_smfmac_f32_16x16x64_bf16 v[44:47], v[4:7], a[48:55], v68\n\t" \
  "s_waitcnt lgkmcnt(0)\n\t" \
  "v_add_f32 v75, v40, v42\n\t" \
  "v_smfmac_f32_16x16x64_bf16 v[44:47], v[8:11], v[60:67], v68\n\t" \
  "ds_read_b128 v[60:63], v70 offset:10240\n\t" \
  "v_add_f32 v41, v41, v43\n\t" \
  "v_cvt_pk_bf16_f32 v75, v75, v41\n\t" \
  "v_smfmac_f32_16x16x64_bf16 v[44:47], v[12:15], a[128:135], v68\n\t" \
  "ds_write2st64_b32 v71, v74, v75 offset0:40 offset1:42\n\t" \
  "v_smfmac_f32_16x16x64_bf16 v[44:47], v[16:19], a[176:183], v68\n\t" \
  "ds_read_b128 v[64:67], v70 offset:11264\n\t" \
  "v_smfmac_f32_16x16x64_bf16 v[44:47], v[20:23], a[208:215], v68\n\t" \
  "v_smfmac_f32_16x16x64_bf16 v[44:47], v[24:27], v[80:87], v68\n\t" \
  "v_smfmac_f32_16x16x64_bf16 v[44:47], v[28:31], v[128:135], v68\n\t" \
  "s_waitcnt lgkmcnt(0)\n\t" \
  "v_smfmac_f32_16x16x64_bf16 v[44:47], v[32:35], v[60:67], v68\n\t" \
  "s_barrier\n\t" \
  "ds_read_b64_tr_b16 v[76:77], v72 offset:10240\n\t" \
  "v_smfmac_f32_16x16x64_bf16 v[48:51], v[0:3], a[8:15], v68\n\t" \
  "ds_read_b64_tr_b16 v[78:79], v72 offset:10752\n\t" \
  "v_smfmac_f32_16x16x64_bf16 v[48:51], v[4:7], a[56:63], v68\n\t" \
  "buffer_load_dwordx4 v[60:63], v69, s[28:31], s64 offen sc1\n\t" \
  "v_add_f32 v74, v44, v46\n\t" \
  "v_smfmac_f32_16x16x64_bf16 v[48:51], v[8:11], a[96:103], v68\n\t" \
  "v_add_f32 v45, v45, v47\n\t" \
  "v_smfmac_f32_16x16x64_bf16 v[48:51], v[12:15], a[136:143], v68\n\t" \
  "buffer_load_dwordx4 v[64:67], v69, s[28:31], s64 offen offset:1024 sc1\n\t" \
  "s_waitcnt vmcnt(4)\n\t" \
  "s_waitcnt lgkmcnt(0)\n\t" \
  "v_cvt_pk_bf16_f32 v74, v74, v45\n\t" \
  "v_mfma_f32_16x16x32_bf16 v[36:39], v[52:55], v[76:79], 0\n\t" \
  "s_waitcnt vmcnt(3)\n\t" \
  "v_mfma_f32_16x16x32_bf16 v[40:43], v[56:59], v[76:79], 0\n\t" \
  "v_mov_b64 v[52:53], 0\n\t" \
  "v_mov_b64 v[54:55], 0\n\t" \
  "v_smfmac_f32_16x16x64_bf16 v[48:51], v[16:19], a[184:191], v68\n\t" \
  "v_mov_b64 v[56:57], 0\n\t" \
  "v_mov_b64 v[58:59], 0\n\t" \
  "v_smfmac_f32_16x16x64_bf16 v[48:51], v[20:23], a[216:223], v68\n\t" \
  "ds_write_b32 v71, v74 offset:1024\n\t" \
  "v_add_f32_dpp v36, v40, v36 quad_perm:[2,3,0,1] row_mask:0xf bank_mask:0xf\n\t" \
  "v_smfmac_f32_16x16x64_bf16 v[48:51], v[24:27], v[88:95], v68\n\t" \
  "v_add_f32_dpp v37, v41, v37 quad_perm:[2,3,0,1] row_mask:0xf bank_mask:0xf\n\t" \
  "v_smfmac_f32_16x16x64_bf16 v[48:51], v[28:31], v[136:143], v68\n\t" \
  "v_add_f32_dpp v38, v42, v38 quad_perm:[2,3,0,1] row_mask:0xf bank_mask:0xf\n\t" \
  "v_smfmac_f32_16x16x64_bf16 v[48:51], v[32:35], v[176:183], v68\n\t" \
  "v_add_f32_dpp v39, v43, v39 quad_perm:[2,3,0,1] row_mask:0xf bank_mask:0xf\n\t" \
  "v_smfmac_f32_16x16x64_bf16 v[52:55], v[0:3], a[16:23], v68\n\t" \
  "s_mov_b64 exec, s[58:59]\n\t" \
  "ds_write_b128 v69, v[36:39] offset:3072\n\t" \
  "s_mov_b64 exec, -1\n\t" \
  "v_smfmac_f32_16x16x64_bf16 v[52:55], v[4:7], a[64:71], v68\n\t" \
  "v_add_f32 v75, v48, v50\n\t" \
  "v_add_f32 v49, v49, v51\n\t" \
  "v_cvt_pk_bf16_f32 v75, v75, v49\n\t" \
  "v_smfmac_f32_16x16x64_bf16 v[56:59], v[0:3], a[24:31], v68\n\t" \
  "ds_write_b32 v71, v75 offset:1536\n\t" \
  "v_smfmac_f32_16x16x64_bf16 v[56:59], v[4:7], a[72:79], v68\n\t" \
  "ds_read_b64_tr_b16 v[0:1], v72 offset:9216\n\t" \
  "ds_read_b64_tr_b16 v[2:3], v72 offset:9728\n\t" \
  "v_smfmac_f32_16x16x64_bf16 v[52:55], v[8:11], a[104:111], v68\n\t" \
  "ds_read_b128 v[36:39], v70 offset:12288\n\t" \
  "v_smfmac_f32_16x16x64_bf16 v[56:59], v[8:11], a[112:119], v68\n\t" \
  "ds_read_b128 v[40:43], v70 offset:13312\n\t" \
  "v_smfmac_f32_16x16x64_bf16 v[52:55], v[12:15], a[144:151], v68\n\t" \
  "s_waitcnt lgkmcnt(4)\n\t" \
  "s_barrier\n\t" \
  "ds_read_b64_tr_b16 v[44:45], v72 offset:1024\n\t" \
  "ds_read_b64_tr_b16 v[46:47], v72 offset:1536\n\t" \
  "v_smfmac_f32_16x16x64_bf16 v[56:59], v[12:15], a[152:159], v68\n\t" \
  "ds_read_b64_tr_b16 v[48:49], v72 offset:0\n\t" \
  "ds_read_b64_tr_b16 v[50:51], v72 offset:512\n\t" \
  "v_smfmac_f32_16x16x64_bf16 v[52:55], v[16:19], a[192:199], v68\n\t" \
  "ds_read_b128 v[4:7], v69 offset:2048\n\t" \
  "s_waitcnt lgkmcnt(5)\n\t" \
  "v_smfmac_f32_16x16x64_bf16 v[56:59], v[16:19], v[36:43], v68\n\t" \
  "ds_read_b128 v[8:11], v69 offset:3072\n\t" \
  "s_waitcnt lgkmcnt(4)\n\t" \
  "v_mfma_f32_16x16x32_bf16 v[12:15], v[44:47], v[76:79], 0\n\t" \
  "ds_read_b128 v[36:39], v70 offset:14336\n\t" \
  "s_waitcnt lgkmcnt(3)\n\t" \
  "v_mfma_f32_16x16x32_bf16 v[12:15], v[48:51], v[0:3], v[12:15]\n\t" \
  "ds_read_b128 v[40:43], v70 offset:15360\n\t" \
  "v_smfmac_f32_16x16x64_bf16 v[52:55], v[20:23], a[224:231], v68\n\t" \
  "buffer_store_dwordx4 v[44:47], v69, s[28:31], s69 offen offset:1024 sc1\n\t" \
  "s_waitcnt lgkmcnt(2)\n\t" \
  "v_add_f32 v4, v4, v8\n\t" \
  "v_add_f32 v5, v5, v9\n\t" \
  "v_add_f32 v6, v6, v10\n\t" \
  "v_cndmask_b32 v78, v13, v14, s[56:57]\n\t" \
  "v_cndmask_b32 v76, v12, v15, s[56:57]\n\t" \
  "v_add_f32 v7, v7, v11\n\t" \
  "v_max3_f32 v77, v4, v5, v6\n\t" \
  "v_add_f32_dpp v76, v78, v76 quad_perm:[2,3,0,1] row_mask:0xf bank_mask:0xf\n\t" \
  "v_max3_f32 v77, v77, v7, v76\n\t" \
  "v_smfmac_f32_16x16x64_bf16 v[56:59], v[20:23], a[232:239], v68\n\t" \
  "ds_read_b128 v[44:47], v70 offset:16384\n\t" \
  "v_max_f32_dpp v77, v77, v77 quad_perm:[2,3,0,1] row_mask:0xf bank_mask:0xf\n\t" \
  "v_mov_b32 v78, v77\n\t" \
  "v_smfmac_f32_16x16x64_bf16 v[52:55], v[24:27], v[96:103], v68\n\t" \
  "ds_read_b128 v[48:51], v70 offset:17408\n\t" \
  "v_permlane16_swap_b32 v77, v78\n\t" \
  "v_max_f32 v77, v77, v78\n\t" \
  "v_mov_b32 v78, v77\n\t" \
  "v_smfmac_f32_16x16x64_bf16 v[52:55], v[28:31], v[144:151], v68\n\t" \
  "ds_read_b128 v[16:19], v70 offset:18432\n\t" \
  "v_permlane32_swap_b32 v77, v78\n\t" \
  "v_max_f32 v77, v77, v78\n\t" \
  "v_smfmac_f32_16x16x64_bf16 v[52:55], v[32:35], v[184:191], v68\n\t" \
  "ds_read_b128 v[20:23], v70 offset:19456\n\t" \
  "v_mul_f32 v77, 0xbe0293ee, v77\n\t" \
  "v_smfmac_f32_16x16x64_bf16 v[56:59], v[24:27], v[104:111], v68\n\t" \
  "v_fmamk_f32 v4, v4, 0x3e0293ee, v77\n\t" \
  "v_smfmac_f32_16x16x64_bf16 v[56:59], v[28:31], v[152:159], v68\n\t" \
  "ds_read_b128 v[24:27], v70 offset:22528\n\t" \
  "s_waitcnt lgkmcnt(4)\n\t" \
  "v_fmamk_f32 v5, v5, 0x3e0293ee, v77\n\t" \
  "v_smfmac_f32_16x16x64_bf16 v[56:59], v[32:35], v[36:43], v68\n\t" \
  "ds_read_b128 v[28:31], v70 offset:23552\n\t" \
  "v_fmamk_f32 v6, v6, 0x3e0293ee, v77\n\t" \
  "v_fmamk_f32 v7, v7, 0x3e0293ee, v77\n\t" \
  "v_fmamk_f32 v76, v76, 0x3e0293ee, v77\n\t" \
  "v_exp_f32 v4, v4\n\t" \
  "v_exp_f32 v5, v5\n\t" \
  "v_exp_f32 v6, v6\n\t" \
  "v_exp_f32 v7, v7\n\t" \
  "v_cvt_pk_bf16_f32 v4, v4, v5\n\t" \
  "v_exp_f32 v76, v76\n\t" \
  "v_cvt_pk_bf16_f32 v5, v6, v7\n\t" \
  "v_mov_b32_dpp v12, v4 quad_perm:[0,1,0,1] row_mask:0xf bank_mask:0xf\n\t" \
  "v_mov_b32_dpp v14, v4 quad_perm:[2,3,2,3] row_mask:0xf bank_mask:0xf\n\t" \
  "v_mov_b32_dpp v13, v5 quad_perm:[0,1,0,1] row_mask:0xf bank_mask:0xf\n\t" \
  "v_mov_b32_dpp v15, v5 quad_perm:[2,3,2,3] row_mask:0xf bank_mask:0xf\n\t" \
  "v_add_f32 v52, v52, v54\n\t" \
  "v_add_f32 v53, v53, v55\n\t" \
  "s_waitcnt vmcnt(2)\n\t" \
  "v_mfma_f32_16x16x32_bf16 v[0:3], v[60:63], v[12:15], 0\n\t" \
  "ds_read_b128 v[36:39], v70 offset:20480\n\t" \
  "v_add_f32 v56, v56, v58\n\t" \
  "v_add_f32 v57, v57, v59\n\t" \
  "v_cvt_pk_bf16_f32 v52, v52, v53\n\t" \
  "v_cvt_pk_bf16_f32 v53, v56, v57\n\t" \
  "s_waitcnt vmcnt(1)\n\t" \
  "v_mfma_f32_16x16x32_bf16 v[4:7], v[64:67], v[12:15], 0\n\t" \
  "ds_read_b128 v[40:43], v70 offset:21504\n\t" \
  "v_mov_b64 v[56:57], s[88:89]\n\t" \
  "v_mov_b64 v[58:59], s[88:89]\n\t" \
  "v_mov_b32_dpp v78, v76 quad_perm:[0,1,0,1] row_mask:0xf bank_mask:0xf\n\t" \
  "v_mov_b32_dpp v79, v76 quad_perm:[2,3,2,3] row_mask:0xf bank_mask:0xf\n\t" \
  "v_mfma_f32_16x16x32_bf16 v[32:35], v[56:59], v[12:15], 0\n\t" \
  "ds_read_b128 v[60:63], v70 offset:24576\n\t" \
  "v_cvt_pk_bf16_f32 v11, v78, v79\n\t" \
  "v_cvt_pk_bf16_f32 v8, v78, v79\n\t" \
  "v_mov_b64 v[54:55], 0\n\t" \
  "v_mov_b32 v9, 0\n\t" \
  "v_mov_b32 v10, 0\n\t" \
  "v_mov_b64 v[14:15], 0\n\t" \
  "s_nop 0\n\t" \
  "v_mfma_f32_16x16x32_bf16 v[32:35], v[54:57], v[8:11], v[32:35]\n\t" \
  "buffer_store_dwordx2 v[52:53], v71, s[28:31], s69 offen offset:3072 sc1\n\t" \
  "s_cmp_eq_u32 s48, 63\n\t" \
  "v_mfma_f32_16x16x32_bf16 v[0:3], v[52:55], v[8:11], v[0:3]\n\t" \
  "ds_read_b128 v[64:67], v70 offset:25600\n\t" \
  "v_mov_b64 v[56:57], 0\n\t" \
  "v_mov_b64 v[58:59], 0\n\t" \
  "v_mfma_f32_16x16x32_bf16 v[4:7], v[52:55], v[10:13], v[4:7]\n\t" \
  "s_cbranch_scc1 LLAST2_%=\n\t" \
  "v_rcp_f32 v74, v32\n\t" \
  "v_mov_b64 v[12:13], 0\n\t" \
  "v_mov_b32 v8, 0\n\t" \
  "v_pk_mul_f32 v[0:1], v[0:1], v[74:75] op_sel_hi:[1,0]\n\t" \
  "v_pk_mul_f32 v[2:3], v[2:3], v[74:75] op_sel_hi:[1,0]\n\t" \
  "v_cvt_pk_bf16_f32 v76, v0, v1\n\t" \
  "v_cvt_pk_bf16_f32 v77, v2, v3\n\t" \
  "v_pk_mul_f32 v[4:5], v[4:5], v[74:75] op_sel_hi:[1,0]\n\t" \
  "v_pk_mul_f32 v[6:7], v[6:7], v[74:75] op_sel_hi:[1,0]\n\t" \
  "v_cvt_pk_bf16_f32 v78, v4, v5\n\t" \
  "v_cvt_pk_bf16_f32 v79, v6, v7\n\t" \
  "ds_write_b128 v69, v[76:79] offset:6144\n\t" \
  "s_waitcnt lgkmcnt(2)\n\t" \
  "v_smfmac_f32_16x16x64_bf16 v[12:15], v[76:79], v[44:51], v68\n\t" \
  "ds_read_b128 v[44:47], v70 offset:26624\n\t" \
  "v_mov_b32 v11, 0\n\t" \
  "v_smfmac_f32_16x16x64_bf16 v[56:59], v[76:79], v[16:23], v68\n\t" \
  "ds_read_b128 v[48:51], v70 offset:27648\n\t" \
  "v_mov_b64 v[52:53], 0\n\t" \
  "v_smfmac_f32_16x16x64_bf16 v[8:11], v[76:79], v[36:43], v68\n\t" \
  "ds_read_b128 v[16:19], v70 offset:28672\n\t" \
  "v_smfmac_f32_16x16x64_bf16 v[52:55], v[76:79], v[24:31], v68\n\t" \
  "s_waitcnt lgkmcnt(3)\n\t" \
  "s_barrier\n\t" \
  "ds_read_b128 v[0:3], v69 offset:7168\n\t" \
  "ds_read_b128 v[4:7], v69 offset:4096\n\t" \
  "v_lshrrev_b32 v71, 7, v69\n\t" \
  "v_and_b32 v74, 0x70, v69\n\t" \
  "v_lshl_add_u32 v71, v71, 14, v74\n\t" \
  "v_bfe_u32 v72, v69, 2, 7\n\t" \
  "v_and_b32 v74, 0x200, v69\n\t" \
  "s_waitcnt lgkmcnt(1)\n\t" \
  "v_smfmac_f32_16x16x64_bf16 v[8:11], v[0:3], v[60:67], v68\n\t" \
  "ds_read_b128 v[20:23], v70 offset:29696\n\t" \
  "v_mov_b64 v[76:77], 0\n\t" \
  "v_smfmac_f32_16x16x64_bf16 v[12:15], v[0:3], v[200:207], v68\n\t" \
  "buffer_load_dword v75, off, s[28:31], s80 sc1\n\t" \
  "ds_read_b128 v[32:35], v69 offset:5120\n\t" \
  "v_mov_b64 v[78:79], 0\n\t" \
  "v_smfmac_f32_16x16x64_bf16 v[52:55], v[0:3], v[44:51], v68\n\t" \
  "ds_read_b128 v[24:27], v70 offset:30720\n\t" \
  "v_lshl_add_u32 v72, v74, 3, v72\n\t" \
  "v_smfmac_f32_16x16x64_bf16 v[56:59], v[0:3], v[232:239], v68\n\t" \
  "ds_read_b128 v[28:31], v70 offset:31744\n\t" \
  "s_waitcnt lgkmcnt(4)\n\t" \
  "v_smfmac_f32_16x16x64_bf16 v[12:15], v[4:7], v[208:215], v68\n\t" \
  "ds_read_b128 v[60:63], v70 offset:32768\n\t" \
  "s_waitcnt lgkmcnt(4)\n\t" \
  "v_smfmac_f32_16x16x64_bf16 v[8:11], v[4:7], v[16:23], v68\n\t" \
  "ds_read_b128 v[64:67], v70 offset:33792\n\t" \
  "s_waitcnt lgkmcnt(4)\n\t" \
  "v_smfmac_f32_16x16x64_bf16 v[8:11], v[32:35], v[192:199], v68\n\t" \
  "ds_read_b128 v[36:39], v73 offset:4096\n\t" \
  "v_smfmac_f32_16x16x64_bf16 v[12:15], v[32:35], v[216:223], v68\n\t" \
  "ds_read_b128 v[40:43], v73 offset:6144\n\t" \
  "s_waitcnt lgkmcnt(4)\n\t" \
  "v_smfmac_f32_16x16x64_bf16 v[52:55], v[4:7], v[24:31], v68\n\t" \
  "ds_read_b128 v[44:47], v70 offset:34816\n\t" \
  "v_cvt_pk_bf16_f32 v8, v8, v9\n\t" \
  "v_smfmac_f32_16x16x64_bf16 v[52:55], v[32:35], v[224:231], v68\n\t" \
  "ds_read_b128 v[48:51], v70 offset:35840\n\t" \
  "v_cvt_pk_bf16_f32 v9, v10, v11\n\t" \
  "v_smfmac_f32_16x16x64_bf16 v[56:59], v[4:7], v[240:247], v68\n\t" \
  "s_waitcnt lgkmcnt(4)\n\t" \
  "v_cvt_pk_bf16_f32 v10, v12, v13\n\t" \
  "v_smfmac_f32_16x16x64_bf16 v[56:59], v[32:35], v[60:67], v68\n\t" \
  "s_waitcnt lgkmcnt(3)\n\t" \
  "v_cvt_pk_bf16_f32 v11, v14, v15\n\t" \
  "v_smfmac_f32_16x16x64_bf16 v[76:79], v[36:39], v[248:255], v68\n\t" \
  "buffer_store_dwordx4 v[8:11], v71, s[28:31], s54 offen sc1\n\t" \
  "s_waitcnt lgkmcnt(0)\n\t" \
  "v_cvt_pk_bf16_f32 v52, v52, v53\n\t" \
  "v_smfmac_f32_16x16x64_bf16 v[76:79], v[40:43], v[44:51], v68\n\t" \
  "v_cvt_pk_bf16_f32 v53, v54, v55\n\t" \
  "v_cvt_pk_bf16_f32 v54, v56, v57\n\t" \
  "v_cvt_pk_bf16_f32 v55, v58, v59\n\t" \
  "buffer_store_dwordx4 v[52:55], v71, s[28:31], s75 offen sc1\n\t" \
  "s_nop 2\n\t" \
  "v_add_f32 v76, v76, v78\n\t" \
  "v_add_f32 v77, v77, v79\n\t" \
  "v_cvt_pk_bf16_f32 v76, v76, v77\n\t" \
  "buffer_store_dword v76, v72, s[28:31], s55 offen sc1\n\t" \
  "s_memrealtime s[70:71]\n\t" \
  "s_waitcnt vmcnt(3)\n\t" \
  "v_readfirstlane_b32 s65, v75\n\t" \
  "s_barrier\n\t" \
  "s_waitcnt lgkmcnt(0)\n\t" \
  "s_not_b32 s65, s65\n\t" \
  "s_lshl_b32 s61, s70, 8\n\t" \
  "s_andn2_b32 s64, s65, 0xff\n\t" \
  "s_sub_u32 s64, s64, s61\n\t" \
  "s_ashr_i32 s64, s64, 8\n\t" \
  "s_cmp_eq_u32 s65, 0\n\t" \
  "s_cselect_b32 s64, " CH_DLF ", s64\n\t" \
  "s_min_i32 s64, s64, s34\n\t" \
  "s_max_i32 s64, s64, " CH_DLL1 "\n\t" \
  "s_add_u32 s64, s64, s70\n\t" \
  "s_mov_b32 s74, 0\n\t" \
  "s_movk_i32 s60, 8192\n\t" \
  "RWT2_%=:\n\t" \
  "s_memrealtime s[72:73]\n\t" \
  "s_sub_u32 s60, s60, 1\n\t" \
  "s_cselect_b32 s61, 0, -1\n\t" \
  "s_waitcnt lgkmcnt(0)\n\t" \
  "s_sub_i32 s73, s72, s64\n\t" \
  "s_and_b32 s73, s73, s61\n\t" \
  "s_cmp_lt_i32 s73, " CH_FIN "\n\t" \
  "s_cbranch_scc1 RWT2_%=\n\t" \
  "s_cmp_lt_i32 s73, 0\n\t" \
  "s_cbranch_scc1 RWT2_%=f\n\t" \
  "RWT2_%=g:\n\t" \
  "RRETRY2_%=:\n\t" \
  "buffer_load_dwordx4 v[0:3], v69, s[28:31], s97 offen offset:0 sc1\n\t" \
  "buffer_load_dwordx4 v[4:7], v69, s[28:31], s97 offen offset:1024 sc1\n\t" \
  "buffer_load_dwordx4 v[8:11], v69, s[28:31], s97 offen offset:2048 sc1\n\t" \
  "buffer_load_dwordx4 v[12:15], v69, s[28:31], s97 offen offset:3072 sc1\n\t" \
  "v_lshrrev_b32 v20, 2, v69\n\t" \
  "v_and_b32 v20, 16, v20\n\t" \
  "v_mov_b32 v21, 0x3f80\n\t" \
  "v_lshlrev_b32 v21, v20, v21\n\t" \
  "v_bfe_u32 v20, v69, 4, 2\n\t" \
  "v_cmp_eq_u32 vcc, 0, v20\n\t" \
  "s_nop 1\n\t" \
  "v_cndmask_b32 v32, 0, v21, vcc\n\t" \
  "v_cmp_eq_u32 vcc, 1, v20\n\t" \
  "s_nop 1\n\t" \
  "v_cndmask_b32 v33, 0, v21, vcc\n\t" \
  "v_cmp_eq_u32 vcc, 2, v20\n\t" \
  "s_nop 1\n\t" \
  "v_cndmask_b32 v34, 0, v21, vcc\n\t" \
  "v_cmp_eq_u32 vcc, 3, v20\n\t" \
  "s_nop 1\n\t" \
  "v_cndmask_b32 v35, 0, v21, vcc\n\t" \
  "v_bfe_u32 v38, v69, 7, 1\n\t" \
  "v_bfe_u32 v20, v69, 9, 1\n\t" \
  "v_lshl_add_u32 v38, v20, 2, v38\n\t" \
  "v_lshlrev_b32 v38, 12, v38\n\t" \
  "v_and_b32 v20, 0x60, v69\n\t" \
  "v_add_u32 v38, v38, v20\n\t" \
  "v_bfe_u32 v20, v69, 8, 1\n\t" \
  "v_lshl_add_u32 v38, v20, 4, v38\n\t" \
  "v_bfe_u32 v20, v69, 4, 1\n\t" \
  "v_lshl_add_u32 v38, v20, 3, v38\n\t" \
  "v_mov_b32 v44, 0\n\t" \
  "s_waitcnt vmcnt(3)\n\t" \
  "v_mfma_f32_16x16x32_bf16 v[40:43], v[32:35], v[0:3], 0\n\t" \
  "v_max3_u32 v44, v44, v0, v1\n\t" \
  "v_max3_u32 v44, v44, v2, v3\n\t" \
  "s_waitcnt vmcnt(2)\n\t" \
  "v_mfma_f32_16x16x32_bf16 v[40:43], v[32:35], v[4:7], v[40:43]\n\t" \
  "v_max3_u32 v44, v44, v4, v5\n\t" \
  "v_max3_u32 v44, v44, v6, v7\n\t" \
  "s_waitcnt vmcnt(1)\n\t" \
  "v_mfma_f32_16x16x32_bf16 v[40:43], v[32:35], v[8:11], v[40:43]\n\t" \
  "v_max3_u32 v44, v44, v8, v9\n\t" \
  "v_max3_u32 v44, v44, v10, v11\n\t" \
  "s_waitcnt vmcnt(0)\n\t" \
  "v_mfma_f32_16x16x32_bf16 v[40:43], v[32:35], v[12:15], v[40:43]\n\t" \
  "v_max3_u32 v44, v44, v12, v13\n\t" \
  "v_max3_u32 v44, v44, v14, v15\n\t" \
  "v_cmp_eq_u32 vcc, -1, v44\n\t" \
  "s_cbranch_vccnz RFAIL2_%=\n\t" \
  "s_nop 3\n\t" \
  "v_add_f32_dpp v40, v40, v40 row_ror:8 row_mask:0xf bank_mask:0xf\n\t" \
  "v_add_f32_dpp v41, v41, v41 row_ror:8 row_mask:0xf bank_mask:0xf\n\t" \
  "v_add_f32_dpp v42, v42, v42 row_ror:8 row_mask:0xf bank_mask:0xf\n\t" \
  "v_add_f32_dpp v43, v43, v43 row_ror:8 row_mask:0xf bank_mask:0xf\n\t" \
  "v_cvt_pk_bf16_f32 v40, v40, v41\n\t" \
  "v_cvt_pk_bf16_f32 v41, v42, v43\n\t" \
  "buffer_store_dwordx2 v[40:41], v38, s[28:31], s98 offen sc1\n\t" \
  "RDONE2_%=:\n\t" \
  "s_barrier\n\t" \
  "s_add_u32 s48, s48, 1\n\t" \
  "s_mov_b32 s49, s53\n\t" \
  "s_add_u32 s53, s53, " CH_SLOT "\n\t" \
  "s_add_u32 s60, s53, " CH_SKIP "\n\t" \
  "s_lshr_b32 s61, s96, 1\n\t" \
  "s_add_u32 s86, s86, 1\n\t" \
  "s_cmp_eq_u32 s86, 3\n\t" \
  "s_cselect_b32 s53, s60, s53\n\t" \
  "s_cselect_b32 s96, s61, s96\n\t" \
  "s_cselect_b32 s86, 0, s86\n\t" \
  "s_cmp_ge_u32 s53, " CH_RING "\n\t" \
  "s_cselect_b32 s53, 0, s53\n\t" \
  "s_cselect_b32 s96, s21, s96\n\t" \
  "s_andn2_b32 s53, s53, 0x2000\n\t" \
  "s_and_b32 s60, s96, 1\n\t" \
  "s_lshl_b32 s60, s60, 13\n\t" \
  "s_or_b32 s53, s53, s60\n\t" \
  "s_mov_b32 s68, 0\n\t" \
  "s_add_u32 s97, s41, s53\n\t" \
  "s_add_u32 s98, s37, s53\n\t" \
  "s_add_u32 s82, s48, 0\n\t" \
  "s_and_b32 s82, s82, " CH_TRM "\n\t" \
  "s_lshr_b64 s[60:61], s[90:91], s82\n\t" \
  "s_and_b32 s60, s60, 1\n\t" \
  "s_lshl_b32 s60, s60, 13\n\t" \
  "s_add_u32 s82, s82, " CH_SG0 "\n\t" \
  "s_lshl_b32 s82, s82, 21\n\t" \
  "s_or_b32 s82, s82, s60\n\t" \
  "s_add_u32 s50, s82, s38\n\t" \
  "s_add_u32 s69, s82, s39\n\t" \
  "s_cmp_eq_u32 s34, 0\n\t" \
  "s_cbranch_scc1 LINI_%=\n\t" \
  "s_branch LG2_%=\n\t" \
  "RFAIL2_%=:\n\t" \
  "s_add_u32 s74, s74, 1\n\t" \
  "s_sleep " CH_RSL "\n\t" \
  "s_waitcnt vmcnt(0)\n\t" \
  "s_cmp_gt_u32 s74, s84\n\t" \
  "s_cbranch_scc1 RG2_%=\n\t" \
  "s_and_b32 s60, s74, 1023\n\t" \
  "s_cmp_lg_u32 s60, 0\n\t" \
  "s_cbranch_scc1 RRETRY2_%=\n\t" \
  "buffer_load_dword v46, off, s[28:31], s92 sc1\n\t" \
  "s_waitcnt vmcnt(0)\n\t" \
  "v_readfirstlane_b32 s60, v46\n\t" \
  "s_nop 0\n\t" \
  "s_cmp_eq_u32 s60, 0\n\t" \
  "s_cbranch_scc1 RRETRY2_%=\n\t" \
  "RG2_%=:\n\t" \
  "s_mov_b32 s84, 0\n\t" \
  "s_lshl_b32 s60, s36, 4\n\t" \
  "s_or_b32 s60, s60, s48\n\t" \
  "s_or_b32 s60, s60, 536870912\n\t" \
  "v_mov_b32 v76, s60\n\t" \
  "v_mov_b32 v77, 0\n\t" \
  "s_mov_b64 exec, 1\n\t" \
  "buffer_atomic_cmpswap v[76:77], off, s[28:31], s92 sc1\n\t" \
  "s_mov_b64 exec, -1\n\t" \
  "s_branch RDONE2_%=\n\t" \
  "LFAIL2_%=:\n\t" \
  "v_max_u32 v75, v0, v1\n\t" \
  "v_max3_u32 v75, v75, v2, v3\n\t" \
  "v_max3_u32 v75, v75, v4, v5\n\t" \
  "v_max3_u32 v75, v75, v6, v7\n\t" \
  "v_max3_u32 v75, v75, v8, v9\n\t" \
  "v_max3_u32 v75, v75, v10, v11\n\t" \
  "v_max3_u32 v75, v75, v12, v13\n\t" \
  "v_max3_u32 v75, v75, v14, v15\n\t" \
  "v_max3_u32 v75, v75, v16, v17\n\t" \
  "v_max3_u32 v75, v75, v18, v19\n\t" \
  "v_max3_u32 v75, v75, v20, v21\n\t" \
  "v_max3_u32 v75, v75, v22, v23\n\t" \
  "v_max3_u32 v75, v75, v24, v25\n\t" \
  "v_max3_u32 v75, v75, v26, v27\n\t" \
  "v_max3_u32 v75, v75, v28, v29\n\t" \
  "v_max3_u32 v75, v75, v30, v31\n\t" \
  "v_max3_u32 v75, v75, v32, v33\n\t" \
  "v_max3_u32 v75, v75, v34, v35\n\t" \
  "v_cmp_eq_u32 vcc, -1, v75\n\t" \
  "s_cbranch_vccz LGO2_%=\n\t" \
  "s_add_u32 s68, s68, 1\n\t" \
  "s_waitcnt lgkmcnt(0)\n\t" \
  "s_cmp_gt_u32 s68, s83\n\t" \
  "s_cbranch_scc1 LGV2_%=\n\t" \
  "s_barrier\n\t" \
  "s_waitcnt vmcnt(8)\n\t" \
  "s_branch LG2_%=\n\t" \
  "RWT2_%=f:\n\t" \
  "s_cmp_lt_i32 s73, -2\n\t" \
  "s_cbranch_scc0 RWT2_%=g\n\t" \
  "s_nop 4\n\t" \
  "s_branch RWT2_%=g\n\t" \
  "LGV2_%=:\n\t" \
  "s_mov_b32 s83, 0\n\t" \
  "s_lshl_b32 s60, s36, 4\n\t" \
  "s_or_b32 s60, s60, s48\n\t" \
  "s_or_b32 s60, s60, 805306368\n\t" \
  "v_mov_b32 v76, s60\n\t" \
  "v_mov_b32 v77, 0\n\t" \
  "s_mov_b64 exec, 1\n\t" \
  "buffer_atomic_cmpswap v[76:77], off, s[28:31], s92 sc1\n\t" \
  "s_mov_b64 exec, -1\n\t" \
  "s_branch LGO2_%=\n\t" \
  "LLAST2_%=:\n\t" \
  "s_waitcnt vmcnt(0) lgkmcnt(0)\n\t" \
  "s_barrier\n\t" \
  "s_branch LEND_%=\n\t" \
  ".p2align 6\n\t" \
  "LG3_%=:\n\t" \
  "s_barrier\n\t" \
  "buffer_load_dwordx4 v[52:55], v69, s[28:31], s50 offen sc1\n\t" \
  "buffer_load_dwordx4 v[56:59], v69, s[28:31], s50 offen offset:1024 sc1\n\t" \
  "s_add_u32 s64, s50, 16384\n\t" \
  "s_movk_i32 m0, 11264\n\t" \
  "v_lshlrev_b32 v75, 1, v69\n\t" \
  "buffer_load_dword v75, s[28:31], s64 offen sc1 lds\n\t" \
  "s_add_u32 s47, s81, " CH_DL3 "\n\t" \
  "s_lshl_b32 s47, s47, 8\n\t" \
  "s_or_b32 s47, s47, " CH_B0 "\n\t" \
  "s_cmp_lt_u32 s48, " CH_ES "\n\t" \
  "s_cselect_b32 s60, " CH_EX ", 0\n\t" \
  "s_add_u32 s47, s47, s60\n\t" \
  "s_not_b32 s47, s47\n\t" \
  "s_add_u32 s71, s53, s46\n\t" \
  "ds_read_b128 v[44:47], v70 offset:0\n\t" \
  "ds_read_b128 v[48:51], v70 offset:1024\n\t" \
  "ds_read_b128 v[60:63], v70 offset:2048\n\t" \
  "ds_read_b128 v[64:67], v70 offset:3072\n\t" \
  "v_mov_b64 v[36:37], 0\n\t" \
  "v_mov_b64 v[38:39], 0\n\t" \
  "v_mov_b64 v[40:41], 0\n\t" \
  "v_mov_b64 v[42:43], 0\n\t" \
  "v_lshrrev_b32 v71, 1, v69\n\t" \
  "v_add_u32 v71, 4, v71\n\t" \
  "v_lshrrev_b32 v72, 3, v69\n\t" \
  "v_and_b32 v72, 0x78, v72\n\t" \
  "v_and_b32 v74, 0x30, v69\n\t" \
  "v_lshl_add_u32 v72, v74, 3, v72\n\t" \
  "s_add_u32 s54, s53, s36\n\t" \
  "s_add_u32 s55, s54, 573440\n\t" \
  "s_add_u32 s54, s54, 135168\n\t" \
  "s_add_u32 s75, s54, 262144\n\t" \
  "s_add_u32 s80, s53, s26\n\t" \
  "s_or_b32 s87, s52, 0x1fff80\n\t" \
  "s_and_b32 s87, s87, -128\n\t" \
  "s_add_u32 s87, s87, 0x200000\n\t" \
  "s_barrier\n\t" \
  "ds_read_b128 v[0:3], v69 offset:0\n\t" \
  "ds_read_b128 v[4:7], v69 offset:1024\n\t" \
  "ds_read_b128 v[8:11], v69 offset:2048\n\t" \
  "ds_read_b128 v[12:15], v69 offset:3072\n\t" \
  "s_waitcnt lgkmcnt(3)\n\t" \
  "v_smfmac_f32_16x16x64_bf16 v[36:39], v[0:3], v[44:51], v68\n\t" \
  "ds_read_b128 v[44:47], v70 offset:4096\n\t" \
  "v_smfmac_f32_16x16x64_bf16 v[40:43], v[0:3], v[60:67], v68\n\t" \
  "ds_read_b128 v[48:51], v70 offset:5120\n\t" \
  "s_waitcnt lgkmcnt(4)\n\t" \
  "v_smfmac_f32_16x16x64_bf16 v[36:39], v[4:7], a[32:39], v68\n\t" \
  "ds_read_b128 v[60:63], v70 offset:6144\n\t" \
  "v_smfmac_f32_16x16x64_bf16 v[40:43], v[4:7], a[40:47], v68\n\t" \
  "ds_read_b128 v[64:67], v70 offset:7168\n\t" \
  "s_waitcnt lgkmcnt(5)\n\t" \
  "v_smfmac_f32_16x16x64_bf16 v[36:39], v[8:11], a[80:87], v68\n\t" \
  "s_barrier\n\t" \
  "ds_read_b128 v[16:19], v69 offset:4096\n\t" \
  "v_smfmac_f32_16x16x64_bf16 v[40:43], v[8:11], a[88:95], v68\n\t" \
  "ds_read_b128 v[20:23], v69 offset:5120\n\t" \
  "s_waitcnt lgkmcnt(6)\n\t" \
  "v_smfmac_f32_16x16x64_bf16 v[36:39], v[12:15], a[120:127], v68\n\t" \
  "ds_read_b128 v[24:27], v69 offset:6144\n\t" \
  "s_waitcnt lgkmcnt(5)\n\t" \
  "v_smfmac_f32_16x16x64_bf16 v[40:43], v[12:15], v[44:51], v68\n\t" \
  "ds_read_b128 v[28:31], v69 offset:7168\n\t" \
  "s_waitcnt lgkmcnt(3)\n\t" \
  "v_smfmac_f32_16x16x64_bf16 v[36:39], v[16:19], a[160:167], v68\n\t" \
  "ds_read_b128 v[32:35], v69 offset:8192\n\t" \
  "v_smfmac_f32_16x16x64_bf16 v[40:43], v[16:19], a[168:175], v68\n\t" \
  "s_waitcnt lgkmcnt(3)\n\t" \
  "v_mov_b64 v[44:45], 0\n\t" \
  "v_smfmac_f32_16x16x64_bf16 v[36:39], v[20:23], a[200:207], v68\n\t" \
  "s_waitcnt lgkmcnt(2)\n\t" \
  "v_mov_b64 v[46:47], 0\n\t" \
  "v_smfmac_f32_16x16x64_bf16 v[36:39], v[24:27], a[240:247], v68\n\t" \
  "s_waitcnt lgkmcnt(1)\n\t" \
  "v_mov_b64 v[48:49], 0\n\t" \
  "v_smfmac_f32_16x16x64_bf16 v[36:39], v[28:31], v[112:119], v68\n\t" \
  "s_waitcnt lgkmcnt(0)\n\t" \
  "v_mov_b64 v[50:51], 0\n\t" \
  "v_smfmac_f32_16x16x64_bf16 v[36:39], v[32:35], v[160:167], v68\n\t" \
  "v_smfmac_f32_16x16x64_bf16 v[40:43], v[20:23], v[60:67], v68\n\t" \
  "ds_read_b128 v[60:63], v70 offset:8192\n\t" \
  "v_smfmac_f32_16x16x64_bf16 v[40:43], v[24:27], a[248:255], v68\n\t" \
  "ds_read_b128 v[64:67], v70 offset:9216\n\t" \
  "v_add_f32 v74, v36, v38\n\t" \
  "v_smfmac_f32_16x16x64_bf16 v[40:43], v[28:31], v[120:127], v68\n\t" \
  "v_add_f32 v37, v37, v39\n\t" \
  "v_smfmac_f32_16x16x64_bf16 v[40:43], v[32:35], v[168:175], v68\n\t" \
  "v_cmp_u_f32 vcc, v74, v37\n\t" \
  "v_smfmac_f32_16x16x64_bf16 v[44:47], v[0:3], a[0:7], v68\n\t" \
  "s_cbranch_vccnz LFAIL3_%=\n\t" \
  "LGO3_%=:\n\t" \
  "v_cvt_pk_bf16_f32 v74, v74, v37\n\t" \
  "v_smfmac_f32_16x16x64_bf16 v[44:47], v[4:7], a[48:55], v68\n\t" \
  "s_waitcnt lgkmcnt(0)\n\t" \
  "v_add_f32 v75, v40, v42\n\t" \
  "v_smfmac_f32_16x16x64_bf16 v[44:47], v[8:11], v[60:67], v68\n\t" \
  "ds_read_b128 v[60:63], v70 offset:10240\n\t" \
  "v_add_f32 v41, v41, v43\n\t" \
  "v_cvt_pk_bf16_f32 v75, v75, v41\n\t" \
  "v_smfmac_f32_16x16x64_bf16 v[44:47], v[12:15], a[128:135], v68\n\t" \
  "ds_write2st64_b32 v71, v74, v75 offset0:40 offset1:42\n\t" \
  "v_smfmac_f32_16x16x64_bf16 v[44:47], v[16:19], a[176:183], v68\n\t" \
  "ds_read_b128 v[64:67], v70 offset:11264\n\t" \
  "s_memrealtime s[66:67]\n\t" \
  "v_smfmac_f32_16x16x64_bf16 v[44:47], v[20:23], a[208:215], v68\n\t" \
  "v_smfmac_f32_16x16x64_bf16 v[44:47], v[24:27], v[80:87], v68\n\t" \
  "v_smfmac_f32_16x16x64_bf16 v[44:47], v[28:31], v[128:135], v68\n\t" \
  "s_waitcnt lgkmcnt(0)\n\t" \
  "v_smfmac_f32_16x16x64_bf16 v[44:47], v[32:35], v[60:67], v68\n\t" \
  "s_barrier\n\t" \
  "ds_read_b64_tr_b16 v[76:77], v72 offset:10240\n\t" \
  "v_smfmac_f32_16x16x64_bf16 v[48:51], v[0:3], a[8:15], v68\n\t" \
  "s_lshl_b32 s60, s66, 8\n\t" \
  "s_sub_u32 s60, s47, s60\n\t" \
  "ds_read_b64_tr_b16 v[78:79], v72 offset:10752\n\t" \
  "v_mov_b32 v75, s60\n\t" \
  "v_smfmac_f32_16x16x64_bf16 v[48:51], v[4:7], a[56:63], v68\n\t" \
  "s_mov_b64 exec, s[24:25]\n\t" \
  "buffer_atomic_umin v75, v69, s[28:31], s71 offen sc1\n\t" \
  "s_mov_b64 exec, -1\n\t" \
  "v_add_f32 v74, v44, v46\n\t" \
  "v_smfmac_f32_16x16x64_bf16 v[48:51], v[8:11], a[96:103], v68\n\t" \
  "buffer_load_dwordx4 v[60:63], v69, s[28:31], s64 offen sc1\n\t" \
  "v_add_f32 v45, v45, v47\n\t" \
  "v_smfmac_f32_16x16x64_bf16 v[48:51], v[12:15], a[136:143], v68\n\t" \
  "s_waitcnt vmcnt(4)\n\t" \
  "s_waitcnt lgkmcnt(0)\n\t" \
  "v_cvt_pk_bf16_f32 v74, v74, v45\n\t" \
  "v_mfma_f32_16x16x32_bf16 v[36:39], v[52:55], v[76:79], 0\n\t" \
  "s_waitcnt vmcnt(3)\n\t" \
  "v_mfma_f32_16x16x32_bf16 v[40:43], v[56:59], v[76:79], 0\n\t" \
  "v_mov_b64 v[52:53], 0\n\t" \
  "v_mov_b64 v[54:55], 0\n\t" \
  "v_smfmac_f32_16x16x64_bf16 v[48:51], v[16:19], a[184:191], v68\n\t" \
  "buffer_load_dwordx4 v[64:67], v69, s[28:31], s64 offen offset:1024 sc1\n\t" \
  "v_mov_b64 v[56:57], 0\n\t" \
  "v_mov_b64 v[58:59], 0\n\t" \
  "v_smfmac_f32_16x16x64_bf16 v[48:51], v[20:23], a[216:223], v68\n\t" \
  "ds_write_b32 v71, v74 offset:1024\n\t" \
  "v_add_f32_dpp v36, v40, v36 quad_perm:[2,3,0,1] row_mask:0xf bank_mask:0xf\n\t" \
  "v_smfmac_f32_16x16x64_bf16 v[48:51], v[24:27], v[88:95], v68\n\t" \
  "v_add_f32_dpp v37, v41, v37 quad_perm:[2,3,0,1] row_mask:0xf bank_mask:0xf\n\t" \
  "v_smfmac_f32_16x16x64_bf16 v[48:51], v[28:31], v[136:143], v68\n\t" \
  "v_add_f32_dpp v38, v42, v38 quad_perm:[2,3,0,1] row_mask:0xf bank_mask:0xf\n\t" \
  "v_smfmac_f32_16x16x64_bf16 v[48:51], v[32:35], v[176:183], v68\n\t" \
  "v_add_f32_dpp v39, v43, v39 quad_perm:[2,3,0,1] row_mask:0xf bank_mask:0xf\n\t" \
  "v_smfmac_f32_16x16x64_bf16 v[52:55], v[0:3], a[16:23], v68\n\t" \
  "s_mov_b64 exec, s[58:59]\n\t" \
  "ds_write_b128 v69, v[36:39] offset:3104\n\t" \
  "s_mov_b64 exec, -1\n\t" \
  "v_smfmac_f32_16x16x64_bf16 v[52:55], v[4:7], a[64:71], v68\n\t" \
  "v_add_f32 v75, v48, v50\n\t" \
  "v_add_f32 v49, v49, v51\n\t" \
  "v_cvt_pk_bf16_f32 v75, v75, v49\n\t" \
  "v_smfmac_f32_16x16x64_bf16 v[56:59], v[0:3], a[24:31], v68\n\t" \
  "ds_write_b32 v71, v75 offset:1536\n\t" \
  "v_smfmac_f32_16x16x64_bf16 v[56:59], v[4:7], a[72:79], v68\n\t" \
  "ds_read_b64_tr_b16 v[0:1], v72 offset:9216\n\t" \
  "ds_read_b64_tr_b16 v[2:3], v72 offset:9728\n\t" \
  "v_smfmac_f32_16x16x64_bf16 v[52:55], v[8:11], a[104:111], v68\n\t" \
  "ds_read_b128 v[36:39], v70 offset:12288\n\t" \
  "v_smfmac_f32_16x16x64_bf16 v[56:59], v[8:11], a[112:119], v68\n\t" \
  "buffer_load_dword off, s[28:31], s87 sc1 lds\n\t" \
  "ds_read_b128 v[40:43], v70 offset:13312\n\t" \
  "v_smfmac_f32_16x16x64_bf16 v[52:55], v[12:15], a[144:151], v68\n\t" \
  "s_waitcnt lgkmcnt(4)\n\t" \
  "s_barrier\n\t" \
  "ds_read_b64_tr_b16 v[44:45], v72 offset:1024\n\t" \
  "ds_read_b64_tr_b16 v[46:47], v72 offset:1536\n\t" \
  "v_smfmac_f32_16x16x64_bf16 v[56:59], v[12:15], a[152:159], v68\n\t" \
  "ds_read_b64_tr_b16 v[48:49], v72 offset:0\n\t" \
  "ds_read_b64_tr_b16 v[50:51], v72 offset:512\n\t" \
  "v_smfmac_f32_16x16x64_bf16 v[52:55], v[16:19], a[192:199], v68\n\t" \
  "ds_read_b128 v[4:7], v69 offset:2048\n\t" \
  "s_waitcnt lgkmcnt(5)\n\t" \
  "v_smfmac_f32_16x16x64_bf16 v[56:59], v[16:19], v[36:43], v68\n\t" \
  "ds_read_b128 v[8:11], v69 offset:3072\n\t" \
  "s_waitcnt lgkmcnt(4)\n\t" \
  "v_mfma_f32_16x16x32_bf16 v[12:15], v[44:47], v[76:79], 0\n\t" \
  "ds_read_b128 v[36:39], v70 offset:14336\n\t" \
  "s_waitcnt lgkmcnt(3)\n\t" \
  "v_mfma_f32_16x16x32_bf16 v[12:15], v[48:51], v[0:3], v[12:15]\n\t" \
  "ds_read_b128 v[40:43], v70 offset:15360\n\t" \
  "v_smfmac_f32_16x16x64_bf16 v[52:55], v[20:23], a[224:231], v68\n\t" \
  "s_waitcnt lgkmcnt(2)\n\t" \
  "v_add_f32 v4, v4, v8\n\t" \
  "v_add_f32 v5, v5, v9\n\t" \
  "v_add_f32 v6, v6, v10\n\t" \
  "v_cndmask_b32 v78, v13, v14, s[56:57]\n\t" \
  "v_cndmask_b32 v76, v12, v15, s[56:57]\n\t" \
  "v_add_f32 v7, v7, v11\n\t" \
  "v_max3_f32 v77, v4, v5, v6\n\t" \
  "v_add_f32_dpp v76, v78, v76 quad_perm:[2,3,0,1] row_mask:0xf bank_mask:0xf\n\t" \
  "v_max3_f32 v77, v77, v7, v76\n\t" \
  "v_smfmac_f32_16x16x64_bf16 v[56:59], v[20:23], a[232:239], v68\n\t" \
  "ds_read_b128 v[44:47], v70 offset:16384\n\t" \
  "v_max_f32_dpp v77, v77, v77 quad_perm:[2,3,0,1] row_mask:0xf bank_mask:0xf\n\t" \
  "v_mov_b32 v78, v77\n\t" \
  "v_smfmac_f32_16x16x64_bf16 v[52:55], v[24:27], v[96:103], v68\n\t" \
  "ds_read_b128 v[48:51], v70 offset:17408\n\t" \
  "v_permlane16_swap_b32 v77, v78\n\t" \
  "v_max_f32 v77, v77, v78\n\t" \
  "v_mov_b32 v78, v77\n\t" \
  "v_smfmac_f32_16x16x64_bf16 v[52:55], v[28:31], v[144:151], v68\n\t" \
  "ds_read_b128 v[16:19], v70 offset:18432\n\t" \
  "v_permlane32_swap_b32 v77, v78\n\t" \
  "v_max_f32 v77, v77, v78\n\t" \
  "v_smfmac_f32_16x16x64_bf16 v[52:55], v[32:35], v[184:191], v68\n\t" \
  "ds_read_b128 v[20:23], v70 offset:19456\n\t" \
  "v_mul_f32 v77, 0xbe0293ee, v77\n\t" \
  "v_smfmac_f32_16x16x64_bf16 v[56:59], v[24:27], v[104:111], v68\n\t" \
  "v_fmamk_f32 v4, v4, 0x3e0293ee, v77\n\t" \
  "v_smfmac_f32_16x16x64_bf16 v[56:59], v[28:31], v[152:159], v68\n\t" \
  "ds_read_b128 v[24:27], v70 offset:22528\n\t" \
  "s_waitcnt lgkmcnt(4)\n\t" \
  "v_fmamk_f32 v5, v5, 0x3e0293ee, v77\n\t" \
  "v_smfmac_f32_16x16x64_bf16 v[56:59], v[32:35], v[36:43], v68\n\t" \
  "ds_read_b128 v[28:31], v70 offset:23552\n\t" \
  "v_fmamk_f32 v6, v6, 0x3e0293ee, v77\n\t" \
  "v_fmamk_f32 v7, v7, 0x3e0293ee, v77\n\t" \
  "v_fmamk_f32 v76, v76, 0x3e0293ee, v77\n\t" \
  "v_exp_f32 v4, v4\n\t" \
  "v_exp_f32 v5, v5\n\t" \
  "v_exp_f32 v6, v6\n\t" \
  "v_exp_f32 v7, v7\n\t" \
  "v_cvt_pk_bf16_f32 v4, v4, v5\n\t" \
  "v_exp_f32 v76, v76\n\t" \
  "v_cvt_pk_bf16_f32 v5, v6, v7\n\t" \
  "v_mov_b32_dpp v12, v4 quad_perm:[0,1,0,1] row_mask:0xf bank_mask:0xf\n\t" \
  "v_mov_b32_dpp v14, v4 quad_perm:[2,3,2,3] row_mask:0xf bank_mask:0xf\n\t" \
  "v_mov_b32_dpp v13, v5 quad_perm:[0,1,0,1] row_mask:0xf bank_mask:0xf\n\t" \
  "v_mov_b32_dpp v15, v5 quad_perm:[2,3,2,3] row_mask:0xf bank_mask:0xf\n\t" \
  "v_add_f32 v52, v52, v54\n\t" \
  "v_add_f32 v53, v53, v55\n\t" \
  "s_waitcnt vmcnt(2)\n\t" \
  "v_mfma_f32_16x16x32_bf16 v[0:3], v[60:63], v[12:15], 0\n\t" \
  "ds_read_b128 v[36:39], v70 offset:20480\n\t" \
  "v_add_f32 v56, v56, v58\n\t" \
  "v_add_f32 v57, v57, v59\n\t" \
  "v_cvt_pk_bf16_f32 v52, v52, v53\n\t" \
  "v_cvt_pk_bf16_f32 v53, v56, v57\n\t" \
  "s_waitcnt vmcnt(1)\n\t" \
  "v_mfma_f32_16x16x32_bf16 v[4:7], v[64:67], v[12:15], 0\n\t" \
  "ds_read_b128 v[40:43], v70 offset:21504\n\t" \
  "v_mov_b64 v[56:57], s[88:89]\n\t" \
  "v_mov_b64 v[58:59], s[88:89]\n\t" \
  "v_mov_b32_dpp v78, v76 quad_perm:[0,1,0,1] row_mask:0xf bank_mask:0xf\n\t" \
  "v_mov_b32_dpp v79, v76 quad_perm:[2,3,2,3] row_mask:0xf bank_mask:0xf\n\t" \
  "v_mfma_f32_16x16x32_bf16 v[32:35], v[56:59], v[12:15], 0\n\t" \
  "ds_read_b128 v[60:63], v70 offset:24576\n\t" \
  "v_cvt_pk_bf16_f32 v11, v78, v79\n\t" \
  "v_cvt_pk_bf16_f32 v8, v78, v79\n\t" \
  "v_mov_b64 v[54:55], 0\n\t" \
  "v_mov_b32 v9, 0\n\t" \
  "v_mov_b32 v10, 0\n\t" \
  "v_mov_b64 v[14:15], 0\n\t" \
  "s_nop 0\n\t" \
  "v_mfma_f32_16x16x32_bf16 v[32:35], v[54:57], v[8:11], v[32:35]\n\t" \
  "s_cmp_eq_u32 s48, 63\n\t" \
  "v_mfma_f32_16x16x32_bf16 v[0:3], v[52:55], v[8:11], v[0:3]\n\t" \
  "buffer_store_dwordx2 v[52:53], v71, s[28:31], s69 offen offset:3580 sc1\n\t" \
  "ds_read_b128 v[64:67], v70 offset:25600\n\t" \
  "v_mov_b64 v[56:57], 0\n\t" \
  "v_mov_b64 v[58:59], 0\n\t" \
  "v_mfma_f32_16x16x32_bf16 v[4:7], v[52:55], v[10:13], v[4:7]\n\t" \
  "s_cbranch_scc1 LLAST3_%=\n\t" \
  "v_rcp_f32 v74, v32\n\t" \
  "v_mov_b64 v[12:13], 0\n\t" \
  "v_mov_b32 v8, 0\n\t" \
  "v_pk_mul_f32 v[0:1], v[0:1], v[74:75] op_sel_hi:[1,0]\n\t" \
  "v_pk_mul_f32 v[2:3], v[2:3], v[74:75] op_sel_hi:[1,0]\n\t" \
  "v_cvt_pk_bf16_f32 v76, v0, v1\n\t" \
  "v_cvt_pk_bf16_f32 v77, v2, v3\n\t" \
  "v_pk_mul_f32 v[4:5], v[4:5], v[74:75] op_sel_hi:[1,0]\n\t" \
  "v_pk_mul_f32 v[6:7], v[6:7], v[74:75] op_sel_hi:[1,0]\n\t" \
  "v_cvt_pk_bf16_f32 v78, v4, v5\n\t" \
  "v_cvt_pk_bf16_f32 v79, v6, v7\n\t" \
  "ds_write_b128 v69, v[76:79] offset:7168\n\t" \
  "s_waitcnt lgkmcnt(2)\n\t" \
  "v_smfmac_f32_16x16x64_bf16 v[12:15], v[76:79], v[44:51], v68\n\t" \
  "ds_read_b128 v[44:47], v70 offset:26624\n\t" \
  "v_mov_b32 v11, 0\n\t" \
  "v_smfmac_f32_16x16x64_bf16 v[56:59], v[76:79], v[16:23], v68\n\t" \
  "ds_read_b128 v[48:51], v70 offset:27648\n\t" \
  "v_mov_b64 v[52:53], 0\n\t" \
  "v_smfmac_f32_16x16x64_bf16 v[8:11], v[76:79], v[36:43], v68\n\t" \
  "ds_read_b128 v[16:19], v70 offset:28672\n\t" \
  "v_smfmac_f32_16x16x64_bf16 v[52:55], v[76:79], v[24:31], v68\n\t" \
  "s_waitcnt lgkmcnt(3)\n\t" \
  "s_barrier\n\t" \
  "ds_read_b128 v[0:3], v69 offset:6144\n\t" \
  "ds_read_b128 v[4:7], v69 offset:5120\n\t" \
  "v_lshrrev_b32 v71, 7, v69\n\t" \
  "v_and_b32 v74, 0x70, v69\n\t" \
  "v_lshl_add_u32 v71, v71, 14, v74\n\t" \
  "v_bfe_u32 v72, v69, 2, 7\n\t" \
  "v_and_b32 v74, 0x200, v69\n\t" \
  "s_waitcnt lgkmcnt(1)\n\t" \
  "v_smfmac_f32_16x16x64_bf16 v[8:11], v[0:3], v[60:67], v68\n\t" \
  "ds_read_b128 v[20:23], v70 offset:29696\n\t" \
  "v_mov_b64 v[76:77], 0\n\t" \
  "v_smfmac_f32_16x16x64_bf16 v[12:15], v[0:3], v[200:207], v68\n\t" \
  "ds_read_b128 v[32:35], v69 offset:4096\n\t" \
  "v_mov_b64 v[78:79], 0\n\t" \
  "v_smfmac_f32_16x16x64_bf16 v[52:55], v[0:3], v[44:51], v68\n\t" \
  "buffer_load_dword v75, off, s[28:31], s80 sc1\n\t" \
  "ds_read_b128 v[24:27], v70 offset:30720\n\t" \
  "v_lshl_add_u32 v72, v74, 3, v72\n\t" \
  "v_smfmac_f32_16x16x64_bf16 v[56:59], v[0:3], v[232:239], v68\n\t" \
  "ds_read_b128 v[28:31], v70 offset:31744\n\t" \
  "s_waitcnt lgkmcnt(4)\n\t" \
  "v_smfmac_f32_16x16x64_bf16 v[12:15], v[4:7], v[208:215], v68\n\t" \
  "ds_read_b128 v[60:63], v70 offset:32768\n\t" \
  "s_waitcnt lgkmcnt(4)\n\t" \
  "v_smfmac_f32_16x16x64_bf16 v[8:11], v[4:7], v[16:23], v68\n\t" \
  "ds_read_b128 v[64:67], v70 offset:33792\n\t" \
  "s_waitcnt lgkmcnt(4)\n\t" \
  "v_smfmac_f32_16x16x64_bf16 v[8:11], v[32:35], v[192:199], v68\n\t" \
  "ds_read_b128 v[36:39], v73 offset:4096\n\t" \
  "v_smfmac_f32_16x16x64_bf16 v[12:15], v[32:35], v[216:223], v68\n\t" \
  "ds_read_b128 v[40:43], v73 offset:6144\n\t" \
  "s_waitcnt lgkmcnt(4)\n\t" \
  "v_smfmac_f32_16x16x64_bf16 v[52:55], v[4:7], v[24:31], v68\n\t" \
  "ds_read_b128 v[44:47], v70 offset:34816\n\t" \
  "v_cvt_pk_bf16_f32 v8, v8, v9\n\t" \
  "v_smfmac_f32_16x16x64_bf16 v[52:55], v[32:35], v[224:231], v68\n\t" \
  "ds_read_b128 v[48:51], v70 offset:35840\n\t" \
  "v_cvt_pk_bf16_f32 v9, v10, v11\n\t" \
  "v_smfmac_f32_16x16x64_bf16 v[56:59], v[4:7], v[240:247], v68\n\t" \
  "s_waitcnt lgkmcnt(4)\n\t" \
  "v_cvt_pk_bf16_f32 v10, v12, v13\n\t" \
  "v_smfmac_f32_16x16x64_bf16 v[56:59], v[32:35], v[60:67], v68\n\t" \
  "s_waitcnt lgkmcnt(3)\n\t" \
  "v_cvt_pk_bf16_f32 v11, v14, v15\n\t" \
  "v_smfmac_f32_16x16x64_bf16 v[76:79], v[36:39], v[248:255], v68\n\t" \
  "buffer_store_dwordx4 v[8:11], v71, s[28:31], s54 offen sc1\n\t" \
  "s_waitcnt lgkmcnt(0)\n\t" \
  "v_cvt_pk_bf16_f32 v52, v52, v53\n\t" \
  "v_smfmac_f32_16x16x64_bf16 v[76:79], v[40:43], v[44:51], v68\n\t" \
  "v_cvt_pk_bf16_f32 v53, v54, v55\n\t" \
  "v_cvt_pk_bf16_f32 v54, v56, v57\n\t" \
  "v_cvt_pk_bf16_f32 v55, v58, v59\n\t" \
  "buffer_store_dwordx4 v[52:55], v71, s[28:31], s75 offen sc1\n\t" \
  "s_nop 2\n\t" \
  "v_add_f32 v76, v76, v78\n\t" \
  "v_add_f32 v77, v77, v79\n\t" \
  "v_cvt_pk_bf16_f32 v76, v76, v77\n\t" \
  "buffer_store_dword v76, v72, s[28:31], s55 offen sc1\n\t" \
  "s_memrealtime s[70:71]\n\t" \
  "s_waitcnt vmcnt(3)\n\t" \
  "v_readfirstlane_b32 s65, v75\n\t" \
  "s_barrier\n\t" \
  "s_waitcnt lgkmcnt(0)\n\t" \
  "s_not_b32 s65, s65\n\t" \
  "s_lshl_b32 s61, s70, 8\n\t" \
  "s_andn2_b32 s64, s65, 0xff\n\t" \
  "s_sub_u32 s64, s64, s61\n\t" \
  "s_ashr_i32 s64, s64, 8\n\t" \
  "s_cmp_eq_u32 s65, 0\n\t" \
  "s_cselect_b32 s64, " CH_DLF ", s64\n\t" \
  "s_min_i32 s64, s64, s34\n\t" \
  "s_max_i32 s64, s64, " CH_DLL1 "\n\t" \
  "s_add_u32 s64, s64, s70\n\t" \
  "s_mov_b32 s74, 0\n\t" \
  "v_and_b32 v37, 0x30, v69\n\t" \
  "v_add_u32 v37, s52, v37\n\t" \
  "s_mov_b32 exec_lo, 0\n\t" \
  "v_mov_b32 v37, s85\n\t" \
  "s_mov_b32 exec_lo, -1\n\t" \
  "s_cmp_lt_i32 s41, 0\n\t" \
  "s_cbranch_scc1 LIDLE_%=\n\t" \
  "s_movk_i32 s60, 8192\n\t" \
  "RWT3_%=:\n\t" \
  "s_memrealtime s[72:73]\n\t" \
  "s_sub_u32 s60, s60, 1\n\t" \
  "s_cselect_b32 s61, 0, -1\n\t" \
  "s_waitcnt lgkmcnt(0)\n\t" \
  "s_sub_i32 s73, s72, s64\n\t" \
  "s_and_b32 s73, s73, s61\n\t" \
  "s_cmp_lt_i32 s73, " CH_FIN "\n\t" \
  "s_cbranch_scc1 RWT3_%=\n\t" \
  "s_cmp_lt_i32 s73, 0\n\t" \
  "s_cbranch_scc1 RWT3_%=f\n\t" \
  "RWT3_%=g:\n\t" \
  "RRETRY3_%=:\n\t" \
  "buffer_load_dwordx4 v[0:3], v69, s[28:31], s97 offen offset:0 sc1\n\t" \
  "buffer_load_dwordx4 v[4:7], v69, s[28:31], s97 offen offset:1024 sc1\n\t" \
  "buffer_load_dwordx4 v[8:11], v69, s[28:31], s97 offen offset:2048 sc1\n\t" \
  "buffer_load_dwordx4 v[12:15], v69, s[28:31], s97 offen offset:3072 sc1\n\t" \
  "v_lshrrev_b32 v20, 2, v69\n\t" \
  "v_and_b32 v20, 16, v20\n\t" \
  "v_mov_b32 v21, 0x3f80\n\t" \
  "v_lshlrev_b32 v21, v20, v21\n\t" \
  "v_bfe_u32 v20, v69, 4, 2\n\t" \
  "v_cmp_eq_u32 vcc, 0, v20\n\t" \
  "s_nop 1\n\t" \
  "v_cndmask_b32 v32, 0, v21, vcc\n\t" \
  "v_cmp_eq_u32 vcc, 1, v20\n\t" \
  "s_nop 1\n\t" \
  "v_cndmask_b32 v33, 0, v21, vcc\n\t" \
  "v_cmp_eq_u32 vcc, 2, v20\n\t" \
  "s_nop 1\n\t" \
  "v_cndmask_b32 v34, 0, v21, vcc\n\t" \
  "v_cmp_eq_u32 vcc, 3, v20\n\t" \
  "s_nop 1\n\t" \
  "v_cndmask_b32 v35, 0, v21, vcc\n\t" \
  "v_bfe_u32 v38, v69, 7, 1\n\t" \
  "v_bfe_u32 v20, v69, 9, 1\n\t" \
  "v_lshl_add_u32 v38, v20, 2, v38\n\t" \
  "v_lshlrev_b32 v38, 12, v38\n\t" \
  "v_and_b32 v20, 0x60, v69\n\t" \
  "v_add_u32 v38, v38, v20\n\t" \
  "v_bfe_u32 v20, v69, 8, 1\n\t" \
  "v_lshl_add_u32 v38, v20, 4, v38\n\t" \
  "v_bfe_u32 v20, v69, 4, 1\n\t" \
  "v_lshl_add_u32 v38, v20, 3, v38\n\t" \
  "v_mov_b32 v44, 0\n\t" \
  "buffer_load_dword v39, v37, s[28:31], 0 offen sc1\n\t" \
  "s_waitcnt vmcnt(4)\n\t" \
  "v_mfma_f32_16x16x32_bf16 v[40:43], v[32:35], v[0:3], 0\n\t" \
  "v_max3_u32 v44, v44, v0, v1\n\t" \
  "v_max3_u32 v44, v44, v2, v3\n\t" \
  "s_waitcnt vmcnt(3)\n\t" \
  "v_mfma_f32_16x16x32_bf16 v[40:43], v[32:35], v[4:7], v[40:43]\n\t" \
  "v_max3_u32 v44, v44, v4, v5\n\t" \
  "v_max3_u32 v44, v44, v6, v7\n\t" \
  "s_waitcnt vmcnt(2)\n\t" \
  "v_mfma_f32_16x16x32_bf16 v[40:43], v[32:35], v[8:11], v[40:43]\n\t" \
  "v_max3_u32 v44, v44, v8, v9\n\t" \
  "v_max3_u32 v44, v44, v10, v11\n\t" \
  "s_waitcnt vmcnt(1)\n\t" \
  "v_mfma_f32_16x16x32_bf16 v[40:43], v[32:35], v[12:15], v[40:43]\n\t" \
  "v_max3_u32 v44, v44, v12, v13\n\t" \
  "v_max3_u32 v44, v44, v14, v15\n\t" \
  "v_cmp_eq_u32 vcc, -1, v44\n\t" \
  "s_cbranch_vccnz RFAIL3_%=\n\t" \
  "s_nop 3\n\t" \
  "v_add_f32_dpp v40, v40, v40 row_ror:8 row_mask:0xf bank_mask:0xf\n\t" \
  "v_add_f32_dpp v41, v41, v41 row_ror:8 row_mask:0xf bank_mask:0xf\n\t" \
  "v_add_f32_dpp v42, v42, v42 row_ror:8 row_mask:0xf bank_mask:0xf\n\t" \
  "v_add_f32_dpp v43, v43, v43 row_ror:8 row_mask:0xf bank_mask:0xf\n\t" \
  "v_cvt_pk_bf16_f32 v40, v40, v41\n\t" \
  "v_cvt_pk_bf16_f32 v41, v42, v43\n\t" \
  "buffer_store_dwordx2 v[40:41], v38, s[28:31], s98 offen sc1\n\t" \
  "RDONE3_%=:\n\t" \
  "s_barrier\n\t" \
  "s_waitcnt vmcnt(1)\n\t" \
  "v_cmp_eq_u32 vcc, 0, v39\n\t" \
  "s_sub_u32 s60, s70, s66\n\t" \
  "s_add_u32 s81, s81, 1\n\t" \
  "s_min_u32 s81, s81, s60\n\t" \
  "s_add_u32 s48, s48, 1\n\t" \
  "s_mov_b32 s49, s53\n\t" \
  "s_add_u32 s53, s53, " CH_SLOT "\n\t" \
  "s_add_u32 s60, s53, " CH_SKIP "\n\t" \
  "s_lshr_b32 s61, s96, 1\n\t" \
  "s_add_u32 s86, s86, 1\n\t" \
  "s_cmp_eq_u32 s86, 3\n\t" \
  "s_cselect_b32 s53, s60, s53\n\t" \
  "s_cselect_b32 s96, s61, s96\n\t" \
  "s_cselect_b32 s86, 0, s86\n\t" \
  "s_cmp_ge_u32 s53, " CH_RING "\n\t" \
  "s_cselect_b32 s53, 0, s53\n\t" \
  "s_cselect_b32 s96, s21, s96\n\t" \
  "s_andn2_b32 s53, s53, 0x2000\n\t" \
  "s_and_b32 s60, s96, 1\n\t" \
  "s_lshl_b32 s60, s60, 13\n\t" \
  "s_or_b32 s53, s53, s60\n\t" \
  "s_mov_b32 s68, 0\n\t" \
  "s_add_u32 s97, s41, s53\n\t" \
  "s_add_u32 s98, s37, s53\n\t" \
  "s_add_u32 s82, s48, 0\n\t" \
  "s_and_b32 s82, s82, " CH_TRM "\n\t" \
  "s_lshr_b64 s[60:61], s[90:91], s82\n\t" \
  "s_and_b32 s60, s60, 1\n\t" \
  "s_lshl_b32 s60, s60, 13\n\t" \
  "s_add_u32 s82, s82, " CH_SG0 "\n\t" \
  "s_lshl_b32 s82, s82, 21\n\t" \
  "s_or_b32 s82, s82, s60\n\t" \
  "s_add_u32 s50, s82, s38\n\t" \
  "s_add_u32 s69, s82, s39\n\t" \
  "s_add_u32 s82, s48, 1\n\t" \
  "s_lshr_b64 s[60:61], s[90:91], s82\n\t" \
  "s_and_b32 s60, s60, 1\n\t" \
  "s_lshl_b32 s60, s60, 13\n\t" \
  "s_add_u32 s82, s82, " CH_SG0 "\n\t" \
  "s_lshl_b32 s82, s82, 21\n\t" \
  "s_or_b32 s82, s82, s60\n\t" \
  "s_add_u32 s52, s82, s43\n\t" \
  "s_add_u32 s82, s48, " CH_ARMD "\n\t" \
  "s_lshr_b64 s[60:61], s[90:91], s82\n\t" \
  "s_and_b32 s60, s60, 1\n\t" \
  "s_lshl_b32 s60, s60, 13\n\t" \
  "s_add_u32 s82, s82, " CH_SG0 "\n\t" \
  "s_lshl_b32 s82, s82, 21\n\t" \
  "s_or_b32 s82, s82, s60\n\t" \
  "s_add_u32 s85, s82, s45\n\t" \
  "s_cmp_lt_i32 s48, " CH_ARMS "\n\t" \
  "s_cselect_b32 s85, s27, s85\n\t" \
  "s_cmp_eq_u32 s34, 0\n\t" \
  "s_cbranch_scc1 LINI_%=\n\t" \
  "LTK_%=:\n\t" \
  CH_TOKCHK \
  "s_mov_b32 s61, 0\n\t" \
  "LTW_%=:\n\t" \
  "buffer_load_dword v1, v37, s[28:31], 0 offen sc1\n\t" \
  "s_waitcnt vmcnt(0)\n\t" \
  "v_cmp_eq_u32 vcc, 0, v1\n\t" \
  "s_cbranch_vccz LG3_%=\n\t" \
  "s_add_u32 s61, s61, 1\n\t" \
  "s_sleep 2\n\t" \
  "s_cmp_gt_u32 s61, s84\n\t" \
  "s_cbranch_scc1 LTG_%=\n\t" \
  "s_and_b32 s60, s61, 1023\n\t" \
  "s_cmp_lg_u32 s60, 0\n\t" \
  "s_cbranch_scc1 LTW_%=\n\t" \
  "buffer_load_dword v46, off, s[28:31], s92 sc1\n\t" \
  "s_waitcnt vmcnt(0)\n\t" \
  "v_readfirstlane_b32 s60, v46\n\t" \
  "s_nop 0\n\t" \
  "s_cmp_eq_u32 s60, 0\n\t" \
  "s_cbranch_scc1 LTW_%=\n\t" \
  "LTG_%=:\n\t" \
  "s_mov_b32 s84, 0\n\t" \
  "s_lshl_b32 s60, s36, 4\n\t" \
  "s_or_b32 s60, s60, s48\n\t" \
  "s_or_b32 s60, s60, 268435456\n\t" \
  "v_mov_b32 v76, s60\n\t" \
  "v_mov_b32 v77, 0\n\t" \
  "s_mov_b64 exec, 1\n\t" \
  "buffer_atomic_cmpswap v[76:77], off, s[28:31], s92 sc1\n\t" \
  "s_mov_b64 exec, -1\n\t" \
  "s_branch LG3_%=\n\t" \
  "LIDLE_%=:\n\t" \
  "s_add_u32 s64, s64, 8\n\t" \
  "s_movk_i32 s60, 8192\n\t" \
  "IWT_%=:\n\t" \
  "s_memrealtime s[72:73]\n\t" \
  "s_sub_u32 s60, s60, 1\n\t" \
  "s_cselect_b32 s61, 0, -1\n\t" \
  "s_waitcnt lgkmcnt(0)\n\t" \
  "s_sub_i32 s73, s72, s64\n\t" \
  "s_and_b32 s73, s73, s61\n\t" \
  "s_cmp_lt_i32 s73, 0\n\t" \
  "s_cbranch_scc1 IWT_%=\n\t" \
  "buffer_load_dword v39, v37, s[28:31], 0 offen sc1\n\t" \
  "buffer_store_dword v37, off, s[28:31], s41\n\t" \
  "s_branch RDONE3_%=\n\t" \
  "RFAIL3_%=:\n\t" \
  "s_add_u32 s74, s74, 1\n\t" \
  "s_sleep " CH_RSL "\n\t" \
  "s_waitcnt vmcnt(0)\n\t" \
  "s_cmp_gt_u32 s74, s84\n\t" \
  "s_cbranch_scc1 RG3_%=\n\t" \
  "s_and_b32 s60, s74, 1023\n\t" \
  "s_cmp_lg_u32 s60, 0\n\t" \
  "s_cbranch_scc1 RRETRY3_%=\n\t" \
  "buffer_load_dword v46, off, s[28:31], s92 sc1\n\t" \
  "s_waitcnt vmcnt(0)\n\t" \
  "v_readfirstlane_b32 s60, v46\n\t" \
  "s_nop 0\n\t" \
  "s_cmp_eq_u32 s60, 0\n\t" \
  "s_cbranch_scc1 RRETRY3_%=\n\t" \
  "RG3_%=:\n\t" \
  "s_mov_b32 s84, 0\n\t" \
  "s_lshl_b32 s60, s36, 4\n\t" \
  "s_or_b32 s60, s60, s48\n\t" \
  "s_or_b32 s60, s60, 536870912\n\t" \
  "v_mov_b32 v76, s60\n\t" \
  "v_mov_b32 v77, 0\n\t" \
  "s_mov_b64 exec, 1\n\t" \
  "buffer_atomic_cmpswap v[76:77], off, s[28:31], s92 sc1\n\t" \
  "s_mov_b64 exec, -1\n\t" \
  "s_branch RDONE3_%=\n\t" \
  "LFAIL3_%=:\n\t" \
  "v_max_u32 v75, v0, v1\n\t" \
  "v_max3_u32 v75, v75, v2, v3\n\t" \
  "v_max3_u32 v75, v75, v4, v5\n\t" \
  "v_max3_u32 v75, v75, v6, v7\n\t" \
  "v_max3_u32 v75, v75, v8, v9\n\t" \
  "v_max3_u32 v75, v75, v10, v11\n\t" \
  "v_max3_u32 v75, v75, v12, v13\n\t" \
  "v_max3_u32 v75, v75, v14, v15\n\t" \
  "v_max3_u32 v75, v75, v16, v17\n\t" \
  "v_max3_u32 v75, v75, v18, v19\n\t" \
  "v_max3_u32 v75, v75, v20, v21\n\t" \
  "v_max3_u32 v75, v75, v22, v23\n\t" \
  "v_max3_u32 v75, v75, v24, v25\n\t" \
  "v_max3_u32 v75, v75, v26, v27\n\t" \
  "v_max3_u32 v75, v75, v28, v29\n\t" \
  "v_max3_u32 v75, v75, v30, v31\n\t" \
  "v_max3_u32 v75, v75, v32, v33\n\t" \
  "v_max3_u32 v75, v75, v34, v35\n\t" \
  "v_cmp_eq_u32 vcc, -1, v75\n\t" \
  "s_cbranch_vccz LGO3_%=\n\t" \
  "s_add_u32 s68, s68, 1\n\t" \
  "s_waitcnt lgkmcnt(0)\n\t" \
  "s_cmp_gt_u32 s68, s83\n\t" \
  "s_cbranch_scc1 LGV3_%=\n\t" \
  "s_barrier\n\t" \
  "s_waitcnt vmcnt(8)\n\t" \
  "s_branch LG3_%=\n\t" \
  "RWT3_%=f:\n\t" \
  "s_cmp_lt_i32 s73, -2\n\t" \
  "s_cbranch_scc0 RWT3_%=g\n\t" \
  "s_nop 4\n\t" \
  "s_branch RWT3_%=g\n\t" \
  "LGV3_%=:\n\t" \
  "s_mov_b32 s83, 0\n\t" \
  "s_lshl_b32 s60, s36, 4\n\t" \
  "s_or_b32 s60, s60, s48\n\t" \
  "s_or_b32 s60, s60, 805306368\n\t" \
  "v_mov_b32 v76, s60\n\t" \
  "v_mov_b32 v77, 0\n\t" \
  "s_mov_b64 exec, 1\n\t" \
  "buffer_atomic_cmpswap v[76:77], off, s[28:31], s92 sc1\n\t" \
  "s_mov_b64 exec, -1\n\t" \
  "s_branch LGO3_%=\n\t" \
  "LLAST3_%=:\n\t" \
  "s_waitcnt vmcnt(0) lgkmcnt(0)\n\t" \
  "s_barrier\n\t" \
  "LEND_%=:\n\t" \
  ""
typedef unsigned v16u __attribute__((ext_vector_type(16)));
#define CH_WCLOB "a0", "a1", "a2", "a3", "a4", "a5", "a6", "a7", "a8", "a9", "a10", "a11", "a12", "a13", "a14", "a15", "a16", "a17", "a18", "a19", "a20", "a21", "a22", "a23", "a24", "a25", "a26", "a27", "a28", "a29", "a30", "a31", "a32", "a33", "a34", "a35", "a36", "a37", "a38", "a39", "a40", "a41", "a42", "a43", "a44", "a45", "a46", "a47", "a48", "a49", "a50", "a51", "a52", "a53", "a54", "a55", "a56", "a57", "a58", "a59", "a60", "a61", "a62", "a63", "a64", "a65", "a66", "a67", "a68", "a69", "a70", "a71", "a72", "a73", "a74", "a75", "a76", "a77", "a78", "a79", "a80", "a81", "a82", "a83", "a84", "a85", "a86", "a87", "a88", "a89", "a90", "a91", "a92", "a93", "a94", "a95", "a96", "a97", "a98", "a99", "a100", "a101", "a102", "a103", "a104", "a105", "a106", "a107", "a108", "a109", "a110", "a111", "a112", "a113", "a114", "a115", "a116", "a117", "a118", "a119", "a120", "a121", "a122", "a123", "a124", "a125", "a126", "a127", "a128", "a129", "a130", "a131", "a132", "a133", "a134", "a135", "a136", "a137", "a138", "a139", "a140", "a141", "a142", "a143", "a144", "a145", "a146", "a147", "a148", "a149", "a150", "a151", "a152", "a153", "a154", "a155", "a156", "a157", "a158", "a159", "a160", "a161", "a162", "a163", "a164", "a165", "a166", "a167", "a168", "a169", "a170", "a171", "a172", "a173", "a174", "a175", "a176", "a177", "a178", "a179", "a180", "a181", "a182", "a183", "a184", "a185", "a186", "a187", "a188", "a189", "a190", "a191", "a192", "a193", "a194", "a195", "a196", "a197", "a198", "a199", "a200", "a201", "a202", "a203", "a204", "a205", "a206", "a207", "a208", "a209", "a210", "a211", "a212", "a213", "a214", "a215", "a216", "a217", "a218", "a219", "a220", "a221", "a222", "a223", "a224", "a225", "a226", "a227", "a228", "a229", "a230", "a231", "a232", "a233", "a234", "a235", "a236", "a237", "a238", "a239", "a240", "a241", "a242", "a243", "a244", "a245", "a246", "a247", "a248", "a249", "a250", "a251", "a252", "a253", "a254", "a255", "v80", "v81", "v82", "v83", "v84", "v85", "v86", "v87", "v88", "v89", "v90", "v91", "v92", "v93", "v94", "v95", "v96", "v97", "v98", "v99", "v100", "v101", "v102", "v103", "v104", "v105", "v106", "v107", "v108", "v109", "v110", "v111", "v112", "v113", "v114", "v115", "v116", "v117", "v118", "v119", "v120", "v121", "v122", "v123", "v124", "v125", "v126", "v127", "v128", "v129", "v130", "v131", "v132", "v133", "v134", "v135", "v136", "v137", "v138", "v139", "v140", "v141", "v142", "v143", "v144", "v145", "v146", "v147", "v148", "v149", "v150", "v151", "v152", "v153", "v154", "v155", "v156", "v157", "v158", "v159", "v160", "v161", "v162", "v163", "v164", "v165", "v166", "v167", "v168", "v169", "v170", "v171", "v172", "v173", "v174", "v175", "v176", "v177", "v178", "v179", "v180", "v181", "v182", "v183", "v184", "v185", "v186", "v187", "v188", "v189", "v190", "v191", "v192", "v193", "v194", "v195", "v196", "v197", "v198", "v199", "v200", "v201", "v202", "v203", "v204", "v205", "v206", "v207", "v208", "v209", "v210", "v211", "v212", "v213", "v214", "v215", "v216", "v217", "v218", "v219", "v220", "v221", "v222", "v223", "v224", "v225", "v226", "v227", "v228", "v229", "v230", "v231", "v232", "v233", "v234", "v235", "v236", "v237", "v238", "v239", "v240", "v241", "v242", "v243", "v244", "v245", "v246", "v247", "v248", "v249", "v250", "v251", "v252", "v253", "v254", "v255"
#define CH_H(b) (8 * ((b) & 3) + ((b) >> 3))
#define CH_L(b) (((b) >> 2) & 1)
__device__ __forceinline__ void start_work(char* smem, int wk);
__device__ __forceinline__ void chain_wg(char* smem) {
  if (CH_L((int)blockIdx.x)) start_work(smem, CH_H((int)blockIdx.x));
  const MP p = getp();
  const int b = (int)blockIdx.x, h = CH_H(b), L = CH_L(b);
  int w = __builtin_amdgcn_readfirstlane(threadIdx.x >> 6);
  {
    unsigned id; asm volatile("s_getreg_b32 %0, hwreg(HW_REG_HW_ID, 4, 2)" : "=s"(id));
    volatile __attribute__((address_space(3))) unsigned* lw = (volatile __attribute__((address_space(3))) unsigned*)smem;
    lw[w] = 1u << id;
    BAR();
    if (__builtin_amdgcn_readfirstlane(lw[0] | lw[1] | lw[2] | lw[3]) == 15u) w = (int)((SMAP >> (4u * id)) & 3u);
  }
  unsigned lp16 = (threadIdx.x & 63u) * 16u;
  rsrc_t X = mkr(p.xb, XB_BYTES);
  if ((unsigned)(size_t)(__attribute__((address_space(3))) char*)smem != 0u) return;
  const unsigned wsz = DM * HD * 2u - (unsigned)(h * DH) * 2u;
  const rsrc_t RQ = mkr(p.Wq + h * DH, wsz), RK = mkr(p.Wk + h * DH, wsz), RV = mkr(p.Wv + h * DH, wsz), RO = mkr(p.Wo + (long)h * DH * DM, DH * DM * 2u);
  unsigned xcc; asm volatile("s_getreg_b32 %0, hwreg(HW_REG_XCC_ID)" : "=s"(xcc)); xcc &= 3u;
  unsigned d96 = 0u;
  const unsigned c43 = P_FA * PAIR + (unsigned)h * 128u + 32u;
  const unsigned h128 = (unsigned)h * 128u, uw = (unsigned)w, t = uw >> 1, T = uw & 1u, NONE = 0xF0000000u;
  const unsigned u0 = w == 3 ? (unsigned)h >> 2 : (unsigned)((h & 1) + (h >> 3) * 2 + ((h >> 1) & 3) * 8 + (w == 2 ? 32 : 0));
  const bool idle = w == 3 && (h & 3);
  const unsigned c37 = idle ? NONE : (w == 3 ? AGT + u0 * 128u : AGO + (u0 >> 5) * 32768u + (u0 & 31u) * 128u);
  const unsigned c41 = idle ? NONE : (w == 3 ? 524288u + (u0 >> 1) * 16384u + (u0 & 1u) * 4096u : (u0 & 7u) * 16384u + ((u0 >> 3) & 1u) * 4096u + (u0 >> 4) * 131072u);
  const unsigned c42 = AGO + (xcc & 1u) * 16384u + (xcc >> 1) * 4096u;
  const unsigned c46 = (w == 3 ? (h & 7) == 5 && DL3 > -50 : h == 30) ? AGT + 1024u : NONE;
  const unsigned c38 = (unsigned)(3 * h + 1) * PAIR + T * 4096u + t * 2048u, c39 = (P_KV + (unsigned)(h >> 1)) * PAIR + (unsigned)(h & 1) * 4096u;
  const unsigned c45 = P_FB * PAIR + 4096u + (unsigned)(16 + (h & 15)) * 128u;
  unsigned d48 = 0u, d49 = 0u, d53 = SLOT, d86 = 1u, d65 = rtc();
  unsigned mn = 120u;
  v4u stt;
  asm volatile("s_waitcnt vmcnt(0) lgkmcnt(0)\n\t" CH_ASM "LCHEND:"
               : "+{s48}"(d48), "+{s49}"(d49), "+{s53}"(d53), "+{s65}"(d65), "+{s81}"(mn), "+{s86}"(d86), "+{s96}"(d96), "=&{s[76:79]}"(stt), "+{v69}"(lp16), "+{s[28:31]}"(X)
               : [Wq] "s"(RQ), [Wk] "s"(RK), [Wv] "s"(RV), [Wo] "s"(RO), "{s33}"(HT_OFF + 8192u + 256u + (unsigned)b * 4u), "{s36}"(h128), "{s37}"(c37), "{s40}"(w), "{s41}"(c41), "{s42}"(c42), "{s46}"(c46), "{s92}"(TMO_OFF), "{s38}"(c38), "{s39}"(c39), "{s43}"(c43), "{s45}"(c45), "{s32}"(L), "{s26}"(AGT + 1024u + xcc * 128u), "{s27}"(RDY_OFF(0) + (unsigned)((h & 7) * 1024 + (b & 7) * 128)), [ctab] "i"(CTAB_OFF), [ring] "i"(SRING / 3 * XBLK), [trm] "i"(TRING - 1), [ex] "i"(ED * 256 + EB), [dl1l] "i"(DL1 - LATE), [armd] "i"(3 - ARMK), [arms] "i"(ARMK - 3),
                 [cap] "i"(SPIN_CAP)
               : "v0", "v1", "v2", "v3", "v4", "v5", "v6", "v7", "v8", "v9", "v10", "v11", "v12", "v13", "v14", "v15", "v16", "v17", "v18", "v19",
                 "v20", "v21", "v22", "v23", "v24", "v25", "v26", "v27", "v28", "v29", "v30", "v31", "v32", "v33", "v34", "v35", "v36", "v37", "v38", "v39",
                 "v40", "v41", "v42", "v43", "v44", "v45", "v46", "v47", "v48", "v49", "v50", "v51", "v52", "v53", "v54", "v55", "v56", "v57", "v58", "v59",
                 "v60", "v61", "v62", "v63", "v64", "v65", "v66", "v67", "v68", "v70", "v71", "v72", "v73", "v74", "v75", "v76", "v77", "v78", "v79",
                 CH_WCLOB, "s21", "s34", "s35", "s24", "s25", "s90", "s91", "s47", "s87", "s50", "s51", "s52", "s69", "s82", "s85", "s54", "s75", "s80", "s55", "s56", "s57", "s58", "s59", "s60", "s61", "s62", "s63", "s64", "s66", "s67", "s68", "s70", "s71", "s72", "s73", "s74", "s88", "s89",
                 "s83", "s84", "s97", "s98", "s99", "s93", "s94", "s95", "m0", "vcc", "scc", "memory");
}
#define QLEAD 4
#define RONE 2
#define OAUX 0
#define PFM 1
#define PSP 4
#define QLEAD1 2
#define QXW -18
#define QXC QXW
#define TK_QXW "%[qxw]"
#define TK_QXC "%[qxc]"
#define PSG 16
#define TK_PSG CH_S(PSG)
#define AX 30
#define AX1 45
#define AM 22
#define TK_AM CH_S(AM)
#define PSK 2
#define PSH 2
#define TK_CAP "%[cap]"
#define TK_CTAB "%[ctab]"
#define TK_RM "%[rm]"
#define TK_TRM "%[trm]"
#define TK_TRN "%[trn]"
#define TK_SG0 CH_S(SG0)
#define TK_PSK CH_S(PSK)
#define TK_PSH CH_S(PSH)
#define AP_ASM \
  "v_lshrrev_b32 v40, 8, v0\n\t" \
  "v_mul_u32_u24 v40, 4608, v40\n\t" \
  "v_and_b32 v41, 0xf0, v0\n\t" \
  "v_lshrrev_b32 v42, 3, v41\n\t" \
  "v_add3_u32 v41, v41, v40, s64\n\t" \
  "v_add3_u32 v42, v42, v40, s65\n\t" \
  "s_mov_b32 s62, 0x05040100\n\t" \
  "s_mov_b32 s63, 0x07060302\n\t" \
  "s_xor_b32 s60, s66, 0\n\t" \
  "s_add_u32 s60, s60, 0\n\t" \
  "s_add_u32 s61, s60, 18432\n\t" \
  "buffer_load_dwordx4 v[48:51], v41, %[W], s60 offen offset:0\n\t" \
  "buffer_load_dwordx4 v[52:55], v41, %[W], s60 offen offset:1152\n\t" \
  "buffer_load_dwordx4 v[56:59], v41, %[W], s60 offen offset:2304\n\t" \
  "buffer_load_dwordx4 v[60:63], v41, %[W], s60 offen offset:3456\n\t" \
  "buffer_load_dwordx4 v[64:67], v41, %[W], s61 offen offset:0\n\t" \
  "buffer_load_dwordx4 v[68:71], v41, %[W], s61 offen offset:1152\n\t" \
  "buffer_load_dwordx4 v[72:75], v41, %[W], s61 offen offset:2304\n\t" \
  "buffer_load_dwordx4 v[76:79], v41, %[W], s61 offen offset:3456\n\t" \
  "s_xor_b32 s60, s66, 0\n\t" \
  "s_add_u32 s60, s60, 36864\n\t" \
  "s_add_u32 s61, s60, 18432\n\t" \
  "buffer_load_dwordx4 v[80:83], v41, %[W], s60 offen offset:0\n\t" \
  "buffer_load_dwordx4 v[84:87], v41, %[W], s60 offen offset:1152\n\t" \
  "buffer_load_dwordx4 v[88:91], v41, %[W], s60 offen offset:2304\n\t" \
  "buffer_load_dwordx4 v[92:95], v41, %[W], s60 offen offset:3456\n\t" \
  "buffer_load_dwordx4 v[96:99], v41, %[W], s61 offen offset:0\n\t" \
  "buffer_load_dwordx4 v[100:103], v41, %[W], s61 offen offset:1152\n\t" \
  "buffer_load_dwordx4 v[104:107], v41, %[W], s61 offen offset:2304\n\t" \
  "buffer_load_dwordx4 v[108:111], v41, %[W], s61 offen offset:3456\n\t" \
  "s_xor_b32 s60, s66, 0\n\t" \
  "s_add_u32 s60, s60, 73728\n\t" \
  "s_add_u32 s61, s60, 18432\n\t" \
  "buffer_load_dwordx4 v[112:115], v41, %[W], s60 offen offset:0\n\t" \
  "buffer_load_dwordx4 v[116:119], v41, %[W], s60 offen offset:1152\n\t" \
  "buffer_load_dwordx4 v[120:123], v41, %[W], s60 offen offset:2304\n\t" \
  "buffer_load_dwordx4 v[124:127], v41, %[W], s60 offen offset:3456\n\t" \
  "buffer_load_dwordx4 v[128:131], v41, %[W], s61 offen offset:0\n\t" \
  "buffer_load_dwordx4 v[132:135], v41, %[W], s61 offen offset:1152\n\t" \
  "buffer_load_dwordx4 v[136:139], v41, %[W], s61 offen offset:2304\n\t" \
  "buffer_load_dwordx4 v[140:143], v41, %[W], s61 offen offset:3456\n\t" \
  "s_xor_b32 s60, s66, 0\n\t" \
  "s_add_u32 s60, s60, 110592\n\t" \
  "s_add_u32 s61, s60, 18432\n\t" \
  "buffer_load_dwordx4 v[144:147], v41, %[W], s60 offen offset:0\n\t" \
  "buffer_load_dwordx4 v[148:151], v41, %[W], s60 offen offset:1152\n\t" \
  "buffer_load_dwordx4 v[152:155], v41, %[W], s60 offen offset:2304\n\t" \
  "buffer_load_dwordx4 v[156:159], v41, %[W], s60 offen offset:3456\n\t" \
  "buffer_load_dwordx4 v[160:163], v41, %[W], s61 offen offset:0\n\t" \
  "buffer_load_dwordx4 v[164:167], v41, %[W], s61 offen offset:1152\n\t" \
  "buffer_load_dwordx4 v[168:171], v41, %[W], s61 offen offset:2304\n\t" \
  "buffer_load_dwordx4 v[172:175], v41, %[W], s61 offen offset:3456\n\t" \
  "s_xor_b32 s60, s66, 147456\n\t" \
  "s_add_u32 s60, s60, 0\n\t" \
  "s_add_u32 s61, s60, 18432\n\t" \
  "buffer_load_dwordx4 v[176:179], v41, %[W], s60 offen offset:0\n\t" \
  "buffer_load_dwordx4 v[180:183], v41, %[W], s60 offen offset:1152\n\t" \
  "buffer_load_dwordx4 v[184:187], v41, %[W], s60 offen offset:2304\n\t" \
  "buffer_load_dwordx4 v[188:191], v41, %[W], s60 offen offset:3456\n\t" \
  "buffer_load_dwordx4 v[192:195], v41, %[W], s61 offen offset:0\n\t" \
  "buffer_load_dwordx4 v[196:199], v41, %[W], s61 offen offset:1152\n\t" \
  "buffer_load_dwordx4 v[200:203], v41, %[W], s61 offen offset:2304\n\t" \
  "buffer_load_dwordx4 v[204:207], v41, %[W], s61 offen offset:3456\n\t" \
  "s_waitcnt vmcnt(38)\n\t" \
  "v_perm_b32 v44, v52, v48, s62\n\t" \
  "s_waitcnt vmcnt(36)\n\t" \
  "v_perm_b32 v45, v60, v56, s62\n\t" \
  "s_waitcnt vmcnt(34)\n\t" \
  "v_perm_b32 v46, v68, v64, s62\n\t" \
  "s_waitcnt vmcnt(32)\n\t" \
  "v_perm_b32 v47, v76, v72, s62\n\t" \
  "v_accvgpr_write_b32 a0, v44\n\t" \
  "v_accvgpr_write_b32 a1, v45\n\t" \
  "v_accvgpr_write_b32 a2, v46\n\t" \
  "v_accvgpr_write_b32 a3, v47\n\t" \
  "v_perm_b32 v44, v52, v48, s63\n\t" \
  "v_perm_b32 v45, v60, v56, s63\n\t" \
  "v_perm_b32 v46, v68, v64, s63\n\t" \
  "v_perm_b32 v47, v76, v72, s63\n\t" \
  "v_accvgpr_write_b32 a4, v44\n\t" \
  "v_accvgpr_write_b32 a5, v45\n\t" \
  "v_accvgpr_write_b32 a6, v46\n\t" \
  "v_accvgpr_write_b32 a7, v47\n\t" \
  "v_perm_b32 v44, v53, v49, s62\n\t" \
  "v_perm_b32 v45, v61, v57, s62\n\t" \
  "v_perm_b32 v46, v69, v65, s62\n\t" \
  "v_perm_b32 v47, v77, v73, s62\n\t" \
  "v_accvgpr_write_b32 a8, v44\n\t" \
  "v_accvgpr_write_b32 a9, v45\n\t" \
  "v_accvgpr_write_b32 a10, v46\n\t" \
  "v_accvgpr_write_b32 a11, v47\n\t" \
  "v_perm_b32 v44, v53, v49, s63\n\t" \
  "v_perm_b32 v45, v61, v57, s63\n\t" \
  "v_perm_b32 v46, v69, v65, s63\n\t" \
  "v_perm_b32 v47, v77, v73, s63\n\t" \
  "v_accvgpr_write_b32 a12, v44\n\t" \
  "v_accvgpr_write_b32 a13, v45\n\t" \
  "v_accvgpr_write_b32 a14, v46\n\t" \
  "v_accvgpr_write_b32 a15, v47\n\t" \
  "v_perm_b32 v44, v54, v50, s62\n\t" \
  "v_perm_b32 v45, v62, v58, s62\n\t" \
  "v_perm_b32 v46, v70, v66, s62\n\t" \
  "v_perm_b32 v47, v78, v74, s62\n\t" \
  "v_accvgpr_write_b32 a16, v44\n\t" \
  "v_accvgpr_write_b32 a17, v45\n\t" \
  "v_accvgpr_write_b32 a18, v46\n\t" \
  "v_accvgpr_write_b32 a19, v47\n\t" \
  "v_perm_b32 v44, v54, v50, s63\n\t" \
  "v_perm_b32 v45, v62, v58, s63\n\t" \
  "v_perm_b32 v46, v70, v66, s63\n\t" \
  "v_perm_b32 v47, v78, v74, s63\n\t" \
  "v_accvgpr_write_b32 a20, v44\n\t" \
  "v_accvgpr_write_b32 a21, v45\n\t" \
  "v_accvgpr_write_b32 a22, v46\n\t" \
  "v_accvgpr_write_b32 a23, v47\n\t" \
  "v_perm_b32 v44, v55, v51, s62\n\t" \
  "v_perm_b32 v45, v63, v59, s62\n\t" \
  "v_perm_b32 v46, v71, v67, s62\n\t" \
  "v_perm_b32 v47, v79, v75, s62\n\t" \
  "v_accvgpr_write_b32 a24, v44\n\t" \
  "v_accvgpr_write_b32 a25, v45\n\t" \
  "v_accvgpr_write_b32 a26, v46\n\t" \
  "v_accvgpr_write_b32 a27, v47\n\t" \
  "v_perm_b32 v44, v55, v51, s63\n\t" \
  "v_perm_b32 v45, v63, v59, s63\n\t" \
  "v_perm_b32 v46, v71, v67, s63\n\t" \
  "v_perm_b32 v47, v79, v75, s63\n\t" \
  "v_accvgpr_write_b32 a28, v44\n\t" \
  "v_accvgpr_write_b32 a29, v45\n\t" \
  "v_accvgpr_write_b32 a30, v46\n\t" \
  "v_accvgpr_write_b32 a31, v47\n\t" \
  "s_xor_b32 s60, s66, 147456\n\t" \
  "s_add_u32 s60, s60, 36864\n\t" \
  "s_add_u32 s61, s60, 18432\n\t" \
  "buffer_load_dwordx4 v[48:51], v41, %[W], s60 offen offset:0\n\t" \
  "buffer_load_dwordx4 v[52:55], v41, %[W], s60 offen offset:1152\n\t" \
  "buffer_load_dwordx4 v[56:59], v41, %[W], s60 offen offset:2304\n\t" \
  "buffer_load_dwordx4 v[60:63], v41, %[W], s60 offen offset:3456\n\t" \
  "buffer_load_dwordx4 v[64:67], v41, %[W], s61 offen offset:0\n\t" \
  "buffer_load_dwordx4 v[68:71], v41, %[W], s61 offen offset:1152\n\t" \
  "buffer_load_dwordx4 v[72:75], v41, %[W], s61 offen offset:2304\n\t" \
  "buffer_load_dwordx4 v[76:79], v41, %[W], s61 offen offset:3456\n\t" \
  "s_waitcnt vmcnt(38)\n\t" \
  "v_perm_b32 v44, v84, v80, s62\n\t" \
  "s_waitcnt vmcnt(36)\n\t" \
  "v_perm_b32 v45, v92, v88, s62\n\t" \
  "s_waitcnt vmcnt(34)\n\t" \
  "v_perm_b32 v46, v100, v96, s62\n\t" \
  "s_waitcnt vmcnt(32)\n\t" \
  "v_perm_b32 v47, v108, v104, s62\n\t" \
  "v_accvgpr_write_b32 a32, v44\n\t" \
  "v_accvgpr_write_b32 a33, v45\n\t" \
  "v_accvgpr_write_b32 a34, v46\n\t" \
  "v_accvgpr_write_b32 a35, v47\n\t" \
  "v_perm_b32 v44, v84, v80, s63\n\t" \
  "v_perm_b32 v45, v92, v88, s63\n\t" \
  "v_perm_b32 v46, v100, v96, s63\n\t" \
  "v_perm_b32 v47, v108, v104, s63\n\t" \
  "v_accvgpr_write_b32 a36, v44\n\t" \
  "v_accvgpr_write_b32 a37, v45\n\t" \
  "v_accvgpr_write_b32 a38, v46\n\t" \
  "v_accvgpr_write_b32 a39, v47\n\t" \
  "v_perm_b32 v44, v85, v81, s62\n\t" \
  "v_perm_b32 v45, v93, v89, s62\n\t" \
  "v_perm_b32 v46, v101, v97, s62\n\t" \
  "v_perm_b32 v47, v109, v105, s62\n\t" \
  "v_accvgpr_write_b32 a40, v44\n\t" \
  "v_accvgpr_write_b32 a41, v45\n\t" \
  "v_accvgpr_write_b32 a42, v46\n\t" \
  "v_accvgpr_write_b32 a43, v47\n\t" \
  "v_perm_b32 v44, v85, v81, s63\n\t" \
  "v_perm_b32 v45, v93, v89, s63\n\t" \
  "v_perm_b32 v46, v101, v97, s63\n\t" \
  "v_perm_b32 v47, v109, v105, s63\n\t" \
  "v_accvgpr_write_b32 a44, v44\n\t" \
  "v_accvgpr_write_b32 a45, v45\n\t" \
  "v_accvgpr_write_b32 a46, v46\n\t" \
  "v_accvgpr_write_b32 a47, v47\n\t" \
  "v_perm_b32 v44, v86, v82, s62\n\t" \
  "v_perm_b32 v45, v94, v90, s62\n\t" \
  "v_perm_b32 v46, v102, v98, s62\n\t" \
  "v_perm_b32 v47, v110, v106, s62\n\t" \
  "v_accvgpr_write_b32 a48, v44\n\t" \
  "v_accvgpr_write_b32 a49, v45\n\t" \
  "v_accvgpr_write_b32 a50, v46\n\t" \
  "v_accvgpr_write_b32 a51, v47\n\t" \
  "v_perm_b32 v44, v86, v82, s63\n\t" \
  "v_perm_b32 v45, v94, v90, s63\n\t" \
  "v_perm_b32 v46, v102, v98, s63\n\t" \
  "v_perm_b32 v47, v110, v106, s63\n\t" \
  "v_accvgpr_write_b32 a52, v44\n\t" \
  "v_accvgpr_write_b32 a53, v45\n\t" \
  "v_accvgpr_write_b32 a54, v46\n\t" \
  "v_accvgpr_write_b32 a55, v47\n\t" \
  "v_perm_b32 v44, v87, v83, s62\n\t" \
  "v_perm_b32 v45, v95, v91, s62\n\t" \
  "v_perm_b32 v46, v103, v99, s62\n\t" \
  "v_perm_b32 v47, v111, v107, s62\n\t" \
  "v_accvgpr_write_b32 a56, v44\n\t" \
  "v_accvgpr_write_b32 a57, v45\n\t" \
  "v_accvgpr_write_b32 a58, v46\n\t" \
  "v_accvgpr_write_b32 a59, v47\n\t" \
  "v_perm_b32 v44, v87, v83, s63\n\t" \
  "v_perm_b32 v45, v95, v91, s63\n\t" \
  "v_perm_b32 v46, v103, v99, s63\n\t" \
  "v_perm_b32 v47, v111, v107, s63\n\t" \
  "v_accvgpr_write_b32 a60, v44\n\t" \
  "v_accvgpr_write_b32 a61, v45\n\t" \
  "v_accvgpr_write_b32 a62, v46\n\t" \
  "v_accvgpr_write_b32 a63, v47\n\t" \
  "s_xor_b32 s60, s66, 147456\n\t" \
  "s_add_u32 s60, s60, 73728\n\t" \
  "s_add_u32 s61, s60, 18432\n\t" \
  "buffer_load_dwordx4 v[80:83], v41, %[W], s60 offen offset:0\n\t" \
  "buffer_load_dwordx4 v[84:87], v41, %[W], s60 offen offset:1152\n\t" \
  "buffer_load_dwordx4 v[88:91], v41, %[W], s60 offen offset:2304\n\t" \
  "buffer_load_dwordx4 v[92:95], v41, %[W], s60 offen offset:3456\n\t" \
  "buffer_load_dwordx4 v[96:99], v41, %[W], s61 offen offset:0\n\t" \
  "buffer_load_dwordx4 v[100:103], v41, %[W], s61 offen offset:1152\n\t" \
  "buffer_load_dwordx4 v[104:107], v41, %[W], s61 offen offset:2304\n\t" \
  "buffer_load_dwordx4 v[108:111], v41, %[W], s61 offen offset:3456\n\t" \
  "s_waitcnt vmcnt(38)\n\t" \
  "v_perm_b32 v44, v116, v112, s62\n\t" \
  "s_waitcnt vmcnt(36)\n\t" \
  "v_perm_b32 v45, v124, v120, s62\n\t" \
  "s_waitcnt vmcnt(34)\n\t" \
  "v_perm_b32 v46, v132, v128, s62\n\t" \
  "s_waitcnt vmcnt(32)\n\t" \
  "v_perm_b32 v47, v140, v136, s62\n\t" \
  "v_accvgpr_write_b32 a64, v44\n\t" \
  "v_accvgpr_write_b32 a65, v45\n\t" \
  "v_accvgpr_write_b32 a66, v46\n\t" \
  "v_accvgpr_write_b32 a67, v47\n\t" \
  "v_perm_b32 v44, v116, v112, s63\n\t" \
  "v_perm_b32 v45, v124, v120, s63\n\t" \
  "v_perm_b32 v46, v132, v128, s63\n\t" \
  "v_perm_b32 v47, v140, v136, s63\n\t" \
  "v_accvgpr_write_b32 a68, v44\n\t" \
  "v_accvgpr_write_b32 a69, v45\n\t" \
  "v_accvgpr_write_b32 a70, v46\n\t" \
  "v_accvgpr_write_b32 a71, v47\n\t" \
  "v_perm_b32 v44, v117, v113, s62\n\t" \
  "v_perm_b32 v45, v125, v121, s62\n\t" \
  "v_perm_b32 v46, v133, v129, s62\n\t" \
  "v_perm_b32 v47, v141, v137, s62\n\t" \
  "v_accvgpr_write_b32 a72, v44\n\t" \
  "v_accvgpr_write_b32 a73, v45\n\t" \
  "v_accvgpr_write_b32 a74, v46\n\t" \
  "v_accvgpr_write_b32 a75, v47\n\t" \
  "v_perm_b32 v44, v117, v113, s63\n\t" \
  "v_perm_b32 v45, v125, v121, s63\n\t" \
  "v_perm_b32 v46, v133, v129, s63\n\t" \
  "v_perm_b32 v47, v141, v137, s63\n\t" \
  "v_accvgpr_write_b32 a76, v44\n\t" \
  "v_accvgpr_write_b32 a77, v45\n\t" \
  "v_accvgpr_write_b32 a78, v46\n\t" \
  "v_accvgpr_write_b32 a79, v47\n\t" \
  "v_perm_b32 v44, v118, v114, s62\n\t" \
  "v_perm_b32 v45, v126, v122, s62\n\t" \
  "v_perm_b32 v46, v134, v130, s62\n\t" \
  "v_perm_b32 v47, v142, v138, s62\n\t" \
  "v_accvgpr_write_b32 a80, v44\n\t" \
  "v_accvgpr_write_b32 a81, v45\n\t" \
  "v_accvgpr_write_b32 a82, v46\n\t" \
  "v_accvgpr_write_b32 a83, v47\n\t" \
  "v_perm_b32 v44, v118, v114, s63\n\t" \
  "v_perm_b32 v45, v126, v122, s63\n\t" \
  "v_perm_b32 v46, v134, v130, s63\n\t" \
  "v_perm_b32 v47, v142, v138, s63\n\t" \
  "v_accvgpr_write_b32 a84, v44\n\t" \
  "v_accvgpr_write_b32 a85, v45\n\t" \
  "v_accvgpr_write_b32 a86, v46\n\t" \
  "v_accvgpr_write_b32 a87, v47\n\t" \
  "v_perm_b32 v44, v119, v115, s62\n\t" \
  "v_perm_b32 v45, v127, v123, s62\n\t" \
  "v_perm_b32 v46, v135, v131, s62\n\t" \
  "v_perm_b32 v47, v143, v139, s62\n\t" \
  "v_accvgpr_write_b32 a88, v44\n\t" \
  "v_accvgpr_write_b32 a89, v45\n\t" \
  "v_accvgpr_write_b32 a90, v46\n\t" \
  "v_accvgpr_write_b32 a91, v47\n\t" \
  "v_perm_b32 v44, v119, v115, s63\n\t" \
  "v_perm_b32 v45, v127, v123, s63\n\t" \
  "v_perm_b32 v46, v135, v131, s63\n\t" \
  "v_perm_b32 v47, v143, v139, s63\n\t" \
  "v_accvgpr_write_b32 a92, v44\n\t" \
  "v_accvgpr_write_b32 a93, v45\n\t" \
  "v_accvgpr_write_b32 a94, v46\n\t" \
  "v_accvgpr_write_b32 a95, v47\n\t" \
  "s_xor_b32 s60, s66, 147456\n\t" \
  "s_add_u32 s60, s60, 110592\n\t" \
  "s_add_u32 s61, s60, 18432\n\t" \
  "buffer_load_dwordx4 v[112:115], v41, %[W], s60 offen offset:0\n\t" \
  "buffer_load_dwordx4 v[116:119], v41, %[W], s60 offen offset:1152\n\t" \
  "buffer_load_dwordx4 v[120:123], v41, %[W], s60 offen offset:2304\n\t" \
  "buffer_load_dwordx4 v[124:127], v41, %[W], s60 offen offset:3456\n\t" \
  "buffer_load_dwordx4 v[128:131], v41, %[W], s61 offen offset:0\n\t" \
  "buffer_load_dwordx4 v[132:135], v41, %[W], s61 offen offset:1152\n\t" \
  "buffer_load_dwordx4 v[136:139], v41, %[W], s61 offen offset:2304\n\t" \
  "buffer_load_dwordx4 v[140:143], v41, %[W], s61 offen offset:3456\n\t" \
  "s_waitcnt vmcnt(38)\n\t" \
  "v_perm_b32 v44, v148, v144, s62\n\t" \
  "s_waitcnt vmcnt(36)\n\t" \
  "v_perm_b32 v45, v156, v152, s62\n\t" \
  "s_waitcnt vmcnt(34)\n\t" \
  "v_perm_b32 v46, v164, v160, s62\n\t" \
  "s_waitcnt vmcnt(32)\n\t" \
  "v_perm_b32 v47, v172, v168, s62\n\t" \
  "v_accvgpr_write_b32 a96, v44\n\t" \
  "v_accvgpr_write_b32 a97, v45\n\t" \
  "v_accvgpr_write_b32 a98, v46\n\t" \
  "v_accvgpr_write_b32 a99, v47\n\t" \
  "v_perm_b32 v44, v148, v144, s63\n\t" \
  "v_perm_b32 v45, v156, v152, s63\n\t" \
  "v_perm_b32 v46, v164, v160, s63\n\t" \
  "v_perm_b32 v47, v172, v168, s63\n\t" \
  "v_accvgpr_write_b32 a100, v44\n\t" \
  "v_accvgpr_write_b32 a101, v45\n\t" \
  "v_accvgpr_write_b32 a102, v46\n\t" \
  "v_accvgpr_write_b32 a103, v47\n\t" \
  "v_perm_b32 v44, v149, v145, s62\n\t" \
  "v_perm_b32 v45, v157, v153, s62\n\t" \
  "v_perm_b32 v46, v165, v161, s62\n\t" \
  "v_perm_b32 v47, v173, v169, s62\n\t" \
  "v_accvgpr_write_b32 a104, v44\n\t" \
  "v_accvgpr_write_b32 a105, v45\n\t" \
  "v_accvgpr_write_b32 a106, v46\n\t" \
  "v_accvgpr_write_b32 a107, v47\n\t" \
  "v_perm_b32 v44, v149, v145, s63\n\t" \
  "v_perm_b32 v45, v157, v153, s63\n\t" \
  "v_perm_b32 v46, v165, v161, s63\n\t" \
  "v_perm_b32 v47, v173, v169, s63\n\t" \
  "v_accvgpr_write_b32 a108, v44\n\t" \
  "v_accvgpr_write_b32 a109, v45\n\t" \
  "v_accvgpr_write_b32 a110, v46\n\t" \
  "v_accvgpr_write_b32 a111, v47\n\t" \
  "v_perm_b32 v44, v150, v146, s62\n\t" \
  "v_perm_b32 v45, v158, v154, s62\n\t" \
  "v_perm_b32 v46, v166, v162, s62\n\t" \
  "v_perm_b32 v47, v174, v170, s62\n\t" \
  "v_accvgpr_write_b32 a112, v44\n\t" \
  "v_accvgpr_write_b32 a113, v45\n\t" \
  "v_accvgpr_write_b32 a114, v46\n\t" \
  "v_accvgpr_write_b32 a115, v47\n\t" \
  "v_perm_b32 v44, v150, v146, s63\n\t" \
  "v_perm_b32 v45, v158, v154, s63\n\t" \
  "v_perm_b32 v46, v166, v162, s63\n\t" \
  "v_perm_b32 v47, v174, v170, s63\n\t" \
  "v_accvgpr_write_b32 a116, v44\n\t" \
  "v_accvgpr_write_b32 a117, v45\n\t" \
  "v_accvgpr_write_b32 a118, v46\n\t" \
  "v_accvgpr_write_b32 a119, v47\n\t" \
  "v_perm_b32 v44, v151, v147, s62\n\t" \
  "v_perm_b32 v45, v159, v155, s62\n\t" \
  "v_perm_b32 v46, v167, v163, s62\n\t" \
  "v_perm_b32 v47, v175, v171, s62\n\t" \
  "v_accvgpr_write_b32 a120, v44\n\t" \
  "v_accvgpr_write_b32 a121, v45\n\t" \
  "v_accvgpr_write_b32 a122, v46\n\t" \
  "v_accvgpr_write_b32 a123, v47\n\t" \
  "v_perm_b32 v44, v151, v147, s63\n\t" \
  "v_perm_b32 v45, v159, v155, s63\n\t" \
  "v_perm_b32 v46, v167, v163, s63\n\t" \
  "v_perm_b32 v47, v175, v171, s63\n\t" \
  "v_accvgpr_write_b32 a124, v44\n\t" \
  "v_accvgpr_write_b32 a125, v45\n\t" \
  "v_accvgpr_write_b32 a126, v46\n\t" \
  "v_accvgpr_write_b32 a127, v47\n\t" \
  "s_xor_b32 s60, s66, 0\n\t" \
  "s_add_u32 s60, s60, 0\n\t" \
  "s_add_u32 s61, s60, 18432\n\t" \
  "buffer_load_ushort v224, v42, %[W], s60 offen offset:0\n\t" \
  "buffer_load_ushort v144, v42, %[W], s60 offen offset:1152\n\t" \
  "buffer_load_ushort v225, v42, %[W], s60 offen offset:2304\n\t" \
  "buffer_load_ushort v145, v42, %[W], s60 offen offset:3456\n\t" \
  "buffer_load_ushort v226, v42, %[W], s61 offen offset:0\n\t" \
  "buffer_load_ushort v146, v42, %[W], s61 offen offset:1152\n\t" \
  "buffer_load_ushort v227, v42, %[W], s61 offen offset:2304\n\t" \
  "buffer_load_ushort v147, v42, %[W], s61 offen offset:3456\n\t" \
  "s_xor_b32 s60, s66, 0\n\t" \
  "s_add_u32 s60, s60, 36864\n\t" \
  "s_add_u32 s61, s60, 18432\n\t" \
  "buffer_load_ushort v228, v42, %[W], s60 offen offset:0\n\t" \
  "buffer_load_ushort v148, v42, %[W], s60 offen offset:1152\n\t" \
  "buffer_load_ushort v229, v42, %[W], s60 offen offset:2304\n\t" \
  "buffer_load_ushort v149, v42, %[W], s60 offen offset:3456\n\t" \
  "buffer_load_ushort v230, v42, %[W], s61 offen offset:0\n\t" \
  "buffer_load_ushort v150, v42, %[W], s61 offen offset:1152\n\t" \
  "buffer_load_ushort v231, v42, %[W], s61 offen offset:2304\n\t" \
  "buffer_load_ushort v151, v42, %[W], s61 offen offset:3456\n\t" \
  "s_waitcnt vmcnt(46)\n\t" \
  "v_perm_b32 v44, v180, v176, s62\n\t" \
  "s_waitcnt vmcnt(44)\n\t" \
  "v_perm_b32 v45, v188, v184, s62\n\t" \
  "s_waitcnt vmcnt(42)\n\t" \
  "v_perm_b32 v46, v196, v192, s62\n\t" \
  "s_waitcnt vmcnt(40)\n\t" \
  "v_perm_b32 v47, v204, v200, s62\n\t" \
  "v_accvgpr_write_b32 a128, v44\n\t" \
  "v_accvgpr_write_b32 a129, v45\n\t" \
  "v_accvgpr_write_b32 a130, v46\n\t" \
  "v_accvgpr_write_b32 a131, v47\n\t" \
  "v_perm_b32 v44, v180, v176, s63\n\t" \
  "v_perm_b32 v45, v188, v184, s63\n\t" \
  "v_perm_b32 v46, v196, v192, s63\n\t" \
  "v_perm_b32 v47, v204, v200, s63\n\t" \
  "v_accvgpr_write_b32 a132, v44\n\t" \
  "v_accvgpr_write_b32 a133, v45\n\t" \
  "v_accvgpr_write_b32 a134, v46\n\t" \
  "v_accvgpr_write_b32 a135, v47\n\t" \
  "v_perm_b32 v44, v181, v177, s62\n\t" \
  "v_perm_b32 v45, v189, v185, s62\n\t" \
  "v_perm_b32 v46, v197, v193, s62\n\t" \
  "v_perm_b32 v47, v205, v201, s62\n\t" \
  "v_accvgpr_write_b32 a136, v44\n\t" \
  "v_accvgpr_write_b32 a137, v45\n\t" \
  "v_accvgpr_write_b32 a138, v46\n\t" \
  "v_accvgpr_write_b32 a139, v47\n\t" \
  "v_perm_b32 v44, v181, v177, s63\n\t" \
  "v_perm_b32 v45, v189, v185, s63\n\t" \
  "v_perm_b32 v46, v197, v193, s63\n\t" \
  "v_perm_b32 v47, v205, v201, s63\n\t" \
  "v_accvgpr_write_b32 a140, v44\n\t" \
  "v_accvgpr_write_b32 a141, v45\n\t" \
  "v_accvgpr_write_b32 a142, v46\n\t" \
  "v_accvgpr_write_b32 a143, v47\n\t" \
  "v_perm_b32 v44, v182, v178, s62\n\t" \
  "v_perm_b32 v45, v190, v186, s62\n\t" \
  "v_perm_b32 v46, v198, v194, s62\n\t" \
  "v_perm_b32 v47, v206, v202, s62\n\t" \
  "v_accvgpr_write_b32 a144, v44\n\t" \
  "v_accvgpr_write_b32 a145, v45\n\t" \
  "v_accvgpr_write_b32 a146, v46\n\t" \
  "v_accvgpr_write_b32 a147, v47\n\t" \
  "v_perm_b32 v44, v182, v178, s63\n\t" \
  "v_perm_b32 v45, v190, v186, s63\n\t" \
  "v_perm_b32 v46, v198, v194, s63\n\t" \
  "v_perm_b32 v47, v206, v202, s63\n\t" \
  "v_accvgpr_write_b32 a148, v44\n\t" \
  "v_accvgpr_write_b32 a149, v45\n\t" \
  "v_accvgpr_write_b32 a150, v46\n\t" \
  "v_accvgpr_write_b32 a151, v47\n\t" \
  "v_perm_b32 v44, v183, v179, s62\n\t" \
  "v_perm_b32 v45, v191, v187, s62\n\t" \
  "v_perm_b32 v46, v199, v195, s62\n\t" \
  "v_perm_b32 v47, v207, v203, s62\n\t" \
  "v_accvgpr_write_b32 a152, v44\n\t" \
  "v_accvgpr_write_b32 a153, v45\n\t" \
  "v_accvgpr_write_b32 a154, v46\n\t" \
  "v_accvgpr_write_b32 a155, v47\n\t" \
  "v_perm_b32 v44, v183, v179, s63\n\t" \
  "v_perm_b32 v45, v191, v187, s63\n\t" \
  "v_perm_b32 v46, v199, v195, s63\n\t" \
  "v_perm_b32 v47, v207, v203, s63\n\t" \
  "v_accvgpr_write_b32 a156, v44\n\t" \
  "v_accvgpr_write_b32 a157, v45\n\t" \
  "v_accvgpr_write_b32 a158, v46\n\t" \
  "v_accvgpr_write_b32 a159, v47\n\t" \
  "s_xor_b32 s60, s66, 0\n\t" \
  "s_add_u32 s60, s60, 73728\n\t" \
  "s_add_u32 s61, s60, 18432\n\t" \
  "buffer_load_ushort v232, v42, %[W], s60 offen offset:0\n\t" \
  "buffer_load_ushort v152, v42, %[W], s60 offen offset:1152\n\t" \
  "buffer_load_ushort v233, v42, %[W], s60 offen offset:2304\n\t" \
  "buffer_load_ushort v153, v42, %[W], s60 offen offset:3456\n\t" \
  "buffer_load_ushort v234, v42, %[W], s61 offen offset:0\n\t" \
  "buffer_load_ushort v154, v42, %[W], s61 offen offset:1152\n\t" \
  "buffer_load_ushort v235, v42, %[W], s61 offen offset:2304\n\t" \
  "buffer_load_ushort v155, v42, %[W], s61 offen offset:3456\n\t" \
  "s_xor_b32 s60, s66, 0\n\t" \
  "s_add_u32 s60, s60, 110592\n\t" \
  "s_add_u32 s61, s60, 18432\n\t" \
  "buffer_load_ushort v236, v42, %[W], s60 offen offset:0\n\t" \
  "buffer_load_ushort v156, v42, %[W], s60 offen offset:1152\n\t" \
  "buffer_load_ushort v237, v42, %[W], s60 offen offset:2304\n\t" \
  "buffer_load_ushort v157, v42, %[W], s60 offen offset:3456\n\t" \
  "buffer_load_ushort v238, v42, %[W], s61 offen offset:0\n\t" \
  "buffer_load_ushort v158, v42, %[W], s61 offen offset:1152\n\t" \
  "buffer_load_ushort v239, v42, %[W], s61 offen offset:2304\n\t" \
  "buffer_load_ushort v159, v42, %[W], s61 offen offset:3456\n\t" \
  "s_waitcnt vmcnt(54)\n\t" \
  "v_perm_b32 v44, v52, v48, s62\n\t" \
  "s_waitcnt vmcnt(52)\n\t" \
  "v_perm_b32 v45, v60, v56, s62\n\t" \
  "s_waitcnt vmcnt(50)\n\t" \
  "v_perm_b32 v46, v68, v64, s62\n\t" \
  "s_waitcnt vmcnt(48)\n\t" \
  "v_perm_b32 v47, v76, v72, s62\n\t" \
  "v_accvgpr_write_b32 a160, v44\n\t" \
  "v_accvgpr_write_b32 a161, v45\n\t" \
  "v_accvgpr_write_b32 a162, v46\n\t" \
  "v_accvgpr_write_b32 a163, v47\n\t" \
  "v_perm_b32 v44, v52, v48, s63\n\t" \
  "v_perm_b32 v45, v60, v56, s63\n\t" \
  "v_perm_b32 v46, v68, v64, s63\n\t" \
  "v_perm_b32 v47, v76, v72, s63\n\t" \
  "v_accvgpr_write_b32 a164, v44\n\t" \
  "v_accvgpr_write_b32 a165, v45\n\t" \
  "v_accvgpr_write_b32 a166, v46\n\t" \
  "v_accvgpr_write_b32 a167, v47\n\t" \
  "v_perm_b32 v44, v53, v49, s62\n\t" \
  "v_perm_b32 v45, v61, v57, s62\n\t" \
  "v_perm_b32 v46, v69, v65, s62\n\t" \
  "v_perm_b32 v47, v77, v73, s62\n\t" \
  "v_accvgpr_write_b32 a168, v44\n\t" \
  "v_accvgpr_write_b32 a169, v45\n\t" \
  "v_accvgpr_write_b32 a170, v46\n\t" \
  "v_accvgpr_write_b32 a171, v47\n\t" \
  "v_perm_b32 v44, v53, v49, s63\n\t" \
  "v_perm_b32 v45, v61, v57, s63\n\t" \
  "v_perm_b32 v46, v69, v65, s63\n\t" \
  "v_perm_b32 v47, v77, v73, s63\n\t" \
  "v_accvgpr_write_b32 a172, v44\n\t" \
  "v_accvgpr_write_b32 a173, v45\n\t" \
  "v_accvgpr_write_b32 a174, v46\n\t" \
  "v_accvgpr_write_b32 a175, v47\n\t" \
  "v_perm_b32 v44, v54, v50, s62\n\t" \
  "v_perm_b32 v45, v62, v58, s62\n\t" \
  "v_perm_b32 v46, v70, v66, s62\n\t" \
  "v_perm_b32 v47, v78, v74, s62\n\t" \
  "v_accvgpr_write_b32 a176, v44\n\t" \
  "v_accvgpr_write_b32 a177, v45\n\t" \
  "v_accvgpr_write_b32 a178, v46\n\t" \
  "v_accvgpr_write_b32 a179, v47\n\t" \
  "v_perm_b32 v44, v54, v50, s63\n\t" \
  "v_perm_b32 v45, v62, v58, s63\n\t" \
  "v_perm_b32 v46, v70, v66, s63\n\t" \
  "v_perm_b32 v47, v78, v74, s63\n\t" \
  "v_accvgpr_write_b32 a180, v44\n\t" \
  "v_accvgpr_write_b32 a181, v45\n\t" \
  "v_accvgpr_write_b32 a182, v46\n\t" \
  "v_accvgpr_write_b32 a183, v47\n\t" \
  "v_perm_b32 v44, v55, v51, s62\n\t" \
  "v_perm_b32 v45, v63, v59, s62\n\t" \
  "v_perm_b32 v46, v71, v67, s62\n\t" \
  "v_perm_b32 v47, v79, v75, s62\n\t" \
  "v_accvgpr_write_b32 a184, v44\n\t" \
  "v_accvgpr_write_b32 a185, v45\n\t" \
  "v_accvgpr_write_b32 a186, v46\n\t" \
  "v_accvgpr_write_b32 a187, v47\n\t" \
  "v_perm_b32 v44, v55, v51, s63\n\t" \
  "v_perm_b32 v45, v63, v59, s63\n\t" \
  "v_perm_b32 v46, v71, v67, s63\n\t" \
  "v_perm_b32 v47, v79, v75, s63\n\t" \
  "v_accvgpr_write_b32 a188, v44\n\t" \
  "v_accvgpr_write_b32 a189, v45\n\t" \
  "v_accvgpr_write_b32 a190, v46\n\t" \
  "v_accvgpr_write_b32 a191, v47\n\t" \
  "s_xor_b32 s60, s66, 147456\n\t" \
  "s_add_u32 s60, s60, 0\n\t" \
  "s_add_u32 s61, s60, 18432\n\t" \
  "buffer_load_ushort v240, v42, %[W], s60 offen offset:0\n\t" \
  "buffer_load_ushort v160, v42, %[W], s60 offen offset:1152\n\t" \
  "buffer_load_ushort v241, v42, %[W], s60 offen offset:2304\n\t" \
  "buffer_load_ushort v161, v42, %[W], s60 offen offset:3456\n\t" \
  "buffer_load_ushort v242, v42, %[W], s61 offen offset:0\n\t" \
  "buffer_load_ushort v162, v42, %[W], s61 offen offset:1152\n\t" \
  "buffer_load_ushort v243, v42, %[W], s61 offen offset:2304\n\t" \
  "buffer_load_ushort v163, v42, %[W], s61 offen offset:3456\n\t" \
  "s_waitcnt vmcnt(54)\n\t" \
  "v_perm_b32 v44, v84, v80, s62\n\t" \
  "s_waitcnt vmcnt(52)\n\t" \
  "v_perm_b32 v45, v92, v88, s62\n\t" \
  "s_waitcnt vmcnt(50)\n\t" \
  "v_perm_b32 v46, v100, v96, s62\n\t" \
  "s_waitcnt vmcnt(48)\n\t" \
  "v_perm_b32 v47, v108, v104, s62\n\t" \
  "v_accvgpr_write_b32 a192, v44\n\t" \
  "v_accvgpr_write_b32 a193, v45\n\t" \
  "v_accvgpr_write_b32 a194, v46\n\t" \
  "v_accvgpr_write_b32 a195, v47\n\t" \
  "v_perm_b32 v44, v84, v80, s63\n\t" \
  "v_perm_b32 v45, v92, v88, s63\n\t" \
  "v_perm_b32 v46, v100, v96, s63\n\t" \
  "v_perm_b32 v47, v108, v104, s63\n\t" \
  "v_accvgpr_write_b32 a196, v44\n\t" \
  "v_accvgpr_write_b32 a197, v45\n\t" \
  "v_accvgpr_write_b32 a198, v46\n\t" \
  "v_accvgpr_write_b32 a199, v47\n\t" \
  "v_perm_b32 v44, v85, v81, s62\n\t" \
  "v_perm_b32 v45, v93, v89, s62\n\t" \
  "v_perm_b32 v46, v101, v97, s62\n\t" \
  "v_perm_b32 v47, v109, v105, s62\n\t" \
  "v_accvgpr_write_b32 a200, v44\n\t" \
  "v_accvgpr_write_b32 a201, v45\n\t" \
  "v_accvgpr_write_b32 a202, v46\n\t" \
  "v_accvgpr_write_b32 a203, v47\n\t" \
  "v_perm_b32 v44, v85, v81, s63\n\t" \
  "v_perm_b32 v45, v93, v89, s63\n\t" \
  "v_perm_b32 v46, v101, v97, s63\n\t" \
  "v_perm_b32 v47, v109, v105, s63\n\t" \
  "v_accvgpr_write_b32 a204, v44\n\t" \
  "v_accvgpr_write_b32 a205, v45\n\t" \
  "v_accvgpr_write_b32 a206, v46\n\t" \
  "v_accvgpr_write_b32 a207, v47\n\t" \
  "v_perm_b32 v44, v86, v82, s62\n\t" \
  "v_perm_b32 v45, v94, v90, s62\n\t" \
  "v_perm_b32 v46, v102, v98, s62\n\t" \
  "v_perm_b32 v47, v110, v106, s62\n\t" \
  "v_accvgpr_write_b32 a208, v44\n\t" \
  "v_accvgpr_write_b32 a209, v45\n\t" \
  "v_accvgpr_write_b32 a210, v46\n\t" \
  "v_accvgpr_write_b32 a211, v47\n\t" \
  "v_perm_b32 v44, v86, v82, s63\n\t" \
  "v_perm_b32 v45, v94, v90, s63\n\t" \
  "v_perm_b32 v46, v102, v98, s63\n\t" \
  "v_perm_b32 v47, v110, v106, s63\n\t" \
  "v_accvgpr_write_b32 a212, v44\n\t" \
  "v_accvgpr_write_b32 a213, v45\n\t" \
  "v_accvgpr_write_b32 a214, v46\n\t" \
  "v_accvgpr_write_b32 a215, v47\n\t" \
  "v_perm_b32 v44, v87, v83, s62\n\t" \
  "v_perm_b32 v45, v95, v91, s62\n\t" \
  "v_perm_b32 v46, v103, v99, s62\n\t" \
  "v_perm_b32 v47, v111, v107, s62\n\t" \
  "v_accvgpr_write_b32 a216, v44\n\t" \
  "v_accvgpr_write_b32 a217, v45\n\t" \
  "v_accvgpr_write_b32 a218, v46\n\t" \
  "v_accvgpr_write_b32 a219, v47\n\t" \
  "v_perm_b32 v44, v87, v83, s63\n\t" \
  "v_perm_b32 v45, v95, v91, s63\n\t" \
  "v_perm_b32 v46, v103, v99, s63\n\t" \
  "v_perm_b32 v47, v111, v107, s63\n\t" \
  "v_accvgpr_write_b32 a220, v44\n\t" \
  "v_accvgpr_write_b32 a221, v45\n\t" \
  "v_accvgpr_write_b32 a222, v46\n\t" \
  "v_accvgpr_write_b32 a223, v47\n\t" \
  "s_xor_b32 s60, s66, 147456\n\t" \
  "s_add_u32 s60, s60, 36864\n\t" \
  "s_add_u32 s61, s60, 18432\n\t" \
  "buffer_load_ushort v244, v42, %[W], s60 offen offset:0\n\t" \
  "buffer_load_ushort v164, v42, %[W], s60 offen offset:1152\n\t" \
  "buffer_load_ushort v245, v42, %[W], s60 offen offset:2304\n\t" \
  "buffer_load_ushort v165, v42, %[W], s60 offen offset:3456\n\t" \
  "buffer_load_ushort v246, v42, %[W], s61 offen offset:0\n\t" \
  "buffer_load_ushort v166, v42, %[W], s61 offen offset:1152\n\t" \
  "buffer_load_ushort v247, v42, %[W], s61 offen offset:2304\n\t" \
  "buffer_load_ushort v167, v42, %[W], s61 offen offset:3456\n\t" \
  "s_waitcnt vmcnt(54)\n\t" \
  "v_perm_b32 v44, v116, v112, s62\n\t" \
  "s_waitcnt vmcnt(52)\n\t" \
  "v_perm_b32 v45, v124, v120, s62\n\t" \
  "s_waitcnt vmcnt(50)\n\t" \
  "v_perm_b32 v46, v132, v128, s62\n\t" \
  "s_waitcnt vmcnt(48)\n\t" \
  "v_perm_b32 v47, v140, v136, s62\n\t" \
  "v_accvgpr_write_b32 a224, v44\n\t" \
  "v_accvgpr_write_b32 a225, v45\n\t" \
  "v_accvgpr_write_b32 a226, v46\n\t" \
  "v_accvgpr_write_b32 a227, v47\n\t" \
  "v_perm_b32 v44, v116, v112, s63\n\t" \
  "v_perm_b32 v45, v124, v120, s63\n\t" \
  "v_perm_b32 v46, v132, v128, s63\n\t" \
  "v_perm_b32 v47, v140, v136, s63\n\t" \
  "v_accvgpr_write_b32 a228, v44\n\t" \
  "v_accvgpr_write_b32 a229, v45\n\t" \
  "v_accvgpr_write_b32 a230, v46\n\t" \
  "v_accvgpr_write_b32 a231, v47\n\t" \
  "v_perm_b32 v44, v117, v113, s62\n\t" \
  "v_perm_b32 v45, v125, v121, s62\n\t" \
  "v_perm_b32 v46, v133, v129, s62\n\t" \
  "v_perm_b32 v47, v141, v137, s62\n\t" \
  "v_accvgpr_write_b32 a232, v44\n\t" \
  "v_accvgpr_write_b32 a233, v45\n\t" \
  "v_accvgpr_write_b32 a234, v46\n\t" \
  "v_accvgpr_write_b32 a235, v47\n\t" \
  "v_perm_b32 v44, v117, v113, s63\n\t" \
  "v_perm_b32 v45, v125, v121, s63\n\t" \
  "v_perm_b32 v46, v133, v129, s63\n\t" \
  "v_perm_b32 v47, v141, v137, s63\n\t" \
  "v_accvgpr_write_b32 a236, v44\n\t" \
  "v_accvgpr_write_b32 a237, v45\n\t" \
  "v_accvgpr_write_b32 a238, v46\n\t" \
  "v_accvgpr_write_b32 a239, v47\n\t" \
  "v_perm_b32 v44, v118, v114, s62\n\t" \
  "v_perm_b32 v45, v126, v122, s62\n\t" \
  "v_perm_b32 v46, v134, v130, s62\n\t" \
  "v_perm_b32 v47, v142, v138, s62\n\t" \
  "v_accvgpr_write_b32 a240, v44\n\t" \
  "v_accvgpr_write_b32 a241, v45\n\t" \
  "v_accvgpr_write_b32 a242, v46\n\t" \
  "v_accvgpr_write_b32 a243, v47\n\t" \
  "v_perm_b32 v44, v118, v114, s63\n\t" \
  "v_perm_b32 v45, v126, v122, s63\n\t" \
  "v_perm_b32 v46, v134, v130, s63\n\t" \
  "v_perm_b32 v47, v142, v138, s63\n\t" \
  "v_accvgpr_write_b32 a244, v44\n\t" \
  "v_accvgpr_write_b32 a245, v45\n\t" \
  "v_accvgpr_write_b32 a246, v46\n\t" \
  "v_accvgpr_write_b32 a247, v47\n\t" \
  "v_perm_b32 v44, v119, v115, s62\n\t" \
  "v_perm_b32 v45, v127, v123, s62\n\t" \
  "v_perm_b32 v46, v135, v131, s62\n\t" \
  "v_perm_b32 v47, v143, v139, s62\n\t" \
  "v_accvgpr_write_b32 a248, v44\n\t" \
  "v_accvgpr_write_b32 a249, v45\n\t" \
  "v_accvgpr_write_b32 a250, v46\n\t" \
  "v_accvgpr_write_b32 a251, v47\n\t" \
  "v_perm_b32 v44, v119, v115, s63\n\t" \
  "v_perm_b32 v45, v127, v123, s63\n\t" \
  "v_perm_b32 v46, v135, v131, s63\n\t" \
  "v_perm_b32 v47, v143, v139, s63\n\t" \
  "v_accvgpr_write_b32 a252, v44\n\t" \
  "v_accvgpr_write_b32 a253, v45\n\t" \
  "v_accvgpr_write_b32 a254, v46\n\t" \
  "v_accvgpr_write_b32 a255, v47\n\t" \
  "s_xor_b32 s60, s66, 147456\n\t" \
  "s_add_u32 s60, s60, 73728\n\t" \
  "s_add_u32 s61, s60, 18432\n\t" \
  "buffer_load_ushort v248, v42, %[W], s60 offen offset:0\n\t" \
  "buffer_load_ushort v168, v42, %[W], s60 offen offset:1152\n\t" \
  "buffer_load_ushort v249, v42, %[W], s60 offen offset:2304\n\t" \
  "buffer_load_ushort v169, v42, %[W], s60 offen offset:3456\n\t" \
  "buffer_load_ushort v250, v42, %[W], s61 offen offset:0\n\t" \
  "buffer_load_ushort v170, v42, %[W], s61 offen offset:1152\n\t" \
  "buffer_load_ushort v251, v42, %[W], s61 offen offset:2304\n\t" \
  "buffer_load_ushort v171, v42, %[W], s61 offen offset:3456\n\t" \
  "s_xor_b32 s60, s66, 147456\n\t" \
  "s_add_u32 s60, s60, 110592\n\t" \
  "s_add_u32 s61, s60, 18432\n\t" \
  "s_waitcnt vmcnt(40)\n\t" \
  "buffer_load_ushort v252, v42, %[W], s60 offen offset:0\n\t" \
  "buffer_load_ushort v172, v42, %[W], s60 offen offset:1152\n\t" \
  "buffer_load_ushort v253, v42, %[W], s60 offen offset:2304\n\t" \
  "buffer_load_ushort v173, v42, %[W], s60 offen offset:3456\n\t" \
  "buffer_load_ushort v254, v42, %[W], s61 offen offset:0\n\t" \
  "buffer_load_ushort v174, v42, %[W], s61 offen offset:1152\n\t" \
  "buffer_load_ushort v255, v42, %[W], s61 offen offset:2304\n\t" \
  "buffer_load_ushort v175, v42, %[W], s61 offen offset:3456\n\t" \
  "v_lshl_or_b32 v224, v144, 16, v224\n\t" \
  "v_lshl_or_b32 v225, v145, 16, v225\n\t" \
  "v_lshl_or_b32 v226, v146, 16, v226\n\t" \
  "v_lshl_or_b32 v227, v147, 16, v227\n\t" \
  "v_lshl_or_b32 v228, v148, 16, v228\n\t" \
  "v_lshl_or_b32 v229, v149, 16, v229\n\t" \
  "v_lshl_or_b32 v230, v150, 16, v230\n\t" \
  "v_lshl_or_b32 v231, v151, 16, v231\n\t" \
  "s_waitcnt vmcnt(46)\n\t" \
  "v_lshl_or_b32 v232, v152, 16, v232\n\t" \
  "s_waitcnt vmcnt(44)\n\t" \
  "v_lshl_or_b32 v233, v153, 16, v233\n\t" \
  "s_waitcnt vmcnt(42)\n\t" \
  "v_lshl_or_b32 v234, v154, 16, v234\n\t" \
  "s_waitcnt vmcnt(40)\n\t" \
  "v_lshl_or_b32 v235, v155, 16, v235\n\t" \
  "s_waitcnt vmcnt(38)\n\t" \
  "v_lshl_or_b32 v236, v156, 16, v236\n\t" \
  "s_waitcnt vmcnt(36)\n\t" \
  "v_lshl_or_b32 v237, v157, 16, v237\n\t" \
  "s_waitcnt vmcnt(34)\n\t" \
  "v_lshl_or_b32 v238, v158, 16, v238\n\t" \
  "s_waitcnt vmcnt(32)\n\t" \
  "v_lshl_or_b32 v239, v159, 16, v239\n\t" \
  "s_waitcnt vmcnt(30)\n\t" \
  "v_lshl_or_b32 v240, v160, 16, v240\n\t" \
  "s_waitcnt vmcnt(28)\n\t" \
  "v_lshl_or_b32 v241, v161, 16, v241\n\t" \
  "s_waitcnt vmcnt(26)\n\t" \
  "v_lshl_or_b32 v242, v162, 16, v242\n\t" \
  "s_waitcnt vmcnt(24)\n\t" \
  "v_lshl_or_b32 v243, v163, 16, v243\n\t" \
  "s_waitcnt vmcnt(22)\n\t" \
  "v_lshl_or_b32 v244, v164, 16, v244\n\t" \
  "s_waitcnt vmcnt(20)\n\t" \
  "v_lshl_or_b32 v245, v165, 16, v245\n\t" \
  "s_waitcnt vmcnt(18)\n\t" \
  "v_lshl_or_b32 v246, v166, 16, v246\n\t" \
  "s_waitcnt vmcnt(16)\n\t" \
  "v_lshl_or_b32 v247, v167, 16, v247\n\t" \
  "s_waitcnt vmcnt(14)\n\t" \
  "v_lshl_or_b32 v248, v168, 16, v248\n\t" \
  "s_waitcnt vmcnt(12)\n\t" \
  "v_lshl_or_b32 v249, v169, 16, v249\n\t" \
  "s_waitcnt vmcnt(10)\n\t" \
  "v_lshl_or_b32 v250, v170, 16, v250\n\t" \
  "s_waitcnt vmcnt(8)\n\t" \
  "v_lshl_or_b32 v251, v171, 16, v251\n\t" \
  "s_waitcnt vmcnt(6)\n\t" \
  "v_lshl_or_b32 v252, v172, 16, v252\n\t" \
  "s_waitcnt vmcnt(4)\n\t" \
  "v_lshl_or_b32 v253, v173, 16, v253\n\t" \
  "s_waitcnt vmcnt(2)\n\t" \
  "v_lshl_or_b32 v254, v174, 16, v254\n\t" \
  "s_waitcnt vmcnt(0)\n\t" \
  "v_lshl_or_b32 v255, v175, 16, v255\n\t" \
  "s_mov_b32 s84, " TK_CAP "\n\t" \
  "s_mov_b32 s48, 0\n\t" \
  "s_mov_b32 s49, 0xf0000000\n\t" \
  "s_mov_b32 s46, s45\n\t" \
  "v_mov_b32 v5, -1\n\t" \
  "v_lshrrev_b32 v13, 2, v0\n\t" \
  "s_mov_b32 s60, " TK_CTAB "\n\t" \
  "buffer_load_dword v10, v13, %[X], s60 offen offset:88 sc1\n\t" \
  "buffer_load_dword v13, v13, %[X], s60 offen offset:0 sc1\n\t" \
  "s_waitcnt vmcnt(0)\n\t" \
  "v_and_b32 v10, 1, v10\n\t" \
  "v_and_b32 v13, 1, v13\n\t" \
  "v_cmp_ne_u32 s[90:91], s67, v10\n\t" \
  "v_cmp_ne_u32 s[88:89], s67, v13\n\t" \
  "s_nop 1\n\t" \
  "s_mov_b32 s86, s88\n\t" \
  "v_lshrrev_b32 v13, 2, v0\n\t" \
  "s_mov_b32 s60, " TK_CTAB "\n\t" \
  "buffer_load_dword v10, v13, %[X], s60 offen offset:88 sc1\n\t" \
  "buffer_load_dword v13, v13, %[X], s60 offen offset:344 sc1\n\t" \
  "s_waitcnt vmcnt(0)\n\t" \
  "v_and_b32 v10, 1, v10\n\t" \
  "v_and_b32 v13, 1, v13\n\t" \
  "v_cmp_ne_u32 s[90:91], s67, v10\n\t" \
  "v_cmp_ne_u32 s[88:89], s67, v13\n\t" \
  "s_nop 1\n\t" \
  "s_and_b32 s60, s88, 256\n\t" \
  "s_lshl_b32 s60, s60, 5\n\t" \
  "s_add_u32 s44, s44, s60\n\t" \
  "s_add_u32 s83, s48, 0\n\t" \
  "s_lshr_b64 s[60:61], s[90:91], s83\n\t" \
  "s_and_b32 s60, s60, 1\n\t" \
  "s_lshl_b32 s60, s60, 13\n\t" \
  "s_add_u32 s83, s83, " TK_SG0 "\n\t" \
  "s_lshl_b32 s83, s83, 21\n\t" \
  "s_or_b32 s83, s83, s60\n\t" \
  "s_mov_b32 s81, s83\n\t" \
  "LCOLD_%=:\n\t" \
  "s_add_u32 s62, s83, s41\n\t" \
  "s_mov_b32 s61, 0\n\t" \
  "LPQ_%=:\n\t" \
  "buffer_load_dword v10, v9, %[X], s62 offen sc1\n\t" \
  "s_waitcnt vmcnt(0)\n\t" \
  "v_cmp_eq_u32 vcc, 0, v10\n\t" \
  "s_cbranch_vccz LPQ_%=x\n\t" \
  "s_add_u32 s61, s61, 1\n\t" \
  "s_sleep 4\n\t" \
  "s_cmp_gt_u32 s61, s84\n\t" \
  "s_cbranch_scc1 LPQ_%=g\n\t" \
  "s_and_b32 s60, s61, 1023\n\t" \
  "s_cmp_lg_u32 s60, 0\n\t" \
  "s_cbranch_scc1 LPQ_%=\n\t" \
  "buffer_load_dword v10, off, %[X], s92 sc1\n\t" \
  "s_waitcnt vmcnt(0)\n\t" \
  "v_readfirstlane_b32 s60, v10\n\t" \
  "s_nop 0\n\t" \
  "s_cmp_eq_u32 s60, 0\n\t" \
  "s_cbranch_scc1 LPQ_%=\n\t" \
  "LPQ_%=g:\n\t" \
  "s_mov_b32 s84, 0\n\t" \
  "s_or_b32 s60, s48, 1073741824\n\t" \
  "v_mov_b32 v10, s60\n\t" \
  "s_mov_b64 exec, 1\n\t" \
  "buffer_store_dword v10, off, %[X], s92 sc1\n\t" \
  "s_mov_b64 exec, -1\n\t" \
  "LPQ_%=x:\n\t" \
  "s_add_u32 m0, s46, 0\n\t" \
  "s_add_u32 s47, s81, s36\n\t" \
  "buffer_load_dwordx4 v0, %[X], s47 offen offset:0 sc1 lds\n\t" \
  "buffer_load_dwordx4 v0, %[X], s47 offen offset:1024 sc1 lds\n\t" \
  "buffer_load_dwordx4 v0, %[X], s47 offen offset:2048 sc1 lds\n\t" \
  "buffer_load_dwordx4 v0, %[X], s47 offen offset:3072 sc1 lds\n\t" \
  "s_add_u32 m0, s46, 4096\n\t" \
  "s_add_u32 s47, s81, s37\n\t" \
  "buffer_load_dwordx4 v0, %[X], s47 offen offset:0 sc1 lds\n\t" \
  "buffer_load_dwordx4 v0, %[X], s47 offen offset:1024 sc1 lds\n\t" \
  "buffer_load_dwordx4 v0, %[X], s47 offen offset:2048 sc1 lds\n\t" \
  "buffer_load_dwordx4 v0, %[X], s47 offen offset:3072 sc1 lds\n\t" \
  "s_add_u32 m0, s46, 8192\n\t" \
  "s_add_u32 s47, s81, s38\n\t" \
  "buffer_load_dwordx4 v0, %[X], s47 offen offset:0 sc1 lds\n\t" \
  "buffer_load_dwordx4 v0, %[X], s47 offen offset:1024 sc1 lds\n\t" \
  "buffer_load_dwordx4 v0, %[X], s47 offen offset:2048 sc1 lds\n\t" \
  "buffer_load_dwordx4 v0, %[X], s47 offen offset:3072 sc1 lds\n\t" \
  "s_waitcnt vmcnt(0)\n\t" \
  "LTOP_%=:\n\t" \
  "s_memrealtime s[78:79]\n\t" \
  "v_readfirstlane_b32 s76, v5\n\t" \
  "s_waitcnt lgkmcnt(0)\n\t" \
  "s_add_u32 s60, s76, 1\n\t" \
  "s_not_b32 s76, s76\n\t" \
  "s_cmp_gt_u32 s60, 1\n\t" \
  "s_cselect_b32 s61, -1, 0\n\t" \
  "s_lshl_b32 s60, s78, 8\n\t" \
  "s_sub_u32 s76, s76, s60\n\t" \
  "s_ashr_i32 s76, s76, 8\n\t" \
  "s_add_i32 s76, s76, s77\n\t" \
  "s_min_i32 s76, s76, 400\n\t" \
  "s_and_b32 s76, s76, s61\n\t" \
  "s_add_u32 s57, s78, s76\n\t" \
  "s_barrier\n\t" \
  "ds_read_b128 v[48:51], v2 offset:0\n\t" \
  "ds_read_b128 v[64:67], v1 offset:8192\n\t" \
  "ds_read_b128 v[80:83], v1 offset:12288\n\t" \
  "ds_read_b128 v[52:55], v2 offset:1024\n\t" \
  "ds_read_b128 v[68:71], v1 offset:9216\n\t" \
  "ds_read_b128 v[84:87], v1 offset:13312\n\t" \
  "ds_read_b128 v[56:59], v2 offset:2048\n\t" \
  "ds_read_b128 v[72:75], v1 offset:10240\n\t" \
  "ds_read_b128 v[88:91], v1 offset:14336\n\t" \
  "ds_read_b128 v[60:63], v2 offset:3072\n\t" \
  "ds_read_b128 v[76:79], v1 offset:11264\n\t" \
  "ds_read_b128 v[92:95], v1 offset:15360\n\t" \
  "s_mov_b32 s82, s83\n\t" \
  "s_xor_b32 s46, s46, 0x10000\n\t" \
  "s_add_u32 s58, s82, s41\n\t" \
  "s_add_u32 s58, s58, 4096\n\t" \
  "s_add_u32 s59, s81, s39\n\t" \
  "s_cmp_gt_i32 s76, 0\n\t" \
  "s_cselect_b32 s62, 0xf0000000, s58\n\t" \
  "buffer_load_dword v5, off, %[X], s62 sc1\n\t" \
  "s_cmp_gt_i32 s76, " TK_AM "\n\t" \
  "s_cselect_b32 s79, 0xf0000000, s58\n\t" \
  "s_waitcnt lgkmcnt(10)\n\t" \
  "v_mfma_f32_16x16x32_bf16 v[36:39], v[64:67], v[48:51], v[16:19]\n\t" \
  "ds_read_b128 v[96:99], v1 offset:16384\n\t" \
  "s_waitcnt lgkmcnt(10)\n\t" \
  "v_mfma_f32_16x16x32_bf16 v[40:43], v[80:83], v[48:51], v[20:23]\n\t" \
  "ds_read_b128 v[100:103], v1 offset:17408\n\t" \
  "s_waitcnt lgkmcnt(9)\n\t" \
  "v_mfma_f32_16x16x32_bf16 v[36:39], v[68:71], v[52:55], v[36:39]\n\t" \
  "ds_read_b128 v[104:107], v1 offset:18432\n\t" \
  "s_waitcnt lgkmcnt(9)\n\t" \
  "v_mfma_f32_16x16x32_bf16 v[40:43], v[84:87], v[52:55], v[40:43]\n\t" \
  "ds_read_b128 v[108:111], v1 offset:19456\n\t" \
  "s_waitcnt lgkmcnt(8)\n\t" \
  "v_mfma_f32_16x16x32_bf16 v[36:39], v[72:75], v[56:59], v[36:39]\n\t" \
  "ds_read_b128 v[112:115], v1 offset:20480\n\t" \
  "s_waitcnt lgkmcnt(8)\n\t" \
  "v_mfma_f32_16x16x32_bf16 v[40:43], v[88:91], v[56:59], v[40:43]\n\t" \
  "ds_read_b128 v[116:119], v1 offset:21504\n\t" \
  "s_waitcnt lgkmcnt(7)\n\t" \
  "v_mfma_f32_16x16x32_bf16 v[36:39], v[76:79], v[60:63], v[36:39]\n\t" \
  "ds_read_b128 v[120:123], v1 offset:22528\n\t" \
  "s_waitcnt lgkmcnt(7)\n\t" \
  "v_mfma_f32_16x16x32_bf16 v[40:43], v[92:95], v[60:63], v[40:43]\n\t" \
  "ds_read_b128 v[124:127], v1 offset:23552\n\t" \
  "s_add_u32 s83, s48, 1\n\t" \
  "s_lshr_b64 s[60:61], s[90:91], s83\n\t" \
  "s_and_b32 s60, s60, 1\n\t" \
  "s_lshl_b32 s60, s60, 13\n\t" \
  "s_add_u32 s83, s83, " TK_SG0 "\n\t" \
  "s_lshl_b32 s83, s83, 21\n\t" \
  "s_or_b32 s83, s83, s60\n\t" \
  "s_add_u32 s56, s48, 1\n\t" \
  "s_and_b32 s56, s56, " TK_TRM "\n\t" \
  "s_lshr_b64 s[60:61], s[90:91], s56\n\t" \
  "s_and_b32 s60, s60, 1\n\t" \
  "s_lshl_b32 s60, s60, 13\n\t" \
  "s_add_u32 s56, s56, " TK_SG0 "\n\t" \
  "s_lshl_b32 s56, s56, 21\n\t" \
  "s_or_b32 s56, s56, s60\n\t" \
  "v_xor_b32 v1, 0x10000, v1\n\t" \
  "v_xor_b32 v2, 0x10000, v2\n\t" \
  "v_max3_f32 v11, v36, v37, v38\n\t" \
  "v_max3_f32 v11, v11, v39, v40\n\t" \
  "v_max3_f32 v13, v41, v42, v43\n\t" \
  "v_max_f32 v11, v11, v13\n\t" \
  "v_mov_b32 v13, v11\n\t" \
  "s_nop 1\n\t" \
  "v_permlane16_swap_b32 v11, v13\n\t" \
  "v_max_f32 v11, v11, v13\n\t" \
  "v_mov_b32 v13, v11\n\t" \
  "s_nop 1\n\t" \
  "v_permlane32_swap_b32 v11, v13\n\t" \
  "v_max_f32 v11, v11, v13\n\t" \
  "v_mul_f32 v13, 0xbe0293ee, v11\n\t" \
  "v_fmamk_f32 v36, v36, 0x3e0293ee, v13\n\t" \
  "v_fmamk_f32 v37, v37, 0x3e0293ee, v13\n\t" \
  "v_fmamk_f32 v38, v38, 0x3e0293ee, v13\n\t" \
  "v_fmamk_f32 v39, v39, 0x3e0293ee, v13\n\t" \
  "v_fmamk_f32 v40, v40, 0x3e0293ee, v13\n\t" \
  "v_fmamk_f32 v41, v41, 0x3e0293ee, v13\n\t" \
  "v_fmamk_f32 v42, v42, 0x3e0293ee, v13\n\t" \
  "v_fmamk_f32 v43, v43, 0x3e0293ee, v13\n\t" \
  "s_waitcnt vmcnt(0)\n\t" \
  "v_readfirstlane_b32 s76, v5\n\t" \
  "s_barrier\n\t" \
  "s_cmp_eq_u32 s76, 0\n\t" \
  "s_cbranch_scc1 LNG_%=\n\t" \
  "buffer_load_dwordx4 v[196:199], v0, %[X], s59 offen offset:0 sc1\n\t" \
  "buffer_load_dwordx4 v[200:203], v0, %[X], s59 offen offset:1024 sc1\n\t" \
  "buffer_load_dwordx2 v[204:205], v6, %[X], s59 offen offset:2048 sc1\n\t" \
  "buffer_load_dwordx2 v[206:207], v6, %[X], s59 offen offset:2560 sc1\n\t" \
  "buffer_load_dwordx2 v[208:209], v6, %[X], s59 offen offset:3072 sc1\n\t" \
  "buffer_load_dwordx2 v[210:211], v6, %[X], s59 offen offset:3584 sc1\n\t" \
  "LNG_%=:\n\t" \
  "buffer_load_dword v5, off, %[X], s79 sc1\n\t" \
  "s_mov_b64 exec, 1\n\t" \
  "buffer_store_dword v3, off, %[X], s49 sc1\n\t" \
  "s_mov_b64 exec, -1\n\t" \
  "s_mov_b32 s49, 0xf0000000\n\t" \
  "v_exp_f32 v36, v36\n\t" \
  "v_exp_f32 v37, v37\n\t" \
  "v_exp_f32 v38, v38\n\t" \
  "v_exp_f32 v39, v39\n\t" \
  "v_exp_f32 v40, v40\n\t" \
  "v_exp_f32 v41, v41\n\t" \
  "v_exp_f32 v42, v42\n\t" \
  "v_exp_f32 v43, v43\n\t" \
  "v_cvt_pk_bf16_f32 v44, v36, v37\n\t" \
  "v_cvt_pk_bf16_f32 v45, v38, v39\n\t" \
  "v_cvt_pk_bf16_f32 v46, v40, v41\n\t" \
  "v_cvt_pk_bf16_f32 v47, v42, v43\n\t" \
  "v_add_f32 v36, v36, v37\n\t" \
  "v_add_f32 v38, v38, v39\n\t" \
  "s_waitcnt lgkmcnt(7)\n\t" \
  "v_mfma_f32_16x16x32_bf16 v[128:131], v[96:99], v[44:47], 0\n\t" \
  "v_add_f32 v40, v40, v41\n\t" \
  "s_and_b32 s62, s48, 15\n\t" \
  "s_lshr_b32 s63, s62, 1\n\t" \
  "s_waitcnt lgkmcnt(6)\n\t" \
  "v_mfma_f32_16x16x32_bf16 v[132:135], v[100:103], v[44:47], 0\n\t" \
  "v_add_f32 v42, v42, v43\n\t" \
  "s_lshr_b32 s60, s88, s63\n\t" \
  "s_and_b32 s60, s60, 1\n\t" \
  "s_waitcnt lgkmcnt(5)\n\t" \
  "v_mfma_f32_16x16x32_bf16 v[136:139], v[104:107], v[44:47], 0\n\t" \
  "v_add_f32 v36, v36, v38\n\t" \
  "s_lshl_b32 s60, s60, 13\n\t" \
  "s_lshl_b32 s61, s62, 20\n\t" \
  "s_waitcnt lgkmcnt(4)\n\t" \
  "v_mfma_f32_16x16x32_bf16 v[140:143], v[108:111], v[44:47], 0\n\t" \
  "v_add_f32 v40, v40, v42\n\t" \
  "s_add_u32 s50, s61, s60\n\t" \
  "s_add_u32 s50, s50, s43\n\t" \
  "s_waitcnt lgkmcnt(3)\n\t" \
  "v_mfma_f32_16x16x32_bf16 v[144:147], v[112:115], v[44:47], 0\n\t" \
  "v_add_f32 v12, v36, v40\n\t" \
  "s_add_u32 s51, s50, 32768\n\t" \
  "s_add_u32 s52, s51, 32768\n\t" \
  "s_waitcnt lgkmcnt(2)\n\t" \
  "v_mfma_f32_16x16x32_bf16 v[148:151], v[116:119], v[44:47], 0\n\t" \
  "s_add_u32 s53, s52, 32768\n\t" \
  "s_lshl_b32 s61, s62, 17\n\t" \
  "s_waitcnt lgkmcnt(1)\n\t" \
  "v_mfma_f32_16x16x32_bf16 v[152:155], v[120:123], v[44:47], 0\n\t" \
  "s_add_u32 s54, s61, s44\n\t" \
  "s_add_u32 s63, s83, s41\n\t" \
  "s_waitcnt lgkmcnt(0)\n\t" \
  "v_mfma_f32_16x16x32_bf16 v[156:159], v[124:127], v[44:47], 0\n\t" \
  "s_cmp_lg_u32 s76, 0\n\t" \
  "s_cbranch_scc1 LGOT_%=\n\t" \
  "s_waitcnt vmcnt(1)\n\t" \
  "v_readfirstlane_b32 s60, v5\n\t" \
  "s_nop 0\n\t" \
  "s_cmp_lg_u32 s60, 0\n\t" \
  "s_cbranch_scc1 LPK_%=x\n\t" \
  "LKW_%=:\n\t" \
  "s_memrealtime s[78:79]\n\t" \
  "s_waitcnt lgkmcnt(0)\n\t" \
  "s_sub_i32 s79, s78, s57\n\t" \
  "s_cmp_lt_i32 s79, 0\n\t" \
  "s_cbranch_scc1 LKW_%=\n\t" \
  "s_mov_b32 s61, 0\n\t" \
  "LPK_%=:\n\t" \
  "buffer_load_dword v5, off, %[X], s58 sc1\n\t" \
  "s_waitcnt vmcnt(0)\n\t" \
  "v_cmp_eq_u32 vcc, 0, v5\n\t" \
  "s_cbranch_vccz LPK_%=x\n\t" \
  "s_add_u32 s61, s61, 1\n\t" \
  "s_sleep " TK_PSK "\n\t" \
  "s_cmp_gt_u32 s61, s84\n\t" \
  "s_cbranch_scc1 LPK_%=g\n\t" \
  "s_and_b32 s60, s61, 1023\n\t" \
  "s_cmp_lg_u32 s60, 0\n\t" \
  "s_cbranch_scc1 LPK_%=\n\t" \
  "buffer_load_dword v5, off, %[X], s92 sc1\n\t" \
  "s_waitcnt vmcnt(0)\n\t" \
  "v_readfirstlane_b32 s60, v5\n\t" \
  "s_nop 0\n\t" \
  "s_cmp_eq_u32 s60, 0\n\t" \
  "s_cbranch_scc1 LPK_%=\n\t" \
  "LPK_%=g:\n\t" \
  "s_mov_b32 s84, 0\n\t" \
  "s_or_b32 s60, s48, 1342177280\n\t" \
  "v_mov_b32 v5, s60\n\t" \
  "s_mov_b64 exec, 1\n\t" \
  "buffer_store_dword v5, off, %[X], s92 sc1\n\t" \
  "s_mov_b64 exec, -1\n\t" \
  "LPK_%=x:\n\t" \
  "buffer_load_dwordx4 v[196:199], v0, %[X], s59 offen offset:0 sc1\n\t" \
  "buffer_load_dwordx4 v[200:203], v0, %[X], s59 offen offset:1024 sc1\n\t" \
  "buffer_load_dwordx2 v[204:205], v6, %[X], s59 offen offset:2048 sc1\n\t" \
  "buffer_load_dwordx2 v[206:207], v6, %[X], s59 offen offset:2560 sc1\n\t" \
  "buffer_load_dwordx2 v[208:209], v6, %[X], s59 offen offset:3072 sc1\n\t" \
  "buffer_load_dwordx2 v[210:211], v6, %[X], s59 offen offset:3584 sc1\n\t" \
  "LGOT_%=:\n\t" \
  "buffer_load_dword v10, v9, %[X], s63 offen sc1\n\t" \
  "s_add_u32 s49, s82, s42\n\t" \
  "s_waitcnt vmcnt(6)\n\t" \
  "v_mfma_f32_16x16x32_bf16 v[36:39], v[196:199], v[48:51], 0\n\t" \
  "v_mfma_f32_16x16x32_bf16 v[40:43], v[196:199], v[52:55], 0\n\t" \
  "s_waitcnt vmcnt(5)\n\t" \
  "v_mfma_f32_16x16x32_bf16 v[36:39], v[200:203], v[56:59], v[36:39]\n\t" \
  "v_mfma_f32_16x16x32_bf16 v[40:43], v[200:203], v[60:63], v[40:43]\n\t" \
  "s_nop 8\n\t" \
  "v_add_f32 v36, v36, v42\n\t" \
  "v_add_f32 v37, v37, v43\n\t" \
  "v_max3_f32 v14, v36, v37, v11\n\t" \
  "v_mov_b32 v13, v14\n\t" \
  "s_nop 1\n\t" \
  "v_permlane16_swap_b32 v14, v13\n\t" \
  "v_max_f32 v14, v14, v13\n\t" \
  "v_mov_b32 v13, v14\n\t" \
  "s_nop 1\n\t" \
  "v_permlane32_swap_b32 v14, v13\n\t" \
  "v_max_f32 v14, v14, v13\n\t" \
  "v_mul_f32 v14, 0xbe0293ee, v14\n\t" \
  "v_fmamk_f32 v11, v11, 0x3e0293ee, v14\n\t" \
  "v_fmamk_f32 v36, v36, 0x3e0293ee, v14\n\t" \
  "v_fmamk_f32 v37, v37, 0x3e0293ee, v14\n\t" \
  "v_exp_f32 v11, v11\n\t" \
  "v_exp_f32 v36, v36\n\t" \
  "v_exp_f32 v37, v37\n\t" \
  "v_fma_f32 v12, v12, v11, v36\n\t" \
  "v_add_f32 v12, v12, v37\n\t" \
  "v_mov_b32 v13, v12\n\t" \
  "s_nop 1\n\t" \
  "v_permlane16_swap_b32 v12, v13\n\t" \
  "v_add_f32 v12, v12, v13\n\t" \
  "v_mov_b32 v13, v12\n\t" \
  "s_nop 1\n\t" \
  "v_permlane32_swap_b32 v12, v13\n\t" \
  "v_add_f32 v12, v12, v13\n\t" \
  "v_rcp_f32 v12, v12\n\t" \
  "s_nop 0\n\t" \
  "v_mul_f32 v14, v11, v12\n\t" \
  "v_mul_f32 v36, v36, v12\n\t" \
  "v_mul_f32 v37, v37, v12\n\t" \
  "v_cvt_pk_bf16_f32 v26, v36, v37\n\t" \
  "v_cvt_pk_bf16_f32 v33, v36, v37\n\t" \
  "v_pk_mul_f32 v[128:129], v[128:129], v[14:15] op_sel_hi:[1,0]\n\t" \
  "v_pk_mul_f32 v[130:131], v[130:131], v[14:15] op_sel_hi:[1,0]\n\t" \
  "v_pk_mul_f32 v[132:133], v[132:133], v[14:15] op_sel_hi:[1,0]\n\t" \
  "v_pk_mul_f32 v[134:135], v[134:135], v[14:15] op_sel_hi:[1,0]\n\t" \
  "v_pk_mul_f32 v[136:137], v[136:137], v[14:15] op_sel_hi:[1,0]\n\t" \
  "v_pk_mul_f32 v[138:139], v[138:139], v[14:15] op_sel_hi:[1,0]\n\t" \
  "v_pk_mul_f32 v[140:141], v[140:141], v[14:15] op_sel_hi:[1,0]\n\t" \
  "v_pk_mul_f32 v[142:143], v[142:143], v[14:15] op_sel_hi:[1,0]\n\t" \
  "s_waitcnt vmcnt(3)\n\t" \
  "v_mfma_f32_16x16x32_bf16 v[128:131], v[204:207], v[26:29], v[128:131]\n\t" \
  "v_mul_f32 v144, v144, v14\n\t" \
  "v_mul_f32 v145, v145, v14\n\t" \
  "v_mfma_f32_16x16x32_bf16 v[132:135], v[204:207], v[32:35], v[132:135]\n\t" \
  "v_mul_f32 v146, v146, v14\n\t" \
  "v_mul_f32 v147, v147, v14\n\t" \
  "v_mfma_f32_16x16x32_bf16 v[136:139], v[204:207], v[24:27], v[136:139]\n\t" \
  "v_mul_f32 v148, v148, v14\n\t" \
  "v_mul_f32 v149, v149, v14\n\t" \
  "v_mfma_f32_16x16x32_bf16 v[140:143], v[204:207], v[30:33], v[140:143]\n\t" \
  "v_cvt_pk_bf16_f32 v64, v128, v129\n\t" \
  "v_cvt_pk_bf16_f32 v65, v130, v131\n\t" \
  "v_cvt_pk_bf16_f32 v66, v132, v133\n\t" \
  "v_cvt_pk_bf16_f32 v67, v134, v135\n\t" \
  "v_mul_f32 v150, v150, v14\n\t" \
  "v_mul_f32 v151, v151, v14\n\t" \
  "v_mfma_f32_16x16x32_bf16 v[192:195], v[224:227], v[64:67], 0\n\t" \
  "ds_write_b128 v3, v[64:67] offset:0\n\t" \
  "v_cvt_pk_bf16_f32 v68, v136, v137\n\t" \
  "v_cvt_pk_bf16_f32 v69, v138, v139\n\t" \
  "v_mfma_f32_16x16x32_bf16 v[160:163], v[64:67], a[0:3], 0\n\t" \
  "v_cvt_pk_bf16_f32 v70, v140, v141\n\t" \
  "v_cvt_pk_bf16_f32 v71, v142, v143\n\t" \
  "s_waitcnt vmcnt(1)\n\t" \
  "v_mfma_f32_16x16x32_bf16 v[144:147], v[208:211], v[26:29], v[144:147]\n\t" \
  "v_mul_f32 v152, v152, v14\n\t" \
  "v_mul_f32 v153, v153, v14\n\t" \
  "v_mfma_f32_16x16x32_bf16 v[148:151], v[208:211], v[32:35], v[148:151]\n\t" \
  "v_mul_f32 v154, v154, v14\n\t" \
  "v_mul_f32 v155, v155, v14\n\t" \
  "v_mfma_f32_16x16x32_bf16 v[164:167], v[64:67], a[4:7], 0\n\t" \
  "ds_write_b128 v3, v[68:71] offset:1024\n\t" \
  "v_mul_f32 v156, v156, v14\n\t" \
  "v_mul_f32 v157, v157, v14\n\t" \
  "v_mfma_f32_16x16x32_bf16 v[168:171], v[64:67], a[8:11], 0\n\t" \
  "v_mul_f32 v158, v158, v14\n\t" \
  "v_mul_f32 v159, v159, v14\n\t" \
  "v_mfma_f32_16x16x32_bf16 v[172:175], v[64:67], a[12:15], 0\n\t" \
  "v_cvt_pk_bf16_f32 v72, v144, v145\n\t" \
  "v_cvt_pk_bf16_f32 v73, v146, v147\n\t" \
  "v_mfma_f32_16x16x32_bf16 v[152:155], v[208:211], v[24:27], v[152:155]\n\t" \
  "v_cvt_pk_bf16_f32 v74, v148, v149\n\t" \
  "v_cvt_pk_bf16_f32 v75, v150, v151\n\t" \
  "v_mfma_f32_16x16x32_bf16 v[156:159], v[208:211], v[30:33], v[156:159]\n\t" \
  "v_mfma_f32_16x16x32_bf16 v[176:179], v[64:67], a[16:19], 0\n\t" \
  "ds_write_b128 v3, v[72:75] offset:2048\n\t" \
  "v_mfma_f32_16x16x32_bf16 v[180:183], v[64:67], a[20:23], 0\n\t" \
  "v_mfma_f32_16x16x32_bf16 v[184:187], v[64:67], a[24:27], 0\n\t" \
  "v_cvt_pk_bf16_f32 v76, v152, v153\n\t" \
  "v_cvt_pk_bf16_f32 v77, v154, v155\n\t" \
  "v_mfma_f32_16x16x32_bf16 v[188:191], v[64:67], a[28:31], 0\n\t" \
  "v_cvt_pk_bf16_f32 v78, v156, v157\n\t" \
  "v_cvt_pk_bf16_f32 v79, v158, v159\n\t" \
  "v_mfma_f32_16x16x32_bf16 v[192:195], v[228:231], v[68:71], v[192:195]\n\t" \
  "ds_write_b128 v3, v[76:79] offset:3072\n\t" \
  "v_mfma_f32_16x16x32_bf16 v[160:163], v[68:71], a[32:35], v[160:163]\n\t" \
  "s_waitcnt vmcnt(0)\n\t" \
  "v_cmp_eq_u32 vcc, 0, v10\n\t" \
  "v_mfma_f32_16x16x32_bf16 v[164:167], v[68:71], a[36:39], v[164:167]\n\t" \
  "s_cmp_eq_u64 vcc, 0\n\t" \
  "s_cselect_b32 s81, s56, 0xf0000000\n\t" \
  "s_cmp_eq_u32 s48, 63\n\t" \
  "v_mfma_f32_16x16x32_bf16 v[168:171], v[68:71], a[40:43], v[168:171]\n\t" \
  "s_cselect_b32 s81, 0xf0000000, s81\n\t" \
  "v_mfma_f32_16x16x32_bf16 v[172:175], v[68:71], a[44:47], v[172:175]\n\t" \
  "v_mfma_f32_16x16x32_bf16 v[176:179], v[68:71], a[48:51], v[176:179]\n\t" \
  "s_add_u32 m0, s46, 0\n\t" \
  "s_add_u32 s47, s81, s36\n\t" \
  "buffer_load_dwordx4 v0, %[X], s47 offen offset:0 sc1 lds\n\t" \
  "v_mfma_f32_16x16x32_bf16 v[180:183], v[68:71], a[52:55], v[180:183]\n\t" \
  "v_mfma_f32_16x16x32_bf16 v[184:187], v[68:71], a[56:59], v[184:187]\n\t" \
  "s_waitcnt lgkmcnt(0)\n\t" \
  "s_barrier\n\t" \
  "v_mfma_f32_16x16x32_bf16 v[188:191], v[68:71], a[60:63], v[188:191]\n\t" \
  "ds_read_b128 v[80:83], v4 offset:0\n\t" \
  "v_mfma_f32_16x16x32_bf16 v[192:195], v[232:235], v[72:75], v[192:195]\n\t" \
  "buffer_load_dwordx4 v0, %[X], s47 offen offset:1024 sc1 lds\n\t" \
  "v_mfma_f32_16x16x32_bf16 v[160:163], v[72:75], a[64:67], v[160:163]\n\t" \
  "ds_read_b128 v[84:87], v4 offset:1024\n\t" \
  "v_mfma_f32_16x16x32_bf16 v[164:167], v[72:75], a[68:71], v[164:167]\n\t" \
  "v_mfma_f32_16x16x32_bf16 v[168:171], v[72:75], a[72:75], v[168:171]\n\t" \
  "ds_read_b128 v[88:91], v4 offset:2048\n\t" \
  "v_mfma_f32_16x16x32_bf16 v[172:175], v[72:75], a[76:79], v[172:175]\n\t" \
  "buffer_load_dwordx4 v0, %[X], s47 offen offset:2048 sc1 lds\n\t" \
  "v_mfma_f32_16x16x32_bf16 v[176:179], v[72:75], a[80:83], v[176:179]\n\t" \
  "ds_read_b128 v[92:95], v4 offset:3072\n\t" \
  "v_mfma_f32_16x16x32_bf16 v[180:183], v[72:75], a[84:87], v[180:183]\n\t" \
  "v_mfma_f32_16x16x32_bf16 v[184:187], v[72:75], a[88:91], v[184:187]\n\t" \
  "v_mfma_f32_16x16x32_bf16 v[188:191], v[72:75], a[92:95], v[188:191]\n\t" \
  "buffer_load_dwordx4 v0, %[X], s47 offen offset:3072 sc1 lds\n\t" \
  "v_mfma_f32_16x16x32_bf16 v[192:195], v[236:239], v[76:79], v[192:195]\n\t" \
  "s_add_u32 s62, s48, 2\n\t" \
  "v_mfma_f32_16x16x32_bf16 v[160:163], v[76:79], a[96:99], v[160:163]\n\t" \
  "v_mfma_f32_16x16x32_bf16 v[164:167], v[76:79], a[100:103], v[164:167]\n\t" \
  "s_mul_i32 s63, s62, 43\n\t" \
  "s_lshr_b32 s63, s63, 9\n\t" \
  "v_mfma_f32_16x16x32_bf16 v[168:171], v[76:79], a[104:107], v[168:171]\n\t" \
  "s_add_u32 m0, s46, 4096\n\t" \
  "s_add_u32 s47, s81, s37\n\t" \
  "buffer_load_dwordx4 v0, %[X], s47 offen offset:0 sc1 lds\n\t" \
  "v_mfma_f32_16x16x32_bf16 v[172:175], v[76:79], a[108:111], v[172:175]\n\t" \
  "s_mul_i32 s63, s63, " TK_RM "\n\t" \
  "s_sub_u32 s62, s62, s63\n\t" \
  "v_mfma_f32_16x16x32_bf16 v[176:179], v[76:79], a[112:115], v[176:179]\n\t" \
  "v_mfma_f32_16x16x32_bf16 v[180:183], v[76:79], a[116:119], v[180:183]\n\t" \
  "s_mul_i32 s63, s62, 43\n\t" \
  "s_lshr_b32 s63, s63, 7\n\t" \
  "v_mfma_f32_16x16x32_bf16 v[184:187], v[76:79], a[120:123], v[184:187]\n\t" \
  "buffer_load_dwordx4 v0, %[X], s47 offen offset:1024 sc1 lds\n\t" \
  "v_mfma_f32_16x16x32_bf16 v[188:191], v[76:79], a[124:127], v[188:191]\n\t" \
  "s_mul_i32 s62, s62, 688128\n\t" \
  "s_lshl_b32 s60, s63, 15\n\t" \
  "s_waitcnt lgkmcnt(3)\n\t" \
  "v_mfma_f32_16x16x32_bf16 v[192:195], v[240:243], v[80:83], v[192:195]\n\t" \
  "v_mfma_f32_16x16x32_bf16 v[160:163], v[80:83], a[128:131], v[160:163]\n\t" \
  "s_add_u32 s62, s62, s60\n\t" \
  "s_lshr_b32 s60, s86, s63\n\t" \
  "v_mfma_f32_16x16x32_bf16 v[164:167], v[80:83], a[132:135], v[164:167]\n\t" \
  "buffer_load_dwordx4 v0, %[X], s47 offen offset:2048 sc1 lds\n\t" \
  "v_mfma_f32_16x16x32_bf16 v[168:171], v[80:83], a[136:139], v[168:171]\n\t" \
  "s_and_b32 s60, s60, 1\n\t" \
  "s_lshl_b32 s60, s60, 13\n\t" \
  "v_mfma_f32_16x16x32_bf16 v[172:175], v[80:83], a[140:143], v[172:175]\n\t" \
  "v_mfma_f32_16x16x32_bf16 v[176:179], v[80:83], a[144:147], v[176:179]\n\t" \
  "s_add_u32 s62, s62, s60\n\t" \
  "s_add_u32 s62, s62, 656896\n\t" \
  "v_mfma_f32_16x16x32_bf16 v[180:183], v[80:83], a[148:151], v[180:183]\n\t" \
  "buffer_load_dwordx4 v0, %[X], s47 offen offset:3072 sc1 lds\n\t" \
  "v_mfma_f32_16x16x32_bf16 v[184:187], v[80:83], a[152:155], v[184:187]\n\t" \
  "s_cmp_gt_u32 s48, 61\n\t" \
  "s_cselect_b32 s62, 0xf0000000, s62\n\t" \
  "v_mfma_f32_16x16x32_bf16 v[188:191], v[80:83], a[156:159], v[188:191]\n\t" \
  "s_waitcnt lgkmcnt(2)\n\t" \
  "v_mfma_f32_16x16x32_bf16 v[192:195], v[244:247], v[84:87], v[192:195]\n\t" \
  "v_mfma_f32_16x16x32_bf16 v[160:163], v[84:87], a[160:163], v[160:163]\n\t" \
  "s_add_u32 m0, s46, 8192\n\t" \
  "s_add_u32 s47, s81, s38\n\t" \
  "buffer_load_dwordx4 v0, %[X], s47 offen offset:0 sc1 lds\n\t" \
  "v_mfma_f32_16x16x32_bf16 v[164:167], v[84:87], a[164:167], v[164:167]\n\t" \
  "v_mfma_f32_16x16x32_bf16 v[168:171], v[84:87], a[168:171], v[168:171]\n\t" \
  "v_mfma_f32_16x16x32_bf16 v[172:175], v[84:87], a[172:175], v[172:175]\n\t" \
  "v_mfma_f32_16x16x32_bf16 v[176:179], v[84:87], a[176:179], v[176:179]\n\t" \
  "buffer_load_dwordx4 v0, %[X], s47 offen offset:1024 sc1 lds\n\t" \
  "v_mfma_f32_16x16x32_bf16 v[180:183], v[84:87], a[180:183], v[180:183]\n\t" \
  "v_mfma_f32_16x16x32_bf16 v[184:187], v[84:87], a[184:187], v[184:187]\n\t" \
  "v_mfma_f32_16x16x32_bf16 v[188:191], v[84:87], a[188:191], v[188:191]\n\t" \
  "s_waitcnt lgkmcnt(1)\n\t" \
  "v_mfma_f32_16x16x32_bf16 v[192:195], v[248:251], v[88:91], v[192:195]\n\t" \
  "buffer_load_dwordx4 v0, %[X], s47 offen offset:2048 sc1 lds\n\t" \
  "v_mfma_f32_16x16x32_bf16 v[160:163], v[88:91], a[192:195], v[160:163]\n\t" \
  "v_mfma_f32_16x16x32_bf16 v[164:167], v[88:91], a[196:199], v[164:167]\n\t" \
  "v_mfma_f32_16x16x32_bf16 v[168:171], v[88:91], a[200:203], v[168:171]\n\t" \
  "v_mfma_f32_16x16x32_bf16 v[172:175], v[88:91], a[204:207], v[172:175]\n\t" \
  "buffer_load_dwordx4 v0, %[X], s47 offen offset:3072 sc1 lds\n\t" \
  "v_mfma_f32_16x16x32_bf16 v[176:179], v[88:91], a[208:211], v[176:179]\n\t" \
  "v_mfma_f32_16x16x32_bf16 v[180:183], v[88:91], a[212:215], v[180:183]\n\t" \
  "buffer_load_dword v5, off, %[X], s62 sc1\n\t" \
  "v_mfma_f32_16x16x32_bf16 v[184:187], v[88:91], a[216:219], v[184:187]\n\t" \
  "v_mfma_f32_16x16x32_bf16 v[188:191], v[88:91], a[220:223], v[188:191]\n\t" \
  "s_waitcnt lgkmcnt(0)\n\t" \
  "v_mfma_f32_16x16x32_bf16 v[192:195], v[252:255], v[92:95], v[192:195]\n\t" \
  "v_mfma_f32_16x16x32_bf16 v[160:163], v[92:95], a[224:227], v[160:163]\n\t" \
  "v_mfma_f32_16x16x32_bf16 v[164:167], v[92:95], a[228:231], v[164:167]\n\t" \
  "v_mfma_f32_16x16x32_bf16 v[168:171], v[92:95], a[232:235], v[168:171]\n\t" \
  "v_cvt_pk_bf16_f32 v112, v192, v193\n\t" \
  "v_cvt_pk_bf16_f32 v113, v194, v195\n\t" \
  "v_mfma_f32_16x16x32_bf16 v[172:175], v[92:95], a[236:239], v[172:175]\n\t" \
  "buffer_store_dwordx2 v[112:113], v8, %[X], s54 offen sc1\n\t" \
  "v_mfma_f32_16x16x32_bf16 v[176:179], v[92:95], a[240:243], v[176:179]\n\t" \
  "v_cvt_pk_bf16_f32 v96, v160, v164\n\t" \
  "v_cvt_pk_bf16_f32 v100, v161, v165\n\t" \
  "v_mfma_f32_16x16x32_bf16 v[180:183], v[92:95], a[244:247], v[180:183]\n\t" \
  "v_cvt_pk_bf16_f32 v104, v162, v166\n\t" \
  "v_cvt_pk_bf16_f32 v108, v163, v167\n\t" \
  "v_mfma_f32_16x16x32_bf16 v[184:187], v[92:95], a[248:251], v[184:187]\n\t" \
  "v_cvt_pk_bf16_f32 v97, v168, v172\n\t" \
  "v_cvt_pk_bf16_f32 v101, v169, v173\n\t" \
  "v_mfma_f32_16x16x32_bf16 v[188:191], v[92:95], a[252:255], v[188:191]\n\t" \
  "v_cvt_pk_bf16_f32 v105, v170, v174\n\t" \
  "v_cvt_pk_bf16_f32 v109, v171, v175\n\t" \
  "v_cvt_pk_bf16_f32 v98, v176, v180\n\t" \
  "v_cvt_pk_bf16_f32 v102, v177, v181\n\t" \
  "v_cvt_pk_bf16_f32 v106, v178, v182\n\t" \
  "v_cvt_pk_bf16_f32 v110, v179, v183\n\t" \
  "s_nop 1\n\t" \
  "v_cvt_pk_bf16_f32 v99, v184, v188\n\t" \
  "buffer_store_dwordx4 v[96:99], v7, %[X], s50 offen sc1\n\t" \
  "v_cvt_pk_bf16_f32 v103, v185, v189\n\t" \
  "buffer_store_dwordx4 v[100:103], v7, %[X], s51 offen sc1\n\t" \
  "v_cvt_pk_bf16_f32 v107, v186, v190\n\t" \
  "buffer_store_dwordx4 v[104:107], v7, %[X], s52 offen sc1\n\t" \
  "v_cvt_pk_bf16_f32 v111, v187, v191\n\t" \
  "buffer_store_dwordx4 v[108:111], v7, %[X], s53 offen sc1\n\t" \
  "s_cmp_eq_u32 s48, 63\n\t" \
  "s_cbranch_scc1 LEND_%=\n\t" \
  "s_add_u32 s48, s48, 1\n\t" \
  "s_cmp_eq_u32 s81, s56\n\t" \
  "s_mov_b32 s81, s56\n\t" \
  "s_cbranch_scc0 LCOLD_%=\n\t" \
  "s_waitcnt vmcnt(5)\n\t" \
  "s_branch LTOP_%=\n\t" \
  "LEND_%=:\n\t" \
  "s_waitcnt vmcnt(0)\n\t" \
  "s_barrier\n\t" \
  "s_mov_b64 exec, 1\n\t" \
  "buffer_store_dword v3, off, %[X], s49 sc1\n\t" \
  "s_mov_b64 exec, -1\n\t" \
  "s_mov_b32 s49, 0xf0000000\n\t" \
  ""
#define QK_ASM \
  "v_lshrrev_b32 v16, 7, v0\n\t" \
  "v_lshlrev_b32 v16, 13, v16\n\t" \
  "v_lshrrev_b32 v19, 2, v0\n\t" \
  "v_and_b32 v19, 64, v19\n\t" \
  "v_xor_b32 v19, v19, v0\n\t" \
  "v_and_b32 v19, 0x70, v19\n\t" \
  "v_add_u32 v16, v16, v19\n\t" \
  "s_mul_i32 s63, s40, 40960\n\t" \
  "v_and_b32 v19, 0x300, v0\n\t" \
  "v_lshlrev_b32 v17, 2, v19\n\t" \
  "v_and_b32 v19, 0xc0, v0\n\t" \
  "v_lshl_add_u32 v17, v19, 1, v17\n\t" \
  "v_and_b32 v19, 0x30, v0\n\t" \
  "v_add3_u32 v17, v17, v19, s63\n\t" \
  "v_lshrrev_b32 v19, 1, v0\n\t" \
  "v_and_b32 v19, 64, v19\n\t" \
  "v_add_u32 v17, v17, v19\n\t" \
  "v_xor_b32 v18, 64, v17\n\t" \
  "s_add_u32 m0, s63, 0\n\t" \
  "s_mov_b32 s60, 0\n\t" \
  "buffer_load_dwordx4 v16, %[W], s60 offen lds\n\t" \
  "s_add_u32 m0, s63, 1024\n\t" \
  "s_mov_b32 s60, 65536\n\t" \
  "buffer_load_dwordx4 v16, %[W], s60 offen lds\n\t" \
  "s_add_u32 m0, s63, 2048\n\t" \
  "s_mov_b32 s60, 131072\n\t" \
  "buffer_load_dwordx4 v16, %[W], s60 offen lds\n\t" \
  "s_add_u32 m0, s63, 3072\n\t" \
  "s_mov_b32 s60, 196608\n\t" \
  "buffer_load_dwordx4 v16, %[W], s60 offen lds\n\t" \
  "s_add_u32 m0, s63, 4096\n\t" \
  "s_mov_b32 s60, 262144\n\t" \
  "buffer_load_dwordx4 v16, %[W], s60 offen lds\n\t" \
  "s_add_u32 m0, s63, 5120\n\t" \
  "s_mov_b32 s60, 327680\n\t" \
  "buffer_load_dwordx4 v16, %[W], s60 offen lds\n\t" \
  "s_add_u32 m0, s63, 6144\n\t" \
  "s_mov_b32 s60, 393216\n\t" \
  "buffer_load_dwordx4 v16, %[W], s60 offen lds\n\t" \
  "s_add_u32 m0, s63, 7168\n\t" \
  "s_mov_b32 s60, 458752\n\t" \
  "buffer_load_dwordx4 v16, %[W], s60 offen lds\n\t" \
  "s_add_u32 m0, s63, 8192\n\t" \
  "s_mov_b32 s60, 524288\n\t" \
  "buffer_load_dwordx4 v16, %[W], s60 offen lds\n\t" \
  "s_add_u32 m0, s63, 9216\n\t" \
  "s_mov_b32 s60, 589824\n\t" \
  "buffer_load_dwordx4 v16, %[W], s60 offen lds\n\t" \
  "s_add_u32 m0, s63, 10240\n\t" \
  "s_mov_b32 s60, 655360\n\t" \
  "buffer_load_dwordx4 v16, %[W], s60 offen lds\n\t" \
  "s_add_u32 m0, s63, 11264\n\t" \
  "s_mov_b32 s60, 720896\n\t" \
  "buffer_load_dwordx4 v16, %[W], s60 offen lds\n\t" \
  "s_add_u32 m0, s63, 12288\n\t" \
  "s_mov_b32 s60, 786432\n\t" \
  "buffer_load_dwordx4 v16, %[W], s60 offen lds\n\t" \
  "s_add_u32 m0, s63, 13312\n\t" \
  "s_mov_b32 s60, 851968\n\t" \
  "buffer_load_dwordx4 v16, %[W], s60 offen lds\n\t" \
  "s_add_u32 m0, s63, 14336\n\t" \
  "s_mov_b32 s60, 917504\n\t" \
  "buffer_load_dwordx4 v16, %[W], s60 offen lds\n\t" \
  "s_add_u32 m0, s63, 15360\n\t" \
  "s_mov_b32 s60, 983040\n\t" \
  "buffer_load_dwordx4 v16, %[W], s60 offen lds\n\t" \
  "s_add_u32 m0, s63, 16384\n\t" \
  "s_mov_b32 s60, 1048576\n\t" \
  "buffer_load_dwordx4 v16, %[W], s60 offen lds\n\t" \
  "s_add_u32 m0, s63, 17408\n\t" \
  "s_mov_b32 s60, 1114112\n\t" \
  "buffer_load_dwordx4 v16, %[W], s60 offen lds\n\t" \
  "s_add_u32 m0, s63, 18432\n\t" \
  "s_mov_b32 s60, 1179648\n\t" \
  "buffer_load_dwordx4 v16, %[W], s60 offen lds\n\t" \
  "s_add_u32 m0, s63, 19456\n\t" \
  "s_mov_b32 s60, 1245184\n\t" \
  "buffer_load_dwordx4 v16, %[W], s60 offen lds\n\t" \
  "s_add_u32 m0, s63, 20480\n\t" \
  "s_mov_b32 s60, 1310720\n\t" \
  "buffer_load_dwordx4 v16, %[W], s60 offen lds\n\t" \
  "s_add_u32 m0, s63, 21504\n\t" \
  "s_mov_b32 s60, 1376256\n\t" \
  "buffer_load_dwordx4 v16, %[W], s60 offen lds\n\t" \
  "s_add_u32 m0, s63, 22528\n\t" \
  "s_mov_b32 s60, 1441792\n\t" \
  "buffer_load_dwordx4 v16, %[W], s60 offen lds\n\t" \
  "s_add_u32 m0, s63, 23552\n\t" \
  "s_mov_b32 s60, 1507328\n\t" \
  "buffer_load_dwordx4 v16, %[W], s60 offen lds\n\t" \
  "s_add_u32 m0, s63, 24576\n\t" \
  "s_mov_b32 s60, 1572864\n\t" \
  "buffer_load_dwordx4 v16, %[W], s60 offen lds\n\t" \
  "s_add_u32 m0, s63, 25600\n\t" \
  "s_mov_b32 s60, 1638400\n\t" \
  "buffer_load_dwordx4 v16, %[W], s60 offen lds\n\t" \
  "s_add_u32 m0, s63, 26624\n\t" \
  "s_mov_b32 s60, 1703936\n\t" \
  "buffer_load_dwordx4 v16, %[W], s60 offen lds\n\t" \
  "s_add_u32 m0, s63, 27648\n\t" \
  "s_mov_b32 s60, 1769472\n\t" \
  "buffer_load_dwordx4 v16, %[W], s60 offen lds\n\t" \
  "s_add_u32 m0, s63, 28672\n\t" \
  "s_mov_b32 s60, 1835008\n\t" \
  "buffer_load_dwordx4 v16, %[W], s60 offen lds\n\t" \
  "s_add_u32 m0, s63, 29696\n\t" \
  "s_mov_b32 s60, 1900544\n\t" \
  "buffer_load_dwordx4 v16, %[W], s60 offen lds\n\t" \
  "s_add_u32 m0, s63, 30720\n\t" \
  "s_mov_b32 s60, 1966080\n\t" \
  "buffer_load_dwordx4 v16, %[W], s60 offen lds\n\t" \
  "s_add_u32 m0, s63, 31744\n\t" \
  "s_mov_b32 s60, 2031616\n\t" \
  "buffer_load_dwordx4 v16, %[W], s60 offen lds\n\t" \
  "s_add_u32 m0, s63, 32768\n\t" \
  "s_mov_b32 s60, 2097152\n\t" \
  "buffer_load_dwordx4 v16, %[W], s60 offen lds\n\t" \
  "s_add_u32 m0, s63, 33792\n\t" \
  "s_mov_b32 s60, 2162688\n\t" \
  "buffer_load_dwordx4 v16, %[W], s60 offen lds\n\t" \
  "s_add_u32 m0, s63, 34816\n\t" \
  "s_mov_b32 s60, 2228224\n\t" \
  "buffer_load_dwordx4 v16, %[W], s60 offen lds\n\t" \
  "s_add_u32 m0, s63, 35840\n\t" \
  "s_mov_b32 s60, 2293760\n\t" \
  "buffer_load_dwordx4 v16, %[W], s60 offen lds\n\t" \
  "s_add_u32 m0, s63, 36864\n\t" \
  "s_mov_b32 s60, 2359296\n\t" \
  "buffer_load_dwordx4 v16, %[W], s60 offen lds\n\t" \
  "s_add_u32 m0, s63, 37888\n\t" \
  "s_mov_b32 s60, 2424832\n\t" \
  "buffer_load_dwordx4 v16, %[W], s60 offen lds\n\t" \
  "s_add_u32 m0, s63, 38912\n\t" \
  "s_mov_b32 s60, 2490368\n\t" \
  "buffer_load_dwordx4 v16, %[W], s60 offen lds\n\t" \
  "s_add_u32 m0, s63, 39936\n\t" \
  "s_mov_b32 s60, 2555904\n\t" \
  "buffer_load_dwordx4 v16, %[W], s60 offen lds\n\t" \
  "s_waitcnt vmcnt(32)\n\t" \
  "ds_read_b64_tr_b16 a[0:1], v17 offset:0\n\t" \
  "ds_read_b64_tr_b16 a[2:3], v17 offset:512\n\t" \
  "ds_read_b64_tr_b16 a[4:5], v17 offset:4096\n\t" \
  "ds_read_b64_tr_b16 a[6:7], v17 offset:4608\n\t" \
  "ds_read_b64_tr_b16 a[72:73], v17 offset:8\n\t" \
  "ds_read_b64_tr_b16 a[74:75], v17 offset:520\n\t" \
  "ds_read_b64_tr_b16 a[76:77], v17 offset:4104\n\t" \
  "ds_read_b64_tr_b16 a[78:79], v17 offset:4616\n\t" \
  "ds_read_b64_tr_b16 a[144:145], v18 offset:0\n\t" \
  "ds_read_b64_tr_b16 a[146:147], v18 offset:512\n\t" \
  "ds_read_b64_tr_b16 a[148:149], v18 offset:4096\n\t" \
  "ds_read_b64_tr_b16 a[150:151], v18 offset:4608\n\t" \
  "ds_read_b64_tr_b16 a[216:217], v18 offset:8\n\t" \
  "ds_read_b64_tr_b16 a[218:219], v18 offset:520\n\t" \
  "ds_read_b64_tr_b16 a[220:221], v18 offset:4104\n\t" \
  "ds_read_b64_tr_b16 a[222:223], v18 offset:4616\n\t" \
  "s_waitcnt lgkmcnt(0)\n\t" \
  "s_add_u32 m0, s63, 0\n\t" \
  "s_mov_b32 s60, 2621440\n\t" \
  "buffer_load_dwordx4 v16, %[W], s60 offen lds\n\t" \
  "s_add_u32 m0, s63, 1024\n\t" \
  "s_mov_b32 s60, 2686976\n\t" \
  "buffer_load_dwordx4 v16, %[W], s60 offen lds\n\t" \
  "s_add_u32 m0, s63, 2048\n\t" \
  "s_mov_b32 s60, 2752512\n\t" \
  "buffer_load_dwordx4 v16, %[W], s60 offen lds\n\t" \
  "s_add_u32 m0, s63, 3072\n\t" \
  "s_mov_b32 s60, 2818048\n\t" \
  "buffer_load_dwordx4 v16, %[W], s60 offen lds\n\t" \
  "s_add_u32 m0, s63, 4096\n\t" \
  "s_mov_b32 s60, 2883584\n\t" \
  "buffer_load_dwordx4 v16, %[W], s60 offen lds\n\t" \
  "s_add_u32 m0, s63, 5120\n\t" \
  "s_mov_b32 s60, 2949120\n\t" \
  "buffer_load_dwordx4 v16, %[W], s60 offen lds\n\t" \
  "s_add_u32 m0, s63, 6144\n\t" \
  "s_mov_b32 s60, 3014656\n\t" \
  "buffer_load_dwordx4 v16, %[W], s60 offen lds\n\t" \
  "s_add_u32 m0, s63, 7168\n\t" \
  "s_mov_b32 s60, 3080192\n\t" \
  "buffer_load_dwordx4 v16, %[W], s60 offen lds\n\t" \
  "s_waitcnt vmcnt(32)\n\t" \
  "ds_read_b64_tr_b16 a[8:9], v17 offset:8192\n\t" \
  "ds_read_b64_tr_b16 a[10:11], v17 offset:8704\n\t" \
  "ds_read_b64_tr_b16 a[12:13], v17 offset:12288\n\t" \
  "ds_read_b64_tr_b16 a[14:15], v17 offset:12800\n\t" \
  "ds_read_b64_tr_b16 a[80:81], v17 offset:8200\n\t" \
  "ds_read_b64_tr_b16 a[82:83], v17 offset:8712\n\t" \
  "ds_read_b64_tr_b16 a[84:85], v17 offset:12296\n\t" \
  "ds_read_b64_tr_b16 a[86:87], v17 offset:12808\n\t" \
  "ds_read_b64_tr_b16 a[152:153], v18 offset:8192\n\t" \
  "ds_read_b64_tr_b16 a[154:155], v18 offset:8704\n\t" \
  "ds_read_b64_tr_b16 a[156:157], v18 offset:12288\n\t" \
  "ds_read_b64_tr_b16 a[158:159], v18 offset:12800\n\t" \
  "ds_read_b64_tr_b16 a[224:225], v18 offset:8200\n\t" \
  "ds_read_b64_tr_b16 a[226:227], v18 offset:8712\n\t" \
  "ds_read_b64_tr_b16 a[228:229], v18 offset:12296\n\t" \
  "ds_read_b64_tr_b16 a[230:231], v18 offset:12808\n\t" \
  "s_waitcnt lgkmcnt(0)\n\t" \
  "s_add_u32 m0, s63, 8192\n\t" \
  "s_mov_b32 s60, 3145728\n\t" \
  "buffer_load_dwordx4 v16, %[W], s60 offen lds\n\t" \
  "s_add_u32 m0, s63, 9216\n\t" \
  "s_mov_b32 s60, 3211264\n\t" \
  "buffer_load_dwordx4 v16, %[W], s60 offen lds\n\t" \
  "s_add_u32 m0, s63, 10240\n\t" \
  "s_mov_b32 s60, 3276800\n\t" \
  "buffer_load_dwordx4 v16, %[W], s60 offen lds\n\t" \
  "s_add_u32 m0, s63, 11264\n\t" \
  "s_mov_b32 s60, 3342336\n\t" \
  "buffer_load_dwordx4 v16, %[W], s60 offen lds\n\t" \
  "s_add_u32 m0, s63, 12288\n\t" \
  "s_mov_b32 s60, 3407872\n\t" \
  "buffer_load_dwordx4 v16, %[W], s60 offen lds\n\t" \
  "s_add_u32 m0, s63, 13312\n\t" \
  "s_mov_b32 s60, 3473408\n\t" \
  "buffer_load_dwordx4 v16, %[W], s60 offen lds\n\t" \
  "s_add_u32 m0, s63, 14336\n\t" \
  "s_mov_b32 s60, 3538944\n\t" \
  "buffer_load_dwordx4 v16, %[W], s60 offen lds\n\t" \
  "s_add_u32 m0, s63, 15360\n\t" \
  "s_mov_b32 s60, 3604480\n\t" \
  "buffer_load_dwordx4 v16, %[W], s60 offen lds\n\t" \
  "s_waitcnt vmcnt(32)\n\t" \
  "ds_read_b64_tr_b16 a[16:17], v17 offset:16384\n\t" \
  "ds_read_b64_tr_b16 a[18:19], v17 offset:16896\n\t" \
  "ds_read_b64_tr_b16 a[20:21], v17 offset:20480\n\t" \
  "ds_read_b64_tr_b16 a[22:23], v17 offset:20992\n\t" \
  "ds_read_b64_tr_b16 a[88:89], v17 offset:16392\n\t" \
  "ds_read_b64_tr_b16 a[90:91], v17 offset:16904\n\t" \
  "ds_read_b64_tr_b16 a[92:93], v17 offset:20488\n\t" \
  "ds_read_b64_tr_b16 a[94:95], v17 offset:21000\n\t" \
  "ds_read_b64_tr_b16 a[160:161], v18 offset:16384\n\t" \
  "ds_read_b64_tr_b16 a[162:163], v18 offset:16896\n\t" \
  "ds_read_b64_tr_b16 a[164:165], v18 offset:20480\n\t" \
  "ds_read_b64_tr_b16 a[166:167], v18 offset:20992\n\t" \
  "ds_read_b64_tr_b16 a[232:233], v18 offset:16392\n\t" \
  "ds_read_b64_tr_b16 a[234:235], v18 offset:16904\n\t" \
  "ds_read_b64_tr_b16 a[236:237], v18 offset:20488\n\t" \
  "ds_read_b64_tr_b16 a[238:239], v18 offset:21000\n\t" \
  "s_waitcnt lgkmcnt(0)\n\t" \
  "s_add_u32 m0, s63, 16384\n\t" \
  "s_mov_b32 s60, 3670016\n\t" \
  "buffer_load_dwordx4 v16, %[W], s60 offen lds\n\t" \
  "s_add_u32 m0, s63, 17408\n\t" \
  "s_mov_b32 s60, 3735552\n\t" \
  "buffer_load_dwordx4 v16, %[W], s60 offen lds\n\t" \
  "s_add_u32 m0, s63, 18432\n\t" \
  "s_mov_b32 s60, 3801088\n\t" \
  "buffer_load_dwordx4 v16, %[W], s60 offen lds\n\t" \
  "s_add_u32 m0, s63, 19456\n\t" \
  "s_mov_b32 s60, 3866624\n\t" \
  "buffer_load_dwordx4 v16, %[W], s60 offen lds\n\t" \
  "s_add_u32 m0, s63, 20480\n\t" \
  "s_mov_b32 s60, 3932160\n\t" \
  "buffer_load_dwordx4 v16, %[W], s60 offen lds\n\t" \
  "s_add_u32 m0, s63, 21504\n\t" \
  "s_mov_b32 s60, 3997696\n\t" \
  "buffer_load_dwordx4 v16, %[W], s60 offen lds\n\t" \
  "s_add_u32 m0, s63, 22528\n\t" \
  "s_mov_b32 s60, 4063232\n\t" \
  "buffer_load_dwordx4 v16, %[W], s60 offen lds\n\t" \
  "s_add_u32 m0, s63, 23552\n\t" \
  "s_mov_b32 s60, 4128768\n\t" \
  "buffer_load_dwordx4 v16, %[W], s60 offen lds\n\t" \
  "s_waitcnt vmcnt(32)\n\t" \
  "ds_read_b64_tr_b16 a[24:25], v17 offset:24576\n\t" \
  "ds_read_b64_tr_b16 a[26:27], v17 offset:25088\n\t" \
  "ds_read_b64_tr_b16 a[28:29], v17 offset:28672\n\t" \
  "ds_read_b64_tr_b16 a[30:31], v17 offset:29184\n\t" \
  "ds_read_b64_tr_b16 a[96:97], v17 offset:24584\n\t" \
  "ds_read_b64_tr_b16 a[98:99], v17 offset:25096\n\t" \
  "ds_read_b64_tr_b16 a[100:101], v17 offset:28680\n\t" \
  "ds_read_b64_tr_b16 a[102:103], v17 offset:29192\n\t" \
  "ds_read_b64_tr_b16 a[168:169], v18 offset:24576\n\t" \
  "ds_read_b64_tr_b16 a[170:171], v18 offset:25088\n\t" \
  "ds_read_b64_tr_b16 a[172:173], v18 offset:28672\n\t" \
  "ds_read_b64_tr_b16 a[174:175], v18 offset:29184\n\t" \
  "ds_read_b64_tr_b16 a[240:241], v18 offset:24584\n\t" \
  "ds_read_b64_tr_b16 a[242:243], v18 offset:25096\n\t" \
  "ds_read_b64_tr_b16 a[244:245], v18 offset:28680\n\t" \
  "ds_read_b64_tr_b16 a[246:247], v18 offset:29192\n\t" \
  "s_waitcnt lgkmcnt(0)\n\t" \
  "s_add_u32 m0, s63, 24576\n\t" \
  "s_mov_b32 s60, 4194304\n\t" \
  "buffer_load_dwordx4 v16, %[W], s60 offen lds\n\t" \
  "s_add_u32 m0, s63, 25600\n\t" \
  "s_mov_b32 s60, 4259840\n\t" \
  "buffer_load_dwordx4 v16, %[W], s60 offen lds\n\t" \
  "s_add_u32 m0, s63, 26624\n\t" \
  "s_mov_b32 s60, 4325376\n\t" \
  "buffer_load_dwordx4 v16, %[W], s60 offen lds\n\t" \
  "s_add_u32 m0, s63, 27648\n\t" \
  "s_mov_b32 s60, 4390912\n\t" \
  "buffer_load_dwordx4 v16, %[W], s60 offen lds\n\t" \
  "s_add_u32 m0, s63, 28672\n\t" \
  "s_mov_b32 s60, 4456448\n\t" \
  "buffer_load_dwordx4 v16, %[W], s60 offen lds\n\t" \
  "s_add_u32 m0, s63, 29696\n\t" \
  "s_mov_b32 s60, 4521984\n\t" \
  "buffer_load_dwordx4 v16, %[W], s60 offen lds\n\t" \
  "s_add_u32 m0, s63, 30720\n\t" \
  "s_mov_b32 s60, 4587520\n\t" \
  "buffer_load_dwordx4 v16, %[W], s60 offen lds\n\t" \
  "s_add_u32 m0, s63, 31744\n\t" \
  "s_mov_b32 s60, 4653056\n\t" \
  "buffer_load_dwordx4 v16, %[W], s60 offen lds\n\t" \
  "s_waitcnt vmcnt(32)\n\t" \
  "ds_read_b64_tr_b16 a[32:33], v17 offset:32768\n\t" \
  "ds_read_b64_tr_b16 a[34:35], v17 offset:33280\n\t" \
  "ds_read_b64_tr_b16 a[36:37], v17 offset:36864\n\t" \
  "ds_read_b64_tr_b16 a[38:39], v17 offset:37376\n\t" \
  "ds_read_b64_tr_b16 a[104:105], v17 offset:32776\n\t" \
  "ds_read_b64_tr_b16 a[106:107], v17 offset:33288\n\t" \
  "ds_read_b64_tr_b16 a[108:109], v17 offset:36872\n\t" \
  "ds_read_b64_tr_b16 a[110:111], v17 offset:37384\n\t" \
  "ds_read_b64_tr_b16 a[176:177], v18 offset:32768\n\t" \
  "ds_read_b64_tr_b16 a[178:179], v18 offset:33280\n\t" \
  "ds_read_b64_tr_b16 a[180:181], v18 offset:36864\n\t" \
  "ds_read_b64_tr_b16 a[182:183], v18 offset:37376\n\t" \
  "ds_read_b64_tr_b16 a[248:249], v18 offset:32776\n\t" \
  "ds_read_b64_tr_b16 a[250:251], v18 offset:33288\n\t" \
  "ds_read_b64_tr_b16 a[252:253], v18 offset:36872\n\t" \
  "ds_read_b64_tr_b16 a[254:255], v18 offset:37384\n\t" \
  "s_waitcnt lgkmcnt(0)\n\t" \
  "s_waitcnt vmcnt(24)\n\t" \
  "ds_read_b64_tr_b16 a[40:41], v17 offset:0\n\t" \
  "ds_read_b64_tr_b16 a[42:43], v17 offset:512\n\t" \
  "ds_read_b64_tr_b16 a[44:45], v17 offset:4096\n\t" \
  "ds_read_b64_tr_b16 a[46:47], v17 offset:4608\n\t" \
  "ds_read_b64_tr_b16 a[112:113], v17 offset:8\n\t" \
  "ds_read_b64_tr_b16 a[114:115], v17 offset:520\n\t" \
  "ds_read_b64_tr_b16 a[116:117], v17 offset:4104\n\t" \
  "ds_read_b64_tr_b16 a[118:119], v17 offset:4616\n\t" \
  "ds_read_b64_tr_b16 a[184:185], v18 offset:0\n\t" \
  "ds_read_b64_tr_b16 a[186:187], v18 offset:512\n\t" \
  "ds_read_b64_tr_b16 a[188:189], v18 offset:4096\n\t" \
  "ds_read_b64_tr_b16 a[190:191], v18 offset:4608\n\t" \
  "ds_read_b64_tr_b16 v[224:225], v18 offset:8\n\t" \
  "ds_read_b64_tr_b16 v[226:227], v18 offset:520\n\t" \
  "ds_read_b64_tr_b16 v[228:229], v18 offset:4104\n\t" \
  "ds_read_b64_tr_b16 v[230:231], v18 offset:4616\n\t" \
  "s_waitcnt lgkmcnt(0)\n\t" \
  "s_waitcnt vmcnt(16)\n\t" \
  "ds_read_b64_tr_b16 a[48:49], v17 offset:8192\n\t" \
  "ds_read_b64_tr_b16 a[50:51], v17 offset:8704\n\t" \
  "ds_read_b64_tr_b16 a[52:53], v17 offset:12288\n\t" \
  "ds_read_b64_tr_b16 a[54:55], v17 offset:12800\n\t" \
  "ds_read_b64_tr_b16 a[120:121], v17 offset:8200\n\t" \
  "ds_read_b64_tr_b16 a[122:123], v17 offset:8712\n\t" \
  "ds_read_b64_tr_b16 a[124:125], v17 offset:12296\n\t" \
  "ds_read_b64_tr_b16 a[126:127], v17 offset:12808\n\t" \
  "ds_read_b64_tr_b16 a[192:193], v18 offset:8192\n\t" \
  "ds_read_b64_tr_b16 a[194:195], v18 offset:8704\n\t" \
  "ds_read_b64_tr_b16 a[196:197], v18 offset:12288\n\t" \
  "ds_read_b64_tr_b16 a[198:199], v18 offset:12800\n\t" \
  "ds_read_b64_tr_b16 v[232:233], v18 offset:8200\n\t" \
  "ds_read_b64_tr_b16 v[234:235], v18 offset:8712\n\t" \
  "ds_read_b64_tr_b16 v[236:237], v18 offset:12296\n\t" \
  "ds_read_b64_tr_b16 v[238:239], v18 offset:12808\n\t" \
  "s_waitcnt lgkmcnt(0)\n\t" \
  "s_waitcnt vmcnt(8)\n\t" \
  "ds_read_b64_tr_b16 a[56:57], v17 offset:16384\n\t" \
  "ds_read_b64_tr_b16 a[58:59], v17 offset:16896\n\t" \
  "ds_read_b64_tr_b16 a[60:61], v17 offset:20480\n\t" \
  "ds_read_b64_tr_b16 a[62:63], v17 offset:20992\n\t" \
  "ds_read_b64_tr_b16 a[128:129], v17 offset:16392\n\t" \
  "ds_read_b64_tr_b16 a[130:131], v17 offset:16904\n\t" \
  "ds_read_b64_tr_b16 a[132:133], v17 offset:20488\n\t" \
  "ds_read_b64_tr_b16 a[134:135], v17 offset:21000\n\t" \
  "ds_read_b64_tr_b16 a[200:201], v18 offset:16384\n\t" \
  "ds_read_b64_tr_b16 a[202:203], v18 offset:16896\n\t" \
  "ds_read_b64_tr_b16 a[204:205], v18 offset:20480\n\t" \
  "ds_read_b64_tr_b16 a[206:207], v18 offset:20992\n\t" \
  "ds_read_b64_tr_b16 v[240:241], v18 offset:16392\n\t" \
  "ds_read_b64_tr_b16 v[242:243], v18 offset:16904\n\t" \
  "ds_read_b64_tr_b16 v[244:245], v18 offset:20488\n\t" \
  "ds_read_b64_tr_b16 v[246:247], v18 offset:21000\n\t" \
  "s_waitcnt lgkmcnt(0)\n\t" \
  "s_waitcnt vmcnt(0)\n\t" \
  "ds_read_b64_tr_b16 a[64:65], v17 offset:24576\n\t" \
  "ds_read_b64_tr_b16 a[66:67], v17 offset:25088\n\t" \
  "ds_read_b64_tr_b16 a[68:69], v17 offset:28672\n\t" \
  "ds_read_b64_tr_b16 a[70:71], v17 offset:29184\n\t" \
  "ds_read_b64_tr_b16 a[136:137], v17 offset:24584\n\t" \
  "ds_read_b64_tr_b16 a[138:139], v17 offset:25096\n\t" \
  "ds_read_b64_tr_b16 a[140:141], v17 offset:28680\n\t" \
  "ds_read_b64_tr_b16 a[142:143], v17 offset:29192\n\t" \
  "ds_read_b64_tr_b16 a[208:209], v18 offset:24576\n\t" \
  "ds_read_b64_tr_b16 a[210:211], v18 offset:25088\n\t" \
  "ds_read_b64_tr_b16 a[212:213], v18 offset:28672\n\t" \
  "ds_read_b64_tr_b16 a[214:215], v18 offset:29184\n\t" \
  "ds_read_b64_tr_b16 v[248:249], v18 offset:24584\n\t" \
  "ds_read_b64_tr_b16 v[250:251], v18 offset:25096\n\t" \
  "ds_read_b64_tr_b16 v[252:253], v18 offset:28680\n\t" \
  "ds_read_b64_tr_b16 v[254:255], v18 offset:29192\n\t" \
  "s_waitcnt lgkmcnt(0)\n\t" \
  "s_mov_b32 s84, " TK_CAP "\n\t" \
  "s_mov_b32 s48, 0\n\t" \
  "s_mov_b32 s49, 0xf0000000\n\t" \
  "s_mov_b32 s46, s41\n\t" \
  "s_mov_b32 s56, 0\n\t" \
  "v_lshrrev_b32 v7, 2, v0\n\t" \
  "s_mov_b32 s60, " TK_CTAB "\n\t" \
  "buffer_load_dword v4, v7, %[X], s60 offen offset:88 sc1\n\t" \
  "buffer_load_dword v7, v7, %[X], s60 offen offset:0 sc1\n\t" \
  "s_waitcnt vmcnt(0)\n\t" \
  "v_and_b32 v4, 1, v4\n\t" \
  "v_and_b32 v7, 1, v7\n\t" \
  "v_cmp_ne_u32 s[90:91], s67, v4\n\t" \
  "v_cmp_ne_u32 s[88:89], s67, v7\n\t" \
  "s_nop 1\n\t" \
  "s_mov_b32 s57, 0xf0000000\n\t" \
  "s_add_u32 s83, s48, 0\n\t" \
  "s_lshr_b64 s[60:61], s[90:91], s83\n\t" \
  "s_and_b32 s60, s60, 1\n\t" \
  "s_lshl_b32 s60, s60, 13\n\t" \
  "s_add_u32 s83, s83, " TK_SG0 "\n\t" \
  "s_lshl_b32 s83, s83, 21\n\t" \
  "s_or_b32 s83, s83, s60\n\t" \
  "s_add_u32 s80, s48, " TK_TRN "\n\t" \
  "s_lshr_b64 s[60:61], s[90:91], s80\n\t" \
  "s_and_b32 s60, s60, 1\n\t" \
  "s_lshl_b32 s60, s60, 13\n\t" \
  "s_add_u32 s80, s80, " TK_SG0 "\n\t" \
  "s_lshl_b32 s80, s80, 21\n\t" \
  "s_or_b32 s80, s80, s60\n\t" \
  "s_add_u32 s62, s83, s39\n\t" \
  "s_add_u32 s80, s80, s39\n\t" \
  "s_add_u32 s80, s80, 2048\n\t" \
  "s_cmp_lt_u32 s48, " TK_TRM "\n\t" \
  "s_cselect_b32 s80, s62, s80\n\t" \
  "v_add_u32 v7, s62, v3\n\t" \
  "s_mov_b32 exec_lo, 0\n\t" \
  "v_mov_b32 v7, s80\n\t" \
  "s_mov_b32 exec_lo, -1\n\t" \
  "LCOLD_%=:\n\t" \
  "s_waitcnt vmcnt(0)\n\t" \
  "s_mov_b64 exec, 1\n\t" \
  "buffer_store_dword v6, off, %[X], s49 sc1\n\t" \
  "s_mov_b64 exec, -1\n\t" \
  "s_mov_b32 s49, 0xf0000000\n\t" \
  "s_add_u32 s55, s83, s36\n\t" \
  "s_cmp_lg_u32 s40, 0\n\t" \
  "s_cbranch_scc1 LGO_%=\n\t" \
  "s_and_b32 s62, s45, 3\n\t" \
  "s_lshl_b32 s62, s62, 7\n\t" \
  "s_add_u32 s62, s62, s83\n\t" \
  "s_add_u32 s62, s62, s39\n\t" \
  "s_mov_b32 s61, 0\n\t" \
  "LPO_%=:\n\t" \
  "buffer_load_dword v4, off, %[X], s62 sc1\n\t" \
  "s_waitcnt vmcnt(0)\n\t" \
  "v_cmp_eq_u32 vcc, 0, v4\n\t" \
  "s_cbranch_vccz LPO_%=x\n\t" \
  "s_add_u32 s61, s61, 1\n\t" \
  "s_sleep " TK_PSH "\n\t" \
  "s_cmp_gt_u32 s61, s84\n\t" \
  "s_cbranch_scc1 LPO_%=g\n\t" \
  "s_and_b32 s60, s61, 1023\n\t" \
  "s_cmp_lg_u32 s60, 0\n\t" \
  "s_cbranch_scc1 LPO_%=\n\t" \
  "buffer_load_dword v4, off, %[X], s92 sc1\n\t" \
  "s_waitcnt vmcnt(0)\n\t" \
  "v_readfirstlane_b32 s60, v4\n\t" \
  "s_nop 0\n\t" \
  "s_cmp_eq_u32 s60, 0\n\t" \
  "s_cbranch_scc1 LPO_%=\n\t" \
  "LPO_%=g:\n\t" \
  "s_mov_b32 s84, 0\n\t" \
  "s_or_b32 s60, s48, 1610612736\n\t" \
  "v_mov_b32 v4, s60\n\t" \
  "s_mov_b64 exec, 1\n\t" \
  "buffer_store_dword v4, off, %[X], s92 sc1\n\t" \
  "s_mov_b64 exec, -1\n\t" \
  "LPO_%=x:\n\t" \
  "s_mov_b32 s61, 0\n\t" \
  "LPH_%=:\n\t" \
  "buffer_load_dword v4, v7, %[X], 0 offen sc1\n\t" \
  "s_waitcnt vmcnt(0)\n\t" \
  "v_cmp_eq_u32 vcc, 0, v4\n\t" \
  "s_cbranch_vccz LPH_%=x\n\t" \
  "s_add_u32 s61, s61, 1\n\t" \
  "s_sleep " TK_PSH "\n\t" \
  "s_cmp_gt_u32 s61, s84\n\t" \
  "s_cbranch_scc1 LPH_%=g\n\t" \
  "s_and_b32 s60, s61, 1023\n\t" \
  "s_cmp_lg_u32 s60, 0\n\t" \
  "s_cbranch_scc1 LPH_%=\n\t" \
  "buffer_load_dword v4, off, %[X], s92 sc1\n\t" \
  "s_waitcnt vmcnt(0)\n\t" \
  "v_readfirstlane_b32 s60, v4\n\t" \
  "s_nop 0\n\t" \
  "s_cmp_eq_u32 s60, 0\n\t" \
  "s_cbranch_scc1 LPH_%=\n\t" \
  "LPH_%=g:\n\t" \
  "s_mov_b32 s84, 0\n\t" \
  "s_or_b32 s60, s48, 1610612736\n\t" \
  "v_mov_b32 v4, s60\n\t" \
  "s_mov_b64 exec, 1\n\t" \
  "buffer_store_dword v4, off, %[X], s92 sc1\n\t" \
  "s_mov_b64 exec, -1\n\t" \
  "LPH_%=x:\n\t" \
  "s_cmp_lt_i32 s57, 0\n\t" \
  "s_cbranch_scc1 LGO_%=\n\t" \
  "s_movk_i32 s63, 4000\n\t" \
  "s_branch LGC_%=u\n\t" \
  "LGC_%=:\n\t" \
  "s_sleep " TK_PSG "\n\t" \
  "buffer_load_dword v8, off, %[X], s57 sc1\n\t" \
  "s_waitcnt vmcnt(0)\n\t" \
  "LGC_%=u:\n\t" \
  "s_memrealtime s[52:53]\n\t" \
  "v_readfirstlane_b32 s60, v8\n\t" \
  "s_waitcnt lgkmcnt(0)\n\t" \
  "s_sub_u32 s63, s63, 1\n\t" \
  "s_cbranch_scc1 LGO_%=\n\t" \
  "s_cmp_eq_u32 s60, -1\n\t" \
  "s_cbranch_scc1 LGC_%=\n\t" \
  "s_not_b32 s60, s60\n\t" \
  "s_lshl_b32 s61, s52, 8\n\t" \
  "s_sub_u32 s60, s60, s61\n\t" \
  "s_ashr_i32 s60, s60, 8\n\t" \
  "s_sub_i32 s60, s60, " TK_QXC "\n\t" \
  "s_min_i32 s60, s60, 400\n\t" \
  "s_add_u32 s54, s52, s60\n\t" \
  "LGC_%=w:\n\t" \
  "s_sleep 1\n\t" \
  "s_memrealtime s[52:53]\n\t" \
  "s_waitcnt lgkmcnt(0)\n\t" \
  "s_sub_i32 s53, s52, s54\n\t" \
  "s_cmp_lt_i32 s53, 0\n\t" \
  "s_cbranch_scc1 LGC_%=w\n\t" \
  "LGO_%=:\n\t" \
  "s_barrier\n\t" \
  "s_add_u32 m0, s46, 0\n\t" \
  "s_add_u32 s47, s55, 0\n\t" \
  "buffer_load_dwordx4 v0, %[X], s47 offen sc1 lds\n\t" \
  "s_add_u32 m0, s46, 4096\n\t" \
  "s_add_u32 s47, s55, 4096\n\t" \
  "buffer_load_dwordx4 v0, %[X], s47 offen sc1 lds\n\t" \
  "s_add_u32 m0, s46, 8192\n\t" \
  "s_add_u32 s47, s55, 16384\n\t" \
  "buffer_load_dwordx4 v0, %[X], s47 offen sc1 lds\n\t" \
  "s_add_u32 m0, s46, 12288\n\t" \
  "s_add_u32 s47, s55, 20480\n\t" \
  "buffer_load_dwordx4 v0, %[X], s47 offen sc1 lds\n\t" \
  "s_add_u32 m0, s46, 16384\n\t" \
  "s_add_u32 s47, s55, 32768\n\t" \
  "buffer_load_dwordx4 v0, %[X], s47 offen sc1 lds\n\t" \
  "s_add_u32 m0, s46, 20480\n\t" \
  "s_add_u32 s47, s55, 36864\n\t" \
  "buffer_load_dwordx4 v0, %[X], s47 offen sc1 lds\n\t" \
  "s_add_u32 m0, s46, 24576\n\t" \
  "s_add_u32 s47, s55, 49152\n\t" \
  "buffer_load_dwordx4 v0, %[X], s47 offen sc1 lds\n\t" \
  "s_add_u32 m0, s46, 28672\n\t" \
  "s_add_u32 s47, s55, 53248\n\t" \
  "buffer_load_dwordx4 v0, %[X], s47 offen sc1 lds\n\t" \
  "s_add_u32 m0, s46, 32768\n\t" \
  "s_add_u32 s47, s55, 65536\n\t" \
  "buffer_load_dwordx4 v0, %[X], s47 offen sc1 lds\n\t" \
  "s_waitcnt vmcnt(5)\n\t" \
  "LTOP_%=:\n\t" \
  "s_barrier\n\t" \
  "ds_read_b128 v[64:67], v1 offset:0\n\t" \
  "ds_read_b128 v[68:71], v1 offset:2048\n\t" \
  "ds_read_b128 v[72:75], v2 offset:0\n\t" \
  "ds_read_b128 v[76:79], v2 offset:2048\n\t" \
  "ds_read_b128 v[80:83], v1 offset:4096\n\t" \
  "ds_read_b128 v[84:87], v1 offset:6144\n\t" \
  "s_mov_b32 s82, s83\n\t" \
  "s_xor_b32 s46, s46, 0x10000\n\t" \
  "s_waitcnt lgkmcnt(5)\n\t" \
  "v_mfma_f32_16x16x32_bf16 v[16:19], a[0:3], v[64:67], 0\n\t" \
  "ds_read_b128 v[88:91], v2 offset:4096\n\t" \
  "v_mfma_f32_16x16x32_bf16 v[20:23], a[72:75], v[64:67], 0\n\t" \
  "s_add_u32 s50, s48, 0\n\t" \
  "s_and_b32 s50, s50, " TK_TRM "\n\t" \
  "v_mfma_f32_16x16x32_bf16 v[24:27], a[144:147], v[64:67], 0\n\t" \
  "ds_read_b128 v[92:95], v2 offset:6144\n\t" \
  "v_mfma_f32_16x16x32_bf16 v[28:31], a[216:219], v[64:67], 0\n\t" \
  "s_lshr_b64 s[60:61], s[90:91], s50\n\t" \
  "s_and_b32 s60, s60, 1\n\t" \
  "s_waitcnt lgkmcnt(5)\n\t" \
  "v_mfma_f32_16x16x32_bf16 v[16:19], a[4:7], v[72:75], v[16:19]\n\t" \
  "ds_read_b128 v[96:99], v1 offset:8192\n\t" \
  "v_mfma_f32_16x16x32_bf16 v[20:23], a[76:79], v[72:75], v[20:23]\n\t" \
  "s_lshl_b32 s60, s60, 13\n\t" \
  "s_add_u32 s50, s50, " TK_SG0 "\n\t" \
  "v_mfma_f32_16x16x32_bf16 v[24:27], a[148:151], v[72:75], v[24:27]\n\t" \
  "ds_read_b128 v[100:103], v1 offset:10240\n\t" \
  "v_mfma_f32_16x16x32_bf16 v[28:31], a[220:223], v[72:75], v[28:31]\n\t" \
  "s_lshl_b32 s50, s50, 21\n\t" \
  "s_or_b32 s50, s50, s60\n\t" \
  "s_waitcnt lgkmcnt(5)\n\t" \
  "v_mfma_f32_16x16x32_bf16 v[16:19], a[8:11], v[80:83], v[16:19]\n\t" \
  "ds_read_b128 v[104:107], v2 offset:8192\n\t" \
  "v_mfma_f32_16x16x32_bf16 v[20:23], a[80:83], v[80:83], v[20:23]\n\t" \
  "s_add_u32 s50, s50, s37\n\t" \
  "s_add_u32 s51, s50, 4096\n\t" \
  "v_mfma_f32_16x16x32_bf16 v[24:27], a[152:155], v[80:83], v[24:27]\n\t" \
  "ds_read_b128 v[108:111], v2 offset:10240\n\t" \
  "v_mfma_f32_16x16x32_bf16 v[28:31], a[224:227], v[80:83], v[28:31]\n\t" \
  "s_add_u32 s83, s48, 1\n\t" \
  "s_lshr_b64 s[60:61], s[90:91], s83\n\t" \
  "s_waitcnt lgkmcnt(5)\n\t" \
  "v_mfma_f32_16x16x32_bf16 v[16:19], a[12:15], v[88:91], v[16:19]\n\t" \
  "ds_read_b128 v[112:115], v1 offset:12288\n\t" \
  "v_mfma_f32_16x16x32_bf16 v[20:23], a[84:87], v[88:91], v[20:23]\n\t" \
  "s_and_b32 s60, s60, 1\n\t" \
  "s_lshl_b32 s60, s60, 13\n\t" \
  "v_mfma_f32_16x16x32_bf16 v[24:27], a[156:159], v[88:91], v[24:27]\n\t" \
  "ds_read_b128 v[116:119], v1 offset:14336\n\t" \
  "v_mfma_f32_16x16x32_bf16 v[28:31], a[228:231], v[88:91], v[28:31]\n\t" \
  "s_add_u32 s83, s83, " TK_SG0 "\n\t" \
  "s_lshl_b32 s83, s83, 21\n\t" \
  "s_waitcnt lgkmcnt(5)\n\t" \
  "v_mfma_f32_16x16x32_bf16 v[16:19], a[16:19], v[96:99], v[16:19]\n\t" \
  "ds_read_b128 v[120:123], v2 offset:12288\n\t" \
  "v_mfma_f32_16x16x32_bf16 v[20:23], a[88:91], v[96:99], v[20:23]\n\t" \
  "s_or_b32 s83, s83, s60\n\t" \
  "s_cmp_lg_u32 s56, 0\n\t" \
  "v_mfma_f32_16x16x32_bf16 v[24:27], a[160:163], v[96:99], v[24:27]\n\t" \
  "ds_read_b128 v[124:127], v2 offset:14336\n\t" \
  "v_mfma_f32_16x16x32_bf16 v[28:31], a[232:235], v[96:99], v[28:31]\n\t" \
  "s_cbranch_scc1 LWARM_%=\n\t" \
  "s_waitcnt vmcnt(0)\n\t" \
  "LWARM_%=:\n\t" \
  "s_barrier\n\t" \
  "s_waitcnt lgkmcnt(5)\n\t" \
  "v_mfma_f32_16x16x32_bf16 v[16:19], a[20:23], v[104:107], v[16:19]\n\t" \
  "ds_read_b128 v[128:131], v1 offset:16384\n\t" \
  "v_mfma_f32_16x16x32_bf16 v[20:23], a[92:95], v[104:107], v[20:23]\n\t" \
  "v_mfma_f32_16x16x32_bf16 v[24:27], a[164:167], v[104:107], v[24:27]\n\t" \
  "ds_read_b128 v[132:135], v1 offset:18432\n\t" \
  "v_mfma_f32_16x16x32_bf16 v[28:31], a[236:239], v[104:107], v[28:31]\n\t" \
  "s_add_i32 s62, s48, s42\n\t" \
  "s_add_i32 s62, s62, 1\n\t" \
  "s_waitcnt lgkmcnt(5)\n\t" \
  "v_mfma_f32_16x16x32_bf16 v[16:19], a[24:27], v[112:115], v[16:19]\n\t" \
  "ds_read_b128 v[136:139], v2 offset:16384\n\t" \
  "v_mfma_f32_16x16x32_bf16 v[20:23], a[96:99], v[112:115], v[20:23]\n\t" \
  "s_mov_b32 s61, s62\n\t" \
  "v_mfma_f32_16x16x32_bf16 v[24:27], a[168:171], v[112:115], v[24:27]\n\t" \
  "ds_read_b128 v[140:143], v2 offset:18432\n\t" \
  "v_mfma_f32_16x16x32_bf16 v[28:31], a[240:243], v[112:115], v[28:31]\n\t" \
  "s_mul_i32 s63, s62, 43\n\t" \
  "s_lshr_b32 s63, s63, 9\n\t" \
  "s_waitcnt lgkmcnt(5)\n\t" \
  "v_mfma_f32_16x16x32_bf16 v[16:19], a[28:31], v[120:123], v[16:19]\n\t" \
  "ds_read_b128 v[144:147], v1 offset:20480\n\t" \
  "v_mfma_f32_16x16x32_bf16 v[20:23], a[100:103], v[120:123], v[20:23]\n\t" \
  "s_mul_i32 s63, s63, " TK_RM "\n\t" \
  "s_sub_u32 s62, s62, s63\n\t" \
  "v_mfma_f32_16x16x32_bf16 v[24:27], a[172:175], v[120:123], v[24:27]\n\t" \
  "ds_read_b128 v[148:151], v1 offset:22528\n\t" \
  "v_mfma_f32_16x16x32_bf16 v[28:31], a[244:247], v[120:123], v[28:31]\n\t" \
  "s_mul_i32 s63, s62, 43\n\t" \
  "s_lshr_b32 s63, s63, 7\n\t" \
  "s_waitcnt lgkmcnt(5)\n\t" \
  "v_mfma_f32_16x16x32_bf16 v[16:19], a[32:35], v[128:131], v[16:19]\n\t" \
  "ds_read_b128 v[152:155], v2 offset:20480\n\t" \
  "v_mfma_f32_16x16x32_bf16 v[20:23], a[104:107], v[128:131], v[20:23]\n\t" \
  "s_mul_i32 s57, s62, 688128\n\t" \
  "s_lshl_b32 s60, s63, 15\n\t" \
  "v_mfma_f32_16x16x32_bf16 v[24:27], a[176:179], v[128:131], v[24:27]\n\t" \
  "ds_read_b128 v[156:159], v2 offset:22528\n\t" \
  "v_mfma_f32_16x16x32_bf16 v[28:31], a[248:251], v[128:131], v[28:31]\n\t" \
  "s_add_u32 s57, s57, s60\n\t" \
  "s_lshr_b32 s60, s88, s63\n\t" \
  "s_waitcnt lgkmcnt(5)\n\t" \
  "v_mfma_f32_16x16x32_bf16 v[16:19], a[36:39], v[136:139], v[16:19]\n\t" \
  "ds_read_b128 v[160:163], v1 offset:24576\n\t" \
  "v_mfma_f32_16x16x32_bf16 v[20:23], a[108:111], v[136:139], v[20:23]\n\t" \
  "s_and_b32 s60, s60, 1\n\t" \
  "s_lshl_b32 s60, s60, 13\n\t" \
  "v_mfma_f32_16x16x32_bf16 v[24:27], a[180:183], v[136:139], v[24:27]\n\t" \
  "ds_read_b128 v[164:167], v1 offset:26624\n\t" \
  "v_mfma_f32_16x16x32_bf16 v[28:31], a[252:255], v[136:139], v[28:31]\n\t" \
  "s_add_u32 s57, s57, s60\n\t" \
  "s_add_u32 s57, s57, 656896\n\t" \
  "s_waitcnt lgkmcnt(5)\n\t" \
  "v_mfma_f32_16x16x32_bf16 v[16:19], a[40:43], v[144:147], v[16:19]\n\t" \
  "ds_read_b128 v[168:171], v2 offset:24576\n\t" \
  "v_mfma_f32_16x16x32_bf16 v[20:23], a[112:115], v[144:147], v[20:23]\n\t" \
  "s_cmp_lt_i32 s61, 1\n\t" \
  "s_cselect_b32 s57, 0xf0000000, s57\n\t" \
  "v_mfma_f32_16x16x32_bf16 v[24:27], a[184:187], v[144:147], v[24:27]\n\t" \
  "ds_read_b128 v[172:175], v2 offset:26624\n\t" \
  "v_mfma_f32_16x16x32_bf16 v[28:31], v[224:227], v[144:147], v[28:31]\n\t" \
  "s_cmp_lg_u32 s40, 0\n\t" \
  "s_cselect_b32 s57, 0xf0000000, s57\n\t" \
  "s_waitcnt lgkmcnt(5)\n\t" \
  "v_mfma_f32_16x16x32_bf16 v[16:19], a[44:47], v[152:155], v[16:19]\n\t" \
  "ds_read_b128 v[176:179], v1 offset:28672\n\t" \
  "v_mfma_f32_16x16x32_bf16 v[20:23], a[116:119], v[152:155], v[20:23]\n\t" \
  "s_add_u32 s80, s48, " TK_TRN "\n\t" \
  "s_lshr_b64 s[60:61], s[90:91], s80\n\t" \
  "s_and_b32 s60, s60, 1\n\t" \
  "v_mfma_f32_16x16x32_bf16 v[24:27], a[188:191], v[152:155], v[24:27]\n\t" \
  "ds_read_b128 v[180:183], v1 offset:30720\n\t" \
  "v_mfma_f32_16x16x32_bf16 v[28:31], v[228:231], v[152:155], v[28:31]\n\t" \
  "s_lshl_b32 s60, s60, 13\n\t" \
  "s_add_u32 s80, s80, " TK_SG0 "\n\t" \
  "s_lshl_b32 s80, s80, 21\n\t" \
  "s_waitcnt lgkmcnt(5)\n\t" \
  "v_mfma_f32_16x16x32_bf16 v[16:19], a[48:51], v[160:163], v[16:19]\n\t" \
  "ds_read_b128 v[184:187], v2 offset:28672\n\t" \
  "v_mfma_f32_16x16x32_bf16 v[20:23], a[120:123], v[160:163], v[20:23]\n\t" \
  "s_or_b32 s80, s80, s60\n\t" \
  "s_add_u32 s62, s83, s39\n\t" \
  "s_add_u32 s80, s80, s39\n\t" \
  "v_mfma_f32_16x16x32_bf16 v[24:27], a[192:195], v[160:163], v[24:27]\n\t" \
  "ds_read_b128 v[188:191], v2 offset:30720\n\t" \
  "v_mfma_f32_16x16x32_bf16 v[28:31], v[232:235], v[160:163], v[28:31]\n\t" \
  "s_add_u32 s80, s80, 2048\n\t" \
  "s_waitcnt lgkmcnt(5)\n\t" \
  "v_mfma_f32_16x16x32_bf16 v[16:19], a[52:55], v[168:171], v[16:19]\n\t" \
  "ds_read_b128 v[192:195], v1 offset:32768\n\t" \
  "v_mfma_f32_16x16x32_bf16 v[20:23], a[124:127], v[168:171], v[20:23]\n\t" \
  "v_mfma_f32_16x16x32_bf16 v[24:27], a[196:199], v[168:171], v[24:27]\n\t" \
  "ds_read_b128 v[196:199], v1 offset:34816\n\t" \
  "v_mfma_f32_16x16x32_bf16 v[28:31], v[236:239], v[168:171], v[28:31]\n\t" \
  "s_cmp_lt_u32 s48, " TK_TRM "\n\t" \
  "s_cselect_b32 s80, s62, s80\n\t" \
  "s_waitcnt lgkmcnt(5)\n\t" \
  "v_mfma_f32_16x16x32_bf16 v[16:19], a[56:59], v[176:179], v[16:19]\n\t" \
  "ds_read_b128 v[200:203], v2 offset:32768\n\t" \
  "v_mfma_f32_16x16x32_bf16 v[20:23], a[128:131], v[176:179], v[20:23]\n\t" \
  "v_add_u32 v7, s62, v3\n\t" \
  "s_mov_b32 exec_lo, 0\n\t" \
  "v_mov_b32 v7, s80\n\t" \
  "s_mov_b32 exec_lo, -1\n\t" \
  "v_mfma_f32_16x16x32_bf16 v[24:27], a[200:203], v[176:179], v[24:27]\n\t" \
  "ds_read_b128 v[204:207], v2 offset:34816\n\t" \
  "v_mfma_f32_16x16x32_bf16 v[28:31], v[240:243], v[176:179], v[28:31]\n\t" \
  "s_waitcnt lgkmcnt(5)\n\t" \
  "v_mfma_f32_16x16x32_bf16 v[16:19], a[60:63], v[184:187], v[16:19]\n\t" \
  "v_mfma_f32_16x16x32_bf16 v[20:23], a[132:135], v[184:187], v[20:23]\n\t" \
  "buffer_load_dword v4, v7, %[X], 0 offen sc1\n\t" \
  "v_mfma_f32_16x16x32_bf16 v[24:27], a[204:207], v[184:187], v[24:27]\n\t" \
  "v_mfma_f32_16x16x32_bf16 v[28:31], v[244:247], v[184:187], v[28:31]\n\t" \
  "s_waitcnt lgkmcnt(3)\n\t" \
  "v_mfma_f32_16x16x32_bf16 v[16:19], a[64:67], v[192:195], v[16:19]\n\t" \
  "v_xor_b32 v1, 0x10000, v1\n\t" \
  "v_xor_b32 v2, 0x10000, v2\n\t" \
  "v_mfma_f32_16x16x32_bf16 v[20:23], a[136:139], v[192:195], v[20:23]\n\t" \
  "v_mfma_f32_16x16x32_bf16 v[24:27], a[208:211], v[192:195], v[24:27]\n\t" \
  "v_mfma_f32_16x16x32_bf16 v[28:31], v[248:251], v[192:195], v[28:31]\n\t" \
  "s_waitcnt lgkmcnt(1)\n\t" \
  "v_mfma_f32_16x16x32_bf16 v[16:19], a[68:71], v[200:203], v[16:19]\n\t" \
  "v_mfma_f32_16x16x32_bf16 v[20:23], a[140:143], v[200:203], v[20:23]\n\t" \
  "v_mfma_f32_16x16x32_bf16 v[24:27], a[212:215], v[200:203], v[24:27]\n\t" \
  "v_mfma_f32_16x16x32_bf16 v[28:31], v[252:255], v[200:203], v[28:31]\n\t" \
  "v_mfma_f32_16x16x32_bf16 v[32:35], a[0:3], v[68:71], 0\n\t" \
  "v_cvt_pk_bf16_f32 v48, v16, v17\n\t" \
  "v_cvt_pk_bf16_f32 v49, v18, v19\n\t" \
  "v_mfma_f32_16x16x32_bf16 v[36:39], a[72:75], v[68:71], 0\n\t" \
  "v_cvt_pk_bf16_f32 v50, v20, v21\n\t" \
  "v_cvt_pk_bf16_f32 v51, v22, v23\n\t" \
  "v_mfma_f32_16x16x32_bf16 v[32:35], a[4:7], v[76:79], v[32:35]\n\t" \
  "v_cvt_pk_bf16_f32 v52, v24, v25\n\t" \
  "v_cvt_pk_bf16_f32 v53, v26, v27\n\t" \
  "v_mfma_f32_16x16x32_bf16 v[36:39], a[76:79], v[76:79], v[36:39]\n\t" \
  "v_cvt_pk_bf16_f32 v54, v28, v29\n\t" \
  "v_cvt_pk_bf16_f32 v55, v30, v31\n\t" \
  "v_mfma_f32_16x16x32_bf16 v[32:35], a[8:11], v[84:87], v[32:35]\n\t" \
  "buffer_store_dwordx4 v[48:51], v0, %[X], s50 offen offset:0 sc1\n\t" \
  "v_mfma_f32_16x16x32_bf16 v[36:39], a[80:83], v[84:87], v[36:39]\n\t" \
  "v_mfma_f32_16x16x32_bf16 v[32:35], a[12:15], v[92:95], v[32:35]\n\t" \
  "v_mfma_f32_16x16x32_bf16 v[36:39], a[84:87], v[92:95], v[36:39]\n\t" \
  "v_mfma_f32_16x16x32_bf16 v[32:35], a[16:19], v[100:103], v[32:35]\n\t" \
  "v_mfma_f32_16x16x32_bf16 v[36:39], a[88:91], v[100:103], v[36:39]\n\t" \
  "v_mfma_f32_16x16x32_bf16 v[32:35], a[20:23], v[108:111], v[32:35]\n\t" \
  "v_mfma_f32_16x16x32_bf16 v[36:39], a[92:95], v[108:111], v[36:39]\n\t" \
  "v_mfma_f32_16x16x32_bf16 v[32:35], a[24:27], v[116:119], v[32:35]\n\t" \
  "v_mfma_f32_16x16x32_bf16 v[36:39], a[96:99], v[116:119], v[36:39]\n\t" \
  "v_mfma_f32_16x16x32_bf16 v[32:35], a[28:31], v[124:127], v[32:35]\n\t" \
  "v_mfma_f32_16x16x32_bf16 v[36:39], a[100:103], v[124:127], v[36:39]\n\t" \
  "s_waitcnt vmcnt(1)\n\t" \
  "v_cmp_eq_u32 vcc, 0, v4\n\t" \
  "v_mfma_f32_16x16x32_bf16 v[32:35], a[32:35], v[132:135], v[32:35]\n\t" \
  "v_mfma_f32_16x16x32_bf16 v[36:39], a[104:107], v[132:135], v[36:39]\n\t" \
  "s_cmp_eq_u64 vcc, 0\n\t" \
  "s_cselect_b32 s60, 1, 0\n\t" \
  "v_mov_b32 v4, s60\n\t" \
  "v_mfma_f32_16x16x32_bf16 v[32:35], a[36:39], v[140:143], v[32:35]\n\t" \
  "ds_write_b32 v5, v4\n\t" \
  "v_mfma_f32_16x16x32_bf16 v[36:39], a[108:111], v[140:143], v[36:39]\n\t" \
  "v_mfma_f32_16x16x32_bf16 v[32:35], a[40:43], v[148:151], v[32:35]\n\t" \
  "v_mfma_f32_16x16x32_bf16 v[36:39], a[112:115], v[148:151], v[36:39]\n\t" \
  "buffer_store_dwordx4 v[52:55], v0, %[X], s50 offen offset:1024 sc1\n\t" \
  "v_mfma_f32_16x16x32_bf16 v[32:35], a[44:47], v[156:159], v[32:35]\n\t" \
  "v_mfma_f32_16x16x32_bf16 v[36:39], a[116:119], v[156:159], v[36:39]\n\t" \
  "s_waitcnt lgkmcnt(0)\n\t" \
  "s_barrier\n\t" \
  "v_mfma_f32_16x16x32_bf16 v[32:35], a[48:51], v[164:167], v[32:35]\n\t" \
  "ds_read_b32 v4, v6 offset:40959\n\t" \
  "v_mfma_f32_16x16x32_bf16 v[36:39], a[120:123], v[164:167], v[36:39]\n\t" \
  "v_mfma_f32_16x16x32_bf16 v[32:35], a[52:55], v[172:175], v[32:35]\n\t" \
  "v_mfma_f32_16x16x32_bf16 v[36:39], a[124:127], v[172:175], v[36:39]\n\t" \
  "v_mfma_f32_16x16x32_bf16 v[32:35], a[56:59], v[180:183], v[32:35]\n\t" \
  "v_mfma_f32_16x16x32_bf16 v[36:39], a[128:131], v[180:183], v[36:39]\n\t" \
  "s_waitcnt lgkmcnt(0)\n\t" \
  "v_readfirstlane_b32 s60, v4\n\t" \
  "s_add_u32 s55, s83, s36\n\t" \
  "v_mfma_f32_16x16x32_bf16 v[32:35], a[60:63], v[188:191], v[32:35]\n\t" \
  "s_cmp_eq_u32 s48, 63\n\t" \
  "s_cselect_b32 s56, 0, s60\n\t" \
  "v_mfma_f32_16x16x32_bf16 v[36:39], a[132:135], v[188:191], v[36:39]\n\t" \
  "s_cmp_eq_u32 s56, 0\n\t" \
  "s_cselect_b32 s55, 0xf0000000, s55\n\t" \
  "v_mfma_f32_16x16x32_bf16 v[32:35], a[64:67], v[196:199], v[32:35]\n\t" \
  "v_mfma_f32_16x16x32_bf16 v[36:39], a[136:139], v[196:199], v[36:39]\n\t" \
  "v_mfma_f32_16x16x32_bf16 v[32:35], a[68:71], v[204:207], v[32:35]\n\t" \
  "s_add_u32 m0, s46, 0\n\t" \
  "s_add_u32 s47, s55, 0\n\t" \
  "buffer_load_dwordx4 v0, %[X], s47 offen sc1 lds\n\t" \
  "v_mfma_f32_16x16x32_bf16 v[36:39], a[140:143], v[204:207], v[36:39]\n\t" \
  "v_mfma_f32_16x16x32_bf16 v[40:43], a[144:147], v[68:71], 0\n\t" \
  "v_mfma_f32_16x16x32_bf16 v[44:47], a[216:219], v[68:71], 0\n\t" \
  "v_mfma_f32_16x16x32_bf16 v[40:43], a[148:151], v[76:79], v[40:43]\n\t" \
  "v_cvt_pk_bf16_f32 v56, v32, v33\n\t" \
  "v_cvt_pk_bf16_f32 v57, v34, v35\n\t" \
  "s_add_u32 m0, s46, 4096\n\t" \
  "s_add_u32 s47, s55, 4096\n\t" \
  "buffer_load_dwordx4 v0, %[X], s47 offen sc1 lds\n\t" \
  "v_mfma_f32_16x16x32_bf16 v[44:47], a[220:223], v[76:79], v[44:47]\n\t" \
  "v_cvt_pk_bf16_f32 v58, v36, v37\n\t" \
  "v_cvt_pk_bf16_f32 v59, v38, v39\n\t" \
  "v_mfma_f32_16x16x32_bf16 v[40:43], a[152:155], v[84:87], v[40:43]\n\t" \
  "v_mfma_f32_16x16x32_bf16 v[44:47], a[224:227], v[84:87], v[44:47]\n\t" \
  "buffer_store_dwordx4 v[56:59], v0, %[X], s51 offen offset:0 sc1\n\t" \
  "v_mfma_f32_16x16x32_bf16 v[40:43], a[156:159], v[92:95], v[40:43]\n\t" \
  "s_add_u32 m0, s46, 8192\n\t" \
  "s_add_u32 s47, s55, 16384\n\t" \
  "buffer_load_dwordx4 v0, %[X], s47 offen sc1 lds\n\t" \
  "v_mfma_f32_16x16x32_bf16 v[44:47], a[228:231], v[92:95], v[44:47]\n\t" \
  "v_mfma_f32_16x16x32_bf16 v[40:43], a[160:163], v[100:103], v[40:43]\n\t" \
  "v_mfma_f32_16x16x32_bf16 v[44:47], a[232:235], v[100:103], v[44:47]\n\t" \
  "v_mfma_f32_16x16x32_bf16 v[40:43], a[164:167], v[108:111], v[40:43]\n\t" \
  "s_add_u32 m0, s46, 12288\n\t" \
  "s_add_u32 s47, s55, 20480\n\t" \
  "buffer_load_dwordx4 v0, %[X], s47 offen sc1 lds\n\t" \
  "v_mfma_f32_16x16x32_bf16 v[44:47], a[236:239], v[108:111], v[44:47]\n\t" \
  "v_mfma_f32_16x16x32_bf16 v[40:43], a[168:171], v[116:119], v[40:43]\n\t" \
  "v_mfma_f32_16x16x32_bf16 v[44:47], a[240:243], v[116:119], v[44:47]\n\t" \
  "v_mfma_f32_16x16x32_bf16 v[40:43], a[172:175], v[124:127], v[40:43]\n\t" \
  "s_add_u32 m0, s46, 16384\n\t" \
  "s_add_u32 s47, s55, 32768\n\t" \
  "buffer_load_dwordx4 v0, %[X], s47 offen sc1 lds\n\t" \
  "v_mfma_f32_16x16x32_bf16 v[44:47], a[244:247], v[124:127], v[44:47]\n\t" \
  "v_mfma_f32_16x16x32_bf16 v[40:43], a[176:179], v[132:135], v[40:43]\n\t" \
  "v_mfma_f32_16x16x32_bf16 v[44:47], a[248:251], v[132:135], v[44:47]\n\t" \
  "v_mfma_f32_16x16x32_bf16 v[40:43], a[180:183], v[140:143], v[40:43]\n\t" \
  "s_add_u32 m0, s46, 20480\n\t" \
  "s_add_u32 s47, s55, 36864\n\t" \
  "buffer_load_dwordx4 v0, %[X], s47 offen sc1 lds\n\t" \
  "v_mfma_f32_16x16x32_bf16 v[44:47], a[252:255], v[140:143], v[44:47]\n\t" \
  "v_mfma_f32_16x16x32_bf16 v[40:43], a[184:187], v[148:151], v[40:43]\n\t" \
  "v_mfma_f32_16x16x32_bf16 v[44:47], v[224:227], v[148:151], v[44:47]\n\t" \
  "v_mfma_f32_16x16x32_bf16 v[40:43], a[188:191], v[156:159], v[40:43]\n\t" \
  "s_add_u32 m0, s46, 24576\n\t" \
  "s_add_u32 s47, s55, 49152\n\t" \
  "buffer_load_dwordx4 v0, %[X], s47 offen sc1 lds\n\t" \
  "v_mfma_f32_16x16x32_bf16 v[44:47], v[228:231], v[156:159], v[44:47]\n\t" \
  "v_mfma_f32_16x16x32_bf16 v[40:43], a[192:195], v[164:167], v[40:43]\n\t" \
  "v_mfma_f32_16x16x32_bf16 v[44:47], v[232:235], v[164:167], v[44:47]\n\t" \
  "v_mfma_f32_16x16x32_bf16 v[40:43], a[196:199], v[172:175], v[40:43]\n\t" \
  "s_add_u32 m0, s46, 28672\n\t" \
  "s_add_u32 s47, s55, 53248\n\t" \
  "buffer_load_dwordx4 v0, %[X], s47 offen sc1 lds\n\t" \
  "v_mfma_f32_16x16x32_bf16 v[44:47], v[236:239], v[172:175], v[44:47]\n\t" \
  "v_mfma_f32_16x16x32_bf16 v[40:43], a[200:203], v[180:183], v[40:43]\n\t" \
  "v_mfma_f32_16x16x32_bf16 v[44:47], v[240:243], v[180:183], v[44:47]\n\t" \
  "v_mfma_f32_16x16x32_bf16 v[40:43], a[204:207], v[188:191], v[40:43]\n\t" \
  "s_add_u32 m0, s46, 32768\n\t" \
  "s_add_u32 s47, s55, 65536\n\t" \
  "buffer_load_dwordx4 v0, %[X], s47 offen sc1 lds\n\t" \
  "v_mfma_f32_16x16x32_bf16 v[44:47], v[244:247], v[188:191], v[44:47]\n\t" \
  "v_mfma_f32_16x16x32_bf16 v[40:43], a[208:211], v[196:199], v[40:43]\n\t" \
  "v_mfma_f32_16x16x32_bf16 v[44:47], v[248:251], v[196:199], v[44:47]\n\t" \
  "buffer_load_dword v8, off, %[X], s57 sc1\n\t" \
  "v_mfma_f32_16x16x32_bf16 v[40:43], a[212:215], v[204:207], v[40:43]\n\t" \
  "v_mfma_f32_16x16x32_bf16 v[44:47], v[252:255], v[204:207], v[44:47]\n\t" \
  "s_nop 4\n\t" \
  "v_cvt_pk_bf16_f32 v60, v40, v41\n\t" \
  "v_cvt_pk_bf16_f32 v61, v42, v43\n\t" \
  "s_nop 1\n\t" \
  "v_cvt_pk_bf16_f32 v62, v44, v45\n\t" \
  "v_cvt_pk_bf16_f32 v63, v46, v47\n\t" \
  "buffer_store_dwordx4 v[60:63], v0, %[X], s51 offen offset:1024 sc1\n\t" \
  "s_add_u32 s49, s82, s38\n\t" \
  "s_cmp_eq_u32 s48, 63\n\t" \
  "s_cbranch_scc1 LEND_%=\n\t" \
  "s_add_u32 s48, s48, 1\n\t" \
  "s_cmp_eq_u32 s56, 0\n\t" \
  "s_cbranch_scc1 LCOLD_%=\n\t" \
  "s_waitcnt vmcnt(0)\n\t" \
  "s_mov_b64 exec, 1\n\t" \
  "buffer_store_dword v6, off, %[X], s49 sc1\n\t" \
  "s_mov_b64 exec, -1\n\t" \
  "s_mov_b32 s49, 0xf0000000\n\t" \
  "s_cmp_lt_i32 s57, 0\n\t" \
  "s_cbranch_scc1 LTOP_%=\n\t" \
  "s_movk_i32 s63, 4000\n\t" \
  "s_branch LGW_%=u\n\t" \
  "LGW_%=:\n\t" \
  "s_sleep " TK_PSG "\n\t" \
  "buffer_load_dword v8, off, %[X], s57 sc1\n\t" \
  "s_waitcnt vmcnt(0)\n\t" \
  "LGW_%=u:\n\t" \
  "s_memrealtime s[52:53]\n\t" \
  "v_readfirstlane_b32 s60, v8\n\t" \
  "s_waitcnt lgkmcnt(0)\n\t" \
  "s_sub_u32 s63, s63, 1\n\t" \
  "s_cbranch_scc1 LTOP_%=\n\t" \
  "s_cmp_eq_u32 s60, -1\n\t" \
  "s_cbranch_scc1 LGW_%=\n\t" \
  "s_not_b32 s60, s60\n\t" \
  "s_lshl_b32 s61, s52, 8\n\t" \
  "s_sub_u32 s60, s60, s61\n\t" \
  "s_ashr_i32 s60, s60, 8\n\t" \
  "s_sub_i32 s60, s60, " TK_QXW "\n\t" \
  "s_min_i32 s60, s60, 400\n\t" \
  "s_add_u32 s54, s52, s60\n\t" \
  "LGW_%=w:\n\t" \
  "s_sleep 1\n\t" \
  "s_memrealtime s[52:53]\n\t" \
  "s_waitcnt lgkmcnt(0)\n\t" \
  "s_sub_i32 s53, s52, s54\n\t" \
  "s_cmp_lt_i32 s53, 0\n\t" \
  "s_cbranch_scc1 LGW_%=w\n\t" \
  "s_branch LTOP_%=\n\t" \
  "LEND_%=:\n\t" \
  "s_waitcnt vmcnt(0)\n\t" \
  "s_mov_b64 exec, 1\n\t" \
  "buffer_store_dword v6, off, %[X], s49 sc1\n\t" \
  "s_mov_b64 exec, -1\n\t" \
  "s_mov_b32 s49, 0xf0000000\n\t" \
  ""
#define QV_ASM \
  "v_lshrrev_b32 v16, 7, v0\n\t" \
  "v_lshlrev_b32 v16, 13, v16\n\t" \
  "v_lshrrev_b32 v19, 2, v0\n\t" \
  "v_and_b32 v19, 64, v19\n\t" \
  "v_xor_b32 v19, v19, v0\n\t" \
  "v_and_b32 v19, 0x70, v19\n\t" \
  "v_add_u32 v16, v16, v19\n\t" \
  "s_mul_i32 s63, s40, 40960\n\t" \
  "v_and_b32 v19, 0x300, v0\n\t" \
  "v_lshlrev_b32 v17, 2, v19\n\t" \
  "v_and_b32 v19, 0xc0, v0\n\t" \
  "v_lshl_add_u32 v17, v19, 1, v17\n\t" \
  "v_and_b32 v19, 0x30, v0\n\t" \
  "v_lshrrev_b32 v19, 1, v19\n\t" \
  "v_add3_u32 v17, v17, v19, s63\n\t" \
  "v_lshrrev_b32 v19, 1, v0\n\t" \
  "v_and_b32 v19, 64, v19\n\t" \
  "v_add_u32 v17, v17, v19\n\t" \
  "v_xor_b32 v18, 64, v17\n\t" \
  "s_add_u32 m0, s63, 0\n\t" \
  "s_mov_b32 s60, 0\n\t" \
  "buffer_load_dwordx4 v16, %[W], s60 offen lds\n\t" \
  "s_add_u32 m0, s63, 1024\n\t" \
  "s_mov_b32 s60, 65536\n\t" \
  "buffer_load_dwordx4 v16, %[W], s60 offen lds\n\t" \
  "s_add_u32 m0, s63, 2048\n\t" \
  "s_mov_b32 s60, 131072\n\t" \
  "buffer_load_dwordx4 v16, %[W], s60 offen lds\n\t" \
  "s_add_u32 m0, s63, 3072\n\t" \
  "s_mov_b32 s60, 196608\n\t" \
  "buffer_load_dwordx4 v16, %[W], s60 offen lds\n\t" \
  "s_add_u32 m0, s63, 4096\n\t" \
  "s_mov_b32 s60, 262144\n\t" \
  "buffer_load_dwordx4 v16, %[W], s60 offen lds\n\t" \
  "s_add_u32 m0, s63, 5120\n\t" \
  "s_mov_b32 s60, 327680\n\t" \
  "buffer_load_dwordx4 v16, %[W], s60 offen lds\n\t" \
  "s_add_u32 m0, s63, 6144\n\t" \
  "s_mov_b32 s60, 393216\n\t" \
  "buffer_load_dwordx4 v16, %[W], s60 offen lds\n\t" \
  "s_add_u32 m0, s63, 7168\n\t" \
  "s_mov_b32 s60, 458752\n\t" \
  "buffer_load_dwordx4 v16, %[W], s60 offen lds\n\t" \
  "s_add_u32 m0, s63, 8192\n\t" \
  "s_mov_b32 s60, 524288\n\t" \
  "buffer_load_dwordx4 v16, %[W], s60 offen lds\n\t" \
  "s_add_u32 m0, s63, 9216\n\t" \
  "s_mov_b32 s60, 589824\n\t" \
  "buffer_load_dwordx4 v16, %[W], s60 offen lds\n\t" \
  "s_add_u32 m0, s63, 10240\n\t" \
  "s_mov_b32 s60, 655360\n\t" \
  "buffer_load_dwordx4 v16, %[W], s60 offen lds\n\t" \
  "s_add_u32 m0, s63, 11264\n\t" \
  "s_mov_b32 s60, 720896\n\t" \
  "buffer_load_dwordx4 v16, %[W], s60 offen lds\n\t" \
  "s_add_u32 m0, s63, 12288\n\t" \
  "s_mov_b32 s60, 786432\n\t" \
  "buffer_load_dwordx4 v16, %[W], s60 offen lds\n\t" \
  "s_add_u32 m0, s63, 13312\n\t" \
  "s_mov_b32 s60, 851968\n\t" \
  "buffer_load_dwordx4 v16, %[W], s60 offen lds\n\t" \
  "s_add_u32 m0, s63, 14336\n\t" \
  "s_mov_b32 s60, 917504\n\t" \
  "buffer_load_dwordx4 v16, %[W], s60 offen lds\n\t" \
  "s_add_u32 m0, s63, 15360\n\t" \
  "s_mov_b32 s60, 983040\n\t" \
  "buffer_load_dwordx4 v16, %[W], s60 offen lds\n\t" \
  "s_add_u32 m0, s63, 16384\n\t" \
  "s_mov_b32 s60, 1048576\n\t" \
  "buffer_load_dwordx4 v16, %[W], s60 offen lds\n\t" \
  "s_add_u32 m0, s63, 17408\n\t" \
  "s_mov_b32 s60, 1114112\n\t" \
  "buffer_load_dwordx4 v16, %[W], s60 offen lds\n\t" \
  "s_add_u32 m0, s63, 18432\n\t" \
  "s_mov_b32 s60, 1179648\n\t" \
  "buffer_load_dwordx4 v16, %[W], s60 offen lds\n\t" \
  "s_add_u32 m0, s63, 19456\n\t" \
  "s_mov_b32 s60, 1245184\n\t" \
  "buffer_load_dwordx4 v16, %[W], s60 offen lds\n\t" \
  "s_add_u32 m0, s63, 20480\n\t" \
  "s_mov_b32 s60, 1310720\n\t" \
  "buffer_load_dwordx4 v16, %[W], s60 offen lds\n\t" \
  "s_add_u32 m0, s63, 21504\n\t" \
  "s_mov_b32 s60, 1376256\n\t" \
  "buffer_load_dwordx4 v16, %[W], s60 offen lds\n\t" \
  "s_add_u32 m0, s63, 22528\n\t" \
  "s_mov_b32 s60, 1441792\n\t" \
  "buffer_load_dwordx4 v16, %[W], s60 offen lds\n\t" \
  "s_add_u32 m0, s63, 23552\n\t" \
  "s_mov_b32 s60, 1507328\n\t" \
  "buffer_load_dwordx4 v16, %[W], s60 offen lds\n\t" \
  "s_add_u32 m0, s63, 24576\n\t" \
  "s_mov_b32 s60, 1572864\n\t" \
  "buffer_load_dwordx4 v16, %[W], s60 offen lds\n\t" \
  "s_add_u32 m0, s63, 25600\n\t" \
  "s_mov_b32 s60, 1638400\n\t" \
  "buffer_load_dwordx4 v16, %[W], s60 offen lds\n\t" \
  "s_add_u32 m0, s63, 26624\n\t" \
  "s_mov_b32 s60, 1703936\n\t" \
  "buffer_load_dwordx4 v16, %[W], s60 offen lds\n\t" \
  "s_add_u32 m0, s63, 27648\n\t" \
  "s_mov_b32 s60, 1769472\n\t" \
  "buffer_load_dwordx4 v16, %[W], s60 offen lds\n\t" \
  "s_add_u32 m0, s63, 28672\n\t" \
  "s_mov_b32 s60, 1835008\n\t" \
  "buffer_load_dwordx4 v16, %[W], s60 offen lds\n\t" \
  "s_add_u32 m0, s63, 29696\n\t" \
  "s_mov_b32 s60, 1900544\n\t" \
  "buffer_load_dwordx4 v16, %[W], s60 offen lds\n\t" \
  "s_add_u32 m0, s63, 30720\n\t" \
  "s_mov_b32 s60, 1966080\n\t" \
  "buffer_load_dwordx4 v16, %[W], s60 offen lds\n\t" \
  "s_add_u32 m0, s63, 31744\n\t" \
  "s_mov_b32 s60, 2031616\n\t" \
  "buffer_load_dwordx4 v16, %[W], s60 offen lds\n\t" \
  "s_add_u32 m0, s63, 32768\n\t" \
  "s_mov_b32 s60, 2097152\n\t" \
  "buffer_load_dwordx4 v16, %[W], s60 offen lds\n\t" \
  "s_add_u32 m0, s63, 33792\n\t" \
  "s_mov_b32 s60, 2162688\n\t" \
  "buffer_load_dwordx4 v16, %[W], s60 offen lds\n\t" \
  "s_add_u32 m0, s63, 34816\n\t" \
  "s_mov_b32 s60, 2228224\n\t" \
  "buffer_load_dwordx4 v16, %[W], s60 offen lds\n\t" \
  "s_add_u32 m0, s63, 35840\n\t" \
  "s_mov_b32 s60, 2293760\n\t" \
  "buffer_load_dwordx4 v16, %[W], s60 offen lds\n\t" \
  "s_add_u32 m0, s63, 36864\n\t" \
  "s_mov_b32 s60, 2359296\n\t" \
  "buffer_load_dwordx4 v16, %[W], s60 offen lds\n\t" \
  "s_add_u32 m0, s63, 37888\n\t" \
  "s_mov_b32 s60, 2424832\n\t" \
  "buffer_load_dwordx4 v16, %[W], s60 offen lds\n\t" \
  "s_add_u32 m0, s63, 38912\n\t" \
  "s_mov_b32 s60, 2490368\n\t" \
  "buffer_load_dwordx4 v16, %[W], s60 offen lds\n\t" \
  "s_add_u32 m0, s63, 39936\n\t" \
  "s_mov_b32 s60, 2555904\n\t" \
  "buffer_load_dwordx4 v16, %[W], s60 offen lds\n\t" \
  "s_waitcnt vmcnt(32)\n\t" \
  "ds_read_b64_tr_b16 a[0:1], v17 offset:0\n\t" \
  "ds_read_b64_tr_b16 a[2:3], v17 offset:512\n\t" \
  "ds_read_b64_tr_b16 a[4:5], v17 offset:4096\n\t" \
  "ds_read_b64_tr_b16 a[6:7], v17 offset:4608\n\t" \
  "ds_read_b64_tr_b16 a[72:73], v17 offset:32\n\t" \
  "ds_read_b64_tr_b16 a[74:75], v17 offset:544\n\t" \
  "ds_read_b64_tr_b16 a[76:77], v17 offset:4128\n\t" \
  "ds_read_b64_tr_b16 a[78:79], v17 offset:4640\n\t" \
  "ds_read_b64_tr_b16 a[144:145], v18 offset:0\n\t" \
  "ds_read_b64_tr_b16 a[146:147], v18 offset:512\n\t" \
  "ds_read_b64_tr_b16 a[148:149], v18 offset:4096\n\t" \
  "ds_read_b64_tr_b16 a[150:151], v18 offset:4608\n\t" \
  "ds_read_b64_tr_b16 a[216:217], v18 offset:32\n\t" \
  "ds_read_b64_tr_b16 a[218:219], v18 offset:544\n\t" \
  "ds_read_b64_tr_b16 a[220:221], v18 offset:4128\n\t" \
  "ds_read_b64_tr_b16 a[222:223], v18 offset:4640\n\t" \
  "s_waitcnt lgkmcnt(0)\n\t" \
  "s_add_u32 m0, s63, 0\n\t" \
  "s_mov_b32 s60, 2621440\n\t" \
  "buffer_load_dwordx4 v16, %[W], s60 offen lds\n\t" \
  "s_add_u32 m0, s63, 1024\n\t" \
  "s_mov_b32 s60, 2686976\n\t" \
  "buffer_load_dwordx4 v16, %[W], s60 offen lds\n\t" \
  "s_add_u32 m0, s63, 2048\n\t" \
  "s_mov_b32 s60, 2752512\n\t" \
  "buffer_load_dwordx4 v16, %[W], s60 offen lds\n\t" \
  "s_add_u32 m0, s63, 3072\n\t" \
  "s_mov_b32 s60, 2818048\n\t" \
  "buffer_load_dwordx4 v16, %[W], s60 offen lds\n\t" \
  "s_add_u32 m0, s63, 4096\n\t" \
  "s_mov_b32 s60, 2883584\n\t" \
  "buffer_load_dwordx4 v16, %[W], s60 offen lds\n\t" \
  "s_add_u32 m0, s63, 5120\n\t" \
  "s_mov_b32 s60, 2949120\n\t" \
  "buffer_load_dwordx4 v16, %[W], s60 offen lds\n\t" \
  "s_add_u32 m0, s63, 6144\n\t" \
  "s_mov_b32 s60, 3014656\n\t" \
  "buffer_load_dwordx4 v16, %[W], s60 offen lds\n\t" \
  "s_add_u32 m0, s63, 7168\n\t" \
  "s_mov_b32 s60, 3080192\n\t" \
  "buffer_load_dwordx4 v16, %[W], s60 offen lds\n\t" \
  "s_waitcnt vmcnt(32)\n\t" \
  "ds_read_b64_tr_b16 a[8:9], v17 offset:8192\n\t" \
  "ds_read_b64_tr_b16 a[10:11], v17 offset:8704\n\t" \
  "ds_read_b64_tr_b16 a[12:13], v17 offset:12288\n\t" \
  "ds_read_b64_tr_b16 a[14:15], v17 offset:12800\n\t" \
  "ds_read_b64_tr_b16 a[80:81], v17 offset:8224\n\t" \
  "ds_read_b64_tr_b16 a[82:83], v17 offset:8736\n\t" \
  "ds_read_b64_tr_b16 a[84:85], v17 offset:12320\n\t" \
  "ds_read_b64_tr_b16 a[86:87], v17 offset:12832\n\t" \
  "ds_read_b64_tr_b16 a[152:153], v18 offset:8192\n\t" \
  "ds_read_b64_tr_b16 a[154:155], v18 offset:8704\n\t" \
  "ds_read_b64_tr_b16 a[156:157], v18 offset:12288\n\t" \
  "ds_read_b64_tr_b16 a[158:159], v18 offset:12800\n\t" \
  "ds_read_b64_tr_b16 a[224:225], v18 offset:8224\n\t" \
  "ds_read_b64_tr_b16 a[226:227], v18 offset:8736\n\t" \
  "ds_read_b64_tr_b16 a[228:229], v18 offset:12320\n\t" \
  "ds_read_b64_tr_b16 a[230:231], v18 offset:12832\n\t" \
  "s_waitcnt lgkmcnt(0)\n\t" \
  "s_add_u32 m0, s63, 8192\n\t" \
  "s_mov_b32 s60, 3145728\n\t" \
  "buffer_load_dwordx4 v16, %[W], s60 offen lds\n\t" \
  "s_add_u32 m0, s63, 9216\n\t" \
  "s_mov_b32 s60, 3211264\n\t" \
  "buffer_load_dwordx4 v16, %[W], s60 offen lds\n\t" \
  "s_add_u32 m0, s63, 10240\n\t" \
  "s_mov_b32 s60, 3276800\n\t" \
  "buffer_load_dwordx4 v16, %[W], s60 offen lds\n\t" \
  "s_add_u32 m0, s63, 11264\n\t" \
  "s_mov_b32 s60, 3342336\n\t" \
  "buffer_load_dwordx4 v16, %[W], s60 offen lds\n\t" \
  "s_add_u32 m0, s63, 12288\n\t" \
  "s_mov_b32 s60, 3407872\n\t" \
  "buffer_load_dwordx4 v16, %[W], s60 offen lds\n\t" \
  "s_add_u32 m0, s63, 13312\n\t" \
  "s_mov_b32 s60, 3473408\n\t" \
  "buffer_load_dwordx4 v16, %[W], s60 offen lds\n\t" \
  "s_add_u32 m0, s63, 14336\n\t" \
  "s_mov_b32 s60, 3538944\n\t" \
  "buffer_load_dwordx4 v16, %[W], s60 offen lds\n\t" \
  "s_add_u32 m0, s63, 15360\n\t" \
  "s_mov_b32 s60, 3604480\n\t" \
  "buffer_load_dwordx4 v16, %[W], s60 offen lds\n\t" \
  "s_waitcnt vmcnt(32)\n\t" \
  "ds_read_b64_tr_b16 a[16:17], v17 offset:16384\n\t" \
  "ds_read_b64_tr_b16 a[18:19], v17 offset:16896\n\t" \
  "ds_read_b64_tr_b16 a[20:21], v17 offset:20480\n\t" \
  "ds_read_b64_tr_b16 a[22:23], v17 offset:20992\n\t" \
  "ds_read_b64_tr_b16 a[88:89], v17 offset:16416\n\t" \
  "ds_read_b64_tr_b16 a[90:91], v17 offset:16928\n\t" \
  "ds_read_b64_tr_b16 a[92:93], v17 offset:20512\n\t" \
  "ds_read_b64_tr_b16 a[94:95], v17 offset:21024\n\t" \
  "ds_read_b64_tr_b16 a[160:161], v18 offset:16384\n\t" \
  "ds_read_b64_tr_b16 a[162:163], v18 offset:16896\n\t" \
  "ds_read_b64_tr_b16 a[164:165], v18 offset:20480\n\t" \
  "ds_read_b64_tr_b16 a[166:167], v18 offset:20992\n\t" \
  "ds_read_b64_tr_b16 a[232:233], v18 offset:16416\n\t" \
  "ds_read_b64_tr_b16 a[234:235], v18 offset:16928\n\t" \
  "ds_read_b64_tr_b16 a[236:237], v18 offset:20512\n\t" \
  "ds_read_b64_tr_b16 a[238:239], v18 offset:21024\n\t" \
  "s_waitcnt lgkmcnt(0)\n\t" \
  "s_add_u32 m0, s63, 16384\n\t" \
  "s_mov_b32 s60, 3670016\n\t" \
  "buffer_load_dwordx4 v16, %[W], s60 offen lds\n\t" \
  "s_add_u32 m0, s63, 17408\n\t" \
  "s_mov_b32 s60, 3735552\n\t" \
  "buffer_load_dwordx4 v16, %[W], s60 offen lds\n\t" \
  "s_add_u32 m0, s63, 18432\n\t" \
  "s_mov_b32 s60, 3801088\n\t" \
  "buffer_load_dwordx4 v16, %[W], s60 offen lds\n\t" \
  "s_add_u32 m0, s63, 19456\n\t" \
  "s_mov_b32 s60, 3866624\n\t" \
  "buffer_load_dwordx4 v16, %[W], s60 offen lds\n\t" \
  "s_add_u32 m0, s63, 20480\n\t" \
  "s_mov_b32 s60, 3932160\n\t" \
  "buffer_load_dwordx4 v16, %[W], s60 offen lds\n\t" \
  "s_add_u32 m0, s63, 21504\n\t" \
  "s_mov_b32 s60, 3997696\n\t" \
  "buffer_load_dwordx4 v16, %[W], s60 offen lds\n\t" \
  "s_add_u32 m0, s63, 22528\n\t" \
  "s_mov_b32 s60, 4063232\n\t" \
  "buffer_load_dwordx4 v16, %[W], s60 offen lds\n\t" \
  "s_add_u32 m0, s63, 23552\n\t" \
  "s_mov_b32 s60, 4128768\n\t" \
  "buffer_load_dwordx4 v16, %[W], s60 offen lds\n\t" \
  "s_waitcnt vmcnt(32)\n\t" \
  "ds_read_b64_tr_b16 a[24:25], v17 offset:24576\n\t" \
  "ds_read_b64_tr_b16 a[26:27], v17 offset:25088\n\t" \
  "ds_read_b64_tr_b16 a[28:29], v17 offset:28672\n\t" \
  "ds_read_b64_tr_b16 a[30:31], v17 offset:29184\n\t" \
  "ds_read_b64_tr_b16 a[96:97], v17 offset:24608\n\t" \
  "ds_read_b64_tr_b16 a[98:99], v17 offset:25120\n\t" \
  "ds_read_b64_tr_b16 a[100:101], v17 offset:28704\n\t" \
  "ds_read_b64_tr_b16 a[102:103], v17 offset:29216\n\t" \
  "ds_read_b64_tr_b16 a[168:169], v18 offset:24576\n\t" \
  "ds_read_b64_tr_b16 a[170:171], v18 offset:25088\n\t" \
  "ds_read_b64_tr_b16 a[172:173], v18 offset:28672\n\t" \
  "ds_read_b64_tr_b16 a[174:175], v18 offset:29184\n\t" \
  "ds_read_b64_tr_b16 a[240:241], v18 offset:24608\n\t" \
  "ds_read_b64_tr_b16 a[242:243], v18 offset:25120\n\t" \
  "ds_read_b64_tr_b16 a[244:245], v18 offset:28704\n\t" \
  "ds_read_b64_tr_b16 a[246:247], v18 offset:29216\n\t" \
  "s_waitcnt lgkmcnt(0)\n\t" \
  "s_add_u32 m0, s63, 24576\n\t" \
  "s_mov_b32 s60, 4194304\n\t" \
  "buffer_load_dwordx4 v16, %[W], s60 offen lds\n\t" \
  "s_add_u32 m0, s63, 25600\n\t" \
  "s_mov_b32 s60, 4259840\n\t" \
  "buffer_load_dwordx4 v16, %[W], s60 offen lds\n\t" \
  "s_add_u32 m0, s63, 26624\n\t" \
  "s_mov_b32 s60, 4325376\n\t" \
  "buffer_load_dwordx4 v16, %[W], s60 offen lds\n\t" \
  "s_add_u32 m0, s63, 27648\n\t" \
  "s_mov_b32 s60, 4390912\n\t" \
  "buffer_load_dwordx4 v16, %[W], s60 offen lds\n\t" \
  "s_add_u32 m0, s63, 28672\n\t" \
  "s_mov_b32 s60, 4456448\n\t" \
  "buffer_load_dwordx4 v16, %[W], s60 offen lds\n\t" \
  "s_add_u32 m0, s63, 29696\n\t" \
  "s_mov_b32 s60, 4521984\n\t" \
  "buffer_load_dwordx4 v16, %[W], s60 offen lds\n\t" \
  "s_add_u32 m0, s63, 30720\n\t" \
  "s_mov_b32 s60, 4587520\n\t" \
  "buffer_load_dwordx4 v16, %[W], s60 offen lds\n\t" \
  "s_add_u32 m0, s63, 31744\n\t" \
  "s_mov_b32 s60, 4653056\n\t" \
  "buffer_load_dwordx4 v16, %[W], s60 offen lds\n\t" \
  "s_waitcnt vmcnt(32)\n\t" \
  "ds_read_b64_tr_b16 a[32:33], v17 offset:32768\n\t" \
  "ds_read_b64_tr_b16 a[34:35], v17 offset:33280\n\t" \
  "ds_read_b64_tr_b16 a[36:37], v17 offset:36864\n\t" \
  "ds_read_b64_tr_b16 a[38:39], v17 offset:37376\n\t" \
  "ds_read_b64_tr_b16 a[104:105], v17 offset:32800\n\t" \
  "ds_read_b64_tr_b16 a[106:107], v17 offset:33312\n\t" \
  "ds_read_b64_tr_b16 a[108:109], v17 offset:36896\n\t" \
  "ds_read_b64_tr_b16 a[110:111], v17 offset:37408\n\t" \
  "ds_read_b64_tr_b16 a[176:177], v18 offset:32768\n\t" \
  "ds_read_b64_tr_b16 a[178:179], v18 offset:33280\n\t" \
  "ds_read_b64_tr_b16 a[180:181], v18 offset:36864\n\t" \
  "ds_read_b64_tr_b16 a[182:183], v18 offset:37376\n\t" \
  "ds_read_b64_tr_b16 a[248:249], v18 offset:32800\n\t" \
  "ds_read_b64_tr_b16 a[250:251], v18 offset:33312\n\t" \
  "ds_read_b64_tr_b16 a[252:253], v18 offset:36896\n\t" \
  "ds_read_b64_tr_b16 a[254:255], v18 offset:37408\n\t" \
  "s_waitcnt lgkmcnt(0)\n\t" \
  "s_waitcnt vmcnt(24)\n\t" \
  "ds_read_b64_tr_b16 a[40:41], v17 offset:0\n\t" \
  "ds_read_b64_tr_b16 a[42:43], v17 offset:512\n\t" \
  "ds_read_b64_tr_b16 a[44:45], v17 offset:4096\n\t" \
  "ds_read_b64_tr_b16 a[46:47], v17 offset:4608\n\t" \
  "ds_read_b64_tr_b16 a[112:113], v17 offset:32\n\t" \
  "ds_read_b64_tr_b16 a[114:115], v17 offset:544\n\t" \
  "ds_read_b64_tr_b16 a[116:117], v17 offset:4128\n\t" \
  "ds_read_b64_tr_b16 a[118:119], v17 offset:4640\n\t" \
  "ds_read_b64_tr_b16 a[184:185], v18 offset:0\n\t" \
  "ds_read_b64_tr_b16 a[186:187], v18 offset:512\n\t" \
  "ds_read_b64_tr_b16 a[188:189], v18 offset:4096\n\t" \
  "ds_read_b64_tr_b16 a[190:191], v18 offset:4608\n\t" \
  "ds_read_b64_tr_b16 v[224:225], v18 offset:32\n\t" \
  "ds_read_b64_tr_b16 v[226:227], v18 offset:544\n\t" \
  "ds_read_b64_tr_b16 v[228:229], v18 offset:4128\n\t" \
  "ds_read_b64_tr_b16 v[230:231], v18 offset:4640\n\t" \
  "s_waitcnt lgkmcnt(0)\n\t" \
  "s_waitcnt vmcnt(16)\n\t" \
  "ds_read_b64_tr_b16 a[48:49], v17 offset:8192\n\t" \
  "ds_read_b64_tr_b16 a[50:51], v17 offset:8704\n\t" \
  "ds_read_b64_tr_b16 a[52:53], v17 offset:12288\n\t" \
  "ds_read_b64_tr_b16 a[54:55], v17 offset:12800\n\t" \
  "ds_read_b64_tr_b16 a[120:121], v17 offset:8224\n\t" \
  "ds_read_b64_tr_b16 a[122:123], v17 offset:8736\n\t" \
  "ds_read_b64_tr_b16 a[124:125], v17 offset:12320\n\t" \
  "ds_read_b64_tr_b16 a[126:127], v17 offset:12832\n\t" \
  "ds_read_b64_tr_b16 a[192:193], v18 offset:8192\n\t" \
  "ds_read_b64_tr_b16 a[194:195], v18 offset:8704\n\t" \
  "ds_read_b64_tr_b16 a[196:197], v18 offset:12288\n\t" \
  "ds_read_b64_tr_b16 a[198:199], v18 offset:12800\n\t" \
  "ds_read_b64_tr_b16 v[232:233], v18 offset:8224\n\t" \
  "ds_read_b64_tr_b16 v[234:235], v18 offset:8736\n\t" \
  "ds_read_b64_tr_b16 v[236:237], v18 offset:12320\n\t" \
  "ds_read_b64_tr_b16 v[238:239], v18 offset:12832\n\t" \
  "s_waitcnt lgkmcnt(0)\n\t" \
  "s_waitcnt vmcnt(8)\n\t" \
  "ds_read_b64_tr_b16 a[56:57], v17 offset:16384\n\t" \
  "ds_read_b64_tr_b16 a[58:59], v17 offset:16896\n\t" \
  "ds_read_b64_tr_b16 a[60:61], v17 offset:20480\n\t" \
  "ds_read_b64_tr_b16 a[62:63], v17 offset:20992\n\t" \
  "ds_read_b64_tr_b16 a[128:129], v17 offset:16416\n\t" \
  "ds_read_b64_tr_b16 a[130:131], v17 offset:16928\n\t" \
  "ds_read_b64_tr_b16 a[132:133], v17 offset:20512\n\t" \
  "ds_read_b64_tr_b16 a[134:135], v17 offset:21024\n\t" \
  "ds_read_b64_tr_b16 a[200:201], v18 offset:16384\n\t" \
  "ds_read_b64_tr_b16 a[202:203], v18 offset:16896\n\t" \
  "ds_read_b64_tr_b16 a[204:205], v18 offset:20480\n\t" \
  "ds_read_b64_tr_b16 a[206:207], v18 offset:20992\n\t" \
  "ds_read_b64_tr_b16 v[240:241], v18 offset:16416\n\t" \
  "ds_read_b64_tr_b16 v[242:243], v18 offset:16928\n\t" \
  "ds_read_b64_tr_b16 v[244:245], v18 offset:20512\n\t" \
  "ds_read_b64_tr_b16 v[246:247], v18 offset:21024\n\t" \
  "s_waitcnt lgkmcnt(0)\n\t" \
  "s_waitcnt vmcnt(0)\n\t" \
  "ds_read_b64_tr_b16 a[64:65], v17 offset:24576\n\t" \
  "ds_read_b64_tr_b16 a[66:67], v17 offset:25088\n\t" \
  "ds_read_b64_tr_b16 a[68:69], v17 offset:28672\n\t" \
  "ds_read_b64_tr_b16 a[70:71], v17 offset:29184\n\t" \
  "ds_read_b64_tr_b16 a[136:137], v17 offset:24608\n\t" \
  "ds_read_b64_tr_b16 a[138:139], v17 offset:25120\n\t" \
  "ds_read_b64_tr_b16 a[140:141], v17 offset:28704\n\t" \
  "ds_read_b64_tr_b16 a[142:143], v17 offset:29216\n\t" \
  "ds_read_b64_tr_b16 a[208:209], v18 offset:24576\n\t" \
  "ds_read_b64_tr_b16 a[210:211], v18 offset:25088\n\t" \
  "ds_read_b64_tr_b16 a[212:213], v18 offset:28672\n\t" \
  "ds_read_b64_tr_b16 a[214:215], v18 offset:29184\n\t" \
  "ds_read_b64_tr_b16 v[248:249], v18 offset:24608\n\t" \
  "ds_read_b64_tr_b16 v[250:251], v18 offset:25120\n\t" \
  "ds_read_b64_tr_b16 v[252:253], v18 offset:28704\n\t" \
  "ds_read_b64_tr_b16 v[254:255], v18 offset:29216\n\t" \
  "s_waitcnt lgkmcnt(0)\n\t" \
  "s_mov_b32 s84, " TK_CAP "\n\t" \
  "s_mov_b32 s48, 0\n\t" \
  "s_mov_b32 s49, 0xf0000000\n\t" \
  "s_mov_b32 s46, s41\n\t" \
  "s_mov_b32 s56, 0\n\t" \
  "v_lshrrev_b32 v7, 2, v0\n\t" \
  "s_mov_b32 s60, " TK_CTAB "\n\t" \
  "buffer_load_dword v4, v7, %[X], s60 offen offset:88 sc1\n\t" \
  "buffer_load_dword v7, v7, %[X], s60 offen offset:0 sc1\n\t" \
  "s_waitcnt vmcnt(0)\n\t" \
  "v_and_b32 v4, 1, v4\n\t" \
  "v_and_b32 v7, 1, v7\n\t" \
  "v_cmp_ne_u32 s[90:91], s67, v4\n\t" \
  "v_cmp_ne_u32 s[88:89], s67, v7\n\t" \
  "s_nop 1\n\t" \
  "s_mov_b32 s57, 0xf0000000\n\t" \
  "s_add_u32 s83, s48, 0\n\t" \
  "s_lshr_b64 s[60:61], s[90:91], s83\n\t" \
  "s_and_b32 s60, s60, 1\n\t" \
  "s_lshl_b32 s60, s60, 13\n\t" \
  "s_add_u32 s83, s83, " TK_SG0 "\n\t" \
  "s_lshl_b32 s83, s83, 21\n\t" \
  "s_or_b32 s83, s83, s60\n\t" \
  "s_add_u32 s80, s48, " TK_TRN "\n\t" \
  "s_lshr_b64 s[60:61], s[90:91], s80\n\t" \
  "s_and_b32 s60, s60, 1\n\t" \
  "s_lshl_b32 s60, s60, 13\n\t" \
  "s_add_u32 s80, s80, " TK_SG0 "\n\t" \
  "s_lshl_b32 s80, s80, 21\n\t" \
  "s_or_b32 s80, s80, s60\n\t" \
  "s_add_u32 s62, s83, s39\n\t" \
  "s_add_u32 s80, s80, s39\n\t" \
  "s_add_u32 s80, s80, 2048\n\t" \
  "s_cmp_lt_u32 s48, " TK_TRM "\n\t" \
  "s_cselect_b32 s80, s62, s80\n\t" \
  "v_add_u32 v7, s62, v3\n\t" \
  "s_mov_b32 exec_lo, 0\n\t" \
  "v_mov_b32 v7, s80\n\t" \
  "s_mov_b32 exec_lo, -1\n\t" \
  "LCOLD_%=:\n\t" \
  "s_waitcnt vmcnt(0)\n\t" \
  "s_mov_b64 exec, 1\n\t" \
  "buffer_store_dword v6, off, %[X], s49 sc1\n\t" \
  "s_mov_b64 exec, -1\n\t" \
  "s_mov_b32 s49, 0xf0000000\n\t" \
  "s_add_u32 s55, s83, s36\n\t" \
  "s_cmp_lg_u32 s40, 0\n\t" \
  "s_cbranch_scc1 LGO_%=\n\t" \
  "s_and_b32 s62, s45, 3\n\t" \
  "s_lshl_b32 s62, s62, 7\n\t" \
  "s_add_u32 s62, s62, s83\n\t" \
  "s_add_u32 s62, s62, s39\n\t" \
  "s_mov_b32 s61, 0\n\t" \
  "LPO_%=:\n\t" \
  "buffer_load_dword v4, off, %[X], s62 sc1\n\t" \
  "s_waitcnt vmcnt(0)\n\t" \
  "v_cmp_eq_u32 vcc, 0, v4\n\t" \
  "s_cbranch_vccz LPO_%=x\n\t" \
  "s_add_u32 s61, s61, 1\n\t" \
  "s_sleep " TK_PSH "\n\t" \
  "s_cmp_gt_u32 s61, s84\n\t" \
  "s_cbranch_scc1 LPO_%=g\n\t" \
  "s_and_b32 s60, s61, 1023\n\t" \
  "s_cmp_lg_u32 s60, 0\n\t" \
  "s_cbranch_scc1 LPO_%=\n\t" \
  "buffer_load_dword v4, off, %[X], s92 sc1\n\t" \
  "s_waitcnt vmcnt(0)\n\t" \
  "v_readfirstlane_b32 s60, v4\n\t" \
  "s_nop 0\n\t" \
  "s_cmp_eq_u32 s60, 0\n\t" \
  "s_cbranch_scc1 LPO_%=\n\t" \
  "LPO_%=g:\n\t" \
  "s_mov_b32 s84, 0\n\t" \
  "s_or_b32 s60, s48, 1610612736\n\t" \
  "v_mov_b32 v4, s60\n\t" \
  "s_mov_b64 exec, 1\n\t" \
  "buffer_store_dword v4, off, %[X], s92 sc1\n\t" \
  "s_mov_b64 exec, -1\n\t" \
  "LPO_%=x:\n\t" \
  "s_mov_b32 s61, 0\n\t" \
  "LPH_%=:\n\t" \
  "buffer_load_dword v4, v7, %[X], 0 offen sc1\n\t" \
  "s_waitcnt vmcnt(0)\n\t" \
  "v_cmp_eq_u32 vcc, 0, v4\n\t" \
  "s_cbranch_vccz LPH_%=x\n\t" \
  "s_add_u32 s61, s61, 1\n\t" \
  "s_sleep " TK_PSH "\n\t" \
  "s_cmp_gt_u32 s61, s84\n\t" \
  "s_cbranch_scc1 LPH_%=g\n\t" \
  "s_and_b32 s60, s61, 1023\n\t" \
  "s_cmp_lg_u32 s60, 0\n\t" \
  "s_cbranch_scc1 LPH_%=\n\t" \
  "buffer_load_dword v4, off, %[X], s92 sc1\n\t" \
  "s_waitcnt vmcnt(0)\n\t" \
  "v_readfirstlane_b32 s60, v4\n\t" \
  "s_nop 0\n\t" \
  "s_cmp_eq_u32 s60, 0\n\t" \
  "s_cbranch_scc1 LPH_%=\n\t" \
  "LPH_%=g:\n\t" \
  "s_mov_b32 s84, 0\n\t" \
  "s_or_b32 s60, s48, 1610612736\n\t" \
  "v_mov_b32 v4, s60\n\t" \
  "s_mov_b64 exec, 1\n\t" \
  "buffer_store_dword v4, off, %[X], s92 sc1\n\t" \
  "s_mov_b64 exec, -1\n\t" \
  "LPH_%=x:\n\t" \
  "s_cmp_lt_i32 s57, 0\n\t" \
  "s_cbranch_scc1 LGO_%=\n\t" \
  "s_movk_i32 s63, 4000\n\t" \
  "s_branch LGC_%=u\n\t" \
  "LGC_%=:\n\t" \
  "s_sleep " TK_PSG "\n\t" \
  "buffer_load_dword v8, off, %[X], s57 sc1\n\t" \
  "s_waitcnt vmcnt(0)\n\t" \
  "LGC_%=u:\n\t" \
  "s_memrealtime s[52:53]\n\t" \
  "v_readfirstlane_b32 s60, v8\n\t" \
  "s_waitcnt lgkmcnt(0)\n\t" \
  "s_sub_u32 s63, s63, 1\n\t" \
  "s_cbranch_scc1 LGO_%=\n\t" \
  "s_cmp_eq_u32 s60, -1\n\t" \
  "s_cbranch_scc1 LGC_%=\n\t" \
  "s_not_b32 s60, s60\n\t" \
  "s_lshl_b32 s61, s52, 8\n\t" \
  "s_sub_u32 s60, s60, s61\n\t" \
  "s_ashr_i32 s60, s60, 8\n\t" \
  "s_sub_i32 s60, s60, " TK_QXC "\n\t" \
  "s_min_i32 s60, s60, 400\n\t" \
  "s_add_u32 s54, s52, s60\n\t" \
  "LGC_%=w:\n\t" \
  "s_sleep 1\n\t" \
  "s_memrealtime s[52:53]\n\t" \
  "s_waitcnt lgkmcnt(0)\n\t" \
  "s_sub_i32 s53, s52, s54\n\t" \
  "s_cmp_lt_i32 s53, 0\n\t" \
  "s_cbranch_scc1 LGC_%=w\n\t" \
  "LGO_%=:\n\t" \
  "s_barrier\n\t" \
  "s_add_u32 m0, s46, 0\n\t" \
  "s_add_u32 s47, s55, 0\n\t" \
  "buffer_load_dwordx4 v0, %[X], s47 offen sc1 lds\n\t" \
  "s_add_u32 m0, s46, 4096\n\t" \
  "s_add_u32 s47, s55, 4096\n\t" \
  "buffer_load_dwordx4 v0, %[X], s47 offen sc1 lds\n\t" \
  "s_add_u32 m0, s46, 8192\n\t" \
  "s_add_u32 s47, s55, 16384\n\t" \
  "buffer_load_dwordx4 v0, %[X], s47 offen sc1 lds\n\t" \
  "s_add_u32 m0, s46, 12288\n\t" \
  "s_add_u32 s47, s55, 20480\n\t" \
  "buffer_load_dwordx4 v0, %[X], s47 offen sc1 lds\n\t" \
  "s_add_u32 m0, s46, 16384\n\t" \
  "s_add_u32 s47, s55, 32768\n\t" \
  "buffer_load_dwordx4 v0, %[X], s47 offen sc1 lds\n\t" \
  "s_add_u32 m0, s46, 20480\n\t" \
  "s_add_u32 s47, s55, 36864\n\t" \
  "buffer_load_dwordx4 v0, %[X], s47 offen sc1 lds\n\t" \
  "s_add_u32 m0, s46, 24576\n\t" \
  "s_add_u32 s47, s55, 49152\n\t" \
  "buffer_load_dwordx4 v0, %[X], s47 offen sc1 lds\n\t" \
  "s_add_u32 m0, s46, 28672\n\t" \
  "s_add_u32 s47, s55, 53248\n\t" \
  "buffer_load_dwordx4 v0, %[X], s47 offen sc1 lds\n\t" \
  "s_add_u32 m0, s46, 32768\n\t" \
  "s_add_u32 s47, s55, 65536\n\t" \
  "buffer_load_dwordx4 v0, %[X], s47 offen sc1 lds\n\t" \
  "s_waitcnt vmcnt(5)\n\t" \
  "LTOP_%=:\n\t" \
  "s_barrier\n\t" \
  "ds_read_b128 v[64:67], v1 offset:0\n\t" \
  "ds_read_b128 v[68:71], v1 offset:2048\n\t" \
  "ds_read_b128 v[72:75], v2 offset:0\n\t" \
  "ds_read_b128 v[76:79], v2 offset:2048\n\t" \
  "ds_read_b128 v[80:83], v1 offset:4096\n\t" \
  "ds_read_b128 v[84:87], v1 offset:6144\n\t" \
  "s_mov_b32 s82, s83\n\t" \
  "s_xor_b32 s46, s46, 0x10000\n\t" \
  "s_waitcnt lgkmcnt(5)\n\t" \
  "v_mfma_f32_16x16x32_bf16 v[16:19], v[64:67], a[0:3], 0\n\t" \
  "ds_read_b128 v[88:91], v2 offset:4096\n\t" \
  "s_waitcnt lgkmcnt(5)\n\t" \
  "v_mfma_f32_16x16x32_bf16 v[20:23], v[68:71], a[0:3], 0\n\t" \
  "s_add_u32 s50, s48, 0\n\t" \
  "s_and_b32 s50, s50, " TK_TRM "\n\t" \
  "v_mfma_f32_16x16x32_bf16 v[24:27], v[64:67], a[72:75], 0\n\t" \
  "ds_read_b128 v[92:95], v2 offset:6144\n\t" \
  "v_mfma_f32_16x16x32_bf16 v[28:31], v[68:71], a[72:75], 0\n\t" \
  "s_lshr_b64 s[60:61], s[90:91], s50\n\t" \
  "s_and_b32 s60, s60, 1\n\t" \
  "s_waitcnt lgkmcnt(5)\n\t" \
  "v_mfma_f32_16x16x32_bf16 v[16:19], v[72:75], a[4:7], v[16:19]\n\t" \
  "ds_read_b128 v[96:99], v1 offset:8192\n\t" \
  "s_waitcnt lgkmcnt(5)\n\t" \
  "v_mfma_f32_16x16x32_bf16 v[20:23], v[76:79], a[4:7], v[20:23]\n\t" \
  "s_lshl_b32 s60, s60, 13\n\t" \
  "s_add_u32 s50, s50, " TK_SG0 "\n\t" \
  "v_mfma_f32_16x16x32_bf16 v[24:27], v[72:75], a[76:79], v[24:27]\n\t" \
  "ds_read_b128 v[100:103], v1 offset:10240\n\t" \
  "v_mfma_f32_16x16x32_bf16 v[28:31], v[76:79], a[76:79], v[28:31]\n\t" \
  "s_lshl_b32 s50, s50, 21\n\t" \
  "s_or_b32 s50, s50, s60\n\t" \
  "s_waitcnt lgkmcnt(5)\n\t" \
  "v_mfma_f32_16x16x32_bf16 v[16:19], v[80:83], a[8:11], v[16:19]\n\t" \
  "ds_read_b128 v[104:107], v2 offset:8192\n\t" \
  "s_waitcnt lgkmcnt(5)\n\t" \
  "v_mfma_f32_16x16x32_bf16 v[20:23], v[84:87], a[8:11], v[20:23]\n\t" \
  "s_add_u32 s50, s50, s37\n\t" \
  "s_add_u32 s51, s50, 4096\n\t" \
  "v_mfma_f32_16x16x32_bf16 v[24:27], v[80:83], a[80:83], v[24:27]\n\t" \
  "ds_read_b128 v[108:111], v2 offset:10240\n\t" \
  "v_mfma_f32_16x16x32_bf16 v[28:31], v[84:87], a[80:83], v[28:31]\n\t" \
  "s_add_u32 s83, s48, 1\n\t" \
  "s_lshr_b64 s[60:61], s[90:91], s83\n\t" \
  "s_waitcnt lgkmcnt(5)\n\t" \
  "v_mfma_f32_16x16x32_bf16 v[16:19], v[88:91], a[12:15], v[16:19]\n\t" \
  "ds_read_b128 v[112:115], v1 offset:12288\n\t" \
  "s_waitcnt lgkmcnt(5)\n\t" \
  "v_mfma_f32_16x16x32_bf16 v[20:23], v[92:95], a[12:15], v[20:23]\n\t" \
  "s_and_b32 s60, s60, 1\n\t" \
  "s_lshl_b32 s60, s60, 13\n\t" \
  "v_mfma_f32_16x16x32_bf16 v[24:27], v[88:91], a[84:87], v[24:27]\n\t" \
  "ds_read_b128 v[116:119], v1 offset:14336\n\t" \
  "v_mfma_f32_16x16x32_bf16 v[28:31], v[92:95], a[84:87], v[28:31]\n\t" \
  "s_add_u32 s83, s83, " TK_SG0 "\n\t" \
  "s_lshl_b32 s83, s83, 21\n\t" \
  "s_waitcnt lgkmcnt(5)\n\t" \
  "v_mfma_f32_16x16x32_bf16 v[16:19], v[96:99], a[16:19], v[16:19]\n\t" \
  "ds_read_b128 v[120:123], v2 offset:12288\n\t" \
  "s_waitcnt lgkmcnt(5)\n\t" \
  "v_mfma_f32_16x16x32_bf16 v[20:23], v[100:103], a[16:19], v[20:23]\n\t" \
  "s_or_b32 s83, s83, s60\n\t" \
  "s_cmp_lg_u32 s56, 0\n\t" \
  "v_mfma_f32_16x16x32_bf16 v[24:27], v[96:99], a[88:91], v[24:27]\n\t" \
  "ds_read_b128 v[124:127], v2 offset:14336\n\t" \
  "v_mfma_f32_16x16x32_bf16 v[28:31], v[100:103], a[88:91], v[28:31]\n\t" \
  "s_cbranch_scc1 LWARM_%=\n\t" \
  "s_waitcnt vmcnt(0)\n\t" \
  "LWARM_%=:\n\t" \
  "s_barrier\n\t" \
  "s_waitcnt lgkmcnt(5)\n\t" \
  "v_mfma_f32_16x16x32_bf16 v[16:19], v[104:107], a[20:23], v[16:19]\n\t" \
  "ds_read_b128 v[128:131], v1 offset:16384\n\t" \
  "s_waitcnt lgkmcnt(5)\n\t" \
  "v_mfma_f32_16x16x32_bf16 v[20:23], v[108:111], a[20:23], v[20:23]\n\t" \
  "v_mfma_f32_16x16x32_bf16 v[24:27], v[104:107], a[92:95], v[24:27]\n\t" \
  "ds_read_b128 v[132:135], v1 offset:18432\n\t" \
  "v_mfma_f32_16x16x32_bf16 v[28:31], v[108:111], a[92:95], v[28:31]\n\t" \
  "s_add_i32 s62, s48, s42\n\t" \
  "s_add_i32 s62, s62, 1\n\t" \
  "s_waitcnt lgkmcnt(5)\n\t" \
  "v_mfma_f32_16x16x32_bf16 v[16:19], v[112:115], a[24:27], v[16:19]\n\t" \
  "ds_read_b128 v[136:139], v2 offset:16384\n\t" \
  "s_waitcnt lgkmcnt(5)\n\t" \
  "v_mfma_f32_16x16x32_bf16 v[20:23], v[116:119], a[24:27], v[20:23]\n\t" \
  "s_mov_b32 s61, s62\n\t" \
  "v_mfma_f32_16x16x32_bf16 v[24:27], v[112:115], a[96:99], v[24:27]\n\t" \
  "ds_read_b128 v[140:143], v2 offset:18432\n\t" \
  "v_mfma_f32_16x16x32_bf16 v[28:31], v[116:119], a[96:99], v[28:31]\n\t" \
  "s_mul_i32 s63, s62, 43\n\t" \
  "s_lshr_b32 s63, s63, 9\n\t" \
  "s_waitcnt lgkmcnt(5)\n\t" \
  "v_mfma_f32_16x16x32_bf16 v[16:19], v[120:123], a[28:31], v[16:19]\n\t" \
  "ds_read_b128 v[144:147], v1 offset:20480\n\t" \
  "s_waitcnt lgkmcnt(5)\n\t" \
  "v_mfma_f32_16x16x32_bf16 v[20:23], v[124:127], a[28:31], v[20:23]\n\t" \
  "s_mul_i32 s63, s63, " TK_RM "\n\t" \
  "s_sub_u32 s62, s62, s63\n\t" \
  "v_mfma_f32_16x16x32_bf16 v[24:27], v[120:123], a[100:103], v[24:27]\n\t" \
  "ds_read_b128 v[148:151], v1 offset:22528\n\t" \
  "v_mfma_f32_16x16x32_bf16 v[28:31], v[124:127], a[100:103], v[28:31]\n\t" \
  "s_mul_i32 s63, s62, 43\n\t" \
  "s_lshr_b32 s63, s63, 7\n\t" \
  "s_waitcnt lgkmcnt(5)\n\t" \
  "v_mfma_f32_16x16x32_bf16 v[16:19], v[128:131], a[32:35], v[16:19]\n\t" \
  "ds_read_b128 v[152:155], v2 offset:20480\n\t" \
  "s_waitcnt lgkmcnt(5)\n\t" \
  "v_mfma_f32_16x16x32_bf16 v[20:23], v[132:135], a[32:35], v[20:23]\n\t" \
  "s_mul_i32 s57, s62, 688128\n\t" \
  "s_lshl_b32 s60, s63, 15\n\t" \
  "v_mfma_f32_16x16x32_bf16 v[24:27], v[128:131], a[104:107], v[24:27]\n\t" \
  "ds_read_b128 v[156:159], v2 offset:22528\n\t" \
  "v_mfma_f32_16x16x32_bf16 v[28:31], v[132:135], a[104:107], v[28:31]\n\t" \
  "s_add_u32 s57, s57, s60\n\t" \
  "s_lshr_b32 s60, s88, s63\n\t" \
  "s_waitcnt lgkmcnt(5)\n\t" \
  "v_mfma_f32_16x16x32_bf16 v[16:19], v[136:139], a[36:39], v[16:19]\n\t" \
  "ds_read_b128 v[160:163], v1 offset:24576\n\t" \
  "s_waitcnt lgkmcnt(5)\n\t" \
  "v_mfma_f32_16x16x32_bf16 v[20:23], v[140:143], a[36:39], v[20:23]\n\t" \
  "s_and_b32 s60, s60, 1\n\t" \
  "s_lshl_b32 s60, s60, 13\n\t" \
  "v_mfma_f32_16x16x32_bf16 v[24:27], v[136:139], a[108:111], v[24:27]\n\t" \
  "ds_read_b128 v[164:167], v1 offset:26624\n\t" \
  "v_mfma_f32_16x16x32_bf16 v[28:31], v[140:143], a[108:111], v[28:31]\n\t" \
  "s_add_u32 s57, s57, s60\n\t" \
  "s_add_u32 s57, s57, 656896\n\t" \
  "s_waitcnt lgkmcnt(5)\n\t" \
  "v_mfma_f32_16x16x32_bf16 v[16:19], v[144:147], a[40:43], v[16:19]\n\t" \
  "ds_read_b128 v[168:171], v2 offset:24576\n\t" \
  "s_waitcnt lgkmcnt(5)\n\t" \
  "v_mfma_f32_16x16x32_bf16 v[20:23], v[148:151], a[40:43], v[20:23]\n\t" \
  "s_cmp_lt_i32 s61, 1\n\t" \
  "s_cselect_b32 s57, 0xf0000000, s57\n\t" \
  "v_mfma_f32_16x16x32_bf16 v[24:27], v[144:147], a[112:115], v[24:27]\n\t" \
  "ds_read_b128 v[172:175], v2 offset:26624\n\t" \
  "v_mfma_f32_16x16x32_bf16 v[28:31], v[148:151], a[112:115], v[28:31]\n\t" \
  "s_cmp_lg_u32 s40, 0\n\t" \
  "s_cselect_b32 s57, 0xf0000000, s57\n\t" \
  "s_waitcnt lgkmcnt(5)\n\t" \
  "v_mfma_f32_16x16x32_bf16 v[16:19], v[152:155], a[44:47], v[16:19]\n\t" \
  "ds_read_b128 v[176:179], v1 offset:28672\n\t" \
  "s_waitcnt lgkmcnt(5)\n\t" \
  "v_mfma_f32_16x16x32_bf16 v[20:23], v[156:159], a[44:47], v[20:23]\n\t" \
  "s_add_u32 s80, s48, " TK_TRN "\n\t" \
  "s_lshr_b64 s[60:61], s[90:91], s80\n\t" \
  "s_and_b32 s60, s60, 1\n\t" \
  "v_mfma_f32_16x16x32_bf16 v[24:27], v[152:155], a[116:119], v[24:27]\n\t" \
  "ds_read_b128 v[180:183], v1 offset:30720\n\t" \
  "v_mfma_f32_16x16x32_bf16 v[28:31], v[156:159], a[116:119], v[28:31]\n\t" \
  "s_lshl_b32 s60, s60, 13\n\t" \
  "s_add_u32 s80, s80, " TK_SG0 "\n\t" \
  "s_lshl_b32 s80, s80, 21\n\t" \
  "s_waitcnt lgkmcnt(5)\n\t" \
  "v_mfma_f32_16x16x32_bf16 v[16:19], v[160:163], a[48:51], v[16:19]\n\t" \
  "ds_read_b128 v[184:187], v2 offset:28672\n\t" \
  "s_waitcnt lgkmcnt(5)\n\t" \
  "v_mfma_f32_16x16x32_bf16 v[20:23], v[164:167], a[48:51], v[20:23]\n\t" \
  "s_or_b32 s80, s80, s60\n\t" \
  "s_add_u32 s62, s83, s39\n\t" \
  "s_add_u32 s80, s80, s39\n\t" \
  "v_mfma_f32_16x16x32_bf16 v[24:27], v[160:163], a[120:123], v[24:27]\n\t" \
  "ds_read_b128 v[188:191], v2 offset:30720\n\t" \
  "v_mfma_f32_16x16x32_bf16 v[28:31], v[164:167], a[120:123], v[28:31]\n\t" \
  "s_add_u32 s80, s80, 2048\n\t" \
  "s_waitcnt lgkmcnt(5)\n\t" \
  "v_mfma_f32_16x16x32_bf16 v[16:19], v[168:171], a[52:55], v[16:19]\n\t" \
  "ds_read_b128 v[192:195], v1 offset:32768\n\t" \
  "s_waitcnt lgkmcnt(5)\n\t" \
  "v_mfma_f32_16x16x32_bf16 v[20:23], v[172:175], a[52:55], v[20:23]\n\t" \
  "v_mfma_f32_16x16x32_bf16 v[24:27], v[168:171], a[124:127], v[24:27]\n\t" \
  "ds_read_b128 v[196:199], v1 offset:34816\n\t" \
  "v_mfma_f32_16x16x32_bf16 v[28:31], v[172:175], a[124:127], v[28:31]\n\t" \
  "s_cmp_lt_u32 s48, " TK_TRM "\n\t" \
  "s_cselect_b32 s80, s62, s80\n\t" \
  "s_waitcnt lgkmcnt(5)\n\t" \
  "v_mfma_f32_16x16x32_bf16 v[16:19], v[176:179], a[56:59], v[16:19]\n\t" \
  "ds_read_b128 v[200:203], v2 offset:32768\n\t" \
  "s_waitcnt lgkmcnt(5)\n\t" \
  "v_mfma_f32_16x16x32_bf16 v[20:23], v[180:183], a[56:59], v[20:23]\n\t" \
  "v_add_u32 v7, s62, v3\n\t" \
  "s_mov_b32 exec_lo, 0\n\t" \
  "v_mov_b32 v7, s80\n\t" \
  "s_mov_b32 exec_lo, -1\n\t" \
  "v_mfma_f32_16x16x32_bf16 v[24:27], v[176:179], a[128:131], v[24:27]\n\t" \
  "ds_read_b128 v[204:207], v2 offset:34816\n\t" \
  "v_mfma_f32_16x16x32_bf16 v[28:31], v[180:183], a[128:131], v[28:31]\n\t" \
  "s_waitcnt lgkmcnt(5)\n\t" \
  "v_mfma_f32_16x16x32_bf16 v[16:19], v[184:187], a[60:63], v[16:19]\n\t" \
  "s_waitcnt lgkmcnt(4)\n\t" \
  "v_mfma_f32_16x16x32_bf16 v[20:23], v[188:191], a[60:63], v[20:23]\n\t" \
  "buffer_load_dword v4, v7, %[X], 0 offen sc1\n\t" \
  "v_mfma_f32_16x16x32_bf16 v[24:27], v[184:187], a[132:135], v[24:27]\n\t" \
  "v_mfma_f32_16x16x32_bf16 v[28:31], v[188:191], a[132:135], v[28:31]\n\t" \
  "s_waitcnt lgkmcnt(3)\n\t" \
  "v_mfma_f32_16x16x32_bf16 v[16:19], v[192:195], a[64:67], v[16:19]\n\t" \
  "v_xor_b32 v1, 0x10000, v1\n\t" \
  "v_xor_b32 v2, 0x10000, v2\n\t" \
  "s_waitcnt lgkmcnt(2)\n\t" \
  "v_mfma_f32_16x16x32_bf16 v[20:23], v[196:199], a[64:67], v[20:23]\n\t" \
  "v_mfma_f32_16x16x32_bf16 v[24:27], v[192:195], a[136:139], v[24:27]\n\t" \
  "v_mfma_f32_16x16x32_bf16 v[28:31], v[196:199], a[136:139], v[28:31]\n\t" \
  "s_waitcnt lgkmcnt(1)\n\t" \
  "v_mfma_f32_16x16x32_bf16 v[16:19], v[200:203], a[68:71], v[16:19]\n\t" \
  "s_waitcnt lgkmcnt(0)\n\t" \
  "v_mfma_f32_16x16x32_bf16 v[20:23], v[204:207], a[68:71], v[20:23]\n\t" \
  "v_mfma_f32_16x16x32_bf16 v[24:27], v[200:203], a[140:143], v[24:27]\n\t" \
  "v_mfma_f32_16x16x32_bf16 v[28:31], v[204:207], a[140:143], v[28:31]\n\t" \
  "v_mfma_f32_16x16x32_bf16 v[32:35], v[64:67], a[144:147], 0\n\t" \
  "v_cvt_pk_bf16_f32 v48, v16, v17\n\t" \
  "v_cvt_pk_bf16_f32 v49, v18, v19\n\t" \
  "v_mfma_f32_16x16x32_bf16 v[36:39], v[68:71], a[144:147], 0\n\t" \
  "v_cvt_pk_bf16_f32 v50, v20, v21\n\t" \
  "v_cvt_pk_bf16_f32 v51, v22, v23\n\t" \
  "v_mfma_f32_16x16x32_bf16 v[32:35], v[72:75], a[148:151], v[32:35]\n\t" \
  "v_cvt_pk_bf16_f32 v52, v24, v25\n\t" \
  "v_cvt_pk_bf16_f32 v53, v26, v27\n\t" \
  "v_mfma_f32_16x16x32_bf16 v[36:39], v[76:79], a[148:151], v[36:39]\n\t" \
  "v_cvt_pk_bf16_f32 v54, v28, v29\n\t" \
  "v_cvt_pk_bf16_f32 v55, v30, v31\n\t" \
  "v_mfma_f32_16x16x32_bf16 v[32:35], v[80:83], a[152:155], v[32:35]\n\t" \
  "buffer_store_dwordx4 v[48:51], v0, %[X], s50 offen offset:0 sc1\n\t" \
  "v_mfma_f32_16x16x32_bf16 v[36:39], v[84:87], a[152:155], v[36:39]\n\t" \
  "v_mfma_f32_16x16x32_bf16 v[32:35], v[88:91], a[156:159], v[32:35]\n\t" \
  "v_mfma_f32_16x16x32_bf16 v[36:39], v[92:95], a[156:159], v[36:39]\n\t" \
  "v_mfma_f32_16x16x32_bf16 v[32:35], v[96:99], a[160:163], v[32:35]\n\t" \
  "v_mfma_f32_16x16x32_bf16 v[36:39], v[100:103], a[160:163], v[36:39]\n\t" \
  "v_mfma_f32_16x16x32_bf16 v[32:35], v[104:107], a[164:167], v[32:35]\n\t" \
  "v_mfma_f32_16x16x32_bf16 v[36:39], v[108:111], a[164:167], v[36:39]\n\t" \
  "v_mfma_f32_16x16x32_bf16 v[32:35], v[112:115], a[168:171], v[32:35]\n\t" \
  "v_mfma_f32_16x16x32_bf16 v[36:39], v[116:119], a[168:171], v[36:39]\n\t" \
  "v_mfma_f32_16x16x32_bf16 v[32:35], v[120:123], a[172:175], v[32:35]\n\t" \
  "v_mfma_f32_16x16x32_bf16 v[36:39], v[124:127], a[172:175], v[36:39]\n\t" \
  "s_waitcnt vmcnt(1)\n\t" \
  "v_cmp_eq_u32 vcc, 0, v4\n\t" \
  "v_mfma_f32_16x16x32_bf16 v[32:35], v[128:131], a[176:179], v[32:35]\n\t" \
  "v_mfma_f32_16x16x32_bf16 v[36:39], v[132:135], a[176:179], v[36:39]\n\t" \
  "s_cmp_eq_u64 vcc, 0\n\t" \
  "s_cselect_b32 s60, 1, 0\n\t" \
  "v_mov_b32 v4, s60\n\t" \
  "v_mfma_f32_16x16x32_bf16 v[32:35], v[136:139], a[180:183], v[32:35]\n\t" \
  "ds_write_b32 v5, v4\n\t" \
  "v_mfma_f32_16x16x32_bf16 v[36:39], v[140:143], a[180:183], v[36:39]\n\t" \
  "v_mfma_f32_16x16x32_bf16 v[32:35], v[144:147], a[184:187], v[32:35]\n\t" \
  "v_mfma_f32_16x16x32_bf16 v[36:39], v[148:151], a[184:187], v[36:39]\n\t" \
  "buffer_store_dwordx4 v[52:55], v0, %[X], s50 offen offset:1024 sc1\n\t" \
  "v_mfma_f32_16x16x32_bf16 v[32:35], v[152:155], a[188:191], v[32:35]\n\t" \
  "v_mfma_f32_16x16x32_bf16 v[36:39], v[156:159], a[188:191], v[36:39]\n\t" \
  "s_waitcnt lgkmcnt(0)\n\t" \
  "s_barrier\n\t" \
  "v_mfma_f32_16x16x32_bf16 v[32:35], v[160:163], a[192:195], v[32:35]\n\t" \
  "ds_read_b32 v4, v6 offset:40959\n\t" \
  "v_mfma_f32_16x16x32_bf16 v[36:39], v[164:167], a[192:195], v[36:39]\n\t" \
  "v_mfma_f32_16x16x32_bf16 v[32:35], v[168:171], a[196:199], v[32:35]\n\t" \
  "v_mfma_f32_16x16x32_bf16 v[36:39], v[172:175], a[196:199], v[36:39]\n\t" \
  "v_mfma_f32_16x16x32_bf16 v[32:35], v[176:179], a[200:203], v[32:35]\n\t" \
  "v_mfma_f32_16x16x32_bf16 v[36:39], v[180:183], a[200:203], v[36:39]\n\t" \
  "s_waitcnt lgkmcnt(0)\n\t" \
  "v_readfirstlane_b32 s60, v4\n\t" \
  "s_add_u32 s55, s83, s36\n\t" \
  "v_mfma_f32_16x16x32_bf16 v[32:35], v[184:187], a[204:207], v[32:35]\n\t" \
  "s_cmp_eq_u32 s48, 63\n\t" \
  "s_cselect_b32 s56, 0, s60\n\t" \
  "v_mfma_f32_16x16x32_bf16 v[36:39], v[188:191], a[204:207], v[36:39]\n\t" \
  "s_cmp_eq_u32 s56, 0\n\t" \
  "s_cselect_b32 s55, 0xf0000000, s55\n\t" \
  "v_mfma_f32_16x16x32_bf16 v[32:35], v[192:195], a[208:211], v[32:35]\n\t" \
  "v_mfma_f32_16x16x32_bf16 v[36:39], v[196:199], a[208:211], v[36:39]\n\t" \
  "v_mfma_f32_16x16x32_bf16 v[32:35], v[200:203], a[212:215], v[32:35]\n\t" \
  "s_add_u32 m0, s46, 0\n\t" \
  "s_add_u32 s47, s55, 0\n\t" \
  "buffer_load_dwordx4 v0, %[X], s47 offen sc1 lds\n\t" \
  "v_mfma_f32_16x16x32_bf16 v[36:39], v[204:207], a[212:215], v[36:39]\n\t" \
  "v_mfma_f32_16x16x32_bf16 v[40:43], v[64:67], a[216:219], 0\n\t" \
  "v_mfma_f32_16x16x32_bf16 v[44:47], v[68:71], a[216:219], 0\n\t" \
  "v_mfma_f32_16x16x32_bf16 v[40:43], v[72:75], a[220:223], v[40:43]\n\t" \
  "v_cvt_pk_bf16_f32 v56, v32, v33\n\t" \
  "v_cvt_pk_bf16_f32 v57, v34, v35\n\t" \
  "s_add_u32 m0, s46, 4096\n\t" \
  "s_add_u32 s47, s55, 4096\n\t" \
  "buffer_load_dwordx4 v0, %[X], s47 offen sc1 lds\n\t" \
  "v_mfma_f32_16x16x32_bf16 v[44:47], v[76:79], a[220:223], v[44:47]\n\t" \
  "v_cvt_pk_bf16_f32 v58, v36, v37\n\t" \
  "v_cvt_pk_bf16_f32 v59, v38, v39\n\t" \
  "v_mfma_f32_16x16x32_bf16 v[40:43], v[80:83], a[224:227], v[40:43]\n\t" \
  "v_mfma_f32_16x16x32_bf16 v[44:47], v[84:87], a[224:227], v[44:47]\n\t" \
  "buffer_store_dwordx4 v[56:59], v0, %[X], s50 offen offset:2048 sc1\n\t" \
  "v_mfma_f32_16x16x32_bf16 v[40:43], v[88:91], a[228:231], v[40:43]\n\t" \
  "s_add_u32 m0, s46, 8192\n\t" \
  "s_add_u32 s47, s55, 16384\n\t" \
  "buffer_load_dwordx4 v0, %[X], s47 offen sc1 lds\n\t" \
  "v_mfma_f32_16x16x32_bf16 v[44:47], v[92:95], a[228:231], v[44:47]\n\t" \
  "v_mfma_f32_16x16x32_bf16 v[40:43], v[96:99], a[232:235], v[40:43]\n\t" \
  "v_mfma_f32_16x16x32_bf16 v[44:47], v[100:103], a[232:235], v[44:47]\n\t" \
  "v_mfma_f32_16x16x32_bf16 v[40:43], v[104:107], a[236:239], v[40:43]\n\t" \
  "s_add_u32 m0, s46, 12288\n\t" \
  "s_add_u32 s47, s55, 20480\n\t" \
  "buffer_load_dwordx4 v0, %[X], s47 offen sc1 lds\n\t" \
  "v_mfma_f32_16x16x32_bf16 v[44:47], v[108:111], a[236:239], v[44:47]\n\t" \
  "v_mfma_f32_16x16x32_bf16 v[40:43], v[112:115], a[240:243], v[40:43]\n\t" \
  "v_mfma_f32_16x16x32_bf16 v[44:47], v[116:119], a[240:243], v[44:47]\n\t" \
  "v_mfma_f32_16x16x32_bf16 v[40:43], v[120:123], a[244:247], v[40:43]\n\t" \
  "s_add_u32 m0, s46, 16384\n\t" \
  "s_add_u32 s47, s55, 32768\n\t" \
  "buffer_load_dwordx4 v0, %[X], s47 offen sc1 lds\n\t" \
  "v_mfma_f32_16x16x32_bf16 v[44:47], v[124:127], a[244:247], v[44:47]\n\t" \
  "v_mfma_f32_16x16x32_bf16 v[40:43], v[128:131], a[248:251], v[40:43]\n\t" \
  "v_mfma_f32_16x16x32_bf16 v[44:47], v[132:135], a[248:251], v[44:47]\n\t" \
  "v_mfma_f32_16x16x32_bf16 v[40:43], v[136:139], a[252:255], v[40:43]\n\t" \
  "s_add_u32 m0, s46, 20480\n\t" \
  "s_add_u32 s47, s55, 36864\n\t" \
  "buffer_load_dwordx4 v0, %[X], s47 offen sc1 lds\n\t" \
  "v_mfma_f32_16x16x32_bf16 v[44:47], v[140:143], a[252:255], v[44:47]\n\t" \
  "v_mfma_f32_16x16x32_bf16 v[40:43], v[144:147], v[224:227], v[40:43]\n\t" \
  "v_mfma_f32_16x16x32_bf16 v[44:47], v[148:151], v[224:227], v[44:47]\n\t" \
  "v_mfma_f32_16x16x32_bf16 v[40:43], v[152:155], v[228:231], v[40:43]\n\t" \
  "s_add_u32 m0, s46, 24576\n\t" \
  "s_add_u32 s47, s55, 49152\n\t" \
  "buffer_load_dwordx4 v0, %[X], s47 offen sc1 lds\n\t" \
  "v_mfma_f32_16x16x32_bf16 v[44:47], v[156:159], v[228:231], v[44:47]\n\t" \
  "v_mfma_f32_16x16x32_bf16 v[40:43], v[160:163], v[232:235], v[40:43]\n\t" \
  "v_mfma_f32_16x16x32_bf16 v[44:47], v[164:167], v[232:235], v[44:47]\n\t" \
  "v_mfma_f32_16x16x32_bf16 v[40:43], v[168:171], v[236:239], v[40:43]\n\t" \
  "s_add_u32 m0, s46, 28672\n\t" \
  "s_add_u32 s47, s55, 53248\n\t" \
  "buffer_load_dwordx4 v0, %[X], s47 offen sc1 lds\n\t" \
  "v_mfma_f32_16x16x32_bf16 v[44:47], v[172:175], v[236:239], v[44:47]\n\t" \
  "v_mfma_f32_16x16x32_bf16 v[40:43], v[176:179], v[240:243], v[40:43]\n\t" \
  "v_mfma_f32_16x16x32_bf16 v[44:47], v[180:183], v[240:243], v[44:47]\n\t" \
  "v_mfma_f32_16x16x32_bf16 v[40:43], v[184:187], v[244:247], v[40:43]\n\t" \
  "s_add_u32 m0, s46, 32768\n\t" \
  "s_add_u32 s47, s55, 65536\n\t" \
  "buffer_load_dwordx4 v0, %[X], s47 offen sc1 lds\n\t" \
  "v_mfma_f32_16x16x32_bf16 v[44:47], v[188:191], v[244:247], v[44:47]\n\t" \
  "v_mfma_f32_16x16x32_bf16 v[40:43], v[192:195], v[248:251], v[40:43]\n\t" \
  "v_mfma_f32_16x16x32_bf16 v[44:47], v[196:199], v[248:251], v[44:47]\n\t" \
  "buffer_load_dword v8, off, %[X], s57 sc1\n\t" \
  "v_mfma_f32_16x16x32_bf16 v[40:43], v[200:203], v[252:255], v[40:43]\n\t" \
  "v_mfma_f32_16x16x32_bf16 v[44:47], v[204:207], v[252:255], v[44:47]\n\t" \
  "s_nop 4\n\t" \
  "v_cvt_pk_bf16_f32 v60, v40, v41\n\t" \
  "v_cvt_pk_bf16_f32 v61, v42, v43\n\t" \
  "s_nop 1\n\t" \
  "v_cvt_pk_bf16_f32 v62, v44, v45\n\t" \
  "v_cvt_pk_bf16_f32 v63, v46, v47\n\t" \
  "buffer_store_dwordx4 v[60:63], v0, %[X], s50 offen offset:3072 sc1\n\t" \
  "s_add_u32 s49, s82, s38\n\t" \
  "s_cmp_eq_u32 s48, 63\n\t" \
  "s_cbranch_scc1 LEND_%=\n\t" \
  "s_add_u32 s48, s48, 1\n\t" \
  "s_cmp_eq_u32 s56, 0\n\t" \
  "s_cbranch_scc1 LCOLD_%=\n\t" \
  "s_waitcnt vmcnt(0)\n\t" \
  "s_mov_b64 exec, 1\n\t" \
  "buffer_store_dword v6, off, %[X], s49 sc1\n\t" \
  "s_mov_b64 exec, -1\n\t" \
  "s_mov_b32 s49, 0xf0000000\n\t" \
  "s_cmp_lt_i32 s57, 0\n\t" \
  "s_cbranch_scc1 LTOP_%=\n\t" \
  "s_movk_i32 s63, 4000\n\t" \
  "s_branch LGW_%=u\n\t" \
  "LGW_%=:\n\t" \
  "s_sleep " TK_PSG "\n\t" \
  "buffer_load_dword v8, off, %[X], s57 sc1\n\t" \
  "s_waitcnt vmcnt(0)\n\t" \
  "LGW_%=u:\n\t" \
  "s_memrealtime s[52:53]\n\t" \
  "v_readfirstlane_b32 s60, v8\n\t" \
  "s_waitcnt lgkmcnt(0)\n\t" \
  "s_sub_u32 s63, s63, 1\n\t" \
  "s_cbranch_scc1 LTOP_%=\n\t" \
  "s_cmp_eq_u32 s60, -1\n\t" \
  "s_cbranch_scc1 LGW_%=\n\t" \
  "s_not_b32 s60, s60\n\t" \
  "s_lshl_b32 s61, s52, 8\n\t" \
  "s_sub_u32 s60, s60, s61\n\t" \
  "s_ashr_i32 s60, s60, 8\n\t" \
  "s_sub_i32 s60, s60, " TK_QXW "\n\t" \
  "s_min_i32 s60, s60, 400\n\t" \
  "s_add_u32 s54, s52, s60\n\t" \
  "LGW_%=w:\n\t" \
  "s_sleep 1\n\t" \
  "s_memrealtime s[52:53]\n\t" \
  "s_waitcnt lgkmcnt(0)\n\t" \
  "s_sub_i32 s53, s52, s54\n\t" \
  "s_cmp_lt_i32 s53, 0\n\t" \
  "s_cbranch_scc1 LGW_%=w\n\t" \
  "s_branch LTOP_%=\n\t" \
  "LEND_%=:\n\t" \
  "s_waitcnt vmcnt(0)\n\t" \
  "s_mov_b64 exec, 1\n\t" \
  "buffer_store_dword v6, off, %[X], s49 sc1\n\t" \
  "s_mov_b64 exec, -1\n\t" \
  "s_mov_b32 s49, 0xf0000000\n\t" \
  ""
__device__ __forceinline__ unsigned hoff(int r, int piece) {
  const int m = 4 * (r >> 3) + (r & 3), kp = piece >> 3;
  return (P_H + (unsigned)(kp >> 1)) * PAIR + (unsigned)((kp & 1) * 4096 + (16 * ((r >> 2) & 1) + m) * 128 + (((piece ^ m) & 7) << 4));
}
template <int SLEEP>
__device__ __forceinline__ void wait_all(rsrc_t X, unsigned vo, unsigned so) {
  unsigned spins = 0;
  while (true) {
    asm volatile("" ::: "memory");
    if (__all(ld4(X, vo, so) != 0u)) break;
    if (spin_fail(spins, X)) break;
    __builtin_amdgcn_s_sleep(SLEEP);
  }
  asm volatile("" ::: "memory");
}
#define VQ_CLOB "v8", "v16", "v17", "v18", "v19", "v20", "v21", "v22", "v23", "v24", "v25", "v26", "v27", "v28", "v29", "v30", "v31", "v32", "v33", "v34", "v35", "v36", "v37", "v38", "v39", "v40", "v41", "v42", "v43", "v44", "v45", "v46", "v47", "v48", "v49", "v50", "v51", "v52", "v53", "v54", "v55", "v56", "v57", "v58", "v59", "v60", "v61", "v62", "v63", "v64", "v65", "v66", "v67", "v68", "v69", "v70", "v71", "v72", "v73", "v74", "v75", "v76", "v77", "v78", "v79", "v80", "v81", "v82", "v83", "v84", "v85", "v86", "v87", "v88", "v89", "v90", "v91", "v92", "v93", "v94", "v95", "v96", "v97", "v98", "v99", "v100", "v101", "v102", "v103", "v104", "v105", "v106", "v107", "v108", "v109", "v110", "v111", "v112", "v113", "v114", "v115", "v116", "v117", "v118", "v119", "v120", "v121", "v122", "v123", "v124", "v125", "v126", "v127", "v128", "v129", "v130", "v131", "v132", "v133", "v134", "v135", "v136", "v137", "v138", "v139", "v140", "v141", "v142", "v143", "v144", "v145", "v146", "v147", "v148", "v149", "v150", "v151", "v152", "v153", "v154", "v155", "v156", "v157", "v158", "v159", "v160", "v161", "v162", "v163", "v164", "v165", "v166", "v167", "v168", "v169", "v170", "v171", "v172", "v173", "v174", "v175", "v176", "v177", "v178", "v179", "v180", "v181", "v182", "v183", "v184", "v185", "v186", "v187", "v188", "v189", "v190", "v191", "v192", "v193", "v194", "v195", "v196", "v197", "v198", "v199", "v200", "v201", "v202", "v203", "v204", "v205", "v206", "v207", "v220", "v221"
#define VA_CLOB "v40", "v41", "v42", "v43", "v44", "v45", "v46", "v47", "v48", "v49", "v50", "v51", "v52", "v53", "v54", "v55", "v56", "v57", "v58", "v59", "v60", "v61", "v62", "v63", "v64", "v65", "v66", "v67", "v68", "v69", "v70", "v71", "v72", "v73", "v74", "v75", "v76", "v77", "v78", "v79", "v80", "v81", "v82", "v83", "v84", "v85", "v86", "v87", "v88", "v89", "v90", "v91", "v92", "v93", "v94", "v95", "v96", "v97", "v98", "v99", "v100", "v101", "v102", "v103", "v104", "v105", "v106", "v107", "v108", "v109", "v110", "v111", "v112", "v113", "v114", "v115", "v116", "v117", "v118", "v119", "v120", "v121", "v122", "v123", "v124", "v125", "v126", "v127", "v128", "v129", "v130", "v131", "v132", "v133", "v134", "v135", "v136", "v137", "v138", "v139", "v140", "v141", "v142", "v143", "v144", "v145", "v146", "v147", "v148", "v149", "v150", "v151", "v152", "v153", "v154", "v155", "v156", "v157", "v158", "v159", "v160", "v161", "v162", "v163", "v164", "v165", "v166", "v167", "v168", "v169", "v170", "v171", "v172", "v173", "v174", "v175", "v176", "v177", "v178", "v179", "v180", "v181", "v182", "v183", "v184", "v185", "v186", "v187", "v188", "v189", "v190", "v191", "v192", "v193", "v194", "v195", "v196", "v197", "v198", "v199", "v200", "v201", "v202", "v203", "v204", "v205", "v206", "v207", "v208", "v209", "v210", "v211", "v220", "v221"
#define TOK_COMMON() \
  const MP p = getp(); \
  const int c = (int)blockIdx.x - NCHW, xq = c & 3, L = (c >> 2) & 1, i = c >> 3; \
  const int tid = threadIdx.x, lane = tid & 63, w = __builtin_amdgcn_readfirstlane(tid >> 6); \
  const rsrc_t X = mkr(p.xb, XB_BYTES)
#define W_IN [W] "s"(WR)
#define W_CLOB "a0", "a1", "a2", "a3", "a4", "a5", "a6", "a7", "a8", "a9", "a10", "a11", "a12", "a13", "a14", "a15", "a16", "a17", "a18", "a19", "a20", "a21", "a22", "a23", "a24", "a25", "a26", "a27", "a28", "a29", "a30", "a31", "a32", "a33", "a34", "a35", "a36", "a37", "a38", "a39", "a40", "a41", "a42", "a43", "a44", "a45", "a46", "a47", "a48", "a49", "a50", "a51", "a52", "a53", "a54", "a55", "a56", "a57", "a58", "a59", "a60", "a61", "a62", "a63", "a64", "a65", "a66", "a67", "a68", "a69", "a70", "a71", "a72", "a73", "a74", "a75", "a76", "a77", "a78", "a79", "a80", "a81", "a82", "a83", "a84", "a85", "a86", "a87", "a88", "a89", "a90", "a91", "a92", "a93", "a94", "a95", "a96", "a97", "a98", "a99", "a100", "a101", "a102", "a103", "a104", "a105", "a106", "a107", "a108", "a109", "a110", "a111", "a112", "a113", "a114", "a115", "a116", "a117", "a118", "a119", "a120", "a121", "a122", "a123", "a124", "a125", "a126", "a127", "a128", "a129", "a130", "a131", "a132", "a133", "a134", "a135", "a136", "a137", "a138", "a139", "a140", "a141", "a142", "a143", "a144", "a145", "a146", "a147", "a148", "a149", "a150", "a151", "a152", "a153", "a154", "a155", "a156", "a157", "a158", "a159", "a160", "a161", "a162", "a163", "a164", "a165", "a166", "a167", "a168", "a169", "a170", "a171", "a172", "a173", "a174", "a175", "a176", "a177", "a178", "a179", "a180", "a181", "a182", "a183", "a184", "a185", "a186", "a187", "a188", "a189", "a190", "a191", "a192", "a193", "a194", "a195", "a196", "a197", "a198", "a199", "a200", "a201", "a202", "a203", "a204", "a205", "a206", "a207", "a208", "a209", "a210", "a211", "a212", "a213", "a214", "a215", "a216", "a217", "a218", "a219", "a220", "a221", "a222", "a223", "a224", "a225", "a226", "a227", "a228", "a229", "a230", "a231", "a232", "a233", "a234", "a235", "a236", "a237", "a238", "a239", "a240", "a241", "a242", "a243", "a244", "a245", "a246", "a247", "a248", "a249", "a250", "a251", "a252", "a253", "a254", "a255", "v224", "v225", "v226", "v227", "v228", "v229", "v230", "v231", "v232", "v233", "v234", "v235", "v236", "v237", "v238", "v239", "v240", "v241", "v242", "v243", "v244", "v245", "v246", "v247", "v248", "v249", "v250", "v251", "v252", "v253", "v254", "v255"
#define BOOT_IN "{s67}"(L), [ctab] "i"(CTAB_OFF), [rm] "i"(SRING == 12 ? 12 : 0), [trm] "i"(TRING - 1), [trn] "i"(1 - TRING)
#define S_CLOB "s80", "s81", "s76", "s78", "s79", "s86", "s88", "s89", "s90", "s91", "s46", "s47", "s48", "s49", "s50", "s51", "s52", "s53", "s54", "s55", "s56", "s57", "s58", "s59", "s60", "s61", "s62", "s63", "s68", "s69", "s70", "s71", "s72", "s73", "s82", "s83", "s84", "m0", "vcc", "scc", "memory"
typedef unsigned v8w __attribute__((ext_vector_type(8)));

__device__ __forceinline__ void qkv_role() {
  TOK_COMMON();
  const int role = i * 4 + w, hh = 8 * xq + role / 6, part = role % 6, which = part >> 1, half = part & 1;
  const rsrc_t WR = mkr((which == 0 ? p.Wq : (which == 1 ? p.Wk : p.Wv)) + hh * DH + 64 * half, DM * HD * 2u - (unsigned)(hh * DH + 64 * half) * 2u);
  const int m = lane & 15, g = lane >> 4;
  const unsigned a0 = (unsigned)(m * 128 + ((g ^ (m & 7)) << 4));
  v8w lv = {(unsigned)lane * 16u, a0, a0 ^ 64u, (unsigned)(lane & (L ? 15 : 3)) * 128u, 0u, 40960u + 4u * (unsigned)w, 1u, 0u};
  const unsigned c36 = P_H * PAIR + (unsigned)w * 1024u, c37 = (unsigned)(3 * hh + which) * PAIR + (unsigned)half * (which == 2 ? 4096u : 2048u);
  const unsigned c38 = P_FA * PAIR + (unsigned)(hh * 128 + part * 16), c39 = w == 0 ? P_FB * PAIR + 4096u : 0xF0000000u, c41 = (unsigned)w * 1024u;
  const int c42 = L ? 1 - QLEAD1 : 1 - QLEAD;
  unsigned d74 = TRC_OFF + 262144u + (unsigned)((c * 4 + w) * 66) * 8u;
#define Q_CALL(A) asm volatile("s_waitcnt vmcnt(0) lgkmcnt(0)\n\t" A : "+{v[0:7]}"(lv), "+{s74}"(d74) \
               : [X] "s"(X), "{s36}"(c36), "{s37}"(c37), "{s38}"(c38), "{s39}"(c39), "{s40}"(w), "{s41}"(c41), "{s42}"(c42), "{s45}"(i), "{s92}"(TMO_OFF), BOOT_IN, [cap] "i"(SPIN_CAP), [qxw] "i"(QXW), [qxc] "i"(QXC), W_IN : VQ_CLOB, W_CLOB, S_CLOB)
  if (which == 2) { Q_CALL(QV_ASM "LQVEND:"); __builtin_amdgcn_endpgm(); }
  Q_CALL(QK_ASM "LQKEND:");
  __builtin_amdgcn_endpgm();
}

__device__ __forceinline__ void ap_role() {
  TOK_COMMON();
  const int hp = 4 * xq + ((i - 12) >> 1), ch = i & 1, a = w >> 1, rt = w & 1, h = 2 * hp + a;
  const int n = lane & 15, g = lane >> 4;
  const rsrc_t WR = mkr(p.Wo + (long)hp * 2 * DH * DM, 2u * DH * DM * 2u);
  const unsigned l16 = (unsigned)lane * 16u, ab = (unsigned)a * 24576u + l16;
  v16u lv = {l16, ab, ab + (unsigned)rt * 4096u, 49152u + (unsigned)w * 4096u + l16, 49152u + (unsigned)(w ^ 2) * 4096u + l16, l16 & ~32u, l16 >> 1,
             (unsigned)(g * 262144 + n * 16), (unsigned)(n * 32 + g * 8), (unsigned)((lane & 7) % 6) * 16u, 0u, 0u, 0u, 0u, 0u, 0u};
  v8w mk;
  const int qi = 8 * (n >> 2) + (n & 3) + 4 * rt;
#pragma unroll
  for (int k = 0; k < 8; ++k) mk[k] = 8 * g + k > qi ? 0xFF800000u : 0u;
  v16u zz = {0u, 0u, 0u, 0u, 0u, 0u, 0u, 0u, 0u, 0u, 0u, 0u, 0u, 0u, 0u, 0u};
  const unsigned tb = (unsigned)(3 * h) * PAIR;
  const unsigned c36 = tb + (rt ? PAIR + 4096u : 0u), c37 = tb + (rt ? 2u * PAIR : 4096u), c38 = tb + (rt ? 2u * PAIR + 4096u : PAIR);
  const unsigned c39 = (P_KV + (unsigned)(h >> 1)) * PAIR + (unsigned)(h & 1) * 4096u, c41 = P_FA * PAIR + (unsigned)h * 128u;
  const unsigned c42 = w == 0 ? P_FB * PAIR + (unsigned)(2 * hp + ch) * 128u : 0xF0000000u;
  const unsigned c43 = TP0 * XBLK + (unsigned)(8 * rt + (hp >> 3)) * PAIR + (unsigned)((hp & 7) * 1024 + ch * 512 + a * 256);
  unsigned d44 = (TP0 + 8u) * XBLK + (unsigned)(hp >> 1) * PAIR + (unsigned)((((hp & 1) * 4 + 2 * ch + a) * 2 + rt) * 512);
  const unsigned c45 = (unsigned)(a * 24576 + rt * 12288);
  unsigned d74 = TRC_OFF + 262144u + (unsigned)((c * 4 + w) * 66) * 8u;
  asm volatile("s_waitcnt vmcnt(0) lgkmcnt(0)\n\t" AP_ASM "LAPEND:" : "+{v[0:15]}"(lv), "+{v[24:39]}"(zz), "+{s74}"(d74), "+{s44}"(d44)
               : [X] "s"(X), "{s36}"(c36), "{s37}"(c37), "{s38}"(c38), "{s39}"(c39), "{s41}"(c41), "{s42}"(c42), "{s43}"(c43), "{s45}"(c45), "{s64}"(512 * ch + 256 * a), "{s65}"(1024 + 64 * ch + 32 * a), "{s66}"(147456 * a), "{s77}"(L ? AX1 : AX), BOOT_IN,
                 "{s92}"(TMO_OFF), [cap] "i"(SPIN_CAP), "{v[16:23]}"(mk), W_IN : VA_CLOB, W_CLOB, S_CLOB);
  __builtin_amdgcn_endpgm();
}

template <int N> __device__ __forceinline__ float rorN(float v) { return __uint_as_float((unsigned)__builtin_amdgcn_update_dpp(0, (int)__float_as_uint(v), 0x120 + N, 0xf, 0xf, false)); }
__device__ __forceinline__ void up8(v4u v, float* x) {
  x[0] = lo2f(v.x); x[1] = hi2f(v.x); x[2] = lo2f(v.y); x[3] = hi2f(v.y); x[4] = lo2f(v.z); x[5] = hi2f(v.z); x[6] = lo2f(v.w); x[7] = hi2f(v.w);
}
__device__ __forceinline__ v4u pk8(const float* x) { return (v4u){pk2(x[0], x[1]), pk2(x[2], x[3]), pk2(x[4], x[5]), pk2(x[6], x[7])}; }

__device__ __forceinline__ void rb9(float (&x)[9]) {
#pragma unroll
  for (int i = 0; i < 8; i += 2) { const unsigned u = pk2(x[i], x[i + 1]); x[i] = lo2f(u); x[i + 1] = hi2f(u); }
  x[8] = lo2f(pk2(x[8], x[8]));
}
template <int R>
__device__ __forceinline__ void lnR(const float (&x)[R][9], float (&o)[R][9]) {
  float s[2 * R], pv[R];
#pragma unroll
  for (int r = 0; r < R; ++r) {
    pv[r] = __uint_as_float(__builtin_amdgcn_readfirstlane(__float_as_uint(x[r][0])));
    const f32x2 p2 = {pv[r], pv[r]};
    f32x2 d[4];
#pragma unroll
    for (int i = 0; i < 4; ++i) d[i] = (f32x2){x[r][2 * i], x[r][2 * i + 1]} - p2;
    const float d8 = x[r][8] - pv[r];
    const f32x2 a = (d[0] + d[1]) + (d[2] + d[3]);
    const f32x2 q = __builtin_elementwise_fma(d[3], d[3], __builtin_elementwise_fma(d[2], d[2], __builtin_elementwise_fma(d[1], d[1], d[0] * d[0])));
    s[2 * r] = (a[0] + d8) + a[1];
    s[2 * r + 1] = __builtin_fmaf(d8, d8, q[0]) + q[1];
  }
#pragma unroll
  for (int k = 0; k < 2 * R; ++k) s[k] += rorN<8>(s[k]);
#pragma unroll
  for (int k = 0; k < 2 * R; ++k) s[k] += rorN<4>(s[k]);
#pragma unroll
  for (int k = 0; k < 2 * R; ++k) s[k] += rorN<2>(s[k]);
#pragma unroll
  for (int k = 0; k < 2 * R; ++k) s[k] += rorN<1>(s[k]);
#pragma unroll
  for (int r = 0; r < R; ++r) {
    auto u = __builtin_amdgcn_permlane16_swap(__float_as_uint(s[2 * r]), __float_as_uint(s[2 * r + 1]), false, false);
    s[2 * r] = __uint_as_float(u[0]) + __uint_as_float(u[1]);
  }
#pragma unroll
  for (int r = 0; r < R; ++r) {
    const unsigned u = __float_as_uint(s[2 * r]);
    const float md = (__uint_as_float(__builtin_amdgcn_readlane(u, 0)) + __uint_as_float(__builtin_amdgcn_readlane(u, 32))) * (1.0f / DM);
    const float sq = (__uint_as_float(__builtin_amdgcn_readlane(u, 16)) + __uint_as_float(__builtin_amdgcn_readlane(u, 48))) * (1.0f / DM);
    const unsigned mv = pk2(pv[r] + md, __builtin_fmaxf(__builtin_fmaf(-md, md, sq), 0.f));
    const float m = lo2f(mv), var = hi2f(mv);
    const float ve = lo2f(pk2(var + 1e-5f, 0.f));
    const float den = lo2f(pk2(__builtin_amdgcn_sqrtf(ve), 0.f));
    const float inv = __builtin_amdgcn_rcpf(den);
#pragma unroll
    for (int i = 0; i < 9; ++i) o[r][i] = (x[r][i] - m) * inv;
  }
}
__device__ __forceinline__ void ln9(const float (&x)[9], float (&o)[9]) { lnR<1>((const float (&)[1][9])x, (float (&)[1][9])o); }
__device__ __forceinline__ v4u gat8(float v) {
  const unsigned d = pk2(v, __uint_as_float((unsigned)__builtin_amdgcn_mov_dpp((int)__float_as_uint(v), 0xB1, 0xf, 0xf, true)));
  return (v4u){d, (unsigned)__builtin_amdgcn_mov_dpp((int)d, 0x102, 0xf, 0xf, true), (unsigned)__builtin_amdgcn_mov_dpp((int)d, 0x104, 0xf, 0xf, true),
               (unsigned)__builtin_amdgcn_mov_dpp((int)d, 0x106, 0xf, 0xf, true)};
}
__device__ __forceinline__ void tok_row(const MP& p, const rsrc_t X, unsigned sb1, int L, int s, int r, int lane, const v4u (&v)[18], v4u px, v4u py, unsigned px9, unsigned py9) {
  const long tok = (long)s * SEG + r;
  const int m = lane & 15, g = lane >> 4;
  const int xp = 4 * (g & 1) + 2 * (g >> 1) + (m >> 3);
  const unsigned zr = opq(0u);
  const unsigned one = (g == (m >> 2)) ? (opq(0x3F80u) << (16 * (m & 1))) : zr;
  const unsigned ax = (m & 2) ? zr : one, ay = (m & 2) ? one : zr;
  const v4u A1 = {ax, ay, zr, zr}, A2 = {zr, zr, ax, ay};
  const unsigned on2 = opq(0x3F80u) << (16 * (m & 1));
  const int q2 = (m >> 1) & 3;
  const v4u sel = {q2 == 0 ? on2 : zr, q2 == 1 ? on2 : zr, q2 == 2 ? on2 : zr, q2 == 3 ? on2 : zr};
  f32x4 d1, d2, e1;
  asm volatile("s_nop 1\n\t" MF "%0, %1, %3, 0\n\t" MF "%0, %2, %3, %0" : "=&v"(e1) : "v"(v[0]), "v"(v[1]), "v"(sel));
#pragma unroll
  for (int i = 0; i < 16; ++i) {
    if (i == 0) asm volatile("s_nop 1\n\t" MF "%0, %2, %4, 0\n\t" MF "%1, %3, %4, 0" : "=&v"(d1), "=&v"(d2) : "v"(A1), "v"(A2), "v"(v[2]));
    else asm volatile(MF "%0, %2, %4, %0\n\t" MF "%1, %3, %4, %1" : "+v"(d1), "+v"(d2) : "v"(A1), "v"(A2), "v"(v[2 + i]));
  }
  asm volatile("s_nop 7" : "+v"(d1), "+v"(d2), "+v"(e1));
  float o[9];
#pragma unroll
  for (int i = 0; i < 4; ++i) { o[i] = d1[i]; o[4 + i] = d2[i]; }
  {
    auto a02 = __builtin_amdgcn_permlane32_swap(__float_as_uint(e1[0]), __float_as_uint(e1[2]), false, false);
    auto a13 = __builtin_amdgcn_permlane32_swap(__float_as_uint(e1[1]), __float_as_uint(e1[3]), false, false);
    o[8] = m >= 8 ? __uint_as_float(a13[0]) + __uint_as_float(a13[1]) : __uint_as_float(a02[0]) + __uint_as_float(a02[1]);
  }
  float x[9];
  up8(px, x); x[8] = lo2f(px9);
#pragma unroll
  for (int i = 0; i < 9; ++i) x[i] += o[i];
  if (L == 0) {
    float y[9], ln[9];
    up8(py, y); y[8] = lo2f(py9);
    rb9(x);
    ln9(x, ln);
#pragma unroll
    for (int i = 0; i < 9; ++i) x[i] = __builtin_fmaf(ln[i], y[i], x[i]);
    rb9(x);
    st16(X, (unsigned)lane * 16u, sb1 + (P_X3 + (unsigned)(r >> 3)) * PAIR + (unsigned)(r & 7) * 1024u, pk8(x));
    const v4u x9 = gat8(x[8]);
    if ((lane & 7) == 0) st16(X, (unsigned)xp * 16u, sb1 + (P_H + 4u) * PAIR + 4096u + (unsigned)r * 128u, x9);
    ln9(x, ln);
    st16(X, hoff(r, lane), sb1, pk8(ln));
    const v4u h9 = gat8(ln[8]);
    if ((lane & 7) == 0) st16(X, hoff(r, 64 + xp), sb1, h9);
  } else {
    const rsrc_t O = mkr(p.out + tok * DM, DM * 2u);
    __builtin_amdgcn_raw_buffer_store_b128(pk8(x), O, (unsigned)lane * 16u, 0u, OAUX);
    __builtin_amdgcn_raw_buffer_store_b128(gat8(x[8]), O, (lane & 7) == 0 ? 1024u + (unsigned)xp * 16u : 0xF0000000u, 0u, OAUX);
  }
}

__device__ __forceinline__ void row_role(char* smem) {
  TOK_COMMON();
  const int rw = 4 * xq + (i - 20), r = 2 * rw + (w >> 1);
  const unsigned long long sm = die_mask(X, SG0, L, lane);
  const unsigned tm = (unsigned)die_mask(X, TP0, L, lane), dm = (unsigned)die_mask(X, 0u, L, lane);
  __attribute__((address_space(3))) unsigned* cnt = (__attribute__((address_space(3))) unsigned*)smem;
  BAR();
  if (tid < 64) cnt[tid] = 0u;
  BAR();
  for (int s = w & 1; s < NSEG; s += 2) {
    int ln_ = lane; asm volatile("" : "+v"(ln_));
    const int m = ln_ & 15, g = ln_ >> 4;
    const unsigned l16 = (unsigned)ln_ * 16u, xo = (unsigned)(8 * (4 * (g & 1) + 2 * (g >> 1) + (m >> 3)) + (m & 7)) * 2u;
    const unsigned sb = SEGB(sm, s), sb1 = sb ^ (L ? 0u : 8192u);
    if (s + ARMK < NSEG) {
      const unsigned sv = opq(SENT), sl = SLOT_AT((s + ARMK) % SRING) + ((dm >> ((s + ARMK) % SRING / 3)) & 1u) * 8192u;
      for (int j = r; j < 84; j += 32) {
#pragma unroll
        for (int k = 0; k < 4; ++k) st16(X, l16 + (unsigned)k * 1024u, sl + (unsigned)(j >> 1) * PAIR + (unsigned)(j & 1) * 4096u, (v4u){sv, sv, sv, sv});
      }
    }
    wait_all<PSL>(X, 0u, sb + P_FB * PAIR + 4096u + (unsigned)(L ? rw : r >> 3) * 128u);
    const v4u px = ld16(X, l16, sb + (P_X3 + (unsigned)(r >> 3)) * PAIR + (unsigned)(r & 7) * 1024u);
    const unsigned px9 = __builtin_amdgcn_raw_buffer_load_b16(X, xo, sb + (P_H + 4u) * PAIR + 4096u + (unsigned)r * 128u, 16);
    const rsrc_t YX = mkr(p.yT + (2 * s + (r >> 4)) * 9216, L ? 0u : 18432u);
    const v2u ya = ld8(YX, (unsigned)(ln_ * 256 + ((r ^ m) & 15) * 8), 0u), yb = ld8(YX, (unsigned)(ln_ * 256 + 128 + ((r ^ m) & 15) * 8), 0u);
    const v4u py = {ya.x, ya.y, yb.x, yb.y};
    const unsigned py9 = __builtin_amdgcn_raw_buffer_load_b16(YX, 16384u + (xo >> 3) * 128u + (((unsigned)r ^ (xo >> 4)) & 15u) * 8u + (xo & 6u), 0u, 16);
    wait_all<PSP>(X, 0u, sb + P_FB * PAIR + (unsigned)r * 128u);
    __builtin_amdgcn_s_sleep(RONE);
    wait_all<PSP>(X, (unsigned)(ln_ & 31) * 128u, sb + P_FB * PAIR);
    const unsigned j = (unsigned)s & 15u, to = TP0 * XBLK + (j << 20) + ((tm >> (j >> 1)) & 1u) * 8192u + (unsigned)r * 2u * PAIR;
    const unsigned t9 = (TP0 + 8u) * XBLK + ((tm >> 8) & 1u) * 8192u + (j << 17) + (unsigned)(((r >> 2) & 1) * 512 + (4 * (r >> 3) + (r & 3)) * 32);
    const unsigned v9 = (unsigned)(g * (int)PAIR + (m >> 1) * 1024 + (m & 1) * 16);
    v4u v[18];
#pragma unroll
    for (int k = 0; k < 2; ++k) v[k] = ld16(X, v9, t9 + (unsigned)k * 4u * PAIR);
#pragma unroll
    for (int k = 0; k < 16; ++k) v[2 + k] = ld16(X, l16 + (unsigned)(k & 3) * 1024u, to + (unsigned)(k >> 3) * PAIR + (unsigned)(k & 4) * 1024u);
    SB();
    tok_row(p, X, sb1, L, s, r, ln_, v, px, py, px9, py9);
    VM0();
    unsigned o1 = 0u;
    if (ln_ == 0) o1 = __hip_atomic_fetch_add(cnt + s, 1u, __ATOMIC_RELAXED, __HIP_MEMORY_SCOPE_WORKGROUP);
    if (__builtin_amdgcn_readfirstlane(o1) == 1u && ln_ < 2 - L) st4(X, ln_ ? sb1 : sb + 2048u, P_FB * PAIR + 4096u + (unsigned)rw * 128u, opq(1u));
  }
}

__device__ __forceinline__ void rows_task(const MP& p, const rsrc_t X, unsigned long long sm, u16* ys, int grp, int ng) {
  const int tid = threadIdx.x, lane = tid & 63, w = __builtin_amdgcn_readfirstlane(tid >> 6);
  const int m = lane & 15, g = lane >> 4;
  const int xp = 4 * (g & 1) + 2 * (g >> 1) + (m >> 3), c9 = 512 + 8 * xp + (m & 7);
  const unsigned sb = SEGB(sm, grp >> 2);
  v4u yv[5], xa[4];
  unsigned x9[4];
#pragma unroll
  for (int i = 0; i < 5; ++i) yv[i] = ld16(mkr(p.yT + (grp >> 1) * 9216, 18432u), (unsigned)(tid + 256 * i) * 16u, 0u);
#pragma unroll
  for (int rr = 0; rr < 4; ++rr) {
    const long t = grp * 8 + w + 4 * (rr & (2 * ng - 1));
    xa[rr] = *(const v4u*)&p.xin[t * DM + 8 * lane]; x9[rr] = p.xin[t * DM + c9];
  }
#pragma unroll
  for (int i = 0; i < 5; ++i) ((v4u*)ys)[tid + 256 * i] = yv[i];
  BAR();
  const unsigned o9 = (lane & 7) == 0 ? 0u : 0xF0000000u;
#pragma unroll 1
  for (int k = 0; k < ng; ++k) {
    float x[2][9], y[2][9], ln[2][9];
    const int t0 = (grp + k) * 8 + w;
#pragma unroll
    for (int rr = 0; rr < 2; ++rr) {
      const int j = (t0 + 4 * rr) & 15;
      const v2u ya = *(const v2u*)&ys[lane * 128 + (j ^ m) * 4], yb = *(const v2u*)&ys[lane * 128 + 64 + (j ^ m) * 4];
      up8(xa[rr], x[rr]); x[rr][8] = lo2f(x9[rr]); up8((v4u){ya.x, ya.y, yb.x, yb.y}, y[rr]); y[rr][8] = bf2f(ys[(c9 >> 2) * 64 + (j ^ xp) * 4 + (m & 3)]);
      xa[rr] = xa[2 + rr]; x9[rr] = x9[2 + rr];
    }
    lnR<2>(x, ln);
#pragma unroll
    for (int rr = 0; rr < 2; ++rr) {
#pragma unroll
      for (int i = 0; i < 9; ++i) x[rr][i] = __builtin_fmaf(ln[rr][i], y[rr][i], x[rr][i]);
      rb9(x[rr]);
      const int r = (t0 + 4 * rr) & 31;
      st16(X, (unsigned)lane * 16u, sb + (P_X3 + (unsigned)(r >> 3)) * PAIR + (unsigned)(r & 7) * 1024u, pk8(x[rr]));
      st16(X, (unsigned)xp * 16u + o9, sb + (P_H + 4u) * PAIR + 4096u + (unsigned)r * 128u, gat8(x[rr][8]));
    }
    lnR<2>(x, ln);
#pragma unroll
    for (int rr = 0; rr < 2; ++rr) {
      const int r = (t0 + 4 * rr) & 31;
      st16(X, hoff(r, lane), sb, pk8(ln[rr]));
      st16(X, hoff(r, 64 + xp) + o9, sb, gat8(ln[rr][8]));
    }
  }
  VM0();
  BAR();
  if (tid < ng) st4(X, (unsigned)tid * 128u, sb + P_FB * PAIR + 4096u + (unsigned)(grp & 3) * 128u, opq(1u));
}

__device__ __forceinline__ void arrive(const MP& p, int k, int grp) {
  VM0();
  BAR();
  if (threadIdx.x < 64) {
    unsigned old = 0u;
    if (threadIdx.x == 0) old = __hip_atomic_fetch_add(p.xb + MF_OFF / 4u + (k * 16 + grp) * 32, 1u, __ATOMIC_RELAXED, __HIP_MEMORY_SCOPE_AGENT);
    if (__builtin_amdgcn_readfirstlane(old) == 15u && threadIdx.x < 8) st4(mkr(p.xb, XB_BYTES), threadIdx.x * 128u, RDY_OFF(k) + (unsigned)grp * 1024u, opq(1u));
  }
}
__device__ __forceinline__ void wait_ready(const MP& p, int k) {
  if (threadIdx.x < 64) wait_all<PSL>(mkr(p.xb, XB_BYTES), (threadIdx.x % 9u) * 1024u, RDY_OFF(k) + (blockIdx.x & 7u) * 128u);
  BAR();
}
__device__ __forceinline__ void start_work(char* smem, int wk) {
  const MP p = getp();
  const int w = __builtin_amdgcn_readfirstlane(threadIdx.x >> 6);
#define NARM (ARMK - 3u)
  unsigned pf[PFM + 1];
  conv_task(p.kvT, p.ssm_k, p.ssm_D, p.yT, (u16*)(smem + w * 32768), 4 * wk + w, [&]() {
#pragma unroll
    for (int i = 0; i < PFM; ++i) {
      const int z = (i - 1) * 36864 + wk * 256 + threadIdx.x, a = z % 3;
      pf[i] = *(const unsigned*)(i == 0 ? p.Wo + (long)(z + 36864) * 64 : (a == 0 ? p.Wq : (a == 1 ? p.Wk : p.Wv)) + (long)(36863 - z / 3) * 64);
    }
  }, [&]() {
    const unsigned sv = opq(SENT);
    constexpr unsigned S16 = SLOT / 16u;
#pragma unroll
    for (unsigned k = 0; k < (NARM * S16 + 32767u) / 32768u; ++k) {
      const unsigned i = k * 32768u + wk * 256 + threadIdx.x;
      st16(mkr(p.xb, XB_BYTES), i < NARM * S16 && wk < 128 ? SLOT_AT(3u + i / S16) + (i % S16) * 16u : 0xF0000000u, 0u, (v4u){sv, sv, sv, sv});
    }
  });
#pragma unroll
  for (int i = 0; i < PFM; ++i) asm volatile("" :: "v"(pf[i]));
  arrive(p, 0, wk >> 4);
}
__device__ __forceinline__ void token_start(char* smem) {
  TOK_COMMON();
  (void)w;
  const int k = 4 * i + xq;
  if (L || (i >= 12 && i < 16)) start_work(smem, L ? 32 + k : 80 + k);
  if (i >= (L ? 12 : 20)) {
    const unsigned long long sm = die_mask(X, SG0, 0, lane);
    int g = L ? 111 - k : k - 96;
    while (g < 120) {
      if (g >= 0 && g < 64) {
        wait_ready(p, 0);
      }
      rows_task(p, X, sm, (u16*)smem, g < 0 ? g + 16 : 16 + 2 * g, g < 0 ? 1 : 2);
      g += g < 0 ? 16 : (L ? 48 : 120);
    }
  }
}
__device__ __forceinline__ void token_wg(char* smem) {
  if ((unsigned)(size_t)(__attribute__((address_space(3))) char*)smem != 0u) return;
  token_start(smem);
  const int i = ((int)blockIdx.x - NCHW) >> 3;
  if (i < 12) qkv_role();
  else if (i < 20) ap_role();
  else row_role(smem);
}

__global__ __launch_bounds__(256, 1) void k_main(MP) {
  extern __shared__ __attribute__((aligned(16))) char smem[];
  asm volatile("LKBEG:");
  if ((int)blockIdx.x < NCHW) chain_wg(smem);
  else token_wg(smem);
}

extern "C" void kernel_launch(void* const* d_in, const int* in_sizes, int n_in, void* d_out, int out_size, void* d_ws,
                              size_t ws_size, hipStream_t stream) {
  const u16* A_q = (const u16*)d_in[0];
  const u16* A_k = (const u16*)d_in[1];
  const u16* A_v = (const u16*)d_in[2];
  const u16* A_shift = (const u16*)d_in[3];
  const u16* A_ssmk = (const u16*)d_in[4];
  const u16* A_ssmD = (const u16*)d_in[5];
  const u16* B_mem0 = (const u16*)d_in[7];
  const u16* B_Wq = (const u16*)d_in[8];
  const u16* B_Wk = (const u16*)d_in[9];
  const u16* B_Wv = (const u16*)d_in[10];
  const u16* B_Wo = (const u16*)d_in[11];

  char* ws = (char*)d_ws;
  size_t off = (size_t)(-(uintptr_t)ws) & (XBLK - 1u);
  auto alloc = [&](size_t bytes) { char* q = ws + off; off += (bytes + 4095) & ~(size_t)4095; return q; };
  unsigned* xb = (unsigned*)alloc((size_t)XB_BYTES);
  u16* kvT = (u16*)alloc((size_t)DM * S_LEN * 2);
  u16* yT = (u16*)alloc((size_t)DM * S_LEN * 2);

  PreP pp;
  pp.kk = A_k; pp.vv = A_v; pp.shift_k = A_shift; pp.ssm_k = A_ssmk; pp.ssm_D = A_ssmD; pp.yT = yT; pp.kvT = kvT;
  pp.Wq = B_Wq; pp.Wk = B_Wk; pp.Wv = B_Wv; pp.Wo = B_Wo; pp.xb = xb; pp.mem0 = B_mem0;
  k_pre<<<240 + PRE_FILL + 256 + 288, 256, 0, stream>>>(pp);
  MP mp;
  mp.out = (u16*)d_out; mp.xb = xb;
  mp.xin = A_q; mp.yT = yT; mp.kvT = kvT; mp.ssm_k = A_ssmk; mp.ssm_D = A_ssmD;
  mp.Wq = B_Wq; mp.Wk = B_Wk; mp.Wv = B_Wv; mp.Wo = B_Wo;
  const int smem_bytes = 160 * 1024;
  (void)hipFuncSetAttribute((const void*)k_main, hipFuncAttributeMaxDynamicSharedMemorySize, smem_bytes);
  k_main<<<NCHW + NTOKW, 256, smem_bytes, stream>>>(mp);
}
```

```cpp
#include <hip/hip_runtime.h>
#include <stdint.h>

typedef unsigned short u16;
using bf16x8 = __attribute__((ext_vector_type(8))) short;
using f32x4 = __attribute__((ext_vector_type(4))) float;
using f32x2 = __attribute__((ext_vector_type(2))) float;
using bfx8 = __attribute__((ext_vector_type(8))) __bf16;
using bfx2 = __attribute__((ext_vector_type(2))) __bf16;
typedef unsigned v4u __attribute__((ext_vector_type(4)));
typedef unsigned v2u __attribute__((ext_vector_type(2)));
typedef short v4s __attribute__((ext_vector_type(4)));

#define S_LEN 2048
#define DM 576
#define NH 32
#define DH 128
#define HD (NH * DH)
#define SEG 32
#define NSEG 64

__device__ __forceinline__ float bf2f(u16 x) { return __uint_as_float(((unsigned)x) << 16); }
__device__ __forceinline__ unsigned pk2(float a, float b) { f32x2 v = {a, b}; bfx2 r = __builtin_convertvector(v, bfx2); return __builtin_bit_cast(unsigned, r); }
__device__ __forceinline__ float lo2f(unsigned u) { return __uint_as_float(u << 16); }
__device__ __forceinline__ float hi2f(unsigned u) { return __uint_as_float(u & 0xFFFF0000u); }

#define NFC 128
#define XBLK 2097152u
#define PAIR 16384u
#define SLOT 688128u
#define AGO 589824u
#define AGT 655360u
#define SG0 22
#define TP0 86
#define NXB 95
#define XB_BYTES (NXB * XBLK)
#define SLOT_AT(i) ((unsigned)((i) / 3) * XBLK + (unsigned)((i) % 3) * SLOT)
#define HT_OFF SLOT_AT(65)
#define CTAB_OFF (HT_OFF + 16384u)
#define MF_OFF (HT_OFF + 20480u)
#define TMO_OFF (MF_OFF + 15u * 128u)
#define RDY_OFF(k) (MF_OFF + 4096u + (k) * 10240u)
#define MF_BYTES 16384u
#define TRC_OFF SLOT_AT(64)
#define CAL_OFF (XBLK - 16384u)
#define P_KV 96u
#define P_H 112u
#define P_X3 117u
#define P_FA 121u
#define P_FB 122u
#define ARMK 8
#define NCAL 8
#define CALT 560u
#define CALM 1
#define SRING 12
#define TRING 64
__device__ __forceinline__ unsigned kv2(const unsigned (&u)[4], unsigned v, v2u w0, v2u w1) {
  const float a[4] = {hi2f(w0.y), lo2f(w0.y), hi2f(w0.x), lo2f(w0.x)}, b[4] = {hi2f(w1.y), lo2f(w1.y), hi2f(w1.x), lo2f(w1.x)};
  unsigned r = pk2(lo2f(u[0]) * a[0], hi2f(u[0]) * b[0]);
#pragma unroll
  for (int i = 1; i < 4; ++i) { const unsigned q = pk2(lo2f(u[i]) * a[i], hi2f(u[i]) * b[i]); r = pk2(lo2f(r) + lo2f(q), hi2f(r) + hi2f(q)); }
  return pk2(lo2f(r) * lo2f(v), hi2f(r) * hi2f(v));
}
struct PreP {
  const u16* kk; const u16* vv; const u16* shift_k; const u16* ssm_k; const u16* ssm_D; u16* yT; u16* kvT;
  const u16* Wq; const u16* Wk; const u16* Wv; const u16* Wo; unsigned* xb; const u16* mem0;
};
#define PRE_FILL 112
#define PFN 2
__global__ __launch_bounds__(256, 4) void k_pre(PreP p) {
  __shared__ __attribute__((aligned(16))) float sh[64 * 36];
  const int tid = threadIdx.x, b0 = blockIdx.x, b = b0 < 240 ? b0 : (b0 < 528 ? b0 + PRE_FILL + 256 : b0 - 288);
  const __amdgpu_buffer_rsrc_t rx = __builtin_amdgcn_make_buffer_rsrc((void*)p.xb, 0, XB_BYTES, 0x00020000);
  const v4u ones = {0xFFFFFFFFu, 0xFFFFFFFFu, 0xFFFFFFFFu, 0xFFFFFFFFu}, zero = {0, 0, 0, 0};
  if (b < 96) {
    if (tid >= 64) return;
    const unsigned cb = (unsigned)b * XBLK + CAL_OFF;
    __builtin_amdgcn_raw_buffer_store_b128(zero, rx, (unsigned)((tid >> 3) & 1) * 8192u + (unsigned)(tid >> 4) * 256u + (unsigned)(tid & 7) * 16u, cb, 16);
    asm volatile("s_waitcnt vmcnt(0)" ::: "memory");
    unsigned mn[2] = {~0u, ~0u};
    for (int i = 0; i < NCAL; ++i) {
      const unsigned long long c0 = __builtin_amdgcn_s_memtime();
      asm volatile("s_waitcnt lgkmcnt(0)" ::: "memory");
      const unsigned v = __builtin_amdgcn_raw_buffer_load_b32(rx, 0, cb + (unsigned)(i & 1) * 8192u + (unsigned)(i >> 1) * 256u, 16);
      asm volatile("s_waitcnt vmcnt(0)" :: "v"(v) : "memory");
      const unsigned d = (unsigned)(__builtin_amdgcn_s_memtime() - c0);
      if (i & 1) mn[1] = d < mn[1] ? d : mn[1]; else mn[0] = d < mn[0] ? d : mn[0];
      const unsigned lo = mn[0] < mn[1] ? mn[0] : mn[1], hi = mn[0] < mn[1] ? mn[1] : mn[0];
      if (i >= CALM && lo < CALT && hi > lo + 100u) break;
    }
    unsigned xcc; asm volatile("s_getreg_b32 %0, hwreg(HW_REG_XCC_ID)" : "=s"(xcc));
    const unsigned res = mn[1] < mn[0] ? 1u : 0u;
    if (tid == 0) __builtin_amdgcn_raw_buffer_store_b32(res ^ ((xcc >> 2) & 1u), rx, 0, CTAB_OFF + (unsigned)b * 4u, 16);
  } else if (b < 240) {
    const __amdgpu_buffer_rsrc_t rk = __builtin_amdgcn_make_buffer_rsrc((void*)p.kk, 0, S_LEN * DM * 2, 0x00020000);
    float (*kx)[132] = (float (*)[132])sh;
    float (*cf)[140] = (float (*)[140])(sh + 4 * 132);
    const int d0 = (b - 96) * 4, q = tid & 3, dd = (tid >> 2) & 3, t0 = (tid >> 4) * 8;
    const float Dv = bf2f(p.ssm_D[d0 + dd]);
    {
      const int s = tid >> 1, d = d0 + (tid & 1) * 2;
      unsigned u[4];
#pragma unroll
      for (int a = 0; a < 4; ++a) u[a] = __builtin_amdgcn_raw_buffer_load_b32(rk, (unsigned)((s - 3 + a) * DM + d) * 2u, 0, 0);
      const unsigned vr = *(const unsigned*)&p.vv[s * DM + d], c0 = *(const unsigned*)&p.ssm_k[(long)(d0 + (tid >> 6)) * S_LEN + (tid & 63) * 2];
      const v4u w = *(const v4u*)&p.shift_k[d * 4];
      const unsigned r = kv2(u, vr, (v2u){w.x, w.y}, (v2u){w.z, w.w});
      kx[(tid & 1) * 2][s] = lo2f(r); kx[(tid & 1) * 2 + 1][s] = hi2f(r);
      *(f32x2*)&cf[tid >> 6][8 + (tid & 63) * 2] = (f32x2){lo2f(c0), hi2f(c0)};
      if (tid < 32) cf[tid >> 3][tid & 7] = 0.f;
    }
    __syncthreads();
    float y[8];
#pragma unroll
    for (int i = 0; i < 8; ++i) y[i] = 0.f;
#pragma unroll 1
    for (int j = 8 * q; j <= t0; j += 32) {
      f32x4 kk[2], cw[4];
#pragma unroll
      for (int x = 0; x < 2; ++x) kk[x] = *(const f32x4*)&kx[dd][j + 4 * x];
#pragma unroll
      for (int x = 0; x < 4; ++x) cw[x] = *(const f32x4*)&cf[dd][t0 - j + 4 * x];
#pragma unroll
      for (int jj = 0; jj < 8; ++jj)
#pragma unroll
        for (int i = 0; i < 8; ++i) y[i] = __builtin_fmaf(cw[(8 + i - jj) >> 2][(8 + i - jj) & 3], kk[jj >> 2][jj & 3], y[i]);
    }
#pragma unroll
    for (int i = 0; i < 8; ++i) {
      y[i] += __uint_as_float((unsigned)__builtin_amdgcn_mov_dpp((int)__float_as_uint(y[i]), 0xB1, 0xf, 0xf, true));
      y[i] += __uint_as_float((unsigned)__builtin_amdgcn_mov_dpp((int)__float_as_uint(y[i]), 0x4E, 0xf, 0xf, true));
    }
    const float ya = q == 0 ? y[0] : (q == 1 ? y[2] : (q == 2 ? y[4] : y[6])), yb = q == 0 ? y[1] : (q == 1 ? y[3] : (q == 2 ? y[5] : y[7]));
    const int t = t0 + 2 * q, f = (d0 >> 3) & 15;
    const unsigned r0 = pk2(ya, yb), r1 = pk2(kx[dd][t] * Dv, kx[dd][t + 1] * Dv), o = pk2(lo2f(r0) + lo2f(r1), hi2f(r0) + hi2f(r1));
    u16* dst = p.yT + (t >> 4) * 9216 + (d0 >> 2) * 64 + dd;
    dst[((t & 15) ^ f) << 2] = (u16)(o & 0xFFFFu); dst[(((t + 1) & 15) ^ f) << 2] = (u16)(o >> 16);
  } else if (b < 240 + PRE_FILL) {
    const unsigned i0 = (unsigned)(b - 240) * 256u + tid;
    if (i0 < (SLOT - AGO) / 16u) {
      const unsigned o = AGO + i0 * 16u, s = o < AGT ? (i0 >> 11) * 4u + ((o >> 10) & 3u) : 8u, r = o & 1023u;
      const unsigned gm = s < 8u ? r >> 8 : (r >> 6) & 3u, m3 = (r >> 4) & 3u, ga = s < 8u ? (r >> 6) & 3u : r >> 8, row = 4u * (gm & 1u) + 2u * (gm >> 1) + (m3 & 1u), half = m3 >> 1;
      const unsigned* src = (const unsigned*)(p.mem0 + row * DM + 64u * s + 16u * ga + 2u * half);
      __builtin_amdgcn_raw_buffer_store_b128((v4u){src[0], src[2], src[4], src[6]}, rx, o, 0, 16);
    }
    for (unsigned i = i0; i < 131072u + MF_BYTES / 16u; i += PRE_FILL * 256)
      __builtin_amdgcn_raw_buffer_store_b128(zero, rx, i < 131072u ? (SG0 + (i >> 11)) * XBLK + P_FA * PAIR + (i & 2047u) * 16u : MF_OFF + (i & 2047u) * 16u, 0, 16);
    for (unsigned i = i0; i < 2u * SLOT / 16u; i += PRE_FILL * 256) __builtin_amdgcn_raw_buffer_store_b128(ones, rx, SLOT + i * 16u, 0, 16);
  } else if (b >= 240 + PRE_FILL + 256) {
    const __amdgpu_buffer_rsrc_t rk = __builtin_amdgcn_make_buffer_rsrc((void*)p.kk, 0, S_LEN * DM * 2, 0x00020000);
    u16 (*tile)[72] = (u16 (*)[72])sh;
    const int bid = b - 240 - PRE_FILL - 256, d0 = (bid % 9) * 64, s0 = (bid / 9) * 64;
    const int sr = (tid >> 3) * 2, d = d0 + (tid & 7) * 8;
    v4u kr[5], vr[2], w[4];
#pragma unroll
    for (int a = 0; a < 5; ++a) kr[a] = __builtin_amdgcn_raw_buffer_load_b128(rk, (unsigned)((s0 + sr - 3 + a) * DM + d) * 2u, 0, 0);
#pragma unroll
    for (int a = 0; a < 2; ++a) vr[a] = *(const v4u*)&p.vv[(long)(s0 + sr + a) * DM + d];
#pragma unroll
    for (int a = 0; a < 4; ++a) w[a] = *(const v4u*)&p.shift_k[(d + 2 * a) * 4];
    __builtin_amdgcn_sched_barrier(0);
#pragma unroll
    for (int it = 0; it < 2; ++it) {
      v4u o;
#pragma unroll
      for (int j = 0; j < 4; ++j) { const unsigned u[4] = {kr[it][j], kr[it + 1][j], kr[it + 2][j], kr[it + 3][j]}; o[j] = kv2(u, vr[it][j], (v2u){w[j].x, w[j].y}, (v2u){w[j].z, w[j].w}); }
      *(v4u*)&tile[sr + it][(tid & 7) * 8] = o;
    }
    __syncthreads();
    const int lane = tid & 63, wv = tid >> 6, g = lane >> 4, r = (lane >> 2) & 3, c = lane & 3;
    v2u o[4];
#pragma unroll
    for (int i = 0; i < 4; ++i) o[i] = __builtin_bit_cast(v2u, __builtin_amdgcn_ds_read_tr16_b64_v4i16((__attribute__((address_space(3))) v4s*)&tile[16 * g + 4 * i + r][16 * wv + 4 * c]));
    u16* dst = p.kvT + (long)(d0 + 16 * wv + (lane & 15)) * S_LEN + s0 + 16 * g;
    *(v4u*)dst = (v4u){o[0].x, o[0].y, o[1].x, o[1].y};
    *(v4u*)(dst + 8) = (v4u){o[2].x, o[2].y, o[3].x, o[3].y};
  } else {
    const int x = b & 7, k = (b - 240 - PRE_FILL) >> 3;
    unsigned long long pc; unsigned l0, l1, l2, l3; int rel;
    asm volatile("s_getpc_b64 %0\nLPRE:\n\ts_mov_b32 %1, LKBEG-LPRE\n\ts_mov_b32 %2, LCHEND-LKBEG\n\ts_mov_b32 %3, LQVEND-LKBEG\n\ts_mov_b32 %4, LQKEND-LKBEG\n\ts_mov_b32 %5, LAPEND-LKBEG"
                 : "=s"(pc), "=s"(rel), "=s"(l0), "=s"(l1), "=s"(l2), "=s"(l3));
    l0 = l0 > l1 ? l0 : l1; l2 = l2 > l3 ? l2 : l3; l0 = l0 > l2 ? l0 : l2;
    const unsigned o = (unsigned)(k * 64 + (tid & 63)) * 64u;
    unsigned a[PFN + 2];
    a[PFN] = *(const __attribute__((address_space(1))) unsigned*)(pc + rel + (o < l0 ? o : 0u));
    a[PFN + 1] = __builtin_amdgcn_raw_buffer_load_b32(rx, k == 31 ? tid * XBLK + CAL_OFF + 8064u : 0xF0000000u, 0, 16);
#pragma unroll
    for (int i = 0; i < PFN; ++i) {
      const int q = (i * 32 + k) * 256 + tid, r2 = q >> 5, gr = r2 >> 5, row = (gr / 3) * 64 + (x >> 2) * 32 + (r2 & 31), off = (q & 31) * 32, ty = gr % 3;
      a[i] = *(const unsigned*)(gr < 27 ? (ty == 0 ? p.Wq : (ty == 1 ? p.Wk : p.Wv)) + (long)row * HD + (x & 3) * 1024 + off
                                        : p.Wo + ((long)(x & 3) * DM + (gr < 36 ? gr - 27 : 0) * 64 + (x >> 2) * 32 + (r2 & 31)) * 1024 + off);
    }
#pragma unroll
    for (int i = 0; i < PFN + 2; ++i) asm volatile("" :: "v"(a[i]));
  }
}

#define CREV_STRIDE 2208
#define LWAIT() asm volatile("s_waitcnt lgkmcnt(0)" ::: "memory")
template <class E, class F>
__device__ __forceinline__ void conv_task(const u16* __restrict__ kvT, const u16* __restrict__ ssm_k, const u16* __restrict__ ssm_D, u16* yT, u16* shb, int d, E early, F fill) {
  const int lane = threadIdx.x & 63;
  u16* crev = shb + 64;
  u16* kvs = crev + 4 * CREV_STRIDE;
  const int X0 = 2048 + 63;
  v4u cv[4], kq[4];
#pragma unroll
  for (int j = 0; j < 4; ++j) { cv[j] = *(const v4u*)&ssm_k[(long)d * S_LEN + lane * 32 + 8 * j]; kq[j] = *(const v4u*)&kvT[(long)d * S_LEN + (j * 64 + lane) * 8]; }
  const float Dv = bf2f(ssm_D[d]);
  __builtin_amdgcn_sched_barrier(0);
  early();
  __builtin_amdgcn_sched_barrier(0);
  const v4u z4 = {0u, 0u, 0u, 0u};
  *(v4u*)&kvs[(lane >> 2) * 40 + (lane & 3) * 8] = z4;
  if (lane < 32) *(v4u*)&crev[(lane >> 3) * CREV_STRIDE + (lane & 7) * 8] = z4;
  if (lane < 36) *(v4u*)&crev[(lane / 9) * CREV_STRIDE + 2112 + (lane % 9) * 8] = z4;
  {
    unsigned r[18];
#pragma unroll
    for (int k = 0; k < 16; ++k) { const unsigned u = cv[3 - (k >> 2)][3 - (k & 3)]; r[k] = __builtin_amdgcn_alignbyte(u, u, 2); }
#pragma unroll
    for (int k = 0; k < 2; ++k) r[16 + k] = (unsigned)__builtin_amdgcn_update_dpp(0, (int)r[k], 0x138, 0xf, 0xf, true);
    LWAIT();
#pragma unroll
    for (int rho = 0; rho < 4; ++rho)
#pragma unroll
      for (int j = 0; j < 4; ++j) {
        v4u o;
#pragma unroll
        for (int k = 0; k < 4; ++k) { const int q = 4 * j + k + (rho >> 1); o[k] = (rho & 1) ? __builtin_amdgcn_alignbyte(r[q + 1], r[q], 2) : r[q]; }
        *(v4u*)&crev[rho * CREV_STRIDE + 2080 - 32 * lane + 8 * j] = o;
      }
    if (lane == 63) {
      *(v2u*)&crev[1 * CREV_STRIDE + 60] = (v2u){0u, r[0] << 16};
      *(v2u*)&crev[2 * CREV_STRIDE + 60] = (v2u){0u, r[0]};
      *(v2u*)&crev[3 * CREV_STRIDE + 60] = (v2u){r[0] << 16, __builtin_amdgcn_alignbyte(r[1], r[0], 2)};
    }
#pragma unroll
    for (int j = 0; j < 4; ++j) *(v4u*)&kvs[(16 + j * 16 + (lane >> 2)) * 40 + (lane & 3) * 8] = kq[j];
  }
  fill();
  LWAIT();
  const int n = lane & 15, g = lane >> 4;
  const int rho = (X0 - n) & 3;
  f32x4 acc[4][2];
#pragma unroll
  for (int q = 0; q < 8; ++q) acc[q >> 1][q & 1] = (f32x4){0.f, 0.f, 0.f, 0.f};
  {
    typedef __attribute__((address_space(3))) const v2u* l2p;
    typedef __attribute__((address_space(3))) const bf16x8* l4p;
    unsigned a0 = (unsigned)(size_t)(__attribute__((address_space(3))) u16*)&crev[rho * CREV_STRIDE + (X0 - (n - 8 * g) - rho)] - 3232u, a1 = a0 + 8u;
    unsigned b0 = (unsigned)(size_t)(__attribute__((address_space(3))) u16*)&kvs[(14 + n) * 40 + 8 * g];
    asm volatile("" : "+v"(a1));
#define AFR(m, f) ({ const unsigned o_ = 3232u - 64u * (m) - 1024u * ((f) >> 1) - 32u * ((f) & 1); const v2u lo_ = *(l2p)(size_t)(a0 + o_), hi_ = *(l2p)(size_t)(a1 + o_); \
                     __builtin_bit_cast(bf16x8, (v4u){lo_.x, lo_.y, hi_.x, hi_.y}); })
#define BFR(m, j) (*(l4p)(size_t)(b0 + 80u * (2 - (m)) + 1280u * (j)))
    bf16x8 af[2][8], bf[2][4];
#pragma unroll
    for (int f = 0; f < 8; ++f) { af[0][f] = AFR(0, f); af[1][f] = af[0][f]; __builtin_amdgcn_sched_barrier(0); }
#pragma unroll
    for (int j = 0; j < 4; ++j) { bf[0][j] = BFR(0, j); bf[1][j] = bf[0][j]; }
#pragma unroll 1
    for (int mo = 0; mo < 8; ++mo) {
#pragma unroll
      for (int m = 0; m < 2; ++m) {
        int k = 0;
#pragma unroll
        for (int a = 0; a < 4; ++a)
#pragma unroll
          for (int ct = a; ct < 4; ++ct)
#pragma unroll
            for (int ti = 0; ti < 2; ++ti) {
              acc[ct][ti] = __builtin_amdgcn_mfma_f32_16x16x32_bf16(__builtin_bit_cast(bfx8, af[m][2 * a + ti]), __builtin_bit_cast(bfx8, bf[m][ct - a]), acc[ct][ti], 0, 0, 0);
              if (k < 2) af[m ^ 1][k] = AFR(m + 1, k);
              else if (k < 6) bf[m ^ 1][k - 2] = BFR(m + 1, k - 2);
              else if (k < 12) af[m ^ 1][k - 4] = AFR(m + 1, k - 4);
              __builtin_amdgcn_sched_barrier(0);
              ++k;
            }
      }
      a0 -= 128u; a1 -= 128u; b0 -= 160u;
    }
  }
  u16* yo = crev;
  LWAIT();
#pragma unroll
  for (int q = 0; q < 8; ++q) {
    const int o = (16 * (q >> 1) + n) * 40 + 16 * (q & 1) + 4 * g;
    const v2u kx = *(const v2u*)&kvs[16 * 40 + o];
    const f32x4 y = acc[q >> 1][q & 1];
    const unsigned y0 = pk2(y[0], y[1]), y1 = pk2(y[2], y[3]), r0 = pk2(lo2f(kx.x) * Dv, hi2f(kx.x) * Dv), r1 = pk2(lo2f(kx.y) * Dv, hi2f(kx.y) * Dv);
    *(v2u*)&yo[o] = (v2u){pk2(lo2f(y0) + lo2f(r0), hi2f(y0) + hi2f(r0)), pk2(lo2f(y1) + lo2f(r1), hi2f(y1) + hi2f(r1))};
  }
  LWAIT();
  __builtin_amdgcn_s_barrier();
  asm volatile("" ::: "memory");
  {
    const int tid = threadIdx.x, f = (d >> 3) & 15;
    const u16* y0 = shb - (tid >> 6) * 16384 + 64;
#pragma unroll
    for (int k = 0; k < 4; ++k) {
      const int tile = 32 * k + (tid >> 3), t = 16 * tile + 2 * ((tid & 7) ^ (f >> 1));
      unsigned u[4];
#pragma unroll
      for (int c = 0; c < 4; ++c) u[c] = *(const unsigned*)&y0[c * 16384 + (t >> 5) * 40 + (t & 31)];
      const v2u e = {__builtin_amdgcn_perm(u[1], u[0], 0x05040100u), __builtin_amdgcn_perm(u[3], u[2], 0x05040100u)}, o = {__builtin_amdgcn_perm(u[1], u[0], 0x07060302u), __builtin_amdgcn_perm(u[3], u[2], 0x07060302u)};
      if (tile >= NFC / 16) __builtin_amdgcn_raw_buffer_store_b128((f & 1) ? (v4u){o.x, o.y, e.x, e.y} : (v4u){e.x, e.y, o.x, o.y}, __builtin_amdgcn_make_buffer_rsrc((void*)yT, 0, DM * S_LEN * 2, 0x00020000),
                                                                   tile * 18432 + (tid & 7) * 16, (d >> 2) * 128, 16);
    }
  }
}

#define NCHW 64
#define NTOKW 192
#define PSL 4
#define SENT 0xFFFFFFFFu
#define SPIN_CAP 300000u
struct MP {
  const u16* Wq; const u16* Wk; const u16* Wv; const u16* Wo; u16* out; unsigned* xb;
  const u16* xin; u16* yT; u16* kvT; const u16* ssm_k; const u16* ssm_D;
};

__device__ __forceinline__ MP getp() {
  MP r;
#if defined(__HIP_DEVICE_COMPILE__)
  const __attribute__((address_space(4))) unsigned long long* k = (const __attribute__((address_space(4))) unsigned long long*)__builtin_amdgcn_kernarg_segment_ptr();
  asm volatile("" : "+s"(k));
#define GP(T, i) ((T*)(__attribute__((address_space(1))) T*)k[i])
  r.Wq = GP(const u16, 0);
  r.Wk = GP(const u16, 1);
  r.Wv = GP(const u16, 2);
  r.Wo = GP(const u16, 3);
  r.out = GP(u16, 4);
  r.xb = GP(unsigned, 5);
  r.xin = GP(const u16, 6);
  r.yT = GP(u16, 7);
  r.kvT = GP(u16, 8);
  r.ssm_k = GP(const u16, 9);
  r.ssm_D = GP(const u16, 10);
#endif
  return r;
}
typedef __amdgpu_buffer_rsrc_t rsrc_t;
__device__ __forceinline__ rsrc_t mkr(const void* p, unsigned bytes) { return __builtin_amdgcn_make_buffer_rsrc((void*)p, 0, bytes, 0x00020000); }
__device__ __forceinline__ void st16(rsrc_t r, unsigned vo, unsigned so, v4u v) { __builtin_amdgcn_raw_buffer_store_b128(v, r, vo, so, 16); }
__device__ __forceinline__ void st4(rsrc_t r, unsigned vo, unsigned so, unsigned v) { __builtin_amdgcn_raw_buffer_store_b32(v, r, vo, so, 16); }
__device__ __forceinline__ v4u ld16(rsrc_t r, unsigned vo, unsigned so) { return __builtin_amdgcn_raw_buffer_load_b128(r, vo, so, 16); }
__device__ __forceinline__ v2u ld8(rsrc_t r, unsigned vo, unsigned so) { return __builtin_amdgcn_raw_buffer_load_b64(r, vo, so, 16); }
__device__ __forceinline__ unsigned ld4(rsrc_t r, unsigned vo, unsigned so) { return __builtin_amdgcn_raw_buffer_load_b32(r, vo, so, 16); }
__device__ __forceinline__ unsigned opq(unsigned x) { asm volatile("" : "+v"(x)); return x; }

__device__ __forceinline__ unsigned long long die_mask(rsrc_t X, unsigned first, int L, int lane) { return __ballot((ld4(X, (unsigned)lane * 4u, CTAB_OFF + first * 4u) & 1u) != (unsigned)L); }
#define SEGB(m, s) ((unsigned)(SG0 + (s)) * XBLK + (unsigned)(((m) >> (s)) & 1ull) * 8192u)
__device__ __forceinline__ bool spin_fail(unsigned& spins, rsrc_t F) {
  ++spins;
  if (spins > SPIN_CAP) { unsigned one = 0x40000000u | (blockIdx.x << 8) | threadIdx.x; asm volatile("" : "+v"(one)); if (__builtin_amdgcn_readfirstlane(ld4(F, 0, TMO_OFF)) == 0u) st4(F, 0, TMO_OFF, one); return true; }
  if ((spins & 1023u) == 0u) return __builtin_amdgcn_readfirstlane(ld4(F, 0, TMO_OFF)) != 0;
  return false;
}
__device__ __forceinline__ unsigned rtc() { return (unsigned)__builtin_amdgcn_s_memrealtime(); }
#define BAR() do { asm volatile("s_waitcnt lgkmcnt(0)" ::: "memory"); __builtin_amdgcn_s_barrier(); asm volatile("" ::: "memory"); } while (0)
#define SB() __builtin_amdgcn_sched_barrier(0)
#define MF "v_mfma_f32_16x16x32_bf16 "
#define VM0() __builtin_amdgcn_s_waitcnt(0x0F70)
#define DL1 12
#define SCH_B0 48
#define DLL1 6
#define DLL2 0
#define DLF 24
#define SCH_PEN 0
#define DLX 4
#define LATE 2
#define TKL 28
#define FIN -3
#define CH_FIN CH_S(FIN)
#define DLT 16
#define DLY 20
#define CH_DLT CH_S(DLT)
#define CH_DLY CH_S(DLY)
#define ES 6
#define ED 4
#define EB 8
#define CH_ES CH_S(ES)
#define CH_EX "%[ex]"
#define DRYL 1
#define CH_DRYL CH_S(DRYL)
#define SMAP 0x1023u
#define RO1 4
#define CH_RO1 CH_S(RO1)
#define RO3 2
#define CH_RO3 CH_S(RO3)
#define KD 300
#define KB 150
#define CH_KD CH_S(KD)
#define CH_KB CH_S(KB)
#define RSL 2
#define CH_S2(x) #x
#define CH_S(x) CH_S2(x)
#define CH_TOKCHK "s_cbranch_vccz LG3_%=\n\t"
#define CH_SG0 CH_S(SG0)
#define CH_ARMD "%[armd]"
#define CH_ARMS "%[arms]"
#define CH_SLOT "688128"
#define CH_SKIP "32768"
#define CH_DLL1 CH_S(DLL1)
#define CH_DLL2 CH_S(DLL2)
#define CH_DLF CH_S(DLF)
#define CH_B0 CH_S(SCH_B0)
#define CH_PEN CH_S(SCH_PEN)
#define CH_RSL CH_S(RSL)
#define CH_DLX CH_S(DLX)
#define CH_LATE CH_S(LATE)
#define CH_DL1L "%[dl1l]"
#define CH_TKL CH_S(TKL)
#define CH_SCHW "656384"
#define CH_CAP "%[cap]"
#define CH_CTAB "%[ctab]"
#define CH_RING "%[ring]"
#define CH_TRM "%[trm]"
#define FDL 6
#define CH_FDL CH_S(FDL)
#define CH_ASM \
  "s_mov_b32 s84, " CH_CAP "\n\t" \
  "s_mov_b32 s83, " CH_CAP "\n\t" \
  "s_mov_b32 s68, 0\n\t" \
  "s_mov_b32 s74, 0\n\t" \
  "s_mov_b32 s76, 0\n\t" \
  "s_mov_b32 s77, 0\n\t" \
  "s_mov_b32 s78, 0\n\t" \
  "s_mov_b32 s79, 0\n\t" \
  "s_mov_b32 s56, 0xcccccccc\n\t" \
  "s_mov_b32 s57, 0xcccccccc\n\t" \
  "s_mov_b32 s58, 0x33333333\n\t" \
  "s_mov_b32 s59, 0x33333333\n\t" \
  "s_mov_b32 s24, 0x01010101\n\t" \
  "s_mov_b32 s25, 0\n\t" \
  "s_mov_b32 s88, 0x3f803f80\n\t" \
  "s_mov_b32 s89, 0x3f803f80\n\t" \
  "s_mov_b32 s66, s65\n\t" \
  "s_mov_b32 s47, s65\n\t" \
  "s_mov_b32 s72, 0\n\t" \
  "s_mov_b32 s73, 0\n\t" \
  "s_mov_b32 s67, 0\n\t" \
  "s_mul_i32 s60, s40, 36864\n\t" \
  "s_add_u32 s60, s60, 16384\n\t" \
  "v_add_u32 v70, s60, v69\n\t" \
  "v_bfe_i32 v68, v69, 5, 1\n\t" \
  "v_and_b32 v68, 0xaaaaaaaa, v68\n\t" \
  "v_or_b32 v68, 0x44444444, v68\n\t" \
  "v_and_b32 v73, 32, v69\n\t" \
  "v_lshl_add_u32 v73, v73, 5, v69\n\t" \
  "LLOAD_%=:\n\t" \
  "v_lshrrev_b32 v74, 2, v69\n\t" \
  "s_mov_b32 s60, " CH_CTAB "\n\t" \
  "buffer_load_dword v72, v74, s[28:31], s60 offen offset:88 sc1\n\t" \
  "buffer_load_dword v74, v74, s[28:31], s60 offen sc1\n\t" \
  "v_lshrrev_b32 v71, 8, v69\n\t" \
  "v_lshlrev_b32 v1, 6, v71\n\t" \
  "v_and_b32 v2, 0xf0, v69\n\t" \
  "v_xor_b32 v1, v1, v2\n\t" \
  "v_lshl_add_u32 v71, v71, 13, v1\n\t" \
  "s_lshl_b32 s60, s40, 17\n\t" \
  "v_add_u32 v71, s60, v71\n\t" \
  "v_lshrrev_b32 v1, 7, v69\n\t" \
  "v_and_b32 v2, 0x70, v69\n\t" \
  "s_movk_i32 s60, 1152\n\t" \
  "v_mad_u32_u24 v75, v1, s60, v2\n\t" \
  "s_lshl_b32 s54, s40, 12\n\t" \
  "s_lshl_b32 s55, s40, 7\n\t" \
  "s_mul_i32 s75, s40, 36864\n\t" \
  "s_add_u32 s75, s75, 1024\n\t" \
  "s_mov_b32 s62, 0x05040100\n\t" \
  "s_mov_b32 s63, 0x07060302\n\t" \
  "s_barrier\n\t" \
  "s_xor_b32 s61, s40, 0\n\t" \
  "s_mul_i32 s61, s61, 36864\n\t" \
  "s_add_u32 s61, s61, s55\n\t" \
  "s_add_u32 m0, s54, 0\n\t" \
  "s_add_u32 s60, s61, 0\n\t" \
  "buffer_load_dwordx4 v75, %[Wo], s60 offen lds\n\t" \
  "s_add_u32 m0, s54, 1024\n\t" \
  "s_add_u32 s60, s61, 9216\n\t" \
  "buffer_load_dwordx4 v75, %[Wo], s60 offen lds\n\t" \
  "s_add_u32 m0, s54, 2048\n\t" \
  "s_add_u32 s60, s61, 18432\n\t" \
  "buffer_load_dwordx4 v75, %[Wo], s60 offen lds\n\t" \
  "s_add_u32 m0, s54, 3072\n\t" \
  "s_add_u32 s60, s61, 27648\n\t" \
  "buffer_load_dwordx4 v75, %[Wo], s60 offen lds\n\t" \
  "s_add_u32 m0, s54, 16384\n\t" \
  "s_mov_b32 s60, 0\n\t" \
  "buffer_load_dwordx4 v71, %[Wq], s60 offen lds\n\t" \
  "s_add_u32 m0, s54, 17408\n\t" \
  "s_mov_b32 s60, 32768\n\t" \
  "buffer_load_dwordx4 v71, %[Wq], s60 offen lds\n\t" \
  "s_add_u32 m0, s54, 18432\n\t" \
  "s_mov_b32 s60, 65536\n\t" \
  "buffer_load_dwordx4 v71, %[Wq], s60 offen lds\n\t" \
  "s_add_u32 m0, s54, 19456\n\t" \
  "s_mov_b32 s60, 98304\n\t" \
  "buffer_load_dwordx4 v71, %[Wq], s60 offen lds\n\t" \
  "s_add_u32 m0, s54, 53248\n\t" \
  "s_mov_b32 s60, 0\n\t" \
  "buffer_load_dwordx4 v71, %[Wk], s60 offen lds\n\t" \
  "s_add_u32 m0, s54, 54272\n\t" \
  "s_mov_b32 s60, 32768\n\t" \
  "buffer_load_dwordx4 v71, %[Wk], s60 offen lds\n\t" \
  "s_add_u32 m0, s54, 55296\n\t" \
  "s_mov_b32 s60, 65536\n\t" \
  "buffer_load_dwordx4 v71, %[Wk], s60 offen lds\n\t" \
  "s_add_u32 m0, s54, 56320\n\t" \
  "s_mov_b32 s60, 98304\n\t" \
  "buffer_load_dwordx4 v71, %[Wk], s60 offen lds\n\t" \
  "s_xor_b32 s61, s40, 0\n\t" \
  "s_mul_i32 s61, s61, 36864\n\t" \
  "s_add_u32 s61, s61, s55\n\t" \
  "s_add_u32 m0, s54, 90112\n\t" \
  "s_add_u32 s60, s61, 512\n\t" \
  "buffer_load_dwordx4 v75, %[Wo], s60 offen lds\n\t" \
  "s_add_u32 m0, s54, 91136\n\t" \
  "s_add_u32 s60, s61, 9728\n\t" \
  "buffer_load_dwordx4 v75, %[Wo], s60 offen lds\n\t" \
  "s_add_u32 m0, s54, 92160\n\t" \
  "s_add_u32 s60, s61, 18944\n\t" \
  "buffer_load_dwordx4 v75, %[Wo], s60 offen lds\n\t" \
  "s_add_u32 m0, s54, 93184\n\t" \
  "s_add_u32 s60, s61, 28160\n\t" \
  "buffer_load_dwordx4 v75, %[Wo], s60 offen lds\n\t" \
  "s_add_u32 m0, s54, 126976\n\t" \
  "s_mov_b32 s60, 0\n\t" \
  "buffer_load_dwordx4 v71, %[Wv], s60 offen lds\n\t" \
  "s_add_u32 m0, s54, 128000\n\t" \
  "s_mov_b32 s60, 32768\n\t" \
  "buffer_load_dwordx4 v71, %[Wv], s60 offen lds\n\t" \
  "s_add_u32 m0, s54, 129024\n\t" \
  "s_mov_b32 s60, 65536\n\t" \
  "buffer_load_dwordx4 v71, %[Wv], s60 offen lds\n\t" \
  "s_add_u32 m0, s54, 130048\n\t" \
  "s_mov_b32 s60, 98304\n\t" \
  "buffer_load_dwordx4 v71, %[Wv], s60 offen lds\n\t" \
  "v_and_b32 v1, 0x300, v69\n\t" \
  "v_lshlrev_b32 v65, 3, v1\n\t" \
  "v_and_b32 v1, 0xc0, v69\n\t" \
  "v_lshl_add_u32 v65, v1, 2, v65\n\t" \
  "s_lshl_b32 s60, s40, 6\n\t" \
  "v_xor_b32 v1, s60, v1\n\t" \
  "v_and_b32 v2, 48, v69\n\t" \
  "v_add3_u32 v65, v65, v1, v2\n\t" \
  "v_lshrrev_b32 v2, 1, v2\n\t" \
  "v_sub_u32 v66, v65, v2\n\t" \
  "v_bfe_u32 v0, v69, 8, 1\n\t" \
  "v_bfe_u32 v1, v69, 9, 1\n\t" \
  "v_lshlrev_b32 v0, 2, v0\n\t" \
  "v_add_u32 v0, v0, v1\n\t" \
  "v_lshlrev_b32 v0, 9, v0\n\t" \
  "v_lshrrev_b32 v1, 1, v69\n\t" \
  "v_and_b32 v1, 0x60, v1\n\t" \
  "v_and_b32 v2, 16, v69\n\t" \
  "v_bfe_u32 v3, v69, 5, 1\n\t" \
  "v_add3_u32 v64, v0, v1, v2\n\t" \
  "v_lshl_add_u32 v64, v3, 2, v64\n\t" \
  "v_add_u32 v64, s54, v64\n\t" \
  "v_lshl_add_u32 v67, v3, 3, v0\n\t" \
  "v_bfe_u32 v1, v69, 7, 1\n\t" \
  "v_lshl_add_u32 v67, v1, 2, v67\n\t" \
  "v_bfe_u32 v1, v69, 6, 1\n\t" \
  "v_lshl_add_u32 v67, v1, 4, v67\n\t" \
  "v_bfe_u32 v1, v69, 4, 1\n\t" \
  "v_lshl_add_u32 v67, v1, 1, v67\n\t" \
  "s_lshl_b32 s60, s40, 5\n\t" \
  "v_add_u32 v67, s60, v67\n\t" \
  "v_add_u32 v78, 0, v64\n\t" \
  "v_add_u32 v79, 1024, v64\n\t" \
  "s_waitcnt vmcnt(16) lgkmcnt(0)\n\t" \
  "s_barrier\n\t" \
  "ds_read2_b32 v[80:81], v78 offset0:0 offset1:2\n\t" \
  "ds_read2_b32 v[82:83], v78 offset0:32 offset1:34\n\t" \
  "ds_read2_b32 v[84:85], v78 offset0:64 offset1:66\n\t" \
  "ds_read2_b32 v[86:87], v78 offset0:96 offset1:98\n\t" \
  "ds_read2_b32 v[88:89], v79 offset0:0 offset1:2\n\t" \
  "ds_read2_b32 v[90:91], v79 offset0:32 offset1:34\n\t" \
  "ds_read2_b32 v[92:93], v79 offset0:64 offset1:66\n\t" \
  "ds_read2_b32 v[94:95], v79 offset0:96 offset1:98\n\t" \
  "s_waitcnt lgkmcnt(6)\n\t" \
  "v_perm_b32 v112, v82, v80, s62\n\t" \
  "v_perm_b32 v113, v82, v80, s63\n\t" \
  "s_waitcnt lgkmcnt(4)\n\t" \
  "v_perm_b32 v114, v86, v84, s62\n\t" \
  "v_perm_b32 v115, v86, v84, s63\n\t" \
  "s_waitcnt lgkmcnt(2)\n\t" \
  "v_perm_b32 v116, v90, v88, s62\n\t" \
  "v_perm_b32 v117, v90, v88, s63\n\t" \
  "s_waitcnt lgkmcnt(0)\n\t" \
  "v_perm_b32 v118, v94, v92, s62\n\t" \
  "v_perm_b32 v119, v94, v92, s63\n\t" \
  "ds_write_b128 v70, v[112:115] offset:20480\n\t" \
  "ds_write_b128 v70, v[116:119] offset:21504\n\t" \
  "v_perm_b32 v120, v83, v81, s62\n\t" \
  "v_perm_b32 v121, v83, v81, s63\n\t" \
  "v_perm_b32 v122, v87, v85, s62\n\t" \
  "v_perm_b32 v123, v87, v85, s63\n\t" \
  "v_perm_b32 v124, v91, v89, s62\n\t" \
  "v_perm_b32 v125, v91, v89, s63\n\t" \
  "v_perm_b32 v126, v95, v93, s62\n\t" \
  "v_perm_b32 v127, v95, v93, s63\n\t" \
  "ds_write_b128 v70, v[120:123] offset:16384\n\t" \
  "ds_write_b128 v70, v[124:127] offset:17408\n\t" \
  "v_add_u32 v78, 16384, v65\n\t" \
  "s_waitcnt vmcnt(12) lgkmcnt(0)\n\t" \
  "s_barrier\n\t" \
  "ds_read_b64_tr_b16 v[0:1], v78 offset:0\n\t" \
  "ds_read_b64_tr_b16 v[2:3], v78 offset:1024\n\t" \
  "ds_read_b64_tr_b16 v[4:5], v78 offset:8192\n\t" \
  "ds_read_b64_tr_b16 v[6:7], v78 offset:9216\n\t" \
  "ds_read_b64_tr_b16 v[8:9], v78 offset:8\n\t" \
  "ds_read_b64_tr_b16 v[10:11], v78 offset:1032\n\t" \
  "ds_read_b64_tr_b16 v[12:13], v78 offset:8200\n\t" \
  "ds_read_b64_tr_b16 v[14:15], v78 offset:9224\n\t" \
  "s_add_u32 m0, s54, 0\n\t" \
  "s_mov_b32 s60, 524288\n\t" \
  "buffer_load_dwordx4 v71, %[Wq], s60 offen lds\n\t" \
  "s_add_u32 m0, s54, 1024\n\t" \
  "s_mov_b32 s60, 557056\n\t" \
  "buffer_load_dwordx4 v71, %[Wq], s60 offen lds\n\t" \
  "s_add_u32 m0, s54, 2048\n\t" \
  "s_mov_b32 s60, 589824\n\t" \
  "buffer_load_dwordx4 v71, %[Wq], s60 offen lds\n\t" \
  "s_add_u32 m0, s54, 3072\n\t" \
  "s_mov_b32 s60, 622592\n\t" \
  "buffer_load_dwordx4 v71, %[Wq], s60 offen lds\n\t" \
  "v_add_u32 v78, 53248, v65\n\t" \
  "s_waitcnt vmcnt(12) lgkmcnt(0)\n\t" \
  "s_barrier\n\t" \
  "ds_read_b64_tr_b16 a[0:1], v78 offset:0\n\t" \
  "ds_read_b64_tr_b16 a[2:3], v78 offset:1024\n\t" \
  "ds_read_b64_tr_b16 a[4:5], v78 offset:8192\n\t" \
  "ds_read_b64_tr_b16 a[6:7], v78 offset:9216\n\t" \
  "ds_read_b64_tr_b16 a[8:9], v78 offset:8\n\t" \
  "ds_read_b64_tr_b16 a[10:11], v78 offset:1032\n\t" \
  "ds_read_b64_tr_b16 a[12:13], v78 offset:8200\n\t" \
  "ds_read_b64_tr_b16 a[14:15], v78 offset:9224\n\t" \
  "s_xor_b32 s61, s40, 1\n\t" \
  "s_mul_i32 s61, s61, 36864\n\t" \
  "s_add_u32 s61, s61, s55\n\t" \
  "s_add_u32 m0, s54, 16384\n\t" \
  "s_add_u32 s60, s61, 0\n\t" \
  "buffer_load_dwordx4 v75, %[Wo], s60 offen lds\n\t" \
  "s_add_u32 m0, s54, 17408\n\t" \
  "s_add_u32 s60, s61, 9216\n\t" \
  "buffer_load_dwordx4 v75, %[Wo], s60 offen lds\n\t" \
  "s_add_u32 m0, s54, 18432\n\t" \
  "s_add_u32 s60, s61, 18432\n\t" \
  "buffer_load_dwordx4 v75, %[Wo], s60 offen lds\n\t" \
  "s_add_u32 m0, s54, 19456\n\t" \
  "s_add_u32 s60, s61, 27648\n\t" \
  "buffer_load_dwordx4 v75, %[Wo], s60 offen lds\n\t" \
  "v_add_u32 v78, 90112, v64\n\t" \
  "v_add_u32 v79, 91136, v64\n\t" \
  "s_waitcnt vmcnt(12) lgkmcnt(0)\n\t" \
  "s_barrier\n\t" \
  "ds_read2_b32 v[80:81], v78 offset0:0 offset1:2\n\t" \
  "ds_read2_b32 v[82:83], v78 offset0:32 offset1:34\n\t" \
  "ds_read2_b32 v[84:85], v78 offset0:64 offset1:66\n\t" \
  "ds_read2_b32 v[86:87], v78 offset0:96 offset1:98\n\t" \
  "ds_read2_b32 v[88:89], v79 offset0:0 offset1:2\n\t" \
  "ds_read2_b32 v[90:91], v79 offset0:32 offset1:34\n\t" \
  "ds_read2_b32 v[92:93], v79 offset0:64 offset1:66\n\t" \
  "ds_read2_b32 v[94:95], v79 offset0:96 offset1:98\n\t" \
  "s_add_u32 m0, s54, 53248\n\t" \
  "s_mov_b32 s60, 524288\n\t" \
  "buffer_load_dwordx4 v71, %[Wk], s60 offen lds\n\t" \
  "s_add_u32 m0, s54, 54272\n\t" \
  "s_mov_b32 s60, 557056\n\t" \
  "buffer_load_dwordx4 v71, %[Wk], s60 offen lds\n\t" \
  "s_add_u32 m0, s54, 55296\n\t" \
  "s_mov_b32 s60, 589824\n\t" \
  "buffer_load_dwordx4 v71, %[Wk], s60 offen lds\n\t" \
  "s_add_u32 m0, s54, 56320\n\t" \
  "s_mov_b32 s60, 622592\n\t" \
  "buffer_load_dwordx4 v71, %[Wk], s60 offen lds\n\t" \
  "s_waitcnt lgkmcnt(6)\n\t" \
  "v_perm_b32 v112, v82, v80, s62\n\t" \
  "v_perm_b32 v113, v82, v80, s63\n\t" \
  "s_waitcnt lgkmcnt(4)\n\t" \
  "v_perm_b32 v114, v86, v84, s62\n\t" \
  "v_perm_b32 v115, v86, v84, s63\n\t" \
  "s_waitcnt lgkmcnt(2)\n\t" \
  "v_perm_b32 v116, v90, v88, s62\n\t" \
  "v_perm_b32 v117, v90, v88, s63\n\t" \
  "s_waitcnt lgkmcnt(0)\n\t" \
  "v_perm_b32 v118, v94, v92, s62\n\t" \
  "v_perm_b32 v119, v94, v92, s63\n\t" \
  "ds_write_b128 v70, v[112:115] offset:22528\n\t" \
  "ds_write_b128 v70, v[116:119] offset:23552\n\t" \
  "v_perm_b32 v120, v83, v81, s62\n\t" \
  "v_perm_b32 v121, v83, v81, s63\n\t" \
  "v_perm_b32 v122, v87, v85, s62\n\t" \
  "v_perm_b32 v123, v87, v85, s63\n\t" \
  "v_perm_b32 v124, v91, v89, s62\n\t" \
  "v_perm_b32 v125, v91, v89, s63\n\t" \
  "v_perm_b32 v126, v95, v93, s62\n\t" \
  "v_perm_b32 v127, v95, v93, s63\n\t" \
  "ds_write_b128 v70, v[120:123] offset:18432\n\t" \
  "ds_write_b128 v70, v[124:127] offset:19456\n\t" \
  "v_add_u32 v78, 126976, v66\n\t" \
  "s_waitcnt vmcnt(12) lgkmcnt(0)\n\t" \
  "s_barrier\n\t" \
  "ds_read_b64_tr_b16 a[16:17], v78 offset:0\n\t" \
  "ds_read_b64_tr_b16 a[18:19], v78 offset:1024\n\t" \
  "ds_read_b64_tr_b16 a[20:21], v78 offset:8192\n\t" \
  "ds_read_b64_tr_b16 a[22:23], v78 offset:9216\n\t" \
  "ds_read_b64_tr_b16 a[24:25], v78 offset:32\n\t" \
  "ds_read_b64_tr_b16 a[26:27], v78 offset:1056\n\t" \
  "ds_read_b64_tr_b16 a[28:29], v78 offset:8224\n\t" \
  "ds_read_b64_tr_b16 a[30:31], v78 offset:9248\n\t" \
  "s_add_u32 m0, s54, 90112\n\t" \
  "s_mov_b32 s60, 524288\n\t" \
  "buffer_load_dwordx4 v71, %[Wv], s60 offen lds\n\t" \
  "s_add_u32 m0, s54, 91136\n\t" \
  "s_mov_b32 s60, 557056\n\t" \
  "buffer_load_dwordx4 v71, %[Wv], s60 offen lds\n\t" \
  "s_add_u32 m0, s54, 92160\n\t" \
  "s_mov_b32 s60, 589824\n\t" \
  "buffer_load_dwordx4 v71, %[Wv], s60 offen lds\n\t" \
  "s_add_u32 m0, s54, 93184\n\t" \
  "s_mov_b32 s60, 622592\n\t" \
  "buffer_load_dwordx4 v71, %[Wv], s60 offen lds\n\t" \
  "v_add_u32 v78, 0, v65\n\t" \
  "s_waitcnt vmcnt(12) lgkmcnt(0)\n\t" \
  "s_barrier\n\t" \
  "ds_read_b64_tr_b16 a[32:33], v78 offset:0\n\t" \
  "ds_read_b64_tr_b16 a[34:35], v78 offset:1024\n\t" \
  "ds_read_b64_tr_b16 a[36:37], v78 offset:8192\n\t" \
  "ds_read_b64_tr_b16 a[38:39], v78 offset:9216\n\t" \
  "ds_read_b64_tr_b16 a[40:41], v78 offset:8\n\t" \
  "ds_read_b64_tr_b16 a[42:43], v78 offset:1032\n\t" \
  "ds_read_b64_tr_b16 a[44:45], v78 offset:8200\n\t" \
  "ds_read_b64_tr_b16 a[46:47], v78 offset:9224\n\t" \
  "s_xor_b32 s61, s40, 1\n\t" \
  "s_mul_i32 s61, s61, 36864\n\t" \
  "s_add_u32 s61, s61, s55\n\t" \
  "s_add_u32 m0, s54, 126976\n\t" \
  "s_add_u32 s60, s61, 512\n\t" \
  "buffer_load_dwordx4 v75, %[Wo], s60 offen lds\n\t" \
  "s_add_u32 m0, s54, 128000\n\t" \
  "s_add_u32 s60, s61, 9728\n\t" \
  "buffer_load_dwordx4 v75, %[Wo], s60 offen lds\n\t" \
  "s_add_u32 m0, s54, 129024\n\t" \
  "s_add_u32 s60, s61, 18944\n\t" \
  "buffer_load_dwordx4 v75, %[Wo], s60 offen lds\n\t" \
  "s_add_u32 m0, s54, 130048\n\t" \
  "s_add_u32 s60, s61, 28160\n\t" \
  "buffer_load_dwordx4 v75, %[Wo], s60 offen lds\n\t" \
  "v_add_u32 v78, 16384, v64\n\t" \
  "v_add_u32 v79, 17408, v64\n\t" \
  "s_waitcnt vmcnt(12) lgkmcnt(0)\n\t" \
  "s_barrier\n\t" \
  "ds_read2_b32 v[80:81], v78 offset0:0 offset1:2\n\t" \
  "ds_read2_b32 v[82:83], v78 offset0:32 offset1:34\n\t" \
  "ds_read2_b32 v[84:85], v78 offset0:64 offset1:66\n\t" \
  "ds_read2_b32 v[86:87], v78 offset0:96 offset1:98\n\t" \
  "ds_read2_b32 v[88:89], v79 offset0:0 offset1:2\n\t" \
  "ds_read2_b32 v[90:91], v79 offset0:32 offset1:34\n\t" \
  "ds_read2_b32 v[92:93], v79 offset0:64 offset1:66\n\t" \
  "ds_read2_b32 v[94:95], v79 offset0:96 offset1:98\n\t" \
  "s_add_u32 m0, s54, 0\n\t" \
  "s_mov_b32 s60, 1048576\n\t" \
  "buffer_load_dwordx4 v71, %[Wq], s60 offen lds\n\t" \
  "s_add_u32 m0, s54, 1024\n\t" \
  "s_mov_b32 s60, 1081344\n\t" \
  "buffer_load_dwordx4 v71, %[Wq], s60 offen lds\n\t" \
  "s_add_u32 m0, s54, 2048\n\t" \
  "s_mov_b32 s60, 1114112\n\t" \
  "buffer_load_dwordx4 v71, %[Wq], s60 offen lds\n\t" \
  "s_add_u32 m0, s54, 3072\n\t" \
  "s_mov_b32 s60, 1146880\n\t" \
  "buffer_load_dwordx4 v71, %[Wq], s60 offen lds\n\t" \
  "s_waitcnt lgkmcnt(6)\n\t" \
  "v_perm_b32 v112, v82, v80, s62\n\t" \
  "v_perm_b32 v113, v82, v80, s63\n\t" \
  "s_waitcnt lgkmcnt(4)\n\t" \
  "v_perm_b32 v114, v86, v84, s62\n\t" \
  "v_perm_b32 v115, v86, v84, s63\n\t" \
  "s_waitcnt lgkmcnt(2)\n\t" \
  "v_perm_b32 v116, v90, v88, s62\n\t" \
  "v_perm_b32 v117, v90, v88, s63\n\t" \
  "s_waitcnt lgkmcnt(0)\n\t" \
  "v_perm_b32 v118, v94, v92, s62\n\t" \
  "v_perm_b32 v119, v94, v92, s63\n\t" \
  "ds_write_b128 v70, v[112:115] offset:24576\n\t" \
  "ds_write_b128 v70, v[116:119] offset:25600\n\t" \
  "v_perm_b32 v200, v83, v81, s62\n\t" \
  "v_perm_b32 v201, v83, v81, s63\n\t" \
  "v_perm_b32 v202, v87, v85, s62\n\t" \
  "v_perm_b32 v203, v87, v85, s63\n\t" \
  "v_perm_b32 v204, v91, v89, s62\n\t" \
  "v_perm_b32 v205, v91, v89, s63\n\t" \
  "v_perm_b32 v206, v95, v93, s62\n\t" \
  "v_perm_b32 v207, v95, v93, s63\n\t" \
  "v_add_u32 v78, 53248, v65\n\t" \
  "s_waitcnt vmcnt(12) lgkmcnt(0)\n\t" \
  "s_barrier\n\t" \
  "ds_read_b64_tr_b16 a[48:49], v78 offset:0\n\t" \
  "ds_read_b64_tr_b16 a[50:51], v78 offset:1024\n\t" \
  "ds_read_b64_tr_b16 a[52:53], v78 offset:8192\n\t" \
  "ds_read_b64_tr_b16 a[54:55], v78 offset:9216\n\t" \
  "ds_read_b64_tr_b16 a[56:57], v78 offset:8\n\t" \
  "ds_read_b64_tr_b16 a[58:59], v78 offset:1032\n\t" \
  "ds_read_b64_tr_b16 a[60:61], v78 offset:8200\n\t" \
  "ds_read_b64_tr_b16 a[62:63], v78 offset:9224\n\t" \
  "s_add_u32 m0, s54, 16384\n\t" \
  "s_mov_b32 s60, 1048576\n\t" \
  "buffer_load_dwordx4 v71, %[Wk], s60 offen lds\n\t" \
  "s_add_u32 m0, s54, 17408\n\t" \
  "s_mov_b32 s60, 1081344\n\t" \
  "buffer_load_dwordx4 v71, %[Wk], s60 offen lds\n\t" \
  "s_add_u32 m0, s54, 18432\n\t" \
  "s_mov_b32 s60, 1114112\n\t" \
  "buffer_load_dwordx4 v71, %[Wk], s60 offen lds\n\t" \
  "s_add_u32 m0, s54, 19456\n\t" \
  "s_mov_b32 s60, 1146880\n\t" \
  "buffer_load_dwordx4 v71, %[Wk], s60 offen lds\n\t" \
  "v_add_u32 v78, 90112, v66\n\t" \
  "s_waitcnt vmcnt(12) lgkmcnt(0)\n\t" \
  "s_barrier\n\t" \
  "ds_read_b64_tr_b16 a[64:65], v78 offset:0\n\t" \
  "ds_read_b64_tr_b16 a[66:67], v78 offset:1024\n\t" \
  "ds_read_b64_tr_b16 a[68:69], v78 offset:8192\n\t" \
  "ds_read_b64_tr_b16 a[70:71], v78 offset:9216\n\t" \
  "ds_read_b64_tr_b16 a[72:73], v78 offset:32\n\t" \
  "ds_read_b64_tr_b16 a[74:75], v78 offset:1056\n\t" \
  "ds_read_b64_tr_b16 a[76:77], v78 offset:8224\n\t" \
  "ds_read_b64_tr_b16 a[78:79], v78 offset:9248\n\t" \
  "s_xor_b32 s61, s40, 2\n\t" \
  "s_mul_i32 s61, s61, 36864\n\t" \
  "s_add_u32 s61, s61, s55\n\t" \
  "s_add_u32 m0, s54, 53248\n\t" \
  "s_add_u32 s60, s61, 0\n\t" \
  "buffer_load_dwordx4 v75, %[Wo], s60 offen lds\n\t" \
  "s_add_u32 m0, s54, 54272\n\t" \
  "s_add_u32 s60, s61, 9216\n\t" \
  "buffer_load_dwordx4 v75, %[Wo], s60 offen lds\n\t" \
  "s_add_u32 m0, s54, 55296\n\t" \
  "s_add_u32 s60, s61, 18432\n\t" \
  "buffer_load_dwordx4 v75, %[Wo], s60 offen lds\n\t" \
  "s_add_u32 m0, s54, 56320\n\t" \
  "s_add_u32 s60, s61, 27648\n\t" \
  "buffer_load_dwordx4 v75, %[Wo], s60 offen lds\n\t" \
  "v_add_u32 v78, 126976, v64\n\t" \
  "v_add_u32 v79, 128000, v64\n\t" \
  "s_waitcnt vmcnt(12) lgkmcnt(0)\n\t" \
  "s_barrier\n\t" \
  "ds_read2_b32 v[80:81], v78 offset0:0 offset1:2\n\t" \
  "ds_read2_b32 v[82:83], v78 offset0:32 offset1:34\n\t" \
  "ds_read2_b32 v[84:85], v78 offset0:64 offset1:66\n\t" \
  "ds_read2_b32 v[86:87], v78 offset0:96 offset1:98\n\t" \
  "ds_read2_b32 v[88:89], v79 offset0:0 offset1:2\n\t" \
  "ds_read2_b32 v[90:91], v79 offset0:32 offset1:34\n\t" \
  "ds_read2_b32 v[92:93], v79 offset0:64 offset1:66\n\t" \
  "ds_read2_b32 v[94:95], v79 offset0:96 offset1:98\n\t" \
  "s_add_u32 m0, s54, 90112\n\t" \
  "s_mov_b32 s60, 1048576\n\t" \
  "buffer_load_dwordx4 v71, %[Wv], s60 offen lds\n\t" \
  "s_add_u32 m0, s54, 91136\n\t" \
  "s_mov_b32 s60, 1081344\n\t" \
  "buffer_load_dwordx4 v71, %[Wv], s60 offen lds\n\t" \
  "s_add_u32 m0, s54, 92160\n\t" \
  "s_mov_b32 s60, 1114112\n\t" \
  "buffer_load_dwordx4 v71, %[Wv], s60 offen lds\n\t" \
  "s_add_u32 m0, s54, 93184\n\t" \
  "s_mov_b32 s60, 1146880\n\t" \
  "buffer_load_dwordx4 v71, %[Wv], s60 offen lds\n\t" \
  "s_waitcnt lgkmcnt(6)\n\t" \
  "v_perm_b32 v112, v82, v80, s62\n\t" \
  "v_perm_b32 v113, v82, v80, s63\n\t" \
  "s_waitcnt lgkmcnt(4)\n\t" \
  "v_perm_b32 v114, v86, v84, s62\n\t" \
  "v_perm_b32 v115, v86, v84, s63\n\t" \
  "s_waitcnt lgkmcnt(2)\n\t" \
  "v_perm_b32 v116, v90, v88, s62\n\t" \
  "v_perm_b32 v117, v90, v88, s63\n\t" \
  "s_waitcnt lgkmcnt(0)\n\t" \
  "v_perm_b32 v118, v94, v92, s62\n\t" \
  "v_perm_b32 v119, v94, v92, s63\n\t" \
  "ds_write_b128 v70, v[112:115] offset:26624\n\t" \
  "ds_write_b128 v70, v[116:119] offset:27648\n\t" \
  "v_perm_b32 v232, v83, v81, s62\n\t" \
  "v_perm_b32 v233, v83, v81, s63\n\t" \
  "v_perm_b32 v234, v87, v85, s62\n\t" \
  "v_perm_b32 v235, v87, v85, s63\n\t" \
  "v_perm_b32 v236, v91, v89, s62\n\t" \
  "v_perm_b32 v237, v91, v89, s63\n\t" \
  "v_perm_b32 v238, v95, v93, s62\n\t" \
  "v_perm_b32 v239, v95, v93, s63\n\t" \
  "v_add_u32 v78, 0, v65\n\t" \
  "s_waitcnt vmcnt(12) lgkmcnt(0)\n\t" \
  "s_barrier\n\t" \
  "ds_read_b64_tr_b16 a[80:81], v78 offset:0\n\t" \
  "ds_read_b64_tr_b16 a[82:83], v78 offset:1024\n\t" \
  "ds_read_b64_tr_b16 a[84:85], v78 offset:8192\n\t" \
  "ds_read_b64_tr_b16 a[86:87], v78 offset:9216\n\t" \
  "ds_read_b64_tr_b16 a[88:89], v78 offset:8\n\t" \
  "ds_read_b64_tr_b16 a[90:91], v78 offset:1032\n\t" \
  "ds_read_b64_tr_b16 a[92:93], v78 offset:8200\n\t" \
  "ds_read_b64_tr_b16 a[94:95], v78 offset:9224\n\t" \
  "s_add_u32 m0, s54, 126976\n\t" \
  "s_mov_b32 s60, 1572864\n\t" \
  "buffer_load_dwordx4 v71, %[Wq], s60 offen lds\n\t" \
  "s_add_u32 m0, s54, 128000\n\t" \
  "s_mov_b32 s60, 1605632\n\t" \
  "buffer_load_dwordx4 v71, %[Wq], s60 offen lds\n\t" \
  "s_add_u32 m0, s54, 129024\n\t" \
  "s_mov_b32 s60, 1638400\n\t" \
  "buffer_load_dwordx4 v71, %[Wq], s60 offen lds\n\t" \
  "s_add_u32 m0, s54, 130048\n\t" \
  "s_mov_b32 s60, 1671168\n\t" \
  "buffer_load_dwordx4 v71, %[Wq], s60 offen lds\n\t" \
  "v_add_u32 v78, 16384, v65\n\t" \
  "s_waitcnt vmcnt(12) lgkmcnt(0)\n\t" \
  "s_barrier\n\t" \
  "ds_read_b64_tr_b16 v[32:33], v78 offset:0\n\t" \
  "ds_read_b64_tr_b16 v[34:35], v78 offset:1024\n\t" \
  "ds_read_b64_tr_b16 v[36:37], v78 offset:8192\n\t" \
  "ds_read_b64_tr_b16 v[38:39], v78 offset:9216\n\t" \
  "ds_read_b64_tr_b16 a[96:97], v78 offset:8\n\t" \
  "ds_read_b64_tr_b16 a[98:99], v78 offset:1032\n\t" \
  "ds_read_b64_tr_b16 a[100:101], v78 offset:8200\n\t" \
  "ds_read_b64_tr_b16 a[102:103], v78 offset:9224\n\t" \
  "s_xor_b32 s61, s40, 2\n\t" \
  "s_mul_i32 s61, s61, 36864\n\t" \
  "s_add_u32 s61, s61, s55\n\t" \
  "s_add_u32 m0, s54, 0\n\t" \
  "s_add_u32 s60, s61, 512\n\t" \
  "buffer_load_dwordx4 v75, %[Wo], s60 offen lds\n\t" \
  "s_add_u32 m0, s54, 1024\n\t" \
  "s_add_u32 s60, s61, 9728\n\t" \
  "buffer_load_dwordx4 v75, %[Wo], s60 offen lds\n\t" \
  "s_add_u32 m0, s54, 2048\n\t" \
  "s_add_u32 s60, s61, 18944\n\t" \
  "buffer_load_dwordx4 v75, %[Wo], s60 offen lds\n\t" \
  "s_add_u32 m0, s54, 3072\n\t" \
  "s_add_u32 s60, s61, 28160\n\t" \
  "buffer_load_dwordx4 v75, %[Wo], s60 offen lds\n\t" \
  "v_add_u32 v78, 53248, v64\n\t" \
  "v_add_u32 v79, 54272, v64\n\t" \
  "s_waitcnt vmcnt(12) lgkmcnt(0)\n\t" \
  "s_barrier\n\t" \
  "ds_read2_b32 v[80:81], v78 offset0:0 offset1:2\n\t" \
  "ds_read2_b32 v[82:83], v78 offset0:32 offset1:34\n\t" \
  "ds_read2_b32 v[84:85], v78 offset0:64 offset1:66\n\t" \
  "ds_read2_b32 v[86:87], v78 offset0:96 offset1:98\n\t" \
  "ds_read2_b32 v[88:89], v79 offset0:0 offset1:2\n\t" \
  "ds_read2_b32 v[90:91], v79 offset0:32 offset1:34\n\t" \
  "ds_read2_b32 v[92:93], v79 offset0:64 offset1:66\n\t" \
  "ds_read2_b32 v[94:95], v79 offset0:96 offset1:98\n\t" \
  "s_add_u32 m0, s54, 16384\n\t" \
  "s_mov_b32 s60, 1572864\n\t" \
  "buffer_load_dwordx4 v71, %[Wk], s60 offen lds\n\t" \
  "s_add_u32 m0, s54, 17408\n\t" \
  "s_mov_b32 s60, 1605632\n\t" \
  "buffer_load_dwordx4 v71, %[Wk], s60 offen lds\n\t" \
  "s_add_u32 m0, s54, 18432\n\t" \
  "s_mov_b32 s60, 1638400\n\t" \
  "buffer_load_dwordx4 v71, %[Wk], s60 offen lds\n\t" \
  "s_add_u32 m0, s54, 19456\n\t" \
  "s_mov_b32 s60, 1671168\n\t" \
  "buffer_load_dwordx4 v71, %[Wk], s60 offen lds\n\t" \
  "s_waitcnt lgkmcnt(6)\n\t" \
  "v_perm_b32 v112, v82, v80, s62\n\t" \
  "v_perm_b32 v113, v82, v80, s63\n\t" \
  "s_waitcnt lgkmcnt(4)\n\t" \
  "v_perm_b32 v114, v86, v84, s62\n\t" \
  "v_perm_b32 v115, v86, v84, s63\n\t" \
  "s_waitcnt lgkmcnt(2)\n\t" \
  "v_perm_b32 v116, v90, v88, s62\n\t" \
  "v_perm_b32 v117, v90, v88, s63\n\t" \
  "s_waitcnt lgkmcnt(0)\n\t" \
  "v_perm_b32 v118, v94, v92, s62\n\t" \
  "v_perm_b32 v119, v94, v92, s63\n\t" \
  "ds_write_b128 v70, v[112:115] offset:28672\n\t" \
  "ds_write_b128 v70, v[116:119] offset:29696\n\t" \
  "v_perm_b32 v208, v83, v81, s62\n\t" \
  "v_perm_b32 v209, v83, v81, s63\n\t" \
  "v_perm_b32 v210, v87, v85, s62\n\t" \
  "v_perm_b32 v211, v87, v85, s63\n\t" \
  "v_perm_b32 v212, v91, v89, s62\n\t" \
  "v_perm_b32 v213, v91, v89, s63\n\t" \
  "v_perm_b32 v214, v95, v93, s62\n\t" \
  "v_perm_b32 v215, v95, v93, s63\n\t" \
  "v_add_u32 v78, 90112, v66\n\t" \
  "s_waitcnt vmcnt(12) lgkmcnt(0)\n\t" \
  "s_barrier\n\t" \
  "ds_read_b64_tr_b16 a[104:105], v78 offset:0\n\t" \
  "ds_read_b64_tr_b16 a[106:107], v78 offset:1024\n\t" \
  "ds_read_b64_tr_b16 a[108:109], v78 offset:8192\n\t" \
  "ds_read_b64_tr_b16 a[110:111], v78 offset:9216\n\t" \
  "ds_read_b64_tr_b16 a[112:113], v78 offset:32\n\t" \
  "ds_read_b64_tr_b16 a[114:115], v78 offset:1056\n\t" \
  "ds_read_b64_tr_b16 a[116:117], v78 offset:8224\n\t" \
  "ds_read_b64_tr_b16 a[118:119], v78 offset:9248\n\t" \
  "s_add_u32 m0, s54, 53248\n\t" \
  "s_mov_b32 s60, 1572864\n\t" \
  "buffer_load_dwordx4 v71, %[Wv], s60 offen lds\n\t" \
  "s_add_u32 m0, s54, 54272\n\t" \
  "s_mov_b32 s60, 1605632\n\t" \
  "buffer_load_dwordx4 v71, %[Wv], s60 offen lds\n\t" \
  "s_add_u32 m0, s54, 55296\n\t" \
  "s_mov_b32 s60, 1638400\n\t" \
  "buffer_load_dwordx4 v71, %[Wv], s60 offen lds\n\t" \
  "s_add_u32 m0, s54, 56320\n\t" \
  "s_mov_b32 s60, 1671168\n\t" \
  "buffer_load_dwordx4 v71, %[Wv], s60 offen lds\n\t" \
  "v_add_u32 v78, 126976, v65\n\t" \
  "s_waitcnt vmcnt(12) lgkmcnt(0)\n\t" \
  "s_barrier\n\t" \
  "ds_read_b64_tr_b16 a[120:121], v78 offset:0\n\t" \
  "ds_read_b64_tr_b16 a[122:123], v78 offset:1024\n\t" \
  "ds_read_b64_tr_b16 a[124:125], v78 offset:8192\n\t" \
  "ds_read_b64_tr_b16 a[126:127], v78 offset:9216\n\t" \
  "ds_read_b64_tr_b16 v[16:17], v78 offset:8\n\t" \
  "ds_read_b64_tr_b16 v[18:19], v78 offset:1032\n\t" \
  "ds_read_b64_tr_b16 v[20:21], v78 offset:8200\n\t" \
  "ds_read_b64_tr_b16 v[22:23], v78 offset:9224\n\t" \
  "s_xor_b32 s61, s40, 3\n\t" \
  "s_mul_i32 s61, s61, 36864\n\t" \
  "s_add_u32 s61, s61, s55\n\t" \
  "s_add_u32 m0, s54, 90112\n\t" \
  "s_add_u32 s60, s61, 0\n\t" \
  "buffer_load_dwordx4 v75, %[Wo], s60 offen lds\n\t" \
  "s_add_u32 m0, s54, 91136\n\t" \
  "s_add_u32 s60, s61, 9216\n\t" \
  "buffer_load_dwordx4 v75, %[Wo], s60 offen lds\n\t" \
  "s_add_u32 m0, s54, 92160\n\t" \
  "s_add_u32 s60, s61, 18432\n\t" \
  "buffer_load_dwordx4 v75, %[Wo], s60 offen lds\n\t" \
  "s_add_u32 m0, s54, 93184\n\t" \
  "s_add_u32 s60, s61, 27648\n\t" \
  "buffer_load_dwordx4 v75, %[Wo], s60 offen lds\n\t" \
  "v_add_u32 v78, 0, v64\n\t" \
  "v_add_u32 v79, 1024, v64\n\t" \
  "s_waitcnt vmcnt(12) lgkmcnt(0)\n\t" \
  "s_barrier\n\t" \
  "ds_read2_b32 v[80:81], v78 offset0:0 offset1:2\n\t" \
  "ds_read2_b32 v[82:83], v78 offset0:32 offset1:34\n\t" \
  "ds_read2_b32 v[84:85], v78 offset0:64 offset1:66\n\t" \
  "ds_read2_b32 v[86:87], v78 offset0:96 offset1:98\n\t" \
  "ds_read2_b32 v[88:89], v79 offset0:0 offset1:2\n\t" \
  "ds_read2_b32 v[90:91], v79 offset0:32 offset1:34\n\t" \
  "ds_read2_b32 v[92:93], v79 offset0:64 offset1:66\n\t" \
  "ds_read2_b32 v[94:95], v79 offset0:96 offset1:98\n\t" \
  "s_add_u32 m0, s54, 126976\n\t" \
  "s_mov_b32 s60, 2097152\n\t" \
  "buffer_load_dwordx4 v71, %[Wq], s60 offen lds\n\t" \
  "s_add_u32 m0, s54, 128000\n\t" \
  "s_mov_b32 s60, 2129920\n\t" \
  "buffer_load_dwordx4 v71, %[Wq], s60 offen lds\n\t" \
  "s_add_u32 m0, s54, 129024\n\t" \
  "s_mov_b32 s60, 2162688\n\t" \
  "buffer_load_dwordx4 v71, %[Wq], s60 offen lds\n\t" \
  "s_add_u32 m0, s54, 130048\n\t" \
  "s_mov_b32 s60, 2195456\n\t" \
  "buffer_load_dwordx4 v71, %[Wq], s60 offen lds\n\t" \
  "s_waitcnt lgkmcnt(6)\n\t" \
  "v_perm_b32 v112, v82, v80, s62\n\t" \
  "v_perm_b32 v113, v82, v80, s63\n\t" \
  "s_waitcnt lgkmcnt(4)\n\t" \
  "v_perm_b32 v114, v86, v84, s62\n\t" \
  "v_perm_b32 v115, v86, v84, s63\n\t" \
  "s_waitcnt lgkmcnt(2)\n\t" \
  "v_perm_b32 v116, v90, v88, s62\n\t" \
  "v_perm_b32 v117, v90, v88, s63\n\t" \
  "s_waitcnt lgkmcnt(0)\n\t" \
  "v_perm_b32 v118, v94, v92, s62\n\t" \
  "v_perm_b32 v119, v94, v92, s63\n\t" \
  "ds_write_b128 v70, v[112:115] offset:30720\n\t" \
  "ds_write_b128 v70, v[116:119] offset:31744\n\t" \
  "v_perm_b32 v240, v83, v81, s62\n\t" \
  "v_perm_b32 v241, v83, v81, s63\n\t" \
  "v_perm_b32 v242, v87, v85, s62\n\t" \
  "v_perm_b32 v243, v87, v85, s63\n\t" \
  "v_perm_b32 v244, v91, v89, s62\n\t" \
  "v_perm_b32 v245, v91, v89, s63\n\t" \
  "v_perm_b32 v246, v95, v93, s62\n\t" \
  "v_perm_b32 v247, v95, v93, s63\n\t" \
  "v_add_u32 v78, 16384, v65\n\t" \
  "s_waitcnt vmcnt(12) lgkmcnt(0)\n\t" \
  "s_barrier\n\t" \
  "ds_read_b64_tr_b16 a[128:129], v78 offset:0\n\t" \
  "ds_read_b64_tr_b16 a[130:131], v78 offset:1024\n\t" \
  "ds_read_b64_tr_b16 a[132:133], v78 offset:8192\n\t" \
  "ds_read_b64_tr_b16 a[134:135], v78 offset:9216\n\t" \
  "ds_read_b64_tr_b16 a[136:137], v78 offset:8\n\t" \
  "ds_read_b64_tr_b16 a[138:139], v78 offset:1032\n\t" \
  "ds_read_b64_tr_b16 a[140:141], v78 offset:8200\n\t" \
  "ds_read_b64_tr_b16 a[142:143], v78 offset:9224\n\t" \
  "s_add_u32 m0, s54, 0\n\t" \
  "s_mov_b32 s60, 2097152\n\t" \
  "buffer_load_dwordx4 v71, %[Wk], s60 offen lds\n\t" \
  "s_add_u32 m0, s54, 1024\n\t" \
  "s_mov_b32 s60, 2129920\n\t" \
  "buffer_load_dwordx4 v71, %[Wk], s60 offen lds\n\t" \
  "s_add_u32 m0, s54, 2048\n\t" \
  "s_mov_b32 s60, 2162688\n\t" \
  "buffer_load_dwordx4 v71, %[Wk], s60 offen lds\n\t" \
  "s_add_u32 m0, s54, 3072\n\t" \
  "s_mov_b32 s60, 2195456\n\t" \
  "buffer_load_dwordx4 v71, %[Wk], s60 offen lds\n\t" \
  "v_add_u32 v78, 53248, v66\n\t" \
  "s_waitcnt vmcnt(12) lgkmcnt(0)\n\t" \
  "s_barrier\n\t" \
  "ds_read_b64_tr_b16 a[144:145], v78 offset:0\n\t" \
  "ds_read_b64_tr_b16 a[146:147], v78 offset:1024\n\t" \
  "ds_read_b64_tr_b16 a[148:149], v78 offset:8192\n\t" \
  "ds_read_b64_tr_b16 a[150:151], v78 offset:9216\n\t" \
  "ds_read_b64_tr_b16 a[152:153], v78 offset:32\n\t" \
  "ds_read_b64_tr_b16 a[154:155], v78 offset:1056\n\t" \
  "ds_read_b64_tr_b16 a[156:157], v78 offset:8224\n\t" \
  "ds_read_b64_tr_b16 a[158:159], v78 offset:9248\n\t" \
  "s_xor_b32 s61, s40, 3\n\t" \
  "s_mul_i32 s61, s61, 36864\n\t" \
  "s_add_u32 s61, s61, s55\n\t" \
  "s_add_u32 m0, s54, 16384\n\t" \
  "s_add_u32 s60, s61, 512\n\t" \
  "buffer_load_dwordx4 v75, %[Wo], s60 offen lds\n\t" \
  "s_add_u32 m0, s54, 17408\n\t" \
  "s_add_u32 s60, s61, 9728\n\t" \
  "buffer_load_dwordx4 v75, %[Wo], s60 offen lds\n\t" \
  "s_add_u32 m0, s54, 18432\n\t" \
  "s_add_u32 s60, s61, 18944\n\t" \
  "buffer_load_dwordx4 v75, %[Wo], s60 offen lds\n\t" \
  "s_add_u32 m0, s54, 19456\n\t" \
  "s_add_u32 s60, s61, 28160\n\t" \
  "buffer_load_dwordx4 v75, %[Wo], s60 offen lds\n\t" \
  "v_add_u32 v78, 90112, v64\n\t" \
  "v_add_u32 v79, 91136, v64\n\t" \
  "s_waitcnt vmcnt(12) lgkmcnt(0)\n\t" \
  "s_barrier\n\t" \
  "ds_read2_b32 v[80:81], v78 offset0:0 offset1:2\n\t" \
  "ds_read2_b32 v[82:83], v78 offset0:32 offset1:34\n\t" \
  "ds_read2_b32 v[84:85], v78 offset0:64 offset1:66\n\t" \
  "ds_read2_b32 v[86:87], v78 offset0:96 offset1:98\n\t" \
  "ds_read2_b32 v[88:89], v79 offset0:0 offset1:2\n\t" \
  "ds_read2_b32 v[90:91], v79 offset0:32 offset1:34\n\t" \
  "ds_read2_b32 v[92:93], v79 offset0:64 offset1:66\n\t" \
  "ds_read2_b32 v[94:95], v79 offset0:96 offset1:98\n\t" \
  "s_add_u32 m0, s54, 53248\n\t" \
  "s_mov_b32 s60, 2097152\n\t" \
  "buffer_load_dwordx4 v71, %[Wv], s60 offen lds\n\t" \
  "s_add_u32 m0, s54, 54272\n\t" \
  "s_mov_b32 s60, 2129920\n\t" \
  "buffer_load_dwordx4 v71, %[Wv], s60 offen lds\n\t" \
  "s_add_u32 m0, s54, 55296\n\t" \
  "s_mov_b32 s60, 2162688\n\t" \
  "buffer_load_dwordx4 v71, %[Wv], s60 offen lds\n\t" \
  "s_add_u32 m0, s54, 56320\n\t" \
  "s_mov_b32 s60, 2195456\n\t" \
  "buffer_load_dwordx4 v71, %[Wv], s60 offen lds\n\t" \
  "s_waitcnt lgkmcnt(6)\n\t" \
  "v_perm_b32 v192, v82, v80, s62\n\t" \
  "v_perm_b32 v193, v82, v80, s63\n\t" \
  "s_waitcnt lgkmcnt(4)\n\t" \
  "v_perm_b32 v194, v86, v84, s62\n\t" \
  "v_perm_b32 v195, v86, v84, s63\n\t" \
  "s_waitcnt lgkmcnt(2)\n\t" \
  "v_perm_b32 v196, v90, v88, s62\n\t" \
  "v_perm_b32 v197, v90, v88, s63\n\t" \
  "s_waitcnt lgkmcnt(0)\n\t" \
  "v_perm_b32 v198, v94, v92, s62\n\t" \
  "v_perm_b32 v199, v94, v92, s63\n\t" \
  "v_perm_b32 v216, v83, v81, s62\n\t" \
  "v_perm_b32 v217, v83, v81, s63\n\t" \
  "v_perm_b32 v218, v87, v85, s62\n\t" \
  "v_perm_b32 v219, v87, v85, s63\n\t" \
  "v_perm_b32 v220, v91, v89, s62\n\t" \
  "v_perm_b32 v221, v91, v89, s63\n\t" \
  "v_perm_b32 v222, v95, v93, s62\n\t" \
  "v_perm_b32 v223, v95, v93, s63\n\t" \
  "v_add_u32 v78, 126976, v65\n\t" \
  "s_waitcnt vmcnt(12) lgkmcnt(0)\n\t" \
  "s_barrier\n\t" \
  "ds_read_b64_tr_b16 a[160:161], v78 offset:0\n\t" \
  "ds_read_b64_tr_b16 a[162:163], v78 offset:1024\n\t" \
  "ds_read_b64_tr_b16 a[164:165], v78 offset:8192\n\t" \
  "ds_read_b64_tr_b16 a[166:167], v78 offset:9216\n\t" \
  "ds_read_b64_tr_b16 a[168:169], v78 offset:8\n\t" \
  "ds_read_b64_tr_b16 a[170:171], v78 offset:1032\n\t" \
  "ds_read_b64_tr_b16 a[172:173], v78 offset:8200\n\t" \
  "ds_read_b64_tr_b16 a[174:175], v78 offset:9224\n\t" \
  "s_add_u32 m0, s54, 90112\n\t" \
  "s_mov_b32 s60, 2621440\n\t" \
  "buffer_load_dwordx4 v71, %[Wq], s60 offen lds\n\t" \
  "s_add_u32 m0, s54, 91136\n\t" \
  "s_mov_b32 s60, 2654208\n\t" \
  "buffer_load_dwordx4 v71, %[Wq], s60 offen lds\n\t" \
  "s_add_u32 m0, s54, 92160\n\t" \
  "s_mov_b32 s60, 2686976\n\t" \
  "buffer_load_dwordx4 v71, %[Wq], s60 offen lds\n\t" \
  "s_add_u32 m0, s54, 93184\n\t" \
  "s_mov_b32 s60, 2719744\n\t" \
  "buffer_load_dwordx4 v71, %[Wq], s60 offen lds\n\t" \
  "v_add_u32 v78, 0, v65\n\t" \
  "s_waitcnt vmcnt(12) lgkmcnt(0)\n\t" \
  "s_barrier\n\t" \
  "ds_read_b64_tr_b16 a[176:177], v78 offset:0\n\t" \
  "ds_read_b64_tr_b16 a[178:179], v78 offset:1024\n\t" \
  "ds_read_b64_tr_b16 a[180:181], v78 offset:8192\n\t" \
  "ds_read_b64_tr_b16 a[182:183], v78 offset:9216\n\t" \
  "ds_read_b64_tr_b16 a[184:185], v78 offset:8\n\t" \
  "ds_read_b64_tr_b16 a[186:187], v78 offset:1032\n\t" \
  "ds_read_b64_tr_b16 a[188:189], v78 offset:8200\n\t" \
  "ds_read_b64_tr_b16 a[190:191], v78 offset:9224\n\t" \
  "s_add_u32 m0, s54, 126976\n\t" \
  "s_add_u32 s60, s75, 0\n\t" \
  "buffer_load_dwordx4 v75, %[Wo], s60 offen lds\n\t" \
  "s_add_u32 m0, s54, 128000\n\t" \
  "s_add_u32 s60, s75, 9216\n\t" \
  "buffer_load_dwordx4 v75, %[Wo], s60 offen lds\n\t" \
  "s_add_u32 m0, s54, 129024\n\t" \
  "s_add_u32 s60, s75, 18432\n\t" \
  "buffer_load_dwordx4 v75, %[Wo], s60 offen lds\n\t" \
  "s_add_u32 m0, s54, 130048\n\t" \
  "s_add_u32 s60, s75, 27648\n\t" \
  "buffer_load_dwordx4 v75, %[Wo], s60 offen lds\n\t" \
  "v_add_u32 v78, 16384, v64\n\t" \
  "v_add_u32 v79, 17408, v64\n\t" \
  "s_waitcnt vmcnt(12) lgkmcnt(0)\n\t" \
  "s_barrier\n\t" \
  "ds_read2_b32 v[80:81], v78 offset0:0 offset1:2\n\t" \
  "ds_read2_b32 v[82:83], v78 offset0:32 offset1:34\n\t" \
  "ds_read2_b32 v[84:85], v78 offset0:64 offset1:66\n\t" \
  "ds_read2_b32 v[86:87], v78 offset0:96 offset1:98\n\t" \
  "ds_read2_b32 v[88:89], v79 offset0:0 offset1:2\n\t" \
  "ds_read2_b32 v[90:91], v79 offset0:32 offset1:34\n\t" \
  "ds_read2_b32 v[92:93], v79 offset0:64 offset1:66\n\t" \
  "ds_read2_b32 v[94:95], v79 offset0:96 offset1:98\n\t" \
  "s_add_u32 m0, s54, 0\n\t" \
  "s_mov_b32 s60, 2621440\n\t" \
  "buffer_load_dwordx4 v71, %[Wk], s60 offen lds\n\t" \
  "s_add_u32 m0, s54, 1024\n\t" \
  "s_mov_b32 s60, 2654208\n\t" \
  "buffer_load_dwordx4 v71, %[Wk], s60 offen lds\n\t" \
  "s_add_u32 m0, s54, 2048\n\t" \
  "s_mov_b32 s60, 2686976\n\t" \
  "buffer_load_dwordx4 v71, %[Wk], s60 offen lds\n\t" \
  "s_add_u32 m0, s54, 3072\n\t" \
  "s_mov_b32 s60, 2719744\n\t" \
  "buffer_load_dwordx4 v71, %[Wk], s60 offen lds\n\t" \
  "s_waitcnt lgkmcnt(6)\n\t" \
  "v_perm_b32 v224, v82, v80, s62\n\t" \
  "v_perm_b32 v225, v82, v80, s63\n\t" \
  "s_waitcnt lgkmcnt(4)\n\t" \
  "v_perm_b32 v226, v86, v84, s62\n\t" \
  "v_perm_b32 v227, v86, v84, s63\n\t" \
  "s_waitcnt lgkmcnt(2)\n\t" \
  "v_perm_b32 v228, v90, v88, s62\n\t" \
  "v_perm_b32 v229, v90, v88, s63\n\t" \
  "s_waitcnt lgkmcnt(0)\n\t" \
  "v_perm_b32 v230, v94, v92, s62\n\t" \
  "v_perm_b32 v231, v94, v92, s63\n\t" \
  "v_perm_b32 v120, v83, v81, s62\n\t" \
  "v_perm_b32 v121, v83, v81, s63\n\t" \
  "v_perm_b32 v122, v87, v85, s62\n\t" \
  "v_perm_b32 v123, v87, v85, s63\n\t" \
  "v_perm_b32 v124, v91, v89, s62\n\t" \
  "v_perm_b32 v125, v91, v89, s63\n\t" \
  "v_perm_b32 v126, v95, v93, s62\n\t" \
  "v_perm_b32 v127, v95, v93, s63\n\t" \
  "ds_write_b128 v70, v[120:123] offset:32768\n\t" \
  "ds_write_b128 v70, v[124:127] offset:33792\n\t" \
  "v_add_u32 v78, 53248, v66\n\t" \
  "s_waitcnt vmcnt(12) lgkmcnt(0)\n\t" \
  "s_barrier\n\t" \
  "ds_read_b64_tr_b16 a[192:193], v78 offset:0\n\t" \
  "ds_read_b64_tr_b16 a[194:195], v78 offset:1024\n\t" \
  "ds_read_b64_tr_b16 a[196:197], v78 offset:8192\n\t" \
  "ds_read_b64_tr_b16 a[198:199], v78 offset:9216\n\t" \
  "ds_read_b64_tr_b16 v[48:49], v78 offset:32\n\t" \
  "ds_read_b64_tr_b16 v[50:51], v78 offset:1056\n\t" \
  "ds_read_b64_tr_b16 v[52:53], v78 offset:8224\n\t" \
  "ds_read_b64_tr_b16 v[54:55], v78 offset:9248\n\t" \
  "s_add_u32 m0, s54, 16384\n\t" \
  "s_mov_b32 s60, 2621440\n\t" \
  "buffer_load_dwordx4 v71, %[Wv], s60 offen lds\n\t" \
  "s_add_u32 m0, s54, 17408\n\t" \
  "s_mov_b32 s60, 2654208\n\t" \
  "buffer_load_dwordx4 v71, %[Wv], s60 offen lds\n\t" \
  "s_add_u32 m0, s54, 18432\n\t" \
  "s_mov_b32 s60, 2686976\n\t" \
  "buffer_load_dwordx4 v71, %[Wv], s60 offen lds\n\t" \
  "s_add_u32 m0, s54, 19456\n\t" \
  "s_mov_b32 s60, 2719744\n\t" \
  "buffer_load_dwordx4 v71, %[Wv], s60 offen lds\n\t" \
  "v_add_u32 v78, 90112, v65\n\t" \
  "s_waitcnt vmcnt(12) lgkmcnt(0)\n\t" \
  "s_barrier\n\t" \
  "ds_read_b64_tr_b16 a[200:201], v78 offset:0\n\t" \
  "ds_read_b64_tr_b16 a[202:203], v78 offset:1024\n\t" \
  "ds_read_b64_tr_b16 a[204:205], v78 offset:8192\n\t" \
  "ds_read_b64_tr_b16 a[206:207], v78 offset:9216\n\t" \
  "ds_read_b64_tr_b16 v[24:25], v78 offset:8\n\t" \
  "ds_read_b64_tr_b16 v[26:27], v78 offset:1032\n\t" \
  "ds_read_b64_tr_b16 v[28:29], v78 offset:8200\n\t" \
  "ds_read_b64_tr_b16 v[30:31], v78 offset:9224\n\t" \
  "s_add_u32 m0, s54, 53248\n\t" \
  "s_mov_b32 s60, 3145728\n\t" \
  "buffer_load_dwordx4 v71, %[Wq], s60 offen lds\n\t" \
  "s_add_u32 m0, s54, 54272\n\t" \
  "s_mov_b32 s60, 3178496\n\t" \
  "buffer_load_dwordx4 v71, %[Wq], s60 offen lds\n\t" \
  "s_add_u32 m0, s54, 55296\n\t" \
  "s_mov_b32 s60, 3211264\n\t" \
  "buffer_load_dwordx4 v71, %[Wq], s60 offen lds\n\t" \
  "s_add_u32 m0, s54, 56320\n\t" \
  "s_mov_b32 s60, 3244032\n\t" \
  "buffer_load_dwordx4 v71, %[Wq], s60 offen lds\n\t" \
  "v_add_u32 v78, 126976, v67\n\t" \
  "s_waitcnt vmcnt(12) lgkmcnt(0)\n\t" \
  "s_barrier\n\t" \
  "ds_read_u16 v80, v78 offset:0\n\t" \
  "ds_read_u16 v81, v78 offset:128\n\t" \
  "ds_read_u16 v82, v78 offset:4096\n\t" \
  "ds_read_u16 v83, v78 offset:4224\n\t" \
  "ds_read_u16 v84, v78 offset:256\n\t" \
  "ds_read_u16 v85, v78 offset:384\n\t" \
  "ds_read_u16 v86, v78 offset:4352\n\t" \
  "ds_read_u16 v87, v78 offset:4480\n\t" \
  "ds_read_u16 v88, v78 offset:1024\n\t" \
  "ds_read_u16 v89, v78 offset:1152\n\t" \
  "ds_read_u16 v90, v78 offset:5120\n\t" \
  "ds_read_u16 v91, v78 offset:5248\n\t" \
  "ds_read_u16 v92, v78 offset:1280\n\t" \
  "ds_read_u16 v93, v78 offset:1408\n\t" \
  "ds_read_u16 v94, v78 offset:5376\n\t" \
  "ds_read_u16 v95, v78 offset:5504\n\t" \
  "ds_read_u16 v96, v78 offset:8192\n\t" \
  "ds_read_u16 v97, v78 offset:8320\n\t" \
  "ds_read_u16 v98, v78 offset:12288\n\t" \
  "ds_read_u16 v99, v78 offset:12416\n\t" \
  "ds_read_u16 v100, v78 offset:8448\n\t" \
  "ds_read_u16 v101, v78 offset:8576\n\t" \
  "ds_read_u16 v102, v78 offset:12544\n\t" \
  "ds_read_u16 v103, v78 offset:12672\n\t" \
  "ds_read_u16 v104, v78 offset:9216\n\t" \
  "ds_read_u16 v105, v78 offset:9344\n\t" \
  "ds_read_u16 v106, v78 offset:13312\n\t" \
  "ds_read_u16 v107, v78 offset:13440\n\t" \
  "ds_read_u16 v108, v78 offset:9472\n\t" \
  "ds_read_u16 v109, v78 offset:9600\n\t" \
  "ds_read_u16 v110, v78 offset:13568\n\t" \
  "ds_read_u16 v111, v78 offset:13696\n\t" \
  "s_add_u32 m0, s54, 90112\n\t" \
  "s_mov_b32 s60, 3145728\n\t" \
  "buffer_load_dwordx4 v71, %[Wk], s60 offen lds\n\t" \
  "s_add_u32 m0, s54, 91136\n\t" \
  "s_mov_b32 s60, 3178496\n\t" \
  "buffer_load_dwordx4 v71, %[Wk], s60 offen lds\n\t" \
  "s_add_u32 m0, s54, 92160\n\t" \
  "s_mov_b32 s60, 3211264\n\t" \
  "buffer_load_dwordx4 v71, %[Wk], s60 offen lds\n\t" \
  "s_add_u32 m0, s54, 93184\n\t" \
  "s_mov_b32 s60, 3244032\n\t" \
  "buffer_load_dwordx4 v71, %[Wk], s60 offen lds\n\t" \
  "s_waitcnt lgkmcnt(0)\n\t" \
  "v_lshl_or_b32 v248, v81, 16, v80\n\t" \
  "v_lshl_or_b32 v249, v83, 16, v82\n\t" \
  "v_lshl_or_b32 v250, v85, 16, v84\n\t" \
  "v_lshl_or_b32 v251, v87, 16, v86\n\t" \
  "v_lshl_or_b32 v252, v89, 16, v88\n\t" \
  "v_lshl_or_b32 v253, v91, 16, v90\n\t" \
  "v_lshl_or_b32 v254, v93, 16, v92\n\t" \
  "v_lshl_or_b32 v255, v95, 16, v94\n\t" \
  "v_lshl_or_b32 v112, v97, 16, v96\n\t" \
  "v_lshl_or_b32 v113, v99, 16, v98\n\t" \
  "v_lshl_or_b32 v114, v101, 16, v100\n\t" \
  "v_lshl_or_b32 v115, v103, 16, v102\n\t" \
  "v_lshl_or_b32 v116, v105, 16, v104\n\t" \
  "v_lshl_or_b32 v117, v107, 16, v106\n\t" \
  "v_lshl_or_b32 v118, v109, 16, v108\n\t" \
  "v_lshl_or_b32 v119, v111, 16, v110\n\t" \
  "ds_write_b128 v70, v[112:115] offset:34816\n\t" \
  "ds_write_b128 v70, v[116:119] offset:35840\n\t" \
  "v_add_u32 v78, 0, v65\n\t" \
  "s_waitcnt vmcnt(12) lgkmcnt(0)\n\t" \
  "s_barrier\n\t" \
  "ds_read_b64_tr_b16 a[208:209], v78 offset:0\n\t" \
  "ds_read_b64_tr_b16 a[210:211], v78 offset:1024\n\t" \
  "ds_read_b64_tr_b16 a[212:213], v78 offset:8192\n\t" \
  "ds_read_b64_tr_b16 a[214:215], v78 offset:9216\n\t" \
  "ds_read_b64_tr_b16 a[216:217], v78 offset:8\n\t" \
  "ds_read_b64_tr_b16 a[218:219], v78 offset:1032\n\t" \
  "ds_read_b64_tr_b16 a[220:221], v78 offset:8200\n\t" \
  "ds_read_b64_tr_b16 a[222:223], v78 offset:9224\n\t" \
  "s_add_u32 m0, s54, 126976\n\t" \
  "s_mov_b32 s60, 3145728\n\t" \
  "buffer_load_dwordx4 v71, %[Wv], s60 offen lds\n\t" \
  "s_add_u32 m0, s54, 128000\n\t" \
  "s_mov_b32 s60, 3178496\n\t" \
  "buffer_load_dwordx4 v71, %[Wv], s60 offen lds\n\t" \
  "s_add_u32 m0, s54, 129024\n\t" \
  "s_mov_b32 s60, 3211264\n\t" \
  "buffer_load_dwordx4 v71, %[Wv], s60 offen lds\n\t" \
  "s_add_u32 m0, s54, 130048\n\t" \
  "s_mov_b32 s60, 3244032\n\t" \
  "buffer_load_dwordx4 v71, %[Wv], s60 offen lds\n\t" \
  "v_add_u32 v78, 16384, v66\n\t" \
  "s_waitcnt vmcnt(12) lgkmcnt(0)\n\t" \
  "s_barrier\n\t" \
  "ds_read_b64_tr_b16 a[224:225], v78 offset:0\n\t" \
  "ds_read_b64_tr_b16 a[226:227], v78 offset:1024\n\t" \
  "ds_read_b64_tr_b16 a[228:229], v78 offset:8192\n\t" \
  "ds_read_b64_tr_b16 a[230:231], v78 offset:9216\n\t" \
  "ds_read_b64_tr_b16 a[232:233], v78 offset:32\n\t" \
  "ds_read_b64_tr_b16 a[234:235], v78 offset:1056\n\t" \
  "ds_read_b64_tr_b16 a[236:237], v78 offset:8224\n\t" \
  "ds_read_b64_tr_b16 a[238:239], v78 offset:9248\n\t" \
  "s_add_u32 m0, s54, 0\n\t" \
  "s_mov_b32 s60, 3670016\n\t" \
  "buffer_load_dwordx4 v71, %[Wq], s60 offen lds\n\t" \
  "s_add_u32 m0, s54, 1024\n\t" \
  "s_mov_b32 s60, 3702784\n\t" \
  "buffer_load_dwordx4 v71, %[Wq], s60 offen lds\n\t" \
  "s_add_u32 m0, s54, 2048\n\t" \
  "s_mov_b32 s60, 3735552\n\t" \
  "buffer_load_dwordx4 v71, %[Wq], s60 offen lds\n\t" \
  "s_add_u32 m0, s54, 3072\n\t" \
  "s_mov_b32 s60, 3768320\n\t" \
  "buffer_load_dwordx4 v71, %[Wq], s60 offen lds\n\t" \
  "v_add_u32 v78, 53248, v65\n\t" \
  "s_waitcnt vmcnt(12) lgkmcnt(0)\n\t" \
  "s_barrier\n\t" \
  "ds_read_b64_tr_b16 a[240:241], v78 offset:0\n\t" \
  "ds_read_b64_tr_b16 a[242:243], v78 offset:1024\n\t" \
  "ds_read_b64_tr_b16 a[244:245], v78 offset:8192\n\t" \
  "ds_read_b64_tr_b16 a[246:247], v78 offset:9216\n\t" \
  "ds_read_b64_tr_b16 a[248:249], v78 offset:8\n\t" \
  "ds_read_b64_tr_b16 a[250:251], v78 offset:1032\n\t" \
  "ds_read_b64_tr_b16 a[252:253], v78 offset:8200\n\t" \
  "ds_read_b64_tr_b16 a[254:255], v78 offset:9224\n\t" \
  "s_add_u32 m0, s54, 16384\n\t" \
  "s_mov_b32 s60, 3670016\n\t" \
  "buffer_load_dwordx4 v71, %[Wk], s60 offen lds\n\t" \
  "s_add_u32 m0, s54, 17408\n\t" \
  "s_mov_b32 s60, 3702784\n\t" \
  "buffer_load_dwordx4 v71, %[Wk], s60 offen lds\n\t" \
  "s_add_u32 m0, s54, 18432\n\t" \
  "s_mov_b32 s60, 3735552\n\t" \
  "buffer_load_dwordx4 v71, %[Wk], s60 offen lds\n\t" \
  "s_add_u32 m0, s54, 19456\n\t" \
  "s_mov_b32 s60, 3768320\n\t" \
  "buffer_load_dwordx4 v71, %[Wk], s60 offen lds\n\t" \
  "v_add_u32 v78, 90112, v65\n\t" \
  "s_waitcnt vmcnt(12) lgkmcnt(0)\n\t" \
  "s_barrier\n\t" \
  "ds_read_b64_tr_b16 v[80:81], v78 offset:0\n\t" \
  "ds_read_b64_tr_b16 v[82:83], v78 offset:1024\n\t" \
  "ds_read_b64_tr_b16 v[84:85], v78 offset:8192\n\t" \
  "ds_read_b64_tr_b16 v[86:87], v78 offset:9216\n\t" \
  "ds_read_b64_tr_b16 v[88:89], v78 offset:8\n\t" \
  "ds_read_b64_tr_b16 v[90:91], v78 offset:1032\n\t" \
  "ds_read_b64_tr_b16 v[92:93], v78 offset:8200\n\t" \
  "ds_read_b64_tr_b16 v[94:95], v78 offset:9224\n\t" \
  "s_add_u32 m0, s54, 53248\n\t" \
  "s_mov_b32 s60, 3670016\n\t" \
  "buffer_load_dwordx4 v71, %[Wv], s60 offen lds\n\t" \
  "s_add_u32 m0, s54, 54272\n\t" \
  "s_mov_b32 s60, 3702784\n\t" \
  "buffer_load_dwordx4 v71, %[Wv], s60 offen lds\n\t" \
  "s_add_u32 m0, s54, 55296\n\t" \
  "s_mov_b32 s60, 3735552\n\t" \
  "buffer_load_dwordx4 v71, %[Wv], s60 offen lds\n\t" \
  "s_add_u32 m0, s54, 56320\n\t" \
  "s_mov_b32 s60, 3768320\n\t" \
  "buffer_load_dwordx4 v71, %[Wv], s60 offen lds\n\t" \
  "v_add_u32 v78, 126976, v66\n\t" \
  "s_waitcnt vmcnt(12) lgkmcnt(0)\n\t" \
  "s_barrier\n\t" \
  "ds_read_b64_tr_b16 v[96:97], v78 offset:0\n\t" \
  "ds_read_b64_tr_b16 v[98:99], v78 offset:1024\n\t" \
  "ds_read_b64_tr_b16 v[100:101], v78 offset:8192\n\t" \
  "ds_read_b64_tr_b16 v[102:103], v78 offset:9216\n\t" \
  "ds_read_b64_tr_b16 v[104:105], v78 offset:32\n\t" \
  "ds_read_b64_tr_b16 v[106:107], v78 offset:1056\n\t" \
  "ds_read_b64_tr_b16 v[108:109], v78 offset:8224\n\t" \
  "ds_read_b64_tr_b16 v[110:111], v78 offset:9248\n\t" \
  "s_add_u32 m0, s54, 90112\n\t" \
  "s_mov_b32 s60, 4194304\n\t" \
  "buffer_load_dwordx4 v71, %[Wq], s60 offen lds\n\t" \
  "s_add_u32 m0, s54, 91136\n\t" \
  "s_mov_b32 s60, 4227072\n\t" \
  "buffer_load_dwordx4 v71, %[Wq], s60 offen lds\n\t" \
  "s_add_u32 m0, s54, 92160\n\t" \
  "s_mov_b32 s60, 4259840\n\t" \
  "buffer_load_dwordx4 v71, %[Wq], s60 offen lds\n\t" \
  "s_add_u32 m0, s54, 93184\n\t" \
  "s_mov_b32 s60, 4292608\n\t" \
  "buffer_load_dwordx4 v71, %[Wq], s60 offen lds\n\t" \
  "v_add_u32 v78, 0, v65\n\t" \
  "s_waitcnt vmcnt(12) lgkmcnt(0)\n\t" \
  "s_barrier\n\t" \
  "ds_read_b64_tr_b16 v[112:113], v78 offset:0\n\t" \
  "ds_read_b64_tr_b16 v[114:115], v78 offset:1024\n\t" \
  "ds_read_b64_tr_b16 v[116:117], v78 offset:8192\n\t" \
  "ds_read_b64_tr_b16 v[118:119], v78 offset:9216\n\t" \
  "ds_read_b64_tr_b16 v[120:121], v78 offset:8\n\t" \
  "ds_read_b64_tr_b16 v[122:123], v78 offset:1032\n\t" \
  "ds_read_b64_tr_b16 v[124:125], v78 offset:8200\n\t" \
  "ds_read_b64_tr_b16 v[126:127], v78 offset:9224\n\t" \
  "s_add_u32 m0, s54, 126976\n\t" \
  "s_mov_b32 s60, 4194304\n\t" \
  "buffer_load_dwordx4 v71, %[Wk], s60 offen lds\n\t" \
  "s_add_u32 m0, s54, 128000\n\t" \
  "s_mov_b32 s60, 4227072\n\t" \
  "buffer_load_dwordx4 v71, %[Wk], s60 offen lds\n\t" \
  "s_add_u32 m0, s54, 129024\n\t" \
  "s_mov_b32 s60, 4259840\n\t" \
  "buffer_load_dwordx4 v71, %[Wk], s60 offen lds\n\t" \
  "s_add_u32 m0, s54, 130048\n\t" \
  "s_mov_b32 s60, 4292608\n\t" \
  "buffer_load_dwordx4 v71, %[Wk], s60 offen lds\n\t" \
  "v_add_u32 v78, 16384, v65\n\t" \
  "s_waitcnt vmcnt(12) lgkmcnt(0)\n\t" \
  "s_barrier\n\t" \
  "ds_read_b64_tr_b16 v[128:129], v78 offset:0\n\t" \
  "ds_read_b64_tr_b16 v[130:131], v78 offset:1024\n\t" \
  "ds_read_b64_tr_b16 v[132:133], v78 offset:8192\n\t" \
  "ds_read_b64_tr_b16 v[134:135], v78 offset:9216\n\t" \
  "ds_read_b64_tr_b16 v[136:137], v78 offset:8\n\t" \
  "ds_read_b64_tr_b16 v[138:139], v78 offset:1032\n\t" \
  "ds_read_b64_tr_b16 v[140:141], v78 offset:8200\n\t" \
  "ds_read_b64_tr_b16 v[142:143], v78 offset:9224\n\t" \
  "s_add_u32 m0, s54, 0\n\t" \
  "s_mov_b32 s60, 4194304\n\t" \
  "buffer_load_dwordx4 v71, %[Wv], s60 offen lds\n\t" \
  "s_add_u32 m0, s54, 1024\n\t" \
  "s_mov_b32 s60, 4227072\n\t" \
  "buffer_load_dwordx4 v71, %[Wv], s60 offen lds\n\t" \
  "s_add_u32 m0, s54, 2048\n\t" \
  "s_mov_b32 s60, 4259840\n\t" \
  "buffer_load_dwordx4 v71, %[Wv], s60 offen lds\n\t" \
  "s_add_u32 m0, s54, 3072\n\t" \
  "s_mov_b32 s60, 4292608\n\t" \
  "buffer_load_dwordx4 v71, %[Wv], s60 offen lds\n\t" \
  "v_add_u32 v78, 53248, v66\n\t" \
  "s_waitcnt vmcnt(12) lgkmcnt(0)\n\t" \
  "s_barrier\n\t" \
  "ds_read_b64_tr_b16 v[144:145], v78 offset:0\n\t" \
  "ds_read_b64_tr_b16 v[146:147], v78 offset:1024\n\t" \
  "ds_read_b64_tr_b16 v[148:149], v78 offset:8192\n\t" \
  "ds_read_b64_tr_b16 v[150:151], v78 offset:9216\n\t" \
  "ds_read_b64_tr_b16 v[152:153], v78 offset:32\n\t" \
  "ds_read_b64_tr_b16 v[154:155], v78 offset:1056\n\t" \
  "ds_read_b64_tr_b16 v[156:157], v78 offset:8224\n\t" \
  "ds_read_b64_tr_b16 v[158:159], v78 offset:9248\n\t" \
  "v_add_u32 v78, 90112, v65\n\t" \
  "s_waitcnt vmcnt(8) lgkmcnt(0)\n\t" \
  "s_barrier\n\t" \
  "ds_read_b64_tr_b16 v[160:161], v78 offset:0\n\t" \
  "ds_read_b64_tr_b16 v[162:163], v78 offset:1024\n\t" \
  "ds_read_b64_tr_b16 v[164:165], v78 offset:8192\n\t" \
  "ds_read_b64_tr_b16 v[166:167], v78 offset:9216\n\t" \
  "ds_read_b64_tr_b16 v[168:169], v78 offset:8\n\t" \
  "ds_read_b64_tr_b16 v[170:171], v78 offset:1032\n\t" \
  "ds_read_b64_tr_b16 v[172:173], v78 offset:8200\n\t" \
  "ds_read_b64_tr_b16 v[174:175], v78 offset:9224\n\t" \
  "v_add_u32 v78, 126976, v65\n\t" \
  "s_waitcnt vmcnt(4) lgkmcnt(0)\n\t" \
  "s_barrier\n\t" \
  "ds_read_b64_tr_b16 v[40:41], v78 offset:0\n\t" \
  "ds_read_b64_tr_b16 v[42:43], v78 offset:1024\n\t" \
  "ds_read_b64_tr_b16 v[44:45], v78 offset:8192\n\t" \
  "ds_read_b64_tr_b16 v[46:47], v78 offset:9216\n\t" \
  "ds_read_b64_tr_b16 v[176:177], v78 offset:8\n\t" \
  "ds_read_b64_tr_b16 v[178:179], v78 offset:1032\n\t" \
  "ds_read_b64_tr_b16 v[180:181], v78 offset:8200\n\t" \
  "ds_read_b64_tr_b16 v[182:183], v78 offset:9224\n\t" \
  "v_add_u32 v78, 0, v66\n\t" \
  "s_waitcnt vmcnt(0) lgkmcnt(0)\n\t" \
  "s_barrier\n\t" \
  "ds_read_b64_tr_b16 v[184:185], v78 offset:0\n\t" \
  "ds_read_b64_tr_b16 v[186:187], v78 offset:1024\n\t" \
  "ds_read_b64_tr_b16 v[188:189], v78 offset:8192\n\t" \
  "ds_read_b64_tr_b16 v[190:191], v78 offset:9216\n\t" \
  "ds_read_b64_tr_b16 v[56:57], v78 offset:32\n\t" \
  "ds_read_b64_tr_b16 v[58:59], v78 offset:1056\n\t" \
  "ds_read_b64_tr_b16 v[60:61], v78 offset:8224\n\t" \
  "ds_read_b64_tr_b16 v[62:63], v78 offset:9248\n\t" \
  "s_waitcnt lgkmcnt(0)\n\t" \
  "s_barrier\n\t" \
  "ds_write_b128 v70, v[0:3] offset:0\n\t" \
  "ds_write_b128 v70, v[4:7] offset:1024\n\t" \
  "ds_write_b128 v70, v[8:11] offset:2048\n\t" \
  "ds_write_b128 v70, v[12:15] offset:3072\n\t" \
  "ds_write_b128 v70, v[16:19] offset:4096\n\t" \
  "ds_write_b128 v70, v[20:23] offset:5120\n\t" \
  "ds_write_b128 v70, v[24:27] offset:6144\n\t" \
  "ds_write_b128 v70, v[28:31] offset:7168\n\t" \
  "ds_write_b128 v70, v[32:35] offset:8192\n\t" \
  "ds_write_b128 v70, v[36:39] offset:9216\n\t" \
  "ds_write_b128 v70, v[40:43] offset:10240\n\t" \
  "ds_write_b128 v70, v[44:47] offset:11264\n\t" \
  "ds_write_b128 v70, v[48:51] offset:12288\n\t" \
  "ds_write_b128 v70, v[52:55] offset:13312\n\t" \
  "ds_write_b128 v70, v[56:59] offset:14336\n\t" \
  "ds_write_b128 v70, v[60:63] offset:15360\n\t" \
  "s_waitcnt lgkmcnt(0)\n\t" \
  "v_and_b32 v72, 1, v72\n\t" \
  "v_and_b32 v74, 1, v74\n\t" \
  "v_cmp_ne_u32 s[90:91], s32, v72\n\t" \
  "v_cmp_ne_u32 vcc, s32, v74\n\t" \
  "s_nop 1\n\t" \
  "s_mov_b32 s96, vcc_lo\n\t" \
  "s_mov_b32 s21, vcc_lo\n\t" \
  "v_and_b32 v73, 32, v69\n\t" \
  "v_lshl_add_u32 v73, v73, 5, v69\n\t" \
  "s_mov_b32 s35, s30\n\t" \
  "s_cmp_lt_u32 s32, " CH_DRYL "\n\t" \
  "s_cbranch_scc1 LINI_%=\n\t" \
  "s_mov_b32 s30, 0\n\t" \
  "s_mov_b32 s34, 0\n\t" \
  "s_cmp_eq_u32 s40, 0\n\t" \
  "s_cbranch_scc1 LW0_%=\n\t" \
  "s_barrier\n\t" \
  "s_cmp_eq_u32 s40, 1\n\t" \
  "s_cbranch_scc1 LG1_%=\n\t" \
  "s_cmp_eq_u32 s40, 2\n\t" \
  "s_cbranch_scc1 LG2_%=\n\t" \
  "s_branch LG3_%=\n\t" \
  "LINI_%=:\n\t" \
  "s_waitcnt vmcnt(0) lgkmcnt(0)\n\t" \
  "s_mov_b32 s30, s35\n\t" \
  "s_movk_i32 s34, 1000\n\t" \
  "s_mov_b32 s48, -1\n\t" \
  "s_add_u32 s97, s41, s53\n\t" \
  "s_add_u32 s98, s37, s53\n\t" \
  "s_add_u32 s82, s48, 0\n\t" \
  "s_and_b32 s82, s82, " CH_TRM "\n\t" \
  "s_lshr_b64 s[60:61], s[90:91], s82\n\t" \
  "s_and_b32 s60, s60, 1\n\t" \
  "s_lshl_b32 s60, s60, 13\n\t" \
  "s_add_u32 s82, s82, " CH_SG0 "\n\t" \
  "s_lshl_b32 s82, s82, 21\n\t" \
  "s_or_b32 s82, s82, s60\n\t" \
  "s_add_u32 s50, s82, s38\n\t" \
  "s_add_u32 s69, s82, s39\n\t" \
  "s_add_u32 s82, s48, 1\n\t" \
  "s_lshr_b64 s[60:61], s[90:91], s82\n\t" \
  "s_and_b32 s60, s60, 1\n\t" \
  "s_lshl_b32 s60, s60, 13\n\t" \
  "s_add_u32 s82, s82, " CH_SG0 "\n\t" \
  "s_lshl_b32 s82, s82, 21\n\t" \
  "s_or_b32 s82, s82, s60\n\t" \
  "s_add_u32 s52, s82, s43\n\t" \
  "s_add_u32 s82, s48, " CH_ARMD "\n\t" \
  "s_lshr_b64 s[60:61], s[90:91], s82\n\t" \
  "s_and_b32 s60, s60, 1\n\t" \
  "s_lshl_b32 s60, s60, 13\n\t" \
  "s_add_u32 s82, s82, " CH_SG0 "\n\t" \
  "s_lshl_b32 s82, s82, 21\n\t" \
  "s_or_b32 s82, s82, s60\n\t" \
  "s_add_u32 s85, s82, s45\n\t" \
  "s_cmp_lt_i32 s48, " CH_ARMS "\n\t" \
  "s_cselect_b32 s85, s27, s85\n\t" \
  "v_and_b32 v37, 0x30, v69\n\t" \
  "v_add_u32 v37, s52, v37\n\t" \
  "s_mov_b32 s48, 0\n\t" \
  "s_mov_b32 s86, 1\n\t" \
  "s_and_b32 s49, s96, 1\n\t" \
  "s_lshl_b32 s49, s49, 13\n\t" \
  "s_add_u32 s53, s49, " CH_SLOT "\n\t" \
  "s_movk_i32 s20, 2560\n\t" \
  "s_movk_i32 s60, 124\n\t" \
  "s_cmp_eq_u32 s40, 3\n\t" \
  "s_cselect_b32 s81, 76, s60\n\t" \
  "s_add_u32 s82, s48, 0\n\t" \
  "s_and_b32 s82, s82, " CH_TRM "\n\t" \
  "s_lshr_b64 s[60:61], s[90:91], s82\n\t" \
  "s_and_b32 s60, s60, 1\n\t" \
  "s_lshl_b32 s60, s60, 13\n\t" \
  "s_add_u32 s82, s82, " CH_SG0 "\n\t" \
  "s_lshl_b32 s82, s82, 21\n\t" \
  "s_or_b32 s82, s82, s60\n\t" \
  "s_add_u32 s50, s82, s38\n\t" \
  "s_add_u32 s69, s82, s39\n\t" \
  "s_add_u32 s82, s48, 0\n\t" \
  "s_lshr_b64 s[60:61], s[90:91], s82\n\t" \
  "s_and_b32 s60, s60, 1\n\t" \
  "s_lshl_b32 s60, s60, 13\n\t" \
  "s_add_u32 s82, s82, " CH_SG0 "\n\t" \
  "s_lshl_b32 s82, s82, 21\n\t" \
  "s_or_b32 s82, s82, s60\n\t" \
  "s_add_u32 s51, s82, s43\n\t" \
  "s_add_u32 s97, s41, s53\n\t" \
  "s_add_u32 s98, s37, s53\n\t" \
  "s_add_u32 s82, s48, 0\n\t" \
  "s_and_b32 s82, s82, " CH_TRM "\n\t" \
  "s_lshr_b64 s[60:61], s[90:91], s82\n\t" \
  "s_and_b32 s60, s60, 1\n\t" \
  "s_lshl_b32 s60, s60, 13\n\t" \
  "s_add_u32 s82, s82, " CH_SG0 "\n\t" \
  "s_lshl_b32 s82, s82, 21\n\t" \
  "s_or_b32 s82, s82, s60\n\t" \
  "s_add_u32 s50, s82, s38\n\t" \
  "s_add_u32 s69, s82, s39\n\t" \
  "s_add_u32 s82, s48, 1\n\t" \
  "s_lshr_b64 s[60:61], s[90:91], s82\n\t" \
  "s_and_b32 s60, s60, 1\n\t" \
  "s_lshl_b32 s60, s60, 13\n\t" \
  "s_add_u32 s82, s82, " CH_SG0 "\n\t" \
  "s_lshl_b32 s82, s82, 21\n\t" \
  "s_or_b32 s82, s82, s60\n\t" \
  "s_add_u32 s52, s82, s43\n\t" \
  "s_add_u32 s82, s48, " CH_ARMD "\n\t" \
  "s_lshr_b64 s[60:61], s[90:91], s82\n\t" \
  "s_and_b32 s60, s60, 1\n\t" \
  "s_lshl_b32 s60, s60, 13\n\t" \
  "s_add_u32 s82, s82, " CH_SG0 "\n\t" \
  "s_lshl_b32 s82, s82, 21\n\t" \
  "s_or_b32 s82, s82, s60\n\t" \
  "s_add_u32 s85, s82, s45\n\t" \
  "s_cmp_lt_i32 s48, " CH_ARMS "\n\t" \
  "s_cselect_b32 s85, s27, s85\n\t" \
  "s_memrealtime s[62:63]\n\t" \
  "s_waitcnt lgkmcnt(0)\n\t" \
  "s_mov_b32 s65, s62\n\t" \
  "s_sub_u32 s47, s62, 1000\n\t" \
  "s_mov_b64 vcc, -1\n\t" \
  "s_cmp_eq_u32 s40, 0\n\t" \
  "s_cbranch_scc1 LW0_%=\n\t" \
  "s_barrier\n\t" \
  "s_cmp_eq_u32 s40, 1\n\t" \
  "s_cbranch_scc1 LG1_%=\n\t" \
  "s_cmp_eq_u32 s40, 2\n\t" \
  "s_cbranch_scc1 LG2_%=\n\t" \
  "s_branch LTK_%=\n\t" \
  ".p2align 6\n\t" \
  "LW0_%=:\n\t" \
  "v_lshlrev_b32 v78, 2, v69\n\t" \
  "v_and_b32 v78, 0x300, v78\n\t" \
  "v_lshrrev_b32 v79, 8, v69\n\t" \
  "v_lshl_add_u32 v78, v79, 6, v78\n\t" \
  "v_and_b32 v79, 48, v69\n\t" \
  "v_add_u32 v78, v78, v79\n\t" \
  "s_add_u32 s98, s49, s42\n\t" \
  "s_add_u32 s97, s98, 32768\n\t" \
  "s_add_u32 s99, s98, 65536\n\t" \
  "s_barrier\n\t" \
  "s_memrealtime s[62:63]\n\t" \
  "s_waitcnt lgkmcnt(0)\n\t" \
  "s_add_u32 s62, s62, " CH_DLL2 "\n\t" \
  "s_sub_i32 s63, s62, s65\n\t" \
  "s_cmp_gt_i32 s63, " CH_DLT "\n\t" \
  "s_cselect_b32 s60, " CH_DLY ", " CH_DLX "\n\t" \
  "s_add_u32 s62, s62, s60\n\t" \
  "s_cmp_gt_i32 s63, 0\n\t" \
  "s_cselect_b32 s65, s62, s65\n\t" \
  "s_mov_b32 m0, 0\n\t" \
  "s_movk_i32 s60, 8192\n\t" \
  "LWT_%=:\n\t" \
  "s_memrealtime s[62:63]\n\t" \
  "s_sub_u32 s60, s60, 1\n\t" \
  "s_cselect_b32 s61, 0, -1\n\t" \
  "s_waitcnt lgkmcnt(0)\n\t" \
  "s_sub_i32 s63, s62, s65\n\t" \
  "s_and_b32 s63, s63, s61\n\t" \
  "s_cmp_lt_i32 s63, " CH_FIN "\n\t" \
  "s_cbranch_scc1 LWT_%=\n\t" \
  "s_cmp_lt_i32 s63, 0\n\t" \
  "s_cbranch_scc1 LWT_%=f\n\t" \
  "LWT_%=g:\n\t" \
  "LDMA_%=:\n\t" \
  "buffer_load_dwordx4 v78, s[28:31], s98 offen offset:0 sc1 lds\n\t" \
  "buffer_load_dwordx4 v78, s[28:31], s98 offen offset:1024 sc1 lds\n\t" \
  "buffer_load_dwordx4 v78, s[28:31], s98 offen offset:2048 sc1 lds\n\t" \
  "buffer_load_dwordx4 v78, s[28:31], s98 offen offset:3072 sc1 lds\n\t" \
  "s_movk_i32 m0, 4096\n\t" \
  "s_mov_b32 s66, s62\n\t" \
  "buffer_load_dwordx4 v78, s[28:31], s97 offen offset:0 sc1 lds\n\t" \
  "buffer_load_dwordx4 v78, s[28:31], s97 offen offset:1024 sc1 lds\n\t" \
  "buffer_load_dwordx4 v78, s[28:31], s97 offen offset:2048 sc1 lds\n\t" \
  "buffer_load_dwordx4 v78, s[28:31], s97 offen offset:3072 sc1 lds\n\t" \
  "s_movk_i32 m0, 8192\n\t" \
  "s_nop 0\n\t" \
  "buffer_load_dwordx4 v69, s[28:31], s99 offen sc1 lds\n\t" \
  ".p2align 6\n\t" \
  "LG0_%=:\n\t" \
  "s_barrier\n\t" \
  "buffer_load_dwordx4 v[52:55], v69, s[28:31], s50 offen sc1\n\t" \
  "buffer_load_dwordx4 v[56:59], v69, s[28:31], s50 offen offset:1024 sc1\n\t" \
  "s_add_u32 s64, s50, 16384\n\t" \
  "s_movk_i32 m0, 11264\n\t" \
  "v_lshlrev_b32 v75, 1, v69\n\t" \
  "buffer_load_dword v75, s[28:31], s64 offen sc1 lds\n\t" \
  "s_memrealtime s[62:63]\n\t" \
  "s_waitcnt lgkmcnt(0)\n\t" \
  "s_sub_u32 s62, s62, " CH_TKL "\n\t" \
  "s_sub_i32 s63, s62, s65\n\t" \
  "s_cmp_gt_i32 s63, 0\n\t" \
  "s_cselect_b32 s62, s62, s65\n\t" \
  "s_sub_i32 s63, s62, s47\n\t" \
  "s_cmp_gt_i32 s63, " CH_LATE "\n\t" \
  "s_cselect_b32 s71, " CH_SCHW ", s46\n\t" \
  "s_add_u32 s61, s47, " CH_LATE "\n\t" \
  "s_cmp_gt_i32 s63, " CH_LATE "\n\t" \
  "s_cselect_b32 s62, s62, s61\n\t" \
  "s_add_u32 s71, s71, s53\n\t" \
  "s_add_u32 s60, s62, s81\n\t" \
  "s_add_u32 s60, s60, " CH_DL1L "\n\t" \
  "s_lshl_b32 s60, s60, 8\n\t" \
  "s_or_b32 s60, s60, " CH_B0 "\n\t" \
  "s_cmp_lt_u32 s48, " CH_ES "\n\t" \
  "s_cselect_b32 s61, " CH_EX ", 0\n\t" \
  "s_add_u32 s60, s60, s61\n\t" \
  "s_not_b32 s60, s60\n\t" \
  "v_mov_b32 v76, s60\n\t" \
  "s_lshr_b32 s60, s46, 31\n\t" \
  "s_or_b32 s60, s60, s68\n\t" \
  "s_cselect_b32 s25, 0, 1\n\t" \
  "s_mov_b64 exec, s[24:25]\n\t" \
  "buffer_atomic_umin v76, v69, s[28:31], s71 offen sc1\n\t" \
  "s_mov_b64 exec, -1\n\t" \
  "ds_read_b128 v[44:47], v70 offset:0\n\t" \
  "ds_read_b128 v[48:51], v70 offset:1024\n\t" \
  "ds_read_b128 v[60:63], v70 offset:2048\n\t" \
  "ds_read_b128 v[64:67], v70 offset:3072\n\t" \
  "v_mov_b64 v[36:37], 0\n\t" \
  "v_mov_b64 v[38:39], 0\n\t" \
  "v_mov_b64 v[40:41], 0\n\t" \
  "v_mov_b64 v[42:43], 0\n\t" \
  "v_lshrrev_b32 v71, 1, v69\n\t" \
  "v_lshrrev_b32 v72, 3, v69\n\t" \
  "v_and_b32 v72, 0x78, v72\n\t" \
  "v_and_b32 v74, 0x30, v69\n\t" \
  "v_lshl_add_u32 v72, v74, 3, v72\n\t" \
  "s_add_u32 s54, s53, s36\n\t" \
  "s_add_u32 s55, s54, 524288\n\t" \
  "s_add_u32 s54, s54, 0\n\t" \
  "s_add_u32 s75, s54, 262144\n\t" \
  "s_add_u32 s80, s53, s26\n\t" \
  "s_or_b32 s87, s53, 0x1fff80\n\t" \
  "s_and_b32 s87, s87, -128\n\t" \
  "s_add_u32 s87, s87, 0x200000\n\t" \
  "s_waitcnt vmcnt(9)\n\t" \
  "s_barrier\n\t" \
  "ds_read_b128 v[0:3], v69 offset:0\n\t" \
  "ds_read_b128 v[4:7], v69 offset:1024\n\t" \
  "ds_read_b128 v[8:11], v69 offset:2048\n\t" \
  "ds_read_b128 v[12:15], v69 offset:3072\n\t" \
  "s_waitcnt lgkmcnt(3)\n\t" \
  "v_smfmac_f32_16x16x64_bf16 v[36:39], v[0:3], v[44:51], v68\n\t" \
  "ds_read_b128 v[44:47], v70 offset:4096\n\t" \
  "v_smfmac_f32_16x16x64_bf16 v[40:43], v[0:3], v[60:67], v68\n\t" \
  "ds_read_b128 v[48:51], v70 offset:5120\n\t" \
  "s_waitcnt lgkmcnt(4)\n\t" \
  "v_smfmac_f32_16x16x64_bf16 v[36:39], v[4:7], a[32:39], v68\n\t" \
  "ds_read_b128 v[60:63], v70 offset:6144\n\t" \
  "v_smfmac_f32_16x16x64_bf16 v[40:43], v[4:7], a[40:47], v68\n\t" \
  "ds_read_b128 v[64:67], v70 offset:7168\n\t" \
  "s_waitcnt lgkmcnt(5)\n\t" \
  "v_smfmac_f32_16x16x64_bf16 v[36:39], v[8:11], a[80:87], v68\n\t" \
  "s_waitcnt vmcnt(4)\n\t" \
  "s_barrier\n\t" \
  "ds_read_b128 v[16:19], v69 offset:4096\n\t" \
  "v_smfmac_f32_16x16x64_bf16 v[40:43], v[8:11], a[88:95], v68\n\t" \
  "ds_read_b128 v[20:23], v69 offset:5120\n\t" \
  "s_waitcnt lgkmcnt(6)\n\t" \
  "v_smfmac_f32_16x16x64_bf16 v[36:39], v[12:15], a[120:127], v68\n\t" \
  "ds_read_b128 v[24:27], v69 offset:6144\n\t" \
  "s_waitcnt lgkmcnt(5)\n\t" \
  "v_smfmac_f32_16x16x64_bf16 v[40:43], v[12:15], v[44:51], v68\n\t" \
  "ds_read_b128 v[28:31], v69 offset:7168\n\t" \
  "s_waitcnt lgkmcnt(3)\n\t" \
  "v_smfmac_f32_16x16x64_bf16 v[36:39], v[16:19], a[160:167], v68\n\t" \
  "ds_read_b128 v[32:35], v69 offset:8192\n\t" \
  "v_smfmac_f32_16x16x64_bf16 v[40:43], v[16:19], a[168:175], v68\n\t" \
  "s_waitcnt lgkmcnt(3)\n\t" \
  "v_mov_b64 v[44:45], 0\n\t" \
  "v_smfmac_f32_16x16x64_bf16 v[36:39], v[20:23], a[200:207], v68\n\t" \
  "s_waitcnt lgkmcnt(2)\n\t" \
  "v_mov_b64 v[46:47], 0\n\t" \
  "v_smfmac_f32_16x16x64_bf16 v[36:39], v[24:27], a[240:247], v68\n\t" \
  "s_waitcnt lgkmcnt(1)\n\t" \
  "v_mov_b64 v[48:49], 0\n\t" \
  "v_smfmac_f32_16x16x64_bf16 v[36:39], v[28:31], v[112:119], v68\n\t" \
  "s_waitcnt lgkmcnt(0)\n\t" \
  "v_mov_b64 v[50:51], 0\n\t" \
  "v_smfmac_f32_16x16x64_bf16 v[36:39], v[32:35], v[160:167], v68\n\t" \
  "v_smfmac_f32_16x16x64_bf16 v[40:43], v[20:23], v[60:67], v68\n\t" \
  "ds_read_b128 v[60:63], v70 offset:8192\n\t" \
  "v_smfmac_f32_16x16x64_bf16 v[40:43], v[24:27], a[248:255], v68\n\t" \
  "ds_read_b128 v[64:67], v70 offset:9216\n\t" \
  "v_add_f32 v74, v36, v38\n\t" \
  "v_smfmac_f32_16x16x64_bf16 v[40:43], v[28:31], v[120:127], v68\n\t" \
  "v_add_f32 v37, v37, v39\n\t" \
  "v_smfmac_f32_16x16x64_bf16 v[40:43], v[32:35], v[168:175], v68\n\t" \
  "v_cmp_u_f32 vcc, v74, v37\n\t" \
  "v_smfmac_f32_16x16x64_bf16 v[44:47], v[0:3], a[0:7], v68\n\t" \
  "s_cbranch_vccnz LFAIL0_%=\n\t" \
  "LGO0_%=:\n\t" \
  "v_cvt_pk_bf16_f32 v74, v74, v37\n\t" \
  "v_smfmac_f32_16x16x64_bf16 v[44:47], v[4:7], a[48:55], v68\n\t" \
  "s_waitcnt lgkmcnt(0)\n\t" \
  "v_add_f32 v75, v40, v42\n\t" \
  "v_smfmac_f32_16x16x64_bf16 v[44:47], v[8:11], v[60:67], v68\n\t" \
  "ds_read_b128 v[60:63], v70 offset:10240\n\t" \
  "v_add_f32 v41, v41, v43\n\t" \
  "v_cvt_pk_bf16_f32 v75, v75, v41\n\t" \
  "v_smfmac_f32_16x16x64_bf16 v[44:47], v[12:15], a[128:135], v68\n\t" \
  "ds_write2st64_b32 v71, v74, v75 offset0:36 offset1:38\n\t" \
  "v_smfmac_f32_16x16x64_bf16 v[44:47], v[16:19], a[176:183], v68\n\t" \
  "ds_read_b128 v[64:67], v70 offset:11264\n\t" \
  "v_smfmac_f32_16x16x64_bf16 v[44:47], v[20:23], a[208:215], v68\n\t" \
  "v_smfmac_f32_16x16x64_bf16 v[44:47], v[24:27], v[80:87], v68\n\t" \
  "v_smfmac_f32_16x16x64_bf16 v[44:47], v[28:31], v[128:135], v68\n\t" \
  "s_waitcnt lgkmcnt(0)\n\t" \
  "v_smfmac_f32_16x16x64_bf16 v[44:47], v[32:35], v[60:67], v68\n\t" \
  "buffer_load_dwordx4 v[60:63], v69, s[28:31], s64 offen sc1\n\t" \
  "s_barrier\n\t" \
  "ds_read_b64_tr_b16 v[76:77], v72 offset:9216\n\t" \
  "v_smfmac_f32_16x16x64_bf16 v[48:51], v[0:3], a[8:15], v68\n\t" \
  "ds_read_b64_tr_b16 v[78:79], v72 offset:9728\n\t" \
  "v_smfmac_f32_16x16x64_bf16 v[48:51], v[4:7], a[56:63], v68\n\t" \
  "v_add_f32 v74, v44, v46\n\t" \
  "v_smfmac_f32_16x16x64_bf16 v[48:51], v[8:11], a[96:103], v68\n\t" \
  "v_add_f32 v45, v45, v47\n\t" \
  "v_smfmac_f32_16x16x64_bf16 v[48:51], v[12:15], a[136:143], v68\n\t" \
  "s_waitcnt vmcnt(4)\n\t" \
  "s_waitcnt lgkmcnt(0)\n\t" \
  "v_cvt_pk_bf16_f32 v74, v74, v45\n\t" \
  "v_mfma_f32_16x16x32_bf16 v[36:39], v[52:55], v[76:79], 0\n\t" \
  "buffer_load_dwordx4 v[64:67], v69, s[28:31], s64 offen offset:1024 sc1\n\t" \
  "s_waitcnt vmcnt(4)\n\t" \
  "v_mfma_f32_16x16x32_bf16 v[40:43], v[56:59], v[76:79], 0\n\t" \
  "v_mov_b64 v[52:53], 0\n\t" \
  "v_mov_b64 v[54:55], 0\n\t" \
  "v_smfmac_f32_16x16x64_bf16 v[48:51], v[16:19], a[184:191], v68\n\t" \
  "v_mov_b64 v[56:57], 0\n\t" \
  "v_mov_b64 v[58:59], 0\n\t" \
  "v_smfmac_f32_16x16x64_bf16 v[48:51], v[20:23], a[216:223], v68\n\t" \
  "ds_write_b32 v71, v74 offset:0\n\t" \
  "v_add_f32_dpp v36, v40, v36 quad_perm:[2,3,0,1] row_mask:0xf bank_mask:0xf\n\t" \
  "v_smfmac_f32_16x16x64_bf16 v[48:51], v[24:27], v[88:95], v68\n\t" \
  "v_add_f32_dpp v37, v41, v37 quad_perm:[2,3,0,1] row_mask:0xf bank_mask:0xf\n\t" \
  "v_smfmac_f32_16x16x64_bf16 v[48:51], v[28:31], v[136:143], v68\n\t" \
  "v_add_f32_dpp v38, v42, v38 quad_perm:[2,3,0,1] row_mask:0xf bank_mask:0xf\n\t" \
  "v_smfmac_f32_16x16x64_bf16 v[48:51], v[32:35], v[176:183], v68\n\t" \
  "v_add_f32_dpp v39, v43, v39 quad_perm:[2,3,0,1] row_mask:0xf bank_mask:0xf\n\t" \
  "v_smfmac_f32_16x16x64_bf16 v[52:55], v[0:3], a[16:23], v68\n\t" \
  "s_mov_b64 exec, s[58:59]\n\t" \
  "ds_write_b128 v69, v[36:39] offset:2048\n\t" \
  "s_mov_b64 exec, -1\n\t" \
  "v_smfmac_f32_16x16x64_bf16 v[52:55], v[4:7], a[64:71], v68\n\t" \
  "v_add_f32 v75, v48, v50\n\t" \
  "v_add_f32 v49, v49, v51\n\t" \
  "v_cvt_pk_bf16_f32 v75, v75, v49\n\t" \
  "v_smfmac_f32_16x16x64_bf16 v[56:59], v[0:3], a[24:31], v68\n\t" \
  "ds_write_b32 v71, v75 offset:512\n\t" \
  "v_smfmac_f32_16x16x64_bf16 v[56:59], v[4:7], a[72:79], v68\n\t" \
  "ds_read_b64_tr_b16 v[0:1], v72 offset:10240\n\t" \
  "ds_read_b64_tr_b16 v[2:3], v72 offset:10752\n\t" \
  "v_smfmac_f32_16x16x64_bf16 v[52:55], v[8:11], a[104:111], v68\n\t" \
  "ds_read_b128 v[36:39], v70 offset:12288\n\t" \
  "v_smfmac_f32_16x16x64_bf16 v[56:59], v[8:11], a[112:119], v68\n\t" \
  "buffer_load_dword off, s[28:31], s87 sc1 lds\n\t" \
  "ds_read_b128 v[40:43], v70 offset:13312\n\t" \
  "v_smfmac_f32_16x16x64_bf16 v[52:55], v[12:15], a[144:151], v68\n\t" \
  "s_waitcnt lgkmcnt(4)\n\t" \
  "s_barrier\n\t" \
  "ds_read_b64_tr_b16 v[44:45], v72 offset:0\n\t" \
  "ds_read_b64_tr_b16 v[46:47], v72 offset:512\n\t" \
  "v_smfmac_f32_16x16x64_bf16 v[56:59], v[12:15], a[152:159], v68\n\t" \
  "ds_read_b64_tr_b16 v[48:49], v72 offset:1024\n\t" \
  "ds_read_b64_tr_b16 v[50:51], v72 offset:1536\n\t" \
  "v_smfmac_f32_16x16x64_bf16 v[52:55], v[16:19], a[192:199], v68\n\t" \
  "ds_read_b128 v[4:7], v69 offset:2048\n\t" \
  "s_waitcnt lgkmcnt(5)\n\t" \
  "v_smfmac_f32_16x16x64_bf16 v[56:59], v[16:19], v[36:43], v68\n\t" \
  "ds_read_b128 v[8:11], v69 offset:3072\n\t" \
  "s_waitcnt lgkmcnt(4)\n\t" \
  "v_mfma_f32_16x16x32_bf16 v[12:15], v[44:47], v[76:79], 0\n\t" \
  "ds_read_b128 v[36:39], v70 offset:14336\n\t" \
  "s_waitcnt lgkmcnt(3)\n\t" \
  "v_mfma_f32_16x16x32_bf16 v[12:15], v[48:51], v[0:3], v[12:15]\n\t" \
  "ds_read_b128 v[40:43], v70 offset:15360\n\t" \
  "v_smfmac_f32_16x16x64_bf16 v[52:55], v[20:23], a[224:231], v68\n\t" \
  "s_waitcnt lgkmcnt(2)\n\t" \
  "v_add_f32 v4, v4, v8\n\t" \
  "v_add_f32 v5, v5, v9\n\t" \
  "v_add_f32 v6, v6, v10\n\t" \
  "v_cndmask_b32 v78, v13, v14, s[56:57]\n\t" \
  "v_cndmask_b32 v76, v12, v15, s[56:57]\n\t" \
  "v_add_f32 v7, v7, v11\n\t" \
  "v_max3_f32 v77, v4, v5, v6\n\t" \
  "v_add_f32_dpp v76, v78, v76 quad_perm:[2,3,0,1] row_mask:0xf bank_mask:0xf\n\t" \
  "v_max3_f32 v77, v77, v7, v76\n\t" \
  "v_smfmac_f32_16x16x64_bf16 v[56:59], v[20:23], a[232:239], v68\n\t" \
  "ds_read_b128 v[44:47], v70 offset:16384\n\t" \
  "v_max_f32_dpp v77, v77, v77 quad_perm:[2,3,0,1] row_mask:0xf bank_mask:0xf\n\t" \
  "v_mov_b32 v78, v77\n\t" \
  "v_smfmac_f32_16x16x64_bf16 v[52:55], v[24:27], v[96:103], v68\n\t" \
  "ds_read_b128 v[48:51], v70 offset:17408\n\t" \
  "v_permlane16_swap_b32 v77, v78\n\t" \
  "v_max_f32 v77, v77, v78\n\t" \
  "v_mov_b32 v78, v77\n\t" \
  "v_smfmac_f32_16x16x64_bf16 v[52:55], v[28:31], v[144:151], v68\n\t" \
  "ds_read_b128 v[16:19], v70 offset:18432\n\t" \
  "v_permlane32_swap_b32 v77, v78\n\t" \
  "v_max_f32 v77, v77, v78\n\t" \
  "v_smfmac_f32_16x16x64_bf16 v[52:55], v[32:35], v[184:191], v68\n\t" \
  "ds_read_b128 v[20:23], v70 offset:19456\n\t" \
  "v_mul_f32 v77, 0xbe0293ee, v77\n\t" \
  "v_smfmac_f32_16x16x64_bf16 v[56:59], v[24:27], v[104:111], v68\n\t" \
  "v_fmamk_f32 v4, v4, 0x3e0293ee, v77\n\t" \
  "v_smfmac_f32_16x16x64_bf16 v[56:59], v[28:31], v[152:159], v68\n\t" \
  "ds_read_b128 v[24:27], v70 offset:22528\n\t" \
  "s_waitcnt lgkmcnt(4)\n\t" \
  "v_fmamk_f32 v5, v5, 0x3e0293ee, v77\n\t" \
  "v_smfmac_f32_16x16x64_bf16 v[56:59], v[32:35], v[36:43], v68\n\t" \
  "ds_read_b128 v[28:31], v70 offset:23552\n\t" \
  "v_fmamk_f32 v6, v6, 0x3e0293ee, v77\n\t" \
  "v_fmamk_f32 v7, v7, 0x3e0293ee, v77\n\t" \
  "v_fmamk_f32 v76, v76, 0x3e0293ee, v77\n\t" \
  "v_exp_f32 v4, v4\n\t" \
  "v_exp_f32 v5, v5\n\t" \
  "v_exp_f32 v6, v6\n\t" \
  "v_exp_f32 v7, v7\n\t" \
  "v_cvt_pk_bf16_f32 v4, v4, v5\n\t" \
  "v_exp_f32 v76, v76\n\t" \
  "v_cvt_pk_bf16_f32 v5, v6, v7\n\t" \
  "v_mov_b32_dpp v12, v4 quad_perm:[0,1,0,1] row_mask:0xf bank_mask:0xf\n\t" \
  "v_mov_b32_dpp v14, v4 quad_perm:[2,3,2,3] row_mask:0xf bank_mask:0xf\n\t" \
  "v_mov_b32_dpp v13, v5 quad_perm:[0,1,0,1] row_mask:0xf bank_mask:0xf\n\t" \
  "v_mov_b32_dpp v15, v5 quad_perm:[2,3,2,3] row_mask:0xf bank_mask:0xf\n\t" \
  "v_add_f32 v52, v52, v54\n\t" \
  "v_add_f32 v53, v53, v55\n\t" \
  "s_waitcnt vmcnt(2)\n\t" \
  "v_mfma_f32_16x16x32_bf16 v[0:3], v[60:63], v[12:15], 0\n\t" \
  "ds_read_b128 v[36:39], v70 offset:20480\n\t" \
  "v_add_f32 v56, v56, v58\n\t" \
  "v_add_f32 v57, v57, v59\n\t" \
  "v_cvt_pk_bf16_f32 v52, v52, v53\n\t" \
  "v_cvt_pk_bf16_f32 v53, v56, v57\n\t" \
  "s_waitcnt vmcnt(1)\n\t" \
  "v_mfma_f32_16x16x32_bf16 v[4:7], v[64:67], v[12:15], 0\n\t" \
  "buffer_store_dwordx2 v[52:53], v71, s[28:31], s69 offen offset:2048 sc1\n\t" \
  "ds_read_b128 v[40:43], v70 offset:21504\n\t" \
  "v_mov_b64 v[56:57], s[88:89]\n\t" \
  "v_mov_b64 v[58:59], s[88:89]\n\t" \
  "v_mov_b32_dpp v78, v76 quad_perm:[0,1,0,1] row_mask:0xf bank_mask:0xf\n\t" \
  "v_mov_b32_dpp v79, v76 quad_perm:[2,3,2,3] row_mask:0xf bank_mask:0xf\n\t" \
  "v_mfma_f32_16x16x32_bf16 v[32:35], v[56:59], v[12:15], 0\n\t" \
  "ds_read_b128 v[60:63], v70 offset:24576\n\t" \
  "v_cvt_pk_bf16_f32 v11, v78, v79\n\t" \
  "v_cvt_pk_bf16_f32 v8, v78, v79\n\t" \
  "v_mov_b64 v[54:55], 0\n\t" \
  "v_mov_b32 v9, 0\n\t" \
  "v_mov_b32 v10, 0\n\t" \
  "v_mov_b64 v[14:15], 0\n\t" \
  "s_nop 0\n\t" \
  "v_mfma_f32_16x16x32_bf16 v[32:35], v[54:57], v[8:11], v[32:35]\n\t" \
  "s_cmp_eq_u32 s48, 63\n\t" \
  "v_mfma_f32_16x16x32_bf16 v[0:3], v[52:55], v[8:11], v[0:3]\n\t" \
  "ds_read_b128 v[64:67], v70 offset:25600\n\t" \
  "v_mov_b64 v[56:57], 0\n\t" \
  "v_mov_b64 v[58:59], 0\n\t" \
  "v_mfma_f32_16x16x32_bf16 v[4:7], v[52:55], v[10:13], v[4:7]\n\t" \
  "s_cbranch_scc1 LLAST0_%=\n\t" \
  "v_rcp_f32 v74, v32\n\t" \
  "v_mov_b64 v[12:13], 0\n\t" \
  "v_mov_b32 v8, 0\n\t" \
  "v_pk_mul_f32 v[0:1], v[0:1], v[74:75] op_sel_hi:[1,0]\n\t" \
  "v_pk_mul_f32 v[2:3], v[2:3], v[74:75] op_sel_hi:[1,0]\n\t" \
  "v_cvt_pk_bf16_f32 v76, v0, v1\n\t" \
  "v_cvt_pk_bf16_f32 v77, v2, v3\n\t" \
  "v_pk_mul_f32 v[4:5], v[4:5], v[74:75] op_sel_hi:[1,0]\n\t" \
  "v_pk_mul_f32 v[6:7], v[6:7], v[74:75] op_sel_hi:[1,0]\n\t" \
  "v_cvt_pk_bf16_f32 v78, v4, v5\n\t" \
  "v_cvt_pk_bf16_f32 v79, v6, v7\n\t" \
  "ds_write_b128 v69, v[76:79] offset:4096\n\t" \
  "s_waitcnt lgkmcnt(2)\n\t" \
  "v_smfmac_f32_16x16x64_bf16 v[12:15], v[76:79], v[44:51], v68\n\t" \
  "ds_read_b128 v[44:47], v70 offset:26624\n\t" \
  "v_mov_b32 v11, 0\n\t" \
  "v_smfmac_f32_16x16x64_bf16 v[56:59], v[76:79], v[16:23], v68\n\t" \
  "ds_read_b128 v[48:51], v70 offset:27648\n\t" \
  "v_mov_b64 v[52:53], 0\n\t" \
  "v_smfmac_f32_16x16x64_bf16 v[8:11], v[76:79], v[36:43], v68\n\t" \
  "ds_read_b128 v[16:19], v70 offset:28672\n\t" \
  "v_smfmac_f32_16x16x64_bf16 v[52:55], v[76:79], v[24:31], v68\n\t" \
  "s_waitcnt lgkmcnt(3)\n\t" \
  "s_barrier\n\t" \
  "ds_read_b128 v[0:3], v69 offset:5120\n\t" \
  "ds_read_b128 v[4:7], v69 offset:6144\n\t" \
  "v_lshrrev_b32 v71, 7, v69\n\t" \
  "v_and_b32 v74, 0x70, v69\n\t" \
  "v_lshl_add_u32 v71, v71, 14, v74\n\t" \
  "v_bfe_u32 v72, v69, 2, 7\n\t" \
  "v_and_b32 v74, 0x200, v69\n\t" \
  "s_waitcnt lgkmcnt(1)\n\t" \
  "v_smfmac_f32_16x16x64_bf16 v[8:11], v[0:3], v[60:67], v68\n\t" \
  "ds_read_b128 v[20:23], v70 offset:29696\n\t" \
  "v_mov_b64 v[76:77], 0\n\t" \
  "v_smfmac_f32_16x16x64_bf16 v[12:15], v[0:3], v[200:207], v68\n\t" \
  "ds_read_b128 v[32:35], v69 offset:7168\n\t" \
  "v_mov_b64 v[78:79], 0\n\t" \
  "v_smfmac_f32_16x16x64_bf16 v[52:55], v[0:3], v[44:51], v68\n\t" \
  "ds_read_b128 v[24:27], v70 offset:30720\n\t" \
  "v_lshl_add_u32 v72, v74, 3, v72\n\t" \
  "v_smfmac_f32_16x16x64_bf16 v[56:59], v[0:3], v[232:239], v68\n\t" \
  "buffer_load_dword v75, off, s[28:31], s80 sc1\n\t" \
  "ds_read_b128 v[28:31], v70 offset:31744\n\t" \
  "s_waitcnt lgkmcnt(4)\n\t" \
  "v_smfmac_f32_16x16x64_bf16 v[12:15], v[4:7], v[208:215], v68\n\t" \
  "ds_read_b128 v[60:63], v70 offset:32768\n\t" \
  "s_waitcnt lgkmcnt(4)\n\t" \
  "v_smfmac_f32_16x16x64_bf16 v[8:11], v[4:7], v[16:23], v68\n\t" \
  "ds_read_b128 v[64:67], v70 offset:33792\n\t" \
  "s_waitcnt lgkmcnt(4)\n\t" \
  "v_smfmac_f32_16x16x64_bf16 v[8:11], v[32:35], v[192:199], v68\n\t" \
  "ds_read_b128 v[36:39], v73 offset:4096\n\t" \
  "v_smfmac_f32_16x16x64_bf16 v[12:15], v[32:35], v[216:223], v68\n\t" \
  "ds_read_b128 v[40:43], v73 offset:6144\n\t" \
  "s_waitcnt lgkmcnt(4)\n\t" \
  "v_smfmac_f32_16x16x64_bf16 v[52:55], v[4:7], v[24:31], v68\n\t" \
  "ds_read_b128 v[44:47], v70 offset:34816\n\t" \
  "v_cvt_pk_bf16_f32 v8, v8, v9\n\t" \
  "v_smfmac_f32_16x16x64_bf16 v[52:55], v[32:35], v[224:231], v68\n\t" \
  "ds_read_b128 v[48:51], v70 offset:35840\n\t" \
  "v_cvt_pk_bf16_f32 v9, v10, v11\n\t" \
  "v_smfmac_f32_16x16x64_bf16 v[56:59], v[4:7], v[240:247], v68\n\t" \
  "s_waitcnt lgkmcnt(4)\n\t" \
  "v_cvt_pk_bf16_f32 v10, v12, v13\n\t" \
  "v_smfmac_f32_16x16x64_bf16 v[56:59], v[32:35], v[60:67], v68\n\t" \
  "s_waitcnt lgkmcnt(3)\n\t" \
  "v_cvt_pk_bf16_f32 v11, v14, v15\n\t" \
  "v_smfmac_f32_16x16x64_bf16 v[76:79], v[36:39], v[248:255], v68\n\t" \
  "buffer_store_dwordx4 v[8:11], v71, s[28:31], s54 offen sc1\n\t" \
  "s_waitcnt lgkmcnt(0)\n\t" \
  "v_cvt_pk_bf16_f32 v52, v52, v53\n\t" \
  "v_smfmac_f32_16x16x64_bf16 v[76:79], v[40:43], v[44:51], v68\n\t" \
  "v_cvt_pk_bf16_f32 v53, v54, v55\n\t" \
  "v_cvt_pk_bf16_f32 v54, v56, v57\n\t" \
  "v_cvt_pk_bf16_f32 v55, v58, v59\n\t" \
  "buffer_store_dwordx4 v[52:55], v71, s[28:31], s75 offen sc1\n\t" \
  "s_nop 2\n\t" \
  "v_add_f32 v76, v76, v78\n\t" \
  "v_add_f32 v77, v77, v79\n\t" \
  "v_cvt_pk_bf16_f32 v76, v76, v77\n\t" \
  "buffer_store_dword v76, v72, s[28:31], s55 offen sc1\n\t" \
  "s_memrealtime s[70:71]\n\t" \
  "s_waitcnt vmcnt(3)\n\t" \
  "v_readfirstlane_b32 s65, v75\n\t" \
  "s_barrier\n\t" \
  "s_waitcnt lgkmcnt(0)\n\t" \
  "s_not_b32 s65, s65\n\t" \
  "s_lshl_b32 s61, s70, 8\n\t" \
  "s_andn2_b32 s64, s65, 0xff\n\t" \
  "s_sub_u32 s64, s64, s61\n\t" \
  "s_ashr_i32 s64, s64, 8\n\t" \
  "s_cmp_eq_u32 s65, 0\n\t" \
  "s_cselect_b32 s64, " CH_DLF ", s64\n\t" \
  "s_cselect_b32 s65, " CH_B0 ", s65\n\t" \
  "s_add_u32 s47, s64, s70\n\t" \
  "s_min_i32 s64, s64, s34\n\t" \
  "s_max_i32 s64, s64, " CH_DLL1 "\n\t" \
  "s_add_u32 s64, s64, s70\n\t" \
  "s_add_u32 s55, s64, " CH_FDL "\n\t" \
  "s_movk_i32 s60, 8192\n\t" \
  "FWT_%=:\n\t" \
  "s_memrealtime s[62:63]\n\t" \
  "s_sub_u32 s60, s60, 1\n\t" \
  "s_cselect_b32 s61, 0, -1\n\t" \
  "s_waitcnt lgkmcnt(0)\n\t" \
  "s_sub_i32 s63, s62, s55\n\t" \
  "s_and_b32 s63, s63, s61\n\t" \
  "s_cmp_lt_i32 s63, 0\n\t" \
  "s_cbranch_scc1 FWT_%=\n\t" \
  "s_mov_b64 exec, 1\n\t" \
  "buffer_store_dword v68, off, s[28:31], s51 offset:4064 sc1\n\t" \
  "s_mov_b64 exec, -1\n\t" \
  "s_and_b32 s65, s65, 0xff\n\t" \
  "s_add_u32 s47, s47, s65\n\t" \
  "s_add_u32 s65, s65, s64\n\t" \
  "s_sub_u32 s60, s70, s66\n\t" \
  "s_add_u32 s61, s81, 1\n\t" \
  "s_min_u32 s60, s60, s61\n\t" \
  "s_cmp_eq_u32 s68, 0\n\t" \
  "s_cselect_b32 s81, s60, s81\n\t" \
  "s_add_u32 s48, s48, 1\n\t" \
  "s_mov_b32 s49, s53\n\t" \
  "s_add_u32 s53, s53, " CH_SLOT "\n\t" \
  "s_add_u32 s60, s53, " CH_SKIP "\n\t" \
  "s_lshr_b32 s61, s96, 1\n\t" \
  "s_add_u32 s86, s86, 1\n\t" \
  "s_cmp_eq_u32 s86, 3\n\t" \
  "s_cselect_b32 s53, s60, s53\n\t" \
  "s_cselect_b32 s96, s61, s96\n\t" \
  "s_cselect_b32 s86, 0, s86\n\t" \
  "s_cmp_ge_u32 s53, " CH_RING "\n\t" \
  "s_cselect_b32 s53, 0, s53\n\t" \
  "s_cselect_b32 s96, s21, s96\n\t" \
  "s_andn2_b32 s53, s53, 0x2000\n\t" \
  "s_and_b32 s60, s96, 1\n\t" \
  "s_lshl_b32 s60, s60, 13\n\t" \
  "s_or_b32 s53, s53, s60\n\t" \
  "s_mov_b32 s68, 0\n\t" \
  "s_add_u32 s82, s48, 0\n\t" \
  "s_and_b32 s82, s82, " CH_TRM "\n\t" \
  "s_lshr_b64 s[60:61], s[90:91], s82\n\t" \
  "s_and_b32 s60, s60, 1\n\t" \
  "s_lshl_b32 s60, s60, 13\n\t" \
  "s_add_u32 s82, s82, " CH_SG0 "\n\t" \
  "s_lshl_b32 s82, s82, 21\n\t" \
  "s_or_b32 s82, s82, s60\n\t" \
  "s_add_u32 s50, s82, s38\n\t" \
  "s_add_u32 s69, s82, s39\n\t" \
  "s_add_u32 s82, s48, 0\n\t" \
  "s_lshr_b64 s[60:61], s[90:91], s82\n\t" \
  "s_and_b32 s60, s60, 1\n\t" \
  "s_lshl_b32 s60, s60, 13\n\t" \
  "s_add_u32 s82, s82, " CH_SG0 "\n\t" \
  "s_lshl_b32 s82, s82, 21\n\t" \
  "s_or_b32 s82, s82, s60\n\t" \
  "s_add_u32 s51, s82, s43\n\t" \
  "s_cmp_lg_u32 s34, 0\n\t" \
  "s_cbranch_scc1 LW0_%=\n\t" \
  "s_barrier\n\t" \
  "s_branch LINI_%=\n\t" \
  "LFAIL0_%=:\n\t" \
  "v_max_u32 v75, v0, v1\n\t" \
  "v_max3_u32 v75, v75, v2, v3\n\t" \
  "v_max3_u32 v75, v75, v4, v5\n\t" \
  "v_max3_u32 v75, v75, v6, v7\n\t" \
  "v_max3_u32 v75, v75, v8, v9\n\t" \
  "v_max3_u32 v75, v75, v10, v11\n\t" \
  "v_max3_u32 v75, v75, v12, v13\n\t" \
  "v_max3_u32 v75, v75, v14, v15\n\t" \
  "v_max3_u32 v75, v75, v16, v17\n\t" \
  "v_max3_u32 v75, v75, v18, v19\n\t" \
  "v_max3_u32 v75, v75, v20, v21\n\t" \
  "v_max3_u32 v75, v75, v22, v23\n\t" \
  "v_max3_u32 v75, v75, v24, v25\n\t" \
  "v_max3_u32 v75, v75, v26, v27\n\t" \
  "v_max3_u32 v75, v75, v28, v29\n\t" \
  "v_max3_u32 v75, v75, v30, v31\n\t" \
  "v_max3_u32 v75, v75, v32, v33\n\t" \
  "v_max3_u32 v75, v75, v34, v35\n\t" \
  "v_cmp_eq_u32 vcc, -1, v75\n\t" \
  "s_cbranch_vccz LGO0_%=\n\t" \
  "s_add_u32 s68, s68, 1\n\t" \
  "s_waitcnt lgkmcnt(0)\n\t" \
  "s_cmp_gt_u32 s68, s83\n\t" \
  "s_cbranch_scc1 LGV0_%=\n\t" \
  "s_barrier\n\t" \
  "s_memrealtime s[62:63]\n\t" \
  "s_mov_b32 m0, 0\n\t" \
  "s_waitcnt lgkmcnt(0)\n\t" \
  "s_add_u32 s65, s62, " CH_PEN "\n\t" \
  "s_branch LDMA_%=\n\t" \
  "LWT_%=f:\n\t" \
  "s_cmp_lt_i32 s63, -2\n\t" \
  "s_cbranch_scc0 LWT_%=g\n\t" \
  "s_nop 4\n\t" \
  "s_branch LWT_%=g\n\t" \
  "LGV0_%=:\n\t" \
  "s_mov_b32 s83, 0\n\t" \
  "s_lshl_b32 s60, s36, 4\n\t" \
  "s_or_b32 s60, s60, s48\n\t" \
  "s_or_b32 s60, s60, 805306368\n\t" \
  "v_mov_b32 v76, s60\n\t" \
  "v_mov_b32 v77, 0\n\t" \
  "s_mov_b64 exec, 1\n\t" \
  "buffer_atomic_cmpswap v[76:77], off, s[28:31], s92 sc1\n\t" \
  "s_mov_b64 exec, -1\n\t" \
  "s_branch LGO0_%=\n\t" \
  "LLAST0_%=:\n\t" \
  "s_waitcnt vmcnt(0) lgkmcnt(0)\n\t" \
  "s_barrier\n\t" \
  "s_mov_b64 exec, 1\n\t" \
  "buffer_store_dword v68, off, s[28:31], s51 offset:4064 sc1\n\t" \
  "s_mov_b64 exec, -1\n\t" \
  "s_branch LEND_%=\n\t" \
  ".p2align 6\n\t" \
  "LG1_%=:\n\t" \
  "s_barrier\n\t" \
  "buffer_load_dwordx4 v[52:55], v69, s[28:31], s50 offen sc1\n\t" \
  "buffer_load_dwordx4 v[56:59], v69, s[28:31], s50 offen offset:1024 sc1\n\t" \
  "s_add_u32 s64, s50, 14336\n\t" \
  "s_movk_i32 m0, 11264\n\t" \
  "v_lshlrev_b32 v75, 1, v69\n\t" \
  "buffer_load_dword v75, s[28:31], s64 offen sc1 lds\n\t" \
  "ds_read_b128 v[44:47], v70 offset:0\n\t" \
  "ds_read_b128 v[48:51], v70 offset:1024\n\t" \
  "ds_read_b128 v[60:63], v70 offset:2048\n\t" \
  "ds_read_b128 v[64:67], v70 offset:3072\n\t" \
  "v_mov_b64 v[36:37], 0\n\t" \
  "v_mov_b64 v[38:39], 0\n\t" \
  "v_mov_b64 v[40:41], 0\n\t" \
  "v_mov_b64 v[42:43], 0\n\t" \
  "v_lshrrev_b32 v71, 1, v69\n\t" \
  "v_add_u32 v71, 4, v71\n\t" \
  "v_lshrrev_b32 v72, 3, v69\n\t" \
  "v_and_b32 v72, 0x78, v72\n\t" \
  "v_and_b32 v74, 0x30, v69\n\t" \
  "v_lshl_add_u32 v72, v74, 3, v72\n\t" \
  "s_add_u32 s54, s53, s36\n\t" \
  "s_add_u32 s55, s54, 540672\n\t" \
  "s_add_u32 s54, s54, 4096\n\t" \
  "s_add_u32 s75, s54, 262144\n\t" \
  "s_add_u32 s80, s53, s26\n\t" \
  "s_barrier\n\t" \
  "ds_read_b128 v[0:3], v69 offset:0\n\t" \
  "ds_read_b128 v[4:7], v69 offset:1024\n\t" \
  "ds_read_b128 v[8:11], v69 offset:2048\n\t" \
  "ds_read_b128 v[12:15], v69 offset:3072\n\t" \
  "s_waitcnt lgkmcnt(3)\n\t" \
  "v_smfmac_f32_16x16x64_bf16 v[36:39], v[0:3], v[44:51], v68\n\t" \
  "ds_read_b128 v[44:47], v70 offset:4096\n\t" \
  "v_smfmac_f32_16x16x64_bf16 v[40:43], v[0:3], v[60:67], v68\n\t" \
  "ds_read_b128 v[48:51], v70 offset:5120\n\t" \
  "s_waitcnt lgkmcnt(4)\n\t" \
  "v_smfmac_f32_16x16x64_bf16 v[36:39], v[4:7], a[32:39], v68\n\t" \
  "ds_read_b128 v[60:63], v70 offset:6144\n\t" \
  "v_smfmac_f32_16x16x64_bf16 v[40:43], v[4:7], a[40:47], v68\n\t" \
  "ds_read_b128 v[64:67], v70 offset:7168\n\t" \
  "s_waitcnt lgkmcnt(5)\n\t" \
  "v_smfmac_f32_16x16x64_bf16 v[36:39], v[8:11], a[80:87], v68\n\t" \
  "s_barrier\n\t" \
  "ds_read_b128 v[16:19], v69 offset:4096\n\t" \
  "v_smfmac_f32_16x16x64_bf16 v[40:43], v[8:11], a[88:95], v68\n\t" \
  "ds_read_b128 v[20:23], v69 offset:5120\n\t" \
  "s_waitcnt lgkmcnt(6)\n\t" \
  "v_smfmac_f32_16x16x64_bf16 v[36:39], v[12:15], a[120:127], v68\n\t" \
  "ds_read_b128 v[24:27], v69 offset:6144\n\t" \
  "s_waitcnt lgkmcnt(5)\n\t" \
  "v_smfmac_f32_16x16x64_bf16 v[40:43], v[12:15], v[44:51], v68\n\t" \
  "ds_read_b128 v[28:31], v69 offset:7168\n\t" \
  "s_waitcnt lgkmcnt(3)\n\t" \
  "v_smfmac_f32_16x16x64_bf16 v[36:39], v[16:19], a[160:167], v68\n\t" \
  "ds_read_b128 v[32:35], v69 offset:8192\n\t" \
  "v_smfmac_f32_16x16x64_bf16 v[40:43], v[16:19], a[168:175], v68\n\t" \
  "s_waitcnt lgkmcnt(3)\n\t" \
  "v_mov_b64 v[44:45], 0\n\t" \
  "v_smfmac_f32_16x16x64_bf16 v[36:39], v[20:23], a[200:207], v68\n\t" \
  "s_waitcnt lgkmcnt(2)\n\t" \
  "v_mov_b64 v[46:47], 0\n\t" \
  "v_smfmac_f32_16x16x64_bf16 v[36:39], v[24:27], a[240:247], v68\n\t" \
  "s_waitcnt lgkmcnt(1)\n\t" \
  "v_mov_b64 v[48:49], 0\n\t" \
  "v_smfmac_f32_16x16x64_bf16 v[36:39], v[28:31], v[112:119], v68\n\t" \
  "s_waitcnt lgkmcnt(0)\n\t" \
  "v_mov_b64 v[50:51], 0\n\t" \
  "v_smfmac_f32_16x16x64_bf16 v[36:39], v[32:35], v[160:167], v68\n\t" \
  "v_smfmac_f32_16x16x64_bf16 v[40:43], v[20:23], v[60:67], v68\n\t" \
  "ds_read_b128 v[60:63], v70 offset:8192\n\t" \
  "v_smfmac_f32_16x16x64_bf16 v[40:43], v[24:27], a[248:255], v68\n\t" \
  "ds_read_b128 v[64:67], v70 offset:9216\n\t" \
  "v_add_f32 v74, v36, v38\n\t" \
  "v_smfmac_f32_16x16x64_bf16 v[40:43], v[28:31], v[120:127], v68\n\t" \
  "v_add_f32 v37, v37, v39\n\t" \
  "v_smfmac_f32_16x16x64_bf16 v[40:43], v[32:35], v[168:175], v68\n\t" \
  "v_cmp_u_f32 vcc, v74, v37\n\t" \
  "v_smfmac_f32_16x16x64_bf16 v[44:47], v[0:3], a[0:7], v68\n\t" \
  "s_cbranch_vccnz LFAIL1_%=\n\t" \
  "LGO1_%=:\n\t" \
  "v_cvt_pk_bf16_f32 v74, v74, v37\n\t" \
  "v_smfmac_f32_16x16x64_bf16 v[44:47], v[4:7], a[48:55], v68\n\t" \
  "s_waitcnt lgkmcnt(0)\n\t" \
  "v_add_f32 v75, v40, v42\n\t" \
  "v_smfmac_f32_16x16x64_bf16 v[44:47], v[8:11], v[60:67], v68\n\t" \
  "ds_read_b128 v[60:63], v70 offset:10240\n\t" \
  "v_add_f32 v41, v41, v43\n\t" \
  "v_cvt_pk_bf16_f32 v75, v75, v41\n\t" \
  "v_smfmac_f32_16x16x64_bf16 v[44:47], v[12:15], a[128:135], v68\n\t" \
  "ds_write2st64_b32 v71, v74, v75 offset0:36 offset1:38\n\t" \
  "v_smfmac_f32_16x16x64_bf16 v[44:47], v[16:19], a[176:183], v68\n\t" \
  "ds_read_b128 v[64:67], v70 offset:11264\n\t" \
  "v_smfmac_f32_16x16x64_bf16 v[44:47], v[20:23], a[208:215], v68\n\t" \
  "v_smfmac_f32_16x16x64_bf16 v[44:47], v[24:27], v[80:87], v68\n\t" \
  "v_smfmac_f32_16x16x64_bf16 v[44:47], v[28:31], v[128:135], v68\n\t" \
  "s_waitcnt lgkmcnt(0)\n\t" \
  "v_smfmac_f32_16x16x64_bf16 v[44:47], v[32:35], v[60:67], v68\n\t" \
  "s_barrier\n\t" \
  "ds_read_b64_tr_b16 v[76:77], v72 offset:9216\n\t" \
  "v_smfmac_f32_16x16x64_bf16 v[48:51], v[0:3], a[8:15], v68\n\t" \
  "buffer_load_dwordx4 v[60:63], v69, s[28:31], s64 offen sc1\n\t" \
  "ds_read_b64_tr_b16 v[78:79], v72 offset:9728\n\t" \
  "v_smfmac_f32_16x16x64_bf16 v[48:51], v[4:7], a[56:63], v68\n\t" \
  "v_add_f32 v74, v44, v46\n\t" \
  "v_smfmac_f32_16x16x64_bf16 v[48:51], v[8:11], a[96:103], v68\n\t" \
  "v_add_f32 v45, v45, v47\n\t" \
  "v_smfmac_f32_16x16x64_bf16 v[48:51], v[12:15], a[136:143], v68\n\t" \
  "s_waitcnt vmcnt(3)\n\t" \
  "s_waitcnt lgkmcnt(0)\n\t" \
  "v_cvt_pk_bf16_f32 v74, v74, v45\n\t" \
  "v_mfma_f32_16x16x32_bf16 v[36:39], v[52:55], v[76:79], 0\n\t" \
  "s_waitcnt vmcnt(2)\n\t" \
  "v_mfma_f32_16x16x32_bf16 v[40:43], v[56:59], v[76:79], 0\n\t" \
  "buffer_load_dwordx4 v[64:67], v69, s[28:31], s64 offen offset:1024 sc1\n\t" \
  "v_mov_b64 v[52:53], 0\n\t" \
  "v_mov_b64 v[54:55], 0\n\t" \
  "v_smfmac_f32_16x16x64_bf16 v[48:51], v[16:19], a[184:191], v68\n\t" \
  "v_mov_b64 v[56:57], 0\n\t" \
  "v_mov_b64 v[58:59], 0\n\t" \
  "v_smfmac_f32_16x16x64_bf16 v[48:51], v[20:23], a[216:223], v68\n\t" \
  "ds_write_b32 v71, v74 offset:0\n\t" \
  "v_add_f32_dpp v36, v40, v36 quad_perm:[2,3,0,1] row_mask:0xf bank_mask:0xf\n\t" \
  "v_smfmac_f32_16x16x64_bf16 v[48:51], v[24:27], v[88:95], v68\n\t" \
  "v_add_f32_dpp v37, v41, v37 quad_perm:[2,3,0,1] row_mask:0xf bank_mask:0xf\n\t" \
  "v_smfmac_f32_16x16x64_bf16 v[48:51], v[28:31], v[136:143], v68\n\t" \
  "v_add_f32_dpp v38, v42, v38 quad_perm:[2,3,0,1] row_mask:0xf bank_mask:0xf\n\t" \
  "v_smfmac_f32_16x16x64_bf16 v[48:51], v[32:35], v[176:183], v68\n\t" \
  "v_add_f32_dpp v39, v43, v39 quad_perm:[2,3,0,1] row_mask:0xf bank_mask:0xf\n\t" \
  "v_smfmac_f32_16x16x64_bf16 v[52:55], v[0:3], a[16:23], v68\n\t" \
  "s_mov_b64 exec, s[58:59]\n\t" \
  "ds_write_b128 v69, v[36:39] offset:2080\n\t" \
  "s_mov_b64 exec, -1\n\t" \
  "v_smfmac_f32_16x16x64_bf16 v[52:55], v[4:7], a[64:71], v68\n\t" \
  "v_add_f32 v75, v48, v50\n\t" \
  "v_add_f32 v49, v49, v51\n\t" \
  "v_cvt_pk_bf16_f32 v75, v75, v49\n\t" \
  "v_smfmac_f32_16x16x64_bf16 v[56:59], v[0:3], a[24:31], v68\n\t" \
  "ds_write_b32 v71, v75 offset:512\n\t" \
  "v_smfmac_f32_16x16x64_bf16 v[56:59], v[4:7], a[72:79], v68\n\t" \
  "ds_read_b64_tr_b16 v[0:1], v72 offset:10240\n\t" \
  "ds_read_b64_tr_b16 v[2:3], v72 offset:10752\n\t" \
  "v_smfmac_f32_16x16x64_bf16 v[52:55], v[8:11], a[104:111], v68\n\t" \
  "ds_read_b128 v[36:39], v70 offset:12288\n\t" \
  "v_smfmac_f32_16x16x64_bf16 v[56:59], v[8:11], a[112:119], v68\n\t" \
  "ds_read_b128 v[40:43], v70 offset:13312\n\t" \
  "v_smfmac_f32_16x16x64_bf16 v[52:55], v[12:15], a[144:151], v68\n\t" \
  "s_waitcnt lgkmcnt(4)\n\t" \
  "s_barrier\n\t" \
  "ds_read_b64_tr_b16 v[44:45], v72 offset:0\n\t" \
  "ds_read_b64_tr_b16 v[46:47], v72 offset:512\n\t" \
  "v_smfmac_f32_16x16x64_bf16 v[56:59], v[12:15], a[152:159], v68\n\t" \
  "ds_read_b64_tr_b16 v[48:49], v72 offset:1024\n\t" \
  "ds_read_b64_tr_b16 v[50:51], v72 offset:1536\n\t" \
  "v_smfmac_f32_16x16x64_bf16 v[52:55], v[16:19], a[192:199], v68\n\t" \
  "ds_read_b128 v[4:7], v69 offset:2048\n\t" \
  "s_waitcnt lgkmcnt(5)\n\t" \
  "v_smfmac_f32_16x16x64_bf16 v[56:59], v[16:19], v[36:43], v68\n\t" \
  "ds_read_b128 v[8:11], v69 offset:3072\n\t" \
  "s_waitcnt lgkmcnt(4)\n\t" \
  "v_mfma_f32_16x16x32_bf16 v[12:15], v[44:47], v[76:79], 0\n\t" \
  "ds_read_b128 v[36:39], v70 offset:14336\n\t" \
  "s_waitcnt lgkmcnt(3)\n\t" \
  "v_mfma_f32_16x16x32_bf16 v[12:15], v[48:51], v[0:3], v[12:15]\n\t" \
  "buffer_store_dwordx4 v[44:47], v69, s[28:31], s69 offen offset:0 sc1\n\t" \
  "ds_read_b128 v[40:43], v70 offset:15360\n\t" \
  "v_smfmac_f32_16x16x64_bf16 v[52:55], v[20:23], a[224:231], v68\n\t" \
  "s_waitcnt lgkmcnt(2)\n\t" \
  "v_add_f32 v4, v4, v8\n\t" \
  "v_add_f32 v5, v5, v9\n\t" \
  "v_add_f32 v6, v6, v10\n\t" \
  "v_cndmask_b32 v78, v13, v14, s[56:57]\n\t" \
  "v_cndmask_b32 v76, v12, v15, s[56:57]\n\t" \
  "v_add_f32 v7, v7, v11\n\t" \
  "v_max3_f32 v77, v4, v5, v6\n\t" \
  "v_add_f32_dpp v76, v78, v76 quad_perm:[2,3,0,1] row_mask:0xf bank_mask:0xf\n\t" \
  "v_max3_f32 v77, v77, v7, v76\n\t" \
  "v_smfmac_f32_16x16x64_bf16 v[56:59], v[20:23], a[232:239], v68\n\t" \
  "ds_read_b128 v[44:47], v70 offset:16384\n\t" \
  "v_max_f32_dpp v77, v77, v77 quad_perm:[2,3,0,1] row_mask:0xf bank_mask:0xf\n\t" \
  "v_mov_b32 v78, v77\n\t" \
  "v_smfmac_f32_16x16x64_bf16 v[52:55], v[24:27], v[96:103], v68\n\t" \
  "ds_read_b128 v[48:51], v70 offset:17408\n\t" \
  "v_permlane16_swap_b32 v77, v78\n\t" \
  "v_max_f32 v77, v77, v78\n\t" \
  "v_mov_b32 v78, v77\n\t" \
  "v_smfmac_f32_16x16x64_bf16 v[52:55], v[28:31], v[144:151], v68\n\t" \
  "ds_read_b128 v[16:19], v70 offset:18432\n\t" \
  "v_permlane32_swap_b32 v77, v78\n\t" \
  "v_max_f32 v77, v77, v78\n\t" \
  "v_smfmac_f32_16x16x64_bf16 v[52:55], v[32:35], v[184:191], v68\n\t" \
  "ds_read_b128 v[20:23], v70 offset:19456\n\t" \
  "v_mul_f32 v77, 0xbe0293ee, v77\n\t" \
  "v_smfmac_f32_16x16x64_bf16 v[56:59], v[24:27], v[104:111], v68\n\t" \
  "v_fmamk_f32 v4, v4, 0x3e0293ee, v77\n\t" \
  "v_smfmac_f32_16x16x64_bf16 v[56:59], v[28:31], v[152:159], v68\n\t" \
  "ds_read_b128 v[24:27], v70 offset:22528\n\t" \
  "s_waitcnt lgkmcnt(4)\n\t" \
  "v_fmamk_f32 v5, v5, 0x3e0293ee, v77\n\t" \
  "v_smfmac_f32_16x16x64_bf16 v[56:59], v[32:35], v[36:43], v68\n\t" \
  "ds_read_b128 v[28:31], v70 offset:23552\n\t" \
  "v_fmamk_f32 v6, v6, 0x3e0293ee, v77\n\t" \
  "v_fmamk_f32 v7, v7, 0x3e0293ee, v77\n\t" \
  "v_fmamk_f32 v76, v76, 0x3e0293ee, v77\n\t" \
  "v_exp_f32 v4, v4\n\t" \
  "v_exp_f32 v5, v5\n\t" \
  "v_exp_f32 v6, v6\n\t" \
  "v_exp_f32 v7, v7\n\t" \
  "v_cvt_pk_bf16_f32 v4, v4, v5\n\t" \
  "v_exp_f32 v76, v76\n\t" \
  "v_cvt_pk_bf16_f32 v5, v6, v7\n\t" \
  "v_mov_b32_dpp v12, v4 quad_perm:[0,1,0,1] row_mask:0xf bank_mask:0xf\n\t" \
  "v_mov_b32_dpp v14, v4 quad_perm:[2,3,2,3] row_mask:0xf bank_mask:0xf\n\t" \
  "v_mov_b32_dpp v13, v5 quad_perm:[0,1,0,1] row_mask:0xf bank_mask:0xf\n\t" \
  "v_mov_b32_dpp v15, v5 quad_perm:[2,3,2,3] row_mask:0xf bank_mask:0xf\n\t" \
  "v_add_f32 v52, v52, v54\n\t" \
  "v_add_f32 v53, v53, v55\n\t" \
  "s_waitcnt vmcnt(2)\n\t" \
  "v_mfma_f32_16x16x32_bf16 v[0:3], v[60:63], v[12:15], 0\n\t" \
  "ds_read_b128 v[36:39], v70 offset:20480\n\t" \
  "v_add_f32 v56, v56, v58\n\t" \
  "v_add_f32 v57, v57, v59\n\t" \
  "v_cvt_pk_bf16_f32 v52, v52, v53\n\t" \
  "v_cvt_pk_bf16_f32 v53, v56, v57\n\t" \
  "s_waitcnt vmcnt(1)\n\t" \
  "v_mfma_f32_16x16x32_bf16 v[4:7], v[64:67], v[12:15], 0\n\t" \
  "ds_read_b128 v[40:43], v70 offset:21504\n\t" \
  "v_mov_b64 v[56:57], s[88:89]\n\t" \
  "v_mov_b64 v[58:59], s[88:89]\n\t" \
  "v_mov_b32_dpp v78, v76 quad_perm:[0,1,0,1] row_mask:0xf bank_mask:0xf\n\t" \
  "v_mov_b32_dpp v79, v76 quad_perm:[2,3,2,3] row_mask:0xf bank_mask:0xf\n\t" \
  "v_mfma_f32_16x16x32_bf16 v[32:35], v[56:59], v[12:15], 0\n\t" \
  "buffer_store_dwordx2 v[52:53], v71, s[28:31], s69 offen offset:2556 sc1\n\t" \
  "ds_read_b128 v[60:63], v70 offset:24576\n\t" \
  "v_cvt_pk_bf16_f32 v11, v78, v79\n\t" \
  "v_cvt_pk_bf16_f32 v8, v78, v79\n\t" \
  "v_mov_b64 v[54:55], 0\n\t" \
  "v_mov_b32 v9, 0\n\t" \
  "v_mov_b32 v10, 0\n\t" \
  "v_mov_b64 v[14:15], 0\n\t" \
  "s_nop 0\n\t" \
  "v_mfma_f32_16x16x32_bf16 v[32:35], v[54:57], v[8:11], v[32:35]\n\t" \
  "s_cmp_eq_u32 s48, 63\n\t" \
  "v_mfma_f32_16x16x32_bf16 v[0:3], v[52:55], v[8:11], v[0:3]\n\t" \
  "ds_read_b128 v[64:67], v70 offset:25600\n\t" \
  "v_mov_b64 v[56:57], 0\n\t" \
  "v_mov_b64 v[58:59], 0\n\t" \
  "v_mfma_f32_16x16x32_bf16 v[4:7], v[52:55], v[10:13], v[4:7]\n\t" \
  "s_cbranch_scc1 LLAST1_%=\n\t" \
  "v_rcp_f32 v74, v32\n\t" \
  "v_mov_b64 v[12:13], 0\n\t" \
  "v_mov_b32 v8, 0\n\t" \
  "v_pk_mul_f32 v[0:1], v[0:1], v[74:75] op_sel_hi:[1,0]\n\t" \
  "v_pk_mul_f32 v[2:3], v[2:3], v[74:75] op_sel_hi:[1,0]\n\t" \
  "v_cvt_pk_bf16_f32 v76, v0, v1\n\t" \
  "v_cvt_pk_bf16_f32 v77, v2, v3\n\t" \
  "v_pk_mul_f32 v[4:5], v[4:5], v[74:75] op_sel_hi:[1,0]\n\t" \
  "v_pk_mul_f32 v[6:7], v[6:7], v[74:75] op_sel_hi:[1,0]\n\t" \
  "v_cvt_pk_bf16_f32 v78, v4, v5\n\t" \
  "v_cvt_pk_bf16_f32 v79, v6, v7\n\t" \
  "ds_write_b128 v69, v[76:79] offset:5120\n\t" \
  "s_waitcnt lgkmcnt(2)\n\t" \
  "v_smfmac_f32_16x16x64_bf16 v[12:15], v[76:79], v[44:51], v68\n\t" \
  "ds_read_b128 v[44:47], v70 offset:26624\n\t" \
  "v_mov_b32 v11, 0\n\t" \
  "v_smfmac_f32_16x16x64_bf16 v[56:59], v[76:79], v[16:23], v68\n\t" \
  "ds_read_b128 v[48:51], v70 offset:27648\n\t" \
  "v_mov_b64 v[52:53], 0\n\t" \
  "v_smfmac_f32_16x16x64_bf16 v[8:11], v[76:79], v[36:43], v68\n\t" \
  "ds_read_b128 v[16:19], v70 offset:28672\n\t" \
  "v_smfmac_f32_16x16x64_bf16 v[52:55], v[76:79], v[24:31], v68\n\t" \
  "s_waitcnt lgkmcnt(3)\n\t" \
  "s_barrier\n\t" \
  "ds_read_b128 v[0:3], v69 offset:4096\n\t" \
  "ds_read_b128 v[4:7], v69 offset:7168\n\t" \
  "v_lshrrev_b32 v71, 7, v69\n\t" \
  "v_and_b32 v74, 0x70, v69\n\t" \
  "v_lshl_add_u32 v71, v71, 14, v74\n\t" \
  "v_bfe_u32 v72, v69, 2, 7\n\t" \
  "v_and_b32 v74, 0x200, v69\n\t" \
  "s_waitcnt lgkmcnt(1)\n\t" \
  "v_smfmac_f32_16x16x64_bf16 v[8:11], v[0:3], v[60:67], v68\n\t" \
  "buffer_load_dword v75, off, s[28:31], s80 sc1\n\t" \
  "ds_read_b128 v[20:23], v70 offset:29696\n\t" \
  "v_mov_b64 v[76:77], 0\n\t" \
  "v_smfmac_f32_16x16x64_bf16 v[12:15], v[0:3], v[200:207], v68\n\t" \
  "ds_read_b128 v[32:35], v69 offset:6144\n\t" \
  "v_mov_b64 v[78:79], 0\n\t" \
  "v_smfmac_f32_16x16x64_bf16 v[52:55], v[0:3], v[44:51], v68\n\t" \
  "ds_read_b128 v[24:27], v70 offset:30720\n\t" \
  "v_lshl_add_u32 v72, v74, 3, v72\n\t" \
  "v_smfmac_f32_16x16x64_bf16 v[56:59], v[0:3], v[232:239], v68\n\t" \
  "ds_read_b128 v[28:31], v70 offset:31744\n\t" \
  "s_waitcnt lgkmcnt(4)\n\t" \
  "v_smfmac_f32_16x16x64_bf16 v[12:15], v[4:7], v[208:215], v68\n\t" \
  "ds_read_b128 v[60:63], v70 offset:32768\n\t" \
  "s_waitcnt lgkmcnt(4)\n\t" \
  "v_smfmac_f32_16x16x64_bf16 v[8:11], v[4:7], v[16:23], v68\n\t" \
  "ds_read_b128 v[64:67], v70 offset:33792\n\t" \
  "s_waitcnt lgkmcnt(4)\n\t" \
  "v_smfmac_f32_16x16x64_bf16 v[8:11], v[32:35], v[192:199], v68\n\t" \
  "ds_read_b128 v[36:39], v73 offset:4096\n\t" \
  "v_smfmac_f32_16x16x64_bf16 v[12:15], v[32:35], v[216:223], v68\n\t" \
  "ds_read_b128 v[40:43], v73 offset:6144\n\t" \
  "s_waitcnt lgkmcnt(4)\n\t" \
  "v_smfmac_f32_16x16x64_bf16 v[52:55], v[4:7], v[24:31], v68\n\t" \
  "ds_read_b128 v[44:47], v70 offset:34816\n\t" \
  "v_cvt_pk_bf16_f32 v8, v8, v9\n\t" \
  "v_smfmac_f32_16x16x64_bf16 v[52:55], v[32:35], v[224:231], v68\n\t" \
  "ds_read_b128 v[48:51], v70 offset:35840\n\t" \
  "v_cvt_pk_bf16_f32 v9, v10, v11\n\t" \
  "v_smfmac_f32_16x16x64_bf16 v[56:59], v[4:7], v[240:247], v68\n\t" \
  "s_waitcnt lgkmcnt(4)\n\t" \
  "v_cvt_pk_bf16_f32 v10, v12, v13\n\t" \
  "v_smfmac_f32_16x16x64_bf16 v[56:59], v[32:35], v[60:67], v68\n\t" \
  "s_waitcnt lgkmcnt(3)\n\t" \
  "v_cvt_pk_bf16_f32 v11, v14, v15\n\t" \
  "v_smfmac_f32_16x16x64_bf16 v[76:79], v[36:39], v[248:255], v68\n\t" \
  "buffer_store_dwordx4 v[8:11], v71, s[28:31], s54 offen sc1\n\t" \
  "s_waitcnt lgkmcnt(0)\n\t" \
  "v_cvt_pk_bf16_f32 v52, v52, v53\n\t" \
  "v_smfmac_f32_16x16x64_bf16 v[76:79], v[40:43], v[44:51], v68\n\t" \
  "v_cvt_pk_bf16_f32 v53, v54, v55\n\t" \
  "v_cvt_pk_bf16_f32 v54, v56, v57\n\t" \
  "v_cvt_pk_bf16_f32 v55, v58, v59\n\t" \
  "buffer_store_dwordx4 v[52:55], v71, s[28:31], s75 offen sc1\n\t" \
  "s_nop 2\n\t" \
  "v_add_f32 v76, v76, v78\n\t" \
  "v_add_f32 v77, v77, v79\n\t" \
  "v_cvt_pk_bf16_f32 v76, v76, v77\n\t" \
  "buffer_store_dword v76, v72, s[28:31], s55 offen sc1\n\t" \
  "s_memrealtime s[70:71]\n\t" \
  "s_waitcnt vmcnt(3)\n\t" \
  "v_readfirstlane_b32 s65, v75\n\t" \
  "s_barrier\n\t" \
  "s_waitcnt lgkmcnt(0)\n\t" \
  "s_not_b32 s65, s65\n\t" \
  "s_lshl_b32 s61, s70, 8\n\t" \
  "s_andn2_b32 s64, s65, 0xff\n\t" \
  "s_sub_u32 s64, s64, s61\n\t" \
  "s_ashr_i32 s64, s64, 8\n\t" \
  "s_cmp_eq_u32 s65, 0\n\t" \
  "s_cselect_b32 s64, " CH_DLF ", s64\n\t" \
  "s_sub_i32 s64, s64, " CH_RO1 "\n\t" \
  "s_min_i32 s64, s64, s34\n\t" \
  "s_max_i32 s64, s64, " CH_DLL1 "\n\t" \
  "s_add_u32 s64, s64, s70\n\t" \
  "s_mov_b32 s74, 0\n\t" \
  "s_movk_i32 s60, 8192\n\t" \
  "RWT1_%=:\n\t" \
  "s_memrealtime s[72:73]\n\t" \
  "s_sub_u32 s60, s60, 1\n\t" \
  "s_cselect_b32 s61, 0, -1\n\t" \
  "s_waitcnt lgkmcnt(0)\n\t" \
  "s_sub_i32 s73, s72, s64\n\t" \
  "s_and_b32 s73, s73, s61\n\t" \
  "s_cmp_lt_i32 s73, " CH_FIN "\n\t" \
  "s_cbranch_scc1 RWT1_%=\n\t" \
  "s_cmp_lt_i32 s73, 0\n\t" \
  "s_cbranch_scc1 RWT1_%=f\n\t" \
  "RWT1_%=g:\n\t" \
  "RRETRY1_%=:\n\t" \
  "buffer_load_dwordx4 v[0:3], v69, s[28:31], s97 offen offset:0 sc1\n\t" \
  "buffer_load_dwordx4 v[4:7], v69, s[28:31], s97 offen offset:1024 sc1\n\t" \
  "buffer_load_dwordx4 v[8:11], v69, s[28:31], s97 offen offset:2048 sc1\n\t" \
  "buffer_load_dwordx4 v[12:15], v69, s[28:31], s97 offen offset:3072 sc1\n\t" \
  "v_lshrrev_b32 v20, 2, v69\n\t" \
  "v_and_b32 v20, 16, v20\n\t" \
  "v_mov_b32 v21, 0x3f80\n\t" \
  "v_lshlrev_b32 v21, v20, v21\n\t" \
  "v_bfe_u32 v20, v69, 4, 2\n\t" \
  "v_cmp_eq_u32 vcc, 0, v20\n\t" \
  "s_nop 1\n\t" \
  "v_cndmask_b32 v32, 0, v21, vcc\n\t" \
  "v_cmp_eq_u32 vcc, 1, v20\n\t" \
  "s_nop 1\n\t" \
  "v_cndmask_b32 v33, 0, v21, vcc\n\t" \
  "v_cmp_eq_u32 vcc, 2, v20\n\t" \
  "s_nop 1\n\t" \
  "v_cndmask_b32 v34, 0, v21, vcc\n\t" \
  "v_cmp_eq_u32 vcc, 3, v20\n\t" \
  "s_nop 1\n\t" \
  "v_cndmask_b32 v35, 0, v21, vcc\n\t" \
  "v_bfe_u32 v38, v69, 7, 1\n\t" \
  "v_bfe_u32 v20, v69, 9, 1\n\t" \
  "v_lshl_add_u32 v38, v20, 2, v38\n\t" \
  "v_lshlrev_b32 v38, 12, v38\n\t" \
  "v_and_b32 v20, 0x60, v69\n\t" \
  "v_add_u32 v38, v38, v20\n\t" \
  "v_bfe_u32 v20, v69, 8, 1\n\t" \
  "v_lshl_add_u32 v38, v20, 4, v38\n\t" \
  "v_bfe_u32 v20, v69, 4, 1\n\t" \
  "v_lshl_add_u32 v38, v20, 3, v38\n\t" \
  "v_mov_b32 v44, 0\n\t" \
  "s_waitcnt vmcnt(3)\n\t" \
  "v_mfma_f32_16x16x32_bf16 v[40:43], v[32:35], v[0:3], 0\n\t" \
  "v_max3_u32 v44, v44, v0, v1\n\t" \
  "v_max3_u32 v44, v44, v2, v3\n\t" \
  "s_waitcnt vmcnt(2)\n\t" \
  "v_mfma_f32_16x16x32_bf16 v[40:43], v[32:35], v[4:7], v[40:43]\n\t" \
  "v_max3_u32 v44, v44, v4, v5\n\t" \
  "v_max3_u32 v44, v44, v6, v7\n\t" \
  "s_waitcnt vmcnt(1)\n\t" \
  "v_mfma_f32_16x16x32_bf16 v[40:43], v[32:35], v[8:11], v[40:43]\n\t" \
  "v_max3_u32 v44, v44, v8, v9\n\t" \
  "v_max3_u32 v44, v44, v10, v11\n\t" \
  "s_waitcnt vmcnt(0)\n\t" \
  "v_mfma_f32_16x16x32_bf16 v[40:43], v[32:35], v[12:15], v[40:43]\n\t" \
  "v_max3_u32 v44, v44, v12, v13\n\t" \
  "v_max3_u32 v44, v44, v14, v15\n\t" \
  "v_cmp_eq_u32 vcc, -1, v44\n\t" \
  "s_cbranch_vccnz RFAIL1_%=\n\t" \
  "s_nop 3\n\t" \
  "v_add_f32_dpp v40, v40, v40 row_ror:8 row_mask:0xf bank_mask:0xf\n\t" \
  "v_add_f32_dpp v41, v41, v41 row_ror:8 row_mask:0xf bank_mask:0xf\n\t" \
  "v_add_f32_dpp v42, v42, v42 row_ror:8 row_mask:0xf bank_mask:0xf\n\t" \
  "v_add_f32_dpp v43, v43, v43 row_ror:8 row_mask:0xf bank_mask:0xf\n\t" \
  "v_cvt_pk_bf16_f32 v40, v40, v41\n\t" \
  "v_cvt_pk_bf16_f32 v41, v42, v43\n\t" \
  "buffer_store_dwordx2 v[40:41], v38, s[28:31], s98 offen sc1\n\t" \
  "RDONE1_%=:\n\t" \
  "s_barrier\n\t" \
  "s_add_u32 s48, s48, 1\n\t" \
  "s_mov_b32 s49, s53\n\t" \
  "s_add_u32 s53, s53, " CH_SLOT "\n\t" \
  "s_add_u32 s60, s53, " CH_SKIP "\n\t" \
  "s_lshr_b32 s61, s96, 1\n\t" \
  "s_add_u32 s86, s86, 1\n\t" \
  "s_cmp_eq_u32 s86, 3\n\t" \
  "s_cselect_b32 s53, s60, s53\n\t" \
  "s_cselect_b32 s96, s61, s96\n\t" \
  "s_cselect_b32 s86, 0, s86\n\t" \
  "s_cmp_ge_u32 s53, " CH_RING "\n\t" \
  "s_cselect_b32 s53, 0, s53\n\t" \
  "s_cselect_b32 s96, s21, s96\n\t" \
  "s_andn2_b32 s53, s53, 0x2000\n\t" \
  "s_and_b32 s60, s96, 1\n\t" \
  "s_lshl_b32 s60, s60, 13\n\t" \
  "s_or_b32 s53, s53, s60\n\t" \
  "s_mov_b32 s68, 0\n\t" \
  "s_add_u32 s97, s41, s53\n\t" \
  "s_add_u32 s98, s37, s53\n\t" \
  "s_add_u32 s82, s48, 0\n\t" \
  "s_and_b32 s82, s82, " CH_TRM "\n\t" \
  "s_lshr_b64 s[60:61], s[90:91], s82\n\t" \
  "s_and_b32 s60, s60, 1\n\t" \
  "s_lshl_b32 s60, s60, 13\n\t" \
  "s_add_u32 s82, s82, " CH_SG0 "\n\t" \
  "s_lshl_b32 s82, s82, 21\n\t" \
  "s_or_b32 s82, s82, s60\n\t" \
  "s_add_u32 s50, s82, s38\n\t" \
  "s_add_u32 s69, s82, s39\n\t" \
  "s_cmp_eq_u32 s34, 0\n\t" \
  "s_cbranch_scc1 LINI_%=\n\t" \
  "s_branch LG1_%=\n\t" \
  "RFAIL1_%=:\n\t" \
  "s_add_u32 s74, s74, 1\n\t" \
  "s_sleep " CH_RSL "\n\t" \
  "s_waitcnt vmcnt(0)\n\t" \
  "s_cmp_gt_u32 s74, s84\n\t" \
  "s_cbranch_scc1 RG1_%=\n\t" \
  "s_and_b32 s60, s74, 1023\n\t" \
  "s_cmp_lg_u32 s60, 0\n\t" \
  "s_cbranch_scc1 RRETRY1_%=\n\t" \
  "buffer_load_dword v46, off, s[28:31], s92 sc1\n\t" \
  "s_waitcnt vmcnt(0)\n\t" \
  "v_readfirstlane_b32 s60, v46\n\t" \
  "s_nop 0\n\t" \
  "s_cmp_eq_u32 s60, 0\n\t" \
  "s_cbranch_scc1 RRETRY1_%=\n\t" \
  "RG1_%=:\n\t" \
  "s_mov_b32 s84, 0\n\t" \
  "s_lshl_b32 s60, s36, 4\n\t" \
  "s_or_b32 s60, s60, s48\n\t" \
  "s_or_b32 s60, s60, 536870912\n\t" \
  "v_mov_b32 v76, s60\n\t" \
  "v_mov_b32 v77, 0\n\t" \
  "s_mov_b64 exec, 1\n\t" \
  "buffer_atomic_cmpswap v[76:77], off, s[28:31], s92 sc1\n\t" \
  "s_mov_b64 exec, -1\n\t" \
  "s_branch RDONE1_%=\n\t" \
  "LFAIL1_%=:\n\t" \
  "v_max_u32 v75, v0, v1\n\t" \
  "v_max3_u32 v75, v75, v2, v3\n\t" \
  "v_max3_u32 v75, v75, v4, v5\n\t" \
  "v_max3_u32 v75, v75, v6, v7\n\t" \
  "v_max3_u32 v75, v75, v8, v9\n\t" \
  "v_max3_u32 v75, v75, v10, v11\n\t" \
  "v_max3_u32 v75, v75, v12, v13\n\t" \
  "v_max3_u32 v75, v75, v14, v15\n\t" \
  "v_max3_u32 v75, v75, v16, v17\n\t" \
  "v_max3_u32 v75, v75, v18, v19\n\t" \
  "v_max3_u32 v75, v75, v20, v21\n\t" \
  "v_max3_u32 v75, v75, v22, v23\n\t" \
  "v_max3_u32 v75, v75, v24, v25\n\t" \
  "v_max3_u32 v75, v75, v26, v27\n\t" \
  "v_max3_u32 v75, v75, v28, v29\n\t" \
  "v_max3_u32 v75, v75, v30, v31\n\t" \
  "v_max3_u32 v75, v75, v32, v33\n\t" \
  "v_max3_u32 v75, v75, v34, v35\n\t" \
  "v_cmp_eq_u32 vcc, -1, v75\n\t" \
  "s_cbranch_vccz LGO1_%=\n\t" \
  "s_add_u32 s68, s68, 1\n\t" \
  "s_waitcnt lgkmcnt(0)\n\t" \
  "s_cmp_gt_u32 s68, s83\n\t" \
  "s_cbranch_scc1 LGV1_%=\n\t" \
  "s_barrier\n\t" \
  "s_waitcnt vmcnt(8)\n\t" \
  "s_branch LG1_%=\n\t" \
  "RWT1_%=f:\n\t" \
  "s_cmp_lt_i32 s73, -2\n\t" \
  "s_cbranch_scc0 RWT1_%=g\n\t" \
  "s_nop 4\n\t" \
  "s_branch RWT1_%=g\n\t" \
  "LGV1_%=:\n\t" \
  "s_mov_b32 s83, 0\n\t" \
  "s_lshl_b32 s60, s36, 4\n\t" \
  "s_or_b32 s60, s60, s48\n\t" \
  "s_or_b32 s60, s60, 805306368\n\t" \
  "v_mov_b32 v76, s60\n\t" \
  "v_mov_b32 v77, 0\n\t" \
  "s_mov_b64 exec, 1\n\t" \
  "buffer_atomic_cmpswap v[76:77], off, s[28:31], s92 sc1\n\t" \
  "s_mov_b64 exec, -1\n\t" \
  "s_branch LGO1_%=\n\t" \
  "LLAST1_%=:\n\t" \
  "s_waitcnt vmcnt(0) lgkmcnt(0)\n\t" \
  "s_barrier\n\t" \
  "s_branch LEND_%=\n\t" \
  ".p2align 6\n\t" \
  "LG2_%=:\n\t" \
  "s_barrier\n\t" \
  "buffer_load_dwordx4 v[52:55], v69, s[28:31], s50 offen sc1\n\t" \
  "buffer_load_dwordx4 v[56:59], v69, s[28:31], s50 offen offset:1024 sc1\n\t" \
  "s_add_u32 s64, s50, 18432\n\t" \
  "s_movk_i32 m0, 11264\n\t" \
  "v_lshlrev_b32 v75, 1, v69\n\t" \
  "buffer_load_dword v75, s[28:31], s64 offen sc1 lds\n\t" \
  "ds_read_b128 v[44:47], v70 offset:0\n\t" \
  "ds_read_b128 v[48:51], v70 offset:1024\n\t" \
  "ds_read_b128 v[60:63], v70 offset:2048\n\t" \
  "ds_read_b128 v[64:67], v70 offset:3072\n\t" \
  "v_mov_b64 v[36:37], 0\n\t" \
  "v_mov_b64 v[38:39], 0\n\t" \
  "v_mov_b64 v[40:41], 0\n\t" \
  "v_mov_b64 v[42:43], 0\n\t" \
  "v_lshrrev_b32 v71, 1, v69\n\t" \
  "v_lshrrev_b32 v72, 3, v69\n\t" \
  "v_and_b32 v72, 0x78, v72\n\t" \
  "v_and_b32 v74, 0x30, v69\n\t" \
  "v_lshl_add_u32 v72, v74, 3, v72\n\t" \
  "s_add_u32 s54, s53, s36\n\t" \
  "s_add_u32 s55, s54, 557056\n\t" \
  "s_add_u32 s54, s54, 131072\n\t" \
  "s_add_u32 s75, s54, 262144\n\t" \
  "s_add_u32 s80, s53, s26\n\t" \
  "s_barrier\n\t" \
  "ds_read_b128 v[0:3], v69 offset:0\n\t" \
  "ds_read_b128 v[4:7], v69 offset:1024\n\t" \
  "ds_read_b128 v[8:11], v69 offset:2048\n\t" \
  "ds_read_b128 v[12:15], v69 offset:3072\n\t" \
  "s_waitcnt lgkmcnt(3)\n\t" \
  "v_smfmac_f32_16x16x64_bf16 v[36:39], v[0:3], v[44:51], v68\n\t" \
  "ds_read_b128 v[44:47], v70 offset:4096\n\t" \
  "v_smfmac_f32_16x16x64_bf16 v[40:43], v[0:3], v[60:67], v68\n\t" \
  "ds_read_b128 v[48:51], v70 offset:5120\n\t" \
  "s_waitcnt lgkmcnt(4)\n\t" \
  "v_smfmac_f32_16x16x64_bf16 v[36:39], v[4:7], a[32:39], v68\n\t" \
  "ds_read_b128 v[60:63], v70 offset:6144\n\t" \
  "v_smfmac_f32_16x16x64_bf16 v[40:43], v[4:7], a[40:47], v68\n\t" \
  "ds_read_b128 v[64:67], v70 offset:7168\n\t" \
  "s_waitcnt lgkmcnt(5)\n\t" \
  "v_smfmac_f32_16x16x64_bf16 v[36:39], v[8:11], a[80:87], v68\n\t" \
  "s_barrier\n\t" \
  "ds_read_b128 v[16:19], v69 offset:4096\n\t" \
  "v_smfmac_f32_16x16x64_bf16 v[40:43], v[8:11], a[88:95], v68\n\t" \
  "ds_read_b128 v[20:23], v69 offset:5120\n\t" \
  "s_waitcnt lgkmcnt(6)\n\t" \
  "v_smfmac_f32_16x16x64_bf16 v[36:39], v[12:15], a[120:127], v68\n\t" \
  "ds_read_b128 v[24:27], v69 offset:6144\n\t" \
  "s_waitcnt lgkmcnt(5)\n\t" \
  "v_smfmac_f32_16x16x64_bf16 v[40:43], v[12:15], v[44:51], v68\n\t" \
  "ds_read_b128 v[28:31], v69 offset:7168\n\t" \
  "s_waitcnt lgkmcnt(3)\n\t" \
  "v_smfmac_f32_16x16x64_bf16 v[36:39], v[16:19], a[160:167], v68\n\t" \
  "ds_read_b128 v[32:35], v69 offset:8192\n\t" \
  "v_smfmac_f32_16x16x64_bf16 v[40:43], v[16:19], a[168:175], v68\n\t" \
  "s_waitcnt lgkmcnt(3)\n\t" \
  "v_mov_b64 v[44:45], 0\n\t" \
  "v_smfmac_f32_16x16x64_bf16 v[36:39], v[20:23], a[200:207], v68\n\t" \
  "s_waitcnt lgkmcnt(2)\n\t" \
  "v_mov_b64 v[46:47], 0\n\t" \
  "v_smfmac_f32_16x16x64_bf16 v[36:39], v[24:27], a[240:247], v68\n\t" \
  "s_waitcnt lgkmcnt(1)\n\t" \
  "v_mov_b64 v[48:49], 0\n\t" \
  "v_smfmac_f32_16x16x64_bf16 v[36:39], v[28:31], v[112:119], v68\n\t" \
  "s_waitcnt lgkmcnt(0)\n\t" \
  "v_mov_b64 v[50:51], 0\n\t" \
  "v_smfmac_f32_16x16x64_bf16 v[36:39], v[32:35], v[160:167], v68\n\t" \
  "v_smfmac_f32_16x16x64_bf16 v[40:43], v[20:23], v[60:67], v68\n\t" \
  "ds_read_b128 v[60:63], v70 offset:8192\n\t" \
  "v_smfmac_f32_16x16x64_bf16 v[40:43], v[24:27], a[248:255], v68\n\t" \
  "ds_read_b128 v[64:67], v70 offset:9216\n\t" \
  "v_add_f32 v74, v36, v38\n\t" \
  "v_smfmac_f32_16x16x64_bf16 v[40:43], v[28:31], v[120:127], v68\n\t" \
  "v_add_f32 v37, v37, v39\n\t" \
  "v_smfmac_f32_16x16x64_bf16 v[40:43], v[32:35], v[168:175], v68\n\t" \
  "v_cmp_u_f32 vcc, v74, v37\n\t" \
  "v_smfmac_f32_16x16x64_bf16 v[44:47], v[0:3], a[0:7], v68\n\t" \
  "s_cbranch_vccnz LFAIL2_%=\n\t" \
  "LGO2_%=:\n\t" \
  "v_cvt_pk_bf16_f32 v74, v74, v37\n\t" \
  "v_smfmac_f32_16x16x64_bf16 v[44:47], v[4:7], a[48:55], v68\n\t" \
  "s_waitcnt lgkmcnt(0)\n\t" \
  "v_add_f32 v75, v40, v42\n\t" \
  "v_smfmac_f32_16x16x64_bf16 v[44:47], v[8:11], v[60:67], v68\n\t" \
  "ds_read_b128 v[60:63], v70 offset:10240\n\t" \
  "v_add_f32 v41, v41, v43\n\t" \
  "v_cvt_pk_bf16_f32 v75, v75, v41\n\t" \
  "v_smfmac_f32_16x16x64_bf16 v[44:47], v[12:15], a[128:135], v68\n\t" \
  "ds_write2st64_b32 v71, v74, v75 offset0:40 offset1:42\n\t" \
  "v_smfmac_f32_16x16x64_bf16 v[44:47], v[16:19], a[176:183], v68\n\t" \
  "ds_read_b128 v[64:67], v70 offset:11264\n\t" \
  "v_smfmac_f32_16x16x64_bf16 v[44:47], v[20:23], a[208:215], v68\n\t" \
  "v_smfmac_f32_16x16x64_bf16 v[44:47], v[24:27], v[80:87], v68\n\t" \
  "v_smfmac_f32_16x16x64_bf16 v[44:47], v[28:31], v[128:135], v68\n\t" \
  "s_waitcnt lgkmcnt(0)\n\t" \
  "v_smfmac_f32_16x16x64_bf16 v[44:47], v[32:35], v[60:67], v68\n\t" \
  "s_barrier\n\t" \
  "ds_read_b64_tr_b16 v[76:77], v72 offset:10240\n\t" \
  "v_smfmac_f32_16x16x64_bf16 v[48:51], v[0:3], a[8:15], v68\n\t" \
  "ds_read_b64_tr_b16 v[78:79], v72 offset:10752\n\t" \
  "v_smfmac_f32_16x16x64_bf16 v[48:51], v[4:7], a[56:63], v68\n\t" \
  "buffer_load_dwordx4 v[60:63], v69, s[28:31], s64 offen sc1\n\t" \
  "v_add_f32 v74, v44, v46\n\t" \
  "v_smfmac_f32_16x16x64_bf16 v[48:51], v[8:11], a[96:103], v68\n\t" \
  "v_add_f32 v45, v45, v47\n\t" \
  "v_smfmac_f32_16x16x64_bf16 v[48:51], v[12:15], a[136:143], v68\n\t" \
  "buffer_load_dwordx4 v[64:67], v69, s[28:31], s64 offen offset:1024 sc1\n\t" \
  "s_waitcnt vmcnt(4)\n\t" \
  "s_waitcnt lgkmcnt(0)\n\t" \
  "v_cvt_pk_bf16_f32 v74, v74, v45\n\t" \
  "v_mfma_f32_16x16x32_bf16 v[36:39], v[52:55], v[76:79], 0\n\t" \
  "s_waitcnt vmcnt(3)\n\t" \
  "v_mfma_f32_16x16x32_bf16 v[40:43], v[56:59], v[76:79], 0\n\t" \
  "v_mov_b64 v[52:53], 0\n\t" \
  "v_mov_b64 v[54:55], 0\n\t" \
  "v_smfmac_f32_16x16x64_bf16 v[48:51], v[16:19], a[184:191], v68\n\t" \
  "v_mov_b64 v[56:57], 0\n\t" \
  "v_mov_b64 v[58:59], 0\n\t" \
  "v_smfmac_f32_16x16x64_bf16 v[48:51], v[20:23], a[216:223], v68\n\t" \
  "ds_write_b32 v71, v74 offset:1024\n\t" \
  "v_add_f32_dpp v36, v40, v36 quad_perm:[2,3,0,1] row_mask:0xf bank_mask:0xf\n\t" \
  "v_smfmac_f32_16x16x64_bf16 v[48:51], v[24:27], v[88:95], v68\n\t" \
  "v_add_f32_dpp v37, v41, v37 quad_perm:[2,3,0,1] row_mask:0xf bank_mask:0xf\n\t" \
  "v_smfmac_f32_16x16x64_bf16 v[48:51], v[28:31], v[136:143], v68\n\t" \
  "v_add_f32_dpp v38, v42, v38 quad_perm:[2,3,0,1] row_mask:0xf bank_mask:0xf\n\t" \
  "v_smfmac_f32_16x16x64_bf16 v[48:51], v[32:35], v[176:183], v68\n\t" \
  "v_add_f32_dpp v39, v43, v39 quad_perm:[2,3,0,1] row_mask:0xf bank_mask:0xf\n\t" \
  "v_smfmac_f32_16x16x64_bf16 v[52:55], v[0:3], a[16:23], v68\n\t" \
  "s_mov_b64 exec, s[58:59]\n\t" \
  "ds_write_b128 v69, v[36:39] offset:3072\n\t" \
  "s_mov_b64 exec, -1\n\t" \
  "v_smfmac_f32_16x16x64_bf16 v[52:55], v[4:7], a[64:71], v68\n\t" \
  "v_add_f32 v75, v48, v50\n\t" \
  "v_add_f32 v49, v49, v51\n\t" \
  "v_cvt_pk_bf16_f32 v75, v75, v49\n\t" \
  "v_smfmac_f32_16x16x64_bf16 v[56:59], v[0:3], a[24:31], v68\n\t" \
  "ds_write_b32 v71, v75 offset:1536\n\t" \
  "v_smfmac_f32_16x16x64_bf16 v[56:59], v[4:7], a[72:79], v68\n\t" \
  "ds_read_b64_tr_b16 v[0:1], v72 offset:9216\n\t" \
  "ds_read_b64_tr_b16 v[2:3], v72 offset:9728\n\t" \
  "v_smfmac_f32_16x16x64_bf16 v[52:55], v[8:11], a[104:111], v68\n\t" \
  "ds_read_b128 v[36:39], v70 offset:12288\n\t" \
  "v_smfmac_f32_16x16x64_bf16 v[56:59], v[8:11], a[112:119], v68\n\t" \
  "ds_read_b128 v[40:43], v70 offset:13312\n\t" \
  "v_smfmac_f32_16x16x64_bf16 v[52:55], v[12:15], a[144:151], v68\n\t" \
  "s_waitcnt lgkmcnt(4)\n\t" \
  "s_barrier\n\t" \
  "ds_read_b64_tr_b16 v[44:45], v72 offset:1024\n\t" \
  "ds_read_b64_tr_b16 v[46:47], v72 offset:1536\n\t" \
  "v_smfmac_f32_16x16x64_bf16 v[56:59], v[12:15], a[152:159], v68\n\t" \
  "ds_read_b64_tr_b16 v[48:49], v72 offset:0\n\t" \
  "ds_read_b64_tr_b16 v[50:51], v72 offset:512\n\t" \
  "v_smfmac_f32_16x16x64_bf16 v[52:55], v[16:19], a[192:199], v68\n\t" \
  "ds_read_b128 v[4:7], v69 offset:2048\n\t" \
  "s_waitcnt lgkmcnt(5)\n\t" \
  "v_smfmac_f32_16x16x64_bf16 v[56:59], v[16:19], v[36:43], v68\n\t" \
  "ds_read_b128 v[8:11], v69 offset:3072\n\t" \
  "s_waitcnt lgkmcnt(4)\n\t" \
  "v_mfma_f32_16x16x32_bf16 v[12:15], v[44:47], v[76:79], 0\n\t" \
  "ds_read_b128 v[36:39], v70 offset:14336\n\t" \
  "s_waitcnt lgkmcnt(3)\n\t" \
  "v_mfma_f32_16x16x32_bf16 v[12:15], v[48:51], v[0:3], v[12:15]\n\t" \
  "ds_read_b128 v[40:43], v70 offset:15360\n\t" \
  "v_smfmac_f32_16x16x64_bf16 v[52:55], v[20:23], a[224:231], v68\n\t" \
  "buffer_store_dwordx4 v[44:47], v69, s[28:31], s69 offen offset:1024 sc1\n\t" \
  "s_waitcnt lgkmcnt(2)\n\t" \
  "v_add_f32 v4, v4, v8\n\t" \
  "v_add_f32 v5, v5, v9\n\t" \
  "v_add_f32 v6, v6, v10\n\t" \
  "v_cndmask_b32 v78, v13, v14, s[56:57]\n\t" \
  "v_cndmask_b32 v76, v12, v15, s[56:57]\n\t" \
  "v_add_f32 v7, v7, v11\n\t" \
  "v_max3_f32 v77, v4, v5, v6\n\t" \
  "v_add_f32_dpp v76, v78, v76 quad_perm:[2,3,0,1] row_mask:0xf bank_mask:0xf\n\t" \
  "v_max3_f32 v77, v77, v7, v76\n\t" \
  "v_smfmac_f32_16x16x64_bf16 v[56:59], v[20:23], a[232:239], v68\n\t" \
  "ds_read_b128 v[44:47], v70 offset:16384\n\t" \
  "v_max_f32_dpp v77, v77, v77 quad_perm:[2,3,0,1] row_mask:0xf bank_mask:0xf\n\t" \
  "v_mov_b32 v78, v77\n\t" \
  "v_smfmac_f32_16x16x64_bf16 v[52:55], v[24:27], v[96:103], v68\n\t" \
  "ds_read_b128 v[48:51], v70 offset:17408\n\t" \
  "v_permlane16_swap_b32 v77, v78\n\t" \
  "v_max_f32 v77, v77, v78\n\t" \
  "v_mov_b32 v78, v77\n\t" \
  "v_smfmac_f32_16x16x64_bf16 v[52:55], v[28:31], v[144:151], v68\n\t" \
  "ds_read_b128 v[16:19], v70 offset:18432\n\t" \
  "v_permlane32_swap_b32 v77, v78\n\t" \
  "v_max_f32 v77, v77, v78\n\t" \
  "v_smfmac_f32_16x16x64_bf16 v[52:55], v[32:35], v[184:191], v68\n\t" \
  "ds_read_b128 v[20:23], v70 offset:19456\n\t" \
  "v_mul_f32 v77, 0xbe0293ee, v77\n\t" \
  "v_smfmac_f32_16x16x64_bf16 v[56:59], v[24:27], v[104:111], v68\n\t" \
  "v_fmamk_f32 v4, v4, 0x3e0293ee, v77\n\t" \
  "v_smfmac_f32_16x16x64_bf16 v[56:59], v[28:31], v[152:159], v68\n\t" \
  "ds_read_b128 v[24:27], v70 offset:22528\n\t" \
  "s_waitcnt lgkmcnt(4)\n\t" \
  "v_fmamk_f32 v5, v5, 0x3e0293ee, v77\n\t" \
  "v_smfmac_f32_16x16x64_bf16 v[56:59], v[32:35], v[36:43], v68\n\t" \
  "ds_read_b128 v[28:31], v70 offset:23552\n\t" \
  "v_fmamk_f32 v6, v6, 0x3e0293ee, v77\n\t" \
  "v_fmamk_f32 v7, v7, 0x3e0293ee, v77\n\t" \
  "v_fmamk_f32 v76, v76, 0x3e0293ee, v77\n\t" \
  "v_exp_f32 v4, v4\n\t" \
  "v_exp_f32 v5, v5\n\t" \
  "v_exp_f32 v6, v6\n\t" \
  "v_exp_f32 v7, v7\n\t" \
  "v_cvt_pk_bf16_f32 v4, v4, v5\n\t" \
  "v_exp_f32 v76, v76\n\t" \
  "v_cvt_pk_bf16_f32 v5, v6, v7\n\t" \
  "v_mov_b32_dpp v12, v4 quad_perm:[0,1,0,1] row_mask:0xf bank_mask:0xf\n\t" \
  "v_mov_b32_dpp v14, v4 quad_perm:[2,3,2,3] row_mask:0xf bank_mask:0xf\n\t" \
  "v_mov_b32_dpp v13, v5 quad_perm:[0,1,0,1] row_mask:0xf bank_mask:0xf\n\t" \
  "v_mov_b32_dpp v15, v5 quad_perm:[2,3,2,3] row_mask:0xf bank_mask:0xf\n\t" \
  "v_add_f32 v52, v52, v54\n\t" \
  "v_add_f32 v53, v53, v55\n\t" \
  "s_waitcnt vmcnt(2)\n\t" \
  "v_mfma_f32_16x16x32_bf16 v[0:3], v[60:63], v[12:15], 0\n\t" \
  "ds_read_b128 v[36:39], v70 offset:20480\n\t" \
  "v_add_f32 v56, v56, v58\n\t" \
  "v_add_f32 v57, v57, v59\n\t" \
  "v_cvt_pk_bf16_f32 v52, v52, v53\n\t" \
  "v_cvt_pk_bf16_f32 v53, v56, v57\n\t" \
  "s_waitcnt vmcnt(1)\n\t" \
  "v_mfma_f32_16x16x32_bf16 v[4:7], v[64:67], v[12:15], 0\n\t" \
  "ds_read_b128 v[40:43], v70 offset:21504\n\t" \
  "v_mov_b64 v[56:57], s[88:89]\n\t" \
  "v_mov_b64 v[58:59], s[88:89]\n\t" \
  "v_mov_b32_dpp v78, v76 quad_perm:[0,1,0,1] row_mask:0xf bank_mask:0xf\n\t" \
  "v_mov_b32_dpp v79, v76 quad_perm:[2,3,2,3] row_mask:0xf bank_mask:0xf\n\t" \
  "v_mfma_f32_16x16x32_bf16 v[32:35], v[56:59], v[12:15], 0\n\t" \
  "ds_read_b128 v[60:63], v70 offset:24576\n\t" \
  "v_cvt_pk_bf16_f32 v11, v78, v79\n\t" \
  "v_cvt_pk_bf16_f32 v8, v78, v79\n\t" \
  "v_mov_b64 v[54:55], 0\n\t" \
  "v_mov_b32 v9, 0\n\t" \
  "v_mov_b32 v10, 0\n\t" \
  "v_mov_b64 v[14:15], 0\n\t" \
  "s_nop 0\n\t" \
  "v_mfma_f32_16x16x32_bf16 v[32:35], v[54:57], v[8:11], v[32:35]\n\t" \
  "buffer_store_dwordx2 v[52:53], v71, s[28:31], s69 offen offset:3072 sc1\n\t" \
  "s_cmp_eq_u32 s48, 63\n\t" \
  "v_mfma_f32_16x16x32_bf16 v[0:3], v[52:55], v[8:11], v[0:3]\n\t" \
  "ds_read_b128 v[64:67], v70 offset:25600\n\t" \
  "v_mov_b64 v[56:57], 0\n\t" \
  "v_mov_b64 v[58:59], 0\n\t" \
  "v_mfma_f32_16x16x32_bf16 v[4:7], v[52:55], v[10:13], v[4:7]\n\t" \
  "s_cbranch_scc1 LLAST2_%=\n\t" \
  "v_rcp_f32 v74, v32\n\t" \
  "v_mov_b64 v[12:13], 0\n\t" \
  "v_mov_b32 v8, 0\n\t" \
  "v_pk_mul_f32 v[0:1], v[0:1], v[74:75] op_sel_hi:[1,0]\n\t" \
  "v_pk_mul_f32 v[2:3], v[2:3], v[74:75] op_sel_hi:[1,0]\n\t" \
  "v_cvt_pk_bf16_f32 v76, v0, v1\n\t" \
  "v_cvt_pk_bf16_f32 v77, v2, v3\n\t" \
  "v_pk_mul_f32 v[4:5], v[4:5], v[74:75] op_sel_hi:[1,0]\n\t" \
  "v_pk_mul_f32 v[6:7], v[6:7], v[74:75] op_sel_hi:[1,0]\n\t" \
  "v_cvt_pk_bf16_f32 v78, v4, v5\n\t" \
  "v_cvt_pk_bf16_f32 v79, v6, v7\n\t" \
  "ds_write_b128 v69, v[76:79] offset:6144\n\t" \
  "s_waitcnt lgkmcnt(2)\n\t" \
  "v_smfmac_f32_16x16x64_bf16 v[12:15], v[76:79], v[44:51], v68\n\t" \
  "ds_read_b128 v[44:47], v70 offset:26624\n\t" \
  "v_mov_b32 v11, 0\n\t" \
  "v_smfmac_f32_16x16x64_bf16 v[56:59], v[76:79], v[16:23], v68\n\t" \
  "ds_read_b128 v[48:51], v70 offset:27648\n\t" \
  "v_mov_b64 v[52:53], 0\n\t" \
  "v_smfmac_f32_16x16x64_bf16 v[8:11], v[76:79], v[36:43], v68\n\t" \
  "ds_read_b128 v[16:19], v70 offset:28672\n\t" \
  "v_smfmac_f32_16x16x64_bf16 v[52:55], v[76:79], v[24:31], v68\n\t" \
  "s_waitcnt lgkmcnt(3)\n\t" \
  "s_barrier\n\t" \
  "ds_read_b128 v[0:3], v69 offset:7168\n\t" \
  "ds_read_b128 v[4:7], v69 offset:4096\n\t" \
  "v_lshrrev_b32 v71, 7, v69\n\t" \
  "v_and_b32 v74, 0x70, v69\n\t" \
  "v_lshl_add_u32 v71, v71, 14, v74\n\t" \
  "v_bfe_u32 v72, v69, 2, 7\n\t" \
  "v_and_b32 v74, 0x200, v69\n\t" \
  "s_waitcnt lgkmcnt(1)\n\t" \
  "v_smfmac_f32_16x16x64_bf16 v[8:11], v[0:3], v[60:67], v68\n\t" \
  "ds_read_b128 v[20:23], v70 offset:29696\n\t" \
  "v_mov_b64 v[76:77], 0\n\t" \
  "v_smfmac_f32_16x16x64_bf16 v[12:15], v[0:3], v[200:207], v68\n\t" \
  "buffer_load_dword v75, off, s[28:31], s80 sc1\n\t" \
  "ds_read_b128 v[32:35], v69 offset:5120\n\t" \
  "v_mov_b64 v[78:79], 0\n\t" \
  "v_smfmac_f32_16x16x64_bf16 v[52:55], v[0:3], v[44:51], v68\n\t" \
  "ds_read_b128 v[24:27], v70 offset:30720\n\t" \
  "v_lshl_add_u32 v72, v74, 3, v72\n\t" \
  "v_smfmac_f32_16x16x64_bf16 v[56:59], v[0:3], v[232:239], v68\n\t" \
  "ds_read_b128 v[28:31], v70 offset:31744\n\t" \
  "s_waitcnt lgkmcnt(4)\n\t" \
  "v_smfmac_f32_16x16x64_bf16 v[12:15], v[4:7], v[208:215], v68\n\t" \
  "ds_read_b128 v[60:63], v70 offset:32768\n\t" \
  "s_waitcnt lgkmcnt(4)\n\t" \
  "v_smfmac_f32_16x16x64_bf16 v[8:11], v[4:7], v[16:23], v68\n\t" \
  "ds_read_b128 v[64:67], v70 offset:33792\n\t" \
  "s_waitcnt lgkmcnt(4)\n\t" \
  "v_smfmac_f32_16x16x64_bf16 v[8:11], v[32:35], v[192:199], v68\n\t" \
  "ds_read_b128 v[36:39], v73 offset:4096\n\t" \
  "v_smfmac_f32_16x16x64_bf16 v[12:15], v[32:35], v[216:223], v68\n\t" \
  "ds_read_b128 v[40:43], v73 offset:6144\n\t" \
  "s_waitcnt lgkmcnt(4)\n\t" \
  "v_smfmac_f32_16x16x64_bf16 v[52:55], v[4:7], v[24:31], v68\n\t" \
  "ds_read_b128 v[44:47], v70 offset:34816\n\t" \
  "v_cvt_pk_bf16_f32 v8, v8, v9\n\t" \
  "v_smfmac_f32_16x16x64_bf16 v[52:55], v[32:35], v[224:231], v68\n\t" \
  "ds_read_b128 v[48:51], v70 offset:35840\n\t" \
  "v_cvt_pk_bf16_f32 v9, v10, v11\n\t" \
  "v_smfmac_f32_16x16x64_bf16 v[56:59], v[4:7], v[240:247], v68\n\t" \
  "s_waitcnt lgkmcnt(4)\n\t" \
  "v_cvt_pk_bf16_f32 v10, v12, v13\n\t" \
  "v_smfmac_f32_16x16x64_bf16 v[56:59], v[32:35], v[60:67], v68\n\t" \
  "s_waitcnt lgkmcnt(3)\n\t" \
  "v_cvt_pk_bf16_f32 v11, v14, v15\n\t" \
  "v_smfmac_f32_16x16x64_bf16 v[76:79], v[36:39], v[248:255], v68\n\t" \
  "buffer_store_dwordx4 v[8:11], v71, s[28:31], s54 offen sc1\n\t" \
  "s_waitcnt lgkmcnt(0)\n\t" \
  "v_cvt_pk_bf16_f32 v52, v52, v53\n\t" \
  "v_smfmac_f32_16x16x64_bf16 v[76:79], v[40:43], v[44:51], v68\n\t" \
  "v_cvt_pk_bf16_f32 v53, v54, v55\n\t" \
  "v_cvt_pk_bf16_f32 v54, v56, v57\n\t" \
  "v_cvt_pk_bf16_f32 v55, v58, v59\n\t" \
  "buffer_store_dwordx4 v[52:55], v71, s[28:31], s75 offen sc1\n\t" \
  "s_nop 2\n\t" \
  "v_add_f32 v76, v76, v78\n\t" \
  "v_add_f32 v77, v77, v79\n\t" \
  "v_cvt_pk_bf16_f32 v76, v76, v77\n\t" \
  "buffer_store_dword v76, v72, s[28:31], s55 offen sc1\n\t" \
  "s_memrealtime s[70:71]\n\t" \
  "s_waitcnt vmcnt(3)\n\t" \
  "v_readfirstlane_b32 s65, v75\n\t" \
  "s_barrier\n\t" \
  "s_waitcnt lgkmcnt(0)\n\t" \
  "s_not_b32 s65, s65\n\t" \
  "s_lshl_b32 s61, s70, 8\n\t" \
  "s_andn2_b32 s64, s65, 0xff\n\t" \
  "s_sub_u32 s64, s64, s61\n\t" \
  "s_ashr_i32 s64, s64, 8\n\t" \
  "s_cmp_eq_u32 s65, 0\n\t" \
  "s_cselect_b32 s64, " CH_DLF ", s64\n\t" \
  "s_min_i32 s64, s64, s34\n\t" \
  "s_max_i32 s64, s64, " CH_DLL1 "\n\t" \
  "s_add_u32 s64, s64, s70\n\t" \
  "s_mov_b32 s74, 0\n\t" \
  "s_movk_i32 s60, 8192\n\t" \
  "RWT2_%=:\n\t" \
  "s_memrealtime s[72:73]\n\t" \
  "s_sub_u32 s60, s60, 1\n\t" \
  "s_cselect_b32 s61, 0, -1\n\t" \
  "s_waitcnt lgkmcnt(0)\n\t" \
  "s_sub_i32 s73, s72, s64\n\t" \
  "s_and_b32 s73, s73, s61\n\t" \
  "s_cmp_lt_i32 s73, " CH_FIN "\n\t" \
  "s_cbranch_scc1 RWT2_%=\n\t" \
  "s_cmp_lt_i32 s73, 0\n\t" \
  "s_cbranch_scc1 RWT2_%=f\n\t" \
  "RWT2_%=g:\n\t" \
  "RRETRY2_%=:\n\t" \
  "buffer_load_dwordx4 v[0:3], v69, s[28:31], s97 offen offset:0 sc1\n\t" \
  "buffer_load_dwordx4 v[4:7], v69, s[28:31], s97 offen offset:1024 sc1\n\t" \
  "buffer_load_dwordx4 v[8:11], v69, s[28:31], s97 offen offset:2048 sc1\n\t" \
  "buffer_load_dwordx4 v[12:15], v69, s[28:31], s97 offen offset:3072 sc1\n\t" \
  "v_lshrrev_b32 v20, 2, v69\n\t" \
  "v_and_b32 v20, 16, v20\n\t" \
  "v_mov_b32 v21, 0x3f80\n\t" \
  "v_lshlrev_b32 v21, v20, v21\n\t" \
  "v_bfe_u32 v20, v69, 4, 2\n\t" \
  "v_cmp_eq_u32 vcc, 0, v20\n\t" \
  "s_nop 1\n\t" \
  "v_cndmask_b32 v32, 0, v21, vcc\n\t" \
  "v_cmp_eq_u32 vcc, 1, v20\n\t" \
  "s_nop 1\n\t" \
  "v_cndmask_b32 v33, 0, v21, vcc\n\t" \
  "v_cmp_eq_u32 vcc, 2, v20\n\t" \
  "s_nop 1\n\t" \
  "v_cndmask_b32 v34, 0, v21, vcc\n\t" \
  "v_cmp_eq_u32 vcc, 3, v20\n\t" \
  "s_nop 1\n\t" \
  "v_cndmask_b32 v35, 0, v21, vcc\n\t" \
  "v_bfe_u32 v38, v69, 7, 1\n\t" \
  "v_bfe_u32 v20, v69, 9, 1\n\t" \
  "v_lshl_add_u32 v38, v20, 2, v38\n\t" \
  "v_lshlrev_b32 v38, 12, v38\n\t" \
  "v_and_b32 v20, 0x60, v69\n\t" \
  "v_add_u32 v38, v38, v20\n\t" \
  "v_bfe_u32 v20, v69, 8, 1\n\t" \
  "v_lshl_add_u32 v38, v20, 4, v38\n\t" \
  "v_bfe_u32 v20, v69, 4, 1\n\t" \
  "v_lshl_add_u32 v38, v20, 3, v38\n\t" \
  "v_mov_b32 v44, 0\n\t" \
  "s_waitcnt vmcnt(3)\n\t" \
  "v_mfma_f32_16x16x32_bf16 v[40:43], v[32:35], v[0:3], 0\n\t" \
  "v_max3_u32 v44, v44, v0, v1\n\t" \
  "v_max3_u32 v44, v44, v2, v3\n\t" \
  "s_waitcnt vmcnt(2)\n\t" \
  "v_mfma_f32_16x16x32_bf16 v[40:43], v[32:35], v[4:7], v[40:43]\n\t" \
  "v_max3_u32 v44, v44, v4, v5\n\t" \
  "v_max3_u32 v44, v44, v6, v7\n\t" \
  "s_waitcnt vmcnt(1)\n\t" \
  "v_mfma_f32_16x16x32_bf16 v[40:43], v[32:35], v[8:11], v[40:43]\n\t" \
  "v_max3_u32 v44, v44, v8, v9\n\t" \
  "v_max3_u32 v44, v44, v10, v11\n\t" \
  "s_waitcnt vmcnt(0)\n\t" \
  "v_mfma_f32_16x16x32_bf16 v[40:43], v[32:35], v[12:15], v[40:43]\n\t" \
  "v_max3_u32 v44, v44, v12, v13\n\t" \
  "v_max3_u32 v44, v44, v14, v15\n\t" \
  "v_cmp_eq_u32 vcc, -1, v44\n\t" \
  "s_cbranch_vccnz RFAIL2_%=\n\t" \
  "s_nop 3\n\t" \
  "v_add_f32_dpp v40, v40, v40 row_ror:8 row_mask:0xf bank_mask:0xf\n\t" \
  "v_add_f32_dpp v41, v41, v41 row_ror:8 row_mask:0xf bank_mask:0xf\n\t" \
  "v_add_f32_dpp v42, v42, v42 row_ror:8 row_mask:0xf bank_mask:0xf\n\t" \
  "v_add_f32_dpp v43, v43, v43 row_ror:8 row_mask:0xf bank_mask:0xf\n\t" \
  "v_cvt_pk_bf16_f32 v40, v40, v41\n\t" \
  "v_cvt_pk_bf16_f32 v41, v42, v43\n\t" \
  "buffer_store_dwordx2 v[40:41], v38, s[28:31], s98 offen sc1\n\t" \
  "RDONE2_%=:\n\t" \
  "s_barrier\n\t" \
  "s_add_u32 s48, s48, 1\n\t" \
  "s_mov_b32 s49, s53\n\t" \
  "s_add_u32 s53, s53, " CH_SLOT "\n\t" \
  "s_add_u32 s60, s53, " CH_SKIP "\n\t" \
  "s_lshr_b32 s61, s96, 1\n\t" \
  "s_add_u32 s86, s86, 1\n\t" \
  "s_cmp_eq_u32 s86, 3\n\t" \
  "s_cselect_b32 s53, s60, s53\n\t" \
  "s_cselect_b32 s96, s61, s96\n\t" \
  "s_cselect_b32 s86, 0, s86\n\t" \
  "s_cmp_ge_u32 s53, " CH_RING "\n\t" \
  "s_cselect_b32 s53, 0, s53\n\t" \
  "s_cselect_b32 s96, s21, s96\n\t" \
  "s_andn2_b32 s53, s53, 0x2000\n\t" \
  "s_and_b32 s60, s96, 1\n\t" \
  "s_lshl_b32 s60, s60, 13\n\t" \
  "s_or_b32 s53, s53, s60\n\t" \
  "s_mov_b32 s68, 0\n\t" \
  "s_add_u32 s97, s41, s53\n\t" \
  "s_add_u32 s98, s37, s53\n\t" \
  "s_add_u32 s82, s48, 0\n\t" \
  "s_and_b32 s82, s82, " CH_TRM "\n\t" \
  "s_lshr_b64 s[60:61], s[90:91], s82\n\t" \
  "s_and_b32 s60, s60, 1\n\t" \
  "s_lshl_b32 s60, s60, 13\n\t" \
  "s_add_u32 s82, s82, " CH_SG0 "\n\t" \
  "s_lshl_b32 s82, s82, 21\n\t" \
  "s_or_b32 s82, s82, s60\n\t" \
  "s_add_u32 s50, s82, s38\n\t" \
  "s_add_u32 s69, s82, s39\n\t" \
  "s_cmp_eq_u32 s34, 0\n\t" \
  "s_cbranch_scc1 LINI_%=\n\t" \
  "s_branch LG2_%=\n\t" \
  "RFAIL2_%=:\n\t" \
  "s_add_u32 s74, s74, 1\n\t" \
  "s_sleep " CH_RSL "\n\t" \
  "s_waitcnt vmcnt(0)\n\t" \
  "s_cmp_gt_u32 s74, s84\n\t" \
  "s_cbranch_scc1 RG2_%=\n\t" \
  "s_and_b32 s60, s74, 1023\n\t" \
  "s_cmp_lg_u32 s60, 0\n\t" \
  "s_cbranch_scc1 RRETRY2_%=\n\t" \
  "buffer_load_dword v46, off, s[28:31], s92 sc1\n\t" \
  "s_waitcnt vmcnt(0)\n\t" \
  "v_readfirstlane_b32 s60, v46\n\t" \
  "s_nop 0\n\t" \
  "s_cmp_eq_u32 s60, 0\n\t" \
  "s_cbranch_scc1 RRETRY2_%=\n\t" \
  "RG2_%=:\n\t" \
  "s_mov_b32 s84, 0\n\t" \
  "s_lshl_b32 s60, s36, 4\n\t" \
  "s_or_b32 s60, s60, s48\n\t" \
  "s_or_b32 s60, s60, 536870912\n\t" \
  "v_mov_b32 v76, s60\n\t" \
  "v_mov_b32 v77, 0\n\t" \
  "s_mov_b64 exec, 1\n\t" \
  "buffer_atomic_cmpswap v[76:77], off, s[28:31], s92 sc1\n\t" \
  "s_mov_b64 exec, -1\n\t" \
  "s_branch RDONE2_%=\n\t" \
  "LFAIL2_%=:\n\t" \
  "v_max_u32 v75, v0, v1\n\t" \
  "v_max3_u32 v75, v75, v2, v3\n\t" \
  "v_max3_u32 v75, v75, v4, v5\n\t" \
  "v_max3_u32 v75, v75, v6, v7\n\t" \
  "v_max3_u32 v75, v75, v8, v9\n\t" \
  "v_max3_u32 v75, v75, v10, v11\n\t" \
  "v_max3_u32 v75, v75, v12, v13\n\t" \
  "v_max3_u32 v75, v75, v14, v15\n\t" \
  "v_max3_u32 v75, v75, v16, v17\n\t" \
  "v_max3_u32 v75, v75, v18, v19\n\t" \
  "v_max3_u32 v75, v75, v20, v21\n\t" \
  "v_max3_u32 v75, v75, v22, v23\n\t" \
  "v_max3_u32 v75, v75, v24, v25\n\t" \
  "v_max3_u32 v75, v75, v26, v27\n\t" \
  "v_max3_u32 v75, v75, v28, v29\n\t" \
  "v_max3_u32 v75, v75, v30, v31\n\t" \
  "v_max3_u32 v75, v75, v32, v33\n\t" \
  "v_max3_u32 v75, v75, v34, v35\n\t" \
  "v_cmp_eq_u32 vcc, -1, v75\n\t" \
  "s_cbranch_vccz LGO2_%=\n\t" \
  "s_add_u32 s68, s68, 1\n\t" \
  "s_waitcnt lgkmcnt(0)\n\t" \
  "s_cmp_gt_u32 s68, s83\n\t" \
  "s_cbranch_scc1 LGV2_%=\n\t" \
  "s_barrier\n\t" \
  "s_waitcnt vmcnt(8)\n\t" \
  "s_branch LG2_%=\n\t" \
  "RWT2_%=f:\n\t" \
  "s_cmp_lt_i32 s73, -2\n\t" \
  "s_cbranch_scc0 RWT2_%=g\n\t" \
  "s_nop 4\n\t" \
  "s_branch RWT2_%=g\n\t" \
  "LGV2_%=:\n\t" \
  "s_mov_b32 s83, 0\n\t" \
  "s_lshl_b32 s60, s36, 4\n\t" \
  "s_or_b32 s60, s60, s48\n\t" \
  "s_or_b32 s60, s60, 805306368\n\t" \
  "v_mov_b32 v76, s60\n\t" \
  "v_mov_b32 v77, 0\n\t" \
  "s_mov_b64 exec, 1\n\t" \
  "buffer_atomic_cmpswap v[76:77], off, s[28:31], s92 sc1\n\t" \
  "s_mov_b64 exec, -1\n\t" \
  "s_branch LGO2_%=\n\t" \
  "LLAST2_%=:\n\t" \
  "s_waitcnt vmcnt(0) lgkmcnt(0)\n\t" \
  "s_barrier\n\t" \
  "s_branch LEND_%=\n\t" \
  ".p2align 6\n\t" \
  "LG3_%=:\n\t" \
  "s_barrier\n\t" \
  "buffer_load_dwordx4 v[52:55], v69, s[28:31], s50 offen sc1\n\t" \
  "buffer_load_dwordx4 v[56:59], v69, s[28:31], s50 offen offset:1024 sc1\n\t" \
  "s_add_u32 s64, s50, 16384\n\t" \
  "s_movk_i32 m0, 11264\n\t" \
  "v_lshlrev_b32 v75, 1, v69\n\t" \
  "buffer_load_dword v75, s[28:31], s64 offen sc1 lds\n\t" \
  "s_lshr_b32 s61, s20, 3\n\t" \
  "s_mul_i32 s47, s61, " CH_KD "\n\t" \
  "s_lshr_b32 s47, s47, 10\n\t" \
  "s_lshl_b32 s47, s47, 8\n\t" \
  "s_mul_i32 s61, s61, " CH_KB "\n\t" \
  "s_lshr_b32 s61, s61, 10\n\t" \
  "s_min_u32 s61, s61, 100\n\t" \
  "s_or_b32 s47, s47, s61\n\t" \
  "s_cmp_lt_u32 s48, " CH_ES "\n\t" \
  "s_cselect_b32 s60, " CH_EX ", 0\n\t" \
  "s_add_u32 s47, s47, s60\n\t" \
  "s_not_b32 s47, s47\n\t" \
  "s_add_u32 s71, s53, s46\n\t" \
  "ds_read_b128 v[44:47], v70 offset:0\n\t" \
  "ds_read_b128 v[48:51], v70 offset:1024\n\t" \
  "ds_read_b128 v[60:63], v70 offset:2048\n\t" \
  "ds_read_b128 v[64:67], v70 offset:3072\n\t" \
  "v_mov_b64 v[36:37], 0\n\t" \
  "v_mov_b64 v[38:39], 0\n\t" \
  "v_mov_b64 v[40:41], 0\n\t" \
  "v_mov_b64 v[42:43], 0\n\t" \
  "v_lshrrev_b32 v71, 1, v69\n\t" \
  "v_add_u32 v71, 4, v71\n\t" \
  "v_lshrrev_b32 v72, 3, v69\n\t" \
  "v_and_b32 v72, 0x78, v72\n\t" \
  "v_and_b32 v74, 0x30, v69\n\t" \
  "v_lshl_add_u32 v72, v74, 3, v72\n\t" \
  "s_add_u32 s54, s53, s36\n\t" \
  "s_add_u32 s55, s54, 573440\n\t" \
  "s_add_u32 s54, s54, 135168\n\t" \
  "s_add_u32 s75, s54, 262144\n\t" \
  "s_add_u32 s80, s53, s26\n\t" \
  "s_or_b32 s87, s52, 0x1fff80\n\t" \
  "s_and_b32 s87, s87, -128\n\t" \
  "s_add_u32 s87, s87, 0x200000\n\t" \
  "s_barrier\n\t" \
  "ds_read_b128 v[0:3], v69 offset:0\n\t" \
  "ds_read_b128 v[4:7], v69 offset:1024\n\t" \
  "ds_read_b128 v[8:11], v69 offset:2048\n\t" \
  "ds_read_b128 v[12:15], v69 offset:3072\n\t" \
  "s_waitcnt lgkmcnt(3)\n\t" \
  "v_smfmac_f32_16x16x64_bf16 v[36:39], v[0:3], v[44:51], v68\n\t" \
  "ds_read_b128 v[44:47], v70 offset:4096\n\t" \
  "v_smfmac_f32_16x16x64_bf16 v[40:43], v[0:3], v[60:67], v68\n\t" \
  "ds_read_b128 v[48:51], v70 offset:5120\n\t" \
  "s_waitcnt lgkmcnt(4)\n\t" \
  "v_smfmac_f32_16x16x64_bf16 v[36:39], v[4:7], a[32:39], v68\n\t" \
  "ds_read_b128 v[60:63], v70 offset:6144\n\t" \
  "v_smfmac_f32_16x16x64_bf16 v[40:43], v[4:7], a[40:47], v68\n\t" \
  "ds_read_b128 v[64:67], v70 offset:7168\n\t" \
  "s_waitcnt lgkmcnt(5)\n\t" \
  "v_smfmac_f32_16x16x64_bf16 v[36:39], v[8:11], a[80:87], v68\n\t" \
  "s_barrier\n\t" \
  "ds_read_b128 v[16:19], v69 offset:4096\n\t" \
  "v_smfmac_f32_16x16x64_bf16 v[40:43], v[8:11], a[88:95], v68\n\t" \
  "ds_read_b128 v[20:23], v69 offset:5120\n\t" \
  "s_waitcnt lgkmcnt(6)\n\t" \
  "v_smfmac_f32_16x16x64_bf16 v[36:39], v[12:15], a[120:127], v68\n\t" \
  "ds_read_b128 v[24:27], v69 offset:6144\n\t" \
  "s_waitcnt lgkmcnt(5)\n\t" \
  "v_smfmac_f32_16x16x64_bf16 v[40:43], v[12:15], v[44:51], v68\n\t" \
  "ds_read_b128 v[28:31], v69 offset:7168\n\t" \
  "s_waitcnt lgkmcnt(3)\n\t" \
  "v_smfmac_f32_16x16x64_bf16 v[36:39], v[16:19], a[160:167], v68\n\t" \
  "ds_read_b128 v[32:35], v69 offset:8192\n\t" \
  "v_smfmac_f32_16x16x64_bf16 v[40:43], v[16:19], a[168:175], v68\n\t" \
  "s_waitcnt lgkmcnt(3)\n\t" \
  "v_mov_b64 v[44:45], 0\n\t" \
  "v_smfmac_f32_16x16x64_bf16 v[36:39], v[20:23], a[200:207], v68\n\t" \
  "s_waitcnt lgkmcnt(2)\n\t" \
  "v_mov_b64 v[46:47], 0\n\t" \
  "v_smfmac_f32_16x16x64_bf16 v[36:39], v[24:27], a[240:247], v68\n\t" \
  "s_waitcnt lgkmcnt(1)\n\t" \
  "v_mov_b64 v[48:49], 0\n\t" \
  "v_smfmac_f32_16x16x64_bf16 v[36:39], v[28:31], v[112:119], v68\n\t" \
  "s_waitcnt lgkmcnt(0)\n\t" \
  "v_mov_b64 v[50:51], 0\n\t" \
  "v_smfmac_f32_16x16x64_bf16 v[36:39], v[32:35], v[160:167], v68\n\t" \
  "v_smfmac_f32_16x16x64_bf16 v[40:43], v[20:23], v[60:67], v68\n\t" \
  "ds_read_b128 v[60:63], v70 offset:8192\n\t" \
  "v_smfmac_f32_16x16x64_bf16 v[40:43], v[24:27], a[248:255], v68\n\t" \
  "ds_read_b128 v[64:67], v70 offset:9216\n\t" \
  "v_add_f32 v74, v36, v38\n\t" \
  "v_smfmac_f32_16x16x64_bf16 v[40:43], v[28:31], v[120:127], v68\n\t" \
  "v_add_f32 v37, v37, v39\n\t" \
  "v_smfmac_f32_16x16x64_bf16 v[40:43], v[32:35], v[168:175], v68\n\t" \
  "v_cmp_u_f32 vcc, v74, v37\n\t" \
  "v_smfmac_f32_16x16x64_bf16 v[44:47], v[0:3], a[0:7], v68\n\t" \
  "s_cbranch_vccnz LFAIL3_%=\n\t" \
  "LGO3_%=:\n\t" \
  "v_cvt_pk_bf16_f32 v74, v74, v37\n\t" \
  "v_smfmac_f32_16x16x64_bf16 v[44:47], v[4:7], a[48:55], v68\n\t" \
  "s_waitcnt lgkmcnt(0)\n\t" \
  "v_add_f32 v75, v40, v42\n\t" \
  "v_smfmac_f32_16x16x64_bf16 v[44:47], v[8:11], v[60:67], v68\n\t" \
  "ds_read_b128 v[60:63], v70 offset:10240\n\t" \
  "v_add_f32 v41, v41, v43\n\t" \
  "v_cvt_pk_bf16_f32 v75, v75, v41\n\t" \
  "v_smfmac_f32_16x16x64_bf16 v[44:47], v[12:15], a[128:135], v68\n\t" \
  "ds_write2st64_b32 v71, v74, v75 offset0:40 offset1:42\n\t" \
  "v_smfmac_f32_16x16x64_bf16 v[44:47], v[16:19], a[176:183], v68\n\t" \
  "ds_read_b128 v[64:67], v70 offset:11264\n\t" \
  "s_memrealtime s[66:67]\n\t" \
  "v_smfmac_f32_16x16x64_bf16 v[44:47], v[20:23], a[208:215], v68\n\t" \
  "v_smfmac_f32_16x16x64_bf16 v[44:47], v[24:27], v[80:87], v68\n\t" \
  "v_smfmac_f32_16x16x64_bf16 v[44:47], v[28:31], v[128:135], v68\n\t" \
  "s_waitcnt lgkmcnt(0)\n\t" \
  "v_smfmac_f32_16x16x64_bf16 v[44:47], v[32:35], v[60:67], v68\n\t" \
  "s_barrier\n\t" \
  "ds_read_b64_tr_b16 v[76:77], v72 offset:10240\n\t" \
  "v_smfmac_f32_16x16x64_bf16 v[48:51], v[0:3], a[8:15], v68\n\t" \
  "s_lshl_b32 s60, s66, 8\n\t" \
  "s_sub_u32 s60, s47, s60\n\t" \
  "ds_read_b64_tr_b16 v[78:79], v72 offset:10752\n\t" \
  "v_mov_b32 v75, s60\n\t" \
  "v_smfmac_f32_16x16x64_bf16 v[48:51], v[4:7], a[56:63], v68\n\t" \
  "s_mov_b64 exec, s[24:25]\n\t" \
  "buffer_atomic_umin v75, v69, s[28:31], s71 offen sc1\n\t" \
  "s_mov_b64 exec, -1\n\t" \
  "v_add_f32 v74, v44, v46\n\t" \
  "v_smfmac_f32_16x16x64_bf16 v[48:51], v[8:11], a[96:103], v68\n\t" \
  "buffer_load_dwordx4 v[60:63], v69, s[28:31], s64 offen sc1\n\t" \
  "v_add_f32 v45, v45, v47\n\t" \
  "v_smfmac_f32_16x16x64_bf16 v[48:51], v[12:15], a[136:143], v68\n\t" \
  "s_waitcnt vmcnt(4)\n\t" \
  "s_waitcnt lgkmcnt(0)\n\t" \
  "v_cvt_pk_bf16_f32 v74, v74, v45\n\t" \
  "v_mfma_f32_16x16x32_bf16 v[36:39], v[52:55], v[76:79], 0\n\t" \
  "s_waitcnt vmcnt(3)\n\t" \
  "v_mfma_f32_16x16x32_bf16 v[40:43], v[56:59], v[76:79], 0\n\t" \
  "v_mov_b64 v[52:53], 0\n\t" \
  "v_mov_b64 v[54:55], 0\n\t" \
  "v_smfmac_f32_16x16x64_bf16 v[48:51], v[16:19], a[184:191], v68\n\t" \
  "buffer_load_dwordx4 v[64:67], v69, s[28:31], s64 offen offset:1024 sc1\n\t" \
  "v_mov_b64 v[56:57], 0\n\t" \
  "v_mov_b64 v[58:59], 0\n\t" \
  "v_smfmac_f32_16x16x64_bf16 v[48:51], v[20:23], a[216:223], v68\n\t" \
  "ds_write_b32 v71, v74 offset:1024\n\t" \
  "v_add_f32_dpp v36, v40, v36 quad_perm:[2,3,0,1] row_mask:0xf bank_mask:0xf\n\t" \
  "v_smfmac_f32_16x16x64_bf16 v[48:51], v[24:27], v[88:95], v68\n\t" \
  "v_add_f32_dpp v37, v41, v37 quad_perm:[2,3,0,1] row_mask:0xf bank_mask:0xf\n\t" \
  "v_smfmac_f32_16x16x64_bf16 v[48:51], v[28:31], v[136:143], v68\n\t" \
  "v_add_f32_dpp v38, v42, v38 quad_perm:[2,3,0,1] row_mask:0xf bank_mask:0xf\n\t" \
  "v_smfmac_f32_16x16x64_bf16 v[48:51], v[32:35], v[176:183], v68\n\t" \
  "v_add_f32_dpp v39, v43, v39 quad_perm:[2,3,0,1] row_mask:0xf bank_mask:0xf\n\t" \
  "v_smfmac_f32_16x16x64_bf16 v[52:55], v[0:3], a[16:23], v68\n\t" \
  "s_mov_b64 exec, s[58:59]\n\t" \
  "ds_write_b128 v69, v[36:39] offset:3104\n\t" \
  "s_mov_b64 exec, -1\n\t" \
  "v_smfmac_f32_16x16x64_bf16 v[52:55], v[4:7], a[64:71], v68\n\t" \
  "v_add_f32 v75, v48, v50\n\t" \
  "v_add_f32 v49, v49, v51\n\t" \
  "v_cvt_pk_bf16_f32 v75, v75, v49\n\t" \
  "v_smfmac_f32_16x16x64_bf16 v[56:59], v[0:3], a[24:31], v68\n\t" \
  "ds_write_b32 v71, v75 offset:1536\n\t" \
  "v_smfmac_f32_16x16x64_bf16 v[56:59], v[4:7], a[72:79], v68\n\t" \
  "ds_read_b64_tr_b16 v[0:1], v72 offset:9216\n\t" \
  "ds_read_b64_tr_b16 v[2:3], v72 offset:9728\n\t" \
  "v_smfmac_f32_16x16x64_bf16 v[52:55], v[8:11], a[104:111], v68\n\t" \
  "ds_read_b128 v[36:39], v70 offset:12288\n\t" \
  "v_smfmac_f32_16x16x64_bf16 v[56:59], v[8:11], a[112:119], v68\n\t" \
  "buffer_load_dword off, s[28:31], s87 sc1 lds\n\t" \
  "ds_read_b128 v[40:43], v70 offset:13312\n\t" \
  "v_smfmac_f32_16x16x64_bf16 v[52:55], v[12:15], a[144:151], v68\n\t" \
  "s_waitcnt lgkmcnt(4)\n\t" \
  "s_barrier\n\t" \
  "ds_read_b64_tr_b16 v[44:45], v72 offset:1024\n\t" \
  "ds_read_b64_tr_b16 v[46:47], v72 offset:1536\n\t" \
  "v_smfmac_f32_16x16x64_bf16 v[56:59], v[12:15], a[152:159], v68\n\t" \
  "ds_read_b64_tr_b16 v[48:49], v72 offset:0\n\t" \
  "ds_read_b64_tr_b16 v[50:51], v72 offset:512\n\t" \
  "v_smfmac_f32_16x16x64_bf16 v[52:55], v[16:19], a[192:199], v68\n\t" \
  "ds_read_b128 v[4:7], v69 offset:2048\n\t" \
  "s_waitcnt lgkmcnt(5)\n\t" \
  "v_smfmac_f32_16x16x64_bf16 v[56:59], v[16:19], v[36:43], v68\n\t" \
  "ds_read_b128 v[8:11], v69 offset:3072\n\t" \
  "s_waitcnt lgkmcnt(4)\n\t" \
  "v_mfma_f32_16x16x32_bf16 v[12:15], v[44:47], v[76:79], 0\n\t" \
  "ds_read_b128 v[36:39], v70 offset:14336\n\t" \
  "s_waitcnt lgkmcnt(3)\n\t" \
  "v_mfma_f32_16x16x32_bf16 v[12:15], v[48:51], v[0:3], v[12:15]\n\t" \
  "ds_read_b128 v[40:43], v70 offset:15360\n\t" \
  "v_smfmac_f32_16x16x64_bf16 v[52:55], v[20:23], a[224:231], v68\n\t" \
  "s_waitcnt lgkmcnt(2)\n\t" \
  "v_add_f32 v4, v4, v8\n\t" \
  "v_add_f32 v5, v5, v9\n\t" \
  "v_add_f32 v6, v6, v10\n\t" \
  "v_cndmask_b32 v78, v13, v14, s[56:57]\n\t" \
  "v_cndmask_b32 v76, v12, v15, s[56:57]\n\t" \
  "v_add_f32 v7, v7, v11\n\t" \
  "v_max3_f32 v77, v4, v5, v6\n\t" \
  "v_add_f32_dpp v76, v78, v76 quad_perm:[2,3,0,1] row_mask:0xf bank_mask:0xf\n\t" \
  "v_max3_f32 v77, v77, v7, v76\n\t" \
  "v_smfmac_f32_16x16x64_bf16 v[56:59], v[20:23], a[232:239], v68\n\t" \
  "ds_read_b128 v[44:47], v70 offset:16384\n\t" \
  "v_max_f32_dpp v77, v77, v77 quad_perm:[2,3,0,1] row_mask:0xf bank_mask:0xf\n\t" \
  "v_mov_b32 v78, v77\n\t" \
  "v_smfmac_f32_16x16x64_bf16 v[52:55], v[24:27], v[96:103], v68\n\t" \
  "ds_read_b128 v[48:51], v70 offset:17408\n\t" \
  "v_permlane16_swap_b32 v77, v78\n\t" \
  "v_max_f32 v77, v77, v78\n\t" \
  "v_mov_b32 v78, v77\n\t" \
  "v_smfmac_f32_16x16x64_bf16 v[52:55], v[28:31], v[144:151], v68\n\t" \
  "ds_read_b128 v[16:19], v70 offset:18432\n\t" \
  "v_permlane32_swap_b32 v77, v78\n\t" \
  "v_max_f32 v77, v77, v78\n\t" \
  "v_smfmac_f32_16x16x64_bf16 v[52:55], v[32:35], v[184:191], v68\n\t" \
  "ds_read_b128 v[20:23], v70 offset:19456\n\t" \
  "v_mul_f32 v77, 0xbe0293ee, v77\n\t" \
  "v_smfmac_f32_16x16x64_bf16 v[56:59], v[24:27], v[104:111], v68\n\t" \
  "v_fmamk_f32 v4, v4, 0x3e0293ee, v77\n\t" \
  "v_smfmac_f32_16x16x64_bf16 v[56:59], v[28:31], v[152:159], v68\n\t" \
  "ds_read_b128 v[24:27], v70 offset:22528\n\t" \
  "s_waitcnt lgkmcnt(4)\n\t" \
  "v_fmamk_f32 v5, v5, 0x3e0293ee, v77\n\t" \
  "v_smfmac_f32_16x16x64_bf16 v[56:59], v[32:35], v[36:43], v68\n\t" \
  "ds_read_b128 v[28:31], v70 offset:23552\n\t" \
  "v_fmamk_f32 v6, v6, 0x3e0293ee, v77\n\t" \
  "v_fmamk_f32 v7, v7, 0x3e0293ee, v77\n\t" \
  "v_fmamk_f32 v76, v76, 0x3e0293ee, v77\n\t" \
  "v_exp_f32 v4, v4\n\t" \
  "v_exp_f32 v5, v5\n\t" \
  "v_exp_f32 v6, v6\n\t" \
  "v_exp_f32 v7, v7\n\t" \
  "v_cvt_pk_bf16_f32 v4, v4, v5\n\t" \
  "v_exp_f32 v76, v76\n\t" \
  "v_cvt_pk_bf16_f32 v5, v6, v7\n\t" \
  "v_mov_b32_dpp v12, v4 quad_perm:[0,1,0,1] row_mask:0xf bank_mask:0xf\n\t" \
  "v_mov_b32_dpp v14, v4 quad_perm:[2,3,2,3] row_mask:0xf bank_mask:0xf\n\t" \
  "v_mov_b32_dpp v13, v5 quad_perm:[0,1,0,1] row_mask:0xf bank_mask:0xf\n\t" \
  "v_mov_b32_dpp v15, v5 quad_perm:[2,3,2,3] row_mask:0xf bank_mask:0xf\n\t" \
  "v_add_f32 v52, v52, v54\n\t" \
  "v_add_f32 v53, v53, v55\n\t" \
  "s_waitcnt vmcnt(2)\n\t" \
  "v_mfma_f32_16x16x32_bf16 v[0:3], v[60:63], v[12:15], 0\n\t" \
  "ds_read_b128 v[36:39], v70 offset:20480\n\t" \
  "v_add_f32 v56, v56, v58\n\t" \
  "v_add_f32 v57, v57, v59\n\t" \
  "v_cvt_pk_bf16_f32 v52, v52, v53\n\t" \
  "v_cvt_pk_bf16_f32 v53, v56, v57\n\t" \
  "s_waitcnt vmcnt(1)\n\t" \
  "v_mfma_f32_16x16x32_bf16 v[4:7], v[64:67], v[12:15], 0\n\t" \
  "ds_read_b128 v[40:43], v70 offset:21504\n\t" \
  "v_mov_b64 v[56:57], s[88:89]\n\t" \
  "v_mov_b64 v[58:59], s[88:89]\n\t" \
  "v_mov_b32_dpp v78, v76 quad_perm:[0,1,0,1] row_mask:0xf bank_mask:0xf\n\t" \
  "v_mov_b32_dpp v79, v76 quad_perm:[2,3,2,3] row_mask:0xf bank_mask:0xf\n\t" \
  "v_mfma_f32_16x16x32_bf16 v[32:35], v[56:59], v[12:15], 0\n\t" \
  "ds_read_b128 v[60:63], v70 offset:24576\n\t" \
  "v_cvt_pk_bf16_f32 v11, v78, v79\n\t" \
  "v_cvt_pk_bf16_f32 v8, v78, v79\n\t" \
  "v_mov_b64 v[54:55], 0\n\t" \
  "v_mov_b32 v9, 0\n\t" \
  "v_mov_b32 v10, 0\n\t" \
  "v_mov_b64 v[14:15], 0\n\t" \
  "s_nop 0\n\t" \
  "v_mfma_f32_16x16x32_bf16 v[32:35], v[54:57], v[8:11], v[32:35]\n\t" \
  "s_cmp_eq_u32 s48, 63\n\t" \
  "v_mfma_f32_16x16x32_bf16 v[0:3], v[52:55], v[8:11], v[0:3]\n\t" \
  "buffer_store_dwordx2 v[52:53], v71, s[28:31], s69 offen offset:3580 sc1\n\t" \
  "ds_read_b128 v[64:67], v70 offset:25600\n\t" \
  "v_mov_b64 v[56:57], 0\n\t" \
  "v_mov_b64 v[58:59], 0\n\t" \
  "v_mfma_f32_16x16x32_bf16 v[4:7], v[52:55], v[10:13], v[4:7]\n\t" \
  "s_cbranch_scc1 LLAST3_%=\n\t" \
  "v_rcp_f32 v74, v32\n\t" \
  "v_mov_b64 v[12:13], 0\n\t" \
  "v_mov_b32 v8, 0\n\t" \
  "v_pk_mul_f32 v[0:1], v[0:1], v[74:75] op_sel_hi:[1,0]\n\t" \
  "v_pk_mul_f32 v[2:3], v[2:3], v[74:75] op_sel_hi:[1,0]\n\t" \
  "v_cvt_pk_bf16_f32 v76, v0, v1\n\t" \
  "v_cvt_pk_bf16_f32 v77, v2, v3\n\t" \
  "v_pk_mul_f32 v[4:5], v[4:5], v[74:75] op_sel_hi:[1,0]\n\t" \
  "v_pk_mul_f32 v[6:7], v[6:7], v[74:75] op_sel_hi:[1,0]\n\t" \
  "v_cvt_pk_bf16_f32 v78, v4, v5\n\t" \
  "v_cvt_pk_bf16_f32 v79, v6, v7\n\t" \
  "ds_write_b128 v69, v[76:79] offset:7168\n\t" \
  "s_waitcnt lgkmcnt(2)\n\t" \
  "v_smfmac_f32_16x16x64_bf16 v[12:15], v[76:79], v[44:51], v68\n\t" \
  "ds_read_b128 v[44:47], v70 offset:26624\n\t" \
  "v_mov_b32 v11, 0\n\t" \
  "v_smfmac_f32_16x16x64_bf16 v[56:59], v[76:79], v[16:23], v68\n\t" \
  "ds_read_b128 v[48:51], v70 offset:27648\n\t" \
  "v_mov_b64 v[52:53], 0\n\t" \
  "v_smfmac_f32_16x16x64_bf16 v[8:11], v[76:79], v[36:43], v68\n\t" \
  "ds_read_b128 v[16:19], v70 offset:28672\n\t" \
  "v_smfmac_f32_16x16x64_bf16 v[52:55], v[76:79], v[24:31], v68\n\t" \
  "s_waitcnt lgkmcnt(3)\n\t" \
  "s_barrier\n\t" \
  "ds_read_b128 v[0:3], v69 offset:6144\n\t" \
  "ds_read_b128 v[4:7], v69 offset:5120\n\t" \
  "v_lshrrev_b32 v71, 7, v69\n\t" \
  "v_and_b32 v74, 0x70, v69\n\t" \
  "v_lshl_add_u32 v71, v71, 14, v74\n\t" \
  "v_bfe_u32 v72, v69, 2, 7\n\t" \
  "v_and_b32 v74, 0x200, v69\n\t" \
  "s_waitcnt lgkmcnt(1)\n\t" \
  "v_smfmac_f32_16x16x64_bf16 v[8:11], v[0:3], v[60:67], v68\n\t" \
  "ds_read_b128 v[20:23], v70 offset:29696\n\t" \
  "v_mov_b64 v[76:77], 0\n\t" \
  "v_smfmac_f32_16x16x64_bf16 v[12:15], v[0:3], v[200:207], v68\n\t" \
  "ds_read_b128 v[32:35], v69 offset:4096\n\t" \
  "v_mov_b64 v[78:79], 0\n\t" \
  "v_smfmac_f32_16x16x64_bf16 v[52:55], v[0:3], v[44:51], v68\n\t" \
  "buffer_load_dword v75, off, s[28:31], s80 sc1\n\t" \
  "ds_read_b128 v[24:27], v70 offset:30720\n\t" \
  "v_lshl_add_u32 v72, v74, 3, v72\n\t" \
  "v_smfmac_f32_16x16x64_bf16 v[56:59], v[0:3], v[232:239], v68\n\t" \
  "ds_read_b128 v[28:31], v70 offset:31744\n\t" \
  "s_waitcnt lgkmcnt(4)\n\t" \
  "v_smfmac_f32_16x16x64_bf16 v[12:15], v[4:7], v[208:215], v68\n\t" \
  "ds_read_b128 v[60:63], v70 offset:32768\n\t" \
  "s_waitcnt lgkmcnt(4)\n\t" \
  "v_smfmac_f32_16x16x64_bf16 v[8:11], v[4:7], v[16:23], v68\n\t" \
  "ds_read_b128 v[64:67], v70 offset:33792\n\t" \
  "s_waitcnt lgkmcnt(4)\n\t" \
  "v_smfmac_f32_16x16x64_bf16 v[8:11], v[32:35], v[192:199], v68\n\t" \
  "ds_read_b128 v[36:39], v73 offset:4096\n\t" \
  "v_smfmac_f32_16x16x64_bf16 v[12:15], v[32:35], v[216:223], v68\n\t" \
  "ds_read_b128 v[40:43], v73 offset:6144\n\t" \
  "s_waitcnt lgkmcnt(4)\n\t" \
  "v_smfmac_f32_16x16x64_bf16 v[52:55], v[4:7], v[24:31], v68\n\t" \
  "ds_read_b128 v[44:47], v70 offset:34816\n\t" \
  "v_cvt_pk_bf16_f32 v8, v8, v9\n\t" \
  "v_smfmac_f32_16x16x64_bf16 v[52:55], v[32:35], v[224:231], v68\n\t" \
  "ds_read_b128 v[48:51], v70 offset:35840\n\t" \
  "v_cvt_pk_bf16_f32 v9, v10, v11\n\t" \
  "v_smfmac_f32_16x16x64_bf16 v[56:59], v[4:7], v[240:247], v68\n\t" \
  "s_waitcnt lgkmcnt(4)\n\t" \
  "v_cvt_pk_bf16_f32 v10, v12, v13\n\t" \
  "v_smfmac_f32_16x16x64_bf16 v[56:59], v[32:35], v[60:67], v68\n\t" \
  "s_waitcnt lgkmcnt(3)\n\t" \
  "v_cvt_pk_bf16_f32 v11, v14, v15\n\t" \
  "v_smfmac_f32_16x16x64_bf16 v[76:79], v[36:39], v[248:255], v68\n\t" \
  "buffer_store_dwordx4 v[8:11], v71, s[28:31], s54 offen sc1\n\t" \
  "s_waitcnt lgkmcnt(0)\n\t" \
  "v_cvt_pk_bf16_f32 v52, v52, v53\n\t" \
  "v_smfmac_f32_16x16x64_bf16 v[76:79], v[40:43], v[44:51], v68\n\t" \
  "v_cvt_pk_bf16_f32 v53, v54, v55\n\t" \
  "v_cvt_pk_bf16_f32 v54, v56, v57\n\t" \
  "v_cvt_pk_bf16_f32 v55, v58, v59\n\t" \
  "buffer_store_dwordx4 v[52:55], v71, s[28:31], s75 offen sc1\n\t" \
  "s_nop 2\n\t" \
  "v_add_f32 v76, v76, v78\n\t" \
  "v_add_f32 v77, v77, v79\n\t" \
  "v_cvt_pk_bf16_f32 v76, v76, v77\n\t" \
  "buffer_store_dword v76, v72, s[28:31], s55 offen sc1\n\t" \
  "s_memrealtime s[70:71]\n\t" \
  "s_waitcnt vmcnt(3)\n\t" \
  "v_readfirstlane_b32 s65, v75\n\t" \
  "s_barrier\n\t" \
  "s_waitcnt lgkmcnt(0)\n\t" \
  "s_not_b32 s65, s65\n\t" \
  "s_lshl_b32 s61, s70, 8\n\t" \
  "s_andn2_b32 s64, s65, 0xff\n\t" \
  "s_sub_u32 s64, s64, s61\n\t" \
  "s_ashr_i32 s64, s64, 8\n\t" \
  "s_cmp_eq_u32 s65, 0\n\t" \
  "s_cselect_b32 s64, " CH_DLF ", s64\n\t" \
  "s_add_i32 s64, s64, " CH_RO3 "\n\t" \
  "s_min_i32 s64, s64, s34\n\t" \
  "s_max_i32 s64, s64, " CH_DLL1 "\n\t" \
  "s_add_u32 s64, s64, s70\n\t" \
  "s_mov_b32 s74, 0\n\t" \
  "v_and_b32 v37, 0x30, v69\n\t" \
  "v_add_u32 v37, s52, v37\n\t" \
  "s_mov_b32 exec_lo, 0\n\t" \
  "v_mov_b32 v37, s85\n\t" \
  "s_mov_b32 exec_lo, -1\n\t" \
  "s_cmp_lt_i32 s41, 0\n\t" \
  "s_cbranch_scc1 LIDLE_%=\n\t" \
  "s_movk_i32 s60, 8192\n\t" \
  "RWT3_%=:\n\t" \
  "s_memrealtime s[72:73]\n\t" \
  "s_sub_u32 s60, s60, 1\n\t" \
  "s_cselect_b32 s61, 0, -1\n\t" \
  "s_waitcnt lgkmcnt(0)\n\t" \
  "s_sub_i32 s73, s72, s64\n\t" \
  "s_and_b32 s73, s73, s61\n\t" \
  "s_cmp_lt_i32 s73, " CH_FIN "\n\t" \
  "s_cbranch_scc1 RWT3_%=\n\t" \
  "s_cmp_lt_i32 s73, 0\n\t" \
  "s_cbranch_scc1 RWT3_%=f\n\t" \
  "RWT3_%=g:\n\t" \
  "RRETRY3_%=:\n\t" \
  "buffer_load_dwordx4 v[0:3], v69, s[28:31], s97 offen offset:0 sc1\n\t" \
  "buffer_load_dwordx4 v[4:7], v69, s[28:31], s97 offen offset:1024 sc1\n\t" \
  "buffer_load_dwordx4 v[8:11], v69, s[28:31], s97 offen offset:2048 sc1\n\t" \
  "buffer_load_dwordx4 v[12:15], v69, s[28:31], s97 offen offset:3072 sc1\n\t" \
  "v_lshrrev_b32 v20, 2, v69\n\t" \
  "v_and_b32 v20, 16, v20\n\t" \
  "v_mov_b32 v21, 0x3f80\n\t" \
  "v_lshlrev_b32 v21, v20, v21\n\t" \
  "v_bfe_u32 v20, v69, 4, 2\n\t" \
  "v_cmp_eq_u32 vcc, 0, v20\n\t" \
  "s_nop 1\n\t" \
  "v_cndmask_b32 v32, 0, v21, vcc\n\t" \
  "v_cmp_eq_u32 vcc, 1, v20\n\t" \
  "s_nop 1\n\t" \
  "v_cndmask_b32 v33, 0, v21, vcc\n\t" \
  "v_cmp_eq_u32 vcc, 2, v20\n\t" \
  "s_nop 1\n\t" \
  "v_cndmask_b32 v34, 0, v21, vcc\n\t" \
  "v_cmp_eq_u32 vcc, 3, v20\n\t" \
  "s_nop 1\n\t" \
  "v_cndmask_b32 v35, 0, v21, vcc\n\t" \
  "v_bfe_u32 v38, v69, 7, 1\n\t" \
  "v_bfe_u32 v20, v69, 9, 1\n\t" \
  "v_lshl_add_u32 v38, v20, 2, v38\n\t" \
  "v_lshlrev_b32 v38, 12, v38\n\t" \
  "v_and_b32 v20, 0x60, v69\n\t" \
  "v_add_u32 v38, v38, v20\n\t" \
  "v_bfe_u32 v20, v69, 8, 1\n\t" \
  "v_lshl_add_u32 v38, v20, 4, v38\n\t" \
  "v_bfe_u32 v20, v69, 4, 1\n\t" \
  "v_lshl_add_u32 v38, v20, 3, v38\n\t" \
  "v_mov_b32 v44, 0\n\t" \
  "buffer_load_dword v39, v37, s[28:31], 0 offen sc1\n\t" \
  "s_waitcnt vmcnt(4)\n\t" \
  "v_mfma_f32_16x16x32_bf16 v[40:43], v[32:35], v[0:3], 0\n\t" \
  "v_max3_u32 v44, v44, v0, v1\n\t" \
  "v_max3_u32 v44, v44, v2, v3\n\t" \
  "s_waitcnt vmcnt(3)\n\t" \
  "v_mfma_f32_16x16x32_bf16 v[40:43], v[32:35], v[4:7], v[40:43]\n\t" \
  "v_max3_u32 v44, v44, v4, v5\n\t" \
  "v_max3_u32 v44, v44, v6, v7\n\t" \
  "s_waitcnt vmcnt(2)\n\t" \
  "v_mfma_f32_16x16x32_bf16 v[40:43], v[32:35], v[8:11], v[40:43]\n\t" \
  "v_max3_u32 v44, v44, v8, v9\n\t" \
  "v_max3_u32 v44, v44, v10, v11\n\t" \
  "s_waitcnt vmcnt(1)\n\t" \
  "v_mfma_f32_16x16x32_bf16 v[40:43], v[32:35], v[12:15], v[40:43]\n\t" \
  "v_max3_u32 v44, v44, v12, v13\n\t" \
  "v_max3_u32 v44, v44, v14, v15\n\t" \
  "v_cmp_eq_u32 vcc, -1, v44\n\t" \
  "s_cbranch_vccnz RFAIL3_%=\n\t" \
  "s_nop 3\n\t" \
  "v_add_f32_dpp v40, v40, v40 row_ror:8 row_mask:0xf bank_mask:0xf\n\t" \
  "v_add_f32_dpp v41, v41, v41 row_ror:8 row_mask:0xf bank_mask:0xf\n\t" \
  "v_add_f32_dpp v42, v42, v42 row_ror:8 row_mask:0xf bank_mask:0xf\n\t" \
  "v_add_f32_dpp v43, v43, v43 row_ror:8 row_mask:0xf bank_mask:0xf\n\t" \
  "v_cvt_pk_bf16_f32 v40, v40, v41\n\t" \
  "v_cvt_pk_bf16_f32 v41, v42, v43\n\t" \
  "buffer_store_dwordx2 v[40:41], v38, s[28:31], s98 offen sc1\n\t" \
  "RDONE3_%=:\n\t" \
  "s_barrier\n\t" \
  "s_waitcnt vmcnt(1)\n\t" \
  "v_cmp_eq_u32 vcc, 0, v39\n\t" \
  "s_sub_u32 s60, s70, s66\n\t" \
  "s_add_u32 s81, s81, 1\n\t" \
  "s_min_u32 s81, s81, s60\n\t" \
  "s_add_u32 s61, s81, 8\n\t" \
  "s_min_u32 s60, s60, s61\n\t" \
  "s_lshl_b32 s60, s60, 2\n\t" \
  "s_lshr_b32 s61, s20, 3\n\t" \
  "s_sub_u32 s20, s20, s61\n\t" \
  "s_add_u32 s20, s20, s60\n\t" \
  "s_add_u32 s48, s48, 1\n\t" \
  "s_mov_b32 s49, s53\n\t" \
  "s_add_u32 s53, s53, " CH_SLOT "\n\t" \
  "s_add_u32 s60, s53, " CH_SKIP "\n\t" \
  "s_lshr_b32 s61, s96, 1\n\t" \
  "s_add_u32 s86, s86, 1\n\t" \
  "s_cmp_eq_u32 s86, 3\n\t" \
  "s_cselect_b32 s53, s60, s53\n\t" \
  "s_cselect_b32 s96, s61, s96\n\t" \
  "s_cselect_b32 s86, 0, s86\n\t" \
  "s_cmp_ge_u32 s53, " CH_RING "\n\t" \
  "s_cselect_b32 s53, 0, s53\n\t" \
  "s_cselect_b32 s96, s21, s96\n\t" \
  "s_andn2_b32 s53, s53, 0x2000\n\t" \
  "s_and_b32 s60, s96, 1\n\t" \
  "s_lshl_b32 s60, s60, 13\n\t" \
  "s_or_b32 s53, s53, s60\n\t" \
  "s_mov_b32 s68, 0\n\t" \
  "s_add_u32 s97, s41, s53\n\t" \
  "s_add_u32 s98, s37, s53\n\t" \
  "s_add_u32 s82, s48, 0\n\t" \
  "s_and_b32 s82, s82, " CH_TRM "\n\t" \
  "s_lshr_b64 s[60:61], s[90:91], s82\n\t" \
  "s_and_b32 s60, s60, 1\n\t" \
  "s_lshl_b32 s60, s60, 13\n\t" \
  "s_add_u32 s82, s82, " CH_SG0 "\n\t" \
  "s_lshl_b32 s82, s82, 21\n\t" \
  "s_or_b32 s82, s82, s60\n\t" \
  "s_add_u32 s50, s82, s38\n\t" \
  "s_add_u32 s69, s82, s39\n\t" \
  "s_add_u32 s82, s48, 1\n\t" \
  "s_lshr_b64 s[60:61], s[90:91], s82\n\t" \
  "s_and_b32 s60, s60, 1\n\t" \
  "s_lshl_b32 s60, s60, 13\n\t" \
  "s_add_u32 s82, s82, " CH_SG0 "\n\t" \
  "s_lshl_b32 s82, s82, 21\n\t" \
  "s_or_b32 s82, s82, s60\n\t" \
  "s_add_u32 s52, s82, s43\n\t" \
  "s_add_u32 s82, s48, " CH_ARMD "\n\t" \
  "s_lshr_b64 s[60:61], s[90:91], s82\n\t" \
  "s_and_b32 s60, s60, 1\n\t" \
  "s_lshl_b32 s60, s60, 13\n\t" \
  "s_add_u32 s82, s82, " CH_SG0 "\n\t" \
  "s_lshl_b32 s82, s82, 21\n\t" \
  "s_or_b32 s82, s82, s60\n\t" \
  "s_add_u32 s85, s82, s45\n\t" \
  "s_cmp_lt_i32 s48, " CH_ARMS "\n\t" \
  "s_cselect_b32 s85, s27, s85\n\t" \
  "s_cmp_eq_u32 s34, 0\n\t" \
  "s_cbranch_scc1 LINI_%=\n\t" \
  "LTK_%=:\n\t" \
  CH_TOKCHK \
  "s_mov_b32 s61, 0\n\t" \
  "LTW_%=:\n\t" \
  "buffer_load_dword v1, v37, s[28:31], 0 offen sc1\n\t" \
  "s_waitcnt vmcnt(0)\n\t" \
  "v_cmp_eq_u32 vcc, 0, v1\n\t" \
  "s_cbranch_vccz LG3_%=\n\t" \
  "s_add_u32 s61, s61, 1\n\t" \
  "s_sleep 2\n\t" \
  "s_cmp_gt_u32 s61, s84\n\t" \
  "s_cbranch_scc1 LTG_%=\n\t" \
  "s_and_b32 s60, s61, 1023\n\t" \
  "s_cmp_lg_u32 s60, 0\n\t" \
  "s_cbranch_scc1 LTW_%=\n\t" \
  "buffer_load_dword v46, off, s[28:31], s92 sc1\n\t" \
  "s_waitcnt vmcnt(0)\n\t" \
  "v_readfirstlane_b32 s60, v46\n\t" \
  "s_nop 0\n\t" \
  "s_cmp_eq_u32 s60, 0\n\t" \
  "s_cbranch_scc1 LTW_%=\n\t" \
  "LTG_%=:\n\t" \
  "s_mov_b32 s84, 0\n\t" \
  "s_lshl_b32 s60, s36, 4\n\t" \
  "s_or_b32 s60, s60, s48\n\t" \
  "s_or_b32 s60, s60, 268435456\n\t" \
  "v_mov_b32 v76, s60\n\t" \
  "v_mov_b32 v77, 0\n\t" \
  "s_mov_b64 exec, 1\n\t" \
  "buffer_atomic_cmpswap v[76:77], off, s[28:31], s92 sc1\n\t" \
  "s_mov_b64 exec, -1\n\t" \
  "s_branch LG3_%=\n\t" \
  "LIDLE_%=:\n\t" \
  "s_add_u32 s64, s64, 8\n\t" \
  "s_movk_i32 s60, 8192\n\t" \
  "IWT_%=:\n\t" \
  "s_memrealtime s[72:73]\n\t" \
  "s_sub_u32 s60, s60, 1\n\t" \
  "s_cselect_b32 s61, 0, -1\n\t" \
  "s_waitcnt lgkmcnt(0)\n\t" \
  "s_sub_i32 s73, s72, s64\n\t" \
  "s_and_b32 s73, s73, s61\n\t" \
  "s_cmp_lt_i32 s73, 0\n\t" \
  "s_cbranch_scc1 IWT_%=\n\t" \
  "buffer_load_dword v39, v37, s[28:31], 0 offen sc1\n\t" \
  "buffer_store_dword v37, off, s[28:31], s41\n\t" \
  "s_branch RDONE3_%=\n\t" \
  "RFAIL3_%=:\n\t" \
  "s_add_u32 s74, s74, 1\n\t" \
  "s_sleep " CH_RSL "\n\t" \
  "s_waitcnt vmcnt(0)\n\t" \
  "s_cmp_gt_u32 s74, s84\n\t" \
  "s_cbranch_scc1 RG3_%=\n\t" \
  "s_and_b32 s60, s74, 1023\n\t" \
  "s_cmp_lg_u32 s60, 0\n\t" \
  "s_cbranch_scc1 RRETRY3_%=\n\t" \
  "buffer_load_dword v46, off, s[28:31], s92 sc1\n\t" \
  "s_waitcnt vmcnt(0)\n\t" \
  "v_readfirstlane_b32 s60, v46\n\t" \
  "s_nop 0\n\t" \
  "s_cmp_eq_u32 s60, 0\n\t" \
  "s_cbranch_scc1 RRETRY3_%=\n\t" \
  "RG3_%=:\n\t" \
  "s_mov_b32 s84, 0\n\t" \
  "s_lshl_b32 s60, s36, 4\n\t" \
  "s_or_b32 s60, s60, s48\n\t" \
  "s_or_b32 s60, s60, 536870912\n\t" \
  "v_mov_b32 v76, s60\n\t" \
  "v_mov_b32 v77, 0\n\t" \
  "s_mov_b64 exec, 1\n\t" \
  "buffer_atomic_cmpswap v[76:77], off, s[28:31], s92 sc1\n\t" \
  "s_mov_b64 exec, -1\n\t" \
  "s_branch RDONE3_%=\n\t" \
  "LFAIL3_%=:\n\t" \
  "v_max_u32 v75, v0, v1\n\t" \
  "v_max3_u32 v75, v75, v2, v3\n\t" \
  "v_max3_u32 v75, v75, v4, v5\n\t" \
  "v_max3_u32 v75, v75, v6, v7\n\t" \
  "v_max3_u32 v75, v75, v8, v9\n\t" \
  "v_max3_u32 v75, v75, v10, v11\n\t" \
  "v_max3_u32 v75, v75, v12, v13\n\t" \
  "v_max3_u32 v75, v75, v14, v15\n\t" \
  "v_max3_u32 v75, v75, v16, v17\n\t" \
  "v_max3_u32 v75, v75, v18, v19\n\t" \
  "v_max3_u32 v75, v75, v20, v21\n\t" \
  "v_max3_u32 v75, v75, v22, v23\n\t" \
  "v_max3_u32 v75, v75, v24, v25\n\t" \
  "v_max3_u32 v75, v75, v26, v27\n\t" \
  "v_max3_u32 v75, v75, v28, v29\n\t" \
  "v_max3_u32 v75, v75, v30, v31\n\t" \
  "v_max3_u32 v75, v75, v32, v33\n\t" \
  "v_max3_u32 v75, v75, v34, v35\n\t" \
  "v_cmp_eq_u32 vcc, -1, v75\n\t" \
  "s_cbranch_vccz LGO3_%=\n\t" \
  "s_add_u32 s68, s68, 1\n\t" \
  "s_waitcnt lgkmcnt(0)\n\t" \
  "s_cmp_gt_u32 s68, s83\n\t" \
  "s_cbranch_scc1 LGV3_%=\n\t" \
  "s_barrier\n\t" \
  "s_waitcnt vmcnt(8)\n\t" \
  "s_branch LG3_%=\n\t" \
  "RWT3_%=f:\n\t" \
  "s_cmp_lt_i32 s73, -2\n\t" \
  "s_cbranch_scc0 RWT3_%=g\n\t" \
  "s_nop 4\n\t" \
  "s_branch RWT3_%=g\n\t" \
  "LGV3_%=:\n\t" \
  "s_mov_b32 s83, 0\n\t" \
  "s_lshl_b32 s60, s36, 4\n\t" \
  "s_or_b32 s60, s60, s48\n\t" \
  "s_or_b32 s60, s60, 805306368\n\t" \
  "v_mov_b32 v76, s60\n\t" \
  "v_mov_b32 v77, 0\n\t" \
  "s_mov_b64 exec, 1\n\t" \
  "buffer_atomic_cmpswap v[76:77], off, s[28:31], s92 sc1\n\t" \
  "s_mov_b64 exec, -1\n\t" \
  "s_branch LGO3_%=\n\t" \
  "LLAST3_%=:\n\t" \
  "s_waitcnt vmcnt(0) lgkmcnt(0)\n\t" \
  "s_barrier\n\t" \
  "LEND_%=:\n\t" \
  ""
typedef unsigned v16u __attribute__((ext_vector_type(16)));
#define CH_WCLOB "a0", "a1", "a2", "a3", "a4", "a5", "a6", "a7", "a8", "a9", "a10", "a11", "a12", "a13", "a14", "a15", "a16", "a17", "a18", "a19", "a20", "a21", "a22", "a23", "a24", "a25", "a26", "a27", "a28", "a29", "a30", "a31", "a32", "a33", "a34", "a35", "a36", "a37", "a38", "a39", "a40", "a41", "a42", "a43", "a44", "a45", "a46", "a47", "a48", "a49", "a50", "a51", "a52", "a53", "a54", "a55", "a56", "a57", "a58", "a59", "a60", "a61", "a62", "a63", "a64", "a65", "a66", "a67", "a68", "a69", "a70", "a71", "a72", "a73", "a74", "a75", "a76", "a77", "a78", "a79", "a80", "a81", "a82", "a83", "a84", "a85", "a86", "a87", "a88", "a89", "a90", "a91", "a92", "a93", "a94", "a95", "a96", "a97", "a98", "a99", "a100", "a101", "a102", "a103", "a104", "a105", "a106", "a107", "a108", "a109", "a110", "a111", "a112", "a113", "a114", "a115", "a116", "a117", "a118", "a119", "a120", "a121", "a122", "a123", "a124", "a125", "a126", "a127", "a128", "a129", "a130", "a131", "a132", "a133", "a134", "a135", "a136", "a137", "a138", "a139", "a140", "a141", "a142", "a143", "a144", "a145", "a146", "a147", "a148", "a149", "a150", "a151", "a152", "a153", "a154", "a155", "a156", "a157", "a158", "a159", "a160", "a161", "a162", "a163", "a164", "a165", "a166", "a167", "a168", "a169", "a170", "a171", "a172", "a173", "a174", "a175", "a176", "a177", "a178", "a179", "a180", "a181", "a182", "a183", "a184", "a185", "a186", "a187", "a188", "a189", "a190", "a191", "a192", "a193", "a194", "a195", "a196", "a197", "a198", "a199", "a200", "a201", "a202", "a203", "a204", "a205", "a206", "a207", "a208", "a209", "a210", "a211", "a212", "a213", "a214", "a215", "a216", "a217", "a218", "a219", "a220", "a221", "a222", "a223", "a224", "a225", "a226", "a227", "a228", "a229", "a230", "a231", "a232", "a233", "a234", "a235", "a236", "a237", "a238", "a239", "a240", "a241", "a242", "a243", "a244", "a245", "a246", "a247", "a248", "a249", "a250", "a251", "a252", "a253", "a254", "a255", "v80", "v81", "v82", "v83", "v84", "v85", "v86", "v87", "v88", "v89", "v90", "v91", "v92", "v93", "v94", "v95", "v96", "v97", "v98", "v99", "v100", "v101", "v102", "v103", "v104", "v105", "v106", "v107", "v108", "v109", "v110", "v111", "v112", "v113", "v114", "v115", "v116", "v117", "v118", "v119", "v120", "v121", "v122", "v123", "v124", "v125", "v126", "v127", "v128", "v129", "v130", "v131", "v132", "v133", "v134", "v135", "v136", "v137", "v138", "v139", "v140", "v141", "v142", "v143", "v144", "v145", "v146", "v147", "v148", "v149", "v150", "v151", "v152", "v153", "v154", "v155", "v156", "v157", "v158", "v159", "v160", "v161", "v162", "v163", "v164", "v165", "v166", "v167", "v168", "v169", "v170", "v171", "v172", "v173", "v174", "v175", "v176", "v177", "v178", "v179", "v180", "v181", "v182", "v183", "v184", "v185", "v186", "v187", "v188", "v189", "v190", "v191", "v192", "v193", "v194", "v195", "v196", "v197", "v198", "v199", "v200", "v201", "v202", "v203", "v204", "v205", "v206", "v207", "v208", "v209", "v210", "v211", "v212", "v213", "v214", "v215", "v216", "v217", "v218", "v219", "v220", "v221", "v222", "v223", "v224", "v225", "v226", "v227", "v228", "v229", "v230", "v231", "v232", "v233", "v234", "v235", "v236", "v237", "v238", "v239", "v240", "v241", "v242", "v243", "v244", "v245", "v246", "v247", "v248", "v249", "v250", "v251", "v252", "v253", "v254", "v255"
#define CH_H(b) (8 * ((b) & 3) + ((b) >> 3))
#define CH_L(b) (((b) >> 2) & 1)
__device__ __forceinline__ void start_work(char* smem, int wk);
__device__ __forceinline__ void chain_wg(char* smem) {
  if (CH_L((int)blockIdx.x)) start_work(smem, CH_H((int)blockIdx.x));
  const MP p = getp();
  const int b = (int)blockIdx.x, h = CH_H(b), L = CH_L(b);
  int w = __builtin_amdgcn_readfirstlane(threadIdx.x >> 6);
  {
    unsigned id; asm volatile("s_getreg_b32 %0, hwreg(HW_REG_HW_ID, 4, 2)" : "=s"(id));
    volatile __attribute__((address_space(3))) unsigned* lw = (volatile __attribute__((address_space(3))) unsigned*)smem;
    lw[w] = 1u << id;
    BAR();
    if (__builtin_amdgcn_readfirstlane(lw[0] | lw[1] | lw[2] | lw[3]) == 15u) w = (int)((SMAP >> (4u * id)) & 3u);
  }
  unsigned lp16 = (threadIdx.x & 63u) * 16u;
  rsrc_t X = mkr(p.xb, XB_BYTES);
  if ((unsigned)(size_t)(__attribute__((address_space(3))) char*)smem != 0u) return;
  const unsigned wsz = DM * HD * 2u - (unsigned)(h * DH) * 2u;
  const rsrc_t RQ = mkr(p.Wq + h * DH, wsz), RK = mkr(p.Wk + h * DH, wsz), RV = mkr(p.Wv + h * DH, wsz), RO = mkr(p.Wo + (long)h * DH * DM, DH * DM * 2u);
  unsigned xcc; asm volatile("s_getreg_b32 %0, hwreg(HW_REG_XCC_ID)" : "=s"(xcc)); xcc &= 3u;
  unsigned d96 = 0u;
  const unsigned c43 = P_FA * PAIR + (unsigned)h * 128u + 32u;
  const unsigned h128 = (unsigned)h * 128u, uw = (unsigned)w, t = uw >> 1, T = uw & 1u, NONE = 0xF0000000u;
  const unsigned u0 = w == 3 ? (unsigned)h >> 2 : (unsigned)((h & 1) + (h >> 3) * 2 + ((h >> 1) & 3) * 8 + (w == 2 ? 32 : 0));
  const bool idle = w == 3 && (h & 3);
  const unsigned c37 = idle ? NONE : (w == 3 ? AGT + u0 * 128u : AGO + (u0 >> 5) * 32768u + (u0 & 31u) * 128u);
  const unsigned c41 = idle ? NONE : (w == 3 ? 524288u + (u0 >> 1) * 16384u + (u0 & 1u) * 4096u : (u0 & 7u) * 16384u + ((u0 >> 3) & 1u) * 4096u + (u0 >> 4) * 131072u);
  const unsigned c42 = AGO + (xcc & 1u) * 16384u + (xcc >> 1) * 4096u;
  const unsigned c46 = (w == 3 ? (h & 7) == 5 : h == 30) ? AGT + 1024u : NONE;
  const unsigned c38 = (unsigned)(3 * h + 1) * PAIR + T * 4096u + t * 2048u, c39 = (P_KV + (unsigned)(h >> 1)) * PAIR + (unsigned)(h & 1) * 4096u;
  const unsigned c45 = P_FB * PAIR + 4096u + (unsigned)(16 + (h & 15)) * 128u;
  unsigned d48 = 0u, d49 = 0u, d53 = SLOT, d86 = 1u, d65 = rtc();
  unsigned mn = 120u;
  v4u stt;
  asm volatile("s_waitcnt vmcnt(0) lgkmcnt(0)\n\t" CH_ASM "LCHEND:"
               : "+{s48}"(d48), "+{s49}"(d49), "+{s53}"(d53), "+{s65}"(d65), "+{s81}"(mn), "+{s86}"(d86), "+{s96}"(d96), "=&{s[76:79]}"(stt), "+{v69}"(lp16), "+{s[28:31]}"(X)
               : [Wq] "s"(RQ), [Wk] "s"(RK), [Wv] "s"(RV), [Wo] "s"(RO), "{s33}"(HT_OFF + 8192u + 256u + (unsigned)b * 4u), "{s36}"(h128), "{s37}"(c37), "{s40}"(w), "{s41}"(c41), "{s42}"(c42), "{s46}"(c46), "{s92}"(TMO_OFF), "{s38}"(c38), "{s39}"(c39), "{s43}"(c43), "{s45}"(c45), "{s32}"(L), "{s26}"(AGT + 1024u + xcc * 128u), "{s27}"(RDY_OFF(0) + (unsigned)((h & 7) * 1024 + (b & 7) * 128)), [ctab] "i"(CTAB_OFF), [ring] "i"(SRING / 3 * XBLK), [trm] "i"(TRING - 1), [ex] "i"(ED * 256 + EB), [dl1l] "i"(DL1 - LATE), [armd] "i"(3 - ARMK), [arms] "i"(ARMK - 3),
                 [cap] "i"(SPIN_CAP)
               : "v0", "v1", "v2", "v3", "v4", "v5", "v6", "v7", "v8", "v9", "v10", "v11", "v12", "v13", "v14", "v15", "v16", "v17", "v18", "v19",
                 "v20", "v21", "v22", "v23", "v24", "v25", "v26", "v27", "v28", "v29", "v30", "v31", "v32", "v33", "v34", "v35", "v36", "v37", "v38", "v39",
                 "v40", "v41", "v42", "v43", "v44", "v45", "v46", "v47", "v48", "v49", "v50", "v51", "v52", "v53", "v54", "v55", "v56", "v57", "v58", "v59",
                 "v60", "v61", "v62", "v63", "v64", "v65", "v66", "v67", "v68", "v70", "v71", "v72", "v73", "v74", "v75", "v76", "v77", "v78", "v79",
                 CH_WCLOB, "s20", "s21", "s34", "s35", "s24", "s25", "s90", "s91", "s47", "s87", "s50", "s51", "s52", "s69", "s82", "s85", "s54", "s75", "s80", "s55", "s56", "s57", "s58", "s59", "s60", "s61", "s62", "s63", "s64", "s66", "s67", "s68", "s70", "s71", "s72", "s73", "s74", "s88", "s89",
                 "s83", "s84", "s97", "s98", "s99", "s93", "s94", "s95", "m0", "vcc", "scc", "memory");
}
#define QLEAD 4
#define RONE 2
#define OAUX 0
#define PFM 1
#define PSP 4
#define QLEAD1 2
#define QXW -18
#define QXC QXW
#define TK_QXW "%[qxw]"
#define TK_QXC "%[qxc]"
#define PSG 16
#define TK_PSG CH_S(PSG)
#define AX 30
#define AX1 45
#define AM 22
#define TK_AM CH_S(AM)
#define PSK 2
#define PSH 2
#define TK_CAP "%[cap]"
#define TK_CTAB "%[ctab]"
#define TK_RM "%[rm]"
#define TK_TRM "%[trm]"
#define TK_TRN "%[trn]"
#define TK_SG0 CH_S(SG0)
#define TK_PSK CH_S(PSK)
#define TK_PSH CH_S(PSH)
#define AP_ASM \
  "v_lshrrev_b32 v40, 8, v0\n\t" \
  "v_mul_u32_u24 v40, 4608, v40\n\t" \
  "v_and_b32 v41, 0xf0, v0\n\t" \
  "v_lshrrev_b32 v42, 3, v41\n\t" \
  "v_add3_u32 v41, v41, v40, s64\n\t" \
  "v_add3_u32 v42, v42, v40, s65\n\t" \
  "s_mov_b32 s62, 0x05040100\n\t" \
  "s_mov_b32 s63, 0x07060302\n\t" \
  "s_xor_b32 s60, s66, 0\n\t" \
  "s_add_u32 s60, s60, 0\n\t" \
  "s_add_u32 s61, s60, 18432\n\t" \
  "buffer_load_dwordx4 v[48:51], v41, %[W], s60 offen offset:0\n\t" \
  "buffer_load_dwordx4 v[52:55], v41, %[W], s60 offen offset:1152\n\t" \
  "buffer_load_dwordx4 v[56:59], v41, %[W], s60 offen offset:2304\n\t" \
  "buffer_load_dwordx4 v[60:63], v41, %[W], s60 offen offset:3456\n\t" \
  "buffer_load_dwordx4 v[64:67], v41, %[W], s61 offen offset:0\n\t" \
  "buffer_load_dwordx4 v[68:71], v41, %[W], s61 offen offset:1152\n\t" \
  "buffer_load_dwordx4 v[72:75], v41, %[W], s61 offen offset:2304\n\t" \
  "buffer_load_dwordx4 v[76:79], v41, %[W], s61 offen offset:3456\n\t" \
  "s_xor_b32 s60, s66, 0\n\t" \
  "s_add_u32 s60, s60, 36864\n\t" \
  "s_add_u32 s61, s60, 18432\n\t" \
  "buffer_load_dwordx4 v[80:83], v41, %[W], s60 offen offset:0\n\t" \
  "buffer_load_dwordx4 v[84:87], v41, %[W], s60 offen offset:1152\n\t" \
  "buffer_load_dwordx4 v[88:91], v41, %[W], s60 offen offset:2304\n\t" \
  "buffer_load_dwordx4 v[92:95], v41, %[W], s60 offen offset:3456\n\t" \
  "buffer_load_dwordx4 v[96:99], v41, %[W], s61 offen offset:0\n\t" \
  "buffer_load_dwordx4 v[100:103], v41, %[W], s61 offen offset:1152\n\t" \
  "buffer_load_dwordx4 v[104:107], v41, %[W], s61 offen offset:2304\n\t" \
  "buffer_load_dwordx4 v[108:111], v41, %[W], s61 offen offset:3456\n\t" \
  "s_xor_b32 s60, s66, 0\n\t" \
  "s_add_u32 s60, s60, 73728\n\t" \
  "s_add_u32 s61, s60, 18432\n\t" \
  "buffer_load_dwordx4 v[112:115], v41, %[W], s60 offen offset:0\n\t" \
  "buffer_load_dwordx4 v[116:119], v41, %[W], s60 offen offset:1152\n\t" \
  "buffer_load_dwordx4 v[120:123], v41, %[W], s60 offen offset:2304\n\t" \
  "buffer_load_dwordx4 v[124:127], v41, %[W], s60 offen offset:3456\n\t" \
  "buffer_load_dwordx4 v[128:131], v41, %[W], s61 offen offset:0\n\t" \
  "buffer_load_dwordx4 v[132:135], v41, %[W], s61 offen offset:1152\n\t" \
  "buffer_load_dwordx4 v[136:139], v41, %[W], s61 offen offset:2304\n\t" \
  "buffer_load_dwordx4 v[140:143], v41, %[W], s61 offen offset:3456\n\t" \
  "s_xor_b32 s60, s66, 0\n\t" \
  "s_add_u32 s60, s60, 110592\n\t" \
  "s_add_u32 s61, s60, 18432\n\t" \
  "buffer_load_dwordx4 v[144:147], v41, %[W], s60 offen offset:0\n\t" \
  "buffer_load_dwordx4 v[148:151], v41, %[W], s60 offen offset:1152\n\t" \
  "buffer_load_dwordx4 v[152:155], v41, %[W], s60 offen offset:2304\n\t" \
  "buffer_load_dwordx4 v[156:159], v41, %[W], s60 offen offset:3456\n\t" \
  "buffer_load_dwordx4 v[160:163], v41, %[W], s61 offen offset:0\n\t" \
  "buffer_load_dwordx4 v[164:167], v41, %[W], s61 offen offset:1152\n\t" \
  "buffer_load_dwordx4 v[168:171], v41, %[W], s61 offen offset:2304\n\t" \
  "buffer_load_dwordx4 v[172:175], v41, %[W], s61 offen offset:3456\n\t" \
  "s_xor_b32 s60, s66, 147456\n\t" \
  "s_add_u32 s60, s60, 0\n\t" \
  "s_add_u32 s61, s60, 18432\n\t" \
  "buffer_load_dwordx4 v[176:179], v41, %[W], s60 offen offset:0\n\t" \
  "buffer_load_dwordx4 v[180:183], v41, %[W], s60 offen offset:1152\n\t" \
  "buffer_load_dwordx4 v[184:187], v41, %[W], s60 offen offset:2304\n\t" \
  "buffer_load_dwordx4 v[188:191], v41, %[W], s60 offen offset:3456\n\t" \
  "buffer_load_dwordx4 v[192:195], v41, %[W], s61 offen offset:0\n\t" \
  "buffer_load_dwordx4 v[196:199], v41, %[W], s61 offen offset:1152\n\t" \
  "buffer_load_dwordx4 v[200:203], v41, %[W], s61 offen offset:2304\n\t" \
  "buffer_load_dwordx4 v[204:207], v41, %[W], s61 offen offset:3456\n\t" \
  "s_waitcnt vmcnt(38)\n\t" \
  "v_perm_b32 v44, v52, v48, s62\n\t" \
  "s_waitcnt vmcnt(36)\n\t" \
  "v_perm_b32 v45, v60, v56, s62\n\t" \
  "s_waitcnt vmcnt(34)\n\t" \
  "v_perm_b32 v46, v68, v64, s62\n\t" \
  "s_waitcnt vmcnt(32)\n\t" \
  "v_perm_b32 v47, v76, v72, s62\n\t" \
  "v_accvgpr_write_b32 a0, v44\n\t" \
  "v_accvgpr_write_b32 a1, v45\n\t" \
  "v_accvgpr_write_b32 a2, v46\n\t" \
  "v_accvgpr_write_b32 a3, v47\n\t" \
  "v_perm_b32 v44, v52, v48, s63\n\t" \
  "v_perm_b32 v45, v60, v56, s63\n\t" \
  "v_perm_b32 v46, v68, v64, s63\n\t" \
  "v_perm_b32 v47, v76, v72, s63\n\t" \
  "v_accvgpr_write_b32 a4, v44\n\t" \
  "v_accvgpr_write_b32 a5, v45\n\t" \
  "v_accvgpr_write_b32 a6, v46\n\t" \
  "v_accvgpr_write_b32 a7, v47\n\t" \
  "v_perm_b32 v44, v53, v49, s62\n\t" \
  "v_perm_b32 v45, v61, v57, s62\n\t" \
  "v_perm_b32 v46, v69, v65, s62\n\t" \
  "v_perm_b32 v47, v77, v73, s62\n\t" \
  "v_accvgpr_write_b32 a8, v44\n\t" \
  "v_accvgpr_write_b32 a9, v45\n\t" \
  "v_accvgpr_write_b32 a10, v46\n\t" \
  "v_accvgpr_write_b32 a11, v47\n\t" \
  "v_perm_b32 v44, v53, v49, s63\n\t" \
  "v_perm_b32 v45, v61, v57, s63\n\t" \
  "v_perm_b32 v46, v69, v65, s63\n\t" \
  "v_perm_b32 v47, v77, v73, s63\n\t" \
  "v_accvgpr_write_b32 a12, v44\n\t" \
  "v_accvgpr_write_b32 a13, v45\n\t" \
  "v_accvgpr_write_b32 a14, v46\n\t" \
  "v_accvgpr_write_b32 a15, v47\n\t" \
  "v_perm_b32 v44, v54, v50, s62\n\t" \
  "v_perm_b32 v45, v62, v58, s62\n\t" \
  "v_perm_b32 v46, v70, v66, s62\n\t" \
  "v_perm_b32 v47, v78, v74, s62\n\t" \
  "v_accvgpr_write_b32 a16, v44\n\t" \
  "v_accvgpr_write_b32 a17, v45\n\t" \
  "v_accvgpr_write_b32 a18, v46\n\t" \
  "v_accvgpr_write_b32 a19, v47\n\t" \
  "v_perm_b32 v44, v54, v50, s63\n\t" \
  "v_perm_b32 v45, v62, v58, s63\n\t" \
  "v_perm_b32 v46, v70, v66, s63\n\t" \
  "v_perm_b32 v47, v78, v74, s63\n\t" \
  "v_accvgpr_write_b32 a20, v44\n\t" \
  "v_accvgpr_write_b32 a21, v45\n\t" \
  "v_accvgpr_write_b32 a22, v46\n\t" \
  "v_accvgpr_write_b32 a23, v47\n\t" \
  "v_perm_b32 v44, v55, v51, s62\n\t" \
  "v_perm_b32 v45, v63, v59, s62\n\t" \
  "v_perm_b32 v46, v71, v67, s62\n\t" \
  "v_perm_b32 v47, v79, v75, s62\n\t" \
  "v_accvgpr_write_b32 a24, v44\n\t" \
  "v_accvgpr_write_b32 a25, v45\n\t" \
  "v_accvgpr_write_b32 a26, v46\n\t" \
  "v_accvgpr_write_b32 a27, v47\n\t" \
  "v_perm_b32 v44, v55, v51, s63\n\t" \
  "v_perm_b32 v45, v63, v59, s63\n\t" \
  "v_perm_b32 v46, v71, v67, s63\n\t" \
  "v_perm_b32 v47, v79, v75, s63\n\t" \
  "v_accvgpr_write_b32 a28, v44\n\t" \
  "v_accvgpr_write_b32 a29, v45\n\t" \
  "v_accvgpr_write_b32 a30, v46\n\t" \
  "v_accvgpr_write_b32 a31, v47\n\t" \
  "s_xor_b32 s60, s66, 147456\n\t" \
  "s_add_u32 s60, s60, 36864\n\t" \
  "s_add_u32 s61, s60, 18432\n\t" \
  "buffer_load_dwordx4 v[48:51], v41, %[W], s60 offen offset:0\n\t" \
  "buffer_load_dwordx4 v[52:55], v41, %[W], s60 offen offset:1152\n\t" \
  "buffer_load_dwordx4 v[56:59], v41, %[W], s60 offen offset:2304\n\t" \
  "buffer_load_dwordx4 v[60:63], v41, %[W], s60 offen offset:3456\n\t" \
  "buffer_load_dwordx4 v[64:67], v41, %[W], s61 offen offset:0\n\t" \
  "buffer_load_dwordx4 v[68:71], v41, %[W], s61 offen offset:1152\n\t" \
  "buffer_load_dwordx4 v[72:75], v41, %[W], s61 offen offset:2304\n\t" \
  "buffer_load_dwordx4 v[76:79], v41, %[W], s61 offen offset:3456\n\t" \
  "s_waitcnt vmcnt(38)\n\t" \
  "v_perm_b32 v44, v84, v80, s62\n\t" \
  "s_waitcnt vmcnt(36)\n\t" \
  "v_perm_b32 v45, v92, v88, s62\n\t" \
  "s_waitcnt vmcnt(34)\n\t" \
  "v_perm_b32 v46, v100, v96, s62\n\t" \
  "s_waitcnt vmcnt(32)\n\t" \
  "v_perm_b32 v47, v108, v104, s62\n\t" \
  "v_accvgpr_write_b32 a32, v44\n\t" \
  "v_accvgpr_write_b32 a33, v45\n\t" \
  "v_accvgpr_write_b32 a34, v46\n\t" \
  "v_accvgpr_write_b32 a35, v47\n\t" \
  "v_perm_b32 v44, v84, v80, s63\n\t" \
  "v_perm_b32 v45, v92, v88, s63\n\t" \
  "v_perm_b32 v46, v100, v96, s63\n\t" \
  "v_perm_b32 v47, v108, v104, s63\n\t" \
  "v_accvgpr_write_b32 a36, v44\n\t" \
  "v_accvgpr_write_b32 a37, v45\n\t" \
  "v_accvgpr_write_b32 a38, v46\n\t" \
  "v_accvgpr_write_b32 a39, v47\n\t" \
  "v_perm_b32 v44, v85, v81, s62\n\t" \
  "v_perm_b32 v45, v93, v89, s62\n\t" \
  "v_perm_b32 v46, v101, v97, s62\n\t" \
  "v_perm_b32 v47, v109, v105, s62\n\t" \
  "v_accvgpr_write_b32 a40, v44\n\t" \
  "v_accvgpr_write_b32 a41, v45\n\t" \
  "v_accvgpr_write_b32 a42, v46\n\t" \
  "v_accvgpr_write_b32 a43, v47\n\t" \
  "v_perm_b32 v44, v85, v81, s63\n\t" \
  "v_perm_b32 v45, v93, v89, s63\n\t" \
  "v_perm_b32 v46, v101, v97, s63\n\t" \
  "v_perm_b32 v47, v109, v105, s63\n\t" \
  "v_accvgpr_write_b32 a44, v44\n\t" \
  "v_accvgpr_write_b32 a45, v45\n\t" \
  "v_accvgpr_write_b32 a46, v46\n\t" \
  "v_accvgpr_write_b32 a47, v47\n\t" \
  "v_perm_b32 v44, v86, v82, s62\n\t" \
  "v_perm_b32 v45, v94, v90, s62\n\t" \
  "v_perm_b32 v46, v102, v98, s62\n\t" \
  "v_perm_b32 v47, v110, v106, s62\n\t" \
  "v_accvgpr_write_b32 a48, v44\n\t" \
  "v_accvgpr_write_b32 a49, v45\n\t" \
  "v_accvgpr_write_b32 a50, v46\n\t" \
  "v_accvgpr_write_b32 a51, v47\n\t" \
  "v_perm_b32 v44, v86, v82, s63\n\t" \
  "v_perm_b32 v45, v94, v90, s63\n\t" \
  "v_perm_b32 v46, v102, v98, s63\n\t" \
  "v_perm_b32 v47, v110, v106, s63\n\t" \
  "v_accvgpr_write_b32 a52, v44\n\t" \
  "v_accvgpr_write_b32 a53, v45\n\t" \
  "v_accvgpr_write_b32 a54, v46\n\t" \
  "v_accvgpr_write_b32 a55, v47\n\t" \
  "v_perm_b32 v44, v87, v83, s62\n\t" \
  "v_perm_b32 v45, v95, v91, s62\n\t" \
  "v_perm_b32 v46, v103, v99, s62\n\t" \
  "v_perm_b32 v47, v111, v107, s62\n\t" \
  "v_accvgpr_write_b32 a56, v44\n\t" \
  "v_accvgpr_write_b32 a57, v45\n\t" \
  "v_accvgpr_write_b32 a58, v46\n\t" \
  "v_accvgpr_write_b32 a59, v47\n\t" \
  "v_perm_b32 v44, v87, v83, s63\n\t" \
  "v_perm_b32 v45, v95, v91, s63\n\t" \
  "v_perm_b32 v46, v103, v99, s63\n\t" \
  "v_perm_b32 v47, v111, v107, s63\n\t" \
  "v_accvgpr_write_b32 a60, v44\n\t" \
  "v_accvgpr_write_b32 a61, v45\n\t" \
  "v_accvgpr_write_b32 a62, v46\n\t" \
  "v_accvgpr_write_b32 a63, v47\n\t" \
  "s_xor_b32 s60, s66, 147456\n\t" \
  "s_add_u32 s60, s60, 73728\n\t" \
  "s_add_u32 s61, s60, 18432\n\t" \
  "buffer_load_dwordx4 v[80:83], v41, %[W], s60 offen offset:0\n\t" \
  "buffer_load_dwordx4 v[84:87], v41, %[W], s60 offen offset:1152\n\t" \
  "buffer_load_dwordx4 v[88:91], v41, %[W], s60 offen offset:2304\n\t" \
  "buffer_load_dwordx4 v[92:95], v41, %[W], s60 offen offset:3456\n\t" \
  "buffer_load_dwordx4 v[96:99], v41, %[W], s61 offen offset:0\n\t" \
  "buffer_load_dwordx4 v[100:103], v41, %[W], s61 offen offset:1152\n\t" \
  "buffer_load_dwordx4 v[104:107], v41, %[W], s61 offen offset:2304\n\t" \
  "buffer_load_dwordx4 v[108:111], v41, %[W], s61 offen offset:3456\n\t" \
  "s_waitcnt vmcnt(38)\n\t" \
  "v_perm_b32 v44, v116, v112, s62\n\t" \
  "s_waitcnt vmcnt(36)\n\t" \
  "v_perm_b32 v45, v124, v120, s62\n\t" \
  "s_waitcnt vmcnt(34)\n\t" \
  "v_perm_b32 v46, v132, v128, s62\n\t" \
  "s_waitcnt vmcnt(32)\n\t" \
  "v_perm_b32 v47, v140, v136, s62\n\t" \
  "v_accvgpr_write_b32 a64, v44\n\t" \
  "v_accvgpr_write_b32 a65, v45\n\t" \
  "v_accvgpr_write_b32 a66, v46\n\t" \
  "v_accvgpr_write_b32 a67, v47\n\t" \
  "v_perm_b32 v44, v116, v112, s63\n\t" \
  "v_perm_b32 v45, v124, v120, s63\n\t" \
  "v_perm_b32 v46, v132, v128, s63\n\t" \
  "v_perm_b32 v47, v140, v136, s63\n\t" \
  "v_accvgpr_write_b32 a68, v44\n\t" \
  "v_accvgpr_write_b32 a69, v45\n\t" \
  "v_accvgpr_write_b32 a70, v46\n\t" \
  "v_accvgpr_write_b32 a71, v47\n\t" \
  "v_perm_b32 v44, v117, v113, s62\n\t" \
  "v_perm_b32 v45, v125, v121, s62\n\t" \
  "v_perm_b32 v46, v133, v129, s62\n\t" \
  "v_perm_b32 v47, v141, v137, s62\n\t" \
  "v_accvgpr_write_b32 a72, v44\n\t" \
  "v_accvgpr_write_b32 a73, v45\n\t" \
  "v_accvgpr_write_b32 a74, v46\n\t" \
  "v_accvgpr_write_b32 a75, v47\n\t" \
  "v_perm_b32 v44, v117, v113, s63\n\t" \
  "v_perm_b32 v45, v125, v121, s63\n\t" \
  "v_perm_b32 v46, v133, v129, s63\n\t" \
  "v_perm_b32 v47, v141, v137, s63\n\t" \
  "v_accvgpr_write_b32 a76, v44\n\t" \
  "v_accvgpr_write_b32 a77, v45\n\t" \
  "v_accvgpr_write_b32 a78, v46\n\t" \
  "v_accvgpr_write_b32 a79, v47\n\t" \
  "v_perm_b32 v44, v118, v114, s62\n\t" \
  "v_perm_b32 v45, v126, v122, s62\n\t" \
  "v_perm_b32 v46, v134, v130, s62\n\t" \
  "v_perm_b32 v47, v142, v138, s62\n\t" \
  "v_accvgpr_write_b32 a80, v44\n\t" \
  "v_accvgpr_write_b32 a81, v45\n\t" \
  "v_accvgpr_write_b32 a82, v46\n\t" \
  "v_accvgpr_write_b32 a83, v47\n\t" \
  "v_perm_b32 v44, v118, v114, s63\n\t" \
  "v_perm_b32 v45, v126, v122, s63\n\t" \
  "v_perm_b32 v46, v134, v130, s63\n\t" \
  "v_perm_b32 v47, v142, v138, s63\n\t" \
  "v_accvgpr_write_b32 a84, v44\n\t" \
  "v_accvgpr_write_b32 a85, v45\n\t" \
  "v_accvgpr_write_b32 a86, v46\n\t" \
  "v_accvgpr_write_b32 a87, v47\n\t" \
  "v_perm_b32 v44, v119, v115, s62\n\t" \
  "v_perm_b32 v45, v127, v123, s62\n\t" \
  "v_perm_b32 v46, v135, v131, s62\n\t" \
  "v_perm_b32 v47, v143, v139, s62\n\t" \
  "v_accvgpr_write_b32 a88, v44\n\t" \
  "v_accvgpr_write_b32 a89, v45\n\t" \
  "v_accvgpr_write_b32 a90, v46\n\t" \
  "v_accvgpr_write_b32 a91, v47\n\t" \
  "v_perm_b32 v44, v119, v115, s63\n\t" \
  "v_perm_b32 v45, v127, v123, s63\n\t" \
  "v_perm_b32 v46, v135, v131, s63\n\t" \
  "v_perm_b32 v47, v143, v139, s63\n\t" \
  "v_accvgpr_write_b32 a92, v44\n\t" \
  "v_accvgpr_write_b32 a93, v45\n\t" \
  "v_accvgpr_write_b32 a94, v46\n\t" \
  "v_accvgpr_write_b32 a95, v47\n\t" \
  "s_xor_b32 s60, s66, 147456\n\t" \
  "s_add_u32 s60, s60, 110592\n\t" \
  "s_add_u32 s61, s60, 18432\n\t" \
  "buffer_load_dwordx4 v[112:115], v41, %[W], s60 offen offset:0\n\t" \
  "buffer_load_dwordx4 v[116:119], v41, %[W], s60 offen offset:1152\n\t" \
  "buffer_load_dwordx4 v[120:123], v41, %[W], s60 offen offset:2304\n\t" \
  "buffer_load_dwordx4 v[124:127], v41, %[W], s60 offen offset:3456\n\t" \
  "buffer_load_dwordx4 v[128:131], v41, %[W], s61 offen offset:0\n\t" \
  "buffer_load_dwordx4 v[132:135], v41, %[W], s61 offen offset:1152\n\t" \
  "buffer_load_dwordx4 v[136:139], v41, %[W], s61 offen offset:2304\n\t" \
  "buffer_load_dwordx4 v[140:143], v41, %[W], s61 offen offset:3456\n\t" \
  "s_waitcnt vmcnt(38)\n\t" \
  "v_perm_b32 v44, v148, v144, s62\n\t" \
  "s_waitcnt vmcnt(36)\n\t" \
  "v_perm_b32 v45, v156, v152, s62\n\t" \
  "s_waitcnt vmcnt(34)\n\t" \
  "v_perm_b32 v46, v164, v160, s62\n\t" \
  "s_waitcnt vmcnt(32)\n\t" \
  "v_perm_b32 v47, v172, v168, s62\n\t" \
  "v_accvgpr_write_b32 a96, v44\n\t" \
  "v_accvgpr_write_b32 a97, v45\n\t" \
  "v_accvgpr_write_b32 a98, v46\n\t" \
  "v_accvgpr_write_b32 a99, v47\n\t" \
  "v_perm_b32 v44, v148, v144, s63\n\t" \
  "v_perm_b32 v45, v156, v152, s63\n\t" \
  "v_perm_b32 v46, v164, v160, s63\n\t" \
  "v_perm_b32 v47, v172, v168, s63\n\t" \
  "v_accvgpr_write_b32 a100, v44\n\t" \
  "v_accvgpr_write_b32 a101, v45\n\t" \
  "v_accvgpr_write_b32 a102, v46\n\t" \
  "v_accvgpr_write_b32 a103, v47\n\t" \
  "v_perm_b32 v44, v149, v145, s62\n\t" \
  "v_perm_b32 v45, v157, v153, s62\n\t" \
  "v_perm_b32 v46, v165, v161, s62\n\t" \
  "v_perm_b32 v47, v173, v169, s62\n\t" \
  "v_accvgpr_write_b32 a104, v44\n\t" \
  "v_accvgpr_write_b32 a105, v45\n\t" \
  "v_accvgpr_write_b32 a106, v46\n\t" \
  "v_accvgpr_write_b32 a107, v47\n\t" \
  "v_perm_b32 v44, v149, v145, s63\n\t" \
  "v_perm_b32 v45, v157, v153, s63\n\t" \
  "v_perm_b32 v46, v165, v161, s63\n\t" \
  "v_perm_b32 v47, v173, v169, s63\n\t" \
  "v_accvgpr_write_b32 a108, v44\n\t" \
  "v_accvgpr_write_b32 a109, v45\n\t" \
  "v_accvgpr_write_b32 a110, v46\n\t" \
  "v_accvgpr_write_b32 a111, v47\n\t" \
  "v_perm_b32 v44, v150, v146, s62\n\t" \
  "v_perm_b32 v45, v158, v154, s62\n\t" \
  "v_perm_b32 v46, v166, v162, s62\n\t" \
  "v_perm_b32 v47, v174, v170, s62\n\t" \
  "v_accvgpr_write_b32 a112, v44\n\t" \
  "v_accvgpr_write_b32 a113, v45\n\t" \
  "v_accvgpr_write_b32 a114, v46\n\t" \
  "v_accvgpr_write_b32 a115, v47\n\t" \
  "v_perm_b32 v44, v150, v146, s63\n\t" \
  "v_perm_b32 v45, v158, v154, s63\n\t" \
  "v_perm_b32 v46, v166, v162, s63\n\t" \
  "v_perm_b32 v47, v174, v170, s63\n\t" \
  "v_accvgpr_write_b32 a116, v44\n\t" \
  "v_accvgpr_write_b32 a117, v45\n\t" \
  "v_accvgpr_write_b32 a118, v46\n\t" \
  "v_accvgpr_write_b32 a119, v47\n\t" \
  "v_perm_b32 v44, v151, v147, s62\n\t" \
  "v_perm_b32 v45, v159, v155, s62\n\t" \
  "v_perm_b32 v46, v167, v163, s62\n\t" \
  "v_perm_b32 v47, v175, v171, s62\n\t" \
  "v_accvgpr_write_b32 a120, v44\n\t" \
  "v_accvgpr_write_b32 a121, v45\n\t" \
  "v_accvgpr_write_b32 a122, v46\n\t" \
  "v_accvgpr_write_b32 a123, v47\n\t" \
  "v_perm_b32 v44, v151, v147, s63\n\t" \
  "v_perm_b32 v45, v159, v155, s63\n\t" \
  "v_perm_b32 v46, v167, v163, s63\n\t" \
  "v_perm_b32 v47, v175, v171, s63\n\t" \
  "v_accvgpr_write_b32 a124, v44\n\t" \
  "v_accvgpr_write_b32 a125, v45\n\t" \
  "v_accvgpr_write_b32 a126, v46\n\t" \
  "v_accvgpr_write_b32 a127, v47\n\t" \
  "s_xor_b32 s60, s66, 0\n\t" \
  "s_add_u32 s60, s60, 0\n\t" \
  "s_add_u32 s61, s60, 18432\n\t" \
  "buffer_load_ushort v224, v42, %[W], s60 offen offset:0\n\t" \
  "buffer_load_ushort v144, v42, %[W], s60 offen offset:1152\n\t" \
  "buffer_load_ushort v225, v42, %[W], s60 offen offset:2304\n\t" \
  "buffer_load_ushort v145, v42, %[W], s60 offen offset:3456\n\t" \
  "buffer_load_ushort v226, v42, %[W], s61 offen offset:0\n\t" \
  "buffer_load_ushort v146, v42, %[W], s61 offen offset:1152\n\t" \
  "buffer_load_ushort v227, v42, %[W], s61 offen offset:2304\n\t" \
  "buffer_load_ushort v147, v42, %[W], s61 offen offset:3456\n\t" \
  "s_xor_b32 s60, s66, 0\n\t" \
  "s_add_u32 s60, s60, 36864\n\t" \
  "s_add_u32 s61, s60, 18432\n\t" \
  "buffer_load_ushort v228, v42, %[W], s60 offen offset:0\n\t" \
  "buffer_load_ushort v148, v42, %[W], s60 offen offset:1152\n\t" \
  "buffer_load_ushort v229, v42, %[W], s60 offen offset:2304\n\t" \
  "buffer_load_ushort v149, v42, %[W], s60 offen offset:3456\n\t" \
  "buffer_load_ushort v230, v42, %[W], s61 offen offset:0\n\t" \
  "buffer_load_ushort v150, v42, %[W], s61 offen offset:1152\n\t" \
  "buffer_load_ushort v231, v42, %[W], s61 offen offset:2304\n\t" \
  "buffer_load_ushort v151, v42, %[W], s61 offen offset:3456\n\t" \
  "s_waitcnt vmcnt(46)\n\t" \
  "v_perm_b32 v44, v180, v176, s62\n\t" \
  "s_waitcnt vmcnt(44)\n\t" \
  "v_perm_b32 v45, v188, v184, s62\n\t" \
  "s_waitcnt vmcnt(42)\n\t" \
  "v_perm_b32 v46, v196, v192, s62\n\t" \
  "s_waitcnt vmcnt(40)\n\t" \
  "v_perm_b32 v47, v204, v200, s62\n\t" \
  "v_accvgpr_write_b32 a128, v44\n\t" \
  "v_accvgpr_write_b32 a129, v45\n\t" \
  "v_accvgpr_write_b32 a130, v46\n\t" \
  "v_accvgpr_write_b32 a131, v47\n\t" \
  "v_perm_b32 v44, v180, v176, s63\n\t" \
  "v_perm_b32 v45, v188, v184, s63\n\t" \
  "v_perm_b32 v46, v196, v192, s63\n\t" \
  "v_perm_b32 v47, v204, v200, s63\n\t" \
  "v_accvgpr_write_b32 a132, v44\n\t" \
  "v_accvgpr_write_b32 a133, v45\n\t" \
  "v_accvgpr_write_b32 a134, v46\n\t" \
  "v_accvgpr_write_b32 a135, v47\n\t" \
  "v_perm_b32 v44, v181, v177, s62\n\t" \
  "v_perm_b32 v45, v189, v185, s62\n\t" \
  "v_perm_b32 v46, v197, v193, s62\n\t" \
  "v_perm_b32 v47, v205, v201, s62\n\t" \
  "v_accvgpr_write_b32 a136, v44\n\t" \
  "v_accvgpr_write_b32 a137, v45\n\t" \
  "v_accvgpr_write_b32 a138, v46\n\t" \
  "v_accvgpr_write_b32 a139, v47\n\t" \
  "v_perm_b32 v44, v181, v177, s63\n\t" \
  "v_perm_b32 v45, v189, v185, s63\n\t" \
  "v_perm_b32 v46, v197, v193, s63\n\t" \
  "v_perm_b32 v47, v205, v201, s63\n\t" \
  "v_accvgpr_write_b32 a140, v44\n\t" \
  "v_accvgpr_write_b32 a141, v45\n\t" \
  "v_accvgpr_write_b32 a142, v46\n\t" \
  "v_accvgpr_write_b32 a143, v47\n\t" \
  "v_perm_b32 v44, v182, v178, s62\n\t" \
  "v_perm_b32 v45, v190, v186, s62\n\t" \
  "v_perm_b32 v46, v198, v194, s62\n\t" \
  "v_perm_b32 v47, v206, v202, s62\n\t" \
  "v_accvgpr_write_b32 a144, v44\n\t" \
  "v_accvgpr_write_b32 a145, v45\n\t" \
  "v_accvgpr_write_b32 a146, v46\n\t" \
  "v_accvgpr_write_b32 a147, v47\n\t" \
  "v_perm_b32 v44, v182, v178, s63\n\t" \
  "v_perm_b32 v45, v190, v186, s63\n\t" \
  "v_perm_b32 v46, v198, v194, s63\n\t" \
  "v_perm_b32 v47, v206, v202, s63\n\t" \
  "v_accvgpr_write_b32 a148, v44\n\t" \
  "v_accvgpr_write_b32 a149, v45\n\t" \
  "v_accvgpr_write_b32 a150, v46\n\t" \
  "v_accvgpr_write_b32 a151, v47\n\t" \
  "v_perm_b32 v44, v183, v179, s62\n\t" \
  "v_perm_b32 v45, v191, v187, s62\n\t" \
  "v_perm_b32 v46, v199, v195, s62\n\t" \
  "v_perm_b32 v47, v207, v203, s62\n\t" \
  "v_accvgpr_write_b32 a152, v44\n\t" \
  "v_accvgpr_write_b32 a153, v45\n\t" \
  "v_accvgpr_write_b32 a154, v46\n\t" \
  "v_accvgpr_write_b32 a155, v47\n\t" \
  "v_perm_b32 v44, v183, v179, s63\n\t" \
  "v_perm_b32 v45, v191, v187, s63\n\t" \
  "v_perm_b32 v46, v199, v195, s63\n\t" \
  "v_perm_b32 v47, v207, v203, s63\n\t" \
  "v_accvgpr_write_b32 a156, v44\n\t" \
  "v_accvgpr_write_b32 a157, v45\n\t" \
  "v_accvgpr_write_b32 a158, v46\n\t" \
  "v_accvgpr_write_b32 a159, v47\n\t" \
  "s_xor_b32 s60, s66, 0\n\t" \
  "s_add_u32 s60, s60, 73728\n\t" \
  "s_add_u32 s61, s60, 18432\n\t" \
  "buffer_load_ushort v232, v42, %[W], s60 offen offset:0\n\t" \
  "buffer_load_ushort v152, v42, %[W], s60 offen offset:1152\n\t" \
  "buffer_load_ushort v233, v42, %[W], s60 offen offset:2304\n\t" \
  "buffer_load_ushort v153, v42, %[W], s60 offen offset:3456\n\t" \
  "buffer_load_ushort v234, v42, %[W], s61 offen offset:0\n\t" \
  "buffer_load_ushort v154, v42, %[W], s61 offen offset:1152\n\t" \
  "buffer_load_ushort v235, v42, %[W], s61 offen offset:2304\n\t" \
  "buffer_load_ushort v155, v42, %[W], s61 offen offset:3456\n\t" \
  "s_xor_b32 s60, s66, 0\n\t" \
  "s_add_u32 s60, s60, 110592\n\t" \
  "s_add_u32 s61, s60, 18432\n\t" \
  "buffer_load_ushort v236, v42, %[W], s60 offen offset:0\n\t" \
  "buffer_load_ushort v156, v42, %[W], s60 offen offset:1152\n\t" \
  "buffer_load_ushort v237, v42, %[W], s60 offen offset:2304\n\t" \
  "buffer_load_ushort v157, v42, %[W], s60 offen offset:3456\n\t" \
  "buffer_load_ushort v238, v42, %[W], s61 offen offset:0\n\t" \
  "buffer_load_ushort v158, v42, %[W], s61 offen offset:1152\n\t" \
  "buffer_load_ushort v239, v42, %[W], s61 offen offset:2304\n\t" \
  "buffer_load_ushort v159, v42, %[W], s61 offen offset:3456\n\t" \
  "s_waitcnt vmcnt(54)\n\t" \
  "v_perm_b32 v44, v52, v48, s62\n\t" \
  "s_waitcnt vmcnt(52)\n\t" \
  "v_perm_b32 v45, v60, v56, s62\n\t" \
  "s_waitcnt vmcnt(50)\n\t" \
  "v_perm_b32 v46, v68, v64, s62\n\t" \
  "s_waitcnt vmcnt(48)\n\t" \
  "v_perm_b32 v47, v76, v72, s62\n\t" \
  "v_accvgpr_write_b32 a160, v44\n\t" \
  "v_accvgpr_write_b32 a161, v45\n\t" \
  "v_accvgpr_write_b32 a162, v46\n\t" \
  "v_accvgpr_write_b32 a163, v47\n\t" \
  "v_perm_b32 v44, v52, v48, s63\n\t" \
  "v_perm_b32 v45, v60, v56, s63\n\t" \
  "v_perm_b32 v46, v68, v64, s63\n\t" \
  "v_perm_b32 v47, v76, v72, s63\n\t" \
  "v_accvgpr_write_b32 a164, v44\n\t" \
  "v_accvgpr_write_b32 a165, v45\n\t" \
  "v_accvgpr_write_b32 a166, v46\n\t" \
  "v_accvgpr_write_b32 a167, v47\n\t" \
  "v_perm_b32 v44, v53, v49, s62\n\t" \
  "v_perm_b32 v45, v61, v57, s62\n\t" \
  "v_perm_b32 v46, v69, v65, s62\n\t" \
  "v_perm_b32 v47, v77, v73, s62\n\t" \
  "v_accvgpr_write_b32 a168, v44\n\t" \
  "v_accvgpr_write_b32 a169, v45\n\t" \
  "v_accvgpr_write_b32 a170, v46\n\t" \
  "v_accvgpr_write_b32 a171, v47\n\t" \
  "v_perm_b32 v44, v53, v49, s63\n\t" \
  "v_perm_b32 v45, v61, v57, s63\n\t" \
  "v_perm_b32 v46, v69, v65, s63\n\t" \
  "v_perm_b32 v47, v77, v73, s63\n\t" \
  "v_accvgpr_write_b32 a172, v44\n\t" \
  "v_accvgpr_write_b32 a173, v45\n\t" \
  "v_accvgpr_write_b32 a174, v46\n\t" \
  "v_accvgpr_write_b32 a175, v47\n\t" \
  "v_perm_b32 v44, v54, v50, s62\n\t" \
  "v_perm_b32 v45, v62, v58, s62\n\t" \
  "v_perm_b32 v46, v70, v66, s62\n\t" \
  "v_perm_b32 v47, v78, v74, s62\n\t" \
  "v_accvgpr_write_b32 a176, v44\n\t" \
  "v_accvgpr_write_b32 a177, v45\n\t" \
  "v_accvgpr_write_b32 a178, v46\n\t" \
  "v_accvgpr_write_b32 a179, v47\n\t" \
  "v_perm_b32 v44, v54, v50, s63\n\t" \
  "v_perm_b32 v45, v62, v58, s63\n\t" \
  "v_perm_b32 v46, v70, v66, s63\n\t" \
  "v_perm_b32 v47, v78, v74, s63\n\t" \
  "v_accvgpr_write_b32 a180, v44\n\t" \
  "v_accvgpr_write_b32 a181, v45\n\t" \
  "v_accvgpr_write_b32 a182, v46\n\t" \
  "v_accvgpr_write_b32 a183, v47\n\t" \
  "v_perm_b32 v44, v55, v51, s62\n\t" \
  "v_perm_b32 v45, v63, v59, s62\n\t" \
  "v_perm_b32 v46, v71, v67, s62\n\t" \
  "v_perm_b32 v47, v79, v75, s62\n\t" \
  "v_accvgpr_write_b32 a184, v44\n\t" \
  "v_accvgpr_write_b32 a185, v45\n\t" \
  "v_accvgpr_write_b32 a186, v46\n\t" \
  "v_accvgpr_write_b32 a187, v47\n\t" \
  "v_perm_b32 v44, v55, v51, s63\n\t" \
  "v_perm_b32 v45, v63, v59, s63\n\t" \
  "v_perm_b32 v46, v71, v67, s63\n\t" \
  "v_perm_b32 v47, v79, v75, s63\n\t" \
  "v_accvgpr_write_b32 a188, v44\n\t" \
  "v_accvgpr_write_b32 a189, v45\n\t" \
  "v_accvgpr_write_b32 a190, v46\n\t" \
  "v_accvgpr_write_b32 a191, v47\n\t" \
  "s_xor_b32 s60, s66, 147456\n\t" \
  "s_add_u32 s60, s60, 0\n\t" \
  "s_add_u32 s61, s60, 18432\n\t" \
  "buffer_load_ushort v240, v42, %[W], s60 offen offset:0\n\t" \
  "buffer_load_ushort v160, v42, %[W], s60 offen offset:1152\n\t" \
  "buffer_load_ushort v241, v42, %[W], s60 offen offset:2304\n\t" \
  "buffer_load_ushort v161, v42, %[W], s60 offen offset:3456\n\t" \
  "buffer_load_ushort v242, v42, %[W], s61 offen offset:0\n\t" \
  "buffer_load_ushort v162, v42, %[W], s61 offen offset:1152\n\t" \
  "buffer_load_ushort v243, v42, %[W], s61 offen offset:2304\n\t" \
  "buffer_load_ushort v163, v42, %[W], s61 offen offset:3456\n\t" \
  "s_waitcnt vmcnt(54)\n\t" \
  "v_perm_b32 v44, v84, v80, s62\n\t" \
  "s_waitcnt vmcnt(52)\n\t" \
  "v_perm_b32 v45, v92, v88, s62\n\t" \
  "s_waitcnt vmcnt(50)\n\t" \
  "v_perm_b32 v46, v100, v96, s62\n\t" \
  "s_waitcnt vmcnt(48)\n\t" \
  "v_perm_b32 v47, v108, v104, s62\n\t" \
  "v_accvgpr_write_b32 a192, v44\n\t" \
  "v_accvgpr_write_b32 a193, v45\n\t" \
  "v_accvgpr_write_b32 a194, v46\n\t" \
  "v_accvgpr_write_b32 a195, v47\n\t" \
  "v_perm_b32 v44, v84, v80, s63\n\t" \
  "v_perm_b32 v45, v92, v88, s63\n\t" \
  "v_perm_b32 v46, v100, v96, s63\n\t" \
  "v_perm_b32 v47, v108, v104, s63\n\t" \
  "v_accvgpr_write_b32 a196, v44\n\t" \
  "v_accvgpr_write_b32 a197, v45\n\t" \
  "v_accvgpr_write_b32 a198, v46\n\t" \
  "v_accvgpr_write_b32 a199, v47\n\t" \
  "v_perm_b32 v44, v85, v81, s62\n\t" \
  "v_perm_b32 v45, v93, v89, s62\n\t" \
  "v_perm_b32 v46, v101, v97, s62\n\t" \
  "v_perm_b32 v47, v109, v105, s62\n\t" \
  "v_accvgpr_write_b32 a200, v44\n\t" \
  "v_accvgpr_write_b32 a201, v45\n\t" \
  "v_accvgpr_write_b32 a202, v46\n\t" \
  "v_accvgpr_write_b32 a203, v47\n\t" \
  "v_perm_b32 v44, v85, v81, s63\n\t" \
  "v_perm_b32 v45, v93, v89, s63\n\t" \
  "v_perm_b32 v46, v101, v97, s63\n\t" \
  "v_perm_b32 v47, v109, v105, s63\n\t" \
  "v_accvgpr_write_b32 a204, v44\n\t" \
  "v_accvgpr_write_b32 a205, v45\n\t" \
  "v_accvgpr_write_b32 a206, v46\n\t" \
  "v_accvgpr_write_b32 a207, v47\n\t" \
  "v_perm_b32 v44, v86, v82, s62\n\t" \
  "v_perm_b32 v45, v94, v90, s62\n\t" \
  "v_perm_b32 v46, v102, v98, s62\n\t" \
  "v_perm_b32 v47, v110, v106, s62\n\t" \
  "v_accvgpr_write_b32 a208, v44\n\t" \
  "v_accvgpr_write_b32 a209, v45\n\t" \
  "v_accvgpr_write_b32 a210, v46\n\t" \
  "v_accvgpr_write_b32 a211, v47\n\t" \
  "v_perm_b32 v44, v86, v82, s63\n\t" \
  "v_perm_b32 v45, v94, v90, s63\n\t" \
  "v_perm_b32 v46, v102, v98, s63\n\t" \
  "v_perm_b32 v47, v110, v106, s63\n\t" \
  "v_accvgpr_write_b32 a212, v44\n\t" \
  "v_accvgpr_write_b32 a213, v45\n\t" \
  "v_accvgpr_write_b32 a214, v46\n\t" \
  "v_accvgpr_write_b32 a215, v47\n\t" \
  "v_perm_b32 v44, v87, v83, s62\n\t" \
  "v_perm_b32 v45, v95, v91, s62\n\t" \
  "v_perm_b32 v46, v103, v99, s62\n\t" \
  "v_perm_b32 v47, v111, v107, s62\n\t" \
  "v_accvgpr_write_b32 a216, v44\n\t" \
  "v_accvgpr_write_b32 a217, v45\n\t" \
  "v_accvgpr_write_b32 a218, v46\n\t" \
  "v_accvgpr_write_b32 a219, v47\n\t" \
  "v_perm_b32 v44, v87, v83, s63\n\t" \
  "v_perm_b32 v45, v95, v91, s63\n\t" \
  "v_perm_b32 v46, v103, v99, s63\n\t" \
  "v_perm_b32 v47, v111, v107, s63\n\t" \
  "v_accvgpr_write_b32 a220, v44\n\t" \
  "v_accvgpr_write_b32 a221, v45\n\t" \
  "v_accvgpr_write_b32 a222, v46\n\t" \
  "v_accvgpr_write_b32 a223, v47\n\t" \
  "s_xor_b32 s60, s66, 147456\n\t" \
  "s_add_u32 s60, s60, 36864\n\t" \
  "s_add_u32 s61, s60, 18432\n\t" \
  "buffer_load_ushort v244, v42, %[W], s60 offen offset:0\n\t" \
  "buffer_load_ushort v164, v42, %[W], s60 offen offset:1152\n\t" \
  "buffer_load_ushort v245, v42, %[W], s60 offen offset:2304\n\t" \
  "buffer_load_ushort v165, v42, %[W], s60 offen offset:3456\n\t" \
  "buffer_load_ushort v246, v42, %[W], s61 offen offset:0\n\t" \
  "buffer_load_ushort v166, v42, %[W], s61 offen offset:1152\n\t" \
  "buffer_load_ushort v247, v42, %[W], s61 offen offset:2304\n\t" \
  "buffer_load_ushort v167, v42, %[W], s61 offen offset:3456\n\t" \
  "s_waitcnt vmcnt(54)\n\t" \
  "v_perm_b32 v44, v116, v112, s62\n\t" \
  "s_waitcnt vmcnt(52)\n\t" \
  "v_perm_b32 v45, v124, v120, s62\n\t" \
  "s_waitcnt vmcnt(50)\n\t" \
  "v_perm_b32 v46, v132, v128, s62\n\t" \
  "s_waitcnt vmcnt(48)\n\t" \
  "v_perm_b32 v47, v140, v136, s62\n\t" \
  "v_accvgpr_write_b32 a224, v44\n\t" \
  "v_accvgpr_write_b32 a225, v45\n\t" \
  "v_accvgpr_write_b32 a226, v46\n\t" \
  "v_accvgpr_write_b32 a227, v47\n\t" \
  "v_perm_b32 v44, v116, v112, s63\n\t" \
  "v_perm_b32 v45, v124, v120, s63\n\t" \
  "v_perm_b32 v46, v132, v128, s63\n\t" \
  "v_perm_b32 v47, v140, v136, s63\n\t" \
  "v_accvgpr_write_b32 a228, v44\n\t" \
  "v_accvgpr_write_b32 a229, v45\n\t" \
  "v_accvgpr_write_b32 a230, v46\n\t" \
  "v_accvgpr_write_b32 a231, v47\n\t" \
  "v_perm_b32 v44, v117, v113, s62\n\t" \
  "v_perm_b32 v45, v125, v121, s62\n\t" \
  "v_perm_b32 v46, v133, v129, s62\n\t" \
  "v_perm_b32 v47, v141, v137, s62\n\t" \
  "v_accvgpr_write_b32 a232, v44\n\t" \
  "v_accvgpr_write_b32 a233, v45\n\t" \
  "v_accvgpr_write_b32 a234, v46\n\t" \
  "v_accvgpr_write_b32 a235, v47\n\t" \
  "v_perm_b32 v44, v117, v113, s63\n\t" \
  "v_perm_b32 v45, v125, v121, s63\n\t" \
  "v_perm_b32 v46, v133, v129, s63\n\t" \
  "v_perm_b32 v47, v141, v137, s63\n\t" \
  "v_accvgpr_write_b32 a236, v44\n\t" \
  "v_accvgpr_write_b32 a237, v45\n\t" \
  "v_accvgpr_write_b32 a238, v46\n\t" \
  "v_accvgpr_write_b32 a239, v47\n\t" \
  "v_perm_b32 v44, v118, v114, s62\n\t" \
  "v_perm_b32 v45, v126, v122, s62\n\t" \
  "v_perm_b32 v46, v134, v130, s62\n\t" \
  "v_perm_b32 v47, v142, v138, s62\n\t" \
  "v_accvgpr_write_b32 a240, v44\n\t" \
  "v_accvgpr_write_b32 a241, v45\n\t" \
  "v_accvgpr_write_b32 a242, v46\n\t" \
  "v_accvgpr_write_b32 a243, v47\n\t" \
  "v_perm_b32 v44, v118, v114, s63\n\t" \
  "v_perm_b32 v45, v126, v122, s63\n\t" \
  "v_perm_b32 v46, v134, v130, s63\n\t" \
  "v_perm_b32 v47, v142, v138, s63\n\t" \
  "v_accvgpr_write_b32 a244, v44\n\t" \
  "v_accvgpr_write_b32 a245, v45\n\t" \
  "v_accvgpr_write_b32 a246, v46\n\t" \
  "v_accvgpr_write_b32 a247, v47\n\t" \
  "v_perm_b32 v44, v119, v115, s62\n\t" \
  "v_perm_b32 v45, v127, v123, s62\n\t" \
  "v_perm_b32 v46, v135, v131, s62\n\t" \
  "v_perm_b32 v47, v143, v139, s62\n\t" \
  "v_accvgpr_write_b32 a248, v44\n\t" \
  "v_accvgpr_write_b32 a249, v45\n\t" \
  "v_accvgpr_write_b32 a250, v46\n\t" \
  "v_accvgpr_write_b32 a251, v47\n\t" \
  "v_perm_b32 v44, v119, v115, s63\n\t" \
  "v_perm_b32 v45, v127, v123, s63\n\t" \
  "v_perm_b32 v46, v135, v131, s63\n\t" \
  "v_perm_b32 v47, v143, v139, s63\n\t" \
  "v_accvgpr_write_b32 a252, v44\n\t" \
  "v_accvgpr_write_b32 a253, v45\n\t" \
  "v_accvgpr_write_b32 a254, v46\n\t" \
  "v_accvgpr_write_b32 a255, v47\n\t" \
  "s_xor_b32 s60, s66, 147456\n\t" \
  "s_add_u32 s60, s60, 73728\n\t" \
  "s_add_u32 s61, s60, 18432\n\t" \
  "buffer_load_ushort v248, v42, %[W], s60 offen offset:0\n\t" \
  "buffer_load_ushort v168, v42, %[W], s60 offen offset:1152\n\t" \
  "buffer_load_ushort v249, v42, %[W], s60 offen offset:2304\n\t" \
  "buffer_load_ushort v169, v42, %[W], s60 offen offset:3456\n\t" \
  "buffer_load_ushort v250, v42, %[W], s61 offen offset:0\n\t" \
  "buffer_load_ushort v170, v42, %[W], s61 offen offset:1152\n\t" \
  "buffer_load_ushort v251, v42, %[W], s61 offen offset:2304\n\t" \
  "buffer_load_ushort v171, v42, %[W], s61 offen offset:3456\n\t" \
  "s_xor_b32 s60, s66, 147456\n\t" \
  "s_add_u32 s60, s60, 110592\n\t" \
  "s_add_u32 s61, s60, 18432\n\t" \
  "s_waitcnt vmcnt(40)\n\t" \
  "buffer_load_ushort v252, v42, %[W], s60 offen offset:0\n\t" \
  "buffer_load_ushort v172, v42, %[W], s60 offen offset:1152\n\t" \
  "buffer_load_ushort v253, v42, %[W], s60 offen offset:2304\n\t" \
  "buffer_load_ushort v173, v42, %[W], s60 offen offset:3456\n\t" \
  "buffer_load_ushort v254, v42, %[W], s61 offen offset:0\n\t" \
  "buffer_load_ushort v174, v42, %[W], s61 offen offset:1152\n\t" \
  "buffer_load_ushort v255, v42, %[W], s61 offen offset:2304\n\t" \
  "buffer_load_ushort v175, v42, %[W], s61 offen offset:3456\n\t" \
  "v_lshl_or_b32 v224, v144, 16, v224\n\t" \
  "v_lshl_or_b32 v225, v145, 16, v225\n\t" \
  "v_lshl_or_b32 v226, v146, 16, v226\n\t" \
  "v_lshl_or_b32 v227, v147, 16, v227\n\t" \
  "v_lshl_or_b32 v228, v148, 16, v228\n\t" \
  "v_lshl_or_b32 v229, v149, 16, v229\n\t" \
  "v_lshl_or_b32 v230, v150, 16, v230\n\t" \
  "v_lshl_or_b32 v231, v151, 16, v231\n\t" \
  "s_waitcnt vmcnt(46)\n\t" \
  "v_lshl_or_b32 v232, v152, 16, v232\n\t" \
  "s_waitcnt vmcnt(44)\n\t" \
  "v_lshl_or_b32 v233, v153, 16, v233\n\t" \
  "s_waitcnt vmcnt(42)\n\t" \
  "v_lshl_or_b32 v234, v154, 16, v234\n\t" \
  "s_waitcnt vmcnt(40)\n\t" \
  "v_lshl_or_b32 v235, v155, 16, v235\n\t" \
  "s_waitcnt vmcnt(38)\n\t" \
  "v_lshl_or_b32 v236, v156, 16, v236\n\t" \
  "s_waitcnt vmcnt(36)\n\t" \
  "v_lshl_or_b32 v237, v157, 16, v237\n\t" \
  "s_waitcnt vmcnt(34)\n\t" \
  "v_lshl_or_b32 v238, v158, 16, v238\n\t" \
  "s_waitcnt vmcnt(32)\n\t" \
  "v_lshl_or_b32 v239, v159, 16, v239\n\t" \
  "s_waitcnt vmcnt(30)\n\t" \
  "v_lshl_or_b32 v240, v160, 16, v240\n\t" \
  "s_waitcnt vmcnt(28)\n\t" \
  "v_lshl_or_b32 v241, v161, 16, v241\n\t" \
  "s_waitcnt vmcnt(26)\n\t" \
  "v_lshl_or_b32 v242, v162, 16, v242\n\t" \
  "s_waitcnt vmcnt(24)\n\t" \
  "v_lshl_or_b32 v243, v163, 16, v243\n\t" \
  "s_waitcnt vmcnt(22)\n\t" \
  "v_lshl_or_b32 v244, v164, 16, v244\n\t" \
  "s_waitcnt vmcnt(20)\n\t" \
  "v_lshl_or_b32 v245, v165, 16, v245\n\t" \
  "s_waitcnt vmcnt(18)\n\t" \
  "v_lshl_or_b32 v246, v166, 16, v246\n\t" \
  "s_waitcnt vmcnt(16)\n\t" \
  "v_lshl_or_b32 v247, v167, 16, v247\n\t" \
  "s_waitcnt vmcnt(14)\n\t" \
  "v_lshl_or_b32 v248, v168, 16, v248\n\t" \
  "s_waitcnt vmcnt(12)\n\t" \
  "v_lshl_or_b32 v249, v169, 16, v249\n\t" \
  "s_waitcnt vmcnt(10)\n\t" \
  "v_lshl_or_b32 v250, v170, 16, v250\n\t" \
  "s_waitcnt vmcnt(8)\n\t" \
  "v_lshl_or_b32 v251, v171, 16, v251\n\t" \
  "s_waitcnt vmcnt(6)\n\t" \
  "v_lshl_or_b32 v252, v172, 16, v252\n\t" \
  "s_waitcnt vmcnt(4)\n\t" \
  "v_lshl_or_b32 v253, v173, 16, v253\n\t" \
  "s_waitcnt vmcnt(2)\n\t" \
  "v_lshl_or_b32 v254, v174, 16, v254\n\t" \
  "s_waitcnt vmcnt(0)\n\t" \
  "v_lshl_or_b32 v255, v175, 16, v255\n\t" \
  "s_mov_b32 s84, " TK_CAP "\n\t" \
  "s_mov_b32 s48, 0\n\t" \
  "s_mov_b32 s49, 0xf0000000\n\t" \
  "s_mov_b32 s46, s45\n\t" \
  "v_mov_b32 v5, -1\n\t" \
  "v_lshrrev_b32 v13, 2, v0\n\t" \
  "s_mov_b32 s60, " TK_CTAB "\n\t" \
  "buffer_load_dword v10, v13, %[X], s60 offen offset:88 sc1\n\t" \
  "buffer_load_dword v13, v13, %[X], s60 offen offset:0 sc1\n\t" \
  "s_waitcnt vmcnt(0)\n\t" \
  "v_and_b32 v10, 1, v10\n\t" \
  "v_and_b32 v13, 1, v13\n\t" \
  "v_cmp_ne_u32 s[90:91], s67, v10\n\t" \
  "v_cmp_ne_u32 s[88:89], s67, v13\n\t" \
  "s_nop 1\n\t" \
  "s_mov_b32 s86, s88\n\t" \
  "v_lshrrev_b32 v13, 2, v0\n\t" \
  "s_mov_b32 s60, " TK_CTAB "\n\t" \
  "buffer_load_dword v10, v13, %[X], s60 offen offset:88 sc1\n\t" \
  "buffer_load_dword v13, v13, %[X], s60 offen offset:344 sc1\n\t" \
  "s_waitcnt vmcnt(0)\n\t" \
  "v_and_b32 v10, 1, v10\n\t" \
  "v_and_b32 v13, 1, v13\n\t" \
  "v_cmp_ne_u32 s[90:91], s67, v10\n\t" \
  "v_cmp_ne_u32 s[88:89], s67, v13\n\t" \
  "s_nop 1\n\t" \
  "s_and_b32 s60, s88, 256\n\t" \
  "s_lshl_b32 s60, s60, 5\n\t" \
  "s_add_u32 s44, s44, s60\n\t" \
  "s_add_u32 s83, s48, 0\n\t" \
  "s_lshr_b64 s[60:61], s[90:91], s83\n\t" \
  "s_and_b32 s60, s60, 1\n\t" \
  "s_lshl_b32 s60, s60, 13\n\t" \
  "s_add_u32 s83, s83, " TK_SG0 "\n\t" \
  "s_lshl_b32 s83, s83, 21\n\t" \
  "s_or_b32 s83, s83, s60\n\t" \
  "s_mov_b32 s81, s83\n\t" \
  "LCOLD_%=:\n\t" \
  "s_add_u32 s62, s83, s41\n\t" \
  "s_mov_b32 s61, 0\n\t" \
  "LPQ_%=:\n\t" \
  "buffer_load_dword v10, v9, %[X], s62 offen sc1\n\t" \
  "s_waitcnt vmcnt(0)\n\t" \
  "v_cmp_eq_u32 vcc, 0, v10\n\t" \
  "s_cbranch_vccz LPQ_%=x\n\t" \
  "s_add_u32 s61, s61, 1\n\t" \
  "s_sleep 4\n\t" \
  "s_cmp_gt_u32 s61, s84\n\t" \
  "s_cbranch_scc1 LPQ_%=g\n\t" \
  "s_and_b32 s60, s61, 1023\n\t" \
  "s_cmp_lg_u32 s60, 0\n\t" \
  "s_cbranch_scc1 LPQ_%=\n\t" \
  "buffer_load_dword v10, off, %[X], s92 sc1\n\t" \
  "s_waitcnt vmcnt(0)\n\t" \
  "v_readfirstlane_b32 s60, v10\n\t" \
  "s_nop 0\n\t" \
  "s_cmp_eq_u32 s60, 0\n\t" \
  "s_cbranch_scc1 LPQ_%=\n\t" \
  "LPQ_%=g:\n\t" \
  "s_mov_b32 s84, 0\n\t" \
  "s_or_b32 s60, s48, 1073741824\n\t" \
  "v_mov_b32 v10, s60\n\t" \
  "s_mov_b64 exec, 1\n\t" \
  "buffer_store_dword v10, off, %[X], s92 sc1\n\t" \
  "s_mov_b64 exec, -1\n\t" \
  "LPQ_%=x:\n\t" \
  "s_add_u32 m0, s46, 0\n\t" \
  "s_add_u32 s47, s81, s36\n\t" \
  "buffer_load_dwordx4 v0, %[X], s47 offen offset:0 sc1 lds\n\t" \
  "buffer_load_dwordx4 v0, %[X], s47 offen offset:1024 sc1 lds\n\t" \
  "buffer_load_dwordx4 v0, %[X], s47 offen offset:2048 sc1 lds\n\t" \
  "buffer_load_dwordx4 v0, %[X], s47 offen offset:3072 sc1 lds\n\t" \
  "s_add_u32 m0, s46, 4096\n\t" \
  "s_add_u32 s47, s81, s37\n\t" \
  "buffer_load_dwordx4 v0, %[X], s47 offen offset:0 sc1 lds\n\t" \
  "buffer_load_dwordx4 v0, %[X], s47 offen offset:1024 sc1 lds\n\t" \
  "buffer_load_dwordx4 v0, %[X], s47 offen offset:2048 sc1 lds\n\t" \
  "buffer_load_dwordx4 v0, %[X], s47 offen offset:3072 sc1 lds\n\t" \
  "s_add_u32 m0, s46, 8192\n\t" \
  "s_add_u32 s47, s81, s38\n\t" \
  "buffer_load_dwordx4 v0, %[X], s47 offen offset:0 sc1 lds\n\t" \
  "buffer_load_dwordx4 v0, %[X], s47 offen offset:1024 sc1 lds\n\t" \
  "buffer_load_dwordx4 v0, %[X], s47 offen offset:2048 sc1 lds\n\t" \
  "buffer_load_dwordx4 v0, %[X], s47 offen offset:3072 sc1 lds\n\t" \
  "s_waitcnt vmcnt(0)\n\t" \
  "LTOP_%=:\n\t" \
  "s_memrealtime s[78:79]\n\t" \
  "v_readfirstlane_b32 s76, v5\n\t" \
  "s_waitcnt lgkmcnt(0)\n\t" \
  "s_add_u32 s60, s76, 1\n\t" \
  "s_not_b32 s76, s76\n\t" \
  "s_cmp_gt_u32 s60, 1\n\t" \
  "s_cselect_b32 s61, -1, 0\n\t" \
  "s_lshl_b32 s60, s78, 8\n\t" \
  "s_sub_u32 s76, s76, s60\n\t" \
  "s_ashr_i32 s76, s76, 8\n\t" \
  "s_add_i32 s76, s76, s77\n\t" \
  "s_min_i32 s76, s76, 400\n\t" \
  "s_and_b32 s76, s76, s61\n\t" \
  "s_add_u32 s57, s78, s76\n\t" \
  "s_barrier\n\t" \
  "ds_read_b128 v[48:51], v2 offset:0\n\t" \
  "ds_read_b128 v[64:67], v1 offset:8192\n\t" \
  "ds_read_b128 v[80:83], v1 offset:12288\n\t" \
  "ds_read_b128 v[52:55], v2 offset:1024\n\t" \
  "ds_read_b128 v[68:71], v1 offset:9216\n\t" \
  "ds_read_b128 v[84:87], v1 offset:13312\n\t" \
  "ds_read_b128 v[56:59], v2 offset:2048\n\t" \
  "ds_read_b128 v[72:75], v1 offset:10240\n\t" \
  "ds_read_b128 v[88:91], v1 offset:14336\n\t" \
  "ds_read_b128 v[60:63], v2 offset:3072\n\t" \
  "ds_read_b128 v[76:79], v1 offset:11264\n\t" \
  "ds_read_b128 v[92:95], v1 offset:15360\n\t" \
  "s_mov_b32 s82, s83\n\t" \
  "s_xor_b32 s46, s46, 0x10000\n\t" \
  "s_add_u32 s58, s82, s41\n\t" \
  "s_add_u32 s58, s58, 4096\n\t" \
  "s_add_u32 s59, s81, s39\n\t" \
  "s_cmp_gt_i32 s76, 0\n\t" \
  "s_cselect_b32 s62, 0xf0000000, s58\n\t" \
  "buffer_load_dword v5, off, %[X], s62 sc1\n\t" \
  "s_cmp_gt_i32 s76, " TK_AM "\n\t" \
  "s_cselect_b32 s79, 0xf0000000, s58\n\t" \
  "s_waitcnt lgkmcnt(10)\n\t" \
  "v_mfma_f32_16x16x32_bf16 v[36:39], v[64:67], v[48:51], v[16:19]\n\t" \
  "ds_read_b128 v[96:99], v1 offset:16384\n\t" \
  "s_waitcnt lgkmcnt(10)\n\t" \
  "v_mfma_f32_16x16x32_bf16 v[40:43], v[80:83], v[48:51], v[20:23]\n\t" \
  "ds_read_b128 v[100:103], v1 offset:17408\n\t" \
  "s_waitcnt lgkmcnt(9)\n\t" \
  "v_mfma_f32_16x16x32_bf16 v[36:39], v[68:71], v[52:55], v[36:39]\n\t" \
  "ds_read_b128 v[104:107], v1 offset:18432\n\t" \
  "s_waitcnt lgkmcnt(9)\n\t" \
  "v_mfma_f32_16x16x32_bf16 v[40:43], v[84:87], v[52:55], v[40:43]\n\t" \
  "ds_read_b128 v[108:111], v1 offset:19456\n\t" \
  "s_waitcnt lgkmcnt(8)\n\t" \
  "v_mfma_f32_16x16x32_bf16 v[36:39], v[72:75], v[56:59], v[36:39]\n\t" \
  "ds_read_b128 v[112:115], v1 offset:20480\n\t" \
  "s_waitcnt lgkmcnt(8)\n\t" \
  "v_mfma_f32_16x16x32_bf16 v[40:43], v[88:91], v[56:59], v[40:43]\n\t" \
  "ds_read_b128 v[116:119], v1 offset:21504\n\t" \
  "s_waitcnt lgkmcnt(7)\n\t" \
  "v_mfma_f32_16x16x32_bf16 v[36:39], v[76:79], v[60:63], v[36:39]\n\t" \
  "ds_read_b128 v[120:123], v1 offset:22528\n\t" \
  "s_waitcnt lgkmcnt(7)\n\t" \
  "v_mfma_f32_16x16x32_bf16 v[40:43], v[92:95], v[60:63], v[40:43]\n\t" \
  "ds_read_b128 v[124:127], v1 offset:23552\n\t" \
  "s_add_u32 s83, s48, 1\n\t" \
  "s_lshr_b64 s[60:61], s[90:91], s83\n\t" \
  "s_and_b32 s60, s60, 1\n\t" \
  "s_lshl_b32 s60, s60, 13\n\t" \
  "s_add_u32 s83, s83, " TK_SG0 "\n\t" \
  "s_lshl_b32 s83, s83, 21\n\t" \
  "s_or_b32 s83, s83, s60\n\t" \
  "s_add_u32 s56, s48, 1\n\t" \
  "s_and_b32 s56, s56, " TK_TRM "\n\t" \
  "s_lshr_b64 s[60:61], s[90:91], s56\n\t" \
  "s_and_b32 s60, s60, 1\n\t" \
  "s_lshl_b32 s60, s60, 13\n\t" \
  "s_add_u32 s56, s56, " TK_SG0 "\n\t" \
  "s_lshl_b32 s56, s56, 21\n\t" \
  "s_or_b32 s56, s56, s60\n\t" \
  "v_xor_b32 v1, 0x10000, v1\n\t" \
  "v_xor_b32 v2, 0x10000, v2\n\t" \
  "v_max3_f32 v11, v36, v37, v38\n\t" \
  "v_max3_f32 v11, v11, v39, v40\n\t" \
  "v_max3_f32 v13, v41, v42, v43\n\t" \
  "v_max_f32 v11, v11, v13\n\t" \
  "v_mov_b32 v13, v11\n\t" \
  "s_nop 1\n\t" \
  "v_permlane16_swap_b32 v11, v13\n\t" \
  "v_max_f32 v11, v11, v13\n\t" \
  "v_mov_b32 v13, v11\n\t" \
  "s_nop 1\n\t" \
  "v_permlane32_swap_b32 v11, v13\n\t" \
  "v_max_f32 v11, v11, v13\n\t" \
  "v_mul_f32 v13, 0xbe0293ee, v11\n\t" \
  "v_fmamk_f32 v36, v36, 0x3e0293ee, v13\n\t" \
  "v_fmamk_f32 v37, v37, 0x3e0293ee, v13\n\t" \
  "v_fmamk_f32 v38, v38, 0x3e0293ee, v13\n\t" \
  "v_fmamk_f32 v39, v39, 0x3e0293ee, v13\n\t" \
  "v_fmamk_f32 v40, v40, 0x3e0293ee, v13\n\t" \
  "v_fmamk_f32 v41, v41, 0x3e0293ee, v13\n\t" \
  "v_fmamk_f32 v42, v42, 0x3e0293ee, v13\n\t" \
  "v_fmamk_f32 v43, v43, 0x3e0293ee, v13\n\t" \
  "s_waitcnt vmcnt(0)\n\t" \
  "v_readfirstlane_b32 s76, v5\n\t" \
  "s_barrier\n\t" \
  "s_cmp_eq_u32 s76, 0\n\t" \
  "s_cbranch_scc1 LNG_%=\n\t" \
  "buffer_load_dwordx4 v[196:199], v0, %[X], s59 offen offset:0 sc1\n\t" \
  "buffer_load_dwordx4 v[200:203], v0, %[X], s59 offen offset:1024 sc1\n\t" \
  "buffer_load_dwordx2 v[204:205], v6, %[X], s59 offen offset:2048 sc1\n\t" \
  "buffer_load_dwordx2 v[206:207], v6, %[X], s59 offen offset:2560 sc1\n\t" \
  "buffer_load_dwordx2 v[208:209], v6, %[X], s59 offen offset:3072 sc1\n\t" \
  "buffer_load_dwordx2 v[210:211], v6, %[X], s59 offen offset:3584 sc1\n\t" \
  "LNG_%=:\n\t" \
  "buffer_load_dword v5, off, %[X], s79 sc1\n\t" \
  "s_mov_b64 exec, 1\n\t" \
  "buffer_store_dword v3, off, %[X], s49 sc1\n\t" \
  "s_mov_b64 exec, -1\n\t" \
  "s_mov_b32 s49, 0xf0000000\n\t" \
  "v_exp_f32 v36, v36\n\t" \
  "v_exp_f32 v37, v37\n\t" \
  "v_exp_f32 v38, v38\n\t" \
  "v_exp_f32 v39, v39\n\t" \
  "v_exp_f32 v40, v40\n\t" \
  "v_exp_f32 v41, v41\n\t" \
  "v_exp_f32 v42, v42\n\t" \
  "v_exp_f32 v43, v43\n\t" \
  "v_cvt_pk_bf16_f32 v44, v36, v37\n\t" \
  "v_cvt_pk_bf16_f32 v45, v38, v39\n\t" \
  "v_cvt_pk_bf16_f32 v46, v40, v41\n\t" \
  "v_cvt_pk_bf16_f32 v47, v42, v43\n\t" \
  "v_add_f32 v36, v36, v37\n\t" \
  "v_add_f32 v38, v38, v39\n\t" \
  "s_waitcnt lgkmcnt(7)\n\t" \
  "v_mfma_f32_16x16x32_bf16 v[128:131], v[96:99], v[44:47], 0\n\t" \
  "v_add_f32 v40, v40, v41\n\t" \
  "s_and_b32 s62, s48, 15\n\t" \
  "s_lshr_b32 s63, s62, 1\n\t" \
  "s_waitcnt lgkmcnt(6)\n\t" \
  "v_mfma_f32_16x16x32_bf16 v[132:135], v[100:103], v[44:47], 0\n\t" \
  "v_add_f32 v42, v42, v43\n\t" \
  "s_lshr_b32 s60, s88, s63\n\t" \
  "s_and_b32 s60, s60, 1\n\t" \
  "s_waitcnt lgkmcnt(5)\n\t" \
  "v_mfma_f32_16x16x32_bf16 v[136:139], v[104:107], v[44:47], 0\n\t" \
  "v_add_f32 v36, v36, v38\n\t" \
  "s_lshl_b32 s60, s60, 13\n\t" \
  "s_lshl_b32 s61, s62, 20\n\t" \
  "s_waitcnt lgkmcnt(4)\n\t" \
  "v_mfma_f32_16x16x32_bf16 v[140:143], v[108:111], v[44:47], 0\n\t" \
  "v_add_f32 v40, v40, v42\n\t" \
  "s_add_u32 s50, s61, s60\n\t" \
  "s_add_u32 s50, s50, s43\n\t" \
  "s_waitcnt lgkmcnt(3)\n\t" \
  "v_mfma_f32_16x16x32_bf16 v[144:147], v[112:115], v[44:47], 0\n\t" \
  "v_add_f32 v12, v36, v40\n\t" \
  "s_add_u32 s51, s50, 32768\n\t" \
  "s_add_u32 s52, s51, 32768\n\t" \
  "s_waitcnt lgkmcnt(2)\n\t" \
  "v_mfma_f32_16x16x32_bf16 v[148:151], v[116:119], v[44:47], 0\n\t" \
  "s_add_u32 s53, s52, 32768\n\t" \
  "s_lshl_b32 s61, s62, 17\n\t" \
  "s_waitcnt lgkmcnt(1)\n\t" \
  "v_mfma_f32_16x16x32_bf16 v[152:155], v[120:123], v[44:47], 0\n\t" \
  "s_add_u32 s54, s61, s44\n\t" \
  "s_add_u32 s63, s83, s41\n\t" \
  "s_waitcnt lgkmcnt(0)\n\t" \
  "v_mfma_f32_16x16x32_bf16 v[156:159], v[124:127], v[44:47], 0\n\t" \
  "s_cmp_lg_u32 s76, 0\n\t" \
  "s_cbranch_scc1 LGOT_%=\n\t" \
  "s_waitcnt vmcnt(1)\n\t" \
  "v_readfirstlane_b32 s60, v5\n\t" \
  "s_nop 0\n\t" \
  "s_cmp_lg_u32 s60, 0\n\t" \
  "s_cbranch_scc1 LPK_%=x\n\t" \
  "LKW_%=:\n\t" \
  "s_memrealtime s[78:79]\n\t" \
  "s_waitcnt lgkmcnt(0)\n\t" \
  "s_sub_i32 s79, s78, s57\n\t" \
  "s_cmp_lt_i32 s79, 0\n\t" \
  "s_cbranch_scc1 LKW_%=\n\t" \
  "s_mov_b32 s61, 0\n\t" \
  "LPK_%=:\n\t" \
  "buffer_load_dword v5, off, %[X], s58 sc1\n\t" \
  "s_waitcnt vmcnt(0)\n\t" \
  "v_cmp_eq_u32 vcc, 0, v5\n\t" \
  "s_cbranch_vccz LPK_%=x\n\t" \
  "s_add_u32 s61, s61, 1\n\t" \
  "s_sleep " TK_PSK "\n\t" \
  "s_cmp_gt_u32 s61, s84\n\t" \
  "s_cbranch_scc1 LPK_%=g\n\t" \
  "s_and_b32 s60, s61, 1023\n\t" \
  "s_cmp_lg_u32 s60, 0\n\t" \
  "s_cbranch_scc1 LPK_%=\n\t" \
  "buffer_load_dword v5, off, %[X], s92 sc1\n\t" \
  "s_waitcnt vmcnt(0)\n\t" \
  "v_readfirstlane_b32 s60, v5\n\t" \
  "s_nop 0\n\t" \
  "s_cmp_eq_u32 s60, 0\n\t" \
  "s_cbranch_scc1 LPK_%=\n\t" \
  "LPK_%=g:\n\t" \
  "s_mov_b32 s84, 0\n\t" \
  "s_or_b32 s60, s48, 1342177280\n\t" \
  "v_mov_b32 v5, s60\n\t" \
  "s_mov_b64 exec, 1\n\t" \
  "buffer_store_dword v5, off, %[X], s92 sc1\n\t" \
  "s_mov_b64 exec, -1\n\t" \
  "LPK_%=x:\n\t" \
  "buffer_load_dwordx4 v[196:199], v0, %[X], s59 offen offset:0 sc1\n\t" \
  "buffer_load_dwordx4 v[200:203], v0, %[X], s59 offen offset:1024 sc1\n\t" \
  "buffer_load_dwordx2 v[204:205], v6, %[X], s59 offen offset:2048 sc1\n\t" \
  "buffer_load_dwordx2 v[206:207], v6, %[X], s59 offen offset:2560 sc1\n\t" \
  "buffer_load_dwordx2 v[208:209], v6, %[X], s59 offen offset:3072 sc1\n\t" \
  "buffer_load_dwordx2 v[210:211], v6, %[X], s59 offen offset:3584 sc1\n\t" \
  "LGOT_%=:\n\t" \
  "buffer_load_dword v10, v9, %[X], s63 offen sc1\n\t" \
  "s_add_u32 s49, s82, s42\n\t" \
  "s_waitcnt vmcnt(6)\n\t" \
  "v_mfma_f32_16x16x32_bf16 v[36:39], v[196:199], v[48:51], 0\n\t" \
  "v_mfma_f32_16x16x32_bf16 v[40:43], v[196:199], v[52:55], 0\n\t" \
  "s_waitcnt vmcnt(5)\n\t" \
  "v_mfma_f32_16x16x32_bf16 v[36:39], v[200:203], v[56:59], v[36:39]\n\t" \
  "v_mfma_f32_16x16x32_bf16 v[40:43], v[200:203], v[60:63], v[40:43]\n\t" \
  "s_nop 8\n\t" \
  "v_add_f32 v36, v36, v42\n\t" \
  "v_add_f32 v37, v37, v43\n\t" \
  "v_max3_f32 v14, v36, v37, v11\n\t" \
  "v_mov_b32 v13, v14\n\t" \
  "s_nop 1\n\t" \
  "v_permlane16_swap_b32 v14, v13\n\t" \
  "v_max_f32 v14, v14, v13\n\t" \
  "v_mov_b32 v13, v14\n\t" \
  "s_nop 1\n\t" \
  "v_permlane32_swap_b32 v14, v13\n\t" \
  "v_max_f32 v14, v14, v13\n\t" \
  "v_mul_f32 v14, 0xbe0293ee, v14\n\t" \
  "v_fmamk_f32 v11, v11, 0x3e0293ee, v14\n\t" \
  "v_fmamk_f32 v36, v36, 0x3e0293ee, v14\n\t" \
  "v_fmamk_f32 v37, v37, 0x3e0293ee, v14\n\t" \
  "v_exp_f32 v11, v11\n\t" \
  "v_exp_f32 v36, v36\n\t" \
  "v_exp_f32 v37, v37\n\t" \
  "v_fma_f32 v12, v12, v11, v36\n\t" \
  "v_add_f32 v12, v12, v37\n\t" \
  "v_mov_b32 v13, v12\n\t" \
  "s_nop 1\n\t" \
  "v_permlane16_swap_b32 v12, v13\n\t" \
  "v_add_f32 v12, v12, v13\n\t" \
  "v_mov_b32 v13, v12\n\t" \
  "s_nop 1\n\t" \
  "v_permlane32_swap_b32 v12, v13\n\t" \
  "v_add_f32 v12, v12, v13\n\t" \
  "v_rcp_f32 v12, v12\n\t" \
  "s_nop 0\n\t" \
  "v_mul_f32 v14, v11, v12\n\t" \
  "v_mul_f32 v36, v36, v12\n\t" \
  "v_mul_f32 v37, v37, v12\n\t" \
  "v_cvt_pk_bf16_f32 v26, v36, v37\n\t" \
  "v_cvt_pk_bf16_f32 v33, v36, v37\n\t" \
  "v_pk_mul_f32 v[128:129], v[128:129], v[14:15] op_sel_hi:[1,0]\n\t" \
  "v_pk_mul_f32 v[130:131], v[130:131], v[14:15] op_sel_hi:[1,0]\n\t" \
  "v_pk_mul_f32 v[132:133], v[132:133], v[14:15] op_sel_hi:[1,0]\n\t" \
  "v_pk_mul_f32 v[134:135], v[134:135], v[14:15] op_sel_hi:[1,0]\n\t" \
  "v_pk_mul_f32 v[136:137], v[136:137], v[14:15] op_sel_hi:[1,0]\n\t" \
  "v_pk_mul_f32 v[138:139], v[138:139], v[14:15] op_sel_hi:[1,0]\n\t" \
  "v_pk_mul_f32 v[140:141], v[140:141], v[14:15] op_sel_hi:[1,0]\n\t" \
  "v_pk_mul_f32 v[142:143], v[142:143], v[14:15] op_sel_hi:[1,0]\n\t" \
  "s_waitcnt vmcnt(3)\n\t" \
  "v_mfma_f32_16x16x32_bf16 v[128:131], v[204:207], v[26:29], v[128:131]\n\t" \
  "v_mul_f32 v144, v144, v14\n\t" \
  "v_mul_f32 v145, v145, v14\n\t" \
  "v_mfma_f32_16x16x32_bf16 v[132:135], v[204:207], v[32:35], v[132:135]\n\t" \
  "v_mul_f32 v146, v146, v14\n\t" \
  "v_mul_f32 v147, v147, v14\n\t" \
  "v_mfma_f32_16x16x32_bf16 v[136:139], v[204:207], v[24:27], v[136:139]\n\t" \
  "v_mul_f32 v148, v148, v14\n\t" \
  "v_mul_f32 v149, v149, v14\n\t" \
  "v_mfma_f32_16x16x32_bf16 v[140:143], v[204:207], v[30:33], v[140:143]\n\t" \
  "v_cvt_pk_bf16_f32 v64, v128, v129\n\t" \
  "v_cvt_pk_bf16_f32 v65, v130, v131\n\t" \
  "v_cvt_pk_bf16_f32 v66, v132, v133\n\t" \
  "v_cvt_pk_bf16_f32 v67, v134, v135\n\t" \
  "v_mul_f32 v150, v150, v14\n\t" \
  "v_mul_f32 v151, v151, v14\n\t" \
  "v_mfma_f32_16x16x32_bf16 v[192:195], v[224:227], v[64:67], 0\n\t" \
  "ds_write_b128 v3, v[64:67] offset:0\n\t" \
  "v_cvt_pk_bf16_f32 v68, v136, v137\n\t" \
  "v_cvt_pk_bf16_f32 v69, v138, v139\n\t" \
  "v_mfma_f32_16x16x32_bf16 v[160:163], v[64:67], a[0:3], 0\n\t" \
  "v_cvt_pk_bf16_f32 v70, v140, v141\n\t" \
  "v_cvt_pk_bf16_f32 v71, v142, v143\n\t" \
  "s_waitcnt vmcnt(1)\n\t" \
  "v_mfma_f32_16x16x32_bf16 v[144:147], v[208:211], v[26:29], v[144:147]\n\t" \
  "v_mul_f32 v152, v152, v14\n\t" \
  "v_mul_f32 v153, v153, v14\n\t" \
  "v_mfma_f32_16x16x32_bf16 v[148:151], v[208:211], v[32:35], v[148:151]\n\t" \
  "v_mul_f32 v154, v154, v14\n\t" \
  "v_mul_f32 v155, v155, v14\n\t" \
  "v_mfma_f32_16x16x32_bf16 v[164:167], v[64:67], a[4:7], 0\n\t" \
  "ds_write_b128 v3, v[68:71] offset:1024\n\t" \
  "v_mul_f32 v156, v156, v14\n\t" \
  "v_mul_f32 v157, v157, v14\n\t" \
  "v_mfma_f32_16x16x32_bf16 v[168:171], v[64:67], a[8:11], 0\n\t" \
  "v_mul_f32 v158, v158, v14\n\t" \
  "v_mul_f32 v159, v159, v14\n\t" \
  "v_mfma_f32_16x16x32_bf16 v[172:175], v[64:67], a[12:15], 0\n\t" \
  "v_cvt_pk_bf16_f32 v72, v144, v145\n\t" \
  "v_cvt_pk_bf16_f32 v73, v146, v147\n\t" \
  "v_mfma_f32_16x16x32_bf16 v[152:155], v[208:211], v[24:27], v[152:155]\n\t" \
  "v_cvt_pk_bf16_f32 v74, v148, v149\n\t" \
  "v_cvt_pk_bf16_f32 v75, v150, v151\n\t" \
  "v_mfma_f32_16x16x32_bf16 v[156:159], v[208:211], v[30:33], v[156:159]\n\t" \
  "v_mfma_f32_16x16x32_bf16 v[176:179], v[64:67], a[16:19], 0\n\t" \
  "ds_write_b128 v3, v[72:75] offset:2048\n\t" \
  "v_mfma_f32_16x16x32_bf16 v[180:183], v[64:67], a[20:23], 0\n\t" \
  "v_mfma_f32_16x16x32_bf16 v[184:187], v[64:67], a[24:27], 0\n\t" \
  "v_cvt_pk_bf16_f32 v76, v152, v153\n\t" \
  "v_cvt_pk_bf16_f32 v77, v154, v155\n\t" \
  "v_mfma_f32_16x16x32_bf16 v[188:191], v[64:67], a[28:31], 0\n\t" \
  "v_cvt_pk_bf16_f32 v78, v156, v157\n\t" \
  "v_cvt_pk_bf16_f32 v79, v158, v159\n\t" \
  "v_mfma_f32_16x16x32_bf16 v[192:195], v[228:231], v[68:71], v[192:195]\n\t" \
  "ds_write_b128 v3, v[76:79] offset:3072\n\t" \
  "v_mfma_f32_16x16x32_bf16 v[160:163], v[68:71], a[32:35], v[160:163]\n\t" \
  "s_waitcnt vmcnt(0)\n\t" \
  "v_cmp_eq_u32 vcc, 0, v10\n\t" \
  "v_mfma_f32_16x16x32_bf16 v[164:167], v[68:71], a[36:39], v[164:167]\n\t" \
  "s_cmp_eq_u64 vcc, 0\n\t" \
  "s_cselect_b32 s81, s56, 0xf0000000\n\t" \
  "s_cmp_eq_u32 s48, 63\n\t" \
  "v_mfma_f32_16x16x32_bf16 v[168:171], v[68:71], a[40:43], v[168:171]\n\t" \
  "s_cselect_b32 s81, 0xf0000000, s81\n\t" \
  "v_mfma_f32_16x16x32_bf16 v[172:175], v[68:71], a[44:47], v[172:175]\n\t" \
  "v_mfma_f32_16x16x32_bf16 v[176:179], v[68:71], a[48:51], v[176:179]\n\t" \
  "s_add_u32 m0, s46, 0\n\t" \
  "s_add_u32 s47, s81, s36\n\t" \
  "buffer_load_dwordx4 v0, %[X], s47 offen offset:0 sc1 lds\n\t" \
  "v_mfma_f32_16x16x32_bf16 v[180:183], v[68:71], a[52:55], v[180:183]\n\t" \
  "v_mfma_f32_16x16x32_bf16 v[184:187], v[68:71], a[56:59], v[184:187]\n\t" \
  "s_waitcnt lgkmcnt(0)\n\t" \
  "s_barrier\n\t" \
  "v_mfma_f32_16x16x32_bf16 v[188:191], v[68:71], a[60:63], v[188:191]\n\t" \
  "ds_read_b128 v[80:83], v4 offset:0\n\t" \
  "v_mfma_f32_16x16x32_bf16 v[192:195], v[232:235], v[72:75], v[192:195]\n\t" \
  "buffer_load_dwordx4 v0, %[X], s47 offen offset:1024 sc1 lds\n\t" \
  "v_mfma_f32_16x16x32_bf16 v[160:163], v[72:75], a[64:67], v[160:163]\n\t" \
  "ds_read_b128 v[84:87], v4 offset:1024\n\t" \
  "v_mfma_f32_16x16x32_bf16 v[164:167], v[72:75], a[68:71], v[164:167]\n\t" \
  "v_mfma_f32_16x16x32_bf16 v[168:171], v[72:75], a[72:75], v[168:171]\n\t" \
  "ds_read_b128 v[88:91], v4 offset:2048\n\t" \
  "v_mfma_f32_16x16x32_bf16 v[172:175], v[72:75], a[76:79], v[172:175]\n\t" \
  "buffer_load_dwordx4 v0, %[X], s47 offen offset:2048 sc1 lds\n\t" \
  "v_mfma_f32_16x16x32_bf16 v[176:179], v[72:75], a[80:83], v[176:179]\n\t" \
  "ds_read_b128 v[92:95], v4 offset:3072\n\t" \
  "v_mfma_f32_16x16x32_bf16 v[180:183], v[72:75], a[84:87], v[180:183]\n\t" \
  "v_mfma_f32_16x16x32_bf16 v[184:187], v[72:75], a[88:91], v[184:187]\n\t" \
  "v_mfma_f32_16x16x32_bf16 v[188:191], v[72:75], a[92:95], v[188:191]\n\t" \
  "buffer_load_dwordx4 v0, %[X], s47 offen offset:3072 sc1 lds\n\t" \
  "v_mfma_f32_16x16x32_bf16 v[192:195], v[236:239], v[76:79], v[192:195]\n\t" \
  "s_add_u32 s62, s48, 2\n\t" \
  "v_mfma_f32_16x16x32_bf16 v[160:163], v[76:79], a[96:99], v[160:163]\n\t" \
  "v_mfma_f32_16x16x32_bf16 v[164:167], v[76:79], a[100:103], v[164:167]\n\t" \
  "s_mul_i32 s63, s62, 43\n\t" \
  "s_lshr_b32 s63, s63, 9\n\t" \
  "v_mfma_f32_16x16x32_bf16 v[168:171], v[76:79], a[104:107], v[168:171]\n\t" \
  "s_add_u32 m0, s46, 4096\n\t" \
  "s_add_u32 s47, s81, s37\n\t" \
  "buffer_load_dwordx4 v0, %[X], s47 offen offset:0 sc1 lds\n\t" \
  "v_mfma_f32_16x16x32_bf16 v[172:175], v[76:79], a[108:111], v[172:175]\n\t" \
  "s_mul_i32 s63, s63, " TK_RM "\n\t" \
  "s_sub_u32 s62, s62, s63\n\t" \
  "v_mfma_f32_16x16x32_bf16 v[176:179], v[76:79], a[112:115], v[176:179]\n\t" \
  "v_mfma_f32_16x16x32_bf16 v[180:183], v[76:79], a[116:119], v[180:183]\n\t" \
  "s_mul_i32 s63, s62, 43\n\t" \
  "s_lshr_b32 s63, s63, 7\n\t" \
  "v_mfma_f32_16x16x32_bf16 v[184:187], v[76:79], a[120:123], v[184:187]\n\t" \
  "buffer_load_dwordx4 v0, %[X], s47 offen offset:1024 sc1 lds\n\t" \
  "v_mfma_f32_16x16x32_bf16 v[188:191], v[76:79], a[124:127], v[188:191]\n\t" \
  "s_mul_i32 s62, s62, 688128\n\t" \
  "s_lshl_b32 s60, s63, 15\n\t" \
  "s_waitcnt lgkmcnt(3)\n\t" \
  "v_mfma_f32_16x16x32_bf16 v[192:195], v[240:243], v[80:83], v[192:195]\n\t" \
  "v_mfma_f32_16x16x32_bf16 v[160:163], v[80:83], a[128:131], v[160:163]\n\t" \
  "s_add_u32 s62, s62, s60\n\t" \
  "s_lshr_b32 s60, s86, s63\n\t" \
  "v_mfma_f32_16x16x32_bf16 v[164:167], v[80:83], a[132:135], v[164:167]\n\t" \
  "buffer_load_dwordx4 v0, %[X], s47 offen offset:2048 sc1 lds\n\t" \
  "v_mfma_f32_16x16x32_bf16 v[168:171], v[80:83], a[136:139], v[168:171]\n\t" \
  "s_and_b32 s60, s60, 1\n\t" \
  "s_lshl_b32 s60, s60, 13\n\t" \
  "v_mfma_f32_16x16x32_bf16 v[172:175], v[80:83], a[140:143], v[172:175]\n\t" \
  "v_mfma_f32_16x16x32_bf16 v[176:179], v[80:83], a[144:147], v[176:179]\n\t" \
  "s_add_u32 s62, s62, s60\n\t" \
  "s_add_u32 s62, s62, 656896\n\t" \
  "v_mfma_f32_16x16x32_bf16 v[180:183], v[80:83], a[148:151], v[180:183]\n\t" \
  "buffer_load_dwordx4 v0, %[X], s47 offen offset:3072 sc1 lds\n\t" \
  "v_mfma_f32_16x16x32_bf16 v[184:187], v[80:83], a[152:155], v[184:187]\n\t" \
  "s_cmp_gt_u32 s48, 61\n\t" \
  "s_cselect_b32 s62, 0xf0000000, s62\n\t" \
  "v_mfma_f32_16x16x32_bf16 v[188:191], v[80:83], a[156:159], v[188:191]\n\t" \
  "s_waitcnt lgkmcnt(2)\n\t" \
  "v_mfma_f32_16x16x32_bf16 v[192:195], v[244:247], v[84:87], v[192:195]\n\t" \
  "v_mfma_f32_16x16x32_bf16 v[160:163], v[84:87], a[160:163], v[160:163]\n\t" \
  "s_add_u32 m0, s46, 8192\n\t" \
  "s_add_u32 s47, s81, s38\n\t" \
  "buffer_load_dwordx4 v0, %[X], s47 offen offset:0 sc1 lds\n\t" \
  "v_mfma_f32_16x16x32_bf16 v[164:167], v[84:87], a[164:167], v[164:167]\n\t" \
  "v_mfma_f32_16x16x32_bf16 v[168:171], v[84:87], a[168:171], v[168:171]\n\t" \
  "v_mfma_f32_16x16x32_bf16 v[172:175], v[84:87], a[172:175], v[172:175]\n\t" \
  "v_mfma_f32_16x16x32_bf16 v[176:179], v[84:87], a[176:179], v[176:179]\n\t" \
  "buffer_load_dwordx4 v0, %[X], s47 offen offset:1024 sc1 lds\n\t" \
  "v_mfma_f32_16x16x32_bf16 v[180:183], v[84:87], a[180:183], v[180:183]\n\t" \
  "v_mfma_f32_16x16x32_bf16 v[184:187], v[84:87], a[184:187], v[184:187]\n\t" \
  "v_mfma_f32_16x16x32_bf16 v[188:191], v[84:87], a[188:191], v[188:191]\n\t" \
  "s_waitcnt lgkmcnt(1)\n\t" \
  "v_mfma_f32_16x16x32_bf16 v[192:195], v[248:251], v[88:91], v[192:195]\n\t" \
  "buffer_load_dwordx4 v0, %[X], s47 offen offset:2048 sc1 lds\n\t" \
  "v_mfma_f32_16x16x32_bf16 v[160:163], v[88:91], a[192:195], v[160:163]\n\t" \
  "v_mfma_f32_16x16x32_bf16 v[164:167], v[88:91], a[196:199], v[164:167]\n\t" \
  "v_mfma_f32_16x16x32_bf16 v[168:171], v[88:91], a[200:203], v[168:171]\n\t" \
  "v_mfma_f32_16x16x32_bf16 v[172:175], v[88:91], a[204:207], v[172:175]\n\t" \
  "buffer_load_dwordx4 v0, %[X], s47 offen offset:3072 sc1 lds\n\t" \
  "v_mfma_f32_16x16x32_bf16 v[176:179], v[88:91], a[208:211], v[176:179]\n\t" \
  "v_mfma_f32_16x16x32_bf16 v[180:183], v[88:91], a[212:215], v[180:183]\n\t" \
  "buffer_load_dword v5, off, %[X], s62 sc1\n\t" \
  "v_mfma_f32_16x16x32_bf16 v[184:187], v[88:91], a[216:219], v[184:187]\n\t" \
  "v_mfma_f32_16x16x32_bf16 v[188:191], v[88:91], a[220:223], v[188:191]\n\t" \
  "s_waitcnt lgkmcnt(0)\n\t" \
  "v_mfma_f32_16x16x32_bf16 v[192:195], v[252:255], v[92:95], v[192:195]\n\t" \
  "v_mfma_f32_16x16x32_bf16 v[160:163], v[92:95], a[224:227], v[160:163]\n\t" \
  "v_mfma_f32_16x16x32_bf16 v[164:167], v[92:95], a[228:231], v[164:167]\n\t" \
  "v_mfma_f32_16x16x32_bf16 v[168:171], v[92:95], a[232:235], v[168:171]\n\t" \
  "v_cvt_pk_bf16_f32 v112, v192, v193\n\t" \
  "v_cvt_pk_bf16_f32 v113, v194, v195\n\t" \
  "v_mfma_f32_16x16x32_bf16 v[172:175], v[92:95], a[236:239], v[172:175]\n\t" \
  "buffer_store_dwordx2 v[112:113], v8, %[X], s54 offen sc1\n\t" \
  "v_mfma_f32_16x16x32_bf16 v[176:179], v[92:95], a[240:243], v[176:179]\n\t" \
  "v_cvt_pk_bf16_f32 v96, v160, v164\n\t" \
  "v_cvt_pk_bf16_f32 v100, v161, v165\n\t" \
  "v_mfma_f32_16x16x32_bf16 v[180:183], v[92:95], a[244:247], v[180:183]\n\t" \
  "v_cvt_pk_bf16_f32 v104, v162, v166\n\t" \
  "v_cvt_pk_bf16_f32 v108, v163, v167\n\t" \
  "v_mfma_f32_16x16x32_bf16 v[184:187], v[92:95], a[248:251], v[184:187]\n\t" \
  "v_cvt_pk_bf16_f32 v97, v168, v172\n\t" \
  "v_cvt_pk_bf16_f32 v101, v169, v173\n\t" \
  "v_mfma_f32_16x16x32_bf16 v[188:191], v[92:95], a[252:255], v[188:191]\n\t" \
  "v_cvt_pk_bf16_f32 v105, v170, v174\n\t" \
  "v_cvt_pk_bf16_f32 v109, v171, v175\n\t" \
  "v_cvt_pk_bf16_f32 v98, v176, v180\n\t" \
  "v_cvt_pk_bf16_f32 v102, v177, v181\n\t" \
  "v_cvt_pk_bf16_f32 v106, v178, v182\n\t" \
  "v_cvt_pk_bf16_f32 v110, v179, v183\n\t" \
  "s_nop 1\n\t" \
  "v_cvt_pk_bf16_f32 v99, v184, v188\n\t" \
  "buffer_store_dwordx4 v[96:99], v7, %[X], s50 offen sc1\n\t" \
  "v_cvt_pk_bf16_f32 v103, v185, v189\n\t" \
  "buffer_store_dwordx4 v[100:103], v7, %[X], s51 offen sc1\n\t" \
  "v_cvt_pk_bf16_f32 v107, v186, v190\n\t" \
  "buffer_store_dwordx4 v[104:107], v7, %[X], s52 offen sc1\n\t" \
  "v_cvt_pk_bf16_f32 v111, v187, v191\n\t" \
  "buffer_store_dwordx4 v[108:111], v7, %[X], s53 offen sc1\n\t" \
  "s_cmp_eq_u32 s48, 63\n\t" \
  "s_cbranch_scc1 LEND_%=\n\t" \
  "s_add_u32 s48, s48, 1\n\t" \
  "s_cmp_eq_u32 s81, s56\n\t" \
  "s_mov_b32 s81, s56\n\t" \
  "s_cbranch_scc0 LCOLD_%=\n\t" \
  "s_waitcnt vmcnt(5)\n\t" \
  "s_branch LTOP_%=\n\t" \
  "LEND_%=:\n\t" \
  "s_waitcnt vmcnt(0)\n\t" \
  "s_barrier\n\t" \
  "s_mov_b64 exec, 1\n\t" \
  "buffer_store_dword v3, off, %[X], s49 sc1\n\t" \
  "s_mov_b64 exec, -1\n\t" \
  "s_mov_b32 s49, 0xf0000000\n\t" \
  ""
#define QK_ASM \
  "v_lshrrev_b32 v16, 7, v0\n\t" \
  "v_lshlrev_b32 v16, 13, v16\n\t" \
  "v_lshrrev_b32 v19, 2, v0\n\t" \
  "v_and_b32 v19, 64, v19\n\t" \
  "v_xor_b32 v19, v19, v0\n\t" \
  "v_and_b32 v19, 0x70, v19\n\t" \
  "v_add_u32 v16, v16, v19\n\t" \
  "s_mul_i32 s63, s40, 40960\n\t" \
  "v_and_b32 v19, 0x300, v0\n\t" \
  "v_lshlrev_b32 v17, 2, v19\n\t" \
  "v_and_b32 v19, 0xc0, v0\n\t" \
  "v_lshl_add_u32 v17, v19, 1, v17\n\t" \
  "v_and_b32 v19, 0x30, v0\n\t" \
  "v_add3_u32 v17, v17, v19, s63\n\t" \
  "v_lshrrev_b32 v19, 1, v0\n\t" \
  "v_and_b32 v19, 64, v19\n\t" \
  "v_add_u32 v17, v17, v19\n\t" \
  "v_xor_b32 v18, 64, v17\n\t" \
  "s_add_u32 m0, s63, 0\n\t" \
  "s_mov_b32 s60, 0\n\t" \
  "buffer_load_dwordx4 v16, %[W], s60 offen lds\n\t" \
  "s_add_u32 m0, s63, 1024\n\t" \
  "s_mov_b32 s60, 65536\n\t" \
  "buffer_load_dwordx4 v16, %[W], s60 offen lds\n\t" \
  "s_add_u32 m0, s63, 2048\n\t" \
  "s_mov_b32 s60, 131072\n\t" \
  "buffer_load_dwordx4 v16, %[W], s60 offen lds\n\t" \
  "s_add_u32 m0, s63, 3072\n\t" \
  "s_mov_b32 s60, 196608\n\t" \
  "buffer_load_dwordx4 v16, %[W], s60 offen lds\n\t" \
  "s_add_u32 m0, s63, 4096\n\t" \
  "s_mov_b32 s60, 262144\n\t" \
  "buffer_load_dwordx4 v16, %[W], s60 offen lds\n\t" \
  "s_add_u32 m0, s63, 5120\n\t" \
  "s_mov_b32 s60, 327680\n\t" \
  "buffer_load_dwordx4 v16, %[W], s60 offen lds\n\t" \
  "s_add_u32 m0, s63, 6144\n\t" \
  "s_mov_b32 s60, 393216\n\t" \
  "buffer_load_dwordx4 v16, %[W], s60 offen lds\n\t" \
  "s_add_u32 m0, s63, 7168\n\t" \
  "s_mov_b32 s60, 458752\n\t" \
  "buffer_load_dwordx4 v16, %[W], s60 offen lds\n\t" \
  "s_add_u32 m0, s63, 8192\n\t" \
  "s_mov_b32 s60, 524288\n\t" \
  "buffer_load_dwordx4 v16, %[W], s60 offen lds\n\t" \
  "s_add_u32 m0, s63, 9216\n\t" \
  "s_mov_b32 s60, 589824\n\t" \
  "buffer_load_dwordx4 v16, %[W], s60 offen lds\n\t" \
  "s_add_u32 m0, s63, 10240\n\t" \
  "s_mov_b32 s60, 655360\n\t" \
  "buffer_load_dwordx4 v16, %[W], s60 offen lds\n\t" \
  "s_add_u32 m0, s63, 11264\n\t" \
  "s_mov_b32 s60, 720896\n\t" \
  "buffer_load_dwordx4 v16, %[W], s60 offen lds\n\t" \
  "s_add_u32 m0, s63, 12288\n\t" \
  "s_mov_b32 s60, 786432\n\t" \
  "buffer_load_dwordx4 v16, %[W], s60 offen lds\n\t" \
  "s_add_u32 m0, s63, 13312\n\t" \
  "s_mov_b32 s60, 851968\n\t" \
  "buffer_load_dwordx4 v16, %[W], s60 offen lds\n\t" \
  "s_add_u32 m0, s63, 14336\n\t" \
  "s_mov_b32 s60, 917504\n\t" \
  "buffer_load_dwordx4 v16, %[W], s60 offen lds\n\t" \
  "s_add_u32 m0, s63, 15360\n\t" \
  "s_mov_b32 s60, 983040\n\t" \
  "buffer_load_dwordx4 v16, %[W], s60 offen lds\n\t" \
  "s_add_u32 m0, s63, 16384\n\t" \
  "s_mov_b32 s60, 1048576\n\t" \
  "buffer_load_dwordx4 v16, %[W], s60 offen lds\n\t" \
  "s_add_u32 m0, s63, 17408\n\t" \
  "s_mov_b32 s60, 1114112\n\t" \
  "buffer_load_dwordx4 v16, %[W], s60 offen lds\n\t" \
  "s_add_u32 m0, s63, 18432\n\t" \
  "s_mov_b32 s60, 1179648\n\t" \
  "buffer_load_dwordx4 v16, %[W], s60 offen lds\n\t" \
  "s_add_u32 m0, s63, 19456\n\t" \
  "s_mov_b32 s60, 1245184\n\t" \
  "buffer_load_dwordx4 v16, %[W], s60 offen lds\n\t" \
  "s_add_u32 m0, s63, 20480\n\t" \
  "s_mov_b32 s60, 1310720\n\t" \
  "buffer_load_dwordx4 v16, %[W], s60 offen lds\n\t" \
  "s_add_u32 m0, s63, 21504\n\t" \
  "s_mov_b32 s60, 1376256\n\t" \
  "buffer_load_dwordx4 v16, %[W], s60 offen lds\n\t" \
  "s_add_u32 m0, s63, 22528\n\t" \
  "s_mov_b32 s60, 1441792\n\t" \
  "buffer_load_dwordx4 v16, %[W], s60 offen lds\n\t" \
  "s_add_u32 m0, s63, 23552\n\t" \
  "s_mov_b32 s60, 1507328\n\t" \
  "buffer_load_dwordx4 v16, %[W], s60 offen lds\n\t" \
  "s_add_u32 m0, s63, 24576\n\t" \
  "s_mov_b32 s60, 1572864\n\t" \
  "buffer_load_dwordx4 v16, %[W], s60 offen lds\n\t" \
  "s_add_u32 m0, s63, 25600\n\t" \
  "s_mov_b32 s60, 1638400\n\t" \
  "buffer_load_dwordx4 v16, %[W], s60 offen lds\n\t" \
  "s_add_u32 m0, s63, 26624\n\t" \
  "s_mov_b32 s60, 1703936\n\t" \
  "buffer_load_dwordx4 v16, %[W], s60 offen lds\n\t" \
  "s_add_u32 m0, s63, 27648\n\t" \
  "s_mov_b32 s60, 1769472\n\t" \
  "buffer_load_dwordx4 v16, %[W], s60 offen lds\n\t" \
  "s_add_u32 m0, s63, 28672\n\t" \
  "s_mov_b32 s60, 1835008\n\t" \
  "buffer_load_dwordx4 v16, %[W], s60 offen lds\n\t" \
  "s_add_u32 m0, s63, 29696\n\t" \
  "s_mov_b32 s60, 1900544\n\t" \
  "buffer_load_dwordx4 v16, %[W], s60 offen lds\n\t" \
  "s_add_u32 m0, s63, 30720\n\t" \
  "s_mov_b32 s60, 1966080\n\t" \
  "buffer_load_dwordx4 v16, %[W], s60 offen lds\n\t" \
  "s_add_u32 m0, s63, 31744\n\t" \
  "s_mov_b32 s60, 2031616\n\t" \
  "buffer_load_dwordx4 v16, %[W], s60 offen lds\n\t" \
  "s_add_u32 m0, s63, 32768\n\t" \
  "s_mov_b32 s60, 2097152\n\t" \
  "buffer_load_dwordx4 v16, %[W], s60 offen lds\n\t" \
  "s_add_u32 m0, s63, 33792\n\t" \
  "s_mov_b32 s60, 2162688\n\t" \
  "buffer_load_dwordx4 v16, %[W], s60 offen lds\n\t" \
  "s_add_u32 m0, s63, 34816\n\t" \
  "s_mov_b32 s60, 2228224\n\t" \
  "buffer_load_dwordx4 v16, %[W], s60 offen lds\n\t" \
  "s_add_u32 m0, s63, 35840\n\t" \
  "s_mov_b32 s60, 2293760\n\t" \
  "buffer_load_dwordx4 v16, %[W], s60 offen lds\n\t" \
  "s_add_u32 m0, s63, 36864\n\t" \
  "s_mov_b32 s60, 2359296\n\t" \
  "buffer_load_dwordx4 v16, %[W], s60 offen lds\n\t" \
  "s_add_u32 m0, s63, 37888\n\t" \
  "s_mov_b32 s60, 2424832\n\t" \
  "buffer_load_dwordx4 v16, %[W], s60 offen lds\n\t" \
  "s_add_u32 m0, s63, 38912\n\t" \
  "s_mov_b32 s60, 2490368\n\t" \
  "buffer_load_dwordx4 v16, %[W], s60 offen lds\n\t" \
  "s_add_u32 m0, s63, 39936\n\t" \
  "s_mov_b32 s60, 2555904\n\t" \
  "buffer_load_dwordx4 v16, %[W], s60 offen lds\n\t" \
  "s_waitcnt vmcnt(32)\n\t" \
  "ds_read_b64_tr_b16 a[0:1], v17 offset:0\n\t" \
  "ds_read_b64_tr_b16 a[2:3], v17 offset:512\n\t" \
  "ds_read_b64_tr_b16 a[4:5], v17 offset:4096\n\t" \
  "ds_read_b64_tr_b16 a[6:7], v17 offset:4608\n\t" \
  "ds_read_b64_tr_b16 a[72:73], v17 offset:8\n\t" \
  "ds_read_b64_tr_b16 a[74:75], v17 offset:520\n\t" \
  "ds_read_b64_tr_b16 a[76:77], v17 offset:4104\n\t" \
  "ds_read_b64_tr_b16 a[78:79], v17 offset:4616\n\t" \
  "ds_read_b64_tr_b16 a[144:145], v18 offset:0\n\t" \
  "ds_read_b64_tr_b16 a[146:147], v18 offset:512\n\t" \
  "ds_read_b64_tr_b16 a[148:149], v18 offset:4096\n\t" \
  "ds_read_b64_tr_b16 a[150:151], v18 offset:4608\n\t" \
  "ds_read_b64_tr_b16 a[216:217], v18 offset:8\n\t" \
  "ds_read_b64_tr_b16 a[218:219], v18 offset:520\n\t" \
  "ds_read_b64_tr_b16 a[220:221], v18 offset:4104\n\t" \
  "ds_read_b64_tr_b16 a[222:223], v18 offset:4616\n\t" \
  "s_waitcnt lgkmcnt(0)\n\t" \
  "s_add_u32 m0, s63, 0\n\t" \
  "s_mov_b32 s60, 2621440\n\t" \
  "buffer_load_dwordx4 v16, %[W], s60 offen lds\n\t" \
  "s_add_u32 m0, s63, 1024\n\t" \
  "s_mov_b32 s60, 2686976\n\t" \
  "buffer_load_dwordx4 v16, %[W], s60 offen lds\n\t" \
  "s_add_u32 m0, s63, 2048\n\t" \
  "s_mov_b32 s60, 2752512\n\t" \
  "buffer_load_dwordx4 v16, %[W], s60 offen lds\n\t" \
  "s_add_u32 m0, s63, 3072\n\t" \
  "s_mov_b32 s60, 2818048\n\t" \
  "buffer_load_dwordx4 v16, %[W], s60 offen lds\n\t" \
  "s_add_u32 m0, s63, 4096\n\t" \
  "s_mov_b32 s60, 2883584\n\t" \
  "buffer_load_dwordx4 v16, %[W], s60 offen lds\n\t" \
  "s_add_u32 m0, s63, 5120\n\t" \
  "s_mov_b32 s60, 2949120\n\t" \
  "buffer_load_dwordx4 v16, %[W], s60 offen lds\n\t" \
  "s_add_u32 m0, s63, 6144\n\t" \
  "s_mov_b32 s60, 3014656\n\t" \
  "buffer_load_dwordx4 v16, %[W], s60 offen lds\n\t" \
  "s_add_u32 m0, s63, 7168\n\t" \
  "s_mov_b32 s60, 3080192\n\t" \
  "buffer_load_dwordx4 v16, %[W], s60 offen lds\n\t" \
  "s_waitcnt vmcnt(32)\n\t" \
  "ds_read_b64_tr_b16 a[8:9], v17 offset:8192\n\t" \
  "ds_read_b64_tr_b16 a[10:11], v17 offset:8704\n\t" \
  "ds_read_b64_tr_b16 a[12:13], v17 offset:12288\n\t" \
  "ds_read_b64_tr_b16 a[14:15], v17 offset:12800\n\t" \
  "ds_read_b64_tr_b16 a[80:81], v17 offset:8200\n\t" \
  "ds_read_b64_tr_b16 a[82:83], v17 offset:8712\n\t" \
  "ds_read_b64_tr_b16 a[84:85], v17 offset:12296\n\t" \
  "ds_read_b64_tr_b16 a[86:87], v17 offset:12808\n\t" \
  "ds_read_b64_tr_b16 a[152:153], v18 offset:8192\n\t" \
  "ds_read_b64_tr_b16 a[154:155], v18 offset:8704\n\t" \
  "ds_read_b64_tr_b16 a[156:157], v18 offset:12288\n\t" \
  "ds_read_b64_tr_b16 a[158:159], v18 offset:12800\n\t" \
  "ds_read_b64_tr_b16 a[224:225], v18 offset:8200\n\t" \
  "ds_read_b64_tr_b16 a[226:227], v18 offset:8712\n\t" \
  "ds_read_b64_tr_b16 a[228:229], v18 offset:12296\n\t" \
  "ds_read_b64_tr_b16 a[230:231], v18 offset:12808\n\t" \
  "s_waitcnt lgkmcnt(0)\n\t" \
  "s_add_u32 m0, s63, 8192\n\t" \
  "s_mov_b32 s60, 3145728\n\t" \
  "buffer_load_dwordx4 v16, %[W], s60 offen lds\n\t" \
  "s_add_u32 m0, s63, 9216\n\t" \
  "s_mov_b32 s60, 3211264\n\t" \
  "buffer_load_dwordx4 v16, %[W], s60 offen lds\n\t" \
  "s_add_u32 m0, s63, 10240\n\t" \
  "s_mov_b32 s60, 3276800\n\t" \
  "buffer_load_dwordx4 v16, %[W], s60 offen lds\n\t" \
  "s_add_u32 m0, s63, 11264\n\t" \
  "s_mov_b32 s60, 3342336\n\t" \
  "buffer_load_dwordx4 v16, %[W], s60 offen lds\n\t" \
  "s_add_u32 m0, s63, 12288\n\t" \
  "s_mov_b32 s60, 3407872\n\t" \
  "buffer_load_dwordx4 v16, %[W], s60 offen lds\n\t" \
  "s_add_u32 m0, s63, 13312\n\t" \
  "s_mov_b32 s60, 3473408\n\t" \
  "buffer_load_dwordx4 v16, %[W], s60 offen lds\n\t" \
  "s_add_u32 m0, s63, 14336\n\t" \
  "s_mov_b32 s60, 3538944\n\t" \
  "buffer_load_dwordx4 v16, %[W], s60 offen lds\n\t" \
  "s_add_u32 m0, s63, 15360\n\t" \
  "s_mov_b32 s60, 3604480\n\t" \
  "buffer_load_dwordx4 v16, %[W], s60 offen lds\n\t" \
  "s_waitcnt vmcnt(32)\n\t" \
  "ds_read_b64_tr_b16 a[16:17], v17 offset:16384\n\t" \
  "ds_read_b64_tr_b16 a[18:19], v17 offset:16896\n\t" \
  "ds_read_b64_tr_b16 a[20:21], v17 offset:20480\n\t" \
  "ds_read_b64_tr_b16 a[22:23], v17 offset:20992\n\t" \
  "ds_read_b64_tr_b16 a[88:89], v17 offset:16392\n\t" \
  "ds_read_b64_tr_b16 a[90:91], v17 offset:16904\n\t" \
  "ds_read_b64_tr_b16 a[92:93], v17 offset:20488\n\t" \
  "ds_read_b64_tr_b16 a[94:95], v17 offset:21000\n\t" \
  "ds_read_b64_tr_b16 a[160:161], v18 offset:16384\n\t" \
  "ds_read_b64_tr_b16 a[162:163], v18 offset:16896\n\t" \
  "ds_read_b64_tr_b16 a[164:165], v18 offset:20480\n\t" \
  "ds_read_b64_tr_b16 a[166:167], v18 offset:20992\n\t" \
  "ds_read_b64_tr_b16 a[232:233], v18 offset:16392\n\t" \
  "ds_read_b64_tr_b16 a[234:235], v18 offset:16904\n\t" \
  "ds_read_b64_tr_b16 a[236:237], v18 offset:20488\n\t" \
  "ds_read_b64_tr_b16 a[238:239], v18 offset:21000\n\t" \
  "s_waitcnt lgkmcnt(0)\n\t" \
  "s_add_u32 m0, s63, 16384\n\t" \
  "s_mov_b32 s60, 3670016\n\t" \
  "buffer_load_dwordx4 v16, %[W], s60 offen lds\n\t" \
  "s_add_u32 m0, s63, 17408\n\t" \
  "s_mov_b32 s60, 3735552\n\t" \
  "buffer_load_dwordx4 v16, %[W], s60 offen lds\n\t" \
  "s_add_u32 m0, s63, 18432\n\t" \
  "s_mov_b32 s60, 3801088\n\t" \
  "buffer_load_dwordx4 v16, %[W], s60 offen lds\n\t" \
  "s_add_u32 m0, s63, 19456\n\t" \
  "s_mov_b32 s60, 3866624\n\t" \
  "buffer_load_dwordx4 v16, %[W], s60 offen lds\n\t" \
  "s_add_u32 m0, s63, 20480\n\t" \
  "s_mov_b32 s60, 3932160\n\t" \
  "buffer_load_dwordx4 v16, %[W], s60 offen lds\n\t" \
  "s_add_u32 m0, s63, 21504\n\t" \
  "s_mov_b32 s60, 3997696\n\t" \
  "buffer_load_dwordx4 v16, %[W], s60 offen lds\n\t" \
  "s_add_u32 m0, s63, 22528\n\t" \
  "s_mov_b32 s60, 4063232\n\t" \
  "buffer_load_dwordx4 v16, %[W], s60 offen lds\n\t" \
  "s_add_u32 m0, s63, 23552\n\t" \
  "s_mov_b32 s60, 4128768\n\t" \
  "buffer_load_dwordx4 v16, %[W], s60 offen lds\n\t" \
  "s_waitcnt vmcnt(32)\n\t" \
  "ds_read_b64_tr_b16 a[24:25], v17 offset:24576\n\t" \
  "ds_read_b64_tr_b16 a[26:27], v17 offset:25088\n\t" \
  "ds_read_b64_tr_b16 a[28:29], v17 offset:28672\n\t" \
  "ds_read_b64_tr_b16 a[30:31], v17 offset:29184\n\t" \
  "ds_read_b64_tr_b16 a[96:97], v17 offset:24584\n\t" \
  "ds_read_b64_tr_b16 a[98:99], v17 offset:25096\n\t" \
  "ds_read_b64_tr_b16 a[100:101], v17 offset:28680\n\t" \
  "ds_read_b64_tr_b16 a[102:103], v17 offset:29192\n\t" \
  "ds_read_b64_tr_b16 a[168:169], v18 offset:24576\n\t" \
  "ds_read_b64_tr_b16 a[170:171], v18 offset:25088\n\t" \
  "ds_read_b64_tr_b16 a[172:173], v18 offset:28672\n\t" \
  "ds_read_b64_tr_b16 a[174:175], v18 offset:29184\n\t" \
  "ds_read_b64_tr_b16 a[240:241], v18 offset:24584\n\t" \
  "ds_read_b64_tr_b16 a[242:243], v18 offset:25096\n\t" \
  "ds_read_b64_tr_b16 a[244:245], v18 offset:28680\n\t" \
  "ds_read_b64_tr_b16 a[246:247], v18 offset:29192\n\t" \
  "s_waitcnt lgkmcnt(0)\n\t" \
  "s_add_u32 m0, s63, 24576\n\t" \
  "s_mov_b32 s60, 4194304\n\t" \
  "buffer_load_dwordx4 v16, %[W], s60 offen lds\n\t" \
  "s_add_u32 m0, s63, 25600\n\t" \
  "s_mov_b32 s60, 4259840\n\t" \
  "buffer_load_dwordx4 v16, %[W], s60 offen lds\n\t" \
  "s_add_u32 m0, s63, 26624\n\t" \
  "s_mov_b32 s60, 4325376\n\t" \
  "buffer_load_dwordx4 v16, %[W], s60 offen lds\n\t" \
  "s_add_u32 m0, s63, 27648\n\t" \
  "s_mov_b32 s60, 4390912\n\t" \
  "buffer_load_dwordx4 v16, %[W], s60 offen lds\n\t" \
  "s_add_u32 m0, s63, 28672\n\t" \
  "s_mov_b32 s60, 4456448\n\t" \
  "buffer_load_dwordx4 v16, %[W], s60 offen lds\n\t" \
  "s_add_u32 m0, s63, 29696\n\t" \
  "s_mov_b32 s60, 4521984\n\t" \
  "buffer_load_dwordx4 v16, %[W], s60 offen lds\n\t" \
  "s_add_u32 m0, s63, 30720\n\t" \
  "s_mov_b32 s60, 4587520\n\t" \
  "buffer_load_dwordx4 v16, %[W], s60 offen lds\n\t" \
  "s_add_u32 m0, s63, 31744\n\t" \
  "s_mov_b32 s60, 4653056\n\t" \
  "buffer_load_dwordx4 v16, %[W], s60 offen lds\n\t" \
  "s_waitcnt vmcnt(32)\n\t" \
  "ds_read_b64_tr_b16 a[32:33], v17 offset:32768\n\t" \
  "ds_read_b64_tr_b16 a[34:35], v17 offset:33280\n\t" \
  "ds_read_b64_tr_b16 a[36:37], v17 offset:36864\n\t" \
  "ds_read_b64_tr_b16 a[38:39], v17 offset:37376\n\t" \
  "ds_read_b64_tr_b16 a[104:105], v17 offset:32776\n\t" \
  "ds_read_b64_tr_b16 a[106:107], v17 offset:33288\n\t" \
  "ds_read_b64_tr_b16 a[108:109], v17 offset:36872\n\t" \
  "ds_read_b64_tr_b16 a[110:111], v17 offset:37384\n\t" \
  "ds_read_b64_tr_b16 a[176:177], v18 offset:32768\n\t" \
  "ds_read_b64_tr_b16 a[178:179], v18 offset:33280\n\t" \
  "ds_read_b64_tr_b16 a[180:181], v18 offset:36864\n\t" \
  "ds_read_b64_tr_b16 a[182:183], v18 offset:37376\n\t" \
  "ds_read_b64_tr_b16 a[248:249], v18 offset:32776\n\t" \
  "ds_read_b64_tr_b16 a[250:251], v18 offset:33288\n\t" \
  "ds_read_b64_tr_b16 a[252:253], v18 offset:36872\n\t" \
  "ds_read_b64_tr_b16 a[254:255], v18 offset:37384\n\t" \
  "s_waitcnt lgkmcnt(0)\n\t" \
  "s_waitcnt vmcnt(24)\n\t" \
  "ds_read_b64_tr_b16 a[40:41], v17 offset:0\n\t" \
  "ds_read_b64_tr_b16 a[42:43], v17 offset:512\n\t" \
  "ds_read_b64_tr_b16 a[44:45], v17 offset:4096\n\t" \
  "ds_read_b64_tr_b16 a[46:47], v17 offset:4608\n\t" \
  "ds_read_b64_tr_b16 a[112:113], v17 offset:8\n\t" \
  "ds_read_b64_tr_b16 a[114:115], v17 offset:520\n\t" \
  "ds_read_b64_tr_b16 a[116:117], v17 offset:4104\n\t" \
  "ds_read_b64_tr_b16 a[118:119], v17 offset:4616\n\t" \
  "ds_read_b64_tr_b16 a[184:185], v18 offset:0\n\t" \
  "ds_read_b64_tr_b16 a[186:187], v18 offset:512\n\t" \
  "ds_read_b64_tr_b16 a[188:189], v18 offset:4096\n\t" \
  "ds_read_b64_tr_b16 a[190:191], v18 offset:4608\n\t" \
  "ds_read_b64_tr_b16 v[224:225], v18 offset:8\n\t" \
  "ds_read_b64_tr_b16 v[226:227], v18 offset:520\n\t" \
  "ds_read_b64_tr_b16 v[228:229], v18 offset:4104\n\t" \
  "ds_read_b64_tr_b16 v[230:231], v18 offset:4616\n\t" \
  "s_waitcnt lgkmcnt(0)\n\t" \
  "s_waitcnt vmcnt(16)\n\t" \
  "ds_read_b64_tr_b16 a[48:49], v17 offset:8192\n\t" \
  "ds_read_b64_tr_b16 a[50:51], v17 offset:8704\n\t" \
  "ds_read_b64_tr_b16 a[52:53], v17 offset:12288\n\t" \
  "ds_read_b64_tr_b16 a[54:55], v17 offset:12800\n\t" \
  "ds_read_b64_tr_b16 a[120:121], v17 offset:8200\n\t" \
  "ds_read_b64_tr_b16 a[122:123], v17 offset:8712\n\t" \
  "ds_read_b64_tr_b16 a[124:125], v17 offset:12296\n\t" \
  "ds_read_b64_tr_b16 a[126:127], v17 offset:12808\n\t" \
  "ds_read_b64_tr_b16 a[192:193], v18 offset:8192\n\t" \
  "ds_read_b64_tr_b16 a[194:195], v18 offset:8704\n\t" \
  "ds_read_b64_tr_b16 a[196:197], v18 offset:12288\n\t" \
  "ds_read_b64_tr_b16 a[198:199], v18 offset:12800\n\t" \
  "ds_read_b64_tr_b16 v[232:233], v18 offset:8200\n\t" \
  "ds_read_b64_tr_b16 v[234:235], v18 offset:8712\n\t" \
  "ds_read_b64_tr_b16 v[236:237], v18 offset:12296\n\t" \
  "ds_read_b64_tr_b16 v[238:239], v18 offset:12808\n\t" \
  "s_waitcnt lgkmcnt(0)\n\t" \
  "s_waitcnt vmcnt(8)\n\t" \
  "ds_read_b64_tr_b16 a[56:57], v17 offset:16384\n\t" \
  "ds_read_b64_tr_b16 a[58:59], v17 offset:16896\n\t" \
  "ds_read_b64_tr_b16 a[60:61], v17 offset:20480\n\t" \
  "ds_read_b64_tr_b16 a[62:63], v17 offset:20992\n\t" \
  "ds_read_b64_tr_b16 a[128:129], v17 offset:16392\n\t" \
  "ds_read_b64_tr_b16 a[130:131], v17 offset:16904\n\t" \
  "ds_read_b64_tr_b16 a[132:133], v17 offset:20488\n\t" \
  "ds_read_b64_tr_b16 a[134:135], v17 offset:21000\n\t" \
  "ds_read_b64_tr_b16 a[200:201], v18 offset:16384\n\t" \
  "ds_read_b64_tr_b16 a[202:203], v18 offset:16896\n\t" \
  "ds_read_b64_tr_b16 a[204:205], v18 offset:20480\n\t" \
  "ds_read_b64_tr_b16 a[206:207], v18 offset:20992\n\t" \
  "ds_read_b64_tr_b16 v[240:241], v18 offset:16392\n\t" \
  "ds_read_b64_tr_b16 v[242:243], v18 offset:16904\n\t" \
  "ds_read_b64_tr_b16 v[244:245], v18 offset:20488\n\t" \
  "ds_read_b64_tr_b16 v[246:247], v18 offset:21000\n\t" \
  "s_waitcnt lgkmcnt(0)\n\t" \
  "s_waitcnt vmcnt(0)\n\t" \
  "ds_read_b64_tr_b16 a[64:65], v17 offset:24576\n\t" \
  "ds_read_b64_tr_b16 a[66:67], v17 offset:25088\n\t" \
  "ds_read_b64_tr_b16 a[68:69], v17 offset:28672\n\t" \
  "ds_read_b64_tr_b16 a[70:71], v17 offset:29184\n\t" \
  "ds_read_b64_tr_b16 a[136:137], v17 offset:24584\n\t" \
  "ds_read_b64_tr_b16 a[138:139], v17 offset:25096\n\t" \
  "ds_read_b64_tr_b16 a[140:141], v17 offset:28680\n\t" \
  "ds_read_b64_tr_b16 a[142:143], v17 offset:29192\n\t" \
  "ds_read_b64_tr_b16 a[208:209], v18 offset:24576\n\t" \
  "ds_read_b64_tr_b16 a[210:211], v18 offset:25088\n\t" \
  "ds_read_b64_tr_b16 a[212:213], v18 offset:28672\n\t" \
  "ds_read_b64_tr_b16 a[214:215], v18 offset:29184\n\t" \
  "ds_read_b64_tr_b16 v[248:249], v18 offset:24584\n\t" \
  "ds_read_b64_tr_b16 v[250:251], v18 offset:25096\n\t" \
  "ds_read_b64_tr_b16 v[252:253], v18 offset:28680\n\t" \
  "ds_read_b64_tr_b16 v[254:255], v18 offset:29192\n\t" \
  "s_waitcnt lgkmcnt(0)\n\t" \
  "s_mov_b32 s84, " TK_CAP "\n\t" \
  "s_mov_b32 s48, 0\n\t" \
  "s_mov_b32 s49, 0xf0000000\n\t" \
  "s_mov_b32 s46, s41\n\t" \
  "s_mov_b32 s56, 0\n\t" \
  "v_lshrrev_b32 v7, 2, v0\n\t" \
  "s_mov_b32 s60, " TK_CTAB "\n\t" \
  "buffer_load_dword v4, v7, %[X], s60 offen offset:88 sc1\n\t" \
  "buffer_load_dword v7, v7, %[X], s60 offen offset:0 sc1\n\t" \
  "s_waitcnt vmcnt(0)\n\t" \
  "v_and_b32 v4, 1, v4\n\t" \
  "v_and_b32 v7, 1, v7\n\t" \
  "v_cmp_ne_u32 s[90:91], s67, v4\n\t" \
  "v_cmp_ne_u32 s[88:89], s67, v7\n\t" \
  "s_nop 1\n\t" \
  "s_mov_b32 s57, 0xf0000000\n\t" \
  "s_add_u32 s83, s48, 0\n\t" \
  "s_lshr_b64 s[60:61], s[90:91], s83\n\t" \
  "s_and_b32 s60, s60, 1\n\t" \
  "s_lshl_b32 s60, s60, 13\n\t" \
  "s_add_u32 s83, s83, " TK_SG0 "\n\t" \
  "s_lshl_b32 s83, s83, 21\n\t" \
  "s_or_b32 s83, s83, s60\n\t" \
  "s_add_u32 s80, s48, " TK_TRN "\n\t" \
  "s_lshr_b64 s[60:61], s[90:91], s80\n\t" \
  "s_and_b32 s60, s60, 1\n\t" \
  "s_lshl_b32 s60, s60, 13\n\t" \
  "s_add_u32 s80, s80, " TK_SG0 "\n\t" \
  "s_lshl_b32 s80, s80, 21\n\t" \
  "s_or_b32 s80, s80, s60\n\t" \
  "s_add_u32 s62, s83, s39\n\t" \
  "s_add_u32 s80, s80, s39\n\t" \
  "s_add_u32 s80, s80, 2048\n\t" \
  "s_cmp_lt_u32 s48, " TK_TRM "\n\t" \
  "s_cselect_b32 s80, s62, s80\n\t" \
  "v_add_u32 v7, s62, v3\n\t" \
  "s_mov_b32 exec_lo, 0\n\t" \
  "v_mov_b32 v7, s80\n\t" \
  "s_mov_b32 exec_lo, -1\n\t" \
  "LCOLD_%=:\n\t" \
  "s_waitcnt vmcnt(0)\n\t" \
  "s_mov_b64 exec, 1\n\t" \
  "buffer_store_dword v6, off, %[X], s49 sc1\n\t" \
  "s_mov_b64 exec, -1\n\t" \
  "s_mov_b32 s49, 0xf0000000\n\t" \
  "s_add_u32 s55, s83, s36\n\t" \
  "s_cmp_lg_u32 s40, 0\n\t" \
  "s_cbranch_scc1 LGO_%=\n\t" \
  "s_and_b32 s62, s45, 3\n\t" \
  "s_lshl_b32 s62, s62, 7\n\t" \
  "s_add_u32 s62, s62, s83\n\t" \
  "s_add_u32 s62, s62, s39\n\t" \
  "s_mov_b32 s61, 0\n\t" \
  "LPO_%=:\n\t" \
  "buffer_load_dword v4, off, %[X], s62 sc1\n\t" \
  "s_waitcnt vmcnt(0)\n\t" \
  "v_cmp_eq_u32 vcc, 0, v4\n\t" \
  "s_cbranch_vccz LPO_%=x\n\t" \
  "s_add_u32 s61, s61, 1\n\t" \
  "s_sleep " TK_PSH "\n\t" \
  "s_cmp_gt_u32 s61, s84\n\t" \
  "s_cbranch_scc1 LPO_%=g\n\t" \
  "s_and_b32 s60, s61, 1023\n\t" \
  "s_cmp_lg_u32 s60, 0\n\t" \
  "s_cbranch_scc1 LPO_%=\n\t" \
  "buffer_load_dword v4, off, %[X], s92 sc1\n\t" \
  "s_waitcnt vmcnt(0)\n\t" \
  "v_readfirstlane_b32 s60, v4\n\t" \
  "s_nop 0\n\t" \
  "s_cmp_eq_u32 s60, 0\n\t" \
  "s_cbranch_scc1 LPO_%=\n\t" \
  "LPO_%=g:\n\t" \
  "s_mov_b32 s84, 0\n\t" \
  "s_or_b32 s60, s48, 1610612736\n\t" \
  "v_mov_b32 v4, s60\n\t" \
  "s_mov_b64 exec, 1\n\t" \
  "buffer_store_dword v4, off, %[X], s92 sc1\n\t" \
  "s_mov_b64 exec, -1\n\t" \
  "LPO_%=x:\n\t" \
  "s_mov_b32 s61, 0\n\t" \
  "LPH_%=:\n\t" \
  "buffer_load_dword v4, v7, %[X], 0 offen sc1\n\t" \
  "s_waitcnt vmcnt(0)\n\t" \
  "v_cmp_eq_u32 vcc, 0, v4\n\t" \
  "s_cbranch_vccz LPH_%=x\n\t" \
  "s_add_u32 s61, s61, 1\n\t" \
  "s_sleep " TK_PSH "\n\t" \
  "s_cmp_gt_u32 s61, s84\n\t" \
  "s_cbranch_scc1 LPH_%=g\n\t" \
  "s_and_b32 s60, s61, 1023\n\t" \
  "s_cmp_lg_u32 s60, 0\n\t" \
  "s_cbranch_scc1 LPH_%=\n\t" \
  "buffer_load_dword v4, off, %[X], s92 sc1\n\t" \
  "s_waitcnt vmcnt(0)\n\t" \
  "v_readfirstlane_b32 s60, v4\n\t" \
  "s_nop 0\n\t" \
  "s_cmp_eq_u32 s60, 0\n\t" \
  "s_cbranch_scc1 LPH_%=\n\t" \
  "LPH_%=g:\n\t" \
  "s_mov_b32 s84, 0\n\t" \
  "s_or_b32 s60, s48, 1610612736\n\t" \
  "v_mov_b32 v4, s60\n\t" \
  "s_mov_b64 exec, 1\n\t" \
  "buffer_store_dword v4, off, %[X], s92 sc1\n\t" \
  "s_mov_b64 exec, -1\n\t" \
  "LPH_%=x:\n\t" \
  "s_cmp_lt_i32 s57, 0\n\t" \
  "s_cbranch_scc1 LGO_%=\n\t" \
  "s_movk_i32 s63, 4000\n\t" \
  "s_branch LGC_%=u\n\t" \
  "LGC_%=:\n\t" \
  "s_sleep " TK_PSG "\n\t" \
  "buffer_load_dword v8, off, %[X], s57 sc1\n\t" \
  "s_waitcnt vmcnt(0)\n\t" \
  "LGC_%=u:\n\t" \
  "s_memrealtime s[52:53]\n\t" \
  "v_readfirstlane_b32 s60, v8\n\t" \
  "s_waitcnt lgkmcnt(0)\n\t" \
  "s_sub_u32 s63, s63, 1\n\t" \
  "s_cbranch_scc1 LGO_%=\n\t" \
  "s_cmp_eq_u32 s60, -1\n\t" \
  "s_cbranch_scc1 LGC_%=\n\t" \
  "s_not_b32 s60, s60\n\t" \
  "s_lshl_b32 s61, s52, 8\n\t" \
  "s_sub_u32 s60, s60, s61\n\t" \
  "s_ashr_i32 s60, s60, 8\n\t" \
  "s_sub_i32 s60, s60, " TK_QXC "\n\t" \
  "s_min_i32 s60, s60, 400\n\t" \
  "s_add_u32 s54, s52, s60\n\t" \
  "LGC_%=w:\n\t" \
  "s_sleep 1\n\t" \
  "s_memrealtime s[52:53]\n\t" \
  "s_waitcnt lgkmcnt(0)\n\t" \
  "s_sub_i32 s53, s52, s54\n\t" \
  "s_cmp_lt_i32 s53, 0\n\t" \
  "s_cbranch_scc1 LGC_%=w\n\t" \
  "LGO_%=:\n\t" \
  "s_barrier\n\t" \
  "s_add_u32 m0, s46, 0\n\t" \
  "s_add_u32 s47, s55, 0\n\t" \
  "buffer_load_dwordx4 v0, %[X], s47 offen sc1 lds\n\t" \
  "s_add_u32 m0, s46, 4096\n\t" \
  "s_add_u32 s47, s55, 4096\n\t" \
  "buffer_load_dwordx4 v0, %[X], s47 offen sc1 lds\n\t" \
  "s_add_u32 m0, s46, 8192\n\t" \
  "s_add_u32 s47, s55, 16384\n\t" \
  "buffer_load_dwordx4 v0, %[X], s47 offen sc1 lds\n\t" \
  "s_add_u32 m0, s46, 12288\n\t" \
  "s_add_u32 s47, s55, 20480\n\t" \
  "buffer_load_dwordx4 v0, %[X], s47 offen sc1 lds\n\t" \
  "s_add_u32 m0, s46, 16384\n\t" \
  "s_add_u32 s47, s55, 32768\n\t" \
  "buffer_load_dwordx4 v0, %[X], s47 offen sc1 lds\n\t" \
  "s_add_u32 m0, s46, 20480\n\t" \
  "s_add_u32 s47, s55, 36864\n\t" \
  "buffer_load_dwordx4 v0, %[X], s47 offen sc1 lds\n\t" \
  "s_add_u32 m0, s46, 24576\n\t" \
  "s_add_u32 s47, s55, 49152\n\t" \
  "buffer_load_dwordx4 v0, %[X], s47 offen sc1 lds\n\t" \
  "s_add_u32 m0, s46, 28672\n\t" \
  "s_add_u32 s47, s55, 53248\n\t" \
  "buffer_load_dwordx4 v0, %[X], s47 offen sc1 lds\n\t" \
  "s_add_u32 m0, s46, 32768\n\t" \
  "s_add_u32 s47, s55, 65536\n\t" \
  "buffer_load_dwordx4 v0, %[X], s47 offen sc1 lds\n\t" \
  "s_waitcnt vmcnt(5)\n\t" \
  "LTOP_%=:\n\t" \
  "s_barrier\n\t" \
  "ds_read_b128 v[64:67], v1 offset:0\n\t" \
  "ds_read_b128 v[68:71], v1 offset:2048\n\t" \
  "ds_read_b128 v[72:75], v2 offset:0\n\t" \
  "ds_read_b128 v[76:79], v2 offset:2048\n\t" \
  "ds_read_b128 v[80:83], v1 offset:4096\n\t" \
  "ds_read_b128 v[84:87], v1 offset:6144\n\t" \
  "s_mov_b32 s82, s83\n\t" \
  "s_xor_b32 s46, s46, 0x10000\n\t" \
  "s_waitcnt lgkmcnt(5)\n\t" \
  "v_mfma_f32_16x16x32_bf16 v[16:19], a[0:3], v[64:67], 0\n\t" \
  "ds_read_b128 v[88:91], v2 offset:4096\n\t" \
  "v_mfma_f32_16x16x32_bf16 v[20:23], a[72:75], v[64:67], 0\n\t" \
  "s_add_u32 s50, s48, 0\n\t" \
  "s_and_b32 s50, s50, " TK_TRM "\n\t" \
  "v_mfma_f32_16x16x32_bf16 v[24:27], a[144:147], v[64:67], 0\n\t" \
  "ds_read_b128 v[92:95], v2 offset:6144\n\t" \
  "v_mfma_f32_16x16x32_bf16 v[28:31], a[216:219], v[64:67], 0\n\t" \
  "s_lshr_b64 s[60:61], s[90:91], s50\n\t" \
  "s_and_b32 s60, s60, 1\n\t" \
  "s_waitcnt lgkmcnt(5)\n\t" \
  "v_mfma_f32_16x16x32_bf16 v[16:19], a[4:7], v[72:75], v[16:19]\n\t" \
  "ds_read_b128 v[96:99], v1 offset:8192\n\t" \
  "v_mfma_f32_16x16x32_bf16 v[20:23], a[76:79], v[72:75], v[20:23]\n\t" \
  "s_lshl_b32 s60, s60, 13\n\t" \
  "s_add_u32 s50, s50, " TK_SG0 "\n\t" \
  "v_mfma_f32_16x16x32_bf16 v[24:27], a[148:151], v[72:75], v[24:27]\n\t" \
  "ds_read_b128 v[100:103], v1 offset:10240\n\t" \
  "v_mfma_f32_16x16x32_bf16 v[28:31], a[220:223], v[72:75], v[28:31]\n\t" \
  "s_lshl_b32 s50, s50, 21\n\t" \
  "s_or_b32 s50, s50, s60\n\t" \
  "s_waitcnt lgkmcnt(5)\n\t" \
  "v_mfma_f32_16x16x32_bf16 v[16:19], a[8:11], v[80:83], v[16:19]\n\t" \
  "ds_read_b128 v[104:107], v2 offset:8192\n\t" \
  "v_mfma_f32_16x16x32_bf16 v[20:23], a[80:83], v[80:83], v[20:23]\n\t" \
  "s_add_u32 s50, s50, s37\n\t" \
  "s_add_u32 s51, s50, 4096\n\t" \
  "v_mfma_f32_16x16x32_bf16 v[24:27], a[152:155], v[80:83], v[24:27]\n\t" \
  "ds_read_b128 v[108:111], v2 offset:10240\n\t" \
  "v_mfma_f32_16x16x32_bf16 v[28:31], a[224:227], v[80:83], v[28:31]\n\t" \
  "s_add_u32 s83, s48, 1\n\t" \
  "s_lshr_b64 s[60:61], s[90:91], s83\n\t" \
  "s_waitcnt lgkmcnt(5)\n\t" \
  "v_mfma_f32_16x16x32_bf16 v[16:19], a[12:15], v[88:91], v[16:19]\n\t" \
  "ds_read_b128 v[112:115], v1 offset:12288\n\t" \
  "v_mfma_f32_16x16x32_bf16 v[20:23], a[84:87], v[88:91], v[20:23]\n\t" \
  "s_and_b32 s60, s60, 1\n\t" \
  "s_lshl_b32 s60, s60, 13\n\t" \
  "v_mfma_f32_16x16x32_bf16 v[24:27], a[156:159], v[88:91], v[24:27]\n\t" \
  "ds_read_b128 v[116:119], v1 offset:14336\n\t" \
  "v_mfma_f32_16x16x32_bf16 v[28:31], a[228:231], v[88:91], v[28:31]\n\t" \
  "s_add_u32 s83, s83, " TK_SG0 "\n\t" \
  "s_lshl_b32 s83, s83, 21\n\t" \
  "s_waitcnt lgkmcnt(5)\n\t" \
  "v_mfma_f32_16x16x32_bf16 v[16:19], a[16:19], v[96:99], v[16:19]\n\t" \
  "ds_read_b128 v[120:123], v2 offset:12288\n\t" \
  "v_mfma_f32_16x16x32_bf16 v[20:23], a[88:91], v[96:99], v[20:23]\n\t" \
  "s_or_b32 s83, s83, s60\n\t" \
  "s_cmp_lg_u32 s56, 0\n\t" \
  "v_mfma_f32_16x16x32_bf16 v[24:27], a[160:163], v[96:99], v[24:27]\n\t" \
  "ds_read_b128 v[124:127], v2 offset:14336\n\t" \
  "v_mfma_f32_16x16x32_bf16 v[28:31], a[232:235], v[96:99], v[28:31]\n\t" \
  "s_cbranch_scc1 LWARM_%=\n\t" \
  "s_waitcnt vmcnt(0)\n\t" \
  "LWARM_%=:\n\t" \
  "s_barrier\n\t" \
  "s_waitcnt lgkmcnt(5)\n\t" \
  "v_mfma_f32_16x16x32_bf16 v[16:19], a[20:23], v[104:107], v[16:19]\n\t" \
  "ds_read_b128 v[128:131], v1 offset:16384\n\t" \
  "v_mfma_f32_16x16x32_bf16 v[20:23], a[92:95], v[104:107], v[20:23]\n\t" \
  "v_mfma_f32_16x16x32_bf16 v[24:27], a[164:167], v[104:107], v[24:27]\n\t" \
  "ds_read_b128 v[132:135], v1 offset:18432\n\t" \
  "v_mfma_f32_16x16x32_bf16 v[28:31], a[236:239], v[104:107], v[28:31]\n\t" \
  "s_add_i32 s62, s48, s42\n\t" \
  "s_add_i32 s62, s62, 1\n\t" \
  "s_waitcnt lgkmcnt(5)\n\t" \
  "v_mfma_f32_16x16x32_bf16 v[16:19], a[24:27], v[112:115], v[16:19]\n\t" \
  "ds_read_b128 v[136:139], v2 offset:16384\n\t" \
  "v_mfma_f32_16x16x32_bf16 v[20:23], a[96:99], v[112:115], v[20:23]\n\t" \
  "s_mov_b32 s61, s62\n\t" \
  "v_mfma_f32_16x16x32_bf16 v[24:27], a[168:171], v[112:115], v[24:27]\n\t" \
  "ds_read_b128 v[140:143], v2 offset:18432\n\t" \
  "v_mfma_f32_16x16x32_bf16 v[28:31], a[240:243], v[112:115], v[28:31]\n\t" \
  "s_mul_i32 s63, s62, 43\n\t" \
  "s_lshr_b32 s63, s63, 9\n\t" \
  "s_waitcnt lgkmcnt(5)\n\t" \
  "v_mfma_f32_16x16x32_bf16 v[16:19], a[28:31], v[120:123], v[16:19]\n\t" \
  "ds_read_b128 v[144:147], v1 offset:20480\n\t" \
  "v_mfma_f32_16x16x32_bf16 v[20:23], a[100:103], v[120:123], v[20:23]\n\t" \
  "s_mul_i32 s63, s63, " TK_RM "\n\t" \
  "s_sub_u32 s62, s62, s63\n\t" \
  "v_mfma_f32_16x16x32_bf16 v[24:27], a[172:175], v[120:123], v[24:27]\n\t" \
  "ds_read_b128 v[148:151], v1 offset:22528\n\t" \
  "v_mfma_f32_16x16x32_bf16 v[28:31], a[244:247], v[120:123], v[28:31]\n\t" \
  "s_mul_i32 s63, s62, 43\n\t" \
  "s_lshr_b32 s63, s63, 7\n\t" \
  "s_waitcnt lgkmcnt(5)\n\t" \
  "v_mfma_f32_16x16x32_bf16 v[16:19], a[32:35], v[128:131], v[16:19]\n\t" \
  "ds_read_b128 v[152:155], v2 offset:20480\n\t" \
  "v_mfma_f32_16x16x32_bf16 v[20:23], a[104:107], v[128:131], v[20:23]\n\t" \
  "s_mul_i32 s57, s62, 688128\n\t" \
  "s_lshl_b32 s60, s63, 15\n\t" \
  "v_mfma_f32_16x16x32_bf16 v[24:27], a[176:179], v[128:131], v[24:27]\n\t" \
  "ds_read_b128 v[156:159], v2 offset:22528\n\t" \
  "v_mfma_f32_16x16x32_bf16 v[28:31], a[248:251], v[128:131], v[28:31]\n\t" \
  "s_add_u32 s57, s57, s60\n\t" \
  "s_lshr_b32 s60, s88, s63\n\t" \
  "s_waitcnt lgkmcnt(5)\n\t" \
  "v_mfma_f32_16x16x32_bf16 v[16:19], a[36:39], v[136:139], v[16:19]\n\t" \
  "ds_read_b128 v[160:163], v1 offset:24576\n\t" \
  "v_mfma_f32_16x16x32_bf16 v[20:23], a[108:111], v[136:139], v[20:23]\n\t" \
  "s_and_b32 s60, s60, 1\n\t" \
  "s_lshl_b32 s60, s60, 13\n\t" \
  "v_mfma_f32_16x16x32_bf16 v[24:27], a[180:183], v[136:139], v[24:27]\n\t" \
  "ds_read_b128 v[164:167], v1 offset:26624\n\t" \
  "v_mfma_f32_16x16x32_bf16 v[28:31], a[252:255], v[136:139], v[28:31]\n\t" \
  "s_add_u32 s57, s57, s60\n\t" \
  "s_add_u32 s57, s57, 656896\n\t" \
  "s_waitcnt lgkmcnt(5)\n\t" \
  "v_mfma_f32_16x16x32_bf16 v[16:19], a[40:43], v[144:147], v[16:19]\n\t" \
  "ds_read_b128 v[168:171], v2 offset:24576\n\t" \
  "v_mfma_f32_16x16x32_bf16 v[20:23], a[112:115], v[144:147], v[20:23]\n\t" \
  "s_cmp_lt_i32 s61, 1\n\t" \
  "s_cselect_b32 s57, 0xf0000000, s57\n\t" \
  "v_mfma_f32_16x16x32_bf16 v[24:27], a[184:187], v[144:147], v[24:27]\n\t" \
  "ds_read_b128 v[172:175], v2 offset:26624\n\t" \
  "v_mfma_f32_16x16x32_bf16 v[28:31], v[224:227], v[144:147], v[28:31]\n\t" \
  "s_cmp_lg_u32 s40, 0\n\t" \
  "s_cselect_b32 s57, 0xf0000000, s57\n\t" \
  "s_waitcnt lgkmcnt(5)\n\t" \
  "v_mfma_f32_16x16x32_bf16 v[16:19], a[44:47], v[152:155], v[16:19]\n\t" \
  "ds_read_b128 v[176:179], v1 offset:28672\n\t" \
  "v_mfma_f32_16x16x32_bf16 v[20:23], a[116:119], v[152:155], v[20:23]\n\t" \
  "s_add_u32 s80, s48, " TK_TRN "\n\t" \
  "s_lshr_b64 s[60:61], s[90:91], s80\n\t" \
  "s_and_b32 s60, s60, 1\n\t" \
  "v_mfma_f32_16x16x32_bf16 v[24:27], a[188:191], v[152:155], v[24:27]\n\t" \
  "ds_read_b128 v[180:183], v1 offset:30720\n\t" \
  "v_mfma_f32_16x16x32_bf16 v[28:31], v[228:231], v[152:155], v[28:31]\n\t" \
  "s_lshl_b32 s60, s60, 13\n\t" \
  "s_add_u32 s80, s80, " TK_SG0 "\n\t" \
  "s_lshl_b32 s80, s80, 21\n\t" \
  "s_waitcnt lgkmcnt(5)\n\t" \
  "v_mfma_f32_16x16x32_bf16 v[16:19], a[48:51], v[160:163], v[16:19]\n\t" \
  "ds_read_b128 v[184:187], v2 offset:28672\n\t" \
  "v_mfma_f32_16x16x32_bf16 v[20:23], a[120:123], v[160:163], v[20:23]\n\t" \
  "s_or_b32 s80, s80, s60\n\t" \
  "s_add_u32 s62, s83, s39\n\t" \
  "s_add_u32 s80, s80, s39\n\t" \
  "v_mfma_f32_16x16x32_bf16 v[24:27], a[192:195], v[160:163], v[24:27]\n\t" \
  "ds_read_b128 v[188:191], v2 offset:30720\n\t" \
  "v_mfma_f32_16x16x32_bf16 v[28:31], v[232:235], v[160:163], v[28:31]\n\t" \
  "s_add_u32 s80, s80, 2048\n\t" \
  "s_waitcnt lgkmcnt(5)\n\t" \
  "v_mfma_f32_16x16x32_bf16 v[16:19], a[52:55], v[168:171], v[16:19]\n\t" \
  "ds_read_b128 v[192:195], v1 offset:32768\n\t" \
  "v_mfma_f32_16x16x32_bf16 v[20:23], a[124:127], v[168:171], v[20:23]\n\t" \
  "v_mfma_f32_16x16x32_bf16 v[24:27], a[196:199], v[168:171], v[24:27]\n\t" \
  "ds_read_b128 v[196:199], v1 offset:34816\n\t" \
  "v_mfma_f32_16x16x32_bf16 v[28:31], v[236:239], v[168:171], v[28:31]\n\t" \
  "s_cmp_lt_u32 s48, " TK_TRM "\n\t" \
  "s_cselect_b32 s80, s62, s80\n\t" \
  "s_waitcnt lgkmcnt(5)\n\t" \
  "v_mfma_f32_16x16x32_bf16 v[16:19], a[56:59], v[176:179], v[16:19]\n\t" \
  "ds_read_b128 v[200:203], v2 offset:32768\n\t" \
  "v_mfma_f32_16x16x32_bf16 v[20:23], a[128:131], v[176:179], v[20:23]\n\t" \
  "v_add_u32 v7, s62, v3\n\t" \
  "s_mov_b32 exec_lo, 0\n\t" \
  "v_mov_b32 v7, s80\n\t" \
  "s_mov_b32 exec_lo, -1\n\t" \
  "v_mfma_f32_16x16x32_bf16 v[24:27], a[200:203], v[176:179], v[24:27]\n\t" \
  "ds_read_b128 v[204:207], v2 offset:34816\n\t" \
  "v_mfma_f32_16x16x32_bf16 v[28:31], v[240:243], v[176:179], v[28:31]\n\t" \
  "s_waitcnt lgkmcnt(5)\n\t" \
  "v_mfma_f32_16x16x32_bf16 v[16:19], a[60:63], v[184:187], v[16:19]\n\t" \
  "v_mfma_f32_16x16x32_bf16 v[20:23], a[132:135], v[184:187], v[20:23]\n\t" \
  "buffer_load_dword v4, v7, %[X], 0 offen sc1\n\t" \
  "v_mfma_f32_16x16x32_bf16 v[24:27], a[204:207], v[184:187], v[24:27]\n\t" \
  "v_mfma_f32_16x16x32_bf16 v[28:31], v[244:247], v[184:187], v[28:31]\n\t" \
  "s_waitcnt lgkmcnt(3)\n\t" \
  "v_mfma_f32_16x16x32_bf16 v[16:19], a[64:67], v[192:195], v[16:19]\n\t" \
  "v_xor_b32 v1, 0x10000, v1\n\t" \
  "v_xor_b32 v2, 0x10000, v2\n\t" \
  "v_mfma_f32_16x16x32_bf16 v[20:23], a[136:139], v[192:195], v[20:23]\n\t" \
  "v_mfma_f32_16x16x32_bf16 v[24:27], a[208:211], v[192:195], v[24:27]\n\t" \
  "v_mfma_f32_16x16x32_bf16 v[28:31], v[248:251], v[192:195], v[28:31]\n\t" \
  "s_waitcnt lgkmcnt(1)\n\t" \
  "v_mfma_f32_16x16x32_bf16 v[16:19], a[68:71], v[200:203], v[16:19]\n\t" \
  "v_mfma_f32_16x16x32_bf16 v[20:23], a[140:143], v[200:203], v[20:23]\n\t" \
  "v_mfma_f32_16x16x32_bf16 v[24:27], a[212:215], v[200:203], v[24:27]\n\t" \
  "v_mfma_f32_16x16x32_bf16 v[28:31], v[252:255], v[200:203], v[28:31]\n\t" \
  "v_mfma_f32_16x16x32_bf16 v[32:35], a[0:3], v[68:71], 0\n\t" \
  "v_cvt_pk_bf16_f32 v48, v16, v17\n\t" \
  "v_cvt_pk_bf16_f32 v49, v18, v19\n\t" \
  "v_mfma_f32_16x16x32_bf16 v[36:39], a[72:75], v[68:71], 0\n\t" \
  "v_cvt_pk_bf16_f32 v50, v20, v21\n\t" \
  "v_cvt_pk_bf16_f32 v51, v22, v23\n\t" \
  "v_mfma_f32_16x16x32_bf16 v[32:35], a[4:7], v[76:79], v[32:35]\n\t" \
  "v_cvt_pk_bf16_f32 v52, v24, v25\n\t" \
  "v_cvt_pk_bf16_f32 v53, v26, v27\n\t" \
  "v_mfma_f32_16x16x32_bf16 v[36:39], a[76:79], v[76:79], v[36:39]\n\t" \
  "v_cvt_pk_bf16_f32 v54, v28, v29\n\t" \
  "v_cvt_pk_bf16_f32 v55, v30, v31\n\t" \
  "v_mfma_f32_16x16x32_bf16 v[32:35], a[8:11], v[84:87], v[32:35]\n\t" \
  "buffer_store_dwordx4 v[48:51], v0, %[X], s50 offen offset:0 sc1\n\t" \
  "v_mfma_f32_16x16x32_bf16 v[36:39], a[80:83], v[84:87], v[36:39]\n\t" \
  "v_mfma_f32_16x16x32_bf16 v[32:35], a[12:15], v[92:95], v[32:35]\n\t" \
  "v_mfma_f32_16x16x32_bf16 v[36:39], a[84:87], v[92:95], v[36:39]\n\t" \
  "v_mfma_f32_16x16x32_bf16 v[32:35], a[16:19], v[100:103], v[32:35]\n\t" \
  "v_mfma_f32_16x16x32_bf16 v[36:39], a[88:91], v[100:103], v[36:39]\n\t" \
  "v_mfma_f32_16x16x32_bf16 v[32:35], a[20:23], v[108:111], v[32:35]\n\t" \
  "v_mfma_f32_16x16x32_bf16 v[36:39], a[92:95], v[108:111], v[36:39]\n\t" \
  "v_mfma_f32_16x16x32_bf16 v[32:35], a[24:27], v[116:119], v[32:35]\n\t" \
  "v_mfma_f32_16x16x32_bf16 v[36:39], a[96:99], v[116:119], v[36:39]\n\t" \
  "v_mfma_f32_16x16x32_bf16 v[32:35], a[28:31], v[124:127], v[32:35]\n\t" \
  "v_mfma_f32_16x16x32_bf16 v[36:39], a[100:103], v[124:127], v[36:39]\n\t" \
  "s_waitcnt vmcnt(1)\n\t" \
  "v_cmp_eq_u32 vcc, 0, v4\n\t" \
  "v_mfma_f32_16x16x32_bf16 v[32:35], a[32:35], v[132:135], v[32:35]\n\t" \
  "v_mfma_f32_16x16x32_bf16 v[36:39], a[104:107], v[132:135], v[36:39]\n\t" \
  "s_cmp_eq_u64 vcc, 0\n\t" \
  "s_cselect_b32 s60, 1, 0\n\t" \
  "v_mov_b32 v4, s60\n\t" \
  "v_mfma_f32_16x16x32_bf16 v[32:35], a[36:39], v[140:143], v[32:35]\n\t" \
  "ds_write_b32 v5, v4\n\t" \
  "v_mfma_f32_16x16x32_bf16 v[36:39], a[108:111], v[140:143], v[36:39]\n\t" \
  "v_mfma_f32_16x16x32_bf16 v[32:35], a[40:43], v[148:151], v[32:35]\n\t" \
  "v_mfma_f32_16x16x32_bf16 v[36:39], a[112:115], v[148:151], v[36:39]\n\t" \
  "buffer_store_dwordx4 v[52:55], v0, %[X], s50 offen offset:1024 sc1\n\t" \
  "v_mfma_f32_16x16x32_bf16 v[32:35], a[44:47], v[156:159], v[32:35]\n\t" \
  "v_mfma_f32_16x16x32_bf16 v[36:39], a[116:119], v[156:159], v[36:39]\n\t" \
  "s_waitcnt lgkmcnt(0)\n\t" \
  "s_barrier\n\t" \
  "v_mfma_f32_16x16x32_bf16 v[32:35], a[48:51], v[164:167], v[32:35]\n\t" \
  "ds_read_b32 v4, v6 offset:40959\n\t" \
  "v_mfma_f32_16x16x32_bf16 v[36:39], a[120:123], v[164:167], v[36:39]\n\t" \
  "v_mfma_f32_16x16x32_bf16 v[32:35], a[52:55], v[172:175], v[32:35]\n\t" \
  "v_mfma_f32_16x16x32_bf16 v[36:39], a[124:127], v[172:175], v[36:39]\n\t" \
  "v_mfma_f32_16x16x32_bf16 v[32:35], a[56:59], v[180:183], v[32:35]\n\t" \
  "v_mfma_f32_16x16x32_bf16 v[36:39], a[128:131], v[180:183], v[36:39]\n\t" \
  "s_waitcnt lgkmcnt(0)\n\t" \
  "v_readfirstlane_b32 s60, v4\n\t" \
  "s_add_u32 s55, s83, s36\n\t" \
  "v_mfma_f32_16x16x32_bf16 v[32:35], a[60:63], v[188:191], v[32:35]\n\t" \
  "s_cmp_eq_u32 s48, 63\n\t" \
  "s_cselect_b32 s56, 0, s60\n\t" \
  "v_mfma_f32_16x16x32_bf16 v[36:39], a[132:135], v[188:191], v[36:39]\n\t" \
  "s_cmp_eq_u32 s56, 0\n\t" \
  "s_cselect_b32 s55, 0xf0000000, s55\n\t" \
  "v_mfma_f32_16x16x32_bf16 v[32:35], a[64:67], v[196:199], v[32:35]\n\t" \
  "v_mfma_f32_16x16x32_bf16 v[36:39], a[136:139], v[196:199], v[36:39]\n\t" \
  "v_mfma_f32_16x16x32_bf16 v[32:35], a[68:71], v[204:207], v[32:35]\n\t" \
  "s_add_u32 m0, s46, 0\n\t" \
  "s_add_u32 s47, s55, 0\n\t" \
  "buffer_load_dwordx4 v0, %[X], s47 offen sc1 lds\n\t" \
  "v_mfma_f32_16x16x32_bf16 v[36:39], a[140:143], v[204:207], v[36:39]\n\t" \
  "v_mfma_f32_16x16x32_bf16 v[40:43], a[144:147], v[68:71], 0\n\t" \
  "v_mfma_f32_16x16x32_bf16 v[44:47], a[216:219], v[68:71], 0\n\t" \
  "v_mfma_f32_16x16x32_bf16 v[40:43], a[148:151], v[76:79], v[40:43]\n\t" \
  "v_cvt_pk_bf16_f32 v56, v32, v33\n\t" \
  "v_cvt_pk_bf16_f32 v57, v34, v35\n\t" \
  "s_add_u32 m0, s46, 4096\n\t" \
  "s_add_u32 s47, s55, 4096\n\t" \
  "buffer_load_dwordx4 v0, %[X], s47 offen sc1 lds\n\t" \
  "v_mfma_f32_16x16x32_bf16 v[44:47], a[220:223], v[76:79], v[44:47]\n\t" \
  "v_cvt_pk_bf16_f32 v58, v36, v37\n\t" \
  "v_cvt_pk_bf16_f32 v59, v38, v39\n\t" \
  "v_mfma_f32_16x16x32_bf16 v[40:43], a[152:155], v[84:87], v[40:43]\n\t" \
  "v_mfma_f32_16x16x32_bf16 v[44:47], a[224:227], v[84:87], v[44:47]\n\t" \
  "buffer_store_dwordx4 v[56:59], v0, %[X], s51 offen offset:0 sc1\n\t" \
  "v_mfma_f32_16x16x32_bf16 v[40:43], a[156:159], v[92:95], v[40:43]\n\t" \
  "s_add_u32 m0, s46, 8192\n\t" \
  "s_add_u32 s47, s55, 16384\n\t" \
  "buffer_load_dwordx4 v0, %[X], s47 offen sc1 lds\n\t" \
  "v_mfma_f32_16x16x32_bf16 v[44:47], a[228:231], v[92:95], v[44:47]\n\t" \
  "v_mfma_f32_16x16x32_bf16 v[40:43], a[160:163], v[100:103], v[40:43]\n\t" \
  "v_mfma_f32_16x16x32_bf16 v[44:47], a[232:235], v[100:103], v[44:47]\n\t" \
  "v_mfma_f32_16x16x32_bf16 v[40:43], a[164:167], v[108:111], v[40:43]\n\t" \
  "s_add_u32 m0, s46, 12288\n\t" \
  "s_add_u32 s47, s55, 20480\n\t" \
  "buffer_load_dwordx4 v0, %[X], s47 offen sc1 lds\n\t" \
  "v_mfma_f32_16x16x32_bf16 v[44:47], a[236:239], v[108:111], v[44:47]\n\t" \
  "v_mfma_f32_16x16x32_bf16 v[40:43], a[168:171], v[116:119], v[40:43]\n\t" \
  "v_mfma_f32_16x16x32_bf16 v[44:47], a[240:243], v[116:119], v[44:47]\n\t" \
  "v_mfma_f32_16x16x32_bf16 v[40:43], a[172:175], v[124:127], v[40:43]\n\t" \
  "s_add_u32 m0, s46, 16384\n\t" \
  "s_add_u32 s47, s55, 32768\n\t" \
  "buffer_load_dwordx4 v0, %[X], s47 offen sc1 lds\n\t" \
  "v_mfma_f32_16x16x32_bf16 v[44:47], a[244:247], v[124:127], v[44:47]\n\t" \
  "v_mfma_f32_16x16x32_bf16 v[40:43], a[176:179], v[132:135], v[40:43]\n\t" \
  "v_mfma_f32_16x16x32_bf16 v[44:47], a[248:251], v[132:135], v[44:47]\n\t" \
  "v_mfma_f32_16x16x32_bf16 v[40:43], a[180:183], v[140:143], v[40:43]\n\t" \
  "s_add_u32 m0, s46, 20480\n\t" \
  "s_add_u32 s47, s55, 36864\n\t" \
  "buffer_load_dwordx4 v0, %[X], s47 offen sc1 lds\n\t" \
  "v_mfma_f32_16x16x32_bf16 v[44:47], a[252:255], v[140:143], v[44:47]\n\t" \
  "v_mfma_f32_16x16x32_bf16 v[40:43], a[184:187], v[148:151], v[40:43]\n\t" \
  "v_mfma_f32_16x16x32_bf16 v[44:47], v[224:227], v[148:151], v[44:47]\n\t" \
  "v_mfma_f32_16x16x32_bf16 v[40:43], a[188:191], v[156:159], v[40:43]\n\t" \
  "s_add_u32 m0, s46, 24576\n\t" \
  "s_add_u32 s47, s55, 49152\n\t" \
  "buffer_load_dwordx4 v0, %[X], s47 offen sc1 lds\n\t" \
  "v_mfma_f32_16x16x32_bf16 v[44:47], v[228:231], v[156:159], v[44:47]\n\t" \
  "v_mfma_f32_16x16x32_bf16 v[40:43], a[192:195], v[164:167], v[40:43]\n\t" \
  "v_mfma_f32_16x16x32_bf16 v[44:47], v[232:235], v[164:167], v[44:47]\n\t" \
  "v_mfma_f32_16x16x32_bf16 v[40:43], a[196:199], v[172:175], v[40:43]\n\t" \
  "s_add_u32 m0, s46, 28672\n\t" \
  "s_add_u32 s47, s55, 53248\n\t" \
  "buffer_load_dwordx4 v0, %[X], s47 offen sc1 lds\n\t" \
  "v_mfma_f32_16x16x32_bf16 v[44:47], v[236:239], v[172:175], v[44:47]\n\t" \
  "v_mfma_f32_16x16x32_bf16 v[40:43], a[200:203], v[180:183], v[40:43]\n\t" \
  "v_mfma_f32_16x16x32_bf16 v[44:47], v[240:243], v[180:183], v[44:47]\n\t" \
  "v_mfma_f32_16x16x32_bf16 v[40:43], a[204:207], v[188:191], v[40:43]\n\t" \
  "s_add_u32 m0, s46, 32768\n\t" \
  "s_add_u32 s47, s55, 65536\n\t" \
  "buffer_load_dwordx4 v0, %[X], s47 offen sc1 lds\n\t" \
  "v_mfma_f32_16x16x32_bf16 v[44:47], v[244:247], v[188:191], v[44:47]\n\t" \
  "v_mfma_f32_16x16x32_bf16 v[40:43], a[208:211], v[196:199], v[40:43]\n\t" \
  "v_mfma_f32_16x16x32_bf16 v[44:47], v[248:251], v[196:199], v[44:47]\n\t" \
  "buffer_load_dword v8, off, %[X], s57 sc1\n\t" \
  "v_mfma_f32_16x16x32_bf16 v[40:43], a[212:215], v[204:207], v[40:43]\n\t" \
  "v_mfma_f32_16x16x32_bf16 v[44:47], v[252:255], v[204:207], v[44:47]\n\t" \
  "s_nop 4\n\t" \
  "v_cvt_pk_bf16_f32 v60, v40, v41\n\t" \
  "v_cvt_pk_bf16_f32 v61, v42, v43\n\t" \
  "s_nop 1\n\t" \
  "v_cvt_pk_bf16_f32 v62, v44, v45\n\t" \
  "v_cvt_pk_bf16_f32 v63, v46, v47\n\t" \
  "buffer_store_dwordx4 v[60:63], v0, %[X], s51 offen offset:1024 sc1\n\t" \
  "s_add_u32 s49, s82, s38\n\t" \
  "s_cmp_eq_u32 s48, 63\n\t" \
  "s_cbranch_scc1 LEND_%=\n\t" \
  "s_add_u32 s48, s48, 1\n\t" \
  "s_cmp_eq_u32 s56, 0\n\t" \
  "s_cbranch_scc1 LCOLD_%=\n\t" \
  "s_waitcnt vmcnt(0)\n\t" \
  "s_mov_b64 exec, 1\n\t" \
  "buffer_store_dword v6, off, %[X], s49 sc1\n\t" \
  "s_mov_b64 exec, -1\n\t" \
  "s_mov_b32 s49, 0xf0000000\n\t" \
  "s_cmp_lt_i32 s57, 0\n\t" \
  "s_cbranch_scc1 LTOP_%=\n\t" \
  "s_movk_i32 s63, 4000\n\t" \
  "s_branch LGW_%=u\n\t" \
  "LGW_%=:\n\t" \
  "s_sleep " TK_PSG "\n\t" \
  "buffer_load_dword v8, off, %[X], s57 sc1\n\t" \
  "s_waitcnt vmcnt(0)\n\t" \
  "LGW_%=u:\n\t" \
  "s_memrealtime s[52:53]\n\t" \
  "v_readfirstlane_b32 s60, v8\n\t" \
  "s_waitcnt lgkmcnt(0)\n\t" \
  "s_sub_u32 s63, s63, 1\n\t" \
  "s_cbranch_scc1 LTOP_%=\n\t" \
  "s_cmp_eq_u32 s60, -1\n\t" \
  "s_cbranch_scc1 LGW_%=\n\t" \
  "s_not_b32 s60, s60\n\t" \
  "s_lshl_b32 s61, s52, 8\n\t" \
  "s_sub_u32 s60, s60, s61\n\t" \
  "s_ashr_i32 s60, s60, 8\n\t" \
  "s_sub_i32 s60, s60, " TK_QXW "\n\t" \
  "s_min_i32 s60, s60, 400\n\t" \
  "s_add_u32 s54, s52, s60\n\t" \
  "LGW_%=w:\n\t" \
  "s_sleep 1\n\t" \
  "s_memrealtime s[52:53]\n\t" \
  "s_waitcnt lgkmcnt(0)\n\t" \
  "s_sub_i32 s53, s52, s54\n\t" \
  "s_cmp_lt_i32 s53, 0\n\t" \
  "s_cbranch_scc1 LGW_%=w\n\t" \
  "s_branch LTOP_%=\n\t" \
  "LEND_%=:\n\t" \
  "s_waitcnt vmcnt(0)\n\t" \
  "s_mov_b64 exec, 1\n\t" \
  "buffer_store_dword v6, off, %[X], s49 sc1\n\t" \
  "s_mov_b64 exec, -1\n\t" \
  "s_mov_b32 s49, 0xf0000000\n\t" \
  ""
#define QV_ASM \
  "v_lshrrev_b32 v16, 7, v0\n\t" \
  "v_lshlrev_b32 v16, 13, v16\n\t" \
  "v_lshrrev_b32 v19, 2, v0\n\t" \
  "v_and_b32 v19, 64, v19\n\t" \
  "v_xor_b32 v19, v19, v0\n\t" \
  "v_and_b32 v19, 0x70, v19\n\t" \
  "v_add_u32 v16, v16, v19\n\t" \
  "s_mul_i32 s63, s40, 40960\n\t" \
  "v_and_b32 v19, 0x300, v0\n\t" \
  "v_lshlrev_b32 v17, 2, v19\n\t" \
  "v_and_b32 v19, 0xc0, v0\n\t" \
  "v_lshl_add_u32 v17, v19, 1, v17\n\t" \
  "v_and_b32 v19, 0x30, v0\n\t" \
  "v_lshrrev_b32 v19, 1, v19\n\t" \
  "v_add3_u32 v17, v17, v19, s63\n\t" \
  "v_lshrrev_b32 v19, 1, v0\n\t" \
  "v_and_b32 v19, 64, v19\n\t" \
  "v_add_u32 v17, v17, v19\n\t" \
  "v_xor_b32 v18, 64, v17\n\t" \
  "s_add_u32 m0, s63, 0\n\t" \
  "s_mov_b32 s60, 0\n\t" \
  "buffer_load_dwordx4 v16, %[W], s60 offen lds\n\t" \
  "s_add_u32 m0, s63, 1024\n\t" \
  "s_mov_b32 s60, 65536\n\t" \
  "buffer_load_dwordx4 v16, %[W], s60 offen lds\n\t" \
  "s_add_u32 m0, s63, 2048\n\t" \
  "s_mov_b32 s60, 131072\n\t" \
  "buffer_load_dwordx4 v16, %[W], s60 offen lds\n\t" \
  "s_add_u32 m0, s63, 3072\n\t" \
  "s_mov_b32 s60, 196608\n\t" \
  "buffer_load_dwordx4 v16, %[W], s60 offen lds\n\t" \
  "s_add_u32 m0, s63, 4096\n\t" \
  "s_mov_b32 s60, 262144\n\t" \
  "buffer_load_dwordx4 v16, %[W], s60 offen lds\n\t" \
  "s_add_u32 m0, s63, 5120\n\t" \
  "s_mov_b32 s60, 327680\n\t" \
  "buffer_load_dwordx4 v16, %[W], s60 offen lds\n\t" \
  "s_add_u32 m0, s63, 6144\n\t" \
  "s_mov_b32 s60, 393216\n\t" \
  "buffer_load_dwordx4 v16, %[W], s60 offen lds\n\t" \
  "s_add_u32 m0, s63, 7168\n\t" \
  "s_mov_b32 s60, 458752\n\t" \
  "buffer_load_dwordx4 v16, %[W], s60 offen lds\n\t" \
  "s_add_u32 m0, s63, 8192\n\t" \
  "s_mov_b32 s60, 524288\n\t" \
  "buffer_load_dwordx4 v16, %[W], s60 offen lds\n\t" \
  "s_add_u32 m0, s63, 9216\n\t" \
  "s_mov_b32 s60, 589824\n\t" \
  "buffer_load_dwordx4 v16, %[W], s60 offen lds\n\t" \
  "s_add_u32 m0, s63, 10240\n\t" \
  "s_mov_b32 s60, 655360\n\t" \
  "buffer_load_dwordx4 v16, %[W], s60 offen lds\n\t" \
  "s_add_u32 m0, s63, 11264\n\t" \
  "s_mov_b32 s60, 720896\n\t" \
  "buffer_load_dwordx4 v16, %[W], s60 offen lds\n\t" \
  "s_add_u32 m0, s63, 12288\n\t" \
  "s_mov_b32 s60, 786432\n\t" \
  "buffer_load_dwordx4 v16, %[W], s60 offen lds\n\t" \
  "s_add_u32 m0, s63, 13312\n\t" \
  "s_mov_b32 s60, 851968\n\t" \
  "buffer_load_dwordx4 v16, %[W], s60 offen lds\n\t" \
  "s_add_u32 m0, s63, 14336\n\t" \
  "s_mov_b32 s60, 917504\n\t" \
  "buffer_load_dwordx4 v16, %[W], s60 offen lds\n\t" \
  "s_add_u32 m0, s63, 15360\n\t" \
  "s_mov_b32 s60, 983040\n\t" \
  "buffer_load_dwordx4 v16, %[W], s60 offen lds\n\t" \
  "s_add_u32 m0, s63, 16384\n\t" \
  "s_mov_b32 s60, 1048576\n\t" \
  "buffer_load_dwordx4 v16, %[W], s60 offen lds\n\t" \
  "s_add_u32 m0, s63, 17408\n\t" \
  "s_mov_b32 s60, 1114112\n\t" \
  "buffer_load_dwordx4 v16, %[W], s60 offen lds\n\t" \
  "s_add_u32 m0, s63, 18432\n\t" \
  "s_mov_b32 s60, 1179648\n\t" \
  "buffer_load_dwordx4 v16, %[W], s60 offen lds\n\t" \
  "s_add_u32 m0, s63, 19456\n\t" \
  "s_mov_b32 s60, 1245184\n\t" \
  "buffer_load_dwordx4 v16, %[W], s60 offen lds\n\t" \
  "s_add_u32 m0, s63, 20480\n\t" \
  "s_mov_b32 s60, 1310720\n\t" \
  "buffer_load_dwordx4 v16, %[W], s60 offen lds\n\t" \
  "s_add_u32 m0, s63, 21504\n\t" \
  "s_mov_b32 s60, 1376256\n\t" \
  "buffer_load_dwordx4 v16, %[W], s60 offen lds\n\t" \
  "s_add_u32 m0, s63, 22528\n\t" \
  "s_mov_b32 s60, 1441792\n\t" \
  "buffer_load_dwordx4 v16, %[W], s60 offen lds\n\t" \
  "s_add_u32 m0, s63, 23552\n\t" \
  "s_mov_b32 s60, 1507328\n\t" \
  "buffer_load_dwordx4 v16, %[W], s60 offen lds\n\t" \
  "s_add_u32 m0, s63, 24576\n\t" \
  "s_mov_b32 s60, 1572864\n\t" \
  "buffer_load_dwordx4 v16, %[W], s60 offen lds\n\t" \
  "s_add_u32 m0, s63, 25600\n\t" \
  "s_mov_b32 s60, 1638400\n\t" \
  "buffer_load_dwordx4 v16, %[W], s60 offen lds\n\t" \
  "s_add_u32 m0, s63, 26624\n\t" \
  "s_mov_b32 s60, 1703936\n\t" \
  "buffer_load_dwordx4 v16, %[W], s60 offen lds\n\t" \
  "s_add_u32 m0, s63, 27648\n\t" \
  "s_mov_b32 s60, 1769472\n\t" \
  "buffer_load_dwordx4 v16, %[W], s60 offen lds\n\t" \
  "s_add_u32 m0, s63, 28672\n\t" \
  "s_mov_b32 s60, 1835008\n\t" \
  "buffer_load_dwordx4 v16, %[W], s60 offen lds\n\t" \
  "s_add_u32 m0, s63, 29696\n\t" \
  "s_mov_b32 s60, 1900544\n\t" \
  "buffer_load_dwordx4 v16, %[W], s60 offen lds\n\t" \
  "s_add_u32 m0, s63, 30720\n\t" \
  "s_mov_b32 s60, 1966080\n\t" \
  "buffer_load_dwordx4 v16, %[W], s60 offen lds\n\t" \
  "s_add_u32 m0, s63, 31744\n\t" \
  "s_mov_b32 s60, 2031616\n\t" \
  "buffer_load_dwordx4 v16, %[W], s60 offen lds\n\t" \
  "s_add_u32 m0, s63, 32768\n\t" \
  "s_mov_b32 s60, 2097152\n\t" \
  "buffer_load_dwordx4 v16, %[W], s60 offen lds\n\t" \
  "s_add_u32 m0, s63, 33792\n\t" \
  "s_mov_b32 s60, 2162688\n\t" \
  "buffer_load_dwordx4 v16, %[W], s60 offen lds\n\t" \
  "s_add_u32 m0, s63, 34816\n\t" \
  "s_mov_b32 s60, 2228224\n\t" \
  "buffer_load_dwordx4 v16, %[W], s60 offen lds\n\t" \
  "s_add_u32 m0, s63, 35840\n\t" \
  "s_mov_b32 s60, 2293760\n\t" \
  "buffer_load_dwordx4 v16, %[W], s60 offen lds\n\t" \
  "s_add_u32 m0, s63, 36864\n\t" \
  "s_mov_b32 s60, 2359296\n\t" \
  "buffer_load_dwordx4 v16, %[W], s60 offen lds\n\t" \
  "s_add_u32 m0, s63, 37888\n\t" \
  "s_mov_b32 s60, 2424832\n\t" \
  "buffer_load_dwordx4 v16, %[W], s60 offen lds\n\t" \
  "s_add_u32 m0, s63, 38912\n\t" \
  "s_mov_b32 s60, 2490368\n\t" \
  "buffer_load_dwordx4 v16, %[W], s60 offen lds\n\t" \
  "s_add_u32 m0, s63, 39936\n\t" \
  "s_mov_b32 s60, 2555904\n\t" \
  "buffer_load_dwordx4 v16, %[W], s60 offen lds\n\t" \
  "s_waitcnt vmcnt(32)\n\t" \
  "ds_read_b64_tr_b16 a[0:1], v17 offset:0\n\t" \
  "ds_read_b64_tr_b16 a[2:3], v17 offset:512\n\t" \
  "ds_read_b64_tr_b16 a[4:5], v17 offset:4096\n\t" \
  "ds_read_b64_tr_b16 a[6:7], v17 offset:4608\n\t" \
  "ds_read_b64_tr_b16 a[72:73], v17 offset:32\n\t" \
  "ds_read_b64_tr_b16 a[74:75], v17 offset:544\n\t" \
  "ds_read_b64_tr_b16 a[76:77], v17 offset:4128\n\t" \
  "ds_read_b64_tr_b16 a[78:79], v17 offset:4640\n\t" \
  "ds_read_b64_tr_b16 a[144:145], v18 offset:0\n\t" \
  "ds_read_b64_tr_b16 a[146:147], v18 offset:512\n\t" \
  "ds_read_b64_tr_b16 a[148:149], v18 offset:4096\n\t" \
  "ds_read_b64_tr_b16 a[150:151], v18 offset:4608\n\t" \
  "ds_read_b64_tr_b16 a[216:217], v18 offset:32\n\t" \
  "ds_read_b64_tr_b16 a[218:219], v18 offset:544\n\t" \
  "ds_read_b64_tr_b16 a[220:221], v18 offset:4128\n\t" \
  "ds_read_b64_tr_b16 a[222:223], v18 offset:4640\n\t" \
  "s_waitcnt lgkmcnt(0)\n\t" \
  "s_add_u32 m0, s63, 0\n\t" \
  "s_mov_b32 s60, 2621440\n\t" \
  "buffer_load_dwordx4 v16, %[W], s60 offen lds\n\t" \
  "s_add_u32 m0, s63, 1024\n\t" \
  "s_mov_b32 s60, 2686976\n\t" \
  "buffer_load_dwordx4 v16, %[W], s60 offen lds\n\t" \
  "s_add_u32 m0, s63, 2048\n\t" \
  "s_mov_b32 s60, 2752512\n\t" \
  "buffer_load_dwordx4 v16, %[W], s60 offen lds\n\t" \
  "s_add_u32 m0, s63, 3072\n\t" \
  "s_mov_b32 s60, 2818048\n\t" \
  "buffer_load_dwordx4 v16, %[W], s60 offen lds\n\t" \
  "s_add_u32 m0, s63, 4096\n\t" \
  "s_mov_b32 s60, 2883584\n\t" \
  "buffer_load_dwordx4 v16, %[W], s60 offen lds\n\t" \
  "s_add_u32 m0, s63, 5120\n\t" \
  "s_mov_b32 s60, 2949120\n\t" \
  "buffer_load_dwordx4 v16, %[W], s60 offen lds\n\t" \
  "s_add_u32 m0, s63, 6144\n\t" \
  "s_mov_b32 s60, 3014656\n\t" \
  "buffer_load_dwordx4 v16, %[W], s60 offen lds\n\t" \
  "s_add_u32 m0, s63, 7168\n\t" \
  "s_mov_b32 s60, 3080192\n\t" \
  "buffer_load_dwordx4 v16, %[W], s60 offen lds\n\t" \
  "s_waitcnt vmcnt(32)\n\t" \
  "ds_read_b64_tr_b16 a[8:9], v17 offset:8192\n\t" \
  "ds_read_b64_tr_b16 a[10:11], v17 offset:8704\n\t" \
  "ds_read_b64_tr_b16 a[12:13], v17 offset:12288\n\t" \
  "ds_read_b64_tr_b16 a[14:15], v17 offset:12800\n\t" \
  "ds_read_b64_tr_b16 a[80:81], v17 offset:8224\n\t" \
  "ds_read_b64_tr_b16 a[82:83], v17 offset:8736\n\t" \
  "ds_read_b64_tr_b16 a[84:85], v17 offset:12320\n\t" \
  "ds_read_b64_tr_b16 a[86:87], v17 offset:12832\n\t" \
  "ds_read_b64_tr_b16 a[152:153], v18 offset:8192\n\t" \
  "ds_read_b64_tr_b16 a[154:155], v18 offset:8704\n\t" \
  "ds_read_b64_tr_b16 a[156:157], v18 offset:12288\n\t" \
  "ds_read_b64_tr_b16 a[158:159], v18 offset:12800\n\t" \
  "ds_read_b64_tr_b16 a[224:225], v18 offset:8224\n\t" \
  "ds_read_b64_tr_b16 a[226:227], v18 offset:8736\n\t" \
  "ds_read_b64_tr_b16 a[228:229], v18 offset:12320\n\t" \
  "ds_read_b64_tr_b16 a[230:231], v18 offset:12832\n\t" \
  "s_waitcnt lgkmcnt(0)\n\t" \
  "s_add_u32 m0, s63, 8192\n\t" \
  "s_mov_b32 s60, 3145728\n\t" \
  "buffer_load_dwordx4 v16, %[W], s60 offen lds\n\t" \
  "s_add_u32 m0, s63, 9216\n\t" \
  "s_mov_b32 s60, 3211264\n\t" \
  "buffer_load_dwordx4 v16, %[W], s60 offen lds\n\t" \
  "s_add_u32 m0, s63, 10240\n\t" \
  "s_mov_b32 s60, 3276800\n\t" \
  "buffer_load_dwordx4 v16, %[W], s60 offen lds\n\t" \
  "s_add_u32 m0, s63, 11264\n\t" \
  "s_mov_b32 s60, 3342336\n\t" \
  "buffer_load_dwordx4 v16, %[W], s60 offen lds\n\t" \
  "s_add_u32 m0, s63, 12288\n\t" \
  "s_mov_b32 s60, 3407872\n\t" \
  "buffer_load_dwordx4 v16, %[W], s60 offen lds\n\t" \
  "s_add_u32 m0, s63, 13312\n\t" \
  "s_mov_b32 s60, 3473408\n\t" \
  "buffer_load_dwordx4 v16, %[W], s60 offen lds\n\t" \
  "s_add_u32 m0, s63, 14336\n\t" \
  "s_mov_b32 s60, 3538944\n\t" \
  "buffer_load_dwordx4 v16, %[W], s60 offen lds\n\t" \
  "s_add_u32 m0, s63, 15360\n\t" \
  "s_mov_b32 s60, 3604480\n\t" \
  "buffer_load_dwordx4 v16, %[W], s60 offen lds\n\t" \
  "s_waitcnt vmcnt(32)\n\t" \
  "ds_read_b64_tr_b16 a[16:17], v17 offset:16384\n\t" \
  "ds_read_b64_tr_b16 a[18:19], v17 offset:16896\n\t" \
  "ds_read_b64_tr_b16 a[20:21], v17 offset:20480\n\t" \
  "ds_read_b64_tr_b16 a[22:23], v17 offset:20992\n\t" \
  "ds_read_b64_tr_b16 a[88:89], v17 offset:16416\n\t" \
  "ds_read_b64_tr_b16 a[90:91], v17 offset:16928\n\t" \
  "ds_read_b64_tr_b16 a[92:93], v17 offset:20512\n\t" \
  "ds_read_b64_tr_b16 a[94:95], v17 offset:21024\n\t" \
  "ds_read_b64_tr_b16 a[160:161], v18 offset:16384\n\t" \
  "ds_read_b64_tr_b16 a[162:163], v18 offset:16896\n\t" \
  "ds_read_b64_tr_b16 a[164:165], v18 offset:20480\n\t" \
  "ds_read_b64_tr_b16 a[166:167], v18 offset:20992\n\t" \
  "ds_read_b64_tr_b16 a[232:233], v18 offset:16416\n\t" \
  "ds_read_b64_tr_b16 a[234:235], v18 offset:16928\n\t" \
  "ds_read_b64_tr_b16 a[236:237], v18 offset:20512\n\t" \
  "ds_read_b64_tr_b16 a[238:239], v18 offset:21024\n\t" \
  "s_waitcnt lgkmcnt(0)\n\t" \
  "s_add_u32 m0, s63, 16384\n\t" \
  "s_mov_b32 s60, 3670016\n\t" \
  "buffer_load_dwordx4 v16, %[W], s60 offen lds\n\t" \
  "s_add_u32 m0, s63, 17408\n\t" \
  "s_mov_b32 s60, 3735552\n\t" \
  "buffer_load_dwordx4 v16, %[W], s60 offen lds\n\t" \
  "s_add_u32 m0, s63, 18432\n\t" \
  "s_mov_b32 s60, 3801088\n\t" \
  "buffer_load_dwordx4 v16, %[W], s60 offen lds\n\t" \
  "s_add_u32 m0, s63, 19456\n\t" \
  "s_mov_b32 s60, 3866624\n\t" \
  "buffer_load_dwordx4 v16, %[W], s60 offen lds\n\t" \
  "s_add_u32 m0, s63, 20480\n\t" \
  "s_mov_b32 s60, 3932160\n\t" \
  "buffer_load_dwordx4 v16, %[W], s60 offen lds\n\t" \
  "s_add_u32 m0, s63, 21504\n\t" \
  "s_mov_b32 s60, 3997696\n\t" \
  "buffer_load_dwordx4 v16, %[W], s60 offen lds\n\t" \
  "s_add_u32 m0, s63, 22528\n\t" \
  "s_mov_b32 s60, 4063232\n\t" \
  "buffer_load_dwordx4 v16, %[W], s60 offen lds\n\t" \
  "s_add_u32 m0, s63, 23552\n\t" \
  "s_mov_b32 s60, 4128768\n\t" \
  "buffer_load_dwordx4 v16, %[W], s60 offen lds\n\t" \
  "s_waitcnt vmcnt(32)\n\t" \
  "ds_read_b64_tr_b16 a[24:25], v17 offset:24576\n\t" \
  "ds_read_b64_tr_b16 a[26:27], v17 offset:25088\n\t" \
  "ds_read_b64_tr_b16 a[28:29], v17 offset:28672\n\t" \
  "ds_read_b64_tr_b16 a[30:31], v17 offset:29184\n\t" \
  "ds_read_b64_tr_b16 a[96:97], v17 offset:24608\n\t" \
  "ds_read_b64_tr_b16 a[98:99], v17 offset:25120\n\t" \
  "ds_read_b64_tr_b16 a[100:101], v17 offset:28704\n\t" \
  "ds_read_b64_tr_b16 a[102:103], v17 offset:29216\n\t" \
  "ds_read_b64_tr_b16 a[168:169], v18 offset:24576\n\t" \
  "ds_read_b64_tr_b16 a[170:171], v18 offset:25088\n\t" \
  "ds_read_b64_tr_b16 a[172:173], v18 offset:28672\n\t" \
  "ds_read_b64_tr_b16 a[174:175], v18 offset:29184\n\t" \
  "ds_read_b64_tr_b16 a[240:241], v18 offset:24608\n\t" \
  "ds_read_b64_tr_b16 a[242:243], v18 offset:25120\n\t" \
  "ds_read_b64_tr_b16 a[244:245], v18 offset:28704\n\t" \
  "ds_read_b64_tr_b16 a[246:247], v18 offset:29216\n\t" \
  "s_waitcnt lgkmcnt(0)\n\t" \
  "s_add_u32 m0, s63, 24576\n\t" \
  "s_mov_b32 s60, 4194304\n\t" \
  "buffer_load_dwordx4 v16, %[W], s60 offen lds\n\t" \
  "s_add_u32 m0, s63, 25600\n\t" \
  "s_mov_b32 s60, 4259840\n\t" \
  "buffer_load_dwordx4 v16, %[W], s60 offen lds\n\t" \
  "s_add_u32 m0, s63, 26624\n\t" \
  "s_mov_b32 s60, 4325376\n\t" \
  "buffer_load_dwordx4 v16, %[W], s60 offen lds\n\t" \
  "s_add_u32 m0, s63, 27648\n\t" \
  "s_mov_b32 s60, 4390912\n\t" \
  "buffer_load_dwordx4 v16, %[W], s60 offen lds\n\t" \
  "s_add_u32 m0, s63, 28672\n\t" \
  "s_mov_b32 s60, 4456448\n\t" \
  "buffer_load_dwordx4 v16, %[W], s60 offen lds\n\t" \
  "s_add_u32 m0, s63, 29696\n\t" \
  "s_mov_b32 s60, 4521984\n\t" \
  "buffer_load_dwordx4 v16, %[W], s60 offen lds\n\t" \
  "s_add_u32 m0, s63, 30720\n\t" \
  "s_mov_b32 s60, 4587520\n\t" \
  "buffer_load_dwordx4 v16, %[W], s60 offen lds\n\t" \
  "s_add_u32 m0, s63, 31744\n\t" \
  "s_mov_b32 s60, 4653056\n\t" \
  "buffer_load_dwordx4 v16, %[W], s60 offen lds\n\t" \
  "s_waitcnt vmcnt(32)\n\t" \
  "ds_read_b64_tr_b16 a[32:33], v17 offset:32768\n\t" \
  "ds_read_b64_tr_b16 a[34:35], v17 offset:33280\n\t" \
  "ds_read_b64_tr_b16 a[36:37], v17 offset:36864\n\t" \
  "ds_read_b64_tr_b16 a[38:39], v17 offset:37376\n\t" \
  "ds_read_b64_tr_b16 a[104:105], v17 offset:32800\n\t" \
  "ds_read_b64_tr_b16 a[106:107], v17 offset:33312\n\t" \
  "ds_read_b64_tr_b16 a[108:109], v17 offset:36896\n\t" \
  "ds_read_b64_tr_b16 a[110:111], v17 offset:37408\n\t" \
  "ds_read_b64_tr_b16 a[176:177], v18 offset:32768\n\t" \
  "ds_read_b64_tr_b16 a[178:179], v18 offset:33280\n\t" \
  "ds_read_b64_tr_b16 a[180:181], v18 offset:36864\n\t" \
  "ds_read_b64_tr_b16 a[182:183], v18 offset:37376\n\t" \
  "ds_read_b64_tr_b16 a[248:249], v18 offset:32800\n\t" \
  "ds_read_b64_tr_b16 a[250:251], v18 offset:33312\n\t" \
  "ds_read_b64_tr_b16 a[252:253], v18 offset:36896\n\t" \
  "ds_read_b64_tr_b16 a[254:255], v18 offset:37408\n\t" \
  "s_waitcnt lgkmcnt(0)\n\t" \
  "s_waitcnt vmcnt(24)\n\t" \
  "ds_read_b64_tr_b16 a[40:41], v17 offset:0\n\t" \
  "ds_read_b64_tr_b16 a[42:43], v17 offset:512\n\t" \
  "ds_read_b64_tr_b16 a[44:45], v17 offset:4096\n\t" \
  "ds_read_b64_tr_b16 a[46:47], v17 offset:4608\n\t" \
  "ds_read_b64_tr_b16 a[112:113], v17 offset:32\n\t" \
  "ds_read_b64_tr_b16 a[114:115], v17 offset:544\n\t" \
  "ds_read_b64_tr_b16 a[116:117], v17 offset:4128\n\t" \
  "ds_read_b64_tr_b16 a[118:119], v17 offset:4640\n\t" \
  "ds_read_b64_tr_b16 a[184:185], v18 offset:0\n\t" \
  "ds_read_b64_tr_b16 a[186:187], v18 offset:512\n\t" \
  "ds_read_b64_tr_b16 a[188:189], v18 offset:4096\n\t" \
  "ds_read_b64_tr_b16 a[190:191], v18 offset:4608\n\t" \
  "ds_read_b64_tr_b16 v[224:225], v18 offset:32\n\t" \
  "ds_read_b64_tr_b16 v[226:227], v18 offset:544\n\t" \
  "ds_read_b64_tr_b16 v[228:229], v18 offset:4128\n\t" \
  "ds_read_b64_tr_b16 v[230:231], v18 offset:4640\n\t" \
  "s_waitcnt lgkmcnt(0)\n\t" \
  "s_waitcnt vmcnt(16)\n\t" \
  "ds_read_b64_tr_b16 a[48:49], v17 offset:8192\n\t" \
  "ds_read_b64_tr_b16 a[50:51], v17 offset:8704\n\t" \
  "ds_read_b64_tr_b16 a[52:53], v17 offset:12288\n\t" \
  "ds_read_b64_tr_b16 a[54:55], v17 offset:12800\n\t" \
  "ds_read_b64_tr_b16 a[120:121], v17 offset:8224\n\t" \
  "ds_read_b64_tr_b16 a[122:123], v17 offset:8736\n\t" \
  "ds_read_b64_tr_b16 a[124:125], v17 offset:12320\n\t" \
  "ds_read_b64_tr_b16 a[126:127], v17 offset:12832\n\t" \
  "ds_read_b64_tr_b16 a[192:193], v18 offset:8192\n\t" \
  "ds_read_b64_tr_b16 a[194:195], v18 offset:8704\n\t" \
  "ds_read_b64_tr_b16 a[196:197], v18 offset:12288\n\t" \
  "ds_read_b64_tr_b16 a[198:199], v18 offset:12800\n\t" \
  "ds_read_b64_tr_b16 v[232:233], v18 offset:8224\n\t" \
  "ds_read_b64_tr_b16 v[234:235], v18 offset:8736\n\t" \
  "ds_read_b64_tr_b16 v[236:237], v18 offset:12320\n\t" \
  "ds_read_b64_tr_b16 v[238:239], v18 offset:12832\n\t" \
  "s_waitcnt lgkmcnt(0)\n\t" \
  "s_waitcnt vmcnt(8)\n\t" \
  "ds_read_b64_tr_b16 a[56:57], v17 offset:16384\n\t" \
  "ds_read_b64_tr_b16 a[58:59], v17 offset:16896\n\t" \
  "ds_read_b64_tr_b16 a[60:61], v17 offset:20480\n\t" \
  "ds_read_b64_tr_b16 a[62:63], v17 offset:20992\n\t" \
  "ds_read_b64_tr_b16 a[128:129], v17 offset:16416\n\t" \
  "ds_read_b64_tr_b16 a[130:131], v17 offset:16928\n\t" \
  "ds_read_b64_tr_b16 a[132:133], v17 offset:20512\n\t" \
  "ds_read_b64_tr_b16 a[134:135], v17 offset:21024\n\t" \
  "ds_read_b64_tr_b16 a[200:201], v18 offset:16384\n\t" \
  "ds_read_b64_tr_b16 a[202:203], v18 offset:16896\n\t" \
  "ds_read_b64_tr_b16 a[204:205], v18 offset:20480\n\t" \
  "ds_read_b64_tr_b16 a[206:207], v18 offset:20992\n\t" \
  "ds_read_b64_tr_b16 v[240:241], v18 offset:16416\n\t" \
  "ds_read_b64_tr_b16 v[242:243], v18 offset:16928\n\t" \
  "ds_read_b64_tr_b16 v[244:245], v18 offset:20512\n\t" \
  "ds_read_b64_tr_b16 v[246:247], v18 offset:21024\n\t" \
  "s_waitcnt lgkmcnt(0)\n\t" \
  "s_waitcnt vmcnt(0)\n\t" \
  "ds_read_b64_tr_b16 a[64:65], v17 offset:24576\n\t" \
  "ds_read_b64_tr_b16 a[66:67], v17 offset:25088\n\t" \
  "ds_read_b64_tr_b16 a[68:69], v17 offset:28672\n\t" \
  "ds_read_b64_tr_b16 a[70:71], v17 offset:29184\n\t" \
  "ds_read_b64_tr_b16 a[136:137], v17 offset:24608\n\t" \
  "ds_read_b64_tr_b16 a[138:139], v17 offset:25120\n\t" \
  "ds_read_b64_tr_b16 a[140:141], v17 offset:28704\n\t" \
  "ds_read_b64_tr_b16 a[142:143], v17 offset:29216\n\t" \
  "ds_read_b64_tr_b16 a[208:209], v18 offset:24576\n\t" \
  "ds_read_b64_tr_b16 a[210:211], v18 offset:25088\n\t" \
  "ds_read_b64_tr_b16 a[212:213], v18 offset:28672\n\t" \
  "ds_read_b64_tr_b16 a[214:215], v18 offset:29184\n\t" \
  "ds_read_b64_tr_b16 v[248:249], v18 offset:24608\n\t" \
  "ds_read_b64_tr_b16 v[250:251], v18 offset:25120\n\t" \
  "ds_read_b64_tr_b16 v[252:253], v18 offset:28704\n\t" \
  "ds_read_b64_tr_b16 v[254:255], v18 offset:29216\n\t" \
  "s_waitcnt lgkmcnt(0)\n\t" \
  "s_mov_b32 s84, " TK_CAP "\n\t" \
  "s_mov_b32 s48, 0\n\t" \
  "s_mov_b32 s49, 0xf0000000\n\t" \
  "s_mov_b32 s46, s41\n\t" \
  "s_mov_b32 s56, 0\n\t" \
  "v_lshrrev_b32 v7, 2, v0\n\t" \
  "s_mov_b32 s60, " TK_CTAB "\n\t" \
  "buffer_load_dword v4, v7, %[X], s60 offen offset:88 sc1\n\t" \
  "buffer_load_dword v7, v7, %[X], s60 offen offset:0 sc1\n\t" \
  "s_waitcnt vmcnt(0)\n\t" \
  "v_and_b32 v4, 1, v4\n\t" \
  "v_and_b32 v7, 1, v7\n\t" \
  "v_cmp_ne_u32 s[90:91], s67, v4\n\t" \
  "v_cmp_ne_u32 s[88:89], s67, v7\n\t" \
  "s_nop 1\n\t" \
  "s_mov_b32 s57, 0xf0000000\n\t" \
  "s_add_u32 s83, s48, 0\n\t" \
  "s_lshr_b64 s[60:61], s[90:91], s83\n\t" \
  "s_and_b32 s60, s60, 1\n\t" \
  "s_lshl_b32 s60, s60, 13\n\t" \
  "s_add_u32 s83, s83, " TK_SG0 "\n\t" \
  "s_lshl_b32 s83, s83, 21\n\t" \
  "s_or_b32 s83, s83, s60\n\t" \
  "s_add_u32 s80, s48, " TK_TRN "\n\t" \
  "s_lshr_b64 s[60:61], s[90:91], s80\n\t" \
  "s_and_b32 s60, s60, 1\n\t" \
  "s_lshl_b32 s60, s60, 13\n\t" \
  "s_add_u32 s80, s80, " TK_SG0 "\n\t" \
  "s_lshl_b32 s80, s80, 21\n\t" \
  "s_or_b32 s80, s80, s60\n\t" \
  "s_add_u32 s62, s83, s39\n\t" \
  "s_add_u32 s80, s80, s39\n\t" \
  "s_add_u32 s80, s80, 2048\n\t" \
  "s_cmp_lt_u32 s48, " TK_TRM "\n\t" \
  "s_cselect_b32 s80, s62, s80\n\t" \
  "v_add_u32 v7, s62, v3\n\t" \
  "s_mov_b32 exec_lo, 0\n\t" \
  "v_mov_b32 v7, s80\n\t" \
  "s_mov_b32 exec_lo, -1\n\t" \
  "LCOLD_%=:\n\t" \
  "s_waitcnt vmcnt(0)\n\t" \
  "s_mov_b64 exec, 1\n\t" \
  "buffer_store_dword v6, off, %[X], s49 sc1\n\t" \
  "s_mov_b64 exec, -1\n\t" \
  "s_mov_b32 s49, 0xf0000000\n\t" \
  "s_add_u32 s55, s83, s36\n\t" \
  "s_cmp_lg_u32 s40, 0\n\t" \
  "s_cbranch_scc1 LGO_%=\n\t" \
  "s_and_b32 s62, s45, 3\n\t" \
  "s_lshl_b32 s62, s62, 7\n\t" \
  "s_add_u32 s62, s62, s83\n\t" \
  "s_add_u32 s62, s62, s39\n\t" \
  "s_mov_b32 s61, 0\n\t" \
  "LPO_%=:\n\t" \
  "buffer_load_dword v4, off, %[X], s62 sc1\n\t" \
  "s_waitcnt vmcnt(0)\n\t" \
  "v_cmp_eq_u32 vcc, 0, v4\n\t" \
  "s_cbranch_vccz LPO_%=x\n\t" \
  "s_add_u32 s61, s61, 1\n\t" \
  "s_sleep " TK_PSH "\n\t" \
  "s_cmp_gt_u32 s61, s84\n\t" \
  "s_cbranch_scc1 LPO_%=g\n\t" \
  "s_and_b32 s60, s61, 1023\n\t" \
  "s_cmp_lg_u32 s60, 0\n\t" \
  "s_cbranch_scc1 LPO_%=\n\t" \
  "buffer_load_dword v4, off, %[X], s92 sc1\n\t" \
  "s_waitcnt vmcnt(0)\n\t" \
  "v_readfirstlane_b32 s60, v4\n\t" \
  "s_nop 0\n\t" \
  "s_cmp_eq_u32 s60, 0\n\t" \
  "s_cbranch_scc1 LPO_%=\n\t" \
  "LPO_%=g:\n\t" \
  "s_mov_b32 s84, 0\n\t" \
  "s_or_b32 s60, s48, 1610612736\n\t" \
  "v_mov_b32 v4, s60\n\t" \
  "s_mov_b64 exec, 1\n\t" \
  "buffer_store_dword v4, off, %[X], s92 sc1\n\t" \
  "s_mov_b64 exec, -1\n\t" \
  "LPO_%=x:\n\t" \
  "s_mov_b32 s61, 0\n\t" \
  "LPH_%=:\n\t" \
  "buffer_load_dword v4, v7, %[X], 0 offen sc1\n\t" \
  "s_waitcnt vmcnt(0)\n\t" \
  "v_cmp_eq_u32 vcc, 0, v4\n\t" \
  "s_cbranch_vccz LPH_%=x\n\t" \
  "s_add_u32 s61, s61, 1\n\t" \
  "s_sleep " TK_PSH "\n\t" \
  "s_cmp_gt_u32 s61, s84\n\t" \
  "s_cbranch_scc1 LPH_%=g\n\t" \
  "s_and_b32 s60, s61, 1023\n\t" \
  "s_cmp_lg_u32 s60, 0\n\t" \
  "s_cbranch_scc1 LPH_%=\n\t" \
  "buffer_load_dword v4, off, %[X], s92 sc1\n\t" \
  "s_waitcnt vmcnt(0)\n\t" \
  "v_readfirstlane_b32 s60, v4\n\t" \
  "s_nop 0\n\t" \
  "s_cmp_eq_u32 s60, 0\n\t" \
  "s_cbranch_scc1 LPH_%=\n\t" \
  "LPH_%=g:\n\t" \
  "s_mov_b32 s84, 0\n\t" \
  "s_or_b32 s60, s48, 1610612736\n\t" \
  "v_mov_b32 v4, s60\n\t" \
  "s_mov_b64 exec, 1\n\t" \
  "buffer_store_dword v4, off, %[X], s92 sc1\n\t" \
  "s_mov_b64 exec, -1\n\t" \
  "LPH_%=x:\n\t" \
  "s_cmp_lt_i32 s57, 0\n\t" \
  "s_cbranch_scc1 LGO_%=\n\t" \
  "s_movk_i32 s63, 4000\n\t" \
  "s_branch LGC_%=u\n\t" \
  "LGC_%=:\n\t" \
  "s_sleep " TK_PSG "\n\t" \
  "buffer_load_dword v8, off, %[X], s57 sc1\n\t" \
  "s_waitcnt vmcnt(0)\n\t" \
  "LGC_%=u:\n\t" \
  "s_memrealtime s[52:53]\n\t" \
  "v_readfirstlane_b32 s60, v8\n\t" \
  "s_waitcnt lgkmcnt(0)\n\t" \
  "s_sub_u32 s63, s63, 1\n\t" \
  "s_cbranch_scc1 LGO_%=\n\t" \
  "s_cmp_eq_u32 s60, -1\n\t" \
  "s_cbranch_scc1 LGC_%=\n\t" \
  "s_not_b32 s60, s60\n\t" \
  "s_lshl_b32 s61, s52, 8\n\t" \
  "s_sub_u32 s60, s60, s61\n\t" \
  "s_ashr_i32 s60, s60, 8\n\t" \
  "s_sub_i32 s60, s60, " TK_QXC "\n\t" \
  "s_min_i32 s60, s60, 400\n\t" \
  "s_add_u32 s54, s52, s60\n\t" \
  "LGC_%=w:\n\t" \
  "s_sleep 1\n\t" \
  "s_memrealtime s[52:53]\n\t" \
  "s_waitcnt lgkmcnt(0)\n\t" \
  "s_sub_i32 s53, s52, s54\n\t" \
  "s_cmp_lt_i32 s53, 0\n\t" \
  "s_cbranch_scc1 LGC_%=w\n\t" \
  "LGO_%=:\n\t" \
  "s_barrier\n\t" \
  "s_add_u32 m0, s46, 0\n\t" \
  "s_add_u32 s47, s55, 0\n\t" \
  "buffer_load_dwordx4 v0, %[X], s47 offen sc1 lds\n\t" \
  "s_add_u32 m0, s46, 4096\n\t" \
  "s_add_u32 s47, s55, 4096\n\t" \
  "buffer_load_dwordx4 v0, %[X], s47 offen sc1 lds\n\t" \
  "s_add_u32 m0, s46, 8192\n\t" \
  "s_add_u32 s47, s55, 16384\n\t" \
  "buffer_load_dwordx4 v0, %[X], s47 offen sc1 lds\n\t" \
  "s_add_u32 m0, s46, 12288\n\t" \
  "s_add_u32 s47, s55, 20480\n\t" \
  "buffer_load_dwordx4 v0, %[X], s47 offen sc1 lds\n\t" \
  "s_add_u32 m0, s46, 16384\n\t" \
  "s_add_u32 s47, s55, 32768\n\t" \
  "buffer_load_dwordx4 v0, %[X], s47 offen sc1 lds\n\t" \
  "s_add_u32 m0, s46, 20480\n\t" \
  "s_add_u32 s47, s55, 36864\n\t" \
  "buffer_load_dwordx4 v0, %[X], s47 offen sc1 lds\n\t" \
  "s_add_u32 m0, s46, 24576\n\t" \
  "s_add_u32 s47, s55, 49152\n\t" \
  "buffer_load_dwordx4 v0, %[X], s47 offen sc1 lds\n\t" \
  "s_add_u32 m0, s46, 28672\n\t" \
  "s_add_u32 s47, s55, 53248\n\t" \
  "buffer_load_dwordx4 v0, %[X], s47 offen sc1 lds\n\t" \
  "s_add_u32 m0, s46, 32768\n\t" \
  "s_add_u32 s47, s55, 65536\n\t" \
  "buffer_load_dwordx4 v0, %[X], s47 offen sc1 lds\n\t" \
  "s_waitcnt vmcnt(5)\n\t" \
  "LTOP_%=:\n\t" \
  "s_barrier\n\t" \
  "ds_read_b128 v[64:67], v1 offset:0\n\t" \
  "ds_read_b128 v[68:71], v1 offset:2048\n\t" \
  "ds_read_b128 v[72:75], v2 offset:0\n\t" \
  "ds_read_b128 v[76:79], v2 offset:2048\n\t" \
  "ds_read_b128 v[80:83], v1 offset:4096\n\t" \
  "ds_read_b128 v[84:87], v1 offset:6144\n\t" \
  "s_mov_b32 s82, s83\n\t" \
  "s_xor_b32 s46, s46, 0x10000\n\t" \
  "s_waitcnt lgkmcnt(5)\n\t" \
  "v_mfma_f32_16x16x32_bf16 v[16:19], v[64:67], a[0:3], 0\n\t" \
  "ds_read_b128 v[88:91], v2 offset:4096\n\t" \
  "s_waitcnt lgkmcnt(5)\n\t" \
  "v_mfma_f32_16x16x32_bf16 v[20:23], v[68:71], a[0:3], 0\n\t" \
  "s_add_u32 s50, s48, 0\n\t" \
  "s_and_b32 s50, s50, " TK_TRM "\n\t" \
  "v_mfma_f32_16x16x32_bf16 v[24:27], v[64:67], a[72:75], 0\n\t" \
  "ds_read_b128 v[92:95], v2 offset:6144\n\t" \
  "v_mfma_f32_16x16x32_bf16 v[28:31], v[68:71], a[72:75], 0\n\t" \
  "s_lshr_b64 s[60:61], s[90:91], s50\n\t" \
  "s_and_b32 s60, s60, 1\n\t" \
  "s_waitcnt lgkmcnt(5)\n\t" \
  "v_mfma_f32_16x16x32_bf16 v[16:19], v[72:75], a[4:7], v[16:19]\n\t" \
  "ds_read_b128 v[96:99], v1 offset:8192\n\t" \
  "s_waitcnt lgkmcnt(5)\n\t" \
  "v_mfma_f32_16x16x32_bf16 v[20:23], v[76:79], a[4:7], v[20:23]\n\t" \
  "s_lshl_b32 s60, s60, 13\n\t" \
  "s_add_u32 s50, s50, " TK_SG0 "\n\t" \
  "v_mfma_f32_16x16x32_bf16 v[24:27], v[72:75], a[76:79], v[24:27]\n\t" \
  "ds_read_b128 v[100:103], v1 offset:10240\n\t" \
  "v_mfma_f32_16x16x32_bf16 v[28:31], v[76:79], a[76:79], v[28:31]\n\t" \
  "s_lshl_b32 s50, s50, 21\n\t" \
  "s_or_b32 s50, s50, s60\n\t" \
  "s_waitcnt lgkmcnt(5)\n\t" \
  "v_mfma_f32_16x16x32_bf16 v[16:19], v[80:83], a[8:11], v[16:19]\n\t" \
  "ds_read_b128 v[104:107], v2 offset:8192\n\t" \
  "s_waitcnt lgkmcnt(5)\n\t" \
  "v_mfma_f32_16x16x32_bf16 v[20:23], v[84:87], a[8:11], v[20:23]\n\t" \
  "s_add_u32 s50, s50, s37\n\t" \
  "s_add_u32 s51, s50, 4096\n\t" \
  "v_mfma_f32_16x16x32_bf16 v[24:27], v[80:83], a[80:83], v[24:27]\n\t" \
  "ds_read_b128 v[108:111], v2 offset:10240\n\t" \
  "v_mfma_f32_16x16x32_bf16 v[28:31], v[84:87], a[80:83], v[28:31]\n\t" \
  "s_add_u32 s83, s48, 1\n\t" \
  "s_lshr_b64 s[60:61], s[90:91], s83\n\t" \
  "s_waitcnt lgkmcnt(5)\n\t" \
  "v_mfma_f32_16x16x32_bf16 v[16:19], v[88:91], a[12:15], v[16:19]\n\t" \
  "ds_read_b128 v[112:115], v1 offset:12288\n\t" \
  "s_waitcnt lgkmcnt(5)\n\t" \
  "v_mfma_f32_16x16x32_bf16 v[20:23], v[92:95], a[12:15], v[20:23]\n\t" \
  "s_and_b32 s60, s60, 1\n\t" \
  "s_lshl_b32 s60, s60, 13\n\t" \
  "v_mfma_f32_16x16x32_bf16 v[24:27], v[88:91], a[84:87], v[24:27]\n\t" \
  "ds_read_b128 v[116:119], v1 offset:14336\n\t" \
  "v_mfma_f32_16x16x32_bf16 v[28:31], v[92:95], a[84:87], v[28:31]\n\t" \
  "s_add_u32 s83, s83, " TK_SG0 "\n\t" \
  "s_lshl_b32 s83, s83, 21\n\t" \
  "s_waitcnt lgkmcnt(5)\n\t" \
  "v_mfma_f32_16x16x32_bf16 v[16:19], v[96:99], a[16:19], v[16:19]\n\t" \
  "ds_read_b128 v[120:123], v2 offset:12288\n\t" \
  "s_waitcnt lgkmcnt(5)\n\t" \
  "v_mfma_f32_16x16x32_bf16 v[20:23], v[100:103], a[16:19], v[20:23]\n\t" \
  "s_or_b32 s83, s83, s60\n\t" \
  "s_cmp_lg_u32 s56, 0\n\t" \
  "v_mfma_f32_16x16x32_bf16 v[24:27], v[96:99], a[88:91], v[24:27]\n\t" \
  "ds_read_b128 v[124:127], v2 offset:14336\n\t" \
  "v_mfma_f32_16x16x32_bf16 v[28:31], v[100:103], a[88:91], v[28:31]\n\t" \
  "s_cbranch_scc1 LWARM_%=\n\t" \
  "s_waitcnt vmcnt(0)\n\t" \
  "LWARM_%=:\n\t" \
  "s_barrier\n\t" \
  "s_waitcnt lgkmcnt(5)\n\t" \
  "v_mfma_f32_16x16x32_bf16 v[16:19], v[104:107], a[20:23], v[16:19]\n\t" \
  "ds_read_b128 v[128:131], v1 offset:16384\n\t" \
  "s_waitcnt lgkmcnt(5)\n\t" \
  "v_mfma_f32_16x16x32_bf16 v[20:23], v[108:111], a[20:23], v[20:23]\n\t" \
  "v_mfma_f32_16x16x32_bf16 v[24:27], v[104:107], a[92:95], v[24:27]\n\t" \
  "ds_read_b128 v[132:135], v1 offset:18432\n\t" \
  "v_mfma_f32_16x16x32_bf16 v[28:31], v[108:111], a[92:95], v[28:31]\n\t" \
  "s_add_i32 s62, s48, s42\n\t" \
  "s_add_i32 s62, s62, 1\n\t" \
  "s_waitcnt lgkmcnt(5)\n\t" \
  "v_mfma_f32_16x16x32_bf16 v[16:19], v[112:115], a[24:27], v[16:19]\n\t" \
  "ds_read_b128 v[136:139], v2 offset:16384\n\t" \
  "s_waitcnt lgkmcnt(5)\n\t" \
  "v_mfma_f32_16x16x32_bf16 v[20:23], v[116:119], a[24:27], v[20:23]\n\t" \
  "s_mov_b32 s61, s62\n\t" \
  "v_mfma_f32_16x16x32_bf16 v[24:27], v[112:115], a[96:99], v[24:27]\n\t" \
  "ds_read_b128 v[140:143], v2 offset:18432\n\t" \
  "v_mfma_f32_16x16x32_bf16 v[28:31], v[116:119], a[96:99], v[28:31]\n\t" \
  "s_mul_i32 s63, s62, 43\n\t" \
  "s_lshr_b32 s63, s63, 9\n\t" \
  "s_waitcnt lgkmcnt(5)\n\t" \
  "v_mfma_f32_16x16x32_bf16 v[16:19], v[120:123], a[28:31], v[16:19]\n\t" \
  "ds_read_b128 v[144:147], v1 offset:20480\n\t" \
  "s_waitcnt lgkmcnt(5)\n\t" \
  "v_mfma_f32_16x16x32_bf16 v[20:23], v[124:127], a[28:31], v[20:23]\n\t" \
  "s_mul_i32 s63, s63, " TK_RM "\n\t" \
  "s_sub_u32 s62, s62, s63\n\t" \
  "v_mfma_f32_16x16x32_bf16 v[24:27], v[120:123], a[100:103], v[24:27]\n\t" \
  "ds_read_b128 v[148:151], v1 offset:22528\n\t" \
  "v_mfma_f32_16x16x32_bf16 v[28:31], v[124:127], a[100:103], v[28:31]\n\t" \
  "s_mul_i32 s63, s62, 43\n\t" \
  "s_lshr_b32 s63, s63, 7\n\t" \
  "s_waitcnt lgkmcnt(5)\n\t" \
  "v_mfma_f32_16x16x32_bf16 v[16:19], v[128:131], a[32:35], v[16:19]\n\t" \
  "ds_read_b128 v[152:155], v2 offset:20480\n\t" \
  "s_waitcnt lgkmcnt(5)\n\t" \
  "v_mfma_f32_16x16x32_bf16 v[20:23], v[132:135], a[32:35], v[20:23]\n\t" \
  "s_mul_i32 s57, s62, 688128\n\t" \
  "s_lshl_b32 s60, s63, 15\n\t" \
  "v_mfma_f32_16x16x32_bf16 v[24:27], v[128:131], a[104:107], v[24:27]\n\t" \
  "ds_read_b128 v[156:159], v2 offset:22528\n\t" \
  "v_mfma_f32_16x16x32_bf16 v[28:31], v[132:135], a[104:107], v[28:31]\n\t" \
  "s_add_u32 s57, s57, s60\n\t" \
  "s_lshr_b32 s60, s88, s63\n\t" \
  "s_waitcnt lgkmcnt(5)\n\t" \
  "v_mfma_f32_16x16x32_bf16 v[16:19], v[136:139], a[36:39], v[16:19]\n\t" \
  "ds_read_b128 v[160:163], v1 offset:24576\n\t" \
  "s_waitcnt lgkmcnt(5)\n\t" \
  "v_mfma_f32_16x16x32_bf16 v[20:23], v[140:143], a[36:39], v[20:23]\n\t" \
  "s_and_b32 s60, s60, 1\n\t" \
  "s_lshl_b32 s60, s60, 13\n\t" \
  "v_mfma_f32_16x16x32_bf16 v[24:27], v[136:139], a[108:111], v[24:27]\n\t" \
  "ds_read_b128 v[164:167], v1 offset:26624\n\t" \
  "v_mfma_f32_16x16x32_bf16 v[28:31], v[140:143], a[108:111], v[28:31]\n\t" \
  "s_add_u32 s57, s57, s60\n\t" \
  "s_add_u32 s57, s57, 656896\n\t" \
  "s_waitcnt lgkmcnt(5)\n\t" \
  "v_mfma_f32_16x16x32_bf16 v[16:19], v[144:147], a[40:43], v[16:19]\n\t" \
  "ds_read_b128 v[168:171], v2 offset:24576\n\t" \
  "s_waitcnt lgkmcnt(5)\n\t" \
  "v_mfma_f32_16x16x32_bf16 v[20:23], v[148:151], a[40:43], v[20:23]\n\t" \
  "s_cmp_lt_i32 s61, 1\n\t" \
  "s_cselect_b32 s57, 0xf0000000, s57\n\t" \
  "v_mfma_f32_16x16x32_bf16 v[24:27], v[144:147], a[112:115], v[24:27]\n\t" \
  "ds_read_b128 v[172:175], v2 offset:26624\n\t" \
  "v_mfma_f32_16x16x32_bf16 v[28:31], v[148:151], a[112:115], v[28:31]\n\t" \
  "s_cmp_lg_u32 s40, 0\n\t" \
  "s_cselect_b32 s57, 0xf0000000, s57\n\t" \
  "s_waitcnt lgkmcnt(5)\n\t" \
  "v_mfma_f32_16x16x32_bf16 v[16:19], v[152:155], a[44:47], v[16:19]\n\t" \
  "ds_read_b128 v[176:179], v1 offset:28672\n\t" \
  "s_waitcnt lgkmcnt(5)\n\t" \
  "v_mfma_f32_16x16x32_bf16 v[20:23], v[156:159], a[44:47], v[20:23]\n\t" \
  "s_add_u32 s80, s48, " TK_TRN "\n\t" \
  "s_lshr_b64 s[60:61], s[90:91], s80\n\t" \
  "s_and_b32 s60, s60, 1\n\t" \
  "v_mfma_f32_16x16x32_bf16 v[24:27], v[152:155], a[116:119], v[24:27]\n\t" \
  "ds_read_b128 v[180:183], v1 offset:30720\n\t" \
  "v_mfma_f32_16x16x32_bf16 v[28:31], v[156:159], a[116:119], v[28:31]\n\t" \
  "s_lshl_b32 s60, s60, 13\n\t" \
  "s_add_u32 s80, s80, " TK_SG0 "\n\t" \
  "s_lshl_b32 s80, s80, 21\n\t" \
  "s_waitcnt lgkmcnt(5)\n\t" \
  "v_mfma_f32_16x16x32_bf16 v[16:19], v[160:163], a[48:51], v[16:19]\n\t" \
  "ds_read_b128 v[184:187], v2 offset:28672\n\t" \
  "s_waitcnt lgkmcnt(5)\n\t" \
  "v_mfma_f32_16x16x32_bf16 v[20:23], v[164:167], a[48:51], v[20:23]\n\t" \
  "s_or_b32 s80, s80, s60\n\t" \
  "s_add_u32 s62, s83, s39\n\t" \
  "s_add_u32 s80, s80, s39\n\t" \
  "v_mfma_f32_16x16x32_bf16 v[24:27], v[160:163], a[120:123], v[24:27]\n\t" \
  "ds_read_b128 v[188:191], v2 offset:30720\n\t" \
  "v_mfma_f32_16x16x32_bf16 v[28:31], v[164:167], a[120:123], v[28:31]\n\t" \
  "s_add_u32 s80, s80, 2048\n\t" \
  "s_waitcnt lgkmcnt(5)\n\t" \
  "v_mfma_f32_16x16x32_bf16 v[16:19], v[168:171], a[52:55], v[16:19]\n\t" \
  "ds_read_b128 v[192:195], v1 offset:32768\n\t" \
  "s_waitcnt lgkmcnt(5)\n\t" \
  "v_mfma_f32_16x16x32_bf16 v[20:23], v[172:175], a[52:55], v[20:23]\n\t" \
  "v_mfma_f32_16x16x32_bf16 v[24:27], v[168:171], a[124:127], v[24:27]\n\t" \
  "ds_read_b128 v[196:199], v1 offset:34816\n\t" \
  "v_mfma_f32_16x16x32_bf16 v[28:31], v[172:175], a[124:127], v[28:31]\n\t" \
  "s_cmp_lt_u32 s48, " TK_TRM "\n\t" \
  "s_cselect_b32 s80, s62, s80\n\t" \
  "s_waitcnt lgkmcnt(5)\n\t" \
  "v_mfma_f32_16x16x32_bf16 v[16:19], v[176:179], a[56:59], v[16:19]\n\t" \
  "ds_read_b128 v[200:203], v2 offset:32768\n\t" \
  "s_waitcnt lgkmcnt(5)\n\t" \
  "v_mfma_f32_16x16x32_bf16 v[20:23], v[180:183], a[56:59], v[20:23]\n\t" \
  "v_add_u32 v7, s62, v3\n\t" \
  "s_mov_b32 exec_lo, 0\n\t" \
  "v_mov_b32 v7, s80\n\t" \
  "s_mov_b32 exec_lo, -1\n\t" \
  "v_mfma_f32_16x16x32_bf16 v[24:27], v[176:179], a[128:131], v[24:27]\n\t" \
  "ds_read_b128 v[204:207], v2 offset:34816\n\t" \
  "v_mfma_f32_16x16x32_bf16 v[28:31], v[180:183], a[128:131], v[28:31]\n\t" \
  "s_waitcnt lgkmcnt(5)\n\t" \
  "v_mfma_f32_16x16x32_bf16 v[16:19], v[184:187], a[60:63], v[16:19]\n\t" \
  "s_waitcnt lgkmcnt(4)\n\t" \
  "v_mfma_f32_16x16x32_bf16 v[20:23], v[188:191], a[60:63], v[20:23]\n\t" \
  "buffer_load_dword v4, v7, %[X], 0 offen sc1\n\t" \
  "v_mfma_f32_16x16x32_bf16 v[24:27], v[184:187], a[132:135], v[24:27]\n\t" \
  "v_mfma_f32_16x16x32_bf16 v[28:31], v[188:191], a[132:135], v[28:31]\n\t" \
  "s_waitcnt lgkmcnt(3)\n\t" \
  "v_mfma_f32_16x16x32_bf16 v[16:19], v[192:195], a[64:67], v[16:19]\n\t" \
  "v_xor_b32 v1, 0x10000, v1\n\t" \
  "v_xor_b32 v2, 0x10000, v2\n\t" \
  "s_waitcnt lgkmcnt(2)\n\t" \
  "v_mfma_f32_16x16x32_bf16 v[20:23], v[196:199], a[64:67], v[20:23]\n\t" \
  "v_mfma_f32_16x16x32_bf16 v[24:27], v[192:195], a[136:139], v[24:27]\n\t" \
  "v_mfma_f32_16x16x32_bf16 v[28:31], v[196:199], a[136:139], v[28:31]\n\t" \
  "s_waitcnt lgkmcnt(1)\n\t" \
  "v_mfma_f32_16x16x32_bf16 v[16:19], v[200:203], a[68:71], v[16:19]\n\t" \
  "s_waitcnt lgkmcnt(0)\n\t" \
  "v_mfma_f32_16x16x32_bf16 v[20:23], v[204:207], a[68:71], v[20:23]\n\t" \
  "v_mfma_f32_16x16x32_bf16 v[24:27], v[200:203], a[140:143], v[24:27]\n\t" \
  "v_mfma_f32_16x16x32_bf16 v[28:31], v[204:207], a[140:143], v[28:31]\n\t" \
  "v_mfma_f32_16x16x32_bf16 v[32:35], v[64:67], a[144:147], 0\n\t" \
  "v_cvt_pk_bf16_f32 v48, v16, v17\n\t" \
  "v_cvt_pk_bf16_f32 v49, v18, v19\n\t" \
  "v_mfma_f32_16x16x32_bf16 v[36:39], v[68:71], a[144:147], 0\n\t" \
  "v_cvt_pk_bf16_f32 v50, v20, v21\n\t" \
  "v_cvt_pk_bf16_f32 v51, v22, v23\n\t" \
  "v_mfma_f32_16x16x32_bf16 v[32:35], v[72:75], a[148:151], v[32:35]\n\t" \
  "v_cvt_pk_bf16_f32 v52, v24, v25\n\t" \
  "v_cvt_pk_bf16_f32 v53, v26, v27\n\t" \
  "v_mfma_f32_16x16x32_bf16 v[36:39], v[76:79], a[148:151], v[36:39]\n\t" \
  "v_cvt_pk_bf16_f32 v54, v28, v29\n\t" \
  "v_cvt_pk_bf16_f32 v55, v30, v31\n\t" \
  "v_mfma_f32_16x16x32_bf16 v[32:35], v[80:83], a[152:155], v[32:35]\n\t" \
  "buffer_store_dwordx4 v[48:51], v0, %[X], s50 offen offset:0 sc1\n\t" \
  "v_mfma_f32_16x16x32_bf16 v[36:39], v[84:87], a[152:155], v[36:39]\n\t" \
  "v_mfma_f32_16x16x32_bf16 v[32:35], v[88:91], a[156:159], v[32:35]\n\t" \
  "v_mfma_f32_16x16x32_bf16 v[36:39], v[92:95], a[156:159], v[36:39]\n\t" \
  "v_mfma_f32_16x16x32_bf16 v[32:35], v[96:99], a[160:163], v[32:35]\n\t" \
  "v_mfma_f32_16x16x32_bf16 v[36:39], v[100:103], a[160:163], v[36:39]\n\t" \
  "v_mfma_f32_16x16x32_bf16 v[32:35], v[104:107], a[164:167], v[32:35]\n\t" \
  "v_mfma_f32_16x16x32_bf16 v[36:39], v[108:111], a[164:167], v[36:39]\n\t" \
  "v_mfma_f32_16x16x32_bf16 v[32:35], v[112:115], a[168:171], v[32:35]\n\t" \
  "v_mfma_f32_16x16x32_bf16 v[36:39], v[116:119], a[168:171], v[36:39]\n\t" \
  "v_mfma_f32_16x16x32_bf16 v[32:35], v[120:123], a[172:175], v[32:35]\n\t" \
  "v_mfma_f32_16x16x32_bf16 v[36:39], v[124:127], a[172:175], v[36:39]\n\t" \
  "s_waitcnt vmcnt(1)\n\t" \
  "v_cmp_eq_u32 vcc, 0, v4\n\t" \
  "v_mfma_f32_16x16x32_bf16 v[32:35], v[128:131], a[176:179], v[32:35]\n\t" \
  "v_mfma_f32_16x16x32_bf16 v[36:39], v[132:135], a[176:179], v[36:39]\n\t" \
  "s_cmp_eq_u64 vcc, 0\n\t" \
  "s_cselect_b32 s60, 1, 0\n\t" \
  "v_mov_b32 v4, s60\n\t" \
  "v_mfma_f32_16x16x32_bf16 v[32:35], v[136:139], a[180:183], v[32:35]\n\t" \
  "ds_write_b32 v5, v4\n\t" \
  "v_mfma_f32_16x16x32_bf16 v[36:39], v[140:143], a[180:183], v[36:39]\n\t" \
  "v_mfma_f32_16x16x32_bf16 v[32:35], v[144:147], a[184:187], v[32:35]\n\t" \
  "v_mfma_f32_16x16x32_bf16 v[36:39], v[148:151], a[184:187], v[36:39]\n\t" \
  "buffer_store_dwordx4 v[52:55], v0, %[X], s50 offen offset:1024 sc1\n\t" \
  "v_mfma_f32_16x16x32_bf16 v[32:35], v[152:155], a[188:191], v[32:35]\n\t" \
  "v_mfma_f32_16x16x32_bf16 v[36:39], v[156:159], a[188:191], v[36:39]\n\t" \
  "s_waitcnt lgkmcnt(0)\n\t" \
  "s_barrier\n\t" \
  "v_mfma_f32_16x16x32_bf16 v[32:35], v[160:163], a[192:195], v[32:35]\n\t" \
  "ds_read_b32 v4, v6 offset:40959\n\t" \
  "v_mfma_f32_16x16x32_bf16 v[36:39], v[164:167], a[192:195], v[36:39]\n\t" \
  "v_mfma_f32_16x16x32_bf16 v[32:35], v[168:171], a[196:199], v[32:35]\n\t" \
  "v_mfma_f32_16x16x32_bf16 v[36:39], v[172:175], a[196:199], v[36:39]\n\t" \
  "v_mfma_f32_16x16x32_bf16 v[32:35], v[176:179], a[200:203], v[32:35]\n\t" \
  "v_mfma_f32_16x16x32_bf16 v[36:39], v[180:183], a[200:203], v[36:39]\n\t" \
  "s_waitcnt lgkmcnt(0)\n\t" \
  "v_readfirstlane_b32 s60, v4\n\t" \
  "s_add_u32 s55, s83, s36\n\t" \
  "v_mfma_f32_16x16x32_bf16 v[32:35], v[184:187], a[204:207], v[32:35]\n\t" \
  "s_cmp_eq_u32 s48, 63\n\t" \
  "s_cselect_b32 s56, 0, s60\n\t" \
  "v_mfma_f32_16x16x32_bf16 v[36:39], v[188:191], a[204:207], v[36:39]\n\t" \
  "s_cmp_eq_u32 s56, 0\n\t" \
  "s_cselect_b32 s55, 0xf0000000, s55\n\t" \
  "v_mfma_f32_16x16x32_bf16 v[32:35], v[192:195], a[208:211], v[32:35]\n\t" \
  "v_mfma_f32_16x16x32_bf16 v[36:39], v[196:199], a[208:211], v[36:39]\n\t" \
  "v_mfma_f32_16x16x32_bf16 v[32:35], v[200:203], a[212:215], v[32:35]\n\t" \
  "s_add_u32 m0, s46, 0\n\t" \
  "s_add_u32 s47, s55, 0\n\t" \
  "buffer_load_dwordx4 v0, %[X], s47 offen sc1 lds\n\t" \
  "v_mfma_f32_16x16x32_bf16 v[36:39], v[204:207], a[212:215], v[36:39]\n\t" \
  "v_mfma_f32_16x16x32_bf16 v[40:43], v[64:67], a[216:219], 0\n\t" \
  "v_mfma_f32_16x16x32_bf16 v[44:47], v[68:71], a[216:219], 0\n\t" \
  "v_mfma_f32_16x16x32_bf16 v[40:43], v[72:75], a[220:223], v[40:43]\n\t" \
  "v_cvt_pk_bf16_f32 v56, v32, v33\n\t" \
  "v_cvt_pk_bf16_f32 v57, v34, v35\n\t" \
  "s_add_u32 m0, s46, 4096\n\t" \
  "s_add_u32 s47, s55, 4096\n\t" \
  "buffer_load_dwordx4 v0, %[X], s47 offen sc1 lds\n\t" \
  "v_mfma_f32_16x16x32_bf16 v[44:47], v[76:79], a[220:223], v[44:47]\n\t" \
  "v_cvt_pk_bf16_f32 v58, v36, v37\n\t" \
  "v_cvt_pk_bf16_f32 v59, v38, v39\n\t" \
  "v_mfma_f32_16x16x32_bf16 v[40:43], v[80:83], a[224:227], v[40:43]\n\t" \
  "v_mfma_f32_16x16x32_bf16 v[44:47], v[84:87], a[224:227], v[44:47]\n\t" \
  "buffer_store_dwordx4 v[56:59], v0, %[X], s50 offen offset:2048 sc1\n\t" \
  "v_mfma_f32_16x16x32_bf16 v[40:43], v[88:91], a[228:231], v[40:43]\n\t" \
  "s_add_u32 m0, s46, 8192\n\t" \
  "s_add_u32 s47, s55, 16384\n\t" \
  "buffer_load_dwordx4 v0, %[X], s47 offen sc1 lds\n\t" \
  "v_mfma_f32_16x16x32_bf16 v[44:47], v[92:95], a[228:231], v[44:47]\n\t" \
  "v_mfma_f32_16x16x32_bf16 v[40:43], v[96:99], a[232:235], v[40:43]\n\t" \
  "v_mfma_f32_16x16x32_bf16 v[44:47], v[100:103], a[232:235], v[44:47]\n\t" \
  "v_mfma_f32_16x16x32_bf16 v[40:43], v[104:107], a[236:239], v[40:43]\n\t" \
  "s_add_u32 m0, s46, 12288\n\t" \
  "s_add_u32 s47, s55, 20480\n\t" \
  "buffer_load_dwordx4 v0, %[X], s47 offen sc1 lds\n\t" \
  "v_mfma_f32_16x16x32_bf16 v[44:47], v[108:111], a[236:239], v[44:47]\n\t" \
  "v_mfma_f32_16x16x32_bf16 v[40:43], v[112:115], a[240:243], v[40:43]\n\t" \
  "v_mfma_f32_16x16x32_bf16 v[44:47], v[116:119], a[240:243], v[44:47]\n\t" \
  "v_mfma_f32_16x16x32_bf16 v[40:43], v[120:123], a[244:247], v[40:43]\n\t" \
  "s_add_u32 m0, s46, 16384\n\t" \
  "s_add_u32 s47, s55, 32768\n\t" \
  "buffer_load_dwordx4 v0, %[X], s47 offen sc1 lds\n\t" \
  "v_mfma_f32_16x16x32_bf16 v[44:47], v[124:127], a[244:247], v[44:47]\n\t" \
  "v_mfma_f32_16x16x32_bf16 v[40:43], v[128:131], a[248:251], v[40:43]\n\t" \
  "v_mfma_f32_16x16x32_bf16 v[44:47], v[132:135], a[248:251], v[44:47]\n\t" \
  "v_mfma_f32_16x16x32_bf16 v[40:43], v[136:139], a[252:255], v[40:43]\n\t" \
  "s_add_u32 m0, s46, 20480\n\t" \
  "s_add_u32 s47, s55, 36864\n\t" \
  "buffer_load_dwordx4 v0, %[X], s47 offen sc1 lds\n\t" \
  "v_mfma_f32_16x16x32_bf16 v[44:47], v[140:143], a[252:255], v[44:47]\n\t" \
  "v_mfma_f32_16x16x32_bf16 v[40:43], v[144:147], v[224:227], v[40:43]\n\t" \
  "v_mfma_f32_16x16x32_bf16 v[44:47], v[148:151], v[224:227], v[44:47]\n\t" \
  "v_mfma_f32_16x16x32_bf16 v[40:43], v[152:155], v[228:231], v[40:43]\n\t" \
  "s_add_u32 m0, s46, 24576\n\t" \
  "s_add_u32 s47, s55, 49152\n\t" \
  "buffer_load_dwordx4 v0, %[X], s47 offen sc1 lds\n\t" \
  "v_mfma_f32_16x16x32_bf16 v[44:47], v[156:159], v[228:231], v[44:47]\n\t" \
  "v_mfma_f32_16x16x32_bf16 v[40:43], v[160:163], v[232:235], v[40:43]\n\t" \
  "v_mfma_f32_16x16x32_bf16 v[44:47], v[164:167], v[232:235], v[44:47]\n\t" \
  "v_mfma_f32_16x16x32_bf16 v[40:43], v[168:171], v[236:239], v[40:43]\n\t" \
  "s_add_u32 m0, s46, 28672\n\t" \
  "s_add_u32 s47, s55, 53248\n\t" \
  "buffer_load_dwordx4 v0, %[X], s47 offen sc1 lds\n\t" \
  "v_mfma_f32_16x16x32_bf16 v[44:47], v[172:175], v[236:239], v[44:47]\n\t" \
  "v_mfma_f32_16x16x32_bf16 v[40:43], v[176:179], v[240:243], v[40:43]\n\t" \
  "v_mfma_f32_16x16x32_bf16 v[44:47], v[180:183], v[240:243], v[44:47]\n\t" \
  "v_mfma_f32_16x16x32_bf16 v[40:43], v[184:187], v[244:247], v[40:43]\n\t" \
  "s_add_u32 m0, s46, 32768\n\t" \
  "s_add_u32 s47, s55, 65536\n\t" \
  "buffer_load_dwordx4 v0, %[X], s47 offen sc1 lds\n\t" \
  "v_mfma_f32_16x16x32_bf16 v[44:47], v[188:191], v[244:247], v[44:47]\n\t" \
  "v_mfma_f32_16x16x32_bf16 v[40:43], v[192:195], v[248:251], v[40:43]\n\t" \
  "v_mfma_f32_16x16x32_bf16 v[44:47], v[196:199], v[248:251], v[44:47]\n\t" \
  "buffer_load_dword v8, off, %[X], s57 sc1\n\t" \
  "v_mfma_f32_16x16x32_bf16 v[40:43], v[200:203], v[252:255], v[40:43]\n\t" \
  "v_mfma_f32_16x16x32_bf16 v[44:47], v[204:207], v[252:255], v[44:47]\n\t" \
  "s_nop 4\n\t" \
  "v_cvt_pk_bf16_f32 v60, v40, v41\n\t" \
  "v_cvt_pk_bf16_f32 v61, v42, v43\n\t" \
  "s_nop 1\n\t" \
  "v_cvt_pk_bf16_f32 v62, v44, v45\n\t" \
  "v_cvt_pk_bf16_f32 v63, v46, v47\n\t" \
  "buffer_store_dwordx4 v[60:63], v0, %[X], s50 offen offset:3072 sc1\n\t" \
  "s_add_u32 s49, s82, s38\n\t" \
  "s_cmp_eq_u32 s48, 63\n\t" \
  "s_cbranch_scc1 LEND_%=\n\t" \
  "s_add_u32 s48, s48, 1\n\t" \
  "s_cmp_eq_u32 s56, 0\n\t" \
  "s_cbranch_scc1 LCOLD_%=\n\t" \
  "s_waitcnt vmcnt(0)\n\t" \
  "s_mov_b64 exec, 1\n\t" \
  "buffer_store_dword v6, off, %[X], s49 sc1\n\t" \
  "s_mov_b64 exec, -1\n\t" \
  "s_mov_b32 s49, 0xf0000000\n\t" \
  "s_cmp_lt_i32 s57, 0\n\t" \
  "s_cbranch_scc1 LTOP_%=\n\t" \
  "s_movk_i32 s63, 4000\n\t" \
  "s_branch LGW_%=u\n\t" \
  "LGW_%=:\n\t" \
  "s_sleep " TK_PSG "\n\t" \
  "buffer_load_dword v8, off, %[X], s57 sc1\n\t" \
  "s_waitcnt vmcnt(0)\n\t" \
  "LGW_%=u:\n\t" \
  "s_memrealtime s[52:53]\n\t" \
  "v_readfirstlane_b32 s60, v8\n\t" \
  "s_waitcnt lgkmcnt(0)\n\t" \
  "s_sub_u32 s63, s63, 1\n\t" \
  "s_cbranch_scc1 LTOP_%=\n\t" \
  "s_cmp_eq_u32 s60, -1\n\t" \
  "s_cbranch_scc1 LGW_%=\n\t" \
  "s_not_b32 s60, s60\n\t" \
  "s_lshl_b32 s61, s52, 8\n\t" \
  "s_sub_u32 s60, s60, s61\n\t" \
  "s_ashr_i32 s60, s60, 8\n\t" \
  "s_sub_i32 s60, s60, " TK_QXW "\n\t" \
  "s_min_i32 s60, s60, 400\n\t" \
  "s_add_u32 s54, s52, s60\n\t" \
  "LGW_%=w:\n\t" \
  "s_sleep 1\n\t" \
  "s_memrealtime s[52:53]\n\t" \
  "s_waitcnt lgkmcnt(0)\n\t" \
  "s_sub_i32 s53, s52, s54\n\t" \
  "s_cmp_lt_i32 s53, 0\n\t" \
  "s_cbranch_scc1 LGW_%=w\n\t" \
  "s_branch LTOP_%=\n\t" \
  "LEND_%=:\n\t" \
  "s_waitcnt vmcnt(0)\n\t" \
  "s_mov_b64 exec, 1\n\t" \
  "buffer_store_dword v6, off, %[X], s49 sc1\n\t" \
  "s_mov_b64 exec, -1\n\t" \
  "s_mov_b32 s49, 0xf0000000\n\t" \
  ""
__device__ __forceinline__ unsigned hoff(int r, int piece) {
  const int m = 4 * (r >> 3) + (r & 3), kp = piece >> 3;
  return (P_H + (unsigned)(kp >> 1)) * PAIR + (unsigned)((kp & 1) * 4096 + (16 * ((r >> 2) & 1) + m) * 128 + (((piece ^ m) & 7) << 4));
}
template <int SLEEP>
__device__ __forceinline__ void wait_all(rsrc_t X, unsigned vo, unsigned so) {
  unsigned spins = 0;
  while (true) {
    asm volatile("" ::: "memory");
    if (__all(ld4(X, vo, so) != 0u)) break;
    if (spin_fail(spins, X)) break;
    __builtin_amdgcn_s_sleep(SLEEP);
  }
  asm volatile("" ::: "memory");
}
#define VQ_CLOB "v8", "v16", "v17", "v18", "v19", "v20", "v21", "v22", "v23", "v24", "v25", "v26", "v27", "v28", "v29", "v30", "v31", "v32", "v33", "v34", "v35", "v36", "v37", "v38", "v39", "v40", "v41", "v42", "v43", "v44", "v45", "v46", "v47", "v48", "v49", "v50", "v51", "v52", "v53", "v54", "v55", "v56", "v57", "v58", "v59", "v60", "v61", "v62", "v63", "v64", "v65", "v66", "v67", "v68", "v69", "v70", "v71", "v72", "v73", "v74", "v75", "v76", "v77", "v78", "v79", "v80", "v81", "v82", "v83", "v84", "v85", "v86", "v87", "v88", "v89", "v90", "v91", "v92", "v93", "v94", "v95", "v96", "v97", "v98", "v99", "v100", "v101", "v102", "v103", "v104", "v105", "v106", "v107", "v108", "v109", "v110", "v111", "v112", "v113", "v114", "v115", "v116", "v117", "v118", "v119", "v120", "v121", "v122", "v123", "v124", "v125", "v126", "v127", "v128", "v129", "v130", "v131", "v132", "v133", "v134", "v135", "v136", "v137", "v138", "v139", "v140", "v141", "v142", "v143", "v144", "v145", "v146", "v147", "v148", "v149", "v150", "v151", "v152", "v153", "v154", "v155", "v156", "v157", "v158", "v159", "v160", "v161", "v162", "v163", "v164", "v165", "v166", "v167", "v168", "v169", "v170", "v171", "v172", "v173", "v174", "v175", "v176", "v177", "v178", "v179", "v180", "v181", "v182", "v183", "v184", "v185", "v186", "v187", "v188", "v189", "v190", "v191", "v192", "v193", "v194", "v195", "v196", "v197", "v198", "v199", "v200", "v201", "v202", "v203", "v204", "v205", "v206", "v207", "v220", "v221"
#define VA_CLOB "v40", "v41", "v42", "v43", "v44", "v45", "v46", "v47", "v48", "v49", "v50", "v51", "v52", "v53", "v54", "v55", "v56", "v57", "v58", "v59", "v60", "v61", "v62", "v63", "v64", "v65", "v66", "v67", "v68", "v69", "v70", "v71", "v72", "v73", "v74", "v75", "v76", "v77", "v78", "v79", "v80", "v81", "v82", "v83", "v84", "v85", "v86", "v87", "v88", "v89", "v90", "v91", "v92", "v93", "v94", "v95", "v96", "v97", "v98", "v99", "v100", "v101", "v102", "v103", "v104", "v105", "v106", "v107", "v108", "v109", "v110", "v111", "v112", "v113", "v114", "v115", "v116", "v117", "v118", "v119", "v120", "v121", "v122", "v123", "v124", "v125", "v126", "v127", "v128", "v129", "v130", "v131", "v132", "v133", "v134", "v135", "v136", "v137", "v138", "v139", "v140", "v141", "v142", "v143", "v144", "v145", "v146", "v147", "v148", "v149", "v150", "v151", "v152", "v153", "v154", "v155", "v156", "v157", "v158", "v159", "v160", "v161", "v162", "v163", "v164", "v165", "v166", "v167", "v168", "v169", "v170", "v171", "v172", "v173", "v174", "v175", "v176", "v177", "v178", "v179", "v180", "v181", "v182", "v183", "v184", "v185", "v186", "v187", "v188", "v189", "v190", "v191", "v192", "v193", "v194", "v195", "v196", "v197", "v198", "v199", "v200", "v201", "v202", "v203", "v204", "v205", "v206", "v207", "v208", "v209", "v210", "v211", "v220", "v221"
#define TOK_COMMON() \
  const MP p = getp(); \
  const int c = (int)blockIdx.x - NCHW, xq = c & 3, L = (c >> 2) & 1, i = c >> 3; \
  const int tid = threadIdx.x, lane = tid & 63, w = __builtin_amdgcn_readfirstlane(tid >> 6); \
  const rsrc_t X = mkr(p.xb, XB_BYTES)
#define W_IN [W] "s"(WR)
#define W_CLOB "a0", "a1", "a2", "a3", "a4", "a5", "a6", "a7", "a8", "a9", "a10", "a11", "a12", "a13", "a14", "a15", "a16", "a17", "a18", "a19", "a20", "a21", "a22", "a23", "a24", "a25", "a26", "a27", "a28", "a29", "a30", "a31", "a32", "a33", "a34", "a35", "a36", "a37", "a38", "a39", "a40", "a41", "a42", "a43", "a44", "a45", "a46", "a47", "a48", "a49", "a50", "a51", "a52", "a53", "a54", "a55", "a56", "a57", "a58", "a59", "a60", "a61", "a62", "a63", "a64", "a65", "a66", "a67", "a68", "a69", "a70", "a71", "a72", "a73", "a74", "a75", "a76", "a77", "a78", "a79", "a80", "a81", "a82", "a83", "a84", "a85", "a86", "a87", "a88", "a89", "a90", "a91", "a92", "a93", "a94", "a95", "a96", "a97", "a98", "a99", "a100", "a101", "a102", "a103", "a104", "a105", "a106", "a107", "a108", "a109", "a110", "a111", "a112", "a113", "a114", "a115", "a116", "a117", "a118", "a119", "a120", "a121", "a122", "a123", "a124", "a125", "a126", "a127", "a128", "a129", "a130", "a131", "a132", "a133", "a134", "a135", "a136", "a137", "a138", "a139", "a140", "a141", "a142", "a143", "a144", "a145", "a146", "a147", "a148", "a149", "a150", "a151", "a152", "a153", "a154", "a155", "a156", "a157", "a158", "a159", "a160", "a161", "a162", "a163", "a164", "a165", "a166", "a167", "a168", "a169", "a170", "a171", "a172", "a173", "a174", "a175", "a176", "a177", "a178", "a179", "a180", "a181", "a182", "a183", "a184", "a185", "a186", "a187", "a188", "a189", "a190", "a191", "a192", "a193", "a194", "a195", "a196", "a197", "a198", "a199", "a200", "a201", "a202", "a203", "a204", "a205", "a206", "a207", "a208", "a209", "a210", "a211", "a212", "a213", "a214", "a215", "a216", "a217", "a218", "a219", "a220", "a221", "a222", "a223", "a224", "a225", "a226", "a227", "a228", "a229", "a230", "a231", "a232", "a233", "a234", "a235", "a236", "a237", "a238", "a239", "a240", "a241", "a242", "a243", "a244", "a245", "a246", "a247", "a248", "a249", "a250", "a251", "a252", "a253", "a254", "a255", "v224", "v225", "v226", "v227", "v228", "v229", "v230", "v231", "v232", "v233", "v234", "v235", "v236", "v237", "v238", "v239", "v240", "v241", "v242", "v243", "v244", "v245", "v246", "v247", "v248", "v249", "v250", "v251", "v252", "v253", "v254", "v255"
#define BOOT_IN "{s67}"(L), [ctab] "i"(CTAB_OFF), [rm] "i"(SRING == 12 ? 12 : 0), [trm] "i"(TRING - 1), [trn] "i"(1 - TRING)
#define S_CLOB "s80", "s81", "s76", "s78", "s79", "s86", "s88", "s89", "s90", "s91", "s46", "s47", "s48", "s49", "s50", "s51", "s52", "s53", "s54", "s55", "s56", "s57", "s58", "s59", "s60", "s61", "s62", "s63", "s68", "s69", "s70", "s71", "s72", "s73", "s82", "s83", "s84", "m0", "vcc", "scc", "memory"
typedef unsigned v8w __attribute__((ext_vector_type(8)));

__device__ __forceinline__ void qkv_role() {
  TOK_COMMON();
  const int role = i * 4 + w, hh = 8 * xq + role / 6, part = role % 6, which = part >> 1, half = part & 1;
  const rsrc_t WR = mkr((which == 0 ? p.Wq : (which == 1 ? p.Wk : p.Wv)) + hh * DH + 64 * half, DM * HD * 2u - (unsigned)(hh * DH + 64 * half) * 2u);
  const int m = lane & 15, g = lane >> 4;
  const unsigned a0 = (unsigned)(m * 128 + ((g ^ (m & 7)) << 4));
  v8w lv = {(unsigned)lane * 16u, a0, a0 ^ 64u, (unsigned)(lane & (L ? 15 : 3)) * 128u, 0u, 40960u + 4u * (unsigned)w, 1u, 0u};
  const unsigned c36 = P_H * PAIR + (unsigned)w * 1024u, c37 = (unsigned)(3 * hh + which) * PAIR + (unsigned)half * (which == 2 ? 4096u : 2048u);
  const unsigned c38 = P_FA * PAIR + (unsigned)(hh * 128 + part * 16), c39 = w == 0 ? P_FB * PAIR + 4096u : 0xF0000000u, c41 = (unsigned)w * 1024u;
  const int c42 = L ? 1 - QLEAD1 : 1 - QLEAD;
  unsigned d74 = TRC_OFF + 262144u + (unsigned)((c * 4 + w) * 66) * 8u;
#define Q_CALL(A) asm volatile("s_waitcnt vmcnt(0) lgkmcnt(0)\n\t" A : "+{v[0:7]}"(lv), "+{s74}"(d74) \
               : [X] "s"(X), "{s36}"(c36), "{s37}"(c37), "{s38}"(c38), "{s39}"(c39), "{s40}"(w), "{s41}"(c41), "{s42}"(c42), "{s45}"(i), "{s92}"(TMO_OFF), BOOT_IN, [cap] "i"(SPIN_CAP), [qxw] "i"(QXW), [qxc] "i"(QXC), W_IN : VQ_CLOB, W_CLOB, S_CLOB)
  if (which == 2) { Q_CALL(QV_ASM "LQVEND:"); __builtin_amdgcn_endpgm(); }
  Q_CALL(QK_ASM "LQKEND:");
  __builtin_amdgcn_endpgm();
}

__device__ __forceinline__ void ap_role() {
  TOK_COMMON();
  const int hp = 4 * xq + ((i - 12) >> 1), ch = i & 1, a = w >> 1, rt = w & 1, h = 2 * hp + a;
  const int n = lane & 15, g = lane >> 4;
  const rsrc_t WR = mkr(p.Wo + (long)hp * 2 * DH * DM, 2u * DH * DM * 2u);
  const unsigned l16 = (unsigned)lane * 16u, ab = (unsigned)a * 24576u + l16;
  v16u lv = {l16, ab, ab + (unsigned)rt * 4096u, 49152u + (unsigned)w * 4096u + l16, 49152u + (unsigned)(w ^ 2) * 4096u + l16, l16 & ~32u, l16 >> 1,
             (unsigned)(g * 262144 + n * 16), (unsigned)(n * 32 + g * 8), (unsigned)((lane & 7) % 6) * 16u, 0u, 0u, 0u, 0u, 0u, 0u};
  v8w mk;
  const int qi = 8 * (n >> 2) + (n & 3) + 4 * rt;
#pragma unroll
  for (int k = 0; k < 8; ++k) mk[k] = 8 * g + k > qi ? 0xFF800000u : 0u;
  v16u zz = {0u, 0u, 0u, 0u, 0u, 0u, 0u, 0u, 0u, 0u, 0u, 0u, 0u, 0u, 0u, 0u};
  const unsigned tb = (unsigned)(3 * h) * PAIR;
  const unsigned c36 = tb + (rt ? PAIR + 4096u : 0u), c37 = tb + (rt ? 2u * PAIR : 4096u), c38 = tb + (rt ? 2u * PAIR + 4096u : PAIR);
  const unsigned c39 = (P_KV + (unsigned)(h >> 1)) * PAIR + (unsigned)(h & 1) * 4096u, c41 = P_FA * PAIR + (unsigned)h * 128u;
  const unsigned c42 = w == 0 ? P_FB * PAIR + (unsigned)(2 * hp + ch) * 128u : 0xF0000000u;
  const unsigned c43 = TP0 * XBLK + (unsigned)(8 * rt + (hp >> 3)) * PAIR + (unsigned)((hp & 7) * 1024 + ch * 512 + a * 256);
  unsigned d44 = (TP0 + 8u) * XBLK + (unsigned)(hp >> 1) * PAIR + (unsigned)((((hp & 1) * 4 + 2 * ch + a) * 2 + rt) * 512);
  const unsigned c45 = (unsigned)(a * 24576 + rt * 12288);
  unsigned d74 = TRC_OFF + 262144u + (unsigned)((c * 4 + w) * 66) * 8u;
  asm volatile("s_waitcnt vmcnt(0) lgkmcnt(0)\n\t" AP_ASM "LAPEND:" : "+{v[0:15]}"(lv), "+{v[24:39]}"(zz), "+{s74}"(d74), "+{s44}"(d44)
               : [X] "s"(X), "{s36}"(c36), "{s37}"(c37), "{s38}"(c38), "{s39}"(c39), "{s41}"(c41), "{s42}"(c42), "{s43}"(c43), "{s45}"(c45), "{s64}"(512 * ch + 256 * a), "{s65}"(1024 + 64 * ch + 32 * a), "{s66}"(147456 * a), "{s77}"(L ? AX1 : AX), BOOT_IN,
                 "{s92}"(TMO_OFF), [cap] "i"(SPIN_CAP), "{v[16:23]}"(mk), W_IN : VA_CLOB, W_CLOB, S_CLOB);
  __builtin_amdgcn_endpgm();
}

template <int N> __device__ __forceinline__ float rorN(float v) { return __uint_as_float((unsigned)__builtin_amdgcn_update_dpp(0, (int)__float_as_uint(v), 0x120 + N, 0xf, 0xf, false)); }
__device__ __forceinline__ void up8(v4u v, float* x) {
  x[0] = lo2f(v.x); x[1] = hi2f(v.x); x[2] = lo2f(v.y); x[3] = hi2f(v.y); x[4] = lo2f(v.z); x[5] = hi2f(v.z); x[6] = lo2f(v.w); x[7] = hi2f(v.w);
}
__device__ __forceinline__ v4u pk8(const float* x) { return (v4u){pk2(x[0], x[1]), pk2(x[2], x[3]), pk2(x[4], x[5]), pk2(x[6], x[7])}; }

__device__ __forceinline__ void rb9(float (&x)[9]) {
#pragma unroll
  for (int i = 0; i < 8; i += 2) { const unsigned u = pk2(x[i], x[i + 1]); x[i] = lo2f(u); x[i + 1] = hi2f(u); }
  x[8] = lo2f(pk2(x[8], x[8]));
}
template <int R>
__device__ __forceinline__ void lnR(const float (&x)[R][9], float (&o)[R][9]) {
  float s[2 * R], pv[R];
#pragma unroll
  for (int r = 0; r < R; ++r) {
    pv[r] = __uint_as_float(__builtin_amdgcn_readfirstlane(__float_as_uint(x[r][0])));
    const f32x2 p2 = {pv[r], pv[r]};
    f32x2 d[4];
#pragma unroll
    for (int i = 0; i < 4; ++i) d[i] = (f32x2){x[r][2 * i], x[r][2 * i + 1]} - p2;
    const float d8 = x[r][8] - pv[r];
    const f32x2 a = (d[0] + d[1]) + (d[2] + d[3]);
    const f32x2 q = __builtin_elementwise_fma(d[3], d[3], __builtin_elementwise_fma(d[2], d[2], __builtin_elementwise_fma(d[1], d[1], d[0] * d[0])));
    s[2 * r] = (a[0] + d8) + a[1];
    s[2 * r + 1] = __builtin_fmaf(d8, d8, q[0]) + q[1];
  }
#pragma unroll
  for (int k = 0; k < 2 * R; ++k) s[k] += rorN<8>(s[k]);
#pragma unroll
  for (int k = 0; k < 2 * R; ++k) s[k] += rorN<4>(s[k]);
#pragma unroll
  for (int k = 0; k < 2 * R; ++k) s[k] += rorN<2>(s[k]);
#pragma unroll
  for (int k = 0; k < 2 * R; ++k) s[k] += rorN<1>(s[k]);
#pragma unroll
  for (int r = 0; r < R; ++r) {
    auto u = __builtin_amdgcn_permlane16_swap(__float_as_uint(s[2 * r]), __float_as_uint(s[2 * r + 1]), false, false);
    s[2 * r] = __uint_as_float(u[0]) + __uint_as_float(u[1]);
  }
#pragma unroll
  for (int r = 0; r < R; ++r) {
    const unsigned u = __float_as_uint(s[2 * r]);
    const float md = (__uint_as_float(__builtin_amdgcn_readlane(u, 0)) + __uint_as_float(__builtin_amdgcn_readlane(u, 32))) * (1.0f / DM);
    const float sq = (__uint_as_float(__builtin_amdgcn_readlane(u, 16)) + __uint_as_float(__builtin_amdgcn_readlane(u, 48))) * (1.0f / DM);
    const unsigned mv = pk2(pv[r] + md, __builtin_fmaxf(__builtin_fmaf(-md, md, sq), 0.f));
    const float m = lo2f(mv), var = hi2f(mv);
    const float ve = lo2f(pk2(var + 1e-5f, 0.f));
    const float den = lo2f(pk2(__builtin_amdgcn_sqrtf(ve), 0.f));
    const float inv = __builtin_amdgcn_rcpf(den);
#pragma unroll
    for (int i = 0; i < 9; ++i) o[r][i] = (x[r][i] - m) * inv;
  }
}
__device__ __forceinline__ void ln9(const float (&x)[9], float (&o)[9]) { lnR<1>((const float (&)[1][9])x, (float (&)[1][9])o); }
__device__ __forceinline__ v4u gat8(float v) {
  const unsigned d = pk2(v, __uint_as_float((unsigned)__builtin_amdgcn_mov_dpp((int)__float_as_uint(v), 0xB1, 0xf, 0xf, true)));
  return (v4u){d, (unsigned)__builtin_amdgcn_mov_dpp((int)d, 0x102, 0xf, 0xf, true), (unsigned)__builtin_amdgcn_mov_dpp((int)d, 0x104, 0xf, 0xf, true),
               (unsigned)__builtin_amdgcn_mov_dpp((int)d, 0x106, 0xf, 0xf, true)};
}
__device__ __forceinline__ void tok_row(const MP& p, const rsrc_t X, unsigned sb1, int L, int s, int r, int lane, const v4u (&v)[18], v4u px, v4u py, unsigned px9, unsigned py9) {
  const long tok = (long)s * SEG + r;
  const int m = lane & 15, g = lane >> 4;
  const int xp = 4 * (g & 1) + 2 * (g >> 1) + (m >> 3);
  const unsigned zr = opq(0u);
  const unsigned one = (g == (m >> 2)) ? (opq(0x3F80u) << (16 * (m & 1))) : zr;
  const unsigned ax = (m & 2) ? zr : one, ay = (m & 2) ? one : zr;
  const v4u A1 = {ax, ay, zr, zr}, A2 = {zr, zr, ax, ay};
  const unsigned on2 = opq(0x3F80u) << (16 * (m & 1));
  const int q2 = (m >> 1) & 3;
  const v4u sel = {q2 == 0 ? on2 : zr, q2 == 1 ? on2 : zr, q2 == 2 ? on2 : zr, q2 == 3 ? on2 : zr};
  f32x4 d1, d2, e1;
  asm volatile("s_nop 1\n\t" MF "%0, %1, %3, 0\n\t" MF "%0, %2, %3, %0" : "=&v"(e1) : "v"(v[0]), "v"(v[1]), "v"(sel));
#pragma unroll
  for (int i = 0; i < 16; ++i) {
    if (i == 0) asm volatile("s_nop 1\n\t" MF "%0, %2, %4, 0\n\t" MF "%1, %3, %4, 0" : "=&v"(d1), "=&v"(d2) : "v"(A1), "v"(A2), "v"(v[2]));
    else asm volatile(MF "%0, %2, %4, %0\n\t" MF "%1, %3, %4, %1" : "+v"(d1), "+v"(d2) : "v"(A1), "v"(A2), "v"(v[2 + i]));
  }
  asm volatile("s_nop 7" : "+v"(d1), "+v"(d2), "+v"(e1));
  float o[9];
#pragma unroll
  for (int i = 0; i < 4; ++i) { o[i] = d1[i]; o[4 + i] = d2[i]; }
  {
    auto a02 = __builtin_amdgcn_permlane32_swap(__float_as_uint(e1[0]), __float_as_uint(e1[2]), false, false);
    auto a13 = __builtin_amdgcn_permlane32_swap(__float_as_uint(e1[1]), __float_as_uint(e1[3]), false, false);
    o[8] = m >= 8 ? __uint_as_float(a13[0]) + __uint_as_float(a13[1]) : __uint_as_float(a02[0]) + __uint_as_float(a02[1]);
  }
  float x[9];
  up8(px, x); x[8] = lo2f(px9);
#pragma unroll
  for (int i = 0; i < 9; ++i) x[i] += o[i];
  if (L == 0) {
    float y[9], ln[9];
    up8(py, y); y[8] = lo2f(py9);
    rb9(x);
    ln9(x, ln);
#pragma unroll
    for (int i = 0; i < 9; ++i) x[i] = __builtin_fmaf(ln[i], y[i], x[i]);
    rb9(x);
    st16(X, (unsigned)lane * 16u, sb1 + (P_X3 + (unsigned)(r >> 3)) * PAIR + (unsigned)(r & 7) * 1024u, pk8(x));
    const v4u x9 = gat8(x[8]);
    if ((lane & 7) == 0) st16(X, (unsigned)xp * 16u, sb1 + (P_H + 4u) * PAIR + 4096u + (unsigned)r * 128u, x9);
    ln9(x, ln);
    st16(X, hoff(r, lane), sb1, pk8(ln));
    const v4u h9 = gat8(ln[8]);
    if ((lane & 7) == 0) st16(X, hoff(r, 64 + xp), sb1, h9);
  } else {
    const rsrc_t O = mkr(p.out + tok * DM, DM * 2u);
    __builtin_amdgcn_raw_buffer_store_b128(pk8(x), O, (unsigned)lane * 16u, 0u, OAUX);
    __builtin_amdgcn_raw_buffer_store_b128(gat8(x[8]), O, (lane & 7) == 0 ? 1024u + (unsigned)xp * 16u : 0xF0000000u, 0u, OAUX);
  }
}

__device__ __forceinline__ void row_role(char* smem) {
  TOK_COMMON();
  const int rw = 4 * xq + (i - 20), r = 2 * rw + (w >> 1);
  const unsigned long long sm = die_mask(X, SG0, L, lane);
  const unsigned tm = (unsigned)die_mask(X, TP0, L, lane), dm = (unsigned)die_mask(X, 0u, L, lane);
  __attribute__((address_space(3))) unsigned* cnt = (__attribute__((address_space(3))) unsigned*)smem;
  BAR();
  if (tid < 64) cnt[tid] = 0u;
  BAR();
  for (int s = w & 1; s < NSEG; s += 2) {
    int ln_ = lane; asm volatile("" : "+v"(ln_));
    const int m = ln_ & 15, g = ln_ >> 4;
    const unsigned l16 = (unsigned)ln_ * 16u, xo = (unsigned)(8 * (4 * (g & 1) + 2 * (g >> 1) + (m >> 3)) + (m & 7)) * 2u;
    const unsigned sb = SEGB(sm, s), sb1 = sb ^ (L ? 0u : 8192u);
    if (s + ARMK < NSEG) {
      const unsigned sv = opq(SENT), sl = SLOT_AT((s + ARMK) % SRING) + ((dm >> ((s + ARMK) % SRING / 3)) & 1u) * 8192u;
      for (int j = r; j < 84; j += 32) {
#pragma unroll
        for (int k = 0; k < 4; ++k) st16(X, l16 + (unsigned)k * 1024u, sl + (unsigned)(j >> 1) * PAIR + (unsigned)(j & 1) * 4096u, (v4u){sv, sv, sv, sv});
      }
    }
    wait_all<PSL>(X, 0u, sb + P_FB * PAIR + 4096u + (unsigned)(L ? rw : r >> 3) * 128u);
    const v4u px = ld16(X, l16, sb + (P_X3 + (unsigned)(r >> 3)) * PAIR + (unsigned)(r & 7) * 1024u);
    const unsigned px9 = __builtin_amdgcn_raw_buffer_load_b16(X, xo, sb + (P_H + 4u) * PAIR + 4096u + (unsigned)r * 128u, 16);
    const rsrc_t YX = mkr(p.yT + (2 * s + (r >> 4)) * 9216, L ? 0u : 18432u);
    const v2u ya = ld8(YX, (unsigned)(ln_ * 256 + ((r ^ m) & 15) * 8), 0u), yb = ld8(YX, (unsigned)(ln_ * 256 + 128 + ((r ^ m) & 15) * 8), 0u);
    const v4u py = {ya.x, ya.y, yb.x, yb.y};
    const unsigned py9 = __builtin_amdgcn_raw_buffer_load_b16(YX, 16384u + (xo >> 3) * 128u + (((unsigned)r ^ (xo >> 4)) & 15u) * 8u + (xo & 6u), 0u, 16);
    wait_all<PSP>(X, 0u, sb + P_FB * PAIR + (unsigned)r * 128u);
    __builtin_amdgcn_s_sleep(RONE);
    wait_all<PSP>(X, (unsigned)(ln_ & 31) * 128u, sb + P_FB * PAIR);
    const unsigned j = (unsigned)s & 15u, to = TP0 * XBLK + (j << 20) + ((tm >> (j >> 1)) & 1u) * 8192u + (unsigned)r * 2u * PAIR;
    const unsigned t9 = (TP0 + 8u) * XBLK + ((tm >> 8) & 1u) * 8192u + (j << 17) + (unsigned)(((r >> 2) & 1) * 512 + (4 * (r >> 3) + (r & 3)) * 32);
    const unsigned v9 = (unsigned)(g * (int)PAIR + (m >> 1) * 1024 + (m & 1) * 16);
    v4u v[18];
#pragma unroll
    for (int k = 0; k < 2; ++k) v[k] = ld16(X, v9, t9 + (unsigned)k * 4u * PAIR);
#pragma unroll
    for (int k = 0; k < 16; ++k) v[2 + k] = ld16(X, l16 + (unsigned)(k & 3) * 1024u, to + (unsigned)(k >> 3) * PAIR + (unsigned)(k & 4) * 1024u);
    SB();
    tok_row(p, X, sb1, L, s, r, ln_, v, px, py, px9, py9);
    VM0();
    unsigned o1 = 0u;
    if (ln_ == 0) o1 = __hip_atomic_fetch_add(cnt + s, 1u, __ATOMIC_RELAXED, __HIP_MEMORY_SCOPE_WORKGROUP);
    if (__builtin_amdgcn_readfirstlane(o1) == 1u && ln_ < 2 - L) st4(X, ln_ ? sb1 : sb + 2048u, P_FB * PAIR + 4096u + (unsigned)rw * 128u, opq(1u));
  }
}

__device__ __forceinline__ void rows_task(const MP& p, const rsrc_t X, unsigned long long sm, u16* ys, int grp, int ng) {
  const int tid = threadIdx.x, lane = tid & 63, w = __builtin_amdgcn_readfirstlane(tid >> 6);
  const int m = lane & 15, g = lane >> 4;
  const int xp = 4 * (g & 1) + 2 * (g >> 1) + (m >> 3), c9 = 512 + 8 * xp + (m & 7);
  const unsigned sb = SEGB(sm, grp >> 2);
  v4u yv[5], xa[4];
  unsigned x9[4];
#pragma unroll
  for (int i = 0; i < 5; ++i) yv[i] = ld16(mkr(p.yT + (grp >> 1) * 9216, 18432u), (unsigned)(tid + 256 * i) * 16u, 0u);
#pragma unroll
  for (int rr = 0; rr < 4; ++rr) {
    const long t = grp * 8 + w + 4 * (rr & (2 * ng - 1));
    xa[rr] = *(const v4u*)&p.xin[t * DM + 8 * lane]; x9[rr] = p.xin[t * DM + c9];
  }
#pragma unroll
  for (int i = 0; i < 5; ++i) ((v4u*)ys)[tid + 256 * i] = yv[i];
  BAR();
  const unsigned o9 = (lane & 7) == 0 ? 0u : 0xF0000000u;
#pragma unroll 1
  for (int k = 0; k < ng; ++k) {
    float x[2][9], y[2][9], ln[2][9];
    const int t0 = (grp + k) * 8 + w;
#pragma unroll
    for (int rr = 0; rr < 2; ++rr) {
      const int j = (t0 + 4 * rr) & 15;
      const v2u ya = *(const v2u*)&ys[lane * 128 + (j ^ m) * 4], yb = *(const v2u*)&ys[lane * 128 + 64 + (j ^ m) * 4];
      up8(xa[rr], x[rr]); x[rr][8] = lo2f(x9[rr]); up8((v4u){ya.x, ya.y, yb.x, yb.y}, y[rr]); y[rr][8] = bf2f(ys[(c9 >> 2) * 64 + (j ^ xp) * 4 + (m & 3)]);
      xa[rr] = xa[2 + rr]; x9[rr] = x9[2 + rr];
    }
    lnR<2>(x, ln);
#pragma unroll
    for (int rr = 0; rr < 2; ++rr) {
#pragma unroll
      for (int i = 0; i < 9; ++i) x[rr][i] = __builtin_fmaf(ln[rr][i], y[rr][i], x[rr][i]);
      rb9(x[rr]);
      const int r = (t0 + 4 * rr) & 31;
      st16(X, (unsigned)lane * 16u, sb + (P_X3 + (unsigned)(r >> 3)) * PAIR + (unsigned)(r & 7) * 1024u, pk8(x[rr]));
      st16(X, (unsigned)xp * 16u + o9, sb + (P_H + 4u) * PAIR + 4096u + (unsigned)r * 128u, gat8(x[rr][8]));
    }
    lnR<2>(x, ln);
#pragma unroll
    for (int rr = 0; rr < 2; ++rr) {
      const int r = (t0 + 4 * rr) & 31;
      st16(X, hoff(r, lane), sb, pk8(ln[rr]));
      st16(X, hoff(r, 64 + xp) + o9, sb, gat8(ln[rr][8]));
    }
  }
  VM0();
  BAR();
  if (tid < ng) st4(X, (unsigned)tid * 128u, sb + P_FB * PAIR + 4096u + (unsigned)(grp & 3) * 128u, opq(1u));
}

__device__ __forceinline__ void arrive(const MP& p, int k, int grp) {
  VM0();
  BAR();
  if (threadIdx.x < 64) {
    unsigned old = 0u;
    if (threadIdx.x == 0) old = __hip_atomic_fetch_add(p.xb + MF_OFF / 4u + (k * 16 + grp) * 32, 1u, __ATOMIC_RELAXED, __HIP_MEMORY_SCOPE_AGENT);
    if (__builtin_amdgcn_readfirstlane(old) == 15u && threadIdx.x < 8) st4(mkr(p.xb, XB_BYTES), threadIdx.x * 128u, RDY_OFF(k) + (unsigned)grp * 1024u, opq(1u));
  }
}
__device__ __forceinline__ void wait_ready(const MP& p, int k) {
  if (threadIdx.x < 64) wait_all<PSL>(mkr(p.xb, XB_BYTES), (threadIdx.x % 9u) * 1024u, RDY_OFF(k) + (blockIdx.x & 7u) * 128u);
  BAR();
}
__device__ __forceinline__ void start_work(char* smem, int wk) {
  const MP p = getp();
  const int w = __builtin_amdgcn_readfirstlane(threadIdx.x >> 6);
#define NARM (ARMK - 3u)
  unsigned pf[PFM + 1];
  conv_task(p.kvT, p.ssm_k, p.ssm_D, p.yT, (u16*)(smem + w * 32768), 4 * wk + w, [&]() {
#pragma unroll
    for (int i = 0; i < PFM; ++i) {
      const int z = (i - 1) * 36864 + wk * 256 + threadIdx.x, a = z % 3;
      pf[i] = *(const unsigned*)(i == 0 ? p.Wo + (long)(z + 36864) * 64 : (a == 0 ? p.Wq : (a == 1 ? p.Wk : p.Wv)) + (long)(36863 - z / 3) * 64);
    }
  }, [&]() {
    const unsigned sv = opq(SENT);
    constexpr unsigned S16 = SLOT / 16u;
#pragma unroll
    for (unsigned k = 0; k < (NARM * S16 + 32767u) / 32768u; ++k) {
      const unsigned i = k * 32768u + wk * 256 + threadIdx.x;
      st16(mkr(p.xb, XB_BYTES), i < NARM * S16 && wk < 128 ? SLOT_AT(3u + i / S16) + (i % S16) * 16u : 0xF0000000u, 0u, (v4u){sv, sv, sv, sv});
    }
  });
#pragma unroll
  for (int i = 0; i < PFM; ++i) asm volatile("" :: "v"(pf[i]));
  arrive(p, 0, wk >> 4);
}
__device__ __forceinline__ void token_start(char* smem) {
  TOK_COMMON();
  (void)w;
  const int k = 4 * i + xq;
  if (L || (i >= 12 && i < 16)) start_work(smem, L ? 32 + k : 80 + k);
  if (i >= (L ? 12 : 20)) {
    const unsigned long long sm = die_mask(X, SG0, 0, lane);
    int g = L ? 111 - k : k - 96;
    while (g < 120) {
      if (g >= 0 && g < 64) {
        wait_ready(p, 0);
      }
      rows_task(p, X, sm, (u16*)smem, g < 0 ? g + 16 : 16 + 2 * g, g < 0 ? 1 : 2);
      g += g < 0 ? 16 : (L ? 48 : 120);
    }
  }
}
__device__ __forceinline__ void token_wg(char* smem) {
  if ((unsigned)(size_t)(__attribute__((address_space(3))) char*)smem != 0u) return;
  token_start(smem);
  const int i = ((int)blockIdx.x - NCHW) >> 3;
  if (i < 12) qkv_role();
  else if (i < 20) ap_role();
  else row_role(smem);
}

__global__ __launch_bounds__(256, 1) void k_main(MP) {
  extern __shared__ __attribute__((aligned(16))) char smem[];
  asm volatile("LKBEG:");
  if ((int)blockIdx.x < NCHW) chain_wg(smem);
  else token_wg(smem);
}

extern "C" void kernel_launch(void* const* d_in, const int* in_sizes, int n_in, void* d_out, int out_size, void* d_ws,
                              size_t ws_size, hipStream_t stream) {
  const u16* A_q = (const u16*)d_in[0];
  const u16* A_k = (const u16*)d_in[1];
  const u16* A_v = (const u16*)d_in[2];
  const u16* A_shift = (const u16*)d_in[3];
  const u16* A_ssmk = (const u16*)d_in[4];
  const u16* A_ssmD = (const u16*)d_in[5];
  const u16* B_mem0 = (const u16*)d_in[7];
  const u16* B_Wq = (const u16*)d_in[8];
  const u16* B_Wk = (const u16*)d_in[9];
  const u16* B_Wv = (const u16*)d_in[10];
  const u16* B_Wo = (const u16*)d_in[11];

  char* ws = (char*)d_ws;
  size_t off = (size_t)(-(uintptr_t)ws) & (XBLK - 1u);
  auto alloc = [&](size_t bytes) { char* q = ws + off; off += (bytes + 4095) & ~(size_t)4095; return q; };
  unsigned* xb = (unsigned*)alloc((size_t)XB_BYTES);
  u16* kvT = (u16*)alloc((size_t)DM * S_LEN * 2);
  u16* yT = (u16*)alloc((size_t)DM * S_LEN * 2);

  PreP pp;
  pp.kk = A_k; pp.vv = A_v; pp.shift_k = A_shift; pp.ssm_k = A_ssmk; pp.ssm_D = A_ssmD; pp.yT = yT; pp.kvT = kvT;
  pp.Wq = B_Wq; pp.Wk = B_Wk; pp.Wv = B_Wv; pp.Wo = B_Wo; pp.xb = xb; pp.mem0 = B_mem0;
  k_pre<<<240 + PRE_FILL + 256 + 288, 256, 0, stream>>>(pp);
  MP mp;
  mp.out = (u16*)d_out; mp.xb = xb;
  mp.xin = A_q; mp.yT = yT; mp.kvT = kvT; mp.ssm_k = A_ssmk; mp.ssm_D = A_ssmD;
  mp.Wq = B_Wq; mp.Wk = B_Wk; mp.Wv = B_Wv; mp.Wo = B_Wo;
  const int smem_bytes = 160 * 1024;
  (void)hipFuncSetAttribute((const void*)k_main, hipFuncAttributeMaxDynamicSharedMemorySize, smem_bytes);
  k_main<<<NCHW + NTOKW, 256, smem_bytes, stream>>>(mp);
}
```

```cpp
#define KB 146
#include <hip/hip_runtime.h>
#include <stdint.h>

typedef unsigned short u16;
using bf16x8 = __attribute__((ext_vector_type(8))) short;
using f32x4 = __attribute__((ext_vector_type(4))) float;
using f32x2 = __attribute__((ext_vector_type(2))) float;
using bfx8 = __attribute__((ext_vector_type(8))) __bf16;
using bfx2 = __attribute__((ext_vector_type(2))) __bf16;
typedef unsigned v4u __attribute__((ext_vector_type(4)));
typedef unsigned v2u __attribute__((ext_vector_type(2)));
typedef short v4s __attribute__((ext_vector_type(4)));

#define S_LEN 2048
#define DM 576
#define NH 32
#define DH 128
#define HD (NH * DH)
#define SEG 32
#define NSEG 64

__device__ __forceinline__ float bf2f(u16 x) { return __uint_as_float(((unsigned)x) << 16); }
__device__ __forceinline__ unsigned pk2(float a, float b) { f32x2 v = {a, b}; bfx2 r = __builtin_convertvector(v, bfx2); return __builtin_bit_cast(unsigned, r); }
__device__ __forceinline__ float lo2f(unsigned u) { return __uint_as_float(u << 16); }
__device__ __forceinline__ float hi2f(unsigned u) { return __uint_as_float(u & 0xFFFF0000u); }

#define NFC 128
#define XBLK 2097152u
#define PAIR 16384u
#define SLOT 688128u
#define AGO 589824u
#define AGT 655360u
#define SG0 22
#define TP0 86
#define NXB 95
#define XB_BYTES (NXB * XBLK)
#define SLOT_AT(i) ((unsigned)((i) / 3) * XBLK + (unsigned)((i) % 3) * SLOT)
#define HT_OFF SLOT_AT(65)
#define CTAB_OFF (HT_OFF + 16384u)
#define MF_OFF (HT_OFF + 20480u)
#define TMO_OFF (MF_OFF + 15u * 128u)
#define RDY_OFF(k) (MF_OFF + 4096u + (k) * 10240u)
#define MF_BYTES 16384u
#define TRC_OFF SLOT_AT(64)
#define CAL_OFF (XBLK - 16384u)
#define P_KV 96u
#define P_H 112u
#define P_X3 117u
#define P_FA 121u
#define P_FB 122u
#define ARMK 8
#define NCAL 8
#define CALT 560u
#define CALM 1
#define SRING 12
#define TRING 64
#define KPM 0
__device__ __forceinline__ unsigned kv2(const unsigned (&u)[4], unsigned v, v2u w0, v2u w1) {
  const float a[4] = {hi2f(w0.y), lo2f(w0.y), hi2f(w0.x), lo2f(w0.x)}, b[4] = {hi2f(w1.y), lo2f(w1.y), hi2f(w1.x), lo2f(w1.x)};
  unsigned r = pk2(lo2f(u[0]) * a[0], hi2f(u[0]) * b[0]);
#pragma unroll
  for (int i = 1; i < 4; ++i) { const unsigned q = pk2(lo2f(u[i]) * a[i], hi2f(u[i]) * b[i]); r = pk2(lo2f(r) + lo2f(q), hi2f(r) + hi2f(q)); }
  return pk2(lo2f(r) * lo2f(v), hi2f(r) * hi2f(v));
}
struct PreP {
  const u16* kk; const u16* vv; const u16* shift_k; const u16* ssm_k; const u16* ssm_D; u16* yT; u16* kvT;
  const u16* Wq; const u16* Wk; const u16* Wv; const u16* Wo; unsigned* xb; const u16* mem0;
};
#define PRE_FILL 112
#define PFN 2
__global__ __launch_bounds__(256, 4) void k_pre(PreP p) {
  __shared__ __attribute__((aligned(16))) float sh[64 * 36];
  const int tid = threadIdx.x, b0 = blockIdx.x, b = b0 < 240 ? b0 : (b0 < 528 ? b0 + PRE_FILL + 256 : b0 - 288);
  const __amdgpu_buffer_rsrc_t rx = __builtin_amdgcn_make_buffer_rsrc((void*)p.xb, 0, XB_BYTES, 0x00020000);
  const v4u ones = {0xFFFFFFFFu, 0xFFFFFFFFu, 0xFFFFFFFFu, 0xFFFFFFFFu}, zero = {0, 0, 0, 0};
  if (b < 96) {
    if (tid >= 64 || (KPM & 16)) return;
    const unsigned cb = (unsigned)b * XBLK + CAL_OFF;
    __builtin_amdgcn_raw_buffer_store_b128(zero, rx, (unsigned)((tid >> 3) & 1) * 8192u + (unsigned)(tid >> 4) * 256u + (unsigned)(tid & 7) * 16u, cb, 16);
    asm volatile("s_waitcnt vmcnt(0)" ::: "memory");
    unsigned mn[2] = {~0u, ~0u};
    for (int i = 0; i < NCAL; ++i) {
      const unsigned long long c0 = __builtin_amdgcn_s_memtime();
      asm volatile("s_waitcnt lgkmcnt(0)" ::: "memory");
      const unsigned v = __builtin_amdgcn_raw_buffer_load_b32(rx, 0, cb + (unsigned)(i & 1) * 8192u + (unsigned)(i >> 1) * 256u, 16);
      asm volatile("s_waitcnt vmcnt(0)" :: "v"(v) : "memory");
      const unsigned d = (unsigned)(__builtin_amdgcn_s_memtime() - c0);
      if (i & 1) mn[1] = d < mn[1] ? d : mn[1]; else mn[0] = d < mn[0] ? d : mn[0];
      const unsigned lo = mn[0] < mn[1] ? mn[0] : mn[1], hi = mn[0] < mn[1] ? mn[1] : mn[0];
      if (i >= CALM && lo < CALT && hi > lo + 100u) break;
    }
    unsigned xcc; asm volatile("s_getreg_b32 %0, hwreg(HW_REG_XCC_ID)" : "=s"(xcc));
    const unsigned res = mn[1] < mn[0] ? 1u : 0u;
    if (tid == 0) __builtin_amdgcn_raw_buffer_store_b32(res ^ ((xcc >> 2) & 1u), rx, 0, CTAB_OFF + (unsigned)b * 4u, 16);
  } else if (b < 240) {
    if (KPM & 1) return;
    const __amdgpu_buffer_rsrc_t rk = __builtin_amdgcn_make_buffer_rsrc((void*)p.kk, 0, S_LEN * DM * 2, 0x00020000);
    float (*kx)[132] = (float (*)[132])sh;
    float (*cf)[140] = (float (*)[140])(sh + 4 * 132);
    const int d0 = (b - 96) * 4, q = tid & 3, dd = (tid >> 2) & 3, t0 = (tid >> 4) * 8;
    const float Dv = bf2f(p.ssm_D[d0 + dd]);
    {
      const int s = tid >> 1, d = d0 + (tid & 1) * 2;
      unsigned u[4];
#pragma unroll
      for (int a = 0; a < 4; ++a) u[a] = __builtin_amdgcn_raw_buffer_load_b32(rk, (unsigned)((s - 3 + a) * DM + d) * 2u, 0, 0);
      const unsigned vr = *(const unsigned*)&p.vv[s * DM + d], c0 = *(const unsigned*)&p.ssm_k[(long)(d0 + (tid >> 6)) * S_LEN + (tid & 63) * 2];
      const v4u w = *(const v4u*)&p.shift_k[d * 4];
      const unsigned r = kv2(u, vr, (v2u){w.x, w.y}, (v2u){w.z, w.w});
      kx[(tid & 1) * 2][s] = lo2f(r); kx[(tid & 1) * 2 + 1][s] = hi2f(r);
      *(f32x2*)&cf[tid >> 6][8 + (tid & 63) * 2] = (f32x2){lo2f(c0), hi2f(c0)};
      if (tid < 32) cf[tid >> 3][tid & 7] = 0.f;
    }
    __syncthreads();
    float y[8];
#pragma unroll
    for (int i = 0; i < 8; ++i) y[i] = 0.f;
#pragma unroll 1
    for (int j = 8 * q; j <= t0; j += 32) {
      f32x4 kk[2], cw[4];
#pragma unroll
      for (int x = 0; x < 2; ++x) kk[x] = *(const f32x4*)&kx[dd][j + 4 * x];
#pragma unroll
      for (int x = 0; x < 4; ++x) cw[x] = *(const f32x4*)&cf[dd][t0 - j + 4 * x];
#pragma unroll
      for (int jj = 0; jj < 8; ++jj)
#pragma unroll
        for (int i = 0; i < 8; ++i) y[i] = __builtin_fmaf(cw[(8 + i - jj) >> 2][(8 + i - jj) & 3], kk[jj >> 2][jj & 3], y[i]);
    }
#pragma unroll
    for (int i = 0; i < 8; ++i) {
      y[i] += __uint_as_float((unsigned)__builtin_amdgcn_mov_dpp((int)__float_as_uint(y[i]), 0xB1, 0xf, 0xf, true));
      y[i] += __uint_as_float((unsigned)__builtin_amdgcn_mov_dpp((int)__float_as_uint(y[i]), 0x4E, 0xf, 0xf, true));
    }
    const float ya = q == 0 ? y[0] : (q == 1 ? y[2] : (q == 2 ? y[4] : y[6])), yb = q == 0 ? y[1] : (q == 1 ? y[3] : (q == 2 ? y[5] : y[7]));
    const int t = t0 + 2 * q, f = (d0 >> 3) & 15;
    const unsigned r0 = pk2(ya, yb), r1 = pk2(kx[dd][t] * Dv, kx[dd][t + 1] * Dv), o = pk2(lo2f(r0) + lo2f(r1), hi2f(r0) + hi2f(r1));
    u16* dst = p.yT + (t >> 4) * 9216 + (d0 >> 2) * 64 + dd;
    dst[((t & 15) ^ f) << 2] = (u16)(o & 0xFFFFu); dst[(((t + 1) & 15) ^ f) << 2] = (u16)(o >> 16);
  } else if (b < 240 + PRE_FILL) {
    if (KPM & 32) return;
    const unsigned i0 = (unsigned)(b - 240) * 256u + tid;
    if (i0 < (SLOT - AGO) / 16u) {
      const unsigned o = AGO + i0 * 16u, s = o < AGT ? (i0 >> 11) * 4u + ((o >> 10) & 3u) : 8u, r = o & 1023u;
      const unsigned gm = s < 8u ? r >> 8 : (r >> 6) & 3u, m3 = (r >> 4) & 3u, ga = s < 8u ? (r >> 6) & 3u : r >> 8, row = 4u * (gm & 1u) + 2u * (gm >> 1) + (m3 & 1u), half = m3 >> 1;
      const unsigned* src = (const unsigned*)(p.mem0 + row * DM + 64u * s + 16u * ga + 2u * half);
      __builtin_amdgcn_raw_buffer_store_b128((v4u){src[0], src[2], src[4], src[6]}, rx, o, 0, 16);
    }
    for (unsigned i = i0; i < 131072u + MF_BYTES / 16u; i += PRE_FILL * 256)
      __builtin_amdgcn_raw_buffer_store_b128(zero, rx, i < 131072u ? (SG0 + (i >> 11)) * XBLK + P_FA * PAIR + (i & 2047u) * 16u : MF_OFF + (i & 2047u) * 16u, 0, 16);
    for (unsigned i = i0; i < 2u * SLOT / 16u; i += PRE_FILL * 256) __builtin_amdgcn_raw_buffer_store_b128(ones, rx, SLOT + i * 16u, 0, 16);
  } else if (b >= 240 + PRE_FILL + 256) {
    if (KPM & 2) return;
    const __amdgpu_buffer_rsrc_t rk = __builtin_amdgcn_make_buffer_rsrc((void*)p.kk, 0, S_LEN * DM * 2, 0x00020000);
    u16 (*tile)[72] = (u16 (*)[72])sh;
    const int bid = b - 240 - PRE_FILL - 256, d0 = (bid % 9) * 64, s0 = (bid / 9) * 64;
    const int sr = (tid >> 3) * 2, d = d0 + (tid & 7) * 8;
    v4u kr[5], vr[2], w[4];
#pragma unroll
    for (int a = 0; a < 5; ++a) kr[a] = __builtin_amdgcn_raw_buffer_load_b128(rk, (unsigned)((s0 + sr - 3 + a) * DM + d) * 2u, 0, 0);
#pragma unroll
    for (int a = 0; a < 2; ++a) vr[a] = *(const v4u*)&p.vv[(long)(s0 + sr + a) * DM + d];
#pragma unroll
    for (int a = 0; a < 4; ++a) w[a] = *(const v4u*)&p.shift_k[(d + 2 * a) * 4];
    __builtin_amdgcn_sched_barrier(0);
#pragma unroll
    for (int it = 0; it < 2; ++it) {
      v4u o;
#pragma unroll
      for (int j = 0; j < 4; ++j) { const unsigned u[4] = {kr[it][j], kr[it + 1][j], kr[it + 2][j], kr[it + 3][j]}; o[j] = kv2(u, vr[it][j], (v2u){w[j].x, w[j].y}, (v2u){w[j].z, w[j].w}); }
      *(v4u*)&tile[sr + it][(tid & 7) * 8] = o;
    }
    __syncthreads();
    const int lane = tid & 63, wv = tid >> 6, g = lane >> 4, r = (lane >> 2) & 3, c = lane & 3;
    v2u o[4];
#pragma unroll
    for (int i = 0; i < 4; ++i) o[i] = __builtin_bit_cast(v2u, __builtin_amdgcn_ds_read_tr16_b64_v4i16((__attribute__((address_space(3))) v4s*)&tile[16 * g + 4 * i + r][16 * wv + 4 * c]));
    u16* dst = p.kvT + (long)(d0 + 16 * wv + (lane & 15)) * S_LEN + s0 + 16 * g;
    *(v4u*)dst = (v4u){o[0].x, o[0].y, o[1].x, o[1].y};
    *(v4u*)(dst + 8) = (v4u){o[2].x, o[2].y, o[3].x, o[3].y};
  } else {
    if (KPM & 4) return;
    const int x = b & 7, k = (b - 240 - PRE_FILL) >> 3;
    unsigned long long pc; unsigned l0, l1, l2, l3; int rel;
    asm volatile("s_getpc_b64 %0\nLPRE:\n\ts_mov_b32 %1, LKBEG-LPRE\n\ts_mov_b32 %2, LCHEND-LKBEG\n\ts_mov_b32 %3, LQVEND-LKBEG\n\ts_mov_b32 %4, LQKEND-LKBEG\n\ts_mov_b32 %5, LAPEND-LKBEG"
                 : "=s"(pc), "=s"(rel), "=s"(l0), "=s"(l1), "=s"(l2), "=s"(l3));
    l0 = l0 > l1 ? l0 : l1; l2 = l2 > l3 ? l2 : l3; l0 = l0 > l2 ? l0 : l2;
    const unsigned o = (unsigned)(k * 64 + (tid & 63)) * 64u;
    unsigned a[PFN + 2];
    a[PFN] = *(const __attribute__((address_space(1))) unsigned*)(pc + rel + (o < l0 ? o : 0u));
    a[PFN + 1] = __builtin_amdgcn_raw_buffer_load_b32(rx, k == 31 ? tid * XBLK + CAL_OFF + 8064u : 0xF0000000u, 0, 16);
#pragma unroll
    for (int i = 0; i < PFN; ++i) {
      const int q = (i * 32 + k) * 256 + tid, r2 = q >> 5, gr = r2 >> 5, row = (gr / 3) * 64 + (x >> 2) * 32 + (r2 & 31), off = (q & 31) * 32, ty = gr % 3;
      a[i] = *(const unsigned*)(gr < 27 ? (ty == 0 ? p.Wq : (ty == 1 ? p.Wk : p.Wv)) + (long)row * HD + (x & 3) * 1024 + off
                                        : p.Wo + ((long)(x & 3) * DM + (gr < 36 ? gr - 27 : 0) * 64 + (x >> 2) * 32 + (r2 & 31)) * 1024 + off);
    }
#pragma unroll
    for (int i = 0; i < PFN + 2; ++i) asm volatile("" :: "v"(a[i]));
  }
}

#define CREV_STRIDE 2208
#define SP(k)
#define LWAIT() asm volatile("s_waitcnt lgkmcnt(0)" ::: "memory")
template <class E, class F>
__device__ __forceinline__ void conv_task(const u16* __restrict__ kvT, const u16* __restrict__ ssm_k, const u16* __restrict__ ssm_D, u16* yT, u16* shb, int d, E early, F fill) {
  const int lane = threadIdx.x & 63;
  u16* crev = shb + 64;
  u16* kvs = crev + 4 * CREV_STRIDE;
  const int X0 = 2048 + 63;
  SP(0);
  v4u cv[4], kq[4];
#pragma unroll
  for (int j = 0; j < 4; ++j) { cv[j] = *(const v4u*)&ssm_k[(long)d * S_LEN + lane * 32 + 8 * j]; kq[j] = *(const v4u*)&kvT[(long)d * S_LEN + (j * 64 + lane) * 8]; }
  const float Dv = bf2f(ssm_D[d]);
  __builtin_amdgcn_sched_barrier(0);
  early();
  __builtin_amdgcn_sched_barrier(0);
  const v4u z4 = {0u, 0u, 0u, 0u};
  *(v4u*)&kvs[(lane >> 2) * 40 + (lane & 3) * 8] = z4;
  if (lane < 32) *(v4u*)&crev[(lane >> 3) * CREV_STRIDE + (lane & 7) * 8] = z4;
  if (lane < 36) *(v4u*)&crev[(lane / 9) * CREV_STRIDE + 2112 + (lane % 9) * 8] = z4;
  {
    unsigned r[18];
#pragma unroll
    for (int k = 0; k < 16; ++k) { const unsigned u = cv[3 - (k >> 2)][3 - (k & 3)]; r[k] = __builtin_amdgcn_alignbyte(u, u, 2); }
#pragma unroll
    for (int k = 0; k < 2; ++k) r[16 + k] = (unsigned)__builtin_amdgcn_update_dpp(0, (int)r[k], 0x138, 0xf, 0xf, true);
    LWAIT();
#pragma unroll
    for (int rho = 0; rho < 4; ++rho)
#pragma unroll
      for (int j = 0; j < 4; ++j) {
        v4u o;
#pragma unroll
        for (int k = 0; k < 4; ++k) { const int q = 4 * j + k + (rho >> 1); o[k] = (rho & 1) ? __builtin_amdgcn_alignbyte(r[q + 1], r[q], 2) : r[q]; }
        *(v4u*)&crev[rho * CREV_STRIDE + 2080 - 32 * lane + 8 * j] = o;
      }
    if (lane == 63) {
      *(v2u*)&crev[1 * CREV_STRIDE + 60] = (v2u){0u, r[0] << 16};
      *(v2u*)&crev[2 * CREV_STRIDE + 60] = (v2u){0u, r[0]};
      *(v2u*)&crev[3 * CREV_STRIDE + 60] = (v2u){r[0] << 16, __builtin_amdgcn_alignbyte(r[1], r[0], 2)};
    }
#pragma unroll
    for (int j = 0; j < 4; ++j) *(v4u*)&kvs[(16 + j * 16 + (lane >> 2)) * 40 + (lane & 3) * 8] = kq[j];
  }
  fill();
  LWAIT();
  SP(1);
  const int n = lane & 15, g = lane >> 4;
  const int rho = (X0 - n) & 3;
  f32x4 acc[4][2];
#pragma unroll
  for (int q = 0; q < 8; ++q) acc[q >> 1][q & 1] = (f32x4){0.f, 0.f, 0.f, 0.f};
  {
    typedef __attribute__((address_space(3))) const v2u* l2p;
    typedef __attribute__((address_space(3))) const bf16x8* l4p;
    unsigned a0 = (unsigned)(size_t)(__attribute__((address_space(3))) u16*)&crev[rho * CREV_STRIDE + (X0 - (n - 8 * g) - rho)] - 3232u, a1 = a0 + 8u;
    unsigned b0 = (unsigned)(size_t)(__attribute__((address_space(3))) u16*)&kvs[(14 + n) * 40 + 8 * g];
    asm volatile("" : "+v"(a1));
#define AFR(m, f) ({ const unsigned o_ = 3232u - 64u * (m) - 1024u * ((f) >> 1) - 32u * ((f) & 1); const v2u lo_ = *(l2p)(size_t)(a0 + o_), hi_ = *(l2p)(size_t)(a1 + o_); \
                     __builtin_bit_cast(bf16x8, (v4u){lo_.x, lo_.y, hi_.x, hi_.y}); })
#define BFR(m, j) (*(l4p)(size_t)(b0 + 80u * (2 - (m)) + 1280u * (j)))
    bf16x8 af[2][8], bf[2][4];
#pragma unroll
    for (int f = 0; f < 8; ++f) { af[0][f] = AFR(0, f); af[1][f] = af[0][f]; __builtin_amdgcn_sched_barrier(0); }
#pragma unroll
    for (int j = 0; j < 4; ++j) { bf[0][j] = BFR(0, j); bf[1][j] = bf[0][j]; }
#pragma unroll 1
    for (int mo = 0; mo < 8; ++mo) {
#pragma unroll
      for (int m = 0; m < 2; ++m) {
        int k = 0;
#pragma unroll
        for (int a = 0; a < 4; ++a)
#pragma unroll
          for (int ct = a; ct < 4; ++ct)
#pragma unroll
            for (int ti = 0; ti < 2; ++ti) {
              acc[ct][ti] = __builtin_amdgcn_mfma_f32_16x16x32_bf16(__builtin_bit_cast(bfx8, af[m][2 * a + ti]), __builtin_bit_cast(bfx8, bf[m][ct - a]), acc[ct][ti], 0, 0, 0);
              if (k < 2) af[m ^ 1][k] = AFR(m + 1, k);
              else if (k < 6) bf[m ^ 1][k - 2] = BFR(m + 1, k - 2);
              else if (k < 12) af[m ^ 1][k - 4] = AFR(m + 1, k - 4);
              __builtin_amdgcn_sched_barrier(0);
              ++k;
            }
      }
      a0 -= 128u; a1 -= 128u; b0 -= 160u;
    }
  }
  SP(2);
  u16* yo = crev;
  LWAIT();
#pragma unroll
  for (int q = 0; q < 8; ++q) {
    const int o = (16 * (q >> 1) + n) * 40 + 16 * (q & 1) + 4 * g;
    const v2u kx = *(const v2u*)&kvs[16 * 40 + o];
    const f32x4 y = acc[q >> 1][q & 1];
    const unsigned y0 = pk2(y[0], y[1]), y1 = pk2(y[2], y[3]), r0 = pk2(lo2f(kx.x) * Dv, hi2f(kx.x) * Dv), r1 = pk2(lo2f(kx.y) * Dv, hi2f(kx.y) * Dv);
    *(v2u*)&yo[o] = (v2u){pk2(lo2f(y0) + lo2f(r0), hi2f(y0) + hi2f(r0)), pk2(lo2f(y1) + lo2f(r1), hi2f(y1) + hi2f(r1))};
  }
  LWAIT();
  __builtin_amdgcn_s_barrier();
  asm volatile("" ::: "memory");
  {
    const int tid = threadIdx.x, f = (d >> 3) & 15;
    const u16* y0 = shb - (tid >> 6) * 16384 + 64;
#pragma unroll
    for (int k = 0; k < 4; ++k) {
      const int tile = 32 * k + (tid >> 3), t = 16 * tile + 2 * ((tid & 7) ^ (f >> 1));
      unsigned u[4];
#pragma unroll
      for (int c = 0; c < 4; ++c) u[c] = *(const unsigned*)&y0[c * 16384 + (t >> 5) * 40 + (t & 31)];
      const v2u e = {__builtin_amdgcn_perm(u[1], u[0], 0x05040100u), __builtin_amdgcn_perm(u[3], u[2], 0x05040100u)}, o = {__builtin_amdgcn_perm(u[1], u[0], 0x07060302u), __builtin_amdgcn_perm(u[3], u[2], 0x07060302u)};
      if (tile >= NFC / 16) __builtin_amdgcn_raw_buffer_store_b128((f & 1) ? (v4u){o.x, o.y, e.x, e.y} : (v4u){e.x, e.y, o.x, o.y}, __builtin_amdgcn_make_buffer_rsrc((void*)yT, 0, DM * S_LEN * 2, 0x00020000),
                                                                   tile * 18432 + (tid & 7) * 16, (d >> 2) * 128, 16);
    }
  }
}

#define NCHW 64
#define NTOKW 192
#define PSL 4
#define SENT 0xFFFFFFFFu
#define SPIN_CAP 300000u
struct MP {
  const u16* Wq; const u16* Wk; const u16* Wv; const u16* Wo; u16* out; unsigned* xb;
  const u16* xin; u16* yT; u16* kvT; const u16* ssm_k; const u16* ssm_D;
};

__device__ __forceinline__ MP getp() {
  MP r;
#if defined(__HIP_DEVICE_COMPILE__)
  const __attribute__((address_space(4))) unsigned long long* k = (const __attribute__((address_space(4))) unsigned long long*)__builtin_amdgcn_kernarg_segment_ptr();
  asm volatile("" : "+s"(k));
#define GP(T, i) ((T*)(__attribute__((address_space(1))) T*)k[i])
  r.Wq = GP(const u16, 0);
  r.Wk = GP(const u16, 1);
  r.Wv = GP(const u16, 2);
  r.Wo = GP(const u16, 3);
  r.out = GP(u16, 4);
  r.xb = GP(unsigned, 5);
  r.xin = GP(const u16, 6);
  r.yT = GP(u16, 7);
  r.kvT = GP(u16, 8);
  r.ssm_k = GP(const u16, 9);
  r.ssm_D = GP(const u16, 10);
#endif
  return r;
}
typedef __amdgpu_buffer_rsrc_t rsrc_t;
__device__ __forceinline__ rsrc_t mkr(const void* p, unsigned bytes) { return __builtin_amdgcn_make_buffer_rsrc((void*)p, 0, bytes, 0x00020000); }
__device__ __forceinline__ void st16(rsrc_t r, unsigned vo, unsigned so, v4u v) { __builtin_amdgcn_raw_buffer_store_b128(v, r, vo, so, 16); }
__device__ __forceinline__ void st4(rsrc_t r, unsigned vo, unsigned so, unsigned v) { __builtin_amdgcn_raw_buffer_store_b32(v, r, vo, so, 16); }
__device__ __forceinline__ v4u ld16(rsrc_t r, unsigned vo, unsigned so) { return __builtin_amdgcn_raw_buffer_load_b128(r, vo, so, 16); }
__device__ __forceinline__ v2u ld8(rsrc_t r, unsigned vo, unsigned so) { return __builtin_amdgcn_raw_buffer_load_b64(r, vo, so, 16); }
__device__ __forceinline__ unsigned ld4(rsrc_t r, unsigned vo, unsigned so) { return __builtin_amdgcn_raw_buffer_load_b32(r, vo, so, 16); }
__device__ __forceinline__ unsigned opq(unsigned x) { asm volatile("" : "+v"(x)); return x; }

__device__ __forceinline__ unsigned long long die_mask(rsrc_t X, unsigned first, int L, int lane) { return __ballot((ld4(X, (unsigned)lane * 4u, CTAB_OFF + first * 4u) & 1u) != (unsigned)L); }
#define SEGB(m, s) ((unsigned)(SG0 + (s)) * XBLK + (unsigned)(((m) >> (s)) & 1ull) * 8192u)
__device__ __forceinline__ bool spin_fail(unsigned& spins, rsrc_t F) {
  ++spins;
  if (spins > SPIN_CAP) { unsigned one = 0x40000000u | (blockIdx.x << 8) | threadIdx.x; asm volatile("" : "+v"(one)); if (__builtin_amdgcn_readfirstlane(ld4(F, 0, TMO_OFF)) == 0u) st4(F, 0, TMO_OFF, one); return true; }
  if ((spins & 1023u) == 0u) return __builtin_amdgcn_readfirstlane(ld4(F, 0, TMO_OFF)) != 0;
  return false;
}
__device__ __forceinline__ unsigned rtc() { return (unsigned)__builtin_amdgcn_s_memrealtime(); }
#define BAR() do { asm volatile("s_waitcnt lgkmcnt(0)" ::: "memory"); __builtin_amdgcn_s_barrier(); asm volatile("" ::: "memory"); } while (0)
#define SB() __builtin_amdgcn_sched_barrier(0)
#define MF "v_mfma_f32_16x16x32_bf16 "
#define VM0() __builtin_amdgcn_s_waitcnt(0x0F70)
#define DL1 12
#define SCH_B0 48
#define DLL1 6
#define DLL2 0
#define DLF 24
#define SCH_PEN 0
#define DLX 4
#define LATE 2
#define TKL 28
#define FIN -3
#define CH_FIN CH_S(FIN)
#define DLT 16
#define DLY 20
#define CH_DLT CH_S(DLT)
#define CH_DLY CH_S(DLY)
#define ES 6
#define ED 4
#define EB 8
#define CH_ES CH_S(ES)
#define CH_EX "%[ex]"
#define DRYL 1
#define CH_DRYL CH_S(DRYL)
#define SMAP 0x1023u
#define RO1 4
#define CH_HNP
#define CH_RO1 CH_S(RO1)
#define RO3 2
#define CH_RO3 CH_S(RO3)
#define KD 300
#define CH_KD CH_S(KD)
#define CH_KB CH_S(KB)
#define RSL 2
#define CH_S2(x) #x
#define CH_S(x) CH_S2(x)
#define CH_ST "s_memtime s[62:63]\n\ts_waitcnt lgkmcnt(0)\n\ts_sub_u32 s67, s62, s67\n\t"
#define CH_BASE "s_memtime s[94:95]\n\ts_waitcnt lgkmcnt(0)\n\ts_mov_b32 s67, s94\n\t"
#define CH_DPC
#define CH_TOKCHK "s_cbranch_vccz LG3_%=\n\t"
#define CH_SEGC
#define CH_SG0 CH_S(SG0)
#define CH_ARMD "%[armd]"
#define CH_ARMS "%[arms]"
#define CH_SLOT "688128"
#define CH_SKIP "32768"
#define CH_DL1 CH_S(DL1)
#define CH_DLL1 CH_S(DLL1)
#define CH_DLL2 CH_S(DLL2)
#define CH_DLF CH_S(DLF)
#define CH_B0 CH_S(SCH_B0)
#define CH_PEN CH_S(SCH_PEN)
#define CH_RSL CH_S(RSL)
#define CH_DLX CH_S(DLX)
#define CH_LATE CH_S(LATE)
#define CH_DL1L "%[dl1l]"
#define CH_TKL CH_S(TKL)
#define CH_SCHW "656384"
#define CH_CAP "%[cap]"
#define CH_CTAB "%[ctab]"
#define CH_RING "%[ring]"
#define CH_TRM "%[trm]"
#define CH_COW
#define CH_INW "s_sub_u32 s93, s48, 10\n\ts_cmp_lt_u32 s93, 50\n\t"
#define CH_ST1
#define CH_ST2
#define FDL 6
#define CH_FDL CH_S(FDL)
#define CH_ST3
#define CH_ST4
#define CH_TRC
#define CH_TG2
#define CH_TG5
#define CH_STV
#define CH_STW
#define CH_DP0
#define CH_DP1
#define CH_DP2
#define CH_DP3
#define CH_DP4
#define CH_DP5
#define CH_DP6
#define CH_DP7
#define CH_DP8
#define CH_DP9
#define CH_DP10
#define CH_DP11
#define CH_DP12
#define CH_DP13
#define CH_DP14
#define CH_DP15
#define CH_DP16
#define CH_DP17
#define CH_OPS {64, 80, 128, 144, 1, 17, 65, 81, 129, 145, 2, 18, 82, 130, 146, 3, 67, 83, 131, 147, 4, 20, 68, 84, 132, 5, 69, 85, 133, 149, 6, 22, 70, 86, 134, 150, 7, 23, 71, 87, 135, 151, 8, 24, 88, 136, 195, 209, 210, 211, 227, 241, 242, 256, 0, 16, 19, 21, 66, 72, 148, 152, 208, 240, 192, 224, 193, 225, 194, 226, 243, 257}
#define CH_ASM \
  "s_mov_b32 s84, " CH_CAP "\n\t" \
  "s_mov_b32 s83, " CH_CAP "\n\t" \
  "s_mov_b32 s68, 0\n\t" \
  "s_mov_b32 s74, 0\n\t" \
  "s_mov_b32 s76, 0\n\t" \
  "s_mov_b32 s77, 0\n\t" \
  "s_mov_b32 s78, 0\n\t" \
  "s_mov_b32 s79, 0\n\t" \
  "s_mov_b32 s56, 0xcccccccc\n\t" \
  "s_mov_b32 s57, 0xcccccccc\n\t" \
  "s_mov_b32 s58, 0x33333333\n\t" \
  "s_mov_b32 s59, 0x33333333\n\t" \
  "s_mov_b32 s24, 0x01010101\n\t" \
  "s_mov_b32 s25, 0\n\t" \
  "s_mov_b32 s88, 0x3f803f80\n\t" \
  "s_mov_b32 s89, 0x3f803f80\n\t" \
  "s_mov_b32 s66, s65\n\t" \
  "s_mov_b32 s47, s65\n\t" \
  "s_mov_b32 s72, 0\n\t" \
  "s_mov_b32 s73, 0\n\t" \
  "s_mov_b32 s67, 0\n\t" \
  "s_mul_i32 s60, s40, 36864\n\t" \
  "s_add_u32 s60, s60, 16384\n\t" \
  "v_add_u32 v70, s60, v69\n\t" \
  "v_bfe_i32 v68, v69, 5, 1\n\t" \
  "v_and_b32 v68, 0xaaaaaaaa, v68\n\t" \
  "v_or_b32 v68, 0x44444444, v68\n\t" \
  "v_and_b32 v73, 32, v69\n\t" \
  "v_lshl_add_u32 v73, v73, 5, v69\n\t" \
  "LLOAD_%=:\n\t" \
  "v_lshrrev_b32 v74, 2, v69\n\t" \
  "s_mov_b32 s60, " CH_CTAB "\n\t" \
  "buffer_load_dword v72, v74, s[28:31], s60 offen offset:88 sc1\n\t" \
  "buffer_load_dword v74, v74, s[28:31], s60 offen sc1\n\t" \
  "v_lshrrev_b32 v71, 8, v69\n\t" \
  "v_lshlrev_b32 v1, 6, v71\n\t" \
  "v_and_b32 v2, 0xf0, v69\n\t" \
  "v_xor_b32 v1, v1, v2\n\t" \
  "v_lshl_add_u32 v71, v71, 13, v1\n\t" \
  "s_lshl_b32 s60, s40, 17\n\t" \
  "v_add_u32 v71, s60, v71\n\t" \
  "v_lshrrev_b32 v1, 7, v69\n\t" \
  "v_and_b32 v2, 0x70, v69\n\t" \
  "s_movk_i32 s60, 1152\n\t" \
  "v_mad_u32_u24 v75, v1, s60, v2\n\t" \
  "s_lshl_b32 s54, s40, 12\n\t" \
  "s_lshl_b32 s55, s40, 7\n\t" \
  "s_mul_i32 s75, s40, 36864\n\t" \
  "s_add_u32 s75, s75, 1024\n\t" \
  "s_mov_b32 s62, 0x05040100\n\t" \
  "s_mov_b32 s63, 0x07060302\n\t" \
  "s_barrier\n\t" \
  "s_xor_b32 s61, s40, 0\n\t" \
  "s_mul_i32 s61, s61, 36864\n\t" \
  "s_add_u32 s61, s61, s55\n\t" \
  "s_add_u32 m0, s54, 0\n\t" \
  "s_add_u32 s60, s61, 0\n\t" \
  "buffer_load_dwordx4 v75, %[Wo], s60 offen lds\n\t" \
  "s_add_u32 m0, s54, 1024\n\t" \
  "s_add_u32 s60, s61, 9216\n\t" \
  "buffer_load_dwordx4 v75, %[Wo], s60 offen lds\n\t" \
  "s_add_u32 m0, s54, 2048\n\t" \
  "s_add_u32 s60, s61, 18432\n\t" \
  "buffer_load_dwordx4 v75, %[Wo], s60 offen lds\n\t" \
  "s_add_u32 m0, s54, 3072\n\t" \
  "s_add_u32 s60, s61, 27648\n\t" \
  "buffer_load_dwordx4 v75, %[Wo], s60 offen lds\n\t" \
  "s_add_u32 m0, s54, 16384\n\t" \
  "s_mov_b32 s60, 0\n\t" \
  "buffer_load_dwordx4 v71, %[Wq], s60 offen lds\n\t" \
  "s_add_u32 m0, s54, 17408\n\t" \
  "s_mov_b32 s60, 32768\n\t" \
  "buffer_load_dwordx4 v71, %[Wq], s60 offen lds\n\t" \
  "s_add_u32 m0, s54, 18432\n\t" \
  "s_mov_b32 s60, 65536\n\t" \
  "buffer_load_dwordx4 v71, %[Wq], s60 offen lds\n\t" \
  "s_add_u32 m0, s54, 19456\n\t" \
  "s_mov_b32 s60, 98304\n\t" \
  "buffer_load_dwordx4 v71, %[Wq], s60 offen lds\n\t" \
  "s_add_u32 m0, s54, 53248\n\t" \
  "s_mov_b32 s60, 0\n\t" \
  "buffer_load_dwordx4 v71, %[Wk], s60 offen lds\n\t" \
  "s_add_u32 m0, s54, 54272\n\t" \
  "s_mov_b32 s60, 32768\n\t" \
  "buffer_load_dwordx4 v71, %[Wk], s60 offen lds\n\t" \
  "s_add_u32 m0, s54, 55296\n\t" \
  "s_mov_b32 s60, 65536\n\t" \
  "buffer_load_dwordx4 v71, %[Wk], s60 offen lds\n\t" \
  "s_add_u32 m0, s54, 56320\n\t" \
  "s_mov_b32 s60, 98304\n\t" \
  "buffer_load_dwordx4 v71, %[Wk], s60 offen lds\n\t" \
  "s_xor_b32 s61, s40, 0\n\t" \
  "s_mul_i32 s61, s61, 36864\n\t" \
  "s_add_u32 s61, s61, s55\n\t" \
  "s_add_u32 m0, s54, 90112\n\t" \
  "s_add_u32 s60, s61, 512\n\t" \
  "buffer_load_dwordx4 v75, %[Wo], s60 offen lds\n\t" \
  "s_add_u32 m0, s54, 91136\n\t" \
  "s_add_u32 s60, s61, 9728\n\t" \
  "buffer_load_dwordx4 v75, %[Wo], s60 offen lds\n\t" \
  "s_add_u32 m0, s54, 92160\n\t" \
  "s_add_u32 s60, s61, 18944\n\t" \
  "buffer_load_dwordx4 v75, %[Wo], s60 offen lds\n\t" \
  "s_add_u32 m0, s54, 93184\n\t" \
  "s_add_u32 s60, s61, 28160\n\t" \
  "buffer_load_dwordx4 v75, %[Wo], s60 offen lds\n\t" \
  "s_add_u32 m0, s54, 126976\n\t" \
  "s_mov_b32 s60, 0\n\t" \
  "buffer_load_dwordx4 v71, %[Wv], s60 offen lds\n\t" \
  "s_add_u32 m0, s54, 128000\n\t" \
  "s_mov_b32 s60, 32768\n\t" \
  "buffer_load_dwordx4 v71, %[Wv], s60 offen lds\n\t" \
  "s_add_u32 m0, s54, 129024\n\t" \
  "s_mov_b32 s60, 65536\n\t" \
  "buffer_load_dwordx4 v71, %[Wv], s60 offen lds\n\t" \
  "s_add_u32 m0, s54, 130048\n\t" \
  "s_mov_b32 s60, 98304\n\t" \
  "buffer_load_dwordx4 v71, %[Wv], s60 offen lds\n\t" \
  "v_and_b32 v1, 0x300, v69\n\t" \
  "v_lshlrev_b32 v65, 3, v1\n\t" \
  "v_and_b32 v1, 0xc0, v69\n\t" \
  "v_lshl_add_u32 v65, v1, 2, v65\n\t" \
  "s_lshl_b32 s60, s40, 6\n\t" \
  "v_xor_b32 v1, s60, v1\n\t" \
  "v_and_b32 v2, 48, v69\n\t" \
  "v_add3_u32 v65, v65, v1, v2\n\t" \
  "v_lshrrev_b32 v2, 1, v2\n\t" \
  "v_sub_u32 v66, v65, v2\n\t" \
  "v_bfe_u32 v0, v69, 8, 1\n\t" \
  "v_bfe_u32 v1, v69, 9, 1\n\t" \
  "v_lshlrev_b32 v0, 2, v0\n\t" \
  "v_add_u32 v0, v0, v1\n\t" \
  "v_lshlrev_b32 v0, 9, v0\n\t" \
  "v_lshrrev_b32 v1, 1, v69\n\t" \
  "v_and_b32 v1, 0x60, v1\n\t" \
  "v_and_b32 v2, 16, v69\n\t" \
  "v_bfe_u32 v3, v69, 5, 1\n\t" \
  "v_add3_u32 v64, v0, v1, v2\n\t" \
  "v_lshl_add_u32 v64, v3, 2, v64\n\t" \
  "v_add_u32 v64, s54, v64\n\t" \
  "v_lshl_add_u32 v67, v3, 3, v0\n\t" \
  "v_bfe_u32 v1, v69, 7, 1\n\t" \
  "v_lshl_add_u32 v67, v1, 2, v67\n\t" \
  "v_bfe_u32 v1, v69, 6, 1\n\t" \
  "v_lshl_add_u32 v67, v1, 4, v67\n\t" \
  "v_bfe_u32 v1, v69, 4, 1\n\t" \
  "v_lshl_add_u32 v67, v1, 1, v67\n\t" \
  "s_lshl_b32 s60, s40, 5\n\t" \
  "v_add_u32 v67, s60, v67\n\t" \
  "v_add_u32 v78, 0, v64\n\t" \
  "v_add_u32 v79, 1024, v64\n\t" \
  "s_waitcnt vmcnt(16) lgkmcnt(0)\n\t" \
  "s_barrier\n\t" \
  "ds_read2_b32 v[80:81], v78 offset0:0 offset1:2\n\t" \
  "ds_read2_b32 v[82:83], v78 offset0:32 offset1:34\n\t" \
  "ds_read2_b32 v[84:85], v78 offset0:64 offset1:66\n\t" \
  "ds_read2_b32 v[86:87], v78 offset0:96 offset1:98\n\t" \
  "ds_read2_b32 v[88:89], v79 offset0:0 offset1:2\n\t" \
  "ds_read2_b32 v[90:91], v79 offset0:32 offset1:34\n\t" \
  "ds_read2_b32 v[92:93], v79 offset0:64 offset1:66\n\t" \
  "ds_read2_b32 v[94:95], v79 offset0:96 offset1:98\n\t" \
  "s_waitcnt lgkmcnt(6)\n\t" \
  "v_perm_b32 v112, v82, v80, s62\n\t" \
  "v_perm_b32 v113, v82, v80, s63\n\t" \
  "s_waitcnt lgkmcnt(4)\n\t" \
  "v_perm_b32 v114, v86, v84, s62\n\t" \
  "v_perm_b32 v115, v86, v84, s63\n\t" \
  "s_waitcnt lgkmcnt(2)\n\t" \
  "v_perm_b32 v116, v90, v88, s62\n\t" \
  "v_perm_b32 v117, v90, v88, s63\n\t" \
  "s_waitcnt lgkmcnt(0)\n\t" \
  "v_perm_b32 v118, v94, v92, s62\n\t" \
  "v_perm_b32 v119, v94, v92, s63\n\t" \
  "ds_write_b128 v70, v[112:115] offset:20480\n\t" \
  "ds_write_b128 v70, v[116:119] offset:21504\n\t" \
  "v_perm_b32 v120, v83, v81, s62\n\t" \
  "v_perm_b32 v121, v83, v81, s63\n\t" \
  "v_perm_b32 v122, v87, v85, s62\n\t" \
  "v_perm_b32 v123, v87, v85, s63\n\t" \
  "v_perm_b32 v124, v91, v89, s62\n\t" \
  "v_perm_b32 v125, v91, v89, s63\n\t" \
  "v_perm_b32 v126, v95, v93, s62\n\t" \
  "v_perm_b32 v127, v95, v93, s63\n\t" \
  "ds_write_b128 v70, v[120:123] offset:16384\n\t" \
  "ds_write_b128 v70, v[124:127] offset:17408\n\t" \
  "v_add_u32 v78, 16384, v65\n\t" \
  "s_waitcnt vmcnt(12) lgkmcnt(0)\n\t" \
  "s_barrier\n\t" \
  "ds_read_b64_tr_b16 v[0:1], v78 offset:0\n\t" \
  "ds_read_b64_tr_b16 v[2:3], v78 offset:1024\n\t" \
  "ds_read_b64_tr_b16 v[4:5], v78 offset:8192\n\t" \
  "ds_read_b64_tr_b16 v[6:7], v78 offset:9216\n\t" \
  "ds_read_b64_tr_b16 v[8:9], v78 offset:8\n\t" \
  "ds_read_b64_tr_b16 v[10:11], v78 offset:1032\n\t" \
  "ds_read_b64_tr_b16 v[12:13], v78 offset:8200\n\t" \
  "ds_read_b64_tr_b16 v[14:15], v78 offset:9224\n\t" \
  "s_add_u32 m0, s54, 0\n\t" \
  "s_mov_b32 s60, 524288\n\t" \
  "buffer_load_dwordx4 v71, %[Wq], s60 offen lds\n\t" \
  "s_add_u32 m0, s54, 1024\n\t" \
  "s_mov_b32 s60, 557056\n\t" \
  "buffer_load_dwordx4 v71, %[Wq], s60 offen lds\n\t" \
  "s_add_u32 m0, s54, 2048\n\t" \
  "s_mov_b32 s60, 589824\n\t" \
  "buffer_load_dwordx4 v71, %[Wq], s60 offen lds\n\t" \
  "s_add_u32 m0, s54, 3072\n\t" \
  "s_mov_b32 s60, 622592\n\t" \
  "buffer_load_dwordx4 v71, %[Wq], s60 offen lds\n\t" \
  "v_add_u32 v78, 53248, v65\n\t" \
  "s_waitcnt vmcnt(12) lgkmcnt(0)\n\t" \
  "s_barrier\n\t" \
  "ds_read_b64_tr_b16 a[0:1], v78 offset:0\n\t" \
  "ds_read_b64_tr_b16 a[2:3], v78 offset:1024\n\t" \
  "ds_read_b64_tr_b16 a[4:5], v78 offset:8192\n\t" \
  "ds_read_b64_tr_b16 a[6:7], v78 offset:9216\n\t" \
  "ds_read_b64_tr_b16 a[8:9], v78 offset:8\n\t" \
  "ds_read_b64_tr_b16 a[10:11], v78 offset:1032\n\t" \
  "ds_read_b64_tr_b16 a[12:13], v78 offset:8200\n\t" \
  "ds_read_b64_tr_b16 a[14:15], v78 offset:9224\n\t" \
  "s_xor_b32 s61, s40, 1\n\t" \
  "s_mul_i32 s61, s61, 36864\n\t" \
  "s_add_u32 s61, s61, s55\n\t" \
  "s_add_u32 m0, s54, 16384\n\t" \
  "s_add_u32 s60, s61, 0\n\t" \
  "buffer_load_dwordx4 v75, %[Wo], s60 offen lds\n\t" \
  "s_add_u32 m0, s54, 17408\n\t" \
  "s_add_u32 s60, s61, 9216\n\t" \
  "buffer_load_dwordx4 v75, %[Wo], s60 offen lds\n\t" \
  "s_add_u32 m0, s54, 18432\n\t" \
  "s_add_u32 s60, s61, 18432\n\t" \
  "buffer_load_dwordx4 v75, %[Wo], s60 offen lds\n\t" \
  "s_add_u32 m0, s54, 19456\n\t" \
  "s_add_u32 s60, s61, 27648\n\t" \
  "buffer_load_dwordx4 v75, %[Wo], s60 offen lds\n\t" \
  "v_add_u32 v78, 90112, v64\n\t" \
  "v_add_u32 v79, 91136, v64\n\t" \
  "s_waitcnt vmcnt(12) lgkmcnt(0)\n\t" \
  "s_barrier\n\t" \
  "ds_read2_b32 v[80:81], v78 offset0:0 offset1:2\n\t" \
  "ds_read2_b32 v[82:83], v78 offset0:32 offset1:34\n\t" \
  "ds_read2_b32 v[84:85], v78 offset0:64 offset1:66\n\t" \
  "ds_read2_b32 v[86:87], v78 offset0:96 offset1:98\n\t" \
  "ds_read2_b32 v[88:89], v79 offset0:0 offset1:2\n\t" \
  "ds_read2_b32 v[90:91], v79 offset0:32 offset1:34\n\t" \
  "ds_read2_b32 v[92:93], v79 offset0:64 offset1:66\n\t" \
  "ds_read2_b32 v[94:95], v79 offset0:96 offset1:98\n\t" \
  "s_add_u32 m0, s54, 53248\n\t" \
  "s_mov_b32 s60, 524288\n\t" \
  "buffer_load_dwordx4 v71, %[Wk], s60 offen lds\n\t" \
  "s_add_u32 m0, s54, 54272\n\t" \
  "s_mov_b32 s60, 557056\n\t" \
  "buffer_load_dwordx4 v71, %[Wk], s60 offen lds\n\t" \
  "s_add_u32 m0, s54, 55296\n\t" \
  "s_mov_b32 s60, 589824\n\t" \
  "buffer_load_dwordx4 v71, %[Wk], s60 offen lds\n\t" \
  "s_add_u32 m0, s54, 56320\n\t" \
  "s_mov_b32 s60, 622592\n\t" \
  "buffer_load_dwordx4 v71, %[Wk], s60 offen lds\n\t" \
  "s_waitcnt lgkmcnt(6)\n\t" \
  "v_perm_b32 v112, v82, v80, s62\n\t" \
  "v_perm_b32 v113, v82, v80, s63\n\t" \
  "s_waitcnt lgkmcnt(4)\n\t" \
  "v_perm_b32 v114, v86, v84, s62\n\t" \
  "v_perm_b32 v115, v86, v84, s63\n\t" \
  "s_waitcnt lgkmcnt(2)\n\t" \
  "v_perm_b32 v116, v90, v88, s62\n\t" \
  "v_perm_b32 v117, v90, v88, s63\n\t" \
  "s_waitcnt lgkmcnt(0)\n\t" \
  "v_perm_b32 v118, v94, v92, s62\n\t" \
  "v_perm_b32 v119, v94, v92, s63\n\t" \
  "ds_write_b128 v70, v[112:115] offset:22528\n\t" \
  "ds_write_b128 v70, v[116:119] offset:23552\n\t" \
  "v_perm_b32 v120, v83, v81, s62\n\t" \
  "v_perm_b32 v121, v83, v81, s63\n\t" \
  "v_perm_b32 v122, v87, v85, s62\n\t" \
  "v_perm_b32 v123, v87, v85, s63\n\t" \
  "v_perm_b32 v124, v91, v89, s62\n\t" \
  "v_perm_b32 v125, v91, v89, s63\n\t" \
  "v_perm_b32 v126, v95, v93, s62\n\t" \
  "v_perm_b32 v127, v95, v93, s63\n\t" \
  "ds_write_b128 v70, v[120:123] offset:18432\n\t" \
  "ds_write_b128 v70, v[124:127] offset:19456\n\t" \
  "v_add_u32 v78, 126976, v66\n\t" \
  "s_waitcnt vmcnt(12) lgkmcnt(0)\n\t" \
  "s_barrier\n\t" \
  "ds_read_b64_tr_b16 a[16:17], v78 offset:0\n\t" \
  "ds_read_b64_tr_b16 a[18:19], v78 offset:1024\n\t" \
  "ds_read_b64_tr_b16 a[20:21], v78 offset:8192\n\t" \
  "ds_read_b64_tr_b16 a[22:23], v78 offset:9216\n\t" \
  "ds_read_b64_tr_b16 a[24:25], v78 offset:32\n\t" \
  "ds_read_b64_tr_b16 a[26:27], v78 offset:1056\n\t" \
  "ds_read_b64_tr_b16 a[28:29], v78 offset:8224\n\t" \
  "ds_read_b64_tr_b16 a[30:31], v78 offset:9248\n\t" \
  "s_add_u32 m0, s54, 90112\n\t" \
  "s_mov_b32 s60, 524288\n\t" \
  "buffer_load_dwordx4 v71, %[Wv], s60 offen lds\n\t" \
  "s_add_u32 m0, s54, 91136\n\t" \
  "s_mov_b32 s60, 557056\n\t" \
  "buffer_load_dwordx4 v71, %[Wv], s60 offen lds\n\t" \
  "s_add_u32 m0, s54, 92160\n\t" \
  "s_mov_b32 s60, 589824\n\t" \
  "buffer_load_dwordx4 v71, %[Wv], s60 offen lds\n\t" \
  "s_add_u32 m0, s54, 93184\n\t" \
  "s_mov_b32 s60, 622592\n\t" \
  "buffer_load_dwordx4 v71, %[Wv], s60 offen lds\n\t" \
  "v_add_u32 v78, 0, v65\n\t" \
  "s_waitcnt vmcnt(12) lgkmcnt(0)\n\t" \
  "s_barrier\n\t" \
  "ds_read_b64_tr_b16 a[32:33], v78 offset:0\n\t" \
  "ds_read_b64_tr_b16 a[34:35], v78 offset:1024\n\t" \
  "ds_read_b64_tr_b16 a[36:37], v78 offset:8192\n\t" \
  "ds_read_b64_tr_b16 a[38:39], v78 offset:9216\n\t" \
  "ds_read_b64_tr_b16 a[40:41], v78 offset:8\n\t" \
  "ds_read_b64_tr_b16 a[42:43], v78 offset:1032\n\t" \
  "ds_read_b64_tr_b16 a[44:45], v78 offset:8200\n\t" \
  "ds_read_b64_tr_b16 a[46:47], v78 offset:9224\n\t" \
  "s_xor_b32 s61, s40, 1\n\t" \
  "s_mul_i32 s61, s61, 36864\n\t" \
  "s_add_u32 s61, s61, s55\n\t" \
  "s_add_u32 m0, s54, 126976\n\t" \
  "s_add_u32 s60, s61, 512\n\t" \
  "buffer_load_dwordx4 v75, %[Wo], s60 offen lds\n\t" \
  "s_add_u32 m0, s54, 128000\n\t" \
  "s_add_u32 s60, s61, 9728\n\t" \
  "buffer_load_dwordx4 v75, %[Wo], s60 offen lds\n\t" \
  "s_add_u32 m0, s54, 129024\n\t" \
  "s_add_u32 s60, s61, 18944\n\t" \
  "buffer_load_dwordx4 v75, %[Wo], s60 offen lds\n\t" \
  "s_add_u32 m0, s54, 130048\n\t" \
  "s_add_u32 s60, s61, 28160\n\t" \
  "buffer_load_dwordx4 v75, %[Wo], s60 offen lds\n\t" \
  "v_add_u32 v78, 16384, v64\n\t" \
  "v_add_u32 v79, 17408, v64\n\t" \
  "s_waitcnt vmcnt(12) lgkmcnt(0)\n\t" \
  "s_barrier\n\t" \
  "ds_read2_b32 v[80:81], v78 offset0:0 offset1:2\n\t" \
  "ds_read2_b32 v[82:83], v78 offset0:32 offset1:34\n\t" \
  "ds_read2_b32 v[84:85], v78 offset0:64 offset1:66\n\t" \
  "ds_read2_b32 v[86:87], v78 offset0:96 offset1:98\n\t" \
  "ds_read2_b32 v[88:89], v79 offset0:0 offset1:2\n\t" \
  "ds_read2_b32 v[90:91], v79 offset0:32 offset1:34\n\t" \
  "ds_read2_b32 v[92:93], v79 offset0:64 offset1:66\n\t" \
  "ds_read2_b32 v[94:95], v79 offset0:96 offset1:98\n\t" \
  "s_add_u32 m0, s54, 0\n\t" \
  "s_mov_b32 s60, 1048576\n\t" \
  "buffer_load_dwordx4 v71, %[Wq], s60 offen lds\n\t" \
  "s_add_u32 m0, s54, 1024\n\t" \
  "s_mov_b32 s60, 1081344\n\t" \
  "buffer_load_dwordx4 v71, %[Wq], s60 offen lds\n\t" \
  "s_add_u32 m0, s54, 2048\n\t" \
  "s_mov_b32 s60, 1114112\n\t" \
  "buffer_load_dwordx4 v71, %[Wq], s60 offen lds\n\t" \
  "s_add_u32 m0, s54, 3072\n\t" \
  "s_mov_b32 s60, 1146880\n\t" \
  "buffer_load_dwordx4 v71, %[Wq], s60 offen lds\n\t" \
  "s_waitcnt lgkmcnt(6)\n\t" \
  "v_perm_b32 v112, v82, v80, s62\n\t" \
  "v_perm_b32 v113, v82, v80, s63\n\t" \
  "s_waitcnt lgkmcnt(4)\n\t" \
  "v_perm_b32 v114, v86, v84, s62\n\t" \
  "v_perm_b32 v115, v86, v84, s63\n\t" \
  "s_waitcnt lgkmcnt(2)\n\t" \
  "v_perm_b32 v116, v90, v88, s62\n\t" \
  "v_perm_b32 v117, v90, v88, s63\n\t" \
  "s_waitcnt lgkmcnt(0)\n\t" \
  "v_perm_b32 v118, v94, v92, s62\n\t" \
  "v_perm_b32 v119, v94, v92, s63\n\t" \
  "ds_write_b128 v70, v[112:115] offset:24576\n\t" \
  "ds_write_b128 v70, v[116:119] offset:25600\n\t" \
  "v_perm_b32 v200, v83, v81, s62\n\t" \
  "v_perm_b32 v201, v83, v81, s63\n\t" \
  "v_perm_b32 v202, v87, v85, s62\n\t" \
  "v_perm_b32 v203, v87, v85, s63\n\t" \
  "v_perm_b32 v204, v91, v89, s62\n\t" \
  "v_perm_b32 v205, v91, v89, s63\n\t" \
  "v_perm_b32 v206, v95, v93, s62\n\t" \
  "v_perm_b32 v207, v95, v93, s63\n\t" \
  "v_add_u32 v78, 53248, v65\n\t" \
  "s_waitcnt vmcnt(12) lgkmcnt(0)\n\t" \
  "s_barrier\n\t" \
  "ds_read_b64_tr_b16 a[48:49], v78 offset:0\n\t" \
  "ds_read_b64_tr_b16 a[50:51], v78 offset:1024\n\t" \
  "ds_read_b64_tr_b16 a[52:53], v78 offset:8192\n\t" \
  "ds_read_b64_tr_b16 a[54:55], v78 offset:9216\n\t" \
  "ds_read_b64_tr_b16 a[56:57], v78 offset:8\n\t" \
  "ds_read_b64_tr_b16 a[58:59], v78 offset:1032\n\t" \
  "ds_read_b64_tr_b16 a[60:61], v78 offset:8200\n\t" \
  "ds_read_b64_tr_b16 a[62:63], v78 offset:9224\n\t" \
  "s_add_u32 m0, s54, 16384\n\t" \
  "s_mov_b32 s60, 1048576\n\t" \
  "buffer_load_dwordx4 v71, %[Wk], s60 offen lds\n\t" \
  "s_add_u32 m0, s54, 17408\n\t" \
  "s_mov_b32 s60, 1081344\n\t" \
  "buffer_load_dwordx4 v71, %[Wk], s60 offen lds\n\t" \
  "s_add_u32 m0, s54, 18432\n\t" \
  "s_mov_b32 s60, 1114112\n\t" \
  "buffer_load_dwordx4 v71, %[Wk], s60 offen lds\n\t" \
  "s_add_u32 m0, s54, 19456\n\t" \
  "s_mov_b32 s60, 1146880\n\t" \
  "buffer_load_dwordx4 v71, %[Wk], s60 offen lds\n\t" \
  "v_add_u32 v78, 90112, v66\n\t" \
  "s_waitcnt vmcnt(12) lgkmcnt(0)\n\t" \
  "s_barrier\n\t" \
  "ds_read_b64_tr_b16 a[64:65], v78 offset:0\n\t" \
  "ds_read_b64_tr_b16 a[66:67], v78 offset:1024\n\t" \
  "ds_read_b64_tr_b16 a[68:69], v78 offset:8192\n\t" \
  "ds_read_b64_tr_b16 a[70:71], v78 offset:9216\n\t" \
  "ds_read_b64_tr_b16 a[72:73], v78 offset:32\n\t" \
  "ds_read_b64_tr_b16 a[74:75], v78 offset:1056\n\t" \
  "ds_read_b64_tr_b16 a[76:77], v78 offset:8224\n\t" \
  "ds_read_b64_tr_b16 a[78:79], v78 offset:9248\n\t" \
  "s_xor_b32 s61, s40, 2\n\t" \
  "s_mul_i32 s61, s61, 36864\n\t" \
  "s_add_u32 s61, s61, s55\n\t" \
  "s_add_u32 m0, s54, 53248\n\t" \
  "s_add_u32 s60, s61, 0\n\t" \
  "buffer_load_dwordx4 v75, %[Wo], s60 offen lds\n\t" \
  "s_add_u32 m0, s54, 54272\n\t" \
  "s_add_u32 s60, s61, 9216\n\t" \
  "buffer_load_dwordx4 v75, %[Wo], s60 offen lds\n\t" \
  "s_add_u32 m0, s54, 55296\n\t" \
  "s_add_u32 s60, s61, 18432\n\t" \
  "buffer_load_dwordx4 v75, %[Wo], s60 offen lds\n\t" \
  "s_add_u32 m0, s54, 56320\n\t" \
  "s_add_u32 s60, s61, 27648\n\t" \
  "buffer_load_dwordx4 v75, %[Wo], s60 offen lds\n\t" \
  "v_add_u32 v78, 126976, v64\n\t" \
  "v_add_u32 v79, 128000, v64\n\t" \
  "s_waitcnt vmcnt(12) lgkmcnt(0)\n\t" \
  "s_barrier\n\t" \
  "ds_read2_b32 v[80:81], v78 offset0:0 offset1:2\n\t" \
  "ds_read2_b32 v[82:83], v78 offset0:32 offset1:34\n\t" \
  "ds_read2_b32 v[84:85], v78 offset0:64 offset1:66\n\t" \
  "ds_read2_b32 v[86:87], v78 offset0:96 offset1:98\n\t" \
  "ds_read2_b32 v[88:89], v79 offset0:0 offset1:2\n\t" \
  "ds_read2_b32 v[90:91], v79 offset0:32 offset1:34\n\t" \
  "ds_read2_b32 v[92:93], v79 offset0:64 offset1:66\n\t" \
  "ds_read2_b32 v[94:95], v79 offset0:96 offset1:98\n\t" \
  "s_add_u32 m0, s54, 90112\n\t" \
  "s_mov_b32 s60, 1048576\n\t" \
  "buffer_load_dwordx4 v71, %[Wv], s60 offen lds\n\t" \
  "s_add_u32 m0, s54, 91136\n\t" \
  "s_mov_b32 s60, 1081344\n\t" \
  "buffer_load_dwordx4 v71, %[Wv], s60 offen lds\n\t" \
  "s_add_u32 m0, s54, 92160\n\t" \
  "s_mov_b32 s60, 1114112\n\t" \
  "buffer_load_dwordx4 v71, %[Wv], s60 offen lds\n\t" \
  "s_add_u32 m0, s54, 93184\n\t" \
  "s_mov_b32 s60, 1146880\n\t" \
  "buffer_load_dwordx4 v71, %[Wv], s60 offen lds\n\t" \
  "s_waitcnt lgkmcnt(6)\n\t" \
  "v_perm_b32 v112, v82, v80, s62\n\t" \
  "v_perm_b32 v113, v82, v80, s63\n\t" \
  "s_waitcnt lgkmcnt(4)\n\t" \
  "v_perm_b32 v114, v86, v84, s62\n\t" \
  "v_perm_b32 v115, v86, v84, s63\n\t" \
  "s_waitcnt lgkmcnt(2)\n\t" \
  "v_perm_b32 v116, v90, v88, s62\n\t" \
  "v_perm_b32 v117, v90, v88, s63\n\t" \
  "s_waitcnt lgkmcnt(0)\n\t" \
  "v_perm_b32 v118, v94, v92, s62\n\t" \
  "v_perm_b32 v119, v94, v92, s63\n\t" \
  "ds_write_b128 v70, v[112:115] offset:26624\n\t" \
  "ds_write_b128 v70, v[116:119] offset:27648\n\t" \
  "v_perm_b32 v232, v83, v81, s62\n\t" \
  "v_perm_b32 v233, v83, v81, s63\n\t" \
  "v_perm_b32 v234, v87, v85, s62\n\t" \
  "v_perm_b32 v235, v87, v85, s63\n\t" \
  "v_perm_b32 v236, v91, v89, s62\n\t" \
  "v_perm_b32 v237, v91, v89, s63\n\t" \
  "v_perm_b32 v238, v95, v93, s62\n\t" \
  "v_perm_b32 v239, v95, v93, s63\n\t" \
  "v_add_u32 v78, 0, v65\n\t" \
  "s_waitcnt vmcnt(12) lgkmcnt(0)\n\t" \
  "s_barrier\n\t" \
  "ds_read_b64_tr_b16 a[80:81], v78 offset:0\n\t" \
  "ds_read_b64_tr_b16 a[82:83], v78 offset:1024\n\t" \
  "ds_read_b64_tr_b16 a[84:85], v78 offset:8192\n\t" \
  "ds_read_b64_tr_b16 a[86:87], v78 offset:9216\n\t" \
  "ds_read_b64_tr_b16 a[88:89], v78 offset:8\n\t" \
  "ds_read_b64_tr_b16 a[90:91], v78 offset:1032\n\t" \
  "ds_read_b64_tr_b16 a[92:93], v78 offset:8200\n\t" \
  "ds_read_b64_tr_b16 a[94:95], v78 offset:9224\n\t" \
  "s_add_u32 m0, s54, 126976\n\t" \
  "s_mov_b32 s60, 1572864\n\t" \
  "buffer_load_dwordx4 v71, %[Wq], s60 offen lds\n\t" \
  "s_add_u32 m0, s54, 128000\n\t" \
  "s_mov_b32 s60, 1605632\n\t" \
  "buffer_load_dwordx4 v71, %[Wq], s60 offen lds\n\t" \
  "s_add_u32 m0, s54, 129024\n\t" \
  "s_mov_b32 s60, 1638400\n\t" \
  "buffer_load_dwordx4 v71, %[Wq], s60 offen lds\n\t" \
  "s_add_u32 m0, s54, 130048\n\t" \
  "s_mov_b32 s60, 1671168\n\t" \
  "buffer_load_dwordx4 v71, %[Wq], s60 offen lds\n\t" \
  "v_add_u32 v78, 16384, v65\n\t" \
  "s_waitcnt vmcnt(12) lgkmcnt(0)\n\t" \
  "s_barrier\n\t" \
  "ds_read_b64_tr_b16 v[32:33], v78 offset:0\n\t" \
  "ds_read_b64_tr_b16 v[34:35], v78 offset:1024\n\t" \
  "ds_read_b64_tr_b16 v[36:37], v78 offset:8192\n\t" \
  "ds_read_b64_tr_b16 v[38:39], v78 offset:9216\n\t" \
  "ds_read_b64_tr_b16 a[96:97], v78 offset:8\n\t" \
  "ds_read_b64_tr_b16 a[98:99], v78 offset:1032\n\t" \
  "ds_read_b64_tr_b16 a[100:101], v78 offset:8200\n\t" \
  "ds_read_b64_tr_b16 a[102:103], v78 offset:9224\n\t" \
  "s_xor_b32 s61, s40, 2\n\t" \
  "s_mul_i32 s61, s61, 36864\n\t" \
  "s_add_u32 s61, s61, s55\n\t" \
  "s_add_u32 m0, s54, 0\n\t" \
  "s_add_u32 s60, s61, 512\n\t" \
  "buffer_load_dwordx4 v75, %[Wo], s60 offen lds\n\t" \
  "s_add_u32 m0, s54, 1024\n\t" \
  "s_add_u32 s60, s61, 9728\n\t" \
  "buffer_load_dwordx4 v75, %[Wo], s60 offen lds\n\t" \
  "s_add_u32 m0, s54, 2048\n\t" \
  "s_add_u32 s60, s61, 18944\n\t" \
  "buffer_load_dwordx4 v75, %[Wo], s60 offen lds\n\t" \
  "s_add_u32 m0, s54, 3072\n\t" \
  "s_add_u32 s60, s61, 28160\n\t" \
  "buffer_load_dwordx4 v75, %[Wo], s60 offen lds\n\t" \
  "v_add_u32 v78, 53248, v64\n\t" \
  "v_add_u32 v79, 54272, v64\n\t" \
  "s_waitcnt vmcnt(12) lgkmcnt(0)\n\t" \
  "s_barrier\n\t" \
  "ds_read2_b32 v[80:81], v78 offset0:0 offset1:2\n\t" \
  "ds_read2_b32 v[82:83], v78 offset0:32 offset1:34\n\t" \
  "ds_read2_b32 v[84:85], v78 offset0:64 offset1:66\n\t" \
  "ds_read2_b32 v[86:87], v78 offset0:96 offset1:98\n\t" \
  "ds_read2_b32 v[88:89], v79 offset0:0 offset1:2\n\t" \
  "ds_read2_b32 v[90:91], v79 offset0:32 offset1:34\n\t" \
  "ds_read2_b32 v[92:93], v79 offset0:64 offset1:66\n\t" \
  "ds_read2_b32 v[94:95], v79 offset0:96 offset1:98\n\t" \
  "s_add_u32 m0, s54, 16384\n\t" \
  "s_mov_b32 s60, 1572864\n\t" \
  "buffer_load_dwordx4 v71, %[Wk], s60 offen lds\n\t" \
  "s_add_u32 m0, s54, 17408\n\t" \
  "s_mov_b32 s60, 1605632\n\t" \
  "buffer_load_dwordx4 v71, %[Wk], s60 offen lds\n\t" \
  "s_add_u32 m0, s54, 18432\n\t" \
  "s_mov_b32 s60, 1638400\n\t" \
  "buffer_load_dwordx4 v71, %[Wk], s60 offen lds\n\t" \
  "s_add_u32 m0, s54, 19456\n\t" \
  "s_mov_b32 s60, 1671168\n\t" \
  "buffer_load_dwordx4 v71, %[Wk], s60 offen lds\n\t" \
  "s_waitcnt lgkmcnt(6)\n\t" \
  "v_perm_b32 v112, v82, v80, s62\n\t" \
  "v_perm_b32 v113, v82, v80, s63\n\t" \
  "s_waitcnt lgkmcnt(4)\n\t" \
  "v_perm_b32 v114, v86, v84, s62\n\t" \
  "v_perm_b32 v115, v86, v84, s63\n\t" \
  "s_waitcnt lgkmcnt(2)\n\t" \
  "v_perm_b32 v116, v90, v88, s62\n\t" \
  "v_perm_b32 v117, v90, v88, s63\n\t" \
  "s_waitcnt lgkmcnt(0)\n\t" \
  "v_perm_b32 v118, v94, v92, s62\n\t" \
  "v_perm_b32 v119, v94, v92, s63\n\t" \
  "ds_write_b128 v70, v[112:115] offset:28672\n\t" \
  "ds_write_b128 v70, v[116:119] offset:29696\n\t" \
  "v_perm_b32 v208, v83, v81, s62\n\t" \
  "v_perm_b32 v209, v83, v81, s63\n\t" \
  "v_perm_b32 v210, v87, v85, s62\n\t" \
  "v_perm_b32 v211, v87, v85, s63\n\t" \
  "v_perm_b32 v212, v91, v89, s62\n\t" \
  "v_perm_b32 v213, v91, v89, s63\n\t" \
  "v_perm_b32 v214, v95, v93, s62\n\t" \
  "v_perm_b32 v215, v95, v93, s63\n\t" \
  "v_add_u32 v78, 90112, v66\n\t" \
  "s_waitcnt vmcnt(12) lgkmcnt(0)\n\t" \
  "s_barrier\n\t" \
  "ds_read_b64_tr_b16 a[104:105], v78 offset:0\n\t" \
  "ds_read_b64_tr_b16 a[106:107], v78 offset:1024\n\t" \
  "ds_read_b64_tr_b16 a[108:109], v78 offset:8192\n\t" \
  "ds_read_b64_tr_b16 a[110:111], v78 offset:9216\n\t" \
  "ds_read_b64_tr_b16 a[112:113], v78 offset:32\n\t" \
  "ds_read_b64_tr_b16 a[114:115], v78 offset:1056\n\t" \
  "ds_read_b64_tr_b16 a[116:117], v78 offset:8224\n\t" \
  "ds_read_b64_tr_b16 a[118:119], v78 offset:9248\n\t" \
  "s_add_u32 m0, s54, 53248\n\t" \
  "s_mov_b32 s60, 1572864\n\t" \
  "buffer_load_dwordx4 v71, %[Wv], s60 offen lds\n\t" \
  "s_add_u32 m0, s54, 54272\n\t" \
  "s_mov_b32 s60, 1605632\n\t" \
  "buffer_load_dwordx4 v71, %[Wv], s60 offen lds\n\t" \
  "s_add_u32 m0, s54, 55296\n\t" \
  "s_mov_b32 s60, 1638400\n\t" \
  "buffer_load_dwordx4 v71, %[Wv], s60 offen lds\n\t" \
  "s_add_u32 m0, s54, 56320\n\t" \
  "s_mov_b32 s60, 1671168\n\t" \
  "buffer_load_dwordx4 v71, %[Wv], s60 offen lds\n\t" \
  "v_add_u32 v78, 126976, v65\n\t" \
  "s_waitcnt vmcnt(12) lgkmcnt(0)\n\t" \
  "s_barrier\n\t" \
  "ds_read_b64_tr_b16 a[120:121], v78 offset:0\n\t" \
  "ds_read_b64_tr_b16 a[122:123], v78 offset:1024\n\t" \
  "ds_read_b64_tr_b16 a[124:125], v78 offset:8192\n\t" \
  "ds_read_b64_tr_b16 a[126:127], v78 offset:9216\n\t" \
  "ds_read_b64_tr_b16 v[16:17], v78 offset:8\n\t" \
  "ds_read_b64_tr_b16 v[18:19], v78 offset:1032\n\t" \
  "ds_read_b64_tr_b16 v[20:21], v78 offset:8200\n\t" \
  "ds_read_b64_tr_b16 v[22:23], v78 offset:9224\n\t" \
  "s_xor_b32 s61, s40, 3\n\t" \
  "s_mul_i32 s61, s61, 36864\n\t" \
  "s_add_u32 s61, s61, s55\n\t" \
  "s_add_u32 m0, s54, 90112\n\t" \
  "s_add_u32 s60, s61, 0\n\t" \
  "buffer_load_dwordx4 v75, %[Wo], s60 offen lds\n\t" \
  "s_add_u32 m0, s54, 91136\n\t" \
  "s_add_u32 s60, s61, 9216\n\t" \
  "buffer_load_dwordx4 v75, %[Wo], s60 offen lds\n\t" \
  "s_add_u32 m0, s54, 92160\n\t" \
  "s_add_u32 s60, s61, 18432\n\t" \
  "buffer_load_dwordx4 v75, %[Wo], s60 offen lds\n\t" \
  "s_add_u32 m0, s54, 93184\n\t" \
  "s_add_u32 s60, s61, 27648\n\t" \
  "buffer_load_dwordx4 v75, %[Wo], s60 offen lds\n\t" \
  "v_add_u32 v78, 0, v64\n\t" \
  "v_add_u32 v79, 1024, v64\n\t" \
  "s_waitcnt vmcnt(12) lgkmcnt(0)\n\t" \
  "s_barrier\n\t" \
  "ds_read2_b32 v[80:81], v78 offset0:0 offset1:2\n\t" \
  "ds_read2_b32 v[82:83], v78 offset0:32 offset1:34\n\t" \
  "ds_read2_b32 v[84:85], v78 offset0:64 offset1:66\n\t" \
  "ds_read2_b32 v[86:87], v78 offset0:96 offset1:98\n\t" \
  "ds_read2_b32 v[88:89], v79 offset0:0 offset1:2\n\t" \
  "ds_read2_b32 v[90:91], v79 offset0:32 offset1:34\n\t" \
  "ds_read2_b32 v[92:93], v79 offset0:64 offset1:66\n\t" \
  "ds_read2_b32 v[94:95], v79 offset0:96 offset1:98\n\t" \
  "s_add_u32 m0, s54, 126976\n\t" \
  "s_mov_b32 s60, 2097152\n\t" \
  "buffer_load_dwordx4 v71, %[Wq], s60 offen lds\n\t" \
  "s_add_u32 m0, s54, 128000\n\t" \
  "s_mov_b32 s60, 2129920\n\t" \
  "buffer_load_dwordx4 v71, %[Wq], s60 offen lds\n\t" \
  "s_add_u32 m0, s54, 129024\n\t" \
  "s_mov_b32 s60, 2162688\n\t" \
  "buffer_load_dwordx4 v71, %[Wq], s60 offen lds\n\t" \
  "s_add_u32 m0, s54, 130048\n\t" \
  "s_mov_b32 s60, 2195456\n\t" \
  "buffer_load_dwordx4 v71, %[Wq], s60 offen lds\n\t" \
  "s_waitcnt lgkmcnt(6)\n\t" \
  "v_perm_b32 v112, v82, v80, s62\n\t" \
  "v_perm_b32 v113, v82, v80, s63\n\t" \
  "s_waitcnt lgkmcnt(4)\n\t" \
  "v_perm_b32 v114, v86, v84, s62\n\t" \
  "v_perm_b32 v115, v86, v84, s63\n\t" \
  "s_waitcnt lgkmcnt(2)\n\t" \
  "v_perm_b32 v116, v90, v88, s62\n\t" \
  "v_perm_b32 v117, v90, v88, s63\n\t" \
  "s_waitcnt lgkmcnt(0)\n\t" \
  "v_perm_b32 v118, v94, v92, s62\n\t" \
  "v_perm_b32 v119, v94, v92, s63\n\t" \
  "ds_write_b128 v70, v[112:115] offset:30720\n\t" \
  "ds_write_b128 v70, v[116:119] offset:31744\n\t" \
  "v_perm_b32 v240, v83, v81, s62\n\t" \
  "v_perm_b32 v241, v83, v81, s63\n\t" \
  "v_perm_b32 v242, v87, v85, s62\n\t" \
  "v_perm_b32 v243, v87, v85, s63\n\t" \
  "v_perm_b32 v244, v91, v89, s62\n\t" \
  "v_perm_b32 v245, v91, v89, s63\n\t" \
  "v_perm_b32 v246, v95, v93, s62\n\t" \
  "v_perm_b32 v247, v95, v93, s63\n\t" \
  "v_add_u32 v78, 16384, v65\n\t" \
  "s_waitcnt vmcnt(12) lgkmcnt(0)\n\t" \
  "s_barrier\n\t" \
  "ds_read_b64_tr_b16 a[128:129], v78 offset:0\n\t" \
  "ds_read_b64_tr_b16 a[130:131], v78 offset:1024\n\t" \
  "ds_read_b64_tr_b16 a[132:133], v78 offset:8192\n\t" \
  "ds_read_b64_tr_b16 a[134:135], v78 offset:9216\n\t" \
  "ds_read_b64_tr_b16 a[136:137], v78 offset:8\n\t" \
  "ds_read_b64_tr_b16 a[138:139], v78 offset:1032\n\t" \
  "ds_read_b64_tr_b16 a[140:141], v78 offset:8200\n\t" \
  "ds_read_b64_tr_b16 a[142:143], v78 offset:9224\n\t" \
  "s_add_u32 m0, s54, 0\n\t" \
  "s_mov_b32 s60, 2097152\n\t" \
  "buffer_load_dwordx4 v71, %[Wk], s60 offen lds\n\t" \
  "s_add_u32 m0, s54, 1024\n\t" \
  "s_mov_b32 s60, 2129920\n\t" \
  "buffer_load_dwordx4 v71, %[Wk], s60 offen lds\n\t" \
  "s_add_u32 m0, s54, 2048\n\t" \
  "s_mov_b32 s60, 2162688\n\t" \
  "buffer_load_dwordx4 v71, %[Wk], s60 offen lds\n\t" \
  "s_add_u32 m0, s54, 3072\n\t" \
  "s_mov_b32 s60, 2195456\n\t" \
  "buffer_load_dwordx4 v71, %[Wk], s60 offen lds\n\t" \
  "v_add_u32 v78, 53248, v66\n\t" \
  "s_waitcnt vmcnt(12) lgkmcnt(0)\n\t" \
  "s_barrier\n\t" \
  "ds_read_b64_tr_b16 a[144:145], v78 offset:0\n\t" \
  "ds_read_b64_tr_b16 a[146:147], v78 offset:1024\n\t" \
  "ds_read_b64_tr_b16 a[148:149], v78 offset:8192\n\t" \
  "ds_read_b64_tr_b16 a[150:151], v78 offset:9216\n\t" \
  "ds_read_b64_tr_b16 a[152:153], v78 offset:32\n\t" \
  "ds_read_b64_tr_b16 a[154:155], v78 offset:1056\n\t" \
  "ds_read_b64_tr_b16 a[156:157], v78 offset:8224\n\t" \
  "ds_read_b64_tr_b16 a[158:159], v78 offset:9248\n\t" \
  "s_xor_b32 s61, s40, 3\n\t" \
  "s_mul_i32 s61, s61, 36864\n\t" \
  "s_add_u32 s61, s61, s55\n\t" \
  "s_add_u32 m0, s54, 16384\n\t" \
  "s_add_u32 s60, s61, 512\n\t" \
  "buffer_load_dwordx4 v75, %[Wo], s60 offen lds\n\t" \
  "s_add_u32 m0, s54, 17408\n\t" \
  "s_add_u32 s60, s61, 9728\n\t" \
  "buffer_load_dwordx4 v75, %[Wo], s60 offen lds\n\t" \
  "s_add_u32 m0, s54, 18432\n\t" \
  "s_add_u32 s60, s61, 18944\n\t" \
  "buffer_load_dwordx4 v75, %[Wo], s60 offen lds\n\t" \
  "s_add_u32 m0, s54, 19456\n\t" \
  "s_add_u32 s60, s61, 28160\n\t" \
  "buffer_load_dwordx4 v75, %[Wo], s60 offen lds\n\t" \
  "v_add_u32 v78, 90112, v64\n\t" \
  "v_add_u32 v79, 91136, v64\n\t" \
  "s_waitcnt vmcnt(12) lgkmcnt(0)\n\t" \
  "s_barrier\n\t" \
  "ds_read2_b32 v[80:81], v78 offset0:0 offset1:2\n\t" \
  "ds_read2_b32 v[82:83], v78 offset0:32 offset1:34\n\t" \
  "ds_read2_b32 v[84:85], v78 offset0:64 offset1:66\n\t" \
  "ds_read2_b32 v[86:87], v78 offset0:96 offset1:98\n\t" \
  "ds_read2_b32 v[88:89], v79 offset0:0 offset1:2\n\t" \
  "ds_read2_b32 v[90:91], v79 offset0:32 offset1:34\n\t" \
  "ds_read2_b32 v[92:93], v79 offset0:64 offset1:66\n\t" \
  "ds_read2_b32 v[94:95], v79 offset0:96 offset1:98\n\t" \
  "s_add_u32 m0, s54, 53248\n\t" \
  "s_mov_b32 s60, 2097152\n\t" \
  "buffer_load_dwordx4 v71, %[Wv], s60 offen lds\n\t" \
  "s_add_u32 m0, s54, 54272\n\t" \
  "s_mov_b32 s60, 2129920\n\t" \
  "buffer_load_dwordx4 v71, %[Wv], s60 offen lds\n\t" \
  "s_add_u32 m0, s54, 55296\n\t" \
  "s_mov_b32 s60, 2162688\n\t" \
  "buffer_load_dwordx4 v71, %[Wv], s60 offen lds\n\t" \
  "s_add_u32 m0, s54, 56320\n\t" \
  "s_mov_b32 s60, 2195456\n\t" \
  "buffer_load_dwordx4 v71, %[Wv], s60 offen lds\n\t" \
  "s_waitcnt lgkmcnt(6)\n\t" \
  "v_perm_b32 v192, v82, v80, s62\n\t" \
  "v_perm_b32 v193, v82, v80, s63\n\t" \
  "s_waitcnt lgkmcnt(4)\n\t" \
  "v_perm_b32 v194, v86, v84, s62\n\t" \
  "v_perm_b32 v195, v86, v84, s63\n\t" \
  "s_waitcnt lgkmcnt(2)\n\t" \
  "v_perm_b32 v196, v90, v88, s62\n\t" \
  "v_perm_b32 v197, v90, v88, s63\n\t" \
  "s_waitcnt lgkmcnt(0)\n\t" \
  "v_perm_b32 v198, v94, v92, s62\n\t" \
  "v_perm_b32 v199, v94, v92, s63\n\t" \
  "v_perm_b32 v216, v83, v81, s62\n\t" \
  "v_perm_b32 v217, v83, v81, s63\n\t" \
  "v_perm_b32 v218, v87, v85, s62\n\t" \
  "v_perm_b32 v219, v87, v85, s63\n\t" \
  "v_perm_b32 v220, v91, v89, s62\n\t" \
  "v_perm_b32 v221, v91, v89, s63\n\t" \
  "v_perm_b32 v222, v95, v93, s62\n\t" \
  "v_perm_b32 v223, v95, v93, s63\n\t" \
  "v_add_u32 v78, 126976, v65\n\t" \
  "s_waitcnt vmcnt(12) lgkmcnt(0)\n\t" \
  "s_barrier\n\t" \
  "ds_read_b64_tr_b16 a[160:161], v78 offset:0\n\t" \
  "ds_read_b64_tr_b16 a[162:163], v78 offset:1024\n\t" \
  "ds_read_b64_tr_b16 a[164:165], v78 offset:8192\n\t" \
  "ds_read_b64_tr_b16 a[166:167], v78 offset:9216\n\t" \
  "ds_read_b64_tr_b16 a[168:169], v78 offset:8\n\t" \
  "ds_read_b64_tr_b16 a[170:171], v78 offset:1032\n\t" \
  "ds_read_b64_tr_b16 a[172:173], v78 offset:8200\n\t" \
  "ds_read_b64_tr_b16 a[174:175], v78 offset:9224\n\t" \
  "s_add_u32 m0, s54, 90112\n\t" \
  "s_mov_b32 s60, 2621440\n\t" \
  "buffer_load_dwordx4 v71, %[Wq], s60 offen lds\n\t" \
  "s_add_u32 m0, s54, 91136\n\t" \
  "s_mov_b32 s60, 2654208\n\t" \
  "buffer_load_dwordx4 v71, %[Wq], s60 offen lds\n\t" \
  "s_add_u32 m0, s54, 92160\n\t" \
  "s_mov_b32 s60, 2686976\n\t" \
  "buffer_load_dwordx4 v71, %[Wq], s60 offen lds\n\t" \
  "s_add_u32 m0, s54, 93184\n\t" \
  "s_mov_b32 s60, 2719744\n\t" \
  "buffer_load_dwordx4 v71, %[Wq], s60 offen lds\n\t" \
  "v_add_u32 v78, 0, v65\n\t" \
  "s_waitcnt vmcnt(12) lgkmcnt(0)\n\t" \
  "s_barrier\n\t" \
  "ds_read_b64_tr_b16 a[176:177], v78 offset:0\n\t" \
  "ds_read_b64_tr_b16 a[178:179], v78 offset:1024\n\t" \
  "ds_read_b64_tr_b16 a[180:181], v78 offset:8192\n\t" \
  "ds_read_b64_tr_b16 a[182:183], v78 offset:9216\n\t" \
  "ds_read_b64_tr_b16 a[184:185], v78 offset:8\n\t" \
  "ds_read_b64_tr_b16 a[186:187], v78 offset:1032\n\t" \
  "ds_read_b64_tr_b16 a[188:189], v78 offset:8200\n\t" \
  "ds_read_b64_tr_b16 a[190:191], v78 offset:9224\n\t" \
  "s_add_u32 m0, s54, 126976\n\t" \
  "s_add_u32 s60, s75, 0\n\t" \
  "buffer_load_dwordx4 v75, %[Wo], s60 offen lds\n\t" \
  "s_add_u32 m0, s54, 128000\n\t" \
  "s_add_u32 s60, s75, 9216\n\t" \
  "buffer_load_dwordx4 v75, %[Wo], s60 offen lds\n\t" \
  "s_add_u32 m0, s54, 129024\n\t" \
  "s_add_u32 s60, s75, 18432\n\t" \
  "buffer_load_dwordx4 v75, %[Wo], s60 offen lds\n\t" \
  "s_add_u32 m0, s54, 130048\n\t" \
  "s_add_u32 s60, s75, 27648\n\t" \
  "buffer_load_dwordx4 v75, %[Wo], s60 offen lds\n\t" \
  "v_add_u32 v78, 16384, v64\n\t" \
  "v_add_u32 v79, 17408, v64\n\t" \
  "s_waitcnt vmcnt(12) lgkmcnt(0)\n\t" \
  "s_barrier\n\t" \
  "ds_read2_b32 v[80:81], v78 offset0:0 offset1:2\n\t" \
  "ds_read2_b32 v[82:83], v78 offset0:32 offset1:34\n\t" \
  "ds_read2_b32 v[84:85], v78 offset0:64 offset1:66\n\t" \
  "ds_read2_b32 v[86:87], v78 offset0:96 offset1:98\n\t" \
  "ds_read2_b32 v[88:89], v79 offset0:0 offset1:2\n\t" \
  "ds_read2_b32 v[90:91], v79 offset0:32 offset1:34\n\t" \
  "ds_read2_b32 v[92:93], v79 offset0:64 offset1:66\n\t" \
  "ds_read2_b32 v[94:95], v79 offset0:96 offset1:98\n\t" \
  "s_add_u32 m0, s54, 0\n\t" \
  "s_mov_b32 s60, 2621440\n\t" \
  "buffer_load_dwordx4 v71, %[Wk], s60 offen lds\n\t" \
  "s_add_u32 m0, s54, 1024\n\t" \
  "s_mov_b32 s60, 2654208\n\t" \
  "buffer_load_dwordx4 v71, %[Wk], s60 offen lds\n\t" \
  "s_add_u32 m0, s54, 2048\n\t" \
  "s_mov_b32 s60, 2686976\n\t" \
  "buffer_load_dwordx4 v71, %[Wk], s60 offen lds\n\t" \
  "s_add_u32 m0, s54, 3072\n\t" \
  "s_mov_b32 s60, 2719744\n\t" \
  "buffer_load_dwordx4 v71, %[Wk], s60 offen lds\n\t" \
  "s_waitcnt lgkmcnt(6)\n\t" \
  "v_perm_b32 v224, v82, v80, s62\n\t" \
  "v_perm_b32 v225, v82, v80, s63\n\t" \
  "s_waitcnt lgkmcnt(4)\n\t" \
  "v_perm_b32 v226, v86, v84, s62\n\t" \
  "v_perm_b32 v227, v86, v84, s63\n\t" \
  "s_waitcnt lgkmcnt(2)\n\t" \
  "v_perm_b32 v228, v90, v88, s62\n\t" \
  "v_perm_b32 v229, v90, v88, s63\n\t" \
  "s_waitcnt lgkmcnt(0)\n\t" \
  "v_perm_b32 v230, v94, v92, s62\n\t" \
  "v_perm_b32 v231, v94, v92, s63\n\t" \
  "v_perm_b32 v120, v83, v81, s62\n\t" \
  "v_perm_b32 v121, v83, v81, s63\n\t" \
  "v_perm_b32 v122, v87, v85, s62\n\t" \
  "v_perm_b32 v123, v87, v85, s63\n\t" \
  "v_perm_b32 v124, v91, v89, s62\n\t" \
  "v_perm_b32 v125, v91, v89, s63\n\t" \
  "v_perm_b32 v126, v95, v93, s62\n\t" \
  "v_perm_b32 v127, v95, v93, s63\n\t" \
  "ds_write_b128 v70, v[120:123] offset:32768\n\t" \
  "ds_write_b128 v70, v[124:127] offset:33792\n\t" \
  "v_add_u32 v78, 53248, v66\n\t" \
  "s_waitcnt vmcnt(12) lgkmcnt(0)\n\t" \
  "s_barrier\n\t" \
  "ds_read_b64_tr_b16 a[192:193], v78 offset:0\n\t" \
  "ds_read_b64_tr_b16 a[194:195], v78 offset:1024\n\t" \
  "ds_read_b64_tr_b16 a[196:197], v78 offset:8192\n\t" \
  "ds_read_b64_tr_b16 a[198:199], v78 offset:9216\n\t" \
  "ds_read_b64_tr_b16 v[48:49], v78 offset:32\n\t" \
  "ds_read_b64_tr_b16 v[50:51], v78 offset:1056\n\t" \
  "ds_read_b64_tr_b16 v[52:53], v78 offset:8224\n\t" \
  "ds_read_b64_tr_b16 v[54:55], v78 offset:9248\n\t" \
  "s_add_u32 m0, s54, 16384\n\t" \
  "s_mov_b32 s60, 2621440\n\t" \
  "buffer_load_dwordx4 v71, %[Wv], s60 offen lds\n\t" \
  "s_add_u32 m0, s54, 17408\n\t" \
  "s_mov_b32 s60, 2654208\n\t" \
  "buffer_load_dwordx4 v71, %[Wv], s60 offen lds\n\t" \
  "s_add_u32 m0, s54, 18432\n\t" \
  "s_mov_b32 s60, 2686976\n\t" \
  "buffer_load_dwordx4 v71, %[Wv], s60 offen lds\n\t" \
  "s_add_u32 m0, s54, 19456\n\t" \
  "s_mov_b32 s60, 2719744\n\t" \
  "buffer_load_dwordx4 v71, %[Wv], s60 offen lds\n\t" \
  "v_add_u32 v78, 90112, v65\n\t" \
  "s_waitcnt vmcnt(12) lgkmcnt(0)\n\t" \
  "s_barrier\n\t" \
  "ds_read_b64_tr_b16 a[200:201], v78 offset:0\n\t" \
  "ds_read_b64_tr_b16 a[202:203], v78 offset:1024\n\t" \
  "ds_read_b64_tr_b16 a[204:205], v78 offset:8192\n\t" \
  "ds_read_b64_tr_b16 a[206:207], v78 offset:9216\n\t" \
  "ds_read_b64_tr_b16 v[24:25], v78 offset:8\n\t" \
  "ds_read_b64_tr_b16 v[26:27], v78 offset:1032\n\t" \
  "ds_read_b64_tr_b16 v[28:29], v78 offset:8200\n\t" \
  "ds_read_b64_tr_b16 v[30:31], v78 offset:9224\n\t" \
  "s_add_u32 m0, s54, 53248\n\t" \
  "s_mov_b32 s60, 3145728\n\t" \
  "buffer_load_dwordx4 v71, %[Wq], s60 offen lds\n\t" \
  "s_add_u32 m0, s54, 54272\n\t" \
  "s_mov_b32 s60, 3178496\n\t" \
  "buffer_load_dwordx4 v71, %[Wq], s60 offen lds\n\t" \
  "s_add_u32 m0, s54, 55296\n\t" \
  "s_mov_b32 s60, 3211264\n\t" \
  "buffer_load_dwordx4 v71, %[Wq], s60 offen lds\n\t" \
  "s_add_u32 m0, s54, 56320\n\t" \
  "s_mov_b32 s60, 3244032\n\t" \
  "buffer_load_dwordx4 v71, %[Wq], s60 offen lds\n\t" \
  "v_add_u32 v78, 126976, v67\n\t" \
  "s_waitcnt vmcnt(12) lgkmcnt(0)\n\t" \
  "s_barrier\n\t" \
  "ds_read_u16 v80, v78 offset:0\n\t" \
  "ds_read_u16 v81, v78 offset:128\n\t" \
  "ds_read_u16 v82, v78 offset:4096\n\t" \
  "ds_read_u16 v83, v78 offset:4224\n\t" \
  "ds_read_u16 v84, v78 offset:256\n\t" \
  "ds_read_u16 v85, v78 offset:384\n\t" \
  "ds_read_u16 v86, v78 offset:4352\n\t" \
  "ds_read_u16 v87, v78 offset:4480\n\t" \
  "ds_read_u16 v88, v78 offset:1024\n\t" \
  "ds_read_u16 v89, v78 offset:1152\n\t" \
  "ds_read_u16 v90, v78 offset:5120\n\t" \
  "ds_read_u16 v91, v78 offset:5248\n\t" \
  "ds_read_u16 v92, v78 offset:1280\n\t" \
  "ds_read_u16 v93, v78 offset:1408\n\t" \
  "ds_read_u16 v94, v78 offset:5376\n\t" \
  "ds_read_u16 v95, v78 offset:5504\n\t" \
  "ds_read_u16 v96, v78 offset:8192\n\t" \
  "ds_read_u16 v97, v78 offset:8320\n\t" \
  "ds_read_u16 v98, v78 offset:12288\n\t" \
  "ds_read_u16 v99, v78 offset:12416\n\t" \
  "ds_read_u16 v100, v78 offset:8448\n\t" \
  "ds_read_u16 v101, v78 offset:8576\n\t" \
  "ds_read_u16 v102, v78 offset:12544\n\t" \
  "ds_read_u16 v103, v78 offset:12672\n\t" \
  "ds_read_u16 v104, v78 offset:9216\n\t" \
  "ds_read_u16 v105, v78 offset:9344\n\t" \
  "ds_read_u16 v106, v78 offset:13312\n\t" \
  "ds_read_u16 v107, v78 offset:13440\n\t" \
  "ds_read_u16 v108, v78 offset:9472\n\t" \
  "ds_read_u16 v109, v78 offset:9600\n\t" \
  "ds_read_u16 v110, v78 offset:13568\n\t" \
  "ds_read_u16 v111, v78 offset:13696\n\t" \
  "s_add_u32 m0, s54, 90112\n\t" \
  "s_mov_b32 s60, 3145728\n\t" \
  "buffer_load_dwordx4 v71, %[Wk], s60 offen lds\n\t" \
  "s_add_u32 m0, s54, 91136\n\t" \
  "s_mov_b32 s60, 3178496\n\t" \
  "buffer_load_dwordx4 v71, %[Wk], s60 offen lds\n\t" \
  "s_add_u32 m0, s54, 92160\n\t" \
  "s_mov_b32 s60, 3211264\n\t" \
  "buffer_load_dwordx4 v71, %[Wk], s60 offen lds\n\t" \
  "s_add_u32 m0, s54, 93184\n\t" \
  "s_mov_b32 s60, 3244032\n\t" \
  "buffer_load_dwordx4 v71, %[Wk], s60 offen lds\n\t" \
  "s_waitcnt lgkmcnt(0)\n\t" \
  "v_lshl_or_b32 v248, v81, 16, v80\n\t" \
  "v_lshl_or_b32 v249, v83, 16, v82\n\t" \
  "v_lshl_or_b32 v250, v85, 16, v84\n\t" \
  "v_lshl_or_b32 v251, v87, 16, v86\n\t" \
  "v_lshl_or_b32 v252, v89, 16, v88\n\t" \
  "v_lshl_or_b32 v253, v91, 16, v90\n\t" \
  "v_lshl_or_b32 v254, v93, 16, v92\n\t" \
  "v_lshl_or_b32 v255, v95, 16, v94\n\t" \
  "v_lshl_or_b32 v112, v97, 16, v96\n\t" \
  "v_lshl_or_b32 v113, v99, 16, v98\n\t" \
  "v_lshl_or_b32 v114, v101, 16, v100\n\t" \
  "v_lshl_or_b32 v115, v103, 16, v102\n\t" \
  "v_lshl_or_b32 v116, v105, 16, v104\n\t" \
  "v_lshl_or_b32 v117, v107, 16, v106\n\t" \
  "v_lshl_or_b32 v118, v109, 16, v108\n\t" \
  "v_lshl_or_b32 v119, v111, 16, v110\n\t" \
  "ds_write_b128 v70, v[112:115] offset:34816\n\t" \
  "ds_write_b128 v70, v[116:119] offset:35840\n\t" \
  "v_add_u32 v78, 0, v65\n\t" \
  "s_waitcnt vmcnt(12) lgkmcnt(0)\n\t" \
  "s_barrier\n\t" \
  "ds_read_b64_tr_b16 a[208:209], v78 offset:0\n\t" \
  "ds_read_b64_tr_b16 a[210:211], v78 offset:1024\n\t" \
  "ds_read_b64_tr_b16 a[212:213], v78 offset:8192\n\t" \
  "ds_read_b64_tr_b16 a[214:215], v78 offset:9216\n\t" \
  "ds_read_b64_tr_b16 a[216:217], v78 offset:8\n\t" \
  "ds_read_b64_tr_b16 a[218:219], v78 offset:1032\n\t" \
  "ds_read_b64_tr_b16 a[220:221], v78 offset:8200\n\t" \
  "ds_read_b64_tr_b16 a[222:223], v78 offset:9224\n\t" \
  "s_add_u32 m0, s54, 126976\n\t" \
  "s_mov_b32 s60, 3145728\n\t" \
  "buffer_load_dwordx4 v71, %[Wv], s60 offen lds\n\t" \
  "s_add_u32 m0, s54, 128000\n\t" \
  "s_mov_b32 s60, 3178496\n\t" \
  "buffer_load_dwordx4 v71, %[Wv], s60 offen lds\n\t" \
  "s_add_u32 m0, s54, 129024\n\t" \
  "s_mov_b32 s60, 3211264\n\t" \
  "buffer_load_dwordx4 v71, %[Wv], s60 offen lds\n\t" \
  "s_add_u32 m0, s54, 130048\n\t" \
  "s_mov_b32 s60, 3244032\n\t" \
  "buffer_load_dwordx4 v71, %[Wv], s60 offen lds\n\t" \
  "v_add_u32 v78, 16384, v66\n\t" \
  "s_waitcnt vmcnt(12) lgkmcnt(0)\n\t" \
  "s_barrier\n\t" \
  "ds_read_b64_tr_b16 a[224:225], v78 offset:0\n\t" \
  "ds_read_b64_tr_b16 a[226:227], v78 offset:1024\n\t" \
  "ds_read_b64_tr_b16 a[228:229], v78 offset:8192\n\t" \
  "ds_read_b64_tr_b16 a[230:231], v78 offset:9216\n\t" \
  "ds_read_b64_tr_b16 a[232:233], v78 offset:32\n\t" \
  "ds_read_b64_tr_b16 a[234:235], v78 offset:1056\n\t" \
  "ds_read_b64_tr_b16 a[236:237], v78 offset:8224\n\t" \
  "ds_read_b64_tr_b16 a[238:239], v78 offset:9248\n\t" \
  "s_add_u32 m0, s54, 0\n\t" \
  "s_mov_b32 s60, 3670016\n\t" \
  "buffer_load_dwordx4 v71, %[Wq], s60 offen lds\n\t" \
  "s_add_u32 m0, s54, 1024\n\t" \
  "s_mov_b32 s60, 3702784\n\t" \
  "buffer_load_dwordx4 v71, %[Wq], s60 offen lds\n\t" \
  "s_add_u32 m0, s54, 2048\n\t" \
  "s_mov_b32 s60, 3735552\n\t" \
  "buffer_load_dwordx4 v71, %[Wq], s60 offen lds\n\t" \
  "s_add_u32 m0, s54, 3072\n\t" \
  "s_mov_b32 s60, 3768320\n\t" \
  "buffer_load_dwordx4 v71, %[Wq], s60 offen lds\n\t" \
  "v_add_u32 v78, 53248, v65\n\t" \
  "s_waitcnt vmcnt(12) lgkmcnt(0)\n\t" \
  "s_barrier\n\t" \
  "ds_read_b64_tr_b16 a[240:241], v78 offset:0\n\t" \
  "ds_read_b64_tr_b16 a[242:243], v78 offset:1024\n\t" \
  "ds_read_b64_tr_b16 a[244:245], v78 offset:8192\n\t" \
  "ds_read_b64_tr_b16 a[246:247], v78 offset:9216\n\t" \
  "ds_read_b64_tr_b16 a[248:249], v78 offset:8\n\t" \
  "ds_read_b64_tr_b16 a[250:251], v78 offset:1032\n\t" \
  "ds_read_b64_tr_b16 a[252:253], v78 offset:8200\n\t" \
  "ds_read_b64_tr_b16 a[254:255], v78 offset:9224\n\t" \
  "s_add_u32 m0, s54, 16384\n\t" \
  "s_mov_b32 s60, 3670016\n\t" \
  "buffer_load_dwordx4 v71, %[Wk], s60 offen lds\n\t" \
  "s_add_u32 m0, s54, 17408\n\t" \
  "s_mov_b32 s60, 3702784\n\t" \
  "buffer_load_dwordx4 v71, %[Wk], s60 offen lds\n\t" \
  "s_add_u32 m0, s54, 18432\n\t" \
  "s_mov_b32 s60, 3735552\n\t" \
  "buffer_load_dwordx4 v71, %[Wk], s60 offen lds\n\t" \
  "s_add_u32 m0, s54, 19456\n\t" \
  "s_mov_b32 s60, 3768320\n\t" \
  "buffer_load_dwordx4 v71, %[Wk], s60 offen lds\n\t" \
  "v_add_u32 v78, 90112, v65\n\t" \
  "s_waitcnt vmcnt(12) lgkmcnt(0)\n\t" \
  "s_barrier\n\t" \
  "ds_read_b64_tr_b16 v[80:81], v78 offset:0\n\t" \
  "ds_read_b64_tr_b16 v[82:83], v78 offset:1024\n\t" \
  "ds_read_b64_tr_b16 v[84:85], v78 offset:8192\n\t" \
  "ds_read_b64_tr_b16 v[86:87], v78 offset:9216\n\t" \
  "ds_read_b64_tr_b16 v[88:89], v78 offset:8\n\t" \
  "ds_read_b64_tr_b16 v[90:91], v78 offset:1032\n\t" \
  "ds_read_b64_tr_b16 v[92:93], v78 offset:8200\n\t" \
  "ds_read_b64_tr_b16 v[94:95], v78 offset:9224\n\t" \
  "s_add_u32 m0, s54, 53248\n\t" \
  "s_mov_b32 s60, 3670016\n\t" \
  "buffer_load_dwordx4 v71, %[Wv], s60 offen lds\n\t" \
  "s_add_u32 m0, s54, 54272\n\t" \
  "s_mov_b32 s60, 3702784\n\t" \
  "buffer_load_dwordx4 v71, %[Wv], s60 offen lds\n\t" \
  "s_add_u32 m0, s54, 55296\n\t" \
  "s_mov_b32 s60, 3735552\n\t" \
  "buffer_load_dwordx4 v71, %[Wv], s60 offen lds\n\t" \
  "s_add_u32 m0, s54, 56320\n\t" \
  "s_mov_b32 s60, 3768320\n\t" \
  "buffer_load_dwordx4 v71, %[Wv], s60 offen lds\n\t" \
  "v_add_u32 v78, 126976, v66\n\t" \
  "s_waitcnt vmcnt(12) lgkmcnt(0)\n\t" \
  "s_barrier\n\t" \
  "ds_read_b64_tr_b16 v[96:97], v78 offset:0\n\t" \
  "ds_read_b64_tr_b16 v[98:99], v78 offset:1024\n\t" \
  "ds_read_b64_tr_b16 v[100:101], v78 offset:8192\n\t" \
  "ds_read_b64_tr_b16 v[102:103], v78 offset:9216\n\t" \
  "ds_read_b64_tr_b16 v[104:105], v78 offset:32\n\t" \
  "ds_read_b64_tr_b16 v[106:107], v78 offset:1056\n\t" \
  "ds_read_b64_tr_b16 v[108:109], v78 offset:8224\n\t" \
  "ds_read_b64_tr_b16 v[110:111], v78 offset:9248\n\t" \
  "s_add_u32 m0, s54, 90112\n\t" \
  "s_mov_b32 s60, 4194304\n\t" \
  "buffer_load_dwordx4 v71, %[Wq], s60 offen lds\n\t" \
  "s_add_u32 m0, s54, 91136\n\t" \
  "s_mov_b32 s60, 4227072\n\t" \
  "buffer_load_dwordx4 v71, %[Wq], s60 offen lds\n\t" \
  "s_add_u32 m0, s54, 92160\n\t" \
  "s_mov_b32 s60, 4259840\n\t" \
  "buffer_load_dwordx4 v71, %[Wq], s60 offen lds\n\t" \
  "s_add_u32 m0, s54, 93184\n\t" \
  "s_mov_b32 s60, 4292608\n\t" \
  "buffer_load_dwordx4 v71, %[Wq], s60 offen lds\n\t" \
  "v_add_u32 v78, 0, v65\n\t" \
  "s_waitcnt vmcnt(12) lgkmcnt(0)\n\t" \
  "s_barrier\n\t" \
  "ds_read_b64_tr_b16 v[112:113], v78 offset:0\n\t" \
  "ds_read_b64_tr_b16 v[114:115], v78 offset:1024\n\t" \
  "ds_read_b64_tr_b16 v[116:117], v78 offset:8192\n\t" \
  "ds_read_b64_tr_b16 v[118:119], v78 offset:9216\n\t" \
  "ds_read_b64_tr_b16 v[120:121], v78 offset:8\n\t" \
  "ds_read_b64_tr_b16 v[122:123], v78 offset:1032\n\t" \
  "ds_read_b64_tr_b16 v[124:125], v78 offset:8200\n\t" \
  "ds_read_b64_tr_b16 v[126:127], v78 offset:9224\n\t" \
  "s_add_u32 m0, s54, 126976\n\t" \
  "s_mov_b32 s60, 4194304\n\t" \
  "buffer_load_dwordx4 v71, %[Wk], s60 offen lds\n\t" \
  "s_add_u32 m0, s54, 128000\n\t" \
  "s_mov_b32 s60, 4227072\n\t" \
  "buffer_load_dwordx4 v71, %[Wk], s60 offen lds\n\t" \
  "s_add_u32 m0, s54, 129024\n\t" \
  "s_mov_b32 s60, 4259840\n\t" \
  "buffer_load_dwordx4 v71, %[Wk], s60 offen lds\n\t" \
  "s_add_u32 m0, s54, 130048\n\t" \
  "s_mov_b32 s60, 4292608\n\t" \
  "buffer_load_dwordx4 v71, %[Wk], s60 offen lds\n\t" \
  "v_add_u32 v78, 16384, v65\n\t" \
  "s_waitcnt vmcnt(12) lgkmcnt(0)\n\t" \
  "s_barrier\n\t" \
  "ds_read_b64_tr_b16 v[128:129], v78 offset:0\n\t" \
  "ds_read_b64_tr_b16 v[130:131], v78 offset:1024\n\t" \
  "ds_read_b64_tr_b16 v[132:133], v78 offset:8192\n\t" \
  "ds_read_b64_tr_b16 v[134:135], v78 offset:9216\n\t" \
  "ds_read_b64_tr_b16 v[136:137], v78 offset:8\n\t" \
  "ds_read_b64_tr_b16 v[138:139], v78 offset:1032\n\t" \
  "ds_read_b64_tr_b16 v[140:141], v78 offset:8200\n\t" \
  "ds_read_b64_tr_b16 v[142:143], v78 offset:9224\n\t" \
  "s_add_u32 m0, s54, 0\n\t" \
  "s_mov_b32 s60, 4194304\n\t" \
  "buffer_load_dwordx4 v71, %[Wv], s60 offen lds\n\t" \
  "s_add_u32 m0, s54, 1024\n\t" \
  "s_mov_b32 s60, 4227072\n\t" \
  "buffer_load_dwordx4 v71, %[Wv], s60 offen lds\n\t" \
  "s_add_u32 m0, s54, 2048\n\t" \
  "s_mov_b32 s60, 4259840\n\t" \
  "buffer_load_dwordx4 v71, %[Wv], s60 offen lds\n\t" \
  "s_add_u32 m0, s54, 3072\n\t" \
  "s_mov_b32 s60, 4292608\n\t" \
  "buffer_load_dwordx4 v71, %[Wv], s60 offen lds\n\t" \
  "v_add_u32 v78, 53248, v66\n\t" \
  "s_waitcnt vmcnt(12) lgkmcnt(0)\n\t" \
  "s_barrier\n\t" \
  "ds_read_b64_tr_b16 v[144:145], v78 offset:0\n\t" \
  "ds_read_b64_tr_b16 v[146:147], v78 offset:1024\n\t" \
  "ds_read_b64_tr_b16 v[148:149], v78 offset:8192\n\t" \
  "ds_read_b64_tr_b16 v[150:151], v78 offset:9216\n\t" \
  "ds_read_b64_tr_b16 v[152:153], v78 offset:32\n\t" \
  "ds_read_b64_tr_b16 v[154:155], v78 offset:1056\n\t" \
  "ds_read_b64_tr_b16 v[156:157], v78 offset:8224\n\t" \
  "ds_read_b64_tr_b16 v[158:159], v78 offset:9248\n\t" \
  "v_add_u32 v78, 90112, v65\n\t" \
  "s_waitcnt vmcnt(8) lgkmcnt(0)\n\t" \
  "s_barrier\n\t" \
  "ds_read_b64_tr_b16 v[160:161], v78 offset:0\n\t" \
  "ds_read_b64_tr_b16 v[162:163], v78 offset:1024\n\t" \
  "ds_read_b64_tr_b16 v[164:165], v78 offset:8192\n\t" \
  "ds_read_b64_tr_b16 v[166:167], v78 offset:9216\n\t" \
  "ds_read_b64_tr_b16 v[168:169], v78 offset:8\n\t" \
  "ds_read_b64_tr_b16 v[170:171], v78 offset:1032\n\t" \
  "ds_read_b64_tr_b16 v[172:173], v78 offset:8200\n\t" \
  "ds_read_b64_tr_b16 v[174:175], v78 offset:9224\n\t" \
  "v_add_u32 v78, 126976, v65\n\t" \
  "s_waitcnt vmcnt(4) lgkmcnt(0)\n\t" \
  "s_barrier\n\t" \
  "ds_read_b64_tr_b16 v[40:41], v78 offset:0\n\t" \
  "ds_read_b64_tr_b16 v[42:43], v78 offset:1024\n\t" \
  "ds_read_b64_tr_b16 v[44:45], v78 offset:8192\n\t" \
  "ds_read_b64_tr_b16 v[46:47], v78 offset:9216\n\t" \
  "ds_read_b64_tr_b16 v[176:177], v78 offset:8\n\t" \
  "ds_read_b64_tr_b16 v[178:179], v78 offset:1032\n\t" \
  "ds_read_b64_tr_b16 v[180:181], v78 offset:8200\n\t" \
  "ds_read_b64_tr_b16 v[182:183], v78 offset:9224\n\t" \
  "v_add_u32 v78, 0, v66\n\t" \
  "s_waitcnt vmcnt(0) lgkmcnt(0)\n\t" \
  "s_barrier\n\t" \
  "ds_read_b64_tr_b16 v[184:185], v78 offset:0\n\t" \
  "ds_read_b64_tr_b16 v[186:187], v78 offset:1024\n\t" \
  "ds_read_b64_tr_b16 v[188:189], v78 offset:8192\n\t" \
  "ds_read_b64_tr_b16 v[190:191], v78 offset:9216\n\t" \
  "ds_read_b64_tr_b16 v[56:57], v78 offset:32\n\t" \
  "ds_read_b64_tr_b16 v[58:59], v78 offset:1056\n\t" \
  "ds_read_b64_tr_b16 v[60:61], v78 offset:8224\n\t" \
  "ds_read_b64_tr_b16 v[62:63], v78 offset:9248\n\t" \
  CH_STV \
  "s_waitcnt lgkmcnt(0)\n\t" \
  "s_barrier\n\t" \
  "ds_write_b128 v70, v[0:3] offset:0\n\t" \
  "ds_write_b128 v70, v[4:7] offset:1024\n\t" \
  "ds_write_b128 v70, v[8:11] offset:2048\n\t" \
  "ds_write_b128 v70, v[12:15] offset:3072\n\t" \
  "ds_write_b128 v70, v[16:19] offset:4096\n\t" \
  "ds_write_b128 v70, v[20:23] offset:5120\n\t" \
  "ds_write_b128 v70, v[24:27] offset:6144\n\t" \
  "ds_write_b128 v70, v[28:31] offset:7168\n\t" \
  "ds_write_b128 v70, v[32:35] offset:8192\n\t" \
  "ds_write_b128 v70, v[36:39] offset:9216\n\t" \
  "ds_write_b128 v70, v[40:43] offset:10240\n\t" \
  "ds_write_b128 v70, v[44:47] offset:11264\n\t" \
  "ds_write_b128 v70, v[48:51] offset:12288\n\t" \
  "ds_write_b128 v70, v[52:55] offset:13312\n\t" \
  "ds_write_b128 v70, v[56:59] offset:14336\n\t" \
  "ds_write_b128 v70, v[60:63] offset:15360\n\t" \
  "s_waitcnt lgkmcnt(0)\n\t" \
  CH_STW \
  CH_COW \
  "v_and_b32 v72, 1, v72\n\t" \
  "v_and_b32 v74, 1, v74\n\t" \
  "v_cmp_ne_u32 s[90:91], s32, v72\n\t" \
  "v_cmp_ne_u32 vcc, s32, v74\n\t" \
  "s_nop 1\n\t" \
  "s_mov_b32 s96, vcc_lo\n\t" \
  "s_mov_b32 s21, vcc_lo\n\t" \
  "v_and_b32 v73, 32, v69\n\t" \
  "v_lshl_add_u32 v73, v73, 5, v69\n\t" \
  "s_mov_b32 s35, s30\n\t" \
  "s_cmp_lt_u32 s32, " CH_DRYL "\n\t" \
  "s_cbranch_scc1 LINI_%=\n\t" \
  "s_mov_b32 s30, 0\n\t" \
  "s_mov_b32 s34, 0\n\t" \
  "s_cmp_eq_u32 s40, 0\n\t" \
  "s_cbranch_scc1 LW0_%=\n\t" \
  "s_barrier\n\t" \
  "s_cmp_eq_u32 s40, 1\n\t" \
  "s_cbranch_scc1 LG1_%=\n\t" \
  "s_cmp_eq_u32 s40, 2\n\t" \
  "s_cbranch_scc1 LG2_%=\n\t" \
  "s_branch LG3_%=\n\t" \
  "LINI_%=:\n\t" \
  "s_waitcnt vmcnt(0) lgkmcnt(0)\n\t" \
  "s_mov_b32 s30, s35\n\t" \
  "s_movk_i32 s34, 1000\n\t" \
  "s_mov_b32 s48, -1\n\t" \
  "s_add_u32 s97, s41, s53\n\t" \
  "s_add_u32 s98, s37, s53\n\t" \
  "s_add_u32 s82, s48, 0\n\t" \
  "s_and_b32 s82, s82, " CH_TRM "\n\t" \
  CH_SEGC \
  "s_lshr_b64 s[60:61], s[90:91], s82\n\t" \
  "s_and_b32 s60, s60, 1\n\t" \
  "s_lshl_b32 s60, s60, 13\n\t" \
  "s_add_u32 s82, s82, " CH_SG0 "\n\t" \
  "s_lshl_b32 s82, s82, 21\n\t" \
  "s_or_b32 s82, s82, s60\n\t" \
  "s_add_u32 s50, s82, s38\n\t" \
  "s_add_u32 s69, s82, s39\n\t" \
  "s_add_u32 s82, s48, 1\n\t" \
  CH_SEGC \
  "s_lshr_b64 s[60:61], s[90:91], s82\n\t" \
  "s_and_b32 s60, s60, 1\n\t" \
  "s_lshl_b32 s60, s60, 13\n\t" \
  "s_add_u32 s82, s82, " CH_SG0 "\n\t" \
  "s_lshl_b32 s82, s82, 21\n\t" \
  "s_or_b32 s82, s82, s60\n\t" \
  "s_add_u32 s52, s82, s43\n\t" \
  "s_add_u32 s82, s48, " CH_ARMD "\n\t" \
  CH_SEGC \
  "s_lshr_b64 s[60:61], s[90:91], s82\n\t" \
  "s_and_b32 s60, s60, 1\n\t" \
  "s_lshl_b32 s60, s60, 13\n\t" \
  "s_add_u32 s82, s82, " CH_SG0 "\n\t" \
  "s_lshl_b32 s82, s82, 21\n\t" \
  "s_or_b32 s82, s82, s60\n\t" \
  "s_add_u32 s85, s82, s45\n\t" \
  "s_cmp_lt_i32 s48, " CH_ARMS "\n\t" \
  "s_cselect_b32 s85, s27, s85\n\t" \
  "v_and_b32 v37, 0x30, v69\n\t" \
  "v_add_u32 v37, s52, v37\n\t" \
  "s_mov_b32 s48, 0\n\t" \
  "s_mov_b32 s86, 1\n\t" \
  "s_and_b32 s49, s96, 1\n\t" \
  "s_lshl_b32 s49, s49, 13\n\t" \
  "s_add_u32 s53, s49, " CH_SLOT "\n\t" \
  "s_movk_i32 s20, 2560\n\t" \
  "s_movk_i32 s60, 124\n\t" \
  "s_cmp_eq_u32 s40, 3\n\t" \
  "s_cselect_b32 s81, 76, s60\n\t" \
  "s_add_u32 s82, s48, 0\n\t" \
  "s_and_b32 s82, s82, " CH_TRM "\n\t" \
  CH_SEGC \
  "s_lshr_b64 s[60:61], s[90:91], s82\n\t" \
  "s_and_b32 s60, s60, 1\n\t" \
  "s_lshl_b32 s60, s60, 13\n\t" \
  "s_add_u32 s82, s82, " CH_SG0 "\n\t" \
  "s_lshl_b32 s82, s82, 21\n\t" \
  "s_or_b32 s82, s82, s60\n\t" \
  "s_add_u32 s50, s82, s38\n\t" \
  "s_add_u32 s69, s82, s39\n\t" \
  "s_add_u32 s82, s48, 0\n\t" \
  CH_SEGC \
  "s_lshr_b64 s[60:61], s[90:91], s82\n\t" \
  "s_and_b32 s60, s60, 1\n\t" \
  "s_lshl_b32 s60, s60, 13\n\t" \
  "s_add_u32 s82, s82, " CH_SG0 "\n\t" \
  "s_lshl_b32 s82, s82, 21\n\t" \
  "s_or_b32 s82, s82, s60\n\t" \
  "s_add_u32 s51, s82, s43\n\t" \
  "s_add_u32 s97, s41, s53\n\t" \
  "s_add_u32 s98, s37, s53\n\t" \
  "s_add_u32 s82, s48, 0\n\t" \
  "s_and_b32 s82, s82, " CH_TRM "\n\t" \
  CH_SEGC \
  "s_lshr_b64 s[60:61], s[90:91], s82\n\t" \
  "s_and_b32 s60, s60, 1\n\t" \
  "s_lshl_b32 s60, s60, 13\n\t" \
  "s_add_u32 s82, s82, " CH_SG0 "\n\t" \
  "s_lshl_b32 s82, s82, 21\n\t" \
  "s_or_b32 s82, s82, s60\n\t" \
  "s_add_u32 s50, s82, s38\n\t" \
  "s_add_u32 s69, s82, s39\n\t" \
  "s_add_u32 s82, s48, 1\n\t" \
  CH_SEGC \
  "s_lshr_b64 s[60:61], s[90:91], s82\n\t" \
  "s_and_b32 s60, s60, 1\n\t" \
  "s_lshl_b32 s60, s60, 13\n\t" \
  "s_add_u32 s82, s82, " CH_SG0 "\n\t" \
  "s_lshl_b32 s82, s82, 21\n\t" \
  "s_or_b32 s82, s82, s60\n\t" \
  "s_add_u32 s52, s82, s43\n\t" \
  "s_add_u32 s82, s48, " CH_ARMD "\n\t" \
  CH_SEGC \
  "s_lshr_b64 s[60:61], s[90:91], s82\n\t" \
  "s_and_b32 s60, s60, 1\n\t" \
  "s_lshl_b32 s60, s60, 13\n\t" \
  "s_add_u32 s82, s82, " CH_SG0 "\n\t" \
  "s_lshl_b32 s82, s82, 21\n\t" \
  "s_or_b32 s82, s82, s60\n\t" \
  "s_add_u32 s85, s82, s45\n\t" \
  "s_cmp_lt_i32 s48, " CH_ARMS "\n\t" \
  "s_cselect_b32 s85, s27, s85\n\t" \
  "s_memrealtime s[62:63]\n\t" \
  "s_waitcnt lgkmcnt(0)\n\t" \
  "s_mov_b32 s65, s62\n\t" \
  "s_sub_u32 s47, s62, 1000\n\t" \
  "s_mov_b64 vcc, -1\n\t" \
  "s_cmp_eq_u32 s40, 0\n\t" \
  "s_cbranch_scc1 LW0_%=\n\t" \
  "s_barrier\n\t" \
  "s_cmp_eq_u32 s40, 1\n\t" \
  "s_cbranch_scc1 LG1_%=\n\t" \
  "s_cmp_eq_u32 s40, 2\n\t" \
  "s_cbranch_scc1 LG2_%=\n\t" \
  "s_branch LTK_%=\n\t" \
  ".p2align 6\n\t" \
  "LW0_%=:\n\t" \
  "v_lshlrev_b32 v78, 2, v69\n\t" \
  "v_and_b32 v78, 0x300, v78\n\t" \
  "v_lshrrev_b32 v79, 8, v69\n\t" \
  "v_lshl_add_u32 v78, v79, 6, v78\n\t" \
  "v_and_b32 v79, 48, v69\n\t" \
  "v_add_u32 v78, v78, v79\n\t" \
  "s_add_u32 s98, s49, s42\n\t" \
  "s_add_u32 s97, s98, 32768\n\t" \
  "s_add_u32 s99, s98, 65536\n\t" \
  "s_barrier\n\t" \
  "s_memrealtime s[62:63]\n\t" \
  "s_waitcnt lgkmcnt(0)\n\t" \
  "s_add_u32 s62, s62, " CH_DLL2 "\n\t" \
  "s_sub_i32 s63, s62, s65\n\t" \
  "s_cmp_gt_i32 s63, " CH_DLT "\n\t" \
  "s_cselect_b32 s60, " CH_DLY ", " CH_DLX "\n\t" \
  "s_add_u32 s62, s62, s60\n\t" \
  "s_cmp_gt_i32 s63, 0\n\t" \
  "s_cselect_b32 s65, s62, s65\n\t" \
  "s_mov_b32 m0, 0\n\t" \
  "s_movk_i32 s60, 8192\n\t" \
  "LWT_%=:\n\t" \
  "s_memrealtime s[62:63]\n\t" \
  "s_sub_u32 s60, s60, 1\n\t" \
  "s_cselect_b32 s61, 0, -1\n\t" \
  "s_waitcnt lgkmcnt(0)\n\t" \
  "s_sub_i32 s63, s62, s65\n\t" \
  "s_and_b32 s63, s63, s61\n\t" \
  "s_cmp_lt_i32 s63, " CH_FIN "\n\t" \
  "s_cbranch_scc1 LWT_%=\n\t" \
  "s_cmp_lt_i32 s63, 0\n\t" \
  "s_cbranch_scc1 LWT_%=f\n\t" \
  "LWT_%=g:\n\t" \
  "LDMA_%=:\n\t" \
  CH_DPC \
  "buffer_load_dwordx4 v78, s[28:31], s98 offen offset:0 sc1 lds\n\t" \
  "buffer_load_dwordx4 v78, s[28:31], s98 offen offset:1024 sc1 lds\n\t" \
  "buffer_load_dwordx4 v78, s[28:31], s98 offen offset:2048 sc1 lds\n\t" \
  "buffer_load_dwordx4 v78, s[28:31], s98 offen offset:3072 sc1 lds\n\t" \
  "s_movk_i32 m0, 4096\n\t" \
  "s_mov_b32 s66, s62\n\t" \
  CH_HNP \
  "buffer_load_dwordx4 v78, s[28:31], s97 offen offset:0 sc1 lds\n\t" \
  "buffer_load_dwordx4 v78, s[28:31], s97 offen offset:1024 sc1 lds\n\t" \
  "buffer_load_dwordx4 v78, s[28:31], s97 offen offset:2048 sc1 lds\n\t" \
  "buffer_load_dwordx4 v78, s[28:31], s97 offen offset:3072 sc1 lds\n\t" \
  "s_movk_i32 m0, 8192\n\t" \
  "s_nop 0\n\t" \
  "buffer_load_dwordx4 v69, s[28:31], s99 offen sc1 lds\n\t" \
  CH_ST1 \
  ".p2align 6\n\t" \
  "LG0_%=:\n\t" \
  "s_barrier\n\t" \
  "buffer_load_dwordx4 v[52:55], v69, s[28:31], s50 offen sc1\n\t" \
  "buffer_load_dwordx4 v[56:59], v69, s[28:31], s50 offen offset:1024 sc1\n\t" \
  "s_add_u32 s64, s50, 16384\n\t" \
  "s_movk_i32 m0, 11264\n\t" \
  "v_lshlrev_b32 v75, 1, v69\n\t" \
  "buffer_load_dword v75, s[28:31], s64 offen sc1 lds\n\t" \
  "s_memrealtime s[62:63]\n\t" \
  "s_waitcnt lgkmcnt(0)\n\t" \
  "s_sub_u32 s62, s62, " CH_TKL "\n\t" \
  "s_sub_i32 s63, s62, s65\n\t" \
  "s_cmp_gt_i32 s63, 0\n\t" \
  "s_cselect_b32 s62, s62, s65\n\t" \
  "s_sub_i32 s63, s62, s47\n\t" \
  "s_cmp_gt_i32 s63, " CH_LATE "\n\t" \
  "s_cselect_b32 s71, " CH_SCHW ", s46\n\t" \
  "s_add_u32 s61, s47, " CH_LATE "\n\t" \
  "s_cmp_gt_i32 s63, " CH_LATE "\n\t" \
  "s_cselect_b32 s62, s62, s61\n\t" \
  "s_add_u32 s71, s71, s53\n\t" \
  "s_add_u32 s60, s62, s81\n\t" \
  "s_add_u32 s60, s60, " CH_DL1L "\n\t" \
  "s_lshl_b32 s60, s60, 8\n\t" \
  "s_or_b32 s60, s60, " CH_B0 "\n\t" \
  "s_cmp_lt_u32 s48, " CH_ES "\n\t" \
  "s_cselect_b32 s61, " CH_EX ", 0\n\t" \
  "s_add_u32 s60, s60, s61\n\t" \
  "s_not_b32 s60, s60\n\t" \
  "v_mov_b32 v76, s60\n\t" \
  "s_lshr_b32 s60, s46, 31\n\t" \
  "s_or_b32 s60, s60, s68\n\t" \
  "s_cselect_b32 s25, 0, 1\n\t" \
  "s_mov_b64 exec, s[24:25]\n\t" \
  "buffer_atomic_umin v76, v69, s[28:31], s71 offen sc1\n\t" \
  "s_mov_b64 exec, -1\n\t" \
  "ds_read_b128 v[44:47], v70 offset:0\n\t" \
  "ds_read_b128 v[48:51], v70 offset:1024\n\t" \
  "ds_read_b128 v[60:63], v70 offset:2048\n\t" \
  "ds_read_b128 v[64:67], v70 offset:3072\n\t" \
  "v_mov_b64 v[36:37], 0\n\t" \
  "v_mov_b64 v[38:39], 0\n\t" \
  "v_mov_b64 v[40:41], 0\n\t" \
  "v_mov_b64 v[42:43], 0\n\t" \
  "v_lshrrev_b32 v71, 1, v69\n\t" \
  "v_lshrrev_b32 v72, 3, v69\n\t" \
  "v_and_b32 v72, 0x78, v72\n\t" \
  "v_and_b32 v74, 0x30, v69\n\t" \
  "v_lshl_add_u32 v72, v74, 3, v72\n\t" \
  "s_add_u32 s54, s53, s36\n\t" \
  "s_add_u32 s55, s54, 524288\n\t" \
  "s_add_u32 s54, s54, 0\n\t" \
  "s_add_u32 s75, s54, 262144\n\t" \
  "s_add_u32 s80, s53, s26\n\t" \
  "s_or_b32 s87, s53, 0x1fff80\n\t" \
  "s_and_b32 s87, s87, -128\n\t" \
  "s_add_u32 s87, s87, 0x200000\n\t" \
  "s_waitcnt vmcnt(9)\n\t" \
  "s_barrier\n\t" \
  CH_DP0 \
  "ds_read_b128 v[0:3], v69 offset:0\n\t" \
  "ds_read_b128 v[4:7], v69 offset:1024\n\t" \
  "ds_read_b128 v[8:11], v69 offset:2048\n\t" \
  "ds_read_b128 v[12:15], v69 offset:3072\n\t" \
  "s_waitcnt lgkmcnt(3)\n\t" \
  "v_smfmac_f32_16x16x64_bf16 v[36:39], v[0:3], v[44:51], v68\n\t" \
  "ds_read_b128 v[44:47], v70 offset:4096\n\t" \
  "v_smfmac_f32_16x16x64_bf16 v[40:43], v[0:3], v[60:67], v68\n\t" \
  "ds_read_b128 v[48:51], v70 offset:5120\n\t" \
  "s_waitcnt lgkmcnt(4)\n\t" \
  "v_smfmac_f32_16x16x64_bf16 v[36:39], v[4:7], a[32:39], v68\n\t" \
  "ds_read_b128 v[60:63], v70 offset:6144\n\t" \
  "v_smfmac_f32_16x16x64_bf16 v[40:43], v[4:7], a[40:47], v68\n\t" \
  "ds_read_b128 v[64:67], v70 offset:7168\n\t" \
  "s_waitcnt lgkmcnt(5)\n\t" \
  "v_smfmac_f32_16x16x64_bf16 v[36:39], v[8:11], a[80:87], v68\n\t" \
  "s_waitcnt vmcnt(4)\n\t" \
  "s_barrier\n\t" \
  CH_DP1 \
  CH_TG2 \
  "ds_read_b128 v[16:19], v69 offset:4096\n\t" \
  "v_smfmac_f32_16x16x64_bf16 v[40:43], v[8:11], a[88:95], v68\n\t" \
  "ds_read_b128 v[20:23], v69 offset:5120\n\t" \
  "s_waitcnt lgkmcnt(6)\n\t" \
  "v_smfmac_f32_16x16x64_bf16 v[36:39], v[12:15], a[120:127], v68\n\t" \
  "ds_read_b128 v[24:27], v69 offset:6144\n\t" \
  "s_waitcnt lgkmcnt(5)\n\t" \
  "v_smfmac_f32_16x16x64_bf16 v[40:43], v[12:15], v[44:51], v68\n\t" \
  "ds_read_b128 v[28:31], v69 offset:7168\n\t" \
  "s_waitcnt lgkmcnt(3)\n\t" \
  "v_smfmac_f32_16x16x64_bf16 v[36:39], v[16:19], a[160:167], v68\n\t" \
  "ds_read_b128 v[32:35], v69 offset:8192\n\t" \
  "v_smfmac_f32_16x16x64_bf16 v[40:43], v[16:19], a[168:175], v68\n\t" \
  "s_waitcnt lgkmcnt(3)\n\t" \
  "v_mov_b64 v[44:45], 0\n\t" \
  "v_smfmac_f32_16x16x64_bf16 v[36:39], v[20:23], a[200:207], v68\n\t" \
  "s_waitcnt lgkmcnt(2)\n\t" \
  "v_mov_b64 v[46:47], 0\n\t" \
  "v_smfmac_f32_16x16x64_bf16 v[36:39], v[24:27], a[240:247], v68\n\t" \
  "s_waitcnt lgkmcnt(1)\n\t" \
  "v_mov_b64 v[48:49], 0\n\t" \
  "v_smfmac_f32_16x16x64_bf16 v[36:39], v[28:31], v[112:119], v68\n\t" \
  "s_waitcnt lgkmcnt(0)\n\t" \
  "v_mov_b64 v[50:51], 0\n\t" \
  "v_smfmac_f32_16x16x64_bf16 v[36:39], v[32:35], v[160:167], v68\n\t" \
  "v_smfmac_f32_16x16x64_bf16 v[40:43], v[20:23], v[60:67], v68\n\t" \
  "ds_read_b128 v[60:63], v70 offset:8192\n\t" \
  "v_smfmac_f32_16x16x64_bf16 v[40:43], v[24:27], a[248:255], v68\n\t" \
  "ds_read_b128 v[64:67], v70 offset:9216\n\t" \
  "v_add_f32 v74, v36, v38\n\t" \
  "v_smfmac_f32_16x16x64_bf16 v[40:43], v[28:31], v[120:127], v68\n\t" \
  "v_add_f32 v37, v37, v39\n\t" \
  "v_smfmac_f32_16x16x64_bf16 v[40:43], v[32:35], v[168:175], v68\n\t" \
  "v_cmp_u_f32 vcc, v74, v37\n\t" \
  CH_DP2 \
  "v_smfmac_f32_16x16x64_bf16 v[44:47], v[0:3], a[0:7], v68\n\t" \
  "s_cbranch_vccnz LFAIL0_%=\n\t" \
  "LGO0_%=:\n\t" \
  "v_cvt_pk_bf16_f32 v74, v74, v37\n\t" \
  "v_smfmac_f32_16x16x64_bf16 v[44:47], v[4:7], a[48:55], v68\n\t" \
  "s_waitcnt lgkmcnt(0)\n\t" \
  "v_add_f32 v75, v40, v42\n\t" \
  "v_smfmac_f32_16x16x64_bf16 v[44:47], v[8:11], v[60:67], v68\n\t" \
  "ds_read_b128 v[60:63], v70 offset:10240\n\t" \
  "v_add_f32 v41, v41, v43\n\t" \
  "v_cvt_pk_bf16_f32 v75, v75, v41\n\t" \
  "v_smfmac_f32_16x16x64_bf16 v[44:47], v[12:15], a[128:135], v68\n\t" \
  "ds_write2st64_b32 v71, v74, v75 offset0:36 offset1:38\n\t" \
  "v_smfmac_f32_16x16x64_bf16 v[44:47], v[16:19], a[176:183], v68\n\t" \
  "ds_read_b128 v[64:67], v70 offset:11264\n\t" \
  "v_smfmac_f32_16x16x64_bf16 v[44:47], v[20:23], a[208:215], v68\n\t" \
  "v_smfmac_f32_16x16x64_bf16 v[44:47], v[24:27], v[80:87], v68\n\t" \
  "v_smfmac_f32_16x16x64_bf16 v[44:47], v[28:31], v[128:135], v68\n\t" \
  "s_waitcnt lgkmcnt(0)\n\t" \
  "v_smfmac_f32_16x16x64_bf16 v[44:47], v[32:35], v[60:67], v68\n\t" \
  "buffer_load_dwordx4 v[60:63], v69, s[28:31], s64 offen sc1\n\t" \
  "s_barrier\n\t" \
  "ds_read_b64_tr_b16 v[76:77], v72 offset:9216\n\t" \
  CH_DP3 \
  "v_smfmac_f32_16x16x64_bf16 v[48:51], v[0:3], a[8:15], v68\n\t" \
  "ds_read_b64_tr_b16 v[78:79], v72 offset:9728\n\t" \
  "v_smfmac_f32_16x16x64_bf16 v[48:51], v[4:7], a[56:63], v68\n\t" \
  "v_add_f32 v74, v44, v46\n\t" \
  "v_smfmac_f32_16x16x64_bf16 v[48:51], v[8:11], a[96:103], v68\n\t" \
  "v_add_f32 v45, v45, v47\n\t" \
  "v_smfmac_f32_16x16x64_bf16 v[48:51], v[12:15], a[136:143], v68\n\t" \
  "s_waitcnt vmcnt(4)\n\t" \
  "s_waitcnt lgkmcnt(0)\n\t" \
  "v_cvt_pk_bf16_f32 v74, v74, v45\n\t" \
  "v_mfma_f32_16x16x32_bf16 v[36:39], v[52:55], v[76:79], 0\n\t" \
  "buffer_load_dwordx4 v[64:67], v69, s[28:31], s64 offen offset:1024 sc1\n\t" \
  "s_waitcnt vmcnt(4)\n\t" \
  "v_mfma_f32_16x16x32_bf16 v[40:43], v[56:59], v[76:79], 0\n\t" \
  "v_mov_b64 v[52:53], 0\n\t" \
  "v_mov_b64 v[54:55], 0\n\t" \
  "v_smfmac_f32_16x16x64_bf16 v[48:51], v[16:19], a[184:191], v68\n\t" \
  "v_mov_b64 v[56:57], 0\n\t" \
  "v_mov_b64 v[58:59], 0\n\t" \
  "v_smfmac_f32_16x16x64_bf16 v[48:51], v[20:23], a[216:223], v68\n\t" \
  "ds_write_b32 v71, v74 offset:0\n\t" \
  "v_add_f32_dpp v36, v40, v36 quad_perm:[2,3,0,1] row_mask:0xf bank_mask:0xf\n\t" \
  "v_smfmac_f32_16x16x64_bf16 v[48:51], v[24:27], v[88:95], v68\n\t" \
  "v_add_f32_dpp v37, v41, v37 quad_perm:[2,3,0,1] row_mask:0xf bank_mask:0xf\n\t" \
  "v_smfmac_f32_16x16x64_bf16 v[48:51], v[28:31], v[136:143], v68\n\t" \
  "v_add_f32_dpp v38, v42, v38 quad_perm:[2,3,0,1] row_mask:0xf bank_mask:0xf\n\t" \
  "v_smfmac_f32_16x16x64_bf16 v[48:51], v[32:35], v[176:183], v68\n\t" \
  "v_add_f32_dpp v39, v43, v39 quad_perm:[2,3,0,1] row_mask:0xf bank_mask:0xf\n\t" \
  CH_DP4 \
  "v_smfmac_f32_16x16x64_bf16 v[52:55], v[0:3], a[16:23], v68\n\t" \
  "s_mov_b64 exec, s[58:59]\n\t" \
  "ds_write_b128 v69, v[36:39] offset:2048\n\t" \
  "s_mov_b64 exec, -1\n\t" \
  "v_smfmac_f32_16x16x64_bf16 v[52:55], v[4:7], a[64:71], v68\n\t" \
  "v_add_f32 v75, v48, v50\n\t" \
  "v_add_f32 v49, v49, v51\n\t" \
  "v_cvt_pk_bf16_f32 v75, v75, v49\n\t" \
  "v_smfmac_f32_16x16x64_bf16 v[56:59], v[0:3], a[24:31], v68\n\t" \
  "ds_write_b32 v71, v75 offset:512\n\t" \
  "v_smfmac_f32_16x16x64_bf16 v[56:59], v[4:7], a[72:79], v68\n\t" \
  "ds_read_b64_tr_b16 v[0:1], v72 offset:10240\n\t" \
  "ds_read_b64_tr_b16 v[2:3], v72 offset:10752\n\t" \
  "v_smfmac_f32_16x16x64_bf16 v[52:55], v[8:11], a[104:111], v68\n\t" \
  "ds_read_b128 v[36:39], v70 offset:12288\n\t" \
  "v_smfmac_f32_16x16x64_bf16 v[56:59], v[8:11], a[112:119], v68\n\t" \
  "buffer_load_dword off, s[28:31], s87 sc1 lds\n\t" \
  "ds_read_b128 v[40:43], v70 offset:13312\n\t" \
  "v_smfmac_f32_16x16x64_bf16 v[52:55], v[12:15], a[144:151], v68\n\t" \
  "s_waitcnt lgkmcnt(4)\n\t" \
  "s_barrier\n\t" \
  CH_DP5 \
  CH_TG5 \
  "ds_read_b64_tr_b16 v[44:45], v72 offset:0\n\t" \
  "ds_read_b64_tr_b16 v[46:47], v72 offset:512\n\t" \
  "v_smfmac_f32_16x16x64_bf16 v[56:59], v[12:15], a[152:159], v68\n\t" \
  "ds_read_b64_tr_b16 v[48:49], v72 offset:1024\n\t" \
  "ds_read_b64_tr_b16 v[50:51], v72 offset:1536\n\t" \
  "v_smfmac_f32_16x16x64_bf16 v[52:55], v[16:19], a[192:199], v68\n\t" \
  "ds_read_b128 v[4:7], v69 offset:2048\n\t" \
  "s_waitcnt lgkmcnt(5)\n\t" \
  "v_smfmac_f32_16x16x64_bf16 v[56:59], v[16:19], v[36:43], v68\n\t" \
  "ds_read_b128 v[8:11], v69 offset:3072\n\t" \
  "s_waitcnt lgkmcnt(4)\n\t" \
  "v_mfma_f32_16x16x32_bf16 v[12:15], v[44:47], v[76:79], 0\n\t" \
  "ds_read_b128 v[36:39], v70 offset:14336\n\t" \
  "s_waitcnt lgkmcnt(3)\n\t" \
  "v_mfma_f32_16x16x32_bf16 v[12:15], v[48:51], v[0:3], v[12:15]\n\t" \
  "ds_read_b128 v[40:43], v70 offset:15360\n\t" \
  "v_smfmac_f32_16x16x64_bf16 v[52:55], v[20:23], a[224:231], v68\n\t" \
  "s_waitcnt lgkmcnt(2)\n\t" \
  "v_add_f32 v4, v4, v8\n\t" \
  "v_add_f32 v5, v5, v9\n\t" \
  "v_add_f32 v6, v6, v10\n\t" \
  CH_DP6 \
  "v_cndmask_b32 v78, v13, v14, s[56:57]\n\t" \
  "v_cndmask_b32 v76, v12, v15, s[56:57]\n\t" \
  "v_add_f32 v7, v7, v11\n\t" \
  "v_max3_f32 v77, v4, v5, v6\n\t" \
  "v_add_f32_dpp v76, v78, v76 quad_perm:[2,3,0,1] row_mask:0xf bank_mask:0xf\n\t" \
  "v_max3_f32 v77, v77, v7, v76\n\t" \
  "v_smfmac_f32_16x16x64_bf16 v[56:59], v[20:23], a[232:239], v68\n\t" \
  "ds_read_b128 v[44:47], v70 offset:16384\n\t" \
  "v_max_f32_dpp v77, v77, v77 quad_perm:[2,3,0,1] row_mask:0xf bank_mask:0xf\n\t" \
  "v_mov_b32 v78, v77\n\t" \
  "v_smfmac_f32_16x16x64_bf16 v[52:55], v[24:27], v[96:103], v68\n\t" \
  "ds_read_b128 v[48:51], v70 offset:17408\n\t" \
  "v_permlane16_swap_b32 v77, v78\n\t" \
  "v_max_f32 v77, v77, v78\n\t" \
  "v_mov_b32 v78, v77\n\t" \
  "v_smfmac_f32_16x16x64_bf16 v[52:55], v[28:31], v[144:151], v68\n\t" \
  "ds_read_b128 v[16:19], v70 offset:18432\n\t" \
  "v_permlane32_swap_b32 v77, v78\n\t" \
  "v_max_f32 v77, v77, v78\n\t" \
  "v_smfmac_f32_16x16x64_bf16 v[52:55], v[32:35], v[184:191], v68\n\t" \
  "ds_read_b128 v[20:23], v70 offset:19456\n\t" \
  "v_mul_f32 v77, 0xbe0293ee, v77\n\t" \
  CH_DP7 \
  "v_smfmac_f32_16x16x64_bf16 v[56:59], v[24:27], v[104:111], v68\n\t" \
  "v_fmamk_f32 v4, v4, 0x3e0293ee, v77\n\t" \
  "v_smfmac_f32_16x16x64_bf16 v[56:59], v[28:31], v[152:159], v68\n\t" \
  "ds_read_b128 v[24:27], v70 offset:22528\n\t" \
  "s_waitcnt lgkmcnt(4)\n\t" \
  "v_fmamk_f32 v5, v5, 0x3e0293ee, v77\n\t" \
  "v_smfmac_f32_16x16x64_bf16 v[56:59], v[32:35], v[36:43], v68\n\t" \
  "ds_read_b128 v[28:31], v70 offset:23552\n\t" \
  "v_fmamk_f32 v6, v6, 0x3e0293ee, v77\n\t" \
  "v_fmamk_f32 v7, v7, 0x3e0293ee, v77\n\t" \
  "v_fmamk_f32 v76, v76, 0x3e0293ee, v77\n\t" \
  "v_exp_f32 v4, v4\n\t" \
  "v_exp_f32 v5, v5\n\t" \
  "v_exp_f32 v6, v6\n\t" \
  "v_exp_f32 v7, v7\n\t" \
  "v_cvt_pk_bf16_f32 v4, v4, v5\n\t" \
  "v_exp_f32 v76, v76\n\t" \
  "v_cvt_pk_bf16_f32 v5, v6, v7\n\t" \
  "v_mov_b32_dpp v12, v4 quad_perm:[0,1,0,1] row_mask:0xf bank_mask:0xf\n\t" \
  "v_mov_b32_dpp v14, v4 quad_perm:[2,3,2,3] row_mask:0xf bank_mask:0xf\n\t" \
  "v_mov_b32_dpp v13, v5 quad_perm:[0,1,0,1] row_mask:0xf bank_mask:0xf\n\t" \
  "v_mov_b32_dpp v15, v5 quad_perm:[2,3,2,3] row_mask:0xf bank_mask:0xf\n\t" \
  "v_add_f32 v52, v52, v54\n\t" \
  "v_add_f32 v53, v53, v55\n\t" \
  "s_waitcnt vmcnt(2)\n\t" \
  CH_DP8 \
  "v_mfma_f32_16x16x32_bf16 v[0:3], v[60:63], v[12:15], 0\n\t" \
  "ds_read_b128 v[36:39], v70 offset:20480\n\t" \
  "v_add_f32 v56, v56, v58\n\t" \
  "v_add_f32 v57, v57, v59\n\t" \
  "v_cvt_pk_bf16_f32 v52, v52, v53\n\t" \
  "v_cvt_pk_bf16_f32 v53, v56, v57\n\t" \
  "s_waitcnt vmcnt(1)\n\t" \
  "v_mfma_f32_16x16x32_bf16 v[4:7], v[64:67], v[12:15], 0\n\t" \
  "buffer_store_dwordx2 v[52:53], v71, s[28:31], s69 offen offset:2048 sc1\n\t" \
  "ds_read_b128 v[40:43], v70 offset:21504\n\t" \
  "v_mov_b64 v[56:57], s[88:89]\n\t" \
  "v_mov_b64 v[58:59], s[88:89]\n\t" \
  "v_mov_b32_dpp v78, v76 quad_perm:[0,1,0,1] row_mask:0xf bank_mask:0xf\n\t" \
  "v_mov_b32_dpp v79, v76 quad_perm:[2,3,2,3] row_mask:0xf bank_mask:0xf\n\t" \
  "v_mfma_f32_16x16x32_bf16 v[32:35], v[56:59], v[12:15], 0\n\t" \
  "ds_read_b128 v[60:63], v70 offset:24576\n\t" \
  "v_cvt_pk_bf16_f32 v11, v78, v79\n\t" \
  "v_cvt_pk_bf16_f32 v8, v78, v79\n\t" \
  "v_mov_b64 v[54:55], 0\n\t" \
  "v_mov_b32 v9, 0\n\t" \
  "v_mov_b32 v10, 0\n\t" \
  "v_mov_b64 v[14:15], 0\n\t" \
  "s_nop 0\n\t" \
  "v_mfma_f32_16x16x32_bf16 v[32:35], v[54:57], v[8:11], v[32:35]\n\t" \
  "s_cmp_eq_u32 s48, 63\n\t" \
  "v_mfma_f32_16x16x32_bf16 v[0:3], v[52:55], v[8:11], v[0:3]\n\t" \
  "ds_read_b128 v[64:67], v70 offset:25600\n\t" \
  "v_mov_b64 v[56:57], 0\n\t" \
  "v_mov_b64 v[58:59], 0\n\t" \
  "v_mfma_f32_16x16x32_bf16 v[4:7], v[52:55], v[10:13], v[4:7]\n\t" \
  "s_cbranch_scc1 LLAST0_%=\n\t" \
  "v_rcp_f32 v74, v32\n\t" \
  "v_mov_b64 v[12:13], 0\n\t" \
  "v_mov_b32 v8, 0\n\t" \
  "v_pk_mul_f32 v[0:1], v[0:1], v[74:75] op_sel_hi:[1,0]\n\t" \
  "v_pk_mul_f32 v[2:3], v[2:3], v[74:75] op_sel_hi:[1,0]\n\t" \
  "v_cvt_pk_bf16_f32 v76, v0, v1\n\t" \
  "v_cvt_pk_bf16_f32 v77, v2, v3\n\t" \
  "v_pk_mul_f32 v[4:5], v[4:5], v[74:75] op_sel_hi:[1,0]\n\t" \
  "v_pk_mul_f32 v[6:7], v[6:7], v[74:75] op_sel_hi:[1,0]\n\t" \
  "v_cvt_pk_bf16_f32 v78, v4, v5\n\t" \
  "v_cvt_pk_bf16_f32 v79, v6, v7\n\t" \
  "ds_write_b128 v69, v[76:79] offset:4096\n\t" \
  CH_DP9 \
  "s_waitcnt lgkmcnt(2)\n\t" \
  "v_smfmac_f32_16x16x64_bf16 v[12:15], v[76:79], v[44:51], v68\n\t" \
  "ds_read_b128 v[44:47], v70 offset:26624\n\t" \
  "v_mov_b32 v11, 0\n\t" \
  "v_smfmac_f32_16x16x64_bf16 v[56:59], v[76:79], v[16:23], v68\n\t" \
  "ds_read_b128 v[48:51], v70 offset:27648\n\t" \
  "v_mov_b64 v[52:53], 0\n\t" \
  "v_smfmac_f32_16x16x64_bf16 v[8:11], v[76:79], v[36:43], v68\n\t" \
  "ds_read_b128 v[16:19], v70 offset:28672\n\t" \
  "v_smfmac_f32_16x16x64_bf16 v[52:55], v[76:79], v[24:31], v68\n\t" \
  "s_waitcnt lgkmcnt(3)\n\t" \
  "s_barrier\n\t" \
  CH_DP10 \
  "ds_read_b128 v[0:3], v69 offset:5120\n\t" \
  "ds_read_b128 v[4:7], v69 offset:6144\n\t" \
  "v_lshrrev_b32 v71, 7, v69\n\t" \
  "v_and_b32 v74, 0x70, v69\n\t" \
  "v_lshl_add_u32 v71, v71, 14, v74\n\t" \
  "v_bfe_u32 v72, v69, 2, 7\n\t" \
  "v_and_b32 v74, 0x200, v69\n\t" \
  "s_waitcnt lgkmcnt(1)\n\t" \
  "v_smfmac_f32_16x16x64_bf16 v[8:11], v[0:3], v[60:67], v68\n\t" \
  "ds_read_b128 v[20:23], v70 offset:29696\n\t" \
  "v_mov_b64 v[76:77], 0\n\t" \
  "v_smfmac_f32_16x16x64_bf16 v[12:15], v[0:3], v[200:207], v68\n\t" \
  "ds_read_b128 v[32:35], v69 offset:7168\n\t" \
  "v_mov_b64 v[78:79], 0\n\t" \
  "v_smfmac_f32_16x16x64_bf16 v[52:55], v[0:3], v[44:51], v68\n\t" \
  "ds_read_b128 v[24:27], v70 offset:30720\n\t" \
  "v_lshl_add_u32 v72, v74, 3, v72\n\t" \
  "v_smfmac_f32_16x16x64_bf16 v[56:59], v[0:3], v[232:239], v68\n\t" \
  "buffer_load_dword v75, off, s[28:31], s80 sc1\n\t" \
  "ds_read_b128 v[28:31], v70 offset:31744\n\t" \
  "s_waitcnt lgkmcnt(4)\n\t" \
  "v_smfmac_f32_16x16x64_bf16 v[12:15], v[4:7], v[208:215], v68\n\t" \
  "ds_read_b128 v[60:63], v70 offset:32768\n\t" \
  "s_waitcnt lgkmcnt(4)\n\t" \
  "v_smfmac_f32_16x16x64_bf16 v[8:11], v[4:7], v[16:23], v68\n\t" \
  "ds_read_b128 v[64:67], v70 offset:33792\n\t" \
  "s_waitcnt lgkmcnt(4)\n\t" \
  "v_smfmac_f32_16x16x64_bf16 v[8:11], v[32:35], v[192:199], v68\n\t" \
  "ds_read_b128 v[36:39], v73 offset:4096\n\t" \
  "v_smfmac_f32_16x16x64_bf16 v[12:15], v[32:35], v[216:223], v68\n\t" \
  "ds_read_b128 v[40:43], v73 offset:6144\n\t" \
  "s_waitcnt lgkmcnt(4)\n\t" \
  "v_smfmac_f32_16x16x64_bf16 v[52:55], v[4:7], v[24:31], v68\n\t" \
  "ds_read_b128 v[44:47], v70 offset:34816\n\t" \
  "v_cvt_pk_bf16_f32 v8, v8, v9\n\t" \
  "v_smfmac_f32_16x16x64_bf16 v[52:55], v[32:35], v[224:231], v68\n\t" \
  "ds_read_b128 v[48:51], v70 offset:35840\n\t" \
  "v_cvt_pk_bf16_f32 v9, v10, v11\n\t" \
  "v_smfmac_f32_16x16x64_bf16 v[56:59], v[4:7], v[240:247], v68\n\t" \
  "s_waitcnt lgkmcnt(4)\n\t" \
  "v_cvt_pk_bf16_f32 v10, v12, v13\n\t" \
  "v_smfmac_f32_16x16x64_bf16 v[56:59], v[32:35], v[60:67], v68\n\t" \
  "s_waitcnt lgkmcnt(3)\n\t" \
  "v_cvt_pk_bf16_f32 v11, v14, v15\n\t" \
  "v_smfmac_f32_16x16x64_bf16 v[76:79], v[36:39], v[248:255], v68\n\t" \
  "buffer_store_dwordx4 v[8:11], v71, s[28:31], s54 offen sc1\n\t" \
  "s_waitcnt lgkmcnt(0)\n\t" \
  CH_DP11 \
  "v_cvt_pk_bf16_f32 v52, v52, v53\n\t" \
  "v_smfmac_f32_16x16x64_bf16 v[76:79], v[40:43], v[44:51], v68\n\t" \
  "v_cvt_pk_bf16_f32 v53, v54, v55\n\t" \
  "v_cvt_pk_bf16_f32 v54, v56, v57\n\t" \
  "v_cvt_pk_bf16_f32 v55, v58, v59\n\t" \
  "buffer_store_dwordx4 v[52:55], v71, s[28:31], s75 offen sc1\n\t" \
  "s_nop 2\n\t" \
  "v_add_f32 v76, v76, v78\n\t" \
  "v_add_f32 v77, v77, v79\n\t" \
  "v_cvt_pk_bf16_f32 v76, v76, v77\n\t" \
  "buffer_store_dword v76, v72, s[28:31], s55 offen sc1\n\t" \
  CH_DP12 \
  "s_memrealtime s[70:71]\n\t" \
  "s_waitcnt vmcnt(3)\n\t" \
  "v_readfirstlane_b32 s65, v75\n\t" \
  "s_barrier\n\t" \
  "s_waitcnt lgkmcnt(0)\n\t" \
  "s_not_b32 s65, s65\n\t" \
  "s_lshl_b32 s61, s70, 8\n\t" \
  "s_andn2_b32 s64, s65, 0xff\n\t" \
  "s_sub_u32 s64, s64, s61\n\t" \
  "s_ashr_i32 s64, s64, 8\n\t" \
  "s_cmp_eq_u32 s65, 0\n\t" \
  "s_cselect_b32 s64, " CH_DLF ", s64\n\t" \
  "s_cselect_b32 s65, " CH_B0 ", s65\n\t" \
  "s_add_u32 s47, s64, s70\n\t" \
  "s_min_i32 s64, s64, s34\n\t" \
  "s_max_i32 s64, s64, " CH_DLL1 "\n\t" \
  "s_add_u32 s64, s64, s70\n\t" \
  CH_ST2 \
  "s_add_u32 s55, s64, " CH_FDL "\n\t" \
  "s_movk_i32 s60, 8192\n\t" \
  "FWT_%=:\n\t" \
  "s_memrealtime s[62:63]\n\t" \
  "s_sub_u32 s60, s60, 1\n\t" \
  "s_cselect_b32 s61, 0, -1\n\t" \
  "s_waitcnt lgkmcnt(0)\n\t" \
  "s_sub_i32 s63, s62, s55\n\t" \
  "s_and_b32 s63, s63, s61\n\t" \
  "s_cmp_lt_i32 s63, 0\n\t" \
  "s_cbranch_scc1 FWT_%=\n\t" \
  "s_mov_b64 exec, 1\n\t" \
  "buffer_store_dword v68, off, s[28:31], s51 offset:4064 sc1\n\t" \
  "s_mov_b64 exec, -1\n\t" \
  "s_and_b32 s65, s65, 0xff\n\t" \
  "s_add_u32 s47, s47, s65\n\t" \
  "s_add_u32 s65, s65, s64\n\t" \
  "s_sub_u32 s60, s70, s66\n\t" \
  "s_add_u32 s61, s81, 1\n\t" \
  "s_min_u32 s60, s60, s61\n\t" \
  "s_cmp_eq_u32 s68, 0\n\t" \
  "s_cselect_b32 s81, s60, s81\n\t" \
  CH_TRC \
  "s_add_u32 s48, s48, 1\n\t" \
  "s_mov_b32 s49, s53\n\t" \
  "s_add_u32 s53, s53, " CH_SLOT "\n\t" \
  "s_add_u32 s60, s53, " CH_SKIP "\n\t" \
  "s_lshr_b32 s61, s96, 1\n\t" \
  "s_add_u32 s86, s86, 1\n\t" \
  "s_cmp_eq_u32 s86, 3\n\t" \
  "s_cselect_b32 s53, s60, s53\n\t" \
  "s_cselect_b32 s96, s61, s96\n\t" \
  "s_cselect_b32 s86, 0, s86\n\t" \
  "s_cmp_ge_u32 s53, " CH_RING "\n\t" \
  "s_cselect_b32 s53, 0, s53\n\t" \
  "s_cselect_b32 s96, s21, s96\n\t" \
  "s_andn2_b32 s53, s53, 0x2000\n\t" \
  "s_and_b32 s60, s96, 1\n\t" \
  "s_lshl_b32 s60, s60, 13\n\t" \
  "s_or_b32 s53, s53, s60\n\t" \
  "s_mov_b32 s68, 0\n\t" \
  "s_add_u32 s82, s48, 0\n\t" \
  "s_and_b32 s82, s82, " CH_TRM "\n\t" \
  CH_SEGC \
  "s_lshr_b64 s[60:61], s[90:91], s82\n\t" \
  "s_and_b32 s60, s60, 1\n\t" \
  "s_lshl_b32 s60, s60, 13\n\t" \
  "s_add_u32 s82, s82, " CH_SG0 "\n\t" \
  "s_lshl_b32 s82, s82, 21\n\t" \
  "s_or_b32 s82, s82, s60\n\t" \
  "s_add_u32 s50, s82, s38\n\t" \
  "s_add_u32 s69, s82, s39\n\t" \
  "s_add_u32 s82, s48, 0\n\t" \
  CH_SEGC \
  "s_lshr_b64 s[60:61], s[90:91], s82\n\t" \
  "s_and_b32 s60, s60, 1\n\t" \
  "s_lshl_b32 s60, s60, 13\n\t" \
  "s_add_u32 s82, s82, " CH_SG0 "\n\t" \
  "s_lshl_b32 s82, s82, 21\n\t" \
  "s_or_b32 s82, s82, s60\n\t" \
  "s_add_u32 s51, s82, s43\n\t" \
  "s_cmp_lg_u32 s34, 0\n\t" \
  "s_cbranch_scc1 LW0_%=\n\t" \
  "s_barrier\n\t" \
  "s_branch LINI_%=\n\t" \
  "LFAIL0_%=:\n\t" \
  "v_max_u32 v75, v0, v1\n\t" \
  "v_max3_u32 v75, v75, v2, v3\n\t" \
  "v_max3_u32 v75, v75, v4, v5\n\t" \
  "v_max3_u32 v75, v75, v6, v7\n\t" \
  "v_max3_u32 v75, v75, v8, v9\n\t" \
  "v_max3_u32 v75, v75, v10, v11\n\t" \
  "v_max3_u32 v75, v75, v12, v13\n\t" \
  "v_max3_u32 v75, v75, v14, v15\n\t" \
  "v_max3_u32 v75, v75, v16, v17\n\t" \
  "v_max3_u32 v75, v75, v18, v19\n\t" \
  "v_max3_u32 v75, v75, v20, v21\n\t" \
  "v_max3_u32 v75, v75, v22, v23\n\t" \
  "v_max3_u32 v75, v75, v24, v25\n\t" \
  "v_max3_u32 v75, v75, v26, v27\n\t" \
  "v_max3_u32 v75, v75, v28, v29\n\t" \
  "v_max3_u32 v75, v75, v30, v31\n\t" \
  "v_max3_u32 v75, v75, v32, v33\n\t" \
  "v_max3_u32 v75, v75, v34, v35\n\t" \
  "v_cmp_eq_u32 vcc, -1, v75\n\t" \
  "s_cbranch_vccz LGO0_%=\n\t" \
  "s_add_u32 s68, s68, 1\n\t" \
  "s_waitcnt lgkmcnt(0)\n\t" \
  "s_cmp_gt_u32 s68, s83\n\t" \
  "s_cbranch_scc1 LGV0_%=\n\t" \
  "s_barrier\n\t" \
  "s_memrealtime s[62:63]\n\t" \
  "s_mov_b32 m0, 0\n\t" \
  "s_waitcnt lgkmcnt(0)\n\t" \
  "s_add_u32 s65, s62, " CH_PEN "\n\t" \
  "s_branch LDMA_%=\n\t" \
  "LWT_%=f:\n\t" \
  "s_cmp_lt_i32 s63, -2\n\t" \
  "s_cbranch_scc0 LWT_%=g\n\t" \
  "s_nop 4\n\t" \
  "s_branch LWT_%=g\n\t" \
  "LGV0_%=:\n\t" \
  "s_mov_b32 s83, 0\n\t" \
  "s_lshl_b32 s60, s36, 4\n\t" \
  "s_or_b32 s60, s60, s48\n\t" \
  "s_or_b32 s60, s60, 805306368\n\t" \
  "v_mov_b32 v76, s60\n\t" \
  "v_mov_b32 v77, 0\n\t" \
  "s_mov_b64 exec, 1\n\t" \
  "buffer_atomic_cmpswap v[76:77], off, s[28:31], s92 sc1\n\t" \
  "s_mov_b64 exec, -1\n\t" \
  "s_branch LGO0_%=\n\t" \
  "LLAST0_%=:\n\t" \
  "s_waitcnt vmcnt(0) lgkmcnt(0)\n\t" \
  "s_barrier\n\t" \
  "s_mov_b64 exec, 1\n\t" \
  "buffer_store_dword v68, off, s[28:31], s51 offset:4064 sc1\n\t" \
  "s_mov_b64 exec, -1\n\t" \
  "s_branch LEND_%=\n\t" \
  ".p2align 6\n\t" \
  "LG1_%=:\n\t" \
  "s_barrier\n\t" \
  "buffer_load_dwordx4 v[52:55], v69, s[28:31], s50 offen sc1\n\t" \
  "buffer_load_dwordx4 v[56:59], v69, s[28:31], s50 offen offset:1024 sc1\n\t" \
  "s_add_u32 s64, s50, 14336\n\t" \
  "s_movk_i32 m0, 11264\n\t" \
  "v_lshlrev_b32 v75, 1, v69\n\t" \
  "buffer_load_dword v75, s[28:31], s64 offen sc1 lds\n\t" \
  "ds_read_b128 v[44:47], v70 offset:0\n\t" \
  "ds_read_b128 v[48:51], v70 offset:1024\n\t" \
  "ds_read_b128 v[60:63], v70 offset:2048\n\t" \
  "ds_read_b128 v[64:67], v70 offset:3072\n\t" \
  "v_mov_b64 v[36:37], 0\n\t" \
  "v_mov_b64 v[38:39], 0\n\t" \
  "v_mov_b64 v[40:41], 0\n\t" \
  "v_mov_b64 v[42:43], 0\n\t" \
  "v_lshrrev_b32 v71, 1, v69\n\t" \
  "v_add_u32 v71, 4, v71\n\t" \
  "v_lshrrev_b32 v72, 3, v69\n\t" \
  "v_and_b32 v72, 0x78, v72\n\t" \
  "v_and_b32 v74, 0x30, v69\n\t" \
  "v_lshl_add_u32 v72, v74, 3, v72\n\t" \
  "s_add_u32 s54, s53, s36\n\t" \
  "s_add_u32 s55, s54, 540672\n\t" \
  "s_add_u32 s54, s54, 4096\n\t" \
  "s_add_u32 s75, s54, 262144\n\t" \
  "s_add_u32 s80, s53, s26\n\t" \
  "s_barrier\n\t" \
  CH_DP0 \
  "ds_read_b128 v[0:3], v69 offset:0\n\t" \
  "ds_read_b128 v[4:7], v69 offset:1024\n\t" \
  "ds_read_b128 v[8:11], v69 offset:2048\n\t" \
  "ds_read_b128 v[12:15], v69 offset:3072\n\t" \
  "s_waitcnt lgkmcnt(3)\n\t" \
  "v_smfmac_f32_16x16x64_bf16 v[36:39], v[0:3], v[44:51], v68\n\t" \
  "ds_read_b128 v[44:47], v70 offset:4096\n\t" \
  "v_smfmac_f32_16x16x64_bf16 v[40:43], v[0:3], v[60:67], v68\n\t" \
  "ds_read_b128 v[48:51], v70 offset:5120\n\t" \
  "s_waitcnt lgkmcnt(4)\n\t" \
  "v_smfmac_f32_16x16x64_bf16 v[36:39], v[4:7], a[32:39], v68\n\t" \
  "ds_read_b128 v[60:63], v70 offset:6144\n\t" \
  "v_smfmac_f32_16x16x64_bf16 v[40:43], v[4:7], a[40:47], v68\n\t" \
  "ds_read_b128 v[64:67], v70 offset:7168\n\t" \
  "s_waitcnt lgkmcnt(5)\n\t" \
  "v_smfmac_f32_16x16x64_bf16 v[36:39], v[8:11], a[80:87], v68\n\t" \
  "s_barrier\n\t" \
  CH_DP1 \
  "ds_read_b128 v[16:19], v69 offset:4096\n\t" \
  "v_smfmac_f32_16x16x64_bf16 v[40:43], v[8:11], a[88:95], v68\n\t" \
  "ds_read_b128 v[20:23], v69 offset:5120\n\t" \
  "s_waitcnt lgkmcnt(6)\n\t" \
  "v_smfmac_f32_16x16x64_bf16 v[36:39], v[12:15], a[120:127], v68\n\t" \
  "ds_read_b128 v[24:27], v69 offset:6144\n\t" \
  "s_waitcnt lgkmcnt(5)\n\t" \
  "v_smfmac_f32_16x16x64_bf16 v[40:43], v[12:15], v[44:51], v68\n\t" \
  "ds_read_b128 v[28:31], v69 offset:7168\n\t" \
  "s_waitcnt lgkmcnt(3)\n\t" \
  "v_smfmac_f32_16x16x64_bf16 v[36:39], v[16:19], a[160:167], v68\n\t" \
  "ds_read_b128 v[32:35], v69 offset:8192\n\t" \
  "v_smfmac_f32_16x16x64_bf16 v[40:43], v[16:19], a[168:175], v68\n\t" \
  "s_waitcnt lgkmcnt(3)\n\t" \
  "v_mov_b64 v[44:45], 0\n\t" \
  "v_smfmac_f32_16x16x64_bf16 v[36:39], v[20:23], a[200:207], v68\n\t" \
  "s_waitcnt lgkmcnt(2)\n\t" \
  "v_mov_b64 v[46:47], 0\n\t" \
  "v_smfmac_f32_16x16x64_bf16 v[36:39], v[24:27], a[240:247], v68\n\t" \
  "s_waitcnt lgkmcnt(1)\n\t" \
  "v_mov_b64 v[48:49], 0\n\t" \
  "v_smfmac_f32_16x16x64_bf16 v[36:39], v[28:31], v[112:119], v68\n\t" \
  "s_waitcnt lgkmcnt(0)\n\t" \
  "v_mov_b64 v[50:51], 0\n\t" \
  "v_smfmac_f32_16x16x64_bf16 v[36:39], v[32:35], v[160:167], v68\n\t" \
  "v_smfmac_f32_16x16x64_bf16 v[40:43], v[20:23], v[60:67], v68\n\t" \
  "ds_read_b128 v[60:63], v70 offset:8192\n\t" \
  "v_smfmac_f32_16x16x64_bf16 v[40:43], v[24:27], a[248:255], v68\n\t" \
  "ds_read_b128 v[64:67], v70 offset:9216\n\t" \
  "v_add_f32 v74, v36, v38\n\t" \
  "v_smfmac_f32_16x16x64_bf16 v[40:43], v[28:31], v[120:127], v68\n\t" \
  "v_add_f32 v37, v37, v39\n\t" \
  "v_smfmac_f32_16x16x64_bf16 v[40:43], v[32:35], v[168:175], v68\n\t" \
  "v_cmp_u_f32 vcc, v74, v37\n\t" \
  CH_DP2 \
  "v_smfmac_f32_16x16x64_bf16 v[44:47], v[0:3], a[0:7], v68\n\t" \
  "s_cbranch_vccnz LFAIL1_%=\n\t" \
  "LGO1_%=:\n\t" \
  "v_cvt_pk_bf16_f32 v74, v74, v37\n\t" \
  "v_smfmac_f32_16x16x64_bf16 v[44:47], v[4:7], a[48:55], v68\n\t" \
  "s_waitcnt lgkmcnt(0)\n\t" \
  "v_add_f32 v75, v40, v42\n\t" \
  "v_smfmac_f32_16x16x64_bf16 v[44:47], v[8:11], v[60:67], v68\n\t" \
  "ds_read_b128 v[60:63], v70 offset:10240\n\t" \
  "v_add_f32 v41, v41, v43\n\t" \
  "v_cvt_pk_bf16_f32 v75, v75, v41\n\t" \
  "v_smfmac_f32_16x16x64_bf16 v[44:47], v[12:15], a[128:135], v68\n\t" \
  "ds_write2st64_b32 v71, v74, v75 offset0:36 offset1:38\n\t" \
  "v_smfmac_f32_16x16x64_bf16 v[44:47], v[16:19], a[176:183], v68\n\t" \
  "ds_read_b128 v[64:67], v70 offset:11264\n\t" \
  "v_smfmac_f32_16x16x64_bf16 v[44:47], v[20:23], a[208:215], v68\n\t" \
  "v_smfmac_f32_16x16x64_bf16 v[44:47], v[24:27], v[80:87], v68\n\t" \
  "v_smfmac_f32_16x16x64_bf16 v[44:47], v[28:31], v[128:135], v68\n\t" \
  "s_waitcnt lgkmcnt(0)\n\t" \
  "v_smfmac_f32_16x16x64_bf16 v[44:47], v[32:35], v[60:67], v68\n\t" \
  "s_barrier\n\t" \
  "ds_read_b64_tr_b16 v[76:77], v72 offset:9216\n\t" \
  CH_DP3 \
  "v_smfmac_f32_16x16x64_bf16 v[48:51], v[0:3], a[8:15], v68\n\t" \
  "buffer_load_dwordx4 v[60:63], v69, s[28:31], s64 offen sc1\n\t" \
  "ds_read_b64_tr_b16 v[78:79], v72 offset:9728\n\t" \
  "v_smfmac_f32_16x16x64_bf16 v[48:51], v[4:7], a[56:63], v68\n\t" \
  "v_add_f32 v74, v44, v46\n\t" \
  "v_smfmac_f32_16x16x64_bf16 v[48:51], v[8:11], a[96:103], v68\n\t" \
  "v_add_f32 v45, v45, v47\n\t" \
  "v_smfmac_f32_16x16x64_bf16 v[48:51], v[12:15], a[136:143], v68\n\t" \
  "s_waitcnt vmcnt(3)\n\t" \
  "s_waitcnt lgkmcnt(0)\n\t" \
  "v_cvt_pk_bf16_f32 v74, v74, v45\n\t" \
  "v_mfma_f32_16x16x32_bf16 v[36:39], v[52:55], v[76:79], 0\n\t" \
  "s_waitcnt vmcnt(2)\n\t" \
  "v_mfma_f32_16x16x32_bf16 v[40:43], v[56:59], v[76:79], 0\n\t" \
  "buffer_load_dwordx4 v[64:67], v69, s[28:31], s64 offen offset:1024 sc1\n\t" \
  "v_mov_b64 v[52:53], 0\n\t" \
  "v_mov_b64 v[54:55], 0\n\t" \
  "v_smfmac_f32_16x16x64_bf16 v[48:51], v[16:19], a[184:191], v68\n\t" \
  "v_mov_b64 v[56:57], 0\n\t" \
  "v_mov_b64 v[58:59], 0\n\t" \
  "v_smfmac_f32_16x16x64_bf16 v[48:51], v[20:23], a[216:223], v68\n\t" \
  "ds_write_b32 v71, v74 offset:0\n\t" \
  "v_add_f32_dpp v36, v40, v36 quad_perm:[2,3,0,1] row_mask:0xf bank_mask:0xf\n\t" \
  "v_smfmac_f32_16x16x64_bf16 v[48:51], v[24:27], v[88:95], v68\n\t" \
  "v_add_f32_dpp v37, v41, v37 quad_perm:[2,3,0,1] row_mask:0xf bank_mask:0xf\n\t" \
  "v_smfmac_f32_16x16x64_bf16 v[48:51], v[28:31], v[136:143], v68\n\t" \
  "v_add_f32_dpp v38, v42, v38 quad_perm:[2,3,0,1] row_mask:0xf bank_mask:0xf\n\t" \
  "v_smfmac_f32_16x16x64_bf16 v[48:51], v[32:35], v[176:183], v68\n\t" \
  "v_add_f32_dpp v39, v43, v39 quad_perm:[2,3,0,1] row_mask:0xf bank_mask:0xf\n\t" \
  CH_DP4 \
  "v_smfmac_f32_16x16x64_bf16 v[52:55], v[0:3], a[16:23], v68\n\t" \
  "s_mov_b64 exec, s[58:59]\n\t" \
  "ds_write_b128 v69, v[36:39] offset:2080\n\t" \
  "s_mov_b64 exec, -1\n\t" \
  "v_smfmac_f32_16x16x64_bf16 v[52:55], v[4:7], a[64:71], v68\n\t" \
  "v_add_f32 v75, v48, v50\n\t" \
  "v_add_f32 v49, v49, v51\n\t" \
  "v_cvt_pk_bf16_f32 v75, v75, v49\n\t" \
  "v_smfmac_f32_16x16x64_bf16 v[56:59], v[0:3], a[24:31], v68\n\t" \
  "ds_write_b32 v71, v75 offset:512\n\t" \
  "v_smfmac_f32_16x16x64_bf16 v[56:59], v[4:7], a[72:79], v68\n\t" \
  "ds_read_b64_tr_b16 v[0:1], v72 offset:10240\n\t" \
  "ds_read_b64_tr_b16 v[2:3], v72 offset:10752\n\t" \
  "v_smfmac_f32_16x16x64_bf16 v[52:55], v[8:11], a[104:111], v68\n\t" \
  "ds_read_b128 v[36:39], v70 offset:12288\n\t" \
  "v_smfmac_f32_16x16x64_bf16 v[56:59], v[8:11], a[112:119], v68\n\t" \
  "ds_read_b128 v[40:43], v70 offset:13312\n\t" \
  "v_smfmac_f32_16x16x64_bf16 v[52:55], v[12:15], a[144:151], v68\n\t" \
  "s_waitcnt lgkmcnt(4)\n\t" \
  "s_barrier\n\t" \
  CH_DP5 \
  "ds_read_b64_tr_b16 v[44:45], v72 offset:0\n\t" \
  "ds_read_b64_tr_b16 v[46:47], v72 offset:512\n\t" \
  "v_smfmac_f32_16x16x64_bf16 v[56:59], v[12:15], a[152:159], v68\n\t" \
  "ds_read_b64_tr_b16 v[48:49], v72 offset:1024\n\t" \
  "ds_read_b64_tr_b16 v[50:51], v72 offset:1536\n\t" \
  "v_smfmac_f32_16x16x64_bf16 v[52:55], v[16:19], a[192:199], v68\n\t" \
  "ds_read_b128 v[4:7], v69 offset:2048\n\t" \
  "s_waitcnt lgkmcnt(5)\n\t" \
  "v_smfmac_f32_16x16x64_bf16 v[56:59], v[16:19], v[36:43], v68\n\t" \
  "ds_read_b128 v[8:11], v69 offset:3072\n\t" \
  "s_waitcnt lgkmcnt(4)\n\t" \
  "v_mfma_f32_16x16x32_bf16 v[12:15], v[44:47], v[76:79], 0\n\t" \
  "ds_read_b128 v[36:39], v70 offset:14336\n\t" \
  "s_waitcnt lgkmcnt(3)\n\t" \
  "v_mfma_f32_16x16x32_bf16 v[12:15], v[48:51], v[0:3], v[12:15]\n\t" \
  "buffer_store_dwordx4 v[44:47], v69, s[28:31], s69 offen offset:0 sc1\n\t" \
  "ds_read_b128 v[40:43], v70 offset:15360\n\t" \
  "v_smfmac_f32_16x16x64_bf16 v[52:55], v[20:23], a[224:231], v68\n\t" \
  "s_waitcnt lgkmcnt(2)\n\t" \
  "v_add_f32 v4, v4, v8\n\t" \
  "v_add_f32 v5, v5, v9\n\t" \
  "v_add_f32 v6, v6, v10\n\t" \
  CH_DP6 \
  "v_cndmask_b32 v78, v13, v14, s[56:57]\n\t" \
  "v_cndmask_b32 v76, v12, v15, s[56:57]\n\t" \
  "v_add_f32 v7, v7, v11\n\t" \
  "v_max3_f32 v77, v4, v5, v6\n\t" \
  "v_add_f32_dpp v76, v78, v76 quad_perm:[2,3,0,1] row_mask:0xf bank_mask:0xf\n\t" \
  "v_max3_f32 v77, v77, v7, v76\n\t" \
  "v_smfmac_f32_16x16x64_bf16 v[56:59], v[20:23], a[232:239], v68\n\t" \
  "ds_read_b128 v[44:47], v70 offset:16384\n\t" \
  "v_max_f32_dpp v77, v77, v77 quad_perm:[2,3,0,1] row_mask:0xf bank_mask:0xf\n\t" \
  "v_mov_b32 v78, v77\n\t" \
  "v_smfmac_f32_16x16x64_bf16 v[52:55], v[24:27], v[96:103], v68\n\t" \
  "ds_read_b128 v[48:51], v70 offset:17408\n\t" \
  "v_permlane16_swap_b32 v77, v78\n\t" \
  "v_max_f32 v77, v77, v78\n\t" \
  "v_mov_b32 v78, v77\n\t" \
  "v_smfmac_f32_16x16x64_bf16 v[52:55], v[28:31], v[144:151], v68\n\t" \
  "ds_read_b128 v[16:19], v70 offset:18432\n\t" \
  "v_permlane32_swap_b32 v77, v78\n\t" \
  "v_max_f32 v77, v77, v78\n\t" \
  "v_smfmac_f32_16x16x64_bf16 v[52:55], v[32:35], v[184:191], v68\n\t" \
  "ds_read_b128 v[20:23], v70 offset:19456\n\t" \
  "v_mul_f32 v77, 0xbe0293ee, v77\n\t" \
  CH_DP7 \
  "v_smfmac_f32_16x16x64_bf16 v[56:59], v[24:27], v[104:111], v68\n\t" \
  "v_fmamk_f32 v4, v4, 0x3e0293ee, v77\n\t" \
  "v_smfmac_f32_16x16x64_bf16 v[56:59], v[28:31], v[152:159], v68\n\t" \
  "ds_read_b128 v[24:27], v70 offset:22528\n\t" \
  "s_waitcnt lgkmcnt(4)\n\t" \
  "v_fmamk_f32 v5, v5, 0x3e0293ee, v77\n\t" \
  "v_smfmac_f32_16x16x64_bf16 v[56:59], v[32:35], v[36:43], v68\n\t" \
  "ds_read_b128 v[28:31], v70 offset:23552\n\t" \
  "v_fmamk_f32 v6, v6, 0x3e0293ee, v77\n\t" \
  "v_fmamk_f32 v7, v7, 0x3e0293ee, v77\n\t" \
  "v_fmamk_f32 v76, v76, 0x3e0293ee, v77\n\t" \
  "v_exp_f32 v4, v4\n\t" \
  "v_exp_f32 v5, v5\n\t" \
  "v_exp_f32 v6, v6\n\t" \
  "v_exp_f32 v7, v7\n\t" \
  "v_cvt_pk_bf16_f32 v4, v4, v5\n\t" \
  "v_exp_f32 v76, v76\n\t" \
  "v_cvt_pk_bf16_f32 v5, v6, v7\n\t" \
  "v_mov_b32_dpp v12, v4 quad_perm:[0,1,0,1] row_mask:0xf bank_mask:0xf\n\t" \
  "v_mov_b32_dpp v14, v4 quad_perm:[2,3,2,3] row_mask:0xf bank_mask:0xf\n\t" \
  "v_mov_b32_dpp v13, v5 quad_perm:[0,1,0,1] row_mask:0xf bank_mask:0xf\n\t" \
  "v_mov_b32_dpp v15, v5 quad_perm:[2,3,2,3] row_mask:0xf bank_mask:0xf\n\t" \
  "v_add_f32 v52, v52, v54\n\t" \
  "v_add_f32 v53, v53, v55\n\t" \
  "s_waitcnt vmcnt(2)\n\t" \
  CH_DP8 \
  "v_mfma_f32_16x16x32_bf16 v[0:3], v[60:63], v[12:15], 0\n\t" \
  "ds_read_b128 v[36:39], v70 offset:20480\n\t" \
  "v_add_f32 v56, v56, v58\n\t" \
  "v_add_f32 v57, v57, v59\n\t" \
  "v_cvt_pk_bf16_f32 v52, v52, v53\n\t" \
  "v_cvt_pk_bf16_f32 v53, v56, v57\n\t" \
  "s_waitcnt vmcnt(1)\n\t" \
  "v_mfma_f32_16x16x32_bf16 v[4:7], v[64:67], v[12:15], 0\n\t" \
  "ds_read_b128 v[40:43], v70 offset:21504\n\t" \
  "v_mov_b64 v[56:57], s[88:89]\n\t" \
  "v_mov_b64 v[58:59], s[88:89]\n\t" \
  "v_mov_b32_dpp v78, v76 quad_perm:[0,1,0,1] row_mask:0xf bank_mask:0xf\n\t" \
  "v_mov_b32_dpp v79, v76 quad_perm:[2,3,2,3] row_mask:0xf bank_mask:0xf\n\t" \
  "v_mfma_f32_16x16x32_bf16 v[32:35], v[56:59], v[12:15], 0\n\t" \
  "buffer_store_dwordx2 v[52:53], v71, s[28:31], s69 offen offset:2556 sc1\n\t" \
  "ds_read_b128 v[60:63], v70 offset:24576\n\t" \
  "v_cvt_pk_bf16_f32 v11, v78, v79\n\t" \
  "v_cvt_pk_bf16_f32 v8, v78, v79\n\t" \
  "v_mov_b64 v[54:55], 0\n\t" \
  "v_mov_b32 v9, 0\n\t" \
  "v_mov_b32 v10, 0\n\t" \
  "v_mov_b64 v[14:15], 0\n\t" \
  "s_nop 0\n\t" \
  "v_mfma_f32_16x16x32_bf16 v[32:35], v[54:57], v[8:11], v[32:35]\n\t" \
  "s_cmp_eq_u32 s48, 63\n\t" \
  "v_mfma_f32_16x16x32_bf16 v[0:3], v[52:55], v[8:11], v[0:3]\n\t" \
  "ds_read_b128 v[64:67], v70 offset:25600\n\t" \
  "v_mov_b64 v[56:57], 0\n\t" \
  "v_mov_b64 v[58:59], 0\n\t" \
  "v_mfma_f32_16x16x32_bf16 v[4:7], v[52:55], v[10:13], v[4:7]\n\t" \
  "s_cbranch_scc1 LLAST1_%=\n\t" \
  "v_rcp_f32 v74, v32\n\t" \
  "v_mov_b64 v[12:13], 0\n\t" \
  "v_mov_b32 v8, 0\n\t" \
  "v_pk_mul_f32 v[0:1], v[0:1], v[74:75] op_sel_hi:[1,0]\n\t" \
  "v_pk_mul_f32 v[2:3], v[2:3], v[74:75] op_sel_hi:[1,0]\n\t" \
  "v_cvt_pk_bf16_f32 v76, v0, v1\n\t" \
  "v_cvt_pk_bf16_f32 v77, v2, v3\n\t" \
  "v_pk_mul_f32 v[4:5], v[4:5], v[74:75] op_sel_hi:[1,0]\n\t" \
  "v_pk_mul_f32 v[6:7], v[6:7], v[74:75] op_sel_hi:[1,0]\n\t" \
  "v_cvt_pk_bf16_f32 v78, v4, v5\n\t" \
  "v_cvt_pk_bf16_f32 v79, v6, v7\n\t" \
  "ds_write_b128 v69, v[76:79] offset:5120\n\t" \
  CH_DP9 \
  "s_waitcnt lgkmcnt(2)\n\t" \
  "v_smfmac_f32_16x16x64_bf16 v[12:15], v[76:79], v[44:51], v68\n\t" \
  "ds_read_b128 v[44:47], v70 offset:26624\n\t" \
  "v_mov_b32 v11, 0\n\t" \
  "v_smfmac_f32_16x16x64_bf16 v[56:59], v[76:79], v[16:23], v68\n\t" \
  "ds_read_b128 v[48:51], v70 offset:27648\n\t" \
  "v_mov_b64 v[52:53], 0\n\t" \
  "v_smfmac_f32_16x16x64_bf16 v[8:11], v[76:79], v[36:43], v68\n\t" \
  "ds_read_b128 v[16:19], v70 offset:28672\n\t" \
  "v_smfmac_f32_16x16x64_bf16 v[52:55], v[76:79], v[24:31], v68\n\t" \
  "s_waitcnt lgkmcnt(3)\n\t" \
  "s_barrier\n\t" \
  CH_DP10 \
  "ds_read_b128 v[0:3], v69 offset:4096\n\t" \
  "ds_read_b128 v[4:7], v69 offset:7168\n\t" \
  "v_lshrrev_b32 v71, 7, v69\n\t" \
  "v_and_b32 v74, 0x70, v69\n\t" \
  "v_lshl_add_u32 v71, v71, 14, v74\n\t" \
  "v_bfe_u32 v72, v69, 2, 7\n\t" \
  "v_and_b32 v74, 0x200, v69\n\t" \
  "s_waitcnt lgkmcnt(1)\n\t" \
  "v_smfmac_f32_16x16x64_bf16 v[8:11], v[0:3], v[60:67], v68\n\t" \
  "buffer_load_dword v75, off, s[28:31], s80 sc1\n\t" \
  "ds_read_b128 v[20:23], v70 offset:29696\n\t" \
  "v_mov_b64 v[76:77], 0\n\t" \
  "v_smfmac_f32_16x16x64_bf16 v[12:15], v[0:3], v[200:207], v68\n\t" \
  "ds_read_b128 v[32:35], v69 offset:6144\n\t" \
  "v_mov_b64 v[78:79], 0\n\t" \
  "v_smfmac_f32_16x16x64_bf16 v[52:55], v[0:3], v[44:51], v68\n\t" \
  "ds_read_b128 v[24:27], v70 offset:30720\n\t" \
  "v_lshl_add_u32 v72, v74, 3, v72\n\t" \
  "v_smfmac_f32_16x16x64_bf16 v[56:59], v[0:3], v[232:239], v68\n\t" \
  "ds_read_b128 v[28:31], v70 offset:31744\n\t" \
  "s_waitcnt lgkmcnt(4)\n\t" \
  "v_smfmac_f32_16x16x64_bf16 v[12:15], v[4:7], v[208:215], v68\n\t" \
  "ds_read_b128 v[60:63], v70 offset:32768\n\t" \
  "s_waitcnt lgkmcnt(4)\n\t" \
  "v_smfmac_f32_16x16x64_bf16 v[8:11], v[4:7], v[16:23], v68\n\t" \
  "ds_read_b128 v[64:67], v70 offset:33792\n\t" \
  "s_waitcnt lgkmcnt(4)\n\t" \
  "v_smfmac_f32_16x16x64_bf16 v[8:11], v[32:35], v[192:199], v68\n\t" \
  "ds_read_b128 v[36:39], v73 offset:4096\n\t" \
  "v_smfmac_f32_16x16x64_bf16 v[12:15], v[32:35], v[216:223], v68\n\t" \
  "ds_read_b128 v[40:43], v73 offset:6144\n\t" \
  "s_waitcnt lgkmcnt(4)\n\t" \
  "v_smfmac_f32_16x16x64_bf16 v[52:55], v[4:7], v[24:31], v68\n\t" \
  "ds_read_b128 v[44:47], v70 offset:34816\n\t" \
  "v_cvt_pk_bf16_f32 v8, v8, v9\n\t" \
  "v_smfmac_f32_16x16x64_bf16 v[52:55], v[32:35], v[224:231], v68\n\t" \
  "ds_read_b128 v[48:51], v70 offset:35840\n\t" \
  "v_cvt_pk_bf16_f32 v9, v10, v11\n\t" \
  "v_smfmac_f32_16x16x64_bf16 v[56:59], v[4:7], v[240:247], v68\n\t" \
  "s_waitcnt lgkmcnt(4)\n\t" \
  "v_cvt_pk_bf16_f32 v10, v12, v13\n\t" \
  "v_smfmac_f32_16x16x64_bf16 v[56:59], v[32:35], v[60:67], v68\n\t" \
  "s_waitcnt lgkmcnt(3)\n\t" \
  "v_cvt_pk_bf16_f32 v11, v14, v15\n\t" \
  "v_smfmac_f32_16x16x64_bf16 v[76:79], v[36:39], v[248:255], v68\n\t" \
  "buffer_store_dwordx4 v[8:11], v71, s[28:31], s54 offen sc1\n\t" \
  "s_waitcnt lgkmcnt(0)\n\t" \
  CH_DP11 \
  "v_cvt_pk_bf16_f32 v52, v52, v53\n\t" \
  "v_smfmac_f32_16x16x64_bf16 v[76:79], v[40:43], v[44:51], v68\n\t" \
  "v_cvt_pk_bf16_f32 v53, v54, v55\n\t" \
  "v_cvt_pk_bf16_f32 v54, v56, v57\n\t" \
  "v_cvt_pk_bf16_f32 v55, v58, v59\n\t" \
  "buffer_store_dwordx4 v[52:55], v71, s[28:31], s75 offen sc1\n\t" \
  "s_nop 2\n\t" \
  "v_add_f32 v76, v76, v78\n\t" \
  "v_add_f32 v77, v77, v79\n\t" \
  "v_cvt_pk_bf16_f32 v76, v76, v77\n\t" \
  "buffer_store_dword v76, v72, s[28:31], s55 offen sc1\n\t" \
  CH_DP12 \
  "s_memrealtime s[70:71]\n\t" \
  "s_waitcnt vmcnt(3)\n\t" \
  "v_readfirstlane_b32 s65, v75\n\t" \
  "s_barrier\n\t" \
  "s_waitcnt lgkmcnt(0)\n\t" \
  "s_not_b32 s65, s65\n\t" \
  "s_lshl_b32 s61, s70, 8\n\t" \
  "s_andn2_b32 s64, s65, 0xff\n\t" \
  "s_sub_u32 s64, s64, s61\n\t" \
  "s_ashr_i32 s64, s64, 8\n\t" \
  "s_cmp_eq_u32 s65, 0\n\t" \
  "s_cselect_b32 s64, " CH_DLF ", s64\n\t" \
  "s_sub_i32 s64, s64, " CH_RO1 "\n\t" \
  "s_min_i32 s64, s64, s34\n\t" \
  "s_max_i32 s64, s64, " CH_DLL1 "\n\t" \
  "s_add_u32 s64, s64, s70\n\t" \
  CH_ST2 \
  "s_mov_b32 s74, 0\n\t" \
  "s_movk_i32 s60, 8192\n\t" \
  "RWT1_%=:\n\t" \
  "s_memrealtime s[72:73]\n\t" \
  "s_sub_u32 s60, s60, 1\n\t" \
  "s_cselect_b32 s61, 0, -1\n\t" \
  "s_waitcnt lgkmcnt(0)\n\t" \
  "s_sub_i32 s73, s72, s64\n\t" \
  "s_and_b32 s73, s73, s61\n\t" \
  "s_cmp_lt_i32 s73, " CH_FIN "\n\t" \
  "s_cbranch_scc1 RWT1_%=\n\t" \
  "s_cmp_lt_i32 s73, 0\n\t" \
  "s_cbranch_scc1 RWT1_%=f\n\t" \
  "RWT1_%=g:\n\t" \
  "RRETRY1_%=:\n\t" \
  CH_DP13 \
  "buffer_load_dwordx4 v[0:3], v69, s[28:31], s97 offen offset:0 sc1\n\t" \
  "buffer_load_dwordx4 v[4:7], v69, s[28:31], s97 offen offset:1024 sc1\n\t" \
  "buffer_load_dwordx4 v[8:11], v69, s[28:31], s97 offen offset:2048 sc1\n\t" \
  "buffer_load_dwordx4 v[12:15], v69, s[28:31], s97 offen offset:3072 sc1\n\t" \
  "v_lshrrev_b32 v20, 2, v69\n\t" \
  "v_and_b32 v20, 16, v20\n\t" \
  "v_mov_b32 v21, 0x3f80\n\t" \
  "v_lshlrev_b32 v21, v20, v21\n\t" \
  "v_bfe_u32 v20, v69, 4, 2\n\t" \
  "v_cmp_eq_u32 vcc, 0, v20\n\t" \
  "s_nop 1\n\t" \
  "v_cndmask_b32 v32, 0, v21, vcc\n\t" \
  "v_cmp_eq_u32 vcc, 1, v20\n\t" \
  "s_nop 1\n\t" \
  "v_cndmask_b32 v33, 0, v21, vcc\n\t" \
  "v_cmp_eq_u32 vcc, 2, v20\n\t" \
  "s_nop 1\n\t" \
  "v_cndmask_b32 v34, 0, v21, vcc\n\t" \
  "v_cmp_eq_u32 vcc, 3, v20\n\t" \
  "s_nop 1\n\t" \
  "v_cndmask_b32 v35, 0, v21, vcc\n\t" \
  "v_bfe_u32 v38, v69, 7, 1\n\t" \
  "v_bfe_u32 v20, v69, 9, 1\n\t" \
  "v_lshl_add_u32 v38, v20, 2, v38\n\t" \
  "v_lshlrev_b32 v38, 12, v38\n\t" \
  "v_and_b32 v20, 0x60, v69\n\t" \
  "v_add_u32 v38, v38, v20\n\t" \
  "v_bfe_u32 v20, v69, 8, 1\n\t" \
  "v_lshl_add_u32 v38, v20, 4, v38\n\t" \
  "v_bfe_u32 v20, v69, 4, 1\n\t" \
  "v_lshl_add_u32 v38, v20, 3, v38\n\t" \
  "v_mov_b32 v44, 0\n\t" \
  CH_DP17 \
  "s_waitcnt vmcnt(3)\n\t" \
  CH_DP14 \
  "v_mfma_f32_16x16x32_bf16 v[40:43], v[32:35], v[0:3], 0\n\t" \
  "v_max3_u32 v44, v44, v0, v1\n\t" \
  "v_max3_u32 v44, v44, v2, v3\n\t" \
  "s_waitcnt vmcnt(2)\n\t" \
  "v_mfma_f32_16x16x32_bf16 v[40:43], v[32:35], v[4:7], v[40:43]\n\t" \
  "v_max3_u32 v44, v44, v4, v5\n\t" \
  "v_max3_u32 v44, v44, v6, v7\n\t" \
  "s_waitcnt vmcnt(1)\n\t" \
  "v_mfma_f32_16x16x32_bf16 v[40:43], v[32:35], v[8:11], v[40:43]\n\t" \
  "v_max3_u32 v44, v44, v8, v9\n\t" \
  "v_max3_u32 v44, v44, v10, v11\n\t" \
  "s_waitcnt vmcnt(0)\n\t" \
  CH_DP15 \
  "v_mfma_f32_16x16x32_bf16 v[40:43], v[32:35], v[12:15], v[40:43]\n\t" \
  "v_max3_u32 v44, v44, v12, v13\n\t" \
  "v_max3_u32 v44, v44, v14, v15\n\t" \
  "v_cmp_eq_u32 vcc, -1, v44\n\t" \
  "s_cbranch_vccnz RFAIL1_%=\n\t" \
  "s_nop 3\n\t" \
  "v_add_f32_dpp v40, v40, v40 row_ror:8 row_mask:0xf bank_mask:0xf\n\t" \
  "v_add_f32_dpp v41, v41, v41 row_ror:8 row_mask:0xf bank_mask:0xf\n\t" \
  "v_add_f32_dpp v42, v42, v42 row_ror:8 row_mask:0xf bank_mask:0xf\n\t" \
  "v_add_f32_dpp v43, v43, v43 row_ror:8 row_mask:0xf bank_mask:0xf\n\t" \
  "v_cvt_pk_bf16_f32 v40, v40, v41\n\t" \
  "v_cvt_pk_bf16_f32 v41, v42, v43\n\t" \
  "buffer_store_dwordx2 v[40:41], v38, s[28:31], s98 offen sc1\n\t" \
  "RDONE1_%=:\n\t" \
  CH_DP16 \
  CH_ST4 \
  "s_barrier\n\t" \
  CH_ST3 \
  CH_TRC \
  "s_add_u32 s48, s48, 1\n\t" \
  "s_mov_b32 s49, s53\n\t" \
  "s_add_u32 s53, s53, " CH_SLOT "\n\t" \
  "s_add_u32 s60, s53, " CH_SKIP "\n\t" \
  "s_lshr_b32 s61, s96, 1\n\t" \
  "s_add_u32 s86, s86, 1\n\t" \
  "s_cmp_eq_u32 s86, 3\n\t" \
  "s_cselect_b32 s53, s60, s53\n\t" \
  "s_cselect_b32 s96, s61, s96\n\t" \
  "s_cselect_b32 s86, 0, s86\n\t" \
  "s_cmp_ge_u32 s53, " CH_RING "\n\t" \
  "s_cselect_b32 s53, 0, s53\n\t" \
  "s_cselect_b32 s96, s21, s96\n\t" \
  "s_andn2_b32 s53, s53, 0x2000\n\t" \
  "s_and_b32 s60, s96, 1\n\t" \
  "s_lshl_b32 s60, s60, 13\n\t" \
  "s_or_b32 s53, s53, s60\n\t" \
  "s_mov_b32 s68, 0\n\t" \
  "s_add_u32 s97, s41, s53\n\t" \
  "s_add_u32 s98, s37, s53\n\t" \
  "s_add_u32 s82, s48, 0\n\t" \
  "s_and_b32 s82, s82, " CH_TRM "\n\t" \
  CH_SEGC \
  "s_lshr_b64 s[60:61], s[90:91], s82\n\t" \
  "s_and_b32 s60, s60, 1\n\t" \
  "s_lshl_b32 s60, s60, 13\n\t" \
  "s_add_u32 s82, s82, " CH_SG0 "\n\t" \
  "s_lshl_b32 s82, s82, 21\n\t" \
  "s_or_b32 s82, s82, s60\n\t" \
  "s_add_u32 s50, s82, s38\n\t" \
  "s_add_u32 s69, s82, s39\n\t" \
  "s_cmp_eq_u32 s34, 0\n\t" \
  "s_cbranch_scc1 LINI_%=\n\t" \
  "s_branch LG1_%=\n\t" \
  "RFAIL1_%=:\n\t" \
  "s_add_u32 s74, s74, 1\n\t" \
  "s_sleep " CH_RSL "\n\t" \
  "s_waitcnt vmcnt(0)\n\t" \
  "s_cmp_gt_u32 s74, s84\n\t" \
  "s_cbranch_scc1 RG1_%=\n\t" \
  "s_and_b32 s60, s74, 1023\n\t" \
  "s_cmp_lg_u32 s60, 0\n\t" \
  "s_cbranch_scc1 RRETRY1_%=\n\t" \
  "buffer_load_dword v46, off, s[28:31], s92 sc1\n\t" \
  "s_waitcnt vmcnt(0)\n\t" \
  "v_readfirstlane_b32 s60, v46\n\t" \
  "s_nop 0\n\t" \
  "s_cmp_eq_u32 s60, 0\n\t" \
  "s_cbranch_scc1 RRETRY1_%=\n\t" \
  "RG1_%=:\n\t" \
  "s_mov_b32 s84, 0\n\t" \
  "s_lshl_b32 s60, s36, 4\n\t" \
  "s_or_b32 s60, s60, s48\n\t" \
  "s_or_b32 s60, s60, 536870912\n\t" \
  "v_mov_b32 v76, s60\n\t" \
  "v_mov_b32 v77, 0\n\t" \
  "s_mov_b64 exec, 1\n\t" \
  "buffer_atomic_cmpswap v[76:77], off, s[28:31], s92 sc1\n\t" \
  "s_mov_b64 exec, -1\n\t" \
  "s_branch RDONE1_%=\n\t" \
  "LFAIL1_%=:\n\t" \
  "v_max_u32 v75, v0, v1\n\t" \
  "v_max3_u32 v75, v75, v2, v3\n\t" \
  "v_max3_u32 v75, v75, v4, v5\n\t" \
  "v_max3_u32 v75, v75, v6, v7\n\t" \
  "v_max3_u32 v75, v75, v8, v9\n\t" \
  "v_max3_u32 v75, v75, v10, v11\n\t" \
  "v_max3_u32 v75, v75, v12, v13\n\t" \
  "v_max3_u32 v75, v75, v14, v15\n\t" \
  "v_max3_u32 v75, v75, v16, v17\n\t" \
  "v_max3_u32 v75, v75, v18, v19\n\t" \
  "v_max3_u32 v75, v75, v20, v21\n\t" \
  "v_max3_u32 v75, v75, v22, v23\n\t" \
  "v_max3_u32 v75, v75, v24, v25\n\t" \
  "v_max3_u32 v75, v75, v26, v27\n\t" \
  "v_max3_u32 v75, v75, v28, v29\n\t" \
  "v_max3_u32 v75, v75, v30, v31\n\t" \
  "v_max3_u32 v75, v75, v32, v33\n\t" \
  "v_max3_u32 v75, v75, v34, v35\n\t" \
  "v_cmp_eq_u32 vcc, -1, v75\n\t" \
  "s_cbranch_vccz LGO1_%=\n\t" \
  "s_add_u32 s68, s68, 1\n\t" \
  "s_waitcnt lgkmcnt(0)\n\t" \
  "s_cmp_gt_u32 s68, s83\n\t" \
  "s_cbranch_scc1 LGV1_%=\n\t" \
  "s_barrier\n\t" \
  "s_waitcnt vmcnt(8)\n\t" \
  "s_branch LG1_%=\n\t" \
  "RWT1_%=f:\n\t" \
  "s_cmp_lt_i32 s73, -2\n\t" \
  "s_cbranch_scc0 RWT1_%=g\n\t" \
  "s_nop 4\n\t" \
  "s_branch RWT1_%=g\n\t" \
  "LGV1_%=:\n\t" \
  "s_mov_b32 s83, 0\n\t" \
  "s_lshl_b32 s60, s36, 4\n\t" \
  "s_or_b32 s60, s60, s48\n\t" \
  "s_or_b32 s60, s60, 805306368\n\t" \
  "v_mov_b32 v76, s60\n\t" \
  "v_mov_b32 v77, 0\n\t" \
  "s_mov_b64 exec, 1\n\t" \
  "buffer_atomic_cmpswap v[76:77], off, s[28:31], s92 sc1\n\t" \
  "s_mov_b64 exec, -1\n\t" \
  "s_branch LGO1_%=\n\t" \
  "LLAST1_%=:\n\t" \
  "s_waitcnt vmcnt(0) lgkmcnt(0)\n\t" \
  "s_barrier\n\t" \
  "s_branch LEND_%=\n\t" \
  ".p2align 6\n\t" \
  "LG2_%=:\n\t" \
  "s_barrier\n\t" \
  "buffer_load_dwordx4 v[52:55], v69, s[28:31], s50 offen sc1\n\t" \
  "buffer_load_dwordx4 v[56:59], v69, s[28:31], s50 offen offset:1024 sc1\n\t" \
  "s_add_u32 s64, s50, 18432\n\t" \
  "s_movk_i32 m0, 11264\n\t" \
  "v_lshlrev_b32 v75, 1, v69\n\t" \
  "buffer_load_dword v75, s[28:31], s64 offen sc1 lds\n\t" \
  "ds_read_b128 v[44:47], v70 offset:0\n\t" \
  "ds_read_b128 v[48:51], v70 offset:1024\n\t" \
  "ds_read_b128 v[60:63], v70 offset:2048\n\t" \
  "ds_read_b128 v[64:67], v70 offset:3072\n\t" \
  "v_mov_b64 v[36:37], 0\n\t" \
  "v_mov_b64 v[38:39], 0\n\t" \
  "v_mov_b64 v[40:41], 0\n\t" \
  "v_mov_b64 v[42:43], 0\n\t" \
  "v_lshrrev_b32 v71, 1, v69\n\t" \
  "v_lshrrev_b32 v72, 3, v69\n\t" \
  "v_and_b32 v72, 0x78, v72\n\t" \
  "v_and_b32 v74, 0x30, v69\n\t" \
  "v_lshl_add_u32 v72, v74, 3, v72\n\t" \
  "s_add_u32 s54, s53, s36\n\t" \
  "s_add_u32 s55, s54, 557056\n\t" \
  "s_add_u32 s54, s54, 131072\n\t" \
  "s_add_u32 s75, s54, 262144\n\t" \
  "s_add_u32 s80, s53, s26\n\t" \
  "s_barrier\n\t" \
  CH_DP0 \
  "ds_read_b128 v[0:3], v69 offset:0\n\t" \
  "ds_read_b128 v[4:7], v69 offset:1024\n\t" \
  "ds_read_b128 v[8:11], v69 offset:2048\n\t" \
  "ds_read_b128 v[12:15], v69 offset:3072\n\t" \
  "s_waitcnt lgkmcnt(3)\n\t" \
  "v_smfmac_f32_16x16x64_bf16 v[36:39], v[0:3], v[44:51], v68\n\t" \
  "ds_read_b128 v[44:47], v70 offset:4096\n\t" \
  "v_smfmac_f32_16x16x64_bf16 v[40:43], v[0:3], v[60:67], v68\n\t" \
  "ds_read_b128 v[48:51], v70 offset:5120\n\t" \
  "s_waitcnt lgkmcnt(4)\n\t" \
  "v_smfmac_f32_16x16x64_bf16 v[36:39], v[4:7], a[32:39], v68\n\t" \
  "ds_read_b128 v[60:63], v70 offset:6144\n\t" \
  "v_smfmac_f32_16x16x64_bf16 v[40:43], v[4:7], a[40:47], v68\n\t" \
  "ds_read_b128 v[64:67], v70 offset:7168\n\t" \
  "s_waitcnt lgkmcnt(5)\n\t" \
  "v_smfmac_f32_16x16x64_bf16 v[36:39], v[8:11], a[80:87], v68\n\t" \
  "s_barrier\n\t" \
  CH_DP1 \
  "ds_read_b128 v[16:19], v69 offset:4096\n\t" \
  "v_smfmac_f32_16x16x64_bf16 v[40:43], v[8:11], a[88:95], v68\n\t" \
  "ds_read_b128 v[20:23], v69 offset:5120\n\t" \
  "s_waitcnt lgkmcnt(6)\n\t" \
  "v_smfmac_f32_16x16x64_bf16 v[36:39], v[12:15], a[120:127], v68\n\t" \
  "ds_read_b128 v[24:27], v69 offset:6144\n\t" \
  "s_waitcnt lgkmcnt(5)\n\t" \
  "v_smfmac_f32_16x16x64_bf16 v[40:43], v[12:15], v[44:51], v68\n\t" \
  "ds_read_b128 v[28:31], v69 offset:7168\n\t" \
  "s_waitcnt lgkmcnt(3)\n\t" \
  "v_smfmac_f32_16x16x64_bf16 v[36:39], v[16:19], a[160:167], v68\n\t" \
  "ds_read_b128 v[32:35], v69 offset:8192\n\t" \
  "v_smfmac_f32_16x16x64_bf16 v[40:43], v[16:19], a[168:175], v68\n\t" \
  "s_waitcnt lgkmcnt(3)\n\t" \
  "v_mov_b64 v[44:45], 0\n\t" \
  "v_smfmac_f32_16x16x64_bf16 v[36:39], v[20:23], a[200:207], v68\n\t" \
  "s_waitcnt lgkmcnt(2)\n\t" \
  "v_mov_b64 v[46:47], 0\n\t" \
  "v_smfmac_f32_16x16x64_bf16 v[36:39], v[24:27], a[240:247], v68\n\t" \
  "s_waitcnt lgkmcnt(1)\n\t" \
  "v_mov_b64 v[48:49], 0\n\t" \
  "v_smfmac_f32_16x16x64_bf16 v[36:39], v[28:31], v[112:119], v68\n\t" \
  "s_waitcnt lgkmcnt(0)\n\t" \
  "v_mov_b64 v[50:51], 0\n\t" \
  "v_smfmac_f32_16x16x64_bf16 v[36:39], v[32:35], v[160:167], v68\n\t" \
  "v_smfmac_f32_16x16x64_bf16 v[40:43], v[20:23], v[60:67], v68\n\t" \
  "ds_read_b128 v[60:63], v70 offset:8192\n\t" \
  "v_smfmac_f32_16x16x64_bf16 v[40:43], v[24:27], a[248:255], v68\n\t" \
  "ds_read_b128 v[64:67], v70 offset:9216\n\t" \
  "v_add_f32 v74, v36, v38\n\t" \
  "v_smfmac_f32_16x16x64_bf16 v[40:43], v[28:31], v[120:127], v68\n\t" \
  "v_add_f32 v37, v37, v39\n\t" \
  "v_smfmac_f32_16x16x64_bf16 v[40:43], v[32:35], v[168:175], v68\n\t" \
  "v_cmp_u_f32 vcc, v74, v37\n\t" \
  CH_DP2 \
  "v_smfmac_f32_16x16x64_bf16 v[44:47], v[0:3], a[0:7], v68\n\t" \
  "s_cbranch_vccnz LFAIL2_%=\n\t" \
  "LGO2_%=:\n\t" \
  "v_cvt_pk_bf16_f32 v74, v74, v37\n\t" \
  "v_smfmac_f32_16x16x64_bf16 v[44:47], v[4:7], a[48:55], v68\n\t" \
  "s_waitcnt lgkmcnt(0)\n\t" \
  "v_add_f32 v75, v40, v42\n\t" \
  "v_smfmac_f32_16x16x64_bf16 v[44:47], v[8:11], v[60:67], v68\n\t" \
  "ds_read_b128 v[60:63], v70 offset:10240\n\t" \
  "v_add_f32 v41, v41, v43\n\t" \
  "v_cvt_pk_bf16_f32 v75, v75, v41\n\t" \
  "v_smfmac_f32_16x16x64_bf16 v[44:47], v[12:15], a[128:135], v68\n\t" \
  "ds_write2st64_b32 v71, v74, v75 offset0:40 offset1:42\n\t" \
  "v_smfmac_f32_16x16x64_bf16 v[44:47], v[16:19], a[176:183], v68\n\t" \
  "ds_read_b128 v[64:67], v70 offset:11264\n\t" \
  "v_smfmac_f32_16x16x64_bf16 v[44:47], v[20:23], a[208:215], v68\n\t" \
  "v_smfmac_f32_16x16x64_bf16 v[44:47], v[24:27], v[80:87], v68\n\t" \
  "v_smfmac_f32_16x16x64_bf16 v[44:47], v[28:31], v[128:135], v68\n\t" \
  "s_waitcnt lgkmcnt(0)\n\t" \
  "v_smfmac_f32_16x16x64_bf16 v[44:47], v[32:35], v[60:67], v68\n\t" \
  "s_barrier\n\t" \
  "ds_read_b64_tr_b16 v[76:77], v72 offset:10240\n\t" \
  CH_DP3 \
  "v_smfmac_f32_16x16x64_bf16 v[48:51], v[0:3], a[8:15], v68\n\t" \
  "ds_read_b64_tr_b16 v[78:79], v72 offset:10752\n\t" \
  "v_smfmac_f32_16x16x64_bf16 v[48:51], v[4:7], a[56:63], v68\n\t" \
  "buffer_load_dwordx4 v[60:63], v69, s[28:31], s64 offen sc1\n\t" \
  "v_add_f32 v74, v44, v46\n\t" \
  "v_smfmac_f32_16x16x64_bf16 v[48:51], v[8:11], a[96:103], v68\n\t" \
  "v_add_f32 v45, v45, v47\n\t" \
  "v_smfmac_f32_16x16x64_bf16 v[48:51], v[12:15], a[136:143], v68\n\t" \
  "buffer_load_dwordx4 v[64:67], v69, s[28:31], s64 offen offset:1024 sc1\n\t" \
  "s_waitcnt vmcnt(4)\n\t" \
  "s_waitcnt lgkmcnt(0)\n\t" \
  "v_cvt_pk_bf16_f32 v74, v74, v45\n\t" \
  "v_mfma_f32_16x16x32_bf16 v[36:39], v[52:55], v[76:79], 0\n\t" \
  "s_waitcnt vmcnt(3)\n\t" \
  "v_mfma_f32_16x16x32_bf16 v[40:43], v[56:59], v[76:79], 0\n\t" \
  "v_mov_b64 v[52:53], 0\n\t" \
  "v_mov_b64 v[54:55], 0\n\t" \
  "v_smfmac_f32_16x16x64_bf16 v[48:51], v[16:19], a[184:191], v68\n\t" \
  "v_mov_b64 v[56:57], 0\n\t" \
  "v_mov_b64 v[58:59], 0\n\t" \
  "v_smfmac_f32_16x16x64_bf16 v[48:51], v[20:23], a[216:223], v68\n\t" \
  "ds_write_b32 v71, v74 offset:1024\n\t" \
  "v_add_f32_dpp v36, v40, v36 quad_perm:[2,3,0,1] row_mask:0xf bank_mask:0xf\n\t" \
  "v_smfmac_f32_16x16x64_bf16 v[48:51], v[24:27], v[88:95], v68\n\t" \
  "v_add_f32_dpp v37, v41, v37 quad_perm:[2,3,0,1] row_mask:0xf bank_mask:0xf\n\t" \
  "v_smfmac_f32_16x16x64_bf16 v[48:51], v[28:31], v[136:143], v68\n\t" \
  "v_add_f32_dpp v38, v42, v38 quad_perm:[2,3,0,1] row_mask:0xf bank_mask:0xf\n\t" \
  "v_smfmac_f32_16x16x64_bf16 v[48:51], v[32:35], v[176:183], v68\n\t" \
  "v_add_f32_dpp v39, v43, v39 quad_perm:[2,3,0,1] row_mask:0xf bank_mask:0xf\n\t" \
  CH_DP4 \
  "v_smfmac_f32_16x16x64_bf16 v[52:55], v[0:3], a[16:23], v68\n\t" \
  "s_mov_b64 exec, s[58:59]\n\t" \
  "ds_write_b128 v69, v[36:39] offset:3072\n\t" \
  "s_mov_b64 exec, -1\n\t" \
  "v_smfmac_f32_16x16x64_bf16 v[52:55], v[4:7], a[64:71], v68\n\t" \
  "v_add_f32 v75, v48, v50\n\t" \
  "v_add_f32 v49, v49, v51\n\t" \
  "v_cvt_pk_bf16_f32 v75, v75, v49\n\t" \
  "v_smfmac_f32_16x16x64_bf16 v[56:59], v[0:3], a[24:31], v68\n\t" \
  "ds_write_b32 v71, v75 offset:1536\n\t" \
  "v_smfmac_f32_16x16x64_bf16 v[56:59], v[4:7], a[72:79], v68\n\t" \
  "ds_read_b64_tr_b16 v[0:1], v72 offset:9216\n\t" \
  "ds_read_b64_tr_b16 v[2:3], v72 offset:9728\n\t" \
  "v_smfmac_f32_16x16x64_bf16 v[52:55], v[8:11], a[104:111], v68\n\t" \
  "ds_read_b128 v[36:39], v70 offset:12288\n\t" \
  "v_smfmac_f32_16x16x64_bf16 v[56:59], v[8:11], a[112:119], v68\n\t" \
  "ds_read_b128 v[40:43], v70 offset:13312\n\t" \
  "v_smfmac_f32_16x16x64_bf16 v[52:55], v[12:15], a[144:151], v68\n\t" \
  "s_waitcnt lgkmcnt(4)\n\t" \
  "s_barrier\n\t" \
  CH_DP5 \
  "ds_read_b64_tr_b16 v[44:45], v72 offset:1024\n\t" \
  "ds_read_b64_tr_b16 v[46:47], v72 offset:1536\n\t" \
  "v_smfmac_f32_16x16x64_bf16 v[56:59], v[12:15], a[152:159], v68\n\t" \
  "ds_read_b64_tr_b16 v[48:49], v72 offset:0\n\t" \
  "ds_read_b64_tr_b16 v[50:51], v72 offset:512\n\t" \
  "v_smfmac_f32_16x16x64_bf16 v[52:55], v[16:19], a[192:199], v68\n\t" \
  "ds_read_b128 v[4:7], v69 offset:2048\n\t" \
  "s_waitcnt lgkmcnt(5)\n\t" \
  "v_smfmac_f32_16x16x64_bf16 v[56:59], v[16:19], v[36:43], v68\n\t" \
  "ds_read_b128 v[8:11], v69 offset:3072\n\t" \
  "s_waitcnt lgkmcnt(4)\n\t" \
  "v_mfma_f32_16x16x32_bf16 v[12:15], v[44:47], v[76:79], 0\n\t" \
  "ds_read_b128 v[36:39], v70 offset:14336\n\t" \
  "s_waitcnt lgkmcnt(3)\n\t" \
  "v_mfma_f32_16x16x32_bf16 v[12:15], v[48:51], v[0:3], v[12:15]\n\t" \
  "ds_read_b128 v[40:43], v70 offset:15360\n\t" \
  "v_smfmac_f32_16x16x64_bf16 v[52:55], v[20:23], a[224:231], v68\n\t" \
  "buffer_store_dwordx4 v[44:47], v69, s[28:31], s69 offen offset:1024 sc1\n\t" \
  "s_waitcnt lgkmcnt(2)\n\t" \
  "v_add_f32 v4, v4, v8\n\t" \
  "v_add_f32 v5, v5, v9\n\t" \
  "v_add_f32 v6, v6, v10\n\t" \
  CH_DP6 \
  "v_cndmask_b32 v78, v13, v14, s[56:57]\n\t" \
  "v_cndmask_b32 v76, v12, v15, s[56:57]\n\t" \
  "v_add_f32 v7, v7, v11\n\t" \
  "v_max3_f32 v77, v4, v5, v6\n\t" \
  "v_add_f32_dpp v76, v78, v76 quad_perm:[2,3,0,1] row_mask:0xf bank_mask:0xf\n\t" \
  "v_max3_f32 v77, v77, v7, v76\n\t" \
  "v_smfmac_f32_16x16x64_bf16 v[56:59], v[20:23], a[232:239], v68\n\t" \
  "ds_read_b128 v[44:47], v70 offset:16384\n\t" \
  "v_max_f32_dpp v77, v77, v77 quad_perm:[2,3,0,1] row_mask:0xf bank_mask:0xf\n\t" \
  "v_mov_b32 v78, v77\n\t" \
  "v_smfmac_f32_16x16x64_bf16 v[52:55], v[24:27], v[96:103], v68\n\t" \
  "ds_read_b128 v[48:51], v70 offset:17408\n\t" \
  "v_permlane16_swap_b32 v77, v78\n\t" \
  "v_max_f32 v77, v77, v78\n\t" \
  "v_mov_b32 v78, v77\n\t" \
  "v_smfmac_f32_16x16x64_bf16 v[52:55], v[28:31], v[144:151], v68\n\t" \
  "ds_read_b128 v[16:19], v70 offset:18432\n\t" \
  "v_permlane32_swap_b32 v77, v78\n\t" \
  "v_max_f32 v77, v77, v78\n\t" \
  "v_smfmac_f32_16x16x64_bf16 v[52:55], v[32:35], v[184:191], v68\n\t" \
  "ds_read_b128 v[20:23], v70 offset:19456\n\t" \
  "v_mul_f32 v77, 0xbe0293ee, v77\n\t" \
  CH_DP7 \
  "v_smfmac_f32_16x16x64_bf16 v[56:59], v[24:27], v[104:111], v68\n\t" \
  "v_fmamk_f32 v4, v4, 0x3e0293ee, v77\n\t" \
  "v_smfmac_f32_16x16x64_bf16 v[56:59], v[28:31], v[152:159], v68\n\t" \
  "ds_read_b128 v[24:27], v70 offset:22528\n\t" \
  "s_waitcnt lgkmcnt(4)\n\t" \
  "v_fmamk_f32 v5, v5, 0x3e0293ee, v77\n\t" \
  "v_smfmac_f32_16x16x64_bf16 v[56:59], v[32:35], v[36:43], v68\n\t" \
  "ds_read_b128 v[28:31], v70 offset:23552\n\t" \
  "v_fmamk_f32 v6, v6, 0x3e0293ee, v77\n\t" \
  "v_fmamk_f32 v7, v7, 0x3e0293ee, v77\n\t" \
  "v_fmamk_f32 v76, v76, 0x3e0293ee, v77\n\t" \
  "v_exp_f32 v4, v4\n\t" \
  "v_exp_f32 v5, v5\n\t" \
  "v_exp_f32 v6, v6\n\t" \
  "v_exp_f32 v7, v7\n\t" \
  "v_cvt_pk_bf16_f32 v4, v4, v5\n\t" \
  "v_exp_f32 v76, v76\n\t" \
  "v_cvt_pk_bf16_f32 v5, v6, v7\n\t" \
  "v_mov_b32_dpp v12, v4 quad_perm:[0,1,0,1] row_mask:0xf bank_mask:0xf\n\t" \
  "v_mov_b32_dpp v14, v4 quad_perm:[2,3,2,3] row_mask:0xf bank_mask:0xf\n\t" \
  "v_mov_b32_dpp v13, v5 quad_perm:[0,1,0,1] row_mask:0xf bank_mask:0xf\n\t" \
  "v_mov_b32_dpp v15, v5 quad_perm:[2,3,2,3] row_mask:0xf bank_mask:0xf\n\t" \
  "v_add_f32 v52, v52, v54\n\t" \
  "v_add_f32 v53, v53, v55\n\t" \
  "s_waitcnt vmcnt(2)\n\t" \
  CH_DP8 \
  "v_mfma_f32_16x16x32_bf16 v[0:3], v[60:63], v[12:15], 0\n\t" \
  "ds_read_b128 v[36:39], v70 offset:20480\n\t" \
  "v_add_f32 v56, v56, v58\n\t" \
  "v_add_f32 v57, v57, v59\n\t" \
  "v_cvt_pk_bf16_f32 v52, v52, v53\n\t" \
  "v_cvt_pk_bf16_f32 v53, v56, v57\n\t" \
  "s_waitcnt vmcnt(1)\n\t" \
  "v_mfma_f32_16x16x32_bf16 v[4:7], v[64:67], v[12:15], 0\n\t" \
  "ds_read_b128 v[40:43], v70 offset:21504\n\t" \
  "v_mov_b64 v[56:57], s[88:89]\n\t" \
  "v_mov_b64 v[58:59], s[88:89]\n\t" \
  "v_mov_b32_dpp v78, v76 quad_perm:[0,1,0,1] row_mask:0xf bank_mask:0xf\n\t" \
  "v_mov_b32_dpp v79, v76 quad_perm:[2,3,2,3] row_mask:0xf bank_mask:0xf\n\t" \
  "v_mfma_f32_16x16x32_bf16 v[32:35], v[56:59], v[12:15], 0\n\t" \
  "ds_read_b128 v[60:63], v70 offset:24576\n\t" \
  "v_cvt_pk_bf16_f32 v11, v78, v79\n\t" \
  "v_cvt_pk_bf16_f32 v8, v78, v79\n\t" \
  "v_mov_b64 v[54:55], 0\n\t" \
  "v_mov_b32 v9, 0\n\t" \
  "v_mov_b32 v10, 0\n\t" \
  "v_mov_b64 v[14:15], 0\n\t" \
  "s_nop 0\n\t" \
  "v_mfma_f32_16x16x32_bf16 v[32:35], v[54:57], v[8:11], v[32:35]\n\t" \
  "buffer_store_dwordx2 v[52:53], v71, s[28:31], s69 offen offset:3072 sc1\n\t" \
  "s_cmp_eq_u32 s48, 63\n\t" \
  "v_mfma_f32_16x16x32_bf16 v[0:3], v[52:55], v[8:11], v[0:3]\n\t" \
  "ds_read_b128 v[64:67], v70 offset:25600\n\t" \
  "v_mov_b64 v[56:57], 0\n\t" \
  "v_mov_b64 v[58:59], 0\n\t" \
  "v_mfma_f32_16x16x32_bf16 v[4:7], v[52:55], v[10:13], v[4:7]\n\t" \
  "s_cbranch_scc1 LLAST2_%=\n\t" \
  "v_rcp_f32 v74, v32\n\t" \
  "v_mov_b64 v[12:13], 0\n\t" \
  "v_mov_b32 v8, 0\n\t" \
  "v_pk_mul_f32 v[0:1], v[0:1], v[74:75] op_sel_hi:[1,0]\n\t" \
  "v_pk_mul_f32 v[2:3], v[2:3], v[74:75] op_sel_hi:[1,0]\n\t" \
  "v_cvt_pk_bf16_f32 v76, v0, v1\n\t" \
  "v_cvt_pk_bf16_f32 v77, v2, v3\n\t" \
  "v_pk_mul_f32 v[4:5], v[4:5], v[74:75] op_sel_hi:[1,0]\n\t" \
  "v_pk_mul_f32 v[6:7], v[6:7], v[74:75] op_sel_hi:[1,0]\n\t" \
  "v_cvt_pk_bf16_f32 v78, v4, v5\n\t" \
  "v_cvt_pk_bf16_f32 v79, v6, v7\n\t" \
  "ds_write_b128 v69, v[76:79] offset:6144\n\t" \
  CH_DP9 \
  "s_waitcnt lgkmcnt(2)\n\t" \
  "v_smfmac_f32_16x16x64_bf16 v[12:15], v[76:79], v[44:51], v68\n\t" \
  "ds_read_b128 v[44:47], v70 offset:26624\n\t" \
  "v_mov_b32 v11, 0\n\t" \
  "v_smfmac_f32_16x16x64_bf16 v[56:59], v[76:79], v[16:23], v68\n\t" \
  "ds_read_b128 v[48:51], v70 offset:27648\n\t" \
  "v_mov_b64 v[52:53], 0\n\t" \
  "v_smfmac_f32_16x16x64_bf16 v[8:11], v[76:79], v[36:43], v68\n\t" \
  "ds_read_b128 v[16:19], v70 offset:28672\n\t" \
  "v_smfmac_f32_16x16x64_bf16 v[52:55], v[76:79], v[24:31], v68\n\t" \
  "s_waitcnt lgkmcnt(3)\n\t" \
  "s_barrier\n\t" \
  CH_DP10 \
  "ds_read_b128 v[0:3], v69 offset:7168\n\t" \
  "ds_read_b128 v[4:7], v69 offset:4096\n\t" \
  "v_lshrrev_b32 v71, 7, v69\n\t" \
  "v_and_b32 v74, 0x70, v69\n\t" \
  "v_lshl_add_u32 v71, v71, 14, v74\n\t" \
  "v_bfe_u32 v72, v69, 2, 7\n\t" \
  "v_and_b32 v74, 0x200, v69\n\t" \
  "s_waitcnt lgkmcnt(1)\n\t" \
  "v_smfmac_f32_16x16x64_bf16 v[8:11], v[0:3], v[60:67], v68\n\t" \
  "ds_read_b128 v[20:23], v70 offset:29696\n\t" \
  "v_mov_b64 v[76:77], 0\n\t" \
  "v_smfmac_f32_16x16x64_bf16 v[12:15], v[0:3], v[200:207], v68\n\t" \
  "buffer_load_dword v75, off, s[28:31], s80 sc1\n\t" \
  "ds_read_b128 v[32:35], v69 offset:5120\n\t" \
  "v_mov_b64 v[78:79], 0\n\t" \
  "v_smfmac_f32_16x16x64_bf16 v[52:55], v[0:3], v[44:51], v68\n\t" \
  "ds_read_b128 v[24:27], v70 offset:30720\n\t" \
  "v_lshl_add_u32 v72, v74, 3, v72\n\t" \
  "v_smfmac_f32_16x16x64_bf16 v[56:59], v[0:3], v[232:239], v68\n\t" \
  "ds_read_b128 v[28:31], v70 offset:31744\n\t" \
  "s_waitcnt lgkmcnt(4)\n\t" \
  "v_smfmac_f32_16x16x64_bf16 v[12:15], v[4:7], v[208:215], v68\n\t" \
  "ds_read_b128 v[60:63], v70 offset:32768\n\t" \
  "s_waitcnt lgkmcnt(4)\n\t" \
  "v_smfmac_f32_16x16x64_bf16 v[8:11], v[4:7], v[16:23], v68\n\t" \
  "ds_read_b128 v[64:67], v70 offset:33792\n\t" \
  "s_waitcnt lgkmcnt(4)\n\t" \
  "v_smfmac_f32_16x16x64_bf16 v[8:11], v[32:35], v[192:199], v68\n\t" \
  "ds_read_b128 v[36:39], v73 offset:4096\n\t" \
  "v_smfmac_f32_16x16x64_bf16 v[12:15], v[32:35], v[216:223], v68\n\t" \
  "ds_read_b128 v[40:43], v73 offset:6144\n\t" \
  "s_waitcnt lgkmcnt(4)\n\t" \
  "v_smfmac_f32_16x16x64_bf16 v[52:55], v[4:7], v[24:31], v68\n\t" \
  "ds_read_b128 v[44:47], v70 offset:34816\n\t" \
  "v_cvt_pk_bf16_f32 v8, v8, v9\n\t" \
  "v_smfmac_f32_16x16x64_bf16 v[52:55], v[32:35], v[224:231], v68\n\t" \
  "ds_read_b128 v[48:51], v70 offset:35840\n\t" \
  "v_cvt_pk_bf16_f32 v9, v10, v11\n\t" \
  "v_smfmac_f32_16x16x64_bf16 v[56:59], v[4:7], v[240:247], v68\n\t" \
  "s_waitcnt lgkmcnt(4)\n\t" \
  "v_cvt_pk_bf16_f32 v10, v12, v13\n\t" \
  "v_smfmac_f32_16x16x64_bf16 v[56:59], v[32:35], v[60:67], v68\n\t" \
  "s_waitcnt lgkmcnt(3)\n\t" \
  "v_cvt_pk_bf16_f32 v11, v14, v15\n\t" \
  "v_smfmac_f32_16x16x64_bf16 v[76:79], v[36:39], v[248:255], v68\n\t" \
  "buffer_store_dwordx4 v[8:11], v71, s[28:31], s54 offen sc1\n\t" \
  "s_waitcnt lgkmcnt(0)\n\t" \
  CH_DP11 \
  "v_cvt_pk_bf16_f32 v52, v52, v53\n\t" \
  "v_smfmac_f32_16x16x64_bf16 v[76:79], v[40:43], v[44:51], v68\n\t" \
  "v_cvt_pk_bf16_f32 v53, v54, v55\n\t" \
  "v_cvt_pk_bf16_f32 v54, v56, v57\n\t" \
  "v_cvt_pk_bf16_f32 v55, v58, v59\n\t" \
  "buffer_store_dwordx4 v[52:55], v71, s[28:31], s75 offen sc1\n\t" \
  "s_nop 2\n\t" \
  "v_add_f32 v76, v76, v78\n\t" \
  "v_add_f32 v77, v77, v79\n\t" \
  "v_cvt_pk_bf16_f32 v76, v76, v77\n\t" \
  "buffer_store_dword v76, v72, s[28:31], s55 offen sc1\n\t" \
  CH_DP12 \
  "s_memrealtime s[70:71]\n\t" \
  "s_waitcnt vmcnt(3)\n\t" \
  "v_readfirstlane_b32 s65, v75\n\t" \
  "s_barrier\n\t" \
  "s_waitcnt lgkmcnt(0)\n\t" \
  "s_not_b32 s65, s65\n\t" \
  "s_lshl_b32 s61, s70, 8\n\t" \
  "s_andn2_b32 s64, s65, 0xff\n\t" \
  "s_sub_u32 s64, s64, s61\n\t" \
  "s_ashr_i32 s64, s64, 8\n\t" \
  "s_cmp_eq_u32 s65, 0\n\t" \
  "s_cselect_b32 s64, " CH_DLF ", s64\n\t" \
  "s_min_i32 s64, s64, s34\n\t" \
  "s_max_i32 s64, s64, " CH_DLL1 "\n\t" \
  "s_add_u32 s64, s64, s70\n\t" \
  CH_ST2 \
  "s_mov_b32 s74, 0\n\t" \
  "s_movk_i32 s60, 8192\n\t" \
  "RWT2_%=:\n\t" \
  "s_memrealtime s[72:73]\n\t" \
  "s_sub_u32 s60, s60, 1\n\t" \
  "s_cselect_b32 s61, 0, -1\n\t" \
  "s_waitcnt lgkmcnt(0)\n\t" \
  "s_sub_i32 s73, s72, s64\n\t" \
  "s_and_b32 s73, s73, s61\n\t" \
  "s_cmp_lt_i32 s73, " CH_FIN "\n\t" \
  "s_cbranch_scc1 RWT2_%=\n\t" \
  "s_cmp_lt_i32 s73, 0\n\t" \
  "s_cbranch_scc1 RWT2_%=f\n\t" \
  "RWT2_%=g:\n\t" \
  "RRETRY2_%=:\n\t" \
  CH_DP13 \
  "buffer_load_dwordx4 v[0:3], v69, s[28:31], s97 offen offset:0 sc1\n\t" \
  "buffer_load_dwordx4 v[4:7], v69, s[28:31], s97 offen offset:1024 sc1\n\t" \
  "buffer_load_dwordx4 v[8:11], v69, s[28:31], s97 offen offset:2048 sc1\n\t" \
  "buffer_load_dwordx4 v[12:15], v69, s[28:31], s97 offen offset:3072 sc1\n\t" \
  "v_lshrrev_b32 v20, 2, v69\n\t" \
  "v_and_b32 v20, 16, v20\n\t" \
  "v_mov_b32 v21, 0x3f80\n\t" \
  "v_lshlrev_b32 v21, v20, v21\n\t" \
  "v_bfe_u32 v20, v69, 4, 2\n\t" \
  "v_cmp_eq_u32 vcc, 0, v20\n\t" \
  "s_nop 1\n\t" \
  "v_cndmask_b32 v32, 0, v21, vcc\n\t" \
  "v_cmp_eq_u32 vcc, 1, v20\n\t" \
  "s_nop 1\n\t" \
  "v_cndmask_b32 v33, 0, v21, vcc\n\t" \
  "v_cmp_eq_u32 vcc, 2, v20\n\t" \
  "s_nop 1\n\t" \
  "v_cndmask_b32 v34, 0, v21, vcc\n\t" \
  "v_cmp_eq_u32 vcc, 3, v20\n\t" \
  "s_nop 1\n\t" \
  "v_cndmask_b32 v35, 0, v21, vcc\n\t" \
  "v_bfe_u32 v38, v69, 7, 1\n\t" \
  "v_bfe_u32 v20, v69, 9, 1\n\t" \
  "v_lshl_add_u32 v38, v20, 2, v38\n\t" \
  "v_lshlrev_b32 v38, 12, v38\n\t" \
  "v_and_b32 v20, 0x60, v69\n\t" \
  "v_add_u32 v38, v38, v20\n\t" \
  "v_bfe_u32 v20, v69, 8, 1\n\t" \
  "v_lshl_add_u32 v38, v20, 4, v38\n\t" \
  "v_bfe_u32 v20, v69, 4, 1\n\t" \
  "v_lshl_add_u32 v38, v20, 3, v38\n\t" \
  "v_mov_b32 v44, 0\n\t" \
  CH_DP17 \
  "s_waitcnt vmcnt(3)\n\t" \
  CH_DP14 \
  "v_mfma_f32_16x16x32_bf16 v[40:43], v[32:35], v[0:3], 0\n\t" \
  "v_max3_u32 v44, v44, v0, v1\n\t" \
  "v_max3_u32 v44, v44, v2, v3\n\t" \
  "s_waitcnt vmcnt(2)\n\t" \
  "v_mfma_f32_16x16x32_bf16 v[40:43], v[32:35], v[4:7], v[40:43]\n\t" \
  "v_max3_u32 v44, v44, v4, v5\n\t" \
  "v_max3_u32 v44, v44, v6, v7\n\t" \
  "s_waitcnt vmcnt(1)\n\t" \
  "v_mfma_f32_16x16x32_bf16 v[40:43], v[32:35], v[8:11], v[40:43]\n\t" \
  "v_max3_u32 v44, v44, v8, v9\n\t" \
  "v_max3_u32 v44, v44, v10, v11\n\t" \
  "s_waitcnt vmcnt(0)\n\t" \
  CH_DP15 \
  "v_mfma_f32_16x16x32_bf16 v[40:43], v[32:35], v[12:15], v[40:43]\n\t" \
  "v_max3_u32 v44, v44, v12, v13\n\t" \
  "v_max3_u32 v44, v44, v14, v15\n\t" \
  "v_cmp_eq_u32 vcc, -1, v44\n\t" \
  "s_cbranch_vccnz RFAIL2_%=\n\t" \
  "s_nop 3\n\t" \
  "v_add_f32_dpp v40, v40, v40 row_ror:8 row_mask:0xf bank_mask:0xf\n\t" \
  "v_add_f32_dpp v41, v41, v41 row_ror:8 row_mask:0xf bank_mask:0xf\n\t" \
  "v_add_f32_dpp v42, v42, v42 row_ror:8 row_mask:0xf bank_mask:0xf\n\t" \
  "v_add_f32_dpp v43, v43, v43 row_ror:8 row_mask:0xf bank_mask:0xf\n\t" \
  "v_cvt_pk_bf16_f32 v40, v40, v41\n\t" \
  "v_cvt_pk_bf16_f32 v41, v42, v43\n\t" \
  "buffer_store_dwordx2 v[40:41], v38, s[28:31], s98 offen sc1\n\t" \
  "RDONE2_%=:\n\t" \
  CH_DP16 \
  CH_ST4 \
  "s_barrier\n\t" \
  CH_ST3 \
  CH_TRC \
  "s_add_u32 s48, s48, 1\n\t" \
  "s_mov_b32 s49, s53\n\t" \
  "s_add_u32 s53, s53, " CH_SLOT "\n\t" \
  "s_add_u32 s60, s53, " CH_SKIP "\n\t" \
  "s_lshr_b32 s61, s96, 1\n\t" \
  "s_add_u32 s86, s86, 1\n\t" \
  "s_cmp_eq_u32 s86, 3\n\t" \
  "s_cselect_b32 s53, s60, s53\n\t" \
  "s_cselect_b32 s96, s61, s96\n\t" \
  "s_cselect_b32 s86, 0, s86\n\t" \
  "s_cmp_ge_u32 s53, " CH_RING "\n\t" \
  "s_cselect_b32 s53, 0, s53\n\t" \
  "s_cselect_b32 s96, s21, s96\n\t" \
  "s_andn2_b32 s53, s53, 0x2000\n\t" \
  "s_and_b32 s60, s96, 1\n\t" \
  "s_lshl_b32 s60, s60, 13\n\t" \
  "s_or_b32 s53, s53, s60\n\t" \
  "s_mov_b32 s68, 0\n\t" \
  "s_add_u32 s97, s41, s53\n\t" \
  "s_add_u32 s98, s37, s53\n\t" \
  "s_add_u32 s82, s48, 0\n\t" \
  "s_and_b32 s82, s82, " CH_TRM "\n\t" \
  CH_SEGC \
  "s_lshr_b64 s[60:61], s[90:91], s82\n\t" \
  "s_and_b32 s60, s60, 1\n\t" \
  "s_lshl_b32 s60, s60, 13\n\t" \
  "s_add_u32 s82, s82, " CH_SG0 "\n\t" \
  "s_lshl_b32 s82, s82, 21\n\t" \
  "s_or_b32 s82, s82, s60\n\t" \
  "s_add_u32 s50, s82, s38\n\t" \
  "s_add_u32 s69, s82, s39\n\t" \
  "s_cmp_eq_u32 s34, 0\n\t" \
  "s_cbranch_scc1 LINI_%=\n\t" \
  "s_branch LG2_%=\n\t" \
  "RFAIL2_%=:\n\t" \
  "s_add_u32 s74, s74, 1\n\t" \
  "s_sleep " CH_RSL "\n\t" \
  "s_waitcnt vmcnt(0)\n\t" \
  "s_cmp_gt_u32 s74, s84\n\t" \
  "s_cbranch_scc1 RG2_%=\n\t" \
  "s_and_b32 s60, s74, 1023\n\t" \
  "s_cmp_lg_u32 s60, 0\n\t" \
  "s_cbranch_scc1 RRETRY2_%=\n\t" \
  "buffer_load_dword v46, off, s[28:31], s92 sc1\n\t" \
  "s_waitcnt vmcnt(0)\n\t" \
  "v_readfirstlane_b32 s60, v46\n\t" \
  "s_nop 0\n\t" \
  "s_cmp_eq_u32 s60, 0\n\t" \
  "s_cbranch_scc1 RRETRY2_%=\n\t" \
  "RG2_%=:\n\t" \
  "s_mov_b32 s84, 0\n\t" \
  "s_lshl_b32 s60, s36, 4\n\t" \
  "s_or_b32 s60, s60, s48\n\t" \
  "s_or_b32 s60, s60, 536870912\n\t" \
  "v_mov_b32 v76, s60\n\t" \
  "v_mov_b32 v77, 0\n\t" \
  "s_mov_b64 exec, 1\n\t" \
  "buffer_atomic_cmpswap v[76:77], off, s[28:31], s92 sc1\n\t" \
  "s_mov_b64 exec, -1\n\t" \
  "s_branch RDONE2_%=\n\t" \
  "LFAIL2_%=:\n\t" \
  "v_max_u32 v75, v0, v1\n\t" \
  "v_max3_u32 v75, v75, v2, v3\n\t" \
  "v_max3_u32 v75, v75, v4, v5\n\t" \
  "v_max3_u32 v75, v75, v6, v7\n\t" \
  "v_max3_u32 v75, v75, v8, v9\n\t" \
  "v_max3_u32 v75, v75, v10, v11\n\t" \
  "v_max3_u32 v75, v75, v12, v13\n\t" \
  "v_max3_u32 v75, v75, v14, v15\n\t" \
  "v_max3_u32 v75, v75, v16, v17\n\t" \
  "v_max3_u32 v75, v75, v18, v19\n\t" \
  "v_max3_u32 v75, v75, v20, v21\n\t" \
  "v_max3_u32 v75, v75, v22, v23\n\t" \
  "v_max3_u32 v75, v75, v24, v25\n\t" \
  "v_max3_u32 v75, v75, v26, v27\n\t" \
  "v_max3_u32 v75, v75, v28, v29\n\t" \
  "v_max3_u32 v75, v75, v30, v31\n\t" \
  "v_max3_u32 v75, v75, v32, v33\n\t" \
  "v_max3_u32 v75, v75, v34, v35\n\t" \
  "v_cmp_eq_u32 vcc, -1, v75\n\t" \
  "s_cbranch_vccz LGO2_%=\n\t" \
  "s_add_u32 s68, s68, 1\n\t" \
  "s_waitcnt lgkmcnt(0)\n\t" \
  "s_cmp_gt_u32 s68, s83\n\t" \
  "s_cbranch_scc1 LGV2_%=\n\t" \
  "s_barrier\n\t" \
  "s_waitcnt vmcnt(8)\n\t" \
  "s_branch LG2_%=\n\t" \
  "RWT2_%=f:\n\t" \
  "s_cmp_lt_i32 s73, -2\n\t" \
  "s_cbranch_scc0 RWT2_%=g\n\t" \
  "s_nop 4\n\t" \
  "s_branch RWT2_%=g\n\t" \
  "LGV2_%=:\n\t" \
  "s_mov_b32 s83, 0\n\t" \
  "s_lshl_b32 s60, s36, 4\n\t" \
  "s_or_b32 s60, s60, s48\n\t" \
  "s_or_b32 s60, s60, 805306368\n\t" \
  "v_mov_b32 v76, s60\n\t" \
  "v_mov_b32 v77, 0\n\t" \
  "s_mov_b64 exec, 1\n\t" \
  "buffer_atomic_cmpswap v[76:77], off, s[28:31], s92 sc1\n\t" \
  "s_mov_b64 exec, -1\n\t" \
  "s_branch LGO2_%=\n\t" \
  "LLAST2_%=:\n\t" \
  "s_waitcnt vmcnt(0) lgkmcnt(0)\n\t" \
  "s_barrier\n\t" \
  "s_branch LEND_%=\n\t" \
  ".p2align 6\n\t" \
  "LG3_%=:\n\t" \
  "s_barrier\n\t" \
  "buffer_load_dwordx4 v[52:55], v69, s[28:31], s50 offen sc1\n\t" \
  "buffer_load_dwordx4 v[56:59], v69, s[28:31], s50 offen offset:1024 sc1\n\t" \
  "s_add_u32 s64, s50, 16384\n\t" \
  "s_movk_i32 m0, 11264\n\t" \
  "v_lshlrev_b32 v75, 1, v69\n\t" \
  "buffer_load_dword v75, s[28:31], s64 offen sc1 lds\n\t" \
  "s_lshr_b32 s61, s20, 3\n\t" \
  "s_mul_i32 s47, s61, " CH_KD "\n\t" \
  "s_lshr_b32 s47, s47, 10\n\t" \
  "s_lshl_b32 s47, s47, 8\n\t" \
  "s_mul_i32 s61, s61, " CH_KB "\n\t" \
  "s_lshr_b32 s61, s61, 10\n\t" \
  "s_min_u32 s61, s61, 100\n\t" \
  "s_or_b32 s47, s47, s61\n\t" \
  "s_cmp_lt_u32 s48, " CH_ES "\n\t" \
  "s_cselect_b32 s60, " CH_EX ", 0\n\t" \
  "s_add_u32 s47, s47, s60\n\t" \
  "s_not_b32 s47, s47\n\t" \
  "s_add_u32 s71, s53, s46\n\t" \
  "ds_read_b128 v[44:47], v70 offset:0\n\t" \
  "ds_read_b128 v[48:51], v70 offset:1024\n\t" \
  "ds_read_b128 v[60:63], v70 offset:2048\n\t" \
  "ds_read_b128 v[64:67], v70 offset:3072\n\t" \
  "v_mov_b64 v[36:37], 0\n\t" \
  "v_mov_b64 v[38:39], 0\n\t" \
  "v_mov_b64 v[40:41], 0\n\t" \
  "v_mov_b64 v[42:43], 0\n\t" \
  "v_lshrrev_b32 v71, 1, v69\n\t" \
  "v_add_u32 v71, 4, v71\n\t" \
  "v_lshrrev_b32 v72, 3, v69\n\t" \
  "v_and_b32 v72, 0x78, v72\n\t" \
  "v_and_b32 v74, 0x30, v69\n\t" \
  "v_lshl_add_u32 v72, v74, 3, v72\n\t" \
  "s_add_u32 s54, s53, s36\n\t" \
  "s_add_u32 s55, s54, 573440\n\t" \
  "s_add_u32 s54, s54, 135168\n\t" \
  "s_add_u32 s75, s54, 262144\n\t" \
  "s_add_u32 s80, s53, s26\n\t" \
  "s_or_b32 s87, s52, 0x1fff80\n\t" \
  "s_and_b32 s87, s87, -128\n\t" \
  "s_add_u32 s87, s87, 0x200000\n\t" \
  "s_barrier\n\t" \
  CH_DP0 \
  "ds_read_b128 v[0:3], v69 offset:0\n\t" \
  "ds_read_b128 v[4:7], v69 offset:1024\n\t" \
  "ds_read_b128 v[8:11], v69 offset:2048\n\t" \
  "ds_read_b128 v[12:15], v69 offset:3072\n\t" \
  "s_waitcnt lgkmcnt(3)\n\t" \
  "v_smfmac_f32_16x16x64_bf16 v[36:39], v[0:3], v[44:51], v68\n\t" \
  "ds_read_b128 v[44:47], v70 offset:4096\n\t" \
  "v_smfmac_f32_16x16x64_bf16 v[40:43], v[0:3], v[60:67], v68\n\t" \
  "ds_read_b128 v[48:51], v70 offset:5120\n\t" \
  "s_waitcnt lgkmcnt(4)\n\t" \
  "v_smfmac_f32_16x16x64_bf16 v[36:39], v[4:7], a[32:39], v68\n\t" \
  "ds_read_b128 v[60:63], v70 offset:6144\n\t" \
  "v_smfmac_f32_16x16x64_bf16 v[40:43], v[4:7], a[40:47], v68\n\t" \
  "ds_read_b128 v[64:67], v70 offset:7168\n\t" \
  "s_waitcnt lgkmcnt(5)\n\t" \
  "v_smfmac_f32_16x16x64_bf16 v[36:39], v[8:11], a[80:87], v68\n\t" \
  "s_barrier\n\t" \
  CH_DP1 \
  "ds_read_b128 v[16:19], v69 offset:4096\n\t" \
  "v_smfmac_f32_16x16x64_bf16 v[40:43], v[8:11], a[88:95], v68\n\t" \
  "ds_read_b128 v[20:23], v69 offset:5120\n\t" \
  "s_waitcnt lgkmcnt(6)\n\t" \
  "v_smfmac_f32_16x16x64_bf16 v[36:39], v[12:15], a[120:127], v68\n\t" \
  "ds_read_b128 v[24:27], v69 offset:6144\n\t" \
  "s_waitcnt lgkmcnt(5)\n\t" \
  "v_smfmac_f32_16x16x64_bf16 v[40:43], v[12:15], v[44:51], v68\n\t" \
  "ds_read_b128 v[28:31], v69 offset:7168\n\t" \
  "s_waitcnt lgkmcnt(3)\n\t" \
  "v_smfmac_f32_16x16x64_bf16 v[36:39], v[16:19], a[160:167], v68\n\t" \
  "ds_read_b128 v[32:35], v69 offset:8192\n\t" \
  "v_smfmac_f32_16x16x64_bf16 v[40:43], v[16:19], a[168:175], v68\n\t" \
  "s_waitcnt lgkmcnt(3)\n\t" \
  "v_mov_b64 v[44:45], 0\n\t" \
  "v_smfmac_f32_16x16x64_bf16 v[36:39], v[20:23], a[200:207], v68\n\t" \
  "s_waitcnt lgkmcnt(2)\n\t" \
  "v_mov_b64 v[46:47], 0\n\t" \
  "v_smfmac_f32_16x16x64_bf16 v[36:39], v[24:27], a[240:247], v68\n\t" \
  "s_waitcnt lgkmcnt(1)\n\t" \
  "v_mov_b64 v[48:49], 0\n\t" \
  "v_smfmac_f32_16x16x64_bf16 v[36:39], v[28:31], v[112:119], v68\n\t" \
  "s_waitcnt lgkmcnt(0)\n\t" \
  "v_mov_b64 v[50:51], 0\n\t" \
  "v_smfmac_f32_16x16x64_bf16 v[36:39], v[32:35], v[160:167], v68\n\t" \
  "v_smfmac_f32_16x16x64_bf16 v[40:43], v[20:23], v[60:67], v68\n\t" \
  "ds_read_b128 v[60:63], v70 offset:8192\n\t" \
  "v_smfmac_f32_16x16x64_bf16 v[40:43], v[24:27], a[248:255], v68\n\t" \
  "ds_read_b128 v[64:67], v70 offset:9216\n\t" \
  "v_add_f32 v74, v36, v38\n\t" \
  "v_smfmac_f32_16x16x64_bf16 v[40:43], v[28:31], v[120:127], v68\n\t" \
  "v_add_f32 v37, v37, v39\n\t" \
  "v_smfmac_f32_16x16x64_bf16 v[40:43], v[32:35], v[168:175], v68\n\t" \
  "v_cmp_u_f32 vcc, v74, v37\n\t" \
  CH_DP2 \
  "v_smfmac_f32_16x16x64_bf16 v[44:47], v[0:3], a[0:7], v68\n\t" \
  "s_cbranch_vccnz LFAIL3_%=\n\t" \
  "LGO3_%=:\n\t" \
  "v_cvt_pk_bf16_f32 v74, v74, v37\n\t" \
  "v_smfmac_f32_16x16x64_bf16 v[44:47], v[4:7], a[48:55], v68\n\t" \
  "s_waitcnt lgkmcnt(0)\n\t" \
  "v_add_f32 v75, v40, v42\n\t" \
  "v_smfmac_f32_16x16x64_bf16 v[44:47], v[8:11], v[60:67], v68\n\t" \
  "ds_read_b128 v[60:63], v70 offset:10240\n\t" \
  "v_add_f32 v41, v41, v43\n\t" \
  "v_cvt_pk_bf16_f32 v75, v75, v41\n\t" \
  "v_smfmac_f32_16x16x64_bf16 v[44:47], v[12:15], a[128:135], v68\n\t" \
  "ds_write2st64_b32 v71, v74, v75 offset0:40 offset1:42\n\t" \
  "v_smfmac_f32_16x16x64_bf16 v[44:47], v[16:19], a[176:183], v68\n\t" \
  "ds_read_b128 v[64:67], v70 offset:11264\n\t" \
  "s_memrealtime s[66:67]\n\t" \
  "v_smfmac_f32_16x16x64_bf16 v[44:47], v[20:23], a[208:215], v68\n\t" \
  "v_smfmac_f32_16x16x64_bf16 v[44:47], v[24:27], v[80:87], v68\n\t" \
  "v_smfmac_f32_16x16x64_bf16 v[44:47], v[28:31], v[128:135], v68\n\t" \
  "s_waitcnt lgkmcnt(0)\n\t" \
  "v_smfmac_f32_16x16x64_bf16 v[44:47], v[32:35], v[60:67], v68\n\t" \
  "s_barrier\n\t" \
  "ds_read_b64_tr_b16 v[76:77], v72 offset:10240\n\t" \
  CH_DP3 \
  "v_smfmac_f32_16x16x64_bf16 v[48:51], v[0:3], a[8:15], v68\n\t" \
  "s_lshl_b32 s60, s66, 8\n\t" \
  "s_sub_u32 s60, s47, s60\n\t" \
  "ds_read_b64_tr_b16 v[78:79], v72 offset:10752\n\t" \
  "v_mov_b32 v75, s60\n\t" \
  "v_smfmac_f32_16x16x64_bf16 v[48:51], v[4:7], a[56:63], v68\n\t" \
  "s_mov_b64 exec, s[24:25]\n\t" \
  "buffer_atomic_umin v75, v69, s[28:31], s71 offen sc1\n\t" \
  "s_mov_b64 exec, -1\n\t" \
  "v_add_f32 v74, v44, v46\n\t" \
  "v_smfmac_f32_16x16x64_bf16 v[48:51], v[8:11], a[96:103], v68\n\t" \
  "buffer_load_dwordx4 v[60:63], v69, s[28:31], s64 offen sc1\n\t" \
  "v_add_f32 v45, v45, v47\n\t" \
  "v_smfmac_f32_16x16x64_bf16 v[48:51], v[12:15], a[136:143], v68\n\t" \
  "s_waitcnt vmcnt(4)\n\t" \
  "s_waitcnt lgkmcnt(0)\n\t" \
  "v_cvt_pk_bf16_f32 v74, v74, v45\n\t" \
  "v_mfma_f32_16x16x32_bf16 v[36:39], v[52:55], v[76:79], 0\n\t" \
  "s_waitcnt vmcnt(3)\n\t" \
  "v_mfma_f32_16x16x32_bf16 v[40:43], v[56:59], v[76:79], 0\n\t" \
  "v_mov_b64 v[52:53], 0\n\t" \
  "v_mov_b64 v[54:55], 0\n\t" \
  "v_smfmac_f32_16x16x64_bf16 v[48:51], v[16:19], a[184:191], v68\n\t" \
  "buffer_load_dwordx4 v[64:67], v69, s[28:31], s64 offen offset:1024 sc1\n\t" \
  "v_mov_b64 v[56:57], 0\n\t" \
  "v_mov_b64 v[58:59], 0\n\t" \
  "v_smfmac_f32_16x16x64_bf16 v[48:51], v[20:23], a[216:223], v68\n\t" \
  "ds_write_b32 v71, v74 offset:1024\n\t" \
  "v_add_f32_dpp v36, v40, v36 quad_perm:[2,3,0,1] row_mask:0xf bank_mask:0xf\n\t" \
  "v_smfmac_f32_16x16x64_bf16 v[48:51], v[24:27], v[88:95], v68\n\t" \
  "v_add_f32_dpp v37, v41, v37 quad_perm:[2,3,0,1] row_mask:0xf bank_mask:0xf\n\t" \
  "v_smfmac_f32_16x16x64_bf16 v[48:51], v[28:31], v[136:143], v68\n\t" \
  "v_add_f32_dpp v38, v42, v38 quad_perm:[2,3,0,1] row_mask:0xf bank_mask:0xf\n\t" \
  "v_smfmac_f32_16x16x64_bf16 v[48:51], v[32:35], v[176:183], v68\n\t" \
  "v_add_f32_dpp v39, v43, v39 quad_perm:[2,3,0,1] row_mask:0xf bank_mask:0xf\n\t" \
  CH_DP4 \
  "v_smfmac_f32_16x16x64_bf16 v[52:55], v[0:3], a[16:23], v68\n\t" \
  "s_mov_b64 exec, s[58:59]\n\t" \
  "ds_write_b128 v69, v[36:39] offset:3104\n\t" \
  "s_mov_b64 exec, -1\n\t" \
  "v_smfmac_f32_16x16x64_bf16 v[52:55], v[4:7], a[64:71], v68\n\t" \
  "v_add_f32 v75, v48, v50\n\t" \
  "v_add_f32 v49, v49, v51\n\t" \
  "v_cvt_pk_bf16_f32 v75, v75, v49\n\t" \
  "v_smfmac_f32_16x16x64_bf16 v[56:59], v[0:3], a[24:31], v68\n\t" \
  "ds_write_b32 v71, v75 offset:1536\n\t" \
  "v_smfmac_f32_16x16x64_bf16 v[56:59], v[4:7], a[72:79], v68\n\t" \
  "ds_read_b64_tr_b16 v[0:1], v72 offset:9216\n\t" \
  "ds_read_b64_tr_b16 v[2:3], v72 offset:9728\n\t" \
  "v_smfmac_f32_16x16x64_bf16 v[52:55], v[8:11], a[104:111], v68\n\t" \
  "ds_read_b128 v[36:39], v70 offset:12288\n\t" \
  "v_smfmac_f32_16x16x64_bf16 v[56:59], v[8:11], a[112:119], v68\n\t" \
  "buffer_load_dword off, s[28:31], s87 sc1 lds\n\t" \
  "ds_read_b128 v[40:43], v70 offset:13312\n\t" \
  "v_smfmac_f32_16x16x64_bf16 v[52:55], v[12:15], a[144:151], v68\n\t" \
  "s_waitcnt lgkmcnt(4)\n\t" \
  "s_barrier\n\t" \
  CH_DP5 \
  "ds_read_b64_tr_b16 v[44:45], v72 offset:1024\n\t" \
  "ds_read_b64_tr_b16 v[46:47], v72 offset:1536\n\t" \
  "v_smfmac_f32_16x16x64_bf16 v[56:59], v[12:15], a[152:159], v68\n\t" \
  "ds_read_b64_tr_b16 v[48:49], v72 offset:0\n\t" \
  "ds_read_b64_tr_b16 v[50:51], v72 offset:512\n\t" \
  "v_smfmac_f32_16x16x64_bf16 v[52:55], v[16:19], a[192:199], v68\n\t" \
  "ds_read_b128 v[4:7], v69 offset:2048\n\t" \
  "s_waitcnt lgkmcnt(5)\n\t" \
  "v_smfmac_f32_16x16x64_bf16 v[56:59], v[16:19], v[36:43], v68\n\t" \
  "ds_read_b128 v[8:11], v69 offset:3072\n\t" \
  "s_waitcnt lgkmcnt(4)\n\t" \
  "v_mfma_f32_16x16x32_bf16 v[12:15], v[44:47], v[76:79], 0\n\t" \
  "ds_read_b128 v[36:39], v70 offset:14336\n\t" \
  "s_waitcnt lgkmcnt(3)\n\t" \
  "v_mfma_f32_16x16x32_bf16 v[12:15], v[48:51], v[0:3], v[12:15]\n\t" \
  "ds_read_b128 v[40:43], v70 offset:15360\n\t" \
  "v_smfmac_f32_16x16x64_bf16 v[52:55], v[20:23], a[224:231], v68\n\t" \
  "s_waitcnt lgkmcnt(2)\n\t" \
  "v_add_f32 v4, v4, v8\n\t" \
  "v_add_f32 v5, v5, v9\n\t" \
  "v_add_f32 v6, v6, v10\n\t" \
  CH_DP6 \
  "v_cndmask_b32 v78, v13, v14, s[56:57]\n\t" \
  "v_cndmask_b32 v76, v12, v15, s[56:57]\n\t" \
  "v_add_f32 v7, v7, v11\n\t" \
  "v_max3_f32 v77, v4, v5, v6\n\t" \
  "v_add_f32_dpp v76, v78, v76 quad_perm:[2,3,0,1] row_mask:0xf bank_mask:0xf\n\t" \
  "v_max3_f32 v77, v77, v7, v76\n\t" \
  "v_smfmac_f32_16x16x64_bf16 v[56:59], v[20:23], a[232:239], v68\n\t" \
  "ds_read_b128 v[44:47], v70 offset:16384\n\t" \
  "v_max_f32_dpp v77, v77, v77 quad_perm:[2,3,0,1] row_mask:0xf bank_mask:0xf\n\t" \
  "v_mov_b32 v78, v77\n\t" \
  "v_smfmac_f32_16x16x64_bf16 v[52:55], v[24:27], v[96:103], v68\n\t" \
  "ds_read_b128 v[48:51], v70 offset:17408\n\t" \
  "v_permlane16_swap_b32 v77, v78\n\t" \
  "v_max_f32 v77, v77, v78\n\t" \
  "v_mov_b32 v78, v77\n\t" \
  "v_smfmac_f32_16x16x64_bf16 v[52:55], v[28:31], v[144:151], v68\n\t" \
  "ds_read_b128 v[16:19], v70 offset:18432\n\t" \
  "v_permlane32_swap_b32 v77, v78\n\t" \
  "v_max_f32 v77, v77, v78\n\t" \
  "v_smfmac_f32_16x16x64_bf16 v[52:55], v[32:35], v[184:191], v68\n\t" \
  "ds_read_b128 v[20:23], v70 offset:19456\n\t" \
  "v_mul_f32 v77, 0xbe0293ee, v77\n\t" \
  CH_DP7 \
  "v_smfmac_f32_16x16x64_bf16 v[56:59], v[24:27], v[104:111], v68\n\t" \
  "v_fmamk_f32 v4, v4, 0x3e0293ee, v77\n\t" \
  "v_smfmac_f32_16x16x64_bf16 v[56:59], v[28:31], v[152:159], v68\n\t" \
  "ds_read_b128 v[24:27], v70 offset:22528\n\t" \
  "s_waitcnt lgkmcnt(4)\n\t" \
  "v_fmamk_f32 v5, v5, 0x3e0293ee, v77\n\t" \
  "v_smfmac_f32_16x16x64_bf16 v[56:59], v[32:35], v[36:43], v68\n\t" \
  "ds_read_b128 v[28:31], v70 offset:23552\n\t" \
  "v_fmamk_f32 v6, v6, 0x3e0293ee, v77\n\t" \
  "v_fmamk_f32 v7, v7, 0x3e0293ee, v77\n\t" \
  "v_fmamk_f32 v76, v76, 0x3e0293ee, v77\n\t" \
  "v_exp_f32 v4, v4\n\t" \
  "v_exp_f32 v5, v5\n\t" \
  "v_exp_f32 v6, v6\n\t" \
  "v_exp_f32 v7, v7\n\t" \
  "v_cvt_pk_bf16_f32 v4, v4, v5\n\t" \
  "v_exp_f32 v76, v76\n\t" \
  "v_cvt_pk_bf16_f32 v5, v6, v7\n\t" \
  "v_mov_b32_dpp v12, v4 quad_perm:[0,1,0,1] row_mask:0xf bank_mask:0xf\n\t" \
  "v_mov_b32_dpp v14, v4 quad_perm:[2,3,2,3] row_mask:0xf bank_mask:0xf\n\t" \
  "v_mov_b32_dpp v13, v5 quad_perm:[0,1,0,1] row_mask:0xf bank_mask:0xf\n\t" \
  "v_mov_b32_dpp v15, v5 quad_perm:[2,3,2,3] row_mask:0xf bank_mask:0xf\n\t" \
  "v_add_f32 v52, v52, v54\n\t" \
  "v_add_f32 v53, v53, v55\n\t" \
  "s_waitcnt vmcnt(2)\n\t" \
  CH_DP8 \
  "v_mfma_f32_16x16x32_bf16 v[0:3], v[60:63], v[12:15], 0\n\t" \
  "ds_read_b128 v[36:39], v70 offset:20480\n\t" \
  "v_add_f32 v56, v56, v58\n\t" \
  "v_add_f32 v57, v57, v59\n\t" \
  "v_cvt_pk_bf16_f32 v52, v52, v53\n\t" \
  "v_cvt_pk_bf16_f32 v53, v56, v57\n\t" \
  "s_waitcnt vmcnt(1)\n\t" \
  "v_mfma_f32_16x16x32_bf16 v[4:7], v[64:67], v[12:15], 0\n\t" \
  "ds_read_b128 v[40:43], v70 offset:21504\n\t" \
  "v_mov_b64 v[56:57], s[88:89]\n\t" \
  "v_mov_b64 v[58:59], s[88:89]\n\t" \
  "v_mov_b32_dpp v78, v76 quad_perm:[0,1,0,1] row_mask:0xf bank_mask:0xf\n\t" \
  "v_mov_b32_dpp v79, v76 quad_perm:[2,3,2,3] row_mask:0xf bank_mask:0xf\n\t" \
  "v_mfma_f32_16x16x32_bf16 v[32:35], v[56:59], v[12:15], 0\n\t" \
  "ds_read_b128 v[60:63], v70 offset:24576\n\t" \
  "v_cvt_pk_bf16_f32 v11, v78, v79\n\t" \
  "v_cvt_pk_bf16_f32 v8, v78, v79\n\t" \
  "v_mov_b64 v[54:55], 0\n\t" \
  "v_mov_b32 v9, 0\n\t" \
  "v_mov_b32 v10, 0\n\t" \
  "v_mov_b64 v[14:15], 0\n\t" \
  "s_nop 0\n\t" \
  "v_mfma_f32_16x16x32_bf16 v[32:35], v[54:57], v[8:11], v[32:35]\n\t" \
  "s_cmp_eq_u32 s48, 63\n\t" \
  "v_mfma_f32_16x16x32_bf16 v[0:3], v[52:55], v[8:11], v[0:3]\n\t" \
  "buffer_store_dwordx2 v[52:53], v71, s[28:31], s69 offen offset:3580 sc1\n\t" \
  "ds_read_b128 v[64:67], v70 offset:25600\n\t" \
  "v_mov_b64 v[56:57], 0\n\t" \
  "v_mov_b64 v[58:59], 0\n\t" \
  "v_mfma_f32_16x16x32_bf16 v[4:7], v[52:55], v[10:13], v[4:7]\n\t" \
  "s_cbranch_scc1 LLAST3_%=\n\t" \
  "v_rcp_f32 v74, v32\n\t" \
  "v_mov_b64 v[12:13], 0\n\t" \
  "v_mov_b32 v8, 0\n\t" \
  "v_pk_mul_f32 v[0:1], v[0:1], v[74:75] op_sel_hi:[1,0]\n\t" \
  "v_pk_mul_f32 v[2:3], v[2:3], v[74:75] op_sel_hi:[1,0]\n\t" \
  "v_cvt_pk_bf16_f32 v76, v0, v1\n\t" \
  "v_cvt_pk_bf16_f32 v77, v2, v3\n\t" \
  "v_pk_mul_f32 v[4:5], v[4:5], v[74:75] op_sel_hi:[1,0]\n\t" \
  "v_pk_mul_f32 v[6:7], v[6:7], v[74:75] op_sel_hi:[1,0]\n\t" \
  "v_cvt_pk_bf16_f32 v78, v4, v5\n\t" \
  "v_cvt_pk_bf16_f32 v79, v6, v7\n\t" \
  "ds_write_b128 v69, v[76:79] offset:7168\n\t" \
  CH_DP9 \
  "s_waitcnt lgkmcnt(2)\n\t" \
  "v_smfmac_f32_16x16x64_bf16 v[12:15], v[76:79], v[44:51], v68\n\t" \
  "ds_read_b128 v[44:47], v70 offset:26624\n\t" \
  "v_mov_b32 v11, 0\n\t" \
  "v_smfmac_f32_16x16x64_bf16 v[56:59], v[76:79], v[16:23], v68\n\t" \
  "ds_read_b128 v[48:51], v70 offset:27648\n\t" \
  "v_mov_b64 v[52:53], 0\n\t" \
  "v_smfmac_f32_16x16x64_bf16 v[8:11], v[76:79], v[36:43], v68\n\t" \
  "ds_read_b128 v[16:19], v70 offset:28672\n\t" \
  "v_smfmac_f32_16x16x64_bf16 v[52:55], v[76:79], v[24:31], v68\n\t" \
  "s_waitcnt lgkmcnt(3)\n\t" \
  "s_barrier\n\t" \
  CH_DP10 \
  "ds_read_b128 v[0:3], v69 offset:6144\n\t" \
  "ds_read_b128 v[4:7], v69 offset:5120\n\t" \
  "v_lshrrev_b32 v71, 7, v69\n\t" \
  "v_and_b32 v74, 0x70, v69\n\t" \
  "v_lshl_add_u32 v71, v71, 14, v74\n\t" \
  "v_bfe_u32 v72, v69, 2, 7\n\t" \
  "v_and_b32 v74, 0x200, v69\n\t" \
  "s_waitcnt lgkmcnt(1)\n\t" \
  "v_smfmac_f32_16x16x64_bf16 v[8:11], v[0:3], v[60:67], v68\n\t" \
  "ds_read_b128 v[20:23], v70 offset:29696\n\t" \
  "v_mov_b64 v[76:77], 0\n\t" \
  "v_smfmac_f32_16x16x64_bf16 v[12:15], v[0:3], v[200:207], v68\n\t" \
  "ds_read_b128 v[32:35], v69 offset:4096\n\t" \
  "v_mov_b64 v[78:79], 0\n\t" \
  "v_smfmac_f32_16x16x64_bf16 v[52:55], v[0:3], v[44:51], v68\n\t" \
  "buffer_load_dword v75, off, s[28:31], s80 sc1\n\t" \
  "ds_read_b128 v[24:27], v70 offset:30720\n\t" \
  "v_lshl_add_u32 v72, v74, 3, v72\n\t" \
  "v_smfmac_f32_16x16x64_bf16 v[56:59], v[0:3], v[232:239], v68\n\t" \
  "ds_read_b128 v[28:31], v70 offset:31744\n\t" \
  "s_waitcnt lgkmcnt(4)\n\t" \
  "v_smfmac_f32_16x16x64_bf16 v[12:15], v[4:7], v[208:215], v68\n\t" \
  "ds_read_b128 v[60:63], v70 offset:32768\n\t" \
  "s_waitcnt lgkmcnt(4)\n\t" \
  "v_smfmac_f32_16x16x64_bf16 v[8:11], v[4:7], v[16:23], v68\n\t" \
  "ds_read_b128 v[64:67], v70 offset:33792\n\t" \
  "s_waitcnt lgkmcnt(4)\n\t" \
  "v_smfmac_f32_16x16x64_bf16 v[8:11], v[32:35], v[192:199], v68\n\t" \
  "ds_read_b128 v[36:39], v73 offset:4096\n\t" \
  "v_smfmac_f32_16x16x64_bf16 v[12:15], v[32:35], v[216:223], v68\n\t" \
  "ds_read_b128 v[40:43], v73 offset:6144\n\t" \
  "s_waitcnt lgkmcnt(4)\n\t" \
  "v_smfmac_f32_16x16x64_bf16 v[52:55], v[4:7], v[24:31], v68\n\t" \
  "ds_read_b128 v[44:47], v70 offset:34816\n\t" \
  "v_cvt_pk_bf16_f32 v8, v8, v9\n\t" \
  "v_smfmac_f32_16x16x64_bf16 v[52:55], v[32:35], v[224:231], v68\n\t" \
  "ds_read_b128 v[48:51], v70 offset:35840\n\t" \
  "v_cvt_pk_bf16_f32 v9, v10, v11\n\t" \
  "v_smfmac_f32_16x16x64_bf16 v[56:59], v[4:7], v[240:247], v68\n\t" \
  "s_waitcnt lgkmcnt(4)\n\t" \
  "v_cvt_pk_bf16_f32 v10, v12, v13\n\t" \
  "v_smfmac_f32_16x16x64_bf16 v[56:59], v[32:35], v[60:67], v68\n\t" \
  "s_waitcnt lgkmcnt(3)\n\t" \
  "v_cvt_pk_bf16_f32 v11, v14, v15\n\t" \
  "v_smfmac_f32_16x16x64_bf16 v[76:79], v[36:39], v[248:255], v68\n\t" \
  "buffer_store_dwordx4 v[8:11], v71, s[28:31], s54 offen sc1\n\t" \
  "s_waitcnt lgkmcnt(0)\n\t" \
  CH_DP11 \
  "v_cvt_pk_bf16_f32 v52, v52, v53\n\t" \
  "v_smfmac_f32_16x16x64_bf16 v[76:79], v[40:43], v[44:51], v68\n\t" \
  "v_cvt_pk_bf16_f32 v53, v54, v55\n\t" \
  "v_cvt_pk_bf16_f32 v54, v56, v57\n\t" \
  "v_cvt_pk_bf16_f32 v55, v58, v59\n\t" \
  "buffer_store_dwordx4 v[52:55], v71, s[28:31], s75 offen sc1\n\t" \
  "s_nop 2\n\t" \
  "v_add_f32 v76, v76, v78\n\t" \
  "v_add_f32 v77, v77, v79\n\t" \
  "v_cvt_pk_bf16_f32 v76, v76, v77\n\t" \
  "buffer_store_dword v76, v72, s[28:31], s55 offen sc1\n\t" \
  CH_DP12 \
  "s_memrealtime s[70:71]\n\t" \
  "s_waitcnt vmcnt(3)\n\t" \
  "v_readfirstlane_b32 s65, v75\n\t" \
  "s_barrier\n\t" \
  "s_waitcnt lgkmcnt(0)\n\t" \
  "s_not_b32 s65, s65\n\t" \
  "s_lshl_b32 s61, s70, 8\n\t" \
  "s_andn2_b32 s64, s65, 0xff\n\t" \
  "s_sub_u32 s64, s64, s61\n\t" \
  "s_ashr_i32 s64, s64, 8\n\t" \
  "s_cmp_eq_u32 s65, 0\n\t" \
  "s_cselect_b32 s64, " CH_DLF ", s64\n\t" \
  "s_add_i32 s64, s64, " CH_RO3 "\n\t" \
  "s_min_i32 s64, s64, s34\n\t" \
  "s_max_i32 s64, s64, " CH_DLL1 "\n\t" \
  "s_add_u32 s64, s64, s70\n\t" \
  CH_ST2 \
  "s_mov_b32 s74, 0\n\t" \
  "v_and_b32 v37, 0x30, v69\n\t" \
  "v_add_u32 v37, s52, v37\n\t" \
  "s_mov_b32 exec_lo, 0\n\t" \
  "v_mov_b32 v37, s85\n\t" \
  "s_mov_b32 exec_lo, -1\n\t" \
  "s_cmp_lt_i32 s41, 0\n\t" \
  "s_cbranch_scc1 LIDLE_%=\n\t" \
  "s_movk_i32 s60, 8192\n\t" \
  "RWT3_%=:\n\t" \
  "s_memrealtime s[72:73]\n\t" \
  "s_sub_u32 s60, s60, 1\n\t" \
  "s_cselect_b32 s61, 0, -1\n\t" \
  "s_waitcnt lgkmcnt(0)\n\t" \
  "s_sub_i32 s73, s72, s64\n\t" \
  "s_and_b32 s73, s73, s61\n\t" \
  "s_cmp_lt_i32 s73, " CH_FIN "\n\t" \
  "s_cbranch_scc1 RWT3_%=\n\t" \
  "s_cmp_lt_i32 s73, 0\n\t" \
  "s_cbranch_scc1 RWT3_%=f\n\t" \
  "RWT3_%=g:\n\t" \
  "RRETRY3_%=:\n\t" \
  CH_DP13 \
  "buffer_load_dwordx4 v[0:3], v69, s[28:31], s97 offen offset:0 sc1\n\t" \
  "buffer_load_dwordx4 v[4:7], v69, s[28:31], s97 offen offset:1024 sc1\n\t" \
  "buffer_load_dwordx4 v[8:11], v69, s[28:31], s97 offen offset:2048 sc1\n\t" \
  "buffer_load_dwordx4 v[12:15], v69, s[28:31], s97 offen offset:3072 sc1\n\t" \
  "v_lshrrev_b32 v20, 2, v69\n\t" \
  "v_and_b32 v20, 16, v20\n\t" \
  "v_mov_b32 v21, 0x3f80\n\t" \
  "v_lshlrev_b32 v21, v20, v21\n\t" \
  "v_bfe_u32 v20, v69, 4, 2\n\t" \
  "v_cmp_eq_u32 vcc, 0, v20\n\t" \
  "s_nop 1\n\t" \
  "v_cndmask_b32 v32, 0, v21, vcc\n\t" \
  "v_cmp_eq_u32 vcc, 1, v20\n\t" \
  "s_nop 1\n\t" \
  "v_cndmask_b32 v33, 0, v21, vcc\n\t" \
  "v_cmp_eq_u32 vcc, 2, v20\n\t" \
  "s_nop 1\n\t" \
  "v_cndmask_b32 v34, 0, v21, vcc\n\t" \
  "v_cmp_eq_u32 vcc, 3, v20\n\t" \
  "s_nop 1\n\t" \
  "v_cndmask_b32 v35, 0, v21, vcc\n\t" \
  "v_bfe_u32 v38, v69, 7, 1\n\t" \
  "v_bfe_u32 v20, v69, 9, 1\n\t" \
  "v_lshl_add_u32 v38, v20, 2, v38\n\t" \
  "v_lshlrev_b32 v38, 12, v38\n\t" \
  "v_and_b32 v20, 0x60, v69\n\t" \
  "v_add_u32 v38, v38, v20\n\t" \
  "v_bfe_u32 v20, v69, 8, 1\n\t" \
  "v_lshl_add_u32 v38, v20, 4, v38\n\t" \
  "v_bfe_u32 v20, v69, 4, 1\n\t" \
  "v_lshl_add_u32 v38, v20, 3, v38\n\t" \
  "v_mov_b32 v44, 0\n\t" \
  "buffer_load_dword v39, v37, s[28:31], 0 offen sc1\n\t" \
  CH_DP17 \
  "s_waitcnt vmcnt(4)\n\t" \
  CH_DP14 \
  "v_mfma_f32_16x16x32_bf16 v[40:43], v[32:35], v[0:3], 0\n\t" \
  "v_max3_u32 v44, v44, v0, v1\n\t" \
  "v_max3_u32 v44, v44, v2, v3\n\t" \
  "s_waitcnt vmcnt(3)\n\t" \
  "v_mfma_f32_16x16x32_bf16 v[40:43], v[32:35], v[4:7], v[40:43]\n\t" \
  "v_max3_u32 v44, v44, v4, v5\n\t" \
  "v_max3_u32 v44, v44, v6, v7\n\t" \
  "s_waitcnt vmcnt(2)\n\t" \
  "v_mfma_f32_16x16x32_bf16 v[40:43], v[32:35], v[8:11], v[40:43]\n\t" \
  "v_max3_u32 v44, v44, v8, v9\n\t" \
  "v_max3_u32 v44, v44, v10, v11\n\t" \
  "s_waitcnt vmcnt(1)\n\t" \
  CH_DP15 \
  "v_mfma_f32_16x16x32_bf16 v[40:43], v[32:35], v[12:15], v[40:43]\n\t" \
  "v_max3_u32 v44, v44, v12, v13\n\t" \
  "v_max3_u32 v44, v44, v14, v15\n\t" \
  "v_cmp_eq_u32 vcc, -1, v44\n\t" \
  "s_cbranch_vccnz RFAIL3_%=\n\t" \
  "s_nop 3\n\t" \
  "v_add_f32_dpp v40, v40, v40 row_ror:8 row_mask:0xf bank_mask:0xf\n\t" \
  "v_add_f32_dpp v41, v41, v41 row_ror:8 row_mask:0xf bank_mask:0xf\n\t" \
  "v_add_f32_dpp v42, v42, v42 row_ror:8 row_mask:0xf bank_mask:0xf\n\t" \
  "v_add_f32_dpp v43, v43, v43 row_ror:8 row_mask:0xf bank_mask:0xf\n\t" \
  "v_cvt_pk_bf16_f32 v40, v40, v41\n\t" \
  "v_cvt_pk_bf16_f32 v41, v42, v43\n\t" \
  "buffer_store_dwordx2 v[40:41], v38, s[28:31], s98 offen sc1\n\t" \
  "RDONE3_%=:\n\t" \
  CH_DP16 \
  CH_ST4 \
  "s_barrier\n\t" \
  CH_ST3 \
  "s_waitcnt vmcnt(1)\n\t" \
  "v_cmp_eq_u32 vcc, 0, v39\n\t" \
  "s_sub_u32 s60, s70, s66\n\t" \
  "s_add_u32 s81, s81, 1\n\t" \
  "s_min_u32 s81, s81, s60\n\t" \
  "s_add_u32 s61, s81, 8\n\t" \
  "s_min_u32 s60, s60, s61\n\t" \
  "s_lshl_b32 s60, s60, 2\n\t" \
  "s_lshr_b32 s61, s20, 3\n\t" \
  "s_sub_u32 s20, s20, s61\n\t" \
  "s_add_u32 s20, s20, s60\n\t" \
  CH_TRC \
  "s_add_u32 s48, s48, 1\n\t" \
  "s_mov_b32 s49, s53\n\t" \
  "s_add_u32 s53, s53, " CH_SLOT "\n\t" \
  "s_add_u32 s60, s53, " CH_SKIP "\n\t" \
  "s_lshr_b32 s61, s96, 1\n\t" \
  "s_add_u32 s86, s86, 1\n\t" \
  "s_cmp_eq_u32 s86, 3\n\t" \
  "s_cselect_b32 s53, s60, s53\n\t" \
  "s_cselect_b32 s96, s61, s96\n\t" \
  "s_cselect_b32 s86, 0, s86\n\t" \
  "s_cmp_ge_u32 s53, " CH_RING "\n\t" \
  "s_cselect_b32 s53, 0, s53\n\t" \
  "s_cselect_b32 s96, s21, s96\n\t" \
  "s_andn2_b32 s53, s53, 0x2000\n\t" \
  "s_and_b32 s60, s96, 1\n\t" \
  "s_lshl_b32 s60, s60, 13\n\t" \
  "s_or_b32 s53, s53, s60\n\t" \
  "s_mov_b32 s68, 0\n\t" \
  "s_add_u32 s97, s41, s53\n\t" \
  "s_add_u32 s98, s37, s53\n\t" \
  "s_add_u32 s82, s48, 0\n\t" \
  "s_and_b32 s82, s82, " CH_TRM "\n\t" \
  CH_SEGC \
  "s_lshr_b64 s[60:61], s[90:91], s82\n\t" \
  "s_and_b32 s60, s60, 1\n\t" \
  "s_lshl_b32 s60, s60, 13\n\t" \
  "s_add_u32 s82, s82, " CH_SG0 "\n\t" \
  "s_lshl_b32 s82, s82, 21\n\t" \
  "s_or_b32 s82, s82, s60\n\t" \
  "s_add_u32 s50, s82, s38\n\t" \
  "s_add_u32 s69, s82, s39\n\t" \
  "s_add_u32 s82, s48, 1\n\t" \
  CH_SEGC \
  "s_lshr_b64 s[60:61], s[90:91], s82\n\t" \
  "s_and_b32 s60, s60, 1\n\t" \
  "s_lshl_b32 s60, s60, 13\n\t" \
  "s_add_u32 s82, s82, " CH_SG0 "\n\t" \
  "s_lshl_b32 s82, s82, 21\n\t" \
  "s_or_b32 s82, s82, s60\n\t" \
  "s_add_u32 s52, s82, s43\n\t" \
  "s_add_u32 s82, s48, " CH_ARMD "\n\t" \
  CH_SEGC \
  "s_lshr_b64 s[60:61], s[90:91], s82\n\t" \
  "s_and_b32 s60, s60, 1\n\t" \
  "s_lshl_b32 s60, s60, 13\n\t" \
  "s_add_u32 s82, s82, " CH_SG0 "\n\t" \
  "s_lshl_b32 s82, s82, 21\n\t" \
  "s_or_b32 s82, s82, s60\n\t" \
  "s_add_u32 s85, s82, s45\n\t" \
  "s_cmp_lt_i32 s48, " CH_ARMS "\n\t" \
  "s_cselect_b32 s85, s27, s85\n\t" \
  "s_cmp_eq_u32 s34, 0\n\t" \
  "s_cbranch_scc1 LINI_%=\n\t" \
  "LTK_%=:\n\t" \
  CH_TOKCHK \
  "s_mov_b32 s61, 0\n\t" \
  "LTW_%=:\n\t" \
  "buffer_load_dword v1, v37, s[28:31], 0 offen sc1\n\t" \
  "s_waitcnt vmcnt(0)\n\t" \
  "v_cmp_eq_u32 vcc, 0, v1\n\t" \
  "s_cbranch_vccz LG3_%=\n\t" \
  "s_add_u32 s61, s61, 1\n\t" \
  "s_sleep 2\n\t" \
  "s_cmp_gt_u32 s61, s84\n\t" \
  "s_cbranch_scc1 LTG_%=\n\t" \
  "s_and_b32 s60, s61, 1023\n\t" \
  "s_cmp_lg_u32 s60, 0\n\t" \
  "s_cbranch_scc1 LTW_%=\n\t" \
  "buffer_load_dword v46, off, s[28:31], s92 sc1\n\t" \
  "s_waitcnt vmcnt(0)\n\t" \
  "v_readfirstlane_b32 s60, v46\n\t" \
  "s_nop 0\n\t" \
  "s_cmp_eq_u32 s60, 0\n\t" \
  "s_cbranch_scc1 LTW_%=\n\t" \
  "LTG_%=:\n\t" \
  "s_mov_b32 s84, 0\n\t" \
  "s_lshl_b32 s60, s36, 4\n\t" \
  "s_or_b32 s60, s60, s48\n\t" \
  "s_or_b32 s60, s60, 268435456\n\t" \
  "v_mov_b32 v76, s60\n\t" \
  "v_mov_b32 v77, 0\n\t" \
  "s_mov_b64 exec, 1\n\t" \
  "buffer_atomic_cmpswap v[76:77], off, s[28:31], s92 sc1\n\t" \
  "s_mov_b64 exec, -1\n\t" \
  "s_branch LG3_%=\n\t" \
  "LIDLE_%=:\n\t" \
  "s_add_u32 s64, s64, 8\n\t" \
  "s_movk_i32 s60, 8192\n\t" \
  "IWT_%=:\n\t" \
  "s_memrealtime s[72:73]\n\t" \
  "s_sub_u32 s60, s60, 1\n\t" \
  "s_cselect_b32 s61, 0, -1\n\t" \
  "s_waitcnt lgkmcnt(0)\n\t" \
  "s_sub_i32 s73, s72, s64\n\t" \
  "s_and_b32 s73, s73, s61\n\t" \
  "s_cmp_lt_i32 s73, 0\n\t" \
  "s_cbranch_scc1 IWT_%=\n\t" \
  "buffer_load_dword v39, v37, s[28:31], 0 offen sc1\n\t" \
  "buffer_store_dword v37, off, s[28:31], s41\n\t" \
  "s_branch RDONE3_%=\n\t" \
  "RFAIL3_%=:\n\t" \
  "s_add_u32 s74, s74, 1\n\t" \
  "s_sleep " CH_RSL "\n\t" \
  "s_waitcnt vmcnt(0)\n\t" \
  "s_cmp_gt_u32 s74, s84\n\t" \
  "s_cbranch_scc1 RG3_%=\n\t" \
  "s_and_b32 s60, s74, 1023\n\t" \
  "s_cmp_lg_u32 s60, 0\n\t" \
  "s_cbranch_scc1 RRETRY3_%=\n\t" \
  "buffer_load_dword v46, off, s[28:31], s92 sc1\n\t" \
  "s_waitcnt vmcnt(0)\n\t" \
  "v_readfirstlane_b32 s60, v46\n\t" \
  "s_nop 0\n\t" \
  "s_cmp_eq_u32 s60, 0\n\t" \
  "s_cbranch_scc1 RRETRY3_%=\n\t" \
  "RG3_%=:\n\t" \
  "s_mov_b32 s84, 0\n\t" \
  "s_lshl_b32 s60, s36, 4\n\t" \
  "s_or_b32 s60, s60, s48\n\t" \
  "s_or_b32 s60, s60, 536870912\n\t" \
  "v_mov_b32 v76, s60\n\t" \
  "v_mov_b32 v77, 0\n\t" \
  "s_mov_b64 exec, 1\n\t" \
  "buffer_atomic_cmpswap v[76:77], off, s[28:31], s92 sc1\n\t" \
  "s_mov_b64 exec, -1\n\t" \
  "s_branch RDONE3_%=\n\t" \
  "LFAIL3_%=:\n\t" \
  "v_max_u32 v75, v0, v1\n\t" \
  "v_max3_u32 v75, v75, v2, v3\n\t" \
  "v_max3_u32 v75, v75, v4, v5\n\t" \
  "v_max3_u32 v75, v75, v6, v7\n\t" \
  "v_max3_u32 v75, v75, v8, v9\n\t" \
  "v_max3_u32 v75, v75, v10, v11\n\t" \
  "v_max3_u32 v75, v75, v12, v13\n\t" \
  "v_max3_u32 v75, v75, v14, v15\n\t" \
  "v_max3_u32 v75, v75, v16, v17\n\t" \
  "v_max3_u32 v75, v75, v18, v19\n\t" \
  "v_max3_u32 v75, v75, v20, v21\n\t" \
  "v_max3_u32 v75, v75, v22, v23\n\t" \
  "v_max3_u32 v75, v75, v24, v25\n\t" \
  "v_max3_u32 v75, v75, v26, v27\n\t" \
  "v_max3_u32 v75, v75, v28, v29\n\t" \
  "v_max3_u32 v75, v75, v30, v31\n\t" \
  "v_max3_u32 v75, v75, v32, v33\n\t" \
  "v_max3_u32 v75, v75, v34, v35\n\t" \
  "v_cmp_eq_u32 vcc, -1, v75\n\t" \
  "s_cbranch_vccz LGO3_%=\n\t" \
  "s_add_u32 s68, s68, 1\n\t" \
  "s_waitcnt lgkmcnt(0)\n\t" \
  "s_cmp_gt_u32 s68, s83\n\t" \
  "s_cbranch_scc1 LGV3_%=\n\t" \
  "s_barrier\n\t" \
  "s_waitcnt vmcnt(8)\n\t" \
  "s_branch LG3_%=\n\t" \
  "RWT3_%=f:\n\t" \
  "s_cmp_lt_i32 s73, -2\n\t" \
  "s_cbranch_scc0 RWT3_%=g\n\t" \
  "s_nop 4\n\t" \
  "s_branch RWT3_%=g\n\t" \
  "LGV3_%=:\n\t" \
  "s_mov_b32 s83, 0\n\t" \
  "s_lshl_b32 s60, s36, 4\n\t" \
  "s_or_b32 s60, s60, s48\n\t" \
  "s_or_b32 s60, s60, 805306368\n\t" \
  "v_mov_b32 v76, s60\n\t" \
  "v_mov_b32 v77, 0\n\t" \
  "s_mov_b64 exec, 1\n\t" \
  "buffer_atomic_cmpswap v[76:77], off, s[28:31], s92 sc1\n\t" \
  "s_mov_b64 exec, -1\n\t" \
  "s_branch LGO3_%=\n\t" \
  "LLAST3_%=:\n\t" \
  "s_waitcnt vmcnt(0) lgkmcnt(0)\n\t" \
  "s_barrier\n\t" \
  "LEND_%=:\n\t" \
  ""
typedef unsigned v16u __attribute__((ext_vector_type(16)));
#define CH_WCLOB "a0", "a1", "a2", "a3", "a4", "a5", "a6", "a7", "a8", "a9", "a10", "a11", "a12", "a13", "a14", "a15", "a16", "a17", "a18", "a19", "a20", "a21", "a22", "a23", "a24", "a25", "a26", "a27", "a28", "a29", "a30", "a31", "a32", "a33", "a34", "a35", "a36", "a37", "a38", "a39", "a40", "a41", "a42", "a43", "a44", "a45", "a46", "a47", "a48", "a49", "a50", "a51", "a52", "a53", "a54", "a55", "a56", "a57", "a58", "a59", "a60", "a61", "a62", "a63", "a64", "a65", "a66", "a67", "a68", "a69", "a70", "a71", "a72", "a73", "a74", "a75", "a76", "a77", "a78", "a79", "a80", "a81", "a82", "a83", "a84", "a85", "a86", "a87", "a88", "a89", "a90", "a91", "a92", "a93", "a94", "a95", "a96", "a97", "a98", "a99", "a100", "a101", "a102", "a103", "a104", "a105", "a106", "a107", "a108", "a109", "a110", "a111", "a112", "a113", "a114", "a115", "a116", "a117", "a118", "a119", "a120", "a121", "a122", "a123", "a124", "a125", "a126", "a127", "a128", "a129", "a130", "a131", "a132", "a133", "a134", "a135", "a136", "a137", "a138", "a139", "a140", "a141", "a142", "a143", "a144", "a145", "a146", "a147", "a148", "a149", "a150", "a151", "a152", "a153", "a154", "a155", "a156", "a157", "a158", "a159", "a160", "a161", "a162", "a163", "a164", "a165", "a166", "a167", "a168", "a169", "a170", "a171", "a172", "a173", "a174", "a175", "a176", "a177", "a178", "a179", "a180", "a181", "a182", "a183", "a184", "a185", "a186", "a187", "a188", "a189", "a190", "a191", "a192", "a193", "a194", "a195", "a196", "a197", "a198", "a199", "a200", "a201", "a202", "a203", "a204", "a205", "a206", "a207", "a208", "a209", "a210", "a211", "a212", "a213", "a214", "a215", "a216", "a217", "a218", "a219", "a220", "a221", "a222", "a223", "a224", "a225", "a226", "a227", "a228", "a229", "a230", "a231", "a232", "a233", "a234", "a235", "a236", "a237", "a238", "a239", "a240", "a241", "a242", "a243", "a244", "a245", "a246", "a247", "a248", "a249", "a250", "a251", "a252", "a253", "a254", "a255", "v80", "v81", "v82", "v83", "v84", "v85", "v86", "v87", "v88", "v89", "v90", "v91", "v92", "v93", "v94", "v95", "v96", "v97", "v98", "v99", "v100", "v101", "v102", "v103", "v104", "v105", "v106", "v107", "v108", "v109", "v110", "v111", "v112", "v113", "v114", "v115", "v116", "v117", "v118", "v119", "v120", "v121", "v122", "v123", "v124", "v125", "v126", "v127", "v128", "v129", "v130", "v131", "v132", "v133", "v134", "v135", "v136", "v137", "v138", "v139", "v140", "v141", "v142", "v143", "v144", "v145", "v146", "v147", "v148", "v149", "v150", "v151", "v152", "v153", "v154", "v155", "v156", "v157", "v158", "v159", "v160", "v161", "v162", "v163", "v164", "v165", "v166", "v167", "v168", "v169", "v170", "v171", "v172", "v173", "v174", "v175", "v176", "v177", "v178", "v179", "v180", "v181", "v182", "v183", "v184", "v185", "v186", "v187", "v188", "v189", "v190", "v191", "v192", "v193", "v194", "v195", "v196", "v197", "v198", "v199", "v200", "v201", "v202", "v203", "v204", "v205", "v206", "v207", "v208", "v209", "v210", "v211", "v212", "v213", "v214", "v215", "v216", "v217", "v218", "v219", "v220", "v221", "v222", "v223", "v224", "v225", "v226", "v227", "v228", "v229", "v230", "v231", "v232", "v233", "v234", "v235", "v236", "v237", "v238", "v239", "v240", "v241", "v242", "v243", "v244", "v245", "v246", "v247", "v248", "v249", "v250", "v251", "v252", "v253", "v254", "v255"
#define CH_H(b) (8 * ((b) & 3) + ((b) >> 3))
#define CH_L(b) (((b) >> 2) & 1)
__device__ __forceinline__ void start_work(char* smem, int wk);
__device__ __forceinline__ void chain_wg(char* smem) {
  if (CH_L((int)blockIdx.x)) start_work(smem, CH_H((int)blockIdx.x));
  const MP p = getp();
  const int b = (int)blockIdx.x, h = CH_H(b), L = CH_L(b);
  int w = __builtin_amdgcn_readfirstlane(threadIdx.x >> 6);
  {
    unsigned id; asm volatile("s_getreg_b32 %0, hwreg(HW_REG_HW_ID, 4, 2)" : "=s"(id));
    volatile __attribute__((address_space(3))) unsigned* lw = (volatile __attribute__((address_space(3))) unsigned*)smem;
    lw[w] = 1u << id;
    BAR();
    if (__builtin_amdgcn_readfirstlane(lw[0] | lw[1] | lw[2] | lw[3]) == 15u) w = (int)((SMAP >> (4u * id)) & 3u);
  }
  unsigned lp16 = (threadIdx.x & 63u) * 16u;
  rsrc_t X = mkr(p.xb, XB_BYTES);
  if ((unsigned)(size_t)(__attribute__((address_space(3))) char*)smem != 0u) return;
  const unsigned wsz = DM * HD * 2u - (unsigned)(h * DH) * 2u;
  const rsrc_t RQ = mkr(p.Wq + h * DH, wsz), RK = mkr(p.Wk + h * DH, wsz), RV = mkr(p.Wv + h * DH, wsz), RO = mkr(p.Wo + (long)h * DH * DM, DH * DM * 2u);
  unsigned xcc; asm volatile("s_getreg_b32 %0, hwreg(HW_REG_XCC_ID)" : "=s"(xcc)); xcc &= 3u;
  unsigned d96 = 0u;
  const unsigned c43 = P_FA * PAIR + (unsigned)h * 128u + 32u;
  const unsigned h128 = (unsigned)h * 128u, uw = (unsigned)w, t = uw >> 1, T = uw & 1u, NONE = 0xF0000000u;
  const unsigned u0 = w == 3 ? (unsigned)h >> 2 : (unsigned)((h & 1) + (h >> 3) * 2 + ((h >> 1) & 3) * 8 + (w == 2 ? 32 : 0));
  const bool idle = w == 3 && (h & 3);
  const unsigned c37 = idle ? NONE : (w == 3 ? AGT + u0 * 128u : AGO + (u0 >> 5) * 32768u + (u0 & 31u) * 128u);
  const unsigned c41 = idle ? NONE : (w == 3 ? 524288u + (u0 >> 1) * 16384u + (u0 & 1u) * 4096u : (u0 & 7u) * 16384u + ((u0 >> 3) & 1u) * 4096u + (u0 >> 4) * 131072u);
  const unsigned c42 = AGO + (xcc & 1u) * 16384u + (xcc >> 1) * 4096u;
  const unsigned c46 = (w == 3 ? (h & 7) == 5 : h == 30) ? AGT + 1024u : NONE;
  const unsigned c38 = (unsigned)(3 * h + 1) * PAIR + T * 4096u + t * 2048u, c39 = (P_KV + (unsigned)(h >> 1)) * PAIR + (unsigned)(h & 1) * 4096u;
  const unsigned c45 = P_FB * PAIR + 4096u + (unsigned)(16 + (h & 15)) * 128u;
  unsigned d48 = 0u, d49 = 0u, d53 = SLOT, d86 = 1u, d65 = rtc();
  unsigned mn = 120u;
  v4u stt;
  asm volatile("s_waitcnt vmcnt(0) lgkmcnt(0)\n\t" CH_ASM "LCHEND:"
               : "+{s48}"(d48), "+{s49}"(d49), "+{s53}"(d53), "+{s65}"(d65), "+{s81}"(mn), "+{s86}"(d86), "+{s96}"(d96), "=&{s[76:79]}"(stt), "+{v69}"(lp16), "+{s[28:31]}"(X)
               : [Wq] "s"(RQ), [Wk] "s"(RK), [Wv] "s"(RV), [Wo] "s"(RO), "{s33}"(HT_OFF + 8192u + 256u + (unsigned)b * 4u), "{s36}"(h128), "{s37}"(c37), "{s40}"(w), "{s41}"(c41), "{s42}"(c42), "{s46}"(c46), "{s92}"(TMO_OFF), "{s38}"(c38), "{s39}"(c39), "{s43}"(c43), "{s45}"(c45), "{s32}"(L), "{s26}"(AGT + 1024u + xcc * 128u), "{s27}"(RDY_OFF(0) + (unsigned)((h & 7) * 1024 + (b & 7) * 128)), [ctab] "i"(CTAB_OFF), [ring] "i"(SRING / 3 * XBLK), [trm] "i"(TRING - 1), [ex] "i"(ED * 256 + EB), [dl1l] "i"(DL1 - LATE), [armd] "i"(3 - ARMK), [arms] "i"(ARMK - 3),
                 [cap] "i"(SPIN_CAP)
               : "v0", "v1", "v2", "v3", "v4", "v5", "v6", "v7", "v8", "v9", "v10", "v11", "v12", "v13", "v14", "v15", "v16", "v17", "v18", "v19",
                 "v20", "v21", "v22", "v23", "v24", "v25", "v26", "v27", "v28", "v29", "v30", "v31", "v32", "v33", "v34", "v35", "v36", "v37", "v38", "v39",
                 "v40", "v41", "v42", "v43", "v44", "v45", "v46", "v47", "v48", "v49", "v50", "v51", "v52", "v53", "v54", "v55", "v56", "v57", "v58", "v59",
                 "v60", "v61", "v62", "v63", "v64", "v65", "v66", "v67", "v68", "v70", "v71", "v72", "v73", "v74", "v75", "v76", "v77", "v78", "v79",
                 CH_WCLOB, "s20", "s21", "s34", "s35", "s24", "s25", "s90", "s91", "s47", "s87", "s50", "s51", "s52", "s69", "s82", "s85", "s54", "s75", "s80", "s55", "s56", "s57", "s58", "s59", "s60", "s61", "s62", "s63", "s64", "s66", "s67", "s68", "s70", "s71", "s72", "s73", "s74", "s88", "s89",
                 "s83", "s84", "s97", "s98", "s99", "s93", "s94", "s95", "m0", "vcc", "scc", "memory");
}
#define XQ 0u
#define XQL 0u
#define XK 0u
#define XT 0u
#define XA 0u
#define XAL 0u
#define XRL 0u
#define QLEAD 4
#define RONE 2
#define OAUX 0
#define PFM 1
#define PSP 4
#define PSR 8
#define RDLY 40
#define QLEAD1 2
#define QXW -18
#define QXC QXW
#define TK_QXW "%[qxw]"
#define TK_QXC "%[qxc]"
#define PSG 16
#define TK_PSG CH_S(PSG)
#define AX 30
#define AX1 45
#define AM 22
#define TK_AM CH_S(AM)
#define PSK 2
#define PSH 2
#define TK_CAP "%[cap]"
#define TK_CTAB "%[ctab]"
#define TK_RM "%[rm]"
#define TK_TRM "%[trm]"
#define TK_TRN "%[trn]"
#define TK_SG0 CH_S(SG0)
#define TK_PSK CH_S(PSK)
#define TK_PSH CH_S(PSH)
#define TK_PA0
#define TK_PA1
#define TK_PA2
#define TK_PQ0
#define TK_PQ1
#define TK_PQ2
#define TT(s, i)
#define TK_AC(n)
#define TK_ACE
#define TK_AC0 TK_AC(212)
#define TK_AC1 TK_AC(213)
#define TK_AC2 TK_AC(214)
#define TK_AC3 TK_AC(215)
#define TK_AC4 TK_AC(216)
#define TK_AC5 TK_AC(217)
#define TK_AC6 TK_AC(218)
#define TK_AC7 TK_AC(219)
#define TK_AC8 TK_AC(220)
#define AP_ASM \
  "v_lshrrev_b32 v40, 8, v0\n\t" \
  "v_mul_u32_u24 v40, 4608, v40\n\t" \
  "v_and_b32 v41, 0xf0, v0\n\t" \
  "v_lshrrev_b32 v42, 3, v41\n\t" \
  "v_add3_u32 v41, v41, v40, s64\n\t" \
  "v_add3_u32 v42, v42, v40, s65\n\t" \
  "s_mov_b32 s62, 0x05040100\n\t" \
  "s_mov_b32 s63, 0x07060302\n\t" \
  "s_xor_b32 s60, s66, 0\n\t" \
  "s_add_u32 s60, s60, 0\n\t" \
  "s_add_u32 s61, s60, 18432\n\t" \
  "buffer_load_dwordx4 v[48:51], v41, %[W], s60 offen offset:0\n\t" \
  "buffer_load_dwordx4 v[52:55], v41, %[W], s60 offen offset:1152\n\t" \
  "buffer_load_dwordx4 v[56:59], v41, %[W], s60 offen offset:2304\n\t" \
  "buffer_load_dwordx4 v[60:63], v41, %[W], s60 offen offset:3456\n\t" \
  "buffer_load_dwordx4 v[64:67], v41, %[W], s61 offen offset:0\n\t" \
  "buffer_load_dwordx4 v[68:71], v41, %[W], s61 offen offset:1152\n\t" \
  "buffer_load_dwordx4 v[72:75], v41, %[W], s61 offen offset:2304\n\t" \
  "buffer_load_dwordx4 v[76:79], v41, %[W], s61 offen offset:3456\n\t" \
  "s_xor_b32 s60, s66, 0\n\t" \
  "s_add_u32 s60, s60, 36864\n\t" \
  "s_add_u32 s61, s60, 18432\n\t" \
  "buffer_load_dwordx4 v[80:83], v41, %[W], s60 offen offset:0\n\t" \
  "buffer_load_dwordx4 v[84:87], v41, %[W], s60 offen offset:1152\n\t" \
  "buffer_load_dwordx4 v[88:91], v41, %[W], s60 offen offset:2304\n\t" \
  "buffer_load_dwordx4 v[92:95], v41, %[W], s60 offen offset:3456\n\t" \
  "buffer_load_dwordx4 v[96:99], v41, %[W], s61 offen offset:0\n\t" \
  "buffer_load_dwordx4 v[100:103], v41, %[W], s61 offen offset:1152\n\t" \
  "buffer_load_dwordx4 v[104:107], v41, %[W], s61 offen offset:2304\n\t" \
  "buffer_load_dwordx4 v[108:111], v41, %[W], s61 offen offset:3456\n\t" \
  "s_xor_b32 s60, s66, 0\n\t" \
  "s_add_u32 s60, s60, 73728\n\t" \
  "s_add_u32 s61, s60, 18432\n\t" \
  "buffer_load_dwordx4 v[112:115], v41, %[W], s60 offen offset:0\n\t" \
  "buffer_load_dwordx4 v[116:119], v41, %[W], s60 offen offset:1152\n\t" \
  "buffer_load_dwordx4 v[120:123], v41, %[W], s60 offen offset:2304\n\t" \
  "buffer_load_dwordx4 v[124:127], v41, %[W], s60 offen offset:3456\n\t" \
  "buffer_load_dwordx4 v[128:131], v41, %[W], s61 offen offset:0\n\t" \
  "buffer_load_dwordx4 v[132:135], v41, %[W], s61 offen offset:1152\n\t" \
  "buffer_load_dwordx4 v[136:139], v41, %[W], s61 offen offset:2304\n\t" \
  "buffer_load_dwordx4 v[140:143], v41, %[W], s61 offen offset:3456\n\t" \
  "s_xor_b32 s60, s66, 0\n\t" \
  "s_add_u32 s60, s60, 110592\n\t" \
  "s_add_u32 s61, s60, 18432\n\t" \
  "buffer_load_dwordx4 v[144:147], v41, %[W], s60 offen offset:0\n\t" \
  "buffer_load_dwordx4 v[148:151], v41, %[W], s60 offen offset:1152\n\t" \
  "buffer_load_dwordx4 v[152:155], v41, %[W], s60 offen offset:2304\n\t" \
  "buffer_load_dwordx4 v[156:159], v41, %[W], s60 offen offset:3456\n\t" \
  "buffer_load_dwordx4 v[160:163], v41, %[W], s61 offen offset:0\n\t" \
  "buffer_load_dwordx4 v[164:167], v41, %[W], s61 offen offset:1152\n\t" \
  "buffer_load_dwordx4 v[168:171], v41, %[W], s61 offen offset:2304\n\t" \
  "buffer_load_dwordx4 v[172:175], v41, %[W], s61 offen offset:3456\n\t" \
  "s_xor_b32 s60, s66, 147456\n\t" \
  "s_add_u32 s60, s60, 0\n\t" \
  "s_add_u32 s61, s60, 18432\n\t" \
  "buffer_load_dwordx4 v[176:179], v41, %[W], s60 offen offset:0\n\t" \
  "buffer_load_dwordx4 v[180:183], v41, %[W], s60 offen offset:1152\n\t" \
  "buffer_load_dwordx4 v[184:187], v41, %[W], s60 offen offset:2304\n\t" \
  "buffer_load_dwordx4 v[188:191], v41, %[W], s60 offen offset:3456\n\t" \
  "buffer_load_dwordx4 v[192:195], v41, %[W], s61 offen offset:0\n\t" \
  "buffer_load_dwordx4 v[196:199], v41, %[W], s61 offen offset:1152\n\t" \
  "buffer_load_dwordx4 v[200:203], v41, %[W], s61 offen offset:2304\n\t" \
  "buffer_load_dwordx4 v[204:207], v41, %[W], s61 offen offset:3456\n\t" \
  "s_waitcnt vmcnt(38)\n\t" \
  "v_perm_b32 v44, v52, v48, s62\n\t" \
  "s_waitcnt vmcnt(36)\n\t" \
  "v_perm_b32 v45, v60, v56, s62\n\t" \
  "s_waitcnt vmcnt(34)\n\t" \
  "v_perm_b32 v46, v68, v64, s62\n\t" \
  "s_waitcnt vmcnt(32)\n\t" \
  "v_perm_b32 v47, v76, v72, s62\n\t" \
  "v_accvgpr_write_b32 a0, v44\n\t" \
  "v_accvgpr_write_b32 a1, v45\n\t" \
  "v_accvgpr_write_b32 a2, v46\n\t" \
  "v_accvgpr_write_b32 a3, v47\n\t" \
  "v_perm_b32 v44, v52, v48, s63\n\t" \
  "v_perm_b32 v45, v60, v56, s63\n\t" \
  "v_perm_b32 v46, v68, v64, s63\n\t" \
  "v_perm_b32 v47, v76, v72, s63\n\t" \
  "v_accvgpr_write_b32 a4, v44\n\t" \
  "v_accvgpr_write_b32 a5, v45\n\t" \
  "v_accvgpr_write_b32 a6, v46\n\t" \
  "v_accvgpr_write_b32 a7, v47\n\t" \
  "v_perm_b32 v44, v53, v49, s62\n\t" \
  "v_perm_b32 v45, v61, v57, s62\n\t" \
  "v_perm_b32 v46, v69, v65, s62\n\t" \
  "v_perm_b32 v47, v77, v73, s62\n\t" \
  "v_accvgpr_write_b32 a8, v44\n\t" \
  "v_accvgpr_write_b32 a9, v45\n\t" \
  "v_accvgpr_write_b32 a10, v46\n\t" \
  "v_accvgpr_write_b32 a11, v47\n\t" \
  "v_perm_b32 v44, v53, v49, s63\n\t" \
  "v_perm_b32 v45, v61, v57, s63\n\t" \
  "v_perm_b32 v46, v69, v65, s63\n\t" \
  "v_perm_b32 v47, v77, v73, s63\n\t" \
  "v_accvgpr_write_b32 a12, v44\n\t" \
  "v_accvgpr_write_b32 a13, v45\n\t" \
  "v_accvgpr_write_b32 a14, v46\n\t" \
  "v_accvgpr_write_b32 a15, v47\n\t" \
  "v_perm_b32 v44, v54, v50, s62\n\t" \
  "v_perm_b32 v45, v62, v58, s62\n\t" \
  "v_perm_b32 v46, v70, v66, s62\n\t" \
  "v_perm_b32 v47, v78, v74, s62\n\t" \
  "v_accvgpr_write_b32 a16, v44\n\t" \
  "v_accvgpr_write_b32 a17, v45\n\t" \
  "v_accvgpr_write_b32 a18, v46\n\t" \
  "v_accvgpr_write_b32 a19, v47\n\t" \
  "v_perm_b32 v44, v54, v50, s63\n\t" \
  "v_perm_b32 v45, v62, v58, s63\n\t" \
  "v_perm_b32 v46, v70, v66, s63\n\t" \
  "v_perm_b32 v47, v78, v74, s63\n\t" \
  "v_accvgpr_write_b32 a20, v44\n\t" \
  "v_accvgpr_write_b32 a21, v45\n\t" \
  "v_accvgpr_write_b32 a22, v46\n\t" \
  "v_accvgpr_write_b32 a23, v47\n\t" \
  "v_perm_b32 v44, v55, v51, s62\n\t" \
  "v_perm_b32 v45, v63, v59, s62\n\t" \
  "v_perm_b32 v46, v71, v67, s62\n\t" \
  "v_perm_b32 v47, v79, v75, s62\n\t" \
  "v_accvgpr_write_b32 a24, v44\n\t" \
  "v_accvgpr_write_b32 a25, v45\n\t" \
  "v_accvgpr_write_b32 a26, v46\n\t" \
  "v_accvgpr_write_b32 a27, v47\n\t" \
  "v_perm_b32 v44, v55, v51, s63\n\t" \
  "v_perm_b32 v45, v63, v59, s63\n\t" \
  "v_perm_b32 v46, v71, v67, s63\n\t" \
  "v_perm_b32 v47, v79, v75, s63\n\t" \
  "v_accvgpr_write_b32 a28, v44\n\t" \
  "v_accvgpr_write_b32 a29, v45\n\t" \
  "v_accvgpr_write_b32 a30, v46\n\t" \
  "v_accvgpr_write_b32 a31, v47\n\t" \
  "s_xor_b32 s60, s66, 147456\n\t" \
  "s_add_u32 s60, s60, 36864\n\t" \
  "s_add_u32 s61, s60, 18432\n\t" \
  "buffer_load_dwordx4 v[48:51], v41, %[W], s60 offen offset:0\n\t" \
  "buffer_load_dwordx4 v[52:55], v41, %[W], s60 offen offset:1152\n\t" \
  "buffer_load_dwordx4 v[56:59], v41, %[W], s60 offen offset:2304\n\t" \
  "buffer_load_dwordx4 v[60:63], v41, %[W], s60 offen offset:3456\n\t" \
  "buffer_load_dwordx4 v[64:67], v41, %[W], s61 offen offset:0\n\t" \
  "buffer_load_dwordx4 v[68:71], v41, %[W], s61 offen offset:1152\n\t" \
  "buffer_load_dwordx4 v[72:75], v41, %[W], s61 offen offset:2304\n\t" \
  "buffer_load_dwordx4 v[76:79], v41, %[W], s61 offen offset:3456\n\t" \
  "s_waitcnt vmcnt(38)\n\t" \
  "v_perm_b32 v44, v84, v80, s62\n\t" \
  "s_waitcnt vmcnt(36)\n\t" \
  "v_perm_b32 v45, v92, v88, s62\n\t" \
  "s_waitcnt vmcnt(34)\n\t" \
  "v_perm_b32 v46, v100, v96, s62\n\t" \
  "s_waitcnt vmcnt(32)\n\t" \
  "v_perm_b32 v47, v108, v104, s62\n\t" \
  "v_accvgpr_write_b32 a32, v44\n\t" \
  "v_accvgpr_write_b32 a33, v45\n\t" \
  "v_accvgpr_write_b32 a34, v46\n\t" \
  "v_accvgpr_write_b32 a35, v47\n\t" \
  "v_perm_b32 v44, v84, v80, s63\n\t" \
  "v_perm_b32 v45, v92, v88, s63\n\t" \
  "v_perm_b32 v46, v100, v96, s63\n\t" \
  "v_perm_b32 v47, v108, v104, s63\n\t" \
  "v_accvgpr_write_b32 a36, v44\n\t" \
  "v_accvgpr_write_b32 a37, v45\n\t" \
  "v_accvgpr_write_b32 a38, v46\n\t" \
  "v_accvgpr_write_b32 a39, v47\n\t" \
  "v_perm_b32 v44, v85, v81, s62\n\t" \
  "v_perm_b32 v45, v93, v89, s62\n\t" \
  "v_perm_b32 v46, v101, v97, s62\n\t" \
  "v_perm_b32 v47, v109, v105, s62\n\t" \
  "v_accvgpr_write_b32 a40, v44\n\t" \
  "v_accvgpr_write_b32 a41, v45\n\t" \
  "v_accvgpr_write_b32 a42, v46\n\t" \
  "v_accvgpr_write_b32 a43, v47\n\t" \
  "v_perm_b32 v44, v85, v81, s63\n\t" \
  "v_perm_b32 v45, v93, v89, s63\n\t" \
  "v_perm_b32 v46, v101, v97, s63\n\t" \
  "v_perm_b32 v47, v109, v105, s63\n\t" \
  "v_accvgpr_write_b32 a44, v44\n\t" \
  "v_accvgpr_write_b32 a45, v45\n\t" \
  "v_accvgpr_write_b32 a46, v46\n\t" \
  "v_accvgpr_write_b32 a47, v47\n\t" \
  "v_perm_b32 v44, v86, v82, s62\n\t" \
  "v_perm_b32 v45, v94, v90, s62\n\t" \
  "v_perm_b32 v46, v102, v98, s62\n\t" \
  "v_perm_b32 v47, v110, v106, s62\n\t" \
  "v_accvgpr_write_b32 a48, v44\n\t" \
  "v_accvgpr_write_b32 a49, v45\n\t" \
  "v_accvgpr_write_b32 a50, v46\n\t" \
  "v_accvgpr_write_b32 a51, v47\n\t" \
  "v_perm_b32 v44, v86, v82, s63\n\t" \
  "v_perm_b32 v45, v94, v90, s63\n\t" \
  "v_perm_b32 v46, v102, v98, s63\n\t" \
  "v_perm_b32 v47, v110, v106, s63\n\t" \
  "v_accvgpr_write_b32 a52, v44\n\t" \
  "v_accvgpr_write_b32 a53, v45\n\t" \
  "v_accvgpr_write_b32 a54, v46\n\t" \
  "v_accvgpr_write_b32 a55, v47\n\t" \
  "v_perm_b32 v44, v87, v83, s62\n\t" \
  "v_perm_b32 v45, v95, v91, s62\n\t" \
  "v_perm_b32 v46, v103, v99, s62\n\t" \
  "v_perm_b32 v47, v111, v107, s62\n\t" \
  "v_accvgpr_write_b32 a56, v44\n\t" \
  "v_accvgpr_write_b32 a57, v45\n\t" \
  "v_accvgpr_write_b32 a58, v46\n\t" \
  "v_accvgpr_write_b32 a59, v47\n\t" \
  "v_perm_b32 v44, v87, v83, s63\n\t" \
  "v_perm_b32 v45, v95, v91, s63\n\t" \
  "v_perm_b32 v46, v103, v99, s63\n\t" \
  "v_perm_b32 v47, v111, v107, s63\n\t" \
  "v_accvgpr_write_b32 a60, v44\n\t" \
  "v_accvgpr_write_b32 a61, v45\n\t" \
  "v_accvgpr_write_b32 a62, v46\n\t" \
  "v_accvgpr_write_b32 a63, v47\n\t" \
  "s_xor_b32 s60, s66, 147456\n\t" \
  "s_add_u32 s60, s60, 73728\n\t" \
  "s_add_u32 s61, s60, 18432\n\t" \
  "buffer_load_dwordx4 v[80:83], v41, %[W], s60 offen offset:0\n\t" \
  "buffer_load_dwordx4 v[84:87], v41, %[W], s60 offen offset:1152\n\t" \
  "buffer_load_dwordx4 v[88:91], v41, %[W], s60 offen offset:2304\n\t" \
  "buffer_load_dwordx4 v[92:95], v41, %[W], s60 offen offset:3456\n\t" \
  "buffer_load_dwordx4 v[96:99], v41, %[W], s61 offen offset:0\n\t" \
  "buffer_load_dwordx4 v[100:103], v41, %[W], s61 offen offset:1152\n\t" \
  "buffer_load_dwordx4 v[104:107], v41, %[W], s61 offen offset:2304\n\t" \
  "buffer_load_dwordx4 v[108:111], v41, %[W], s61 offen offset:3456\n\t" \
  "s_waitcnt vmcnt(38)\n\t" \
  "v_perm_b32 v44, v116, v112, s62\n\t" \
  "s_waitcnt vmcnt(36)\n\t" \
  "v_perm_b32 v45, v124, v120, s62\n\t" \
  "s_waitcnt vmcnt(34)\n\t" \
  "v_perm_b32 v46, v132, v128, s62\n\t" \
  "s_waitcnt vmcnt(32)\n\t" \
  "v_perm_b32 v47, v140, v136, s62\n\t" \
  "v_accvgpr_write_b32 a64, v44\n\t" \
  "v_accvgpr_write_b32 a65, v45\n\t" \
  "v_accvgpr_write_b32 a66, v46\n\t" \
  "v_accvgpr_write_b32 a67, v47\n\t" \
  "v_perm_b32 v44, v116, v112, s63\n\t" \
  "v_perm_b32 v45, v124, v120, s63\n\t" \
  "v_perm_b32 v46, v132, v128, s63\n\t" \
  "v_perm_b32 v47, v140, v136, s63\n\t" \
  "v_accvgpr_write_b32 a68, v44\n\t" \
  "v_accvgpr_write_b32 a69, v45\n\t" \
  "v_accvgpr_write_b32 a70, v46\n\t" \
  "v_accvgpr_write_b32 a71, v47\n\t" \
  "v_perm_b32 v44, v117, v113, s62\n\t" \
  "v_perm_b32 v45, v125, v121, s62\n\t" \
  "v_perm_b32 v46, v133, v129, s62\n\t" \
  "v_perm_b32 v47, v141, v137, s62\n\t" \
  "v_accvgpr_write_b32 a72, v44\n\t" \
  "v_accvgpr_write_b32 a73, v45\n\t" \
  "v_accvgpr_write_b32 a74, v46\n\t" \
  "v_accvgpr_write_b32 a75, v47\n\t" \
  "v_perm_b32 v44, v117, v113, s63\n\t" \
  "v_perm_b32 v45, v125, v121, s63\n\t" \
  "v_perm_b32 v46, v133, v129, s63\n\t" \
  "v_perm_b32 v47, v141, v137, s63\n\t" \
  "v_accvgpr_write_b32 a76, v44\n\t" \
  "v_accvgpr_write_b32 a77, v45\n\t" \
  "v_accvgpr_write_b32 a78, v46\n\t" \
  "v_accvgpr_write_b32 a79, v47\n\t" \
  "v_perm_b32 v44, v118, v114, s62\n\t" \
  "v_perm_b32 v45, v126, v122, s62\n\t" \
  "v_perm_b32 v46, v134, v130, s62\n\t" \
  "v_perm_b32 v47, v142, v138, s62\n\t" \
  "v_accvgpr_write_b32 a80, v44\n\t" \
  "v_accvgpr_write_b32 a81, v45\n\t" \
  "v_accvgpr_write_b32 a82, v46\n\t" \
  "v_accvgpr_write_b32 a83, v47\n\t" \
  "v_perm_b32 v44, v118, v114, s63\n\t" \
  "v_perm_b32 v45, v126, v122, s63\n\t" \
  "v_perm_b32 v46, v134, v130, s63\n\t" \
  "v_perm_b32 v47, v142, v138, s63\n\t" \
  "v_accvgpr_write_b32 a84, v44\n\t" \
  "v_accvgpr_write_b32 a85, v45\n\t" \
  "v_accvgpr_write_b32 a86, v46\n\t" \
  "v_accvgpr_write_b32 a87, v47\n\t" \
  "v_perm_b32 v44, v119, v115, s62\n\t" \
  "v_perm_b32 v45, v127, v123, s62\n\t" \
  "v_perm_b32 v46, v135, v131, s62\n\t" \
  "v_perm_b32 v47, v143, v139, s62\n\t" \
  "v_accvgpr_write_b32 a88, v44\n\t" \
  "v_accvgpr_write_b32 a89, v45\n\t" \
  "v_accvgpr_write_b32 a90, v46\n\t" \
  "v_accvgpr_write_b32 a91, v47\n\t" \
  "v_perm_b32 v44, v119, v115, s63\n\t" \
  "v_perm_b32 v45, v127, v123, s63\n\t" \
  "v_perm_b32 v46, v135, v131, s63\n\t" \
  "v_perm_b32 v47, v143, v139, s63\n\t" \
  "v_accvgpr_write_b32 a92, v44\n\t" \
  "v_accvgpr_write_b32 a93, v45\n\t" \
  "v_accvgpr_write_b32 a94, v46\n\t" \
  "v_accvgpr_write_b32 a95, v47\n\t" \
  "s_xor_b32 s60, s66, 147456\n\t" \
  "s_add_u32 s60, s60, 110592\n\t" \
  "s_add_u32 s61, s60, 18432\n\t" \
  "buffer_load_dwordx4 v[112:115], v41, %[W], s60 offen offset:0\n\t" \
  "buffer_load_dwordx4 v[116:119], v41, %[W], s60 offen offset:1152\n\t" \
  "buffer_load_dwordx4 v[120:123], v41, %[W], s60 offen offset:2304\n\t" \
  "buffer_load_dwordx4 v[124:127], v41, %[W], s60 offen offset:3456\n\t" \
  "buffer_load_dwordx4 v[128:131], v41, %[W], s61 offen offset:0\n\t" \
  "buffer_load_dwordx4 v[132:135], v41, %[W], s61 offen offset:1152\n\t" \
  "buffer_load_dwordx4 v[136:139], v41, %[W], s61 offen offset:2304\n\t" \
  "buffer_load_dwordx4 v[140:143], v41, %[W], s61 offen offset:3456\n\t" \
  "s_waitcnt vmcnt(38)\n\t" \
  "v_perm_b32 v44, v148, v144, s62\n\t" \
  "s_waitcnt vmcnt(36)\n\t" \
  "v_perm_b32 v45, v156, v152, s62\n\t" \
  "s_waitcnt vmcnt(34)\n\t" \
  "v_perm_b32 v46, v164, v160, s62\n\t" \
  "s_waitcnt vmcnt(32)\n\t" \
  "v_perm_b32 v47, v172, v168, s62\n\t" \
  "v_accvgpr_write_b32 a96, v44\n\t" \
  "v_accvgpr_write_b32 a97, v45\n\t" \
  "v_accvgpr_write_b32 a98, v46\n\t" \
  "v_accvgpr_write_b32 a99, v47\n\t" \
  "v_perm_b32 v44, v148, v144, s63\n\t" \
  "v_perm_b32 v45, v156, v152, s63\n\t" \
  "v_perm_b32 v46, v164, v160, s63\n\t" \
  "v_perm_b32 v47, v172, v168, s63\n\t" \
  "v_accvgpr_write_b32 a100, v44\n\t" \
  "v_accvgpr_write_b32 a101, v45\n\t" \
  "v_accvgpr_write_b32 a102, v46\n\t" \
  "v_accvgpr_write_b32 a103, v47\n\t" \
  "v_perm_b32 v44, v149, v145, s62\n\t" \
  "v_perm_b32 v45, v157, v153, s62\n\t" \
  "v_perm_b32 v46, v165, v161, s62\n\t" \
  "v_perm_b32 v47, v173, v169, s62\n\t" \
  "v_accvgpr_write_b32 a104, v44\n\t" \
  "v_accvgpr_write_b32 a105, v45\n\t" \
  "v_accvgpr_write_b32 a106, v46\n\t" \
  "v_accvgpr_write_b32 a107, v47\n\t" \
  "v_perm_b32 v44, v149, v145, s63\n\t" \
  "v_perm_b32 v45, v157, v153, s63\n\t" \
  "v_perm_b32 v46, v165, v161, s63\n\t" \
  "v_perm_b32 v47, v173, v169, s63\n\t" \
  "v_accvgpr_write_b32 a108, v44\n\t" \
  "v_accvgpr_write_b32 a109, v45\n\t" \
  "v_accvgpr_write_b32 a110, v46\n\t" \
  "v_accvgpr_write_b32 a111, v47\n\t" \
  "v_perm_b32 v44, v150, v146, s62\n\t" \
  "v_perm_b32 v45, v158, v154, s62\n\t" \
  "v_perm_b32 v46, v166, v162, s62\n\t" \
  "v_perm_b32 v47, v174, v170, s62\n\t" \
  "v_accvgpr_write_b32 a112, v44\n\t" \
  "v_accvgpr_write_b32 a113, v45\n\t" \
  "v_accvgpr_write_b32 a114, v46\n\t" \
  "v_accvgpr_write_b32 a115, v47\n\t" \
  "v_perm_b32 v44, v150, v146, s63\n\t" \
  "v_perm_b32 v45, v158, v154, s63\n\t" \
  "v_perm_b32 v46, v166, v162, s63\n\t" \
  "v_perm_b32 v47, v174, v170, s63\n\t" \
  "v_accvgpr_write_b32 a116, v44\n\t" \
  "v_accvgpr_write_b32 a117, v45\n\t" \
  "v_accvgpr_write_b32 a118, v46\n\t" \
  "v_accvgpr_write_b32 a119, v47\n\t" \
  "v_perm_b32 v44, v151, v147, s62\n\t" \
  "v_perm_b32 v45, v159, v155, s62\n\t" \
  "v_perm_b32 v46, v167, v163, s62\n\t" \
  "v_perm_b32 v47, v175, v171, s62\n\t" \
  "v_accvgpr_write_b32 a120, v44\n\t" \
  "v_accvgpr_write_b32 a121, v45\n\t" \
  "v_accvgpr_write_b32 a122, v46\n\t" \
  "v_accvgpr_write_b32 a123, v47\n\t" \
  "v_perm_b32 v44, v151, v147, s63\n\t" \
  "v_perm_b32 v45, v159, v155, s63\n\t" \
  "v_perm_b32 v46, v167, v163, s63\n\t" \
  "v_perm_b32 v47, v175, v171, s63\n\t" \
  "v_accvgpr_write_b32 a124, v44\n\t" \
  "v_accvgpr_write_b32 a125, v45\n\t" \
  "v_accvgpr_write_b32 a126, v46\n\t" \
  "v_accvgpr_write_b32 a127, v47\n\t" \
  "s_xor_b32 s60, s66, 0\n\t" \
  "s_add_u32 s60, s60, 0\n\t" \
  "s_add_u32 s61, s60, 18432\n\t" \
  "buffer_load_ushort v224, v42, %[W], s60 offen offset:0\n\t" \
  "buffer_load_ushort v144, v42, %[W], s60 offen offset:1152\n\t" \
  "buffer_load_ushort v225, v42, %[W], s60 offen offset:2304\n\t" \
  "buffer_load_ushort v145, v42, %[W], s60 offen offset:3456\n\t" \
  "buffer_load_ushort v226, v42, %[W], s61 offen offset:0\n\t" \
  "buffer_load_ushort v146, v42, %[W], s61 offen offset:1152\n\t" \
  "buffer_load_ushort v227, v42, %[W], s61 offen offset:2304\n\t" \
  "buffer_load_ushort v147, v42, %[W], s61 offen offset:3456\n\t" \
  "s_xor_b32 s60, s66, 0\n\t" \
  "s_add_u32 s60, s60, 36864\n\t" \
  "s_add_u32 s61, s60, 18432\n\t" \
  "buffer_load_ushort v228, v42, %[W], s60 offen offset:0\n\t" \
  "buffer_load_ushort v148, v42, %[W], s60 offen offset:1152\n\t" \
  "buffer_load_ushort v229, v42, %[W], s60 offen offset:2304\n\t" \
  "buffer_load_ushort v149, v42, %[W], s60 offen offset:3456\n\t" \
  "buffer_load_ushort v230, v42, %[W], s61 offen offset:0\n\t" \
  "buffer_load_ushort v150, v42, %[W], s61 offen offset:1152\n\t" \
  "buffer_load_ushort v231, v42, %[W], s61 offen offset:2304\n\t" \
  "buffer_load_ushort v151, v42, %[W], s61 offen offset:3456\n\t" \
  "s_waitcnt vmcnt(46)\n\t" \
  "v_perm_b32 v44, v180, v176, s62\n\t" \
  "s_waitcnt vmcnt(44)\n\t" \
  "v_perm_b32 v45, v188, v184, s62\n\t" \
  "s_waitcnt vmcnt(42)\n\t" \
  "v_perm_b32 v46, v196, v192, s62\n\t" \
  "s_waitcnt vmcnt(40)\n\t" \
  "v_perm_b32 v47, v204, v200, s62\n\t" \
  "v_accvgpr_write_b32 a128, v44\n\t" \
  "v_accvgpr_write_b32 a129, v45\n\t" \
  "v_accvgpr_write_b32 a130, v46\n\t" \
  "v_accvgpr_write_b32 a131, v47\n\t" \
  "v_perm_b32 v44, v180, v176, s63\n\t" \
  "v_perm_b32 v45, v188, v184, s63\n\t" \
  "v_perm_b32 v46, v196, v192, s63\n\t" \
  "v_perm_b32 v47, v204, v200, s63\n\t" \
  "v_accvgpr_write_b32 a132, v44\n\t" \
  "v_accvgpr_write_b32 a133, v45\n\t" \
  "v_accvgpr_write_b32 a134, v46\n\t" \
  "v_accvgpr_write_b32 a135, v47\n\t" \
  "v_perm_b32 v44, v181, v177, s62\n\t" \
  "v_perm_b32 v45, v189, v185, s62\n\t" \
  "v_perm_b32 v46, v197, v193, s62\n\t" \
  "v_perm_b32 v47, v205, v201, s62\n\t" \
  "v_accvgpr_write_b32 a136, v44\n\t" \
  "v_accvgpr_write_b32 a137, v45\n\t" \
  "v_accvgpr_write_b32 a138, v46\n\t" \
  "v_accvgpr_write_b32 a139, v47\n\t" \
  "v_perm_b32 v44, v181, v177, s63\n\t" \
  "v_perm_b32 v45, v189, v185, s63\n\t" \
  "v_perm_b32 v46, v197, v193, s63\n\t" \
  "v_perm_b32 v47, v205, v201, s63\n\t" \
  "v_accvgpr_write_b32 a140, v44\n\t" \
  "v_accvgpr_write_b32 a141, v45\n\t" \
  "v_accvgpr_write_b32 a142, v46\n\t" \
  "v_accvgpr_write_b32 a143, v47\n\t" \
  "v_perm_b32 v44, v182, v178, s62\n\t" \
  "v_perm_b32 v45, v190, v186, s62\n\t" \
  "v_perm_b32 v46, v198, v194, s62\n\t" \
  "v_perm_b32 v47, v206, v202, s62\n\t" \
  "v_accvgpr_write_b32 a144, v44\n\t" \
  "v_accvgpr_write_b32 a145, v45\n\t" \
  "v_accvgpr_write_b32 a146, v46\n\t" \
  "v_accvgpr_write_b32 a147, v47\n\t" \
  "v_perm_b32 v44, v182, v178, s63\n\t" \
  "v_perm_b32 v45, v190, v186, s63\n\t" \
  "v_perm_b32 v46, v198, v194, s63\n\t" \
  "v_perm_b32 v47, v206, v202, s63\n\t" \
  "v_accvgpr_write_b32 a148, v44\n\t" \
  "v_accvgpr_write_b32 a149, v45\n\t" \
  "v_accvgpr_write_b32 a150, v46\n\t" \
  "v_accvgpr_write_b32 a151, v47\n\t" \
  "v_perm_b32 v44, v183, v179, s62\n\t" \
  "v_perm_b32 v45, v191, v187, s62\n\t" \
  "v_perm_b32 v46, v199, v195, s62\n\t" \
  "v_perm_b32 v47, v207, v203, s62\n\t" \
  "v_accvgpr_write_b32 a152, v44\n\t" \
  "v_accvgpr_write_b32 a153, v45\n\t" \
  "v_accvgpr_write_b32 a154, v46\n\t" \
  "v_accvgpr_write_b32 a155, v47\n\t" \
  "v_perm_b32 v44, v183, v179, s63\n\t" \
  "v_perm_b32 v45, v191, v187, s63\n\t" \
  "v_perm_b32 v46, v199, v195, s63\n\t" \
  "v_perm_b32 v47, v207, v203, s63\n\t" \
  "v_accvgpr_write_b32 a156, v44\n\t" \
  "v_accvgpr_write_b32 a157, v45\n\t" \
  "v_accvgpr_write_b32 a158, v46\n\t" \
  "v_accvgpr_write_b32 a159, v47\n\t" \
  "s_xor_b32 s60, s66, 0\n\t" \
  "s_add_u32 s60, s60, 73728\n\t" \
  "s_add_u32 s61, s60, 18432\n\t" \
  "buffer_load_ushort v232, v42, %[W], s60 offen offset:0\n\t" \
  "buffer_load_ushort v152, v42, %[W], s60 offen offset:1152\n\t" \
  "buffer_load_ushort v233, v42, %[W], s60 offen offset:2304\n\t" \
  "buffer_load_ushort v153, v42, %[W], s60 offen offset:3456\n\t" \
  "buffer_load_ushort v234, v42, %[W], s61 offen offset:0\n\t" \
  "buffer_load_ushort v154, v42, %[W], s61 offen offset:1152\n\t" \
  "buffer_load_ushort v235, v42, %[W], s61 offen offset:2304\n\t" \
  "buffer_load_ushort v155, v42, %[W], s61 offen offset:3456\n\t" \
  "s_xor_b32 s60, s66, 0\n\t" \
  "s_add_u32 s60, s60, 110592\n\t" \
  "s_add_u32 s61, s60, 18432\n\t" \
  "buffer_load_ushort v236, v42, %[W], s60 offen offset:0\n\t" \
  "buffer_load_ushort v156, v42, %[W], s60 offen offset:1152\n\t" \
  "buffer_load_ushort v237, v42, %[W], s60 offen offset:2304\n\t" \
  "buffer_load_ushort v157, v42, %[W], s60 offen offset:3456\n\t" \
  "buffer_load_ushort v238, v42, %[W], s61 offen offset:0\n\t" \
  "buffer_load_ushort v158, v42, %[W], s61 offen offset:1152\n\t" \
  "buffer_load_ushort v239, v42, %[W], s61 offen offset:2304\n\t" \
  "buffer_load_ushort v159, v42, %[W], s61 offen offset:3456\n\t" \
  "s_waitcnt vmcnt(54)\n\t" \
  "v_perm_b32 v44, v52, v48, s62\n\t" \
  "s_waitcnt vmcnt(52)\n\t" \
  "v_perm_b32 v45, v60, v56, s62\n\t" \
  "s_waitcnt vmcnt(50)\n\t" \
  "v_perm_b32 v46, v68, v64, s62\n\t" \
  "s_waitcnt vmcnt(48)\n\t" \
  "v_perm_b32 v47, v76, v72, s62\n\t" \
  "v_accvgpr_write_b32 a160, v44\n\t" \
  "v_accvgpr_write_b32 a161, v45\n\t" \
  "v_accvgpr_write_b32 a162, v46\n\t" \
  "v_accvgpr_write_b32 a163, v47\n\t" \
  "v_perm_b32 v44, v52, v48, s63\n\t" \
  "v_perm_b32 v45, v60, v56, s63\n\t" \
  "v_perm_b32 v46, v68, v64, s63\n\t" \
  "v_perm_b32 v47, v76, v72, s63\n\t" \
  "v_accvgpr_write_b32 a164, v44\n\t" \
  "v_accvgpr_write_b32 a165, v45\n\t" \
  "v_accvgpr_write_b32 a166, v46\n\t" \
  "v_accvgpr_write_b32 a167, v47\n\t" \
  "v_perm_b32 v44, v53, v49, s62\n\t" \
  "v_perm_b32 v45, v61, v57, s62\n\t" \
  "v_perm_b32 v46, v69, v65, s62\n\t" \
  "v_perm_b32 v47, v77, v73, s62\n\t" \
  "v_accvgpr_write_b32 a168, v44\n\t" \
  "v_accvgpr_write_b32 a169, v45\n\t" \
  "v_accvgpr_write_b32 a170, v46\n\t" \
  "v_accvgpr_write_b32 a171, v47\n\t" \
  "v_perm_b32 v44, v53, v49, s63\n\t" \
  "v_perm_b32 v45, v61, v57, s63\n\t" \
  "v_perm_b32 v46, v69, v65, s63\n\t" \
  "v_perm_b32 v47, v77, v73, s63\n\t" \
  "v_accvgpr_write_b32 a172, v44\n\t" \
  "v_accvgpr_write_b32 a173, v45\n\t" \
  "v_accvgpr_write_b32 a174, v46\n\t" \
  "v_accvgpr_write_b32 a175, v47\n\t" \
  "v_perm_b32 v44, v54, v50, s62\n\t" \
  "v_perm_b32 v45, v62, v58, s62\n\t" \
  "v_perm_b32 v46, v70, v66, s62\n\t" \
  "v_perm_b32 v47, v78, v74, s62\n\t" \
  "v_accvgpr_write_b32 a176, v44\n\t" \
  "v_accvgpr_write_b32 a177, v45\n\t" \
  "v_accvgpr_write_b32 a178, v46\n\t" \
  "v_accvgpr_write_b32 a179, v47\n\t" \
  "v_perm_b32 v44, v54, v50, s63\n\t" \
  "v_perm_b32 v45, v62, v58, s63\n\t" \
  "v_perm_b32 v46, v70, v66, s63\n\t" \
  "v_perm_b32 v47, v78, v74, s63\n\t" \
  "v_accvgpr_write_b32 a180, v44\n\t" \
  "v_accvgpr_write_b32 a181, v45\n\t" \
  "v_accvgpr_write_b32 a182, v46\n\t" \
  "v_accvgpr_write_b32 a183, v47\n\t" \
  "v_perm_b32 v44, v55, v51, s62\n\t" \
  "v_perm_b32 v45, v63, v59, s62\n\t" \
  "v_perm_b32 v46, v71, v67, s62\n\t" \
  "v_perm_b32 v47, v79, v75, s62\n\t" \
  "v_accvgpr_write_b32 a184, v44\n\t" \
  "v_accvgpr_write_b32 a185, v45\n\t" \
  "v_accvgpr_write_b32 a186, v46\n\t" \
  "v_accvgpr_write_b32 a187, v47\n\t" \
  "v_perm_b32 v44, v55, v51, s63\n\t" \
  "v_perm_b32 v45, v63, v59, s63\n\t" \
  "v_perm_b32 v46, v71, v67, s63\n\t" \
  "v_perm_b32 v47, v79, v75, s63\n\t" \
  "v_accvgpr_write_b32 a188, v44\n\t" \
  "v_accvgpr_write_b32 a189, v45\n\t" \
  "v_accvgpr_write_b32 a190, v46\n\t" \
  "v_accvgpr_write_b32 a191, v47\n\t" \
  "s_xor_b32 s60, s66, 147456\n\t" \
  "s_add_u32 s60, s60, 0\n\t" \
  "s_add_u32 s61, s60, 18432\n\t" \
  "buffer_load_ushort v240, v42, %[W], s60 offen offset:0\n\t" \
  "buffer_load_ushort v160, v42, %[W], s60 offen offset:1152\n\t" \
  "buffer_load_ushort v241, v42, %[W], s60 offen offset:2304\n\t" \
  "buffer_load_ushort v161, v42, %[W], s60 offen offset:3456\n\t" \
  "buffer_load_ushort v242, v42, %[W], s61 offen offset:0\n\t" \
  "buffer_load_ushort v162, v42, %[W], s61 offen offset:1152\n\t" \
  "buffer_load_ushort v243, v42, %[W], s61 offen offset:2304\n\t" \
  "buffer_load_ushort v163, v42, %[W], s61 offen offset:3456\n\t" \
  "s_waitcnt vmcnt(54)\n\t" \
  "v_perm_b32 v44, v84, v80, s62\n\t" \
  "s_waitcnt vmcnt(52)\n\t" \
  "v_perm_b32 v45, v92, v88, s62\n\t" \
  "s_waitcnt vmcnt(50)\n\t" \
  "v_perm_b32 v46, v100, v96, s62\n\t" \
  "s_waitcnt vmcnt(48)\n\t" \
  "v_perm_b32 v47, v108, v104, s62\n\t" \
  "v_accvgpr_write_b32 a192, v44\n\t" \
  "v_accvgpr_write_b32 a193, v45\n\t" \
  "v_accvgpr_write_b32 a194, v46\n\t" \
  "v_accvgpr_write_b32 a195, v47\n\t" \
  "v_perm_b32 v44, v84, v80, s63\n\t" \
  "v_perm_b32 v45, v92, v88, s63\n\t" \
  "v_perm_b32 v46, v100, v96, s63\n\t" \
  "v_perm_b32 v47, v108, v104, s63\n\t" \
  "v_accvgpr_write_b32 a196, v44\n\t" \
  "v_accvgpr_write_b32 a197, v45\n\t" \
  "v_accvgpr_write_b32 a198, v46\n\t" \
  "v_accvgpr_write_b32 a199, v47\n\t" \
  "v_perm_b32 v44, v85, v81, s62\n\t" \
  "v_perm_b32 v45, v93, v89, s62\n\t" \
  "v_perm_b32 v46, v101, v97, s62\n\t" \
  "v_perm_b32 v47, v109, v105, s62\n\t" \
  "v_accvgpr_write_b32 a200, v44\n\t" \
  "v_accvgpr_write_b32 a201, v45\n\t" \
  "v_accvgpr_write_b32 a202, v46\n\t" \
  "v_accvgpr_write_b32 a203, v47\n\t" \
  "v_perm_b32 v44, v85, v81, s63\n\t" \
  "v_perm_b32 v45, v93, v89, s63\n\t" \
  "v_perm_b32 v46, v101, v97, s63\n\t" \
  "v_perm_b32 v47, v109, v105, s63\n\t" \
  "v_accvgpr_write_b32 a204, v44\n\t" \
  "v_accvgpr_write_b32 a205, v45\n\t" \
  "v_accvgpr_write_b32 a206, v46\n\t" \
  "v_accvgpr_write_b32 a207, v47\n\t" \
  "v_perm_b32 v44, v86, v82, s62\n\t" \
  "v_perm_b32 v45, v94, v90, s62\n\t" \
  "v_perm_b32 v46, v102, v98, s62\n\t" \
  "v_perm_b32 v47, v110, v106, s62\n\t" \
  "v_accvgpr_write_b32 a208, v44\n\t" \
  "v_accvgpr_write_b32 a209, v45\n\t" \
  "v_accvgpr_write_b32 a210, v46\n\t" \
  "v_accvgpr_write_b32 a211, v47\n\t" \
  "v_perm_b32 v44, v86, v82, s63\n\t" \
  "v_perm_b32 v45, v94, v90, s63\n\t" \
  "v_perm_b32 v46, v102, v98, s63\n\t" \
  "v_perm_b32 v47, v110, v106, s63\n\t" \
  "v_accvgpr_write_b32 a212, v44\n\t" \
  "v_accvgpr_write_b32 a213, v45\n\t" \
  "v_accvgpr_write_b32 a214, v46\n\t" \
  "v_accvgpr_write_b32 a215, v47\n\t" \
  "v_perm_b32 v44, v87, v83, s62\n\t" \
  "v_perm_b32 v45, v95, v91, s62\n\t" \
  "v_perm_b32 v46, v103, v99, s62\n\t" \
  "v_perm_b32 v47, v111, v107, s62\n\t" \
  "v_accvgpr_write_b32 a216, v44\n\t" \
  "v_accvgpr_write_b32 a217, v45\n\t" \
  "v_accvgpr_write_b32 a218, v46\n\t" \
  "v_accvgpr_write_b32 a219, v47\n\t" \
  "v_perm_b32 v44, v87, v83, s63\n\t" \
  "v_perm_b32 v45, v95, v91, s63\n\t" \
  "v_perm_b32 v46, v103, v99, s63\n\t" \
  "v_perm_b32 v47, v111, v107, s63\n\t" \
  "v_accvgpr_write_b32 a220, v44\n\t" \
  "v_accvgpr_write_b32 a221, v45\n\t" \
  "v_accvgpr_write_b32 a222, v46\n\t" \
  "v_accvgpr_write_b32 a223, v47\n\t" \
  "s_xor_b32 s60, s66, 147456\n\t" \
  "s_add_u32 s60, s60, 36864\n\t" \
  "s_add_u32 s61, s60, 18432\n\t" \
  "buffer_load_ushort v244, v42, %[W], s60 offen offset:0\n\t" \
  "buffer_load_ushort v164, v42, %[W], s60 offen offset:1152\n\t" \
  "buffer_load_ushort v245, v42, %[W], s60 offen offset:2304\n\t" \
  "buffer_load_ushort v165, v42, %[W], s60 offen offset:3456\n\t" \
  "buffer_load_ushort v246, v42, %[W], s61 offen offset:0\n\t" \
  "buffer_load_ushort v166, v42, %[W], s61 offen offset:1152\n\t" \
  "buffer_load_ushort v247, v42, %[W], s61 offen offset:2304\n\t" \
  "buffer_load_ushort v167, v42, %[W], s61 offen offset:3456\n\t" \
  "s_waitcnt vmcnt(54)\n\t" \
  "v_perm_b32 v44, v116, v112, s62\n\t" \
  "s_waitcnt vmcnt(52)\n\t" \
  "v_perm_b32 v45, v124, v120, s62\n\t" \
  "s_waitcnt vmcnt(50)\n\t" \
  "v_perm_b32 v46, v132, v128, s62\n\t" \
  "s_waitcnt vmcnt(48)\n\t" \
  "v_perm_b32 v47, v140, v136, s62\n\t" \
  "v_accvgpr_write_b32 a224, v44\n\t" \
  "v_accvgpr_write_b32 a225, v45\n\t" \
  "v_accvgpr_write_b32 a226, v46\n\t" \
  "v_accvgpr_write_b32 a227, v47\n\t" \
  "v_perm_b32 v44, v116, v112, s63\n\t" \
  "v_perm_b32 v45, v124, v120, s63\n\t" \
  "v_perm_b32 v46, v132, v128, s63\n\t" \
  "v_perm_b32 v47, v140, v136, s63\n\t" \
  "v_accvgpr_write_b32 a228, v44\n\t" \
  "v_accvgpr_write_b32 a229, v45\n\t" \
  "v_accvgpr_write_b32 a230, v46\n\t" \
  "v_accvgpr_write_b32 a231, v47\n\t" \
  "v_perm_b32 v44, v117, v113, s62\n\t" \
  "v_perm_b32 v45, v125, v121, s62\n\t" \
  "v_perm_b32 v46, v133, v129, s62\n\t" \
  "v_perm_b32 v47, v141, v137, s62\n\t" \
  "v_accvgpr_write_b32 a232, v44\n\t" \
  "v_accvgpr_write_b32 a233, v45\n\t" \
  "v_accvgpr_write_b32 a234, v46\n\t" \
  "v_accvgpr_write_b32 a235, v47\n\t" \
  "v_perm_b32 v44, v117, v113, s63\n\t" \
  "v_perm_b32 v45, v125, v121, s63\n\t" \
  "v_perm_b32 v46, v133, v129, s63\n\t" \
  "v_perm_b32 v47, v141, v137, s63\n\t" \
  "v_accvgpr_write_b32 a236, v44\n\t" \
  "v_accvgpr_write_b32 a237, v45\n\t" \
  "v_accvgpr_write_b32 a238, v46\n\t" \
  "v_accvgpr_write_b32 a239, v47\n\t" \
  "v_perm_b32 v44, v118, v114, s62\n\t" \
  "v_perm_b32 v45, v126, v122, s62\n\t" \
  "v_perm_b32 v46, v134, v130, s62\n\t" \
  "v_perm_b32 v47, v142, v138, s62\n\t" \
  "v_accvgpr_write_b32 a240, v44\n\t" \
  "v_accvgpr_write_b32 a241, v45\n\t" \
  "v_accvgpr_write_b32 a242, v46\n\t" \
  "v_accvgpr_write_b32 a243, v47\n\t" \
  "v_perm_b32 v44, v118, v114, s63\n\t" \
  "v_perm_b32 v45, v126, v122, s63\n\t" \
  "v_perm_b32 v46, v134, v130, s63\n\t" \
  "v_perm_b32 v47, v142, v138, s63\n\t" \
  "v_accvgpr_write_b32 a244, v44\n\t" \
  "v_accvgpr_write_b32 a245, v45\n\t" \
  "v_accvgpr_write_b32 a246, v46\n\t" \
  "v_accvgpr_write_b32 a247, v47\n\t" \
  "v_perm_b32 v44, v119, v115, s62\n\t" \
  "v_perm_b32 v45, v127, v123, s62\n\t" \
  "v_perm_b32 v46, v135, v131, s62\n\t" \
  "v_perm_b32 v47, v143, v139, s62\n\t" \
  "v_accvgpr_write_b32 a248, v44\n\t" \
  "v_accvgpr_write_b32 a249, v45\n\t" \
  "v_accvgpr_write_b32 a250, v46\n\t" \
  "v_accvgpr_write_b32 a251, v47\n\t" \
  "v_perm_b32 v44, v119, v115, s63\n\t" \
  "v_perm_b32 v45, v127, v123, s63\n\t" \
  "v_perm_b32 v46, v135, v131, s63\n\t" \
  "v_perm_b32 v47, v143, v139, s63\n\t" \
  "v_accvgpr_write_b32 a252, v44\n\t" \
  "v_accvgpr_write_b32 a253, v45\n\t" \
  "v_accvgpr_write_b32 a254, v46\n\t" \
  "v_accvgpr_write_b32 a255, v47\n\t" \
  "s_xor_b32 s60, s66, 147456\n\t" \
  "s_add_u32 s60, s60, 73728\n\t" \
  "s_add_u32 s61, s60, 18432\n\t" \
  "buffer_load_ushort v248, v42, %[W], s60 offen offset:0\n\t" \
  "buffer_load_ushort v168, v42, %[W], s60 offen offset:1152\n\t" \
  "buffer_load_ushort v249, v42, %[W], s60 offen offset:2304\n\t" \
  "buffer_load_ushort v169, v42, %[W], s60 offen offset:3456\n\t" \
  "buffer_load_ushort v250, v42, %[W], s61 offen offset:0\n\t" \
  "buffer_load_ushort v170, v42, %[W], s61 offen offset:1152\n\t" \
  "buffer_load_ushort v251, v42, %[W], s61 offen offset:2304\n\t" \
  "buffer_load_ushort v171, v42, %[W], s61 offen offset:3456\n\t" \
  "s_xor_b32 s60, s66, 147456\n\t" \
  "s_add_u32 s60, s60, 110592\n\t" \
  "s_add_u32 s61, s60, 18432\n\t" \
  "s_waitcnt vmcnt(40)\n\t" \
  "buffer_load_ushort v252, v42, %[W], s60 offen offset:0\n\t" \
  "buffer_load_ushort v172, v42, %[W], s60 offen offset:1152\n\t" \
  "buffer_load_ushort v253, v42, %[W], s60 offen offset:2304\n\t" \
  "buffer_load_ushort v173, v42, %[W], s60 offen offset:3456\n\t" \
  "buffer_load_ushort v254, v42, %[W], s61 offen offset:0\n\t" \
  "buffer_load_ushort v174, v42, %[W], s61 offen offset:1152\n\t" \
  "buffer_load_ushort v255, v42, %[W], s61 offen offset:2304\n\t" \
  "buffer_load_ushort v175, v42, %[W], s61 offen offset:3456\n\t" \
  "v_lshl_or_b32 v224, v144, 16, v224\n\t" \
  "v_lshl_or_b32 v225, v145, 16, v225\n\t" \
  "v_lshl_or_b32 v226, v146, 16, v226\n\t" \
  "v_lshl_or_b32 v227, v147, 16, v227\n\t" \
  "v_lshl_or_b32 v228, v148, 16, v228\n\t" \
  "v_lshl_or_b32 v229, v149, 16, v229\n\t" \
  "v_lshl_or_b32 v230, v150, 16, v230\n\t" \
  "v_lshl_or_b32 v231, v151, 16, v231\n\t" \
  "s_waitcnt vmcnt(46)\n\t" \
  "v_lshl_or_b32 v232, v152, 16, v232\n\t" \
  "s_waitcnt vmcnt(44)\n\t" \
  "v_lshl_or_b32 v233, v153, 16, v233\n\t" \
  "s_waitcnt vmcnt(42)\n\t" \
  "v_lshl_or_b32 v234, v154, 16, v234\n\t" \
  "s_waitcnt vmcnt(40)\n\t" \
  "v_lshl_or_b32 v235, v155, 16, v235\n\t" \
  "s_waitcnt vmcnt(38)\n\t" \
  "v_lshl_or_b32 v236, v156, 16, v236\n\t" \
  "s_waitcnt vmcnt(36)\n\t" \
  "v_lshl_or_b32 v237, v157, 16, v237\n\t" \
  "s_waitcnt vmcnt(34)\n\t" \
  "v_lshl_or_b32 v238, v158, 16, v238\n\t" \
  "s_waitcnt vmcnt(32)\n\t" \
  "v_lshl_or_b32 v239, v159, 16, v239\n\t" \
  "s_waitcnt vmcnt(30)\n\t" \
  "v_lshl_or_b32 v240, v160, 16, v240\n\t" \
  "s_waitcnt vmcnt(28)\n\t" \
  "v_lshl_or_b32 v241, v161, 16, v241\n\t" \
  "s_waitcnt vmcnt(26)\n\t" \
  "v_lshl_or_b32 v242, v162, 16, v242\n\t" \
  "s_waitcnt vmcnt(24)\n\t" \
  "v_lshl_or_b32 v243, v163, 16, v243\n\t" \
  "s_waitcnt vmcnt(22)\n\t" \
  "v_lshl_or_b32 v244, v164, 16, v244\n\t" \
  "s_waitcnt vmcnt(20)\n\t" \
  "v_lshl_or_b32 v245, v165, 16, v245\n\t" \
  "s_waitcnt vmcnt(18)\n\t" \
  "v_lshl_or_b32 v246, v166, 16, v246\n\t" \
  "s_waitcnt vmcnt(16)\n\t" \
  "v_lshl_or_b32 v247, v167, 16, v247\n\t" \
  "s_waitcnt vmcnt(14)\n\t" \
  "v_lshl_or_b32 v248, v168, 16, v248\n\t" \
  "s_waitcnt vmcnt(12)\n\t" \
  "v_lshl_or_b32 v249, v169, 16, v249\n\t" \
  "s_waitcnt vmcnt(10)\n\t" \
  "v_lshl_or_b32 v250, v170, 16, v250\n\t" \
  "s_waitcnt vmcnt(8)\n\t" \
  "v_lshl_or_b32 v251, v171, 16, v251\n\t" \
  "s_waitcnt vmcnt(6)\n\t" \
  "v_lshl_or_b32 v252, v172, 16, v252\n\t" \
  "s_waitcnt vmcnt(4)\n\t" \
  "v_lshl_or_b32 v253, v173, 16, v253\n\t" \
  "s_waitcnt vmcnt(2)\n\t" \
  "v_lshl_or_b32 v254, v174, 16, v254\n\t" \
  "s_waitcnt vmcnt(0)\n\t" \
  "v_lshl_or_b32 v255, v175, 16, v255\n\t" \
  "s_mov_b32 s84, " TK_CAP "\n\t" \
  "s_mov_b32 s48, 0\n\t" \
  "s_mov_b32 s49, 0xf0000000\n\t" \
  "s_mov_b32 s46, s45\n\t" \
  "v_mov_b32 v5, -1\n\t" \
  "v_lshrrev_b32 v13, 2, v0\n\t" \
  "s_mov_b32 s60, " TK_CTAB "\n\t" \
  "buffer_load_dword v10, v13, %[X], s60 offen offset:88 sc1\n\t" \
  "buffer_load_dword v13, v13, %[X], s60 offen offset:0 sc1\n\t" \
  "s_waitcnt vmcnt(0)\n\t" \
  "v_and_b32 v10, 1, v10\n\t" \
  "v_and_b32 v13, 1, v13\n\t" \
  "v_cmp_ne_u32 s[90:91], s67, v10\n\t" \
  "v_cmp_ne_u32 s[88:89], s67, v13\n\t" \
  "s_nop 1\n\t" \
  "s_mov_b32 s86, s88\n\t" \
  "v_lshrrev_b32 v13, 2, v0\n\t" \
  "s_mov_b32 s60, " TK_CTAB "\n\t" \
  "buffer_load_dword v10, v13, %[X], s60 offen offset:88 sc1\n\t" \
  "buffer_load_dword v13, v13, %[X], s60 offen offset:344 sc1\n\t" \
  "s_waitcnt vmcnt(0)\n\t" \
  "v_and_b32 v10, 1, v10\n\t" \
  "v_and_b32 v13, 1, v13\n\t" \
  "v_cmp_ne_u32 s[90:91], s67, v10\n\t" \
  "v_cmp_ne_u32 s[88:89], s67, v13\n\t" \
  "s_nop 1\n\t" \
  "s_and_b32 s60, s88, 256\n\t" \
  "s_lshl_b32 s60, s60, 5\n\t" \
  "s_add_u32 s44, s44, s60\n\t" \
  "s_add_u32 s83, s48, 0\n\t" \
  "s_lshr_b64 s[60:61], s[90:91], s83\n\t" \
  "s_and_b32 s60, s60, 1\n\t" \
  "s_lshl_b32 s60, s60, 13\n\t" \
  "s_add_u32 s83, s83, " TK_SG0 "\n\t" \
  "s_lshl_b32 s83, s83, 21\n\t" \
  "s_or_b32 s83, s83, s60\n\t" \
  "s_mov_b32 s81, s83\n\t" \
  "LCOLD_%=:\n\t" \
  "s_add_u32 s62, s83, s41\n\t" \
  "s_mov_b32 s61, 0\n\t" \
  "LPQ_%=:\n\t" \
  "buffer_load_dword v10, v9, %[X], s62 offen sc1\n\t" \
  "s_waitcnt vmcnt(0)\n\t" \
  "v_cmp_eq_u32 vcc, 0, v10\n\t" \
  "s_cbranch_vccz LPQ_%=x\n\t" \
  "s_add_u32 s61, s61, 1\n\t" \
  "s_sleep 4\n\t" \
  "s_cmp_gt_u32 s61, s84\n\t" \
  "s_cbranch_scc1 LPQ_%=g\n\t" \
  "s_and_b32 s60, s61, 1023\n\t" \
  "s_cmp_lg_u32 s60, 0\n\t" \
  "s_cbranch_scc1 LPQ_%=\n\t" \
  "buffer_load_dword v10, off, %[X], s92 sc1\n\t" \
  "s_waitcnt vmcnt(0)\n\t" \
  "v_readfirstlane_b32 s60, v10\n\t" \
  "s_nop 0\n\t" \
  "s_cmp_eq_u32 s60, 0\n\t" \
  "s_cbranch_scc1 LPQ_%=\n\t" \
  "LPQ_%=g:\n\t" \
  "s_mov_b32 s84, 0\n\t" \
  "s_or_b32 s60, s48, 1073741824\n\t" \
  "v_mov_b32 v10, s60\n\t" \
  "s_mov_b64 exec, 1\n\t" \
  "buffer_store_dword v10, off, %[X], s92 sc1\n\t" \
  "s_mov_b64 exec, -1\n\t" \
  "LPQ_%=x:\n\t" \
  "s_add_u32 m0, s46, 0\n\t" \
  "s_add_u32 s47, s81, s36\n\t" \
  "buffer_load_dwordx4 v0, %[X], s47 offen offset:0 sc1 lds\n\t" \
  "buffer_load_dwordx4 v0, %[X], s47 offen offset:1024 sc1 lds\n\t" \
  "buffer_load_dwordx4 v0, %[X], s47 offen offset:2048 sc1 lds\n\t" \
  "buffer_load_dwordx4 v0, %[X], s47 offen offset:3072 sc1 lds\n\t" \
  "s_add_u32 m0, s46, 4096\n\t" \
  "s_add_u32 s47, s81, s37\n\t" \
  "buffer_load_dwordx4 v0, %[X], s47 offen offset:0 sc1 lds\n\t" \
  "buffer_load_dwordx4 v0, %[X], s47 offen offset:1024 sc1 lds\n\t" \
  "buffer_load_dwordx4 v0, %[X], s47 offen offset:2048 sc1 lds\n\t" \
  "buffer_load_dwordx4 v0, %[X], s47 offen offset:3072 sc1 lds\n\t" \
  "s_add_u32 m0, s46, 8192\n\t" \
  "s_add_u32 s47, s81, s38\n\t" \
  "buffer_load_dwordx4 v0, %[X], s47 offen offset:0 sc1 lds\n\t" \
  "buffer_load_dwordx4 v0, %[X], s47 offen offset:1024 sc1 lds\n\t" \
  "buffer_load_dwordx4 v0, %[X], s47 offen offset:2048 sc1 lds\n\t" \
  "buffer_load_dwordx4 v0, %[X], s47 offen offset:3072 sc1 lds\n\t" \
  "s_waitcnt vmcnt(0)\n\t" \
  "LTOP_%=:\n\t" \
  "s_memrealtime s[78:79]\n\t" \
  "v_readfirstlane_b32 s76, v5\n\t" \
  "s_waitcnt lgkmcnt(0)\n\t" \
  "s_add_u32 s60, s76, 1\n\t" \
  "s_not_b32 s76, s76\n\t" \
  "s_cmp_gt_u32 s60, 1\n\t" \
  "s_cselect_b32 s61, -1, 0\n\t" \
  "s_lshl_b32 s60, s78, 8\n\t" \
  "s_sub_u32 s76, s76, s60\n\t" \
  "s_ashr_i32 s76, s76, 8\n\t" \
  "s_add_i32 s76, s76, s77\n\t" \
  "s_min_i32 s76, s76, 400\n\t" \
  "s_and_b32 s76, s76, s61\n\t" \
  "s_add_u32 s57, s78, s76\n\t" \
  TK_AC0 \
  "s_barrier\n\t" \
  TK_AC1 \
  "ds_read_b128 v[48:51], v2 offset:0\n\t" \
  "ds_read_b128 v[64:67], v1 offset:8192\n\t" \
  "ds_read_b128 v[80:83], v1 offset:12288\n\t" \
  "ds_read_b128 v[52:55], v2 offset:1024\n\t" \
  "ds_read_b128 v[68:71], v1 offset:9216\n\t" \
  "ds_read_b128 v[84:87], v1 offset:13312\n\t" \
  "ds_read_b128 v[56:59], v2 offset:2048\n\t" \
  "ds_read_b128 v[72:75], v1 offset:10240\n\t" \
  "ds_read_b128 v[88:91], v1 offset:14336\n\t" \
  "ds_read_b128 v[60:63], v2 offset:3072\n\t" \
  "ds_read_b128 v[76:79], v1 offset:11264\n\t" \
  "ds_read_b128 v[92:95], v1 offset:15360\n\t" \
  "s_mov_b32 s82, s83\n\t" \
  "s_xor_b32 s46, s46, 0x10000\n\t" \
  "s_add_u32 s58, s82, s41\n\t" \
  "s_add_u32 s58, s58, 4096\n\t" \
  "s_add_u32 s59, s81, s39\n\t" \
  "s_cmp_gt_i32 s76, 0\n\t" \
  "s_cselect_b32 s62, 0xf0000000, s58\n\t" \
  "buffer_load_dword v5, off, %[X], s62 sc1\n\t" \
  "s_cmp_gt_i32 s76, " TK_AM "\n\t" \
  "s_cselect_b32 s79, 0xf0000000, s58\n\t" \
  "s_waitcnt lgkmcnt(10)\n\t" \
  "v_mfma_f32_16x16x32_bf16 v[36:39], v[64:67], v[48:51], v[16:19]\n\t" \
  "ds_read_b128 v[96:99], v1 offset:16384\n\t" \
  "s_waitcnt lgkmcnt(10)\n\t" \
  "v_mfma_f32_16x16x32_bf16 v[40:43], v[80:83], v[48:51], v[20:23]\n\t" \
  "ds_read_b128 v[100:103], v1 offset:17408\n\t" \
  "s_waitcnt lgkmcnt(9)\n\t" \
  "v_mfma_f32_16x16x32_bf16 v[36:39], v[68:71], v[52:55], v[36:39]\n\t" \
  "ds_read_b128 v[104:107], v1 offset:18432\n\t" \
  "s_waitcnt lgkmcnt(9)\n\t" \
  "v_mfma_f32_16x16x32_bf16 v[40:43], v[84:87], v[52:55], v[40:43]\n\t" \
  "ds_read_b128 v[108:111], v1 offset:19456\n\t" \
  "s_waitcnt lgkmcnt(8)\n\t" \
  "v_mfma_f32_16x16x32_bf16 v[36:39], v[72:75], v[56:59], v[36:39]\n\t" \
  "ds_read_b128 v[112:115], v1 offset:20480\n\t" \
  "s_waitcnt lgkmcnt(8)\n\t" \
  "v_mfma_f32_16x16x32_bf16 v[40:43], v[88:91], v[56:59], v[40:43]\n\t" \
  "ds_read_b128 v[116:119], v1 offset:21504\n\t" \
  "s_waitcnt lgkmcnt(7)\n\t" \
  "v_mfma_f32_16x16x32_bf16 v[36:39], v[76:79], v[60:63], v[36:39]\n\t" \
  "ds_read_b128 v[120:123], v1 offset:22528\n\t" \
  "s_waitcnt lgkmcnt(7)\n\t" \
  "v_mfma_f32_16x16x32_bf16 v[40:43], v[92:95], v[60:63], v[40:43]\n\t" \
  "ds_read_b128 v[124:127], v1 offset:23552\n\t" \
  "s_add_u32 s83, s48, 1\n\t" \
  "s_lshr_b64 s[60:61], s[90:91], s83\n\t" \
  "s_and_b32 s60, s60, 1\n\t" \
  "s_lshl_b32 s60, s60, 13\n\t" \
  "s_add_u32 s83, s83, " TK_SG0 "\n\t" \
  "s_lshl_b32 s83, s83, 21\n\t" \
  "s_or_b32 s83, s83, s60\n\t" \
  "s_add_u32 s56, s48, 1\n\t" \
  "s_and_b32 s56, s56, " TK_TRM "\n\t" \
  "s_lshr_b64 s[60:61], s[90:91], s56\n\t" \
  "s_and_b32 s60, s60, 1\n\t" \
  "s_lshl_b32 s60, s60, 13\n\t" \
  "s_add_u32 s56, s56, " TK_SG0 "\n\t" \
  "s_lshl_b32 s56, s56, 21\n\t" \
  "s_or_b32 s56, s56, s60\n\t" \
  "v_xor_b32 v1, 0x10000, v1\n\t" \
  "v_xor_b32 v2, 0x10000, v2\n\t" \
  "v_max3_f32 v11, v36, v37, v38\n\t" \
  "v_max3_f32 v11, v11, v39, v40\n\t" \
  "v_max3_f32 v13, v41, v42, v43\n\t" \
  "v_max_f32 v11, v11, v13\n\t" \
  "v_mov_b32 v13, v11\n\t" \
  "s_nop 1\n\t" \
  "v_permlane16_swap_b32 v11, v13\n\t" \
  "v_max_f32 v11, v11, v13\n\t" \
  "v_mov_b32 v13, v11\n\t" \
  "s_nop 1\n\t" \
  "v_permlane32_swap_b32 v11, v13\n\t" \
  "v_max_f32 v11, v11, v13\n\t" \
  "v_mul_f32 v13, 0xbe0293ee, v11\n\t" \
  "v_fmamk_f32 v36, v36, 0x3e0293ee, v13\n\t" \
  "v_fmamk_f32 v37, v37, 0x3e0293ee, v13\n\t" \
  "v_fmamk_f32 v38, v38, 0x3e0293ee, v13\n\t" \
  "v_fmamk_f32 v39, v39, 0x3e0293ee, v13\n\t" \
  "v_fmamk_f32 v40, v40, 0x3e0293ee, v13\n\t" \
  "v_fmamk_f32 v41, v41, 0x3e0293ee, v13\n\t" \
  "v_fmamk_f32 v42, v42, 0x3e0293ee, v13\n\t" \
  "v_fmamk_f32 v43, v43, 0x3e0293ee, v13\n\t" \
  "s_waitcnt vmcnt(0)\n\t" \
  "v_readfirstlane_b32 s76, v5\n\t" \
  "s_barrier\n\t" \
  "s_cmp_eq_u32 s76, 0\n\t" \
  "s_cbranch_scc1 LNG_%=\n\t" \
  "buffer_load_dwordx4 v[196:199], v0, %[X], s59 offen offset:0 sc1\n\t" \
  "buffer_load_dwordx4 v[200:203], v0, %[X], s59 offen offset:1024 sc1\n\t" \
  "buffer_load_dwordx2 v[204:205], v6, %[X], s59 offen offset:2048 sc1\n\t" \
  "buffer_load_dwordx2 v[206:207], v6, %[X], s59 offen offset:2560 sc1\n\t" \
  "buffer_load_dwordx2 v[208:209], v6, %[X], s59 offen offset:3072 sc1\n\t" \
  "buffer_load_dwordx2 v[210:211], v6, %[X], s59 offen offset:3584 sc1\n\t" \
  "LNG_%=:\n\t" \
  "buffer_load_dword v5, off, %[X], s79 sc1\n\t" \
  "s_mov_b64 exec, 1\n\t" \
  "buffer_store_dword v3, off, %[X], s49 sc1\n\t" \
  "s_mov_b64 exec, -1\n\t" \
  "s_mov_b32 s49, 0xf0000000\n\t" \
  "v_exp_f32 v36, v36\n\t" \
  "v_exp_f32 v37, v37\n\t" \
  "v_exp_f32 v38, v38\n\t" \
  "v_exp_f32 v39, v39\n\t" \
  "v_exp_f32 v40, v40\n\t" \
  "v_exp_f32 v41, v41\n\t" \
  "v_exp_f32 v42, v42\n\t" \
  "v_exp_f32 v43, v43\n\t" \
  "v_cvt_pk_bf16_f32 v44, v36, v37\n\t" \
  "v_cvt_pk_bf16_f32 v45, v38, v39\n\t" \
  "v_cvt_pk_bf16_f32 v46, v40, v41\n\t" \
  "v_cvt_pk_bf16_f32 v47, v42, v43\n\t" \
  "v_add_f32 v36, v36, v37\n\t" \
  "v_add_f32 v38, v38, v39\n\t" \
  "s_waitcnt lgkmcnt(7)\n\t" \
  "v_mfma_f32_16x16x32_bf16 v[128:131], v[96:99], v[44:47], 0\n\t" \
  "v_add_f32 v40, v40, v41\n\t" \
  "s_and_b32 s62, s48, 15\n\t" \
  "s_lshr_b32 s63, s62, 1\n\t" \
  "s_waitcnt lgkmcnt(6)\n\t" \
  "v_mfma_f32_16x16x32_bf16 v[132:135], v[100:103], v[44:47], 0\n\t" \
  "v_add_f32 v42, v42, v43\n\t" \
  "s_lshr_b32 s60, s88, s63\n\t" \
  "s_and_b32 s60, s60, 1\n\t" \
  "s_waitcnt lgkmcnt(5)\n\t" \
  "v_mfma_f32_16x16x32_bf16 v[136:139], v[104:107], v[44:47], 0\n\t" \
  "v_add_f32 v36, v36, v38\n\t" \
  "s_lshl_b32 s60, s60, 13\n\t" \
  "s_lshl_b32 s61, s62, 20\n\t" \
  "s_waitcnt lgkmcnt(4)\n\t" \
  "v_mfma_f32_16x16x32_bf16 v[140:143], v[108:111], v[44:47], 0\n\t" \
  "v_add_f32 v40, v40, v42\n\t" \
  "s_add_u32 s50, s61, s60\n\t" \
  "s_add_u32 s50, s50, s43\n\t" \
  "s_waitcnt lgkmcnt(3)\n\t" \
  "v_mfma_f32_16x16x32_bf16 v[144:147], v[112:115], v[44:47], 0\n\t" \
  "v_add_f32 v12, v36, v40\n\t" \
  "s_add_u32 s51, s50, 32768\n\t" \
  "s_add_u32 s52, s51, 32768\n\t" \
  "s_waitcnt lgkmcnt(2)\n\t" \
  "v_mfma_f32_16x16x32_bf16 v[148:151], v[116:119], v[44:47], 0\n\t" \
  "s_add_u32 s53, s52, 32768\n\t" \
  "s_lshl_b32 s61, s62, 17\n\t" \
  "s_waitcnt lgkmcnt(1)\n\t" \
  "v_mfma_f32_16x16x32_bf16 v[152:155], v[120:123], v[44:47], 0\n\t" \
  "s_add_u32 s54, s61, s44\n\t" \
  "s_add_u32 s63, s83, s41\n\t" \
  "s_waitcnt lgkmcnt(0)\n\t" \
  "v_mfma_f32_16x16x32_bf16 v[156:159], v[124:127], v[44:47], 0\n\t" \
  TK_AC2 \
  TK_AC3 \
  "s_cmp_lg_u32 s76, 0\n\t" \
  "s_cbranch_scc1 LGOT_%=\n\t" \
  "s_waitcnt vmcnt(1)\n\t" \
  "v_readfirstlane_b32 s60, v5\n\t" \
  "s_nop 0\n\t" \
  "s_cmp_lg_u32 s60, 0\n\t" \
  "s_cbranch_scc1 LPK_%=x\n\t" \
  "LKW_%=:\n\t" \
  "s_memrealtime s[78:79]\n\t" \
  "s_waitcnt lgkmcnt(0)\n\t" \
  "s_sub_i32 s79, s78, s57\n\t" \
  "s_cmp_lt_i32 s79, 0\n\t" \
  "s_cbranch_scc1 LKW_%=\n\t" \
  "s_mov_b32 s61, 0\n\t" \
  "LPK_%=:\n\t" \
  "buffer_load_dword v5, off, %[X], s58 sc1\n\t" \
  "s_waitcnt vmcnt(0)\n\t" \
  "v_cmp_eq_u32 vcc, 0, v5\n\t" \
  "s_cbranch_vccz LPK_%=x\n\t" \
  "s_add_u32 s61, s61, 1\n\t" \
  "s_sleep " TK_PSK "\n\t" \
  "s_cmp_gt_u32 s61, s84\n\t" \
  "s_cbranch_scc1 LPK_%=g\n\t" \
  "s_and_b32 s60, s61, 1023\n\t" \
  "s_cmp_lg_u32 s60, 0\n\t" \
  "s_cbranch_scc1 LPK_%=\n\t" \
  "buffer_load_dword v5, off, %[X], s92 sc1\n\t" \
  "s_waitcnt vmcnt(0)\n\t" \
  "v_readfirstlane_b32 s60, v5\n\t" \
  "s_nop 0\n\t" \
  "s_cmp_eq_u32 s60, 0\n\t" \
  "s_cbranch_scc1 LPK_%=\n\t" \
  "LPK_%=g:\n\t" \
  "s_mov_b32 s84, 0\n\t" \
  "s_or_b32 s60, s48, 1342177280\n\t" \
  "v_mov_b32 v5, s60\n\t" \
  "s_mov_b64 exec, 1\n\t" \
  "buffer_store_dword v5, off, %[X], s92 sc1\n\t" \
  "s_mov_b64 exec, -1\n\t" \
  "LPK_%=x:\n\t" \
  "buffer_load_dwordx4 v[196:199], v0, %[X], s59 offen offset:0 sc1\n\t" \
  "buffer_load_dwordx4 v[200:203], v0, %[X], s59 offen offset:1024 sc1\n\t" \
  "buffer_load_dwordx2 v[204:205], v6, %[X], s59 offen offset:2048 sc1\n\t" \
  "buffer_load_dwordx2 v[206:207], v6, %[X], s59 offen offset:2560 sc1\n\t" \
  "buffer_load_dwordx2 v[208:209], v6, %[X], s59 offen offset:3072 sc1\n\t" \
  "buffer_load_dwordx2 v[210:211], v6, %[X], s59 offen offset:3584 sc1\n\t" \
  "LGOT_%=:\n\t" \
  TK_PA0 \
  TK_PA1 \
  TK_AC4 \
  "buffer_load_dword v10, v9, %[X], s63 offen sc1\n\t" \
  "s_add_u32 s49, s82, s42\n\t" \
  "s_waitcnt vmcnt(6)\n\t" \
  "v_mfma_f32_16x16x32_bf16 v[36:39], v[196:199], v[48:51], 0\n\t" \
  "v_mfma_f32_16x16x32_bf16 v[40:43], v[196:199], v[52:55], 0\n\t" \
  "s_waitcnt vmcnt(5)\n\t" \
  "v_mfma_f32_16x16x32_bf16 v[36:39], v[200:203], v[56:59], v[36:39]\n\t" \
  "v_mfma_f32_16x16x32_bf16 v[40:43], v[200:203], v[60:63], v[40:43]\n\t" \
  TK_AC5 \
  "s_nop 8\n\t" \
  "v_add_f32 v36, v36, v42\n\t" \
  "v_add_f32 v37, v37, v43\n\t" \
  "v_max3_f32 v14, v36, v37, v11\n\t" \
  "v_mov_b32 v13, v14\n\t" \
  "s_nop 1\n\t" \
  "v_permlane16_swap_b32 v14, v13\n\t" \
  "v_max_f32 v14, v14, v13\n\t" \
  "v_mov_b32 v13, v14\n\t" \
  "s_nop 1\n\t" \
  "v_permlane32_swap_b32 v14, v13\n\t" \
  "v_max_f32 v14, v14, v13\n\t" \
  "v_mul_f32 v14, 0xbe0293ee, v14\n\t" \
  "v_fmamk_f32 v11, v11, 0x3e0293ee, v14\n\t" \
  "v_fmamk_f32 v36, v36, 0x3e0293ee, v14\n\t" \
  "v_fmamk_f32 v37, v37, 0x3e0293ee, v14\n\t" \
  "v_exp_f32 v11, v11\n\t" \
  "v_exp_f32 v36, v36\n\t" \
  "v_exp_f32 v37, v37\n\t" \
  "v_fma_f32 v12, v12, v11, v36\n\t" \
  "v_add_f32 v12, v12, v37\n\t" \
  "v_mov_b32 v13, v12\n\t" \
  "s_nop 1\n\t" \
  "v_permlane16_swap_b32 v12, v13\n\t" \
  "v_add_f32 v12, v12, v13\n\t" \
  "v_mov_b32 v13, v12\n\t" \
  "s_nop 1\n\t" \
  "v_permlane32_swap_b32 v12, v13\n\t" \
  "v_add_f32 v12, v12, v13\n\t" \
  "v_rcp_f32 v12, v12\n\t" \
  "s_nop 0\n\t" \
  "v_mul_f32 v14, v11, v12\n\t" \
  "v_mul_f32 v36, v36, v12\n\t" \
  "v_mul_f32 v37, v37, v12\n\t" \
  "v_cvt_pk_bf16_f32 v26, v36, v37\n\t" \
  "v_cvt_pk_bf16_f32 v33, v36, v37\n\t" \
  "v_pk_mul_f32 v[128:129], v[128:129], v[14:15] op_sel_hi:[1,0]\n\t" \
  "v_pk_mul_f32 v[130:131], v[130:131], v[14:15] op_sel_hi:[1,0]\n\t" \
  "v_pk_mul_f32 v[132:133], v[132:133], v[14:15] op_sel_hi:[1,0]\n\t" \
  "v_pk_mul_f32 v[134:135], v[134:135], v[14:15] op_sel_hi:[1,0]\n\t" \
  "v_pk_mul_f32 v[136:137], v[136:137], v[14:15] op_sel_hi:[1,0]\n\t" \
  "v_pk_mul_f32 v[138:139], v[138:139], v[14:15] op_sel_hi:[1,0]\n\t" \
  "v_pk_mul_f32 v[140:141], v[140:141], v[14:15] op_sel_hi:[1,0]\n\t" \
  "v_pk_mul_f32 v[142:143], v[142:143], v[14:15] op_sel_hi:[1,0]\n\t" \
  "s_waitcnt vmcnt(3)\n\t" \
  "v_mfma_f32_16x16x32_bf16 v[128:131], v[204:207], v[26:29], v[128:131]\n\t" \
  "v_mul_f32 v144, v144, v14\n\t" \
  "v_mul_f32 v145, v145, v14\n\t" \
  "v_mfma_f32_16x16x32_bf16 v[132:135], v[204:207], v[32:35], v[132:135]\n\t" \
  "v_mul_f32 v146, v146, v14\n\t" \
  "v_mul_f32 v147, v147, v14\n\t" \
  "v_mfma_f32_16x16x32_bf16 v[136:139], v[204:207], v[24:27], v[136:139]\n\t" \
  "v_mul_f32 v148, v148, v14\n\t" \
  "v_mul_f32 v149, v149, v14\n\t" \
  "v_mfma_f32_16x16x32_bf16 v[140:143], v[204:207], v[30:33], v[140:143]\n\t" \
  "v_cvt_pk_bf16_f32 v64, v128, v129\n\t" \
  "v_cvt_pk_bf16_f32 v65, v130, v131\n\t" \
  "v_cvt_pk_bf16_f32 v66, v132, v133\n\t" \
  "v_cvt_pk_bf16_f32 v67, v134, v135\n\t" \
  "v_mul_f32 v150, v150, v14\n\t" \
  "v_mul_f32 v151, v151, v14\n\t" \
  TK_AC6 \
  "v_mfma_f32_16x16x32_bf16 v[192:195], v[224:227], v[64:67], 0\n\t" \
  "ds_write_b128 v3, v[64:67] offset:0\n\t" \
  "v_cvt_pk_bf16_f32 v68, v136, v137\n\t" \
  "v_cvt_pk_bf16_f32 v69, v138, v139\n\t" \
  "v_mfma_f32_16x16x32_bf16 v[160:163], v[64:67], a[0:3], 0\n\t" \
  "v_cvt_pk_bf16_f32 v70, v140, v141\n\t" \
  "v_cvt_pk_bf16_f32 v71, v142, v143\n\t" \
  "s_waitcnt vmcnt(1)\n\t" \
  "v_mfma_f32_16x16x32_bf16 v[144:147], v[208:211], v[26:29], v[144:147]\n\t" \
  "v_mul_f32 v152, v152, v14\n\t" \
  "v_mul_f32 v153, v153, v14\n\t" \
  "v_mfma_f32_16x16x32_bf16 v[148:151], v[208:211], v[32:35], v[148:151]\n\t" \
  "v_mul_f32 v154, v154, v14\n\t" \
  "v_mul_f32 v155, v155, v14\n\t" \
  "v_mfma_f32_16x16x32_bf16 v[164:167], v[64:67], a[4:7], 0\n\t" \
  "ds_write_b128 v3, v[68:71] offset:1024\n\t" \
  "v_mul_f32 v156, v156, v14\n\t" \
  "v_mul_f32 v157, v157, v14\n\t" \
  "v_mfma_f32_16x16x32_bf16 v[168:171], v[64:67], a[8:11], 0\n\t" \
  "v_mul_f32 v158, v158, v14\n\t" \
  "v_mul_f32 v159, v159, v14\n\t" \
  "v_mfma_f32_16x16x32_bf16 v[172:175], v[64:67], a[12:15], 0\n\t" \
  "v_cvt_pk_bf16_f32 v72, v144, v145\n\t" \
  "v_cvt_pk_bf16_f32 v73, v146, v147\n\t" \
  "v_mfma_f32_16x16x32_bf16 v[152:155], v[208:211], v[24:27], v[152:155]\n\t" \
  "v_cvt_pk_bf16_f32 v74, v148, v149\n\t" \
  "v_cvt_pk_bf16_f32 v75, v150, v151\n\t" \
  "v_mfma_f32_16x16x32_bf16 v[156:159], v[208:211], v[30:33], v[156:159]\n\t" \
  "v_mfma_f32_16x16x32_bf16 v[176:179], v[64:67], a[16:19], 0\n\t" \
  "ds_write_b128 v3, v[72:75] offset:2048\n\t" \
  "v_mfma_f32_16x16x32_bf16 v[180:183], v[64:67], a[20:23], 0\n\t" \
  "v_mfma_f32_16x16x32_bf16 v[184:187], v[64:67], a[24:27], 0\n\t" \
  "v_cvt_pk_bf16_f32 v76, v152, v153\n\t" \
  "v_cvt_pk_bf16_f32 v77, v154, v155\n\t" \
  "v_mfma_f32_16x16x32_bf16 v[188:191], v[64:67], a[28:31], 0\n\t" \
  "v_cvt_pk_bf16_f32 v78, v156, v157\n\t" \
  "v_cvt_pk_bf16_f32 v79, v158, v159\n\t" \
  "v_mfma_f32_16x16x32_bf16 v[192:195], v[228:231], v[68:71], v[192:195]\n\t" \
  "ds_write_b128 v3, v[76:79] offset:3072\n\t" \
  "v_mfma_f32_16x16x32_bf16 v[160:163], v[68:71], a[32:35], v[160:163]\n\t" \
  "s_waitcnt vmcnt(0)\n\t" \
  "v_cmp_eq_u32 vcc, 0, v10\n\t" \
  "v_mfma_f32_16x16x32_bf16 v[164:167], v[68:71], a[36:39], v[164:167]\n\t" \
  "s_cmp_eq_u64 vcc, 0\n\t" \
  "s_cselect_b32 s81, s56, 0xf0000000\n\t" \
  "s_cmp_eq_u32 s48, 63\n\t" \
  "v_mfma_f32_16x16x32_bf16 v[168:171], v[68:71], a[40:43], v[168:171]\n\t" \
  "s_cselect_b32 s81, 0xf0000000, s81\n\t" \
  "v_mfma_f32_16x16x32_bf16 v[172:175], v[68:71], a[44:47], v[172:175]\n\t" \
  "v_mfma_f32_16x16x32_bf16 v[176:179], v[68:71], a[48:51], v[176:179]\n\t" \
  "s_add_u32 m0, s46, 0\n\t" \
  "s_add_u32 s47, s81, s36\n\t" \
  "buffer_load_dwordx4 v0, %[X], s47 offen offset:0 sc1 lds\n\t" \
  "v_mfma_f32_16x16x32_bf16 v[180:183], v[68:71], a[52:55], v[180:183]\n\t" \
  "v_mfma_f32_16x16x32_bf16 v[184:187], v[68:71], a[56:59], v[184:187]\n\t" \
  "s_waitcnt lgkmcnt(0)\n\t" \
  "s_barrier\n\t" \
  "v_mfma_f32_16x16x32_bf16 v[188:191], v[68:71], a[60:63], v[188:191]\n\t" \
  "ds_read_b128 v[80:83], v4 offset:0\n\t" \
  "v_mfma_f32_16x16x32_bf16 v[192:195], v[232:235], v[72:75], v[192:195]\n\t" \
  "buffer_load_dwordx4 v0, %[X], s47 offen offset:1024 sc1 lds\n\t" \
  "v_mfma_f32_16x16x32_bf16 v[160:163], v[72:75], a[64:67], v[160:163]\n\t" \
  "ds_read_b128 v[84:87], v4 offset:1024\n\t" \
  "v_mfma_f32_16x16x32_bf16 v[164:167], v[72:75], a[68:71], v[164:167]\n\t" \
  "v_mfma_f32_16x16x32_bf16 v[168:171], v[72:75], a[72:75], v[168:171]\n\t" \
  "ds_read_b128 v[88:91], v4 offset:2048\n\t" \
  "v_mfma_f32_16x16x32_bf16 v[172:175], v[72:75], a[76:79], v[172:175]\n\t" \
  "buffer_load_dwordx4 v0, %[X], s47 offen offset:2048 sc1 lds\n\t" \
  "v_mfma_f32_16x16x32_bf16 v[176:179], v[72:75], a[80:83], v[176:179]\n\t" \
  "ds_read_b128 v[92:95], v4 offset:3072\n\t" \
  "v_mfma_f32_16x16x32_bf16 v[180:183], v[72:75], a[84:87], v[180:183]\n\t" \
  "v_mfma_f32_16x16x32_bf16 v[184:187], v[72:75], a[88:91], v[184:187]\n\t" \
  "v_mfma_f32_16x16x32_bf16 v[188:191], v[72:75], a[92:95], v[188:191]\n\t" \
  "buffer_load_dwordx4 v0, %[X], s47 offen offset:3072 sc1 lds\n\t" \
  "v_mfma_f32_16x16x32_bf16 v[192:195], v[236:239], v[76:79], v[192:195]\n\t" \
  "s_add_u32 s62, s48, 2\n\t" \
  "v_mfma_f32_16x16x32_bf16 v[160:163], v[76:79], a[96:99], v[160:163]\n\t" \
  "v_mfma_f32_16x16x32_bf16 v[164:167], v[76:79], a[100:103], v[164:167]\n\t" \
  "s_mul_i32 s63, s62, 43\n\t" \
  "s_lshr_b32 s63, s63, 9\n\t" \
  "v_mfma_f32_16x16x32_bf16 v[168:171], v[76:79], a[104:107], v[168:171]\n\t" \
  "s_add_u32 m0, s46, 4096\n\t" \
  "s_add_u32 s47, s81, s37\n\t" \
  "buffer_load_dwordx4 v0, %[X], s47 offen offset:0 sc1 lds\n\t" \
  "v_mfma_f32_16x16x32_bf16 v[172:175], v[76:79], a[108:111], v[172:175]\n\t" \
  "s_mul_i32 s63, s63, " TK_RM "\n\t" \
  "s_sub_u32 s62, s62, s63\n\t" \
  "v_mfma_f32_16x16x32_bf16 v[176:179], v[76:79], a[112:115], v[176:179]\n\t" \
  "v_mfma_f32_16x16x32_bf16 v[180:183], v[76:79], a[116:119], v[180:183]\n\t" \
  "s_mul_i32 s63, s62, 43\n\t" \
  "s_lshr_b32 s63, s63, 7\n\t" \
  "v_mfma_f32_16x16x32_bf16 v[184:187], v[76:79], a[120:123], v[184:187]\n\t" \
  "buffer_load_dwordx4 v0, %[X], s47 offen offset:1024 sc1 lds\n\t" \
  "v_mfma_f32_16x16x32_bf16 v[188:191], v[76:79], a[124:127], v[188:191]\n\t" \
  "s_mul_i32 s62, s62, 688128\n\t" \
  "s_lshl_b32 s60, s63, 15\n\t" \
  "s_waitcnt lgkmcnt(3)\n\t" \
  "v_mfma_f32_16x16x32_bf16 v[192:195], v[240:243], v[80:83], v[192:195]\n\t" \
  "v_mfma_f32_16x16x32_bf16 v[160:163], v[80:83], a[128:131], v[160:163]\n\t" \
  "s_add_u32 s62, s62, s60\n\t" \
  "s_lshr_b32 s60, s86, s63\n\t" \
  "v_mfma_f32_16x16x32_bf16 v[164:167], v[80:83], a[132:135], v[164:167]\n\t" \
  "buffer_load_dwordx4 v0, %[X], s47 offen offset:2048 sc1 lds\n\t" \
  "v_mfma_f32_16x16x32_bf16 v[168:171], v[80:83], a[136:139], v[168:171]\n\t" \
  "s_and_b32 s60, s60, 1\n\t" \
  "s_lshl_b32 s60, s60, 13\n\t" \
  "v_mfma_f32_16x16x32_bf16 v[172:175], v[80:83], a[140:143], v[172:175]\n\t" \
  "v_mfma_f32_16x16x32_bf16 v[176:179], v[80:83], a[144:147], v[176:179]\n\t" \
  "s_add_u32 s62, s62, s60\n\t" \
  "s_add_u32 s62, s62, 656896\n\t" \
  "v_mfma_f32_16x16x32_bf16 v[180:183], v[80:83], a[148:151], v[180:183]\n\t" \
  "buffer_load_dwordx4 v0, %[X], s47 offen offset:3072 sc1 lds\n\t" \
  "v_mfma_f32_16x16x32_bf16 v[184:187], v[80:83], a[152:155], v[184:187]\n\t" \
  "s_cmp_gt_u32 s48, 61\n\t" \
  "s_cselect_b32 s62, 0xf0000000, s62\n\t" \
  "v_mfma_f32_16x16x32_bf16 v[188:191], v[80:83], a[156:159], v[188:191]\n\t" \
  "s_waitcnt lgkmcnt(2)\n\t" \
  "v_mfma_f32_16x16x32_bf16 v[192:195], v[244:247], v[84:87], v[192:195]\n\t" \
  "v_mfma_f32_16x16x32_bf16 v[160:163], v[84:87], a[160:163], v[160:163]\n\t" \
  "s_add_u32 m0, s46, 8192\n\t" \
  "s_add_u32 s47, s81, s38\n\t" \
  "buffer_load_dwordx4 v0, %[X], s47 offen offset:0 sc1 lds\n\t" \
  "v_mfma_f32_16x16x32_bf16 v[164:167], v[84:87], a[164:167], v[164:167]\n\t" \
  "v_mfma_f32_16x16x32_bf16 v[168:171], v[84:87], a[168:171], v[168:171]\n\t" \
  "v_mfma_f32_16x16x32_bf16 v[172:175], v[84:87], a[172:175], v[172:175]\n\t" \
  "v_mfma_f32_16x16x32_bf16 v[176:179], v[84:87], a[176:179], v[176:179]\n\t" \
  "buffer_load_dwordx4 v0, %[X], s47 offen offset:1024 sc1 lds\n\t" \
  "v_mfma_f32_16x16x32_bf16 v[180:183], v[84:87], a[180:183], v[180:183]\n\t" \
  "v_mfma_f32_16x16x32_bf16 v[184:187], v[84:87], a[184:187], v[184:187]\n\t" \
  "v_mfma_f32_16x16x32_bf16 v[188:191], v[84:87], a[188:191], v[188:191]\n\t" \
  "s_waitcnt lgkmcnt(1)\n\t" \
  "v_mfma_f32_16x16x32_bf16 v[192:195], v[248:251], v[88:91], v[192:195]\n\t" \
  "buffer_load_dwordx4 v0, %[X], s47 offen offset:2048 sc1 lds\n\t" \
  "v_mfma_f32_16x16x32_bf16 v[160:163], v[88:91], a[192:195], v[160:163]\n\t" \
  "v_mfma_f32_16x16x32_bf16 v[164:167], v[88:91], a[196:199], v[164:167]\n\t" \
  "v_mfma_f32_16x16x32_bf16 v[168:171], v[88:91], a[200:203], v[168:171]\n\t" \
  "v_mfma_f32_16x16x32_bf16 v[172:175], v[88:91], a[204:207], v[172:175]\n\t" \
  "buffer_load_dwordx4 v0, %[X], s47 offen offset:3072 sc1 lds\n\t" \
  "v_mfma_f32_16x16x32_bf16 v[176:179], v[88:91], a[208:211], v[176:179]\n\t" \
  "v_mfma_f32_16x16x32_bf16 v[180:183], v[88:91], a[212:215], v[180:183]\n\t" \
  "buffer_load_dword v5, off, %[X], s62 sc1\n\t" \
  "v_mfma_f32_16x16x32_bf16 v[184:187], v[88:91], a[216:219], v[184:187]\n\t" \
  "v_mfma_f32_16x16x32_bf16 v[188:191], v[88:91], a[220:223], v[188:191]\n\t" \
  "s_waitcnt lgkmcnt(0)\n\t" \
  "v_mfma_f32_16x16x32_bf16 v[192:195], v[252:255], v[92:95], v[192:195]\n\t" \
  "v_mfma_f32_16x16x32_bf16 v[160:163], v[92:95], a[224:227], v[160:163]\n\t" \
  "v_mfma_f32_16x16x32_bf16 v[164:167], v[92:95], a[228:231], v[164:167]\n\t" \
  "v_mfma_f32_16x16x32_bf16 v[168:171], v[92:95], a[232:235], v[168:171]\n\t" \
  "v_cvt_pk_bf16_f32 v112, v192, v193\n\t" \
  "v_cvt_pk_bf16_f32 v113, v194, v195\n\t" \
  "v_mfma_f32_16x16x32_bf16 v[172:175], v[92:95], a[236:239], v[172:175]\n\t" \
  "buffer_store_dwordx2 v[112:113], v8, %[X], s54 offen sc1\n\t" \
  "v_mfma_f32_16x16x32_bf16 v[176:179], v[92:95], a[240:243], v[176:179]\n\t" \
  "v_cvt_pk_bf16_f32 v96, v160, v164\n\t" \
  "v_cvt_pk_bf16_f32 v100, v161, v165\n\t" \
  "v_mfma_f32_16x16x32_bf16 v[180:183], v[92:95], a[244:247], v[180:183]\n\t" \
  "v_cvt_pk_bf16_f32 v104, v162, v166\n\t" \
  "v_cvt_pk_bf16_f32 v108, v163, v167\n\t" \
  "v_mfma_f32_16x16x32_bf16 v[184:187], v[92:95], a[248:251], v[184:187]\n\t" \
  "v_cvt_pk_bf16_f32 v97, v168, v172\n\t" \
  "v_cvt_pk_bf16_f32 v101, v169, v173\n\t" \
  "v_mfma_f32_16x16x32_bf16 v[188:191], v[92:95], a[252:255], v[188:191]\n\t" \
  "v_cvt_pk_bf16_f32 v105, v170, v174\n\t" \
  "v_cvt_pk_bf16_f32 v109, v171, v175\n\t" \
  "v_cvt_pk_bf16_f32 v98, v176, v180\n\t" \
  "v_cvt_pk_bf16_f32 v102, v177, v181\n\t" \
  "v_cvt_pk_bf16_f32 v106, v178, v182\n\t" \
  "v_cvt_pk_bf16_f32 v110, v179, v183\n\t" \
  TK_AC7 \
  "s_nop 1\n\t" \
  "v_cvt_pk_bf16_f32 v99, v184, v188\n\t" \
  "buffer_store_dwordx4 v[96:99], v7, %[X], s50 offen sc1\n\t" \
  "v_cvt_pk_bf16_f32 v103, v185, v189\n\t" \
  "buffer_store_dwordx4 v[100:103], v7, %[X], s51 offen sc1\n\t" \
  "v_cvt_pk_bf16_f32 v107, v186, v190\n\t" \
  "buffer_store_dwordx4 v[104:107], v7, %[X], s52 offen sc1\n\t" \
  "v_cvt_pk_bf16_f32 v111, v187, v191\n\t" \
  "buffer_store_dwordx4 v[108:111], v7, %[X], s53 offen sc1\n\t" \
  TK_PA2 \
  TK_AC8 \
  "s_cmp_eq_u32 s48, 63\n\t" \
  "s_cbranch_scc1 LEND_%=\n\t" \
  "s_add_u32 s48, s48, 1\n\t" \
  "s_cmp_eq_u32 s81, s56\n\t" \
  "s_mov_b32 s81, s56\n\t" \
  "s_cbranch_scc0 LCOLD_%=\n\t" \
  "s_waitcnt vmcnt(5)\n\t" \
  "s_branch LTOP_%=\n\t" \
  "LEND_%=:\n\t" \
  "s_waitcnt vmcnt(0)\n\t" \
  "s_barrier\n\t" \
  "s_mov_b64 exec, 1\n\t" \
  "buffer_store_dword v3, off, %[X], s49 sc1\n\t" \
  "s_mov_b64 exec, -1\n\t" \
  "s_mov_b32 s49, 0xf0000000\n\t" \
  TK_PA0 \
  TK_ACE \
  ""
#define QK_ASM \
  "v_lshrrev_b32 v16, 7, v0\n\t" \
  "v_lshlrev_b32 v16, 13, v16\n\t" \
  "v_lshrrev_b32 v19, 2, v0\n\t" \
  "v_and_b32 v19, 64, v19\n\t" \
  "v_xor_b32 v19, v19, v0\n\t" \
  "v_and_b32 v19, 0x70, v19\n\t" \
  "v_add_u32 v16, v16, v19\n\t" \
  "s_mul_i32 s63, s40, 40960\n\t" \
  "v_and_b32 v19, 0x300, v0\n\t" \
  "v_lshlrev_b32 v17, 2, v19\n\t" \
  "v_and_b32 v19, 0xc0, v0\n\t" \
  "v_lshl_add_u32 v17, v19, 1, v17\n\t" \
  "v_and_b32 v19, 0x30, v0\n\t" \
  "v_add3_u32 v17, v17, v19, s63\n\t" \
  "v_lshrrev_b32 v19, 1, v0\n\t" \
  "v_and_b32 v19, 64, v19\n\t" \
  "v_add_u32 v17, v17, v19\n\t" \
  "v_xor_b32 v18, 64, v17\n\t" \
  "s_add_u32 m0, s63, 0\n\t" \
  "s_mov_b32 s60, 0\n\t" \
  "buffer_load_dwordx4 v16, %[W], s60 offen lds\n\t" \
  "s_add_u32 m0, s63, 1024\n\t" \
  "s_mov_b32 s60, 65536\n\t" \
  "buffer_load_dwordx4 v16, %[W], s60 offen lds\n\t" \
  "s_add_u32 m0, s63, 2048\n\t" \
  "s_mov_b32 s60, 131072\n\t" \
  "buffer_load_dwordx4 v16, %[W], s60 offen lds\n\t" \
  "s_add_u32 m0, s63, 3072\n\t" \
  "s_mov_b32 s60, 196608\n\t" \
  "buffer_load_dwordx4 v16, %[W], s60 offen lds\n\t" \
  "s_add_u32 m0, s63, 4096\n\t" \
  "s_mov_b32 s60, 262144\n\t" \
  "buffer_load_dwordx4 v16, %[W], s60 offen lds\n\t" \
  "s_add_u32 m0, s63, 5120\n\t" \
  "s_mov_b32 s60, 327680\n\t" \
  "buffer_load_dwordx4 v16, %[W], s60 offen lds\n\t" \
  "s_add_u32 m0, s63, 6144\n\t" \
  "s_mov_b32 s60, 393216\n\t" \
  "buffer_load_dwordx4 v16, %[W], s60 offen lds\n\t" \
  "s_add_u32 m0, s63, 7168\n\t" \
  "s_mov_b32 s60, 458752\n\t" \
  "buffer_load_dwordx4 v16, %[W], s60 offen lds\n\t" \
  "s_add_u32 m0, s63, 8192\n\t" \
  "s_mov_b32 s60, 524288\n\t" \
  "buffer_load_dwordx4 v16, %[W], s60 offen lds\n\t" \
  "s_add_u32 m0, s63, 9216\n\t" \
  "s_mov_b32 s60, 589824\n\t" \
  "buffer_load_dwordx4 v16, %[W], s60 offen lds\n\t" \
  "s_add_u32 m0, s63, 10240\n\t" \
  "s_mov_b32 s60, 655360\n\t" \
  "buffer_load_dwordx4 v16, %[W], s60 offen lds\n\t" \
  "s_add_u32 m0, s63, 11264\n\t" \
  "s_mov_b32 s60, 720896\n\t" \
  "buffer_load_dwordx4 v16, %[W], s60 offen lds\n\t" \
  "s_add_u32 m0, s63, 12288\n\t" \
  "s_mov_b32 s60, 786432\n\t" \
  "buffer_load_dwordx4 v16, %[W], s60 offen lds\n\t" \
  "s_add_u32 m0, s63, 13312\n\t" \
  "s_mov_b32 s60, 851968\n\t" \
  "buffer_load_dwordx4 v16, %[W], s60 offen lds\n\t" \
  "s_add_u32 m0, s63, 14336\n\t" \
  "s_mov_b32 s60, 917504\n\t" \
  "buffer_load_dwordx4 v16, %[W], s60 offen lds\n\t" \
  "s_add_u32 m0, s63, 15360\n\t" \
  "s_mov_b32 s60, 983040\n\t" \
  "buffer_load_dwordx4 v16, %[W], s60 offen lds\n\t" \
  "s_add_u32 m0, s63, 16384\n\t" \
  "s_mov_b32 s60, 1048576\n\t" \
  "buffer_load_dwordx4 v16, %[W], s60 offen lds\n\t" \
  "s_add_u32 m0, s63, 17408\n\t" \
  "s_mov_b32 s60, 1114112\n\t" \
  "buffer_load_dwordx4 v16, %[W], s60 offen lds\n\t" \
  "s_add_u32 m0, s63, 18432\n\t" \
  "s_mov_b32 s60, 1179648\n\t" \
  "buffer_load_dwordx4 v16, %[W], s60 offen lds\n\t" \
  "s_add_u32 m0, s63, 19456\n\t" \
  "s_mov_b32 s60, 1245184\n\t" \
  "buffer_load_dwordx4 v16, %[W], s60 offen lds\n\t" \
  "s_add_u32 m0, s63, 20480\n\t" \
  "s_mov_b32 s60, 1310720\n\t" \
  "buffer_load_dwordx4 v16, %[W], s60 offen lds\n\t" \
  "s_add_u32 m0, s63, 21504\n\t" \
  "s_mov_b32 s60, 1376256\n\t" \
  "buffer_load_dwordx4 v16, %[W], s60 offen lds\n\t" \
  "s_add_u32 m0, s63, 22528\n\t" \
  "s_mov_b32 s60, 1441792\n\t" \
  "buffer_load_dwordx4 v16, %[W], s60 offen lds\n\t" \
  "s_add_u32 m0, s63, 23552\n\t" \
  "s_mov_b32 s60, 1507328\n\t" \
  "buffer_load_dwordx4 v16, %[W], s60 offen lds\n\t" \
  "s_add_u32 m0, s63, 24576\n\t" \
  "s_mov_b32 s60, 1572864\n\t" \
  "buffer_load_dwordx4 v16, %[W], s60 offen lds\n\t" \
  "s_add_u32 m0, s63, 25600\n\t" \
  "s_mov_b32 s60, 1638400\n\t" \
  "buffer_load_dwordx4 v16, %[W], s60 offen lds\n\t" \
  "s_add_u32 m0, s63, 26624\n\t" \
  "s_mov_b32 s60, 1703936\n\t" \
  "buffer_load_dwordx4 v16, %[W], s60 offen lds\n\t" \
  "s_add_u32 m0, s63, 27648\n\t" \
  "s_mov_b32 s60, 1769472\n\t" \
  "buffer_load_dwordx4 v16, %[W], s60 offen lds\n\t" \
  "s_add_u32 m0, s63, 28672\n\t" \
  "s_mov_b32 s60, 1835008\n\t" \
  "buffer_load_dwordx4 v16, %[W], s60 offen lds\n\t" \
  "s_add_u32 m0, s63, 29696\n\t" \
  "s_mov_b32 s60, 1900544\n\t" \
  "buffer_load_dwordx4 v16, %[W], s60 offen lds\n\t" \
  "s_add_u32 m0, s63, 30720\n\t" \
  "s_mov_b32 s60, 1966080\n\t" \
  "buffer_load_dwordx4 v16, %[W], s60 offen lds\n\t" \
  "s_add_u32 m0, s63, 31744\n\t" \
  "s_mov_b32 s60, 2031616\n\t" \
  "buffer_load_dwordx4 v16, %[W], s60 offen lds\n\t" \
  "s_add_u32 m0, s63, 32768\n\t" \
  "s_mov_b32 s60, 2097152\n\t" \
  "buffer_load_dwordx4 v16, %[W], s60 offen lds\n\t" \
  "s_add_u32 m0, s63, 33792\n\t" \
  "s_mov_b32 s60, 2162688\n\t" \
  "buffer_load_dwordx4 v16, %[W], s60 offen lds\n\t" \
  "s_add_u32 m0, s63, 34816\n\t" \
  "s_mov_b32 s60, 2228224\n\t" \
  "buffer_load_dwordx4 v16, %[W], s60 offen lds\n\t" \
  "s_add_u32 m0, s63, 35840\n\t" \
  "s_mov_b32 s60, 2293760\n\t" \
  "buffer_load_dwordx4 v16, %[W], s60 offen lds\n\t" \
  "s_add_u32 m0, s63, 36864\n\t" \
  "s_mov_b32 s60, 2359296\n\t" \
  "buffer_load_dwordx4 v16, %[W], s60 offen lds\n\t" \
  "s_add_u32 m0, s63, 37888\n\t" \
  "s_mov_b32 s60, 2424832\n\t" \
  "buffer_load_dwordx4 v16, %[W], s60 offen lds\n\t" \
  "s_add_u32 m0, s63, 38912\n\t" \
  "s_mov_b32 s60, 2490368\n\t" \
  "buffer_load_dwordx4 v16, %[W], s60 offen lds\n\t" \
  "s_add_u32 m0, s63, 39936\n\t" \
  "s_mov_b32 s60, 2555904\n\t" \
  "buffer_load_dwordx4 v16, %[W], s60 offen lds\n\t" \
  "s_waitcnt vmcnt(32)\n\t" \
  "ds_read_b64_tr_b16 a[0:1], v17 offset:0\n\t" \
  "ds_read_b64_tr_b16 a[2:3], v17 offset:512\n\t" \
  "ds_read_b64_tr_b16 a[4:5], v17 offset:4096\n\t" \
  "ds_read_b64_tr_b16 a[6:7], v17 offset:4608\n\t" \
  "ds_read_b64_tr_b16 a[72:73], v17 offset:8\n\t" \
  "ds_read_b64_tr_b16 a[74:75], v17 offset:520\n\t" \
  "ds_read_b64_tr_b16 a[76:77], v17 offset:4104\n\t" \
  "ds_read_b64_tr_b16 a[78:79], v17 offset:4616\n\t" \
  "ds_read_b64_tr_b16 a[144:145], v18 offset:0\n\t" \
  "ds_read_b64_tr_b16 a[146:147], v18 offset:512\n\t" \
  "ds_read_b64_tr_b16 a[148:149], v18 offset:4096\n\t" \
  "ds_read_b64_tr_b16 a[150:151], v18 offset:4608\n\t" \
  "ds_read_b64_tr_b16 a[216:217], v18 offset:8\n\t" \
  "ds_read_b64_tr_b16 a[218:219], v18 offset:520\n\t" \
  "ds_read_b64_tr_b16 a[220:221], v18 offset:4104\n\t" \
  "ds_read_b64_tr_b16 a[222:223], v18 offset:4616\n\t" \
  "s_waitcnt lgkmcnt(0)\n\t" \
  "s_add_u32 m0, s63, 0\n\t" \
  "s_mov_b32 s60, 2621440\n\t" \
  "buffer_load_dwordx4 v16, %[W], s60 offen lds\n\t" \
  "s_add_u32 m0, s63, 1024\n\t" \
  "s_mov_b32 s60, 2686976\n\t" \
  "buffer_load_dwordx4 v16, %[W], s60 offen lds\n\t" \
  "s_add_u32 m0, s63, 2048\n\t" \
  "s_mov_b32 s60, 2752512\n\t" \
  "buffer_load_dwordx4 v16, %[W], s60 offen lds\n\t" \
  "s_add_u32 m0, s63, 3072\n\t" \
  "s_mov_b32 s60, 2818048\n\t" \
  "buffer_load_dwordx4 v16, %[W], s60 offen lds\n\t" \
  "s_add_u32 m0, s63, 4096\n\t" \
  "s_mov_b32 s60, 2883584\n\t" \
  "buffer_load_dwordx4 v16, %[W], s60 offen lds\n\t" \
  "s_add_u32 m0, s63, 5120\n\t" \
  "s_mov_b32 s60, 2949120\n\t" \
  "buffer_load_dwordx4 v16, %[W], s60 offen lds\n\t" \
  "s_add_u32 m0, s63, 6144\n\t" \
  "s_mov_b32 s60, 3014656\n\t" \
  "buffer_load_dwordx4 v16, %[W], s60 offen lds\n\t" \
  "s_add_u32 m0, s63, 7168\n\t" \
  "s_mov_b32 s60, 3080192\n\t" \
  "buffer_load_dwordx4 v16, %[W], s60 offen lds\n\t" \
  "s_waitcnt vmcnt(32)\n\t" \
  "ds_read_b64_tr_b16 a[8:9], v17 offset:8192\n\t" \
  "ds_read_b64_tr_b16 a[10:11], v17 offset:8704\n\t" \
  "ds_read_b64_tr_b16 a[12:13], v17 offset:12288\n\t" \
  "ds_read_b64_tr_b16 a[14:15], v17 offset:12800\n\t" \
  "ds_read_b64_tr_b16 a[80:81], v17 offset:8200\n\t" \
  "ds_read_b64_tr_b16 a[82:83], v17 offset:8712\n\t" \
  "ds_read_b64_tr_b16 a[84:85], v17 offset:12296\n\t" \
  "ds_read_b64_tr_b16 a[86:87], v17 offset:12808\n\t" \
  "ds_read_b64_tr_b16 a[152:153], v18 offset:8192\n\t" \
  "ds_read_b64_tr_b16 a[154:155], v18 offset:8704\n\t" \
  "ds_read_b64_tr_b16 a[156:157], v18 offset:12288\n\t" \
  "ds_read_b64_tr_b16 a[158:159], v18 offset:12800\n\t" \
  "ds_read_b64_tr_b16 a[224:225], v18 offset:8200\n\t" \
  "ds_read_b64_tr_b16 a[226:227], v18 offset:8712\n\t" \
  "ds_read_b64_tr_b16 a[228:229], v18 offset:12296\n\t" \
  "ds_read_b64_tr_b16 a[230:231], v18 offset:12808\n\t" \
  "s_waitcnt lgkmcnt(0)\n\t" \
  "s_add_u32 m0, s63, 8192\n\t" \
  "s_mov_b32 s60, 3145728\n\t" \
  "buffer_load_dwordx4 v16, %[W], s60 offen lds\n\t" \
  "s_add_u32 m0, s63, 9216\n\t" \
  "s_mov_b32 s60, 3211264\n\t" \
  "buffer_load_dwordx4 v16, %[W], s60 offen lds\n\t" \
  "s_add_u32 m0, s63, 10240\n\t" \
  "s_mov_b32 s60, 3276800\n\t" \
  "buffer_load_dwordx4 v16, %[W], s60 offen lds\n\t" \
  "s_add_u32 m0, s63, 11264\n\t" \
  "s_mov_b32 s60, 3342336\n\t" \
  "buffer_load_dwordx4 v16, %[W], s60 offen lds\n\t" \
  "s_add_u32 m0, s63, 12288\n\t" \
  "s_mov_b32 s60, 3407872\n\t" \
  "buffer_load_dwordx4 v16, %[W], s60 offen lds\n\t" \
  "s_add_u32 m0, s63, 13312\n\t" \
  "s_mov_b32 s60, 3473408\n\t" \
  "buffer_load_dwordx4 v16, %[W], s60 offen lds\n\t" \
  "s_add_u32 m0, s63, 14336\n\t" \
  "s_mov_b32 s60, 3538944\n\t" \
  "buffer_load_dwordx4 v16, %[W], s60 offen lds\n\t" \
  "s_add_u32 m0, s63, 15360\n\t" \
  "s_mov_b32 s60, 3604480\n\t" \
  "buffer_load_dwordx4 v16, %[W], s60 offen lds\n\t" \
  "s_waitcnt vmcnt(32)\n\t" \
  "ds_read_b64_tr_b16 a[16:17], v17 offset:16384\n\t" \
  "ds_read_b64_tr_b16 a[18:19], v17 offset:16896\n\t" \
  "ds_read_b64_tr_b16 a[20:21], v17 offset:20480\n\t" \
  "ds_read_b64_tr_b16 a[22:23], v17 offset:20992\n\t" \
  "ds_read_b64_tr_b16 a[88:89], v17 offset:16392\n\t" \
  "ds_read_b64_tr_b16 a[90:91], v17 offset:16904\n\t" \
  "ds_read_b64_tr_b16 a[92:93], v17 offset:20488\n\t" \
  "ds_read_b64_tr_b16 a[94:95], v17 offset:21000\n\t" \
  "ds_read_b64_tr_b16 a[160:161], v18 offset:16384\n\t" \
  "ds_read_b64_tr_b16 a[162:163], v18 offset:16896\n\t" \
  "ds_read_b64_tr_b16 a[164:165], v18 offset:20480\n\t" \
  "ds_read_b64_tr_b16 a[166:167], v18 offset:20992\n\t" \
  "ds_read_b64_tr_b16 a[232:233], v18 offset:16392\n\t" \
  "ds_read_b64_tr_b16 a[234:235], v18 offset:16904\n\t" \
  "ds_read_b64_tr_b16 a[236:237], v18 offset:20488\n\t" \
  "ds_read_b64_tr_b16 a[238:239], v18 offset:21000\n\t" \
  "s_waitcnt lgkmcnt(0)\n\t" \
  "s_add_u32 m0, s63, 16384\n\t" \
  "s_mov_b32 s60, 3670016\n\t" \
  "buffer_load_dwordx4 v16, %[W], s60 offen lds\n\t" \
  "s_add_u32 m0, s63, 17408\n\t" \
  "s_mov_b32 s60, 3735552\n\t" \
  "buffer_load_dwordx4 v16, %[W], s60 offen lds\n\t" \
  "s_add_u32 m0, s63, 18432\n\t" \
  "s_mov_b32 s60, 3801088\n\t" \
  "buffer_load_dwordx4 v16, %[W], s60 offen lds\n\t" \
  "s_add_u32 m0, s63, 19456\n\t" \
  "s_mov_b32 s60, 3866624\n\t" \
  "buffer_load_dwordx4 v16, %[W], s60 offen lds\n\t" \
  "s_add_u32 m0, s63, 20480\n\t" \
  "s_mov_b32 s60, 3932160\n\t" \
  "buffer_load_dwordx4 v16, %[W], s60 offen lds\n\t" \
  "s_add_u32 m0, s63, 21504\n\t" \
  "s_mov_b32 s60, 3997696\n\t" \
  "buffer_load_dwordx4 v16, %[W], s60 offen lds\n\t" \
  "s_add_u32 m0, s63, 22528\n\t" \
  "s_mov_b32 s60, 4063232\n\t" \
  "buffer_load_dwordx4 v16, %[W], s60 offen lds\n\t" \
  "s_add_u32 m0, s63, 23552\n\t" \
  "s_mov_b32 s60, 4128768\n\t" \
  "buffer_load_dwordx4 v16, %[W], s60 offen lds\n\t" \
  "s_waitcnt vmcnt(32)\n\t" \
  "ds_read_b64_tr_b16 a[24:25], v17 offset:24576\n\t" \
  "ds_read_b64_tr_b16 a[26:27], v17 offset:25088\n\t" \
  "ds_read_b64_tr_b16 a[28:29], v17 offset:28672\n\t" \
  "ds_read_b64_tr_b16 a[30:31], v17 offset:29184\n\t" \
  "ds_read_b64_tr_b16 a[96:97], v17 offset:24584\n\t" \
  "ds_read_b64_tr_b16 a[98:99], v17 offset:25096\n\t" \
  "ds_read_b64_tr_b16 a[100:101], v17 offset:28680\n\t" \
  "ds_read_b64_tr_b16 a[102:103], v17 offset:29192\n\t" \
  "ds_read_b64_tr_b16 a[168:169], v18 offset:24576\n\t" \
  "ds_read_b64_tr_b16 a[170:171], v18 offset:25088\n\t" \
  "ds_read_b64_tr_b16 a[172:173], v18 offset:28672\n\t" \
  "ds_read_b64_tr_b16 a[174:175], v18 offset:29184\n\t" \
  "ds_read_b64_tr_b16 a[240:241], v18 offset:24584\n\t" \
  "ds_read_b64_tr_b16 a[242:243], v18 offset:25096\n\t" \
  "ds_read_b64_tr_b16 a[244:245], v18 offset:28680\n\t" \
  "ds_read_b64_tr_b16 a[246:247], v18 offset:29192\n\t" \
  "s_waitcnt lgkmcnt(0)\n\t" \
  "s_add_u32 m0, s63, 24576\n\t" \
  "s_mov_b32 s60, 4194304\n\t" \
  "buffer_load_dwordx4 v16, %[W], s60 offen lds\n\t" \
  "s_add_u32 m0, s63, 25600\n\t" \
  "s_mov_b32 s60, 4259840\n\t" \
  "buffer_load_dwordx4 v16, %[W], s60 offen lds\n\t" \
  "s_add_u32 m0, s63, 26624\n\t" \
  "s_mov_b32 s60, 4325376\n\t" \
  "buffer_load_dwordx4 v16, %[W], s60 offen lds\n\t" \
  "s_add_u32 m0, s63, 27648\n\t" \
  "s_mov_b32 s60, 4390912\n\t" \
  "buffer_load_dwordx4 v16, %[W], s60 offen lds\n\t" \
  "s_add_u32 m0, s63, 28672\n\t" \
  "s_mov_b32 s60, 4456448\n\t" \
  "buffer_load_dwordx4 v16, %[W], s60 offen lds\n\t" \
  "s_add_u32 m0, s63, 29696\n\t" \
  "s_mov_b32 s60, 4521984\n\t" \
  "buffer_load_dwordx4 v16, %[W], s60 offen lds\n\t" \
  "s_add_u32 m0, s63, 30720\n\t" \
  "s_mov_b32 s60, 4587520\n\t" \
  "buffer_load_dwordx4 v16, %[W], s60 offen lds\n\t" \
  "s_add_u32 m0, s63, 31744\n\t" \
  "s_mov_b32 s60, 4653056\n\t" \
  "buffer_load_dwordx4 v16, %[W], s60 offen lds\n\t" \
  "s_waitcnt vmcnt(32)\n\t" \
  "ds_read_b64_tr_b16 a[32:33], v17 offset:32768\n\t" \
  "ds_read_b64_tr_b16 a[34:35], v17 offset:33280\n\t" \
  "ds_read_b64_tr_b16 a[36:37], v17 offset:36864\n\t" \
  "ds_read_b64_tr_b16 a[38:39], v17 offset:37376\n\t" \
  "ds_read_b64_tr_b16 a[104:105], v17 offset:32776\n\t" \
  "ds_read_b64_tr_b16 a[106:107], v17 offset:33288\n\t" \
  "ds_read_b64_tr_b16 a[108:109], v17 offset:36872\n\t" \
  "ds_read_b64_tr_b16 a[110:111], v17 offset:37384\n\t" \
  "ds_read_b64_tr_b16 a[176:177], v18 offset:32768\n\t" \
  "ds_read_b64_tr_b16 a[178:179], v18 offset:33280\n\t" \
  "ds_read_b64_tr_b16 a[180:181], v18 offset:36864\n\t" \
  "ds_read_b64_tr_b16 a[182:183], v18 offset:37376\n\t" \
  "ds_read_b64_tr_b16 a[248:249], v18 offset:32776\n\t" \
  "ds_read_b64_tr_b16 a[250:251], v18 offset:33288\n\t" \
  "ds_read_b64_tr_b16 a[252:253], v18 offset:36872\n\t" \
  "ds_read_b64_tr_b16 a[254:255], v18 offset:37384\n\t" \
  "s_waitcnt lgkmcnt(0)\n\t" \
  "s_waitcnt vmcnt(24)\n\t" \
  "ds_read_b64_tr_b16 a[40:41], v17 offset:0\n\t" \
  "ds_read_b64_tr_b16 a[42:43], v17 offset:512\n\t" \
  "ds_read_b64_tr_b16 a[44:45], v17 offset:4096\n\t" \
  "ds_read_b64_tr_b16 a[46:47], v17 offset:4608\n\t" \
  "ds_read_b64_tr_b16 a[112:113], v17 offset:8\n\t" \
  "ds_read_b64_tr_b16 a[114:115], v17 offset:520\n\t" \
  "ds_read_b64_tr_b16 a[116:117], v17 offset:4104\n\t" \
  "ds_read_b64_tr_b16 a[118:119], v17 offset:4616\n\t" \
  "ds_read_b64_tr_b16 a[184:185], v18 offset:0\n\t" \
  "ds_read_b64_tr_b16 a[186:187], v18 offset:512\n\t" \
  "ds_read_b64_tr_b16 a[188:189], v18 offset:4096\n\t" \
  "ds_read_b64_tr_b16 a[190:191], v18 offset:4608\n\t" \
  "ds_read_b64_tr_b16 v[224:225], v18 offset:8\n\t" \
  "ds_read_b64_tr_b16 v[226:227], v18 offset:520\n\t" \
  "ds_read_b64_tr_b16 v[228:229], v18 offset:4104\n\t" \
  "ds_read_b64_tr_b16 v[230:231], v18 offset:4616\n\t" \
  "s_waitcnt lgkmcnt(0)\n\t" \
  "s_waitcnt vmcnt(16)\n\t" \
  "ds_read_b64_tr_b16 a[48:49], v17 offset:8192\n\t" \
  "ds_read_b64_tr_b16 a[50:51], v17 offset:8704\n\t" \
  "ds_read_b64_tr_b16 a[52:53], v17 offset:12288\n\t" \
  "ds_read_b64_tr_b16 a[54:55], v17 offset:12800\n\t" \
  "ds_read_b64_tr_b16 a[120:121], v17 offset:8200\n\t" \
  "ds_read_b64_tr_b16 a[122:123], v17 offset:8712\n\t" \
  "ds_read_b64_tr_b16 a[124:125], v17 offset:12296\n\t" \
  "ds_read_b64_tr_b16 a[126:127], v17 offset:12808\n\t" \
  "ds_read_b64_tr_b16 a[192:193], v18 offset:8192\n\t" \
  "ds_read_b64_tr_b16 a[194:195], v18 offset:8704\n\t" \
  "ds_read_b64_tr_b16 a[196:197], v18 offset:12288\n\t" \
  "ds_read_b64_tr_b16 a[198:199], v18 offset:12800\n\t" \
  "ds_read_b64_tr_b16 v[232:233], v18 offset:8200\n\t" \
  "ds_read_b64_tr_b16 v[234:235], v18 offset:8712\n\t" \
  "ds_read_b64_tr_b16 v[236:237], v18 offset:12296\n\t" \
  "ds_read_b64_tr_b16 v[238:239], v18 offset:12808\n\t" \
  "s_waitcnt lgkmcnt(0)\n\t" \
  "s_waitcnt vmcnt(8)\n\t" \
  "ds_read_b64_tr_b16 a[56:57], v17 offset:16384\n\t" \
  "ds_read_b64_tr_b16 a[58:59], v17 offset:16896\n\t" \
  "ds_read_b64_tr_b16 a[60:61], v17 offset:20480\n\t" \
  "ds_read_b64_tr_b16 a[62:63], v17 offset:20992\n\t" \
  "ds_read_b64_tr_b16 a[128:129], v17 offset:16392\n\t" \
  "ds_read_b64_tr_b16 a[130:131], v17 offset:16904\n\t" \
  "ds_read_b64_tr_b16 a[132:133], v17 offset:20488\n\t" \
  "ds_read_b64_tr_b16 a[134:135], v17 offset:21000\n\t" \
  "ds_read_b64_tr_b16 a[200:201], v18 offset:16384\n\t" \
  "ds_read_b64_tr_b16 a[202:203], v18 offset:16896\n\t" \
  "ds_read_b64_tr_b16 a[204:205], v18 offset:20480\n\t" \
  "ds_read_b64_tr_b16 a[206:207], v18 offset:20992\n\t" \
  "ds_read_b64_tr_b16 v[240:241], v18 offset:16392\n\t" \
  "ds_read_b64_tr_b16 v[242:243], v18 offset:16904\n\t" \
  "ds_read_b64_tr_b16 v[244:245], v18 offset:20488\n\t" \
  "ds_read_b64_tr_b16 v[246:247], v18 offset:21000\n\t" \
  "s_waitcnt lgkmcnt(0)\n\t" \
  "s_waitcnt vmcnt(0)\n\t" \
  "ds_read_b64_tr_b16 a[64:65], v17 offset:24576\n\t" \
  "ds_read_b64_tr_b16 a[66:67], v17 offset:25088\n\t" \
  "ds_read_b64_tr_b16 a[68:69], v17 offset:28672\n\t" \
  "ds_read_b64_tr_b16 a[70:71], v17 offset:29184\n\t" \
  "ds_read_b64_tr_b16 a[136:137], v17 offset:24584\n\t" \
  "ds_read_b64_tr_b16 a[138:139], v17 offset:25096\n\t" \
  "ds_read_b64_tr_b16 a[140:141], v17 offset:28680\n\t" \
  "ds_read_b64_tr_b16 a[142:143], v17 offset:29192\n\t" \
  "ds_read_b64_tr_b16 a[208:209], v18 offset:24576\n\t" \
  "ds_read_b64_tr_b16 a[210:211], v18 offset:25088\n\t" \
  "ds_read_b64_tr_b16 a[212:213], v18 offset:28672\n\t" \
  "ds_read_b64_tr_b16 a[214:215], v18 offset:29184\n\t" \
  "ds_read_b64_tr_b16 v[248:249], v18 offset:24584\n\t" \
  "ds_read_b64_tr_b16 v[250:251], v18 offset:25096\n\t" \
  "ds_read_b64_tr_b16 v[252:253], v18 offset:28680\n\t" \
  "ds_read_b64_tr_b16 v[254:255], v18 offset:29192\n\t" \
  "s_waitcnt lgkmcnt(0)\n\t" \
  "s_mov_b32 s84, " TK_CAP "\n\t" \
  "s_mov_b32 s48, 0\n\t" \
  "s_mov_b32 s49, 0xf0000000\n\t" \
  "s_mov_b32 s46, s41\n\t" \
  "s_mov_b32 s56, 0\n\t" \
  "v_lshrrev_b32 v7, 2, v0\n\t" \
  "s_mov_b32 s60, " TK_CTAB "\n\t" \
  "buffer_load_dword v4, v7, %[X], s60 offen offset:88 sc1\n\t" \
  "buffer_load_dword v7, v7, %[X], s60 offen offset:0 sc1\n\t" \
  "s_waitcnt vmcnt(0)\n\t" \
  "v_and_b32 v4, 1, v4\n\t" \
  "v_and_b32 v7, 1, v7\n\t" \
  "v_cmp_ne_u32 s[90:91], s67, v4\n\t" \
  "v_cmp_ne_u32 s[88:89], s67, v7\n\t" \
  "s_nop 1\n\t" \
  "s_mov_b32 s57, 0xf0000000\n\t" \
  "s_add_u32 s83, s48, 0\n\t" \
  "s_lshr_b64 s[60:61], s[90:91], s83\n\t" \
  "s_and_b32 s60, s60, 1\n\t" \
  "s_lshl_b32 s60, s60, 13\n\t" \
  "s_add_u32 s83, s83, " TK_SG0 "\n\t" \
  "s_lshl_b32 s83, s83, 21\n\t" \
  "s_or_b32 s83, s83, s60\n\t" \
  "s_add_u32 s80, s48, " TK_TRN "\n\t" \
  "s_lshr_b64 s[60:61], s[90:91], s80\n\t" \
  "s_and_b32 s60, s60, 1\n\t" \
  "s_lshl_b32 s60, s60, 13\n\t" \
  "s_add_u32 s80, s80, " TK_SG0 "\n\t" \
  "s_lshl_b32 s80, s80, 21\n\t" \
  "s_or_b32 s80, s80, s60\n\t" \
  "s_add_u32 s62, s83, s39\n\t" \
  "s_add_u32 s80, s80, s39\n\t" \
  "s_add_u32 s80, s80, 2048\n\t" \
  "s_cmp_lt_u32 s48, " TK_TRM "\n\t" \
  "s_cselect_b32 s80, s62, s80\n\t" \
  "v_add_u32 v7, s62, v3\n\t" \
  "s_mov_b32 exec_lo, 0\n\t" \
  "v_mov_b32 v7, s80\n\t" \
  "s_mov_b32 exec_lo, -1\n\t" \
  "LCOLD_%=:\n\t" \
  "s_waitcnt vmcnt(0)\n\t" \
  "s_mov_b64 exec, 1\n\t" \
  "buffer_store_dword v6, off, %[X], s49 sc1\n\t" \
  "s_mov_b64 exec, -1\n\t" \
  "s_mov_b32 s49, 0xf0000000\n\t" \
  "s_add_u32 s55, s83, s36\n\t" \
  "s_cmp_lg_u32 s40, 0\n\t" \
  "s_cbranch_scc1 LGO_%=\n\t" \
  "s_and_b32 s62, s45, 3\n\t" \
  "s_lshl_b32 s62, s62, 7\n\t" \
  "s_add_u32 s62, s62, s83\n\t" \
  "s_add_u32 s62, s62, s39\n\t" \
  "s_mov_b32 s61, 0\n\t" \
  "LPO_%=:\n\t" \
  "buffer_load_dword v4, off, %[X], s62 sc1\n\t" \
  "s_waitcnt vmcnt(0)\n\t" \
  "v_cmp_eq_u32 vcc, 0, v4\n\t" \
  "s_cbranch_vccz LPO_%=x\n\t" \
  "s_add_u32 s61, s61, 1\n\t" \
  "s_sleep " TK_PSH "\n\t" \
  "s_cmp_gt_u32 s61, s84\n\t" \
  "s_cbranch_scc1 LPO_%=g\n\t" \
  "s_and_b32 s60, s61, 1023\n\t" \
  "s_cmp_lg_u32 s60, 0\n\t" \
  "s_cbranch_scc1 LPO_%=\n\t" \
  "buffer_load_dword v4, off, %[X], s92 sc1\n\t" \
  "s_waitcnt vmcnt(0)\n\t" \
  "v_readfirstlane_b32 s60, v4\n\t" \
  "s_nop 0\n\t" \
  "s_cmp_eq_u32 s60, 0\n\t" \
  "s_cbranch_scc1 LPO_%=\n\t" \
  "LPO_%=g:\n\t" \
  "s_mov_b32 s84, 0\n\t" \
  "s_or_b32 s60, s48, 1610612736\n\t" \
  "v_mov_b32 v4, s60\n\t" \
  "s_mov_b64 exec, 1\n\t" \
  "buffer_store_dword v4, off, %[X], s92 sc1\n\t" \
  "s_mov_b64 exec, -1\n\t" \
  "LPO_%=x:\n\t" \
  "s_mov_b32 s61, 0\n\t" \
  "LPH_%=:\n\t" \
  "buffer_load_dword v4, v7, %[X], 0 offen sc1\n\t" \
  "s_waitcnt vmcnt(0)\n\t" \
  "v_cmp_eq_u32 vcc, 0, v4\n\t" \
  "s_cbranch_vccz LPH_%=x\n\t" \
  "s_add_u32 s61, s61, 1\n\t" \
  "s_sleep " TK_PSH "\n\t" \
  "s_cmp_gt_u32 s61, s84\n\t" \
  "s_cbranch_scc1 LPH_%=g\n\t" \
  "s_and_b32 s60, s61, 1023\n\t" \
  "s_cmp_lg_u32 s60, 0\n\t" \
  "s_cbranch_scc1 LPH_%=\n\t" \
  "buffer_load_dword v4, off, %[X], s92 sc1\n\t" \
  "s_waitcnt vmcnt(0)\n\t" \
  "v_readfirstlane_b32 s60, v4\n\t" \
  "s_nop 0\n\t" \
  "s_cmp_eq_u32 s60, 0\n\t" \
  "s_cbranch_scc1 LPH_%=\n\t" \
  "LPH_%=g:\n\t" \
  "s_mov_b32 s84, 0\n\t" \
  "s_or_b32 s60, s48, 1610612736\n\t" \
  "v_mov_b32 v4, s60\n\t" \
  "s_mov_b64 exec, 1\n\t" \
  "buffer_store_dword v4, off, %[X], s92 sc1\n\t" \
  "s_mov_b64 exec, -1\n\t" \
  "LPH_%=x:\n\t" \
  "s_cmp_lt_i32 s57, 0\n\t" \
  "s_cbranch_scc1 LGO_%=\n\t" \
  "s_movk_i32 s63, 4000\n\t" \
  "s_branch LGC_%=u\n\t" \
  "LGC_%=:\n\t" \
  "s_sleep " TK_PSG "\n\t" \
  "buffer_load_dword v8, off, %[X], s57 sc1\n\t" \
  "s_waitcnt vmcnt(0)\n\t" \
  "LGC_%=u:\n\t" \
  "s_memrealtime s[52:53]\n\t" \
  "v_readfirstlane_b32 s60, v8\n\t" \
  "s_waitcnt lgkmcnt(0)\n\t" \
  "s_sub_u32 s63, s63, 1\n\t" \
  "s_cbranch_scc1 LGO_%=\n\t" \
  "s_cmp_eq_u32 s60, -1\n\t" \
  "s_cbranch_scc1 LGC_%=\n\t" \
  "s_not_b32 s60, s60\n\t" \
  "s_lshl_b32 s61, s52, 8\n\t" \
  "s_sub_u32 s60, s60, s61\n\t" \
  "s_ashr_i32 s60, s60, 8\n\t" \
  "s_sub_i32 s60, s60, " TK_QXC "\n\t" \
  "s_min_i32 s60, s60, 400\n\t" \
  "s_add_u32 s54, s52, s60\n\t" \
  "LGC_%=w:\n\t" \
  "s_sleep 1\n\t" \
  "s_memrealtime s[52:53]\n\t" \
  "s_waitcnt lgkmcnt(0)\n\t" \
  "s_sub_i32 s53, s52, s54\n\t" \
  "s_cmp_lt_i32 s53, 0\n\t" \
  "s_cbranch_scc1 LGC_%=w\n\t" \
  "LGO_%=:\n\t" \
  "s_barrier\n\t" \
  "s_add_u32 m0, s46, 0\n\t" \
  "s_add_u32 s47, s55, 0\n\t" \
  "buffer_load_dwordx4 v0, %[X], s47 offen sc1 lds\n\t" \
  "s_add_u32 m0, s46, 4096\n\t" \
  "s_add_u32 s47, s55, 4096\n\t" \
  "buffer_load_dwordx4 v0, %[X], s47 offen sc1 lds\n\t" \
  "s_add_u32 m0, s46, 8192\n\t" \
  "s_add_u32 s47, s55, 16384\n\t" \
  "buffer_load_dwordx4 v0, %[X], s47 offen sc1 lds\n\t" \
  "s_add_u32 m0, s46, 12288\n\t" \
  "s_add_u32 s47, s55, 20480\n\t" \
  "buffer_load_dwordx4 v0, %[X], s47 offen sc1 lds\n\t" \
  "s_add_u32 m0, s46, 16384\n\t" \
  "s_add_u32 s47, s55, 32768\n\t" \
  "buffer_load_dwordx4 v0, %[X], s47 offen sc1 lds\n\t" \
  "s_add_u32 m0, s46, 20480\n\t" \
  "s_add_u32 s47, s55, 36864\n\t" \
  "buffer_load_dwordx4 v0, %[X], s47 offen sc1 lds\n\t" \
  "s_add_u32 m0, s46, 24576\n\t" \
  "s_add_u32 s47, s55, 49152\n\t" \
  "buffer_load_dwordx4 v0, %[X], s47 offen sc1 lds\n\t" \
  "s_add_u32 m0, s46, 28672\n\t" \
  "s_add_u32 s47, s55, 53248\n\t" \
  "buffer_load_dwordx4 v0, %[X], s47 offen sc1 lds\n\t" \
  "s_add_u32 m0, s46, 32768\n\t" \
  "s_add_u32 s47, s55, 65536\n\t" \
  "buffer_load_dwordx4 v0, %[X], s47 offen sc1 lds\n\t" \
  "s_waitcnt vmcnt(5)\n\t" \
  "LTOP_%=:\n\t" \
  "s_barrier\n\t" \
  TK_PQ1 \
  "ds_read_b128 v[64:67], v1 offset:0\n\t" \
  "ds_read_b128 v[68:71], v1 offset:2048\n\t" \
  "ds_read_b128 v[72:75], v2 offset:0\n\t" \
  "ds_read_b128 v[76:79], v2 offset:2048\n\t" \
  "ds_read_b128 v[80:83], v1 offset:4096\n\t" \
  "ds_read_b128 v[84:87], v1 offset:6144\n\t" \
  "s_mov_b32 s82, s83\n\t" \
  "s_xor_b32 s46, s46, 0x10000\n\t" \
  "s_waitcnt lgkmcnt(5)\n\t" \
  "v_mfma_f32_16x16x32_bf16 v[16:19], a[0:3], v[64:67], 0\n\t" \
  "ds_read_b128 v[88:91], v2 offset:4096\n\t" \
  "v_mfma_f32_16x16x32_bf16 v[20:23], a[72:75], v[64:67], 0\n\t" \
  "s_add_u32 s50, s48, 0\n\t" \
  "s_and_b32 s50, s50, " TK_TRM "\n\t" \
  "v_mfma_f32_16x16x32_bf16 v[24:27], a[144:147], v[64:67], 0\n\t" \
  "ds_read_b128 v[92:95], v2 offset:6144\n\t" \
  "v_mfma_f32_16x16x32_bf16 v[28:31], a[216:219], v[64:67], 0\n\t" \
  "s_lshr_b64 s[60:61], s[90:91], s50\n\t" \
  "s_and_b32 s60, s60, 1\n\t" \
  "s_waitcnt lgkmcnt(5)\n\t" \
  "v_mfma_f32_16x16x32_bf16 v[16:19], a[4:7], v[72:75], v[16:19]\n\t" \
  "ds_read_b128 v[96:99], v1 offset:8192\n\t" \
  "v_mfma_f32_16x16x32_bf16 v[20:23], a[76:79], v[72:75], v[20:23]\n\t" \
  "s_lshl_b32 s60, s60, 13\n\t" \
  "s_add_u32 s50, s50, " TK_SG0 "\n\t" \
  "v_mfma_f32_16x16x32_bf16 v[24:27], a[148:151], v[72:75], v[24:27]\n\t" \
  "ds_read_b128 v[100:103], v1 offset:10240\n\t" \
  "v_mfma_f32_16x16x32_bf16 v[28:31], a[220:223], v[72:75], v[28:31]\n\t" \
  "s_lshl_b32 s50, s50, 21\n\t" \
  "s_or_b32 s50, s50, s60\n\t" \
  "s_waitcnt lgkmcnt(5)\n\t" \
  "v_mfma_f32_16x16x32_bf16 v[16:19], a[8:11], v[80:83], v[16:19]\n\t" \
  "ds_read_b128 v[104:107], v2 offset:8192\n\t" \
  "v_mfma_f32_16x16x32_bf16 v[20:23], a[80:83], v[80:83], v[20:23]\n\t" \
  "s_add_u32 s50, s50, s37\n\t" \
  "s_add_u32 s51, s50, 4096\n\t" \
  "v_mfma_f32_16x16x32_bf16 v[24:27], a[152:155], v[80:83], v[24:27]\n\t" \
  "ds_read_b128 v[108:111], v2 offset:10240\n\t" \
  "v_mfma_f32_16x16x32_bf16 v[28:31], a[224:227], v[80:83], v[28:31]\n\t" \
  "s_add_u32 s83, s48, 1\n\t" \
  "s_lshr_b64 s[60:61], s[90:91], s83\n\t" \
  "s_waitcnt lgkmcnt(5)\n\t" \
  "v_mfma_f32_16x16x32_bf16 v[16:19], a[12:15], v[88:91], v[16:19]\n\t" \
  "ds_read_b128 v[112:115], v1 offset:12288\n\t" \
  "v_mfma_f32_16x16x32_bf16 v[20:23], a[84:87], v[88:91], v[20:23]\n\t" \
  "s_and_b32 s60, s60, 1\n\t" \
  "s_lshl_b32 s60, s60, 13\n\t" \
  "v_mfma_f32_16x16x32_bf16 v[24:27], a[156:159], v[88:91], v[24:27]\n\t" \
  "ds_read_b128 v[116:119], v1 offset:14336\n\t" \
  "v_mfma_f32_16x16x32_bf16 v[28:31], a[228:231], v[88:91], v[28:31]\n\t" \
  "s_add_u32 s83, s83, " TK_SG0 "\n\t" \
  "s_lshl_b32 s83, s83, 21\n\t" \
  "s_waitcnt lgkmcnt(5)\n\t" \
  "v_mfma_f32_16x16x32_bf16 v[16:19], a[16:19], v[96:99], v[16:19]\n\t" \
  "ds_read_b128 v[120:123], v2 offset:12288\n\t" \
  "v_mfma_f32_16x16x32_bf16 v[20:23], a[88:91], v[96:99], v[20:23]\n\t" \
  "s_or_b32 s83, s83, s60\n\t" \
  "s_cmp_lg_u32 s56, 0\n\t" \
  "v_mfma_f32_16x16x32_bf16 v[24:27], a[160:163], v[96:99], v[24:27]\n\t" \
  "ds_read_b128 v[124:127], v2 offset:14336\n\t" \
  "v_mfma_f32_16x16x32_bf16 v[28:31], a[232:235], v[96:99], v[28:31]\n\t" \
  "s_cbranch_scc1 LWARM_%=\n\t" \
  "s_waitcnt vmcnt(0)\n\t" \
  "LWARM_%=:\n\t" \
  "s_barrier\n\t" \
  "s_waitcnt lgkmcnt(5)\n\t" \
  "v_mfma_f32_16x16x32_bf16 v[16:19], a[20:23], v[104:107], v[16:19]\n\t" \
  "ds_read_b128 v[128:131], v1 offset:16384\n\t" \
  "v_mfma_f32_16x16x32_bf16 v[20:23], a[92:95], v[104:107], v[20:23]\n\t" \
  "v_mfma_f32_16x16x32_bf16 v[24:27], a[164:167], v[104:107], v[24:27]\n\t" \
  "ds_read_b128 v[132:135], v1 offset:18432\n\t" \
  "v_mfma_f32_16x16x32_bf16 v[28:31], a[236:239], v[104:107], v[28:31]\n\t" \
  "s_add_i32 s62, s48, s42\n\t" \
  "s_add_i32 s62, s62, 1\n\t" \
  "s_waitcnt lgkmcnt(5)\n\t" \
  "v_mfma_f32_16x16x32_bf16 v[16:19], a[24:27], v[112:115], v[16:19]\n\t" \
  "ds_read_b128 v[136:139], v2 offset:16384\n\t" \
  "v_mfma_f32_16x16x32_bf16 v[20:23], a[96:99], v[112:115], v[20:23]\n\t" \
  "s_mov_b32 s61, s62\n\t" \
  "v_mfma_f32_16x16x32_bf16 v[24:27], a[168:171], v[112:115], v[24:27]\n\t" \
  "ds_read_b128 v[140:143], v2 offset:18432\n\t" \
  "v_mfma_f32_16x16x32_bf16 v[28:31], a[240:243], v[112:115], v[28:31]\n\t" \
  "s_mul_i32 s63, s62, 43\n\t" \
  "s_lshr_b32 s63, s63, 9\n\t" \
  "s_waitcnt lgkmcnt(5)\n\t" \
  "v_mfma_f32_16x16x32_bf16 v[16:19], a[28:31], v[120:123], v[16:19]\n\t" \
  "ds_read_b128 v[144:147], v1 offset:20480\n\t" \
  "v_mfma_f32_16x16x32_bf16 v[20:23], a[100:103], v[120:123], v[20:23]\n\t" \
  "s_mul_i32 s63, s63, " TK_RM "\n\t" \
  "s_sub_u32 s62, s62, s63\n\t" \
  "v_mfma_f32_16x16x32_bf16 v[24:27], a[172:175], v[120:123], v[24:27]\n\t" \
  "ds_read_b128 v[148:151], v1 offset:22528\n\t" \
  "v_mfma_f32_16x16x32_bf16 v[28:31], a[244:247], v[120:123], v[28:31]\n\t" \
  "s_mul_i32 s63, s62, 43\n\t" \
  "s_lshr_b32 s63, s63, 7\n\t" \
  "s_waitcnt lgkmcnt(5)\n\t" \
  "v_mfma_f32_16x16x32_bf16 v[16:19], a[32:35], v[128:131], v[16:19]\n\t" \
  "ds_read_b128 v[152:155], v2 offset:20480\n\t" \
  "v_mfma_f32_16x16x32_bf16 v[20:23], a[104:107], v[128:131], v[20:23]\n\t" \
  "s_mul_i32 s57, s62, 688128\n\t" \
  "s_lshl_b32 s60, s63, 15\n\t" \
  "v_mfma_f32_16x16x32_bf16 v[24:27], a[176:179], v[128:131], v[24:27]\n\t" \
  "ds_read_b128 v[156:159], v2 offset:22528\n\t" \
  "v_mfma_f32_16x16x32_bf16 v[28:31], a[248:251], v[128:131], v[28:31]\n\t" \
  "s_add_u32 s57, s57, s60\n\t" \
  "s_lshr_b32 s60, s88, s63\n\t" \
  "s_waitcnt lgkmcnt(5)\n\t" \
  "v_mfma_f32_16x16x32_bf16 v[16:19], a[36:39], v[136:139], v[16:19]\n\t" \
  "ds_read_b128 v[160:163], v1 offset:24576\n\t" \
  "v_mfma_f32_16x16x32_bf16 v[20:23], a[108:111], v[136:139], v[20:23]\n\t" \
  "s_and_b32 s60, s60, 1\n\t" \
  "s_lshl_b32 s60, s60, 13\n\t" \
  "v_mfma_f32_16x16x32_bf16 v[24:27], a[180:183], v[136:139], v[24:27]\n\t" \
  "ds_read_b128 v[164:167], v1 offset:26624\n\t" \
  "v_mfma_f32_16x16x32_bf16 v[28:31], a[252:255], v[136:139], v[28:31]\n\t" \
  "s_add_u32 s57, s57, s60\n\t" \
  "s_add_u32 s57, s57, 656896\n\t" \
  "s_waitcnt lgkmcnt(5)\n\t" \
  "v_mfma_f32_16x16x32_bf16 v[16:19], a[40:43], v[144:147], v[16:19]\n\t" \
  "ds_read_b128 v[168:171], v2 offset:24576\n\t" \
  "v_mfma_f32_16x16x32_bf16 v[20:23], a[112:115], v[144:147], v[20:23]\n\t" \
  "s_cmp_lt_i32 s61, 1\n\t" \
  "s_cselect_b32 s57, 0xf0000000, s57\n\t" \
  "v_mfma_f32_16x16x32_bf16 v[24:27], a[184:187], v[144:147], v[24:27]\n\t" \
  "ds_read_b128 v[172:175], v2 offset:26624\n\t" \
  "v_mfma_f32_16x16x32_bf16 v[28:31], v[224:227], v[144:147], v[28:31]\n\t" \
  "s_cmp_lg_u32 s40, 0\n\t" \
  "s_cselect_b32 s57, 0xf0000000, s57\n\t" \
  "s_waitcnt lgkmcnt(5)\n\t" \
  "v_mfma_f32_16x16x32_bf16 v[16:19], a[44:47], v[152:155], v[16:19]\n\t" \
  "ds_read_b128 v[176:179], v1 offset:28672\n\t" \
  "v_mfma_f32_16x16x32_bf16 v[20:23], a[116:119], v[152:155], v[20:23]\n\t" \
  "s_add_u32 s80, s48, " TK_TRN "\n\t" \
  "s_lshr_b64 s[60:61], s[90:91], s80\n\t" \
  "s_and_b32 s60, s60, 1\n\t" \
  "v_mfma_f32_16x16x32_bf16 v[24:27], a[188:191], v[152:155], v[24:27]\n\t" \
  "ds_read_b128 v[180:183], v1 offset:30720\n\t" \
  "v_mfma_f32_16x16x32_bf16 v[28:31], v[228:231], v[152:155], v[28:31]\n\t" \
  "s_lshl_b32 s60, s60, 13\n\t" \
  "s_add_u32 s80, s80, " TK_SG0 "\n\t" \
  "s_lshl_b32 s80, s80, 21\n\t" \
  "s_waitcnt lgkmcnt(5)\n\t" \
  "v_mfma_f32_16x16x32_bf16 v[16:19], a[48:51], v[160:163], v[16:19]\n\t" \
  "ds_read_b128 v[184:187], v2 offset:28672\n\t" \
  "v_mfma_f32_16x16x32_bf16 v[20:23], a[120:123], v[160:163], v[20:23]\n\t" \
  "s_or_b32 s80, s80, s60\n\t" \
  "s_add_u32 s62, s83, s39\n\t" \
  "s_add_u32 s80, s80, s39\n\t" \
  TK_PQ0 \
  "v_mfma_f32_16x16x32_bf16 v[24:27], a[192:195], v[160:163], v[24:27]\n\t" \
  "ds_read_b128 v[188:191], v2 offset:30720\n\t" \
  "v_mfma_f32_16x16x32_bf16 v[28:31], v[232:235], v[160:163], v[28:31]\n\t" \
  "s_add_u32 s80, s80, 2048\n\t" \
  "s_waitcnt lgkmcnt(5)\n\t" \
  "v_mfma_f32_16x16x32_bf16 v[16:19], a[52:55], v[168:171], v[16:19]\n\t" \
  "ds_read_b128 v[192:195], v1 offset:32768\n\t" \
  "v_mfma_f32_16x16x32_bf16 v[20:23], a[124:127], v[168:171], v[20:23]\n\t" \
  "v_mfma_f32_16x16x32_bf16 v[24:27], a[196:199], v[168:171], v[24:27]\n\t" \
  "ds_read_b128 v[196:199], v1 offset:34816\n\t" \
  "v_mfma_f32_16x16x32_bf16 v[28:31], v[236:239], v[168:171], v[28:31]\n\t" \
  "s_cmp_lt_u32 s48, " TK_TRM "\n\t" \
  "s_cselect_b32 s80, s62, s80\n\t" \
  "s_waitcnt lgkmcnt(5)\n\t" \
  "v_mfma_f32_16x16x32_bf16 v[16:19], a[56:59], v[176:179], v[16:19]\n\t" \
  "ds_read_b128 v[200:203], v2 offset:32768\n\t" \
  "v_mfma_f32_16x16x32_bf16 v[20:23], a[128:131], v[176:179], v[20:23]\n\t" \
  "v_add_u32 v7, s62, v3\n\t" \
  "s_mov_b32 exec_lo, 0\n\t" \
  "v_mov_b32 v7, s80\n\t" \
  "s_mov_b32 exec_lo, -1\n\t" \
  "v_mfma_f32_16x16x32_bf16 v[24:27], a[200:203], v[176:179], v[24:27]\n\t" \
  "ds_read_b128 v[204:207], v2 offset:34816\n\t" \
  "v_mfma_f32_16x16x32_bf16 v[28:31], v[240:243], v[176:179], v[28:31]\n\t" \
  "s_waitcnt lgkmcnt(5)\n\t" \
  "v_mfma_f32_16x16x32_bf16 v[16:19], a[60:63], v[184:187], v[16:19]\n\t" \
  "v_mfma_f32_16x16x32_bf16 v[20:23], a[132:135], v[184:187], v[20:23]\n\t" \
  "buffer_load_dword v4, v7, %[X], 0 offen sc1\n\t" \
  "v_mfma_f32_16x16x32_bf16 v[24:27], a[204:207], v[184:187], v[24:27]\n\t" \
  "v_mfma_f32_16x16x32_bf16 v[28:31], v[244:247], v[184:187], v[28:31]\n\t" \
  "s_waitcnt lgkmcnt(3)\n\t" \
  "v_mfma_f32_16x16x32_bf16 v[16:19], a[64:67], v[192:195], v[16:19]\n\t" \
  "v_xor_b32 v1, 0x10000, v1\n\t" \
  "v_xor_b32 v2, 0x10000, v2\n\t" \
  "v_mfma_f32_16x16x32_bf16 v[20:23], a[136:139], v[192:195], v[20:23]\n\t" \
  "v_mfma_f32_16x16x32_bf16 v[24:27], a[208:211], v[192:195], v[24:27]\n\t" \
  "v_mfma_f32_16x16x32_bf16 v[28:31], v[248:251], v[192:195], v[28:31]\n\t" \
  "s_waitcnt lgkmcnt(1)\n\t" \
  "v_mfma_f32_16x16x32_bf16 v[16:19], a[68:71], v[200:203], v[16:19]\n\t" \
  "v_mfma_f32_16x16x32_bf16 v[20:23], a[140:143], v[200:203], v[20:23]\n\t" \
  "v_mfma_f32_16x16x32_bf16 v[24:27], a[212:215], v[200:203], v[24:27]\n\t" \
  "v_mfma_f32_16x16x32_bf16 v[28:31], v[252:255], v[200:203], v[28:31]\n\t" \
  "v_mfma_f32_16x16x32_bf16 v[32:35], a[0:3], v[68:71], 0\n\t" \
  "v_cvt_pk_bf16_f32 v48, v16, v17\n\t" \
  "v_cvt_pk_bf16_f32 v49, v18, v19\n\t" \
  "v_mfma_f32_16x16x32_bf16 v[36:39], a[72:75], v[68:71], 0\n\t" \
  "v_cvt_pk_bf16_f32 v50, v20, v21\n\t" \
  "v_cvt_pk_bf16_f32 v51, v22, v23\n\t" \
  "v_mfma_f32_16x16x32_bf16 v[32:35], a[4:7], v[76:79], v[32:35]\n\t" \
  "v_cvt_pk_bf16_f32 v52, v24, v25\n\t" \
  "v_cvt_pk_bf16_f32 v53, v26, v27\n\t" \
  "v_mfma_f32_16x16x32_bf16 v[36:39], a[76:79], v[76:79], v[36:39]\n\t" \
  "v_cvt_pk_bf16_f32 v54, v28, v29\n\t" \
  "v_cvt_pk_bf16_f32 v55, v30, v31\n\t" \
  "v_mfma_f32_16x16x32_bf16 v[32:35], a[8:11], v[84:87], v[32:35]\n\t" \
  "buffer_store_dwordx4 v[48:51], v0, %[X], s50 offen offset:0 sc1\n\t" \
  "v_mfma_f32_16x16x32_bf16 v[36:39], a[80:83], v[84:87], v[36:39]\n\t" \
  "v_mfma_f32_16x16x32_bf16 v[32:35], a[12:15], v[92:95], v[32:35]\n\t" \
  "v_mfma_f32_16x16x32_bf16 v[36:39], a[84:87], v[92:95], v[36:39]\n\t" \
  "v_mfma_f32_16x16x32_bf16 v[32:35], a[16:19], v[100:103], v[32:35]\n\t" \
  "v_mfma_f32_16x16x32_bf16 v[36:39], a[88:91], v[100:103], v[36:39]\n\t" \
  "v_mfma_f32_16x16x32_bf16 v[32:35], a[20:23], v[108:111], v[32:35]\n\t" \
  "v_mfma_f32_16x16x32_bf16 v[36:39], a[92:95], v[108:111], v[36:39]\n\t" \
  "v_mfma_f32_16x16x32_bf16 v[32:35], a[24:27], v[116:119], v[32:35]\n\t" \
  "v_mfma_f32_16x16x32_bf16 v[36:39], a[96:99], v[116:119], v[36:39]\n\t" \
  "v_mfma_f32_16x16x32_bf16 v[32:35], a[28:31], v[124:127], v[32:35]\n\t" \
  "v_mfma_f32_16x16x32_bf16 v[36:39], a[100:103], v[124:127], v[36:39]\n\t" \
  "s_waitcnt vmcnt(1)\n\t" \
  "v_cmp_eq_u32 vcc, 0, v4\n\t" \
  "v_mfma_f32_16x16x32_bf16 v[32:35], a[32:35], v[132:135], v[32:35]\n\t" \
  "v_mfma_f32_16x16x32_bf16 v[36:39], a[104:107], v[132:135], v[36:39]\n\t" \
  "s_cmp_eq_u64 vcc, 0\n\t" \
  "s_cselect_b32 s60, 1, 0\n\t" \
  "v_mov_b32 v4, s60\n\t" \
  "v_mfma_f32_16x16x32_bf16 v[32:35], a[36:39], v[140:143], v[32:35]\n\t" \
  "ds_write_b32 v5, v4\n\t" \
  "v_mfma_f32_16x16x32_bf16 v[36:39], a[108:111], v[140:143], v[36:39]\n\t" \
  "v_mfma_f32_16x16x32_bf16 v[32:35], a[40:43], v[148:151], v[32:35]\n\t" \
  "v_mfma_f32_16x16x32_bf16 v[36:39], a[112:115], v[148:151], v[36:39]\n\t" \
  "buffer_store_dwordx4 v[52:55], v0, %[X], s50 offen offset:1024 sc1\n\t" \
  "v_mfma_f32_16x16x32_bf16 v[32:35], a[44:47], v[156:159], v[32:35]\n\t" \
  "v_mfma_f32_16x16x32_bf16 v[36:39], a[116:119], v[156:159], v[36:39]\n\t" \
  "s_waitcnt lgkmcnt(0)\n\t" \
  "s_barrier\n\t" \
  "v_mfma_f32_16x16x32_bf16 v[32:35], a[48:51], v[164:167], v[32:35]\n\t" \
  "ds_read_b32 v4, v6 offset:40959\n\t" \
  "v_mfma_f32_16x16x32_bf16 v[36:39], a[120:123], v[164:167], v[36:39]\n\t" \
  "v_mfma_f32_16x16x32_bf16 v[32:35], a[52:55], v[172:175], v[32:35]\n\t" \
  "v_mfma_f32_16x16x32_bf16 v[36:39], a[124:127], v[172:175], v[36:39]\n\t" \
  "v_mfma_f32_16x16x32_bf16 v[32:35], a[56:59], v[180:183], v[32:35]\n\t" \
  "v_mfma_f32_16x16x32_bf16 v[36:39], a[128:131], v[180:183], v[36:39]\n\t" \
  "s_waitcnt lgkmcnt(0)\n\t" \
  "v_readfirstlane_b32 s60, v4\n\t" \
  "s_add_u32 s55, s83, s36\n\t" \
  "v_mfma_f32_16x16x32_bf16 v[32:35], a[60:63], v[188:191], v[32:35]\n\t" \
  "s_cmp_eq_u32 s48, 63\n\t" \
  "s_cselect_b32 s56, 0, s60\n\t" \
  "v_mfma_f32_16x16x32_bf16 v[36:39], a[132:135], v[188:191], v[36:39]\n\t" \
  "s_cmp_eq_u32 s56, 0\n\t" \
  "s_cselect_b32 s55, 0xf0000000, s55\n\t" \
  "v_mfma_f32_16x16x32_bf16 v[32:35], a[64:67], v[196:199], v[32:35]\n\t" \
  "v_mfma_f32_16x16x32_bf16 v[36:39], a[136:139], v[196:199], v[36:39]\n\t" \
  "v_mfma_f32_16x16x32_bf16 v[32:35], a[68:71], v[204:207], v[32:35]\n\t" \
  "s_add_u32 m0, s46, 0\n\t" \
  "s_add_u32 s47, s55, 0\n\t" \
  "buffer_load_dwordx4 v0, %[X], s47 offen sc1 lds\n\t" \
  "v_mfma_f32_16x16x32_bf16 v[36:39], a[140:143], v[204:207], v[36:39]\n\t" \
  "v_mfma_f32_16x16x32_bf16 v[40:43], a[144:147], v[68:71], 0\n\t" \
  "v_mfma_f32_16x16x32_bf16 v[44:47], a[216:219], v[68:71], 0\n\t" \
  "v_mfma_f32_16x16x32_bf16 v[40:43], a[148:151], v[76:79], v[40:43]\n\t" \
  "v_cvt_pk_bf16_f32 v56, v32, v33\n\t" \
  "v_cvt_pk_bf16_f32 v57, v34, v35\n\t" \
  "s_add_u32 m0, s46, 4096\n\t" \
  "s_add_u32 s47, s55, 4096\n\t" \
  "buffer_load_dwordx4 v0, %[X], s47 offen sc1 lds\n\t" \
  "v_mfma_f32_16x16x32_bf16 v[44:47], a[220:223], v[76:79], v[44:47]\n\t" \
  "v_cvt_pk_bf16_f32 v58, v36, v37\n\t" \
  "v_cvt_pk_bf16_f32 v59, v38, v39\n\t" \
  "v_mfma_f32_16x16x32_bf16 v[40:43], a[152:155], v[84:87], v[40:43]\n\t" \
  "v_mfma_f32_16x16x32_bf16 v[44:47], a[224:227], v[84:87], v[44:47]\n\t" \
  "buffer_store_dwordx4 v[56:59], v0, %[X], s51 offen offset:0 sc1\n\t" \
  "v_mfma_f32_16x16x32_bf16 v[40:43], a[156:159], v[92:95], v[40:43]\n\t" \
  "s_add_u32 m0, s46, 8192\n\t" \
  "s_add_u32 s47, s55, 16384\n\t" \
  "buffer_load_dwordx4 v0, %[X], s47 offen sc1 lds\n\t" \
  "v_mfma_f32_16x16x32_bf16 v[44:47], a[228:231], v[92:95], v[44:47]\n\t" \
  "v_mfma_f32_16x16x32_bf16 v[40:43], a[160:163], v[100:103], v[40:43]\n\t" \
  "v_mfma_f32_16x16x32_bf16 v[44:47], a[232:235], v[100:103], v[44:47]\n\t" \
  "v_mfma_f32_16x16x32_bf16 v[40:43], a[164:167], v[108:111], v[40:43]\n\t" \
  "s_add_u32 m0, s46, 12288\n\t" \
  "s_add_u32 s47, s55, 20480\n\t" \
  "buffer_load_dwordx4 v0, %[X], s47 offen sc1 lds\n\t" \
  "v_mfma_f32_16x16x32_bf16 v[44:47], a[236:239], v[108:111], v[44:47]\n\t" \
  "v_mfma_f32_16x16x32_bf16 v[40:43], a[168:171], v[116:119], v[40:43]\n\t" \
  "v_mfma_f32_16x16x32_bf16 v[44:47], a[240:243], v[116:119], v[44:47]\n\t" \
  "v_mfma_f32_16x16x32_bf16 v[40:43], a[172:175], v[124:127], v[40:43]\n\t" \
  "s_add_u32 m0, s46, 16384\n\t" \
  "s_add_u32 s47, s55, 32768\n\t" \
  "buffer_load_dwordx4 v0, %[X], s47 offen sc1 lds\n\t" \
  "v_mfma_f32_16x16x32_bf16 v[44:47], a[244:247], v[124:127], v[44:47]\n\t" \
  "v_mfma_f32_16x16x32_bf16 v[40:43], a[176:179], v[132:135], v[40:43]\n\t" \
  "v_mfma_f32_16x16x32_bf16 v[44:47], a[248:251], v[132:135], v[44:47]\n\t" \
  "v_mfma_f32_16x16x32_bf16 v[40:43], a[180:183], v[140:143], v[40:43]\n\t" \
  "s_add_u32 m0, s46, 20480\n\t" \
  "s_add_u32 s47, s55, 36864\n\t" \
  "buffer_load_dwordx4 v0, %[X], s47 offen sc1 lds\n\t" \
  "v_mfma_f32_16x16x32_bf16 v[44:47], a[252:255], v[140:143], v[44:47]\n\t" \
  "v_mfma_f32_16x16x32_bf16 v[40:43], a[184:187], v[148:151], v[40:43]\n\t" \
  "v_mfma_f32_16x16x32_bf16 v[44:47], v[224:227], v[148:151], v[44:47]\n\t" \
  "v_mfma_f32_16x16x32_bf16 v[40:43], a[188:191], v[156:159], v[40:43]\n\t" \
  "s_add_u32 m0, s46, 24576\n\t" \
  "s_add_u32 s47, s55, 49152\n\t" \
  "buffer_load_dwordx4 v0, %[X], s47 offen sc1 lds\n\t" \
  "v_mfma_f32_16x16x32_bf16 v[44:47], v[228:231], v[156:159], v[44:47]\n\t" \
  "v_mfma_f32_16x16x32_bf16 v[40:43], a[192:195], v[164:167], v[40:43]\n\t" \
  "v_mfma_f32_16x16x32_bf16 v[44:47], v[232:235], v[164:167], v[44:47]\n\t" \
  "v_mfma_f32_16x16x32_bf16 v[40:43], a[196:199], v[172:175], v[40:43]\n\t" \
  "s_add_u32 m0, s46, 28672\n\t" \
  "s_add_u32 s47, s55, 53248\n\t" \
  "buffer_load_dwordx4 v0, %[X], s47 offen sc1 lds\n\t" \
  "v_mfma_f32_16x16x32_bf16 v[44:47], v[236:239], v[172:175], v[44:47]\n\t" \
  "v_mfma_f32_16x16x32_bf16 v[40:43], a[200:203], v[180:183], v[40:43]\n\t" \
  "v_mfma_f32_16x16x32_bf16 v[44:47], v[240:243], v[180:183], v[44:47]\n\t" \
  "v_mfma_f32_16x16x32_bf16 v[40:43], a[204:207], v[188:191], v[40:43]\n\t" \
  "s_add_u32 m0, s46, 32768\n\t" \
  "s_add_u32 s47, s55, 65536\n\t" \
  "buffer_load_dwordx4 v0, %[X], s47 offen sc1 lds\n\t" \
  "v_mfma_f32_16x16x32_bf16 v[44:47], v[244:247], v[188:191], v[44:47]\n\t" \
  "v_mfma_f32_16x16x32_bf16 v[40:43], a[208:211], v[196:199], v[40:43]\n\t" \
  "v_mfma_f32_16x16x32_bf16 v[44:47], v[248:251], v[196:199], v[44:47]\n\t" \
  "buffer_load_dword v8, off, %[X], s57 sc1\n\t" \
  "v_mfma_f32_16x16x32_bf16 v[40:43], a[212:215], v[204:207], v[40:43]\n\t" \
  "v_mfma_f32_16x16x32_bf16 v[44:47], v[252:255], v[204:207], v[44:47]\n\t" \
  "s_nop 4\n\t" \
  "v_cvt_pk_bf16_f32 v60, v40, v41\n\t" \
  "v_cvt_pk_bf16_f32 v61, v42, v43\n\t" \
  "s_nop 1\n\t" \
  "v_cvt_pk_bf16_f32 v62, v44, v45\n\t" \
  "v_cvt_pk_bf16_f32 v63, v46, v47\n\t" \
  "buffer_store_dwordx4 v[60:63], v0, %[X], s51 offen offset:1024 sc1\n\t" \
  TK_PQ2 \
  "s_add_u32 s49, s82, s38\n\t" \
  "s_cmp_eq_u32 s48, 63\n\t" \
  "s_cbranch_scc1 LEND_%=\n\t" \
  "s_add_u32 s48, s48, 1\n\t" \
  "s_cmp_eq_u32 s56, 0\n\t" \
  "s_cbranch_scc1 LCOLD_%=\n\t" \
  "s_waitcnt vmcnt(0)\n\t" \
  "s_mov_b64 exec, 1\n\t" \
  "buffer_store_dword v6, off, %[X], s49 sc1\n\t" \
  "s_mov_b64 exec, -1\n\t" \
  "s_mov_b32 s49, 0xf0000000\n\t" \
  "s_cmp_lt_i32 s57, 0\n\t" \
  "s_cbranch_scc1 LTOP_%=\n\t" \
  "s_movk_i32 s63, 4000\n\t" \
  "s_branch LGW_%=u\n\t" \
  "LGW_%=:\n\t" \
  "s_sleep " TK_PSG "\n\t" \
  "buffer_load_dword v8, off, %[X], s57 sc1\n\t" \
  "s_waitcnt vmcnt(0)\n\t" \
  "LGW_%=u:\n\t" \
  "s_memrealtime s[52:53]\n\t" \
  "v_readfirstlane_b32 s60, v8\n\t" \
  "s_waitcnt lgkmcnt(0)\n\t" \
  "s_sub_u32 s63, s63, 1\n\t" \
  "s_cbranch_scc1 LTOP_%=\n\t" \
  "s_cmp_eq_u32 s60, -1\n\t" \
  "s_cbranch_scc1 LGW_%=\n\t" \
  "s_not_b32 s60, s60\n\t" \
  "s_lshl_b32 s61, s52, 8\n\t" \
  "s_sub_u32 s60, s60, s61\n\t" \
  "s_ashr_i32 s60, s60, 8\n\t" \
  "s_sub_i32 s60, s60, " TK_QXW "\n\t" \
  "s_min_i32 s60, s60, 400\n\t" \
  "s_add_u32 s54, s52, s60\n\t" \
  "LGW_%=w:\n\t" \
  "s_sleep 1\n\t" \
  "s_memrealtime s[52:53]\n\t" \
  "s_waitcnt lgkmcnt(0)\n\t" \
  "s_sub_i32 s53, s52, s54\n\t" \
  "s_cmp_lt_i32 s53, 0\n\t" \
  "s_cbranch_scc1 LGW_%=w\n\t" \
  "s_branch LTOP_%=\n\t" \
  "LEND_%=:\n\t" \
  "s_waitcnt vmcnt(0)\n\t" \
  "s_mov_b64 exec, 1\n\t" \
  "buffer_store_dword v6, off, %[X], s49 sc1\n\t" \
  "s_mov_b64 exec, -1\n\t" \
  "s_mov_b32 s49, 0xf0000000\n\t" \
  TK_PQ0 \
  ""
#define QV_ASM \
  "v_lshrrev_b32 v16, 7, v0\n\t" \
  "v_lshlrev_b32 v16, 13, v16\n\t" \
  "v_lshrrev_b32 v19, 2, v0\n\t" \
  "v_and_b32 v19, 64, v19\n\t" \
  "v_xor_b32 v19, v19, v0\n\t" \
  "v_and_b32 v19, 0x70, v19\n\t" \
  "v_add_u32 v16, v16, v19\n\t" \
  "s_mul_i32 s63, s40, 40960\n\t" \
  "v_and_b32 v19, 0x300, v0\n\t" \
  "v_lshlrev_b32 v17, 2, v19\n\t" \
  "v_and_b32 v19, 0xc0, v0\n\t" \
  "v_lshl_add_u32 v17, v19, 1, v17\n\t" \
  "v_and_b32 v19, 0x30, v0\n\t" \
  "v_lshrrev_b32 v19, 1, v19\n\t" \
  "v_add3_u32 v17, v17, v19, s63\n\t" \
  "v_lshrrev_b32 v19, 1, v0\n\t" \
  "v_and_b32 v19, 64, v19\n\t" \
  "v_add_u32 v17, v17, v19\n\t" \
  "v_xor_b32 v18, 64, v17\n\t" \
  "s_add_u32 m0, s63, 0\n\t" \
  "s_mov_b32 s60, 0\n\t" \
  "buffer_load_dwordx4 v16, %[W], s60 offen lds\n\t" \
  "s_add_u32 m0, s63, 1024\n\t" \
  "s_mov_b32 s60, 65536\n\t" \
  "buffer_load_dwordx4 v16, %[W], s60 offen lds\n\t" \
  "s_add_u32 m0, s63, 2048\n\t" \
  "s_mov_b32 s60, 131072\n\t" \
  "buffer_load_dwordx4 v16, %[W], s60 offen lds\n\t" \
  "s_add_u32 m0, s63, 3072\n\t" \
  "s_mov_b32 s60, 196608\n\t" \
  "buffer_load_dwordx4 v16, %[W], s60 offen lds\n\t" \
  "s_add_u32 m0, s63, 4096\n\t" \
  "s_mov_b32 s60, 262144\n\t" \
  "buffer_load_dwordx4 v16, %[W], s60 offen lds\n\t" \
  "s_add_u32 m0, s63, 5120\n\t" \
  "s_mov_b32 s60, 327680\n\t" \
  "buffer_load_dwordx4 v16, %[W], s60 offen lds\n\t" \
  "s_add_u32 m0, s63, 6144\n\t" \
  "s_mov_b32 s60, 393216\n\t" \
  "buffer_load_dwordx4 v16, %[W], s60 offen lds\n\t" \
  "s_add_u32 m0, s63, 7168\n\t" \
  "s_mov_b32 s60, 458752\n\t" \
  "buffer_load_dwordx4 v16, %[W], s60 offen lds\n\t" \
  "s_add_u32 m0, s63, 8192\n\t" \
  "s_mov_b32 s60, 524288\n\t" \
  "buffer_load_dwordx4 v16, %[W], s60 offen lds\n\t" \
  "s_add_u32 m0, s63, 9216\n\t" \
  "s_mov_b32 s60, 589824\n\t" \
  "buffer_load_dwordx4 v16, %[W], s60 offen lds\n\t" \
  "s_add_u32 m0, s63, 10240\n\t" \
  "s_mov_b32 s60, 655360\n\t" \
  "buffer_load_dwordx4 v16, %[W], s60 offen lds\n\t" \
  "s_add_u32 m0, s63, 11264\n\t" \
  "s_mov_b32 s60, 720896\n\t" \
  "buffer_load_dwordx4 v16, %[W], s60 offen lds\n\t" \
  "s_add_u32 m0, s63, 12288\n\t" \
  "s_mov_b32 s60, 786432\n\t" \
  "buffer_load_dwordx4 v16, %[W], s60 offen lds\n\t" \
  "s_add_u32 m0, s63, 13312\n\t" \
  "s_mov_b32 s60, 851968\n\t" \
  "buffer_load_dwordx4 v16, %[W], s60 offen lds\n\t" \
  "s_add_u32 m0, s63, 14336\n\t" \
  "s_mov_b32 s60, 917504\n\t" \
  "buffer_load_dwordx4 v16, %[W], s60 offen lds\n\t" \
  "s_add_u32 m0, s63, 15360\n\t" \
  "s_mov_b32 s60, 983040\n\t" \
  "buffer_load_dwordx4 v16, %[W], s60 offen lds\n\t" \
  "s_add_u32 m0, s63, 16384\n\t" \
  "s_mov_b32 s60, 1048576\n\t" \
  "buffer_load_dwordx4 v16, %[W], s60 offen lds\n\t" \
  "s_add_u32 m0, s63, 17408\n\t" \
  "s_mov_b32 s60, 1114112\n\t" \
  "buffer_load_dwordx4 v16, %[W], s60 offen lds\n\t" \
  "s_add_u32 m0, s63, 18432\n\t" \
  "s_mov_b32 s60, 1179648\n\t" \
  "buffer_load_dwordx4 v16, %[W], s60 offen lds\n\t" \
  "s_add_u32 m0, s63, 19456\n\t" \
  "s_mov_b32 s60, 1245184\n\t" \
  "buffer_load_dwordx4 v16, %[W], s60 offen lds\n\t" \
  "s_add_u32 m0, s63, 20480\n\t" \
  "s_mov_b32 s60, 1310720\n\t" \
  "buffer_load_dwordx4 v16, %[W], s60 offen lds\n\t" \
  "s_add_u32 m0, s63, 21504\n\t" \
  "s_mov_b32 s60, 1376256\n\t" \
  "buffer_load_dwordx4 v16, %[W], s60 offen lds\n\t" \
  "s_add_u32 m0, s63, 22528\n\t" \
  "s_mov_b32 s60, 1441792\n\t" \
  "buffer_load_dwordx4 v16, %[W], s60 offen lds\n\t" \
  "s_add_u32 m0, s63, 23552\n\t" \
  "s_mov_b32 s60, 1507328\n\t" \
  "buffer_load_dwordx4 v16, %[W], s60 offen lds\n\t" \
  "s_add_u32 m0, s63, 24576\n\t" \
  "s_mov_b32 s60, 1572864\n\t" \
  "buffer_load_dwordx4 v16, %[W], s60 offen lds\n\t" \
  "s_add_u32 m0, s63, 25600\n\t" \
  "s_mov_b32 s60, 1638400\n\t" \
  "buffer_load_dwordx4 v16, %[W], s60 offen lds\n\t" \
  "s_add_u32 m0, s63, 26624\n\t" \
  "s_mov_b32 s60, 1703936\n\t" \
  "buffer_load_dwordx4 v16, %[W], s60 offen lds\n\t" \
  "s_add_u32 m0, s63, 27648\n\t" \
  "s_mov_b32 s60, 1769472\n\t" \
  "buffer_load_dwordx4 v16, %[W], s60 offen lds\n\t" \
  "s_add_u32 m0, s63, 28672\n\t" \
  "s_mov_b32 s60, 1835008\n\t" \
  "buffer_load_dwordx4 v16, %[W], s60 offen lds\n\t" \
  "s_add_u32 m0, s63, 29696\n\t" \
  "s_mov_b32 s60, 1900544\n\t" \
  "buffer_load_dwordx4 v16, %[W], s60 offen lds\n\t" \
  "s_add_u32 m0, s63, 30720\n\t" \
  "s_mov_b32 s60, 1966080\n\t" \
  "buffer_load_dwordx4 v16, %[W], s60 offen lds\n\t" \
  "s_add_u32 m0, s63, 31744\n\t" \
  "s_mov_b32 s60, 2031616\n\t" \
  "buffer_load_dwordx4 v16, %[W], s60 offen lds\n\t" \
  "s_add_u32 m0, s63, 32768\n\t" \
  "s_mov_b32 s60, 2097152\n\t" \
  "buffer_load_dwordx4 v16, %[W], s60 offen lds\n\t" \
  "s_add_u32 m0, s63, 33792\n\t" \
  "s_mov_b32 s60, 2162688\n\t" \
  "buffer_load_dwordx4 v16, %[W], s60 offen lds\n\t" \
  "s_add_u32 m0, s63, 34816\n\t" \
  "s_mov_b32 s60, 2228224\n\t" \
  "buffer_load_dwordx4 v16, %[W], s60 offen lds\n\t" \
  "s_add_u32 m0, s63, 35840\n\t" \
  "s_mov_b32 s60, 2293760\n\t" \
  "buffer_load_dwordx4 v16, %[W], s60 offen lds\n\t" \
  "s_add_u32 m0, s63, 36864\n\t" \
  "s_mov_b32 s60, 2359296\n\t" \
  "buffer_load_dwordx4 v16, %[W], s60 offen lds\n\t" \
  "s_add_u32 m0, s63, 37888\n\t" \
  "s_mov_b32 s60, 2424832\n\t" \
  "buffer_load_dwordx4 v16, %[W], s60 offen lds\n\t" \
  "s_add_u32 m0, s63, 38912\n\t" \
  "s_mov_b32 s60, 2490368\n\t" \
  "buffer_load_dwordx4 v16, %[W], s60 offen lds\n\t" \
  "s_add_u32 m0, s63, 39936\n\t" \
  "s_mov_b32 s60, 2555904\n\t" \
  "buffer_load_dwordx4 v16, %[W], s60 offen lds\n\t" \
  "s_waitcnt vmcnt(32)\n\t" \
  "ds_read_b64_tr_b16 a[0:1], v17 offset:0\n\t" \
  "ds_read_b64_tr_b16 a[2:3], v17 offset:512\n\t" \
  "ds_read_b64_tr_b16 a[4:5], v17 offset:4096\n\t" \
  "ds_read_b64_tr_b16 a[6:7], v17 offset:4608\n\t" \
  "ds_read_b64_tr_b16 a[72:73], v17 offset:32\n\t" \
  "ds_read_b64_tr_b16 a[74:75], v17 offset:544\n\t" \
  "ds_read_b64_tr_b16 a[76:77], v17 offset:4128\n\t" \
  "ds_read_b64_tr_b16 a[78:79], v17 offset:4640\n\t" \
  "ds_read_b64_tr_b16 a[144:145], v18 offset:0\n\t" \
  "ds_read_b64_tr_b16 a[146:147], v18 offset:512\n\t" \
  "ds_read_b64_tr_b16 a[148:149], v18 offset:4096\n\t" \
  "ds_read_b64_tr_b16 a[150:151], v18 offset:4608\n\t" \
  "ds_read_b64_tr_b16 a[216:217], v18 offset:32\n\t" \
  "ds_read_b64_tr_b16 a[218:219], v18 offset:544\n\t" \
  "ds_read_b64_tr_b16 a[220:221], v18 offset:4128\n\t" \
  "ds_read_b64_tr_b16 a[222:223], v18 offset:4640\n\t" \
  "s_waitcnt lgkmcnt(0)\n\t" \
  "s_add_u32 m0, s63, 0\n\t" \
  "s_mov_b32 s60, 2621440\n\t" \
  "buffer_load_dwordx4 v16, %[W], s60 offen lds\n\t" \
  "s_add_u32 m0, s63, 1024\n\t" \
  "s_mov_b32 s60, 2686976\n\t" \
  "buffer_load_dwordx4 v16, %[W], s60 offen lds\n\t" \
  "s_add_u32 m0, s63, 2048\n\t" \
  "s_mov_b32 s60, 2752512\n\t" \
  "buffer_load_dwordx4 v16, %[W], s60 offen lds\n\t" \
  "s_add_u32 m0, s63, 3072\n\t" \
  "s_mov_b32 s60, 2818048\n\t" \
  "buffer_load_dwordx4 v16, %[W], s60 offen lds\n\t" \
  "s_add_u32 m0, s63, 4096\n\t" \
  "s_mov_b32 s60, 2883584\n\t" \
  "buffer_load_dwordx4 v16, %[W], s60 offen lds\n\t" \
  "s_add_u32 m0, s63, 5120\n\t" \
  "s_mov_b32 s60, 2949120\n\t" \
  "buffer_load_dwordx4 v16, %[W], s60 offen lds\n\t" \
  "s_add_u32 m0, s63, 6144\n\t" \
  "s_mov_b32 s60, 3014656\n\t" \
  "buffer_load_dwordx4 v16, %[W], s60 offen lds\n\t" \
  "s_add_u32 m0, s63, 7168\n\t" \
  "s_mov_b32 s60, 3080192\n\t" \
  "buffer_load_dwordx4 v16, %[W], s60 offen lds\n\t" \
  "s_waitcnt vmcnt(32)\n\t" \
  "ds_read_b64_tr_b16 a[8:9], v17 offset:8192\n\t" \
  "ds_read_b64_tr_b16 a[10:11], v17 offset:8704\n\t" \
  "ds_read_b64_tr_b16 a[12:13], v17 offset:12288\n\t" \
  "ds_read_b64_tr_b16 a[14:15], v17 offset:12800\n\t" \
  "ds_read_b64_tr_b16 a[80:81], v17 offset:8224\n\t" \
  "ds_read_b64_tr_b16 a[82:83], v17 offset:8736\n\t" \
  "ds_read_b64_tr_b16 a[84:85], v17 offset:12320\n\t" \
  "ds_read_b64_tr_b16 a[86:87], v17 offset:12832\n\t" \
  "ds_read_b64_tr_b16 a[152:153], v18 offset:8192\n\t" \
  "ds_read_b64_tr_b16 a[154:155], v18 offset:8704\n\t" \
  "ds_read_b64_tr_b16 a[156:157], v18 offset:12288\n\t" \
  "ds_read_b64_tr_b16 a[158:159], v18 offset:12800\n\t" \
  "ds_read_b64_tr_b16 a[224:225], v18 offset:8224\n\t" \
  "ds_read_b64_tr_b16 a[226:227], v18 offset:8736\n\t" \
  "ds_read_b64_tr_b16 a[228:229], v18 offset:12320\n\t" \
  "ds_read_b64_tr_b16 a[230:231], v18 offset:12832\n\t" \
  "s_waitcnt lgkmcnt(0)\n\t" \
  "s_add_u32 m0, s63, 8192\n\t" \
  "s_mov_b32 s60, 3145728\n\t" \
  "buffer_load_dwordx4 v16, %[W], s60 offen lds\n\t" \
  "s_add_u32 m0, s63, 9216\n\t" \
  "s_mov_b32 s60, 3211264\n\t" \
  "buffer_load_dwordx4 v16, %[W], s60 offen lds\n\t" \
  "s_add_u32 m0, s63, 10240\n\t" \
  "s_mov_b32 s60, 3276800\n\t" \
  "buffer_load_dwordx4 v16, %[W], s60 offen lds\n\t" \
  "s_add_u32 m0, s63, 11264\n\t" \
  "s_mov_b32 s60, 3342336\n\t" \
  "buffer_load_dwordx4 v16, %[W], s60 offen lds\n\t" \
  "s_add_u32 m0, s63, 12288\n\t" \
  "s_mov_b32 s60, 3407872\n\t" \
  "buffer_load_dwordx4 v16, %[W], s60 offen lds\n\t" \
  "s_add_u32 m0, s63, 13312\n\t" \
  "s_mov_b32 s60, 3473408\n\t" \
  "buffer_load_dwordx4 v16, %[W], s60 offen lds\n\t" \
  "s_add_u32 m0, s63, 14336\n\t" \
  "s_mov_b32 s60, 3538944\n\t" \
  "buffer_load_dwordx4 v16, %[W], s60 offen lds\n\t" \
  "s_add_u32 m0, s63, 15360\n\t" \
  "s_mov_b32 s60, 3604480\n\t" \
  "buffer_load_dwordx4 v16, %[W], s60 offen lds\n\t" \
  "s_waitcnt vmcnt(32)\n\t" \
  "ds_read_b64_tr_b16 a[16:17], v17 offset:16384\n\t" \
  "ds_read_b64_tr_b16 a[18:19], v17 offset:16896\n\t" \
  "ds_read_b64_tr_b16 a[20:21], v17 offset:20480\n\t" \
  "ds_read_b64_tr_b16 a[22:23], v17 offset:20992\n\t" \
  "ds_read_b64_tr_b16 a[88:89], v17 offset:16416\n\t" \
  "ds_read_b64_tr_b16 a[90:91], v17 offset:16928\n\t" \
  "ds_read_b64_tr_b16 a[92:93], v17 offset:20512\n\t" \
  "ds_read_b64_tr_b16 a[94:95], v17 offset:21024\n\t" \
  "ds_read_b64_tr_b16 a[160:161], v18 offset:16384\n\t" \
  "ds_read_b64_tr_b16 a[162:163], v18 offset:16896\n\t" \
  "ds_read_b64_tr_b16 a[164:165], v18 offset:20480\n\t" \
  "ds_read_b64_tr_b16 a[166:167], v18 offset:20992\n\t" \
  "ds_read_b64_tr_b16 a[232:233], v18 offset:16416\n\t" \
  "ds_read_b64_tr_b16 a[234:235], v18 offset:16928\n\t" \
  "ds_read_b64_tr_b16 a[236:237], v18 offset:20512\n\t" \
  "ds_read_b64_tr_b16 a[238:239], v18 offset:21024\n\t" \
  "s_waitcnt lgkmcnt(0)\n\t" \
  "s_add_u32 m0, s63, 16384\n\t" \
  "s_mov_b32 s60, 3670016\n\t" \
  "buffer_load_dwordx4 v16, %[W], s60 offen lds\n\t" \
  "s_add_u32 m0, s63, 17408\n\t" \
  "s_mov_b32 s60, 3735552\n\t" \
  "buffer_load_dwordx4 v16, %[W], s60 offen lds\n\t" \
  "s_add_u32 m0, s63, 18432\n\t" \
  "s_mov_b32 s60, 3801088\n\t" \
  "buffer_load_dwordx4 v16, %[W], s60 offen lds\n\t" \
  "s_add_u32 m0, s63, 19456\n\t" \
  "s_mov_b32 s60, 3866624\n\t" \
  "buffer_load_dwordx4 v16, %[W], s60 offen lds\n\t" \
  "s_add_u32 m0, s63, 20480\n\t" \
  "s_mov_b32 s60, 3932160\n\t" \
  "buffer_load_dwordx4 v16, %[W], s60 offen lds\n\t" \
  "s_add_u32 m0, s63, 21504\n\t" \
  "s_mov_b32 s60, 3997696\n\t" \
  "buffer_load_dwordx4 v16, %[W], s60 offen lds\n\t" \
  "s_add_u32 m0, s63, 22528\n\t" \
  "s_mov_b32 s60, 4063232\n\t" \
  "buffer_load_dwordx4 v16, %[W], s60 offen lds\n\t" \
  "s_add_u32 m0, s63, 23552\n\t" \
  "s_mov_b32 s60, 4128768\n\t" \
  "buffer_load_dwordx4 v16, %[W], s60 offen lds\n\t" \
  "s_waitcnt vmcnt(32)\n\t" \
  "ds_read_b64_tr_b16 a[24:25], v17 offset:24576\n\t" \
  "ds_read_b64_tr_b16 a[26:27], v17 offset:25088\n\t" \
  "ds_read_b64_tr_b16 a[28:29], v17 offset:28672\n\t" \
  "ds_read_b64_tr_b16 a[30:31], v17 offset:29184\n\t" \
  "ds_read_b64_tr_b16 a[96:97], v17 offset:24608\n\t" \
  "ds_read_b64_tr_b16 a[98:99], v17 offset:25120\n\t" \
  "ds_read_b64_tr_b16 a[100:101], v17 offset:28704\n\t" \
  "ds_read_b64_tr_b16 a[102:103], v17 offset:29216\n\t" \
  "ds_read_b64_tr_b16 a[168:169], v18 offset:24576\n\t" \
  "ds_read_b64_tr_b16 a[170:171], v18 offset:25088\n\t" \
  "ds_read_b64_tr_b16 a[172:173], v18 offset:28672\n\t" \
  "ds_read_b64_tr_b16 a[174:175], v18 offset:29184\n\t" \
  "ds_read_b64_tr_b16 a[240:241], v18 offset:24608\n\t" \
  "ds_read_b64_tr_b16 a[242:243], v18 offset:25120\n\t" \
  "ds_read_b64_tr_b16 a[244:245], v18 offset:28704\n\t" \
  "ds_read_b64_tr_b16 a[246:247], v18 offset:29216\n\t" \
  "s_waitcnt lgkmcnt(0)\n\t" \
  "s_add_u32 m0, s63, 24576\n\t" \
  "s_mov_b32 s60, 4194304\n\t" \
  "buffer_load_dwordx4 v16, %[W], s60 offen lds\n\t" \
  "s_add_u32 m0, s63, 25600\n\t" \
  "s_mov_b32 s60, 4259840\n\t" \
  "buffer_load_dwordx4 v16, %[W], s60 offen lds\n\t" \
  "s_add_u32 m0, s63, 26624\n\t" \
  "s_mov_b32 s60, 4325376\n\t" \
  "buffer_load_dwordx4 v16, %[W], s60 offen lds\n\t" \
  "s_add_u32 m0, s63, 27648\n\t" \
  "s_mov_b32 s60, 4390912\n\t" \
  "buffer_load_dwordx4 v16, %[W], s60 offen lds\n\t" \
  "s_add_u32 m0, s63, 28672\n\t" \
  "s_mov_b32 s60, 4456448\n\t" \
  "buffer_load_dwordx4 v16, %[W], s60 offen lds\n\t" \
  "s_add_u32 m0, s63, 29696\n\t" \
  "s_mov_b32 s60, 4521984\n\t" \
  "buffer_load_dwordx4 v16, %[W], s60 offen lds\n\t" \
  "s_add_u32 m0, s63, 30720\n\t" \
  "s_mov_b32 s60, 4587520\n\t" \
  "buffer_load_dwordx4 v16, %[W], s60 offen lds\n\t" \
  "s_add_u32 m0, s63, 31744\n\t" \
  "s_mov_b32 s60, 4653056\n\t" \
  "buffer_load_dwordx4 v16, %[W], s60 offen lds\n\t" \
  "s_waitcnt vmcnt(32)\n\t" \
  "ds_read_b64_tr_b16 a[32:33], v17 offset:32768\n\t" \
  "ds_read_b64_tr_b16 a[34:35], v17 offset:33280\n\t" \
  "ds_read_b64_tr_b16 a[36:37], v17 offset:36864\n\t" \
  "ds_read_b64_tr_b16 a[38:39], v17 offset:37376\n\t" \
  "ds_read_b64_tr_b16 a[104:105], v17 offset:32800\n\t" \
  "ds_read_b64_tr_b16 a[106:107], v17 offset:33312\n\t" \
  "ds_read_b64_tr_b16 a[108:109], v17 offset:36896\n\t" \
  "ds_read_b64_tr_b16 a[110:111], v17 offset:37408\n\t" \
  "ds_read_b64_tr_b16 a[176:177], v18 offset:32768\n\t" \
  "ds_read_b64_tr_b16 a[178:179], v18 offset:33280\n\t" \
  "ds_read_b64_tr_b16 a[180:181], v18 offset:36864\n\t" \
  "ds_read_b64_tr_b16 a[182:183], v18 offset:37376\n\t" \
  "ds_read_b64_tr_b16 a[248:249], v18 offset:32800\n\t" \
  "ds_read_b64_tr_b16 a[250:251], v18 offset:33312\n\t" \
  "ds_read_b64_tr_b16 a[252:253], v18 offset:36896\n\t" \
  "ds_read_b64_tr_b16 a[254:255], v18 offset:37408\n\t" \
  "s_waitcnt lgkmcnt(0)\n\t" \
  "s_waitcnt vmcnt(24)\n\t" \
  "ds_read_b64_tr_b16 a[40:41], v17 offset:0\n\t" \
  "ds_read_b64_tr_b16 a[42:43], v17 offset:512\n\t" \
  "ds_read_b64_tr_b16 a[44:45], v17 offset:4096\n\t" \
  "ds_read_b64_tr_b16 a[46:47], v17 offset:4608\n\t" \
  "ds_read_b64_tr_b16 a[112:113], v17 offset:32\n\t" \
  "ds_read_b64_tr_b16 a[114:115], v17 offset:544\n\t" \
  "ds_read_b64_tr_b16 a[116:117], v17 offset:4128\n\t" \
  "ds_read_b64_tr_b16 a[118:119], v17 offset:4640\n\t" \
  "ds_read_b64_tr_b16 a[184:185], v18 offset:0\n\t" \
  "ds_read_b64_tr_b16 a[186:187], v18 offset:512\n\t" \
  "ds_read_b64_tr_b16 a[188:189], v18 offset:4096\n\t" \
  "ds_read_b64_tr_b16 a[190:191], v18 offset:4608\n\t" \
  "ds_read_b64_tr_b16 v[224:225], v18 offset:32\n\t" \
  "ds_read_b64_tr_b16 v[226:227], v18 offset:544\n\t" \
  "ds_read_b64_tr_b16 v[228:229], v18 offset:4128\n\t" \
  "ds_read_b64_tr_b16 v[230:231], v18 offset:4640\n\t" \
  "s_waitcnt lgkmcnt(0)\n\t" \
  "s_waitcnt vmcnt(16)\n\t" \
  "ds_read_b64_tr_b16 a[48:49], v17 offset:8192\n\t" \
  "ds_read_b64_tr_b16 a[50:51], v17 offset:8704\n\t" \
  "ds_read_b64_tr_b16 a[52:53], v17 offset:12288\n\t" \
  "ds_read_b64_tr_b16 a[54:55], v17 offset:12800\n\t" \
  "ds_read_b64_tr_b16 a[120:121], v17 offset:8224\n\t" \
  "ds_read_b64_tr_b16 a[122:123], v17 offset:8736\n\t" \
  "ds_read_b64_tr_b16 a[124:125], v17 offset:12320\n\t" \
  "ds_read_b64_tr_b16 a[126:127], v17 offset:12832\n\t" \
  "ds_read_b64_tr_b16 a[192:193], v18 offset:8192\n\t" \
  "ds_read_b64_tr_b16 a[194:195], v18 offset:8704\n\t" \
  "ds_read_b64_tr_b16 a[196:197], v18 offset:12288\n\t" \
  "ds_read_b64_tr_b16 a[198:199], v18 offset:12800\n\t" \
  "ds_read_b64_tr_b16 v[232:233], v18 offset:8224\n\t" \
  "ds_read_b64_tr_b16 v[234:235], v18 offset:8736\n\t" \
  "ds_read_b64_tr_b16 v[236:237], v18 offset:12320\n\t" \
  "ds_read_b64_tr_b16 v[238:239], v18 offset:12832\n\t" \
  "s_waitcnt lgkmcnt(0)\n\t" \
  "s_waitcnt vmcnt(8)\n\t" \
  "ds_read_b64_tr_b16 a[56:57], v17 offset:16384\n\t" \
  "ds_read_b64_tr_b16 a[58:59], v17 offset:16896\n\t" \
  "ds_read_b64_tr_b16 a[60:61], v17 offset:20480\n\t" \
  "ds_read_b64_tr_b16 a[62:63], v17 offset:20992\n\t" \
  "ds_read_b64_tr_b16 a[128:129], v17 offset:16416\n\t" \
  "ds_read_b64_tr_b16 a[130:131], v17 offset:16928\n\t" \
  "ds_read_b64_tr_b16 a[132:133], v17 offset:20512\n\t" \
  "ds_read_b64_tr_b16 a[134:135], v17 offset:21024\n\t" \
  "ds_read_b64_tr_b16 a[200:201], v18 offset:16384\n\t" \
  "ds_read_b64_tr_b16 a[202:203], v18 offset:16896\n\t" \
  "ds_read_b64_tr_b16 a[204:205], v18 offset:20480\n\t" \
  "ds_read_b64_tr_b16 a[206:207], v18 offset:20992\n\t" \
  "ds_read_b64_tr_b16 v[240:241], v18 offset:16416\n\t" \
  "ds_read_b64_tr_b16 v[242:243], v18 offset:16928\n\t" \
  "ds_read_b64_tr_b16 v[244:245], v18 offset:20512\n\t" \
  "ds_read_b64_tr_b16 v[246:247], v18 offset:21024\n\t" \
  "s_waitcnt lgkmcnt(0)\n\t" \
  "s_waitcnt vmcnt(0)\n\t" \
  "ds_read_b64_tr_b16 a[64:65], v17 offset:24576\n\t" \
  "ds_read_b64_tr_b16 a[66:67], v17 offset:25088\n\t" \
  "ds_read_b64_tr_b16 a[68:69], v17 offset:28672\n\t" \
  "ds_read_b64_tr_b16 a[70:71], v17 offset:29184\n\t" \
  "ds_read_b64_tr_b16 a[136:137], v17 offset:24608\n\t" \
  "ds_read_b64_tr_b16 a[138:139], v17 offset:25120\n\t" \
  "ds_read_b64_tr_b16 a[140:141], v17 offset:28704\n\t" \
  "ds_read_b64_tr_b16 a[142:143], v17 offset:29216\n\t" \
  "ds_read_b64_tr_b16 a[208:209], v18 offset:24576\n\t" \
  "ds_read_b64_tr_b16 a[210:211], v18 offset:25088\n\t" \
  "ds_read_b64_tr_b16 a[212:213], v18 offset:28672\n\t" \
  "ds_read_b64_tr_b16 a[214:215], v18 offset:29184\n\t" \
  "ds_read_b64_tr_b16 v[248:249], v18 offset:24608\n\t" \
  "ds_read_b64_tr_b16 v[250:251], v18 offset:25120\n\t" \
  "ds_read_b64_tr_b16 v[252:253], v18 offset:28704\n\t" \
  "ds_read_b64_tr_b16 v[254:255], v18 offset:29216\n\t" \
  "s_waitcnt lgkmcnt(0)\n\t" \
  "s_mov_b32 s84, " TK_CAP "\n\t" \
  "s_mov_b32 s48, 0\n\t" \
  "s_mov_b32 s49, 0xf0000000\n\t" \
  "s_mov_b32 s46, s41\n\t" \
  "s_mov_b32 s56, 0\n\t" \
  "v_lshrrev_b32 v7, 2, v0\n\t" \
  "s_mov_b32 s60, " TK_CTAB "\n\t" \
  "buffer_load_dword v4, v7, %[X], s60 offen offset:88 sc1\n\t" \
  "buffer_load_dword v7, v7, %[X], s60 offen offset:0 sc1\n\t" \
  "s_waitcnt vmcnt(0)\n\t" \
  "v_and_b32 v4, 1, v4\n\t" \
  "v_and_b32 v7, 1, v7\n\t" \
  "v_cmp_ne_u32 s[90:91], s67, v4\n\t" \
  "v_cmp_ne_u32 s[88:89], s67, v7\n\t" \
  "s_nop 1\n\t" \
  "s_mov_b32 s57, 0xf0000000\n\t" \
  "s_add_u32 s83, s48, 0\n\t" \
  "s_lshr_b64 s[60:61], s[90:91], s83\n\t" \
  "s_and_b32 s60, s60, 1\n\t" \
  "s_lshl_b32 s60, s60, 13\n\t" \
  "s_add_u32 s83, s83, " TK_SG0 "\n\t" \
  "s_lshl_b32 s83, s83, 21\n\t" \
  "s_or_b32 s83, s83, s60\n\t" \
  "s_add_u32 s80, s48, " TK_TRN "\n\t" \
  "s_lshr_b64 s[60:61], s[90:91], s80\n\t" \
  "s_and_b32 s60, s60, 1\n\t" \
  "s_lshl_b32 s60, s60, 13\n\t" \
  "s_add_u32 s80, s80, " TK_SG0 "\n\t" \
  "s_lshl_b32 s80, s80, 21\n\t" \
  "s_or_b32 s80, s80, s60\n\t" \
  "s_add_u32 s62, s83, s39\n\t" \
  "s_add_u32 s80, s80, s39\n\t" \
  "s_add_u32 s80, s80, 2048\n\t" \
  "s_cmp_lt_u32 s48, " TK_TRM "\n\t" \
  "s_cselect_b32 s80, s62, s80\n\t" \
  "v_add_u32 v7, s62, v3\n\t" \
  "s_mov_b32 exec_lo, 0\n\t" \
  "v_mov_b32 v7, s80\n\t" \
  "s_mov_b32 exec_lo, -1\n\t" \
  "LCOLD_%=:\n\t" \
  "s_waitcnt vmcnt(0)\n\t" \
  "s_mov_b64 exec, 1\n\t" \
  "buffer_store_dword v6, off, %[X], s49 sc1\n\t" \
  "s_mov_b64 exec, -1\n\t" \
  "s_mov_b32 s49, 0xf0000000\n\t" \
  "s_add_u32 s55, s83, s36\n\t" \
  "s_cmp_lg_u32 s40, 0\n\t" \
  "s_cbranch_scc1 LGO_%=\n\t" \
  "s_and_b32 s62, s45, 3\n\t" \
  "s_lshl_b32 s62, s62, 7\n\t" \
  "s_add_u32 s62, s62, s83\n\t" \
  "s_add_u32 s62, s62, s39\n\t" \
  "s_mov_b32 s61, 0\n\t" \
  "LPO_%=:\n\t" \
  "buffer_load_dword v4, off, %[X], s62 sc1\n\t" \
  "s_waitcnt vmcnt(0)\n\t" \
  "v_cmp_eq_u32 vcc, 0, v4\n\t" \
  "s_cbranch_vccz LPO_%=x\n\t" \
  "s_add_u32 s61, s61, 1\n\t" \
  "s_sleep " TK_PSH "\n\t" \
  "s_cmp_gt_u32 s61, s84\n\t" \
  "s_cbranch_scc1 LPO_%=g\n\t" \
  "s_and_b32 s60, s61, 1023\n\t" \
  "s_cmp_lg_u32 s60, 0\n\t" \
  "s_cbranch_scc1 LPO_%=\n\t" \
  "buffer_load_dword v4, off, %[X], s92 sc1\n\t" \
  "s_waitcnt vmcnt(0)\n\t" \
  "v_readfirstlane_b32 s60, v4\n\t" \
  "s_nop 0\n\t" \
  "s_cmp_eq_u32 s60, 0\n\t" \
  "s_cbranch_scc1 LPO_%=\n\t" \
  "LPO_%=g:\n\t" \
  "s_mov_b32 s84, 0\n\t" \
  "s_or_b32 s60, s48, 1610612736\n\t" \
  "v_mov_b32 v4, s60\n\t" \
  "s_mov_b64 exec, 1\n\t" \
  "buffer_store_dword v4, off, %[X], s92 sc1\n\t" \
  "s_mov_b64 exec, -1\n\t" \
  "LPO_%=x:\n\t" \
  "s_mov_b32 s61, 0\n\t" \
  "LPH_%=:\n\t" \
  "buffer_load_dword v4, v7, %[X], 0 offen sc1\n\t" \
  "s_waitcnt vmcnt(0)\n\t" \
  "v_cmp_eq_u32 vcc, 0, v4\n\t" \
  "s_cbranch_vccz LPH_%=x\n\t" \
  "s_add_u32 s61, s61, 1\n\t" \
  "s_sleep " TK_PSH "\n\t" \
  "s_cmp_gt_u32 s61, s84\n\t" \
  "s_cbranch_scc1 LPH_%=g\n\t" \
  "s_and_b32 s60, s61, 1023\n\t" \
  "s_cmp_lg_u32 s60, 0\n\t" \
  "s_cbranch_scc1 LPH_%=\n\t" \
  "buffer_load_dword v4, off, %[X], s92 sc1\n\t" \
  "s_waitcnt vmcnt(0)\n\t" \
  "v_readfirstlane_b32 s60, v4\n\t" \
  "s_nop 0\n\t" \
  "s_cmp_eq_u32 s60, 0\n\t" \
  "s_cbranch_scc1 LPH_%=\n\t" \
  "LPH_%=g:\n\t" \
  "s_mov_b32 s84, 0\n\t" \
  "s_or_b32 s60, s48, 1610612736\n\t" \
  "v_mov_b32 v4, s60\n\t" \
  "s_mov_b64 exec, 1\n\t" \
  "buffer_store_dword v4, off, %[X], s92 sc1\n\t" \
  "s_mov_b64 exec, -1\n\t" \
  "LPH_%=x:\n\t" \
  "s_cmp_lt_i32 s57, 0\n\t" \
  "s_cbranch_scc1 LGO_%=\n\t" \
  "s_movk_i32 s63, 4000\n\t" \
  "s_branch LGC_%=u\n\t" \
  "LGC_%=:\n\t" \
  "s_sleep " TK_PSG "\n\t" \
  "buffer_load_dword v8, off, %[X], s57 sc1\n\t" \
  "s_waitcnt vmcnt(0)\n\t" \
  "LGC_%=u:\n\t" \
  "s_memrealtime s[52:53]\n\t" \
  "v_readfirstlane_b32 s60, v8\n\t" \
  "s_waitcnt lgkmcnt(0)\n\t" \
  "s_sub_u32 s63, s63, 1\n\t" \
  "s_cbranch_scc1 LGO_%=\n\t" \
  "s_cmp_eq_u32 s60, -1\n\t" \
  "s_cbranch_scc1 LGC_%=\n\t" \
  "s_not_b32 s60, s60\n\t" \
  "s_lshl_b32 s61, s52, 8\n\t" \
  "s_sub_u32 s60, s60, s61\n\t" \
  "s_ashr_i32 s60, s60, 8\n\t" \
  "s_sub_i32 s60, s60, " TK_QXC "\n\t" \
  "s_min_i32 s60, s60, 400\n\t" \
  "s_add_u32 s54, s52, s60\n\t" \
  "LGC_%=w:\n\t" \
  "s_sleep 1\n\t" \
  "s_memrealtime s[52:53]\n\t" \
  "s_waitcnt lgkmcnt(0)\n\t" \
  "s_sub_i32 s53, s52, s54\n\t" \
  "s_cmp_lt_i32 s53, 0\n\t" \
  "s_cbranch_scc1 LGC_%=w\n\t" \
  "LGO_%=:\n\t" \
  "s_barrier\n\t" \
  "s_add_u32 m0, s46, 0\n\t" \
  "s_add_u32 s47, s55, 0\n\t" \
  "buffer_load_dwordx4 v0, %[X], s47 offen sc1 lds\n\t" \
  "s_add_u32 m0, s46, 4096\n\t" \
  "s_add_u32 s47, s55, 4096\n\t" \
  "buffer_load_dwordx4 v0, %[X], s47 offen sc1 lds\n\t" \
  "s_add_u32 m0, s46, 8192\n\t" \
  "s_add_u32 s47, s55, 16384\n\t" \
  "buffer_load_dwordx4 v0, %[X], s47 offen sc1 lds\n\t" \
  "s_add_u32 m0, s46, 12288\n\t" \
  "s_add_u32 s47, s55, 20480\n\t" \
  "buffer_load_dwordx4 v0, %[X], s47 offen sc1 lds\n\t" \
  "s_add_u32 m0, s46, 16384\n\t" \
  "s_add_u32 s47, s55, 32768\n\t" \
  "buffer_load_dwordx4 v0, %[X], s47 offen sc1 lds\n\t" \
  "s_add_u32 m0, s46, 20480\n\t" \
  "s_add_u32 s47, s55, 36864\n\t" \
  "buffer_load_dwordx4 v0, %[X], s47 offen sc1 lds\n\t" \
  "s_add_u32 m0, s46, 24576\n\t" \
  "s_add_u32 s47, s55, 49152\n\t" \
  "buffer_load_dwordx4 v0, %[X], s47 offen sc1 lds\n\t" \
  "s_add_u32 m0, s46, 28672\n\t" \
  "s_add_u32 s47, s55, 53248\n\t" \
  "buffer_load_dwordx4 v0, %[X], s47 offen sc1 lds\n\t" \
  "s_add_u32 m0, s46, 32768\n\t" \
  "s_add_u32 s47, s55, 65536\n\t" \
  "buffer_load_dwordx4 v0, %[X], s47 offen sc1 lds\n\t" \
  "s_waitcnt vmcnt(5)\n\t" \
  "LTOP_%=:\n\t" \
  "s_barrier\n\t" \
  TK_PQ1 \
  "ds_read_b128 v[64:67], v1 offset:0\n\t" \
  "ds_read_b128 v[68:71], v1 offset:2048\n\t" \
  "ds_read_b128 v[72:75], v2 offset:0\n\t" \
  "ds_read_b128 v[76:79], v2 offset:2048\n\t" \
  "ds_read_b128 v[80:83], v1 offset:4096\n\t" \
  "ds_read_b128 v[84:87], v1 offset:6144\n\t" \
  "s_mov_b32 s82, s83\n\t" \
  "s_xor_b32 s46, s46, 0x10000\n\t" \
  "s_waitcnt lgkmcnt(5)\n\t" \
  "v_mfma_f32_16x16x32_bf16 v[16:19], v[64:67], a[0:3], 0\n\t" \
  "ds_read_b128 v[88:91], v2 offset:4096\n\t" \
  "s_waitcnt lgkmcnt(5)\n\t" \
  "v_mfma_f32_16x16x32_bf16 v[20:23], v[68:71], a[0:3], 0\n\t" \
  "s_add_u32 s50, s48, 0\n\t" \
  "s_and_b32 s50, s50, " TK_TRM "\n\t" \
  "v_mfma_f32_16x16x32_bf16 v[24:27], v[64:67], a[72:75], 0\n\t" \
  "ds_read_b128 v[92:95], v2 offset:6144\n\t" \
  "v_mfma_f32_16x16x32_bf16 v[28:31], v[68:71], a[72:75], 0\n\t" \
  "s_lshr_b64 s[60:61], s[90:91], s50\n\t" \
  "s_and_b32 s60, s60, 1\n\t" \
  "s_waitcnt lgkmcnt(5)\n\t" \
  "v_mfma_f32_16x16x32_bf16 v[16:19], v[72:75], a[4:7], v[16:19]\n\t" \
  "ds_read_b128 v[96:99], v1 offset:8192\n\t" \
  "s_waitcnt lgkmcnt(5)\n\t" \
  "v_mfma_f32_16x16x32_bf16 v[20:23], v[76:79], a[4:7], v[20:23]\n\t" \
  "s_lshl_b32 s60, s60, 13\n\t" \
  "s_add_u32 s50, s50, " TK_SG0 "\n\t" \
  "v_mfma_f32_16x16x32_bf16 v[24:27], v[72:75], a[76:79], v[24:27]\n\t" \
  "ds_read_b128 v[100:103], v1 offset:10240\n\t" \
  "v_mfma_f32_16x16x32_bf16 v[28:31], v[76:79], a[76:79], v[28:31]\n\t" \
  "s_lshl_b32 s50, s50, 21\n\t" \
  "s_or_b32 s50, s50, s60\n\t" \
  "s_waitcnt lgkmcnt(5)\n\t" \
  "v_mfma_f32_16x16x32_bf16 v[16:19], v[80:83], a[8:11], v[16:19]\n\t" \
  "ds_read_b128 v[104:107], v2 offset:8192\n\t" \
  "s_waitcnt lgkmcnt(5)\n\t" \
  "v_mfma_f32_16x16x32_bf16 v[20:23], v[84:87], a[8:11], v[20:23]\n\t" \
  "s_add_u32 s50, s50, s37\n\t" \
  "s_add_u32 s51, s50, 4096\n\t" \
  "v_mfma_f32_16x16x32_bf16 v[24:27], v[80:83], a[80:83], v[24:27]\n\t" \
  "ds_read_b128 v[108:111], v2 offset:10240\n\t" \
  "v_mfma_f32_16x16x32_bf16 v[28:31], v[84:87], a[80:83], v[28:31]\n\t" \
  "s_add_u32 s83, s48, 1\n\t" \
  "s_lshr_b64 s[60:61], s[90:91], s83\n\t" \
  "s_waitcnt lgkmcnt(5)\n\t" \
  "v_mfma_f32_16x16x32_bf16 v[16:19], v[88:91], a[12:15], v[16:19]\n\t" \
  "ds_read_b128 v[112:115], v1 offset:12288\n\t" \
  "s_waitcnt lgkmcnt(5)\n\t" \
  "v_mfma_f32_16x16x32_bf16 v[20:23], v[92:95], a[12:15], v[20:23]\n\t" \
  "s_and_b32 s60, s60, 1\n\t" \
  "s_lshl_b32 s60, s60, 13\n\t" \
  "v_mfma_f32_16x16x32_bf16 v[24:27], v[88:91], a[84:87], v[24:27]\n\t" \
  "ds_read_b128 v[116:119], v1 offset:14336\n\t" \
  "v_mfma_f32_16x16x32_bf16 v[28:31], v[92:95], a[84:87], v[28:31]\n\t" \
  "s_add_u32 s83, s83, " TK_SG0 "\n\t" \
  "s_lshl_b32 s83, s83, 21\n\t" \
  "s_waitcnt lgkmcnt(5)\n\t" \
  "v_mfma_f32_16x16x32_bf16 v[16:19], v[96:99], a[16:19], v[16:19]\n\t" \
  "ds_read_b128 v[120:123], v2 offset:12288\n\t" \
  "s_waitcnt lgkmcnt(5)\n\t" \
  "v_mfma_f32_16x16x32_bf16 v[20:23], v[100:103], a[16:19], v[20:23]\n\t" \
  "s_or_b32 s83, s83, s60\n\t" \
  "s_cmp_lg_u32 s56, 0\n\t" \
  "v_mfma_f32_16x16x32_bf16 v[24:27], v[96:99], a[88:91], v[24:27]\n\t" \
  "ds_read_b128 v[124:127], v2 offset:14336\n\t" \
  "v_mfma_f32_16x16x32_bf16 v[28:31], v[100:103], a[88:91], v[28:31]\n\t" \
  "s_cbranch_scc1 LWARM_%=\n\t" \
  "s_waitcnt vmcnt(0)\n\t" \
  "LWARM_%=:\n\t" \
  "s_barrier\n\t" \
  "s_waitcnt lgkmcnt(5)\n\t" \
  "v_mfma_f32_16x16x32_bf16 v[16:19], v[104:107], a[20:23], v[16:19]\n\t" \
  "ds_read_b128 v[128:131], v1 offset:16384\n\t" \
  "s_waitcnt lgkmcnt(5)\n\t" \
  "v_mfma_f32_16x16x32_bf16 v[20:23], v[108:111], a[20:23], v[20:23]\n\t" \
  "v_mfma_f32_16x16x32_bf16 v[24:27], v[104:107], a[92:95], v[24:27]\n\t" \
  "ds_read_b128 v[132:135], v1 offset:18432\n\t" \
  "v_mfma_f32_16x16x32_bf16 v[28:31], v[108:111], a[92:95], v[28:31]\n\t" \
  "s_add_i32 s62, s48, s42\n\t" \
  "s_add_i32 s62, s62, 1\n\t" \
  "s_waitcnt lgkmcnt(5)\n\t" \
  "v_mfma_f32_16x16x32_bf16 v[16:19], v[112:115], a[24:27], v[16:19]\n\t" \
  "ds_read_b128 v[136:139], v2 offset:16384\n\t" \
  "s_waitcnt lgkmcnt(5)\n\t" \
  "v_mfma_f32_16x16x32_bf16 v[20:23], v[116:119], a[24:27], v[20:23]\n\t" \
  "s_mov_b32 s61, s62\n\t" \
  "v_mfma_f32_16x16x32_bf16 v[24:27], v[112:115], a[96:99], v[24:27]\n\t" \
  "ds_read_b128 v[140:143], v2 offset:18432\n\t" \
  "v_mfma_f32_16x16x32_bf16 v[28:31], v[116:119], a[96:99], v[28:31]\n\t" \
  "s_mul_i32 s63, s62, 43\n\t" \
  "s_lshr_b32 s63, s63, 9\n\t" \
  "s_waitcnt lgkmcnt(5)\n\t" \
  "v_mfma_f32_16x16x32_bf16 v[16:19], v[120:123], a[28:31], v[16:19]\n\t" \
  "ds_read_b128 v[144:147], v1 offset:20480\n\t" \
  "s_waitcnt lgkmcnt(5)\n\t" \
  "v_mfma_f32_16x16x32_bf16 v[20:23], v[124:127], a[28:31], v[20:23]\n\t" \
  "s_mul_i32 s63, s63, " TK_RM "\n\t" \
  "s_sub_u32 s62, s62, s63\n\t" \
  "v_mfma_f32_16x16x32_bf16 v[24:27], v[120:123], a[100:103], v[24:27]\n\t" \
  "ds_read_b128 v[148:151], v1 offset:22528\n\t" \
  "v_mfma_f32_16x16x32_bf16 v[28:31], v[124:127], a[100:103], v[28:31]\n\t" \
  "s_mul_i32 s63, s62, 43\n\t" \
  "s_lshr_b32 s63, s63, 7\n\t" \
  "s_waitcnt lgkmcnt(5)\n\t" \
  "v_mfma_f32_16x16x32_bf16 v[16:19], v[128:131], a[32:35], v[16:19]\n\t" \
  "ds_read_b128 v[152:155], v2 offset:20480\n\t" \
  "s_waitcnt lgkmcnt(5)\n\t" \
  "v_mfma_f32_16x16x32_bf16 v[20:23], v[132:135], a[32:35], v[20:23]\n\t" \
  "s_mul_i32 s57, s62, 688128\n\t" \
  "s_lshl_b32 s60, s63, 15\n\t" \
  "v_mfma_f32_16x16x32_bf16 v[24:27], v[128:131], a[104:107], v[24:27]\n\t" \
  "ds_read_b128 v[156:159], v2 offset:22528\n\t" \
  "v_mfma_f32_16x16x32_bf16 v[28:31], v[132:135], a[104:107], v[28:31]\n\t" \
  "s_add_u32 s57, s57, s60\n\t" \
  "s_lshr_b32 s60, s88, s63\n\t" \
  "s_waitcnt lgkmcnt(5)\n\t" \
  "v_mfma_f32_16x16x32_bf16 v[16:19], v[136:139], a[36:39], v[16:19]\n\t" \
  "ds_read_b128 v[160:163], v1 offset:24576\n\t" \
  "s_waitcnt lgkmcnt(5)\n\t" \
  "v_mfma_f32_16x16x32_bf16 v[20:23], v[140:143], a[36:39], v[20:23]\n\t" \
  "s_and_b32 s60, s60, 1\n\t" \
  "s_lshl_b32 s60, s60, 13\n\t" \
  "v_mfma_f32_16x16x32_bf16 v[24:27], v[136:139], a[108:111], v[24:27]\n\t" \
  "ds_read_b128 v[164:167], v1 offset:26624\n\t" \
  "v_mfma_f32_16x16x32_bf16 v[28:31], v[140:143], a[108:111], v[28:31]\n\t" \
  "s_add_u32 s57, s57, s60\n\t" \
  "s_add_u32 s57, s57, 656896\n\t" \
  "s_waitcnt lgkmcnt(5)\n\t" \
  "v_mfma_f32_16x16x32_bf16 v[16:19], v[144:147], a[40:43], v[16:19]\n\t" \
  "ds_read_b128 v[168:171], v2 offset:24576\n\t" \
  "s_waitcnt lgkmcnt(5)\n\t" \
  "v_mfma_f32_16x16x32_bf16 v[20:23], v[148:151], a[40:43], v[20:23]\n\t" \
  "s_cmp_lt_i32 s61, 1\n\t" \
  "s_cselect_b32 s57, 0xf0000000, s57\n\t" \
  "v_mfma_f32_16x16x32_bf16 v[24:27], v[144:147], a[112:115], v[24:27]\n\t" \
  "ds_read_b128 v[172:175], v2 offset:26624\n\t" \
  "v_mfma_f32_16x16x32_bf16 v[28:31], v[148:151], a[112:115], v[28:31]\n\t" \
  "s_cmp_lg_u32 s40, 0\n\t" \
  "s_cselect_b32 s57, 0xf0000000, s57\n\t" \
  "s_waitcnt lgkmcnt(5)\n\t" \
  "v_mfma_f32_16x16x32_bf16 v[16:19], v[152:155], a[44:47], v[16:19]\n\t" \
  "ds_read_b128 v[176:179], v1 offset:28672\n\t" \
  "s_waitcnt lgkmcnt(5)\n\t" \
  "v_mfma_f32_16x16x32_bf16 v[20:23], v[156:159], a[44:47], v[20:23]\n\t" \
  "s_add_u32 s80, s48, " TK_TRN "\n\t" \
  "s_lshr_b64 s[60:61], s[90:91], s80\n\t" \
  "s_and_b32 s60, s60, 1\n\t" \
  "v_mfma_f32_16x16x32_bf16 v[24:27], v[152:155], a[116:119], v[24:27]\n\t" \
  "ds_read_b128 v[180:183], v1 offset:30720\n\t" \
  "v_mfma_f32_16x16x32_bf16 v[28:31], v[156:159], a[116:119], v[28:31]\n\t" \
  "s_lshl_b32 s60, s60, 13\n\t" \
  "s_add_u32 s80, s80, " TK_SG0 "\n\t" \
  "s_lshl_b32 s80, s80, 21\n\t" \
  "s_waitcnt lgkmcnt(5)\n\t" \
  "v_mfma_f32_16x16x32_bf16 v[16:19], v[160:163], a[48:51], v[16:19]\n\t" \
  "ds_read_b128 v[184:187], v2 offset:28672\n\t" \
  "s_waitcnt lgkmcnt(5)\n\t" \
  "v_mfma_f32_16x16x32_bf16 v[20:23], v[164:167], a[48:51], v[20:23]\n\t" \
  "s_or_b32 s80, s80, s60\n\t" \
  "s_add_u32 s62, s83, s39\n\t" \
  "s_add_u32 s80, s80, s39\n\t" \
  TK_PQ0 \
  "v_mfma_f32_16x16x32_bf16 v[24:27], v[160:163], a[120:123], v[24:27]\n\t" \
  "ds_read_b128 v[188:191], v2 offset:30720\n\t" \
  "v_mfma_f32_16x16x32_bf16 v[28:31], v[164:167], a[120:123], v[28:31]\n\t" \
  "s_add_u32 s80, s80, 2048\n\t" \
  "s_waitcnt lgkmcnt(5)\n\t" \
  "v_mfma_f32_16x16x32_bf16 v[16:19], v[168:171], a[52:55], v[16:19]\n\t" \
  "ds_read_b128 v[192:195], v1 offset:32768\n\t" \
  "s_waitcnt lgkmcnt(5)\n\t" \
  "v_mfma_f32_16x16x32_bf16 v[20:23], v[172:175], a[52:55], v[20:23]\n\t" \
  "v_mfma_f32_16x16x32_bf16 v[24:27], v[168:171], a[124:127], v[24:27]\n\t" \
  "ds_read_b128 v[196:199], v1 offset:34816\n\t" \
  "v_mfma_f32_16x16x32_bf16 v[28:31], v[172:175], a[124:127], v[28:31]\n\t" \
  "s_cmp_lt_u32 s48, " TK_TRM "\n\t" \
  "s_cselect_b32 s80, s62, s80\n\t" \
  "s_waitcnt lgkmcnt(5)\n\t" \
  "v_mfma_f32_16x16x32_bf16 v[16:19], v[176:179], a[56:59], v[16:19]\n\t" \
  "ds_read_b128 v[200:203], v2 offset:32768\n\t" \
  "s_waitcnt lgkmcnt(5)\n\t" \
  "v_mfma_f32_16x16x32_bf16 v[20:23], v[180:183], a[56:59], v[20:23]\n\t" \
  "v_add_u32 v7, s62, v3\n\t" \
  "s_mov_b32 exec_lo, 0\n\t" \
  "v_mov_b32 v7, s80\n\t" \
  "s_mov_b32 exec_lo, -1\n\t" \
  "v_mfma_f32_16x16x32_bf16 v[24:27], v[176:179], a[128:131], v[24:27]\n\t" \
  "ds_read_b128 v[204:207], v2 offset:34816\n\t" \
  "v_mfma_f32_16x16x32_bf16 v[28:31], v[180:183], a[128:131], v[28:31]\n\t" \
  "s_waitcnt lgkmcnt(5)\n\t" \
  "v_mfma_f32_16x16x32_bf16 v[16:19], v[184:187], a[60:63], v[16:19]\n\t" \
  "s_waitcnt lgkmcnt(4)\n\t" \
  "v_mfma_f32_16x16x32_bf16 v[20:23], v[188:191], a[60:63], v[20:23]\n\t" \
  "buffer_load_dword v4, v7, %[X], 0 offen sc1\n\t" \
  "v_mfma_f32_16x16x32_bf16 v[24:27], v[184:187], a[132:135], v[24:27]\n\t" \
  "v_mfma_f32_16x16x32_bf16 v[28:31], v[188:191], a[132:135], v[28:31]\n\t" \
  "s_waitcnt lgkmcnt(3)\n\t" \
  "v_mfma_f32_16x16x32_bf16 v[16:19], v[192:195], a[64:67], v[16:19]\n\t" \
  "v_xor_b32 v1, 0x10000, v1\n\t" \
  "v_xor_b32 v2, 0x10000, v2\n\t" \
  "s_waitcnt lgkmcnt(2)\n\t" \
  "v_mfma_f32_16x16x32_bf16 v[20:23], v[196:199], a[64:67], v[20:23]\n\t" \
  "v_mfma_f32_16x16x32_bf16 v[24:27], v[192:195], a[136:139], v[24:27]\n\t" \
  "v_mfma_f32_16x16x32_bf16 v[28:31], v[196:199], a[136:139], v[28:31]\n\t" \
  "s_waitcnt lgkmcnt(1)\n\t" \
  "v_mfma_f32_16x16x32_bf16 v[16:19], v[200:203], a[68:71], v[16:19]\n\t" \
  "s_waitcnt lgkmcnt(0)\n\t" \
  "v_mfma_f32_16x16x32_bf16 v[20:23], v[204:207], a[68:71], v[20:23]\n\t" \
  "v_mfma_f32_16x16x32_bf16 v[24:27], v[200:203], a[140:143], v[24:27]\n\t" \
  "v_mfma_f32_16x16x32_bf16 v[28:31], v[204:207], a[140:143], v[28:31]\n\t" \
  "v_mfma_f32_16x16x32_bf16 v[32:35], v[64:67], a[144:147], 0\n\t" \
  "v_cvt_pk_bf16_f32 v48, v16, v17\n\t" \
  "v_cvt_pk_bf16_f32 v49, v18, v19\n\t" \
  "v_mfma_f32_16x16x32_bf16 v[36:39], v[68:71], a[144:147], 0\n\t" \
  "v_cvt_pk_bf16_f32 v50, v20, v21\n\t" \
  "v_cvt_pk_bf16_f32 v51, v22, v23\n\t" \
  "v_mfma_f32_16x16x32_bf16 v[32:35], v[72:75], a[148:151], v[32:35]\n\t" \
  "v_cvt_pk_bf16_f32 v52, v24, v25\n\t" \
  "v_cvt_pk_bf16_f32 v53, v26, v27\n\t" \
  "v_mfma_f32_16x16x32_bf16 v[36:39], v[76:79], a[148:151], v[36:39]\n\t" \
  "v_cvt_pk_bf16_f32 v54, v28, v29\n\t" \
  "v_cvt_pk_bf16_f32 v55, v30, v31\n\t" \
  "v_mfma_f32_16x16x32_bf16 v[32:35], v[80:83], a[152:155], v[32:35]\n\t" \
  "buffer_store_dwordx4 v[48:51], v0, %[X], s50 offen offset:0 sc1\n\t" \
  "v_mfma_f32_16x16x32_bf16 v[36:39], v[84:87], a[152:155], v[36:39]\n\t" \
  "v_mfma_f32_16x16x32_bf16 v[32:35], v[88:91], a[156:159], v[32:35]\n\t" \
  "v_mfma_f32_16x16x32_bf16 v[36:39], v[92:95], a[156:159], v[36:39]\n\t" \
  "v_mfma_f32_16x16x32_bf16 v[32:35], v[96:99], a[160:163], v[32:35]\n\t" \
  "v_mfma_f32_16x16x32_bf16 v[36:39], v[100:103], a[160:163], v[36:39]\n\t" \
  "v_mfma_f32_16x16x32_bf16 v[32:35], v[104:107], a[164:167], v[32:35]\n\t" \
  "v_mfma_f32_16x16x32_bf16 v[36:39], v[108:111], a[164:167], v[36:39]\n\t" \
  "v_mfma_f32_16x16x32_bf16 v[32:35], v[112:115], a[168:171], v[32:35]\n\t" \
  "v_mfma_f32_16x16x32_bf16 v[36:39], v[116:119], a[168:171], v[36:39]\n\t" \
  "v_mfma_f32_16x16x32_bf16 v[32:35], v[120:123], a[172:175], v[32:35]\n\t" \
  "v_mfma_f32_16x16x32_bf16 v[36:39], v[124:127], a[172:175], v[36:39]\n\t" \
  "s_waitcnt vmcnt(1)\n\t" \
  "v_cmp_eq_u32 vcc, 0, v4\n\t" \
  "v_mfma_f32_16x16x32_bf16 v[32:35], v[128:131], a[176:179], v[32:35]\n\t" \
  "v_mfma_f32_16x16x32_bf16 v[36:39], v[132:135], a[176:179], v[36:39]\n\t" \
  "s_cmp_eq_u64 vcc, 0\n\t" \
  "s_cselect_b32 s60, 1, 0\n\t" \
  "v_mov_b32 v4, s60\n\t" \
  "v_mfma_f32_16x16x32_bf16 v[32:35], v[136:139], a[180:183], v[32:35]\n\t" \
  "ds_write_b32 v5, v4\n\t" \
  "v_mfma_f32_16x16x32_bf16 v[36:39], v[140:143], a[180:183], v[36:39]\n\t" \
  "v_mfma_f32_16x16x32_bf16 v[32:35], v[144:147], a[184:187], v[32:35]\n\t" \
  "v_mfma_f32_16x16x32_bf16 v[36:39], v[148:151], a[184:187], v[36:39]\n\t" \
  "buffer_store_dwordx4 v[52:55], v0, %[X], s50 offen offset:1024 sc1\n\t" \
  "v_mfma_f32_16x16x32_bf16 v[32:35], v[152:155], a[188:191], v[32:35]\n\t" \
  "v_mfma_f32_16x16x32_bf16 v[36:39], v[156:159], a[188:191], v[36:39]\n\t" \
  "s_waitcnt lgkmcnt(0)\n\t" \
  "s_barrier\n\t" \
  "v_mfma_f32_16x16x32_bf16 v[32:35], v[160:163], a[192:195], v[32:35]\n\t" \
  "ds_read_b32 v4, v6 offset:40959\n\t" \
  "v_mfma_f32_16x16x32_bf16 v[36:39], v[164:167], a[192:195], v[36:39]\n\t" \
  "v_mfma_f32_16x16x32_bf16 v[32:35], v[168:171], a[196:199], v[32:35]\n\t" \
  "v_mfma_f32_16x16x32_bf16 v[36:39], v[172:175], a[196:199], v[36:39]\n\t" \
  "v_mfma_f32_16x16x32_bf16 v[32:35], v[176:179], a[200:203], v[32:35]\n\t" \
  "v_mfma_f32_16x16x32_bf16 v[36:39], v[180:183], a[200:203], v[36:39]\n\t" \
  "s_waitcnt lgkmcnt(0)\n\t" \
  "v_readfirstlane_b32 s60, v4\n\t" \
  "s_add_u32 s55, s83, s36\n\t" \
  "v_mfma_f32_16x16x32_bf16 v[32:35], v[184:187], a[204:207], v[32:35]\n\t" \
  "s_cmp_eq_u32 s48, 63\n\t" \
  "s_cselect_b32 s56, 0, s60\n\t" \
  "v_mfma_f32_16x16x32_bf16 v[36:39], v[188:191], a[204:207], v[36:39]\n\t" \
  "s_cmp_eq_u32 s56, 0\n\t" \
  "s_cselect_b32 s55, 0xf0000000, s55\n\t" \
  "v_mfma_f32_16x16x32_bf16 v[32:35], v[192:195], a[208:211], v[32:35]\n\t" \
  "v_mfma_f32_16x16x32_bf16 v[36:39], v[196:199], a[208:211], v[36:39]\n\t" \
  "v_mfma_f32_16x16x32_bf16 v[32:35], v[200:203], a[212:215], v[32:35]\n\t" \
  "s_add_u32 m0, s46, 0\n\t" \
  "s_add_u32 s47, s55, 0\n\t" \
  "buffer_load_dwordx4 v0, %[X], s47 offen sc1 lds\n\t" \
  "v_mfma_f32_16x16x32_bf16 v[36:39], v[204:207], a[212:215], v[36:39]\n\t" \
  "v_mfma_f32_16x16x32_bf16 v[40:43], v[64:67], a[216:219], 0\n\t" \
  "v_mfma_f32_16x16x32_bf16 v[44:47], v[68:71], a[216:219], 0\n\t" \
  "v_mfma_f32_16x16x32_bf16 v[40:43], v[72:75], a[220:223], v[40:43]\n\t" \
  "v_cvt_pk_bf16_f32 v56, v32, v33\n\t" \
  "v_cvt_pk_bf16_f32 v57, v34, v35\n\t" \
  "s_add_u32 m0, s46, 4096\n\t" \
  "s_add_u32 s47, s55, 4096\n\t" \
  "buffer_load_dwordx4 v0, %[X], s47 offen sc1 lds\n\t" \
  "v_mfma_f32_16x16x32_bf16 v[44:47], v[76:79], a[220:223], v[44:47]\n\t" \
  "v_cvt_pk_bf16_f32 v58, v36, v37\n\t" \
  "v_cvt_pk_bf16_f32 v59, v38, v39\n\t" \
  "v_mfma_f32_16x16x32_bf16 v[40:43], v[80:83], a[224:227], v[40:43]\n\t" \
  "v_mfma_f32_16x16x32_bf16 v[44:47], v[84:87], a[224:227], v[44:47]\n\t" \
  "buffer_store_dwordx4 v[56:59], v0, %[X], s50 offen offset:2048 sc1\n\t" \
  "v_mfma_f32_16x16x32_bf16 v[40:43], v[88:91], a[228:231], v[40:43]\n\t" \
  "s_add_u32 m0, s46, 8192\n\t" \
  "s_add_u32 s47, s55, 16384\n\t" \
  "buffer_load_dwordx4 v0, %[X], s47 offen sc1 lds\n\t" \
  "v_mfma_f32_16x16x32_bf16 v[44:47], v[92:95], a[228:231], v[44:47]\n\t" \
  "v_mfma_f32_16x16x32_bf16 v[40:43], v[96:99], a[232:235], v[40:43]\n\t" \
  "v_mfma_f32_16x16x32_bf16 v[44:47], v[100:103], a[232:235], v[44:47]\n\t" \
  "v_mfma_f32_16x16x32_bf16 v[40:43], v[104:107], a[236:239], v[40:43]\n\t" \
  "s_add_u32 m0, s46, 12288\n\t" \
  "s_add_u32 s47, s55, 20480\n\t" \
  "buffer_load_dwordx4 v0, %[X], s47 offen sc1 lds\n\t" \
  "v_mfma_f32_16x16x32_bf16 v[44:47], v[108:111], a[236:239], v[44:47]\n\t" \
  "v_mfma_f32_16x16x32_bf16 v[40:43], v[112:115], a[240:243], v[40:43]\n\t" \
  "v_mfma_f32_16x16x32_bf16 v[44:47], v[116:119], a[240:243], v[44:47]\n\t" \
  "v_mfma_f32_16x16x32_bf16 v[40:43], v[120:123], a[244:247], v[40:43]\n\t" \
  "s_add_u32 m0, s46, 16384\n\t" \
  "s_add_u32 s47, s55, 32768\n\t" \
  "buffer_load_dwordx4 v0, %[X], s47 offen sc1 lds\n\t" \
  "v_mfma_f32_16x16x32_bf16 v[44:47], v[124:127], a[244:247], v[44:47]\n\t" \
  "v_mfma_f32_16x16x32_bf16 v[40:43], v[128:131], a[248:251], v[40:43]\n\t" \
  "v_mfma_f32_16x16x32_bf16 v[44:47], v[132:135], a[248:251], v[44:47]\n\t" \
  "v_mfma_f32_16x16x32_bf16 v[40:43], v[136:139], a[252:255], v[40:43]\n\t" \
  "s_add_u32 m0, s46, 20480\n\t" \
  "s_add_u32 s47, s55, 36864\n\t" \
  "buffer_load_dwordx4 v0, %[X], s47 offen sc1 lds\n\t" \
  "v_mfma_f32_16x16x32_bf16 v[44:47], v[140:143], a[252:255], v[44:47]\n\t" \
  "v_mfma_f32_16x16x32_bf16 v[40:43], v[144:147], v[224:227], v[40:43]\n\t" \
  "v_mfma_f32_16x16x32_bf16 v[44:47], v[148:151], v[224:227], v[44:47]\n\t" \
  "v_mfma_f32_16x16x32_bf16 v[40:43], v[152:155], v[228:231], v[40:43]\n\t" \
  "s_add_u32 m0, s46, 24576\n\t" \
  "s_add_u32 s47, s55, 49152\n\t" \
  "buffer_load_dwordx4 v0, %[X], s47 offen sc1 lds\n\t" \
  "v_mfma_f32_16x16x32_bf16 v[44:47], v[156:159], v[228:231], v[44:47]\n\t" \
  "v_mfma_f32_16x16x32_bf16 v[40:43], v[160:163], v[232:235], v[40:43]\n\t" \
  "v_mfma_f32_16x16x32_bf16 v[44:47], v[164:167], v[232:235], v[44:47]\n\t" \
  "v_mfma_f32_16x16x32_bf16 v[40:43], v[168:171], v[236:239], v[40:43]\n\t" \
  "s_add_u32 m0, s46, 28672\n\t" \
  "s_add_u32 s47, s55, 53248\n\t" \
  "buffer_load_dwordx4 v0, %[X], s47 offen sc1 lds\n\t" \
  "v_mfma_f32_16x16x32_bf16 v[44:47], v[172:175], v[236:239], v[44:47]\n\t" \
  "v_mfma_f32_16x16x32_bf16 v[40:43], v[176:179], v[240:243], v[40:43]\n\t" \
  "v_mfma_f32_16x16x32_bf16 v[44:47], v[180:183], v[240:243], v[44:47]\n\t" \
  "v_mfma_f32_16x16x32_bf16 v[40:43], v[184:187], v[244:247], v[40:43]\n\t" \
  "s_add_u32 m0, s46, 32768\n\t" \
  "s_add_u32 s47, s55, 65536\n\t" \
  "buffer_load_dwordx4 v0, %[X], s47 offen sc1 lds\n\t" \
  "v_mfma_f32_16x16x32_bf16 v[44:47], v[188:191], v[244:247], v[44:47]\n\t" \
  "v_mfma_f32_16x16x32_bf16 v[40:43], v[192:195], v[248:251], v[40:43]\n\t" \
  "v_mfma_f32_16x16x32_bf16 v[44:47], v[196:199], v[248:251], v[44:47]\n\t" \
  "buffer_load_dword v8, off, %[X], s57 sc1\n\t" \
  "v_mfma_f32_16x16x32_bf16 v[40:43], v[200:203], v[252:255], v[40:43]\n\t" \
  "v_mfma_f32_16x16x32_bf16 v[44:47], v[204:207], v[252:255], v[44:47]\n\t" \
  "s_nop 4\n\t" \
  "v_cvt_pk_bf16_f32 v60, v40, v41\n\t" \
  "v_cvt_pk_bf16_f32 v61, v42, v43\n\t" \
  "s_nop 1\n\t" \
  "v_cvt_pk_bf16_f32 v62, v44, v45\n\t" \
  "v_cvt_pk_bf16_f32 v63, v46, v47\n\t" \
  "buffer_store_dwordx4 v[60:63], v0, %[X], s50 offen offset:3072 sc1\n\t" \
  TK_PQ2 \
  "s_add_u32 s49, s82, s38\n\t" \
  "s_cmp_eq_u32 s48, 63\n\t" \
  "s_cbranch_scc1 LEND_%=\n\t" \
  "s_add_u32 s48, s48, 1\n\t" \
  "s_cmp_eq_u32 s56, 0\n\t" \
  "s_cbranch_scc1 LCOLD_%=\n\t" \
  "s_waitcnt vmcnt(0)\n\t" \
  "s_mov_b64 exec, 1\n\t" \
  "buffer_store_dword v6, off, %[X], s49 sc1\n\t" \
  "s_mov_b64 exec, -1\n\t" \
  "s_mov_b32 s49, 0xf0000000\n\t" \
  "s_cmp_lt_i32 s57, 0\n\t" \
  "s_cbranch_scc1 LTOP_%=\n\t" \
  "s_movk_i32 s63, 4000\n\t" \
  "s_branch LGW_%=u\n\t" \
  "LGW_%=:\n\t" \
  "s_sleep " TK_PSG "\n\t" \
  "buffer_load_dword v8, off, %[X], s57 sc1\n\t" \
  "s_waitcnt vmcnt(0)\n\t" \
  "LGW_%=u:\n\t" \
  "s_memrealtime s[52:53]\n\t" \
  "v_readfirstlane_b32 s60, v8\n\t" \
  "s_waitcnt lgkmcnt(0)\n\t" \
  "s_sub_u32 s63, s63, 1\n\t" \
  "s_cbranch_scc1 LTOP_%=\n\t" \
  "s_cmp_eq_u32 s60, -1\n\t" \
  "s_cbranch_scc1 LGW_%=\n\t" \
  "s_not_b32 s60, s60\n\t" \
  "s_lshl_b32 s61, s52, 8\n\t" \
  "s_sub_u32 s60, s60, s61\n\t" \
  "s_ashr_i32 s60, s60, 8\n\t" \
  "s_sub_i32 s60, s60, " TK_QXW "\n\t" \
  "s_min_i32 s60, s60, 400\n\t" \
  "s_add_u32 s54, s52, s60\n\t" \
  "LGW_%=w:\n\t" \
  "s_sleep 1\n\t" \
  "s_memrealtime s[52:53]\n\t" \
  "s_waitcnt lgkmcnt(0)\n\t" \
  "s_sub_i32 s53, s52, s54\n\t" \
  "s_cmp_lt_i32 s53, 0\n\t" \
  "s_cbranch_scc1 LGW_%=w\n\t" \
  "s_branch LTOP_%=\n\t" \
  "LEND_%=:\n\t" \
  "s_waitcnt vmcnt(0)\n\t" \
  "s_mov_b64 exec, 1\n\t" \
  "buffer_store_dword v6, off, %[X], s49 sc1\n\t" \
  "s_mov_b64 exec, -1\n\t" \
  "s_mov_b32 s49, 0xf0000000\n\t" \
  TK_PQ0 \
  ""
__device__ __forceinline__ unsigned hoff(int r, int piece) {
  const int m = 4 * (r >> 3) + (r & 3), kp = piece >> 3;
  return (P_H + (unsigned)(kp >> 1)) * PAIR + (unsigned)((kp & 1) * 4096 + (16 * ((r >> 2) & 1) + m) * 128 + (((piece ^ m) & 7) << 4));
}
template <int SLEEP>
__device__ __forceinline__ void wait_all(rsrc_t X, unsigned vo, unsigned so) {
  unsigned spins = 0;
  while (true) {
    asm volatile("" ::: "memory");
    if (__all(ld4(X, vo, so) != 0u)) break;
    if (spin_fail(spins, X)) break;
    __builtin_amdgcn_s_sleep(SLEEP);
  }
  asm volatile("" ::: "memory");
}
#define VQ_CLOB "v8", "v16", "v17", "v18", "v19", "v20", "v21", "v22", "v23", "v24", "v25", "v26", "v27", "v28", "v29", "v30", "v31", "v32", "v33", "v34", "v35", "v36", "v37", "v38", "v39", "v40", "v41", "v42", "v43", "v44", "v45", "v46", "v47", "v48", "v49", "v50", "v51", "v52", "v53", "v54", "v55", "v56", "v57", "v58", "v59", "v60", "v61", "v62", "v63", "v64", "v65", "v66", "v67", "v68", "v69", "v70", "v71", "v72", "v73", "v74", "v75", "v76", "v77", "v78", "v79", "v80", "v81", "v82", "v83", "v84", "v85", "v86", "v87", "v88", "v89", "v90", "v91", "v92", "v93", "v94", "v95", "v96", "v97", "v98", "v99", "v100", "v101", "v102", "v103", "v104", "v105", "v106", "v107", "v108", "v109", "v110", "v111", "v112", "v113", "v114", "v115", "v116", "v117", "v118", "v119", "v120", "v121", "v122", "v123", "v124", "v125", "v126", "v127", "v128", "v129", "v130", "v131", "v132", "v133", "v134", "v135", "v136", "v137", "v138", "v139", "v140", "v141", "v142", "v143", "v144", "v145", "v146", "v147", "v148", "v149", "v150", "v151", "v152", "v153", "v154", "v155", "v156", "v157", "v158", "v159", "v160", "v161", "v162", "v163", "v164", "v165", "v166", "v167", "v168", "v169", "v170", "v171", "v172", "v173", "v174", "v175", "v176", "v177", "v178", "v179", "v180", "v181", "v182", "v183", "v184", "v185", "v186", "v187", "v188", "v189", "v190", "v191", "v192", "v193", "v194", "v195", "v196", "v197", "v198", "v199", "v200", "v201", "v202", "v203", "v204", "v205", "v206", "v207", "v220", "v221"
#define VA_CLOB "v40", "v41", "v42", "v43", "v44", "v45", "v46", "v47", "v48", "v49", "v50", "v51", "v52", "v53", "v54", "v55", "v56", "v57", "v58", "v59", "v60", "v61", "v62", "v63", "v64", "v65", "v66", "v67", "v68", "v69", "v70", "v71", "v72", "v73", "v74", "v75", "v76", "v77", "v78", "v79", "v80", "v81", "v82", "v83", "v84", "v85", "v86", "v87", "v88", "v89", "v90", "v91", "v92", "v93", "v94", "v95", "v96", "v97", "v98", "v99", "v100", "v101", "v102", "v103", "v104", "v105", "v106", "v107", "v108", "v109", "v110", "v111", "v112", "v113", "v114", "v115", "v116", "v117", "v118", "v119", "v120", "v121", "v122", "v123", "v124", "v125", "v126", "v127", "v128", "v129", "v130", "v131", "v132", "v133", "v134", "v135", "v136", "v137", "v138", "v139", "v140", "v141", "v142", "v143", "v144", "v145", "v146", "v147", "v148", "v149", "v150", "v151", "v152", "v153", "v154", "v155", "v156", "v157", "v158", "v159", "v160", "v161", "v162", "v163", "v164", "v165", "v166", "v167", "v168", "v169", "v170", "v171", "v172", "v173", "v174", "v175", "v176", "v177", "v178", "v179", "v180", "v181", "v182", "v183", "v184", "v185", "v186", "v187", "v188", "v189", "v190", "v191", "v192", "v193", "v194", "v195", "v196", "v197", "v198", "v199", "v200", "v201", "v202", "v203", "v204", "v205", "v206", "v207", "v208", "v209", "v210", "v211", "v220", "v221"
#define TOK_COMMON() \
  const MP p = getp(); \
  const int c = (int)blockIdx.x - NCHW, xq = c & 3, L = (c >> 2) & 1, i = c >> 3; \
  const int tid = threadIdx.x, lane = tid & 63, w = __builtin_amdgcn_readfirstlane(tid >> 6); \
  const rsrc_t X = mkr(p.xb, XB_BYTES)
#define W_IN [W] "s"(WR)
#define W_CLOB "a0", "a1", "a2", "a3", "a4", "a5", "a6", "a7", "a8", "a9", "a10", "a11", "a12", "a13", "a14", "a15", "a16", "a17", "a18", "a19", "a20", "a21", "a22", "a23", "a24", "a25", "a26", "a27", "a28", "a29", "a30", "a31", "a32", "a33", "a34", "a35", "a36", "a37", "a38", "a39", "a40", "a41", "a42", "a43", "a44", "a45", "a46", "a47", "a48", "a49", "a50", "a51", "a52", "a53", "a54", "a55", "a56", "a57", "a58", "a59", "a60", "a61", "a62", "a63", "a64", "a65", "a66", "a67", "a68", "a69", "a70", "a71", "a72", "a73", "a74", "a75", "a76", "a77", "a78", "a79", "a80", "a81", "a82", "a83", "a84", "a85", "a86", "a87", "a88", "a89", "a90", "a91", "a92", "a93", "a94", "a95", "a96", "a97", "a98", "a99", "a100", "a101", "a102", "a103", "a104", "a105", "a106", "a107", "a108", "a109", "a110", "a111", "a112", "a113", "a114", "a115", "a116", "a117", "a118", "a119", "a120", "a121", "a122", "a123", "a124", "a125", "a126", "a127", "a128", "a129", "a130", "a131", "a132", "a133", "a134", "a135", "a136", "a137", "a138", "a139", "a140", "a141", "a142", "a143", "a144", "a145", "a146", "a147", "a148", "a149", "a150", "a151", "a152", "a153", "a154", "a155", "a156", "a157", "a158", "a159", "a160", "a161", "a162", "a163", "a164", "a165", "a166", "a167", "a168", "a169", "a170", "a171", "a172", "a173", "a174", "a175", "a176", "a177", "a178", "a179", "a180", "a181", "a182", "a183", "a184", "a185", "a186", "a187", "a188", "a189", "a190", "a191", "a192", "a193", "a194", "a195", "a196", "a197", "a198", "a199", "a200", "a201", "a202", "a203", "a204", "a205", "a206", "a207", "a208", "a209", "a210", "a211", "a212", "a213", "a214", "a215", "a216", "a217", "a218", "a219", "a220", "a221", "a222", "a223", "a224", "a225", "a226", "a227", "a228", "a229", "a230", "a231", "a232", "a233", "a234", "a235", "a236", "a237", "a238", "a239", "a240", "a241", "a242", "a243", "a244", "a245", "a246", "a247", "a248", "a249", "a250", "a251", "a252", "a253", "a254", "a255", "v224", "v225", "v226", "v227", "v228", "v229", "v230", "v231", "v232", "v233", "v234", "v235", "v236", "v237", "v238", "v239", "v240", "v241", "v242", "v243", "v244", "v245", "v246", "v247", "v248", "v249", "v250", "v251", "v252", "v253", "v254", "v255"
#define BOOT_IN "{s67}"(L), [ctab] "i"(CTAB_OFF), [rm] "i"(SRING == 12 ? 12 : 0), [trm] "i"(TRING - 1), [trn] "i"(1 - TRING)
#define S_CLOB "s80", "s81", "s76", "s78", "s79", "s86", "s88", "s89", "s90", "s91", "s46", "s47", "s48", "s49", "s50", "s51", "s52", "s53", "s54", "s55", "s56", "s57", "s58", "s59", "s60", "s61", "s62", "s63", "s68", "s69", "s70", "s71", "s72", "s73", "s82", "s83", "s84", "m0", "vcc", "scc", "memory"
typedef unsigned v8w __attribute__((ext_vector_type(8)));

__device__ __forceinline__ void qkv_role() {
  TOK_COMMON();
  const int role = i * 4 + w, hh = 8 * xq + role / 6, part = role % 6, which = part >> 1, half = part & 1;
  const rsrc_t WR = mkr((which == 0 ? p.Wq : (which == 1 ? p.Wk : p.Wv)) + hh * DH + 64 * half, DM * HD * 2u - (unsigned)(hh * DH + 64 * half) * 2u);
  const int m = lane & 15, g = lane >> 4;
  const unsigned a0 = (unsigned)(m * 128 + ((g ^ (m & 7)) << 4));
  v8w lv = {(unsigned)lane * 16u, a0, a0 ^ 64u, (unsigned)(lane & (L ? 15 : 3)) * 128u, 0u, 40960u + 4u * (unsigned)w, 1u, 0u};
  const unsigned c36 = XQL + P_H * PAIR + (unsigned)w * 1024u, c37 = XQ + (unsigned)(3 * hh + which) * PAIR + (unsigned)half * (which == 2 ? 4096u : 2048u);
  const unsigned c38 = P_FA * PAIR + (unsigned)(hh * 128 + part * 16), c39 = w == 0 ? P_FB * PAIR + 4096u : 0xF0000000u, c41 = (unsigned)w * 1024u;
  const int c42 = L ? 1 - QLEAD1 : 1 - QLEAD;
  unsigned d74 = TRC_OFF + 262144u + (unsigned)((c * 4 + w) * 66) * 8u;
  TT(64, 0);
#define Q_CALL(A) asm volatile("s_waitcnt vmcnt(0) lgkmcnt(0)\n\t" A : "+{v[0:7]}"(lv), "+{s74}"(d74) \
               : [X] "s"(X), "{s36}"(c36), "{s37}"(c37), "{s38}"(c38), "{s39}"(c39), "{s40}"(w), "{s41}"(c41), "{s42}"(c42), "{s45}"(i), "{s92}"(TMO_OFF), BOOT_IN, [cap] "i"(SPIN_CAP), [qxw] "i"(QXW), [qxc] "i"(QXC), W_IN : VQ_CLOB, W_CLOB, S_CLOB)
  if (which == 2) { Q_CALL(QV_ASM "LQVEND:"); __builtin_amdgcn_endpgm(); }
  Q_CALL(QK_ASM "LQKEND:");
  __builtin_amdgcn_endpgm();
}

__device__ __forceinline__ void ap_role() {
  TOK_COMMON();
  const int hp = 4 * xq + ((i - 12) >> 1), ch = i & 1, a = w >> 1, rt = w & 1, h = 2 * hp + a;
  const int n = lane & 15, g = lane >> 4;
  const rsrc_t WR = mkr(p.Wo + (long)hp * 2 * DH * DM, 2u * DH * DM * 2u);
  const unsigned l16 = (unsigned)lane * 16u, ab = (unsigned)a * 24576u + l16;
  v16u lv = {l16, ab, ab + (unsigned)rt * 4096u, 49152u + (unsigned)w * 4096u + l16, 49152u + (unsigned)(w ^ 2) * 4096u + l16, l16 & ~32u, l16 >> 1,
             (unsigned)(g * 262144 + n * 16), (unsigned)(n * 32 + g * 8), (unsigned)((lane & 7) % 6) * 16u, 0u, 0u, 0u, 0u, 0u, 0u};
  v8w mk;
  const int qi = 8 * (n >> 2) + (n & 3) + 4 * rt;
#pragma unroll
  for (int k = 0; k < 8; ++k) mk[k] = 8 * g + k > qi ? 0xFF800000u : 0u;
  v16u zz = {0u, 0u, 0u, 0u, 0u, 0u, 0u, 0u, 0u, 0u, 0u, 0u, 0u, 0u, 0u, 0u};
  const unsigned tb = XAL + (unsigned)(3 * h) * PAIR;
  const unsigned c36 = tb + (rt ? PAIR + 4096u : 0u), c37 = tb + (rt ? 2u * PAIR : 4096u), c38 = tb + (rt ? 2u * PAIR + 4096u : PAIR);
  const unsigned c39 = XK + (P_KV + (unsigned)(h >> 1)) * PAIR + (unsigned)(h & 1) * 4096u, c41 = P_FA * PAIR + (unsigned)h * 128u;
  const unsigned c42 = w == 0 ? P_FB * PAIR + (unsigned)(2 * hp + ch) * 128u : 0xF0000000u;
  const unsigned c43 = XA + TP0 * XBLK + (unsigned)(8 * rt + (hp >> 3)) * PAIR + (unsigned)((hp & 7) * 1024 + ch * 512 + a * 256);
  unsigned d44 = XT + (TP0 + 8u) * XBLK + (unsigned)(hp >> 1) * PAIR + (unsigned)((((hp & 1) * 4 + 2 * ch + a) * 2 + rt) * 512);
  const unsigned c45 = (unsigned)(a * 24576 + rt * 12288);
  unsigned d74 = TRC_OFF + 262144u + (unsigned)((c * 4 + w) * 66) * 8u;
  asm volatile("s_waitcnt vmcnt(0) lgkmcnt(0)\n\t" AP_ASM "LAPEND:" : "+{v[0:15]}"(lv), "+{v[24:39]}"(zz), "+{s74}"(d74), "+{s44}"(d44)
               : [X] "s"(X), "{s36}"(c36), "{s37}"(c37), "{s38}"(c38), "{s39}"(c39), "{s41}"(c41), "{s42}"(c42), "{s43}"(c43), "{s45}"(c45), "{s64}"(512 * ch + 256 * a), "{s65}"(1024 + 64 * ch + 32 * a), "{s66}"(147456 * a), "{s77}"(L ? AX1 : AX), BOOT_IN,
                 "{s92}"(TMO_OFF), [cap] "i"(SPIN_CAP), "{v[16:23]}"(mk), W_IN : VA_CLOB, W_CLOB, S_CLOB);
  __builtin_amdgcn_endpgm();
}

template <int N> __device__ __forceinline__ float rorN(float v) { return __uint_as_float((unsigned)__builtin_amdgcn_update_dpp(0, (int)__float_as_uint(v), 0x120 + N, 0xf, 0xf, false)); }
__device__ __forceinline__ void up8(v4u v, float* x) {
  x[0] = lo2f(v.x); x[1] = hi2f(v.x); x[2] = lo2f(v.y); x[3] = hi2f(v.y); x[4] = lo2f(v.z); x[5] = hi2f(v.z); x[6] = lo2f(v.w); x[7] = hi2f(v.w);
}
__device__ __forceinline__ v4u pk8(const float* x) { return (v4u){pk2(x[0], x[1]), pk2(x[2], x[3]), pk2(x[4], x[5]), pk2(x[6], x[7])}; }

__device__ __forceinline__ void rb9(float (&x)[9]) {
#pragma unroll
  for (int i = 0; i < 8; i += 2) { const unsigned u = pk2(x[i], x[i + 1]); x[i] = lo2f(u); x[i + 1] = hi2f(u); }
  x[8] = lo2f(pk2(x[8], x[8]));
}
template <int R>
__device__ __forceinline__ void lnR(const float (&x)[R][9], float (&o)[R][9]) {
  float s[2 * R], pv[R];
#pragma unroll
  for (int r = 0; r < R; ++r) {
    pv[r] = __uint_as_float(__builtin_amdgcn_readfirstlane(__float_as_uint(x[r][0])));
    const f32x2 p2 = {pv[r], pv[r]};
    f32x2 d[4];
#pragma unroll
    for (int i = 0; i < 4; ++i) d[i] = (f32x2){x[r][2 * i], x[r][2 * i + 1]} - p2;
    const float d8 = x[r][8] - pv[r];
    const f32x2 a = (d[0] + d[1]) + (d[2] + d[3]);
    const f32x2 q = __builtin_elementwise_fma(d[3], d[3], __builtin_elementwise_fma(d[2], d[2], __builtin_elementwise_fma(d[1], d[1], d[0] * d[0])));
    s[2 * r] = (a[0] + d8) + a[1];
    s[2 * r + 1] = __builtin_fmaf(d8, d8, q[0]) + q[1];
  }
#pragma unroll
  for (int k = 0; k < 2 * R; ++k) s[k] += rorN<8>(s[k]);
#pragma unroll
  for (int k = 0; k < 2 * R; ++k) s[k] += rorN<4>(s[k]);
#pragma unroll
  for (int k = 0; k < 2 * R; ++k) s[k] += rorN<2>(s[k]);
#pragma unroll
  for (int k = 0; k < 2 * R; ++k) s[k] += rorN<1>(s[k]);
#pragma unroll
  for (int r = 0; r < R; ++r) {
    auto u = __builtin_amdgcn_permlane16_swap(__float_as_uint(s[2 * r]), __float_as_uint(s[2 * r + 1]), false, false);
    s[2 * r] = __uint_as_float(u[0]) + __uint_as_float(u[1]);
  }
#pragma unroll
  for (int r = 0; r < R; ++r) {
    const unsigned u = __float_as_uint(s[2 * r]);
    const float md = (__uint_as_float(__builtin_amdgcn_readlane(u, 0)) + __uint_as_float(__builtin_amdgcn_readlane(u, 32))) * (1.0f / DM);
    const float sq = (__uint_as_float(__builtin_amdgcn_readlane(u, 16)) + __uint_as_float(__builtin_amdgcn_readlane(u, 48))) * (1.0f / DM);
    const unsigned mv = pk2(pv[r] + md, __builtin_fmaxf(__builtin_fmaf(-md, md, sq), 0.f));
    const float m = lo2f(mv), var = hi2f(mv);
    const float ve = lo2f(pk2(var + 1e-5f, 0.f));
    const float den = lo2f(pk2(__builtin_amdgcn_sqrtf(ve), 0.f));
    const float inv = __builtin_amdgcn_rcpf(den);
#pragma unroll
    for (int i = 0; i < 9; ++i) o[r][i] = (x[r][i] - m) * inv;
  }
}
__device__ __forceinline__ void ln9(const float (&x)[9], float (&o)[9]) { lnR<1>((const float (&)[1][9])x, (float (&)[1][9])o); }
__device__ __forceinline__ v4u gat8(float v) {
  const unsigned d = pk2(v, __uint_as_float((unsigned)__builtin_amdgcn_mov_dpp((int)__float_as_uint(v), 0xB1, 0xf, 0xf, true)));
  return (v4u){d, (unsigned)__builtin_amdgcn_mov_dpp((int)d, 0x102, 0xf, 0xf, true), (unsigned)__builtin_amdgcn_mov_dpp((int)d, 0x104, 0xf, 0xf, true),
               (unsigned)__builtin_amdgcn_mov_dpp((int)d, 0x106, 0xf, 0xf, true)};
}
__device__ __forceinline__ void tok_row(const MP& p, const rsrc_t X, unsigned sb1, int L, int s, int r, int lane, const v4u (&v)[18], v4u px, v4u py, unsigned px9, unsigned py9) {
  const long tok = (long)s * SEG + r;
  const int m = lane & 15, g = lane >> 4;
  const int xp = 4 * (g & 1) + 2 * (g >> 1) + (m >> 3);
  const unsigned zr = opq(0u);
  const unsigned one = (g == (m >> 2)) ? (opq(0x3F80u) << (16 * (m & 1))) : zr;
  const unsigned ax = (m & 2) ? zr : one, ay = (m & 2) ? one : zr;
  const v4u A1 = {ax, ay, zr, zr}, A2 = {zr, zr, ax, ay};
  const unsigned on2 = opq(0x3F80u) << (16 * (m & 1));
  const int q2 = (m >> 1) & 3;
  const v4u sel = {q2 == 0 ? on2 : zr, q2 == 1 ? on2 : zr, q2 == 2 ? on2 : zr, q2 == 3 ? on2 : zr};
  f32x4 d1, d2, e1;
  asm volatile("s_nop 1\n\t" MF "%0, %1, %3, 0\n\t" MF "%0, %2, %3, %0" : "=&v"(e1) : "v"(v[0]), "v"(v[1]), "v"(sel));
#pragma unroll
  for (int i = 0; i < 16; ++i) {
    if (i == 0) asm volatile("s_nop 1\n\t" MF "%0, %2, %4, 0\n\t" MF "%1, %3, %4, 0" : "=&v"(d1), "=&v"(d2) : "v"(A1), "v"(A2), "v"(v[2]));
    else asm volatile(MF "%0, %2, %4, %0\n\t" MF "%1, %3, %4, %1" : "+v"(d1), "+v"(d2) : "v"(A1), "v"(A2), "v"(v[2 + i]));
  }
  asm volatile("s_nop 7" : "+v"(d1), "+v"(d2), "+v"(e1));
  float o[9];
#pragma unroll
  for (int i = 0; i < 4; ++i) { o[i] = d1[i]; o[4 + i] = d2[i]; }
  {
    auto a02 = __builtin_amdgcn_permlane32_swap(__float_as_uint(e1[0]), __float_as_uint(e1[2]), false, false);
    auto a13 = __builtin_amdgcn_permlane32_swap(__float_as_uint(e1[1]), __float_as_uint(e1[3]), false, false);
    o[8] = m >= 8 ? __uint_as_float(a13[0]) + __uint_as_float(a13[1]) : __uint_as_float(a02[0]) + __uint_as_float(a02[1]);
  }
  float x[9];
  up8(px, x); x[8] = lo2f(px9);
#pragma unroll
  for (int i = 0; i < 9; ++i) x[i] += o[i];
  if (L == 0) {
    float y[9], ln[9];
    up8(py, y); y[8] = lo2f(py9);
    rb9(x);
    ln9(x, ln);
#pragma unroll
    for (int i = 0; i < 9; ++i) x[i] = __builtin_fmaf(ln[i], y[i], x[i]);
    rb9(x);
    st16(X, (unsigned)lane * 16u, sb1 + (P_X3 + (unsigned)(r >> 3)) * PAIR + (unsigned)(r & 7) * 1024u, pk8(x));
    const v4u x9 = gat8(x[8]);
    if ((lane & 7) == 0) st16(X, (unsigned)xp * 16u, sb1 + (P_H + 4u) * PAIR + 4096u + (unsigned)r * 128u, x9);
    ln9(x, ln);
    st16(X, hoff(r, lane), sb1, pk8(ln));
    const v4u h9 = gat8(ln[8]);
    if ((lane & 7) == 0) st16(X, hoff(r, 64 + xp), sb1, h9);
  } else {
    const rsrc_t O = mkr(p.out + tok * DM, DM * 2u);
    __builtin_amdgcn_raw_buffer_store_b128(pk8(x), O, (unsigned)lane * 16u, 0u, OAUX);
    __builtin_amdgcn_raw_buffer_store_b128(gat8(x[8]), O, (lane & 7) == 0 ? 1024u + (unsigned)xp * 16u : 0xF0000000u, 0u, OAUX);
  }
}

__device__ __forceinline__ void row_role(char* smem) {
  TOK_COMMON();
  const int rw = 4 * xq + (i - 20), r = 2 * rw + (w >> 1);
  const unsigned long long sm = die_mask(X, SG0, L, lane);
  const unsigned tm = (unsigned)die_mask(X, TP0, L, lane), dm = (unsigned)die_mask(X, 0u, L, lane);
  __attribute__((address_space(3))) unsigned* cnt = (__attribute__((address_space(3))) unsigned*)smem;
  BAR();
  if (tid < 64) cnt[tid] = 0u;
  BAR();
#define ANCT 25000
  for (int s = w & 1; s < NSEG; s += 2) {
    int ln_ = lane; asm volatile("" : "+v"(ln_));
    const int m = ln_ & 15, g = ln_ >> 4;
    const unsigned l16 = (unsigned)ln_ * 16u, xo = (unsigned)(8 * (4 * (g & 1) + 2 * (g >> 1) + (m >> 3)) + (m & 7)) * 2u;
    const unsigned sb = SEGB(sm, s), sb1 = sb ^ (L ? 0u : 8192u);
    if (s + ARMK < NSEG) {
      const unsigned sv = opq(SENT), sl = SLOT_AT((s + ARMK) % SRING) + ((dm >> ((s + ARMK) % SRING / 3)) & 1u) * 8192u;
      for (int j = r; j < 84; j += 32) {
#pragma unroll
        for (int k = 0; k < 4; ++k) st16(X, l16 + (unsigned)k * 1024u, sl + (unsigned)(j >> 1) * PAIR + (unsigned)(j & 1) * 4096u, (v4u){sv, sv, sv, sv});
      }
    }
    wait_all<PSL>(X, 0u, sb + P_FB * PAIR + 4096u + (unsigned)(L ? rw : r >> 3) * 128u);
    const v4u px = ld16(X, l16, sb + (P_X3 + (unsigned)(r >> 3)) * PAIR + (unsigned)(r & 7) * 1024u);
    const unsigned px9 = __builtin_amdgcn_raw_buffer_load_b16(X, xo, sb + (P_H + 4u) * PAIR + 4096u + (unsigned)r * 128u, 16);
    const rsrc_t YX = mkr(p.yT + (2 * s + (r >> 4)) * 9216, L ? 0u : 18432u);
    const v2u ya = ld8(YX, (unsigned)(ln_ * 256 + ((r ^ m) & 15) * 8), 0u), yb = ld8(YX, (unsigned)(ln_ * 256 + 128 + ((r ^ m) & 15) * 8), 0u);
    const v4u py = {ya.x, ya.y, yb.x, yb.y};
    const unsigned py9 = __builtin_amdgcn_raw_buffer_load_b16(YX, 16384u + (xo >> 3) * 128u + (((unsigned)r ^ (xo >> 4)) & 15u) * 8u + (xo & 6u), 0u, 16);
    wait_all<PSP>(X, 0u, sb + P_FB * PAIR + (unsigned)r * 128u);
    __builtin_amdgcn_s_sleep(RONE);
    wait_all<PSP>(X, (unsigned)(ln_ & 31) * 128u, sb + P_FB * PAIR);
    TT(s, 0);
    const unsigned j = (unsigned)s & 15u, to = XRL + TP0 * XBLK + (j << 20) + ((tm >> (j >> 1)) & 1u) * 8192u + (unsigned)r * 2u * PAIR;
    const unsigned t9 = (TP0 + 8u) * XBLK + ((tm >> 8) & 1u) * 8192u + (j << 17) + (unsigned)(((r >> 2) & 1) * 512 + (4 * (r >> 3) + (r & 3)) * 32);
    const unsigned v9 = (unsigned)(g * (int)PAIR + (m >> 1) * 1024 + (m & 1) * 16);
    v4u v[18];
#pragma unroll
    for (int k = 0; k < 2; ++k) v[k] = ld16(X, v9, t9 + (unsigned)k * 4u * PAIR);
#pragma unroll
    for (int k = 0; k < 16; ++k) v[2 + k] = ld16(X, l16 + (unsigned)(k & 3) * 1024u, to + (unsigned)(k >> 3) * PAIR + (unsigned)(k & 4) * 1024u);
    SB();
    tok_row(p, X, sb1, L, s, r, ln_, v, px, py, px9, py9);
    VM0();
    unsigned o1 = 0u;
    if (ln_ == 0) o1 = __hip_atomic_fetch_add(cnt + s, 1u, __ATOMIC_RELAXED, __HIP_MEMORY_SCOPE_WORKGROUP);
    if (__builtin_amdgcn_readfirstlane(o1) == 1u && ln_ < 2 - L) st4(X, ln_ ? sb1 : sb + 2048u, P_FB * PAIR + 4096u + (unsigned)rw * 128u, opq(1u));
    TT(s, 1);
  }
}

__device__ __forceinline__ void rows_task(const MP& p, const rsrc_t X, unsigned long long sm, u16* ys, int grp, int ng) {
  const int tid = threadIdx.x, lane = tid & 63, w = __builtin_amdgcn_readfirstlane(tid >> 6);
  const int m = lane & 15, g = lane >> 4;
  const int xp = 4 * (g & 1) + 2 * (g >> 1) + (m >> 3), c9 = 512 + 8 * xp + (m & 7);
  const unsigned sb = SEGB(sm, grp >> 2);
  v4u yv[5], xa[4];
  unsigned x9[4];
#pragma unroll
  for (int i = 0; i < 5; ++i) yv[i] = ld16(mkr(p.yT + (grp >> 1) * 9216, 18432u), (unsigned)(tid + 256 * i) * 16u, 0u);
#pragma unroll
  for (int rr = 0; rr < 4; ++rr) {
    const long t = grp * 8 + w + 4 * (rr & (2 * ng - 1));
    xa[rr] = *(const v4u*)&p.xin[t * DM + 8 * lane]; x9[rr] = p.xin[t * DM + c9];
  }
#pragma unroll
  for (int i = 0; i < 5; ++i) ((v4u*)ys)[tid + 256 * i] = yv[i];
  BAR();
  SP(13 - 3 * ng);
  const unsigned o9 = (lane & 7) == 0 ? 0u : 0xF0000000u;
#pragma unroll 1
  for (int k = 0; k < ng; ++k) {
    float x[2][9], y[2][9], ln[2][9];
    const int t0 = (grp + k) * 8 + w;
#pragma unroll
    for (int rr = 0; rr < 2; ++rr) {
      const int j = (t0 + 4 * rr) & 15;
      const v2u ya = *(const v2u*)&ys[lane * 128 + (j ^ m) * 4], yb = *(const v2u*)&ys[lane * 128 + 64 + (j ^ m) * 4];
      up8(xa[rr], x[rr]); x[rr][8] = lo2f(x9[rr]); up8((v4u){ya.x, ya.y, yb.x, yb.y}, y[rr]); y[rr][8] = bf2f(ys[(c9 >> 2) * 64 + (j ^ xp) * 4 + (m & 3)]);
      xa[rr] = xa[2 + rr]; x9[rr] = x9[2 + rr];
    }
    lnR<2>(x, ln);
#pragma unroll
    for (int rr = 0; rr < 2; ++rr) {
#pragma unroll
      for (int i = 0; i < 9; ++i) x[rr][i] = __builtin_fmaf(ln[rr][i], y[rr][i], x[rr][i]);
      rb9(x[rr]);
      const int r = (t0 + 4 * rr) & 31;
      st16(X, (unsigned)lane * 16u, sb + (P_X3 + (unsigned)(r >> 3)) * PAIR + (unsigned)(r & 7) * 1024u, pk8(x[rr]));
      st16(X, (unsigned)xp * 16u + o9, sb + (P_H + 4u) * PAIR + 4096u + (unsigned)r * 128u, gat8(x[rr][8]));
    }
    lnR<2>(x, ln);
#pragma unroll
    for (int rr = 0; rr < 2; ++rr) {
      const int r = (t0 + 4 * rr) & 31;
      st16(X, hoff(r, lane), sb, pk8(ln[rr]));
      st16(X, hoff(r, 64 + xp) + o9, sb, gat8(ln[rr][8]));
    }
  }
  SP(14 - 3 * ng);
  VM0();
  BAR();
  SP(15 - 3 * ng);
  if (tid < ng) st4(X, (unsigned)tid * 128u, sb + P_FB * PAIR + 4096u + (unsigned)(grp & 3) * 128u, opq(1u));
}

__device__ __forceinline__ void arrive(const MP& p, int k, int grp) {
  VM0();
  BAR();
  if (threadIdx.x < 64) {
    unsigned old = 0u;
    if (threadIdx.x == 0) old = __hip_atomic_fetch_add(p.xb + MF_OFF / 4u + (k * 16 + grp) * 32, 1u, __ATOMIC_RELAXED, __HIP_MEMORY_SCOPE_AGENT);
    if (__builtin_amdgcn_readfirstlane(old) == 15u && threadIdx.x < 8) st4(mkr(p.xb, XB_BYTES), threadIdx.x * 128u, RDY_OFF(k) + (unsigned)grp * 1024u, opq(1u));
  }
}
__device__ __forceinline__ void wait_ready(const MP& p, int k) {
  if (threadIdx.x < 64) wait_all<PSL>(mkr(p.xb, XB_BYTES), (threadIdx.x % 9u) * 1024u, RDY_OFF(k) + (blockIdx.x & 7u) * 128u);
  BAR();
}
__device__ __forceinline__ void start_work(char* smem, int wk) {
  const MP p = getp();
  const int w = __builtin_amdgcn_readfirstlane(threadIdx.x >> 6);
#define NARM (ARMK - 3u)
  unsigned pf[PFM + 1];
  conv_task(p.kvT, p.ssm_k, p.ssm_D, p.yT, (u16*)(smem + w * 32768), 4 * wk + w, [&]() {
#pragma unroll
    for (int i = 0; i < PFM; ++i) {
      const int z = (i - 1) * 36864 + wk * 256 + threadIdx.x, a = z % 3;
      pf[i] = *(const unsigned*)(i == 0 ? p.Wo + (long)(z + 36864) * 64 : (a == 0 ? p.Wq : (a == 1 ? p.Wk : p.Wv)) + (long)(36863 - z / 3) * 64);
    }
  }, [&]() {
    const unsigned sv = opq(SENT);
    constexpr unsigned S16 = SLOT / 16u;
#pragma unroll
    for (unsigned k = 0; k < (NARM * S16 + 32767u) / 32768u; ++k) {
      const unsigned i = k * 32768u + wk * 256 + threadIdx.x;
      st16(mkr(p.xb, XB_BYTES), i < NARM * S16 && wk < 128 ? SLOT_AT(3u + i / S16) + (i % S16) * 16u : 0xF0000000u, 0u, (v4u){sv, sv, sv, sv});
    }
  });
#pragma unroll
  for (int i = 0; i < PFM; ++i) asm volatile("" :: "v"(pf[i]));
  SP(3);
  arrive(p, 0, wk >> 4);
  SP(4);
}
__device__ __forceinline__ void token_start(char* smem) {
  TOK_COMMON();
  (void)w;
  const int k = 4 * i + xq;
  if (L || (i >= 12 && i < 16)) start_work(smem, L ? 32 + k : 80 + k);
  if (i >= (L ? 12 : 20)) {
    const unsigned long long sm = die_mask(X, SG0, 0, lane);
    int g = L ? 111 - k : k - 96;
    while (g < 120) {
      if (g >= 0 && g < 64) {
        SP(5);
        wait_ready(p, 0);
        SP(6);
      }
      rows_task(p, X, sm, (u16*)smem, g < 0 ? g + 16 : 16 + 2 * g, g < 0 ? 1 : 2);
      g += g < 0 ? 16 : (L ? 48 : 120);
    }
  }
}
__device__ __forceinline__ void token_wg(char* smem) {
  if ((unsigned)(size_t)(__attribute__((address_space(3))) char*)smem != 0u) return;
  token_start(smem);
  const int i = ((int)blockIdx.x - NCHW) >> 3;
  if (i < 12) qkv_role();
  else if (i < 20) ap_role();
  else row_role(smem);
}

__global__ __launch_bounds__(256, 1) void k_main(MP) {
  extern __shared__ __attribute__((aligned(16))) char smem[];
  asm volatile("LKBEG:");
  SP(13);
  if ((int)blockIdx.x < NCHW) chain_wg(smem);
  else token_wg(smem);
}

extern "C" void kernel_launch(void* const* d_in, const int* in_sizes, int n_in, void* d_out, int out_size, void* d_ws,
                              size_t ws_size, hipStream_t stream) {
  const u16* A_q = (const u16*)d_in[0];
  const u16* A_k = (const u16*)d_in[1];
  const u16* A_v = (const u16*)d_in[2];
  const u16* A_shift = (const u16*)d_in[3];
  const u16* A_ssmk = (const u16*)d_in[4];
  const u16* A_ssmD = (const u16*)d_in[5];
  const u16* B_mem0 = (const u16*)d_in[7];
  const u16* B_Wq = (const u16*)d_in[8];
  const u16* B_Wk = (const u16*)d_in[9];
  const u16* B_Wv = (const u16*)d_in[10];
  const u16* B_Wo = (const u16*)d_in[11];

  char* ws = (char*)d_ws;
  size_t off = (size_t)(-(uintptr_t)ws) & (XBLK - 1u);
  auto alloc = [&](size_t bytes) { char* q = ws + off; off += (bytes + 4095) & ~(size_t)4095; return q; };
  unsigned* xb = (unsigned*)alloc((size_t)XB_BYTES);
  u16* kvT = (u16*)alloc((size_t)DM * S_LEN * 2);
  u16* yT = (u16*)alloc((size_t)DM * S_LEN * 2);

  PreP pp;
  pp.kk = A_k; pp.vv = A_v; pp.shift_k = A_shift; pp.ssm_k = A_ssmk; pp.ssm_D = A_ssmD; pp.yT = yT; pp.kvT = kvT;
  pp.Wq = B_Wq; pp.Wk = B_Wk; pp.Wv = B_Wv; pp.Wo = B_Wo; pp.xb = xb; pp.mem0 = B_mem0;
  k_pre<<<240 + PRE_FILL + 256 + 288, 256, 0, stream>>>(pp);
  MP mp;
  mp.out = (u16*)d_out; mp.xb = xb;
  mp.xin = A_q; mp.yT = yT; mp.kvT = kvT; mp.ssm_k = A_ssmk; mp.ssm_D = A_ssmD;
  mp.Wq = B_Wq; mp.Wk = B_Wk; mp.Wv = B_Wv; mp.Wo = B_Wo;
  const int smem_bytes = 160 * 1024;
  (void)hipFuncSetAttribute((const void*)k_main, hipFuncAttributeMaxDynamicSharedMemorySize, smem_bytes);
  k_main<<<NCHW + NTOKW, 256, smem_bytes, stream>>>(mp);
}
```

```cpp
#include <hip/hip_runtime.h>
#include <stdint.h>

typedef unsigned short u16;
using bf16x8 = __attribute__((ext_vector_type(8))) short;
using f32x4 = __attribute__((ext_vector_type(4))) float;
using f32x2 = __attribute__((ext_vector_type(2))) float;
using bfx8 = __attribute__((ext_vector_type(8))) __bf16;
using bfx2 = __attribute__((ext_vector_type(2))) __bf16;
typedef unsigned v4u __attribute__((ext_vector_type(4)));
typedef unsigned v2u __attribute__((ext_vector_type(2)));
typedef short v4s __attribute__((ext_vector_type(4)));

#define S_LEN 2048
#define DM 576
#define NH 32
#define DH 128
#define HD (NH * DH)
#define SEG 32
#define NSEG 64

__device__ __forceinline__ float bf2f(u16 x) { return __uint_as_float(((unsigned)x) << 16); }
__device__ __forceinline__ unsigned pk2(float a, float b) { f32x2 v = {a, b}; bfx2 r = __builtin_convertvector(v, bfx2); return __builtin_bit_cast(unsigned, r); }
__device__ __forceinline__ float lo2f(unsigned u) { return __uint_as_float(u << 16); }
__device__ __forceinline__ float hi2f(unsigned u) { return __uint_as_float(u & 0xFFFF0000u); }

#define NFC 128
#define XBLK 2097152u
#define PAIR 16384u
#define SLOT 688128u
#define AGO 589824u
#define AGT 655360u
#define SG0 22
#define TP0 86
#define NXB 95
#define XB_BYTES (NXB * XBLK)
#define SLOT_AT(i) ((unsigned)((i) / 3) * XBLK + (unsigned)((i) % 3) * SLOT)
#define HT_OFF SLOT_AT(65)
#define CTAB_OFF (HT_OFF + 16384u)
#define MF_OFF (HT_OFF + 20480u)
#define TMO_OFF (MF_OFF + 15u * 128u)
#define RDY_OFF(k) (MF_OFF + 4096u + (k) * 10240u)
#define MF_BYTES 16384u
#define TRC_OFF SLOT_AT(64)
#define CAL_OFF (XBLK - 16384u)
#define P_KV 96u
#define P_H 112u
#define P_X3 117u
#define P_FA 121u
#define P_FB 122u
#define ARMK 8
#define NCAL 8
#define CALT 560u
#define CALM 1
#define SRING 12
#define TRING 64
__device__ __forceinline__ unsigned kv2(const unsigned (&u)[4], unsigned v, v2u w0, v2u w1) {
  const float a[4] = {hi2f(w0.y), lo2f(w0.y), hi2f(w0.x), lo2f(w0.x)}, b[4] = {hi2f(w1.y), lo2f(w1.y), hi2f(w1.x), lo2f(w1.x)};
  unsigned r = pk2(lo2f(u[0]) * a[0], hi2f(u[0]) * b[0]);
#pragma unroll
  for (int i = 1; i < 4; ++i) { const unsigned q = pk2(lo2f(u[i]) * a[i], hi2f(u[i]) * b[i]); r = pk2(lo2f(r) + lo2f(q), hi2f(r) + hi2f(q)); }
  return pk2(lo2f(r) * lo2f(v), hi2f(r) * hi2f(v));
}
struct PreP {
  const u16* kk; const u16* vv; const u16* shift_k; const u16* ssm_k; const u16* ssm_D; u16* yT; u16* kvT;
  const u16* Wq; const u16* Wk; const u16* Wv; const u16* Wo; unsigned* xb; const u16* mem0;
};
#define PRE_FILL 112
#define PFN 2
__global__ __launch_bounds__(256, 4) void k_pre(PreP p) {
  __shared__ __attribute__((aligned(16))) float sh[64 * 36];
  const int tid = threadIdx.x, b0 = blockIdx.x, b = b0 < 240 ? b0 : (b0 < 528 ? b0 + PRE_FILL + 256 : b0 - 288);
  const __amdgpu_buffer_rsrc_t rx = __builtin_amdgcn_make_buffer_rsrc((void*)p.xb, 0, XB_BYTES, 0x00020000);
  const v4u ones = {0xFFFFFFFFu, 0xFFFFFFFFu, 0xFFFFFFFFu, 0xFFFFFFFFu}, zero = {0, 0, 0, 0};
  if (b < 96) {
    if (tid >= 64) return;
    const unsigned cb = (unsigned)b * XBLK + CAL_OFF;
    __builtin_amdgcn_raw_buffer_store_b128(zero, rx, (unsigned)((tid >> 3) & 1) * 8192u + (unsigned)(tid >> 4) * 256u + (unsigned)(tid & 7) * 16u, cb, 16);
    asm volatile("s_waitcnt vmcnt(0)" ::: "memory");
    unsigned mn[2] = {~0u, ~0u};
    for (int i = 0; i < NCAL; ++i) {
      const unsigned long long c0 = __builtin_amdgcn_s_memtime();
      asm volatile("s_waitcnt lgkmcnt(0)" ::: "memory");
      const unsigned v = __builtin_amdgcn_raw_buffer_load_b32(rx, 0, cb + (unsigned)(i & 1) * 8192u + (unsigned)(i >> 1) * 256u, 16);
      asm volatile("s_waitcnt vmcnt(0)" :: "v"(v) : "memory");
      const unsigned d = (unsigned)(__builtin_amdgcn_s_memtime() - c0);
      if (i & 1) mn[1] = d < mn[1] ? d : mn[1]; else mn[0] = d < mn[0] ? d : mn[0];
      const unsigned lo = mn[0] < mn[1] ? mn[0] : mn[1], hi = mn[0] < mn[1] ? mn[1] : mn[0];
      if (i >= CALM && lo < CALT && hi > lo + 100u) break;
    }
    unsigned xcc; asm volatile("s_getreg_b32 %0, hwreg(HW_REG_XCC_ID)" : "=s"(xcc));
    const unsigned res = mn[1] < mn[0] ? 1u : 0u;
    if (tid == 0) __builtin_amdgcn_raw_buffer_store_b32(res ^ ((xcc >> 2) & 1u), rx, 0, CTAB_OFF + (unsigned)b * 4u, 16);
  } else if (b < 240) {
    const __amdgpu_buffer_rsrc_t rk = __builtin_amdgcn_make_buffer_rsrc((void*)p.kk, 0, S_LEN * DM * 2, 0x00020000);
    float (*kx)[132] = (float (*)[132])sh;
    float (*cf)[140] = (float (*)[140])(sh + 4 * 132);
    const int d0 = (b - 96) * 4, q = tid & 3, dd = (tid >> 2) & 3, t0 = (tid >> 4) * 8;
    const float Dv = bf2f(p.ssm_D[d0 + dd]);
    {
      const int s = tid >> 1, d = d0 + (tid & 1) * 2;
      unsigned u[4];
#pragma unroll
      for (int a = 0; a < 4; ++a) u[a] = __builtin_amdgcn_raw_buffer_load_b32(rk, (unsigned)((s - 3 + a) * DM + d) * 2u, 0, 0);
      const unsigned vr = *(const unsigned*)&p.vv[s * DM + d], c0 = *(const unsigned*)&p.ssm_k[(long)(d0 + (tid >> 6)) * S_LEN + (tid & 63) * 2];
      const v4u w = *(const v4u*)&p.shift_k[d * 4];
      const unsigned r = kv2(u, vr, (v2u){w.x, w.y}, (v2u){w.z, w.w});
      kx[(tid & 1) * 2][s] = lo2f(r); kx[(tid & 1) * 2 + 1][s] = hi2f(r);
      *(f32x2*)&cf[tid >> 6][8 + (tid & 63) * 2] = (f32x2){lo2f(c0), hi2f(c0)};
      if (tid < 32) cf[tid >> 3][tid & 7] = 0.f;
    }
    __syncthreads();
    float y[8];
#pragma unroll
    for (int i = 0; i < 8; ++i) y[i] = 0.f;
#pragma unroll 1
    for (int j = 8 * q; j <= t0; j += 32) {
      f32x4 kk[2], cw[4];
#pragma unroll
      for (int x = 0; x < 2; ++x) kk[x] = *(const f32x4*)&kx[dd][j + 4 * x];
#pragma unroll
      for (int x = 0; x < 4; ++x) cw[x] = *(const f32x4*)&cf[dd][t0 - j + 4 * x];
#pragma unroll
      for (int jj = 0; jj < 8; ++jj)
#pragma unroll
        for (int i = 0; i < 8; ++i) y[i] = __builtin_fmaf(cw[(8 + i - jj) >> 2][(8 + i - jj) & 3], kk[jj >> 2][jj & 3], y[i]);
    }
#pragma unroll
    for (int i = 0; i < 8; ++i) {
      y[i] += __uint_as_float((unsigned)__builtin_amdgcn_mov_dpp((int)__float_as_uint(y[i]), 0xB1, 0xf, 0xf, true));
      y[i] += __uint_as_float((unsigned)__builtin_amdgcn_mov_dpp((int)__float_as_uint(y[i]), 0x4E, 0xf, 0xf, true));
    }
    const float ya = q == 0 ? y[0] : (q == 1 ? y[2] : (q == 2 ? y[4] : y[6])), yb = q == 0 ? y[1] : (q == 1 ? y[3] : (q == 2 ? y[5] : y[7]));
    const int t = t0 + 2 * q, f = (d0 >> 3) & 15;
    const unsigned r0 = pk2(ya, yb), r1 = pk2(kx[dd][t] * Dv, kx[dd][t + 1] * Dv), o = pk2(lo2f(r0) + lo2f(r1), hi2f(r0) + hi2f(r1));
    u16* dst = p.yT + (t >> 4) * 9216 + (d0 >> 2) * 64 + dd;
    dst[((t & 15) ^ f) << 2] = (u16)(o & 0xFFFFu); dst[(((t + 1) & 15) ^ f) << 2] = (u16)(o >> 16);
  } else if (b < 240 + PRE_FILL) {
    const unsigned i0 = (unsigned)(b - 240) * 256u + tid;
    if (i0 < (SLOT - AGO) / 16u) {
      const unsigned o = AGO + i0 * 16u, s = o < AGT ? (i0 >> 11) * 4u + ((o >> 10) & 3u) : 8u, r = o & 1023u;
      const unsigned gm = s < 8u ? r >> 8 : (r >> 6) & 3u, m3 = (r >> 4) & 3u, ga = s < 8u ? (r >> 6) & 3u : r >> 8, row = 4u * (gm & 1u) + 2u * (gm >> 1) + (m3 & 1u), half = m3 >> 1;
      const unsigned* src = (const unsigned*)(p.mem0 + row * DM + 64u * s + 16u * ga + 2u * half);
      __builtin_amdgcn_raw_buffer_store_b128((v4u){src[0], src[2], src[4], src[6]}, rx, o, 0, 16);
    }
    for (unsigned i = i0; i < 131072u + MF_BYTES / 16u; i += PRE_FILL * 256)
      __builtin_amdgcn_raw_buffer_store_b128(zero, rx, i < 131072u ? (SG0 + (i >> 11)) * XBLK + P_FA * PAIR + (i & 2047u) * 16u : MF_OFF + (i & 2047u) * 16u, 0, 16);
    for (unsigned i = i0; i < 2u * SLOT / 16u; i += PRE_FILL * 256) __builtin_amdgcn_raw_buffer_store_b128(ones, rx, SLOT + i * 16u, 0, 16);
  } else if (b >= 240 + PRE_FILL + 256) {
    const __amdgpu_buffer_rsrc_t rk = __builtin_amdgcn_make_buffer_rsrc((void*)p.kk, 0, S_LEN * DM * 2, 0x00020000);
    u16 (*tile)[72] = (u16 (*)[72])sh;
    const int bid = b - 240 - PRE_FILL - 256, d0 = (bid % 9) * 64, s0 = (bid / 9) * 64;
    const int sr = (tid >> 3) * 2, d = d0 + (tid & 7) * 8;
    v4u kr[5], vr[2], w[4];
#pragma unroll
    for (int a = 0; a < 5; ++a) kr[a] = __builtin_amdgcn_raw_buffer_load_b128(rk, (unsigned)((s0 + sr - 3 + a) * DM + d) * 2u, 0, 0);
#pragma unroll
    for (int a = 0; a < 2; ++a) vr[a] = *(const v4u*)&p.vv[(long)(s0 + sr + a) * DM + d];
#pragma unroll
    for (int a = 0; a < 4; ++a) w[a] = *(const v4u*)&p.shift_k[(d + 2 * a) * 4];
    __builtin_amdgcn_sched_barrier(0);
#pragma unroll
    for (int it = 0; it < 2; ++it) {
      v4u o;
#pragma unroll
      for (int j = 0; j < 4; ++j) { const unsigned u[4] = {kr[it][j], kr[it + 1][j], kr[it + 2][j], kr[it + 3][j]}; o[j] = kv2(u, vr[it][j], (v2u){w[j].x, w[j].y}, (v2u){w[j].z, w[j].w}); }
      *(v4u*)&tile[sr + it][(tid & 7) * 8] = o;
    }
    __syncthreads();
    const int lane = tid & 63, wv = tid >> 6, g = lane >> 4, r = (lane >> 2) & 3, c = lane & 3;
    v2u o[4];
#pragma unroll
    for (int i = 0; i < 4; ++i) o[i] = __builtin_bit_cast(v2u, __builtin_amdgcn_ds_read_tr16_b64_v4i16((__attribute__((address_space(3))) v4s*)&tile[16 * g + 4 * i + r][16 * wv + 4 * c]));
    u16* dst = p.kvT + (long)(d0 + 16 * wv + (lane & 15)) * S_LEN + s0 + 16 * g;
    *(v4u*)dst = (v4u){o[0].x, o[0].y, o[1].x, o[1].y};
    *(v4u*)(dst + 8) = (v4u){o[2].x, o[2].y, o[3].x, o[3].y};
  } else {
    const int x = b & 7, k = (b - 240 - PRE_FILL) >> 3;
    unsigned long long pc; unsigned l0, l1, l2, l3; int rel;
    asm volatile("s_getpc_b64 %0\nLPRE:\n\ts_mov_b32 %1, LKBEG-LPRE\n\ts_mov_b32 %2, LCHEND-LKBEG\n\ts_mov_b32 %3, LQVEND-LKBEG\n\ts_mov_b32 %4, LQKEND-LKBEG\n\ts_mov_b32 %5, LAPEND-LKBEG"
                 : "=s"(pc), "=s"(rel), "=s"(l0), "=s"(l1), "=s"(l2), "=s"(l3));
    l0 = l0 > l1 ? l0 : l1; l2 = l2 > l3 ? l2 : l3; l0 = l0 > l2 ? l0 : l2;
    const unsigned o = (unsigned)(k * 64 + (tid & 63)) * 64u;
    unsigned a[PFN + 2];
    a[PFN] = *(const __attribute__((address_space(1))) unsigned*)(pc + rel + (o < l0 ? o : 0u));
    a[PFN + 1] = __builtin_amdgcn_raw_buffer_load_b32(rx, k == 31 ? tid * XBLK + CAL_OFF + 8064u : 0xF0000000u, 0, 16);
#pragma unroll
    for (int i = 0; i < PFN; ++i) {
      const int q = (i * 32 + k) * 256 + tid, r2 = q >> 5, gr = r2 >> 5, row = (gr / 3) * 64 + (x >> 2) * 32 + (r2 & 31), off = (q & 31) * 32, ty = gr % 3;
      a[i] = *(const unsigned*)(gr < 27 ? (ty == 0 ? p.Wq : (ty == 1 ? p.Wk : p.Wv)) + (long)row * HD + (x & 3) * 1024 + off
                                        : p.Wo + ((long)(x & 3) * DM + (gr < 36 ? gr - 27 : 0) * 64 + (x >> 2) * 32 + (r2 & 31)) * 1024 + off);
    }
#pragma unroll
    for (int i = 0; i < PFN + 2; ++i) asm volatile("" :: "v"(a[i]));
  }
}

#define CREV_STRIDE 2208
#define LWAIT() asm volatile("s_waitcnt lgkmcnt(0)" ::: "memory")
template <class E, class F>
__device__ __forceinline__ void conv_task(const u16* __restrict__ kvT, const u16* __restrict__ ssm_k, const u16* __restrict__ ssm_D, u16* yT, u16* shb, int d, E early, F fill) {
  const int lane = threadIdx.x & 63;
  u16* crev = shb + 64;
  u16* kvs = crev + 4 * CREV_STRIDE;
  const int X0 = 2048 + 63;
  v4u cv[4], kq[4];
#pragma unroll
  for (int j = 0; j < 4; ++j) { cv[j] = *(const v4u*)&ssm_k[(long)d * S_LEN + lane * 32 + 8 * j]; kq[j] = *(const v4u*)&kvT[(long)d * S_LEN + (j * 64 + lane) * 8]; }
  const float Dv = bf2f(ssm_D[d]);
  __builtin_amdgcn_sched_barrier(0);
  early();
  __builtin_amdgcn_sched_barrier(0);
  const v4u z4 = {0u, 0u, 0u, 0u};
  *(v4u*)&kvs[(lane >> 2) * 40 + (lane & 3) * 8] = z4;
  if (lane < 32) *(v4u*)&crev[(lane >> 3) * CREV_STRIDE + (lane & 7) * 8] = z4;
  if (lane < 36) *(v4u*)&crev[(lane / 9) * CREV_STRIDE + 2112 + (lane % 9) * 8] = z4;
  {
    unsigned r[18];
#pragma unroll
    for (int k = 0; k < 16; ++k) { const unsigned u = cv[3 - (k >> 2)][3 - (k & 3)]; r[k] = __builtin_amdgcn_alignbyte(u, u, 2); }
#pragma unroll
    for (int k = 0; k < 2; ++k) r[16 + k] = (unsigned)__builtin_amdgcn_update_dpp(0, (int)r[k], 0x138, 0xf, 0xf, true);
    LWAIT();
#pragma unroll
    for (int rho = 0; rho < 4; ++rho)
#pragma unroll
      for (int j = 0; j < 4; ++j) {
        v4u o;
#pragma unroll
        for (int k = 0; k < 4; ++k) { const int q = 4 * j + k + (rho >> 1); o[k] = (rho & 1) ? __builtin_amdgcn_alignbyte(r[q + 1], r[q], 2) : r[q]; }
        *(v4u*)&crev[rho * CREV_STRIDE + 2080 - 32 * lane + 8 * j] = o;
      }
    if (lane == 63) {
      *(v2u*)&crev[1 * CREV_STRIDE + 60] = (v2u){0u, r[0] << 16};
      *(v2u*)&crev[2 * CREV_STRIDE + 60] = (v2u){0u, r[0]};
      *(v2u*)&crev[3 * CREV_STRIDE + 60] = (v2u){r[0] << 16, __builtin_amdgcn_alignbyte(r[1], r[0], 2)};
    }
#pragma unroll
    for (int j = 0; j < 4; ++j) *(v4u*)&kvs[(16 + j * 16 + (lane >> 2)) * 40 + (lane & 3) * 8] = kq[j];
  }
  fill();
  LWAIT();
  const int n = lane & 15, g = lane >> 4;
  const int rho = (X0 - n) & 3;
  f32x4 acc[4][2];
#pragma unroll
  for (int q = 0; q < 8; ++q) acc[q >> 1][q & 1] = (f32x4){0.f, 0.f, 0.f, 0.f};
  {
    typedef __attribute__((address_space(3))) const v2u* l2p;
    typedef __attribute__((address_space(3))) const bf16x8* l4p;
    unsigned a0 = (unsigned)(size_t)(__attribute__((address_space(3))) u16*)&crev[rho * CREV_STRIDE + (X0 - (n - 8 * g) - rho)] - 3232u, a1 = a0 + 8u;
    unsigned b0 = (unsigned)(size_t)(__attribute__((address_space(3))) u16*)&kvs[(14 + n) * 40 + 8 * g];
    asm volatile("" : "+v"(a1));
#define AFR(m, f) ({ const unsigned o_ = 3232u - 64u * (m) - 1024u * ((f) >> 1) - 32u * ((f) & 1); const v2u lo_ = *(l2p)(size_t)(a0 + o_), hi_ = *(l2p)(size_t)(a1 + o_); \
                     __builtin_bit_cast(bf16x8, (v4u){lo_.x, lo_.y, hi_.x, hi_.y}); })
#define BFR(m, j) (*(l4p)(size_t)(b0 + 80u * (2 - (m)) + 1280u * (j)))
    bf16x8 af[2][8], bf[2][4];
#pragma unroll
    for (int f = 0; f < 8; ++f) { af[0][f] = AFR(0, f); af[1][f] = af[0][f]; __builtin_amdgcn_sched_barrier(0); }
#pragma unroll
    for (int j = 0; j < 4; ++j) { bf[0][j] = BFR(0, j); bf[1][j] = bf[0][j]; }
#pragma unroll 1
    for (int mo = 0; mo < 8; ++mo) {
#pragma unroll
      for (int m = 0; m < 2; ++m) {
        int k = 0;
#pragma unroll
        for (int a = 0; a < 4; ++a)
#pragma unroll
          for (int ct = a; ct < 4; ++ct)
#pragma unroll
            for (int ti = 0; ti < 2; ++ti) {
              acc[ct][ti] = __builtin_amdgcn_mfma_f32_16x16x32_bf16(__builtin_bit_cast(bfx8, af[m][2 * a + ti]), __builtin_bit_cast(bfx8, bf[m][ct - a]), acc[ct][ti], 0, 0, 0);
              if (k < 2) af[m ^ 1][k] = AFR(m + 1, k);
              else if (k < 6) bf[m ^ 1][k - 2] = BFR(m + 1, k - 2);
              else if (k < 12) af[m ^ 1][k - 4] = AFR(m + 1, k - 4);
              __builtin_amdgcn_sched_barrier(0);
              ++k;
            }
      }
      a0 -= 128u; a1 -= 128u; b0 -= 160u;
    }
  }
  u16* yo = crev;
  LWAIT();
#pragma unroll
  for (int q = 0; q < 8; ++q) {
    const int o = (16 * (q >> 1) + n) * 40 + 16 * (q & 1) + 4 * g;
    const v2u kx = *(const v2u*)&kvs[16 * 40 + o];
    const f32x4 y = acc[q >> 1][q & 1];
    const unsigned y0 = pk2(y[0], y[1]), y1 = pk2(y[2], y[3]), r0 = pk2(lo2f(kx.x) * Dv, hi2f(kx.x) * Dv), r1 = pk2(lo2f(kx.y) * Dv, hi2f(kx.y) * Dv);
    *(v2u*)&yo[o] = (v2u){pk2(lo2f(y0) + lo2f(r0), hi2f(y0) + hi2f(r0)), pk2(lo2f(y1) + lo2f(r1), hi2f(y1) + hi2f(r1))};
  }
  LWAIT();
  __builtin_amdgcn_s_barrier();
  asm volatile("" ::: "memory");
  {
    const int tid = threadIdx.x, f = (d >> 3) & 15;
    const u16* y0 = shb - (tid >> 6) * 16384 + 64;
#pragma unroll
    for (int k = 0; k < 4; ++k) {
      const int tile = 32 * k + (tid >> 3), t = 16 * tile + 2 * ((tid & 7) ^ (f >> 1));
      unsigned u[4];
#pragma unroll
      for (int c = 0; c < 4; ++c) u[c] = *(const unsigned*)&y0[c * 16384 + (t >> 5) * 40 + (t & 31)];
      const v2u e = {__builtin_amdgcn_perm(u[1], u[0], 0x05040100u), __builtin_amdgcn_perm(u[3], u[2], 0x05040100u)}, o = {__builtin_amdgcn_perm(u[1], u[0], 0x07060302u), __builtin_amdgcn_perm(u[3], u[2], 0x07060302u)};
      if (tile >= NFC / 16) __builtin_amdgcn_raw_buffer_store_b128((f & 1) ? (v4u){o.x, o.y, e.x, e.y} : (v4u){e.x, e.y, o.x, o.y}, __builtin_amdgcn_make_buffer_rsrc((void*)yT, 0, DM * S_LEN * 2, 0x00020000),
                                                                   tile * 18432 + (tid & 7) * 16, (d >> 2) * 128, 16);
    }
  }
}

#define NCHW 64
#define NTOKW 192
#define PSL 4
#define SENT 0xFFFFFFFFu
#define SPIN_CAP 300000u
struct MP {
  const u16* Wq; const u16* Wk; const u16* Wv; const u16* Wo; u16* out; unsigned* xb;
  const u16* xin; u16* yT; u16* kvT; const u16* ssm_k; const u16* ssm_D;
};

__device__ __forceinline__ MP getp() {
  MP r;
#if defined(__HIP_DEVICE_COMPILE__)
  const __attribute__((address_space(4))) unsigned long long* k = (const __attribute__((address_space(4))) unsigned long long*)__builtin_amdgcn_kernarg_segment_ptr();
  asm volatile("" : "+s"(k));
#define GP(T, i) ((T*)(__attribute__((address_space(1))) T*)k[i])
  r.Wq = GP(const u16, 0);
  r.Wk = GP(const u16, 1);
  r.Wv = GP(const u16, 2);
  r.Wo = GP(const u16, 3);
  r.out = GP(u16, 4);
  r.xb = GP(unsigned, 5);
  r.xin = GP(const u16, 6);
  r.yT = GP(u16, 7);
  r.kvT = GP(u16, 8);
  r.ssm_k = GP(const u16, 9);
  r.ssm_D = GP(const u16, 10);
#endif
  return r;
}
typedef __amdgpu_buffer_rsrc_t rsrc_t;
__device__ __forceinline__ rsrc_t mkr(const void* p, unsigned bytes) { return __builtin_amdgcn_make_buffer_rsrc((void*)p, 0, bytes, 0x00020000); }
__device__ __forceinline__ void st16(rsrc_t r, unsigned vo, unsigned so, v4u v) { __builtin_amdgcn_raw_buffer_store_b128(v, r, vo, so, 16); }
__device__ __forceinline__ void st4(rsrc_t r, unsigned vo, unsigned so, unsigned v) { __builtin_amdgcn_raw_buffer_store_b32(v, r, vo, so, 16); }
__device__ __forceinline__ v4u ld16(rsrc_t r, unsigned vo, unsigned so) { return __builtin_amdgcn_raw_buffer_load_b128(r, vo, so, 16); }
__device__ __forceinline__ v2u ld8(rsrc_t r, unsigned vo, unsigned so) { return __builtin_amdgcn_raw_buffer_load_b64(r, vo, so, 16); }
__device__ __forceinline__ unsigned ld4(rsrc_t r, unsigned vo, unsigned so) { return __builtin_amdgcn_raw_buffer_load_b32(r, vo, so, 16); }
__device__ __forceinline__ unsigned opq(unsigned x) { asm volatile("" : "+v"(x)); return x; }

__device__ __forceinline__ unsigned long long die_mask(rsrc_t X, unsigned first, int L, int lane) { return __ballot((ld4(X, (unsigned)lane * 4u, CTAB_OFF + first * 4u) & 1u) != (unsigned)L); }
#define SEGB(m, s) ((unsigned)(SG0 + (s)) * XBLK + (unsigned)(((m) >> (s)) & 1ull) * 8192u)
__device__ __forceinline__ bool spin_fail(unsigned& spins, rsrc_t F) {
  ++spins;
  if (spins > SPIN_CAP) { unsigned one = 0x40000000u | (blockIdx.x << 8) | threadIdx.x; asm volatile("" : "+v"(one)); if (__builtin_amdgcn_readfirstlane(ld4(F, 0, TMO_OFF)) == 0u) st4(F, 0, TMO_OFF, one); return true; }
  if ((spins & 1023u) == 0u) return __builtin_amdgcn_readfirstlane(ld4(F, 0, TMO_OFF)) != 0;
  return false;
}
__device__ __forceinline__ unsigned rtc() { return (unsigned)__builtin_amdgcn_s_memrealtime(); }
#define BAR() do { asm volatile("s_waitcnt lgkmcnt(0)" ::: "memory"); __builtin_amdgcn_s_barrier(); asm volatile("" ::: "memory"); } while (0)
#define SB() __builtin_amdgcn_sched_barrier(0)
#define MF "v_mfma_f32_16x16x32_bf16 "
#define VM0() __builtin_amdgcn_s_waitcnt(0x0F70)
#define DL1 12
#define SCH_B0 48
#define DLL1 6
#define DLL2 0
#define DLF 24
#define SCH_PEN 0
#define DLX 4
#define LATE 2
#define TKL 28
#define FIN -3
#define CH_FIN CH_S(FIN)
#define DLT 16
#define DLY 20
#define CH_DLT CH_S(DLT)
#define CH_DLY CH_S(DLY)
#define ES 6
#define ED 4
#define EB 8
#define CH_ES CH_S(ES)
#define CH_EX "%[ex]"
#define DRYL 1
#define CH_DRYL CH_S(DRYL)
#define SMAP 0x1023u
#define RO1 4
#define CH_RO1 CH_S(RO1)
#define RO3 2
#define CH_RO3 CH_S(RO3)
#define KD 300
#define KB 150
#define CH_KD CH_S(KD)
#define CH_KB CH_S(KB)
#define CH_AL(n) ".p2align 5\n\t.fill " CH_S(n) ", 4, 0xbf800000\n\t"
#define ALW 6
#define ALD 5
#define CH_ALW CH_AL(ALW)
#define CH_ALD CH_AL(ALD)
#define RSL 2
#define CH_S2(x) #x
#define CH_S(x) CH_S2(x)
#define CH_TOKCHK "s_cbranch_vccz LG3_%=\n\t"
#define CH_SG0 CH_S(SG0)
#define CH_ARMD "%[armd]"
#define CH_ARMS "%[arms]"
#define CH_SLOT "688128"
#define CH_SKIP "32768"
#define CH_DLL1 CH_S(DLL1)
#define CH_DLL2 CH_S(DLL2)
#define CH_DLF CH_S(DLF)
#define CH_B0 CH_S(SCH_B0)
#define CH_PEN CH_S(SCH_PEN)
#define CH_RSL CH_S(RSL)
#define CH_DLX CH_S(DLX)
#define CH_LATE CH_S(LATE)
#define CH_DL1L "%[dl1l]"
#define CH_TKL CH_S(TKL)
#define CH_SCHW "656384"
#define CH_CAP "%[cap]"
#define CH_CTAB "%[ctab]"
#define CH_RING "%[ring]"
#define CH_TRM "%[trm]"
#define FDL 6
#define CH_FDL CH_S(FDL)
#define CH_ASM \
  "s_mov_b32 s84, " CH_CAP "\n\t" \
  "s_mov_b32 s83, " CH_CAP "\n\t" \
  "s_mov_b32 s68, 0\n\t" \
  "s_mov_b32 s74, 0\n\t" \
  "s_mov_b32 s76, 0\n\t" \
  "s_mov_b32 s77, 0\n\t" \
  "s_mov_b32 s78, 0\n\t" \
  "s_mov_b32 s79, 0\n\t" \
  "s_mov_b32 s56, 0xcccccccc\n\t" \
  "s_mov_b32 s57, 0xcccccccc\n\t" \
  "s_mov_b32 s58, 0x33333333\n\t" \
  "s_mov_b32 s59, 0x33333333\n\t" \
  "s_mov_b32 s24, 0x01010101\n\t" \
  "s_mov_b32 s25, 0\n\t" \
  "s_mov_b32 s88, 0x3f803f80\n\t" \
  "s_mov_b32 s89, 0x3f803f80\n\t" \
  "s_mov_b32 s66, s65\n\t" \
  "s_mov_b32 s47, s65\n\t" \
  "s_mov_b32 s72, 0\n\t" \
  "s_mov_b32 s73, 0\n\t" \
  "s_mov_b32 s67, 0\n\t" \
  "s_mul_i32 s60, s40, 36864\n\t" \
  "s_add_u32 s60, s60, 16384\n\t" \
  "v_add_u32 v70, s60, v69\n\t" \
  "v_bfe_i32 v68, v69, 5, 1\n\t" \
  "v_and_b32 v68, 0xaaaaaaaa, v68\n\t" \
  "v_or_b32 v68, 0x44444444, v68\n\t" \
  "v_and_b32 v73, 32, v69\n\t" \
  "v_lshl_add_u32 v73, v73, 5, v69\n\t" \
  "LLOAD_%=:\n\t" \
  "v_lshrrev_b32 v74, 2, v69\n\t" \
  "s_mov_b32 s60, " CH_CTAB "\n\t" \
  "buffer_load_dword v72, v74, s[28:31], s60 offen offset:88 sc1\n\t" \
  "buffer_load_dword v74, v74, s[28:31], s60 offen sc1\n\t" \
  "v_lshrrev_b32 v71, 8, v69\n\t" \
  "v_lshlrev_b32 v1, 6, v71\n\t" \
  "v_and_b32 v2, 0xf0, v69\n\t" \
  "v_xor_b32 v1, v1, v2\n\t" \
  "v_lshl_add_u32 v71, v71, 13, v1\n\t" \
  "s_lshl_b32 s60, s40, 17\n\t" \
  "v_add_u32 v71, s60, v71\n\t" \
  "v_lshrrev_b32 v1, 7, v69\n\t" \
  "v_and_b32 v2, 0x70, v69\n\t" \
  "s_movk_i32 s60, 1152\n\t" \
  "v_mad_u32_u24 v75, v1, s60, v2\n\t" \
  "s_lshl_b32 s54, s40, 12\n\t" \
  "s_lshl_b32 s55, s40, 7\n\t" \
  "s_mul_i32 s75, s40, 36864\n\t" \
  "s_add_u32 s75, s75, 1024\n\t" \
  "s_mov_b32 s62, 0x05040100\n\t" \
  "s_mov_b32 s63, 0x07060302\n\t" \
  "s_barrier\n\t" \
  "s_xor_b32 s61, s40, 0\n\t" \
  "s_mul_i32 s61, s61, 36864\n\t" \
  "s_add_u32 s61, s61, s55\n\t" \
  "s_add_u32 m0, s54, 0\n\t" \
  "s_add_u32 s60, s61, 0\n\t" \
  "buffer_load_dwordx4 v75, %[Wo], s60 offen lds\n\t" \
  "s_add_u32 m0, s54, 1024\n\t" \
  "s_add_u32 s60, s61, 9216\n\t" \
  "buffer_load_dwordx4 v75, %[Wo], s60 offen lds\n\t" \
  "s_add_u32 m0, s54, 2048\n\t" \
  "s_add_u32 s60, s61, 18432\n\t" \
  "buffer_load_dwordx4 v75, %[Wo], s60 offen lds\n\t" \
  "s_add_u32 m0, s54, 3072\n\t" \
  "s_add_u32 s60, s61, 27648\n\t" \
  "buffer_load_dwordx4 v75, %[Wo], s60 offen lds\n\t" \
  "s_add_u32 m0, s54, 16384\n\t" \
  "s_mov_b32 s60, 0\n\t" \
  "buffer_load_dwordx4 v71, %[Wq], s60 offen lds\n\t" \
  "s_add_u32 m0, s54, 17408\n\t" \
  "s_mov_b32 s60, 32768\n\t" \
  "buffer_load_dwordx4 v71, %[Wq], s60 offen lds\n\t" \
  "s_add_u32 m0, s54, 18432\n\t" \
  "s_mov_b32 s60, 65536\n\t" \
  "buffer_load_dwordx4 v71, %[Wq], s60 offen lds\n\t" \
  "s_add_u32 m0, s54, 19456\n\t" \
  "s_mov_b32 s60, 98304\n\t" \
  "buffer_load_dwordx4 v71, %[Wq], s60 offen lds\n\t" \
  "s_add_u32 m0, s54, 53248\n\t" \
  "s_mov_b32 s60, 0\n\t" \
  "buffer_load_dwordx4 v71, %[Wk], s60 offen lds\n\t" \
  "s_add_u32 m0, s54, 54272\n\t" \
  "s_mov_b32 s60, 32768\n\t" \
  "buffer_load_dwordx4 v71, %[Wk], s60 offen lds\n\t" \
  "s_add_u32 m0, s54, 55296\n\t" \
  "s_mov_b32 s60, 65536\n\t" \
  "buffer_load_dwordx4 v71, %[Wk], s60 offen lds\n\t" \
  "s_add_u32 m0, s54, 56320\n\t" \
  "s_mov_b32 s60, 98304\n\t" \
  "buffer_load_dwordx4 v71, %[Wk], s60 offen lds\n\t" \
  "s_xor_b32 s61, s40, 0\n\t" \
  "s_mul_i32 s61, s61, 36864\n\t" \
  "s_add_u32 s61, s61, s55\n\t" \
  "s_add_u32 m0, s54, 90112\n\t" \
  "s_add_u32 s60, s61, 512\n\t" \
  "buffer_load_dwordx4 v75, %[Wo], s60 offen lds\n\t" \
  "s_add_u32 m0, s54, 91136\n\t" \
  "s_add_u32 s60, s61, 9728\n\t" \
  "buffer_load_dwordx4 v75, %[Wo], s60 offen lds\n\t" \
  "s_add_u32 m0, s54, 92160\n\t" \
  "s_add_u32 s60, s61, 18944\n\t" \
  "buffer_load_dwordx4 v75, %[Wo], s60 offen lds\n\t" \
  "s_add_u32 m0, s54, 93184\n\t" \
  "s_add_u32 s60, s61, 28160\n\t" \
  "buffer_load_dwordx4 v75, %[Wo], s60 offen lds\n\t" \
  "s_add_u32 m0, s54, 126976\n\t" \
  "s_mov_b32 s60, 0\n\t" \
  "buffer_load_dwordx4 v71, %[Wv], s60 offen lds\n\t" \
  "s_add_u32 m0, s54, 128000\n\t" \
  "s_mov_b32 s60, 32768\n\t" \
  "buffer_load_dwordx4 v71, %[Wv], s60 offen lds\n\t" \
  "s_add_u32 m0, s54, 129024\n\t" \
  "s_mov_b32 s60, 65536\n\t" \
  "buffer_load_dwordx4 v71, %[Wv], s60 offen lds\n\t" \
  "s_add_u32 m0, s54, 130048\n\t" \
  "s_mov_b32 s60, 98304\n\t" \
  "buffer_load_dwordx4 v71, %[Wv], s60 offen lds\n\t" \
  "v_and_b32 v1, 0x300, v69\n\t" \
  "v_lshlrev_b32 v65, 3, v1\n\t" \
  "v_and_b32 v1, 0xc0, v69\n\t" \
  "v_lshl_add_u32 v65, v1, 2, v65\n\t" \
  "s_lshl_b32 s60, s40, 6\n\t" \
  "v_xor_b32 v1, s60, v1\n\t" \
  "v_and_b32 v2, 48, v69\n\t" \
  "v_add3_u32 v65, v65, v1, v2\n\t" \
  "v_lshrrev_b32 v2, 1, v2\n\t" \
  "v_sub_u32 v66, v65, v2\n\t" \
  "v_bfe_u32 v0, v69, 8, 1\n\t" \
  "v_bfe_u32 v1, v69, 9, 1\n\t" \
  "v_lshlrev_b32 v0, 2, v0\n\t" \
  "v_add_u32 v0, v0, v1\n\t" \
  "v_lshlrev_b32 v0, 9, v0\n\t" \
  "v_lshrrev_b32 v1, 1, v69\n\t" \
  "v_and_b32 v1, 0x60, v1\n\t" \
  "v_and_b32 v2, 16, v69\n\t" \
  "v_bfe_u32 v3, v69, 5, 1\n\t" \
  "v_add3_u32 v64, v0, v1, v2\n\t" \
  "v_lshl_add_u32 v64, v3, 2, v64\n\t" \
  "v_add_u32 v64, s54, v64\n\t" \
  "v_lshl_add_u32 v67, v3, 3, v0\n\t" \
  "v_bfe_u32 v1, v69, 7, 1\n\t" \
  "v_lshl_add_u32 v67, v1, 2, v67\n\t" \
  "v_bfe_u32 v1, v69, 6, 1\n\t" \
  "v_lshl_add_u32 v67, v1, 4, v67\n\t" \
  "v_bfe_u32 v1, v69, 4, 1\n\t" \
  "v_lshl_add_u32 v67, v1, 1, v67\n\t" \
  "s_lshl_b32 s60, s40, 5\n\t" \
  "v_add_u32 v67, s60, v67\n\t" \
  "v_add_u32 v78, 0, v64\n\t" \
  "v_add_u32 v79, 1024, v64\n\t" \
  "s_waitcnt vmcnt(16) lgkmcnt(0)\n\t" \
  "s_barrier\n\t" \
  "ds_read2_b32 v[80:81], v78 offset0:0 offset1:2\n\t" \
  "ds_read2_b32 v[82:83], v78 offset0:32 offset1:34\n\t" \
  "ds_read2_b32 v[84:85], v78 offset0:64 offset1:66\n\t" \
  "ds_read2_b32 v[86:87], v78 offset0:96 offset1:98\n\t" \
  "ds_read2_b32 v[88:89], v79 offset0:0 offset1:2\n\t" \
  "ds_read2_b32 v[90:91], v79 offset0:32 offset1:34\n\t" \
  "ds_read2_b32 v[92:93], v79 offset0:64 offset1:66\n\t" \
  "ds_read2_b32 v[94:95], v79 offset0:96 offset1:98\n\t" \
  "s_waitcnt lgkmcnt(6)\n\t" \
  "v_perm_b32 v112, v82, v80, s62\n\t" \
  "v_perm_b32 v113, v82, v80, s63\n\t" \
  "s_waitcnt lgkmcnt(4)\n\t" \
  "v_perm_b32 v114, v86, v84, s62\n\t" \
  "v_perm_b32 v115, v86, v84, s63\n\t" \
  "s_waitcnt lgkmcnt(2)\n\t" \
  "v_perm_b32 v116, v90, v88, s62\n\t" \
  "v_perm_b32 v117, v90, v88, s63\n\t" \
  "s_waitcnt lgkmcnt(0)\n\t" \
  "v_perm_b32 v118, v94, v92, s62\n\t" \
  "v_perm_b32 v119, v94, v92, s63\n\t" \
  "ds_write_b128 v70, v[112:115] offset:20480\n\t" \
  "ds_write_b128 v70, v[116:119] offset:21504\n\t" \
  "v_perm_b32 v120, v83, v81, s62\n\t" \
  "v_perm_b32 v121, v83, v81, s63\n\t" \
  "v_perm_b32 v122, v87, v85, s62\n\t" \
  "v_perm_b32 v123, v87, v85, s63\n\t" \
  "v_perm_b32 v124, v91, v89, s62\n\t" \
  "v_perm_b32 v125, v91, v89, s63\n\t" \
  "v_perm_b32 v126, v95, v93, s62\n\t" \
  "v_perm_b32 v127, v95, v93, s63\n\t" \
  "ds_write_b128 v70, v[120:123] offset:16384\n\t" \
  "ds_write_b128 v70, v[124:127] offset:17408\n\t" \
  "v_add_u32 v78, 16384, v65\n\t" \
  "s_waitcnt vmcnt(12) lgkmcnt(0)\n\t" \
  "s_barrier\n\t" \
  "ds_read_b64_tr_b16 v[0:1], v78 offset:0\n\t" \
  "ds_read_b64_tr_b16 v[2:3], v78 offset:1024\n\t" \
  "ds_read_b64_tr_b16 v[4:5], v78 offset:8192\n\t" \
  "ds_read_b64_tr_b16 v[6:7], v78 offset:9216\n\t" \
  "ds_read_b64_tr_b16 v[8:9], v78 offset:8\n\t" \
  "ds_read_b64_tr_b16 v[10:11], v78 offset:1032\n\t" \
  "ds_read_b64_tr_b16 v[12:13], v78 offset:8200\n\t" \
  "ds_read_b64_tr_b16 v[14:15], v78 offset:9224\n\t" \
  "s_add_u32 m0, s54, 0\n\t" \
  "s_mov_b32 s60, 524288\n\t" \
  "buffer_load_dwordx4 v71, %[Wq], s60 offen lds\n\t" \
  "s_add_u32 m0, s54, 1024\n\t" \
  "s_mov_b32 s60, 557056\n\t" \
  "buffer_load_dwordx4 v71, %[Wq], s60 offen lds\n\t" \
  "s_add_u32 m0, s54, 2048\n\t" \
  "s_mov_b32 s60, 589824\n\t" \
  "buffer_load_dwordx4 v71, %[Wq], s60 offen lds\n\t" \
  "s_add_u32 m0, s54, 3072\n\t" \
  "s_mov_b32 s60, 622592\n\t" \
  "buffer_load_dwordx4 v71, %[Wq], s60 offen lds\n\t" \
  "v_add_u32 v78, 53248, v65\n\t" \
  "s_waitcnt vmcnt(12) lgkmcnt(0)\n\t" \
  "s_barrier\n\t" \
  "ds_read_b64_tr_b16 a[0:1], v78 offset:0\n\t" \
  "ds_read_b64_tr_b16 a[2:3], v78 offset:1024\n\t" \
  "ds_read_b64_tr_b16 a[4:5], v78 offset:8192\n\t" \
  "ds_read_b64_tr_b16 a[6:7], v78 offset:9216\n\t" \
  "ds_read_b64_tr_b16 a[8:9], v78 offset:8\n\t" \
  "ds_read_b64_tr_b16 a[10:11], v78 offset:1032\n\t" \
  "ds_read_b64_tr_b16 a[12:13], v78 offset:8200\n\t" \
  "ds_read_b64_tr_b16 a[14:15], v78 offset:9224\n\t" \
  "s_xor_b32 s61, s40, 1\n\t" \
  "s_mul_i32 s61, s61, 36864\n\t" \
  "s_add_u32 s61, s61, s55\n\t" \
  "s_add_u32 m0, s54, 16384\n\t" \
  "s_add_u32 s60, s61, 0\n\t" \
  "buffer_load_dwordx4 v75, %[Wo], s60 offen lds\n\t" \
  "s_add_u32 m0, s54, 17408\n\t" \
  "s_add_u32 s60, s61, 9216\n\t" \
  "buffer_load_dwordx4 v75, %[Wo], s60 offen lds\n\t" \
  "s_add_u32 m0, s54, 18432\n\t" \
  "s_add_u32 s60, s61, 18432\n\t" \
  "buffer_load_dwordx4 v75, %[Wo], s60 offen lds\n\t" \
  "s_add_u32 m0, s54, 19456\n\t" \
  "s_add_u32 s60, s61, 27648\n\t" \
  "buffer_load_dwordx4 v75, %[Wo], s60 offen lds\n\t" \
  "v_add_u32 v78, 90112, v64\n\t" \
  "v_add_u32 v79, 91136, v64\n\t" \
  "s_waitcnt vmcnt(12) lgkmcnt(0)\n\t" \
  "s_barrier\n\t" \
  "ds_read2_b32 v[80:81], v78 offset0:0 offset1:2\n\t" \
  "ds_read2_b32 v[82:83], v78 offset0:32 offset1:34\n\t" \
  "ds_read2_b32 v[84:85], v78 offset0:64 offset1:66\n\t" \
  "ds_read2_b32 v[86:87], v78 offset0:96 offset1:98\n\t" \
  "ds_read2_b32 v[88:89], v79 offset0:0 offset1:2\n\t" \
  "ds_read2_b32 v[90:91], v79 offset0:32 offset1:34\n\t" \
  "ds_read2_b32 v[92:93], v79 offset0:64 offset1:66\n\t" \
  "ds_read2_b32 v[94:95], v79 offset0:96 offset1:98\n\t" \
  "s_add_u32 m0, s54, 53248\n\t" \
  "s_mov_b32 s60, 524288\n\t" \
  "buffer_load_dwordx4 v71, %[Wk], s60 offen lds\n\t" \
  "s_add_u32 m0, s54, 54272\n\t" \
  "s_mov_b32 s60, 557056\n\t" \
  "buffer_load_dwordx4 v71, %[Wk], s60 offen lds\n\t" \
  "s_add_u32 m0, s54, 55296\n\t" \
  "s_mov_b32 s60, 589824\n\t" \
  "buffer_load_dwordx4 v71, %[Wk], s60 offen lds\n\t" \
  "s_add_u32 m0, s54, 56320\n\t" \
  "s_mov_b32 s60, 622592\n\t" \
  "buffer_load_dwordx4 v71, %[Wk], s60 offen lds\n\t" \
  "s_waitcnt lgkmcnt(6)\n\t" \
  "v_perm_b32 v112, v82, v80, s62\n\t" \
  "v_perm_b32 v113, v82, v80, s63\n\t" \
  "s_waitcnt lgkmcnt(4)\n\t" \
  "v_perm_b32 v114, v86, v84, s62\n\t" \
  "v_perm_b32 v115, v86, v84, s63\n\t" \
  "s_waitcnt lgkmcnt(2)\n\t" \
  "v_perm_b32 v116, v90, v88, s62\n\t" \
  "v_perm_b32 v117, v90, v88, s63\n\t" \
  "s_waitcnt lgkmcnt(0)\n\t" \
  "v_perm_b32 v118, v94, v92, s62\n\t" \
  "v_perm_b32 v119, v94, v92, s63\n\t" \
  "ds_write_b128 v70, v[112:115] offset:22528\n\t" \
  "ds_write_b128 v70, v[116:119] offset:23552\n\t" \
  "v_perm_b32 v120, v83, v81, s62\n\t" \
  "v_perm_b32 v121, v83, v81, s63\n\t" \
  "v_perm_b32 v122, v87, v85, s62\n\t" \
  "v_perm_b32 v123, v87, v85, s63\n\t" \
  "v_perm_b32 v124, v91, v89, s62\n\t" \
  "v_perm_b32 v125, v91, v89, s63\n\t" \
  "v_perm_b32 v126, v95, v93, s62\n\t" \
  "v_perm_b32 v127, v95, v93, s63\n\t" \
  "ds_write_b128 v70, v[120:123] offset:18432\n\t" \
  "ds_write_b128 v70, v[124:127] offset:19456\n\t" \
  "v_add_u32 v78, 126976, v66\n\t" \
  "s_waitcnt vmcnt(12) lgkmcnt(0)\n\t" \
  "s_barrier\n\t" \
  "ds_read_b64_tr_b16 a[16:17], v78 offset:0\n\t" \
  "ds_read_b64_tr_b16 a[18:19], v78 offset:1024\n\t" \
  "ds_read_b64_tr_b16 a[20:21], v78 offset:8192\n\t" \
  "ds_read_b64_tr_b16 a[22:23], v78 offset:9216\n\t" \
  "ds_read_b64_tr_b16 a[24:25], v78 offset:32\n\t" \
  "ds_read_b64_tr_b16 a[26:27], v78 offset:1056\n\t" \
  "ds_read_b64_tr_b16 a[28:29], v78 offset:8224\n\t" \
  "ds_read_b64_tr_b16 a[30:31], v78 offset:9248\n\t" \
  "s_add_u32 m0, s54, 90112\n\t" \
  "s_mov_b32 s60, 524288\n\t" \
  "buffer_load_dwordx4 v71, %[Wv], s60 offen lds\n\t" \
  "s_add_u32 m0, s54, 91136\n\t" \
  "s_mov_b32 s60, 557056\n\t" \
  "buffer_load_dwordx4 v71, %[Wv], s60 offen lds\n\t" \
  "s_add_u32 m0, s54, 92160\n\t" \
  "s_mov_b32 s60, 589824\n\t" \
  "buffer_load_dwordx4 v71, %[Wv], s60 offen lds\n\t" \
  "s_add_u32 m0, s54, 93184\n\t" \
  "s_mov_b32 s60, 622592\n\t" \
  "buffer_load_dwordx4 v71, %[Wv], s60 offen lds\n\t" \
  "v_add_u32 v78, 0, v65\n\t" \
  "s_waitcnt vmcnt(12) lgkmcnt(0)\n\t" \
  "s_barrier\n\t" \
  "ds_read_b64_tr_b16 a[32:33], v78 offset:0\n\t" \
  "ds_read_b64_tr_b16 a[34:35], v78 offset:1024\n\t" \
  "ds_read_b64_tr_b16 a[36:37], v78 offset:8192\n\t" \
  "ds_read_b64_tr_b16 a[38:39], v78 offset:9216\n\t" \
  "ds_read_b64_tr_b16 a[40:41], v78 offset:8\n\t" \
  "ds_read_b64_tr_b16 a[42:43], v78 offset:1032\n\t" \
  "ds_read_b64_tr_b16 a[44:45], v78 offset:8200\n\t" \
  "ds_read_b64_tr_b16 a[46:47], v78 offset:9224\n\t" \
  "s_xor_b32 s61, s40, 1\n\t" \
  "s_mul_i32 s61, s61, 36864\n\t" \
  "s_add_u32 s61, s61, s55\n\t" \
  "s_add_u32 m0, s54, 126976\n\t" \
  "s_add_u32 s60, s61, 512\n\t" \
  "buffer_load_dwordx4 v75, %[Wo], s60 offen lds\n\t" \
  "s_add_u32 m0, s54, 128000\n\t" \
  "s_add_u32 s60, s61, 9728\n\t" \
  "buffer_load_dwordx4 v75, %[Wo], s60 offen lds\n\t" \
  "s_add_u32 m0, s54, 129024\n\t" \
  "s_add_u32 s60, s61, 18944\n\t" \
  "buffer_load_dwordx4 v75, %[Wo], s60 offen lds\n\t" \
  "s_add_u32 m0, s54, 130048\n\t" \
  "s_add_u32 s60, s61, 28160\n\t" \
  "buffer_load_dwordx4 v75, %[Wo], s60 offen lds\n\t" \
  "v_add_u32 v78, 16384, v64\n\t" \
  "v_add_u32 v79, 17408, v64\n\t" \
  "s_waitcnt vmcnt(12) lgkmcnt(0)\n\t" \
  "s_barrier\n\t" \
  "ds_read2_b32 v[80:81], v78 offset0:0 offset1:2\n\t" \
  "ds_read2_b32 v[82:83], v78 offset0:32 offset1:34\n\t" \
  "ds_read2_b32 v[84:85], v78 offset0:64 offset1:66\n\t" \
  "ds_read2_b32 v[86:87], v78 offset0:96 offset1:98\n\t" \
  "ds_read2_b32 v[88:89], v79 offset0:0 offset1:2\n\t" \
  "ds_read2_b32 v[90:91], v79 offset0:32 offset1:34\n\t" \
  "ds_read2_b32 v[92:93], v79 offset0:64 offset1:66\n\t" \
  "ds_read2_b32 v[94:95], v79 offset0:96 offset1:98\n\t" \
  "s_add_u32 m0, s54, 0\n\t" \
  "s_mov_b32 s60, 1048576\n\t" \
  "buffer_load_dwordx4 v71, %[Wq], s60 offen lds\n\t" \
  "s_add_u32 m0, s54, 1024\n\t" \
  "s_mov_b32 s60, 1081344\n\t" \
  "buffer_load_dwordx4 v71, %[Wq], s60 offen lds\n\t" \
  "s_add_u32 m0, s54, 2048\n\t" \
  "s_mov_b32 s60, 1114112\n\t" \
  "buffer_load_dwordx4 v71, %[Wq], s60 offen lds\n\t" \
  "s_add_u32 m0, s54, 3072\n\t" \
  "s_mov_b32 s60, 1146880\n\t" \
  "buffer_load_dwordx4 v71, %[Wq], s60 offen lds\n\t" \
  "s_waitcnt lgkmcnt(6)\n\t" \
  "v_perm_b32 v112, v82, v80, s62\n\t" \
  "v_perm_b32 v113, v82, v80, s63\n\t" \
  "s_waitcnt lgkmcnt(4)\n\t" \
  "v_perm_b32 v114, v86, v84, s62\n\t" \
  "v_perm_b32 v115, v86, v84, s63\n\t" \
  "s_waitcnt lgkmcnt(2)\n\t" \
  "v_perm_b32 v116, v90, v88, s62\n\t" \
  "v_perm_b32 v117, v90, v88, s63\n\t" \
  "s_waitcnt lgkmcnt(0)\n\t" \
  "v_perm_b32 v118, v94, v92, s62\n\t" \
  "v_perm_b32 v119, v94, v92, s63\n\t" \
  "ds_write_b128 v70, v[112:115] offset:24576\n\t" \
  "ds_write_b128 v70, v[116:119] offset:25600\n\t" \
  "v_perm_b32 v200, v83, v81, s62\n\t" \
  "v_perm_b32 v201, v83, v81, s63\n\t" \
  "v_perm_b32 v202, v87, v85, s62\n\t" \
  "v_perm_b32 v203, v87, v85, s63\n\t" \
  "v_perm_b32 v204, v91, v89, s62\n\t" \
  "v_perm_b32 v205, v91, v89, s63\n\t" \
  "v_perm_b32 v206, v95, v93, s62\n\t" \
  "v_perm_b32 v207, v95, v93, s63\n\t" \
  "v_add_u32 v78, 53248, v65\n\t" \
  "s_waitcnt vmcnt(12) lgkmcnt(0)\n\t" \
  "s_barrier\n\t" \
  "ds_read_b64_tr_b16 a[48:49], v78 offset:0\n\t" \
  "ds_read_b64_tr_b16 a[50:51], v78 offset:1024\n\t" \
  "ds_read_b64_tr_b16 a[52:53], v78 offset:8192\n\t" \
  "ds_read_b64_tr_b16 a[54:55], v78 offset:9216\n\t" \
  "ds_read_b64_tr_b16 a[56:57], v78 offset:8\n\t" \
  "ds_read_b64_tr_b16 a[58:59], v78 offset:1032\n\t" \
  "ds_read_b64_tr_b16 a[60:61], v78 offset:8200\n\t" \
  "ds_read_b64_tr_b16 a[62:63], v78 offset:9224\n\t" \
  "s_add_u32 m0, s54, 16384\n\t" \
  "s_mov_b32 s60, 1048576\n\t" \
  "buffer_load_dwordx4 v71, %[Wk], s60 offen lds\n\t" \
  "s_add_u32 m0, s54, 17408\n\t" \
  "s_mov_b32 s60, 1081344\n\t" \
  "buffer_load_dwordx4 v71, %[Wk], s60 offen lds\n\t" \
  "s_add_u32 m0, s54, 18432\n\t" \
  "s_mov_b32 s60, 1114112\n\t" \
  "buffer_load_dwordx4 v71, %[Wk], s60 offen lds\n\t" \
  "s_add_u32 m0, s54, 19456\n\t" \
  "s_mov_b32 s60, 1146880\n\t" \
  "buffer_load_dwordx4 v71, %[Wk], s60 offen lds\n\t" \
  "v_add_u32 v78, 90112, v66\n\t" \
  "s_waitcnt vmcnt(12) lgkmcnt(0)\n\t" \
  "s_barrier\n\t" \
  "ds_read_b64_tr_b16 a[64:65], v78 offset:0\n\t" \
  "ds_read_b64_tr_b16 a[66:67], v78 offset:1024\n\t" \
  "ds_read_b64_tr_b16 a[68:69], v78 offset:8192\n\t" \
  "ds_read_b64_tr_b16 a[70:71], v78 offset:9216\n\t" \
  "ds_read_b64_tr_b16 a[72:73], v78 offset:32\n\t" \
  "ds_read_b64_tr_b16 a[74:75], v78 offset:1056\n\t" \
  "ds_read_b64_tr_b16 a[76:77], v78 offset:8224\n\t" \
  "ds_read_b64_tr_b16 a[78:79], v78 offset:9248\n\t" \
  "s_xor_b32 s61, s40, 2\n\t" \
  "s_mul_i32 s61, s61, 36864\n\t" \
  "s_add_u32 s61, s61, s55\n\t" \
  "s_add_u32 m0, s54, 53248\n\t" \
  "s_add_u32 s60, s61, 0\n\t" \
  "buffer_load_dwordx4 v75, %[Wo], s60 offen lds\n\t" \
  "s_add_u32 m0, s54, 54272\n\t" \
  "s_add_u32 s60, s61, 9216\n\t" \
  "buffer_load_dwordx4 v75, %[Wo], s60 offen lds\n\t" \
  "s_add_u32 m0, s54, 55296\n\t" \
  "s_add_u32 s60, s61, 18432\n\t" \
  "buffer_load_dwordx4 v75, %[Wo], s60 offen lds\n\t" \
  "s_add_u32 m0, s54, 56320\n\t" \
  "s_add_u32 s60, s61, 27648\n\t" \
  "buffer_load_dwordx4 v75, %[Wo], s60 offen lds\n\t" \
  "v_add_u32 v78, 126976, v64\n\t" \
  "v_add_u32 v79, 128000, v64\n\t" \
  "s_waitcnt vmcnt(12) lgkmcnt(0)\n\t" \
  "s_barrier\n\t" \
  "ds_read2_b32 v[80:81], v78 offset0:0 offset1:2\n\t" \
  "ds_read2_b32 v[82:83], v78 offset0:32 offset1:34\n\t" \
  "ds_read2_b32 v[84:85], v78 offset0:64 offset1:66\n\t" \
  "ds_read2_b32 v[86:87], v78 offset0:96 offset1:98\n\t" \
  "ds_read2_b32 v[88:89], v79 offset0:0 offset1:2\n\t" \
  "ds_read2_b32 v[90:91], v79 offset0:32 offset1:34\n\t" \
  "ds_read2_b32 v[92:93], v79 offset0:64 offset1:66\n\t" \
  "ds_read2_b32 v[94:95], v79 offset0:96 offset1:98\n\t" \
  "s_add_u32 m0, s54, 90112\n\t" \
  "s_mov_b32 s60, 1048576\n\t" \
  "buffer_load_dwordx4 v71, %[Wv], s60 offen lds\n\t" \
  "s_add_u32 m0, s54, 91136\n\t" \
  "s_mov_b32 s60, 1081344\n\t" \
  "buffer_load_dwordx4 v71, %[Wv], s60 offen lds\n\t" \
  "s_add_u32 m0, s54, 92160\n\t" \
  "s_mov_b32 s60, 1114112\n\t" \
  "buffer_load_dwordx4 v71, %[Wv], s60 offen lds\n\t" \
  "s_add_u32 m0, s54, 93184\n\t" \
  "s_mov_b32 s60, 1146880\n\t" \
  "buffer_load_dwordx4 v71, %[Wv], s60 offen lds\n\t" \
  "s_waitcnt lgkmcnt(6)\n\t" \
  "v_perm_b32 v112, v82, v80, s62\n\t" \
  "v_perm_b32 v113, v82, v80, s63\n\t" \
  "s_waitcnt lgkmcnt(4)\n\t" \
  "v_perm_b32 v114, v86, v84, s62\n\t" \
  "v_perm_b32 v115, v86, v84, s63\n\t" \
  "s_waitcnt lgkmcnt(2)\n\t" \
  "v_perm_b32 v116, v90, v88, s62\n\t" \
  "v_perm_b32 v117, v90, v88, s63\n\t" \
  "s_waitcnt lgkmcnt(0)\n\t" \
  "v_perm_b32 v118, v94, v92, s62\n\t" \
  "v_perm_b32 v119, v94, v92, s63\n\t" \
  "ds_write_b128 v70, v[112:115] offset:26624\n\t" \
  "ds_write_b128 v70, v[116:119] offset:27648\n\t" \
  "v_perm_b32 v232, v83, v81, s62\n\t" \
  "v_perm_b32 v233, v83, v81, s63\n\t" \
  "v_perm_b32 v234, v87, v85, s62\n\t" \
  "v_perm_b32 v235, v87, v85, s63\n\t" \
  "v_perm_b32 v236, v91, v89, s62\n\t" \
  "v_perm_b32 v237, v91, v89, s63\n\t" \
  "v_perm_b32 v238, v95, v93, s62\n\t" \
  "v_perm_b32 v239, v95, v93, s63\n\t" \
  "v_add_u32 v78, 0, v65\n\t" \
  "s_waitcnt vmcnt(12) lgkmcnt(0)\n\t" \
  "s_barrier\n\t" \
  "ds_read_b64_tr_b16 a[80:81], v78 offset:0\n\t" \
  "ds_read_b64_tr_b16 a[82:83], v78 offset:1024\n\t" \
  "ds_read_b64_tr_b16 a[84:85], v78 offset:8192\n\t" \
  "ds_read_b64_tr_b16 a[86:87], v78 offset:9216\n\t" \
  "ds_read_b64_tr_b16 a[88:89], v78 offset:8\n\t" \
  "ds_read_b64_tr_b16 a[90:91], v78 offset:1032\n\t" \
  "ds_read_b64_tr_b16 a[92:93], v78 offset:8200\n\t" \
  "ds_read_b64_tr_b16 a[94:95], v78 offset:9224\n\t" \
  "s_add_u32 m0, s54, 126976\n\t" \
  "s_mov_b32 s60, 1572864\n\t" \
  "buffer_load_dwordx4 v71, %[Wq], s60 offen lds\n\t" \
  "s_add_u32 m0, s54, 128000\n\t" \
  "s_mov_b32 s60, 1605632\n\t" \
  "buffer_load_dwordx4 v71, %[Wq], s60 offen lds\n\t" \
  "s_add_u32 m0, s54, 129024\n\t" \
  "s_mov_b32 s60, 1638400\n\t" \
  "buffer_load_dwordx4 v71, %[Wq], s60 offen lds\n\t" \
  "s_add_u32 m0, s54, 130048\n\t" \
  "s_mov_b32 s60, 1671168\n\t" \
  "buffer_load_dwordx4 v71, %[Wq], s60 offen lds\n\t" \
  "v_add_u32 v78, 16384, v65\n\t" \
  "s_waitcnt vmcnt(12) lgkmcnt(0)\n\t" \
  "s_barrier\n\t" \
  "ds_read_b64_tr_b16 v[32:33], v78 offset:0\n\t" \
  "ds_read_b64_tr_b16 v[34:35], v78 offset:1024\n\t" \
  "ds_read_b64_tr_b16 v[36:37], v78 offset:8192\n\t" \
  "ds_read_b64_tr_b16 v[38:39], v78 offset:9216\n\t" \
  "ds_read_b64_tr_b16 a[96:97], v78 offset:8\n\t" \
  "ds_read_b64_tr_b16 a[98:99], v78 offset:1032\n\t" \
  "ds_read_b64_tr_b16 a[100:101], v78 offset:8200\n\t" \
  "ds_read_b64_tr_b16 a[102:103], v78 offset:9224\n\t" \
  "s_xor_b32 s61, s40, 2\n\t" \
  "s_mul_i32 s61, s61, 36864\n\t" \
  "s_add_u32 s61, s61, s55\n\t" \
  "s_add_u32 m0, s54, 0\n\t" \
  "s_add_u32 s60, s61, 512\n\t" \
  "buffer_load_dwordx4 v75, %[Wo], s60 offen lds\n\t" \
  "s_add_u32 m0, s54, 1024\n\t" \
  "s_add_u32 s60, s61, 9728\n\t" \
  "buffer_load_dwordx4 v75, %[Wo], s60 offen lds\n\t" \
  "s_add_u32 m0, s54, 2048\n\t" \
  "s_add_u32 s60, s61, 18944\n\t" \
  "buffer_load_dwordx4 v75, %[Wo], s60 offen lds\n\t" \
  "s_add_u32 m0, s54, 3072\n\t" \
  "s_add_u32 s60, s61, 28160\n\t" \
  "buffer_load_dwordx4 v75, %[Wo], s60 offen lds\n\t" \
  "v_add_u32 v78, 53248, v64\n\t" \
  "v_add_u32 v79, 54272, v64\n\t" \
  "s_waitcnt vmcnt(12) lgkmcnt(0)\n\t" \
  "s_barrier\n\t" \
  "ds_read2_b32 v[80:81], v78 offset0:0 offset1:2\n\t" \
  "ds_read2_b32 v[82:83], v78 offset0:32 offset1:34\n\t" \
  "ds_read2_b32 v[84:85], v78 offset0:64 offset1:66\n\t" \
  "ds_read2_b32 v[86:87], v78 offset0:96 offset1:98\n\t" \
  "ds_read2_b32 v[88:89], v79 offset0:0 offset1:2\n\t" \
  "ds_read2_b32 v[90:91], v79 offset0:32 offset1:34\n\t" \
  "ds_read2_b32 v[92:93], v79 offset0:64 offset1:66\n\t" \
  "ds_read2_b32 v[94:95], v79 offset0:96 offset1:98\n\t" \
  "s_add_u32 m0, s54, 16384\n\t" \
  "s_mov_b32 s60, 1572864\n\t" \
  "buffer_load_dwordx4 v71, %[Wk], s60 offen lds\n\t" \
  "s_add_u32 m0, s54, 17408\n\t" \
  "s_mov_b32 s60, 1605632\n\t" \
  "buffer_load_dwordx4 v71, %[Wk], s60 offen lds\n\t" \
  "s_add_u32 m0, s54, 18432\n\t" \
  "s_mov_b32 s60, 1638400\n\t" \
  "buffer_load_dwordx4 v71, %[Wk], s60 offen lds\n\t" \
  "s_add_u32 m0, s54, 19456\n\t" \
  "s_mov_b32 s60, 1671168\n\t" \
  "buffer_load_dwordx4 v71, %[Wk], s60 offen lds\n\t" \
  "s_waitcnt lgkmcnt(6)\n\t" \
  "v_perm_b32 v112, v82, v80, s62\n\t" \
  "v_perm_b32 v113, v82, v80, s63\n\t" \
  "s_waitcnt lgkmcnt(4)\n\t" \
  "v_perm_b32 v114, v86, v84, s62\n\t" \
  "v_perm_b32 v115, v86, v84, s63\n\t" \
  "s_waitcnt lgkmcnt(2)\n\t" \
  "v_perm_b32 v116, v90, v88, s62\n\t" \
  "v_perm_b32 v117, v90, v88, s63\n\t" \
  "s_waitcnt lgkmcnt(0)\n\t" \
  "v_perm_b32 v118, v94, v92, s62\n\t" \
  "v_perm_b32 v119, v94, v92, s63\n\t" \
  "ds_write_b128 v70, v[112:115] offset:28672\n\t" \
  "ds_write_b128 v70, v[116:119] offset:29696\n\t" \
  "v_perm_b32 v208, v83, v81, s62\n\t" \
  "v_perm_b32 v209, v83, v81, s63\n\t" \
  "v_perm_b32 v210, v87, v85, s62\n\t" \
  "v_perm_b32 v211, v87, v85, s63\n\t" \
  "v_perm_b32 v212, v91, v89, s62\n\t" \
  "v_perm_b32 v213, v91, v89, s63\n\t" \
  "v_perm_b32 v214, v95, v93, s62\n\t" \
  "v_perm_b32 v215, v95, v93, s63\n\t" \
  "v_add_u32 v78, 90112, v66\n\t" \
  "s_waitcnt vmcnt(12) lgkmcnt(0)\n\t" \
  "s_barrier\n\t" \
  "ds_read_b64_tr_b16 a[104:105], v78 offset:0\n\t" \
  "ds_read_b64_tr_b16 a[106:107], v78 offset:1024\n\t" \
  "ds_read_b64_tr_b16 a[108:109], v78 offset:8192\n\t" \
  "ds_read_b64_tr_b16 a[110:111], v78 offset:9216\n\t" \
  "ds_read_b64_tr_b16 a[112:113], v78 offset:32\n\t" \
  "ds_read_b64_tr_b16 a[114:115], v78 offset:1056\n\t" \
  "ds_read_b64_tr_b16 a[116:117], v78 offset:8224\n\t" \
  "ds_read_b64_tr_b16 a[118:119], v78 offset:9248\n\t" \
  "s_add_u32 m0, s54, 53248\n\t" \
  "s_mov_b32 s60, 1572864\n\t" \
  "buffer_load_dwordx4 v71, %[Wv], s60 offen lds\n\t" \
  "s_add_u32 m0, s54, 54272\n\t" \
  "s_mov_b32 s60, 1605632\n\t" \
  "buffer_load_dwordx4 v71, %[Wv], s60 offen lds\n\t" \
  "s_add_u32 m0, s54, 55296\n\t" \
  "s_mov_b32 s60, 1638400\n\t" \
  "buffer_load_dwordx4 v71, %[Wv], s60 offen lds\n\t" \
  "s_add_u32 m0, s54, 56320\n\t" \
  "s_mov_b32 s60, 1671168\n\t" \
  "buffer_load_dwordx4 v71, %[Wv], s60 offen lds\n\t" \
  "v_add_u32 v78, 126976, v65\n\t" \
  "s_waitcnt vmcnt(12) lgkmcnt(0)\n\t" \
  "s_barrier\n\t" \
  "ds_read_b64_tr_b16 a[120:121], v78 offset:0\n\t" \
  "ds_read_b64_tr_b16 a[122:123], v78 offset:1024\n\t" \
  "ds_read_b64_tr_b16 a[124:125], v78 offset:8192\n\t" \
  "ds_read_b64_tr_b16 a[126:127], v78 offset:9216\n\t" \
  "ds_read_b64_tr_b16 v[16:17], v78 offset:8\n\t" \
  "ds_read_b64_tr_b16 v[18:19], v78 offset:1032\n\t" \
  "ds_read_b64_tr_b16 v[20:21], v78 offset:8200\n\t" \
  "ds_read_b64_tr_b16 v[22:23], v78 offset:9224\n\t" \
  "s_xor_b32 s61, s40, 3\n\t" \
  "s_mul_i32 s61, s61, 36864\n\t" \
  "s_add_u32 s61, s61, s55\n\t" \
  "s_add_u32 m0, s54, 90112\n\t" \
  "s_add_u32 s60, s61, 0\n\t" \
  "buffer_load_dwordx4 v75, %[Wo], s60 offen lds\n\t" \
  "s_add_u32 m0, s54, 91136\n\t" \
  "s_add_u32 s60, s61, 9216\n\t" \
  "buffer_load_dwordx4 v75, %[Wo], s60 offen lds\n\t" \
  "s_add_u32 m0, s54, 92160\n\t" \
  "s_add_u32 s60, s61, 18432\n\t" \
  "buffer_load_dwordx4 v75, %[Wo], s60 offen lds\n\t" \
  "s_add_u32 m0, s54, 93184\n\t" \
  "s_add_u32 s60, s61, 27648\n\t" \
  "buffer_load_dwordx4 v75, %[Wo], s60 offen lds\n\t" \
  "v_add_u32 v78, 0, v64\n\t" \
  "v_add_u32 v79, 1024, v64\n\t" \
  "s_waitcnt vmcnt(12) lgkmcnt(0)\n\t" \
  "s_barrier\n\t" \
  "ds_read2_b32 v[80:81], v78 offset0:0 offset1:2\n\t" \
  "ds_read2_b32 v[82:83], v78 offset0:32 offset1:34\n\t" \
  "ds_read2_b32 v[84:85], v78 offset0:64 offset1:66\n\t" \
  "ds_read2_b32 v[86:87], v78 offset0:96 offset1:98\n\t" \
  "ds_read2_b32 v[88:89], v79 offset0:0 offset1:2\n\t" \
  "ds_read2_b32 v[90:91], v79 offset0:32 offset1:34\n\t" \
  "ds_read2_b32 v[92:93], v79 offset0:64 offset1:66\n\t" \
  "ds_read2_b32 v[94:95], v79 offset0:96 offset1:98\n\t" \
  "s_add_u32 m0, s54, 126976\n\t" \
  "s_mov_b32 s60, 2097152\n\t" \
  "buffer_load_dwordx4 v71, %[Wq], s60 offen lds\n\t" \
  "s_add_u32 m0, s54, 128000\n\t" \
  "s_mov_b32 s60, 2129920\n\t" \
  "buffer_load_dwordx4 v71, %[Wq], s60 offen lds\n\t" \
  "s_add_u32 m0, s54, 129024\n\t" \
  "s_mov_b32 s60, 2162688\n\t" \
  "buffer_load_dwordx4 v71, %[Wq], s60 offen lds\n\t" \
  "s_add_u32 m0, s54, 130048\n\t" \
  "s_mov_b32 s60, 2195456\n\t" \
  "buffer_load_dwordx4 v71, %[Wq], s60 offen lds\n\t" \
  "s_waitcnt lgkmcnt(6)\n\t" \
  "v_perm_b32 v112, v82, v80, s62\n\t" \
  "v_perm_b32 v113, v82, v80, s63\n\t" \
  "s_waitcnt lgkmcnt(4)\n\t" \
  "v_perm_b32 v114, v86, v84, s62\n\t" \
  "v_perm_b32 v115, v86, v84, s63\n\t" \
  "s_waitcnt lgkmcnt(2)\n\t" \
  "v_perm_b32 v116, v90, v88, s62\n\t" \
  "v_perm_b32 v117, v90, v88, s63\n\t" \
  "s_waitcnt lgkmcnt(0)\n\t" \
  "v_perm_b32 v118, v94, v92, s62\n\t" \
  "v_perm_b32 v119, v94, v92, s63\n\t" \
  "ds_write_b128 v70, v[112:115] offset:30720\n\t" \
  "ds_write_b128 v70, v[116:119] offset:31744\n\t" \
  "v_perm_b32 v240, v83, v81, s62\n\t" \
  "v_perm_b32 v241, v83, v81, s63\n\t" \
  "v_perm_b32 v242, v87, v85, s62\n\t" \
  "v_perm_b32 v243, v87, v85, s63\n\t" \
  "v_perm_b32 v244, v91, v89, s62\n\t" \
  "v_perm_b32 v245, v91, v89, s63\n\t" \
  "v_perm_b32 v246, v95, v93, s62\n\t" \
  "v_perm_b32 v247, v95, v93, s63\n\t" \
  "v_add_u32 v78, 16384, v65\n\t" \
  "s_waitcnt vmcnt(12) lgkmcnt(0)\n\t" \
  "s_barrier\n\t" \
  "ds_read_b64_tr_b16 a[128:129], v78 offset:0\n\t" \
  "ds_read_b64_tr_b16 a[130:131], v78 offset:1024\n\t" \
  "ds_read_b64_tr_b16 a[132:133], v78 offset:8192\n\t" \
  "ds_read_b64_tr_b16 a[134:135], v78 offset:9216\n\t" \
  "ds_read_b64_tr_b16 a[136:137], v78 offset:8\n\t" \
  "ds_read_b64_tr_b16 a[138:139], v78 offset:1032\n\t" \
  "ds_read_b64_tr_b16 a[140:141], v78 offset:8200\n\t" \
  "ds_read_b64_tr_b16 a[142:143], v78 offset:9224\n\t" \
  "s_add_u32 m0, s54, 0\n\t" \
  "s_mov_b32 s60, 2097152\n\t" \
  "buffer_load_dwordx4 v71, %[Wk], s60 offen lds\n\t" \
  "s_add_u32 m0, s54, 1024\n\t" \
  "s_mov_b32 s60, 2129920\n\t" \
  "buffer_load_dwordx4 v71, %[Wk], s60 offen lds\n\t" \
  "s_add_u32 m0, s54, 2048\n\t" \
  "s_mov_b32 s60, 2162688\n\t" \
  "buffer_load_dwordx4 v71, %[Wk], s60 offen lds\n\t" \
  "s_add_u32 m0, s54, 3072\n\t" \
  "s_mov_b32 s60, 2195456\n\t" \
  "buffer_load_dwordx4 v71, %[Wk], s60 offen lds\n\t" \
  "v_add_u32 v78, 53248, v66\n\t" \
  "s_waitcnt vmcnt(12) lgkmcnt(0)\n\t" \
  "s_barrier\n\t" \
  "ds_read_b64_tr_b16 a[144:145], v78 offset:0\n\t" \
  "ds_read_b64_tr_b16 a[146:147], v78 offset:1024\n\t" \
  "ds_read_b64_tr_b16 a[148:149], v78 offset:8192\n\t" \
  "ds_read_b64_tr_b16 a[150:151], v78 offset:9216\n\t" \
  "ds_read_b64_tr_b16 a[152:153], v78 offset:32\n\t" \
  "ds_read_b64_tr_b16 a[154:155], v78 offset:1056\n\t" \
  "ds_read_b64_tr_b16 a[156:157], v78 offset:8224\n\t" \
  "ds_read_b64_tr_b16 a[158:159], v78 offset:9248\n\t" \
  "s_xor_b32 s61, s40, 3\n\t" \
  "s_mul_i32 s61, s61, 36864\n\t" \
  "s_add_u32 s61, s61, s55\n\t" \
  "s_add_u32 m0, s54, 16384\n\t" \
  "s_add_u32 s60, s61, 512\n\t" \
  "buffer_load_dwordx4 v75, %[Wo], s60 offen lds\n\t" \
  "s_add_u32 m0, s54, 17408\n\t" \
  "s_add_u32 s60, s61, 9728\n\t" \
  "buffer_load_dwordx4 v75, %[Wo], s60 offen lds\n\t" \
  "s_add_u32 m0, s54, 18432\n\t" \
  "s_add_u32 s60, s61, 18944\n\t" \
  "buffer_load_dwordx4 v75, %[Wo], s60 offen lds\n\t" \
  "s_add_u32 m0, s54, 19456\n\t" \
  "s_add_u32 s60, s61, 28160\n\t" \
  "buffer_load_dwordx4 v75, %[Wo], s60 offen lds\n\t" \
  "v_add_u32 v78, 90112, v64\n\t" \
  "v_add_u32 v79, 91136, v64\n\t" \
  "s_waitcnt vmcnt(12) lgkmcnt(0)\n\t" \
  "s_barrier\n\t" \
  "ds_read2_b32 v[80:81], v78 offset0:0 offset1:2\n\t" \
  "ds_read2_b32 v[82:83], v78 offset0:32 offset1:34\n\t" \
  "ds_read2_b32 v[84:85], v78 offset0:64 offset1:66\n\t" \
  "ds_read2_b32 v[86:87], v78 offset0:96 offset1:98\n\t" \
  "ds_read2_b32 v[88:89], v79 offset0:0 offset1:2\n\t" \
  "ds_read2_b32 v[90:91], v79 offset0:32 offset1:34\n\t" \
  "ds_read2_b32 v[92:93], v79 offset0:64 offset1:66\n\t" \
  "ds_read2_b32 v[94:95], v79 offset0:96 offset1:98\n\t" \
  "s_add_u32 m0, s54, 53248\n\t" \
  "s_mov_b32 s60, 2097152\n\t" \
  "buffer_load_dwordx4 v71, %[Wv], s60 offen lds\n\t" \
  "s_add_u32 m0, s54, 54272\n\t" \
  "s_mov_b32 s60, 2129920\n\t" \
  "buffer_load_dwordx4 v71, %[Wv], s60 offen lds\n\t" \
  "s_add_u32 m0, s54, 55296\n\t" \
  "s_mov_b32 s60, 2162688\n\t" \
  "buffer_load_dwordx4 v71, %[Wv], s60 offen lds\n\t" \
  "s_add_u32 m0, s54, 56320\n\t" \
  "s_mov_b32 s60, 2195456\n\t" \
  "buffer_load_dwordx4 v71, %[Wv], s60 offen lds\n\t" \
  "s_waitcnt lgkmcnt(6)\n\t" \
  "v_perm_b32 v192, v82, v80, s62\n\t" \
  "v_perm_b32 v193, v82, v80, s63\n\t" \
  "s_waitcnt lgkmcnt(4)\n\t" \
  "v_perm_b32 v194, v86, v84, s62\n\t" \
  "v_perm_b32 v195, v86, v84, s63\n\t" \
  "s_waitcnt lgkmcnt(2)\n\t" \
  "v_perm_b32 v196, v90, v88, s62\n\t" \
  "v_perm_b32 v197, v90, v88, s63\n\t" \
  "s_waitcnt lgkmcnt(0)\n\t" \
  "v_perm_b32 v198, v94, v92, s62\n\t" \
  "v_perm_b32 v199, v94, v92, s63\n\t" \
  "v_perm_b32 v216, v83, v81, s62\n\t" \
  "v_perm_b32 v217, v83, v81, s63\n\t" \
  "v_perm_b32 v218, v87, v85, s62\n\t" \
  "v_perm_b32 v219, v87, v85, s63\n\t" \
  "v_perm_b32 v220, v91, v89, s62\n\t" \
  "v_perm_b32 v221, v91, v89, s63\n\t" \
  "v_perm_b32 v222, v95, v93, s62\n\t" \
  "v_perm_b32 v223, v95, v93, s63\n\t" \
  "v_add_u32 v78, 126976, v65\n\t" \
  "s_waitcnt vmcnt(12) lgkmcnt(0)\n\t" \
  "s_barrier\n\t" \
  "ds_read_b64_tr_b16 a[160:161], v78 offset:0\n\t" \
  "ds_read_b64_tr_b16 a[162:163], v78 offset:1024\n\t" \
  "ds_read_b64_tr_b16 a[164:165], v78 offset:8192\n\t" \
  "ds_read_b64_tr_b16 a[166:167], v78 offset:9216\n\t" \
  "ds_read_b64_tr_b16 a[168:169], v78 offset:8\n\t" \
  "ds_read_b64_tr_b16 a[170:171], v78 offset:1032\n\t" \
  "ds_read_b64_tr_b16 a[172:173], v78 offset:8200\n\t" \
  "ds_read_b64_tr_b16 a[174:175], v78 offset:9224\n\t" \
  "s_add_u32 m0, s54, 90112\n\t" \
  "s_mov_b32 s60, 2621440\n\t" \
  "buffer_load_dwordx4 v71, %[Wq], s60 offen lds\n\t" \
  "s_add_u32 m0, s54, 91136\n\t" \
  "s_mov_b32 s60, 2654208\n\t" \
  "buffer_load_dwordx4 v71, %[Wq], s60 offen lds\n\t" \
  "s_add_u32 m0, s54, 92160\n\t" \
  "s_mov_b32 s60, 2686976\n\t" \
  "buffer_load_dwordx4 v71, %[Wq], s60 offen lds\n\t" \
  "s_add_u32 m0, s54, 93184\n\t" \
  "s_mov_b32 s60, 2719744\n\t" \
  "buffer_load_dwordx4 v71, %[Wq], s60 offen lds\n\t" \
  "v_add_u32 v78, 0, v65\n\t" \
  "s_waitcnt vmcnt(12) lgkmcnt(0)\n\t" \
  "s_barrier\n\t" \
  "ds_read_b64_tr_b16 a[176:177], v78 offset:0\n\t" \
  "ds_read_b64_tr_b16 a[178:179], v78 offset:1024\n\t" \
  "ds_read_b64_tr_b16 a[180:181], v78 offset:8192\n\t" \
  "ds_read_b64_tr_b16 a[182:183], v78 offset:9216\n\t" \
  "ds_read_b64_tr_b16 a[184:185], v78 offset:8\n\t" \
  "ds_read_b64_tr_b16 a[186:187], v78 offset:1032\n\t" \
  "ds_read_b64_tr_b16 a[188:189], v78 offset:8200\n\t" \
  "ds_read_b64_tr_b16 a[190:191], v78 offset:9224\n\t" \
  "s_add_u32 m0, s54, 126976\n\t" \
  "s_add_u32 s60, s75, 0\n\t" \
  "buffer_load_dwordx4 v75, %[Wo], s60 offen lds\n\t" \
  "s_add_u32 m0, s54, 128000\n\t" \
  "s_add_u32 s60, s75, 9216\n\t" \
  "buffer_load_dwordx4 v75, %[Wo], s60 offen lds\n\t" \
  "s_add_u32 m0, s54, 129024\n\t" \
  "s_add_u32 s60, s75, 18432\n\t" \
  "buffer_load_dwordx4 v75, %[Wo], s60 offen lds\n\t" \
  "s_add_u32 m0, s54, 130048\n\t" \
  "s_add_u32 s60, s75, 27648\n\t" \
  "buffer_load_dwordx4 v75, %[Wo], s60 offen lds\n\t" \
  "v_add_u32 v78, 16384, v64\n\t" \
  "v_add_u32 v79, 17408, v64\n\t" \
  "s_waitcnt vmcnt(12) lgkmcnt(0)\n\t" \
  "s_barrier\n\t" \
  "ds_read2_b32 v[80:81], v78 offset0:0 offset1:2\n\t" \
  "ds_read2_b32 v[82:83], v78 offset0:32 offset1:34\n\t" \
  "ds_read2_b32 v[84:85], v78 offset0:64 offset1:66\n\t" \
  "ds_read2_b32 v[86:87], v78 offset0:96 offset1:98\n\t" \
  "ds_read2_b32 v[88:89], v79 offset0:0 offset1:2\n\t" \
  "ds_read2_b32 v[90:91], v79 offset0:32 offset1:34\n\t" \
  "ds_read2_b32 v[92:93], v79 offset0:64 offset1:66\n\t" \
  "ds_read2_b32 v[94:95], v79 offset0:96 offset1:98\n\t" \
  "s_add_u32 m0, s54, 0\n\t" \
  "s_mov_b32 s60, 2621440\n\t" \
  "buffer_load_dwordx4 v71, %[Wk], s60 offen lds\n\t" \
  "s_add_u32 m0, s54, 1024\n\t" \
  "s_mov_b32 s60, 2654208\n\t" \
  "buffer_load_dwordx4 v71, %[Wk], s60 offen lds\n\t" \
  "s_add_u32 m0, s54, 2048\n\t" \
  "s_mov_b32 s60, 2686976\n\t" \
  "buffer_load_dwordx4 v71, %[Wk], s60 offen lds\n\t" \
  "s_add_u32 m0, s54, 3072\n\t" \
  "s_mov_b32 s60, 2719744\n\t" \
  "buffer_load_dwordx4 v71, %[Wk], s60 offen lds\n\t" \
  "s_waitcnt lgkmcnt(6)\n\t" \
  "v_perm_b32 v224, v82, v80, s62\n\t" \
  "v_perm_b32 v225, v82, v80, s63\n\t" \
  "s_waitcnt lgkmcnt(4)\n\t" \
  "v_perm_b32 v226, v86, v84, s62\n\t" \
  "v_perm_b32 v227, v86, v84, s63\n\t" \
  "s_waitcnt lgkmcnt(2)\n\t" \
  "v_perm_b32 v228, v90, v88, s62\n\t" \
  "v_perm_b32 v229, v90, v88, s63\n\t" \
  "s_waitcnt lgkmcnt(0)\n\t" \
  "v_perm_b32 v230, v94, v92, s62\n\t" \
  "v_perm_b32 v231, v94, v92, s63\n\t" \
  "v_perm_b32 v120, v83, v81, s62\n\t" \
  "v_perm_b32 v121, v83, v81, s63\n\t" \
  "v_perm_b32 v122, v87, v85, s62\n\t" \
  "v_perm_b32 v123, v87, v85, s63\n\t" \
  "v_perm_b32 v124, v91, v89, s62\n\t" \
  "v_perm_b32 v125, v91, v89, s63\n\t" \
  "v_perm_b32 v126, v95, v93, s62\n\t" \
  "v_perm_b32 v127, v95, v93, s63\n\t" \
  "ds_write_b128 v70, v[120:123] offset:32768\n\t" \
  "ds_write_b128 v70, v[124:127] offset:33792\n\t" \
  "v_add_u32 v78, 53248, v66\n\t" \
  "s_waitcnt vmcnt(12) lgkmcnt(0)\n\t" \
  "s_barrier\n\t" \
  "ds_read_b64_tr_b16 a[192:193], v78 offset:0\n\t" \
  "ds_read_b64_tr_b16 a[194:195], v78 offset:1024\n\t" \
  "ds_read_b64_tr_b16 a[196:197], v78 offset:8192\n\t" \
  "ds_read_b64_tr_b16 a[198:199], v78 offset:9216\n\t" \
  "ds_read_b64_tr_b16 v[48:49], v78 offset:32\n\t" \
  "ds_read_b64_tr_b16 v[50:51], v78 offset:1056\n\t" \
  "ds_read_b64_tr_b16 v[52:53], v78 offset:8224\n\t" \
  "ds_read_b64_tr_b16 v[54:55], v78 offset:9248\n\t" \
  "s_add_u32 m0, s54, 16384\n\t" \
  "s_mov_b32 s60, 2621440\n\t" \
  "buffer_load_dwordx4 v71, %[Wv], s60 offen lds\n\t" \
  "s_add_u32 m0, s54, 17408\n\t" \
  "s_mov_b32 s60, 2654208\n\t" \
  "buffer_load_dwordx4 v71, %[Wv], s60 offen lds\n\t" \
  "s_add_u32 m0, s54, 18432\n\t" \
  "s_mov_b32 s60, 2686976\n\t" \
  "buffer_load_dwordx4 v71, %[Wv], s60 offen lds\n\t" \
  "s_add_u32 m0, s54, 19456\n\t" \
  "s_mov_b32 s60, 2719744\n\t" \
  "buffer_load_dwordx4 v71, %[Wv], s60 offen lds\n\t" \
  "v_add_u32 v78, 90112, v65\n\t" \
  "s_waitcnt vmcnt(12) lgkmcnt(0)\n\t" \
  "s_barrier\n\t" \
  "ds_read_b64_tr_b16 a[200:201], v78 offset:0\n\t" \
  "ds_read_b64_tr_b16 a[202:203], v78 offset:1024\n\t" \
  "ds_read_b64_tr_b16 a[204:205], v78 offset:8192\n\t" \
  "ds_read_b64_tr_b16 a[206:207], v78 offset:9216\n\t" \
  "ds_read_b64_tr_b16 v[24:25], v78 offset:8\n\t" \
  "ds_read_b64_tr_b16 v[26:27], v78 offset:1032\n\t" \
  "ds_read_b64_tr_b16 v[28:29], v78 offset:8200\n\t" \
  "ds_read_b64_tr_b16 v[30:31], v78 offset:9224\n\t" \
  "s_add_u32 m0, s54, 53248\n\t" \
  "s_mov_b32 s60, 3145728\n\t" \
  "buffer_load_dwordx4 v71, %[Wq], s60 offen lds\n\t" \
  "s_add_u32 m0, s54, 54272\n\t" \
  "s_mov_b32 s60, 3178496\n\t" \
  "buffer_load_dwordx4 v71, %[Wq], s60 offen lds\n\t" \
  "s_add_u32 m0, s54, 55296\n\t" \
  "s_mov_b32 s60, 3211264\n\t" \
  "buffer_load_dwordx4 v71, %[Wq], s60 offen lds\n\t" \
  "s_add_u32 m0, s54, 56320\n\t" \
  "s_mov_b32 s60, 3244032\n\t" \
  "buffer_load_dwordx4 v71, %[Wq], s60 offen lds\n\t" \
  "v_add_u32 v78, 126976, v67\n\t" \
  "s_waitcnt vmcnt(12) lgkmcnt(0)\n\t" \
  "s_barrier\n\t" \
  "ds_read_u16 v80, v78 offset:0\n\t" \
  "ds_read_u16 v81, v78 offset:128\n\t" \
  "ds_read_u16 v82, v78 offset:4096\n\t" \
  "ds_read_u16 v83, v78 offset:4224\n\t" \
  "ds_read_u16 v84, v78 offset:256\n\t" \
  "ds_read_u16 v85, v78 offset:384\n\t" \
  "ds_read_u16 v86, v78 offset:4352\n\t" \
  "ds_read_u16 v87, v78 offset:4480\n\t" \
  "ds_read_u16 v88, v78 offset:1024\n\t" \
  "ds_read_u16 v89, v78 offset:1152\n\t" \
  "ds_read_u16 v90, v78 offset:5120\n\t" \
  "ds_read_u16 v91, v78 offset:5248\n\t" \
  "ds_read_u16 v92, v78 offset:1280\n\t" \
  "ds_read_u16 v93, v78 offset:1408\n\t" \
  "ds_read_u16 v94, v78 offset:5376\n\t" \
  "ds_read_u16 v95, v78 offset:5504\n\t" \
  "ds_read_u16 v96, v78 offset:8192\n\t" \
  "ds_read_u16 v97, v78 offset:8320\n\t" \
  "ds_read_u16 v98, v78 offset:12288\n\t" \
  "ds_read_u16 v99, v78 offset:12416\n\t" \
  "ds_read_u16 v100, v78 offset:8448\n\t" \
  "ds_read_u16 v101, v78 offset:8576\n\t" \
  "ds_read_u16 v102, v78 offset:12544\n\t" \
  "ds_read_u16 v103, v78 offset:12672\n\t" \
  "ds_read_u16 v104, v78 offset:9216\n\t" \
  "ds_read_u16 v105, v78 offset:9344\n\t" \
  "ds_read_u16 v106, v78 offset:13312\n\t" \
  "ds_read_u16 v107, v78 offset:13440\n\t" \
  "ds_read_u16 v108, v78 offset:9472\n\t" \
  "ds_read_u16 v109, v78 offset:9600\n\t" \
  "ds_read_u16 v110, v78 offset:13568\n\t" \
  "ds_read_u16 v111, v78 offset:13696\n\t" \
  "s_add_u32 m0, s54, 90112\n\t" \
  "s_mov_b32 s60, 3145728\n\t" \
  "buffer_load_dwordx4 v71, %[Wk], s60 offen lds\n\t" \
  "s_add_u32 m0, s54, 91136\n\t" \
  "s_mov_b32 s60, 3178496\n\t" \
  "buffer_load_dwordx4 v71, %[Wk], s60 offen lds\n\t" \
  "s_add_u32 m0, s54, 92160\n\t" \
  "s_mov_b32 s60, 3211264\n\t" \
  "buffer_load_dwordx4 v71, %[Wk], s60 offen lds\n\t" \
  "s_add_u32 m0, s54, 93184\n\t" \
  "s_mov_b32 s60, 3244032\n\t" \
  "buffer_load_dwordx4 v71, %[Wk], s60 offen lds\n\t" \
  "s_waitcnt lgkmcnt(0)\n\t" \
  "v_lshl_or_b32 v248, v81, 16, v80\n\t" \
  "v_lshl_or_b32 v249, v83, 16, v82\n\t" \
  "v_lshl_or_b32 v250, v85, 16, v84\n\t" \
  "v_lshl_or_b32 v251, v87, 16, v86\n\t" \
  "v_lshl_or_b32 v252, v89, 16, v88\n\t" \
  "v_lshl_or_b32 v253, v91, 16, v90\n\t" \
  "v_lshl_or_b32 v254, v93, 16, v92\n\t" \
  "v_lshl_or_b32 v255, v95, 16, v94\n\t" \
  "v_lshl_or_b32 v112, v97, 16, v96\n\t" \
  "v_lshl_or_b32 v113, v99, 16, v98\n\t" \
  "v_lshl_or_b32 v114, v101, 16, v100\n\t" \
  "v_lshl_or_b32 v115, v103, 16, v102\n\t" \
  "v_lshl_or_b32 v116, v105, 16, v104\n\t" \
  "v_lshl_or_b32 v117, v107, 16, v106\n\t" \
  "v_lshl_or_b32 v118, v109, 16, v108\n\t" \
  "v_lshl_or_b32 v119, v111, 16, v110\n\t" \
  "ds_write_b128 v70, v[112:115] offset:34816\n\t" \
  "ds_write_b128 v70, v[116:119] offset:35840\n\t" \
  "v_add_u32 v78, 0, v65\n\t" \
  "s_waitcnt vmcnt(12) lgkmcnt(0)\n\t" \
  "s_barrier\n\t" \
  "ds_read_b64_tr_b16 a[208:209], v78 offset:0\n\t" \
  "ds_read_b64_tr_b16 a[210:211], v78 offset:1024\n\t" \
  "ds_read_b64_tr_b16 a[212:213], v78 offset:8192\n\t" \
  "ds_read_b64_tr_b16 a[214:215], v78 offset:9216\n\t" \
  "ds_read_b64_tr_b16 a[216:217], v78 offset:8\n\t" \
  "ds_read_b64_tr_b16 a[218:219], v78 offset:1032\n\t" \
  "ds_read_b64_tr_b16 a[220:221], v78 offset:8200\n\t" \
  "ds_read_b64_tr_b16 a[222:223], v78 offset:9224\n\t" \
  "s_add_u32 m0, s54, 126976\n\t" \
  "s_mov_b32 s60, 3145728\n\t" \
  "buffer_load_dwordx4 v71, %[Wv], s60 offen lds\n\t" \
  "s_add_u32 m0, s54, 128000\n\t" \
  "s_mov_b32 s60, 3178496\n\t" \
  "buffer_load_dwordx4 v71, %[Wv], s60 offen lds\n\t" \
  "s_add_u32 m0, s54, 129024\n\t" \
  "s_mov_b32 s60, 3211264\n\t" \
  "buffer_load_dwordx4 v71, %[Wv], s60 offen lds\n\t" \
  "s_add_u32 m0, s54, 130048\n\t" \
  "s_mov_b32 s60, 3244032\n\t" \
  "buffer_load_dwordx4 v71, %[Wv], s60 offen lds\n\t" \
  "v_add_u32 v78, 16384, v66\n\t" \
  "s_waitcnt vmcnt(12) lgkmcnt(0)\n\t" \
  "s_barrier\n\t" \
  "ds_read_b64_tr_b16 a[224:225], v78 offset:0\n\t" \
  "ds_read_b64_tr_b16 a[226:227], v78 offset:1024\n\t" \
  "ds_read_b64_tr_b16 a[228:229], v78 offset:8192\n\t" \
  "ds_read_b64_tr_b16 a[230:231], v78 offset:9216\n\t" \
  "ds_read_b64_tr_b16 a[232:233], v78 offset:32\n\t" \
  "ds_read_b64_tr_b16 a[234:235], v78 offset:1056\n\t" \
  "ds_read_b64_tr_b16 a[236:237], v78 offset:8224\n\t" \
  "ds_read_b64_tr_b16 a[238:239], v78 offset:9248\n\t" \
  "s_add_u32 m0, s54, 0\n\t" \
  "s_mov_b32 s60, 3670016\n\t" \
  "buffer_load_dwordx4 v71, %[Wq], s60 offen lds\n\t" \
  "s_add_u32 m0, s54, 1024\n\t" \
  "s_mov_b32 s60, 3702784\n\t" \
  "buffer_load_dwordx4 v71, %[Wq], s60 offen lds\n\t" \
  "s_add_u32 m0, s54, 2048\n\t" \
  "s_mov_b32 s60, 3735552\n\t" \
  "buffer_load_dwordx4 v71, %[Wq], s60 offen lds\n\t" \
  "s_add_u32 m0, s54, 3072\n\t" \
  "s_mov_b32 s60, 3768320\n\t" \
  "buffer_load_dwordx4 v71, %[Wq], s60 offen lds\n\t" \
  "v_add_u32 v78, 53248, v65\n\t" \
  "s_waitcnt vmcnt(12) lgkmcnt(0)\n\t" \
  "s_barrier\n\t" \
  "ds_read_b64_tr_b16 a[240:241], v78 offset:0\n\t" \
  "ds_read_b64_tr_b16 a[242:243], v78 offset:1024\n\t" \
  "ds_read_b64_tr_b16 a[244:245], v78 offset:8192\n\t" \
  "ds_read_b64_tr_b16 a[246:247], v78 offset:9216\n\t" \
  "ds_read_b64_tr_b16 a[248:249], v78 offset:8\n\t" \
  "ds_read_b64_tr_b16 a[250:251], v78 offset:1032\n\t" \
  "ds_read_b64_tr_b16 a[252:253], v78 offset:8200\n\t" \
  "ds_read_b64_tr_b16 a[254:255], v78 offset:9224\n\t" \
  "s_add_u32 m0, s54, 16384\n\t" \
  "s_mov_b32 s60, 3670016\n\t" \
  "buffer_load_dwordx4 v71, %[Wk], s60 offen lds\n\t" \
  "s_add_u32 m0, s54, 17408\n\t" \
  "s_mov_b32 s60, 3702784\n\t" \
  "buffer_load_dwordx4 v71, %[Wk], s60 offen lds\n\t" \
  "s_add_u32 m0, s54, 18432\n\t" \
  "s_mov_b32 s60, 3735552\n\t" \
  "buffer_load_dwordx4 v71, %[Wk], s60 offen lds\n\t" \
  "s_add_u32 m0, s54, 19456\n\t" \
  "s_mov_b32 s60, 3768320\n\t" \
  "buffer_load_dwordx4 v71, %[Wk], s60 offen lds\n\t" \
  "v_add_u32 v78, 90112, v65\n\t" \
  "s_waitcnt vmcnt(12) lgkmcnt(0)\n\t" \
  "s_barrier\n\t" \
  "ds_read_b64_tr_b16 v[80:81], v78 offset:0\n\t" \
  "ds_read_b64_tr_b16 v[82:83], v78 offset:1024\n\t" \
  "ds_read_b64_tr_b16 v[84:85], v78 offset:8192\n\t" \
  "ds_read_b64_tr_b16 v[86:87], v78 offset:9216\n\t" \
  "ds_read_b64_tr_b16 v[88:89], v78 offset:8\n\t" \
  "ds_read_b64_tr_b16 v[90:91], v78 offset:1032\n\t" \
  "ds_read_b64_tr_b16 v[92:93], v78 offset:8200\n\t" \
  "ds_read_b64_tr_b16 v[94:95], v78 offset:9224\n\t" \
  "s_add_u32 m0, s54, 53248\n\t" \
  "s_mov_b32 s60, 3670016\n\t" \
  "buffer_load_dwordx4 v71, %[Wv], s60 offen lds\n\t" \
  "s_add_u32 m0, s54, 54272\n\t" \
  "s_mov_b32 s60, 3702784\n\t" \
  "buffer_load_dwordx4 v71, %[Wv], s60 offen lds\n\t" \
  "s_add_u32 m0, s54, 55296\n\t" \
  "s_mov_b32 s60, 3735552\n\t" \
  "buffer_load_dwordx4 v71, %[Wv], s60 offen lds\n\t" \
  "s_add_u32 m0, s54, 56320\n\t" \
  "s_mov_b32 s60, 3768320\n\t" \
  "buffer_load_dwordx4 v71, %[Wv], s60 offen lds\n\t" \
  "v_add_u32 v78, 126976, v66\n\t" \
  "s_waitcnt vmcnt(12) lgkmcnt(0)\n\t" \
  "s_barrier\n\t" \
  "ds_read_b64_tr_b16 v[96:97], v78 offset:0\n\t" \
  "ds_read_b64_tr_b16 v[98:99], v78 offset:1024\n\t" \
  "ds_read_b64_tr_b16 v[100:101], v78 offset:8192\n\t" \
  "ds_read_b64_tr_b16 v[102:103], v78 offset:9216\n\t" \
  "ds_read_b64_tr_b16 v[104:105], v78 offset:32\n\t" \
  "ds_read_b64_tr_b16 v[106:107], v78 offset:1056\n\t" \
  "ds_read_b64_tr_b16 v[108:109], v78 offset:8224\n\t" \
  "ds_read_b64_tr_b16 v[110:111], v78 offset:9248\n\t" \
  "s_add_u32 m0, s54, 90112\n\t" \
  "s_mov_b32 s60, 4194304\n\t" \
  "buffer_load_dwordx4 v71, %[Wq], s60 offen lds\n\t" \
  "s_add_u32 m0, s54, 91136\n\t" \
  "s_mov_b32 s60, 4227072\n\t" \
  "buffer_load_dwordx4 v71, %[Wq], s60 offen lds\n\t" \
  "s_add_u32 m0, s54, 92160\n\t" \
  "s_mov_b32 s60, 4259840\n\t" \
  "buffer_load_dwordx4 v71, %[Wq], s60 offen lds\n\t" \
  "s_add_u32 m0, s54, 93184\n\t" \
  "s_mov_b32 s60, 4292608\n\t" \
  "buffer_load_dwordx4 v71, %[Wq], s60 offen lds\n\t" \
  "v_add_u32 v78, 0, v65\n\t" \
  "s_waitcnt vmcnt(12) lgkmcnt(0)\n\t" \
  "s_barrier\n\t" \
  "ds_read_b64_tr_b16 v[112:113], v78 offset:0\n\t" \
  "ds_read_b64_tr_b16 v[114:115], v78 offset:1024\n\t" \
  "ds_read_b64_tr_b16 v[116:117], v78 offset:8192\n\t" \
  "ds_read_b64_tr_b16 v[118:119], v78 offset:9216\n\t" \
  "ds_read_b64_tr_b16 v[120:121], v78 offset:8\n\t" \
  "ds_read_b64_tr_b16 v[122:123], v78 offset:1032\n\t" \
  "ds_read_b64_tr_b16 v[124:125], v78 offset:8200\n\t" \
  "ds_read_b64_tr_b16 v[126:127], v78 offset:9224\n\t" \
  "s_add_u32 m0, s54, 126976\n\t" \
  "s_mov_b32 s60, 4194304\n\t" \
  "buffer_load_dwordx4 v71, %[Wk], s60 offen lds\n\t" \
  "s_add_u32 m0, s54, 128000\n\t" \
  "s_mov_b32 s60, 4227072\n\t" \
  "buffer_load_dwordx4 v71, %[Wk], s60 offen lds\n\t" \
  "s_add_u32 m0, s54, 129024\n\t" \
  "s_mov_b32 s60, 4259840\n\t" \
  "buffer_load_dwordx4 v71, %[Wk], s60 offen lds\n\t" \
  "s_add_u32 m0, s54, 130048\n\t" \
  "s_mov_b32 s60, 4292608\n\t" \
  "buffer_load_dwordx4 v71, %[Wk], s60 offen lds\n\t" \
  "v_add_u32 v78, 16384, v65\n\t" \
  "s_waitcnt vmcnt(12) lgkmcnt(0)\n\t" \
  "s_barrier\n\t" \
  "ds_read_b64_tr_b16 v[128:129], v78 offset:0\n\t" \
  "ds_read_b64_tr_b16 v[130:131], v78 offset:1024\n\t" \
  "ds_read_b64_tr_b16 v[132:133], v78 offset:8192\n\t" \
  "ds_read_b64_tr_b16 v[134:135], v78 offset:9216\n\t" \
  "ds_read_b64_tr_b16 v[136:137], v78 offset:8\n\t" \
  "ds_read_b64_tr_b16 v[138:139], v78 offset:1032\n\t" \
  "ds_read_b64_tr_b16 v[140:141], v78 offset:8200\n\t" \
  "ds_read_b64_tr_b16 v[142:143], v78 offset:9224\n\t" \
  "s_add_u32 m0, s54, 0\n\t" \
  "s_mov_b32 s60, 4194304\n\t" \
  "buffer_load_dwordx4 v71, %[Wv], s60 offen lds\n\t" \
  "s_add_u32 m0, s54, 1024\n\t" \
  "s_mov_b32 s60, 4227072\n\t" \
  "buffer_load_dwordx4 v71, %[Wv], s60 offen lds\n\t" \
  "s_add_u32 m0, s54, 2048\n\t" \
  "s_mov_b32 s60, 4259840\n\t" \
  "buffer_load_dwordx4 v71, %[Wv], s60 offen lds\n\t" \
  "s_add_u32 m0, s54, 3072\n\t" \
  "s_mov_b32 s60, 4292608\n\t" \
  "buffer_load_dwordx4 v71, %[Wv], s60 offen lds\n\t" \
  "v_add_u32 v78, 53248, v66\n\t" \
  "s_waitcnt vmcnt(12) lgkmcnt(0)\n\t" \
  "s_barrier\n\t" \
  "ds_read_b64_tr_b16 v[144:145], v78 offset:0\n\t" \
  "ds_read_b64_tr_b16 v[146:147], v78 offset:1024\n\t" \
  "ds_read_b64_tr_b16 v[148:149], v78 offset:8192\n\t" \
  "ds_read_b64_tr_b16 v[150:151], v78 offset:9216\n\t" \
  "ds_read_b64_tr_b16 v[152:153], v78 offset:32\n\t" \
  "ds_read_b64_tr_b16 v[154:155], v78 offset:1056\n\t" \
  "ds_read_b64_tr_b16 v[156:157], v78 offset:8224\n\t" \
  "ds_read_b64_tr_b16 v[158:159], v78 offset:9248\n\t" \
  "v_add_u32 v78, 90112, v65\n\t" \
  "s_waitcnt vmcnt(8) lgkmcnt(0)\n\t" \
  "s_barrier\n\t" \
  "ds_read_b64_tr_b16 v[160:161], v78 offset:0\n\t" \
  "ds_read_b64_tr_b16 v[162:163], v78 offset:1024\n\t" \
  "ds_read_b64_tr_b16 v[164:165], v78 offset:8192\n\t" \
  "ds_read_b64_tr_b16 v[166:167], v78 offset:9216\n\t" \
  "ds_read_b64_tr_b16 v[168:169], v78 offset:8\n\t" \
  "ds_read_b64_tr_b16 v[170:171], v78 offset:1032\n\t" \
  "ds_read_b64_tr_b16 v[172:173], v78 offset:8200\n\t" \
  "ds_read_b64_tr_b16 v[174:175], v78 offset:9224\n\t" \
  "v_add_u32 v78, 126976, v65\n\t" \
  "s_waitcnt vmcnt(4) lgkmcnt(0)\n\t" \
  "s_barrier\n\t" \
  "ds_read_b64_tr_b16 v[40:41], v78 offset:0\n\t" \
  "ds_read_b64_tr_b16 v[42:43], v78 offset:1024\n\t" \
  "ds_read_b64_tr_b16 v[44:45], v78 offset:8192\n\t" \
  "ds_read_b64_tr_b16 v[46:47], v78 offset:9216\n\t" \
  "ds_read_b64_tr_b16 v[176:177], v78 offset:8\n\t" \
  "ds_read_b64_tr_b16 v[178:179], v78 offset:1032\n\t" \
  "ds_read_b64_tr_b16 v[180:181], v78 offset:8200\n\t" \
  "ds_read_b64_tr_b16 v[182:183], v78 offset:9224\n\t" \
  "v_add_u32 v78, 0, v66\n\t" \
  "s_waitcnt vmcnt(0) lgkmcnt(0)\n\t" \
  "s_barrier\n\t" \
  "ds_read_b64_tr_b16 v[184:185], v78 offset:0\n\t" \
  "ds_read_b64_tr_b16 v[186:187], v78 offset:1024\n\t" \
  "ds_read_b64_tr_b16 v[188:189], v78 offset:8192\n\t" \
  "ds_read_b64_tr_b16 v[190:191], v78 offset:9216\n\t" \
  "ds_read_b64_tr_b16 v[56:57], v78 offset:32\n\t" \
  "ds_read_b64_tr_b16 v[58:59], v78 offset:1056\n\t" \
  "ds_read_b64_tr_b16 v[60:61], v78 offset:8224\n\t" \
  "ds_read_b64_tr_b16 v[62:63], v78 offset:9248\n\t" \
  "s_waitcnt lgkmcnt(0)\n\t" \
  "s_barrier\n\t" \
  "ds_write_b128 v70, v[0:3] offset:0\n\t" \
  "ds_write_b128 v70, v[4:7] offset:1024\n\t" \
  "ds_write_b128 v70, v[8:11] offset:2048\n\t" \
  "ds_write_b128 v70, v[12:15] offset:3072\n\t" \
  "ds_write_b128 v70, v[16:19] offset:4096\n\t" \
  "ds_write_b128 v70, v[20:23] offset:5120\n\t" \
  "ds_write_b128 v70, v[24:27] offset:6144\n\t" \
  "ds_write_b128 v70, v[28:31] offset:7168\n\t" \
  "ds_write_b128 v70, v[32:35] offset:8192\n\t" \
  "ds_write_b128 v70, v[36:39] offset:9216\n\t" \
  "ds_write_b128 v70, v[40:43] offset:10240\n\t" \
  "ds_write_b128 v70, v[44:47] offset:11264\n\t" \
  "ds_write_b128 v70, v[48:51] offset:12288\n\t" \
  "ds_write_b128 v70, v[52:55] offset:13312\n\t" \
  "ds_write_b128 v70, v[56:59] offset:14336\n\t" \
  "ds_write_b128 v70, v[60:63] offset:15360\n\t" \
  "s_waitcnt lgkmcnt(0)\n\t" \
  "v_and_b32 v72, 1, v72\n\t" \
  "v_and_b32 v74, 1, v74\n\t" \
  "v_cmp_ne_u32 s[90:91], s32, v72\n\t" \
  "v_cmp_ne_u32 vcc, s32, v74\n\t" \
  "s_nop 1\n\t" \
  "s_mov_b32 s96, vcc_lo\n\t" \
  "s_mov_b32 s21, vcc_lo\n\t" \
  "v_and_b32 v73, 32, v69\n\t" \
  "v_lshl_add_u32 v73, v73, 5, v69\n\t" \
  "s_mov_b32 s35, s30\n\t" \
  "s_cmp_lt_u32 s32, " CH_DRYL "\n\t" \
  "s_cbranch_scc1 LINI_%=\n\t" \
  "s_mov_b32 s30, 0\n\t" \
  "s_mov_b32 s34, 0\n\t" \
  "s_cmp_eq_u32 s40, 0\n\t" \
  "s_cbranch_scc1 LW0_%=\n\t" \
  "s_barrier\n\t" \
  "s_cmp_eq_u32 s40, 1\n\t" \
  "s_cbranch_scc1 LG1_%=\n\t" \
  "s_cmp_eq_u32 s40, 2\n\t" \
  "s_cbranch_scc1 LG2_%=\n\t" \
  "s_branch LG3_%=\n\t" \
  "LINI_%=:\n\t" \
  "s_waitcnt vmcnt(0) lgkmcnt(0)\n\t" \
  "s_mov_b32 s30, s35\n\t" \
  "s_movk_i32 s34, 1000\n\t" \
  "s_mov_b32 s48, -1\n\t" \
  "s_add_u32 s97, s41, s53\n\t" \
  "s_add_u32 s98, s37, s53\n\t" \
  "s_add_u32 s82, s48, 0\n\t" \
  "s_and_b32 s82, s82, " CH_TRM "\n\t" \
  "s_lshr_b64 s[60:61], s[90:91], s82\n\t" \
  "s_and_b32 s60, s60, 1\n\t" \
  "s_lshl_b32 s60, s60, 13\n\t" \
  "s_add_u32 s82, s82, " CH_SG0 "\n\t" \
  "s_lshl_b32 s82, s82, 21\n\t" \
  "s_or_b32 s82, s82, s60\n\t" \
  "s_add_u32 s50, s82, s38\n\t" \
  "s_add_u32 s69, s82, s39\n\t" \
  "s_add_u32 s82, s48, 1\n\t" \
  "s_lshr_b64 s[60:61], s[90:91], s82\n\t" \
  "s_and_b32 s60, s60, 1\n\t" \
  "s_lshl_b32 s60, s60, 13\n\t" \
  "s_add_u32 s82, s82, " CH_SG0 "\n\t" \
  "s_lshl_b32 s82, s82, 21\n\t" \
  "s_or_b32 s82, s82, s60\n\t" \
  "s_add_u32 s52, s82, s43\n\t" \
  "s_add_u32 s82, s48, " CH_ARMD "\n\t" \
  "s_lshr_b64 s[60:61], s[90:91], s82\n\t" \
  "s_and_b32 s60, s60, 1\n\t" \
  "s_lshl_b32 s60, s60, 13\n\t" \
  "s_add_u32 s82, s82, " CH_SG0 "\n\t" \
  "s_lshl_b32 s82, s82, 21\n\t" \
  "s_or_b32 s82, s82, s60\n\t" \
  "s_add_u32 s85, s82, s45\n\t" \
  "s_cmp_lt_i32 s48, " CH_ARMS "\n\t" \
  "s_cselect_b32 s85, s27, s85\n\t" \
  "v_and_b32 v37, 0x30, v69\n\t" \
  "v_add_u32 v37, s52, v37\n\t" \
  "s_mov_b32 s48, 0\n\t" \
  "s_mov_b32 s86, 1\n\t" \
  "s_and_b32 s49, s96, 1\n\t" \
  "s_lshl_b32 s49, s49, 13\n\t" \
  "s_add_u32 s53, s49, " CH_SLOT "\n\t" \
  "s_movk_i32 s20, 2560\n\t" \
  "s_movk_i32 s60, 124\n\t" \
  "s_cmp_eq_u32 s40, 3\n\t" \
  "s_cselect_b32 s81, 76, s60\n\t" \
  "s_add_u32 s82, s48, 0\n\t" \
  "s_and_b32 s82, s82, " CH_TRM "\n\t" \
  "s_lshr_b64 s[60:61], s[90:91], s82\n\t" \
  "s_and_b32 s60, s60, 1\n\t" \
  "s_lshl_b32 s60, s60, 13\n\t" \
  "s_add_u32 s82, s82, " CH_SG0 "\n\t" \
  "s_lshl_b32 s82, s82, 21\n\t" \
  "s_or_b32 s82, s82, s60\n\t" \
  "s_add_u32 s50, s82, s38\n\t" \
  "s_add_u32 s69, s82, s39\n\t" \
  "s_add_u32 s82, s48, 0\n\t" \
  "s_lshr_b64 s[60:61], s[90:91], s82\n\t" \
  "s_and_b32 s60, s60, 1\n\t" \
  "s_lshl_b32 s60, s60, 13\n\t" \
  "s_add_u32 s82, s82, " CH_SG0 "\n\t" \
  "s_lshl_b32 s82, s82, 21\n\t" \
  "s_or_b32 s82, s82, s60\n\t" \
  "s_add_u32 s51, s82, s43\n\t" \
  "s_add_u32 s97, s41, s53\n\t" \
  "s_add_u32 s98, s37, s53\n\t" \
  "s_add_u32 s82, s48, 0\n\t" \
  "s_and_b32 s82, s82, " CH_TRM "\n\t" \
  "s_lshr_b64 s[60:61], s[90:91], s82\n\t" \
  "s_and_b32 s60, s60, 1\n\t" \
  "s_lshl_b32 s60, s60, 13\n\t" \
  "s_add_u32 s82, s82, " CH_SG0 "\n\t" \
  "s_lshl_b32 s82, s82, 21\n\t" \
  "s_or_b32 s82, s82, s60\n\t" \
  "s_add_u32 s50, s82, s38\n\t" \
  "s_add_u32 s69, s82, s39\n\t" \
  "s_add_u32 s82, s48, 1\n\t" \
  "s_lshr_b64 s[60:61], s[90:91], s82\n\t" \
  "s_and_b32 s60, s60, 1\n\t" \
  "s_lshl_b32 s60, s60, 13\n\t" \
  "s_add_u32 s82, s82, " CH_SG0 "\n\t" \
  "s_lshl_b32 s82, s82, 21\n\t" \
  "s_or_b32 s82, s82, s60\n\t" \
  "s_add_u32 s52, s82, s43\n\t" \
  "s_add_u32 s82, s48, " CH_ARMD "\n\t" \
  "s_lshr_b64 s[60:61], s[90:91], s82\n\t" \
  "s_and_b32 s60, s60, 1\n\t" \
  "s_lshl_b32 s60, s60, 13\n\t" \
  "s_add_u32 s82, s82, " CH_SG0 "\n\t" \
  "s_lshl_b32 s82, s82, 21\n\t" \
  "s_or_b32 s82, s82, s60\n\t" \
  "s_add_u32 s85, s82, s45\n\t" \
  "s_cmp_lt_i32 s48, " CH_ARMS "\n\t" \
  "s_cselect_b32 s85, s27, s85\n\t" \
  "s_memrealtime s[62:63]\n\t" \
  "s_waitcnt lgkmcnt(0)\n\t" \
  "s_mov_b32 s65, s62\n\t" \
  "s_sub_u32 s47, s62, 1000\n\t" \
  "s_mov_b64 vcc, -1\n\t" \
  "s_cmp_eq_u32 s40, 0\n\t" \
  "s_cbranch_scc1 LW0_%=\n\t" \
  "s_barrier\n\t" \
  "s_cmp_eq_u32 s40, 1\n\t" \
  "s_cbranch_scc1 LG1_%=\n\t" \
  "s_cmp_eq_u32 s40, 2\n\t" \
  "s_cbranch_scc1 LG2_%=\n\t" \
  "s_branch LTK_%=\n\t" \
  ".p2align 6\n\t" \
  "LW0_%=:\n\t" \
  "v_lshlrev_b32 v78, 2, v69\n\t" \
  "v_and_b32 v78, 0x300, v78\n\t" \
  "v_lshrrev_b32 v79, 8, v69\n\t" \
  "v_lshl_add_u32 v78, v79, 6, v78\n\t" \
  "v_and_b32 v79, 48, v69\n\t" \
  "v_add_u32 v78, v78, v79\n\t" \
  "s_add_u32 s98, s49, s42\n\t" \
  "s_add_u32 s97, s98, 32768\n\t" \
  "s_add_u32 s99, s98, 65536\n\t" \
  "s_barrier\n\t" \
  "s_memrealtime s[62:63]\n\t" \
  "s_waitcnt lgkmcnt(0)\n\t" \
  "s_add_u32 s62, s62, " CH_DLL2 "\n\t" \
  "s_sub_i32 s63, s62, s65\n\t" \
  "s_cmp_gt_i32 s63, " CH_DLT "\n\t" \
  "s_cselect_b32 s60, " CH_DLY ", " CH_DLX "\n\t" \
  "s_add_u32 s62, s62, s60\n\t" \
  "s_cmp_gt_i32 s63, 0\n\t" \
  "s_cselect_b32 s65, s62, s65\n\t" \
  "s_mov_b32 m0, 0\n\t" \
  "s_movk_i32 s60, 8192\n\t" \
  CH_ALW \
  "LWT_%=:\n\t" \
  "s_memrealtime s[62:63]\n\t" \
  "s_sub_u32 s60, s60, 1\n\t" \
  "s_cselect_b32 s61, 0, -1\n\t" \
  "s_waitcnt lgkmcnt(0)\n\t" \
  "s_sub_i32 s63, s62, s65\n\t" \
  "s_and_b32 s63, s63, s61\n\t" \
  "s_cmp_lt_i32 s63, " CH_FIN "\n\t" \
  "s_cbranch_scc1 LWT_%=\n\t" \
  "s_cmp_lt_i32 s63, 0\n\t" \
  "s_cbranch_scc1 LWT_%=f\n\t" \
  "LWT_%=g:\n\t" \
  "LDMA_%=:\n\t" \
  "buffer_load_dwordx4 v78, s[28:31], s98 offen offset:0 sc1 lds\n\t" \
  "buffer_load_dwordx4 v78, s[28:31], s98 offen offset:1024 sc1 lds\n\t" \
  "buffer_load_dwordx4 v78, s[28:31], s98 offen offset:2048 sc1 lds\n\t" \
  "buffer_load_dwordx4 v78, s[28:31], s98 offen offset:3072 sc1 lds\n\t" \
  "s_movk_i32 m0, 4096\n\t" \
  "s_mov_b32 s66, s62\n\t" \
  "buffer_load_dwordx4 v78, s[28:31], s97 offen offset:0 sc1 lds\n\t" \
  "buffer_load_dwordx4 v78, s[28:31], s97 offen offset:1024 sc1 lds\n\t" \
  "buffer_load_dwordx4 v78, s[28:31], s97 offen offset:2048 sc1 lds\n\t" \
  "buffer_load_dwordx4 v78, s[28:31], s97 offen offset:3072 sc1 lds\n\t" \
  "s_movk_i32 m0, 8192\n\t" \
  "s_nop 0\n\t" \
  "buffer_load_dwordx4 v69, s[28:31], s99 offen sc1 lds\n\t" \
  ".p2align 6\n\t" \
  "LG0_%=:\n\t" \
  "s_barrier\n\t" \
  "buffer_load_dwordx4 v[52:55], v69, s[28:31], s50 offen sc1\n\t" \
  "buffer_load_dwordx4 v[56:59], v69, s[28:31], s50 offen offset:1024 sc1\n\t" \
  "s_add_u32 s64, s50, 16384\n\t" \
  "s_movk_i32 m0, 11264\n\t" \
  "v_lshlrev_b32 v75, 1, v69\n\t" \
  "buffer_load_dword v75, s[28:31], s64 offen sc1 lds\n\t" \
  "s_memrealtime s[62:63]\n\t" \
  "s_waitcnt lgkmcnt(0)\n\t" \
  "s_sub_u32 s62, s62, " CH_TKL "\n\t" \
  "s_sub_i32 s63, s62, s65\n\t" \
  "s_cmp_gt_i32 s63, 0\n\t" \
  "s_cselect_b32 s62, s62, s65\n\t" \
  "s_sub_i32 s63, s62, s47\n\t" \
  "s_cmp_gt_i32 s63, " CH_LATE "\n\t" \
  "s_cselect_b32 s71, " CH_SCHW ", s46\n\t" \
  "s_add_u32 s61, s47, " CH_LATE "\n\t" \
  "s_cmp_gt_i32 s63, " CH_LATE "\n\t" \
  "s_cselect_b32 s62, s62, s61\n\t" \
  "s_add_u32 s71, s71, s53\n\t" \
  "s_add_u32 s60, s62, s81\n\t" \
  "s_add_u32 s60, s60, " CH_DL1L "\n\t" \
  "s_lshl_b32 s60, s60, 8\n\t" \
  "s_or_b32 s60, s60, " CH_B0 "\n\t" \
  "s_cmp_lt_u32 s48, " CH_ES "\n\t" \
  "s_cselect_b32 s61, " CH_EX ", 0\n\t" \
  "s_add_u32 s60, s60, s61\n\t" \
  "s_not_b32 s60, s60\n\t" \
  "v_mov_b32 v76, s60\n\t" \
  "s_lshr_b32 s60, s46, 31\n\t" \
  "s_or_b32 s60, s60, s68\n\t" \
  "s_cselect_b32 s25, 0, 1\n\t" \
  "s_mov_b64 exec, s[24:25]\n\t" \
  "buffer_atomic_umin v76, v69, s[28:31], s71 offen sc1\n\t" \
  "s_mov_b64 exec, -1\n\t" \
  "ds_read_b128 v[44:47], v70 offset:0\n\t" \
  "ds_read_b128 v[48:51], v70 offset:1024\n\t" \
  "ds_read_b128 v[60:63], v70 offset:2048\n\t" \
  "ds_read_b128 v[64:67], v70 offset:3072\n\t" \
  "v_mov_b64 v[36:37], 0\n\t" \
  "v_mov_b64 v[38:39], 0\n\t" \
  "v_mov_b64 v[40:41], 0\n\t" \
  "v_mov_b64 v[42:43], 0\n\t" \
  "v_lshrrev_b32 v71, 1, v69\n\t" \
  "v_lshrrev_b32 v72, 3, v69\n\t" \
  "v_and_b32 v72, 0x78, v72\n\t" \
  "v_and_b32 v74, 0x30, v69\n\t" \
  "v_lshl_add_u32 v72, v74, 3, v72\n\t" \
  "s_add_u32 s54, s53, s36\n\t" \
  "s_add_u32 s55, s54, 524288\n\t" \
  "s_add_u32 s54, s54, 0\n\t" \
  "s_add_u32 s75, s54, 262144\n\t" \
  "s_add_u32 s80, s53, s26\n\t" \
  "s_or_b32 s87, s53, 0x1fff80\n\t" \
  "s_and_b32 s87, s87, -128\n\t" \
  "s_add_u32 s87, s87, 0x200000\n\t" \
  ".p2align 5\n\t" \
  ".fill 2, 4, 0xbf800000\n\t" \
  "s_waitcnt vmcnt(9)\n\t" \
  "s_barrier\n\t" \
  "ds_read_b128 v[0:3], v69 offset:0\n\t" \
  "ds_read_b128 v[4:7], v69 offset:1024\n\t" \
  "ds_read_b128 v[8:11], v69 offset:2048\n\t" \
  "ds_read_b128 v[12:15], v69 offset:3072\n\t" \
  "s_waitcnt lgkmcnt(3)\n\t" \
  "v_smfmac_f32_16x16x64_bf16 v[36:39], v[0:3], v[44:51], v68\n\t" \
  "ds_read_b128 v[44:47], v70 offset:4096\n\t" \
  "v_smfmac_f32_16x16x64_bf16 v[40:43], v[0:3], v[60:67], v68\n\t" \
  "ds_read_b128 v[48:51], v70 offset:5120\n\t" \
  "s_waitcnt lgkmcnt(4)\n\t" \
  "v_smfmac_f32_16x16x64_bf16 v[36:39], v[4:7], a[32:39], v68\n\t" \
  "ds_read_b128 v[60:63], v70 offset:6144\n\t" \
  "v_smfmac_f32_16x16x64_bf16 v[40:43], v[4:7], a[40:47], v68\n\t" \
  "ds_read_b128 v[64:67], v70 offset:7168\n\t" \
  "s_waitcnt lgkmcnt(5)\n\t" \
  "v_smfmac_f32_16x16x64_bf16 v[36:39], v[8:11], a[80:87], v68\n\t" \
  "s_waitcnt vmcnt(4)\n\t" \
  "s_barrier\n\t" \
  "ds_read_b128 v[16:19], v69 offset:4096\n\t" \
  "v_smfmac_f32_16x16x64_bf16 v[40:43], v[8:11], a[88:95], v68\n\t" \
  "ds_read_b128 v[20:23], v69 offset:5120\n\t" \
  "s_waitcnt lgkmcnt(6)\n\t" \
  "v_smfmac_f32_16x16x64_bf16 v[36:39], v[12:15], a[120:127], v68\n\t" \
  "ds_read_b128 v[24:27], v69 offset:6144\n\t" \
  "s_waitcnt lgkmcnt(5)\n\t" \
  "v_smfmac_f32_16x16x64_bf16 v[40:43], v[12:15], v[44:51], v68\n\t" \
  "ds_read_b128 v[28:31], v69 offset:7168\n\t" \
  "s_waitcnt lgkmcnt(3)\n\t" \
  "v_smfmac_f32_16x16x64_bf16 v[36:39], v[16:19], a[160:167], v68\n\t" \
  "ds_read_b128 v[32:35], v69 offset:8192\n\t" \
  "v_smfmac_f32_16x16x64_bf16 v[40:43], v[16:19], a[168:175], v68\n\t" \
  "s_waitcnt lgkmcnt(3)\n\t" \
  "v_mov_b64 v[44:45], 0\n\t" \
  "v_smfmac_f32_16x16x64_bf16 v[36:39], v[20:23], a[200:207], v68\n\t" \
  "s_waitcnt lgkmcnt(2)\n\t" \
  "v_mov_b64 v[46:47], 0\n\t" \
  "v_smfmac_f32_16x16x64_bf16 v[36:39], v[24:27], a[240:247], v68\n\t" \
  "s_waitcnt lgkmcnt(1)\n\t" \
  "v_mov_b64 v[48:49], 0\n\t" \
  "v_smfmac_f32_16x16x64_bf16 v[36:39], v[28:31], v[112:119], v68\n\t" \
  "s_waitcnt lgkmcnt(0)\n\t" \
  "v_mov_b64 v[50:51], 0\n\t" \
  "v_smfmac_f32_16x16x64_bf16 v[36:39], v[32:35], v[160:167], v68\n\t" \
  "v_smfmac_f32_16x16x64_bf16 v[40:43], v[20:23], v[60:67], v68\n\t" \
  "ds_read_b128 v[60:63], v70 offset:8192\n\t" \
  "v_smfmac_f32_16x16x64_bf16 v[40:43], v[24:27], a[248:255], v68\n\t" \
  "ds_read_b128 v[64:67], v70 offset:9216\n\t" \
  "v_add_f32 v74, v36, v38\n\t" \
  "v_smfmac_f32_16x16x64_bf16 v[40:43], v[28:31], v[120:127], v68\n\t" \
  "v_add_f32 v37, v37, v39\n\t" \
  "v_smfmac_f32_16x16x64_bf16 v[40:43], v[32:35], v[168:175], v68\n\t" \
  "v_cmp_u_f32 vcc, v74, v37\n\t" \
  "v_smfmac_f32_16x16x64_bf16 v[44:47], v[0:3], a[0:7], v68\n\t" \
  "s_cbranch_vccnz LFAIL0_%=\n\t" \
  "LGO0_%=:\n\t" \
  "v_cvt_pk_bf16_f32 v74, v74, v37\n\t" \
  "v_smfmac_f32_16x16x64_bf16 v[44:47], v[4:7], a[48:55], v68\n\t" \
  "s_waitcnt lgkmcnt(0)\n\t" \
  "v_add_f32 v75, v40, v42\n\t" \
  "v_smfmac_f32_16x16x64_bf16 v[44:47], v[8:11], v[60:67], v68\n\t" \
  "ds_read_b128 v[60:63], v70 offset:10240\n\t" \
  "v_add_f32 v41, v41, v43\n\t" \
  "v_cvt_pk_bf16_f32 v75, v75, v41\n\t" \
  "v_smfmac_f32_16x16x64_bf16 v[44:47], v[12:15], a[128:135], v68\n\t" \
  "ds_write2st64_b32 v71, v74, v75 offset0:36 offset1:38\n\t" \
  "v_smfmac_f32_16x16x64_bf16 v[44:47], v[16:19], a[176:183], v68\n\t" \
  "ds_read_b128 v[64:67], v70 offset:11264\n\t" \
  "v_smfmac_f32_16x16x64_bf16 v[44:47], v[20:23], a[208:215], v68\n\t" \
  "v_smfmac_f32_16x16x64_bf16 v[44:47], v[24:27], v[80:87], v68\n\t" \
  "v_smfmac_f32_16x16x64_bf16 v[44:47], v[28:31], v[128:135], v68\n\t" \
  "s_waitcnt lgkmcnt(0)\n\t" \
  "v_smfmac_f32_16x16x64_bf16 v[44:47], v[32:35], v[60:67], v68\n\t" \
  "buffer_load_dwordx4 v[60:63], v69, s[28:31], s64 offen sc1\n\t" \
  "s_barrier\n\t" \
  "ds_read_b64_tr_b16 v[76:77], v72 offset:9216\n\t" \
  "v_smfmac_f32_16x16x64_bf16 v[48:51], v[0:3], a[8:15], v68\n\t" \
  "ds_read_b64_tr_b16 v[78:79], v72 offset:9728\n\t" \
  "v_smfmac_f32_16x16x64_bf16 v[48:51], v[4:7], a[56:63], v68\n\t" \
  "v_add_f32 v74, v44, v46\n\t" \
  "v_smfmac_f32_16x16x64_bf16 v[48:51], v[8:11], a[96:103], v68\n\t" \
  "v_add_f32 v45, v45, v47\n\t" \
  "v_smfmac_f32_16x16x64_bf16 v[48:51], v[12:15], a[136:143], v68\n\t" \
  "s_waitcnt vmcnt(4)\n\t" \
  "s_waitcnt lgkmcnt(0)\n\t" \
  "v_cvt_pk_bf16_f32 v74, v74, v45\n\t" \
  "v_mfma_f32_16x16x32_bf16 v[36:39], v[52:55], v[76:79], 0\n\t" \
  "buffer_load_dwordx4 v[64:67], v69, s[28:31], s64 offen offset:1024 sc1\n\t" \
  "s_waitcnt vmcnt(4)\n\t" \
  "v_mfma_f32_16x16x32_bf16 v[40:43], v[56:59], v[76:79], 0\n\t" \
  "v_mov_b64 v[52:53], 0\n\t" \
  "v_mov_b64 v[54:55], 0\n\t" \
  "v_smfmac_f32_16x16x64_bf16 v[48:51], v[16:19], a[184:191], v68\n\t" \
  "v_mov_b64 v[56:57], 0\n\t" \
  "v_mov_b64 v[58:59], 0\n\t" \
  "v_smfmac_f32_16x16x64_bf16 v[48:51], v[20:23], a[216:223], v68\n\t" \
  "ds_write_b32 v71, v74 offset:0\n\t" \
  "v_add_f32_dpp v36, v40, v36 quad_perm:[2,3,0,1] row_mask:0xf bank_mask:0xf\n\t" \
  "v_smfmac_f32_16x16x64_bf16 v[48:51], v[24:27], v[88:95], v68\n\t" \
  "v_add_f32_dpp v37, v41, v37 quad_perm:[2,3,0,1] row_mask:0xf bank_mask:0xf\n\t" \
  "v_smfmac_f32_16x16x64_bf16 v[48:51], v[28:31], v[136:143], v68\n\t" \
  "v_add_f32_dpp v38, v42, v38 quad_perm:[2,3,0,1] row_mask:0xf bank_mask:0xf\n\t" \
  "v_smfmac_f32_16x16x64_bf16 v[48:51], v[32:35], v[176:183], v68\n\t" \
  "v_add_f32_dpp v39, v43, v39 quad_perm:[2,3,0,1] row_mask:0xf bank_mask:0xf\n\t" \
  "v_smfmac_f32_16x16x64_bf16 v[52:55], v[0:3], a[16:23], v68\n\t" \
  "s_mov_b64 exec, s[58:59]\n\t" \
  "ds_write_b128 v69, v[36:39] offset:2048\n\t" \
  "s_mov_b64 exec, -1\n\t" \
  "v_smfmac_f32_16x16x64_bf16 v[52:55], v[4:7], a[64:71], v68\n\t" \
  "v_add_f32 v75, v48, v50\n\t" \
  "v_add_f32 v49, v49, v51\n\t" \
  "v_cvt_pk_bf16_f32 v75, v75, v49\n\t" \
  "v_smfmac_f32_16x16x64_bf16 v[56:59], v[0:3], a[24:31], v68\n\t" \
  "ds_write_b32 v71, v75 offset:512\n\t" \
  "v_smfmac_f32_16x16x64_bf16 v[56:59], v[4:7], a[72:79], v68\n\t" \
  "ds_read_b64_tr_b16 v[0:1], v72 offset:10240\n\t" \
  "ds_read_b64_tr_b16 v[2:3], v72 offset:10752\n\t" \
  "v_smfmac_f32_16x16x64_bf16 v[52:55], v[8:11], a[104:111], v68\n\t" \
  "ds_read_b128 v[36:39], v70 offset:12288\n\t" \
  "v_smfmac_f32_16x16x64_bf16 v[56:59], v[8:11], a[112:119], v68\n\t" \
  "buffer_load_dword off, s[28:31], s87 sc1 lds\n\t" \
  "ds_read_b128 v[40:43], v70 offset:13312\n\t" \
  "s_waitcnt lgkmcnt(4)\n\t" \
  "s_barrier\n\t" \
  "ds_read_b64_tr_b16 v[44:45], v72 offset:0\n\t" \
  "ds_read_b64_tr_b16 v[46:47], v72 offset:512\n\t" \
  "v_smfmac_f32_16x16x64_bf16 v[52:55], v[12:15], a[144:151], v68\n\t" \
  "ds_read_b64_tr_b16 v[48:49], v72 offset:1024\n\t" \
  "ds_read_b64_tr_b16 v[50:51], v72 offset:1536\n\t" \
  "v_smfmac_f32_16x16x64_bf16 v[56:59], v[12:15], a[152:159], v68\n\t" \
  "v_smfmac_f32_16x16x64_bf16 v[52:55], v[16:19], a[192:199], v68\n\t" \
  "ds_read_b128 v[4:7], v69 offset:2048\n\t" \
  "s_waitcnt lgkmcnt(5)\n\t" \
  "v_smfmac_f32_16x16x64_bf16 v[56:59], v[16:19], v[36:43], v68\n\t" \
  "ds_read_b128 v[8:11], v69 offset:3072\n\t" \
  "s_waitcnt lgkmcnt(4)\n\t" \
  "v_mfma_f32_16x16x32_bf16 v[12:15], v[44:47], v[76:79], 0\n\t" \
  "ds_read_b128 v[36:39], v70 offset:14336\n\t" \
  "s_waitcnt lgkmcnt(3)\n\t" \
  "v_mfma_f32_16x16x32_bf16 v[12:15], v[48:51], v[0:3], v[12:15]\n\t" \
  "ds_read_b128 v[40:43], v70 offset:15360\n\t" \
  "v_smfmac_f32_16x16x64_bf16 v[52:55], v[20:23], a[224:231], v68\n\t" \
  "s_waitcnt lgkmcnt(2)\n\t" \
  "v_add_f32 v4, v4, v8\n\t" \
  "v_add_f32 v5, v5, v9\n\t" \
  "v_add_f32 v6, v6, v10\n\t" \
  "v_cndmask_b32 v78, v13, v14, s[56:57]\n\t" \
  "v_cndmask_b32 v76, v12, v15, s[56:57]\n\t" \
  "v_add_f32 v7, v7, v11\n\t" \
  "v_max3_f32 v77, v4, v5, v6\n\t" \
  "v_add_f32_dpp v76, v78, v76 quad_perm:[2,3,0,1] row_mask:0xf bank_mask:0xf\n\t" \
  "v_max3_f32 v77, v77, v7, v76\n\t" \
  "v_smfmac_f32_16x16x64_bf16 v[56:59], v[20:23], a[232:239], v68\n\t" \
  "ds_read_b128 v[44:47], v70 offset:16384\n\t" \
  "v_max_f32_dpp v77, v77, v77 quad_perm:[2,3,0,1] row_mask:0xf bank_mask:0xf\n\t" \
  "v_mov_b32 v78, v77\n\t" \
  "v_smfmac_f32_16x16x64_bf16 v[52:55], v[24:27], v[96:103], v68\n\t" \
  "ds_read_b128 v[48:51], v70 offset:17408\n\t" \
  "v_permlane16_swap_b32 v77, v78\n\t" \
  "v_max_f32 v77, v77, v78\n\t" \
  "v_mov_b32 v78, v77\n\t" \
  "v_smfmac_f32_16x16x64_bf16 v[52:55], v[28:31], v[144:151], v68\n\t" \
  "ds_read_b128 v[16:19], v70 offset:18432\n\t" \
  "v_permlane32_swap_b32 v77, v78\n\t" \
  "v_max_f32 v77, v77, v78\n\t" \
  "v_smfmac_f32_16x16x64_bf16 v[52:55], v[32:35], v[184:191], v68\n\t" \
  "ds_read_b128 v[20:23], v70 offset:19456\n\t" \
  "v_mul_f32 v77, 0xbe0293ee, v77\n\t" \
  "v_smfmac_f32_16x16x64_bf16 v[56:59], v[24:27], v[104:111], v68\n\t" \
  "v_fmamk_f32 v4, v4, 0x3e0293ee, v77\n\t" \
  "v_smfmac_f32_16x16x64_bf16 v[56:59], v[28:31], v[152:159], v68\n\t" \
  "ds_read_b128 v[24:27], v70 offset:22528\n\t" \
  "s_waitcnt lgkmcnt(4)\n\t" \
  "v_fmamk_f32 v5, v5, 0x3e0293ee, v77\n\t" \
  "v_smfmac_f32_16x16x64_bf16 v[56:59], v[32:35], v[36:43], v68\n\t" \
  "ds_read_b128 v[28:31], v70 offset:23552\n\t" \
  "v_fmamk_f32 v6, v6, 0x3e0293ee, v77\n\t" \
  "v_fmamk_f32 v7, v7, 0x3e0293ee, v77\n\t" \
  "v_fmamk_f32 v76, v76, 0x3e0293ee, v77\n\t" \
  "v_exp_f32 v4, v4\n\t" \
  "v_exp_f32 v5, v5\n\t" \
  "v_exp_f32 v6, v6\n\t" \
  "v_exp_f32 v7, v7\n\t" \
  "v_cvt_pk_bf16_f32 v4, v4, v5\n\t" \
  "v_exp_f32 v76, v76\n\t" \
  "v_cvt_pk_bf16_f32 v5, v6, v7\n\t" \
  "v_mov_b32_dpp v12, v4 quad_perm:[0,1,0,1] row_mask:0xf bank_mask:0xf\n\t" \
  "v_mov_b32_dpp v14, v4 quad_perm:[2,3,2,3] row_mask:0xf bank_mask:0xf\n\t" \
  "v_mov_b32_dpp v13, v5 quad_perm:[0,1,0,1] row_mask:0xf bank_mask:0xf\n\t" \
  "v_mov_b32_dpp v15, v5 quad_perm:[2,3,2,3] row_mask:0xf bank_mask:0xf\n\t" \
  "v_add_f32 v52, v52, v54\n\t" \
  "v_add_f32 v53, v53, v55\n\t" \
  "s_waitcnt vmcnt(2)\n\t" \
  "v_mfma_f32_16x16x32_bf16 v[0:3], v[60:63], v[12:15], 0\n\t" \
  "ds_read_b128 v[36:39], v70 offset:20480\n\t" \
  "v_add_f32 v56, v56, v58\n\t" \
  "v_add_f32 v57, v57, v59\n\t" \
  "v_cvt_pk_bf16_f32 v52, v52, v53\n\t" \
  "v_cvt_pk_bf16_f32 v53, v56, v57\n\t" \
  "s_waitcnt vmcnt(1)\n\t" \
  "v_mfma_f32_16x16x32_bf16 v[4:7], v[64:67], v[12:15], 0\n\t" \
  "buffer_store_dwordx2 v[52:53], v71, s[28:31], s69 offen offset:2048 sc1\n\t" \
  "ds_read_b128 v[40:43], v70 offset:21504\n\t" \
  "v_mov_b64 v[56:57], s[88:89]\n\t" \
  "v_mov_b64 v[58:59], s[88:89]\n\t" \
  "v_mov_b32_dpp v78, v76 quad_perm:[0,1,0,1] row_mask:0xf bank_mask:0xf\n\t" \
  "v_mov_b32_dpp v79, v76 quad_perm:[2,3,2,3] row_mask:0xf bank_mask:0xf\n\t" \
  "v_mfma_f32_16x16x32_bf16 v[32:35], v[56:59], v[12:15], 0\n\t" \
  "ds_read_b128 v[60:63], v70 offset:24576\n\t" \
  "v_cvt_pk_bf16_f32 v11, v78, v79\n\t" \
  "v_cvt_pk_bf16_f32 v8, v78, v79\n\t" \
  "v_mov_b64 v[54:55], 0\n\t" \
  "v_mov_b32 v9, 0\n\t" \
  "v_mov_b32 v10, 0\n\t" \
  "v_mov_b64 v[14:15], 0\n\t" \
  "v_mov_b64 v[12:13], 0\n\t" \
  "v_mfma_f32_16x16x32_bf16 v[32:35], v[54:57], v[8:11], v[32:35]\n\t" \
  "s_cmp_eq_u32 s48, 63\n\t" \
  "v_mfma_f32_16x16x32_bf16 v[0:3], v[52:55], v[8:11], v[0:3]\n\t" \
  "ds_read_b128 v[64:67], v70 offset:25600\n\t" \
  "v_mov_b64 v[56:57], 0\n\t" \
  "v_mov_b64 v[58:59], 0\n\t" \
  "v_mfma_f32_16x16x32_bf16 v[4:7], v[52:55], v[10:13], v[4:7]\n\t" \
  "s_cbranch_scc1 LLAST0_%=\n\t" \
  "v_rcp_f32 v74, v32\n\t" \
  "v_mov_b32 v11, 0\n\t" \
  "v_mov_b32 v8, 0\n\t" \
  "v_pk_mul_f32 v[0:1], v[0:1], v[74:75] op_sel_hi:[1,0]\n\t" \
  "v_pk_mul_f32 v[2:3], v[2:3], v[74:75] op_sel_hi:[1,0]\n\t" \
  "v_cvt_pk_bf16_f32 v76, v0, v1\n\t" \
  "v_cvt_pk_bf16_f32 v77, v2, v3\n\t" \
  "v_pk_mul_f32 v[4:5], v[4:5], v[74:75] op_sel_hi:[1,0]\n\t" \
  "v_pk_mul_f32 v[6:7], v[6:7], v[74:75] op_sel_hi:[1,0]\n\t" \
  "v_cvt_pk_bf16_f32 v78, v4, v5\n\t" \
  "v_cvt_pk_bf16_f32 v79, v6, v7\n\t" \
  "ds_write_b128 v69, v[76:79] offset:4096\n\t" \
  "s_waitcnt lgkmcnt(2)\n\t" \
  "v_smfmac_f32_16x16x64_bf16 v[12:15], v[76:79], v[44:51], v68\n\t" \
  "ds_read_b128 v[44:47], v70 offset:26624\n\t" \
  "v_smfmac_f32_16x16x64_bf16 v[56:59], v[76:79], v[16:23], v68\n\t" \
  "ds_read_b128 v[48:51], v70 offset:27648\n\t" \
  "v_mov_b64 v[52:53], 0\n\t" \
  "v_smfmac_f32_16x16x64_bf16 v[8:11], v[76:79], v[36:43], v68\n\t" \
  "ds_read_b128 v[16:19], v70 offset:28672\n\t" \
  "v_smfmac_f32_16x16x64_bf16 v[52:55], v[76:79], v[24:31], v68\n\t" \
  "s_waitcnt lgkmcnt(3)\n\t" \
  "s_barrier\n\t" \
  "ds_read_b128 v[0:3], v69 offset:5120\n\t" \
  "ds_read_b128 v[4:7], v69 offset:6144\n\t" \
  "v_lshrrev_b32 v71, 7, v69\n\t" \
  "v_and_b32 v74, 0x70, v69\n\t" \
  "v_lshl_add_u32 v71, v71, 14, v74\n\t" \
  "v_bfe_u32 v72, v69, 2, 7\n\t" \
  "v_and_b32 v74, 0x200, v69\n\t" \
  "s_waitcnt lgkmcnt(1)\n\t" \
  "v_smfmac_f32_16x16x64_bf16 v[8:11], v[0:3], v[60:67], v68\n\t" \
  "ds_read_b128 v[20:23], v70 offset:29696\n\t" \
  "v_mov_b64 v[76:77], 0\n\t" \
  "v_smfmac_f32_16x16x64_bf16 v[12:15], v[0:3], v[200:207], v68\n\t" \
  "ds_read_b128 v[32:35], v69 offset:7168\n\t" \
  "v_mov_b64 v[78:79], 0\n\t" \
  "v_smfmac_f32_16x16x64_bf16 v[52:55], v[0:3], v[44:51], v68\n\t" \
  "ds_read_b128 v[24:27], v70 offset:30720\n\t" \
  "v_lshl_add_u32 v72, v74, 3, v72\n\t" \
  "v_smfmac_f32_16x16x64_bf16 v[56:59], v[0:3], v[232:239], v68\n\t" \
  "buffer_load_dword v75, off, s[28:31], s80 sc1\n\t" \
  "ds_read_b128 v[28:31], v70 offset:31744\n\t" \
  "s_waitcnt lgkmcnt(4)\n\t" \
  "v_smfmac_f32_16x16x64_bf16 v[12:15], v[4:7], v[208:215], v68\n\t" \
  "ds_read_b128 v[60:63], v70 offset:32768\n\t" \
  "s_waitcnt lgkmcnt(4)\n\t" \
  "v_smfmac_f32_16x16x64_bf16 v[8:11], v[4:7], v[16:23], v68\n\t" \
  "ds_read_b128 v[64:67], v70 offset:33792\n\t" \
  "s_waitcnt lgkmcnt(4)\n\t" \
  "v_smfmac_f32_16x16x64_bf16 v[8:11], v[32:35], v[192:199], v68\n\t" \
  "ds_read_b128 v[36:39], v73 offset:4096\n\t" \
  "v_smfmac_f32_16x16x64_bf16 v[12:15], v[32:35], v[216:223], v68\n\t" \
  "ds_read_b128 v[40:43], v73 offset:6144\n\t" \
  "s_waitcnt lgkmcnt(4)\n\t" \
  "v_smfmac_f32_16x16x64_bf16 v[52:55], v[4:7], v[24:31], v68\n\t" \
  "ds_read_b128 v[44:47], v70 offset:34816\n\t" \
  "v_cvt_pk_bf16_f32 v8, v8, v9\n\t" \
  "v_smfmac_f32_16x16x64_bf16 v[52:55], v[32:35], v[224:231], v68\n\t" \
  "ds_read_b128 v[48:51], v70 offset:35840\n\t" \
  "v_cvt_pk_bf16_f32 v9, v10, v11\n\t" \
  "v_smfmac_f32_16x16x64_bf16 v[56:59], v[4:7], v[240:247], v68\n\t" \
  "s_waitcnt lgkmcnt(4)\n\t" \
  "v_cvt_pk_bf16_f32 v10, v12, v13\n\t" \
  "v_smfmac_f32_16x16x64_bf16 v[56:59], v[32:35], v[60:67], v68\n\t" \
  "s_waitcnt lgkmcnt(3)\n\t" \
  "v_cvt_pk_bf16_f32 v11, v14, v15\n\t" \
  "v_smfmac_f32_16x16x64_bf16 v[76:79], v[36:39], v[248:255], v68\n\t" \
  "buffer_store_dwordx4 v[8:11], v71, s[28:31], s54 offen sc1\n\t" \
  "s_waitcnt lgkmcnt(0)\n\t" \
  "v_cvt_pk_bf16_f32 v52, v52, v53\n\t" \
  "v_smfmac_f32_16x16x64_bf16 v[76:79], v[40:43], v[44:51], v68\n\t" \
  "v_cvt_pk_bf16_f32 v53, v54, v55\n\t" \
  "v_cvt_pk_bf16_f32 v54, v56, v57\n\t" \
  "v_cvt_pk_bf16_f32 v55, v58, v59\n\t" \
  "buffer_store_dwordx4 v[52:55], v71, s[28:31], s75 offen sc1\n\t" \
  "s_nop 2\n\t" \
  "v_add_f32 v76, v76, v78\n\t" \
  "v_add_f32 v77, v77, v79\n\t" \
  "v_cvt_pk_bf16_f32 v76, v76, v77\n\t" \
  "buffer_store_dword v76, v72, s[28:31], s55 offen sc1\n\t" \
  "s_memrealtime s[70:71]\n\t" \
  "s_waitcnt vmcnt(3)\n\t" \
  "v_readfirstlane_b32 s65, v75\n\t" \
  "s_barrier\n\t" \
  "s_waitcnt lgkmcnt(0)\n\t" \
  "s_not_b32 s65, s65\n\t" \
  "s_lshl_b32 s61, s70, 8\n\t" \
  "s_andn2_b32 s64, s65, 0xff\n\t" \
  "s_sub_u32 s64, s64, s61\n\t" \
  "s_ashr_i32 s64, s64, 8\n\t" \
  "s_cmp_eq_u32 s65, 0\n\t" \
  "s_cselect_b32 s64, " CH_DLF ", s64\n\t" \
  "s_cselect_b32 s65, " CH_B0 ", s65\n\t" \
  "s_add_u32 s47, s64, s70\n\t" \
  "s_min_i32 s64, s64, s34\n\t" \
  "s_max_i32 s64, s64, " CH_DLL1 "\n\t" \
  "s_add_u32 s64, s64, s70\n\t" \
  "s_add_u32 s55, s64, " CH_FDL "\n\t" \
  "s_movk_i32 s60, 8192\n\t" \
  "FWT_%=:\n\t" \
  "s_memrealtime s[62:63]\n\t" \
  "s_sub_u32 s60, s60, 1\n\t" \
  "s_cselect_b32 s61, 0, -1\n\t" \
  "s_waitcnt lgkmcnt(0)\n\t" \
  "s_sub_i32 s63, s62, s55\n\t" \
  "s_and_b32 s63, s63, s61\n\t" \
  "s_cmp_lt_i32 s63, 0\n\t" \
  "s_cbranch_scc1 FWT_%=\n\t" \
  "s_mov_b64 exec, 1\n\t" \
  "buffer_store_dword v68, off, s[28:31], s51 offset:4064 sc1\n\t" \
  "s_mov_b64 exec, -1\n\t" \
  "s_and_b32 s65, s65, 0xff\n\t" \
  "s_add_u32 s47, s47, s65\n\t" \
  "s_add_u32 s65, s65, s64\n\t" \
  "s_sub_u32 s60, s70, s66\n\t" \
  "s_add_u32 s61, s81, 1\n\t" \
  "s_min_u32 s60, s60, s61\n\t" \
  "s_cmp_eq_u32 s68, 0\n\t" \
  "s_cselect_b32 s81, s60, s81\n\t" \
  "s_add_u32 s48, s48, 1\n\t" \
  "s_mov_b32 s49, s53\n\t" \
  "s_add_u32 s53, s53, " CH_SLOT "\n\t" \
  "s_add_u32 s60, s53, " CH_SKIP "\n\t" \
  "s_lshr_b32 s61, s96, 1\n\t" \
  "s_add_u32 s86, s86, 1\n\t" \
  "s_cmp_eq_u32 s86, 3\n\t" \
  "s_cselect_b32 s53, s60, s53\n\t" \
  "s_cselect_b32 s96, s61, s96\n\t" \
  "s_cselect_b32 s86, 0, s86\n\t" \
  "s_cmp_ge_u32 s53, " CH_RING "\n\t" \
  "s_cselect_b32 s53, 0, s53\n\t" \
  "s_cselect_b32 s96, s21, s96\n\t" \
  "s_andn2_b32 s53, s53, 0x2000\n\t" \
  "s_and_b32 s60, s96, 1\n\t" \
  "s_lshl_b32 s60, s60, 13\n\t" \
  "s_or_b32 s53, s53, s60\n\t" \
  "s_mov_b32 s68, 0\n\t" \
  "s_add_u32 s82, s48, 0\n\t" \
  "s_and_b32 s82, s82, " CH_TRM "\n\t" \
  "s_lshr_b64 s[60:61], s[90:91], s82\n\t" \
  "s_and_b32 s60, s60, 1\n\t" \
  "s_lshl_b32 s60, s60, 13\n\t" \
  "s_add_u32 s82, s82, " CH_SG0 "\n\t" \
  "s_lshl_b32 s82, s82, 21\n\t" \
  "s_or_b32 s82, s82, s60\n\t" \
  "s_add_u32 s50, s82, s38\n\t" \
  "s_add_u32 s69, s82, s39\n\t" \
  "s_add_u32 s82, s48, 0\n\t" \
  "s_lshr_b64 s[60:61], s[90:91], s82\n\t" \
  "s_and_b32 s60, s60, 1\n\t" \
  "s_lshl_b32 s60, s60, 13\n\t" \
  "s_add_u32 s82, s82, " CH_SG0 "\n\t" \
  "s_lshl_b32 s82, s82, 21\n\t" \
  "s_or_b32 s82, s82, s60\n\t" \
  "s_add_u32 s51, s82, s43\n\t" \
  "s_cmp_lg_u32 s34, 0\n\t" \
  "s_cbranch_scc1 LW0_%=\n\t" \
  "s_barrier\n\t" \
  "s_branch LINI_%=\n\t" \
  "LFAIL0_%=:\n\t" \
  "v_max_u32 v75, v0, v1\n\t" \
  "v_max3_u32 v75, v75, v2, v3\n\t" \
  "v_max3_u32 v75, v75, v4, v5\n\t" \
  "v_max3_u32 v75, v75, v6, v7\n\t" \
  "v_max3_u32 v75, v75, v8, v9\n\t" \
  "v_max3_u32 v75, v75, v10, v11\n\t" \
  "v_max3_u32 v75, v75, v12, v13\n\t" \
  "v_max3_u32 v75, v75, v14, v15\n\t" \
  "v_max3_u32 v75, v75, v16, v17\n\t" \
  "v_max3_u32 v75, v75, v18, v19\n\t" \
  "v_max3_u32 v75, v75, v20, v21\n\t" \
  "v_max3_u32 v75, v75, v22, v23\n\t" \
  "v_max3_u32 v75, v75, v24, v25\n\t" \
  "v_max3_u32 v75, v75, v26, v27\n\t" \
  "v_max3_u32 v75, v75, v28, v29\n\t" \
  "v_max3_u32 v75, v75, v30, v31\n\t" \
  "v_max3_u32 v75, v75, v32, v33\n\t" \
  "v_max3_u32 v75, v75, v34, v35\n\t" \
  "v_cmp_eq_u32 vcc, -1, v75\n\t" \
  "s_cbranch_vccz LGO0_%=\n\t" \
  "s_add_u32 s68, s68, 1\n\t" \
  "s_waitcnt lgkmcnt(0)\n\t" \
  "s_cmp_gt_u32 s68, s83\n\t" \
  "s_cbranch_scc1 LGV0_%=\n\t" \
  "s_barrier\n\t" \
  "s_memrealtime s[62:63]\n\t" \
  "s_mov_b32 m0, 0\n\t" \
  "s_waitcnt lgkmcnt(0)\n\t" \
  "s_add_u32 s65, s62, " CH_PEN "\n\t" \
  "s_branch LDMA_%=\n\t" \
  "LWT_%=f:\n\t" \
  "s_cmp_lt_i32 s63, -2\n\t" \
  "s_cbranch_scc0 LWT_%=g\n\t" \
  "s_nop 4\n\t" \
  "s_branch LWT_%=g\n\t" \
  "LGV0_%=:\n\t" \
  "s_mov_b32 s83, 0\n\t" \
  "s_lshl_b32 s60, s36, 4\n\t" \
  "s_or_b32 s60, s60, s48\n\t" \
  "s_or_b32 s60, s60, 805306368\n\t" \
  "v_mov_b32 v76, s60\n\t" \
  "v_mov_b32 v77, 0\n\t" \
  "s_mov_b64 exec, 1\n\t" \
  "buffer_atomic_cmpswap v[76:77], off, s[28:31], s92 sc1\n\t" \
  "s_mov_b64 exec, -1\n\t" \
  "s_branch LGO0_%=\n\t" \
  "LLAST0_%=:\n\t" \
  "s_waitcnt vmcnt(0) lgkmcnt(0)\n\t" \
  "s_barrier\n\t" \
  "s_mov_b64 exec, 1\n\t" \
  "buffer_store_dword v68, off, s[28:31], s51 offset:4064 sc1\n\t" \
  "s_mov_b64 exec, -1\n\t" \
  "s_branch LEND_%=\n\t" \
  ".p2align 6\n\t" \
  "LG1_%=:\n\t" \
  "s_barrier\n\t" \
  "buffer_load_dwordx4 v[52:55], v69, s[28:31], s50 offen sc1\n\t" \
  "buffer_load_dwordx4 v[56:59], v69, s[28:31], s50 offen offset:1024 sc1\n\t" \
  "s_add_u32 s64, s50, 14336\n\t" \
  "s_movk_i32 m0, 11264\n\t" \
  "v_lshlrev_b32 v75, 1, v69\n\t" \
  "buffer_load_dword v75, s[28:31], s64 offen sc1 lds\n\t" \
  "ds_read_b128 v[44:47], v70 offset:0\n\t" \
  "ds_read_b128 v[48:51], v70 offset:1024\n\t" \
  "ds_read_b128 v[60:63], v70 offset:2048\n\t" \
  "ds_read_b128 v[64:67], v70 offset:3072\n\t" \
  "v_mov_b64 v[36:37], 0\n\t" \
  "v_mov_b64 v[38:39], 0\n\t" \
  "v_mov_b64 v[40:41], 0\n\t" \
  "v_mov_b64 v[42:43], 0\n\t" \
  "v_lshrrev_b32 v71, 1, v69\n\t" \
  "v_add_u32 v71, 4, v71\n\t" \
  "v_lshrrev_b32 v72, 3, v69\n\t" \
  "v_and_b32 v72, 0x78, v72\n\t" \
  "v_and_b32 v74, 0x30, v69\n\t" \
  "v_lshl_add_u32 v72, v74, 3, v72\n\t" \
  "s_add_u32 s54, s53, s36\n\t" \
  "s_add_u32 s55, s54, 540672\n\t" \
  "s_add_u32 s54, s54, 4096\n\t" \
  "s_add_u32 s75, s54, 262144\n\t" \
  "s_add_u32 s80, s53, s26\n\t" \
  ".p2align 5\n\t" \
  ".fill 2, 4, 0xbf800000\n\t" \
  "s_waitcnt vmcnt(9)\n\t" \
  "s_barrier\n\t" \
  "ds_read_b128 v[0:3], v69 offset:0\n\t" \
  "ds_read_b128 v[4:7], v69 offset:1024\n\t" \
  "ds_read_b128 v[8:11], v69 offset:2048\n\t" \
  "ds_read_b128 v[12:15], v69 offset:3072\n\t" \
  "s_waitcnt lgkmcnt(3)\n\t" \
  "v_smfmac_f32_16x16x64_bf16 v[36:39], v[0:3], v[44:51], v68\n\t" \
  "ds_read_b128 v[44:47], v70 offset:4096\n\t" \
  "v_smfmac_f32_16x16x64_bf16 v[40:43], v[0:3], v[60:67], v68\n\t" \
  "ds_read_b128 v[48:51], v70 offset:5120\n\t" \
  "s_waitcnt lgkmcnt(4)\n\t" \
  "v_smfmac_f32_16x16x64_bf16 v[36:39], v[4:7], a[32:39], v68\n\t" \
  "ds_read_b128 v[60:63], v70 offset:6144\n\t" \
  "v_smfmac_f32_16x16x64_bf16 v[40:43], v[4:7], a[40:47], v68\n\t" \
  "ds_read_b128 v[64:67], v70 offset:7168\n\t" \
  "s_waitcnt lgkmcnt(5)\n\t" \
  "v_smfmac_f32_16x16x64_bf16 v[36:39], v[8:11], a[80:87], v68\n\t" \
  "s_barrier\n\t" \
  "ds_read_b128 v[16:19], v69 offset:4096\n\t" \
  "v_smfmac_f32_16x16x64_bf16 v[40:43], v[8:11], a[88:95], v68\n\t" \
  "ds_read_b128 v[20:23], v69 offset:5120\n\t" \
  "s_waitcnt lgkmcnt(6)\n\t" \
  "v_smfmac_f32_16x16x64_bf16 v[36:39], v[12:15], a[120:127], v68\n\t" \
  "ds_read_b128 v[24:27], v69 offset:6144\n\t" \
  "s_waitcnt lgkmcnt(5)\n\t" \
  "v_smfmac_f32_16x16x64_bf16 v[40:43], v[12:15], v[44:51], v68\n\t" \
  "ds_read_b128 v[28:31], v69 offset:7168\n\t" \
  "s_waitcnt lgkmcnt(3)\n\t" \
  "v_smfmac_f32_16x16x64_bf16 v[36:39], v[16:19], a[160:167], v68\n\t" \
  "ds_read_b128 v[32:35], v69 offset:8192\n\t" \
  "v_smfmac_f32_16x16x64_bf16 v[40:43], v[16:19], a[168:175], v68\n\t" \
  "s_waitcnt lgkmcnt(3)\n\t" \
  "v_mov_b64 v[44:45], 0\n\t" \
  "v_smfmac_f32_16x16x64_bf16 v[36:39], v[20:23], a[200:207], v68\n\t" \
  "s_waitcnt lgkmcnt(2)\n\t" \
  "v_mov_b64 v[46:47], 0\n\t" \
  "v_smfmac_f32_16x16x64_bf16 v[36:39], v[24:27], a[240:247], v68\n\t" \
  "s_waitcnt lgkmcnt(1)\n\t" \
  "v_mov_b64 v[48:49], 0\n\t" \
  "v_smfmac_f32_16x16x64_bf16 v[36:39], v[28:31], v[112:119], v68\n\t" \
  "s_waitcnt lgkmcnt(0)\n\t" \
  "v_mov_b64 v[50:51], 0\n\t" \
  "v_smfmac_f32_16x16x64_bf16 v[36:39], v[32:35], v[160:167], v68\n\t" \
  "v_smfmac_f32_16x16x64_bf16 v[40:43], v[20:23], v[60:67], v68\n\t" \
  "ds_read_b128 v[60:63], v70 offset:8192\n\t" \
  "v_smfmac_f32_16x16x64_bf16 v[40:43], v[24:27], a[248:255], v68\n\t" \
  "ds_read_b128 v[64:67], v70 offset:9216\n\t" \
  "v_add_f32 v74, v36, v38\n\t" \
  "v_smfmac_f32_16x16x64_bf16 v[40:43], v[28:31], v[120:127], v68\n\t" \
  "v_add_f32 v37, v37, v39\n\t" \
  "v_smfmac_f32_16x16x64_bf16 v[40:43], v[32:35], v[168:175], v68\n\t" \
  "v_cmp_u_f32 vcc, v74, v37\n\t" \
  "v_smfmac_f32_16x16x64_bf16 v[44:47], v[0:3], a[0:7], v68\n\t" \
  "s_cbranch_vccnz LFAIL1_%=\n\t" \
  "LGO1_%=:\n\t" \
  "v_cvt_pk_bf16_f32 v74, v74, v37\n\t" \
  "v_smfmac_f32_16x16x64_bf16 v[44:47], v[4:7], a[48:55], v68\n\t" \
  "s_waitcnt lgkmcnt(0)\n\t" \
  "v_add_f32 v75, v40, v42\n\t" \
  "v_smfmac_f32_16x16x64_bf16 v[44:47], v[8:11], v[60:67], v68\n\t" \
  "ds_read_b128 v[60:63], v70 offset:10240\n\t" \
  "v_add_f32 v41, v41, v43\n\t" \
  "v_cvt_pk_bf16_f32 v75, v75, v41\n\t" \
  "v_smfmac_f32_16x16x64_bf16 v[44:47], v[12:15], a[128:135], v68\n\t" \
  "ds_write2st64_b32 v71, v74, v75 offset0:36 offset1:38\n\t" \
  "v_smfmac_f32_16x16x64_bf16 v[44:47], v[16:19], a[176:183], v68\n\t" \
  "ds_read_b128 v[64:67], v70 offset:11264\n\t" \
  "v_smfmac_f32_16x16x64_bf16 v[44:47], v[20:23], a[208:215], v68\n\t" \
  "v_smfmac_f32_16x16x64_bf16 v[44:47], v[24:27], v[80:87], v68\n\t" \
  "v_smfmac_f32_16x16x64_bf16 v[44:47], v[28:31], v[128:135], v68\n\t" \
  "s_waitcnt lgkmcnt(0)\n\t" \
  "v_smfmac_f32_16x16x64_bf16 v[44:47], v[32:35], v[60:67], v68\n\t" \
  "s_barrier\n\t" \
  "ds_read_b64_tr_b16 v[76:77], v72 offset:9216\n\t" \
  "v_smfmac_f32_16x16x64_bf16 v[48:51], v[0:3], a[8:15], v68\n\t" \
  "buffer_load_dwordx4 v[60:63], v69, s[28:31], s64 offen sc1\n\t" \
  "ds_read_b64_tr_b16 v[78:79], v72 offset:9728\n\t" \
  "v_smfmac_f32_16x16x64_bf16 v[48:51], v[4:7], a[56:63], v68\n\t" \
  "v_add_f32 v74, v44, v46\n\t" \
  "v_smfmac_f32_16x16x64_bf16 v[48:51], v[8:11], a[96:103], v68\n\t" \
  "v_add_f32 v45, v45, v47\n\t" \
  "v_smfmac_f32_16x16x64_bf16 v[48:51], v[12:15], a[136:143], v68\n\t" \
  "s_waitcnt vmcnt(3)\n\t" \
  "s_waitcnt lgkmcnt(0)\n\t" \
  "v_cvt_pk_bf16_f32 v74, v74, v45\n\t" \
  "v_mfma_f32_16x16x32_bf16 v[36:39], v[52:55], v[76:79], 0\n\t" \
  "s_waitcnt vmcnt(2)\n\t" \
  "v_mfma_f32_16x16x32_bf16 v[40:43], v[56:59], v[76:79], 0\n\t" \
  "buffer_load_dwordx4 v[64:67], v69, s[28:31], s64 offen offset:1024 sc1\n\t" \
  "v_mov_b64 v[52:53], 0\n\t" \
  "v_mov_b64 v[54:55], 0\n\t" \
  "v_smfmac_f32_16x16x64_bf16 v[48:51], v[16:19], a[184:191], v68\n\t" \
  "v_mov_b64 v[56:57], 0\n\t" \
  "v_mov_b64 v[58:59], 0\n\t" \
  "v_smfmac_f32_16x16x64_bf16 v[48:51], v[20:23], a[216:223], v68\n\t" \
  "ds_write_b32 v71, v74 offset:0\n\t" \
  "v_add_f32_dpp v36, v40, v36 quad_perm:[2,3,0,1] row_mask:0xf bank_mask:0xf\n\t" \
  "v_smfmac_f32_16x16x64_bf16 v[48:51], v[24:27], v[88:95], v68\n\t" \
  "v_add_f32_dpp v37, v41, v37 quad_perm:[2,3,0,1] row_mask:0xf bank_mask:0xf\n\t" \
  "v_smfmac_f32_16x16x64_bf16 v[48:51], v[28:31], v[136:143], v68\n\t" \
  "v_add_f32_dpp v38, v42, v38 quad_perm:[2,3,0,1] row_mask:0xf bank_mask:0xf\n\t" \
  "v_smfmac_f32_16x16x64_bf16 v[48:51], v[32:35], v[176:183], v68\n\t" \
  "v_add_f32_dpp v39, v43, v39 quad_perm:[2,3,0,1] row_mask:0xf bank_mask:0xf\n\t" \
  "v_smfmac_f32_16x16x64_bf16 v[52:55], v[0:3], a[16:23], v68\n\t" \
  "s_mov_b64 exec, s[58:59]\n\t" \
  "ds_write_b128 v69, v[36:39] offset:2080\n\t" \
  "s_mov_b64 exec, -1\n\t" \
  "v_smfmac_f32_16x16x64_bf16 v[52:55], v[4:7], a[64:71], v68\n\t" \
  "v_add_f32 v75, v48, v50\n\t" \
  "v_add_f32 v49, v49, v51\n\t" \
  "v_cvt_pk_bf16_f32 v75, v75, v49\n\t" \
  "v_smfmac_f32_16x16x64_bf16 v[56:59], v[0:3], a[24:31], v68\n\t" \
  "ds_write_b32 v71, v75 offset:512\n\t" \
  "v_smfmac_f32_16x16x64_bf16 v[56:59], v[4:7], a[72:79], v68\n\t" \
  "ds_read_b64_tr_b16 v[0:1], v72 offset:10240\n\t" \
  "ds_read_b64_tr_b16 v[2:3], v72 offset:10752\n\t" \
  "v_smfmac_f32_16x16x64_bf16 v[52:55], v[8:11], a[104:111], v68\n\t" \
  "ds_read_b128 v[36:39], v70 offset:12288\n\t" \
  "v_smfmac_f32_16x16x64_bf16 v[56:59], v[8:11], a[112:119], v68\n\t" \
  "ds_read_b128 v[40:43], v70 offset:13312\n\t" \
  "s_waitcnt lgkmcnt(4)\n\t" \
  "s_barrier\n\t" \
  "ds_read_b64_tr_b16 v[44:45], v72 offset:0\n\t" \
  "ds_read_b64_tr_b16 v[46:47], v72 offset:512\n\t" \
  "v_smfmac_f32_16x16x64_bf16 v[52:55], v[12:15], a[144:151], v68\n\t" \
  "ds_read_b64_tr_b16 v[48:49], v72 offset:1024\n\t" \
  "ds_read_b64_tr_b16 v[50:51], v72 offset:1536\n\t" \
  "v_smfmac_f32_16x16x64_bf16 v[56:59], v[12:15], a[152:159], v68\n\t" \
  "v_smfmac_f32_16x16x64_bf16 v[52:55], v[16:19], a[192:199], v68\n\t" \
  "ds_read_b128 v[4:7], v69 offset:2048\n\t" \
  "s_waitcnt lgkmcnt(5)\n\t" \
  "v_smfmac_f32_16x16x64_bf16 v[56:59], v[16:19], v[36:43], v68\n\t" \
  "ds_read_b128 v[8:11], v69 offset:3072\n\t" \
  "s_waitcnt lgkmcnt(4)\n\t" \
  "v_mfma_f32_16x16x32_bf16 v[12:15], v[44:47], v[76:79], 0\n\t" \
  "ds_read_b128 v[36:39], v70 offset:14336\n\t" \
  "s_waitcnt lgkmcnt(3)\n\t" \
  "v_mfma_f32_16x16x32_bf16 v[12:15], v[48:51], v[0:3], v[12:15]\n\t" \
  "buffer_store_dwordx4 v[44:47], v69, s[28:31], s69 offen offset:0 sc1\n\t" \
  "ds_read_b128 v[40:43], v70 offset:15360\n\t" \
  "v_smfmac_f32_16x16x64_bf16 v[52:55], v[20:23], a[224:231], v68\n\t" \
  "s_waitcnt lgkmcnt(2)\n\t" \
  "v_add_f32 v4, v4, v8\n\t" \
  "v_add_f32 v5, v5, v9\n\t" \
  "v_add_f32 v6, v6, v10\n\t" \
  "v_cndmask_b32 v78, v13, v14, s[56:57]\n\t" \
  "v_cndmask_b32 v76, v12, v15, s[56:57]\n\t" \
  "v_add_f32 v7, v7, v11\n\t" \
  "v_max3_f32 v77, v4, v5, v6\n\t" \
  "v_add_f32_dpp v76, v78, v76 quad_perm:[2,3,0,1] row_mask:0xf bank_mask:0xf\n\t" \
  "v_max3_f32 v77, v77, v7, v76\n\t" \
  "v_smfmac_f32_16x16x64_bf16 v[56:59], v[20:23], a[232:239], v68\n\t" \
  "ds_read_b128 v[44:47], v70 offset:16384\n\t" \
  "v_max_f32_dpp v77, v77, v77 quad_perm:[2,3,0,1] row_mask:0xf bank_mask:0xf\n\t" \
  "v_mov_b32 v78, v77\n\t" \
  "v_smfmac_f32_16x16x64_bf16 v[52:55], v[24:27], v[96:103], v68\n\t" \
  "ds_read_b128 v[48:51], v70 offset:17408\n\t" \
  "v_permlane16_swap_b32 v77, v78\n\t" \
  "v_max_f32 v77, v77, v78\n\t" \
  "v_mov_b32 v78, v77\n\t" \
  "v_smfmac_f32_16x16x64_bf16 v[52:55], v[28:31], v[144:151], v68\n\t" \
  "ds_read_b128 v[16:19], v70 offset:18432\n\t" \
  "v_permlane32_swap_b32 v77, v78\n\t" \
  "v_max_f32 v77, v77, v78\n\t" \
  "v_smfmac_f32_16x16x64_bf16 v[52:55], v[32:35], v[184:191], v68\n\t" \
  "ds_read_b128 v[20:23], v70 offset:19456\n\t" \
  "v_mul_f32 v77, 0xbe0293ee, v77\n\t" \
  "v_smfmac_f32_16x16x64_bf16 v[56:59], v[24:27], v[104:111], v68\n\t" \
  "v_fmamk_f32 v4, v4, 0x3e0293ee, v77\n\t" \
  "v_smfmac_f32_16x16x64_bf16 v[56:59], v[28:31], v[152:159], v68\n\t" \
  "ds_read_b128 v[24:27], v70 offset:22528\n\t" \
  "s_waitcnt lgkmcnt(4)\n\t" \
  "v_fmamk_f32 v5, v5, 0x3e0293ee, v77\n\t" \
  "v_smfmac_f32_16x16x64_bf16 v[56:59], v[32:35], v[36:43], v68\n\t" \
  "ds_read_b128 v[28:31], v70 offset:23552\n\t" \
  "v_fmamk_f32 v6, v6, 0x3e0293ee, v77\n\t" \
  "v_fmamk_f32 v7, v7, 0x3e0293ee, v77\n\t" \
  "v_fmamk_f32 v76, v76, 0x3e0293ee, v77\n\t" \
  "v_exp_f32 v4, v4\n\t" \
  "v_exp_f32 v5, v5\n\t" \
  "v_exp_f32 v6, v6\n\t" \
  "v_exp_f32 v7, v7\n\t" \
  "v_cvt_pk_bf16_f32 v4, v4, v5\n\t" \
  "v_exp_f32 v76, v76\n\t" \
  "v_cvt_pk_bf16_f32 v5, v6, v7\n\t" \
  "v_mov_b32_dpp v12, v4 quad_perm:[0,1,0,1] row_mask:0xf bank_mask:0xf\n\t" \
  "v_mov_b32_dpp v14, v4 quad_perm:[2,3,2,3] row_mask:0xf bank_mask:0xf\n\t" \
  "v_mov_b32_dpp v13, v5 quad_perm:[0,1,0,1] row_mask:0xf bank_mask:0xf\n\t" \
  "v_mov_b32_dpp v15, v5 quad_perm:[2,3,2,3] row_mask:0xf bank_mask:0xf\n\t" \
  "v_add_f32 v52, v52, v54\n\t" \
  "v_add_f32 v53, v53, v55\n\t" \
  "s_waitcnt vmcnt(2)\n\t" \
  "v_mfma_f32_16x16x32_bf16 v[0:3], v[60:63], v[12:15], 0\n\t" \
  "ds_read_b128 v[36:39], v70 offset:20480\n\t" \
  "v_add_f32 v56, v56, v58\n\t" \
  "v_add_f32 v57, v57, v59\n\t" \
  "v_cvt_pk_bf16_f32 v52, v52, v53\n\t" \
  "v_cvt_pk_bf16_f32 v53, v56, v57\n\t" \
  "s_waitcnt vmcnt(1)\n\t" \
  "v_mfma_f32_16x16x32_bf16 v[4:7], v[64:67], v[12:15], 0\n\t" \
  "ds_read_b128 v[40:43], v70 offset:21504\n\t" \
  "v_mov_b64 v[56:57], s[88:89]\n\t" \
  "v_mov_b64 v[58:59], s[88:89]\n\t" \
  "v_mov_b32_dpp v78, v76 quad_perm:[0,1,0,1] row_mask:0xf bank_mask:0xf\n\t" \
  "v_mov_b32_dpp v79, v76 quad_perm:[2,3,2,3] row_mask:0xf bank_mask:0xf\n\t" \
  "v_mfma_f32_16x16x32_bf16 v[32:35], v[56:59], v[12:15], 0\n\t" \
  "buffer_store_dwordx2 v[52:53], v71, s[28:31], s69 offen offset:2556 sc1\n\t" \
  "ds_read_b128 v[60:63], v70 offset:24576\n\t" \
  "v_cvt_pk_bf16_f32 v11, v78, v79\n\t" \
  "v_cvt_pk_bf16_f32 v8, v78, v79\n\t" \
  "v_mov_b64 v[54:55], 0\n\t" \
  "v_mov_b32 v9, 0\n\t" \
  "v_mov_b32 v10, 0\n\t" \
  "v_mov_b64 v[14:15], 0\n\t" \
  "v_mov_b64 v[12:13], 0\n\t" \
  "v_mfma_f32_16x16x32_bf16 v[32:35], v[54:57], v[8:11], v[32:35]\n\t" \
  "s_cmp_eq_u32 s48, 63\n\t" \
  "v_mfma_f32_16x16x32_bf16 v[0:3], v[52:55], v[8:11], v[0:3]\n\t" \
  "ds_read_b128 v[64:67], v70 offset:25600\n\t" \
  "v_mov_b64 v[56:57], 0\n\t" \
  "v_mov_b64 v[58:59], 0\n\t" \
  "v_mfma_f32_16x16x32_bf16 v[4:7], v[52:55], v[10:13], v[4:7]\n\t" \
  "s_cbranch_scc1 LLAST1_%=\n\t" \
  "v_rcp_f32 v74, v32\n\t" \
  "v_mov_b32 v11, 0\n\t" \
  "v_mov_b32 v8, 0\n\t" \
  "v_pk_mul_f32 v[0:1], v[0:1], v[74:75] op_sel_hi:[1,0]\n\t" \
  "v_pk_mul_f32 v[2:3], v[2:3], v[74:75] op_sel_hi:[1,0]\n\t" \
  "v_cvt_pk_bf16_f32 v76, v0, v1\n\t" \
  "v_cvt_pk_bf16_f32 v77, v2, v3\n\t" \
  "v_pk_mul_f32 v[4:5], v[4:5], v[74:75] op_sel_hi:[1,0]\n\t" \
  "v_pk_mul_f32 v[6:7], v[6:7], v[74:75] op_sel_hi:[1,0]\n\t" \
  "v_cvt_pk_bf16_f32 v78, v4, v5\n\t" \
  "v_cvt_pk_bf16_f32 v79, v6, v7\n\t" \
  "ds_write_b128 v69, v[76:79] offset:5120\n\t" \
  "s_waitcnt lgkmcnt(2)\n\t" \
  "v_smfmac_f32_16x16x64_bf16 v[12:15], v[76:79], v[44:51], v68\n\t" \
  "ds_read_b128 v[44:47], v70 offset:26624\n\t" \
  "v_smfmac_f32_16x16x64_bf16 v[56:59], v[76:79], v[16:23], v68\n\t" \
  "ds_read_b128 v[48:51], v70 offset:27648\n\t" \
  "v_mov_b64 v[52:53], 0\n\t" \
  "v_smfmac_f32_16x16x64_bf16 v[8:11], v[76:79], v[36:43], v68\n\t" \
  "ds_read_b128 v[16:19], v70 offset:28672\n\t" \
  "v_smfmac_f32_16x16x64_bf16 v[52:55], v[76:79], v[24:31], v68\n\t" \
  "s_waitcnt lgkmcnt(3)\n\t" \
  "s_barrier\n\t" \
  "ds_read_b128 v[0:3], v69 offset:4096\n\t" \
  "ds_read_b128 v[4:7], v69 offset:7168\n\t" \
  "v_lshrrev_b32 v71, 7, v69\n\t" \
  "v_and_b32 v74, 0x70, v69\n\t" \
  "v_lshl_add_u32 v71, v71, 14, v74\n\t" \
  "v_bfe_u32 v72, v69, 2, 7\n\t" \
  "v_and_b32 v74, 0x200, v69\n\t" \
  "s_waitcnt lgkmcnt(1)\n\t" \
  "v_smfmac_f32_16x16x64_bf16 v[8:11], v[0:3], v[60:67], v68\n\t" \
  "buffer_load_dword v75, off, s[28:31], s80 sc1\n\t" \
  "ds_read_b128 v[20:23], v70 offset:29696\n\t" \
  "v_mov_b64 v[76:77], 0\n\t" \
  "v_smfmac_f32_16x16x64_bf16 v[12:15], v[0:3], v[200:207], v68\n\t" \
  "ds_read_b128 v[32:35], v69 offset:6144\n\t" \
  "v_mov_b64 v[78:79], 0\n\t" \
  "v_smfmac_f32_16x16x64_bf16 v[52:55], v[0:3], v[44:51], v68\n\t" \
  "ds_read_b128 v[24:27], v70 offset:30720\n\t" \
  "v_lshl_add_u32 v72, v74, 3, v72\n\t" \
  "v_smfmac_f32_16x16x64_bf16 v[56:59], v[0:3], v[232:239], v68\n\t" \
  "ds_read_b128 v[28:31], v70 offset:31744\n\t" \
  "s_waitcnt lgkmcnt(4)\n\t" \
  "v_smfmac_f32_16x16x64_bf16 v[12:15], v[4:7], v[208:215], v68\n\t" \
  "ds_read_b128 v[60:63], v70 offset:32768\n\t" \
  "s_waitcnt lgkmcnt(4)\n\t" \
  "v_smfmac_f32_16x16x64_bf16 v[8:11], v[4:7], v[16:23], v68\n\t" \
  "ds_read_b128 v[64:67], v70 offset:33792\n\t" \
  "s_waitcnt lgkmcnt(4)\n\t" \
  "v_smfmac_f32_16x16x64_bf16 v[8:11], v[32:35], v[192:199], v68\n\t" \
  "ds_read_b128 v[36:39], v73 offset:4096\n\t" \
  "v_smfmac_f32_16x16x64_bf16 v[12:15], v[32:35], v[216:223], v68\n\t" \
  "ds_read_b128 v[40:43], v73 offset:6144\n\t" \
  "s_waitcnt lgkmcnt(4)\n\t" \
  "v_smfmac_f32_16x16x64_bf16 v[52:55], v[4:7], v[24:31], v68\n\t" \
  "ds_read_b128 v[44:47], v70 offset:34816\n\t" \
  "v_cvt_pk_bf16_f32 v8, v8, v9\n\t" \
  "v_smfmac_f32_16x16x64_bf16 v[52:55], v[32:35], v[224:231], v68\n\t" \
  "ds_read_b128 v[48:51], v70 offset:35840\n\t" \
  "v_cvt_pk_bf16_f32 v9, v10, v11\n\t" \
  "v_smfmac_f32_16x16x64_bf16 v[56:59], v[4:7], v[240:247], v68\n\t" \
  "s_waitcnt lgkmcnt(4)\n\t" \
  "v_cvt_pk_bf16_f32 v10, v12, v13\n\t" \
  "v_smfmac_f32_16x16x64_bf16 v[56:59], v[32:35], v[60:67], v68\n\t" \
  "s_waitcnt lgkmcnt(3)\n\t" \
  "v_cvt_pk_bf16_f32 v11, v14, v15\n\t" \
  "v_smfmac_f32_16x16x64_bf16 v[76:79], v[36:39], v[248:255], v68\n\t" \
  "buffer_store_dwordx4 v[8:11], v71, s[28:31], s54 offen sc1\n\t" \
  "s_waitcnt lgkmcnt(0)\n\t" \
  "v_cvt_pk_bf16_f32 v52, v52, v53\n\t" \
  "v_smfmac_f32_16x16x64_bf16 v[76:79], v[40:43], v[44:51], v68\n\t" \
  "v_cvt_pk_bf16_f32 v53, v54, v55\n\t" \
  "v_cvt_pk_bf16_f32 v54, v56, v57\n\t" \
  "v_cvt_pk_bf16_f32 v55, v58, v59\n\t" \
  "buffer_store_dwordx4 v[52:55], v71, s[28:31], s75 offen sc1\n\t" \
  "s_nop 2\n\t" \
  "v_add_f32 v76, v76, v78\n\t" \
  "v_add_f32 v77, v77, v79\n\t" \
  "v_cvt_pk_bf16_f32 v76, v76, v77\n\t" \
  "buffer_store_dword v76, v72, s[28:31], s55 offen sc1\n\t" \
  "s_memrealtime s[70:71]\n\t" \
  "s_waitcnt vmcnt(3)\n\t" \
  "v_readfirstlane_b32 s65, v75\n\t" \
  "s_barrier\n\t" \
  "s_waitcnt lgkmcnt(0)\n\t" \
  "s_not_b32 s65, s65\n\t" \
  "s_lshl_b32 s61, s70, 8\n\t" \
  "s_andn2_b32 s64, s65, 0xff\n\t" \
  "s_sub_u32 s64, s64, s61\n\t" \
  "s_ashr_i32 s64, s64, 8\n\t" \
  "s_cmp_eq_u32 s65, 0\n\t" \
  "s_cselect_b32 s64, " CH_DLF ", s64\n\t" \
  "s_sub_i32 s64, s64, " CH_RO1 "\n\t" \
  "s_min_i32 s64, s64, s34\n\t" \
  "s_max_i32 s64, s64, " CH_DLL1 "\n\t" \
  "s_add_u32 s64, s64, s70\n\t" \
  "s_mov_b32 s74, 0\n\t" \
  "s_movk_i32 s60, 8192\n\t" \
  "RWT1_%=:\n\t" \
  "s_memrealtime s[72:73]\n\t" \
  "s_sub_u32 s60, s60, 1\n\t" \
  "s_cselect_b32 s61, 0, -1\n\t" \
  "s_waitcnt lgkmcnt(0)\n\t" \
  "s_sub_i32 s73, s72, s64\n\t" \
  "s_and_b32 s73, s73, s61\n\t" \
  "s_cmp_lt_i32 s73, " CH_FIN "\n\t" \
  "s_cbranch_scc1 RWT1_%=\n\t" \
  "s_cmp_lt_i32 s73, 0\n\t" \
  "s_cbranch_scc1 RWT1_%=f\n\t" \
  "RWT1_%=g:\n\t" \
  "RRETRY1_%=:\n\t" \
  "buffer_load_dwordx4 v[0:3], v69, s[28:31], s97 offen offset:0 sc1\n\t" \
  "buffer_load_dwordx4 v[4:7], v69, s[28:31], s97 offen offset:1024 sc1\n\t" \
  "buffer_load_dwordx4 v[8:11], v69, s[28:31], s97 offen offset:2048 sc1\n\t" \
  "buffer_load_dwordx4 v[12:15], v69, s[28:31], s97 offen offset:3072 sc1\n\t" \
  "v_lshrrev_b32 v20, 2, v69\n\t" \
  "v_and_b32 v20, 16, v20\n\t" \
  "v_mov_b32 v21, 0x3f80\n\t" \
  "v_lshlrev_b32 v21, v20, v21\n\t" \
  "v_bfe_u32 v20, v69, 4, 2\n\t" \
  "v_cmp_eq_u32 vcc, 0, v20\n\t" \
  "s_nop 1\n\t" \
  "v_cndmask_b32 v32, 0, v21, vcc\n\t" \
  "v_cmp_eq_u32 vcc, 1, v20\n\t" \
  "s_nop 1\n\t" \
  "v_cndmask_b32 v33, 0, v21, vcc\n\t" \
  "v_cmp_eq_u32 vcc, 2, v20\n\t" \
  "s_nop 1\n\t" \
  "v_cndmask_b32 v34, 0, v21, vcc\n\t" \
  "v_cmp_eq_u32 vcc, 3, v20\n\t" \
  "s_nop 1\n\t" \
  "v_cndmask_b32 v35, 0, v21, vcc\n\t" \
  "v_bfe_u32 v38, v69, 7, 1\n\t" \
  "v_bfe_u32 v20, v69, 9, 1\n\t" \
  "v_lshl_add_u32 v38, v20, 2, v38\n\t" \
  "v_lshlrev_b32 v38, 12, v38\n\t" \
  "v_and_b32 v20, 0x60, v69\n\t" \
  "v_add_u32 v38, v38, v20\n\t" \
  "v_bfe_u32 v20, v69, 8, 1\n\t" \
  "v_lshl_add_u32 v38, v20, 4, v38\n\t" \
  "v_bfe_u32 v20, v69, 4, 1\n\t" \
  "v_lshl_add_u32 v38, v20, 3, v38\n\t" \
  "v_mov_b32 v44, 0\n\t" \
  CH_ALD \
  "s_waitcnt vmcnt(3)\n\t" \
  "v_mfma_f32_16x16x32_bf16 v[40:43], v[32:35], v[0:3], 0\n\t" \
  "v_max3_u32 v44, v44, v0, v1\n\t" \
  "v_max3_u32 v44, v44, v2, v3\n\t" \
  "s_waitcnt vmcnt(2)\n\t" \
  "v_mfma_f32_16x16x32_bf16 v[40:43], v[32:35], v[4:7], v[40:43]\n\t" \
  "v_max3_u32 v44, v44, v4, v5\n\t" \
  "v_max3_u32 v44, v44, v6, v7\n\t" \
  "s_waitcnt vmcnt(1)\n\t" \
  "v_mfma_f32_16x16x32_bf16 v[40:43], v[32:35], v[8:11], v[40:43]\n\t" \
  "v_max3_u32 v44, v44, v8, v9\n\t" \
  "v_max3_u32 v44, v44, v10, v11\n\t" \
  "s_waitcnt vmcnt(0)\n\t" \
  "v_mfma_f32_16x16x32_bf16 v[40:43], v[32:35], v[12:15], v[40:43]\n\t" \
  "v_max3_u32 v44, v44, v12, v13\n\t" \
  "v_max3_u32 v44, v44, v14, v15\n\t" \
  "v_cmp_eq_u32 vcc, -1, v44\n\t" \
  "s_cbranch_vccnz RFAIL1_%=\n\t" \
  "s_nop 3\n\t" \
  "v_add_f32_dpp v40, v40, v40 row_ror:8 row_mask:0xf bank_mask:0xf\n\t" \
  "v_add_f32_dpp v41, v41, v41 row_ror:8 row_mask:0xf bank_mask:0xf\n\t" \
  "v_add_f32_dpp v42, v42, v42 row_ror:8 row_mask:0xf bank_mask:0xf\n\t" \
  "v_add_f32_dpp v43, v43, v43 row_ror:8 row_mask:0xf bank_mask:0xf\n\t" \
  "v_cvt_pk_bf16_f32 v40, v40, v41\n\t" \
  "v_cvt_pk_bf16_f32 v41, v42, v43\n\t" \
  "buffer_store_dwordx2 v[40:41], v38, s[28:31], s98 offen sc1\n\t" \
  "RDONE1_%=:\n\t" \
  "s_barrier\n\t" \
  "s_add_u32 s48, s48, 1\n\t" \
  "s_mov_b32 s49, s53\n\t" \
  "s_add_u32 s53, s53, " CH_SLOT "\n\t" \
  "s_add_u32 s60, s53, " CH_SKIP "\n\t" \
  "s_lshr_b32 s61, s96, 1\n\t" \
  "s_add_u32 s86, s86, 1\n\t" \
  "s_cmp_eq_u32 s86, 3\n\t" \
  "s_cselect_b32 s53, s60, s53\n\t" \
  "s_cselect_b32 s96, s61, s96\n\t" \
  "s_cselect_b32 s86, 0, s86\n\t" \
  "s_cmp_ge_u32 s53, " CH_RING "\n\t" \
  "s_cselect_b32 s53, 0, s53\n\t" \
  "s_cselect_b32 s96, s21, s96\n\t" \
  "s_andn2_b32 s53, s53, 0x2000\n\t" \
  "s_and_b32 s60, s96, 1\n\t" \
  "s_lshl_b32 s60, s60, 13\n\t" \
  "s_or_b32 s53, s53, s60\n\t" \
  "s_mov_b32 s68, 0\n\t" \
  "s_add_u32 s97, s41, s53\n\t" \
  "s_add_u32 s98, s37, s53\n\t" \
  "s_add_u32 s82, s48, 0\n\t" \
  "s_and_b32 s82, s82, " CH_TRM "\n\t" \
  "s_lshr_b64 s[60:61], s[90:91], s82\n\t" \
  "s_and_b32 s60, s60, 1\n\t" \
  "s_lshl_b32 s60, s60, 13\n\t" \
  "s_add_u32 s82, s82, " CH_SG0 "\n\t" \
  "s_lshl_b32 s82, s82, 21\n\t" \
  "s_or_b32 s82, s82, s60\n\t" \
  "s_add_u32 s50, s82, s38\n\t" \
  "s_add_u32 s69, s82, s39\n\t" \
  "s_cmp_eq_u32 s34, 0\n\t" \
  "s_cbranch_scc1 LINI_%=\n\t" \
  "s_branch LG1_%=\n\t" \
  "RFAIL1_%=:\n\t" \
  "s_add_u32 s74, s74, 1\n\t" \
  "s_sleep " CH_RSL "\n\t" \
  "s_waitcnt vmcnt(0)\n\t" \
  "s_cmp_gt_u32 s74, s84\n\t" \
  "s_cbranch_scc1 RG1_%=\n\t" \
  "s_and_b32 s60, s74, 1023\n\t" \
  "s_cmp_lg_u32 s60, 0\n\t" \
  "s_cbranch_scc1 RRETRY1_%=\n\t" \
  "buffer_load_dword v46, off, s[28:31], s92 sc1\n\t" \
  "s_waitcnt vmcnt(0)\n\t" \
  "v_readfirstlane_b32 s60, v46\n\t" \
  "s_nop 0\n\t" \
  "s_cmp_eq_u32 s60, 0\n\t" \
  "s_cbranch_scc1 RRETRY1_%=\n\t" \
  "RG1_%=:\n\t" \
  "s_mov_b32 s84, 0\n\t" \
  "s_lshl_b32 s60, s36, 4\n\t" \
  "s_or_b32 s60, s60, s48\n\t" \
  "s_or_b32 s60, s60, 536870912\n\t" \
  "v_mov_b32 v76, s60\n\t" \
  "v_mov_b32 v77, 0\n\t" \
  "s_mov_b64 exec, 1\n\t" \
  "buffer_atomic_cmpswap v[76:77], off, s[28:31], s92 sc1\n\t" \
  "s_mov_b64 exec, -1\n\t" \
  "s_branch RDONE1_%=\n\t" \
  "LFAIL1_%=:\n\t" \
  "v_max_u32 v75, v0, v1\n\t" \
  "v_max3_u32 v75, v75, v2, v3\n\t" \
  "v_max3_u32 v75, v75, v4, v5\n\t" \
  "v_max3_u32 v75, v75, v6, v7\n\t" \
  "v_max3_u32 v75, v75, v8, v9\n\t" \
  "v_max3_u32 v75, v75, v10, v11\n\t" \
  "v_max3_u32 v75, v75, v12, v13\n\t" \
  "v_max3_u32 v75, v75, v14, v15\n\t" \
  "v_max3_u32 v75, v75, v16, v17\n\t" \
  "v_max3_u32 v75, v75, v18, v19\n\t" \
  "v_max3_u32 v75, v75, v20, v21\n\t" \
  "v_max3_u32 v75, v75, v22, v23\n\t" \
  "v_max3_u32 v75, v75, v24, v25\n\t" \
  "v_max3_u32 v75, v75, v26, v27\n\t" \
  "v_max3_u32 v75, v75, v28, v29\n\t" \
  "v_max3_u32 v75, v75, v30, v31\n\t" \
  "v_max3_u32 v75, v75, v32, v33\n\t" \
  "v_max3_u32 v75, v75, v34, v35\n\t" \
  "v_cmp_eq_u32 vcc, -1, v75\n\t" \
  "s_cbranch_vccz LGO1_%=\n\t" \
  "s_add_u32 s68, s68, 1\n\t" \
  "s_waitcnt lgkmcnt(0)\n\t" \
  "s_cmp_gt_u32 s68, s83\n\t" \
  "s_cbranch_scc1 LGV1_%=\n\t" \
  "s_barrier\n\t" \
  "s_waitcnt vmcnt(8)\n\t" \
  "s_branch LG1_%=\n\t" \
  "RWT1_%=f:\n\t" \
  "s_cmp_lt_i32 s73, -2\n\t" \
  "s_cbranch_scc0 RWT1_%=g\n\t" \
  "s_nop 4\n\t" \
  "s_branch RWT1_%=g\n\t" \
  "LGV1_%=:\n\t" \
  "s_mov_b32 s83, 0\n\t" \
  "s_lshl_b32 s60, s36, 4\n\t" \
  "s_or_b32 s60, s60, s48\n\t" \
  "s_or_b32 s60, s60, 805306368\n\t" \
  "v_mov_b32 v76, s60\n\t" \
  "v_mov_b32 v77, 0\n\t" \
  "s_mov_b64 exec, 1\n\t" \
  "buffer_atomic_cmpswap v[76:77], off, s[28:31], s92 sc1\n\t" \
  "s_mov_b64 exec, -1\n\t" \
  "s_branch LGO1_%=\n\t" \
  "LLAST1_%=:\n\t" \
  "s_waitcnt vmcnt(0) lgkmcnt(0)\n\t" \
  "s_barrier\n\t" \
  "s_branch LEND_%=\n\t" \
  ".p2align 6\n\t" \
  "LG2_%=:\n\t" \
  "s_barrier\n\t" \
  "buffer_load_dwordx4 v[52:55], v69, s[28:31], s50 offen sc1\n\t" \
  "buffer_load_dwordx4 v[56:59], v69, s[28:31], s50 offen offset:1024 sc1\n\t" \
  "s_add_u32 s64, s50, 18432\n\t" \
  "s_movk_i32 m0, 11264\n\t" \
  "v_lshlrev_b32 v75, 1, v69\n\t" \
  "buffer_load_dword v75, s[28:31], s64 offen sc1 lds\n\t" \
  "ds_read_b128 v[44:47], v70 offset:0\n\t" \
  "ds_read_b128 v[48:51], v70 offset:1024\n\t" \
  "ds_read_b128 v[60:63], v70 offset:2048\n\t" \
  "ds_read_b128 v[64:67], v70 offset:3072\n\t" \
  "v_mov_b64 v[36:37], 0\n\t" \
  "v_mov_b64 v[38:39], 0\n\t" \
  "v_mov_b64 v[40:41], 0\n\t" \
  "v_mov_b64 v[42:43], 0\n\t" \
  "v_lshrrev_b32 v71, 1, v69\n\t" \
  "v_lshrrev_b32 v72, 3, v69\n\t" \
  "v_and_b32 v72, 0x78, v72\n\t" \
  "v_and_b32 v74, 0x30, v69\n\t" \
  "v_lshl_add_u32 v72, v74, 3, v72\n\t" \
  "s_add_u32 s54, s53, s36\n\t" \
  "s_add_u32 s55, s54, 557056\n\t" \
  "s_add_u32 s54, s54, 131072\n\t" \
  "s_add_u32 s75, s54, 262144\n\t" \
  "s_add_u32 s80, s53, s26\n\t" \
  ".p2align 5\n\t" \
  ".fill 2, 4, 0xbf800000\n\t" \
  "s_waitcnt vmcnt(9)\n\t" \
  "s_barrier\n\t" \
  "ds_read_b128 v[0:3], v69 offset:0\n\t" \
  "ds_read_b128 v[4:7], v69 offset:1024\n\t" \
  "ds_read_b128 v[8:11], v69 offset:2048\n\t" \
  "ds_read_b128 v[12:15], v69 offset:3072\n\t" \
  "s_waitcnt lgkmcnt(3)\n\t" \
  "v_smfmac_f32_16x16x64_bf16 v[36:39], v[0:3], v[44:51], v68\n\t" \
  "ds_read_b128 v[44:47], v70 offset:4096\n\t" \
  "v_smfmac_f32_16x16x64_bf16 v[40:43], v[0:3], v[60:67], v68\n\t" \
  "ds_read_b128 v[48:51], v70 offset:5120\n\t" \
  "s_waitcnt lgkmcnt(4)\n\t" \
  "v_smfmac_f32_16x16x64_bf16 v[36:39], v[4:7], a[32:39], v68\n\t" \
  "ds_read_b128 v[60:63], v70 offset:6144\n\t" \
  "v_smfmac_f32_16x16x64_bf16 v[40:43], v[4:7], a[40:47], v68\n\t" \
  "ds_read_b128 v[64:67], v70 offset:7168\n\t" \
  "s_waitcnt lgkmcnt(5)\n\t" \
  "v_smfmac_f32_16x16x64_bf16 v[36:39], v[8:11], a[80:87], v68\n\t" \
  "s_barrier\n\t" \
  "ds_read_b128 v[16:19], v69 offset:4096\n\t" \
  "v_smfmac_f32_16x16x64_bf16 v[40:43], v[8:11], a[88:95], v68\n\t" \
  "ds_read_b128 v[20:23], v69 offset:5120\n\t" \
  "s_waitcnt lgkmcnt(6)\n\t" \
  "v_smfmac_f32_16x16x64_bf16 v[36:39], v[12:15], a[120:127], v68\n\t" \
  "ds_read_b128 v[24:27], v69 offset:6144\n\t" \
  "s_waitcnt lgkmcnt(5)\n\t" \
  "v_smfmac_f32_16x16x64_bf16 v[40:43], v[12:15], v[44:51], v68\n\t" \
  "ds_read_b128 v[28:31], v69 offset:7168\n\t" \
  "s_waitcnt lgkmcnt(3)\n\t" \
  "v_smfmac_f32_16x16x64_bf16 v[36:39], v[16:19], a[160:167], v68\n\t" \
  "ds_read_b128 v[32:35], v69 offset:8192\n\t" \
  "v_smfmac_f32_16x16x64_bf16 v[40:43], v[16:19], a[168:175], v68\n\t" \
  "s_waitcnt lgkmcnt(3)\n\t" \
  "v_mov_b64 v[44:45], 0\n\t" \
  "v_smfmac_f32_16x16x64_bf16 v[36:39], v[20:23], a[200:207], v68\n\t" \
  "s_waitcnt lgkmcnt(2)\n\t" \
  "v_mov_b64 v[46:47], 0\n\t" \
  "v_smfmac_f32_16x16x64_bf16 v[36:39], v[24:27], a[240:247], v68\n\t" \
  "s_waitcnt lgkmcnt(1)\n\t" \
  "v_mov_b64 v[48:49], 0\n\t" \
  "v_smfmac_f32_16x16x64_bf16 v[36:39], v[28:31], v[112:119], v68\n\t" \
  "s_waitcnt lgkmcnt(0)\n\t" \
  "v_mov_b64 v[50:51], 0\n\t" \
  "v_smfmac_f32_16x16x64_bf16 v[36:39], v[32:35], v[160:167], v68\n\t" \
  "v_smfmac_f32_16x16x64_bf16 v[40:43], v[20:23], v[60:67], v68\n\t" \
  "ds_read_b128 v[60:63], v70 offset:8192\n\t" \
  "v_smfmac_f32_16x16x64_bf16 v[40:43], v[24:27], a[248:255], v68\n\t" \
  "ds_read_b128 v[64:67], v70 offset:9216\n\t" \
  "v_add_f32 v74, v36, v38\n\t" \
  "v_smfmac_f32_16x16x64_bf16 v[40:43], v[28:31], v[120:127], v68\n\t" \
  "v_add_f32 v37, v37, v39\n\t" \
  "v_smfmac_f32_16x16x64_bf16 v[40:43], v[32:35], v[168:175], v68\n\t" \
  "v_cmp_u_f32 vcc, v74, v37\n\t" \
  "v_smfmac_f32_16x16x64_bf16 v[44:47], v[0:3], a[0:7], v68\n\t" \
  "s_cbranch_vccnz LFAIL2_%=\n\t" \
  "LGO2_%=:\n\t" \
  "v_cvt_pk_bf16_f32 v74, v74, v37\n\t" \
  "v_smfmac_f32_16x16x64_bf16 v[44:47], v[4:7], a[48:55], v68\n\t" \
  "s_waitcnt lgkmcnt(0)\n\t" \
  "v_add_f32 v75, v40, v42\n\t" \
  "v_smfmac_f32_16x16x64_bf16 v[44:47], v[8:11], v[60:67], v68\n\t" \
  "ds_read_b128 v[60:63], v70 offset:10240\n\t" \
  "v_add_f32 v41, v41, v43\n\t" \
  "v_cvt_pk_bf16_f32 v75, v75, v41\n\t" \
  "v_smfmac_f32_16x16x64_bf16 v[44:47], v[12:15], a[128:135], v68\n\t" \
  "ds_write2st64_b32 v71, v74, v75 offset0:40 offset1:42\n\t" \
  "v_smfmac_f32_16x16x64_bf16 v[44:47], v[16:19], a[176:183], v68\n\t" \
  "ds_read_b128 v[64:67], v70 offset:11264\n\t" \
  "v_smfmac_f32_16x16x64_bf16 v[44:47], v[20:23], a[208:215], v68\n\t" \
  "v_smfmac_f32_16x16x64_bf16 v[44:47], v[24:27], v[80:87], v68\n\t" \
  "v_smfmac_f32_16x16x64_bf16 v[44:47], v[28:31], v[128:135], v68\n\t" \
  "s_waitcnt lgkmcnt(0)\n\t" \
  "v_smfmac_f32_16x16x64_bf16 v[44:47], v[32:35], v[60:67], v68\n\t" \
  "s_barrier\n\t" \
  "ds_read_b64_tr_b16 v[76:77], v72 offset:10240\n\t" \
  "v_smfmac_f32_16x16x64_bf16 v[48:51], v[0:3], a[8:15], v68\n\t" \
  "ds_read_b64_tr_b16 v[78:79], v72 offset:10752\n\t" \
  "v_smfmac_f32_16x16x64_bf16 v[48:51], v[4:7], a[56:63], v68\n\t" \
  "buffer_load_dwordx4 v[60:63], v69, s[28:31], s64 offen sc1\n\t" \
  "v_add_f32 v74, v44, v46\n\t" \
  "v_smfmac_f32_16x16x64_bf16 v[48:51], v[8:11], a[96:103], v68\n\t" \
  "v_add_f32 v45, v45, v47\n\t" \
  "v_smfmac_f32_16x16x64_bf16 v[48:51], v[12:15], a[136:143], v68\n\t" \
  "buffer_load_dwordx4 v[64:67], v69, s[28:31], s64 offen offset:1024 sc1\n\t" \
  "s_waitcnt vmcnt(4)\n\t" \
  "s_waitcnt lgkmcnt(0)\n\t" \
  "v_cvt_pk_bf16_f32 v74, v74, v45\n\t" \
  "v_mfma_f32_16x16x32_bf16 v[36:39], v[52:55], v[76:79], 0\n\t" \
  "s_waitcnt vmcnt(3)\n\t" \
  "v_mfma_f32_16x16x32_bf16 v[40:43], v[56:59], v[76:79], 0\n\t" \
  "v_mov_b64 v[52:53], 0\n\t" \
  "v_mov_b64 v[54:55], 0\n\t" \
  "v_smfmac_f32_16x16x64_bf16 v[48:51], v[16:19], a[184:191], v68\n\t" \
  "v_mov_b64 v[56:57], 0\n\t" \
  "v_mov_b64 v[58:59], 0\n\t" \
  "v_smfmac_f32_16x16x64_bf16 v[48:51], v[20:23], a[216:223], v68\n\t" \
  "ds_write_b32 v71, v74 offset:1024\n\t" \
  "v_add_f32_dpp v36, v40, v36 quad_perm:[2,3,0,1] row_mask:0xf bank_mask:0xf\n\t" \
  "v_smfmac_f32_16x16x64_bf16 v[48:51], v[24:27], v[88:95], v68\n\t" \
  "v_add_f32_dpp v37, v41, v37 quad_perm:[2,3,0,1] row_mask:0xf bank_mask:0xf\n\t" \
  "v_smfmac_f32_16x16x64_bf16 v[48:51], v[28:31], v[136:143], v68\n\t" \
  "v_add_f32_dpp v38, v42, v38 quad_perm:[2,3,0,1] row_mask:0xf bank_mask:0xf\n\t" \
  "v_smfmac_f32_16x16x64_bf16 v[48:51], v[32:35], v[176:183], v68\n\t" \
  "v_add_f32_dpp v39, v43, v39 quad_perm:[2,3,0,1] row_mask:0xf bank_mask:0xf\n\t" \
  "v_smfmac_f32_16x16x64_bf16 v[52:55], v[0:3], a[16:23], v68\n\t" \
  "s_mov_b64 exec, s[58:59]\n\t" \
  "ds_write_b128 v69, v[36:39] offset:3072\n\t" \
  "s_mov_b64 exec, -1\n\t" \
  "v_smfmac_f32_16x16x64_bf16 v[52:55], v[4:7], a[64:71], v68\n\t" \
  "v_add_f32 v75, v48, v50\n\t" \
  "v_add_f32 v49, v49, v51\n\t" \
  "v_cvt_pk_bf16_f32 v75, v75, v49\n\t" \
  "v_smfmac_f32_16x16x64_bf16 v[56:59], v[0:3], a[24:31], v68\n\t" \
  "ds_write_b32 v71, v75 offset:1536\n\t" \
  "v_smfmac_f32_16x16x64_bf16 v[56:59], v[4:7], a[72:79], v68\n\t" \
  "ds_read_b64_tr_b16 v[0:1], v72 offset:9216\n\t" \
  "ds_read_b64_tr_b16 v[2:3], v72 offset:9728\n\t" \
  "v_smfmac_f32_16x16x64_bf16 v[52:55], v[8:11], a[104:111], v68\n\t" \
  "ds_read_b128 v[36:39], v70 offset:12288\n\t" \
  "v_smfmac_f32_16x16x64_bf16 v[56:59], v[8:11], a[112:119], v68\n\t" \
  "ds_read_b128 v[40:43], v70 offset:13312\n\t" \
  "s_waitcnt lgkmcnt(4)\n\t" \
  "s_barrier\n\t" \
  "ds_read_b64_tr_b16 v[44:45], v72 offset:1024\n\t" \
  "ds_read_b64_tr_b16 v[46:47], v72 offset:1536\n\t" \
  "v_smfmac_f32_16x16x64_bf16 v[52:55], v[12:15], a[144:151], v68\n\t" \
  "ds_read_b64_tr_b16 v[48:49], v72 offset:0\n\t" \
  "ds_read_b64_tr_b16 v[50:51], v72 offset:512\n\t" \
  "v_smfmac_f32_16x16x64_bf16 v[56:59], v[12:15], a[152:159], v68\n\t" \
  "v_smfmac_f32_16x16x64_bf16 v[52:55], v[16:19], a[192:199], v68\n\t" \
  "ds_read_b128 v[4:7], v69 offset:2048\n\t" \
  "s_waitcnt lgkmcnt(5)\n\t" \
  "v_smfmac_f32_16x16x64_bf16 v[56:59], v[16:19], v[36:43], v68\n\t" \
  "ds_read_b128 v[8:11], v69 offset:3072\n\t" \
  "s_waitcnt lgkmcnt(4)\n\t" \
  "v_mfma_f32_16x16x32_bf16 v[12:15], v[44:47], v[76:79], 0\n\t" \
  "ds_read_b128 v[36:39], v70 offset:14336\n\t" \
  "s_waitcnt lgkmcnt(3)\n\t" \
  "v_mfma_f32_16x16x32_bf16 v[12:15], v[48:51], v[0:3], v[12:15]\n\t" \
  "ds_read_b128 v[40:43], v70 offset:15360\n\t" \
  "v_smfmac_f32_16x16x64_bf16 v[52:55], v[20:23], a[224:231], v68\n\t" \
  "buffer_store_dwordx4 v[44:47], v69, s[28:31], s69 offen offset:1024 sc1\n\t" \
  "s_waitcnt lgkmcnt(2)\n\t" \
  "v_add_f32 v4, v4, v8\n\t" \
  "v_add_f32 v5, v5, v9\n\t" \
  "v_add_f32 v6, v6, v10\n\t" \
  "v_cndmask_b32 v78, v13, v14, s[56:57]\n\t" \
  "v_cndmask_b32 v76, v12, v15, s[56:57]\n\t" \
  "v_add_f32 v7, v7, v11\n\t" \
  "v_max3_f32 v77, v4, v5, v6\n\t" \
  "v_add_f32_dpp v76, v78, v76 quad_perm:[2,3,0,1] row_mask:0xf bank_mask:0xf\n\t" \
  "v_max3_f32 v77, v77, v7, v76\n\t" \
  "v_smfmac_f32_16x16x64_bf16 v[56:59], v[20:23], a[232:239], v68\n\t" \
  "ds_read_b128 v[44:47], v70 offset:16384\n\t" \
  "v_max_f32_dpp v77, v77, v77 quad_perm:[2,3,0,1] row_mask:0xf bank_mask:0xf\n\t" \
  "v_mov_b32 v78, v77\n\t" \
  "v_smfmac_f32_16x16x64_bf16 v[52:55], v[24:27], v[96:103], v68\n\t" \
  "ds_read_b128 v[48:51], v70 offset:17408\n\t" \
  "v_permlane16_swap_b32 v77, v78\n\t" \
  "v_max_f32 v77, v77, v78\n\t" \
  "v_mov_b32 v78, v77\n\t" \
  "v_smfmac_f32_16x16x64_bf16 v[52:55], v[28:31], v[144:151], v68\n\t" \
  "ds_read_b128 v[16:19], v70 offset:18432\n\t" \
  "v_permlane32_swap_b32 v77, v78\n\t" \
  "v_max_f32 v77, v77, v78\n\t" \
  "v_smfmac_f32_16x16x64_bf16 v[52:55], v[32:35], v[184:191], v68\n\t" \
  "ds_read_b128 v[20:23], v70 offset:19456\n\t" \
  "v_mul_f32 v77, 0xbe0293ee, v77\n\t" \
  "v_smfmac_f32_16x16x64_bf16 v[56:59], v[24:27], v[104:111], v68\n\t" \
  "v_fmamk_f32 v4, v4, 0x3e0293ee, v77\n\t" \
  "v_smfmac_f32_16x16x64_bf16 v[56:59], v[28:31], v[152:159], v68\n\t" \
  "ds_read_b128 v[24:27], v70 offset:22528\n\t" \
  "s_waitcnt lgkmcnt(4)\n\t" \
  "v_fmamk_f32 v5, v5, 0x3e0293ee, v77\n\t" \
  "v_smfmac_f32_16x16x64_bf16 v[56:59], v[32:35], v[36:43], v68\n\t" \
  "ds_read_b128 v[28:31], v70 offset:23552\n\t" \
  "v_fmamk_f32 v6, v6, 0x3e0293ee, v77\n\t" \
  "v_fmamk_f32 v7, v7, 0x3e0293ee, v77\n\t" \
  "v_fmamk_f32 v76, v76, 0x3e0293ee, v77\n\t" \
  "v_exp_f32 v4, v4\n\t" \
  "v_exp_f32 v5, v5\n\t" \
  "v_exp_f32 v6, v6\n\t" \
  "v_exp_f32 v7, v7\n\t" \
  "v_cvt_pk_bf16_f32 v4, v4, v5\n\t" \
  "v_exp_f32 v76, v76\n\t" \
  "v_cvt_pk_bf16_f32 v5, v6, v7\n\t" \
  "v_mov_b32_dpp v12, v4 quad_perm:[0,1,0,1] row_mask:0xf bank_mask:0xf\n\t" \
  "v_mov_b32_dpp v14, v4 quad_perm:[2,3,2,3] row_mask:0xf bank_mask:0xf\n\t" \
  "v_mov_b32_dpp v13, v5 quad_perm:[0,1,0,1] row_mask:0xf bank_mask:0xf\n\t" \
  "v_mov_b32_dpp v15, v5 quad_perm:[2,3,2,3] row_mask:0xf bank_mask:0xf\n\t" \
  "v_add_f32 v52, v52, v54\n\t" \
  "v_add_f32 v53, v53, v55\n\t" \
  "s_waitcnt vmcnt(2)\n\t" \
  "v_mfma_f32_16x16x32_bf16 v[0:3], v[60:63], v[12:15], 0\n\t" \
  "ds_read_b128 v[36:39], v70 offset:20480\n\t" \
  "v_add_f32 v56, v56, v58\n\t" \
  "v_add_f32 v57, v57, v59\n\t" \
  "v_cvt_pk_bf16_f32 v52, v52, v53\n\t" \
  "v_cvt_pk_bf16_f32 v53, v56, v57\n\t" \
  "s_waitcnt vmcnt(1)\n\t" \
  "v_mfma_f32_16x16x32_bf16 v[4:7], v[64:67], v[12:15], 0\n\t" \
  "ds_read_b128 v[40:43], v70 offset:21504\n\t" \
  "v_mov_b64 v[56:57], s[88:89]\n\t" \
  "v_mov_b64 v[58:59], s[88:89]\n\t" \
  "v_mov_b32_dpp v78, v76 quad_perm:[0,1,0,1] row_mask:0xf bank_mask:0xf\n\t" \
  "v_mov_b32_dpp v79, v76 quad_perm:[2,3,2,3] row_mask:0xf bank_mask:0xf\n\t" \
  "v_mfma_f32_16x16x32_bf16 v[32:35], v[56:59], v[12:15], 0\n\t" \
  "ds_read_b128 v[60:63], v70 offset:24576\n\t" \
  "v_cvt_pk_bf16_f32 v11, v78, v79\n\t" \
  "v_cvt_pk_bf16_f32 v8, v78, v79\n\t" \
  "v_mov_b64 v[54:55], 0\n\t" \
  "v_mov_b32 v9, 0\n\t" \
  "v_mov_b32 v10, 0\n\t" \
  "v_mov_b64 v[14:15], 0\n\t" \
  "v_mov_b64 v[12:13], 0\n\t" \
  "v_mfma_f32_16x16x32_bf16 v[32:35], v[54:57], v[8:11], v[32:35]\n\t" \
  "buffer_store_dwordx2 v[52:53], v71, s[28:31], s69 offen offset:3072 sc1\n\t" \
  "s_cmp_eq_u32 s48, 63\n\t" \
  "v_mfma_f32_16x16x32_bf16 v[0:3], v[52:55], v[8:11], v[0:3]\n\t" \
  "ds_read_b128 v[64:67], v70 offset:25600\n\t" \
  "v_mov_b64 v[56:57], 0\n\t" \
  "v_mov_b64 v[58:59], 0\n\t" \
  "v_mfma_f32_16x16x32_bf16 v[4:7], v[52:55], v[10:13], v[4:7]\n\t" \
  "s_cbranch_scc1 LLAST2_%=\n\t" \
  "v_rcp_f32 v74, v32\n\t" \
  "v_mov_b32 v11, 0\n\t" \
  "v_mov_b32 v8, 0\n\t" \
  "v_pk_mul_f32 v[0:1], v[0:1], v[74:75] op_sel_hi:[1,0]\n\t" \
  "v_pk_mul_f32 v[2:3], v[2:3], v[74:75] op_sel_hi:[1,0]\n\t" \
  "v_cvt_pk_bf16_f32 v76, v0, v1\n\t" \
  "v_cvt_pk_bf16_f32 v77, v2, v3\n\t" \
  "v_pk_mul_f32 v[4:5], v[4:5], v[74:75] op_sel_hi:[1,0]\n\t" \
  "v_pk_mul_f32 v[6:7], v[6:7], v[74:75] op_sel_hi:[1,0]\n\t" \
  "v_cvt_pk_bf16_f32 v78, v4, v5\n\t" \
  "v_cvt_pk_bf16_f32 v79, v6, v7\n\t" \
  "ds_write_b128 v69, v[76:79] offset:6144\n\t" \
  "s_waitcnt lgkmcnt(2)\n\t" \
  "v_smfmac_f32_16x16x64_bf16 v[12:15], v[76:79], v[44:51], v68\n\t" \
  "ds_read_b128 v[44:47], v70 offset:26624\n\t" \
  "v_smfmac_f32_16x16x64_bf16 v[56:59], v[76:79], v[16:23], v68\n\t" \
  "ds_read_b128 v[48:51], v70 offset:27648\n\t" \
  "v_mov_b64 v[52:53], 0\n\t" \
  "v_smfmac_f32_16x16x64_bf16 v[8:11], v[76:79], v[36:43], v68\n\t" \
  "ds_read_b128 v[16:19], v70 offset:28672\n\t" \
  "v_smfmac_f32_16x16x64_bf16 v[52:55], v[76:79], v[24:31], v68\n\t" \
  "s_waitcnt lgkmcnt(3)\n\t" \
  "s_barrier\n\t" \
  "ds_read_b128 v[0:3], v69 offset:7168\n\t" \
  "ds_read_b128 v[4:7], v69 offset:4096\n\t" \
  "v_lshrrev_b32 v71, 7, v69\n\t" \
  "v_and_b32 v74, 0x70, v69\n\t" \
  "v_lshl_add_u32 v71, v71, 14, v74\n\t" \
  "v_bfe_u32 v72, v69, 2, 7\n\t" \
  "v_and_b32 v74, 0x200, v69\n\t" \
  "s_waitcnt lgkmcnt(1)\n\t" \
  "v_smfmac_f32_16x16x64_bf16 v[8:11], v[0:3], v[60:67], v68\n\t" \
  "ds_read_b128 v[20:23], v70 offset:29696\n\t" \
  "v_mov_b64 v[76:77], 0\n\t" \
  "v_smfmac_f32_16x16x64_bf16 v[12:15], v[0:3], v[200:207], v68\n\t" \
  "buffer_load_dword v75, off, s[28:31], s80 sc1\n\t" \
  "ds_read_b128 v[32:35], v69 offset:5120\n\t" \
  "v_mov_b64 v[78:79], 0\n\t" \
  "v_smfmac_f32_16x16x64_bf16 v[52:55], v[0:3], v[44:51], v68\n\t" \
  "ds_read_b128 v[24:27], v70 offset:30720\n\t" \
  "v_lshl_add_u32 v72, v74, 3, v72\n\t" \
  "v_smfmac_f32_16x16x64_bf16 v[56:59], v[0:3], v[232:239], v68\n\t" \
  "ds_read_b128 v[28:31], v70 offset:31744\n\t" \
  "s_waitcnt lgkmcnt(4)\n\t" \
  "v_smfmac_f32_16x16x64_bf16 v[12:15], v[4:7], v[208:215], v68\n\t" \
  "ds_read_b128 v[60:63], v70 offset:32768\n\t" \
  "s_waitcnt lgkmcnt(4)\n\t" \
  "v_smfmac_f32_16x16x64_bf16 v[8:11], v[4:7], v[16:23], v68\n\t" \
  "ds_read_b128 v[64:67], v70 offset:33792\n\t" \
  "s_waitcnt lgkmcnt(4)\n\t" \
  "v_smfmac_f32_16x16x64_bf16 v[8:11], v[32:35], v[192:199], v68\n\t" \
  "ds_read_b128 v[36:39], v73 offset:4096\n\t" \
  "v_smfmac_f32_16x16x64_bf16 v[12:15], v[32:35], v[216:223], v68\n\t" \
  "ds_read_b128 v[40:43], v73 offset:6144\n\t" \
  "s_waitcnt lgkmcnt(4)\n\t" \
  "v_smfmac_f32_16x16x64_bf16 v[52:55], v[4:7], v[24:31], v68\n\t" \
  "ds_read_b128 v[44:47], v70 offset:34816\n\t" \
  "v_cvt_pk_bf16_f32 v8, v8, v9\n\t" \
  "v_smfmac_f32_16x16x64_bf16 v[52:55], v[32:35], v[224:231], v68\n\t" \
  "ds_read_b128 v[48:51], v70 offset:35840\n\t" \
  "v_cvt_pk_bf16_f32 v9, v10, v11\n\t" \
  "v_smfmac_f32_16x16x64_bf16 v[56:59], v[4:7], v[240:247], v68\n\t" \
  "s_waitcnt lgkmcnt(4)\n\t" \
  "v_cvt_pk_bf16_f32 v10, v12, v13\n\t" \
  "v_smfmac_f32_16x16x64_bf16 v[56:59], v[32:35], v[60:67], v68\n\t" \
  "s_waitcnt lgkmcnt(3)\n\t" \
  "v_cvt_pk_bf16_f32 v11, v14, v15\n\t" \
  "v_smfmac_f32_16x16x64_bf16 v[76:79], v[36:39], v[248:255], v68\n\t" \
  "buffer_store_dwordx4 v[8:11], v71, s[28:31], s54 offen sc1\n\t" \
  "s_waitcnt lgkmcnt(0)\n\t" \
  "v_cvt_pk_bf16_f32 v52, v52, v53\n\t" \
  "v_smfmac_f32_16x16x64_bf16 v[76:79], v[40:43], v[44:51], v68\n\t" \
  "v_cvt_pk_bf16_f32 v53, v54, v55\n\t" \
  "v_cvt_pk_bf16_f32 v54, v56, v57\n\t" \
  "v_cvt_pk_bf16_f32 v55, v58, v59\n\t" \
  "buffer_store_dwordx4 v[52:55], v71, s[28:31], s75 offen sc1\n\t" \
  "s_nop 2\n\t" \
  "v_add_f32 v76, v76, v78\n\t" \
  "v_add_f32 v77, v77, v79\n\t" \
  "v_cvt_pk_bf16_f32 v76, v76, v77\n\t" \
  "buffer_store_dword v76, v72, s[28:31], s55 offen sc1\n\t" \
  "s_memrealtime s[70:71]\n\t" \
  "s_waitcnt vmcnt(3)\n\t" \
  "v_readfirstlane_b32 s65, v75\n\t" \
  "s_barrier\n\t" \
  "s_waitcnt lgkmcnt(0)\n\t" \
  "s_not_b32 s65, s65\n\t" \
  "s_lshl_b32 s61, s70, 8\n\t" \
  "s_andn2_b32 s64, s65, 0xff\n\t" \
  "s_sub_u32 s64, s64, s61\n\t" \
  "s_ashr_i32 s64, s64, 8\n\t" \
  "s_cmp_eq_u32 s65, 0\n\t" \
  "s_cselect_b32 s64, " CH_DLF ", s64\n\t" \
  "s_min_i32 s64, s64, s34\n\t" \
  "s_max_i32 s64, s64, " CH_DLL1 "\n\t" \
  "s_add_u32 s64, s64, s70\n\t" \
  "s_mov_b32 s74, 0\n\t" \
  "s_movk_i32 s60, 8192\n\t" \
  "RWT2_%=:\n\t" \
  "s_memrealtime s[72:73]\n\t" \
  "s_sub_u32 s60, s60, 1\n\t" \
  "s_cselect_b32 s61, 0, -1\n\t" \
  "s_waitcnt lgkmcnt(0)\n\t" \
  "s_sub_i32 s73, s72, s64\n\t" \
  "s_and_b32 s73, s73, s61\n\t" \
  "s_cmp_lt_i32 s73, " CH_FIN "\n\t" \
  "s_cbranch_scc1 RWT2_%=\n\t" \
  "s_cmp_lt_i32 s73, 0\n\t" \
  "s_cbranch_scc1 RWT2_%=f\n\t" \
  "RWT2_%=g:\n\t" \
  "RRETRY2_%=:\n\t" \
  "buffer_load_dwordx4 v[0:3], v69, s[28:31], s97 offen offset:0 sc1\n\t" \
  "buffer_load_dwordx4 v[4:7], v69, s[28:31], s97 offen offset:1024 sc1\n\t" \
  "buffer_load_dwordx4 v[8:11], v69, s[28:31], s97 offen offset:2048 sc1\n\t" \
  "buffer_load_dwordx4 v[12:15], v69, s[28:31], s97 offen offset:3072 sc1\n\t" \
  "v_lshrrev_b32 v20, 2, v69\n\t" \
  "v_and_b32 v20, 16, v20\n\t" \
  "v_mov_b32 v21, 0x3f80\n\t" \
  "v_lshlrev_b32 v21, v20, v21\n\t" \
  "v_bfe_u32 v20, v69, 4, 2\n\t" \
  "v_cmp_eq_u32 vcc, 0, v20\n\t" \
  "s_nop 1\n\t" \
  "v_cndmask_b32 v32, 0, v21, vcc\n\t" \
  "v_cmp_eq_u32 vcc, 1, v20\n\t" \
  "s_nop 1\n\t" \
  "v_cndmask_b32 v33, 0, v21, vcc\n\t" \
  "v_cmp_eq_u32 vcc, 2, v20\n\t" \
  "s_nop 1\n\t" \
  "v_cndmask_b32 v34, 0, v21, vcc\n\t" \
  "v_cmp_eq_u32 vcc, 3, v20\n\t" \
  "s_nop 1\n\t" \
  "v_cndmask_b32 v35, 0, v21, vcc\n\t" \
  "v_bfe_u32 v38, v69, 7, 1\n\t" \
  "v_bfe_u32 v20, v69, 9, 1\n\t" \
  "v_lshl_add_u32 v38, v20, 2, v38\n\t" \
  "v_lshlrev_b32 v38, 12, v38\n\t" \
  "v_and_b32 v20, 0x60, v69\n\t" \
  "v_add_u32 v38, v38, v20\n\t" \
  "v_bfe_u32 v20, v69, 8, 1\n\t" \
  "v_lshl_add_u32 v38, v20, 4, v38\n\t" \
  "v_bfe_u32 v20, v69, 4, 1\n\t" \
  "v_lshl_add_u32 v38, v20, 3, v38\n\t" \
  "v_mov_b32 v44, 0\n\t" \
  CH_ALD \
  "s_waitcnt vmcnt(3)\n\t" \
  "v_mfma_f32_16x16x32_bf16 v[40:43], v[32:35], v[0:3], 0\n\t" \
  "v_max3_u32 v44, v44, v0, v1\n\t" \
  "v_max3_u32 v44, v44, v2, v3\n\t" \
  "s_waitcnt vmcnt(2)\n\t" \
  "v_mfma_f32_16x16x32_bf16 v[40:43], v[32:35], v[4:7], v[40:43]\n\t" \
  "v_max3_u32 v44, v44, v4, v5\n\t" \
  "v_max3_u32 v44, v44, v6, v7\n\t" \
  "s_waitcnt vmcnt(1)\n\t" \
  "v_mfma_f32_16x16x32_bf16 v[40:43], v[32:35], v[8:11], v[40:43]\n\t" \
  "v_max3_u32 v44, v44, v8, v9\n\t" \
  "v_max3_u32 v44, v44, v10, v11\n\t" \
  "s_waitcnt vmcnt(0)\n\t" \
  "v_mfma_f32_16x16x32_bf16 v[40:43], v[32:35], v[12:15], v[40:43]\n\t" \
  "v_max3_u32 v44, v44, v12, v13\n\t" \
  "v_max3_u32 v44, v44, v14, v15\n\t" \
  "v_cmp_eq_u32 vcc, -1, v44\n\t" \
  "s_cbranch_vccnz RFAIL2_%=\n\t" \
  "s_nop 3\n\t" \
  "v_add_f32_dpp v40, v40, v40 row_ror:8 row_mask:0xf bank_mask:0xf\n\t" \
  "v_add_f32_dpp v41, v41, v41 row_ror:8 row_mask:0xf bank_mask:0xf\n\t" \
  "v_add_f32_dpp v42, v42, v42 row_ror:8 row_mask:0xf bank_mask:0xf\n\t" \
  "v_add_f32_dpp v43, v43, v43 row_ror:8 row_mask:0xf bank_mask:0xf\n\t" \
  "v_cvt_pk_bf16_f32 v40, v40, v41\n\t" \
  "v_cvt_pk_bf16_f32 v41, v42, v43\n\t" \
  "buffer_store_dwordx2 v[40:41], v38, s[28:31], s98 offen sc1\n\t" \
  "RDONE2_%=:\n\t" \
  "s_barrier\n\t" \
  "s_add_u32 s48, s48, 1\n\t" \
  "s_mov_b32 s49, s53\n\t" \
  "s_add_u32 s53, s53, " CH_SLOT "\n\t" \
  "s_add_u32 s60, s53, " CH_SKIP "\n\t" \
  "s_lshr_b32 s61, s96, 1\n\t" \
  "s_add_u32 s86, s86, 1\n\t" \
  "s_cmp_eq_u32 s86, 3\n\t" \
  "s_cselect_b32 s53, s60, s53\n\t" \
  "s_cselect_b32 s96, s61, s96\n\t" \
  "s_cselect_b32 s86, 0, s86\n\t" \
  "s_cmp_ge_u32 s53, " CH_RING "\n\t" \
  "s_cselect_b32 s53, 0, s53\n\t" \
  "s_cselect_b32 s96, s21, s96\n\t" \
  "s_andn2_b32 s53, s53, 0x2000\n\t" \
  "s_and_b32 s60, s96, 1\n\t" \
  "s_lshl_b32 s60, s60, 13\n\t" \
  "s_or_b32 s53, s53, s60\n\t" \
  "s_mov_b32 s68, 0\n\t" \
  "s_add_u32 s97, s41, s53\n\t" \
  "s_add_u32 s98, s37, s53\n\t" \
  "s_add_u32 s82, s48, 0\n\t" \
  "s_and_b32 s82, s82, " CH_TRM "\n\t" \
  "s_lshr_b64 s[60:61], s[90:91], s82\n\t" \
  "s_and_b32 s60, s60, 1\n\t" \
  "s_lshl_b32 s60, s60, 13\n\t" \
  "s_add_u32 s82, s82, " CH_SG0 "\n\t" \
  "s_lshl_b32 s82, s82, 21\n\t" \
  "s_or_b32 s82, s82, s60\n\t" \
  "s_add_u32 s50, s82, s38\n\t" \
  "s_add_u32 s69, s82, s39\n\t" \
  "s_cmp_eq_u32 s34, 0\n\t" \
  "s_cbranch_scc1 LINI_%=\n\t" \
  "s_branch LG2_%=\n\t" \
  "RFAIL2_%=:\n\t" \
  "s_add_u32 s74, s74, 1\n\t" \
  "s_sleep " CH_RSL "\n\t" \
  "s_waitcnt vmcnt(0)\n\t" \
  "s_cmp_gt_u32 s74, s84\n\t" \
  "s_cbranch_scc1 RG2_%=\n\t" \
  "s_and_b32 s60, s74, 1023\n\t" \
  "s_cmp_lg_u32 s60, 0\n\t" \
  "s_cbranch_scc1 RRETRY2_%=\n\t" \
  "buffer_load_dword v46, off, s[28:31], s92 sc1\n\t" \
  "s_waitcnt vmcnt(0)\n\t" \
  "v_readfirstlane_b32 s60, v46\n\t" \
  "s_nop 0\n\t" \
  "s_cmp_eq_u32 s60, 0\n\t" \
  "s_cbranch_scc1 RRETRY2_%=\n\t" \
  "RG2_%=:\n\t" \
  "s_mov_b32 s84, 0\n\t" \
  "s_lshl_b32 s60, s36, 4\n\t" \
  "s_or_b32 s60, s60, s48\n\t" \
  "s_or_b32 s60, s60, 536870912\n\t" \
  "v_mov_b32 v76, s60\n\t" \
  "v_mov_b32 v77, 0\n\t" \
  "s_mov_b64 exec, 1\n\t" \
  "buffer_atomic_cmpswap v[76:77], off, s[28:31], s92 sc1\n\t" \
  "s_mov_b64 exec, -1\n\t" \
  "s_branch RDONE2_%=\n\t" \
  "LFAIL2_%=:\n\t" \
  "v_max_u32 v75, v0, v1\n\t" \
  "v_max3_u32 v75, v75, v2, v3\n\t" \
  "v_max3_u32 v75, v75, v4, v5\n\t" \
  "v_max3_u32 v75, v75, v6, v7\n\t" \
  "v_max3_u32 v75, v75, v8, v9\n\t" \
  "v_max3_u32 v75, v75, v10, v11\n\t" \
  "v_max3_u32 v75, v75, v12, v13\n\t" \
  "v_max3_u32 v75, v75, v14, v15\n\t" \
  "v_max3_u32 v75, v75, v16, v17\n\t" \
  "v_max3_u32 v75, v75, v18, v19\n\t" \
  "v_max3_u32 v75, v75, v20, v21\n\t" \
  "v_max3_u32 v75, v75, v22, v23\n\t" \
  "v_max3_u32 v75, v75, v24, v25\n\t" \
  "v_max3_u32 v75, v75, v26, v27\n\t" \
  "v_max3_u32 v75, v75, v28, v29\n\t" \
  "v_max3_u32 v75, v75, v30, v31\n\t" \
  "v_max3_u32 v75, v75, v32, v33\n\t" \
  "v_max3_u32 v75, v75, v34, v35\n\t" \
  "v_cmp_eq_u32 vcc, -1, v75\n\t" \
  "s_cbranch_vccz LGO2_%=\n\t" \
  "s_add_u32 s68, s68, 1\n\t" \
  "s_waitcnt lgkmcnt(0)\n\t" \
  "s_cmp_gt_u32 s68, s83\n\t" \
  "s_cbranch_scc1 LGV2_%=\n\t" \
  "s_barrier\n\t" \
  "s_waitcnt vmcnt(8)\n\t" \
  "s_branch LG2_%=\n\t" \
  "RWT2_%=f:\n\t" \
  "s_cmp_lt_i32 s73, -2\n\t" \
  "s_cbranch_scc0 RWT2_%=g\n\t" \
  "s_nop 4\n\t" \
  "s_branch RWT2_%=g\n\t" \
  "LGV2_%=:\n\t" \
  "s_mov_b32 s83, 0\n\t" \
  "s_lshl_b32 s60, s36, 4\n\t" \
  "s_or_b32 s60, s60, s48\n\t" \
  "s_or_b32 s60, s60, 805306368\n\t" \
  "v_mov_b32 v76, s60\n\t" \
  "v_mov_b32 v77, 0\n\t" \
  "s_mov_b64 exec, 1\n\t" \
  "buffer_atomic_cmpswap v[76:77], off, s[28:31], s92 sc1\n\t" \
  "s_mov_b64 exec, -1\n\t" \
  "s_branch LGO2_%=\n\t" \
  "LLAST2_%=:\n\t" \
  "s_waitcnt vmcnt(0) lgkmcnt(0)\n\t" \
  "s_barrier\n\t" \
  "s_branch LEND_%=\n\t" \
  ".p2align 6\n\t" \
  "LG3_%=:\n\t" \
  "s_barrier\n\t" \
  "buffer_load_dwordx4 v[52:55], v69, s[28:31], s50 offen sc1\n\t" \
  "buffer_load_dwordx4 v[56:59], v69, s[28:31], s50 offen offset:1024 sc1\n\t" \
  "s_add_u32 s64, s50, 16384\n\t" \
  "s_movk_i32 m0, 11264\n\t" \
  "v_lshlrev_b32 v75, 1, v69\n\t" \
  "buffer_load_dword v75, s[28:31], s64 offen sc1 lds\n\t" \
  "s_lshr_b32 s61, s20, 3\n\t" \
  "s_mul_i32 s47, s61, " CH_KD "\n\t" \
  "s_lshr_b32 s47, s47, 10\n\t" \
  "s_lshl_b32 s47, s47, 8\n\t" \
  "s_mul_i32 s61, s61, " CH_KB "\n\t" \
  "s_lshr_b32 s61, s61, 10\n\t" \
  "s_min_u32 s61, s61, 100\n\t" \
  "s_or_b32 s47, s47, s61\n\t" \
  "s_cmp_lt_u32 s48, " CH_ES "\n\t" \
  "s_cselect_b32 s60, " CH_EX ", 0\n\t" \
  "s_add_u32 s47, s47, s60\n\t" \
  "s_not_b32 s47, s47\n\t" \
  "s_add_u32 s71, s53, s46\n\t" \
  "ds_read_b128 v[44:47], v70 offset:0\n\t" \
  "ds_read_b128 v[48:51], v70 offset:1024\n\t" \
  "ds_read_b128 v[60:63], v70 offset:2048\n\t" \
  "ds_read_b128 v[64:67], v70 offset:3072\n\t" \
  "v_mov_b64 v[36:37], 0\n\t" \
  "v_mov_b64 v[38:39], 0\n\t" \
  "v_mov_b64 v[40:41], 0\n\t" \
  "v_mov_b64 v[42:43], 0\n\t" \
  "v_lshrrev_b32 v71, 1, v69\n\t" \
  "v_add_u32 v71, 4, v71\n\t" \
  "v_lshrrev_b32 v72, 3, v69\n\t" \
  "v_and_b32 v72, 0x78, v72\n\t" \
  "v_and_b32 v74, 0x30, v69\n\t" \
  "v_lshl_add_u32 v72, v74, 3, v72\n\t" \
  "s_add_u32 s54, s53, s36\n\t" \
  "s_add_u32 s55, s54, 573440\n\t" \
  "s_add_u32 s54, s54, 135168\n\t" \
  "s_add_u32 s75, s54, 262144\n\t" \
  "s_add_u32 s80, s53, s26\n\t" \
  "s_or_b32 s87, s52, 0x1fff80\n\t" \
  "s_and_b32 s87, s87, -128\n\t" \
  "s_add_u32 s87, s87, 0x200000\n\t" \
  ".p2align 5\n\t" \
  ".fill 2, 4, 0xbf800000\n\t" \
  "s_waitcnt vmcnt(9)\n\t" \
  "s_barrier\n\t" \
  "ds_read_b128 v[0:3], v69 offset:0\n\t" \
  "ds_read_b128 v[4:7], v69 offset:1024\n\t" \
  "ds_read_b128 v[8:11], v69 offset:2048\n\t" \
  "ds_read_b128 v[12:15], v69 offset:3072\n\t" \
  "s_waitcnt lgkmcnt(3)\n\t" \
  "v_smfmac_f32_16x16x64_bf16 v[36:39], v[0:3], v[44:51], v68\n\t" \
  "ds_read_b128 v[44:47], v70 offset:4096\n\t" \
  "v_smfmac_f32_16x16x64_bf16 v[40:43], v[0:3], v[60:67], v68\n\t" \
  "ds_read_b128 v[48:51], v70 offset:5120\n\t" \
  "s_waitcnt lgkmcnt(4)\n\t" \
  "v_smfmac_f32_16x16x64_bf16 v[36:39], v[4:7], a[32:39], v68\n\t" \
  "ds_read_b128 v[60:63], v70 offset:6144\n\t" \
  "v_smfmac_f32_16x16x64_bf16 v[40:43], v[4:7], a[40:47], v68\n\t" \
  "ds_read_b128 v[64:67], v70 offset:7168\n\t" \
  "s_waitcnt lgkmcnt(5)\n\t" \
  "v_smfmac_f32_16x16x64_bf16 v[36:39], v[8:11], a[80:87], v68\n\t" \
  "s_barrier\n\t" \
  "ds_read_b128 v[16:19], v69 offset:4096\n\t" \
  "v_smfmac_f32_16x16x64_bf16 v[40:43], v[8:11], a[88:95], v68\n\t" \
  "ds_read_b128 v[20:23], v69 offset:5120\n\t" \
  "s_waitcnt lgkmcnt(6)\n\t" \
  "v_smfmac_f32_16x16x64_bf16 v[36:39], v[12:15], a[120:127], v68\n\t" \
  "ds_read_b128 v[24:27], v69 offset:6144\n\t" \
  "s_waitcnt lgkmcnt(5)\n\t" \
  "v_smfmac_f32_16x16x64_bf16 v[40:43], v[12:15], v[44:51], v68\n\t" \
  "ds_read_b128 v[28:31], v69 offset:7168\n\t" \
  "s_waitcnt lgkmcnt(3)\n\t" \
  "v_smfmac_f32_16x16x64_bf16 v[36:39], v[16:19], a[160:167], v68\n\t" \
  "ds_read_b128 v[32:35], v69 offset:8192\n\t" \
  "v_smfmac_f32_16x16x64_bf16 v[40:43], v[16:19], a[168:175], v68\n\t" \
  "s_waitcnt lgkmcnt(3)\n\t" \
  "v_mov_b64 v[44:45], 0\n\t" \
  "v_smfmac_f32_16x16x64_bf16 v[36:39], v[20:23], a[200:207], v68\n\t" \
  "s_waitcnt lgkmcnt(2)\n\t" \
  "v_mov_b64 v[46:47], 0\n\t" \
  "v_smfmac_f32_16x16x64_bf16 v[36:39], v[24:27], a[240:247], v68\n\t" \
  "s_waitcnt lgkmcnt(1)\n\t" \
  "v_mov_b64 v[48:49], 0\n\t" \
  "v_smfmac_f32_16x16x64_bf16 v[36:39], v[28:31], v[112:119], v68\n\t" \
  "s_waitcnt lgkmcnt(0)\n\t" \
  "v_mov_b64 v[50:51], 0\n\t" \
  "v_smfmac_f32_16x16x64_bf16 v[36:39], v[32:35], v[160:167], v68\n\t" \
  "v_smfmac_f32_16x16x64_bf16 v[40:43], v[20:23], v[60:67], v68\n\t" \
  "ds_read_b128 v[60:63], v70 offset:8192\n\t" \
  "v_smfmac_f32_16x16x64_bf16 v[40:43], v[24:27], a[248:255], v68\n\t" \
  "ds_read_b128 v[64:67], v70 offset:9216\n\t" \
  "v_add_f32 v74, v36, v38\n\t" \
  "v_smfmac_f32_16x16x64_bf16 v[40:43], v[28:31], v[120:127], v68\n\t" \
  "v_add_f32 v37, v37, v39\n\t" \
  "v_smfmac_f32_16x16x64_bf16 v[40:43], v[32:35], v[168:175], v68\n\t" \
  "v_cmp_u_f32 vcc, v74, v37\n\t" \
  "v_smfmac_f32_16x16x64_bf16 v[44:47], v[0:3], a[0:7], v68\n\t" \
  "s_cbranch_vccnz LFAIL3_%=\n\t" \
  "LGO3_%=:\n\t" \
  "v_cvt_pk_bf16_f32 v74, v74, v37\n\t" \
  "v_smfmac_f32_16x16x64_bf16 v[44:47], v[4:7], a[48:55], v68\n\t" \
  "s_waitcnt lgkmcnt(0)\n\t" \
  "v_add_f32 v75, v40, v42\n\t" \
  "v_smfmac_f32_16x16x64_bf16 v[44:47], v[8:11], v[60:67], v68\n\t" \
  "ds_read_b128 v[60:63], v70 offset:10240\n\t" \
  "v_add_f32 v41, v41, v43\n\t" \
  "v_cvt_pk_bf16_f32 v75, v75, v41\n\t" \
  "v_smfmac_f32_16x16x64_bf16 v[44:47], v[12:15], a[128:135], v68\n\t" \
  "ds_write2st64_b32 v71, v74, v75 offset0:40 offset1:42\n\t" \
  "v_smfmac_f32_16x16x64_bf16 v[44:47], v[16:19], a[176:183], v68\n\t" \
  "ds_read_b128 v[64:67], v70 offset:11264\n\t" \
  "s_memrealtime s[66:67]\n\t" \
  "v_smfmac_f32_16x16x64_bf16 v[44:47], v[20:23], a[208:215], v68\n\t" \
  "v_smfmac_f32_16x16x64_bf16 v[44:47], v[24:27], v[80:87], v68\n\t" \
  "v_smfmac_f32_16x16x64_bf16 v[44:47], v[28:31], v[128:135], v68\n\t" \
  "s_waitcnt lgkmcnt(0)\n\t" \
  "v_smfmac_f32_16x16x64_bf16 v[44:47], v[32:35], v[60:67], v68\n\t" \
  "s_barrier\n\t" \
  "ds_read_b64_tr_b16 v[76:77], v72 offset:10240\n\t" \
  "v_smfmac_f32_16x16x64_bf16 v[48:51], v[0:3], a[8:15], v68\n\t" \
  "s_lshl_b32 s60, s66, 8\n\t" \
  "s_sub_u32 s60, s47, s60\n\t" \
  "ds_read_b64_tr_b16 v[78:79], v72 offset:10752\n\t" \
  "v_mov_b32 v75, s60\n\t" \
  "v_smfmac_f32_16x16x64_bf16 v[48:51], v[4:7], a[56:63], v68\n\t" \
  "s_mov_b64 exec, s[24:25]\n\t" \
  "buffer_atomic_umin v75, v69, s[28:31], s71 offen sc1\n\t" \
  "s_mov_b64 exec, -1\n\t" \
  "v_add_f32 v74, v44, v46\n\t" \
  "v_smfmac_f32_16x16x64_bf16 v[48:51], v[8:11], a[96:103], v68\n\t" \
  "buffer_load_dwordx4 v[60:63], v69, s[28:31], s64 offen sc1\n\t" \
  "v_add_f32 v45, v45, v47\n\t" \
  "v_smfmac_f32_16x16x64_bf16 v[48:51], v[12:15], a[136:143], v68\n\t" \
  "s_waitcnt vmcnt(4)\n\t" \
  "s_waitcnt lgkmcnt(0)\n\t" \
  "v_cvt_pk_bf16_f32 v74, v74, v45\n\t" \
  "v_mfma_f32_16x16x32_bf16 v[36:39], v[52:55], v[76:79], 0\n\t" \
  "s_waitcnt vmcnt(3)\n\t" \
  "v_mfma_f32_16x16x32_bf16 v[40:43], v[56:59], v[76:79], 0\n\t" \
  "v_mov_b64 v[52:53], 0\n\t" \
  "v_mov_b64 v[54:55], 0\n\t" \
  "v_smfmac_f32_16x16x64_bf16 v[48:51], v[16:19], a[184:191], v68\n\t" \
  "buffer_load_dwordx4 v[64:67], v69, s[28:31], s64 offen offset:1024 sc1\n\t" \
  "v_mov_b64 v[56:57], 0\n\t" \
  "v_mov_b64 v[58:59], 0\n\t" \
  "v_smfmac_f32_16x16x64_bf16 v[48:51], v[20:23], a[216:223], v68\n\t" \
  "ds_write_b32 v71, v74 offset:1024\n\t" \
  "v_add_f32_dpp v36, v40, v36 quad_perm:[2,3,0,1] row_mask:0xf bank_mask:0xf\n\t" \
  "v_smfmac_f32_16x16x64_bf16 v[48:51], v[24:27], v[88:95], v68\n\t" \
  "v_add_f32_dpp v37, v41, v37 quad_perm:[2,3,0,1] row_mask:0xf bank_mask:0xf\n\t" \
  "v_smfmac_f32_16x16x64_bf16 v[48:51], v[28:31], v[136:143], v68\n\t" \
  "v_add_f32_dpp v38, v42, v38 quad_perm:[2,3,0,1] row_mask:0xf bank_mask:0xf\n\t" \
  "v_smfmac_f32_16x16x64_bf16 v[48:51], v[32:35], v[176:183], v68\n\t" \
  "v_add_f32_dpp v39, v43, v39 quad_perm:[2,3,0,1] row_mask:0xf bank_mask:0xf\n\t" \
  "v_smfmac_f32_16x16x64_bf16 v[52:55], v[0:3], a[16:23], v68\n\t" \
  "s_mov_b64 exec, s[58:59]\n\t" \
  "ds_write_b128 v69, v[36:39] offset:3104\n\t" \
  "s_mov_b64 exec, -1\n\t" \
  "v_smfmac_f32_16x16x64_bf16 v[52:55], v[4:7], a[64:71], v68\n\t" \
  "v_add_f32 v75, v48, v50\n\t" \
  "v_add_f32 v49, v49, v51\n\t" \
  "v_cvt_pk_bf16_f32 v75, v75, v49\n\t" \
  "v_smfmac_f32_16x16x64_bf16 v[56:59], v[0:3], a[24:31], v68\n\t" \
  "ds_write_b32 v71, v75 offset:1536\n\t" \
  "v_smfmac_f32_16x16x64_bf16 v[56:59], v[4:7], a[72:79], v68\n\t" \
  "ds_read_b64_tr_b16 v[0:1], v72 offset:9216\n\t" \
  "ds_read_b64_tr_b16 v[2:3], v72 offset:9728\n\t" \
  "v_smfmac_f32_16x16x64_bf16 v[52:55], v[8:11], a[104:111], v68\n\t" \
  "ds_read_b128 v[36:39], v70 offset:12288\n\t" \
  "v_smfmac_f32_16x16x64_bf16 v[56:59], v[8:11], a[112:119], v68\n\t" \
  "buffer_load_dword off, s[28:31], s87 sc1 lds\n\t" \
  "ds_read_b128 v[40:43], v70 offset:13312\n\t" \
  "s_waitcnt lgkmcnt(4)\n\t" \
  "s_barrier\n\t" \
  "ds_read_b64_tr_b16 v[44:45], v72 offset:1024\n\t" \
  "ds_read_b64_tr_b16 v[46:47], v72 offset:1536\n\t" \
  "v_smfmac_f32_16x16x64_bf16 v[52:55], v[12:15], a[144:151], v68\n\t" \
  "ds_read_b64_tr_b16 v[48:49], v72 offset:0\n\t" \
  "ds_read_b64_tr_b16 v[50:51], v72 offset:512\n\t" \
  "v_smfmac_f32_16x16x64_bf16 v[56:59], v[12:15], a[152:159], v68\n\t" \
  "v_smfmac_f32_16x16x64_bf16 v[52:55], v[16:19], a[192:199], v68\n\t" \
  "ds_read_b128 v[4:7], v69 offset:2048\n\t" \
  "s_waitcnt lgkmcnt(5)\n\t" \
  "v_smfmac_f32_16x16x64_bf16 v[56:59], v[16:19], v[36:43], v68\n\t" \
  "ds_read_b128 v[8:11], v69 offset:3072\n\t" \
  "s_waitcnt lgkmcnt(4)\n\t" \
  "v_mfma_f32_16x16x32_bf16 v[12:15], v[44:47], v[76:79], 0\n\t" \
  "ds_read_b128 v[36:39], v70 offset:14336\n\t" \
  "s_waitcnt lgkmcnt(3)\n\t" \
  "v_mfma_f32_16x16x32_bf16 v[12:15], v[48:51], v[0:3], v[12:15]\n\t" \
  "ds_read_b128 v[40:43], v70 offset:15360\n\t" \
  "v_smfmac_f32_16x16x64_bf16 v[52:55], v[20:23], a[224:231], v68\n\t" \
  "s_waitcnt lgkmcnt(2)\n\t" \
  "v_add_f32 v4, v4, v8\n\t" \
  "v_add_f32 v5, v5, v9\n\t" \
  "v_add_f32 v6, v6, v10\n\t" \
  "v_cndmask_b32 v78, v13, v14, s[56:57]\n\t" \
  "v_cndmask_b32 v76, v12, v15, s[56:57]\n\t" \
  "v_add_f32 v7, v7, v11\n\t" \
  "v_max3_f32 v77, v4, v5, v6\n\t" \
  "v_add_f32_dpp v76, v78, v76 quad_perm:[2,3,0,1] row_mask:0xf bank_mask:0xf\n\t" \
  "v_max3_f32 v77, v77, v7, v76\n\t" \
  "v_smfmac_f32_16x16x64_bf16 v[56:59], v[20:23], a[232:239], v68\n\t" \
  "ds_read_b128 v[44:47], v70 offset:16384\n\t" \
  "v_max_f32_dpp v77, v77, v77 quad_perm:[2,3,0,1] row_mask:0xf bank_mask:0xf\n\t" \
  "v_mov_b32 v78, v77\n\t" \
  "v_smfmac_f32_16x16x64_bf16 v[52:55], v[24:27], v[96:103], v68\n\t" \
  "ds_read_b128 v[48:51], v70 offset:17408\n\t" \
  "v_permlane16_swap_b32 v77, v78\n\t" \
  "v_max_f32 v77, v77, v78\n\t" \
  "v_mov_b32 v78, v77\n\t" \
  "v_smfmac_f32_16x16x64_bf16 v[52:55], v[28:31], v[144:151], v68\n\t" \
  "ds_read_b128 v[16:19], v70 offset:18432\n\t" \
  "v_permlane32_swap_b32 v77, v78\n\t" \
  "v_max_f32 v77, v77, v78\n\t" \
  "v_smfmac_f32_16x16x64_bf16 v[52:55], v[32:35], v[184:191], v68\n\t" \
  "ds_read_b128 v[20:23], v70 offset:19456\n\t" \
  "v_mul_f32 v77, 0xbe0293ee, v77\n\t" \
  "v_smfmac_f32_16x16x64_bf16 v[56:59], v[24:27], v[104:111], v68\n\t" \
  "v_fmamk_f32 v4, v4, 0x3e0293ee, v77\n\t" \
  "v_smfmac_f32_16x16x64_bf16 v[56:59], v[28:31], v[152:159], v68\n\t" \
  "ds_read_b128 v[24:27], v70 offset:22528\n\t" \
  "s_waitcnt lgkmcnt(4)\n\t" \
  "v_fmamk_f32 v5, v5, 0x3e0293ee, v77\n\t" \
  "v_smfmac_f32_16x16x64_bf16 v[56:59], v[32:35], v[36:43], v68\n\t" \
  "ds_read_b128 v[28:31], v70 offset:23552\n\t" \
  "v_fmamk_f32 v6, v6, 0x3e0293ee, v77\n\t" \
  "v_fmamk_f32 v7, v7, 0x3e0293ee, v77\n\t" \
  "v_fmamk_f32 v76, v76, 0x3e0293ee, v77\n\t" \
  "v_exp_f32 v4, v4\n\t" \
  "v_exp_f32 v5, v5\n\t" \
  "v_exp_f32 v6, v6\n\t" \
  "v_exp_f32 v7, v7\n\t" \
  "v_cvt_pk_bf16_f32 v4, v4, v5\n\t" \
  "v_exp_f32 v76, v76\n\t" \
  "v_cvt_pk_bf16_f32 v5, v6, v7\n\t" \
  "v_mov_b32_dpp v12, v4 quad_perm:[0,1,0,1] row_mask:0xf bank_mask:0xf\n\t" \
  "v_mov_b32_dpp v14, v4 quad_perm:[2,3,2,3] row_mask:0xf bank_mask:0xf\n\t" \
  "v_mov_b32_dpp v13, v5 quad_perm:[0,1,0,1] row_mask:0xf bank_mask:0xf\n\t" \
  "v_mov_b32_dpp v15, v5 quad_perm:[2,3,2,3] row_mask:0xf bank_mask:0xf\n\t" \
  "v_add_f32 v52, v52, v54\n\t" \
  "v_add_f32 v53, v53, v55\n\t" \
  "s_waitcnt vmcnt(2)\n\t" \
  "v_mfma_f32_16x16x32_bf16 v[0:3], v[60:63], v[12:15], 0\n\t" \
  "ds_read_b128 v[36:39], v70 offset:20480\n\t" \
  "v_add_f32 v56, v56, v58\n\t" \
  "v_add_f32 v57, v57, v59\n\t" \
  "v_cvt_pk_bf16_f32 v52, v52, v53\n\t" \
  "v_cvt_pk_bf16_f32 v53, v56, v57\n\t" \
  "s_waitcnt vmcnt(1)\n\t" \
  "v_mfma_f32_16x16x32_bf16 v[4:7], v[64:67], v[12:15], 0\n\t" \
  "ds_read_b128 v[40:43], v70 offset:21504\n\t" \
  "v_mov_b64 v[56:57], s[88:89]\n\t" \
  "v_mov_b64 v[58:59], s[88:89]\n\t" \
  "v_mov_b32_dpp v78, v76 quad_perm:[0,1,0,1] row_mask:0xf bank_mask:0xf\n\t" \
  "v_mov_b32_dpp v79, v76 quad_perm:[2,3,2,3] row_mask:0xf bank_mask:0xf\n\t" \
  "v_mfma_f32_16x16x32_bf16 v[32:35], v[56:59], v[12:15], 0\n\t" \
  "ds_read_b128 v[60:63], v70 offset:24576\n\t" \
  "v_cvt_pk_bf16_f32 v11, v78, v79\n\t" \
  "v_cvt_pk_bf16_f32 v8, v78, v79\n\t" \
  "v_mov_b64 v[54:55], 0\n\t" \
  "v_mov_b32 v9, 0\n\t" \
  "v_mov_b32 v10, 0\n\t" \
  "v_mov_b64 v[14:15], 0\n\t" \
  "v_mov_b64 v[12:13], 0\n\t" \
  "v_mfma_f32_16x16x32_bf16 v[32:35], v[54:57], v[8:11], v[32:35]\n\t" \
  "s_cmp_eq_u32 s48, 63\n\t" \
  "v_mfma_f32_16x16x32_bf16 v[0:3], v[52:55], v[8:11], v[0:3]\n\t" \
  "buffer_store_dwordx2 v[52:53], v71, s[28:31], s69 offen offset:3580 sc1\n\t" \
  "ds_read_b128 v[64:67], v70 offset:25600\n\t" \
  "v_mov_b64 v[56:57], 0\n\t" \
  "v_mov_b64 v[58:59], 0\n\t" \
  "v_mfma_f32_16x16x32_bf16 v[4:7], v[52:55], v[10:13], v[4:7]\n\t" \
  "s_cbranch_scc1 LLAST3_%=\n\t" \
  "v_rcp_f32 v74, v32\n\t" \
  "v_mov_b32 v11, 0\n\t" \
  "v_mov_b32 v8, 0\n\t" \
  "v_pk_mul_f32 v[0:1], v[0:1], v[74:75] op_sel_hi:[1,0]\n\t" \
  "v_pk_mul_f32 v[2:3], v[2:3], v[74:75] op_sel_hi:[1,0]\n\t" \
  "v_cvt_pk_bf16_f32 v76, v0, v1\n\t" \
  "v_cvt_pk_bf16_f32 v77, v2, v3\n\t" \
  "v_pk_mul_f32 v[4:5], v[4:5], v[74:75] op_sel_hi:[1,0]\n\t" \
  "v_pk_mul_f32 v[6:7], v[6:7], v[74:75] op_sel_hi:[1,0]\n\t" \
  "v_cvt_pk_bf16_f32 v78, v4, v5\n\t" \
  "v_cvt_pk_bf16_f32 v79, v6, v7\n\t" \
  "ds_write_b128 v69, v[76:79] offset:7168\n\t" \
  "s_waitcnt lgkmcnt(2)\n\t" \
  "v_smfmac_f32_16x16x64_bf16 v[12:15], v[76:79], v[44:51], v68\n\t" \
  "ds_read_b128 v[44:47], v70 offset:26624\n\t" \
  "v_smfmac_f32_16x16x64_bf16 v[56:59], v[76:79], v[16:23], v68\n\t" \
  "ds_read_b128 v[48:51], v70 offset:27648\n\t" \
  "v_mov_b64 v[52:53], 0\n\t" \
  "v_smfmac_f32_16x16x64_bf16 v[8:11], v[76:79], v[36:43], v68\n\t" \
  "ds_read_b128 v[16:19], v70 offset:28672\n\t" \
  "v_smfmac_f32_16x16x64_bf16 v[52:55], v[76:79], v[24:31], v68\n\t" \
  "s_waitcnt lgkmcnt(3)\n\t" \
  "s_barrier\n\t" \
  "ds_read_b128 v[0:3], v69 offset:6144\n\t" \
  "ds_read_b128 v[4:7], v69 offset:5120\n\t" \
  "v_lshrrev_b32 v71, 7, v69\n\t" \
  "v_and_b32 v74, 0x70, v69\n\t" \
  "v_lshl_add_u32 v71, v71, 14, v74\n\t" \
  "v_bfe_u32 v72, v69, 2, 7\n\t" \
  "v_and_b32 v74, 0x200, v69\n\t" \
  "s_waitcnt lgkmcnt(1)\n\t" \
  "v_smfmac_f32_16x16x64_bf16 v[8:11], v[0:3], v[60:67], v68\n\t" \
  "ds_read_b128 v[20:23], v70 offset:29696\n\t" \
  "v_mov_b64 v[76:77], 0\n\t" \
  "v_smfmac_f32_16x16x64_bf16 v[12:15], v[0:3], v[200:207], v68\n\t" \
  "ds_read_b128 v[32:35], v69 offset:4096\n\t" \
  "v_mov_b64 v[78:79], 0\n\t" \
  "v_smfmac_f32_16x16x64_bf16 v[52:55], v[0:3], v[44:51], v68\n\t" \
  "buffer_load_dword v75, off, s[28:31], s80 sc1\n\t" \
  "ds_read_b128 v[24:27], v70 offset:30720\n\t" \
  "v_lshl_add_u32 v72, v74, 3, v72\n\t" \
  "v_smfmac_f32_16x16x64_bf16 v[56:59], v[0:3], v[232:239], v68\n\t" \
  "ds_read_b128 v[28:31], v70 offset:31744\n\t" \
  "s_waitcnt lgkmcnt(4)\n\t" \
  "v_smfmac_f32_16x16x64_bf16 v[12:15], v[4:7], v[208:215], v68\n\t" \
  "ds_read_b128 v[60:63], v70 offset:32768\n\t" \
  "s_waitcnt lgkmcnt(4)\n\t" \
  "v_smfmac_f32_16x16x64_bf16 v[8:11], v[4:7], v[16:23], v68\n\t" \
  "ds_read_b128 v[64:67], v70 offset:33792\n\t" \
  "s_waitcnt lgkmcnt(4)\n\t" \
  "v_smfmac_f32_16x16x64_bf16 v[8:11], v[32:35], v[192:199], v68\n\t" \
  "ds_read_b128 v[36:39], v73 offset:4096\n\t" \
  "v_smfmac_f32_16x16x64_bf16 v[12:15], v[32:35], v[216:223], v68\n\t" \
  "ds_read_b128 v[40:43], v73 offset:6144\n\t" \
  "s_waitcnt lgkmcnt(4)\n\t" \
  "v_smfmac_f32_16x16x64_bf16 v[52:55], v[4:7], v[24:31], v68\n\t" \
  "ds_read_b128 v[44:47], v70 offset:34816\n\t" \
  "v_cvt_pk_bf16_f32 v8, v8, v9\n\t" \
  "v_smfmac_f32_16x16x64_bf16 v[52:55], v[32:35], v[224:231], v68\n\t" \
  "ds_read_b128 v[48:51], v70 offset:35840\n\t" \
  "v_cvt_pk_bf16_f32 v9, v10, v11\n\t" \
  "v_smfmac_f32_16x16x64_bf16 v[56:59], v[4:7], v[240:247], v68\n\t" \
  "s_waitcnt lgkmcnt(4)\n\t" \
  "v_cvt_pk_bf16_f32 v10, v12, v13\n\t" \
  "v_smfmac_f32_16x16x64_bf16 v[56:59], v[32:35], v[60:67], v68\n\t" \
  "s_waitcnt lgkmcnt(3)\n\t" \
  "v_cvt_pk_bf16_f32 v11, v14, v15\n\t" \
  "v_smfmac_f32_16x16x64_bf16 v[76:79], v[36:39], v[248:255], v68\n\t" \
  "buffer_store_dwordx4 v[8:11], v71, s[28:31], s54 offen sc1\n\t" \
  "s_waitcnt lgkmcnt(0)\n\t" \
  "v_cvt_pk_bf16_f32 v52, v52, v53\n\t" \
  "v_smfmac_f32_16x16x64_bf16 v[76:79], v[40:43], v[44:51], v68\n\t" \
  "v_cvt_pk_bf16_f32 v53, v54, v55\n\t" \
  "v_cvt_pk_bf16_f32 v54, v56, v57\n\t" \
  "v_cvt_pk_bf16_f32 v55, v58, v59\n\t" \
  "buffer_store_dwordx4 v[52:55], v71, s[28:31], s75 offen sc1\n\t" \
  "s_nop 2\n\t" \
  "v_add_f32 v76, v76, v78\n\t" \
  "v_add_f32 v77, v77, v79\n\t" \
  "v_cvt_pk_bf16_f32 v76, v76, v77\n\t" \
  "buffer_store_dword v76, v72, s[28:31], s55 offen sc1\n\t" \
  "s_memrealtime s[70:71]\n\t" \
  "s_waitcnt vmcnt(3)\n\t" \
  "v_readfirstlane_b32 s65, v75\n\t" \
  "s_barrier\n\t" \
  "s_waitcnt lgkmcnt(0)\n\t" \
  "s_not_b32 s65, s65\n\t" \
  "s_lshl_b32 s61, s70, 8\n\t" \
  "s_andn2_b32 s64, s65, 0xff\n\t" \
  "s_sub_u32 s64, s64, s61\n\t" \
  "s_ashr_i32 s64, s64, 8\n\t" \
  "s_cmp_eq_u32 s65, 0\n\t" \
  "s_cselect_b32 s64, " CH_DLF ", s64\n\t" \
  "s_add_i32 s64, s64, " CH_RO3 "\n\t" \
  "s_min_i32 s64, s64, s34\n\t" \
  "s_max_i32 s64, s64, " CH_DLL1 "\n\t" \
  "s_add_u32 s64, s64, s70\n\t" \
  "s_mov_b32 s74, 0\n\t" \
  "v_and_b32 v37, 0x30, v69\n\t" \
  "v_add_u32 v37, s52, v37\n\t" \
  "s_mov_b32 exec_lo, 0\n\t" \
  "v_mov_b32 v37, s85\n\t" \
  "s_mov_b32 exec_lo, -1\n\t" \
  "s_cmp_lt_i32 s41, 0\n\t" \
  "s_cbranch_scc1 LIDLE_%=\n\t" \
  "s_movk_i32 s60, 8192\n\t" \
  "RWT3_%=:\n\t" \
  "s_memrealtime s[72:73]\n\t" \
  "s_sub_u32 s60, s60, 1\n\t" \
  "s_cselect_b32 s61, 0, -1\n\t" \
  "s_waitcnt lgkmcnt(0)\n\t" \
  "s_sub_i32 s73, s72, s64\n\t" \
  "s_and_b32 s73, s73, s61\n\t" \
  "s_cmp_lt_i32 s73, " CH_FIN "\n\t" \
  "s_cbranch_scc1 RWT3_%=\n\t" \
  "s_cmp_lt_i32 s73, 0\n\t" \
  "s_cbranch_scc1 RWT3_%=f\n\t" \
  "RWT3_%=g:\n\t" \
  "RRETRY3_%=:\n\t" \
  "buffer_load_dwordx4 v[0:3], v69, s[28:31], s97 offen offset:0 sc1\n\t" \
  "buffer_load_dwordx4 v[4:7], v69, s[28:31], s97 offen offset:1024 sc1\n\t" \
  "buffer_load_dwordx4 v[8:11], v69, s[28:31], s97 offen offset:2048 sc1\n\t" \
  "buffer_load_dwordx4 v[12:15], v69, s[28:31], s97 offen offset:3072 sc1\n\t" \
  "v_lshrrev_b32 v20, 2, v69\n\t" \
  "v_and_b32 v20, 16, v20\n\t" \
  "v_mov_b32 v21, 0x3f80\n\t" \
  "v_lshlrev_b32 v21, v20, v21\n\t" \
  "v_bfe_u32 v20, v69, 4, 2\n\t" \
  "v_cmp_eq_u32 vcc, 0, v20\n\t" \
  "s_nop 1\n\t" \
  "v_cndmask_b32 v32, 0, v21, vcc\n\t" \
  "v_cmp_eq_u32 vcc, 1, v20\n\t" \
  "s_nop 1\n\t" \
  "v_cndmask_b32 v33, 0, v21, vcc\n\t" \
  "v_cmp_eq_u32 vcc, 2, v20\n\t" \
  "s_nop 1\n\t" \
  "v_cndmask_b32 v34, 0, v21, vcc\n\t" \
  "v_cmp_eq_u32 vcc, 3, v20\n\t" \
  "s_nop 1\n\t" \
  "v_cndmask_b32 v35, 0, v21, vcc\n\t" \
  "v_bfe_u32 v38, v69, 7, 1\n\t" \
  "v_bfe_u32 v20, v69, 9, 1\n\t" \
  "v_lshl_add_u32 v38, v20, 2, v38\n\t" \
  "v_lshlrev_b32 v38, 12, v38\n\t" \
  "v_and_b32 v20, 0x60, v69\n\t" \
  "v_add_u32 v38, v38, v20\n\t" \
  "v_bfe_u32 v20, v69, 8, 1\n\t" \
  "v_lshl_add_u32 v38, v20, 4, v38\n\t" \
  "v_bfe_u32 v20, v69, 4, 1\n\t" \
  "v_lshl_add_u32 v38, v20, 3, v38\n\t" \
  "v_mov_b32 v44, 0\n\t" \
  "buffer_load_dword v39, v37, s[28:31], 0 offen sc1\n\t" \
  CH_ALD \
  "s_waitcnt vmcnt(4)\n\t" \
  "v_mfma_f32_16x16x32_bf16 v[40:43], v[32:35], v[0:3], 0\n\t" \
  "v_max3_u32 v44, v44, v0, v1\n\t" \
  "v_max3_u32 v44, v44, v2, v3\n\t" \
  "s_waitcnt vmcnt(3)\n\t" \
  "v_mfma_f32_16x16x32_bf16 v[40:43], v[32:35], v[4:7], v[40:43]\n\t" \
  "v_max3_u32 v44, v44, v4, v5\n\t" \
  "v_max3_u32 v44, v44, v6, v7\n\t" \
  "s_waitcnt vmcnt(2)\n\t" \
  "v_mfma_f32_16x16x32_bf16 v[40:43], v[32:35], v[8:11], v[40:43]\n\t" \
  "v_max3_u32 v44, v44, v8, v9\n\t" \
  "v_max3_u32 v44, v44, v10, v11\n\t" \
  "s_waitcnt vmcnt(1)\n\t" \
  "v_mfma_f32_16x16x32_bf16 v[40:43], v[32:35], v[12:15], v[40:43]\n\t" \
  "v_max3_u32 v44, v44, v12, v13\n\t" \
  "v_max3_u32 v44, v44, v14, v15\n\t" \
  "v_cmp_eq_u32 vcc, -1, v44\n\t" \
  "s_cbranch_vccnz RFAIL3_%=\n\t" \
  "s_nop 3\n\t" \
  "v_add_f32_dpp v40, v40, v40 row_ror:8 row_mask:0xf bank_mask:0xf\n\t" \
  "v_add_f32_dpp v41, v41, v41 row_ror:8 row_mask:0xf bank_mask:0xf\n\t" \
  "v_add_f32_dpp v42, v42, v42 row_ror:8 row_mask:0xf bank_mask:0xf\n\t" \
  "v_add_f32_dpp v43, v43, v43 row_ror:8 row_mask:0xf bank_mask:0xf\n\t" \
  "v_cvt_pk_bf16_f32 v40, v40, v41\n\t" \
  "v_cvt_pk_bf16_f32 v41, v42, v43\n\t" \
  "buffer_store_dwordx2 v[40:41], v38, s[28:31], s98 offen sc1\n\t" \
  "RDONE3_%=:\n\t" \
  "s_barrier\n\t" \
  "s_waitcnt vmcnt(1)\n\t" \
  "v_cmp_eq_u32 vcc, 0, v39\n\t" \
  "s_sub_u32 s60, s70, s66\n\t" \
  "s_add_u32 s81, s81, 1\n\t" \
  "s_min_u32 s81, s81, s60\n\t" \
  "s_add_u32 s61, s81, 8\n\t" \
  "s_min_u32 s60, s60, s61\n\t" \
  "s_lshl_b32 s60, s60, 2\n\t" \
  "s_lshr_b32 s61, s20, 3\n\t" \
  "s_sub_u32 s20, s20, s61\n\t" \
  "s_add_u32 s20, s20, s60\n\t" \
  "s_add_u32 s48, s48, 1\n\t" \
  "s_mov_b32 s49, s53\n\t" \
  "s_add_u32 s53, s53, " CH_SLOT "\n\t" \
  "s_add_u32 s60, s53, " CH_SKIP "\n\t" \
  "s_lshr_b32 s61, s96, 1\n\t" \
  "s_add_u32 s86, s86, 1\n\t" \
  "s_cmp_eq_u32 s86, 3\n\t" \
  "s_cselect_b32 s53, s60, s53\n\t" \
  "s_cselect_b32 s96, s61, s96\n\t" \
  "s_cselect_b32 s86, 0, s86\n\t" \
  "s_cmp_ge_u32 s53, " CH_RING "\n\t" \
  "s_cselect_b32 s53, 0, s53\n\t" \
  "s_cselect_b32 s96, s21, s96\n\t" \
  "s_andn2_b32 s53, s53, 0x2000\n\t" \
  "s_and_b32 s60, s96, 1\n\t" \
  "s_lshl_b32 s60, s60, 13\n\t" \
  "s_or_b32 s53, s53, s60\n\t" \
  "s_mov_b32 s68, 0\n\t" \
  "s_add_u32 s97, s41, s53\n\t" \
  "s_add_u32 s98, s37, s53\n\t" \
  "s_add_u32 s82, s48, 0\n\t" \
  "s_and_b32 s82, s82, " CH_TRM "\n\t" \
  "s_lshr_b64 s[60:61], s[90:91], s82\n\t" \
  "s_and_b32 s60, s60, 1\n\t" \
  "s_lshl_b32 s60, s60, 13\n\t" \
  "s_add_u32 s82, s82, " CH_SG0 "\n\t" \
  "s_lshl_b32 s82, s82, 21\n\t" \
  "s_or_b32 s82, s82, s60\n\t" \
  "s_add_u32 s50, s82, s38\n\t" \
  "s_add_u32 s69, s82, s39\n\t" \
  "s_add_u32 s82, s48, 1\n\t" \
  "s_lshr_b64 s[60:61], s[90:91], s82\n\t" \
  "s_and_b32 s60, s60, 1\n\t" \
  "s_lshl_b32 s60, s60, 13\n\t" \
  "s_add_u32 s82, s82, " CH_SG0 "\n\t" \
  "s_lshl_b32 s82, s82, 21\n\t" \
  "s_or_b32 s82, s82, s60\n\t" \
  "s_add_u32 s52, s82, s43\n\t" \
  "s_add_u32 s82, s48, " CH_ARMD "\n\t" \
  "s_lshr_b64 s[60:61], s[90:91], s82\n\t" \
  "s_and_b32 s60, s60, 1\n\t" \
  "s_lshl_b32 s60, s60, 13\n\t" \
  "s_add_u32 s82, s82, " CH_SG0 "\n\t" \
  "s_lshl_b32 s82, s82, 21\n\t" \
  "s_or_b32 s82, s82, s60\n\t" \
  "s_add_u32 s85, s82, s45\n\t" \
  "s_cmp_lt_i32 s48, " CH_ARMS "\n\t" \
  "s_cselect_b32 s85, s27, s85\n\t" \
  "s_cmp_eq_u32 s34, 0\n\t" \
  "s_cbranch_scc1 LINI_%=\n\t" \
  "LTK_%=:\n\t" \
  CH_TOKCHK \
  "s_mov_b32 s61, 0\n\t" \
  "LTW_%=:\n\t" \
  "buffer_load_dword v1, v37, s[28:31], 0 offen sc1\n\t" \
  "s_waitcnt vmcnt(0)\n\t" \
  "v_cmp_eq_u32 vcc, 0, v1\n\t" \
  "s_cbranch_vccz LG3_%=\n\t" \
  "s_add_u32 s61, s61, 1\n\t" \
  "s_sleep 2\n\t" \
  "s_cmp_gt_u32 s61, s84\n\t" \
  "s_cbranch_scc1 LTG_%=\n\t" \
  "s_and_b32 s60, s61, 1023\n\t" \
  "s_cmp_lg_u32 s60, 0\n\t" \
  "s_cbranch_scc1 LTW_%=\n\t" \
  "buffer_load_dword v46, off, s[28:31], s92 sc1\n\t" \
  "s_waitcnt vmcnt(0)\n\t" \
  "v_readfirstlane_b32 s60, v46\n\t" \
  "s_nop 0\n\t" \
  "s_cmp_eq_u32 s60, 0\n\t" \
  "s_cbranch_scc1 LTW_%=\n\t" \
  "LTG_%=:\n\t" \
  "s_mov_b32 s84, 0\n\t" \
  "s_lshl_b32 s60, s36, 4\n\t" \
  "s_or_b32 s60, s60, s48\n\t" \
  "s_or_b32 s60, s60, 268435456\n\t" \
  "v_mov_b32 v76, s60\n\t" \
  "v_mov_b32 v77, 0\n\t" \
  "s_mov_b64 exec, 1\n\t" \
  "buffer_atomic_cmpswap v[76:77], off, s[28:31], s92 sc1\n\t" \
  "s_mov_b64 exec, -1\n\t" \
  "s_branch LG3_%=\n\t" \
  "LIDLE_%=:\n\t" \
  "s_add_u32 s64, s64, 8\n\t" \
  "s_movk_i32 s60, 8192\n\t" \
  "IWT_%=:\n\t" \
  "s_memrealtime s[72:73]\n\t" \
  "s_sub_u32 s60, s60, 1\n\t" \
  "s_cselect_b32 s61, 0, -1\n\t" \
  "s_waitcnt lgkmcnt(0)\n\t" \
  "s_sub_i32 s73, s72, s64\n\t" \
  "s_and_b32 s73, s73, s61\n\t" \
  "s_cmp_lt_i32 s73, 0\n\t" \
  "s_cbranch_scc1 IWT_%=\n\t" \
  "buffer_load_dword v39, v37, s[28:31], 0 offen sc1\n\t" \
  "buffer_store_dword v37, off, s[28:31], s41\n\t" \
  "s_branch RDONE3_%=\n\t" \
  "RFAIL3_%=:\n\t" \
  "s_add_u32 s74, s74, 1\n\t" \
  "s_sleep " CH_RSL "\n\t" \
  "s_waitcnt vmcnt(0)\n\t" \
  "s_cmp_gt_u32 s74, s84\n\t" \
  "s_cbranch_scc1 RG3_%=\n\t" \
  "s_and_b32 s60, s74, 1023\n\t" \
  "s_cmp_lg_u32 s60, 0\n\t" \
  "s_cbranch_scc1 RRETRY3_%=\n\t" \
  "buffer_load_dword v46, off, s[28:31], s92 sc1\n\t" \
  "s_waitcnt vmcnt(0)\n\t" \
  "v_readfirstlane_b32 s60, v46\n\t" \
  "s_nop 0\n\t" \
  "s_cmp_eq_u32 s60, 0\n\t" \
  "s_cbranch_scc1 RRETRY3_%=\n\t" \
  "RG3_%=:\n\t" \
  "s_mov_b32 s84, 0\n\t" \
  "s_lshl_b32 s60, s36, 4\n\t" \
  "s_or_b32 s60, s60, s48\n\t" \
  "s_or_b32 s60, s60, 536870912\n\t" \
  "v_mov_b32 v76, s60\n\t" \
  "v_mov_b32 v77, 0\n\t" \
  "s_mov_b64 exec, 1\n\t" \
  "buffer_atomic_cmpswap v[76:77], off, s[28:31], s92 sc1\n\t" \
  "s_mov_b64 exec, -1\n\t" \
  "s_branch RDONE3_%=\n\t" \
  "LFAIL3_%=:\n\t" \
  "v_max_u32 v75, v0, v1\n\t" \
  "v_max3_u32 v75, v75, v2, v3\n\t" \
  "v_max3_u32 v75, v75, v4, v5\n\t" \
  "v_max3_u32 v75, v75, v6, v7\n\t" \
  "v_max3_u32 v75, v75, v8, v9\n\t" \
  "v_max3_u32 v75, v75, v10, v11\n\t" \
  "v_max3_u32 v75, v75, v12, v13\n\t" \
  "v_max3_u32 v75, v75, v14, v15\n\t" \
  "v_max3_u32 v75, v75, v16, v17\n\t" \
  "v_max3_u32 v75, v75, v18, v19\n\t" \
  "v_max3_u32 v75, v75, v20, v21\n\t" \
  "v_max3_u32 v75, v75, v22, v23\n\t" \
  "v_max3_u32 v75, v75, v24, v25\n\t" \
  "v_max3_u32 v75, v75, v26, v27\n\t" \
  "v_max3_u32 v75, v75, v28, v29\n\t" \
  "v_max3_u32 v75, v75, v30, v31\n\t" \
  "v_max3_u32 v75, v75, v32, v33\n\t" \
  "v_max3_u32 v75, v75, v34, v35\n\t" \
  "v_cmp_eq_u32 vcc, -1, v75\n\t" \
  "s_cbranch_vccz LGO3_%=\n\t" \
  "s_add_u32 s68, s68, 1\n\t" \
  "s_waitcnt lgkmcnt(0)\n\t" \
  "s_cmp_gt_u32 s68, s83\n\t" \
  "s_cbranch_scc1 LGV3_%=\n\t" \
  "s_barrier\n\t" \
  "s_waitcnt vmcnt(8)\n\t" \
  "s_branch LG3_%=\n\t" \
  "RWT3_%=f:\n\t" \
  "s_cmp_lt_i32 s73, -2\n\t" \
  "s_cbranch_scc0 RWT3_%=g\n\t" \
  "s_nop 4\n\t" \
  "s_branch RWT3_%=g\n\t" \
  "LGV3_%=:\n\t" \
  "s_mov_b32 s83, 0\n\t" \
  "s_lshl_b32 s60, s36, 4\n\t" \
  "s_or_b32 s60, s60, s48\n\t" \
  "s_or_b32 s60, s60, 805306368\n\t" \
  "v_mov_b32 v76, s60\n\t" \
  "v_mov_b32 v77, 0\n\t" \
  "s_mov_b64 exec, 1\n\t" \
  "buffer_atomic_cmpswap v[76:77], off, s[28:31], s92 sc1\n\t" \
  "s_mov_b64 exec, -1\n\t" \
  "s_branch LGO3_%=\n\t" \
  "LLAST3_%=:\n\t" \
  "s_waitcnt vmcnt(0) lgkmcnt(0)\n\t" \
  "s_barrier\n\t" \
  "LEND_%=:\n\t" \
  ""
typedef unsigned v16u __attribute__((ext_vector_type(16)));
#define CH_WCLOB "a0", "a1", "a2", "a3", "a4", "a5", "a6", "a7", "a8", "a9", "a10", "a11", "a12", "a13", "a14", "a15", "a16", "a17", "a18", "a19", "a20", "a21", "a22", "a23", "a24", "a25", "a26", "a27", "a28", "a29", "a30", "a31", "a32", "a33", "a34", "a35", "a36", "a37", "a38", "a39", "a40", "a41", "a42", "a43", "a44", "a45", "a46", "a47", "a48", "a49", "a50", "a51", "a52", "a53", "a54", "a55", "a56", "a57", "a58", "a59", "a60", "a61", "a62", "a63", "a64", "a65", "a66", "a67", "a68", "a69", "a70", "a71", "a72", "a73", "a74", "a75", "a76", "a77", "a78", "a79", "a80", "a81", "a82", "a83", "a84", "a85", "a86", "a87", "a88", "a89", "a90", "a91", "a92", "a93", "a94", "a95", "a96", "a97", "a98", "a99", "a100", "a101", "a102", "a103", "a104", "a105", "a106", "a107", "a108", "a109", "a110", "a111", "a112", "a113", "a114", "a115", "a116", "a117", "a118", "a119", "a120", "a121", "a122", "a123", "a124", "a125", "a126", "a127", "a128", "a129", "a130", "a131", "a132", "a133", "a134", "a135", "a136", "a137", "a138", "a139", "a140", "a141", "a142", "a143", "a144", "a145", "a146", "a147", "a148", "a149", "a150", "a151", "a152", "a153", "a154", "a155", "a156", "a157", "a158", "a159", "a160", "a161", "a162", "a163", "a164", "a165", "a166", "a167", "a168", "a169", "a170", "a171", "a172", "a173", "a174", "a175", "a176", "a177", "a178", "a179", "a180", "a181", "a182", "a183", "a184", "a185", "a186", "a187", "a188", "a189", "a190", "a191", "a192", "a193", "a194", "a195", "a196", "a197", "a198", "a199", "a200", "a201", "a202", "a203", "a204", "a205", "a206", "a207", "a208", "a209", "a210", "a211", "a212", "a213", "a214", "a215", "a216", "a217", "a218", "a219", "a220", "a221", "a222", "a223", "a224", "a225", "a226", "a227", "a228", "a229", "a230", "a231", "a232", "a233", "a234", "a235", "a236", "a237", "a238", "a239", "a240", "a241", "a242", "a243", "a244", "a245", "a246", "a247", "a248", "a249", "a250", "a251", "a252", "a253", "a254", "a255", "v80", "v81", "v82", "v83", "v84", "v85", "v86", "v87", "v88", "v89", "v90", "v91", "v92", "v93", "v94", "v95", "v96", "v97", "v98", "v99", "v100", "v101", "v102", "v103", "v104", "v105", "v106", "v107", "v108", "v109", "v110", "v111", "v112", "v113", "v114", "v115", "v116", "v117", "v118", "v119", "v120", "v121", "v122", "v123", "v124", "v125", "v126", "v127", "v128", "v129", "v130", "v131", "v132", "v133", "v134", "v135", "v136", "v137", "v138", "v139", "v140", "v141", "v142", "v143", "v144", "v145", "v146", "v147", "v148", "v149", "v150", "v151", "v152", "v153", "v154", "v155", "v156", "v157", "v158", "v159", "v160", "v161", "v162", "v163", "v164", "v165", "v166", "v167", "v168", "v169", "v170", "v171", "v172", "v173", "v174", "v175", "v176", "v177", "v178", "v179", "v180", "v181", "v182", "v183", "v184", "v185", "v186", "v187", "v188", "v189", "v190", "v191", "v192", "v193", "v194", "v195", "v196", "v197", "v198", "v199", "v200", "v201", "v202", "v203", "v204", "v205", "v206", "v207", "v208", "v209", "v210", "v211", "v212", "v213", "v214", "v215", "v216", "v217", "v218", "v219", "v220", "v221", "v222", "v223", "v224", "v225", "v226", "v227", "v228", "v229", "v230", "v231", "v232", "v233", "v234", "v235", "v236", "v237", "v238", "v239", "v240", "v241", "v242", "v243", "v244", "v245", "v246", "v247", "v248", "v249", "v250", "v251", "v252", "v253", "v254", "v255"
#define CH_H(b) (8 * ((b) & 3) + ((b) >> 3))
#define CH_L(b) (((b) >> 2) & 1)
__device__ __forceinline__ void start_work(char* smem, int wk);
__device__ __forceinline__ void chain_wg(char* smem) {
  if (CH_L((int)blockIdx.x)) start_work(smem, CH_H((int)blockIdx.x));
  const MP p = getp();
  const int b = (int)blockIdx.x, h = CH_H(b), L = CH_L(b);
  int w = __builtin_amdgcn_readfirstlane(threadIdx.x >> 6);
  {
    unsigned id; asm volatile("s_getreg_b32 %0, hwreg(HW_REG_HW_ID, 4, 2)" : "=s"(id));
    volatile __attribute__((address_space(3))) unsigned* lw = (volatile __attribute__((address_space(3))) unsigned*)smem;
    lw[w] = 1u << id;
    BAR();
    if (__builtin_amdgcn_readfirstlane(lw[0] | lw[1] | lw[2] | lw[3]) == 15u) w = (int)((SMAP >> (4u * id)) & 3u);
  }
  unsigned lp16 = (threadIdx.x & 63u) * 16u;
  rsrc_t X = mkr(p.xb, XB_BYTES);
  if ((unsigned)(size_t)(__attribute__((address_space(3))) char*)smem != 0u) return;
  const unsigned wsz = DM * HD * 2u - (unsigned)(h * DH) * 2u;
  const rsrc_t RQ = mkr(p.Wq + h * DH, wsz), RK = mkr(p.Wk + h * DH, wsz), RV = mkr(p.Wv + h * DH, wsz), RO = mkr(p.Wo + (long)h * DH * DM, DH * DM * 2u);
  unsigned xcc; asm volatile("s_getreg_b32 %0, hwreg(HW_REG_XCC_ID)" : "=s"(xcc)); xcc &= 3u;
  unsigned d96 = 0u;
  const unsigned c43 = P_FA * PAIR + (unsigned)h * 128u + 32u;
  const unsigned h128 = (unsigned)h * 128u, uw = (unsigned)w, t = uw >> 1, T = uw & 1u, NONE = 0xF0000000u;
  const unsigned u0 = w == 3 ? (unsigned)h >> 2 : (unsigned)((h & 1) + (h >> 3) * 2 + ((h >> 1) & 3) * 8 + (w == 2 ? 32 : 0));
  const bool idle = w == 3 && (h & 3);
  const unsigned c37 = idle ? NONE : (w == 3 ? AGT + u0 * 128u : AGO + (u0 >> 5) * 32768u + (u0 & 31u) * 128u);
  const unsigned c41 = idle ? NONE : (w == 3 ? 524288u + (u0 >> 1) * 16384u + (u0 & 1u) * 4096u : (u0 & 7u) * 16384u + ((u0 >> 3) & 1u) * 4096u + (u0 >> 4) * 131072u);
  const unsigned c42 = AGO + (xcc & 1u) * 16384u + (xcc >> 1) * 4096u;
  const unsigned c46 = (w == 3 ? (h & 7) == 5 : h == 30) ? AGT + 1024u : NONE;
  const unsigned c38 = (unsigned)(3 * h + 1) * PAIR + T * 4096u + t * 2048u, c39 = (P_KV + (unsigned)(h >> 1)) * PAIR + (unsigned)(h & 1) * 4096u;
  const unsigned c45 = P_FB * PAIR + 4096u + (unsigned)(16 + (h & 15)) * 128u;
  unsigned d48 = 0u, d49 = 0u, d53 = SLOT, d86 = 1u, d65 = rtc();
  unsigned mn = 120u;
  v4u stt;
  asm volatile("s_waitcnt vmcnt(0) lgkmcnt(0)\n\t" CH_ASM "LCHEND:"
               : "+{s48}"(d48), "+{s49}"(d49), "+{s53}"(d53), "+{s65}"(d65), "+{s81}"(mn), "+{s86}"(d86), "+{s96}"(d96), "=&{s[76:79]}"(stt), "+{v69}"(lp16), "+{s[28:31]}"(X)
               : [Wq] "s"(RQ), [Wk] "s"(RK), [Wv] "s"(RV), [Wo] "s"(RO), "{s33}"(HT_OFF + 8192u + 256u + (unsigned)b * 4u), "{s36}"(h128), "{s37}"(c37), "{s40}"(w), "{s41}"(c41), "{s42}"(c42), "{s46}"(c46), "{s92}"(TMO_OFF), "{s38}"(c38), "{s39}"(c39), "{s43}"(c43), "{s45}"(c45), "{s32}"(L), "{s26}"(AGT + 1024u + xcc * 128u), "{s27}"(RDY_OFF(0) + (unsigned)((h & 7) * 1024 + (b & 7) * 128)), [ctab] "i"(CTAB_OFF), [ring] "i"(SRING / 3 * XBLK), [trm] "i"(TRING - 1), [ex] "i"(ED * 256 + EB), [dl1l] "i"(DL1 - LATE), [armd] "i"(3 - ARMK), [arms] "i"(ARMK - 3),
                 [cap] "i"(SPIN_CAP)
               : "v0", "v1", "v2", "v3", "v4", "v5", "v6", "v7", "v8", "v9", "v10", "v11", "v12", "v13", "v14", "v15", "v16", "v17", "v18", "v19",
                 "v20", "v21", "v22", "v23", "v24", "v25", "v26", "v27", "v28", "v29", "v30", "v31", "v32", "v33", "v34", "v35", "v36", "v37", "v38", "v39",
                 "v40", "v41", "v42", "v43", "v44", "v45", "v46", "v47", "v48", "v49", "v50", "v51", "v52", "v53", "v54", "v55", "v56", "v57", "v58", "v59",
                 "v60", "v61", "v62", "v63", "v64", "v65", "v66", "v67", "v68", "v70", "v71", "v72", "v73", "v74", "v75", "v76", "v77", "v78", "v79",
                 CH_WCLOB, "s20", "s21", "s34", "s35", "s24", "s25", "s90", "s91", "s47", "s87", "s50", "s51", "s52", "s69", "s82", "s85", "s54", "s75", "s80", "s55", "s56", "s57", "s58", "s59", "s60", "s61", "s62", "s63", "s64", "s66", "s67", "s68", "s70", "s71", "s72", "s73", "s74", "s88", "s89",
                 "s83", "s84", "s97", "s98", "s99", "s93", "s94", "s95", "m0", "vcc", "scc", "memory");
}
#define QLEAD 4
#define RONE 2
#define OAUX 0
#define PFM 1
#define PSP 4
#define QLEAD1 2
#define QXW -18
#define QXC QXW
#define TK_QXW "%[qxw]"
#define TK_QXC "%[qxc]"
#define PSG 16
#define TK_PSG CH_S(PSG)
#define AX 30
#define AX1 45
#define AM 22
#define TK_AM CH_S(AM)
#define AE 61
#define TK_AE CH_S(AE)
#define PSK 2
#define PSH 2
#define TK_CAP "%[cap]"
#define TK_CTAB "%[ctab]"
#define TK_RM "%[rm]"
#define TK_TRM "%[trm]"
#define TK_TRN "%[trn]"
#define TK_SG0 CH_S(SG0)
#define TK_PSK CH_S(PSK)
#define TK_PSH CH_S(PSH)
#define AP_ASM \
  "v_lshrrev_b32 v40, 8, v0\n\t" \
  "v_mul_u32_u24 v40, 4608, v40\n\t" \
  "v_and_b32 v41, 0xf0, v0\n\t" \
  "v_lshrrev_b32 v42, 3, v41\n\t" \
  "v_add3_u32 v41, v41, v40, s64\n\t" \
  "v_add3_u32 v42, v42, v40, s65\n\t" \
  "s_mov_b32 s62, 0x05040100\n\t" \
  "s_mov_b32 s63, 0x07060302\n\t" \
  "s_xor_b32 s60, s66, 0\n\t" \
  "s_add_u32 s60, s60, 0\n\t" \
  "s_add_u32 s61, s60, 18432\n\t" \
  "buffer_load_dwordx4 v[48:51], v41, %[W], s60 offen offset:0\n\t" \
  "buffer_load_dwordx4 v[52:55], v41, %[W], s60 offen offset:1152\n\t" \
  "buffer_load_dwordx4 v[56:59], v41, %[W], s60 offen offset:2304\n\t" \
  "buffer_load_dwordx4 v[60:63], v41, %[W], s60 offen offset:3456\n\t" \
  "buffer_load_dwordx4 v[64:67], v41, %[W], s61 offen offset:0\n\t" \
  "buffer_load_dwordx4 v[68:71], v41, %[W], s61 offen offset:1152\n\t" \
  "buffer_load_dwordx4 v[72:75], v41, %[W], s61 offen offset:2304\n\t" \
  "buffer_load_dwordx4 v[76:79], v41, %[W], s61 offen offset:3456\n\t" \
  "s_xor_b32 s60, s66, 0\n\t" \
  "s_add_u32 s60, s60, 36864\n\t" \
  "s_add_u32 s61, s60, 18432\n\t" \
  "buffer_load_dwordx4 v[80:83], v41, %[W], s60 offen offset:0\n\t" \
  "buffer_load_dwordx4 v[84:87], v41, %[W], s60 offen offset:1152\n\t" \
  "buffer_load_dwordx4 v[88:91], v41, %[W], s60 offen offset:2304\n\t" \
  "buffer_load_dwordx4 v[92:95], v41, %[W], s60 offen offset:3456\n\t" \
  "buffer_load_dwordx4 v[96:99], v41, %[W], s61 offen offset:0\n\t" \
  "buffer_load_dwordx4 v[100:103], v41, %[W], s61 offen offset:1152\n\t" \
  "buffer_load_dwordx4 v[104:107], v41, %[W], s61 offen offset:2304\n\t" \
  "buffer_load_dwordx4 v[108:111], v41, %[W], s61 offen offset:3456\n\t" \
  "s_xor_b32 s60, s66, 0\n\t" \
  "s_add_u32 s60, s60, 73728\n\t" \
  "s_add_u32 s61, s60, 18432\n\t" \
  "buffer_load_dwordx4 v[112:115], v41, %[W], s60 offen offset:0\n\t" \
  "buffer_load_dwordx4 v[116:119], v41, %[W], s60 offen offset:1152\n\t" \
  "buffer_load_dwordx4 v[120:123], v41, %[W], s60 offen offset:2304\n\t" \
  "buffer_load_dwordx4 v[124:127], v41, %[W], s60 offen offset:3456\n\t" \
  "buffer_load_dwordx4 v[128:131], v41, %[W], s61 offen offset:0\n\t" \
  "buffer_load_dwordx4 v[132:135], v41, %[W], s61 offen offset:1152\n\t" \
  "buffer_load_dwordx4 v[136:139], v41, %[W], s61 offen offset:2304\n\t" \
  "buffer_load_dwordx4 v[140:143], v41, %[W], s61 offen offset:3456\n\t" \
  "s_xor_b32 s60, s66, 0\n\t" \
  "s_add_u32 s60, s60, 110592\n\t" \
  "s_add_u32 s61, s60, 18432\n\t" \
  "buffer_load_dwordx4 v[144:147], v41, %[W], s60 offen offset:0\n\t" \
  "buffer_load_dwordx4 v[148:151], v41, %[W], s60 offen offset:1152\n\t" \
  "buffer_load_dwordx4 v[152:155], v41, %[W], s60 offen offset:2304\n\t" \
  "buffer_load_dwordx4 v[156:159], v41, %[W], s60 offen offset:3456\n\t" \
  "buffer_load_dwordx4 v[160:163], v41, %[W], s61 offen offset:0\n\t" \
  "buffer_load_dwordx4 v[164:167], v41, %[W], s61 offen offset:1152\n\t" \
  "buffer_load_dwordx4 v[168:171], v41, %[W], s61 offen offset:2304\n\t" \
  "buffer_load_dwordx4 v[172:175], v41, %[W], s61 offen offset:3456\n\t" \
  "s_xor_b32 s60, s66, 147456\n\t" \
  "s_add_u32 s60, s60, 0\n\t" \
  "s_add_u32 s61, s60, 18432\n\t" \
  "buffer_load_dwordx4 v[176:179], v41, %[W], s60 offen offset:0\n\t" \
  "buffer_load_dwordx4 v[180:183], v41, %[W], s60 offen offset:1152\n\t" \
  "buffer_load_dwordx4 v[184:187], v41, %[W], s60 offen offset:2304\n\t" \
  "buffer_load_dwordx4 v[188:191], v41, %[W], s60 offen offset:3456\n\t" \
  "buffer_load_dwordx4 v[192:195], v41, %[W], s61 offen offset:0\n\t" \
  "buffer_load_dwordx4 v[196:199], v41, %[W], s61 offen offset:1152\n\t" \
  "buffer_load_dwordx4 v[200:203], v41, %[W], s61 offen offset:2304\n\t" \
  "buffer_load_dwordx4 v[204:207], v41, %[W], s61 offen offset:3456\n\t" \
  "s_waitcnt vmcnt(38)\n\t" \
  "v_perm_b32 v44, v52, v48, s62\n\t" \
  "s_waitcnt vmcnt(36)\n\t" \
  "v_perm_b32 v45, v60, v56, s62\n\t" \
  "s_waitcnt vmcnt(34)\n\t" \
  "v_perm_b32 v46, v68, v64, s62\n\t" \
  "s_waitcnt vmcnt(32)\n\t" \
  "v_perm_b32 v47, v76, v72, s62\n\t" \
  "v_accvgpr_write_b32 a0, v44\n\t" \
  "v_accvgpr_write_b32 a1, v45\n\t" \
  "v_accvgpr_write_b32 a2, v46\n\t" \
  "v_accvgpr_write_b32 a3, v47\n\t" \
  "v_perm_b32 v44, v52, v48, s63\n\t" \
  "v_perm_b32 v45, v60, v56, s63\n\t" \
  "v_perm_b32 v46, v68, v64, s63\n\t" \
  "v_perm_b32 v47, v76, v72, s63\n\t" \
  "v_accvgpr_write_b32 a4, v44\n\t" \
  "v_accvgpr_write_b32 a5, v45\n\t" \
  "v_accvgpr_write_b32 a6, v46\n\t" \
  "v_accvgpr_write_b32 a7, v47\n\t" \
  "v_perm_b32 v44, v53, v49, s62\n\t" \
  "v_perm_b32 v45, v61, v57, s62\n\t" \
  "v_perm_b32 v46, v69, v65, s62\n\t" \
  "v_perm_b32 v47, v77, v73, s62\n\t" \
  "v_accvgpr_write_b32 a8, v44\n\t" \
  "v_accvgpr_write_b32 a9, v45\n\t" \
  "v_accvgpr_write_b32 a10, v46\n\t" \
  "v_accvgpr_write_b32 a11, v47\n\t" \
  "v_perm_b32 v44, v53, v49, s63\n\t" \
  "v_perm_b32 v45, v61, v57, s63\n\t" \
  "v_perm_b32 v46, v69, v65, s63\n\t" \
  "v_perm_b32 v47, v77, v73, s63\n\t" \
  "v_accvgpr_write_b32 a12, v44\n\t" \
  "v_accvgpr_write_b32 a13, v45\n\t" \
  "v_accvgpr_write_b32 a14, v46\n\t" \
  "v_accvgpr_write_b32 a15, v47\n\t" \
  "v_perm_b32 v44, v54, v50, s62\n\t" \
  "v_perm_b32 v45, v62, v58, s62\n\t" \
  "v_perm_b32 v46, v70, v66, s62\n\t" \
  "v_perm_b32 v47, v78, v74, s62\n\t" \
  "v_accvgpr_write_b32 a16, v44\n\t" \
  "v_accvgpr_write_b32 a17, v45\n\t" \
  "v_accvgpr_write_b32 a18, v46\n\t" \
  "v_accvgpr_write_b32 a19, v47\n\t" \
  "v_perm_b32 v44, v54, v50, s63\n\t" \
  "v_perm_b32 v45, v62, v58, s63\n\t" \
  "v_perm_b32 v46, v70, v66, s63\n\t" \
  "v_perm_b32 v47, v78, v74, s63\n\t" \
  "v_accvgpr_write_b32 a20, v44\n\t" \
  "v_accvgpr_write_b32 a21, v45\n\t" \
  "v_accvgpr_write_b32 a22, v46\n\t" \
  "v_accvgpr_write_b32 a23, v47\n\t" \
  "v_perm_b32 v44, v55, v51, s62\n\t" \
  "v_perm_b32 v45, v63, v59, s62\n\t" \
  "v_perm_b32 v46, v71, v67, s62\n\t" \
  "v_perm_b32 v47, v79, v75, s62\n\t" \
  "v_accvgpr_write_b32 a24, v44\n\t" \
  "v_accvgpr_write_b32 a25, v45\n\t" \
  "v_accvgpr_write_b32 a26, v46\n\t" \
  "v_accvgpr_write_b32 a27, v47\n\t" \
  "v_perm_b32 v44, v55, v51, s63\n\t" \
  "v_perm_b32 v45, v63, v59, s63\n\t" \
  "v_perm_b32 v46, v71, v67, s63\n\t" \
  "v_perm_b32 v47, v79, v75, s63\n\t" \
  "v_accvgpr_write_b32 a28, v44\n\t" \
  "v_accvgpr_write_b32 a29, v45\n\t" \
  "v_accvgpr_write_b32 a30, v46\n\t" \
  "v_accvgpr_write_b32 a31, v47\n\t" \
  "s_xor_b32 s60, s66, 147456\n\t" \
  "s_add_u32 s60, s60, 36864\n\t" \
  "s_add_u32 s61, s60, 18432\n\t" \
  "buffer_load_dwordx4 v[48:51], v41, %[W], s60 offen offset:0\n\t" \
  "buffer_load_dwordx4 v[52:55], v41, %[W], s60 offen offset:1152\n\t" \
  "buffer_load_dwordx4 v[56:59], v41, %[W], s60 offen offset:2304\n\t" \
  "buffer_load_dwordx4 v[60:63], v41, %[W], s60 offen offset:3456\n\t" \
  "buffer_load_dwordx4 v[64:67], v41, %[W], s61 offen offset:0\n\t" \
  "buffer_load_dwordx4 v[68:71], v41, %[W], s61 offen offset:1152\n\t" \
  "buffer_load_dwordx4 v[72:75], v41, %[W], s61 offen offset:2304\n\t" \
  "buffer_load_dwordx4 v[76:79], v41, %[W], s61 offen offset:3456\n\t" \
  "s_waitcnt vmcnt(38)\n\t" \
  "v_perm_b32 v44, v84, v80, s62\n\t" \
  "s_waitcnt vmcnt(36)\n\t" \
  "v_perm_b32 v45, v92, v88, s62\n\t" \
  "s_waitcnt vmcnt(34)\n\t" \
  "v_perm_b32 v46, v100, v96, s62\n\t" \
  "s_waitcnt vmcnt(32)\n\t" \
  "v_perm_b32 v47, v108, v104, s62\n\t" \
  "v_accvgpr_write_b32 a32, v44\n\t" \
  "v_accvgpr_write_b32 a33, v45\n\t" \
  "v_accvgpr_write_b32 a34, v46\n\t" \
  "v_accvgpr_write_b32 a35, v47\n\t" \
  "v_perm_b32 v44, v84, v80, s63\n\t" \
  "v_perm_b32 v45, v92, v88, s63\n\t" \
  "v_perm_b32 v46, v100, v96, s63\n\t" \
  "v_perm_b32 v47, v108, v104, s63\n\t" \
  "v_accvgpr_write_b32 a36, v44\n\t" \
  "v_accvgpr_write_b32 a37, v45\n\t" \
  "v_accvgpr_write_b32 a38, v46\n\t" \
  "v_accvgpr_write_b32 a39, v47\n\t" \
  "v_perm_b32 v44, v85, v81, s62\n\t" \
  "v_perm_b32 v45, v93, v89, s62\n\t" \
  "v_perm_b32 v46, v101, v97, s62\n\t" \
  "v_perm_b32 v47, v109, v105, s62\n\t" \
  "v_accvgpr_write_b32 a40, v44\n\t" \
  "v_accvgpr_write_b32 a41, v45\n\t" \
  "v_accvgpr_write_b32 a42, v46\n\t" \
  "v_accvgpr_write_b32 a43, v47\n\t" \
  "v_perm_b32 v44, v85, v81, s63\n\t" \
  "v_perm_b32 v45, v93, v89, s63\n\t" \
  "v_perm_b32 v46, v101, v97, s63\n\t" \
  "v_perm_b32 v47, v109, v105, s63\n\t" \
  "v_accvgpr_write_b32 a44, v44\n\t" \
  "v_accvgpr_write_b32 a45, v45\n\t" \
  "v_accvgpr_write_b32 a46, v46\n\t" \
  "v_accvgpr_write_b32 a47, v47\n\t" \
  "v_perm_b32 v44, v86, v82, s62\n\t" \
  "v_perm_b32 v45, v94, v90, s62\n\t" \
  "v_perm_b32 v46, v102, v98, s62\n\t" \
  "v_perm_b32 v47, v110, v106, s62\n\t" \
  "v_accvgpr_write_b32 a48, v44\n\t" \
  "v_accvgpr_write_b32 a49, v45\n\t" \
  "v_accvgpr_write_b32 a50, v46\n\t" \
  "v_accvgpr_write_b32 a51, v47\n\t" \
  "v_perm_b32 v44, v86, v82, s63\n\t" \
  "v_perm_b32 v45, v94, v90, s63\n\t" \
  "v_perm_b32 v46, v102, v98, s63\n\t" \
  "v_perm_b32 v47, v110, v106, s63\n\t" \
  "v_accvgpr_write_b32 a52, v44\n\t" \
  "v_accvgpr_write_b32 a53, v45\n\t" \
  "v_accvgpr_write_b32 a54, v46\n\t" \
  "v_accvgpr_write_b32 a55, v47\n\t" \
  "v_perm_b32 v44, v87, v83, s62\n\t" \
  "v_perm_b32 v45, v95, v91, s62\n\t" \
  "v_perm_b32 v46, v103, v99, s62\n\t" \
  "v_perm_b32 v47, v111, v107, s62\n\t" \
  "v_accvgpr_write_b32 a56, v44\n\t" \
  "v_accvgpr_write_b32 a57, v45\n\t" \
  "v_accvgpr_write_b32 a58, v46\n\t" \
  "v_accvgpr_write_b32 a59, v47\n\t" \
  "v_perm_b32 v44, v87, v83, s63\n\t" \
  "v_perm_b32 v45, v95, v91, s63\n\t" \
  "v_perm_b32 v46, v103, v99, s63\n\t" \
  "v_perm_b32 v47, v111, v107, s63\n\t" \
  "v_accvgpr_write_b32 a60, v44\n\t" \
  "v_accvgpr_write_b32 a61, v45\n\t" \
  "v_accvgpr_write_b32 a62, v46\n\t" \
  "v_accvgpr_write_b32 a63, v47\n\t" \
  "s_xor_b32 s60, s66, 147456\n\t" \
  "s_add_u32 s60, s60, 73728\n\t" \
  "s_add_u32 s61, s60, 18432\n\t" \
  "buffer_load_dwordx4 v[80:83], v41, %[W], s60 offen offset:0\n\t" \
  "buffer_load_dwordx4 v[84:87], v41, %[W], s60 offen offset:1152\n\t" \
  "buffer_load_dwordx4 v[88:91], v41, %[W], s60 offen offset:2304\n\t" \
  "buffer_load_dwordx4 v[92:95], v41, %[W], s60 offen offset:3456\n\t" \
  "buffer_load_dwordx4 v[96:99], v41, %[W], s61 offen offset:0\n\t" \
  "buffer_load_dwordx4 v[100:103], v41, %[W], s61 offen offset:1152\n\t" \
  "buffer_load_dwordx4 v[104:107], v41, %[W], s61 offen offset:2304\n\t" \
  "buffer_load_dwordx4 v[108:111], v41, %[W], s61 offen offset:3456\n\t" \
  "s_waitcnt vmcnt(38)\n\t" \
  "v_perm_b32 v44, v116, v112, s62\n\t" \
  "s_waitcnt vmcnt(36)\n\t" \
  "v_perm_b32 v45, v124, v120, s62\n\t" \
  "s_waitcnt vmcnt(34)\n\t" \
  "v_perm_b32 v46, v132, v128, s62\n\t" \
  "s_waitcnt vmcnt(32)\n\t" \
  "v_perm_b32 v47, v140, v136, s62\n\t" \
  "v_accvgpr_write_b32 a64, v44\n\t" \
  "v_accvgpr_write_b32 a65, v45\n\t" \
  "v_accvgpr_write_b32 a66, v46\n\t" \
  "v_accvgpr_write_b32 a67, v47\n\t" \
  "v_perm_b32 v44, v116, v112, s63\n\t" \
  "v_perm_b32 v45, v124, v120, s63\n\t" \
  "v_perm_b32 v46, v132, v128, s63\n\t" \
  "v_perm_b32 v47, v140, v136, s63\n\t" \
  "v_accvgpr_write_b32 a68, v44\n\t" \
  "v_accvgpr_write_b32 a69, v45\n\t" \
  "v_accvgpr_write_b32 a70, v46\n\t" \
  "v_accvgpr_write_b32 a71, v47\n\t" \
  "v_perm_b32 v44, v117, v113, s62\n\t" \
  "v_perm_b32 v45, v125, v121, s62\n\t" \
  "v_perm_b32 v46, v133, v129, s62\n\t" \
  "v_perm_b32 v47, v141, v137, s62\n\t" \
  "v_accvgpr_write_b32 a72, v44\n\t" \
  "v_accvgpr_write_b32 a73, v45\n\t" \
  "v_accvgpr_write_b32 a74, v46\n\t" \
  "v_accvgpr_write_b32 a75, v47\n\t" \
  "v_perm_b32 v44, v117, v113, s63\n\t" \
  "v_perm_b32 v45, v125, v121, s63\n\t" \
  "v_perm_b32 v46, v133, v129, s63\n\t" \
  "v_perm_b32 v47, v141, v137, s63\n\t" \
  "v_accvgpr_write_b32 a76, v44\n\t" \
  "v_accvgpr_write_b32 a77, v45\n\t" \
  "v_accvgpr_write_b32 a78, v46\n\t" \
  "v_accvgpr_write_b32 a79, v47\n\t" \
  "v_perm_b32 v44, v118, v114, s62\n\t" \
  "v_perm_b32 v45, v126, v122, s62\n\t" \
  "v_perm_b32 v46, v134, v130, s62\n\t" \
  "v_perm_b32 v47, v142, v138, s62\n\t" \
  "v_accvgpr_write_b32 a80, v44\n\t" \
  "v_accvgpr_write_b32 a81, v45\n\t" \
  "v_accvgpr_write_b32 a82, v46\n\t" \
  "v_accvgpr_write_b32 a83, v47\n\t" \
  "v_perm_b32 v44, v118, v114, s63\n\t" \
  "v_perm_b32 v45, v126, v122, s63\n\t" \
  "v_perm_b32 v46, v134, v130, s63\n\t" \
  "v_perm_b32 v47, v142, v138, s63\n\t" \
  "v_accvgpr_write_b32 a84, v44\n\t" \
  "v_accvgpr_write_b32 a85, v45\n\t" \
  "v_accvgpr_write_b32 a86, v46\n\t" \
  "v_accvgpr_write_b32 a87, v47\n\t" \
  "v_perm_b32 v44, v119, v115, s62\n\t" \
  "v_perm_b32 v45, v127, v123, s62\n\t" \
  "v_perm_b32 v46, v135, v131, s62\n\t" \
  "v_perm_b32 v47, v143, v139, s62\n\t" \
  "v_accvgpr_write_b32 a88, v44\n\t" \
  "v_accvgpr_write_b32 a89, v45\n\t" \
  "v_accvgpr_write_b32 a90, v46\n\t" \
  "v_accvgpr_write_b32 a91, v47\n\t" \
  "v_perm_b32 v44, v119, v115, s63\n\t" \
  "v_perm_b32 v45, v127, v123, s63\n\t" \
  "v_perm_b32 v46, v135, v131, s63\n\t" \
  "v_perm_b32 v47, v143, v139, s63\n\t" \
  "v_accvgpr_write_b32 a92, v44\n\t" \
  "v_accvgpr_write_b32 a93, v45\n\t" \
  "v_accvgpr_write_b32 a94, v46\n\t" \
  "v_accvgpr_write_b32 a95, v47\n\t" \
  "s_xor_b32 s60, s66, 147456\n\t" \
  "s_add_u32 s60, s60, 110592\n\t" \
  "s_add_u32 s61, s60, 18432\n\t" \
  "buffer_load_dwordx4 v[112:115], v41, %[W], s60 offen offset:0\n\t" \
  "buffer_load_dwordx4 v[116:119], v41, %[W], s60 offen offset:1152\n\t" \
  "buffer_load_dwordx4 v[120:123], v41, %[W], s60 offen offset:2304\n\t" \
  "buffer_load_dwordx4 v[124:127], v41, %[W], s60 offen offset:3456\n\t" \
  "buffer_load_dwordx4 v[128:131], v41, %[W], s61 offen offset:0\n\t" \
  "buffer_load_dwordx4 v[132:135], v41, %[W], s61 offen offset:1152\n\t" \
  "buffer_load_dwordx4 v[136:139], v41, %[W], s61 offen offset:2304\n\t" \
  "buffer_load_dwordx4 v[140:143], v41, %[W], s61 offen offset:3456\n\t" \
  "s_waitcnt vmcnt(38)\n\t" \
  "v_perm_b32 v44, v148, v144, s62\n\t" \
  "s_waitcnt vmcnt(36)\n\t" \
  "v_perm_b32 v45, v156, v152, s62\n\t" \
  "s_waitcnt vmcnt(34)\n\t" \
  "v_perm_b32 v46, v164, v160, s62\n\t" \
  "s_waitcnt vmcnt(32)\n\t" \
  "v_perm_b32 v47, v172, v168, s62\n\t" \
  "v_accvgpr_write_b32 a96, v44\n\t" \
  "v_accvgpr_write_b32 a97, v45\n\t" \
  "v_accvgpr_write_b32 a98, v46\n\t" \
  "v_accvgpr_write_b32 a99, v47\n\t" \
  "v_perm_b32 v44, v148, v144, s63\n\t" \
  "v_perm_b32 v45, v156, v152, s63\n\t" \
  "v_perm_b32 v46, v164, v160, s63\n\t" \
  "v_perm_b32 v47, v172, v168, s63\n\t" \
  "v_accvgpr_write_b32 a100, v44\n\t" \
  "v_accvgpr_write_b32 a101, v45\n\t" \
  "v_accvgpr_write_b32 a102, v46\n\t" \
  "v_accvgpr_write_b32 a103, v47\n\t" \
  "v_perm_b32 v44, v149, v145, s62\n\t" \
  "v_perm_b32 v45, v157, v153, s62\n\t" \
  "v_perm_b32 v46, v165, v161, s62\n\t" \
  "v_perm_b32 v47, v173, v169, s62\n\t" \
  "v_accvgpr_write_b32 a104, v44\n\t" \
  "v_accvgpr_write_b32 a105, v45\n\t" \
  "v_accvgpr_write_b32 a106, v46\n\t" \
  "v_accvgpr_write_b32 a107, v47\n\t" \
  "v_perm_b32 v44, v149, v145, s63\n\t" \
  "v_perm_b32 v45, v157, v153, s63\n\t" \
  "v_perm_b32 v46, v165, v161, s63\n\t" \
  "v_perm_b32 v47, v173, v169, s63\n\t" \
  "v_accvgpr_write_b32 a108, v44\n\t" \
  "v_accvgpr_write_b32 a109, v45\n\t" \
  "v_accvgpr_write_b32 a110, v46\n\t" \
  "v_accvgpr_write_b32 a111, v47\n\t" \
  "v_perm_b32 v44, v150, v146, s62\n\t" \
  "v_perm_b32 v45, v158, v154, s62\n\t" \
  "v_perm_b32 v46, v166, v162, s62\n\t" \
  "v_perm_b32 v47, v174, v170, s62\n\t" \
  "v_accvgpr_write_b32 a112, v44\n\t" \
  "v_accvgpr_write_b32 a113, v45\n\t" \
  "v_accvgpr_write_b32 a114, v46\n\t" \
  "v_accvgpr_write_b32 a115, v47\n\t" \
  "v_perm_b32 v44, v150, v146, s63\n\t" \
  "v_perm_b32 v45, v158, v154, s63\n\t" \
  "v_perm_b32 v46, v166, v162, s63\n\t" \
  "v_perm_b32 v47, v174, v170, s63\n\t" \
  "v_accvgpr_write_b32 a116, v44\n\t" \
  "v_accvgpr_write_b32 a117, v45\n\t" \
  "v_accvgpr_write_b32 a118, v46\n\t" \
  "v_accvgpr_write_b32 a119, v47\n\t" \
  "v_perm_b32 v44, v151, v147, s62\n\t" \
  "v_perm_b32 v45, v159, v155, s62\n\t" \
  "v_perm_b32 v46, v167, v163, s62\n\t" \
  "v_perm_b32 v47, v175, v171, s62\n\t" \
  "v_accvgpr_write_b32 a120, v44\n\t" \
  "v_accvgpr_write_b32 a121, v45\n\t" \
  "v_accvgpr_write_b32 a122, v46\n\t" \
  "v_accvgpr_write_b32 a123, v47\n\t" \
  "v_perm_b32 v44, v151, v147, s63\n\t" \
  "v_perm_b32 v45, v159, v155, s63\n\t" \
  "v_perm_b32 v46, v167, v163, s63\n\t" \
  "v_perm_b32 v47, v175, v171, s63\n\t" \
  "v_accvgpr_write_b32 a124, v44\n\t" \
  "v_accvgpr_write_b32 a125, v45\n\t" \
  "v_accvgpr_write_b32 a126, v46\n\t" \
  "v_accvgpr_write_b32 a127, v47\n\t" \
  "s_xor_b32 s60, s66, 0\n\t" \
  "s_add_u32 s60, s60, 0\n\t" \
  "s_add_u32 s61, s60, 18432\n\t" \
  "buffer_load_ushort v224, v42, %[W], s60 offen offset:0\n\t" \
  "buffer_load_ushort v144, v42, %[W], s60 offen offset:1152\n\t" \
  "buffer_load_ushort v225, v42, %[W], s60 offen offset:2304\n\t" \
  "buffer_load_ushort v145, v42, %[W], s60 offen offset:3456\n\t" \
  "buffer_load_ushort v226, v42, %[W], s61 offen offset:0\n\t" \
  "buffer_load_ushort v146, v42, %[W], s61 offen offset:1152\n\t" \
  "buffer_load_ushort v227, v42, %[W], s61 offen offset:2304\n\t" \
  "buffer_load_ushort v147, v42, %[W], s61 offen offset:3456\n\t" \
  "s_xor_b32 s60, s66, 0\n\t" \
  "s_add_u32 s60, s60, 36864\n\t" \
  "s_add_u32 s61, s60, 18432\n\t" \
  "buffer_load_ushort v228, v42, %[W], s60 offen offset:0\n\t" \
  "buffer_load_ushort v148, v42, %[W], s60 offen offset:1152\n\t" \
  "buffer_load_ushort v229, v42, %[W], s60 offen offset:2304\n\t" \
  "buffer_load_ushort v149, v42, %[W], s60 offen offset:3456\n\t" \
  "buffer_load_ushort v230, v42, %[W], s61 offen offset:0\n\t" \
  "buffer_load_ushort v150, v42, %[W], s61 offen offset:1152\n\t" \
  "buffer_load_ushort v231, v42, %[W], s61 offen offset:2304\n\t" \
  "buffer_load_ushort v151, v42, %[W], s61 offen offset:3456\n\t" \
  "s_waitcnt vmcnt(46)\n\t" \
  "v_perm_b32 v44, v180, v176, s62\n\t" \
  "s_waitcnt vmcnt(44)\n\t" \
  "v_perm_b32 v45, v188, v184, s62\n\t" \
  "s_waitcnt vmcnt(42)\n\t" \
  "v_perm_b32 v46, v196, v192, s62\n\t" \
  "s_waitcnt vmcnt(40)\n\t" \
  "v_perm_b32 v47, v204, v200, s62\n\t" \
  "v_accvgpr_write_b32 a128, v44\n\t" \
  "v_accvgpr_write_b32 a129, v45\n\t" \
  "v_accvgpr_write_b32 a130, v46\n\t" \
  "v_accvgpr_write_b32 a131, v47\n\t" \
  "v_perm_b32 v44, v180, v176, s63\n\t" \
  "v_perm_b32 v45, v188, v184, s63\n\t" \
  "v_perm_b32 v46, v196, v192, s63\n\t" \
  "v_perm_b32 v47, v204, v200, s63\n\t" \
  "v_accvgpr_write_b32 a132, v44\n\t" \
  "v_accvgpr_write_b32 a133, v45\n\t" \
  "v_accvgpr_write_b32 a134, v46\n\t" \
  "v_accvgpr_write_b32 a135, v47\n\t" \
  "v_perm_b32 v44, v181, v177, s62\n\t" \
  "v_perm_b32 v45, v189, v185, s62\n\t" \
  "v_perm_b32 v46, v197, v193, s62\n\t" \
  "v_perm_b32 v47, v205, v201, s62\n\t" \
  "v_accvgpr_write_b32 a136, v44\n\t" \
  "v_accvgpr_write_b32 a137, v45\n\t" \
  "v_accvgpr_write_b32 a138, v46\n\t" \
  "v_accvgpr_write_b32 a139, v47\n\t" \
  "v_perm_b32 v44, v181, v177, s63\n\t" \
  "v_perm_b32 v45, v189, v185, s63\n\t" \
  "v_perm_b32 v46, v197, v193, s63\n\t" \
  "v_perm_b32 v47, v205, v201, s63\n\t" \
  "v_accvgpr_write_b32 a140, v44\n\t" \
  "v_accvgpr_write_b32 a141, v45\n\t" \
  "v_accvgpr_write_b32 a142, v46\n\t" \
  "v_accvgpr_write_b32 a143, v47\n\t" \
  "v_perm_b32 v44, v182, v178, s62\n\t" \
  "v_perm_b32 v45, v190, v186, s62\n\t" \
  "v_perm_b32 v46, v198, v194, s62\n\t" \
  "v_perm_b32 v47, v206, v202, s62\n\t" \
  "v_accvgpr_write_b32 a144, v44\n\t" \
  "v_accvgpr_write_b32 a145, v45\n\t" \
  "v_accvgpr_write_b32 a146, v46\n\t" \
  "v_accvgpr_write_b32 a147, v47\n\t" \
  "v_perm_b32 v44, v182, v178, s63\n\t" \
  "v_perm_b32 v45, v190, v186, s63\n\t" \
  "v_perm_b32 v46, v198, v194, s63\n\t" \
  "v_perm_b32 v47, v206, v202, s63\n\t" \
  "v_accvgpr_write_b32 a148, v44\n\t" \
  "v_accvgpr_write_b32 a149, v45\n\t" \
  "v_accvgpr_write_b32 a150, v46\n\t" \
  "v_accvgpr_write_b32 a151, v47\n\t" \
  "v_perm_b32 v44, v183, v179, s62\n\t" \
  "v_perm_b32 v45, v191, v187, s62\n\t" \
  "v_perm_b32 v46, v199, v195, s62\n\t" \
  "v_perm_b32 v47, v207, v203, s62\n\t" \
  "v_accvgpr_write_b32 a152, v44\n\t" \
  "v_accvgpr_write_b32 a153, v45\n\t" \
  "v_accvgpr_write_b32 a154, v46\n\t" \
  "v_accvgpr_write_b32 a155, v47\n\t" \
  "v_perm_b32 v44, v183, v179, s63\n\t" \
  "v_perm_b32 v45, v191, v187, s63\n\t" \
  "v_perm_b32 v46, v199, v195, s63\n\t" \
  "v_perm_b32 v47, v207, v203, s63\n\t" \
  "v_accvgpr_write_b32 a156, v44\n\t" \
  "v_accvgpr_write_b32 a157, v45\n\t" \
  "v_accvgpr_write_b32 a158, v46\n\t" \
  "v_accvgpr_write_b32 a159, v47\n\t" \
  "s_xor_b32 s60, s66, 0\n\t" \
  "s_add_u32 s60, s60, 73728\n\t" \
  "s_add_u32 s61, s60, 18432\n\t" \
  "buffer_load_ushort v232, v42, %[W], s60 offen offset:0\n\t" \
  "buffer_load_ushort v152, v42, %[W], s60 offen offset:1152\n\t" \
  "buffer_load_ushort v233, v42, %[W], s60 offen offset:2304\n\t" \
  "buffer_load_ushort v153, v42, %[W], s60 offen offset:3456\n\t" \
  "buffer_load_ushort v234, v42, %[W], s61 offen offset:0\n\t" \
  "buffer_load_ushort v154, v42, %[W], s61 offen offset:1152\n\t" \
  "buffer_load_ushort v235, v42, %[W], s61 offen offset:2304\n\t" \
  "buffer_load_ushort v155, v42, %[W], s61 offen offset:3456\n\t" \
  "s_xor_b32 s60, s66, 0\n\t" \
  "s_add_u32 s60, s60, 110592\n\t" \
  "s_add_u32 s61, s60, 18432\n\t" \
  "buffer_load_ushort v236, v42, %[W], s60 offen offset:0\n\t" \
  "buffer_load_ushort v156, v42, %[W], s60 offen offset:1152\n\t" \
  "buffer_load_ushort v237, v42, %[W], s60 offen offset:2304\n\t" \
  "buffer_load_ushort v157, v42, %[W], s60 offen offset:3456\n\t" \
  "buffer_load_ushort v238, v42, %[W], s61 offen offset:0\n\t" \
  "buffer_load_ushort v158, v42, %[W], s61 offen offset:1152\n\t" \
  "buffer_load_ushort v239, v42, %[W], s61 offen offset:2304\n\t" \
  "buffer_load_ushort v159, v42, %[W], s61 offen offset:3456\n\t" \
  "s_waitcnt vmcnt(54)\n\t" \
  "v_perm_b32 v44, v52, v48, s62\n\t" \
  "s_waitcnt vmcnt(52)\n\t" \
  "v_perm_b32 v45, v60, v56, s62\n\t" \
  "s_waitcnt vmcnt(50)\n\t" \
  "v_perm_b32 v46, v68, v64, s62\n\t" \
  "s_waitcnt vmcnt(48)\n\t" \
  "v_perm_b32 v47, v76, v72, s62\n\t" \
  "v_accvgpr_write_b32 a160, v44\n\t" \
  "v_accvgpr_write_b32 a161, v45\n\t" \
  "v_accvgpr_write_b32 a162, v46\n\t" \
  "v_accvgpr_write_b32 a163, v47\n\t" \
  "v_perm_b32 v44, v52, v48, s63\n\t" \
  "v_perm_b32 v45, v60, v56, s63\n\t" \
  "v_perm_b32 v46, v68, v64, s63\n\t" \
  "v_perm_b32 v47, v76, v72, s63\n\t" \
  "v_accvgpr_write_b32 a164, v44\n\t" \
  "v_accvgpr_write_b32 a165, v45\n\t" \
  "v_accvgpr_write_b32 a166, v46\n\t" \
  "v_accvgpr_write_b32 a167, v47\n\t" \
  "v_perm_b32 v44, v53, v49, s62\n\t" \
  "v_perm_b32 v45, v61, v57, s62\n\t" \
  "v_perm_b32 v46, v69, v65, s62\n\t" \
  "v_perm_b32 v47, v77, v73, s62\n\t" \
  "v_accvgpr_write_b32 a168, v44\n\t" \
  "v_accvgpr_write_b32 a169, v45\n\t" \
  "v_accvgpr_write_b32 a170, v46\n\t" \
  "v_accvgpr_write_b32 a171, v47\n\t" \
  "v_perm_b32 v44, v53, v49, s63\n\t" \
  "v_perm_b32 v45, v61, v57, s63\n\t" \
  "v_perm_b32 v46, v69, v65, s63\n\t" \
  "v_perm_b32 v47, v77, v73, s63\n\t" \
  "v_accvgpr_write_b32 a172, v44\n\t" \
  "v_accvgpr_write_b32 a173, v45\n\t" \
  "v_accvgpr_write_b32 a174, v46\n\t" \
  "v_accvgpr_write_b32 a175, v47\n\t" \
  "v_perm_b32 v44, v54, v50, s62\n\t" \
  "v_perm_b32 v45, v62, v58, s62\n\t" \
  "v_perm_b32 v46, v70, v66, s62\n\t" \
  "v_perm_b32 v47, v78, v74, s62\n\t" \
  "v_accvgpr_write_b32 a176, v44\n\t" \
  "v_accvgpr_write_b32 a177, v45\n\t" \
  "v_accvgpr_write_b32 a178, v46\n\t" \
  "v_accvgpr_write_b32 a179, v47\n\t" \
  "v_perm_b32 v44, v54, v50, s63\n\t" \
  "v_perm_b32 v45, v62, v58, s63\n\t" \
  "v_perm_b32 v46, v70, v66, s63\n\t" \
  "v_perm_b32 v47, v78, v74, s63\n\t" \
  "v_accvgpr_write_b32 a180, v44\n\t" \
  "v_accvgpr_write_b32 a181, v45\n\t" \
  "v_accvgpr_write_b32 a182, v46\n\t" \
  "v_accvgpr_write_b32 a183, v47\n\t" \
  "v_perm_b32 v44, v55, v51, s62\n\t" \
  "v_perm_b32 v45, v63, v59, s62\n\t" \
  "v_perm_b32 v46, v71, v67, s62\n\t" \
  "v_perm_b32 v47, v79, v75, s62\n\t" \
  "v_accvgpr_write_b32 a184, v44\n\t" \
  "v_accvgpr_write_b32 a185, v45\n\t" \
  "v_accvgpr_write_b32 a186, v46\n\t" \
  "v_accvgpr_write_b32 a187, v47\n\t" \
  "v_perm_b32 v44, v55, v51, s63\n\t" \
  "v_perm_b32 v45, v63, v59, s63\n\t" \
  "v_perm_b32 v46, v71, v67, s63\n\t" \
  "v_perm_b32 v47, v79, v75, s63\n\t" \
  "v_accvgpr_write_b32 a188, v44\n\t" \
  "v_accvgpr_write_b32 a189, v45\n\t" \
  "v_accvgpr_write_b32 a190, v46\n\t" \
  "v_accvgpr_write_b32 a191, v47\n\t" \
  "s_xor_b32 s60, s66, 147456\n\t" \
  "s_add_u32 s60, s60, 0\n\t" \
  "s_add_u32 s61, s60, 18432\n\t" \
  "buffer_load_ushort v240, v42, %[W], s60 offen offset:0\n\t" \
  "buffer_load_ushort v160, v42, %[W], s60 offen offset:1152\n\t" \
  "buffer_load_ushort v241, v42, %[W], s60 offen offset:2304\n\t" \
  "buffer_load_ushort v161, v42, %[W], s60 offen offset:3456\n\t" \
  "buffer_load_ushort v242, v42, %[W], s61 offen offset:0\n\t" \
  "buffer_load_ushort v162, v42, %[W], s61 offen offset:1152\n\t" \
  "buffer_load_ushort v243, v42, %[W], s61 offen offset:2304\n\t" \
  "buffer_load_ushort v163, v42, %[W], s61 offen offset:3456\n\t" \
  "s_waitcnt vmcnt(54)\n\t" \
  "v_perm_b32 v44, v84, v80, s62\n\t" \
  "s_waitcnt vmcnt(52)\n\t" \
  "v_perm_b32 v45, v92, v88, s62\n\t" \
  "s_waitcnt vmcnt(50)\n\t" \
  "v_perm_b32 v46, v100, v96, s62\n\t" \
  "s_waitcnt vmcnt(48)\n\t" \
  "v_perm_b32 v47, v108, v104, s62\n\t" \
  "v_accvgpr_write_b32 a192, v44\n\t" \
  "v_accvgpr_write_b32 a193, v45\n\t" \
  "v_accvgpr_write_b32 a194, v46\n\t" \
  "v_accvgpr_write_b32 a195, v47\n\t" \
  "v_perm_b32 v44, v84, v80, s63\n\t" \
  "v_perm_b32 v45, v92, v88, s63\n\t" \
  "v_perm_b32 v46, v100, v96, s63\n\t" \
  "v_perm_b32 v47, v108, v104, s63\n\t" \
  "v_accvgpr_write_b32 a196, v44\n\t" \
  "v_accvgpr_write_b32 a197, v45\n\t" \
  "v_accvgpr_write_b32 a198, v46\n\t" \
  "v_accvgpr_write_b32 a199, v47\n\t" \
  "v_perm_b32 v44, v85, v81, s62\n\t" \
  "v_perm_b32 v45, v93, v89, s62\n\t" \
  "v_perm_b32 v46, v101, v97, s62\n\t" \
  "v_perm_b32 v47, v109, v105, s62\n\t" \
  "v_accvgpr_write_b32 a200, v44\n\t" \
  "v_accvgpr_write_b32 a201, v45\n\t" \
  "v_accvgpr_write_b32 a202, v46\n\t" \
  "v_accvgpr_write_b32 a203, v47\n\t" \
  "v_perm_b32 v44, v85, v81, s63\n\t" \
  "v_perm_b32 v45, v93, v89, s63\n\t" \
  "v_perm_b32 v46, v101, v97, s63\n\t" \
  "v_perm_b32 v47, v109, v105, s63\n\t" \
  "v_accvgpr_write_b32 a204, v44\n\t" \
  "v_accvgpr_write_b32 a205, v45\n\t" \
  "v_accvgpr_write_b32 a206, v46\n\t" \
  "v_accvgpr_write_b32 a207, v47\n\t" \
  "v_perm_b32 v44, v86, v82, s62\n\t" \
  "v_perm_b32 v45, v94, v90, s62\n\t" \
  "v_perm_b32 v46, v102, v98, s62\n\t" \
  "v_perm_b32 v47, v110, v106, s62\n\t" \
  "v_accvgpr_write_b32 a208, v44\n\t" \
  "v_accvgpr_write_b32 a209, v45\n\t" \
  "v_accvgpr_write_b32 a210, v46\n\t" \
  "v_accvgpr_write_b32 a211, v47\n\t" \
  "v_perm_b32 v44, v86, v82, s63\n\t" \
  "v_perm_b32 v45, v94, v90, s63\n\t" \
  "v_perm_b32 v46, v102, v98, s63\n\t" \
  "v_perm_b32 v47, v110, v106, s63\n\t" \
  "v_accvgpr_write_b32 a212, v44\n\t" \
  "v_accvgpr_write_b32 a213, v45\n\t" \
  "v_accvgpr_write_b32 a214, v46\n\t" \
  "v_accvgpr_write_b32 a215, v47\n\t" \
  "v_perm_b32 v44, v87, v83, s62\n\t" \
  "v_perm_b32 v45, v95, v91, s62\n\t" \
  "v_perm_b32 v46, v103, v99, s62\n\t" \
  "v_perm_b32 v47, v111, v107, s62\n\t" \
  "v_accvgpr_write_b32 a216, v44\n\t" \
  "v_accvgpr_write_b32 a217, v45\n\t" \
  "v_accvgpr_write_b32 a218, v46\n\t" \
  "v_accvgpr_write_b32 a219, v47\n\t" \
  "v_perm_b32 v44, v87, v83, s63\n\t" \
  "v_perm_b32 v45, v95, v91, s63\n\t" \
  "v_perm_b32 v46, v103, v99, s63\n\t" \
  "v_perm_b32 v47, v111, v107, s63\n\t" \
  "v_accvgpr_write_b32 a220, v44\n\t" \
  "v_accvgpr_write_b32 a221, v45\n\t" \
  "v_accvgpr_write_b32 a222, v46\n\t" \
  "v_accvgpr_write_b32 a223, v47\n\t" \
  "s_xor_b32 s60, s66, 147456\n\t" \
  "s_add_u32 s60, s60, 36864\n\t" \
  "s_add_u32 s61, s60, 18432\n\t" \
  "buffer_load_ushort v244, v42, %[W], s60 offen offset:0\n\t" \
  "buffer_load_ushort v164, v42, %[W], s60 offen offset:1152\n\t" \
  "buffer_load_ushort v245, v42, %[W], s60 offen offset:2304\n\t" \
  "buffer_load_ushort v165, v42, %[W], s60 offen offset:3456\n\t" \
  "buffer_load_ushort v246, v42, %[W], s61 offen offset:0\n\t" \
  "buffer_load_ushort v166, v42, %[W], s61 offen offset:1152\n\t" \
  "buffer_load_ushort v247, v42, %[W], s61 offen offset:2304\n\t" \
  "buffer_load_ushort v167, v42, %[W], s61 offen offset:3456\n\t" \
  "s_waitcnt vmcnt(54)\n\t" \
  "v_perm_b32 v44, v116, v112, s62\n\t" \
  "s_waitcnt vmcnt(52)\n\t" \
  "v_perm_b32 v45, v124, v120, s62\n\t" \
  "s_waitcnt vmcnt(50)\n\t" \
  "v_perm_b32 v46, v132, v128, s62\n\t" \
  "s_waitcnt vmcnt(48)\n\t" \
  "v_perm_b32 v47, v140, v136, s62\n\t" \
  "v_accvgpr_write_b32 a224, v44\n\t" \
  "v_accvgpr_write_b32 a225, v45\n\t" \
  "v_accvgpr_write_b32 a226, v46\n\t" \
  "v_accvgpr_write_b32 a227, v47\n\t" \
  "v_perm_b32 v44, v116, v112, s63\n\t" \
  "v_perm_b32 v45, v124, v120, s63\n\t" \
  "v_perm_b32 v46, v132, v128, s63\n\t" \
  "v_perm_b32 v47, v140, v136, s63\n\t" \
  "v_accvgpr_write_b32 a228, v44\n\t" \
  "v_accvgpr_write_b32 a229, v45\n\t" \
  "v_accvgpr_write_b32 a230, v46\n\t" \
  "v_accvgpr_write_b32 a231, v47\n\t" \
  "v_perm_b32 v44, v117, v113, s62\n\t" \
  "v_perm_b32 v45, v125, v121, s62\n\t" \
  "v_perm_b32 v46, v133, v129, s62\n\t" \
  "v_perm_b32 v47, v141, v137, s62\n\t" \
  "v_accvgpr_write_b32 a232, v44\n\t" \
  "v_accvgpr_write_b32 a233, v45\n\t" \
  "v_accvgpr_write_b32 a234, v46\n\t" \
  "v_accvgpr_write_b32 a235, v47\n\t" \
  "v_perm_b32 v44, v117, v113, s63\n\t" \
  "v_perm_b32 v45, v125, v121, s63\n\t" \
  "v_perm_b32 v46, v133, v129, s63\n\t" \
  "v_perm_b32 v47, v141, v137, s63\n\t" \
  "v_accvgpr_write_b32 a236, v44\n\t" \
  "v_accvgpr_write_b32 a237, v45\n\t" \
  "v_accvgpr_write_b32 a238, v46\n\t" \
  "v_accvgpr_write_b32 a239, v47\n\t" \
  "v_perm_b32 v44, v118, v114, s62\n\t" \
  "v_perm_b32 v45, v126, v122, s62\n\t" \
  "v_perm_b32 v46, v134, v130, s62\n\t" \
  "v_perm_b32 v47, v142, v138, s62\n\t" \
  "v_accvgpr_write_b32 a240, v44\n\t" \
  "v_accvgpr_write_b32 a241, v45\n\t" \
  "v_accvgpr_write_b32 a242, v46\n\t" \
  "v_accvgpr_write_b32 a243, v47\n\t" \
  "v_perm_b32 v44, v118, v114, s63\n\t" \
  "v_perm_b32 v45, v126, v122, s63\n\t" \
  "v_perm_b32 v46, v134, v130, s63\n\t" \
  "v_perm_b32 v47, v142, v138, s63\n\t" \
  "v_accvgpr_write_b32 a244, v44\n\t" \
  "v_accvgpr_write_b32 a245, v45\n\t" \
  "v_accvgpr_write_b32 a246, v46\n\t" \
  "v_accvgpr_write_b32 a247, v47\n\t" \
  "v_perm_b32 v44, v119, v115, s62\n\t" \
  "v_perm_b32 v45, v127, v123, s62\n\t" \
  "v_perm_b32 v46, v135, v131, s62\n\t" \
  "v_perm_b32 v47, v143, v139, s62\n\t" \
  "v_accvgpr_write_b32 a248, v44\n\t" \
  "v_accvgpr_write_b32 a249, v45\n\t" \
  "v_accvgpr_write_b32 a250, v46\n\t" \
  "v_accvgpr_write_b32 a251, v47\n\t" \
  "v_perm_b32 v44, v119, v115, s63\n\t" \
  "v_perm_b32 v45, v127, v123, s63\n\t" \
  "v_perm_b32 v46, v135, v131, s63\n\t" \
  "v_perm_b32 v47, v143, v139, s63\n\t" \
  "v_accvgpr_write_b32 a252, v44\n\t" \
  "v_accvgpr_write_b32 a253, v45\n\t" \
  "v_accvgpr_write_b32 a254, v46\n\t" \
  "v_accvgpr_write_b32 a255, v47\n\t" \
  "s_xor_b32 s60, s66, 147456\n\t" \
  "s_add_u32 s60, s60, 73728\n\t" \
  "s_add_u32 s61, s60, 18432\n\t" \
  "buffer_load_ushort v248, v42, %[W], s60 offen offset:0\n\t" \
  "buffer_load_ushort v168, v42, %[W], s60 offen offset:1152\n\t" \
  "buffer_load_ushort v249, v42, %[W], s60 offen offset:2304\n\t" \
  "buffer_load_ushort v169, v42, %[W], s60 offen offset:3456\n\t" \
  "buffer_load_ushort v250, v42, %[W], s61 offen offset:0\n\t" \
  "buffer_load_ushort v170, v42, %[W], s61 offen offset:1152\n\t" \
  "buffer_load_ushort v251, v42, %[W], s61 offen offset:2304\n\t" \
  "buffer_load_ushort v171, v42, %[W], s61 offen offset:3456\n\t" \
  "s_xor_b32 s60, s66, 147456\n\t" \
  "s_add_u32 s60, s60, 110592\n\t" \
  "s_add_u32 s61, s60, 18432\n\t" \
  "s_waitcnt vmcnt(40)\n\t" \
  "buffer_load_ushort v252, v42, %[W], s60 offen offset:0\n\t" \
  "buffer_load_ushort v172, v42, %[W], s60 offen offset:1152\n\t" \
  "buffer_load_ushort v253, v42, %[W], s60 offen offset:2304\n\t" \
  "buffer_load_ushort v173, v42, %[W], s60 offen offset:3456\n\t" \
  "buffer_load_ushort v254, v42, %[W], s61 offen offset:0\n\t" \
  "buffer_load_ushort v174, v42, %[W], s61 offen offset:1152\n\t" \
  "buffer_load_ushort v255, v42, %[W], s61 offen offset:2304\n\t" \
  "buffer_load_ushort v175, v42, %[W], s61 offen offset:3456\n\t" \
  "v_lshl_or_b32 v224, v144, 16, v224\n\t" \
  "v_lshl_or_b32 v225, v145, 16, v225\n\t" \
  "v_lshl_or_b32 v226, v146, 16, v226\n\t" \
  "v_lshl_or_b32 v227, v147, 16, v227\n\t" \
  "v_lshl_or_b32 v228, v148, 16, v228\n\t" \
  "v_lshl_or_b32 v229, v149, 16, v229\n\t" \
  "v_lshl_or_b32 v230, v150, 16, v230\n\t" \
  "v_lshl_or_b32 v231, v151, 16, v231\n\t" \
  "s_waitcnt vmcnt(46)\n\t" \
  "v_lshl_or_b32 v232, v152, 16, v232\n\t" \
  "s_waitcnt vmcnt(44)\n\t" \
  "v_lshl_or_b32 v233, v153, 16, v233\n\t" \
  "s_waitcnt vmcnt(42)\n\t" \
  "v_lshl_or_b32 v234, v154, 16, v234\n\t" \
  "s_waitcnt vmcnt(40)\n\t" \
  "v_lshl_or_b32 v235, v155, 16, v235\n\t" \
  "s_waitcnt vmcnt(38)\n\t" \
  "v_lshl_or_b32 v236, v156, 16, v236\n\t" \
  "s_waitcnt vmcnt(36)\n\t" \
  "v_lshl_or_b32 v237, v157, 16, v237\n\t" \
  "s_waitcnt vmcnt(34)\n\t" \
  "v_lshl_or_b32 v238, v158, 16, v238\n\t" \
  "s_waitcnt vmcnt(32)\n\t" \
  "v_lshl_or_b32 v239, v159, 16, v239\n\t" \
  "s_waitcnt vmcnt(30)\n\t" \
  "v_lshl_or_b32 v240, v160, 16, v240\n\t" \
  "s_waitcnt vmcnt(28)\n\t" \
  "v_lshl_or_b32 v241, v161, 16, v241\n\t" \
  "s_waitcnt vmcnt(26)\n\t" \
  "v_lshl_or_b32 v242, v162, 16, v242\n\t" \
  "s_waitcnt vmcnt(24)\n\t" \
  "v_lshl_or_b32 v243, v163, 16, v243\n\t" \
  "s_waitcnt vmcnt(22)\n\t" \
  "v_lshl_or_b32 v244, v164, 16, v244\n\t" \
  "s_waitcnt vmcnt(20)\n\t" \
  "v_lshl_or_b32 v245, v165, 16, v245\n\t" \
  "s_waitcnt vmcnt(18)\n\t" \
  "v_lshl_or_b32 v246, v166, 16, v246\n\t" \
  "s_waitcnt vmcnt(16)\n\t" \
  "v_lshl_or_b32 v247, v167, 16, v247\n\t" \
  "s_waitcnt vmcnt(14)\n\t" \
  "v_lshl_or_b32 v248, v168, 16, v248\n\t" \
  "s_waitcnt vmcnt(12)\n\t" \
  "v_lshl_or_b32 v249, v169, 16, v249\n\t" \
  "s_waitcnt vmcnt(10)\n\t" \
  "v_lshl_or_b32 v250, v170, 16, v250\n\t" \
  "s_waitcnt vmcnt(8)\n\t" \
  "v_lshl_or_b32 v251, v171, 16, v251\n\t" \
  "s_waitcnt vmcnt(6)\n\t" \
  "v_lshl_or_b32 v252, v172, 16, v252\n\t" \
  "s_waitcnt vmcnt(4)\n\t" \
  "v_lshl_or_b32 v253, v173, 16, v253\n\t" \
  "s_waitcnt vmcnt(2)\n\t" \
  "v_lshl_or_b32 v254, v174, 16, v254\n\t" \
  "s_waitcnt vmcnt(0)\n\t" \
  "v_lshl_or_b32 v255, v175, 16, v255\n\t" \
  "s_mov_b32 s84, " TK_CAP "\n\t" \
  "s_mov_b32 s48, 0\n\t" \
  "s_mov_b32 s49, 0xf0000000\n\t" \
  "s_mov_b32 s46, s45\n\t" \
  "v_mov_b32 v5, -1\n\t" \
  "v_lshrrev_b32 v13, 2, v0\n\t" \
  "s_mov_b32 s60, " TK_CTAB "\n\t" \
  "buffer_load_dword v10, v13, %[X], s60 offen offset:88 sc1\n\t" \
  "buffer_load_dword v13, v13, %[X], s60 offen offset:0 sc1\n\t" \
  "s_waitcnt vmcnt(0)\n\t" \
  "v_and_b32 v10, 1, v10\n\t" \
  "v_and_b32 v13, 1, v13\n\t" \
  "v_cmp_ne_u32 s[90:91], s67, v10\n\t" \
  "v_cmp_ne_u32 s[88:89], s67, v13\n\t" \
  "s_nop 1\n\t" \
  "s_mov_b32 s86, s88\n\t" \
  "v_lshrrev_b32 v13, 2, v0\n\t" \
  "s_mov_b32 s60, " TK_CTAB "\n\t" \
  "buffer_load_dword v10, v13, %[X], s60 offen offset:88 sc1\n\t" \
  "buffer_load_dword v13, v13, %[X], s60 offen offset:344 sc1\n\t" \
  "s_waitcnt vmcnt(0)\n\t" \
  "v_and_b32 v10, 1, v10\n\t" \
  "v_and_b32 v13, 1, v13\n\t" \
  "v_cmp_ne_u32 s[90:91], s67, v10\n\t" \
  "v_cmp_ne_u32 s[88:89], s67, v13\n\t" \
  "s_nop 1\n\t" \
  "s_and_b32 s60, s88, 256\n\t" \
  "s_lshl_b32 s60, s60, 5\n\t" \
  "s_add_u32 s44, s44, s60\n\t" \
  "s_add_u32 s83, s48, 0\n\t" \
  "s_lshr_b64 s[60:61], s[90:91], s83\n\t" \
  "s_and_b32 s60, s60, 1\n\t" \
  "s_lshl_b32 s60, s60, 13\n\t" \
  "s_add_u32 s83, s83, " TK_SG0 "\n\t" \
  "s_lshl_b32 s83, s83, 21\n\t" \
  "s_or_b32 s83, s83, s60\n\t" \
  "s_mov_b32 s81, s83\n\t" \
  "LCOLD_%=:\n\t" \
  "s_add_u32 s62, s83, s41\n\t" \
  "s_mov_b32 s61, 0\n\t" \
  "LPQ_%=:\n\t" \
  "buffer_load_dword v10, v9, %[X], s62 offen sc1\n\t" \
  "s_waitcnt vmcnt(0)\n\t" \
  "v_cmp_eq_u32 vcc, 0, v10\n\t" \
  "s_cbranch_vccz LPQ_%=x\n\t" \
  "s_add_u32 s61, s61, 1\n\t" \
  "s_sleep 4\n\t" \
  "s_cmp_gt_u32 s61, s84\n\t" \
  "s_cbranch_scc1 LPQ_%=g\n\t" \
  "s_and_b32 s60, s61, 1023\n\t" \
  "s_cmp_lg_u32 s60, 0\n\t" \
  "s_cbranch_scc1 LPQ_%=\n\t" \
  "buffer_load_dword v10, off, %[X], s92 sc1\n\t" \
  "s_waitcnt vmcnt(0)\n\t" \
  "v_readfirstlane_b32 s60, v10\n\t" \
  "s_nop 0\n\t" \
  "s_cmp_eq_u32 s60, 0\n\t" \
  "s_cbranch_scc1 LPQ_%=\n\t" \
  "LPQ_%=g:\n\t" \
  "s_mov_b32 s84, 0\n\t" \
  "s_or_b32 s60, s48, 1073741824\n\t" \
  "v_mov_b32 v10, s60\n\t" \
  "s_mov_b64 exec, 1\n\t" \
  "buffer_store_dword v10, off, %[X], s92 sc1\n\t" \
  "s_mov_b64 exec, -1\n\t" \
  "LPQ_%=x:\n\t" \
  "s_add_u32 m0, s46, 0\n\t" \
  "s_add_u32 s47, s81, s36\n\t" \
  "buffer_load_dwordx4 v0, %[X], s47 offen offset:0 sc1 lds\n\t" \
  "buffer_load_dwordx4 v0, %[X], s47 offen offset:1024 sc1 lds\n\t" \
  "buffer_load_dwordx4 v0, %[X], s47 offen offset:2048 sc1 lds\n\t" \
  "buffer_load_dwordx4 v0, %[X], s47 offen offset:3072 sc1 lds\n\t" \
  "s_add_u32 m0, s46, 4096\n\t" \
  "s_add_u32 s47, s81, s37\n\t" \
  "buffer_load_dwordx4 v0, %[X], s47 offen offset:0 sc1 lds\n\t" \
  "buffer_load_dwordx4 v0, %[X], s47 offen offset:1024 sc1 lds\n\t" \
  "buffer_load_dwordx4 v0, %[X], s47 offen offset:2048 sc1 lds\n\t" \
  "buffer_load_dwordx4 v0, %[X], s47 offen offset:3072 sc1 lds\n\t" \
  "s_add_u32 m0, s46, 8192\n\t" \
  "s_add_u32 s47, s81, s38\n\t" \
  "buffer_load_dwordx4 v0, %[X], s47 offen offset:0 sc1 lds\n\t" \
  "buffer_load_dwordx4 v0, %[X], s47 offen offset:1024 sc1 lds\n\t" \
  "buffer_load_dwordx4 v0, %[X], s47 offen offset:2048 sc1 lds\n\t" \
  "buffer_load_dwordx4 v0, %[X], s47 offen offset:3072 sc1 lds\n\t" \
  "s_waitcnt vmcnt(0)\n\t" \
  "LTOP_%=:\n\t" \
  "s_memrealtime s[78:79]\n\t" \
  "v_readfirstlane_b32 s76, v5\n\t" \
  "s_waitcnt lgkmcnt(0)\n\t" \
  "s_add_u32 s60, s76, 1\n\t" \
  "s_not_b32 s76, s76\n\t" \
  "s_cmp_gt_u32 s60, 1\n\t" \
  "s_cselect_b32 s61, -1, 0\n\t" \
  "s_lshl_b32 s60, s78, 8\n\t" \
  "s_sub_u32 s76, s76, s60\n\t" \
  "s_ashr_i32 s76, s76, 8\n\t" \
  "s_add_i32 s76, s76, s77\n\t" \
  "s_min_i32 s76, s76, 400\n\t" \
  "s_and_b32 s76, s76, s61\n\t" \
  "s_add_u32 s57, s78, s76\n\t" \
  "s_barrier\n\t" \
  "ds_read_b128 v[48:51], v2 offset:0\n\t" \
  "ds_read_b128 v[64:67], v1 offset:8192\n\t" \
  "ds_read_b128 v[80:83], v1 offset:12288\n\t" \
  "ds_read_b128 v[52:55], v2 offset:1024\n\t" \
  "ds_read_b128 v[68:71], v1 offset:9216\n\t" \
  "ds_read_b128 v[84:87], v1 offset:13312\n\t" \
  "ds_read_b128 v[56:59], v2 offset:2048\n\t" \
  "ds_read_b128 v[72:75], v1 offset:10240\n\t" \
  "ds_read_b128 v[88:91], v1 offset:14336\n\t" \
  "ds_read_b128 v[60:63], v2 offset:3072\n\t" \
  "ds_read_b128 v[76:79], v1 offset:11264\n\t" \
  "ds_read_b128 v[92:95], v1 offset:15360\n\t" \
  "s_mov_b32 s82, s83\n\t" \
  "s_xor_b32 s46, s46, 0x10000\n\t" \
  "s_add_u32 s58, s82, s41\n\t" \
  "s_add_u32 s58, s58, 4096\n\t" \
  "s_add_u32 s59, s81, s39\n\t" \
  "s_cmp_gt_i32 s76, 0\n\t" \
  "s_cselect_b32 s62, 0xf0000000, s58\n\t" \
  "buffer_load_dword v5, off, %[X], s62 sc1\n\t" \
  "s_cmp_gt_i32 s76, " TK_AM "\n\t" \
  "s_cselect_b32 s79, 0xf0000000, s58\n\t" \
  "s_waitcnt lgkmcnt(10)\n\t" \
  "v_mfma_f32_16x16x32_bf16 v[36:39], v[64:67], v[48:51], v[16:19]\n\t" \
  "ds_read_b128 v[96:99], v1 offset:16384\n\t" \
  "s_waitcnt lgkmcnt(10)\n\t" \
  "v_mfma_f32_16x16x32_bf16 v[40:43], v[80:83], v[48:51], v[20:23]\n\t" \
  "ds_read_b128 v[100:103], v1 offset:17408\n\t" \
  "s_waitcnt lgkmcnt(9)\n\t" \
  "v_mfma_f32_16x16x32_bf16 v[36:39], v[68:71], v[52:55], v[36:39]\n\t" \
  "ds_read_b128 v[104:107], v1 offset:18432\n\t" \
  "s_waitcnt lgkmcnt(9)\n\t" \
  "v_mfma_f32_16x16x32_bf16 v[40:43], v[84:87], v[52:55], v[40:43]\n\t" \
  "ds_read_b128 v[108:111], v1 offset:19456\n\t" \
  "s_waitcnt lgkmcnt(8)\n\t" \
  "v_mfma_f32_16x16x32_bf16 v[36:39], v[72:75], v[56:59], v[36:39]\n\t" \
  "ds_read_b128 v[112:115], v1 offset:20480\n\t" \
  "s_waitcnt lgkmcnt(8)\n\t" \
  "v_mfma_f32_16x16x32_bf16 v[40:43], v[88:91], v[56:59], v[40:43]\n\t" \
  "ds_read_b128 v[116:119], v1 offset:21504\n\t" \
  "s_waitcnt lgkmcnt(7)\n\t" \
  "v_mfma_f32_16x16x32_bf16 v[36:39], v[76:79], v[60:63], v[36:39]\n\t" \
  "ds_read_b128 v[120:123], v1 offset:22528\n\t" \
  "s_waitcnt lgkmcnt(7)\n\t" \
  "v_mfma_f32_16x16x32_bf16 v[40:43], v[92:95], v[60:63], v[40:43]\n\t" \
  "ds_read_b128 v[124:127], v1 offset:23552\n\t" \
  "s_add_u32 s83, s48, 1\n\t" \
  "s_lshr_b64 s[60:61], s[90:91], s83\n\t" \
  "s_and_b32 s60, s60, 1\n\t" \
  "s_lshl_b32 s60, s60, 13\n\t" \
  "s_add_u32 s83, s83, " TK_SG0 "\n\t" \
  "s_lshl_b32 s83, s83, 21\n\t" \
  "s_or_b32 s83, s83, s60\n\t" \
  "s_add_u32 s56, s48, 1\n\t" \
  "s_and_b32 s56, s56, " TK_TRM "\n\t" \
  "s_lshr_b64 s[60:61], s[90:91], s56\n\t" \
  "s_and_b32 s60, s60, 1\n\t" \
  "s_lshl_b32 s60, s60, 13\n\t" \
  "s_add_u32 s56, s56, " TK_SG0 "\n\t" \
  "s_lshl_b32 s56, s56, 21\n\t" \
  "s_or_b32 s56, s56, s60\n\t" \
  "v_xor_b32 v1, 0x10000, v1\n\t" \
  "v_xor_b32 v2, 0x10000, v2\n\t" \
  "v_max3_f32 v11, v36, v37, v38\n\t" \
  "v_max3_f32 v11, v11, v39, v40\n\t" \
  "v_max3_f32 v13, v41, v42, v43\n\t" \
  "v_max_f32 v11, v11, v13\n\t" \
  "v_mov_b32 v13, v11\n\t" \
  "s_nop 1\n\t" \
  "v_permlane16_swap_b32 v11, v13\n\t" \
  "v_max_f32 v11, v11, v13\n\t" \
  "v_mov_b32 v13, v11\n\t" \
  "s_nop 1\n\t" \
  "v_permlane32_swap_b32 v11, v13\n\t" \
  "v_max_f32 v11, v11, v13\n\t" \
  "v_mul_f32 v13, 0xbe0293ee, v11\n\t" \
  "v_fmamk_f32 v36, v36, 0x3e0293ee, v13\n\t" \
  "v_fmamk_f32 v37, v37, 0x3e0293ee, v13\n\t" \
  "v_fmamk_f32 v38, v38, 0x3e0293ee, v13\n\t" \
  "v_fmamk_f32 v39, v39, 0x3e0293ee, v13\n\t" \
  "v_fmamk_f32 v40, v40, 0x3e0293ee, v13\n\t" \
  "v_fmamk_f32 v41, v41, 0x3e0293ee, v13\n\t" \
  "v_fmamk_f32 v42, v42, 0x3e0293ee, v13\n\t" \
  "v_fmamk_f32 v43, v43, 0x3e0293ee, v13\n\t" \
  "s_waitcnt vmcnt(0)\n\t" \
  "v_readfirstlane_b32 s76, v5\n\t" \
  "s_barrier\n\t" \
  "s_cmp_eq_u32 s76, 0\n\t" \
  "s_cbranch_scc1 LNG_%=\n\t" \
  "buffer_load_dwordx4 v[196:199], v0, %[X], s59 offen offset:0 sc1\n\t" \
  "buffer_load_dwordx4 v[200:203], v0, %[X], s59 offen offset:1024 sc1\n\t" \
  "buffer_load_dwordx2 v[204:205], v6, %[X], s59 offen offset:2048 sc1\n\t" \
  "buffer_load_dwordx2 v[206:207], v6, %[X], s59 offen offset:2560 sc1\n\t" \
  "buffer_load_dwordx2 v[208:209], v6, %[X], s59 offen offset:3072 sc1\n\t" \
  "buffer_load_dwordx2 v[210:211], v6, %[X], s59 offen offset:3584 sc1\n\t" \
  "LNG_%=:\n\t" \
  "buffer_load_dword v5, off, %[X], s79 sc1\n\t" \
  "s_mov_b64 exec, 1\n\t" \
  "buffer_store_dword v3, off, %[X], s49 sc1\n\t" \
  "s_mov_b64 exec, -1\n\t" \
  "s_mov_b32 s49, 0xf0000000\n\t" \
  "v_exp_f32 v36, v36\n\t" \
  "v_exp_f32 v37, v37\n\t" \
  "v_exp_f32 v38, v38\n\t" \
  "v_exp_f32 v39, v39\n\t" \
  "v_exp_f32 v40, v40\n\t" \
  "v_exp_f32 v41, v41\n\t" \
  "v_exp_f32 v42, v42\n\t" \
  "v_exp_f32 v43, v43\n\t" \
  "v_cvt_pk_bf16_f32 v44, v36, v37\n\t" \
  "v_cvt_pk_bf16_f32 v45, v38, v39\n\t" \
  "v_cvt_pk_bf16_f32 v46, v40, v41\n\t" \
  "v_cvt_pk_bf16_f32 v47, v42, v43\n\t" \
  "v_add_f32 v36, v36, v37\n\t" \
  "v_add_f32 v38, v38, v39\n\t" \
  "s_waitcnt lgkmcnt(7)\n\t" \
  "v_mfma_f32_16x16x32_bf16 v[128:131], v[96:99], v[44:47], 0\n\t" \
  "v_add_f32 v40, v40, v41\n\t" \
  "s_and_b32 s62, s48, 15\n\t" \
  "s_lshr_b32 s63, s62, 1\n\t" \
  "s_waitcnt lgkmcnt(6)\n\t" \
  "v_mfma_f32_16x16x32_bf16 v[132:135], v[100:103], v[44:47], 0\n\t" \
  "v_add_f32 v42, v42, v43\n\t" \
  "s_lshr_b32 s60, s88, s63\n\t" \
  "s_and_b32 s60, s60, 1\n\t" \
  "s_waitcnt lgkmcnt(5)\n\t" \
  "v_mfma_f32_16x16x32_bf16 v[136:139], v[104:107], v[44:47], 0\n\t" \
  "v_add_f32 v36, v36, v38\n\t" \
  "s_lshl_b32 s60, s60, 13\n\t" \
  "s_lshl_b32 s61, s62, 20\n\t" \
  "s_waitcnt lgkmcnt(4)\n\t" \
  "v_mfma_f32_16x16x32_bf16 v[140:143], v[108:111], v[44:47], 0\n\t" \
  "v_add_f32 v40, v40, v42\n\t" \
  "s_add_u32 s50, s61, s60\n\t" \
  "s_add_u32 s50, s50, s43\n\t" \
  "s_waitcnt lgkmcnt(3)\n\t" \
  "v_mfma_f32_16x16x32_bf16 v[144:147], v[112:115], v[44:47], 0\n\t" \
  "v_add_f32 v12, v36, v40\n\t" \
  "s_add_u32 s51, s50, 32768\n\t" \
  "s_add_u32 s52, s51, 32768\n\t" \
  "s_waitcnt lgkmcnt(2)\n\t" \
  "v_mfma_f32_16x16x32_bf16 v[148:151], v[116:119], v[44:47], 0\n\t" \
  "s_add_u32 s53, s52, 32768\n\t" \
  "s_lshl_b32 s61, s62, 17\n\t" \
  "s_waitcnt lgkmcnt(1)\n\t" \
  "v_mfma_f32_16x16x32_bf16 v[152:155], v[120:123], v[44:47], 0\n\t" \
  "s_add_u32 s54, s61, s44\n\t" \
  "s_add_u32 s63, s83, s41\n\t" \
  "s_waitcnt lgkmcnt(0)\n\t" \
  "v_mfma_f32_16x16x32_bf16 v[156:159], v[124:127], v[44:47], 0\n\t" \
  "s_cmp_lg_u32 s76, 0\n\t" \
  "s_cbranch_scc1 LGOT_%=\n\t" \
  "s_waitcnt vmcnt(1)\n\t" \
  "v_readfirstlane_b32 s60, v5\n\t" \
  "s_nop 0\n\t" \
  "s_cmp_lg_u32 s60, 0\n\t" \
  "s_cbranch_scc1 LPK_%=x\n\t" \
  "LKW_%=:\n\t" \
  "s_memrealtime s[78:79]\n\t" \
  "s_waitcnt lgkmcnt(0)\n\t" \
  "s_sub_i32 s79, s78, s57\n\t" \
  "s_cmp_lt_i32 s79, 0\n\t" \
  "s_cbranch_scc1 LKW_%=\n\t" \
  "s_mov_b32 s61, 0\n\t" \
  "LPK_%=:\n\t" \
  "buffer_load_dword v5, off, %[X], s58 sc1\n\t" \
  "s_waitcnt vmcnt(0)\n\t" \
  "v_cmp_eq_u32 vcc, 0, v5\n\t" \
  "s_cbranch_vccz LPK_%=x\n\t" \
  "s_add_u32 s61, s61, 1\n\t" \
  "s_sleep " TK_PSK "\n\t" \
  "s_cmp_gt_u32 s61, s84\n\t" \
  "s_cbranch_scc1 LPK_%=g\n\t" \
  "s_and_b32 s60, s61, 1023\n\t" \
  "s_cmp_lg_u32 s60, 0\n\t" \
  "s_cbranch_scc1 LPK_%=\n\t" \
  "buffer_load_dword v5, off, %[X], s92 sc1\n\t" \
  "s_waitcnt vmcnt(0)\n\t" \
  "v_readfirstlane_b32 s60, v5\n\t" \
  "s_nop 0\n\t" \
  "s_cmp_eq_u32 s60, 0\n\t" \
  "s_cbranch_scc1 LPK_%=\n\t" \
  "LPK_%=g:\n\t" \
  "s_mov_b32 s84, 0\n\t" \
  "s_or_b32 s60, s48, 1342177280\n\t" \
  "v_mov_b32 v5, s60\n\t" \
  "s_mov_b64 exec, 1\n\t" \
  "buffer_store_dword v5, off, %[X], s92 sc1\n\t" \
  "s_mov_b64 exec, -1\n\t" \
  "LPK_%=x:\n\t" \
  "buffer_load_dwordx4 v[196:199], v0, %[X], s59 offen offset:0 sc1\n\t" \
  "buffer_load_dwordx4 v[200:203], v0, %[X], s59 offen offset:1024 sc1\n\t" \
  "buffer_load_dwordx2 v[204:205], v6, %[X], s59 offen offset:2048 sc1\n\t" \
  "buffer_load_dwordx2 v[206:207], v6, %[X], s59 offen offset:2560 sc1\n\t" \
  "buffer_load_dwordx2 v[208:209], v6, %[X], s59 offen offset:3072 sc1\n\t" \
  "buffer_load_dwordx2 v[210:211], v6, %[X], s59 offen offset:3584 sc1\n\t" \
  "LGOT_%=:\n\t" \
  "buffer_load_dword v10, v9, %[X], s63 offen sc1\n\t" \
  "s_add_u32 s49, s82, s42\n\t" \
  "s_waitcnt vmcnt(6)\n\t" \
  "v_mfma_f32_16x16x32_bf16 v[36:39], v[196:199], v[48:51], 0\n\t" \
  "v_mfma_f32_16x16x32_bf16 v[40:43], v[196:199], v[52:55], 0\n\t" \
  "s_waitcnt vmcnt(5)\n\t" \
  "v_mfma_f32_16x16x32_bf16 v[36:39], v[200:203], v[56:59], v[36:39]\n\t" \
  "v_mfma_f32_16x16x32_bf16 v[40:43], v[200:203], v[60:63], v[40:43]\n\t" \
  "s_nop 8\n\t" \
  "v_add_f32 v36, v36, v42\n\t" \
  "v_add_f32 v37, v37, v43\n\t" \
  "v_max3_f32 v14, v36, v37, v11\n\t" \
  "v_mov_b32 v13, v14\n\t" \
  "s_nop 1\n\t" \
  "v_permlane16_swap_b32 v14, v13\n\t" \
  "v_max_f32 v14, v14, v13\n\t" \
  "v_mov_b32 v13, v14\n\t" \
  "s_nop 1\n\t" \
  "v_permlane32_swap_b32 v14, v13\n\t" \
  "v_max_f32 v14, v14, v13\n\t" \
  "v_mul_f32 v14, 0xbe0293ee, v14\n\t" \
  "v_fmamk_f32 v11, v11, 0x3e0293ee, v14\n\t" \
  "v_fmamk_f32 v36, v36, 0x3e0293ee, v14\n\t" \
  "v_fmamk_f32 v37, v37, 0x3e0293ee, v14\n\t" \
  "v_exp_f32 v11, v11\n\t" \
  "v_exp_f32 v36, v36\n\t" \
  "v_exp_f32 v37, v37\n\t" \
  "v_fma_f32 v12, v12, v11, v36\n\t" \
  "v_add_f32 v12, v12, v37\n\t" \
  "v_mov_b32 v13, v12\n\t" \
  "s_nop 1\n\t" \
  "v_permlane16_swap_b32 v12, v13\n\t" \
  "v_add_f32 v12, v12, v13\n\t" \
  "v_mov_b32 v13, v12\n\t" \
  "s_nop 1\n\t" \
  "v_permlane32_swap_b32 v12, v13\n\t" \
  "v_add_f32 v12, v12, v13\n\t" \
  "v_rcp_f32 v12, v12\n\t" \
  "s_nop 0\n\t" \
  "v_mul_f32 v14, v11, v12\n\t" \
  "v_mul_f32 v36, v36, v12\n\t" \
  "v_mul_f32 v37, v37, v12\n\t" \
  "v_cvt_pk_bf16_f32 v26, v36, v37\n\t" \
  "v_cvt_pk_bf16_f32 v33, v36, v37\n\t" \
  "v_pk_mul_f32 v[128:129], v[128:129], v[14:15] op_sel_hi:[1,0]\n\t" \
  "v_pk_mul_f32 v[130:131], v[130:131], v[14:15] op_sel_hi:[1,0]\n\t" \
  "v_pk_mul_f32 v[132:133], v[132:133], v[14:15] op_sel_hi:[1,0]\n\t" \
  "v_pk_mul_f32 v[134:135], v[134:135], v[14:15] op_sel_hi:[1,0]\n\t" \
  "v_pk_mul_f32 v[136:137], v[136:137], v[14:15] op_sel_hi:[1,0]\n\t" \
  "v_pk_mul_f32 v[138:139], v[138:139], v[14:15] op_sel_hi:[1,0]\n\t" \
  "v_pk_mul_f32 v[140:141], v[140:141], v[14:15] op_sel_hi:[1,0]\n\t" \
  "v_pk_mul_f32 v[142:143], v[142:143], v[14:15] op_sel_hi:[1,0]\n\t" \
  "s_waitcnt vmcnt(3)\n\t" \
  "v_mfma_f32_16x16x32_bf16 v[128:131], v[204:207], v[26:29], v[128:131]\n\t" \
  "v_mul_f32 v144, v144, v14\n\t" \
  "v_mul_f32 v145, v145, v14\n\t" \
  "v_mfma_f32_16x16x32_bf16 v[132:135], v[204:207], v[32:35], v[132:135]\n\t" \
  "v_mul_f32 v146, v146, v14\n\t" \
  "v_mul_f32 v147, v147, v14\n\t" \
  "v_mfma_f32_16x16x32_bf16 v[136:139], v[204:207], v[24:27], v[136:139]\n\t" \
  "v_mul_f32 v148, v148, v14\n\t" \
  "v_mul_f32 v149, v149, v14\n\t" \
  "v_mfma_f32_16x16x32_bf16 v[140:143], v[204:207], v[30:33], v[140:143]\n\t" \
  "v_cvt_pk_bf16_f32 v64, v128, v129\n\t" \
  "v_cvt_pk_bf16_f32 v65, v130, v131\n\t" \
  "v_cvt_pk_bf16_f32 v66, v132, v133\n\t" \
  "v_cvt_pk_bf16_f32 v67, v134, v135\n\t" \
  "v_mul_f32 v150, v150, v14\n\t" \
  "v_mul_f32 v151, v151, v14\n\t" \
  "v_mfma_f32_16x16x32_bf16 v[192:195], v[224:227], v[64:67], 0\n\t" \
  "ds_write_b128 v3, v[64:67] offset:0\n\t" \
  "v_cvt_pk_bf16_f32 v68, v136, v137\n\t" \
  "v_cvt_pk_bf16_f32 v69, v138, v139\n\t" \
  "v_mfma_f32_16x16x32_bf16 v[160:163], v[64:67], a[0:3], 0\n\t" \
  "v_cvt_pk_bf16_f32 v70, v140, v141\n\t" \
  "v_cvt_pk_bf16_f32 v71, v142, v143\n\t" \
  "s_waitcnt vmcnt(1)\n\t" \
  "v_mfma_f32_16x16x32_bf16 v[144:147], v[208:211], v[26:29], v[144:147]\n\t" \
  "v_mul_f32 v152, v152, v14\n\t" \
  "v_mul_f32 v153, v153, v14\n\t" \
  "v_mfma_f32_16x16x32_bf16 v[148:151], v[208:211], v[32:35], v[148:151]\n\t" \
  "v_mul_f32 v154, v154, v14\n\t" \
  "v_mul_f32 v155, v155, v14\n\t" \
  "v_mfma_f32_16x16x32_bf16 v[164:167], v[64:67], a[4:7], 0\n\t" \
  "ds_write_b128 v3, v[68:71] offset:1024\n\t" \
  "v_mul_f32 v156, v156, v14\n\t" \
  "v_mul_f32 v157, v157, v14\n\t" \
  "v_mfma_f32_16x16x32_bf16 v[168:171], v[64:67], a[8:11], 0\n\t" \
  "v_mul_f32 v158, v158, v14\n\t" \
  "v_mul_f32 v159, v159, v14\n\t" \
  "v_mfma_f32_16x16x32_bf16 v[172:175], v[64:67], a[12:15], 0\n\t" \
  "v_cvt_pk_bf16_f32 v72, v144, v145\n\t" \
  "v_cvt_pk_bf16_f32 v73, v146, v147\n\t" \
  "v_mfma_f32_16x16x32_bf16 v[152:155], v[208:211], v[24:27], v[152:155]\n\t" \
  "v_cvt_pk_bf16_f32 v74, v148, v149\n\t" \
  "v_cvt_pk_bf16_f32 v75, v150, v151\n\t" \
  "v_mfma_f32_16x16x32_bf16 v[156:159], v[208:211], v[30:33], v[156:159]\n\t" \
  "v_mfma_f32_16x16x32_bf16 v[176:179], v[64:67], a[16:19], 0\n\t" \
  "ds_write_b128 v3, v[72:75] offset:2048\n\t" \
  "v_mfma_f32_16x16x32_bf16 v[180:183], v[64:67], a[20:23], 0\n\t" \
  "v_mfma_f32_16x16x32_bf16 v[184:187], v[64:67], a[24:27], 0\n\t" \
  "v_cvt_pk_bf16_f32 v76, v152, v153\n\t" \
  "v_cvt_pk_bf16_f32 v77, v154, v155\n\t" \
  "v_mfma_f32_16x16x32_bf16 v[188:191], v[64:67], a[28:31], 0\n\t" \
  "v_cvt_pk_bf16_f32 v78, v156, v157\n\t" \
  "v_cvt_pk_bf16_f32 v79, v158, v159\n\t" \
  "v_mfma_f32_16x16x32_bf16 v[192:195], v[228:231], v[68:71], v[192:195]\n\t" \
  "ds_write_b128 v3, v[76:79] offset:3072\n\t" \
  "v_mfma_f32_16x16x32_bf16 v[160:163], v[68:71], a[32:35], v[160:163]\n\t" \
  "s_waitcnt vmcnt(0)\n\t" \
  "v_cmp_eq_u32 vcc, 0, v10\n\t" \
  "v_mfma_f32_16x16x32_bf16 v[164:167], v[68:71], a[36:39], v[164:167]\n\t" \
  "s_cmp_eq_u64 vcc, 0\n\t" \
  "s_cselect_b32 s81, s56, 0xf0000000\n\t" \
  "s_cmp_eq_u32 s48, 63\n\t" \
  "v_mfma_f32_16x16x32_bf16 v[168:171], v[68:71], a[40:43], v[168:171]\n\t" \
  "s_cselect_b32 s81, 0xf0000000, s81\n\t" \
  "v_mfma_f32_16x16x32_bf16 v[172:175], v[68:71], a[44:47], v[172:175]\n\t" \
  "v_mfma_f32_16x16x32_bf16 v[176:179], v[68:71], a[48:51], v[176:179]\n\t" \
  "s_add_u32 m0, s46, 0\n\t" \
  "s_add_u32 s47, s81, s36\n\t" \
  "buffer_load_dwordx4 v0, %[X], s47 offen offset:0 sc1 lds\n\t" \
  "v_mfma_f32_16x16x32_bf16 v[180:183], v[68:71], a[52:55], v[180:183]\n\t" \
  "v_mfma_f32_16x16x32_bf16 v[184:187], v[68:71], a[56:59], v[184:187]\n\t" \
  "s_waitcnt lgkmcnt(0)\n\t" \
  "s_barrier\n\t" \
  "v_mfma_f32_16x16x32_bf16 v[188:191], v[68:71], a[60:63], v[188:191]\n\t" \
  "ds_read_b128 v[80:83], v4 offset:0\n\t" \
  "v_mfma_f32_16x16x32_bf16 v[192:195], v[232:235], v[72:75], v[192:195]\n\t" \
  "buffer_load_dwordx4 v0, %[X], s47 offen offset:1024 sc1 lds\n\t" \
  "v_mfma_f32_16x16x32_bf16 v[160:163], v[72:75], a[64:67], v[160:163]\n\t" \
  "ds_read_b128 v[84:87], v4 offset:1024\n\t" \
  "v_mfma_f32_16x16x32_bf16 v[164:167], v[72:75], a[68:71], v[164:167]\n\t" \
  "v_mfma_f32_16x16x32_bf16 v[168:171], v[72:75], a[72:75], v[168:171]\n\t" \
  "ds_read_b128 v[88:91], v4 offset:2048\n\t" \
  "v_mfma_f32_16x16x32_bf16 v[172:175], v[72:75], a[76:79], v[172:175]\n\t" \
  "buffer_load_dwordx4 v0, %[X], s47 offen offset:2048 sc1 lds\n\t" \
  "v_mfma_f32_16x16x32_bf16 v[176:179], v[72:75], a[80:83], v[176:179]\n\t" \
  "ds_read_b128 v[92:95], v4 offset:3072\n\t" \
  "v_mfma_f32_16x16x32_bf16 v[180:183], v[72:75], a[84:87], v[180:183]\n\t" \
  "v_mfma_f32_16x16x32_bf16 v[184:187], v[72:75], a[88:91], v[184:187]\n\t" \
  "v_mfma_f32_16x16x32_bf16 v[188:191], v[72:75], a[92:95], v[188:191]\n\t" \
  "buffer_load_dwordx4 v0, %[X], s47 offen offset:3072 sc1 lds\n\t" \
  "v_mfma_f32_16x16x32_bf16 v[192:195], v[236:239], v[76:79], v[192:195]\n\t" \
  "s_add_u32 s62, s48, 2\n\t" \
  "v_mfma_f32_16x16x32_bf16 v[160:163], v[76:79], a[96:99], v[160:163]\n\t" \
  "v_mfma_f32_16x16x32_bf16 v[164:167], v[76:79], a[100:103], v[164:167]\n\t" \
  "s_mul_i32 s63, s62, 43\n\t" \
  "s_lshr_b32 s63, s63, 9\n\t" \
  "v_mfma_f32_16x16x32_bf16 v[168:171], v[76:79], a[104:107], v[168:171]\n\t" \
  "s_add_u32 m0, s46, 4096\n\t" \
  "s_add_u32 s47, s81, s37\n\t" \
  "buffer_load_dwordx4 v0, %[X], s47 offen offset:0 sc1 lds\n\t" \
  "v_mfma_f32_16x16x32_bf16 v[172:175], v[76:79], a[108:111], v[172:175]\n\t" \
  "s_mul_i32 s63, s63, " TK_RM "\n\t" \
  "s_sub_u32 s62, s62, s63\n\t" \
  "v_mfma_f32_16x16x32_bf16 v[176:179], v[76:79], a[112:115], v[176:179]\n\t" \
  "v_mfma_f32_16x16x32_bf16 v[180:183], v[76:79], a[116:119], v[180:183]\n\t" \
  "s_mul_i32 s63, s62, 43\n\t" \
  "s_lshr_b32 s63, s63, 7\n\t" \
  "v_mfma_f32_16x16x32_bf16 v[184:187], v[76:79], a[120:123], v[184:187]\n\t" \
  "buffer_load_dwordx4 v0, %[X], s47 offen offset:1024 sc1 lds\n\t" \
  "v_mfma_f32_16x16x32_bf16 v[188:191], v[76:79], a[124:127], v[188:191]\n\t" \
  "s_mul_i32 s62, s62, 688128\n\t" \
  "s_lshl_b32 s60, s63, 15\n\t" \
  "s_waitcnt lgkmcnt(3)\n\t" \
  "v_mfma_f32_16x16x32_bf16 v[192:195], v[240:243], v[80:83], v[192:195]\n\t" \
  "v_mfma_f32_16x16x32_bf16 v[160:163], v[80:83], a[128:131], v[160:163]\n\t" \
  "s_add_u32 s62, s62, s60\n\t" \
  "s_lshr_b32 s60, s86, s63\n\t" \
  "v_mfma_f32_16x16x32_bf16 v[164:167], v[80:83], a[132:135], v[164:167]\n\t" \
  "buffer_load_dwordx4 v0, %[X], s47 offen offset:2048 sc1 lds\n\t" \
  "v_mfma_f32_16x16x32_bf16 v[168:171], v[80:83], a[136:139], v[168:171]\n\t" \
  "s_and_b32 s60, s60, 1\n\t" \
  "s_lshl_b32 s60, s60, 13\n\t" \
  "v_mfma_f32_16x16x32_bf16 v[172:175], v[80:83], a[140:143], v[172:175]\n\t" \
  "v_mfma_f32_16x16x32_bf16 v[176:179], v[80:83], a[144:147], v[176:179]\n\t" \
  "s_add_u32 s62, s62, s60\n\t" \
  "s_add_u32 s62, s62, 656896\n\t" \
  "v_mfma_f32_16x16x32_bf16 v[180:183], v[80:83], a[148:151], v[180:183]\n\t" \
  "buffer_load_dwordx4 v0, %[X], s47 offen offset:3072 sc1 lds\n\t" \
  "v_mfma_f32_16x16x32_bf16 v[184:187], v[80:83], a[152:155], v[184:187]\n\t" \
  "s_cmp_gt_u32 s48, " TK_AE "\n\t" \
  "s_cselect_b32 s62, 0xf0000000, s62\n\t" \
  "v_mfma_f32_16x16x32_bf16 v[188:191], v[80:83], a[156:159], v[188:191]\n\t" \
  "s_waitcnt lgkmcnt(2)\n\t" \
  "v_mfma_f32_16x16x32_bf16 v[192:195], v[244:247], v[84:87], v[192:195]\n\t" \
  "v_mfma_f32_16x16x32_bf16 v[160:163], v[84:87], a[160:163], v[160:163]\n\t" \
  "s_add_u32 m0, s46, 8192\n\t" \
  "s_add_u32 s47, s81, s38\n\t" \
  "buffer_load_dwordx4 v0, %[X], s47 offen offset:0 sc1 lds\n\t" \
  "v_mfma_f32_16x16x32_bf16 v[164:167], v[84:87], a[164:167], v[164:167]\n\t" \
  "v_mfma_f32_16x16x32_bf16 v[168:171], v[84:87], a[168:171], v[168:171]\n\t" \
  "v_mfma_f32_16x16x32_bf16 v[172:175], v[84:87], a[172:175], v[172:175]\n\t" \
  "v_mfma_f32_16x16x32_bf16 v[176:179], v[84:87], a[176:179], v[176:179]\n\t" \
  "buffer_load_dwordx4 v0, %[X], s47 offen offset:1024 sc1 lds\n\t" \
  "v_mfma_f32_16x16x32_bf16 v[180:183], v[84:87], a[180:183], v[180:183]\n\t" \
  "v_mfma_f32_16x16x32_bf16 v[184:187], v[84:87], a[184:187], v[184:187]\n\t" \
  "v_mfma_f32_16x16x32_bf16 v[188:191], v[84:87], a[188:191], v[188:191]\n\t" \
  "s_waitcnt lgkmcnt(1)\n\t" \
  "v_mfma_f32_16x16x32_bf16 v[192:195], v[248:251], v[88:91], v[192:195]\n\t" \
  "buffer_load_dwordx4 v0, %[X], s47 offen offset:2048 sc1 lds\n\t" \
  "v_mfma_f32_16x16x32_bf16 v[160:163], v[88:91], a[192:195], v[160:163]\n\t" \
  "v_mfma_f32_16x16x32_bf16 v[164:167], v[88:91], a[196:199], v[164:167]\n\t" \
  "v_mfma_f32_16x16x32_bf16 v[168:171], v[88:91], a[200:203], v[168:171]\n\t" \
  "v_mfma_f32_16x16x32_bf16 v[172:175], v[88:91], a[204:207], v[172:175]\n\t" \
  "buffer_load_dwordx4 v0, %[X], s47 offen offset:3072 sc1 lds\n\t" \
  "v_mfma_f32_16x16x32_bf16 v[176:179], v[88:91], a[208:211], v[176:179]\n\t" \
  "v_mfma_f32_16x16x32_bf16 v[180:183], v[88:91], a[212:215], v[180:183]\n\t" \
  "buffer_load_dword v5, off, %[X], s62 sc1\n\t" \
  "v_mfma_f32_16x16x32_bf16 v[184:187], v[88:91], a[216:219], v[184:187]\n\t" \
  "v_mfma_f32_16x16x32_bf16 v[188:191], v[88:91], a[220:223], v[188:191]\n\t" \
  "s_waitcnt lgkmcnt(0)\n\t" \
  "v_mfma_f32_16x16x32_bf16 v[192:195], v[252:255], v[92:95], v[192:195]\n\t" \
  "v_mfma_f32_16x16x32_bf16 v[160:163], v[92:95], a[224:227], v[160:163]\n\t" \
  "v_mfma_f32_16x16x32_bf16 v[164:167], v[92:95], a[228:231], v[164:167]\n\t" \
  "v_mfma_f32_16x16x32_bf16 v[168:171], v[92:95], a[232:235], v[168:171]\n\t" \
  "v_cvt_pk_bf16_f32 v112, v192, v193\n\t" \
  "v_cvt_pk_bf16_f32 v113, v194, v195\n\t" \
  "v_mfma_f32_16x16x32_bf16 v[172:175], v[92:95], a[236:239], v[172:175]\n\t" \
  "buffer_store_dwordx2 v[112:113], v8, %[X], s54 offen sc1\n\t" \
  "v_mfma_f32_16x16x32_bf16 v[176:179], v[92:95], a[240:243], v[176:179]\n\t" \
  "v_cvt_pk_bf16_f32 v96, v160, v164\n\t" \
  "v_cvt_pk_bf16_f32 v100, v161, v165\n\t" \
  "v_mfma_f32_16x16x32_bf16 v[180:183], v[92:95], a[244:247], v[180:183]\n\t" \
  "v_cvt_pk_bf16_f32 v104, v162, v166\n\t" \
  "v_cvt_pk_bf16_f32 v108, v163, v167\n\t" \
  "v_mfma_f32_16x16x32_bf16 v[184:187], v[92:95], a[248:251], v[184:187]\n\t" \
  "v_cvt_pk_bf16_f32 v97, v168, v172\n\t" \
  "v_cvt_pk_bf16_f32 v101, v169, v173\n\t" \
  "v_mfma_f32_16x16x32_bf16 v[188:191], v[92:95], a[252:255], v[188:191]\n\t" \
  "v_cvt_pk_bf16_f32 v105, v170, v174\n\t" \
  "v_cvt_pk_bf16_f32 v109, v171, v175\n\t" \
  "v_cvt_pk_bf16_f32 v98, v176, v180\n\t" \
  "v_cvt_pk_bf16_f32 v102, v177, v181\n\t" \
  "v_cvt_pk_bf16_f32 v106, v178, v182\n\t" \
  "v_cvt_pk_bf16_f32 v110, v179, v183\n\t" \
  "s_nop 1\n\t" \
  "v_cvt_pk_bf16_f32 v99, v184, v188\n\t" \
  "buffer_store_dwordx4 v[96:99], v7, %[X], s50 offen sc1\n\t" \
  "v_cvt_pk_bf16_f32 v103, v185, v189\n\t" \
  "buffer_store_dwordx4 v[100:103], v7, %[X], s51 offen sc1\n\t" \
  "v_cvt_pk_bf16_f32 v107, v186, v190\n\t" \
  "buffer_store_dwordx4 v[104:107], v7, %[X], s52 offen sc1\n\t" \
  "v_cvt_pk_bf16_f32 v111, v187, v191\n\t" \
  "buffer_store_dwordx4 v[108:111], v7, %[X], s53 offen sc1\n\t" \
  "s_cmp_eq_u32 s48, 63\n\t" \
  "s_cbranch_scc1 LEND_%=\n\t" \
  "s_add_u32 s48, s48, 1\n\t" \
  "s_cmp_eq_u32 s81, s56\n\t" \
  "s_mov_b32 s81, s56\n\t" \
  "s_cbranch_scc0 LCOLD_%=\n\t" \
  "s_waitcnt vmcnt(5)\n\t" \
  "s_branch LTOP_%=\n\t" \
  "LEND_%=:\n\t" \
  "s_waitcnt vmcnt(0)\n\t" \
  "s_barrier\n\t" \
  "s_mov_b64 exec, 1\n\t" \
  "buffer_store_dword v3, off, %[X], s49 sc1\n\t" \
  "s_mov_b64 exec, -1\n\t" \
  "s_mov_b32 s49, 0xf0000000\n\t" \
  ""
#define QK_ASM \
  "v_lshrrev_b32 v16, 7, v0\n\t" \
  "v_lshlrev_b32 v16, 13, v16\n\t" \
  "v_lshrrev_b32 v19, 2, v0\n\t" \
  "v_and_b32 v19, 64, v19\n\t" \
  "v_xor_b32 v19, v19, v0\n\t" \
  "v_and_b32 v19, 0x70, v19\n\t" \
  "v_add_u32 v16, v16, v19\n\t" \
  "s_mul_i32 s63, s40, 40960\n\t" \
  "v_and_b32 v19, 0x300, v0\n\t" \
  "v_lshlrev_b32 v17, 2, v19\n\t" \
  "v_and_b32 v19, 0xc0, v0\n\t" \
  "v_lshl_add_u32 v17, v19, 1, v17\n\t" \
  "v_and_b32 v19, 0x30, v0\n\t" \
  "v_add3_u32 v17, v17, v19, s63\n\t" \
  "v_lshrrev_b32 v19, 1, v0\n\t" \
  "v_and_b32 v19, 64, v19\n\t" \
  "v_add_u32 v17, v17, v19\n\t" \
  "v_xor_b32 v18, 64, v17\n\t" \
  "s_add_u32 m0, s63, 0\n\t" \
  "s_mov_b32 s60, 0\n\t" \
  "buffer_load_dwordx4 v16, %[W], s60 offen lds\n\t" \
  "s_add_u32 m0, s63, 1024\n\t" \
  "s_mov_b32 s60, 65536\n\t" \
  "buffer_load_dwordx4 v16, %[W], s60 offen lds\n\t" \
  "s_add_u32 m0, s63, 2048\n\t" \
  "s_mov_b32 s60, 131072\n\t" \
  "buffer_load_dwordx4 v16, %[W], s60 offen lds\n\t" \
  "s_add_u32 m0, s63, 3072\n\t" \
  "s_mov_b32 s60, 196608\n\t" \
  "buffer_load_dwordx4 v16, %[W], s60 offen lds\n\t" \
  "s_add_u32 m0, s63, 4096\n\t" \
  "s_mov_b32 s60, 262144\n\t" \
  "buffer_load_dwordx4 v16, %[W], s60 offen lds\n\t" \
  "s_add_u32 m0, s63, 5120\n\t" \
  "s_mov_b32 s60, 327680\n\t" \
  "buffer_load_dwordx4 v16, %[W], s60 offen lds\n\t" \
  "s_add_u32 m0, s63, 6144\n\t" \
  "s_mov_b32 s60, 393216\n\t" \
  "buffer_load_dwordx4 v16, %[W], s60 offen lds\n\t" \
  "s_add_u32 m0, s63, 7168\n\t" \
  "s_mov_b32 s60, 458752\n\t" \
  "buffer_load_dwordx4 v16, %[W], s60 offen lds\n\t" \
  "s_add_u32 m0, s63, 8192\n\t" \
  "s_mov_b32 s60, 524288\n\t" \
  "buffer_load_dwordx4 v16, %[W], s60 offen lds\n\t" \
  "s_add_u32 m0, s63, 9216\n\t" \
  "s_mov_b32 s60, 589824\n\t" \
  "buffer_load_dwordx4 v16, %[W], s60 offen lds\n\t" \
  "s_add_u32 m0, s63, 10240\n\t" \
  "s_mov_b32 s60, 655360\n\t" \
  "buffer_load_dwordx4 v16, %[W], s60 offen lds\n\t" \
  "s_add_u32 m0, s63, 11264\n\t" \
  "s_mov_b32 s60, 720896\n\t" \
  "buffer_load_dwordx4 v16, %[W], s60 offen lds\n\t" \
  "s_add_u32 m0, s63, 12288\n\t" \
  "s_mov_b32 s60, 786432\n\t" \
  "buffer_load_dwordx4 v16, %[W], s60 offen lds\n\t" \
  "s_add_u32 m0, s63, 13312\n\t" \
  "s_mov_b32 s60, 851968\n\t" \
  "buffer_load_dwordx4 v16, %[W], s60 offen lds\n\t" \
  "s_add_u32 m0, s63, 14336\n\t" \
  "s_mov_b32 s60, 917504\n\t" \
  "buffer_load_dwordx4 v16, %[W], s60 offen lds\n\t" \
  "s_add_u32 m0, s63, 15360\n\t" \
  "s_mov_b32 s60, 983040\n\t" \
  "buffer_load_dwordx4 v16, %[W], s60 offen lds\n\t" \
  "s_add_u32 m0, s63, 16384\n\t" \
  "s_mov_b32 s60, 1048576\n\t" \
  "buffer_load_dwordx4 v16, %[W], s60 offen lds\n\t" \
  "s_add_u32 m0, s63, 17408\n\t" \
  "s_mov_b32 s60, 1114112\n\t" \
  "buffer_load_dwordx4 v16, %[W], s60 offen lds\n\t" \
  "s_add_u32 m0, s63, 18432\n\t" \
  "s_mov_b32 s60, 1179648\n\t" \
  "buffer_load_dwordx4 v16, %[W], s60 offen lds\n\t" \
  "s_add_u32 m0, s63, 19456\n\t" \
  "s_mov_b32 s60, 1245184\n\t" \
  "buffer_load_dwordx4 v16, %[W], s60 offen lds\n\t" \
  "s_add_u32 m0, s63, 20480\n\t" \
  "s_mov_b32 s60, 1310720\n\t" \
  "buffer_load_dwordx4 v16, %[W], s60 offen lds\n\t" \
  "s_add_u32 m0, s63, 21504\n\t" \
  "s_mov_b32 s60, 1376256\n\t" \
  "buffer_load_dwordx4 v16, %[W], s60 offen lds\n\t" \
  "s_add_u32 m0, s63, 22528\n\t" \
  "s_mov_b32 s60, 1441792\n\t" \
  "buffer_load_dwordx4 v16, %[W], s60 offen lds\n\t" \
  "s_add_u32 m0, s63, 23552\n\t" \
  "s_mov_b32 s60, 1507328\n\t" \
  "buffer_load_dwordx4 v16, %[W], s60 offen lds\n\t" \
  "s_add_u32 m0, s63, 24576\n\t" \
  "s_mov_b32 s60, 1572864\n\t" \
  "buffer_load_dwordx4 v16, %[W], s60 offen lds\n\t" \
  "s_add_u32 m0, s63, 25600\n\t" \
  "s_mov_b32 s60, 1638400\n\t" \
  "buffer_load_dwordx4 v16, %[W], s60 offen lds\n\t" \
  "s_add_u32 m0, s63, 26624\n\t" \
  "s_mov_b32 s60, 1703936\n\t" \
  "buffer_load_dwordx4 v16, %[W], s60 offen lds\n\t" \
  "s_add_u32 m0, s63, 27648\n\t" \
  "s_mov_b32 s60, 1769472\n\t" \
  "buffer_load_dwordx4 v16, %[W], s60 offen lds\n\t" \
  "s_add_u32 m0, s63, 28672\n\t" \
  "s_mov_b32 s60, 1835008\n\t" \
  "buffer_load_dwordx4 v16, %[W], s60 offen lds\n\t" \
  "s_add_u32 m0, s63, 29696\n\t" \
  "s_mov_b32 s60, 1900544\n\t" \
  "buffer_load_dwordx4 v16, %[W], s60 offen lds\n\t" \
  "s_add_u32 m0, s63, 30720\n\t" \
  "s_mov_b32 s60, 1966080\n\t" \
  "buffer_load_dwordx4 v16, %[W], s60 offen lds\n\t" \
  "s_add_u32 m0, s63, 31744\n\t" \
  "s_mov_b32 s60, 2031616\n\t" \
  "buffer_load_dwordx4 v16, %[W], s60 offen lds\n\t" \
  "s_add_u32 m0, s63, 32768\n\t" \
  "s_mov_b32 s60, 2097152\n\t" \
  "buffer_load_dwordx4 v16, %[W], s60 offen lds\n\t" \
  "s_add_u32 m0, s63, 33792\n\t" \
  "s_mov_b32 s60, 2162688\n\t" \
  "buffer_load_dwordx4 v16, %[W], s60 offen lds\n\t" \
  "s_add_u32 m0, s63, 34816\n\t" \
  "s_mov_b32 s60, 2228224\n\t" \
  "buffer_load_dwordx4 v16, %[W], s60 offen lds\n\t" \
  "s_add_u32 m0, s63, 35840\n\t" \
  "s_mov_b32 s60, 2293760\n\t" \
  "buffer_load_dwordx4 v16, %[W], s60 offen lds\n\t" \
  "s_add_u32 m0, s63, 36864\n\t" \
  "s_mov_b32 s60, 2359296\n\t" \
  "buffer_load_dwordx4 v16, %[W], s60 offen lds\n\t" \
  "s_add_u32 m0, s63, 37888\n\t" \
  "s_mov_b32 s60, 2424832\n\t" \
  "buffer_load_dwordx4 v16, %[W], s60 offen lds\n\t" \
  "s_add_u32 m0, s63, 38912\n\t" \
  "s_mov_b32 s60, 2490368\n\t" \
  "buffer_load_dwordx4 v16, %[W], s60 offen lds\n\t" \
  "s_add_u32 m0, s63, 39936\n\t" \
  "s_mov_b32 s60, 2555904\n\t" \
  "buffer_load_dwordx4 v16, %[W], s60 offen lds\n\t" \
  "s_waitcnt vmcnt(32)\n\t" \
  "ds_read_b64_tr_b16 a[0:1], v17 offset:0\n\t" \
  "ds_read_b64_tr_b16 a[2:3], v17 offset:512\n\t" \
  "ds_read_b64_tr_b16 a[4:5], v17 offset:4096\n\t" \
  "ds_read_b64_tr_b16 a[6:7], v17 offset:4608\n\t" \
  "ds_read_b64_tr_b16 a[72:73], v17 offset:8\n\t" \
  "ds_read_b64_tr_b16 a[74:75], v17 offset:520\n\t" \
  "ds_read_b64_tr_b16 a[76:77], v17 offset:4104\n\t" \
  "ds_read_b64_tr_b16 a[78:79], v17 offset:4616\n\t" \
  "ds_read_b64_tr_b16 a[144:145], v18 offset:0\n\t" \
  "ds_read_b64_tr_b16 a[146:147], v18 offset:512\n\t" \
  "ds_read_b64_tr_b16 a[148:149], v18 offset:4096\n\t" \
  "ds_read_b64_tr_b16 a[150:151], v18 offset:4608\n\t" \
  "ds_read_b64_tr_b16 a[216:217], v18 offset:8\n\t" \
  "ds_read_b64_tr_b16 a[218:219], v18 offset:520\n\t" \
  "ds_read_b64_tr_b16 a[220:221], v18 offset:4104\n\t" \
  "ds_read_b64_tr_b16 a[222:223], v18 offset:4616\n\t" \
  "s_waitcnt lgkmcnt(0)\n\t" \
  "s_add_u32 m0, s63, 0\n\t" \
  "s_mov_b32 s60, 2621440\n\t" \
  "buffer_load_dwordx4 v16, %[W], s60 offen lds\n\t" \
  "s_add_u32 m0, s63, 1024\n\t" \
  "s_mov_b32 s60, 2686976\n\t" \
  "buffer_load_dwordx4 v16, %[W], s60 offen lds\n\t" \
  "s_add_u32 m0, s63, 2048\n\t" \
  "s_mov_b32 s60, 2752512\n\t" \
  "buffer_load_dwordx4 v16, %[W], s60 offen lds\n\t" \
  "s_add_u32 m0, s63, 3072\n\t" \
  "s_mov_b32 s60, 2818048\n\t" \
  "buffer_load_dwordx4 v16, %[W], s60 offen lds\n\t" \
  "s_add_u32 m0, s63, 4096\n\t" \
  "s_mov_b32 s60, 2883584\n\t" \
  "buffer_load_dwordx4 v16, %[W], s60 offen lds\n\t" \
  "s_add_u32 m0, s63, 5120\n\t" \
  "s_mov_b32 s60, 2949120\n\t" \
  "buffer_load_dwordx4 v16, %[W], s60 offen lds\n\t" \
  "s_add_u32 m0, s63, 6144\n\t" \
  "s_mov_b32 s60, 3014656\n\t" \
  "buffer_load_dwordx4 v16, %[W], s60 offen lds\n\t" \
  "s_add_u32 m0, s63, 7168\n\t" \
  "s_mov_b32 s60, 3080192\n\t" \
  "buffer_load_dwordx4 v16, %[W], s60 offen lds\n\t" \
  "s_waitcnt vmcnt(32)\n\t" \
  "ds_read_b64_tr_b16 a[8:9], v17 offset:8192\n\t" \
  "ds_read_b64_tr_b16 a[10:11], v17 offset:8704\n\t" \
  "ds_read_b64_tr_b16 a[12:13], v17 offset:12288\n\t" \
  "ds_read_b64_tr_b16 a[14:15], v17 offset:12800\n\t" \
  "ds_read_b64_tr_b16 a[80:81], v17 offset:8200\n\t" \
  "ds_read_b64_tr_b16 a[82:83], v17 offset:8712\n\t" \
  "ds_read_b64_tr_b16 a[84:85], v17 offset:12296\n\t" \
  "ds_read_b64_tr_b16 a[86:87], v17 offset:12808\n\t" \
  "ds_read_b64_tr_b16 a[152:153], v18 offset:8192\n\t" \
  "ds_read_b64_tr_b16 a[154:155], v18 offset:8704\n\t" \
  "ds_read_b64_tr_b16 a[156:157], v18 offset:12288\n\t" \
  "ds_read_b64_tr_b16 a[158:159], v18 offset:12800\n\t" \
  "ds_read_b64_tr_b16 a[224:225], v18 offset:8200\n\t" \
  "ds_read_b64_tr_b16 a[226:227], v18 offset:8712\n\t" \
  "ds_read_b64_tr_b16 a[228:229], v18 offset:12296\n\t" \
  "ds_read_b64_tr_b16 a[230:231], v18 offset:12808\n\t" \
  "s_waitcnt lgkmcnt(0)\n\t" \
  "s_add_u32 m0, s63, 8192\n\t" \
  "s_mov_b32 s60, 3145728\n\t" \
  "buffer_load_dwordx4 v16, %[W], s60 offen lds\n\t" \
  "s_add_u32 m0, s63, 9216\n\t" \
  "s_mov_b32 s60, 3211264\n\t" \
  "buffer_load_dwordx4 v16, %[W], s60 offen lds\n\t" \
  "s_add_u32 m0, s63, 10240\n\t" \
  "s_mov_b32 s60, 3276800\n\t" \
  "buffer_load_dwordx4 v16, %[W], s60 offen lds\n\t" \
  "s_add_u32 m0, s63, 11264\n\t" \
  "s_mov_b32 s60, 3342336\n\t" \
  "buffer_load_dwordx4 v16, %[W], s60 offen lds\n\t" \
  "s_add_u32 m0, s63, 12288\n\t" \
  "s_mov_b32 s60, 3407872\n\t" \
  "buffer_load_dwordx4 v16, %[W], s60 offen lds\n\t" \
  "s_add_u32 m0, s63, 13312\n\t" \
  "s_mov_b32 s60, 3473408\n\t" \
  "buffer_load_dwordx4 v16, %[W], s60 offen lds\n\t" \
  "s_add_u32 m0, s63, 14336\n\t" \
  "s_mov_b32 s60, 3538944\n\t" \
  "buffer_load_dwordx4 v16, %[W], s60 offen lds\n\t" \
  "s_add_u32 m0, s63, 15360\n\t" \
  "s_mov_b32 s60, 3604480\n\t" \
  "buffer_load_dwordx4 v16, %[W], s60 offen lds\n\t" \
  "s_waitcnt vmcnt(32)\n\t" \
  "ds_read_b64_tr_b16 a[16:17], v17 offset:16384\n\t" \
  "ds_read_b64_tr_b16 a[18:19], v17 offset:16896\n\t" \
  "ds_read_b64_tr_b16 a[20:21], v17 offset:20480\n\t" \
  "ds_read_b64_tr_b16 a[22:23], v17 offset:20992\n\t" \
  "ds_read_b64_tr_b16 a[88:89], v17 offset:16392\n\t" \
  "ds_read_b64_tr_b16 a[90:91], v17 offset:16904\n\t" \
  "ds_read_b64_tr_b16 a[92:93], v17 offset:20488\n\t" \
  "ds_read_b64_tr_b16 a[94:95], v17 offset:21000\n\t" \
  "ds_read_b64_tr_b16 a[160:161], v18 offset:16384\n\t" \
  "ds_read_b64_tr_b16 a[162:163], v18 offset:16896\n\t" \
  "ds_read_b64_tr_b16 a[164:165], v18 offset:20480\n\t" \
  "ds_read_b64_tr_b16 a[166:167], v18 offset:20992\n\t" \
  "ds_read_b64_tr_b16 a[232:233], v18 offset:16392\n\t" \
  "ds_read_b64_tr_b16 a[234:235], v18 offset:16904\n\t" \
  "ds_read_b64_tr_b16 a[236:237], v18 offset:20488\n\t" \
  "ds_read_b64_tr_b16 a[238:239], v18 offset:21000\n\t" \
  "s_waitcnt lgkmcnt(0)\n\t" \
  "s_add_u32 m0, s63, 16384\n\t" \
  "s_mov_b32 s60, 3670016\n\t" \
  "buffer_load_dwordx4 v16, %[W], s60 offen lds\n\t" \
  "s_add_u32 m0, s63, 17408\n\t" \
  "s_mov_b32 s60, 3735552\n\t" \
  "buffer_load_dwordx4 v16, %[W], s60 offen lds\n\t" \
  "s_add_u32 m0, s63, 18432\n\t" \
  "s_mov_b32 s60, 3801088\n\t" \
  "buffer_load_dwordx4 v16, %[W], s60 offen lds\n\t" \
  "s_add_u32 m0, s63, 19456\n\t" \
  "s_mov_b32 s60, 3866624\n\t" \
  "buffer_load_dwordx4 v16, %[W], s60 offen lds\n\t" \
  "s_add_u32 m0, s63, 20480\n\t" \
  "s_mov_b32 s60, 3932160\n\t" \
  "buffer_load_dwordx4 v16, %[W], s60 offen lds\n\t" \
  "s_add_u32 m0, s63, 21504\n\t" \
  "s_mov_b32 s60, 3997696\n\t" \
  "buffer_load_dwordx4 v16, %[W], s60 offen lds\n\t" \
  "s_add_u32 m0, s63, 22528\n\t" \
  "s_mov_b32 s60, 4063232\n\t" \
  "buffer_load_dwordx4 v16, %[W], s60 offen lds\n\t" \
  "s_add_u32 m0, s63, 23552\n\t" \
  "s_mov_b32 s60, 4128768\n\t" \
  "buffer_load_dwordx4 v16, %[W], s60 offen lds\n\t" \
  "s_waitcnt vmcnt(32)\n\t" \
  "ds_read_b64_tr_b16 a[24:25], v17 offset:24576\n\t" \
  "ds_read_b64_tr_b16 a[26:27], v17 offset:25088\n\t" \
  "ds_read_b64_tr_b16 a[28:29], v17 offset:28672\n\t" \
  "ds_read_b64_tr_b16 a[30:31], v17 offset:29184\n\t" \
  "ds_read_b64_tr_b16 a[96:97], v17 offset:24584\n\t" \
  "ds_read_b64_tr_b16 a[98:99], v17 offset:25096\n\t" \
  "ds_read_b64_tr_b16 a[100:101], v17 offset:28680\n\t" \
  "ds_read_b64_tr_b16 a[102:103], v17 offset:29192\n\t" \
  "ds_read_b64_tr_b16 a[168:169], v18 offset:24576\n\t" \
  "ds_read_b64_tr_b16 a[170:171], v18 offset:25088\n\t" \
  "ds_read_b64_tr_b16 a[172:173], v18 offset:28672\n\t" \
  "ds_read_b64_tr_b16 a[174:175], v18 offset:29184\n\t" \
  "ds_read_b64_tr_b16 a[240:241], v18 offset:24584\n\t" \
  "ds_read_b64_tr_b16 a[242:243], v18 offset:25096\n\t" \
  "ds_read_b64_tr_b16 a[244:245], v18 offset:28680\n\t" \
  "ds_read_b64_tr_b16 a[246:247], v18 offset:29192\n\t" \
  "s_waitcnt lgkmcnt(0)\n\t" \
  "s_add_u32 m0, s63, 24576\n\t" \
  "s_mov_b32 s60, 4194304\n\t" \
  "buffer_load_dwordx4 v16, %[W], s60 offen lds\n\t" \
  "s_add_u32 m0, s63, 25600\n\t" \
  "s_mov_b32 s60, 4259840\n\t" \
  "buffer_load_dwordx4 v16, %[W], s60 offen lds\n\t" \
  "s_add_u32 m0, s63, 26624\n\t" \
  "s_mov_b32 s60, 4325376\n\t" \
  "buffer_load_dwordx4 v16, %[W], s60 offen lds\n\t" \
  "s_add_u32 m0, s63, 27648\n\t" \
  "s_mov_b32 s60, 4390912\n\t" \
  "buffer_load_dwordx4 v16, %[W], s60 offen lds\n\t" \
  "s_add_u32 m0, s63, 28672\n\t" \
  "s_mov_b32 s60, 4456448\n\t" \
  "buffer_load_dwordx4 v16, %[W], s60 offen lds\n\t" \
  "s_add_u32 m0, s63, 29696\n\t" \
  "s_mov_b32 s60, 4521984\n\t" \
  "buffer_load_dwordx4 v16, %[W], s60 offen lds\n\t" \
  "s_add_u32 m0, s63, 30720\n\t" \
  "s_mov_b32 s60, 4587520\n\t" \
  "buffer_load_dwordx4 v16, %[W], s60 offen lds\n\t" \
  "s_add_u32 m0, s63, 31744\n\t" \
  "s_mov_b32 s60, 4653056\n\t" \
  "buffer_load_dwordx4 v16, %[W], s60 offen lds\n\t" \
  "s_waitcnt vmcnt(32)\n\t" \
  "ds_read_b64_tr_b16 a[32:33], v17 offset:32768\n\t" \
  "ds_read_b64_tr_b16 a[34:35], v17 offset:33280\n\t" \
  "ds_read_b64_tr_b16 a[36:37], v17 offset:36864\n\t" \
  "ds_read_b64_tr_b16 a[38:39], v17 offset:37376\n\t" \
  "ds_read_b64_tr_b16 a[104:105], v17 offset:32776\n\t" \
  "ds_read_b64_tr_b16 a[106:107], v17 offset:33288\n\t" \
  "ds_read_b64_tr_b16 a[108:109], v17 offset:36872\n\t" \
  "ds_read_b64_tr_b16 a[110:111], v17 offset:37384\n\t" \
  "ds_read_b64_tr_b16 a[176:177], v18 offset:32768\n\t" \
  "ds_read_b64_tr_b16 a[178:179], v18 offset:33280\n\t" \
  "ds_read_b64_tr_b16 a[180:181], v18 offset:36864\n\t" \
  "ds_read_b64_tr_b16 a[182:183], v18 offset:37376\n\t" \
  "ds_read_b64_tr_b16 a[248:249], v18 offset:32776\n\t" \
  "ds_read_b64_tr_b16 a[250:251], v18 offset:33288\n\t" \
  "ds_read_b64_tr_b16 a[252:253], v18 offset:36872\n\t" \
  "ds_read_b64_tr_b16 a[254:255], v18 offset:37384\n\t" \
  "s_waitcnt lgkmcnt(0)\n\t" \
  "s_waitcnt vmcnt(24)\n\t" \
  "ds_read_b64_tr_b16 a[40:41], v17 offset:0\n\t" \
  "ds_read_b64_tr_b16 a[42:43], v17 offset:512\n\t" \
  "ds_read_b64_tr_b16 a[44:45], v17 offset:4096\n\t" \
  "ds_read_b64_tr_b16 a[46:47], v17 offset:4608\n\t" \
  "ds_read_b64_tr_b16 a[112:113], v17 offset:8\n\t" \
  "ds_read_b64_tr_b16 a[114:115], v17 offset:520\n\t" \
  "ds_read_b64_tr_b16 a[116:117], v17 offset:4104\n\t" \
  "ds_read_b64_tr_b16 a[118:119], v17 offset:4616\n\t" \
  "ds_read_b64_tr_b16 a[184:185], v18 offset:0\n\t" \
  "ds_read_b64_tr_b16 a[186:187], v18 offset:512\n\t" \
  "ds_read_b64_tr_b16 a[188:189], v18 offset:4096\n\t" \
  "ds_read_b64_tr_b16 a[190:191], v18 offset:4608\n\t" \
  "ds_read_b64_tr_b16 v[224:225], v18 offset:8\n\t" \
  "ds_read_b64_tr_b16 v[226:227], v18 offset:520\n\t" \
  "ds_read_b64_tr_b16 v[228:229], v18 offset:4104\n\t" \
  "ds_read_b64_tr_b16 v[230:231], v18 offset:4616\n\t" \
  "s_waitcnt lgkmcnt(0)\n\t" \
  "s_waitcnt vmcnt(16)\n\t" \
  "ds_read_b64_tr_b16 a[48:49], v17 offset:8192\n\t" \
  "ds_read_b64_tr_b16 a[50:51], v17 offset:8704\n\t" \
  "ds_read_b64_tr_b16 a[52:53], v17 offset:12288\n\t" \
  "ds_read_b64_tr_b16 a[54:55], v17 offset:12800\n\t" \
  "ds_read_b64_tr_b16 a[120:121], v17 offset:8200\n\t" \
  "ds_read_b64_tr_b16 a[122:123], v17 offset:8712\n\t" \
  "ds_read_b64_tr_b16 a[124:125], v17 offset:12296\n\t" \
  "ds_read_b64_tr_b16 a[126:127], v17 offset:12808\n\t" \
  "ds_read_b64_tr_b16 a[192:193], v18 offset:8192\n\t" \
  "ds_read_b64_tr_b16 a[194:195], v18 offset:8704\n\t" \
  "ds_read_b64_tr_b16 a[196:197], v18 offset:12288\n\t" \
  "ds_read_b64_tr_b16 a[198:199], v18 offset:12800\n\t" \
  "ds_read_b64_tr_b16 v[232:233], v18 offset:8200\n\t" \
  "ds_read_b64_tr_b16 v[234:235], v18 offset:8712\n\t" \
  "ds_read_b64_tr_b16 v[236:237], v18 offset:12296\n\t" \
  "ds_read_b64_tr_b16 v[238:239], v18 offset:12808\n\t" \
  "s_waitcnt lgkmcnt(0)\n\t" \
  "s_waitcnt vmcnt(8)\n\t" \
  "ds_read_b64_tr_b16 a[56:57], v17 offset:16384\n\t" \
  "ds_read_b64_tr_b16 a[58:59], v17 offset:16896\n\t" \
  "ds_read_b64_tr_b16 a[60:61], v17 offset:20480\n\t" \
  "ds_read_b64_tr_b16 a[62:63], v17 offset:20992\n\t" \
  "ds_read_b64_tr_b16 a[128:129], v17 offset:16392\n\t" \
  "ds_read_b64_tr_b16 a[130:131], v17 offset:16904\n\t" \
  "ds_read_b64_tr_b16 a[132:133], v17 offset:20488\n\t" \
  "ds_read_b64_tr_b16 a[134:135], v17 offset:21000\n\t" \
  "ds_read_b64_tr_b16 a[200:201], v18 offset:16384\n\t" \
  "ds_read_b64_tr_b16 a[202:203], v18 offset:16896\n\t" \
  "ds_read_b64_tr_b16 a[204:205], v18 offset:20480\n\t" \
  "ds_read_b64_tr_b16 a[206:207], v18 offset:20992\n\t" \
  "ds_read_b64_tr_b16 v[240:241], v18 offset:16392\n\t" \
  "ds_read_b64_tr_b16 v[242:243], v18 offset:16904\n\t" \
  "ds_read_b64_tr_b16 v[244:245], v18 offset:20488\n\t" \
  "ds_read_b64_tr_b16 v[246:247], v18 offset:21000\n\t" \
  "s_waitcnt lgkmcnt(0)\n\t" \
  "s_waitcnt vmcnt(0)\n\t" \
  "ds_read_b64_tr_b16 a[64:65], v17 offset:24576\n\t" \
  "ds_read_b64_tr_b16 a[66:67], v17 offset:25088\n\t" \
  "ds_read_b64_tr_b16 a[68:69], v17 offset:28672\n\t" \
  "ds_read_b64_tr_b16 a[70:71], v17 offset:29184\n\t" \
  "ds_read_b64_tr_b16 a[136:137], v17 offset:24584\n\t" \
  "ds_read_b64_tr_b16 a[138:139], v17 offset:25096\n\t" \
  "ds_read_b64_tr_b16 a[140:141], v17 offset:28680\n\t" \
  "ds_read_b64_tr_b16 a[142:143], v17 offset:29192\n\t" \
  "ds_read_b64_tr_b16 a[208:209], v18 offset:24576\n\t" \
  "ds_read_b64_tr_b16 a[210:211], v18 offset:25088\n\t" \
  "ds_read_b64_tr_b16 a[212:213], v18 offset:28672\n\t" \
  "ds_read_b64_tr_b16 a[214:215], v18 offset:29184\n\t" \
  "ds_read_b64_tr_b16 v[248:249], v18 offset:24584\n\t" \
  "ds_read_b64_tr_b16 v[250:251], v18 offset:25096\n\t" \
  "ds_read_b64_tr_b16 v[252:253], v18 offset:28680\n\t" \
  "ds_read_b64_tr_b16 v[254:255], v18 offset:29192\n\t" \
  "s_waitcnt lgkmcnt(0)\n\t" \
  "s_mov_b32 s84, " TK_CAP "\n\t" \
  "s_mov_b32 s48, 0\n\t" \
  "s_mov_b32 s49, 0xf0000000\n\t" \
  "s_mov_b32 s46, s41\n\t" \
  "s_mov_b32 s56, 0\n\t" \
  "v_lshrrev_b32 v7, 2, v0\n\t" \
  "s_mov_b32 s60, " TK_CTAB "\n\t" \
  "buffer_load_dword v4, v7, %[X], s60 offen offset:88 sc1\n\t" \
  "buffer_load_dword v7, v7, %[X], s60 offen offset:0 sc1\n\t" \
  "s_waitcnt vmcnt(0)\n\t" \
  "v_and_b32 v4, 1, v4\n\t" \
  "v_and_b32 v7, 1, v7\n\t" \
  "v_cmp_ne_u32 s[90:91], s67, v4\n\t" \
  "v_cmp_ne_u32 s[88:89], s67, v7\n\t" \
  "s_nop 1\n\t" \
  "s_mov_b32 s57, 0xf0000000\n\t" \
  "s_add_u32 s83, s48, 0\n\t" \
  "s_lshr_b64 s[60:61], s[90:91], s83\n\t" \
  "s_and_b32 s60, s60, 1\n\t" \
  "s_lshl_b32 s60, s60, 13\n\t" \
  "s_add_u32 s83, s83, " TK_SG0 "\n\t" \
  "s_lshl_b32 s83, s83, 21\n\t" \
  "s_or_b32 s83, s83, s60\n\t" \
  "s_add_u32 s80, s48, " TK_TRN "\n\t" \
  "s_lshr_b64 s[60:61], s[90:91], s80\n\t" \
  "s_and_b32 s60, s60, 1\n\t" \
  "s_lshl_b32 s60, s60, 13\n\t" \
  "s_add_u32 s80, s80, " TK_SG0 "\n\t" \
  "s_lshl_b32 s80, s80, 21\n\t" \
  "s_or_b32 s80, s80, s60\n\t" \
  "s_add_u32 s62, s83, s39\n\t" \
  "s_add_u32 s80, s80, s39\n\t" \
  "s_add_u32 s80, s80, 2048\n\t" \
  "s_cmp_lt_u32 s48, " TK_TRM "\n\t" \
  "s_cselect_b32 s80, s62, s80\n\t" \
  "v_add_u32 v7, s62, v3\n\t" \
  "s_mov_b32 exec_lo, 0\n\t" \
  "v_mov_b32 v7, s80\n\t" \
  "s_mov_b32 exec_lo, -1\n\t" \
  "LCOLD_%=:\n\t" \
  "s_waitcnt vmcnt(0)\n\t" \
  "s_mov_b64 exec, 1\n\t" \
  "buffer_store_dword v6, off, %[X], s49 sc1\n\t" \
  "s_mov_b64 exec, -1\n\t" \
  "s_mov_b32 s49, 0xf0000000\n\t" \
  "s_add_u32 s55, s83, s36\n\t" \
  "s_cmp_lg_u32 s40, 0\n\t" \
  "s_cbranch_scc1 LGO_%=\n\t" \
  "s_and_b32 s62, s45, 3\n\t" \
  "s_lshl_b32 s62, s62, 7\n\t" \
  "s_add_u32 s62, s62, s83\n\t" \
  "s_add_u32 s62, s62, s39\n\t" \
  "s_mov_b32 s61, 0\n\t" \
  "LPO_%=:\n\t" \
  "buffer_load_dword v4, off, %[X], s62 sc1\n\t" \
  "s_waitcnt vmcnt(0)\n\t" \
  "v_cmp_eq_u32 vcc, 0, v4\n\t" \
  "s_cbranch_vccz LPO_%=x\n\t" \
  "s_add_u32 s61, s61, 1\n\t" \
  "s_sleep " TK_PSH "\n\t" \
  "s_cmp_gt_u32 s61, s84\n\t" \
  "s_cbranch_scc1 LPO_%=g\n\t" \
  "s_and_b32 s60, s61, 1023\n\t" \
  "s_cmp_lg_u32 s60, 0\n\t" \
  "s_cbranch_scc1 LPO_%=\n\t" \
  "buffer_load_dword v4, off, %[X], s92 sc1\n\t" \
  "s_waitcnt vmcnt(0)\n\t" \
  "v_readfirstlane_b32 s60, v4\n\t" \
  "s_nop 0\n\t" \
  "s_cmp_eq_u32 s60, 0\n\t" \
  "s_cbranch_scc1 LPO_%=\n\t" \
  "LPO_%=g:\n\t" \
  "s_mov_b32 s84, 0\n\t" \
  "s_or_b32 s60, s48, 1610612736\n\t" \
  "v_mov_b32 v4, s60\n\t" \
  "s_mov_b64 exec, 1\n\t" \
  "buffer_store_dword v4, off, %[X], s92 sc1\n\t" \
  "s_mov_b64 exec, -1\n\t" \
  "LPO_%=x:\n\t" \
  "s_mov_b32 s61, 0\n\t" \
  "LPH_%=:\n\t" \
  "buffer_load_dword v4, v7, %[X], 0 offen sc1\n\t" \
  "s_waitcnt vmcnt(0)\n\t" \
  "v_cmp_eq_u32 vcc, 0, v4\n\t" \
  "s_cbranch_vccz LPH_%=x\n\t" \
  "s_add_u32 s61, s61, 1\n\t" \
  "s_sleep " TK_PSH "\n\t" \
  "s_cmp_gt_u32 s61, s84\n\t" \
  "s_cbranch_scc1 LPH_%=g\n\t" \
  "s_and_b32 s60, s61, 1023\n\t" \
  "s_cmp_lg_u32 s60, 0\n\t" \
  "s_cbranch_scc1 LPH_%=\n\t" \
  "buffer_load_dword v4, off, %[X], s92 sc1\n\t" \
  "s_waitcnt vmcnt(0)\n\t" \
  "v_readfirstlane_b32 s60, v4\n\t" \
  "s_nop 0\n\t" \
  "s_cmp_eq_u32 s60, 0\n\t" \
  "s_cbranch_scc1 LPH_%=\n\t" \
  "LPH_%=g:\n\t" \
  "s_mov_b32 s84, 0\n\t" \
  "s_or_b32 s60, s48, 1610612736\n\t" \
  "v_mov_b32 v4, s60\n\t" \
  "s_mov_b64 exec, 1\n\t" \
  "buffer_store_dword v4, off, %[X], s92 sc1\n\t" \
  "s_mov_b64 exec, -1\n\t" \
  "LPH_%=x:\n\t" \
  "s_cmp_lt_i32 s57, 0\n\t" \
  "s_cbranch_scc1 LGO_%=\n\t" \
  "s_movk_i32 s63, 4000\n\t" \
  "s_branch LGC_%=u\n\t" \
  "LGC_%=:\n\t" \
  "s_sleep " TK_PSG "\n\t" \
  "buffer_load_dword v8, off, %[X], s57 sc1\n\t" \
  "s_waitcnt vmcnt(0)\n\t" \
  "LGC_%=u:\n\t" \
  "s_memrealtime s[52:53]\n\t" \
  "v_readfirstlane_b32 s60, v8\n\t" \
  "s_waitcnt lgkmcnt(0)\n\t" \
  "s_sub_u32 s63, s63, 1\n\t" \
  "s_cbranch_scc1 LGO_%=\n\t" \
  "s_cmp_eq_u32 s60, -1\n\t" \
  "s_cbranch_scc1 LGC_%=\n\t" \
  "s_not_b32 s60, s60\n\t" \
  "s_lshl_b32 s61, s52, 8\n\t" \
  "s_sub_u32 s60, s60, s61\n\t" \
  "s_ashr_i32 s60, s60, 8\n\t" \
  "s_sub_i32 s60, s60, " TK_QXC "\n\t" \
  "s_min_i32 s60, s60, 400\n\t" \
  "s_add_u32 s54, s52, s60\n\t" \
  "LGC_%=w:\n\t" \
  "s_sleep 1\n\t" \
  "s_memrealtime s[52:53]\n\t" \
  "s_waitcnt lgkmcnt(0)\n\t" \
  "s_sub_i32 s53, s52, s54\n\t" \
  "s_cmp_lt_i32 s53, 0\n\t" \
  "s_cbranch_scc1 LGC_%=w\n\t" \
  "LGO_%=:\n\t" \
  "s_barrier\n\t" \
  "s_add_u32 m0, s46, 0\n\t" \
  "s_add_u32 s47, s55, 0\n\t" \
  "buffer_load_dwordx4 v0, %[X], s47 offen sc1 lds\n\t" \
  "s_add_u32 m0, s46, 4096\n\t" \
  "s_add_u32 s47, s55, 4096\n\t" \
  "buffer_load_dwordx4 v0, %[X], s47 offen sc1 lds\n\t" \
  "s_add_u32 m0, s46, 8192\n\t" \
  "s_add_u32 s47, s55, 16384\n\t" \
  "buffer_load_dwordx4 v0, %[X], s47 offen sc1 lds\n\t" \
  "s_add_u32 m0, s46, 12288\n\t" \
  "s_add_u32 s47, s55, 20480\n\t" \
  "buffer_load_dwordx4 v0, %[X], s47 offen sc1 lds\n\t" \
  "s_add_u32 m0, s46, 16384\n\t" \
  "s_add_u32 s47, s55, 32768\n\t" \
  "buffer_load_dwordx4 v0, %[X], s47 offen sc1 lds\n\t" \
  "s_add_u32 m0, s46, 20480\n\t" \
  "s_add_u32 s47, s55, 36864\n\t" \
  "buffer_load_dwordx4 v0, %[X], s47 offen sc1 lds\n\t" \
  "s_add_u32 m0, s46, 24576\n\t" \
  "s_add_u32 s47, s55, 49152\n\t" \
  "buffer_load_dwordx4 v0, %[X], s47 offen sc1 lds\n\t" \
  "s_add_u32 m0, s46, 28672\n\t" \
  "s_add_u32 s47, s55, 53248\n\t" \
  "buffer_load_dwordx4 v0, %[X], s47 offen sc1 lds\n\t" \
  "s_add_u32 m0, s46, 32768\n\t" \
  "s_add_u32 s47, s55, 65536\n\t" \
  "buffer_load_dwordx4 v0, %[X], s47 offen sc1 lds\n\t" \
  "s_waitcnt vmcnt(5)\n\t" \
  "LTOP_%=:\n\t" \
  "s_barrier\n\t" \
  "ds_read_b128 v[64:67], v1 offset:0\n\t" \
  "ds_read_b128 v[68:71], v1 offset:2048\n\t" \
  "ds_read_b128 v[72:75], v2 offset:0\n\t" \
  "ds_read_b128 v[76:79], v2 offset:2048\n\t" \
  "ds_read_b128 v[80:83], v1 offset:4096\n\t" \
  "ds_read_b128 v[84:87], v1 offset:6144\n\t" \
  "s_mov_b32 s82, s83\n\t" \
  "s_xor_b32 s46, s46, 0x10000\n\t" \
  "s_waitcnt lgkmcnt(5)\n\t" \
  "v_mfma_f32_16x16x32_bf16 v[16:19], a[0:3], v[64:67], 0\n\t" \
  "ds_read_b128 v[88:91], v2 offset:4096\n\t" \
  "v_mfma_f32_16x16x32_bf16 v[20:23], a[72:75], v[64:67], 0\n\t" \
  "s_add_u32 s50, s48, 0\n\t" \
  "s_and_b32 s50, s50, " TK_TRM "\n\t" \
  "v_mfma_f32_16x16x32_bf16 v[24:27], a[144:147], v[64:67], 0\n\t" \
  "ds_read_b128 v[92:95], v2 offset:6144\n\t" \
  "v_mfma_f32_16x16x32_bf16 v[28:31], a[216:219], v[64:67], 0\n\t" \
  "s_lshr_b64 s[60:61], s[90:91], s50\n\t" \
  "s_and_b32 s60, s60, 1\n\t" \
  "s_waitcnt lgkmcnt(5)\n\t" \
  "v_mfma_f32_16x16x32_bf16 v[16:19], a[4:7], v[72:75], v[16:19]\n\t" \
  "ds_read_b128 v[96:99], v1 offset:8192\n\t" \
  "v_mfma_f32_16x16x32_bf16 v[20:23], a[76:79], v[72:75], v[20:23]\n\t" \
  "s_lshl_b32 s60, s60, 13\n\t" \
  "s_add_u32 s50, s50, " TK_SG0 "\n\t" \
  "v_mfma_f32_16x16x32_bf16 v[24:27], a[148:151], v[72:75], v[24:27]\n\t" \
  "ds_read_b128 v[100:103], v1 offset:10240\n\t" \
  "v_mfma_f32_16x16x32_bf16 v[28:31], a[220:223], v[72:75], v[28:31]\n\t" \
  "s_lshl_b32 s50, s50, 21\n\t" \
  "s_or_b32 s50, s50, s60\n\t" \
  "s_waitcnt lgkmcnt(5)\n\t" \
  "v_mfma_f32_16x16x32_bf16 v[16:19], a[8:11], v[80:83], v[16:19]\n\t" \
  "ds_read_b128 v[104:107], v2 offset:8192\n\t" \
  "v_mfma_f32_16x16x32_bf16 v[20:23], a[80:83], v[80:83], v[20:23]\n\t" \
  "s_add_u32 s50, s50, s37\n\t" \
  "s_add_u32 s51, s50, 4096\n\t" \
  "v_mfma_f32_16x16x32_bf16 v[24:27], a[152:155], v[80:83], v[24:27]\n\t" \
  "ds_read_b128 v[108:111], v2 offset:10240\n\t" \
  "v_mfma_f32_16x16x32_bf16 v[28:31], a[224:227], v[80:83], v[28:31]\n\t" \
  "s_add_u32 s83, s48, 1\n\t" \
  "s_lshr_b64 s[60:61], s[90:91], s83\n\t" \
  "s_waitcnt lgkmcnt(5)\n\t" \
  "v_mfma_f32_16x16x32_bf16 v[16:19], a[12:15], v[88:91], v[16:19]\n\t" \
  "ds_read_b128 v[112:115], v1 offset:12288\n\t" \
  "v_mfma_f32_16x16x32_bf16 v[20:23], a[84:87], v[88:91], v[20:23]\n\t" \
  "s_and_b32 s60, s60, 1\n\t" \
  "s_lshl_b32 s60, s60, 13\n\t" \
  "v_mfma_f32_16x16x32_bf16 v[24:27], a[156:159], v[88:91], v[24:27]\n\t" \
  "ds_read_b128 v[116:119], v1 offset:14336\n\t" \
  "v_mfma_f32_16x16x32_bf16 v[28:31], a[228:231], v[88:91], v[28:31]\n\t" \
  "s_add_u32 s83, s83, " TK_SG0 "\n\t" \
  "s_lshl_b32 s83, s83, 21\n\t" \
  "s_waitcnt lgkmcnt(5)\n\t" \
  "v_mfma_f32_16x16x32_bf16 v[16:19], a[16:19], v[96:99], v[16:19]\n\t" \
  "ds_read_b128 v[120:123], v2 offset:12288\n\t" \
  "v_mfma_f32_16x16x32_bf16 v[20:23], a[88:91], v[96:99], v[20:23]\n\t" \
  "s_or_b32 s83, s83, s60\n\t" \
  "s_cmp_lg_u32 s56, 0\n\t" \
  "v_mfma_f32_16x16x32_bf16 v[24:27], a[160:163], v[96:99], v[24:27]\n\t" \
  "ds_read_b128 v[124:127], v2 offset:14336\n\t" \
  "v_mfma_f32_16x16x32_bf16 v[28:31], a[232:235], v[96:99], v[28:31]\n\t" \
  "s_cbranch_scc1 LWARM_%=\n\t" \
  "s_waitcnt vmcnt(0)\n\t" \
  "LWARM_%=:\n\t" \
  "s_barrier\n\t" \
  "s_waitcnt lgkmcnt(5)\n\t" \
  "v_mfma_f32_16x16x32_bf16 v[16:19], a[20:23], v[104:107], v[16:19]\n\t" \
  "ds_read_b128 v[128:131], v1 offset:16384\n\t" \
  "v_mfma_f32_16x16x32_bf16 v[20:23], a[92:95], v[104:107], v[20:23]\n\t" \
  "v_mfma_f32_16x16x32_bf16 v[24:27], a[164:167], v[104:107], v[24:27]\n\t" \
  "ds_read_b128 v[132:135], v1 offset:18432\n\t" \
  "v_mfma_f32_16x16x32_bf16 v[28:31], a[236:239], v[104:107], v[28:31]\n\t" \
  "s_add_i32 s62, s48, s42\n\t" \
  "s_add_i32 s62, s62, 1\n\t" \
  "s_waitcnt lgkmcnt(5)\n\t" \
  "v_mfma_f32_16x16x32_bf16 v[16:19], a[24:27], v[112:115], v[16:19]\n\t" \
  "ds_read_b128 v[136:139], v2 offset:16384\n\t" \
  "v_mfma_f32_16x16x32_bf16 v[20:23], a[96:99], v[112:115], v[20:23]\n\t" \
  "s_mov_b32 s61, s62\n\t" \
  "v_mfma_f32_16x16x32_bf16 v[24:27], a[168:171], v[112:115], v[24:27]\n\t" \
  "ds_read_b128 v[140:143], v2 offset:18432\n\t" \
  "v_mfma_f32_16x16x32_bf16 v[28:31], a[240:243], v[112:115], v[28:31]\n\t" \
  "s_mul_i32 s63, s62, 43\n\t" \
  "s_lshr_b32 s63, s63, 9\n\t" \
  "s_waitcnt lgkmcnt(5)\n\t" \
  "v_mfma_f32_16x16x32_bf16 v[16:19], a[28:31], v[120:123], v[16:19]\n\t" \
  "ds_read_b128 v[144:147], v1 offset:20480\n\t" \
  "v_mfma_f32_16x16x32_bf16 v[20:23], a[100:103], v[120:123], v[20:23]\n\t" \
  "s_mul_i32 s63, s63, " TK_RM "\n\t" \
  "s_sub_u32 s62, s62, s63\n\t" \
  "v_mfma_f32_16x16x32_bf16 v[24:27], a[172:175], v[120:123], v[24:27]\n\t" \
  "ds_read_b128 v[148:151], v1 offset:22528\n\t" \
  "v_mfma_f32_16x16x32_bf16 v[28:31], a[244:247], v[120:123], v[28:31]\n\t" \
  "s_mul_i32 s63, s62, 43\n\t" \
  "s_lshr_b32 s63, s63, 7\n\t" \
  "s_waitcnt lgkmcnt(5)\n\t" \
  "v_mfma_f32_16x16x32_bf16 v[16:19], a[32:35], v[128:131], v[16:19]\n\t" \
  "ds_read_b128 v[152:155], v2 offset:20480\n\t" \
  "v_mfma_f32_16x16x32_bf16 v[20:23], a[104:107], v[128:131], v[20:23]\n\t" \
  "s_mul_i32 s57, s62, 688128\n\t" \
  "s_lshl_b32 s60, s63, 15\n\t" \
  "v_mfma_f32_16x16x32_bf16 v[24:27], a[176:179], v[128:131], v[24:27]\n\t" \
  "ds_read_b128 v[156:159], v2 offset:22528\n\t" \
  "v_mfma_f32_16x16x32_bf16 v[28:31], a[248:251], v[128:131], v[28:31]\n\t" \
  "s_add_u32 s57, s57, s60\n\t" \
  "s_lshr_b32 s60, s88, s63\n\t" \
  "s_waitcnt lgkmcnt(5)\n\t" \
  "v_mfma_f32_16x16x32_bf16 v[16:19], a[36:39], v[136:139], v[16:19]\n\t" \
  "ds_read_b128 v[160:163], v1 offset:24576\n\t" \
  "v_mfma_f32_16x16x32_bf16 v[20:23], a[108:111], v[136:139], v[20:23]\n\t" \
  "s_and_b32 s60, s60, 1\n\t" \
  "s_lshl_b32 s60, s60, 13\n\t" \
  "v_mfma_f32_16x16x32_bf16 v[24:27], a[180:183], v[136:139], v[24:27]\n\t" \
  "ds_read_b128 v[164:167], v1 offset:26624\n\t" \
  "v_mfma_f32_16x16x32_bf16 v[28:31], a[252:255], v[136:139], v[28:31]\n\t" \
  "s_add_u32 s57, s57, s60\n\t" \
  "s_add_u32 s57, s57, 656896\n\t" \
  "s_waitcnt lgkmcnt(5)\n\t" \
  "v_mfma_f32_16x16x32_bf16 v[16:19], a[40:43], v[144:147], v[16:19]\n\t" \
  "ds_read_b128 v[168:171], v2 offset:24576\n\t" \
  "v_mfma_f32_16x16x32_bf16 v[20:23], a[112:115], v[144:147], v[20:23]\n\t" \
  "s_cmp_lt_i32 s61, 1\n\t" \
  "s_cselect_b32 s57, 0xf0000000, s57\n\t" \
  "v_mfma_f32_16x16x32_bf16 v[24:27], a[184:187], v[144:147], v[24:27]\n\t" \
  "ds_read_b128 v[172:175], v2 offset:26624\n\t" \
  "v_mfma_f32_16x16x32_bf16 v[28:31], v[224:227], v[144:147], v[28:31]\n\t" \
  "s_cmp_lg_u32 s40, 0\n\t" \
  "s_cselect_b32 s57, 0xf0000000, s57\n\t" \
  "s_waitcnt lgkmcnt(5)\n\t" \
  "v_mfma_f32_16x16x32_bf16 v[16:19], a[44:47], v[152:155], v[16:19]\n\t" \
  "ds_read_b128 v[176:179], v1 offset:28672\n\t" \
  "v_mfma_f32_16x16x32_bf16 v[20:23], a[116:119], v[152:155], v[20:23]\n\t" \
  "s_add_u32 s80, s48, " TK_TRN "\n\t" \
  "s_lshr_b64 s[60:61], s[90:91], s80\n\t" \
  "s_and_b32 s60, s60, 1\n\t" \
  "v_mfma_f32_16x16x32_bf16 v[24:27], a[188:191], v[152:155], v[24:27]\n\t" \
  "ds_read_b128 v[180:183], v1 offset:30720\n\t" \
  "v_mfma_f32_16x16x32_bf16 v[28:31], v[228:231], v[152:155], v[28:31]\n\t" \
  "s_lshl_b32 s60, s60, 13\n\t" \
  "s_add_u32 s80, s80, " TK_SG0 "\n\t" \
  "s_lshl_b32 s80, s80, 21\n\t" \
  "s_waitcnt lgkmcnt(5)\n\t" \
  "v_mfma_f32_16x16x32_bf16 v[16:19], a[48:51], v[160:163], v[16:19]\n\t" \
  "ds_read_b128 v[184:187], v2 offset:28672\n\t" \
  "v_mfma_f32_16x16x32_bf16 v[20:23], a[120:123], v[160:163], v[20:23]\n\t" \
  "s_or_b32 s80, s80, s60\n\t" \
  "s_add_u32 s62, s83, s39\n\t" \
  "s_add_u32 s80, s80, s39\n\t" \
  "v_mfma_f32_16x16x32_bf16 v[24:27], a[192:195], v[160:163], v[24:27]\n\t" \
  "ds_read_b128 v[188:191], v2 offset:30720\n\t" \
  "v_mfma_f32_16x16x32_bf16 v[28:31], v[232:235], v[160:163], v[28:31]\n\t" \
  "s_add_u32 s80, s80, 2048\n\t" \
  "s_waitcnt lgkmcnt(5)\n\t" \
  "v_mfma_f32_16x16x32_bf16 v[16:19], a[52:55], v[168:171], v[16:19]\n\t" \
  "ds_read_b128 v[192:195], v1 offset:32768\n\t" \
  "v_mfma_f32_16x16x32_bf16 v[20:23], a[124:127], v[168:171], v[20:23]\n\t" \
  "v_mfma_f32_16x16x32_bf16 v[24:27], a[196:199], v[168:171], v[24:27]\n\t" \
  "ds_read_b128 v[196:199], v1 offset:34816\n\t" \
  "v_mfma_f32_16x16x32_bf16 v[28:31], v[236:239], v[168:171], v[28:31]\n\t" \
  "s_cmp_lt_u32 s48, " TK_TRM "\n\t" \
  "s_cselect_b32 s80, s62, s80\n\t" \
  "s_waitcnt lgkmcnt(5)\n\t" \
  "v_mfma_f32_16x16x32_bf16 v[16:19], a[56:59], v[176:179], v[16:19]\n\t" \
  "ds_read_b128 v[200:203], v2 offset:32768\n\t" \
  "v_mfma_f32_16x16x32_bf16 v[20:23], a[128:131], v[176:179], v[20:23]\n\t" \
  "v_add_u32 v7, s62, v3\n\t" \
  "s_mov_b32 exec_lo, 0\n\t" \
  "v_mov_b32 v7, s80\n\t" \
  "s_mov_b32 exec_lo, -1\n\t" \
  "v_mfma_f32_16x16x32_bf16 v[24:27], a[200:203], v[176:179], v[24:27]\n\t" \
  "ds_read_b128 v[204:207], v2 offset:34816\n\t" \
  "v_mfma_f32_16x16x32_bf16 v[28:31], v[240:243], v[176:179], v[28:31]\n\t" \
  "s_waitcnt lgkmcnt(5)\n\t" \
  "v_mfma_f32_16x16x32_bf16 v[16:19], a[60:63], v[184:187], v[16:19]\n\t" \
  "v_mfma_f32_16x16x32_bf16 v[20:23], a[132:135], v[184:187], v[20:23]\n\t" \
  "buffer_load_dword v4, v7, %[X], 0 offen sc1\n\t" \
  "v_mfma_f32_16x16x32_bf16 v[24:27], a[204:207], v[184:187], v[24:27]\n\t" \
  "v_mfma_f32_16x16x32_bf16 v[28:31], v[244:247], v[184:187], v[28:31]\n\t" \
  "s_waitcnt lgkmcnt(3)\n\t" \
  "v_mfma_f32_16x16x32_bf16 v[16:19], a[64:67], v[192:195], v[16:19]\n\t" \
  "v_xor_b32 v1, 0x10000, v1\n\t" \
  "v_xor_b32 v2, 0x10000, v2\n\t" \
  "v_mfma_f32_16x16x32_bf16 v[20:23], a[136:139], v[192:195], v[20:23]\n\t" \
  "v_mfma_f32_16x16x32_bf16 v[24:27], a[208:211], v[192:195], v[24:27]\n\t" \
  "v_mfma_f32_16x16x32_bf16 v[28:31], v[248:251], v[192:195], v[28:31]\n\t" \
  "s_waitcnt lgkmcnt(1)\n\t" \
  "v_mfma_f32_16x16x32_bf16 v[16:19], a[68:71], v[200:203], v[16:19]\n\t" \
  "v_mfma_f32_16x16x32_bf16 v[20:23], a[140:143], v[200:203], v[20:23]\n\t" \
  "v_mfma_f32_16x16x32_bf16 v[24:27], a[212:215], v[200:203], v[24:27]\n\t" \
  "v_mfma_f32_16x16x32_bf16 v[28:31], v[252:255], v[200:203], v[28:31]\n\t" \
  "v_mfma_f32_16x16x32_bf16 v[32:35], a[0:3], v[68:71], 0\n\t" \
  "v_cvt_pk_bf16_f32 v48, v16, v17\n\t" \
  "v_cvt_pk_bf16_f32 v49, v18, v19\n\t" \
  "v_mfma_f32_16x16x32_bf16 v[36:39], a[72:75], v[68:71], 0\n\t" \
  "v_cvt_pk_bf16_f32 v50, v20, v21\n\t" \
  "v_cvt_pk_bf16_f32 v51, v22, v23\n\t" \
  "v_mfma_f32_16x16x32_bf16 v[32:35], a[4:7], v[76:79], v[32:35]\n\t" \
  "v_cvt_pk_bf16_f32 v52, v24, v25\n\t" \
  "v_cvt_pk_bf16_f32 v53, v26, v27\n\t" \
  "v_mfma_f32_16x16x32_bf16 v[36:39], a[76:79], v[76:79], v[36:39]\n\t" \
  "v_cvt_pk_bf16_f32 v54, v28, v29\n\t" \
  "v_cvt_pk_bf16_f32 v55, v30, v31\n\t" \
  "v_mfma_f32_16x16x32_bf16 v[32:35], a[8:11], v[84:87], v[32:35]\n\t" \
  "buffer_store_dwordx4 v[48:51], v0, %[X], s50 offen offset:0 sc1\n\t" \
  "v_mfma_f32_16x16x32_bf16 v[36:39], a[80:83], v[84:87], v[36:39]\n\t" \
  "v_mfma_f32_16x16x32_bf16 v[32:35], a[12:15], v[92:95], v[32:35]\n\t" \
  "v_mfma_f32_16x16x32_bf16 v[36:39], a[84:87], v[92:95], v[36:39]\n\t" \
  "v_mfma_f32_16x16x32_bf16 v[32:35], a[16:19], v[100:103], v[32:35]\n\t" \
  "v_mfma_f32_16x16x32_bf16 v[36:39], a[88:91], v[100:103], v[36:39]\n\t" \
  "v_mfma_f32_16x16x32_bf16 v[32:35], a[20:23], v[108:111], v[32:35]\n\t" \
  "v_mfma_f32_16x16x32_bf16 v[36:39], a[92:95], v[108:111], v[36:39]\n\t" \
  "v_mfma_f32_16x16x32_bf16 v[32:35], a[24:27], v[116:119], v[32:35]\n\t" \
  "v_mfma_f32_16x16x32_bf16 v[36:39], a[96:99], v[116:119], v[36:39]\n\t" \
  "v_mfma_f32_16x16x32_bf16 v[32:35], a[28:31], v[124:127], v[32:35]\n\t" \
  "v_mfma_f32_16x16x32_bf16 v[36:39], a[100:103], v[124:127], v[36:39]\n\t" \
  "s_waitcnt vmcnt(1)\n\t" \
  "v_cmp_eq_u32 vcc, 0, v4\n\t" \
  "v_mfma_f32_16x16x32_bf16 v[32:35], a[32:35], v[132:135], v[32:35]\n\t" \
  "v_mfma_f32_16x16x32_bf16 v[36:39], a[104:107], v[132:135], v[36:39]\n\t" \
  "s_cmp_eq_u64 vcc, 0\n\t" \
  "s_cselect_b32 s60, 1, 0\n\t" \
  "v_mov_b32 v4, s60\n\t" \
  "v_mfma_f32_16x16x32_bf16 v[32:35], a[36:39], v[140:143], v[32:35]\n\t" \
  "ds_write_b32 v5, v4\n\t" \
  "v_mfma_f32_16x16x32_bf16 v[36:39], a[108:111], v[140:143], v[36:39]\n\t" \
  "v_mfma_f32_16x16x32_bf16 v[32:35], a[40:43], v[148:151], v[32:35]\n\t" \
  "v_mfma_f32_16x16x32_bf16 v[36:39], a[112:115], v[148:151], v[36:39]\n\t" \
  "buffer_store_dwordx4 v[52:55], v0, %[X], s50 offen offset:1024 sc1\n\t" \
  "v_mfma_f32_16x16x32_bf16 v[32:35], a[44:47], v[156:159], v[32:35]\n\t" \
  "v_mfma_f32_16x16x32_bf16 v[36:39], a[116:119], v[156:159], v[36:39]\n\t" \
  "s_waitcnt lgkmcnt(0)\n\t" \
  "s_barrier\n\t" \
  "v_mfma_f32_16x16x32_bf16 v[32:35], a[48:51], v[164:167], v[32:35]\n\t" \
  "ds_read_b32 v4, v6 offset:40959\n\t" \
  "v_mfma_f32_16x16x32_bf16 v[36:39], a[120:123], v[164:167], v[36:39]\n\t" \
  "v_mfma_f32_16x16x32_bf16 v[32:35], a[52:55], v[172:175], v[32:35]\n\t" \
  "v_mfma_f32_16x16x32_bf16 v[36:39], a[124:127], v[172:175], v[36:39]\n\t" \
  "v_mfma_f32_16x16x32_bf16 v[32:35], a[56:59], v[180:183], v[32:35]\n\t" \
  "v_mfma_f32_16x16x32_bf16 v[36:39], a[128:131], v[180:183], v[36:39]\n\t" \
  "s_waitcnt lgkmcnt(0)\n\t" \
  "v_readfirstlane_b32 s60, v4\n\t" \
  "s_add_u32 s55, s83, s36\n\t" \
  "v_mfma_f32_16x16x32_bf16 v[32:35], a[60:63], v[188:191], v[32:35]\n\t" \
  "s_cmp_eq_u32 s48, 63\n\t" \
  "s_cselect_b32 s56, 0, s60\n\t" \
  "v_mfma_f32_16x16x32_bf16 v[36:39], a[132:135], v[188:191], v[36:39]\n\t" \
  "s_cmp_eq_u32 s56, 0\n\t" \
  "s_cselect_b32 s55, 0xf0000000, s55\n\t" \
  "v_mfma_f32_16x16x32_bf16 v[32:35], a[64:67], v[196:199], v[32:35]\n\t" \
  "v_mfma_f32_16x16x32_bf16 v[36:39], a[136:139], v[196:199], v[36:39]\n\t" \
  "v_mfma_f32_16x16x32_bf16 v[32:35], a[68:71], v[204:207], v[32:35]\n\t" \
  "s_add_u32 m0, s46, 0\n\t" \
  "s_add_u32 s47, s55, 0\n\t" \
  "buffer_load_dwordx4 v0, %[X], s47 offen sc1 lds\n\t" \
  "v_mfma_f32_16x16x32_bf16 v[36:39], a[140:143], v[204:207], v[36:39]\n\t" \
  "v_mfma_f32_16x16x32_bf16 v[40:43], a[144:147], v[68:71], 0\n\t" \
  "v_mfma_f32_16x16x32_bf16 v[44:47], a[216:219], v[68:71], 0\n\t" \
  "v_mfma_f32_16x16x32_bf16 v[40:43], a[148:151], v[76:79], v[40:43]\n\t" \
  "v_cvt_pk_bf16_f32 v56, v32, v33\n\t" \
  "v_cvt_pk_bf16_f32 v57, v34, v35\n\t" \
  "s_add_u32 m0, s46, 4096\n\t" \
  "s_add_u32 s47, s55, 4096\n\t" \
  "buffer_load_dwordx4 v0, %[X], s47 offen sc1 lds\n\t" \
  "v_mfma_f32_16x16x32_bf16 v[44:47], a[220:223], v[76:79], v[44:47]\n\t" \
  "v_cvt_pk_bf16_f32 v58, v36, v37\n\t" \
  "v_cvt_pk_bf16_f32 v59, v38, v39\n\t" \
  "v_mfma_f32_16x16x32_bf16 v[40:43], a[152:155], v[84:87], v[40:43]\n\t" \
  "v_mfma_f32_16x16x32_bf16 v[44:47], a[224:227], v[84:87], v[44:47]\n\t" \
  "buffer_store_dwordx4 v[56:59], v0, %[X], s51 offen offset:0 sc1\n\t" \
  "v_mfma_f32_16x16x32_bf16 v[40:43], a[156:159], v[92:95], v[40:43]\n\t" \
  "s_add_u32 m0, s46, 8192\n\t" \
  "s_add_u32 s47, s55, 16384\n\t" \
  "buffer_load_dwordx4 v0, %[X], s47 offen sc1 lds\n\t" \
  "v_mfma_f32_16x16x32_bf16 v[44:47], a[228:231], v[92:95], v[44:47]\n\t" \
  "v_mfma_f32_16x16x32_bf16 v[40:43], a[160:163], v[100:103], v[40:43]\n\t" \
  "v_mfma_f32_16x16x32_bf16 v[44:47], a[232:235], v[100:103], v[44:47]\n\t" \
  "v_mfma_f32_16x16x32_bf16 v[40:43], a[164:167], v[108:111], v[40:43]\n\t" \
  "s_add_u32 m0, s46, 12288\n\t" \
  "s_add_u32 s47, s55, 20480\n\t" \
  "buffer_load_dwordx4 v0, %[X], s47 offen sc1 lds\n\t" \
  "v_mfma_f32_16x16x32_bf16 v[44:47], a[236:239], v[108:111], v[44:47]\n\t" \
  "v_mfma_f32_16x16x32_bf16 v[40:43], a[168:171], v[116:119], v[40:43]\n\t" \
  "v_mfma_f32_16x16x32_bf16 v[44:47], a[240:243], v[116:119], v[44:47]\n\t" \
  "v_mfma_f32_16x16x32_bf16 v[40:43], a[172:175], v[124:127], v[40:43]\n\t" \
  "s_add_u32 m0, s46, 16384\n\t" \
  "s_add_u32 s47, s55, 32768\n\t" \
  "buffer_load_dwordx4 v0, %[X], s47 offen sc1 lds\n\t" \
  "v_mfma_f32_16x16x32_bf16 v[44:47], a[244:247], v[124:127], v[44:47]\n\t" \
  "v_mfma_f32_16x16x32_bf16 v[40:43], a[176:179], v[132:135], v[40:43]\n\t" \
  "v_mfma_f32_16x16x32_bf16 v[44:47], a[248:251], v[132:135], v[44:47]\n\t" \
  "v_mfma_f32_16x16x32_bf16 v[40:43], a[180:183], v[140:143], v[40:43]\n\t" \
  "s_add_u32 m0, s46, 20480\n\t" \
  "s_add_u32 s47, s55, 36864\n\t" \
  "buffer_load_dwordx4 v0, %[X], s47 offen sc1 lds\n\t" \
  "v_mfma_f32_16x16x32_bf16 v[44:47], a[252:255], v[140:143], v[44:47]\n\t" \
  "v_mfma_f32_16x16x32_bf16 v[40:43], a[184:187], v[148:151], v[40:43]\n\t" \
  "v_mfma_f32_16x16x32_bf16 v[44:47], v[224:227], v[148:151], v[44:47]\n\t" \
  "v_mfma_f32_16x16x32_bf16 v[40:43], a[188:191], v[156:159], v[40:43]\n\t" \
  "s_add_u32 m0, s46, 24576\n\t" \
  "s_add_u32 s47, s55, 49152\n\t" \
  "buffer_load_dwordx4 v0, %[X], s47 offen sc1 lds\n\t" \
  "v_mfma_f32_16x16x32_bf16 v[44:47], v[228:231], v[156:159], v[44:47]\n\t" \
  "v_mfma_f32_16x16x32_bf16 v[40:43], a[192:195], v[164:167], v[40:43]\n\t" \
  "v_mfma_f32_16x16x32_bf16 v[44:47], v[232:235], v[164:167], v[44:47]\n\t" \
  "v_mfma_f32_16x16x32_bf16 v[40:43], a[196:199], v[172:175], v[40:43]\n\t" \
  "s_add_u32 m0, s46, 28672\n\t" \
  "s_add_u32 s47, s55, 53248\n\t" \
  "buffer_load_dwordx4 v0, %[X], s47 offen sc1 lds\n\t" \
  "v_mfma_f32_16x16x32_bf16 v[44:47], v[236:239], v[172:175], v[44:47]\n\t" \
  "v_mfma_f32_16x16x32_bf16 v[40:43], a[200:203], v[180:183], v[40:43]\n\t" \
  "v_mfma_f32_16x16x32_bf16 v[44:47], v[240:243], v[180:183], v[44:47]\n\t" \
  "v_mfma_f32_16x16x32_bf16 v[40:43], a[204:207], v[188:191], v[40:43]\n\t" \
  "s_add_u32 m0, s46, 32768\n\t" \
  "s_add_u32 s47, s55, 65536\n\t" \
  "buffer_load_dwordx4 v0, %[X], s47 offen sc1 lds\n\t" \
  "v_mfma_f32_16x16x32_bf16 v[44:47], v[244:247], v[188:191], v[44:47]\n\t" \
  "v_mfma_f32_16x16x32_bf16 v[40:43], a[208:211], v[196:199], v[40:43]\n\t" \
  "v_mfma_f32_16x16x32_bf16 v[44:47], v[248:251], v[196:199], v[44:47]\n\t" \
  "buffer_load_dword v8, off, %[X], s57 sc1\n\t" \
  "v_mfma_f32_16x16x32_bf16 v[40:43], a[212:215], v[204:207], v[40:43]\n\t" \
  "v_mfma_f32_16x16x32_bf16 v[44:47], v[252:255], v[204:207], v[44:47]\n\t" \
  "s_nop 4\n\t" \
  "v_cvt_pk_bf16_f32 v60, v40, v41\n\t" \
  "v_cvt_pk_bf16_f32 v61, v42, v43\n\t" \
  "s_nop 1\n\t" \
  "v_cvt_pk_bf16_f32 v62, v44, v45\n\t" \
  "v_cvt_pk_bf16_f32 v63, v46, v47\n\t" \
  "buffer_store_dwordx4 v[60:63], v0, %[X], s51 offen offset:1024 sc1\n\t" \
  "s_add_u32 s49, s82, s38\n\t" \
  "s_cmp_eq_u32 s48, 63\n\t" \
  "s_cbranch_scc1 LEND_%=\n\t" \
  "s_add_u32 s48, s48, 1\n\t" \
  "s_cmp_eq_u32 s56, 0\n\t" \
  "s_cbranch_scc1 LCOLD_%=\n\t" \
  "s_waitcnt vmcnt(0)\n\t" \
  "s_mov_b64 exec, 1\n\t" \
  "buffer_store_dword v6, off, %[X], s49 sc1\n\t" \
  "s_mov_b64 exec, -1\n\t" \
  "s_mov_b32 s49, 0xf0000000\n\t" \
  "s_cmp_lt_i32 s57, 0\n\t" \
  "s_cbranch_scc1 LTOP_%=\n\t" \
  "s_movk_i32 s63, 4000\n\t" \
  "s_branch LGW_%=u\n\t" \
  "LGW_%=:\n\t" \
  "s_sleep " TK_PSG "\n\t" \
  "buffer_load_dword v8, off, %[X], s57 sc1\n\t" \
  "s_waitcnt vmcnt(0)\n\t" \
  "LGW_%=u:\n\t" \
  "s_memrealtime s[52:53]\n\t" \
  "v_readfirstlane_b32 s60, v8\n\t" \
  "s_waitcnt lgkmcnt(0)\n\t" \
  "s_sub_u32 s63, s63, 1\n\t" \
  "s_cbranch_scc1 LTOP_%=\n\t" \
  "s_cmp_eq_u32 s60, -1\n\t" \
  "s_cbranch_scc1 LGW_%=\n\t" \
  "s_not_b32 s60, s60\n\t" \
  "s_lshl_b32 s61, s52, 8\n\t" \
  "s_sub_u32 s60, s60, s61\n\t" \
  "s_ashr_i32 s60, s60, 8\n\t" \
  "s_sub_i32 s60, s60, " TK_QXW "\n\t" \
  "s_min_i32 s60, s60, 400\n\t" \
  "s_add_u32 s54, s52, s60\n\t" \
  "LGW_%=w:\n\t" \
  "s_sleep 1\n\t" \
  "s_memrealtime s[52:53]\n\t" \
  "s_waitcnt lgkmcnt(0)\n\t" \
  "s_sub_i32 s53, s52, s54\n\t" \
  "s_cmp_lt_i32 s53, 0\n\t" \
  "s_cbranch_scc1 LGW_%=w\n\t" \
  "s_branch LTOP_%=\n\t" \
  "LEND_%=:\n\t" \
  "s_waitcnt vmcnt(0)\n\t" \
  "s_mov_b64 exec, 1\n\t" \
  "buffer_store_dword v6, off, %[X], s49 sc1\n\t" \
  "s_mov_b64 exec, -1\n\t" \
  "s_mov_b32 s49, 0xf0000000\n\t" \
  ""
#define QV_ASM \
  "v_lshrrev_b32 v16, 7, v0\n\t" \
  "v_lshlrev_b32 v16, 13, v16\n\t" \
  "v_lshrrev_b32 v19, 2, v0\n\t" \
  "v_and_b32 v19, 64, v19\n\t" \
  "v_xor_b32 v19, v19, v0\n\t" \
  "v_and_b32 v19, 0x70, v19\n\t" \
  "v_add_u32 v16, v16, v19\n\t" \
  "s_mul_i32 s63, s40, 40960\n\t" \
  "v_and_b32 v19, 0x300, v0\n\t" \
  "v_lshlrev_b32 v17, 2, v19\n\t" \
  "v_and_b32 v19, 0xc0, v0\n\t" \
  "v_lshl_add_u32 v17, v19, 1, v17\n\t" \
  "v_and_b32 v19, 0x30, v0\n\t" \
  "v_lshrrev_b32 v19, 1, v19\n\t" \
  "v_add3_u32 v17, v17, v19, s63\n\t" \
  "v_lshrrev_b32 v19, 1, v0\n\t" \
  "v_and_b32 v19, 64, v19\n\t" \
  "v_add_u32 v17, v17, v19\n\t" \
  "v_xor_b32 v18, 64, v17\n\t" \
  "s_add_u32 m0, s63, 0\n\t" \
  "s_mov_b32 s60, 0\n\t" \
  "buffer_load_dwordx4 v16, %[W], s60 offen lds\n\t" \
  "s_add_u32 m0, s63, 1024\n\t" \
  "s_mov_b32 s60, 65536\n\t" \
  "buffer_load_dwordx4 v16, %[W], s60 offen lds\n\t" \
  "s_add_u32 m0, s63, 2048\n\t" \
  "s_mov_b32 s60, 131072\n\t" \
  "buffer_load_dwordx4 v16, %[W], s60 offen lds\n\t" \
  "s_add_u32 m0, s63, 3072\n\t" \
  "s_mov_b32 s60, 196608\n\t" \
  "buffer_load_dwordx4 v16, %[W], s60 offen lds\n\t" \
  "s_add_u32 m0, s63, 4096\n\t" \
  "s_mov_b32 s60, 262144\n\t" \
  "buffer_load_dwordx4 v16, %[W], s60 offen lds\n\t" \
  "s_add_u32 m0, s63, 5120\n\t" \
  "s_mov_b32 s60, 327680\n\t" \
  "buffer_load_dwordx4 v16, %[W], s60 offen lds\n\t" \
  "s_add_u32 m0, s63, 6144\n\t" \
  "s_mov_b32 s60, 393216\n\t" \
  "buffer_load_dwordx4 v16, %[W], s60 offen lds\n\t" \
  "s_add_u32 m0, s63, 7168\n\t" \
  "s_mov_b32 s60, 458752\n\t" \
  "buffer_load_dwordx4 v16, %[W], s60 offen lds\n\t" \
  "s_add_u32 m0, s63, 8192\n\t" \
  "s_mov_b32 s60, 524288\n\t" \
  "buffer_load_dwordx4 v16, %[W], s60 offen lds\n\t" \
  "s_add_u32 m0, s63, 9216\n\t" \
  "s_mov_b32 s60, 589824\n\t" \
  "buffer_load_dwordx4 v16, %[W], s60 offen lds\n\t" \
  "s_add_u32 m0, s63, 10240\n\t" \
  "s_mov_b32 s60, 655360\n\t" \
  "buffer_load_dwordx4 v16, %[W], s60 offen lds\n\t" \
  "s_add_u32 m0, s63, 11264\n\t" \
  "s_mov_b32 s60, 720896\n\t" \
  "buffer_load_dwordx4 v16, %[W], s60 offen lds\n\t" \
  "s_add_u32 m0, s63, 12288\n\t" \
  "s_mov_b32 s60, 786432\n\t" \
  "buffer_load_dwordx4 v16, %[W], s60 offen lds\n\t" \
  "s_add_u32 m0, s63, 13312\n\t" \
  "s_mov_b32 s60, 851968\n\t" \
  "buffer_load_dwordx4 v16, %[W], s60 offen lds\n\t" \
  "s_add_u32 m0, s63, 14336\n\t" \
  "s_mov_b32 s60, 917504\n\t" \
  "buffer_load_dwordx4 v16, %[W], s60 offen lds\n\t" \
  "s_add_u32 m0, s63, 15360\n\t" \
  "s_mov_b32 s60, 983040\n\t" \
  "buffer_load_dwordx4 v16, %[W], s60 offen lds\n\t" \
  "s_add_u32 m0, s63, 16384\n\t" \
  "s_mov_b32 s60, 1048576\n\t" \
  "buffer_load_dwordx4 v16, %[W], s60 offen lds\n\t" \
  "s_add_u32 m0, s63, 17408\n\t" \
  "s_mov_b32 s60, 1114112\n\t" \
  "buffer_load_dwordx4 v16, %[W], s60 offen lds\n\t" \
  "s_add_u32 m0, s63, 18432\n\t" \
  "s_mov_b32 s60, 1179648\n\t" \
  "buffer_load_dwordx4 v16, %[W], s60 offen lds\n\t" \
  "s_add_u32 m0, s63, 19456\n\t" \
  "s_mov_b32 s60, 1245184\n\t" \
  "buffer_load_dwordx4 v16, %[W], s60 offen lds\n\t" \
  "s_add_u32 m0, s63, 20480\n\t" \
  "s_mov_b32 s60, 1310720\n\t" \
  "buffer_load_dwordx4 v16, %[W], s60 offen lds\n\t" \
  "s_add_u32 m0, s63, 21504\n\t" \
  "s_mov_b32 s60, 1376256\n\t" \
  "buffer_load_dwordx4 v16, %[W], s60 offen lds\n\t" \
  "s_add_u32 m0, s63, 22528\n\t" \
  "s_mov_b32 s60, 1441792\n\t" \
  "buffer_load_dwordx4 v16, %[W], s60 offen lds\n\t" \
  "s_add_u32 m0, s63, 23552\n\t" \
  "s_mov_b32 s60, 1507328\n\t" \
  "buffer_load_dwordx4 v16, %[W], s60 offen lds\n\t" \
  "s_add_u32 m0, s63, 24576\n\t" \
  "s_mov_b32 s60, 1572864\n\t" \
  "buffer_load_dwordx4 v16, %[W], s60 offen lds\n\t" \
  "s_add_u32 m0, s63, 25600\n\t" \
  "s_mov_b32 s60, 1638400\n\t" \
  "buffer_load_dwordx4 v16, %[W], s60 offen lds\n\t" \
  "s_add_u32 m0, s63, 26624\n\t" \
  "s_mov_b32 s60, 1703936\n\t" \
  "buffer_load_dwordx4 v16, %[W], s60 offen lds\n\t" \
  "s_add_u32 m0, s63, 27648\n\t" \
  "s_mov_b32 s60, 1769472\n\t" \
  "buffer_load_dwordx4 v16, %[W], s60 offen lds\n\t" \
  "s_add_u32 m0, s63, 28672\n\t" \
  "s_mov_b32 s60, 1835008\n\t" \
  "buffer_load_dwordx4 v16, %[W], s60 offen lds\n\t" \
  "s_add_u32 m0, s63, 29696\n\t" \
  "s_mov_b32 s60, 1900544\n\t" \
  "buffer_load_dwordx4 v16, %[W], s60 offen lds\n\t" \
  "s_add_u32 m0, s63, 30720\n\t" \
  "s_mov_b32 s60, 1966080\n\t" \
  "buffer_load_dwordx4 v16, %[W], s60 offen lds\n\t" \
  "s_add_u32 m0, s63, 31744\n\t" \
  "s_mov_b32 s60, 2031616\n\t" \
  "buffer_load_dwordx4 v16, %[W], s60 offen lds\n\t" \
  "s_add_u32 m0, s63, 32768\n\t" \
  "s_mov_b32 s60, 2097152\n\t" \
  "buffer_load_dwordx4 v16, %[W], s60 offen lds\n\t" \
  "s_add_u32 m0, s63, 33792\n\t" \
  "s_mov_b32 s60, 2162688\n\t" \
  "buffer_load_dwordx4 v16, %[W], s60 offen lds\n\t" \
  "s_add_u32 m0, s63, 34816\n\t" \
  "s_mov_b32 s60, 2228224\n\t" \
  "buffer_load_dwordx4 v16, %[W], s60 offen lds\n\t" \
  "s_add_u32 m0, s63, 35840\n\t" \
  "s_mov_b32 s60, 2293760\n\t" \
  "buffer_load_dwordx4 v16, %[W], s60 offen lds\n\t" \
  "s_add_u32 m0, s63, 36864\n\t" \
  "s_mov_b32 s60, 2359296\n\t" \
  "buffer_load_dwordx4 v16, %[W], s60 offen lds\n\t" \
  "s_add_u32 m0, s63, 37888\n\t" \
  "s_mov_b32 s60, 2424832\n\t" \
  "buffer_load_dwordx4 v16, %[W], s60 offen lds\n\t" \
  "s_add_u32 m0, s63, 38912\n\t" \
  "s_mov_b32 s60, 2490368\n\t" \
  "buffer_load_dwordx4 v16, %[W], s60 offen lds\n\t" \
  "s_add_u32 m0, s63, 39936\n\t" \
  "s_mov_b32 s60, 2555904\n\t" \
  "buffer_load_dwordx4 v16, %[W], s60 offen lds\n\t" \
  "s_waitcnt vmcnt(32)\n\t" \
  "ds_read_b64_tr_b16 a[0:1], v17 offset:0\n\t" \
  "ds_read_b64_tr_b16 a[2:3], v17 offset:512\n\t" \
  "ds_read_b64_tr_b16 a[4:5], v17 offset:4096\n\t" \
  "ds_read_b64_tr_b16 a[6:7], v17 offset:4608\n\t" \
  "ds_read_b64_tr_b16 a[72:73], v17 offset:32\n\t" \
  "ds_read_b64_tr_b16 a[74:75], v17 offset:544\n\t" \
  "ds_read_b64_tr_b16 a[76:77], v17 offset:4128\n\t" \
  "ds_read_b64_tr_b16 a[78:79], v17 offset:4640\n\t" \
  "ds_read_b64_tr_b16 a[144:145], v18 offset:0\n\t" \
  "ds_read_b64_tr_b16 a[146:147], v18 offset:512\n\t" \
  "ds_read_b64_tr_b16 a[148:149], v18 offset:4096\n\t" \
  "ds_read_b64_tr_b16 a[150:151], v18 offset:4608\n\t" \
  "ds_read_b64_tr_b16 a[216:217], v18 offset:32\n\t" \
  "ds_read_b64_tr_b16 a[218:219], v18 offset:544\n\t" \
  "ds_read_b64_tr_b16 a[220:221], v18 offset:4128\n\t" \
  "ds_read_b64_tr_b16 a[222:223], v18 offset:4640\n\t" \
  "s_waitcnt lgkmcnt(0)\n\t" \
  "s_add_u32 m0, s63, 0\n\t" \
  "s_mov_b32 s60, 2621440\n\t" \
  "buffer_load_dwordx4 v16, %[W], s60 offen lds\n\t" \
  "s_add_u32 m0, s63, 1024\n\t" \
  "s_mov_b32 s60, 2686976\n\t" \
  "buffer_load_dwordx4 v16, %[W], s60 offen lds\n\t" \
  "s_add_u32 m0, s63, 2048\n\t" \
  "s_mov_b32 s60, 2752512\n\t" \
  "buffer_load_dwordx4 v16, %[W], s60 offen lds\n\t" \
  "s_add_u32 m0, s63, 3072\n\t" \
  "s_mov_b32 s60, 2818048\n\t" \
  "buffer_load_dwordx4 v16, %[W], s60 offen lds\n\t" \
  "s_add_u32 m0, s63, 4096\n\t" \
  "s_mov_b32 s60, 2883584\n\t" \
  "buffer_load_dwordx4 v16, %[W], s60 offen lds\n\t" \
  "s_add_u32 m0, s63, 5120\n\t" \
  "s_mov_b32 s60, 2949120\n\t" \
  "buffer_load_dwordx4 v16, %[W], s60 offen lds\n\t" \
  "s_add_u32 m0, s63, 6144\n\t" \
  "s_mov_b32 s60, 3014656\n\t" \
  "buffer_load_dwordx4 v16, %[W], s60 offen lds\n\t" \
  "s_add_u32 m0, s63, 7168\n\t" \
  "s_mov_b32 s60, 3080192\n\t" \
  "buffer_load_dwordx4 v16, %[W], s60 offen lds\n\t" \
  "s_waitcnt vmcnt(32)\n\t" \
  "ds_read_b64_tr_b16 a[8:9], v17 offset:8192\n\t" \
  "ds_read_b64_tr_b16 a[10:11], v17 offset:8704\n\t" \
  "ds_read_b64_tr_b16 a[12:13], v17 offset:12288\n\t" \
  "ds_read_b64_tr_b16 a[14:15], v17 offset:12800\n\t" \
  "ds_read_b64_tr_b16 a[80:81], v17 offset:8224\n\t" \
  "ds_read_b64_tr_b16 a[82:83], v17 offset:8736\n\t" \
  "ds_read_b64_tr_b16 a[84:85], v17 offset:12320\n\t" \
  "ds_read_b64_tr_b16 a[86:87], v17 offset:12832\n\t" \
  "ds_read_b64_tr_b16 a[152:153], v18 offset:8192\n\t" \
  "ds_read_b64_tr_b16 a[154:155], v18 offset:8704\n\t" \
  "ds_read_b64_tr_b16 a[156:157], v18 offset:12288\n\t" \
  "ds_read_b64_tr_b16 a[158:159], v18 offset:12800\n\t" \
  "ds_read_b64_tr_b16 a[224:225], v18 offset:8224\n\t" \
  "ds_read_b64_tr_b16 a[226:227], v18 offset:8736\n\t" \
  "ds_read_b64_tr_b16 a[228:229], v18 offset:12320\n\t" \
  "ds_read_b64_tr_b16 a[230:231], v18 offset:12832\n\t" \
  "s_waitcnt lgkmcnt(0)\n\t" \
  "s_add_u32 m0, s63, 8192\n\t" \
  "s_mov_b32 s60, 3145728\n\t" \
  "buffer_load_dwordx4 v16, %[W], s60 offen lds\n\t" \
  "s_add_u32 m0, s63, 9216\n\t" \
  "s_mov_b32 s60, 3211264\n\t" \
  "buffer_load_dwordx4 v16, %[W], s60 offen lds\n\t" \
  "s_add_u32 m0, s63, 10240\n\t" \
  "s_mov_b32 s60, 3276800\n\t" \
  "buffer_load_dwordx4 v16, %[W], s60 offen lds\n\t" \
  "s_add_u32 m0, s63, 11264\n\t" \
  "s_mov_b32 s60, 3342336\n\t" \
  "buffer_load_dwordx4 v16, %[W], s60 offen lds\n\t" \
  "s_add_u32 m0, s63, 12288\n\t" \
  "s_mov_b32 s60, 3407872\n\t" \
  "buffer_load_dwordx4 v16, %[W], s60 offen lds\n\t" \
  "s_add_u32 m0, s63, 13312\n\t" \
  "s_mov_b32 s60, 3473408\n\t" \
  "buffer_load_dwordx4 v16, %[W], s60 offen lds\n\t" \
  "s_add_u32 m0, s63, 14336\n\t" \
  "s_mov_b32 s60, 3538944\n\t" \
  "buffer_load_dwordx4 v16, %[W], s60 offen lds\n\t" \
  "s_add_u32 m0, s63, 15360\n\t" \
  "s_mov_b32 s60, 3604480\n\t" \
  "buffer_load_dwordx4 v16, %[W], s60 offen lds\n\t" \
  "s_waitcnt vmcnt(32)\n\t" \
  "ds_read_b64_tr_b16 a[16:17], v17 offset:16384\n\t" \
  "ds_read_b64_tr_b16 a[18:19], v17 offset:16896\n\t" \
  "ds_read_b64_tr_b16 a[20:21], v17 offset:20480\n\t" \
  "ds_read_b64_tr_b16 a[22:23], v17 offset:20992\n\t" \
  "ds_read_b64_tr_b16 a[88:89], v17 offset:16416\n\t" \
  "ds_read_b64_tr_b16 a[90:91], v17 offset:16928\n\t" \
  "ds_read_b64_tr_b16 a[92:93], v17 offset:20512\n\t" \
  "ds_read_b64_tr_b16 a[94:95], v17 offset:21024\n\t" \
  "ds_read_b64_tr_b16 a[160:161], v18 offset:16384\n\t" \
  "ds_read_b64_tr_b16 a[162:163], v18 offset:16896\n\t" \
  "ds_read_b64_tr_b16 a[164:165], v18 offset:20480\n\t" \
  "ds_read_b64_tr_b16 a[166:167], v18 offset:20992\n\t" \
  "ds_read_b64_tr_b16 a[232:233], v18 offset:16416\n\t" \
  "ds_read_b64_tr_b16 a[234:235], v18 offset:16928\n\t" \
  "ds_read_b64_tr_b16 a[236:237], v18 offset:20512\n\t" \
  "ds_read_b64_tr_b16 a[238:239], v18 offset:21024\n\t" \
  "s_waitcnt lgkmcnt(0)\n\t" \
  "s_add_u32 m0, s63, 16384\n\t" \
  "s_mov_b32 s60, 3670016\n\t" \
  "buffer_load_dwordx4 v16, %[W], s60 offen lds\n\t" \
  "s_add_u32 m0, s63, 17408\n\t" \
  "s_mov_b32 s60, 3735552\n\t" \
  "buffer_load_dwordx4 v16, %[W], s60 offen lds\n\t" \
  "s_add_u32 m0, s63, 18432\n\t" \
  "s_mov_b32 s60, 3801088\n\t" \
  "buffer_load_dwordx4 v16, %[W], s60 offen lds\n\t" \
  "s_add_u32 m0, s63, 19456\n\t" \
  "s_mov_b32 s60, 3866624\n\t" \
  "buffer_load_dwordx4 v16, %[W], s60 offen lds\n\t" \
  "s_add_u32 m0, s63, 20480\n\t" \
  "s_mov_b32 s60, 3932160\n\t" \
  "buffer_load_dwordx4 v16, %[W], s60 offen lds\n\t" \
  "s_add_u32 m0, s63, 21504\n\t" \
  "s_mov_b32 s60, 3997696\n\t" \
  "buffer_load_dwordx4 v16, %[W], s60 offen lds\n\t" \
  "s_add_u32 m0, s63, 22528\n\t" \
  "s_mov_b32 s60, 4063232\n\t" \
  "buffer_load_dwordx4 v16, %[W], s60 offen lds\n\t" \
  "s_add_u32 m0, s63, 23552\n\t" \
  "s_mov_b32 s60, 4128768\n\t" \
  "buffer_load_dwordx4 v16, %[W], s60 offen lds\n\t" \
  "s_waitcnt vmcnt(32)\n\t" \
  "ds_read_b64_tr_b16 a[24:25], v17 offset:24576\n\t" \
  "ds_read_b64_tr_b16 a[26:27], v17 offset:25088\n\t" \
  "ds_read_b64_tr_b16 a[28:29], v17 offset:28672\n\t" \
  "ds_read_b64_tr_b16 a[30:31], v17 offset:29184\n\t" \
  "ds_read_b64_tr_b16 a[96:97], v17 offset:24608\n\t" \
  "ds_read_b64_tr_b16 a[98:99], v17 offset:25120\n\t" \
  "ds_read_b64_tr_b16 a[100:101], v17 offset:28704\n\t" \
  "ds_read_b64_tr_b16 a[102:103], v17 offset:29216\n\t" \
  "ds_read_b64_tr_b16 a[168:169], v18 offset:24576\n\t" \
  "ds_read_b64_tr_b16 a[170:171], v18 offset:25088\n\t" \
  "ds_read_b64_tr_b16 a[172:173], v18 offset:28672\n\t" \
  "ds_read_b64_tr_b16 a[174:175], v18 offset:29184\n\t" \
  "ds_read_b64_tr_b16 a[240:241], v18 offset:24608\n\t" \
  "ds_read_b64_tr_b16 a[242:243], v18 offset:25120\n\t" \
  "ds_read_b64_tr_b16 a[244:245], v18 offset:28704\n\t" \
  "ds_read_b64_tr_b16 a[246:247], v18 offset:29216\n\t" \
  "s_waitcnt lgkmcnt(0)\n\t" \
  "s_add_u32 m0, s63, 24576\n\t" \
  "s_mov_b32 s60, 4194304\n\t" \
  "buffer_load_dwordx4 v16, %[W], s60 offen lds\n\t" \
  "s_add_u32 m0, s63, 25600\n\t" \
  "s_mov_b32 s60, 4259840\n\t" \
  "buffer_load_dwordx4 v16, %[W], s60 offen lds\n\t" \
  "s_add_u32 m0, s63, 26624\n\t" \
  "s_mov_b32 s60, 4325376\n\t" \
  "buffer_load_dwordx4 v16, %[W], s60 offen lds\n\t" \
  "s_add_u32 m0, s63, 27648\n\t" \
  "s_mov_b32 s60, 4390912\n\t" \
  "buffer_load_dwordx4 v16, %[W], s60 offen lds\n\t" \
  "s_add_u32 m0, s63, 28672\n\t" \
  "s_mov_b32 s60, 4456448\n\t" \
  "buffer_load_dwordx4 v16, %[W], s60 offen lds\n\t" \
  "s_add_u32 m0, s63, 29696\n\t" \
  "s_mov_b32 s60, 4521984\n\t" \
  "buffer_load_dwordx4 v16, %[W], s60 offen lds\n\t" \
  "s_add_u32 m0, s63, 30720\n\t" \
  "s_mov_b32 s60, 4587520\n\t" \
  "buffer_load_dwordx4 v16, %[W], s60 offen lds\n\t" \
  "s_add_u32 m0, s63, 31744\n\t" \
  "s_mov_b32 s60, 4653056\n\t" \
  "buffer_load_dwordx4 v16, %[W], s60 offen lds\n\t" \
  "s_waitcnt vmcnt(32)\n\t" \
  "ds_read_b64_tr_b16 a[32:33], v17 offset:32768\n\t" \
  "ds_read_b64_tr_b16 a[34:35], v17 offset:33280\n\t" \
  "ds_read_b64_tr_b16 a[36:37], v17 offset:36864\n\t" \
  "ds_read_b64_tr_b16 a[38:39], v17 offset:37376\n\t" \
  "ds_read_b64_tr_b16 a[104:105], v17 offset:32800\n\t" \
  "ds_read_b64_tr_b16 a[106:107], v17 offset:33312\n\t" \
  "ds_read_b64_tr_b16 a[108:109], v17 offset:36896\n\t" \
  "ds_read_b64_tr_b16 a[110:111], v17 offset:37408\n\t" \
  "ds_read_b64_tr_b16 a[176:177], v18 offset:32768\n\t" \
  "ds_read_b64_tr_b16 a[178:179], v18 offset:33280\n\t" \
  "ds_read_b64_tr_b16 a[180:181], v18 offset:36864\n\t" \
  "ds_read_b64_tr_b16 a[182:183], v18 offset:37376\n\t" \
  "ds_read_b64_tr_b16 a[248:249], v18 offset:32800\n\t" \
  "ds_read_b64_tr_b16 a[250:251], v18 offset:33312\n\t" \
  "ds_read_b64_tr_b16 a[252:253], v18 offset:36896\n\t" \
  "ds_read_b64_tr_b16 a[254:255], v18 offset:37408\n\t" \
  "s_waitcnt lgkmcnt(0)\n\t" \
  "s_waitcnt vmcnt(24)\n\t" \
  "ds_read_b64_tr_b16 a[40:41], v17 offset:0\n\t" \
  "ds_read_b64_tr_b16 a[42:43], v17 offset:512\n\t" \
  "ds_read_b64_tr_b16 a[44:45], v17 offset:4096\n\t" \
  "ds_read_b64_tr_b16 a[46:47], v17 offset:4608\n\t" \
  "ds_read_b64_tr_b16 a[112:113], v17 offset:32\n\t" \
  "ds_read_b64_tr_b16 a[114:115], v17 offset:544\n\t" \
  "ds_read_b64_tr_b16 a[116:117], v17 offset:4128\n\t" \
  "ds_read_b64_tr_b16 a[118:119], v17 offset:4640\n\t" \
  "ds_read_b64_tr_b16 a[184:185], v18 offset:0\n\t" \
  "ds_read_b64_tr_b16 a[186:187], v18 offset:512\n\t" \
  "ds_read_b64_tr_b16 a[188:189], v18 offset:4096\n\t" \
  "ds_read_b64_tr_b16 a[190:191], v18 offset:4608\n\t" \
  "ds_read_b64_tr_b16 v[224:225], v18 offset:32\n\t" \
  "ds_read_b64_tr_b16 v[226:227], v18 offset:544\n\t" \
  "ds_read_b64_tr_b16 v[228:229], v18 offset:4128\n\t" \
  "ds_read_b64_tr_b16 v[230:231], v18 offset:4640\n\t" \
  "s_waitcnt lgkmcnt(0)\n\t" \
  "s_waitcnt vmcnt(16)\n\t" \
  "ds_read_b64_tr_b16 a[48:49], v17 offset:8192\n\t" \
  "ds_read_b64_tr_b16 a[50:51], v17 offset:8704\n\t" \
  "ds_read_b64_tr_b16 a[52:53], v17 offset:12288\n\t" \
  "ds_read_b64_tr_b16 a[54:55], v17 offset:12800\n\t" \
  "ds_read_b64_tr_b16 a[120:121], v17 offset:8224\n\t" \
  "ds_read_b64_tr_b16 a[122:123], v17 offset:8736\n\t" \
  "ds_read_b64_tr_b16 a[124:125], v17 offset:12320\n\t" \
  "ds_read_b64_tr_b16 a[126:127], v17 offset:12832\n\t" \
  "ds_read_b64_tr_b16 a[192:193], v18 offset:8192\n\t" \
  "ds_read_b64_tr_b16 a[194:195], v18 offset:8704\n\t" \
  "ds_read_b64_tr_b16 a[196:197], v18 offset:12288\n\t" \
  "ds_read_b64_tr_b16 a[198:199], v18 offset:12800\n\t" \
  "ds_read_b64_tr_b16 v[232:233], v18 offset:8224\n\t" \
  "ds_read_b64_tr_b16 v[234:235], v18 offset:8736\n\t" \
  "ds_read_b64_tr_b16 v[236:237], v18 offset:12320\n\t" \
  "ds_read_b64_tr_b16 v[238:239], v18 offset:12832\n\t" \
  "s_waitcnt lgkmcnt(0)\n\t" \
  "s_waitcnt vmcnt(8)\n\t" \
  "ds_read_b64_tr_b16 a[56:57], v17 offset:16384\n\t" \
  "ds_read_b64_tr_b16 a[58:59], v17 offset:16896\n\t" \
  "ds_read_b64_tr_b16 a[60:61], v17 offset:20480\n\t" \
  "ds_read_b64_tr_b16 a[62:63], v17 offset:20992\n\t" \
  "ds_read_b64_tr_b16 a[128:129], v17 offset:16416\n\t" \
  "ds_read_b64_tr_b16 a[130:131], v17 offset:16928\n\t" \
  "ds_read_b64_tr_b16 a[132:133], v17 offset:20512\n\t" \
  "ds_read_b64_tr_b16 a[134:135], v17 offset:21024\n\t" \
  "ds_read_b64_tr_b16 a[200:201], v18 offset:16384\n\t" \
  "ds_read_b64_tr_b16 a[202:203], v18 offset:16896\n\t" \
  "ds_read_b64_tr_b16 a[204:205], v18 offset:20480\n\t" \
  "ds_read_b64_tr_b16 a[206:207], v18 offset:20992\n\t" \
  "ds_read_b64_tr_b16 v[240:241], v18 offset:16416\n\t" \
  "ds_read_b64_tr_b16 v[242:243], v18 offset:16928\n\t" \
  "ds_read_b64_tr_b16 v[244:245], v18 offset:20512\n\t" \
  "ds_read_b64_tr_b16 v[246:247], v18 offset:21024\n\t" \
  "s_waitcnt lgkmcnt(0)\n\t" \
  "s_waitcnt vmcnt(0)\n\t" \
  "ds_read_b64_tr_b16 a[64:65], v17 offset:24576\n\t" \
  "ds_read_b64_tr_b16 a[66:67], v17 offset:25088\n\t" \
  "ds_read_b64_tr_b16 a[68:69], v17 offset:28672\n\t" \
  "ds_read_b64_tr_b16 a[70:71], v17 offset:29184\n\t" \
  "ds_read_b64_tr_b16 a[136:137], v17 offset:24608\n\t" \
  "ds_read_b64_tr_b16 a[138:139], v17 offset:25120\n\t" \
  "ds_read_b64_tr_b16 a[140:141], v17 offset:28704\n\t" \
  "ds_read_b64_tr_b16 a[142:143], v17 offset:29216\n\t" \
  "ds_read_b64_tr_b16 a[208:209], v18 offset:24576\n\t" \
  "ds_read_b64_tr_b16 a[210:211], v18 offset:25088\n\t" \
  "ds_read_b64_tr_b16 a[212:213], v18 offset:28672\n\t" \
  "ds_read_b64_tr_b16 a[214:215], v18 offset:29184\n\t" \
  "ds_read_b64_tr_b16 v[248:249], v18 offset:24608\n\t" \
  "ds_read_b64_tr_b16 v[250:251], v18 offset:25120\n\t" \
  "ds_read_b64_tr_b16 v[252:253], v18 offset:28704\n\t" \
  "ds_read_b64_tr_b16 v[254:255], v18 offset:29216\n\t" \
  "s_waitcnt lgkmcnt(0)\n\t" \
  "s_mov_b32 s84, " TK_CAP "\n\t" \
  "s_mov_b32 s48, 0\n\t" \
  "s_mov_b32 s49, 0xf0000000\n\t" \
  "s_mov_b32 s46, s41\n\t" \
  "s_mov_b32 s56, 0\n\t" \
  "v_lshrrev_b32 v7, 2, v0\n\t" \
  "s_mov_b32 s60, " TK_CTAB "\n\t" \
  "buffer_load_dword v4, v7, %[X], s60 offen offset:88 sc1\n\t" \
  "buffer_load_dword v7, v7, %[X], s60 offen offset:0 sc1\n\t" \
  "s_waitcnt vmcnt(0)\n\t" \
  "v_and_b32 v4, 1, v4\n\t" \
  "v_and_b32 v7, 1, v7\n\t" \
  "v_cmp_ne_u32 s[90:91], s67, v4\n\t" \
  "v_cmp_ne_u32 s[88:89], s67, v7\n\t" \
  "s_nop 1\n\t" \
  "s_mov_b32 s57, 0xf0000000\n\t" \
  "s_add_u32 s83, s48, 0\n\t" \
  "s_lshr_b64 s[60:61], s[90:91], s83\n\t" \
  "s_and_b32 s60, s60, 1\n\t" \
  "s_lshl_b32 s60, s60, 13\n\t" \
  "s_add_u32 s83, s83, " TK_SG0 "\n\t" \
  "s_lshl_b32 s83, s83, 21\n\t" \
  "s_or_b32 s83, s83, s60\n\t" \
  "s_add_u32 s80, s48, " TK_TRN "\n\t" \
  "s_lshr_b64 s[60:61], s[90:91], s80\n\t" \
  "s_and_b32 s60, s60, 1\n\t" \
  "s_lshl_b32 s60, s60, 13\n\t" \
  "s_add_u32 s80, s80, " TK_SG0 "\n\t" \
  "s_lshl_b32 s80, s80, 21\n\t" \
  "s_or_b32 s80, s80, s60\n\t" \
  "s_add_u32 s62, s83, s39\n\t" \
  "s_add_u32 s80, s80, s39\n\t" \
  "s_add_u32 s80, s80, 2048\n\t" \
  "s_cmp_lt_u32 s48, " TK_TRM "\n\t" \
  "s_cselect_b32 s80, s62, s80\n\t" \
  "v_add_u32 v7, s62, v3\n\t" \
  "s_mov_b32 exec_lo, 0\n\t" \
  "v_mov_b32 v7, s80\n\t" \
  "s_mov_b32 exec_lo, -1\n\t" \
  "LCOLD_%=:\n\t" \
  "s_waitcnt vmcnt(0)\n\t" \
  "s_mov_b64 exec, 1\n\t" \
  "buffer_store_dword v6, off, %[X], s49 sc1\n\t" \
  "s_mov_b64 exec, -1\n\t" \
  "s_mov_b32 s49, 0xf0000000\n\t" \
  "s_add_u32 s55, s83, s36\n\t" \
  "s_cmp_lg_u32 s40, 0\n\t" \
  "s_cbranch_scc1 LGO_%=\n\t" \
  "s_and_b32 s62, s45, 3\n\t" \
  "s_lshl_b32 s62, s62, 7\n\t" \
  "s_add_u32 s62, s62, s83\n\t" \
  "s_add_u32 s62, s62, s39\n\t" \
  "s_mov_b32 s61, 0\n\t" \
  "LPO_%=:\n\t" \
  "buffer_load_dword v4, off, %[X], s62 sc1\n\t" \
  "s_waitcnt vmcnt(0)\n\t" \
  "v_cmp_eq_u32 vcc, 0, v4\n\t" \
  "s_cbranch_vccz LPO_%=x\n\t" \
  "s_add_u32 s61, s61, 1\n\t" \
  "s_sleep " TK_PSH "\n\t" \
  "s_cmp_gt_u32 s61, s84\n\t" \
  "s_cbranch_scc1 LPO_%=g\n\t" \
  "s_and_b32 s60, s61, 1023\n\t" \
  "s_cmp_lg_u32 s60, 0\n\t" \
  "s_cbranch_scc1 LPO_%=\n\t" \
  "buffer_load_dword v4, off, %[X], s92 sc1\n\t" \
  "s_waitcnt vmcnt(0)\n\t" \
  "v_readfirstlane_b32 s60, v4\n\t" \
  "s_nop 0\n\t" \
  "s_cmp_eq_u32 s60, 0\n\t" \
  "s_cbranch_scc1 LPO_%=\n\t" \
  "LPO_%=g:\n\t" \
  "s_mov_b32 s84, 0\n\t" \
  "s_or_b32 s60, s48, 1610612736\n\t" \
  "v_mov_b32 v4, s60\n\t" \
  "s_mov_b64 exec, 1\n\t" \
  "buffer_store_dword v4, off, %[X], s92 sc1\n\t" \
  "s_mov_b64 exec, -1\n\t" \
  "LPO_%=x:\n\t" \
  "s_mov_b32 s61, 0\n\t" \
  "LPH_%=:\n\t" \
  "buffer_load_dword v4, v7, %[X], 0 offen sc1\n\t" \
  "s_waitcnt vmcnt(0)\n\t" \
  "v_cmp_eq_u32 vcc, 0, v4\n\t" \
  "s_cbranch_vccz LPH_%=x\n\t" \
  "s_add_u32 s61, s61, 1\n\t" \
  "s_sleep " TK_PSH "\n\t" \
  "s_cmp_gt_u32 s61, s84\n\t" \
  "s_cbranch_scc1 LPH_%=g\n\t" \
  "s_and_b32 s60, s61, 1023\n\t" \
  "s_cmp_lg_u32 s60, 0\n\t" \
  "s_cbranch_scc1 LPH_%=\n\t" \
  "buffer_load_dword v4, off, %[X], s92 sc1\n\t" \
  "s_waitcnt vmcnt(0)\n\t" \
  "v_readfirstlane_b32 s60, v4\n\t" \
  "s_nop 0\n\t" \
  "s_cmp_eq_u32 s60, 0\n\t" \
  "s_cbranch_scc1 LPH_%=\n\t" \
  "LPH_%=g:\n\t" \
  "s_mov_b32 s84, 0\n\t" \
  "s_or_b32 s60, s48, 1610612736\n\t" \
  "v_mov_b32 v4, s60\n\t" \
  "s_mov_b64 exec, 1\n\t" \
  "buffer_store_dword v4, off, %[X], s92 sc1\n\t" \
  "s_mov_b64 exec, -1\n\t" \
  "LPH_%=x:\n\t" \
  "s_cmp_lt_i32 s57, 0\n\t" \
  "s_cbranch_scc1 LGO_%=\n\t" \
  "s_movk_i32 s63, 4000\n\t" \
  "s_branch LGC_%=u\n\t" \
  "LGC_%=:\n\t" \
  "s_sleep " TK_PSG "\n\t" \
  "buffer_load_dword v8, off, %[X], s57 sc1\n\t" \
  "s_waitcnt vmcnt(0)\n\t" \
  "LGC_%=u:\n\t" \
  "s_memrealtime s[52:53]\n\t" \
  "v_readfirstlane_b32 s60, v8\n\t" \
  "s_waitcnt lgkmcnt(0)\n\t" \
  "s_sub_u32 s63, s63, 1\n\t" \
  "s_cbranch_scc1 LGO_%=\n\t" \
  "s_cmp_eq_u32 s60, -1\n\t" \
  "s_cbranch_scc1 LGC_%=\n\t" \
  "s_not_b32 s60, s60\n\t" \
  "s_lshl_b32 s61, s52, 8\n\t" \
  "s_sub_u32 s60, s60, s61\n\t" \
  "s_ashr_i32 s60, s60, 8\n\t" \
  "s_sub_i32 s60, s60, " TK_QXC "\n\t" \
  "s_min_i32 s60, s60, 400\n\t" \
  "s_add_u32 s54, s52, s60\n\t" \
  "LGC_%=w:\n\t" \
  "s_sleep 1\n\t" \
  "s_memrealtime s[52:53]\n\t" \
  "s_waitcnt lgkmcnt(0)\n\t" \
  "s_sub_i32 s53, s52, s54\n\t" \
  "s_cmp_lt_i32 s53, 0\n\t" \
  "s_cbranch_scc1 LGC_%=w\n\t" \
  "LGO_%=:\n\t" \
  "s_barrier\n\t" \
  "s_add_u32 m0, s46, 0\n\t" \
  "s_add_u32 s47, s55, 0\n\t" \
  "buffer_load_dwordx4 v0, %[X], s47 offen sc1 lds\n\t" \
  "s_add_u32 m0, s46, 4096\n\t" \
  "s_add_u32 s47, s55, 4096\n\t" \
  "buffer_load_dwordx4 v0, %[X], s47 offen sc1 lds\n\t" \
  "s_add_u32 m0, s46, 8192\n\t" \
  "s_add_u32 s47, s55, 16384\n\t" \
  "buffer_load_dwordx4 v0, %[X], s47 offen sc1 lds\n\t" \
  "s_add_u32 m0, s46, 12288\n\t" \
  "s_add_u32 s47, s55, 20480\n\t" \
  "buffer_load_dwordx4 v0, %[X], s47 offen sc1 lds\n\t" \
  "s_add_u32 m0, s46, 16384\n\t" \
  "s_add_u32 s47, s55, 32768\n\t" \
  "buffer_load_dwordx4 v0, %[X], s47 offen sc1 lds\n\t" \
  "s_add_u32 m0, s46, 20480\n\t" \
  "s_add_u32 s47, s55, 36864\n\t" \
  "buffer_load_dwordx4 v0, %[X], s47 offen sc1 lds\n\t" \
  "s_add_u32 m0, s46, 24576\n\t" \
  "s_add_u32 s47, s55, 49152\n\t" \
  "buffer_load_dwordx4 v0, %[X], s47 offen sc1 lds\n\t" \
  "s_add_u32 m0, s46, 28672\n\t" \
  "s_add_u32 s47, s55, 53248\n\t" \
  "buffer_load_dwordx4 v0, %[X], s47 offen sc1 lds\n\t" \
  "s_add_u32 m0, s46, 32768\n\t" \
  "s_add_u32 s47, s55, 65536\n\t" \
  "buffer_load_dwordx4 v0, %[X], s47 offen sc1 lds\n\t" \
  "s_waitcnt vmcnt(5)\n\t" \
  "LTOP_%=:\n\t" \
  "s_barrier\n\t" \
  "ds_read_b128 v[64:67], v1 offset:0\n\t" \
  "ds_read_b128 v[68:71], v1 offset:2048\n\t" \
  "ds_read_b128 v[72:75], v2 offset:0\n\t" \
  "ds_read_b128 v[76:79], v2 offset:2048\n\t" \
  "ds_read_b128 v[80:83], v1 offset:4096\n\t" \
  "ds_read_b128 v[84:87], v1 offset:6144\n\t" \
  "s_mov_b32 s82, s83\n\t" \
  "s_xor_b32 s46, s46, 0x10000\n\t" \
  "s_waitcnt lgkmcnt(5)\n\t" \
  "v_mfma_f32_16x16x32_bf16 v[16:19], v[64:67], a[0:3], 0\n\t" \
  "ds_read_b128 v[88:91], v2 offset:4096\n\t" \
  "s_waitcnt lgkmcnt(5)\n\t" \
  "v_mfma_f32_16x16x32_bf16 v[20:23], v[68:71], a[0:3], 0\n\t" \
  "s_add_u32 s50, s48, 0\n\t" \
  "s_and_b32 s50, s50, " TK_TRM "\n\t" \
  "v_mfma_f32_16x16x32_bf16 v[24:27], v[64:67], a[72:75], 0\n\t" \
  "ds_read_b128 v[92:95], v2 offset:6144\n\t" \
  "v_mfma_f32_16x16x32_bf16 v[28:31], v[68:71], a[72:75], 0\n\t" \
  "s_lshr_b64 s[60:61], s[90:91], s50\n\t" \
  "s_and_b32 s60, s60, 1\n\t" \
  "s_waitcnt lgkmcnt(5)\n\t" \
  "v_mfma_f32_16x16x32_bf16 v[16:19], v[72:75], a[4:7], v[16:19]\n\t" \
  "ds_read_b128 v[96:99], v1 offset:8192\n\t" \
  "s_waitcnt lgkmcnt(5)\n\t" \
  "v_mfma_f32_16x16x32_bf16 v[20:23], v[76:79], a[4:7], v[20:23]\n\t" \
  "s_lshl_b32 s60, s60, 13\n\t" \
  "s_add_u32 s50, s50, " TK_SG0 "\n\t" \
  "v_mfma_f32_16x16x32_bf16 v[24:27], v[72:75], a[76:79], v[24:27]\n\t" \
  "ds_read_b128 v[100:103], v1 offset:10240\n\t" \
  "v_mfma_f32_16x16x32_bf16 v[28:31], v[76:79], a[76:79], v[28:31]\n\t" \
  "s_lshl_b32 s50, s50, 21\n\t" \
  "s_or_b32 s50, s50, s60\n\t" \
  "s_waitcnt lgkmcnt(5)\n\t" \
  "v_mfma_f32_16x16x32_bf16 v[16:19], v[80:83], a[8:11], v[16:19]\n\t" \
  "ds_read_b128 v[104:107], v2 offset:8192\n\t" \
  "s_waitcnt lgkmcnt(5)\n\t" \
  "v_mfma_f32_16x16x32_bf16 v[20:23], v[84:87], a[8:11], v[20:23]\n\t" \
  "s_add_u32 s50, s50, s37\n\t" \
  "s_add_u32 s51, s50, 4096\n\t" \
  "v_mfma_f32_16x16x32_bf16 v[24:27], v[80:83], a[80:83], v[24:27]\n\t" \
  "ds_read_b128 v[108:111], v2 offset:10240\n\t" \
  "v_mfma_f32_16x16x32_bf16 v[28:31], v[84:87], a[80:83], v[28:31]\n\t" \
  "s_add_u32 s83, s48, 1\n\t" \
  "s_lshr_b64 s[60:61], s[90:91], s83\n\t" \
  "s_waitcnt lgkmcnt(5)\n\t" \
  "v_mfma_f32_16x16x32_bf16 v[16:19], v[88:91], a[12:15], v[16:19]\n\t" \
  "ds_read_b128 v[112:115], v1 offset:12288\n\t" \
  "s_waitcnt lgkmcnt(5)\n\t" \
  "v_mfma_f32_16x16x32_bf16 v[20:23], v[92:95], a[12:15], v[20:23]\n\t" \
  "s_and_b32 s60, s60, 1\n\t" \
  "s_lshl_b32 s60, s60, 13\n\t" \
  "v_mfma_f32_16x16x32_bf16 v[24:27], v[88:91], a[84:87], v[24:27]\n\t" \
  "ds_read_b128 v[116:119], v1 offset:14336\n\t" \
  "v_mfma_f32_16x16x32_bf16 v[28:31], v[92:95], a[84:87], v[28:31]\n\t" \
  "s_add_u32 s83, s83, " TK_SG0 "\n\t" \
  "s_lshl_b32 s83, s83, 21\n\t" \
  "s_waitcnt lgkmcnt(5)\n\t" \
  "v_mfma_f32_16x16x32_bf16 v[16:19], v[96:99], a[16:19], v[16:19]\n\t" \
  "ds_read_b128 v[120:123], v2 offset:12288\n\t" \
  "s_waitcnt lgkmcnt(5)\n\t" \
  "v_mfma_f32_16x16x32_bf16 v[20:23], v[100:103], a[16:19], v[20:23]\n\t" \
  "s_or_b32 s83, s83, s60\n\t" \
  "s_cmp_lg_u32 s56, 0\n\t" \
  "v_mfma_f32_16x16x32_bf16 v[24:27], v[96:99], a[88:91], v[24:27]\n\t" \
  "ds_read_b128 v[124:127], v2 offset:14336\n\t" \
  "v_mfma_f32_16x16x32_bf16 v[28:31], v[100:103], a[88:91], v[28:31]\n\t" \
  "s_cbranch_scc1 LWARM_%=\n\t" \
  "s_waitcnt vmcnt(0)\n\t" \
  "LWARM_%=:\n\t" \
  "s_barrier\n\t" \
  "s_waitcnt lgkmcnt(5)\n\t" \
  "v_mfma_f32_16x16x32_bf16 v[16:19], v[104:107], a[20:23], v[16:19]\n\t" \
  "ds_read_b128 v[128:131], v1 offset:16384\n\t" \
  "s_waitcnt lgkmcnt(5)\n\t" \
  "v_mfma_f32_16x16x32_bf16 v[20:23], v[108:111], a[20:23], v[20:23]\n\t" \
  "v_mfma_f32_16x16x32_bf16 v[24:27], v[104:107], a[92:95], v[24:27]\n\t" \
  "ds_read_b128 v[132:135], v1 offset:18432\n\t" \
  "v_mfma_f32_16x16x32_bf16 v[28:31], v[108:111], a[92:95], v[28:31]\n\t" \
  "s_add_i32 s62, s48, s42\n\t" \
  "s_add_i32 s62, s62, 1\n\t" \
  "s_waitcnt lgkmcnt(5)\n\t" \
  "v_mfma_f32_16x16x32_bf16 v[16:19], v[112:115], a[24:27], v[16:19]\n\t" \
  "ds_read_b128 v[136:139], v2 offset:16384\n\t" \
  "s_waitcnt lgkmcnt(5)\n\t" \
  "v_mfma_f32_16x16x32_bf16 v[20:23], v[116:119], a[24:27], v[20:23]\n\t" \
  "s_mov_b32 s61, s62\n\t" \
  "v_mfma_f32_16x16x32_bf16 v[24:27], v[112:115], a[96:99], v[24:27]\n\t" \
  "ds_read_b128 v[140:143], v2 offset:18432\n\t" \
  "v_mfma_f32_16x16x32_bf16 v[28:31], v[116:119], a[96:99], v[28:31]\n\t" \
  "s_mul_i32 s63, s62, 43\n\t" \
  "s_lshr_b32 s63, s63, 9\n\t" \
  "s_waitcnt lgkmcnt(5)\n\t" \
  "v_mfma_f32_16x16x32_bf16 v[16:19], v[120:123], a[28:31], v[16:19]\n\t" \
  "ds_read_b128 v[144:147], v1 offset:20480\n\t" \
  "s_waitcnt lgkmcnt(5)\n\t" \
  "v_mfma_f32_16x16x32_bf16 v[20:23], v[124:127], a[28:31], v[20:23]\n\t" \
  "s_mul_i32 s63, s63, " TK_RM "\n\t" \
  "s_sub_u32 s62, s62, s63\n\t" \
  "v_mfma_f32_16x16x32_bf16 v[24:27], v[120:123], a[100:103], v[24:27]\n\t" \
  "ds_read_b128 v[148:151], v1 offset:22528\n\t" \
  "v_mfma_f32_16x16x32_bf16 v[28:31], v[124:127], a[100:103], v[28:31]\n\t" \
  "s_mul_i32 s63, s62, 43\n\t" \
  "s_lshr_b32 s63, s63, 7\n\t" \
  "s_waitcnt lgkmcnt(5)\n\t" \
  "v_mfma_f32_16x16x32_bf16 v[16:19], v[128:131], a[32:35], v[16:19]\n\t" \
  "ds_read_b128 v[152:155], v2 offset:20480\n\t" \
  "s_waitcnt lgkmcnt(5)\n\t" \
  "v_mfma_f32_16x16x32_bf16 v[20:23], v[132:135], a[32:35], v[20:23]\n\t" \
  "s_mul_i32 s57, s62, 688128\n\t" \
  "s_lshl_b32 s60, s63, 15\n\t" \
  "v_mfma_f32_16x16x32_bf16 v[24:27], v[128:131], a[104:107], v[24:27]\n\t" \
  "ds_read_b128 v[156:159], v2 offset:22528\n\t" \
  "v_mfma_f32_16x16x32_bf16 v[28:31], v[132:135], a[104:107], v[28:31]\n\t" \
  "s_add_u32 s57, s57, s60\n\t" \
  "s_lshr_b32 s60, s88, s63\n\t" \
  "s_waitcnt lgkmcnt(5)\n\t" \
  "v_mfma_f32_16x16x32_bf16 v[16:19], v[136:139], a[36:39], v[16:19]\n\t" \
  "ds_read_b128 v[160:163], v1 offset:24576\n\t" \
  "s_waitcnt lgkmcnt(5)\n\t" \
  "v_mfma_f32_16x16x32_bf16 v[20:23], v[140:143], a[36:39], v[20:23]\n\t" \
  "s_and_b32 s60, s60, 1\n\t" \
  "s_lshl_b32 s60, s60, 13\n\t" \
  "v_mfma_f32_16x16x32_bf16 v[24:27], v[136:139], a[108:111], v[24:27]\n\t" \
  "ds_read_b128 v[164:167], v1 offset:26624\n\t" \
  "v_mfma_f32_16x16x32_bf16 v[28:31], v[140:143], a[108:111], v[28:31]\n\t" \
  "s_add_u32 s57, s57, s60\n\t" \
  "s_add_u32 s57, s57, 656896\n\t" \
  "s_waitcnt lgkmcnt(5)\n\t" \
  "v_mfma_f32_16x16x32_bf16 v[16:19], v[144:147], a[40:43], v[16:19]\n\t" \
  "ds_read_b128 v[168:171], v2 offset:24576\n\t" \
  "s_waitcnt lgkmcnt(5)\n\t" \
  "v_mfma_f32_16x16x32_bf16 v[20:23], v[148:151], a[40:43], v[20:23]\n\t" \
  "s_cmp_lt_i32 s61, 1\n\t" \
  "s_cselect_b32 s57, 0xf0000000, s57\n\t" \
  "v_mfma_f32_16x16x32_bf16 v[24:27], v[144:147], a[112:115], v[24:27]\n\t" \
  "ds_read_b128 v[172:175], v2 offset:26624\n\t" \
  "v_mfma_f32_16x16x32_bf16 v[28:31], v[148:151], a[112:115], v[28:31]\n\t" \
  "s_cmp_lg_u32 s40, 0\n\t" \
  "s_cselect_b32 s57, 0xf0000000, s57\n\t" \
  "s_waitcnt lgkmcnt(5)\n\t" \
  "v_mfma_f32_16x16x32_bf16 v[16:19], v[152:155], a[44:47], v[16:19]\n\t" \
  "ds_read_b128 v[176:179], v1 offset:28672\n\t" \
  "s_waitcnt lgkmcnt(5)\n\t" \
  "v_mfma_f32_16x16x32_bf16 v[20:23], v[156:159], a[44:47], v[20:23]\n\t" \
  "s_add_u32 s80, s48, " TK_TRN "\n\t" \
  "s_lshr_b64 s[60:61], s[90:91], s80\n\t" \
  "s_and_b32 s60, s60, 1\n\t" \
  "v_mfma_f32_16x16x32_bf16 v[24:27], v[152:155], a[116:119], v[24:27]\n\t" \
  "ds_read_b128 v[180:183], v1 offset:30720\n\t" \
  "v_mfma_f32_16x16x32_bf16 v[28:31], v[156:159], a[116:119], v[28:31]\n\t" \
  "s_lshl_b32 s60, s60, 13\n\t" \
  "s_add_u32 s80, s80, " TK_SG0 "\n\t" \
  "s_lshl_b32 s80, s80, 21\n\t" \
  "s_waitcnt lgkmcnt(5)\n\t" \
  "v_mfma_f32_16x16x32_bf16 v[16:19], v[160:163], a[48:51], v[16:19]\n\t" \
  "ds_read_b128 v[184:187], v2 offset:28672\n\t" \
  "s_waitcnt lgkmcnt(5)\n\t" \
  "v_mfma_f32_16x16x32_bf16 v[20:23], v[164:167], a[48:51], v[20:23]\n\t" \
  "s_or_b32 s80, s80, s60\n\t" \
  "s_add_u32 s62, s83, s39\n\t" \
  "s_add_u32 s80, s80, s39\n\t" \
  "v_mfma_f32_16x16x32_bf16 v[24:27], v[160:163], a[120:123], v[24:27]\n\t" \
  "ds_read_b128 v[188:191], v2 offset:30720\n\t" \
  "v_mfma_f32_16x16x32_bf16 v[28:31], v[164:167], a[120:123], v[28:31]\n\t" \
  "s_add_u32 s80, s80, 2048\n\t" \
  "s_waitcnt lgkmcnt(5)\n\t" \
  "v_mfma_f32_16x16x32_bf16 v[16:19], v[168:171], a[52:55], v[16:19]\n\t" \
  "ds_read_b128 v[192:195], v1 offset:32768\n\t" \
  "s_waitcnt lgkmcnt(5)\n\t" \
  "v_mfma_f32_16x16x32_bf16 v[20:23], v[172:175], a[52:55], v[20:23]\n\t" \
  "v_mfma_f32_16x16x32_bf16 v[24:27], v[168:171], a[124:127], v[24:27]\n\t" \
  "ds_read_b128 v[196:199], v1 offset:34816\n\t" \
  "v_mfma_f32_16x16x32_bf16 v[28:31], v[172:175], a[124:127], v[28:31]\n\t" \
  "s_cmp_lt_u32 s48, " TK_TRM "\n\t" \
  "s_cselect_b32 s80, s62, s80\n\t" \
  "s_waitcnt lgkmcnt(5)\n\t" \
  "v_mfma_f32_16x16x32_bf16 v[16:19], v[176:179], a[56:59], v[16:19]\n\t" \
  "ds_read_b128 v[200:203], v2 offset:32768\n\t" \
  "s_waitcnt lgkmcnt(5)\n\t" \
  "v_mfma_f32_16x16x32_bf16 v[20:23], v[180:183], a[56:59], v[20:23]\n\t" \
  "v_add_u32 v7, s62, v3\n\t" \
  "s_mov_b32 exec_lo, 0\n\t" \
  "v_mov_b32 v7, s80\n\t" \
  "s_mov_b32 exec_lo, -1\n\t" \
  "v_mfma_f32_16x16x32_bf16 v[24:27], v[176:179], a[128:131], v[24:27]\n\t" \
  "ds_read_b128 v[204:207], v2 offset:34816\n\t" \
  "v_mfma_f32_16x16x32_bf16 v[28:31], v[180:183], a[128:131], v[28:31]\n\t" \
  "s_waitcnt lgkmcnt(5)\n\t" \
  "v_mfma_f32_16x16x32_bf16 v[16:19], v[184:187], a[60:63], v[16:19]\n\t" \
  "s_waitcnt lgkmcnt(4)\n\t" \
  "v_mfma_f32_16x16x32_bf16 v[20:23], v[188:191], a[60:63], v[20:23]\n\t" \
  "buffer_load_dword v4, v7, %[X], 0 offen sc1\n\t" \
  "v_mfma_f32_16x16x32_bf16 v[24:27], v[184:187], a[132:135], v[24:27]\n\t" \
  "v_mfma_f32_16x16x32_bf16 v[28:31], v[188:191], a[132:135], v[28:31]\n\t" \
  "s_waitcnt lgkmcnt(3)\n\t" \
  "v_mfma_f32_16x16x32_bf16 v[16:19], v[192:195], a[64:67], v[16:19]\n\t" \
  "v_xor_b32 v1, 0x10000, v1\n\t" \
  "v_xor_b32 v2, 0x10000, v2\n\t" \
  "s_waitcnt lgkmcnt(2)\n\t" \
  "v_mfma_f32_16x16x32_bf16 v[20:23], v[196:199], a[64:67], v[20:23]\n\t" \
  "v_mfma_f32_16x16x32_bf16 v[24:27], v[192:195], a[136:139], v[24:27]\n\t" \
  "v_mfma_f32_16x16x32_bf16 v[28:31], v[196:199], a[136:139], v[28:31]\n\t" \
  "s_waitcnt lgkmcnt(1)\n\t" \
  "v_mfma_f32_16x16x32_bf16 v[16:19], v[200:203], a[68:71], v[16:19]\n\t" \
  "s_waitcnt lgkmcnt(0)\n\t" \
  "v_mfma_f32_16x16x32_bf16 v[20:23], v[204:207], a[68:71], v[20:23]\n\t" \
  "v_mfma_f32_16x16x32_bf16 v[24:27], v[200:203], a[140:143], v[24:27]\n\t" \
  "v_mfma_f32_16x16x32_bf16 v[28:31], v[204:207], a[140:143], v[28:31]\n\t" \
  "v_mfma_f32_16x16x32_bf16 v[32:35], v[64:67], a[144:147], 0\n\t" \
  "v_cvt_pk_bf16_f32 v48, v16, v17\n\t" \
  "v_cvt_pk_bf16_f32 v49, v18, v19\n\t" \
  "v_mfma_f32_16x16x32_bf16 v[36:39], v[68:71], a[144:147], 0\n\t" \
  "v_cvt_pk_bf16_f32 v50, v20, v21\n\t" \
  "v_cvt_pk_bf16_f32 v51, v22, v23\n\t" \
  "v_mfma_f32_16x16x32_bf16 v[32:35], v[72:75], a[148:151], v[32:35]\n\t" \
  "v_cvt_pk_bf16_f32 v52, v24, v25\n\t" \
  "v_cvt_pk_bf16_f32 v53, v26, v27\n\t" \
  "v_mfma_f32_16x16x32_bf16 v[36:39], v[76:79], a[148:151], v[36:39]\n\t" \
  "v_cvt_pk_bf16_f32 v54, v28, v29\n\t" \
  "v_cvt_pk_bf16_f32 v55, v30, v31\n\t" \
  "v_mfma_f32_16x16x32_bf16 v[32:35], v[80:83], a[152:155], v[32:35]\n\t" \
  "buffer_store_dwordx4 v[48:51], v0, %[X], s50 offen offset:0 sc1\n\t" \
  "v_mfma_f32_16x16x32_bf16 v[36:39], v[84:87], a[152:155], v[36:39]\n\t" \
  "v_mfma_f32_16x16x32_bf16 v[32:35], v[88:91], a[156:159], v[32:35]\n\t" \
  "v_mfma_f32_16x16x32_bf16 v[36:39], v[92:95], a[156:159], v[36:39]\n\t" \
  "v_mfma_f32_16x16x32_bf16 v[32:35], v[96:99], a[160:163], v[32:35]\n\t" \
  "v_mfma_f32_16x16x32_bf16 v[36:39], v[100:103], a[160:163], v[36:39]\n\t" \
  "v_mfma_f32_16x16x32_bf16 v[32:35], v[104:107], a[164:167], v[32:35]\n\t" \
  "v_mfma_f32_16x16x32_bf16 v[36:39], v[108:111], a[164:167], v[36:39]\n\t" \
  "v_mfma_f32_16x16x32_bf16 v[32:35], v[112:115], a[168:171], v[32:35]\n\t" \
  "v_mfma_f32_16x16x32_bf16 v[36:39], v[116:119], a[168:171], v[36:39]\n\t" \
  "v_mfma_f32_16x16x32_bf16 v[32:35], v[120:123], a[172:175], v[32:35]\n\t" \
  "v_mfma_f32_16x16x32_bf16 v[36:39], v[124:127], a[172:175], v[36:39]\n\t" \
  "s_waitcnt vmcnt(1)\n\t" \
  "v_cmp_eq_u32 vcc, 0, v4\n\t" \
  "v_mfma_f32_16x16x32_bf16 v[32:35], v[128:131], a[176:179], v[32:35]\n\t" \
  "v_mfma_f32_16x16x32_bf16 v[36:39], v[132:135], a[176:179], v[36:39]\n\t" \
  "s_cmp_eq_u64 vcc, 0\n\t" \
  "s_cselect_b32 s60, 1, 0\n\t" \
  "v_mov_b32 v4, s60\n\t" \
  "v_mfma_f32_16x16x32_bf16 v[32:35], v[136:139], a[180:183], v[32:35]\n\t" \
  "ds_write_b32 v5, v4\n\t" \
  "v_mfma_f32_16x16x32_bf16 v[36:39], v[140:143], a[180:183], v[36:39]\n\t" \
  "v_mfma_f32_16x16x32_bf16 v[32:35], v[144:147], a[184:187], v[32:35]\n\t" \
  "v_mfma_f32_16x16x32_bf16 v[36:39], v[148:151], a[184:187], v[36:39]\n\t" \
  "buffer_store_dwordx4 v[52:55], v0, %[X], s50 offen offset:1024 sc1\n\t" \
  "v_mfma_f32_16x16x32_bf16 v[32:35], v[152:155], a[188:191], v[32:35]\n\t" \
  "v_mfma_f32_16x16x32_bf16 v[36:39], v[156:159], a[188:191], v[36:39]\n\t" \
  "s_waitcnt lgkmcnt(0)\n\t" \
  "s_barrier\n\t" \
  "v_mfma_f32_16x16x32_bf16 v[32:35], v[160:163], a[192:195], v[32:35]\n\t" \
  "ds_read_b32 v4, v6 offset:40959\n\t" \
  "v_mfma_f32_16x16x32_bf16 v[36:39], v[164:167], a[192:195], v[36:39]\n\t" \
  "v_mfma_f32_16x16x32_bf16 v[32:35], v[168:171], a[196:199], v[32:35]\n\t" \
  "v_mfma_f32_16x16x32_bf16 v[36:39], v[172:175], a[196:199], v[36:39]\n\t" \
  "v_mfma_f32_16x16x32_bf16 v[32:35], v[176:179], a[200:203], v[32:35]\n\t" \
  "v_mfma_f32_16x16x32_bf16 v[36:39], v[180:183], a[200:203], v[36:39]\n\t" \
  "s_waitcnt lgkmcnt(0)\n\t" \
  "v_readfirstlane_b32 s60, v4\n\t" \
  "s_add_u32 s55, s83, s36\n\t" \
  "v_mfma_f32_16x16x32_bf16 v[32:35], v[184:187], a[204:207], v[32:35]\n\t" \
  "s_cmp_eq_u32 s48, 63\n\t" \
  "s_cselect_b32 s56, 0, s60\n\t" \
  "v_mfma_f32_16x16x32_bf16 v[36:39], v[188:191], a[204:207], v[36:39]\n\t" \
  "s_cmp_eq_u32 s56, 0\n\t" \
  "s_cselect_b32 s55, 0xf0000000, s55\n\t" \
  "v_mfma_f32_16x16x32_bf16 v[32:35], v[192:195], a[208:211], v[32:35]\n\t" \
  "v_mfma_f32_16x16x32_bf16 v[36:39], v[196:199], a[208:211], v[36:39]\n\t" \
  "v_mfma_f32_16x16x32_bf16 v[32:35], v[200:203], a[212:215], v[32:35]\n\t" \
  "s_add_u32 m0, s46, 0\n\t" \
  "s_add_u32 s47, s55, 0\n\t" \
  "buffer_load_dwordx4 v0, %[X], s47 offen sc1 lds\n\t" \
  "v_mfma_f32_16x16x32_bf16 v[36:39], v[204:207], a[212:215], v[36:39]\n\t" \
  "v_mfma_f32_16x16x32_bf16 v[40:43], v[64:67], a[216:219], 0\n\t" \
  "v_mfma_f32_16x16x32_bf16 v[44:47], v[68:71], a[216:219], 0\n\t" \
  "v_mfma_f32_16x16x32_bf16 v[40:43], v[72:75], a[220:223], v[40:43]\n\t" \
  "v_cvt_pk_bf16_f32 v56, v32, v33\n\t" \
  "v_cvt_pk_bf16_f32 v57, v34, v35\n\t" \
  "s_add_u32 m0, s46, 4096\n\t" \
  "s_add_u32 s47, s55, 4096\n\t" \
  "buffer_load_dwordx4 v0, %[X], s47 offen sc1 lds\n\t" \
  "v_mfma_f32_16x16x32_bf16 v[44:47], v[76:79], a[220:223], v[44:47]\n\t" \
  "v_cvt_pk_bf16_f32 v58, v36, v37\n\t" \
  "v_cvt_pk_bf16_f32 v59, v38, v39\n\t" \
  "v_mfma_f32_16x16x32_bf16 v[40:43], v[80:83], a[224:227], v[40:43]\n\t" \
  "v_mfma_f32_16x16x32_bf16 v[44:47], v[84:87], a[224:227], v[44:47]\n\t" \
  "buffer_store_dwordx4 v[56:59], v0, %[X], s50 offen offset:2048 sc1\n\t" \
  "v_mfma_f32_16x16x32_bf16 v[40:43], v[88:91], a[228:231], v[40:43]\n\t" \
  "s_add_u32 m0, s46, 8192\n\t" \
  "s_add_u32 s47, s55, 16384\n\t" \
  "buffer_load_dwordx4 v0, %[X], s47 offen sc1 lds\n\t" \
  "v_mfma_f32_16x16x32_bf16 v[44:47], v[92:95], a[228:231], v[44:47]\n\t" \
  "v_mfma_f32_16x16x32_bf16 v[40:43], v[96:99], a[232:235], v[40:43]\n\t" \
  "v_mfma_f32_16x16x32_bf16 v[44:47], v[100:103], a[232:235], v[44:47]\n\t" \
  "v_mfma_f32_16x16x32_bf16 v[40:43], v[104:107], a[236:239], v[40:43]\n\t" \
  "s_add_u32 m0, s46, 12288\n\t" \
  "s_add_u32 s47, s55, 20480\n\t" \
  "buffer_load_dwordx4 v0, %[X], s47 offen sc1 lds\n\t" \
  "v_mfma_f32_16x16x32_bf16 v[44:47], v[108:111], a[236:239], v[44:47]\n\t" \
  "v_mfma_f32_16x16x32_bf16 v[40:43], v[112:115], a[240:243], v[40:43]\n\t" \
  "v_mfma_f32_16x16x32_bf16 v[44:47], v[116:119], a[240:243], v[44:47]\n\t" \
  "v_mfma_f32_16x16x32_bf16 v[40:43], v[120:123], a[244:247], v[40:43]\n\t" \
  "s_add_u32 m0, s46, 16384\n\t" \
  "s_add_u32 s47, s55, 32768\n\t" \
  "buffer_load_dwordx4 v0, %[X], s47 offen sc1 lds\n\t" \
  "v_mfma_f32_16x16x32_bf16 v[44:47], v[124:127], a[244:247], v[44:47]\n\t" \
  "v_mfma_f32_16x16x32_bf16 v[40:43], v[128:131], a[248:251], v[40:43]\n\t" \
  "v_mfma_f32_16x16x32_bf16 v[44:47], v[132:135], a[248:251], v[44:47]\n\t" \
  "v_mfma_f32_16x16x32_bf16 v[40:43], v[136:139], a[252:255], v[40:43]\n\t" \
  "s_add_u32 m0, s46, 20480\n\t" \
  "s_add_u32 s47, s55, 36864\n\t" \
  "buffer_load_dwordx4 v0, %[X], s47 offen sc1 lds\n\t" \
  "v_mfma_f32_16x16x32_bf16 v[44:47], v[140:143], a[252:255], v[44:47]\n\t" \
  "v_mfma_f32_16x16x32_bf16 v[40:43], v[144:147], v[224:227], v[40:43]\n\t" \
  "v_mfma_f32_16x16x32_bf16 v[44:47], v[148:151], v[224:227], v[44:47]\n\t" \
  "v_mfma_f32_16x16x32_bf16 v[40:43], v[152:155], v[228:231], v[40:43]\n\t" \
  "s_add_u32 m0, s46, 24576\n\t" \
  "s_add_u32 s47, s55, 49152\n\t" \
  "buffer_load_dwordx4 v0, %[X], s47 offen sc1 lds\n\t" \
  "v_mfma_f32_16x16x32_bf16 v[44:47], v[156:159], v[228:231], v[44:47]\n\t" \
  "v_mfma_f32_16x16x32_bf16 v[40:43], v[160:163], v[232:235], v[40:43]\n\t" \
  "v_mfma_f32_16x16x32_bf16 v[44:47], v[164:167], v[232:235], v[44:47]\n\t" \
  "v_mfma_f32_16x16x32_bf16 v[40:43], v[168:171], v[236:239], v[40:43]\n\t" \
  "s_add_u32 m0, s46, 28672\n\t" \
  "s_add_u32 s47, s55, 53248\n\t" \
  "buffer_load_dwordx4 v0, %[X], s47 offen sc1 lds\n\t" \
  "v_mfma_f32_16x16x32_bf16 v[44:47], v[172:175], v[236:239], v[44:47]\n\t" \
  "v_mfma_f32_16x16x32_bf16 v[40:43], v[176:179], v[240:243], v[40:43]\n\t" \
  "v_mfma_f32_16x16x32_bf16 v[44:47], v[180:183], v[240:243], v[44:47]\n\t" \
  "v_mfma_f32_16x16x32_bf16 v[40:43], v[184:187], v[244:247], v[40:43]\n\t" \
  "s_add_u32 m0, s46, 32768\n\t" \
  "s_add_u32 s47, s55, 65536\n\t" \
  "buffer_load_dwordx4 v0, %[X], s47 offen sc1 lds\n\t" \
  "v_mfma_f32_16x16x32_bf16 v[44:47], v[188:191], v[244:247], v[44:47]\n\t" \
  "v_mfma_f32_16x16x32_bf16 v[40:43], v[192:195], v[248:251], v[40:43]\n\t" \
  "v_mfma_f32_16x16x32_bf16 v[44:47], v[196:199], v[248:251], v[44:47]\n\t" \
  "buffer_load_dword v8, off, %[X], s57 sc1\n\t" \
  "v_mfma_f32_16x16x32_bf16 v[40:43], v[200:203], v[252:255], v[40:43]\n\t" \
  "v_mfma_f32_16x16x32_bf16 v[44:47], v[204:207], v[252:255], v[44:47]\n\t" \
  "s_nop 4\n\t" \
  "v_cvt_pk_bf16_f32 v60, v40, v41\n\t" \
  "v_cvt_pk_bf16_f32 v61, v42, v43\n\t" \
  "s_nop 1\n\t" \
  "v_cvt_pk_bf16_f32 v62, v44, v45\n\t" \
  "v_cvt_pk_bf16_f32 v63, v46, v47\n\t" \
  "buffer_store_dwordx4 v[60:63], v0, %[X], s50 offen offset:3072 sc1\n\t" \
  "s_add_u32 s49, s82, s38\n\t" \
  "s_cmp_eq_u32 s48, 63\n\t" \
  "s_cbranch_scc1 LEND_%=\n\t" \
  "s_add_u32 s48, s48, 1\n\t" \
  "s_cmp_eq_u32 s56, 0\n\t" \
  "s_cbranch_scc1 LCOLD_%=\n\t" \
  "s_waitcnt vmcnt(0)\n\t" \
  "s_mov_b64 exec, 1\n\t" \
  "buffer_store_dword v6, off, %[X], s49 sc1\n\t" \
  "s_mov_b64 exec, -1\n\t" \
  "s_mov_b32 s49, 0xf0000000\n\t" \
  "s_cmp_lt_i32 s57, 0\n\t" \
  "s_cbranch_scc1 LTOP_%=\n\t" \
  "s_movk_i32 s63, 4000\n\t" \
  "s_branch LGW_%=u\n\t" \
  "LGW_%=:\n\t" \
  "s_sleep " TK_PSG "\n\t" \
  "buffer_load_dword v8, off, %[X], s57 sc1\n\t" \
  "s_waitcnt vmcnt(0)\n\t" \
  "LGW_%=u:\n\t" \
  "s_memrealtime s[52:53]\n\t" \
  "v_readfirstlane_b32 s60, v8\n\t" \
  "s_waitcnt lgkmcnt(0)\n\t" \
  "s_sub_u32 s63, s63, 1\n\t" \
  "s_cbranch_scc1 LTOP_%=\n\t" \
  "s_cmp_eq_u32 s60, -1\n\t" \
  "s_cbranch_scc1 LGW_%=\n\t" \
  "s_not_b32 s60, s60\n\t" \
  "s_lshl_b32 s61, s52, 8\n\t" \
  "s_sub_u32 s60, s60, s61\n\t" \
  "s_ashr_i32 s60, s60, 8\n\t" \
  "s_sub_i32 s60, s60, " TK_QXW "\n\t" \
  "s_min_i32 s60, s60, 400\n\t" \
  "s_add_u32 s54, s52, s60\n\t" \
  "LGW_%=w:\n\t" \
  "s_sleep 1\n\t" \
  "s_memrealtime s[52:53]\n\t" \
  "s_waitcnt lgkmcnt(0)\n\t" \
  "s_sub_i32 s53, s52, s54\n\t" \
  "s_cmp_lt_i32 s53, 0\n\t" \
  "s_cbranch_scc1 LGW_%=w\n\t" \
  "s_branch LTOP_%=\n\t" \
  "LEND_%=:\n\t" \
  "s_waitcnt vmcnt(0)\n\t" \
  "s_mov_b64 exec, 1\n\t" \
  "buffer_store_dword v6, off, %[X], s49 sc1\n\t" \
  "s_mov_b64 exec, -1\n\t" \
  "s_mov_b32 s49, 0xf0000000\n\t" \
  ""
__device__ __forceinline__ unsigned hoff(int r, int piece) {
  const int m = 4 * (r >> 3) + (r & 3), kp = piece >> 3;
  return (P_H + (unsigned)(kp >> 1)) * PAIR + (unsigned)((kp & 1) * 4096 + (16 * ((r >> 2) & 1) + m) * 128 + (((piece ^ m) & 7) << 4));
}
template <int SLEEP>
__device__ __forceinline__ void wait_all(rsrc_t X, unsigned vo, unsigned so) {
  unsigned spins = 0;
  while (true) {
    asm volatile("" ::: "memory");
    if (__all(ld4(X, vo, so) != 0u)) break;
    if (spin_fail(spins, X)) break;
    __builtin_amdgcn_s_sleep(SLEEP);
  }
  asm volatile("" ::: "memory");
}
#define VQ_CLOB "v8", "v16", "v17", "v18", "v19", "v20", "v21", "v22", "v23", "v24", "v25", "v26", "v27", "v28", "v29", "v30", "v31", "v32", "v33", "v34", "v35", "v36", "v37", "v38", "v39", "v40", "v41", "v42", "v43", "v44", "v45", "v46", "v47", "v48", "v49", "v50", "v51", "v52", "v53", "v54", "v55", "v56", "v57", "v58", "v59", "v60", "v61", "v62", "v63", "v64", "v65", "v66", "v67", "v68", "v69", "v70", "v71", "v72", "v73", "v74", "v75", "v76", "v77", "v78", "v79", "v80", "v81", "v82", "v83", "v84", "v85", "v86", "v87", "v88", "v89", "v90", "v91", "v92", "v93", "v94", "v95", "v96", "v97", "v98", "v99", "v100", "v101", "v102", "v103", "v104", "v105", "v106", "v107", "v108", "v109", "v110", "v111", "v112", "v113", "v114", "v115", "v116", "v117", "v118", "v119", "v120", "v121", "v122", "v123", "v124", "v125", "v126", "v127", "v128", "v129", "v130", "v131", "v132", "v133", "v134", "v135", "v136", "v137", "v138", "v139", "v140", "v141", "v142", "v143", "v144", "v145", "v146", "v147", "v148", "v149", "v150", "v151", "v152", "v153", "v154", "v155", "v156", "v157", "v158", "v159", "v160", "v161", "v162", "v163", "v164", "v165", "v166", "v167", "v168", "v169", "v170", "v171", "v172", "v173", "v174", "v175", "v176", "v177", "v178", "v179", "v180", "v181", "v182", "v183", "v184", "v185", "v186", "v187", "v188", "v189", "v190", "v191", "v192", "v193", "v194", "v195", "v196", "v197", "v198", "v199", "v200", "v201", "v202", "v203", "v204", "v205", "v206", "v207", "v220", "v221"
#define VA_CLOB "v40", "v41", "v42", "v43", "v44", "v45", "v46", "v47", "v48", "v49", "v50", "v51", "v52", "v53", "v54", "v55", "v56", "v57", "v58", "v59", "v60", "v61", "v62", "v63", "v64", "v65", "v66", "v67", "v68", "v69", "v70", "v71", "v72", "v73", "v74", "v75", "v76", "v77", "v78", "v79", "v80", "v81", "v82", "v83", "v84", "v85", "v86", "v87", "v88", "v89", "v90", "v91", "v92", "v93", "v94", "v95", "v96", "v97", "v98", "v99", "v100", "v101", "v102", "v103", "v104", "v105", "v106", "v107", "v108", "v109", "v110", "v111", "v112", "v113", "v114", "v115", "v116", "v117", "v118", "v119", "v120", "v121", "v122", "v123", "v124", "v125", "v126", "v127", "v128", "v129", "v130", "v131", "v132", "v133", "v134", "v135", "v136", "v137", "v138", "v139", "v140", "v141", "v142", "v143", "v144", "v145", "v146", "v147", "v148", "v149", "v150", "v151", "v152", "v153", "v154", "v155", "v156", "v157", "v158", "v159", "v160", "v161", "v162", "v163", "v164", "v165", "v166", "v167", "v168", "v169", "v170", "v171", "v172", "v173", "v174", "v175", "v176", "v177", "v178", "v179", "v180", "v181", "v182", "v183", "v184", "v185", "v186", "v187", "v188", "v189", "v190", "v191", "v192", "v193", "v194", "v195", "v196", "v197", "v198", "v199", "v200", "v201", "v202", "v203", "v204", "v205", "v206", "v207", "v208", "v209", "v210", "v211", "v220", "v221"
#define TOK_COMMON() \
  const MP p = getp(); \
  const int c = (int)blockIdx.x - NCHW, xq = c & 3, L = (c >> 2) & 1, i = c >> 3; \
  const int tid = threadIdx.x, lane = tid & 63, w = __builtin_amdgcn_readfirstlane(tid >> 6); \
  const rsrc_t X = mkr(p.xb, XB_BYTES)
#define W_IN [W] "s"(WR)
#define W_CLOB "a0", "a1", "a2", "a3", "a4", "a5", "a6", "a7", "a8", "a9", "a10", "a11", "a12", "a13", "a14", "a15", "a16", "a17", "a18", "a19", "a20", "a21", "a22", "a23", "a24", "a25", "a26", "a27", "a28", "a29", "a30", "a31", "a32", "a33", "a34", "a35", "a36", "a37", "a38", "a39", "a40", "a41", "a42", "a43", "a44", "a45", "a46", "a47", "a48", "a49", "a50", "a51", "a52", "a53", "a54", "a55", "a56", "a57", "a58", "a59", "a60", "a61", "a62", "a63", "a64", "a65", "a66", "a67", "a68", "a69", "a70", "a71", "a72", "a73", "a74", "a75", "a76", "a77", "a78", "a79", "a80", "a81", "a82", "a83", "a84", "a85", "a86", "a87", "a88", "a89", "a90", "a91", "a92", "a93", "a94", "a95", "a96", "a97", "a98", "a99", "a100", "a101", "a102", "a103", "a104", "a105", "a106", "a107", "a108", "a109", "a110", "a111", "a112", "a113", "a114", "a115", "a116", "a117", "a118", "a119", "a120", "a121", "a122", "a123", "a124", "a125", "a126", "a127", "a128", "a129", "a130", "a131", "a132", "a133", "a134", "a135", "a136", "a137", "a138", "a139", "a140", "a141", "a142", "a143", "a144", "a145", "a146", "a147", "a148", "a149", "a150", "a151", "a152", "a153", "a154", "a155", "a156", "a157", "a158", "a159", "a160", "a161", "a162", "a163", "a164", "a165", "a166", "a167", "a168", "a169", "a170", "a171", "a172", "a173", "a174", "a175", "a176", "a177", "a178", "a179", "a180", "a181", "a182", "a183", "a184", "a185", "a186", "a187", "a188", "a189", "a190", "a191", "a192", "a193", "a194", "a195", "a196", "a197", "a198", "a199", "a200", "a201", "a202", "a203", "a204", "a205", "a206", "a207", "a208", "a209", "a210", "a211", "a212", "a213", "a214", "a215", "a216", "a217", "a218", "a219", "a220", "a221", "a222", "a223", "a224", "a225", "a226", "a227", "a228", "a229", "a230", "a231", "a232", "a233", "a234", "a235", "a236", "a237", "a238", "a239", "a240", "a241", "a242", "a243", "a244", "a245", "a246", "a247", "a248", "a249", "a250", "a251", "a252", "a253", "a254", "a255", "v224", "v225", "v226", "v227", "v228", "v229", "v230", "v231", "v232", "v233", "v234", "v235", "v236", "v237", "v238", "v239", "v240", "v241", "v242", "v243", "v244", "v245", "v246", "v247", "v248", "v249", "v250", "v251", "v252", "v253", "v254", "v255"
#define BOOT_IN "{s67}"(L), [ctab] "i"(CTAB_OFF), [rm] "i"(SRING == 12 ? 12 : 0), [trm] "i"(TRING - 1), [trn] "i"(1 - TRING)
#define S_CLOB "s80", "s81", "s76", "s78", "s79", "s86", "s88", "s89", "s90", "s91", "s46", "s47", "s48", "s49", "s50", "s51", "s52", "s53", "s54", "s55", "s56", "s57", "s58", "s59", "s60", "s61", "s62", "s63", "s68", "s69", "s70", "s71", "s72", "s73", "s82", "s83", "s84", "m0", "vcc", "scc", "memory"
typedef unsigned v8w __attribute__((ext_vector_type(8)));

__device__ __forceinline__ void qkv_role() {
  TOK_COMMON();
  const int role = i * 4 + w, hh = 8 * xq + role / 6, part = role % 6, which = part >> 1, half = part & 1;
  const rsrc_t WR = mkr((which == 0 ? p.Wq : (which == 1 ? p.Wk : p.Wv)) + hh * DH + 64 * half, DM * HD * 2u - (unsigned)(hh * DH + 64 * half) * 2u);
  const int m = lane & 15, g = lane >> 4;
  const unsigned a0 = (unsigned)(m * 128 + ((g ^ (m & 7)) << 4));
  v8w lv = {(unsigned)lane * 16u, a0, a0 ^ 64u, (unsigned)(lane & (L ? 15 : 3)) * 128u, 0u, 40960u + 4u * (unsigned)w, 1u, 0u};
  const unsigned c36 = P_H * PAIR + (unsigned)w * 1024u, c37 = (unsigned)(3 * hh + which) * PAIR + (unsigned)half * (which == 2 ? 4096u : 2048u);
  const unsigned c38 = P_FA * PAIR + (unsigned)(hh * 128 + part * 16), c39 = w == 0 ? P_FB * PAIR + 4096u : 0xF0000000u, c41 = (unsigned)w * 1024u;
  const int c42 = L ? 1 - QLEAD1 : 1 - QLEAD;
  unsigned d74 = TRC_OFF + 262144u + (unsigned)((c * 4 + w) * 66) * 8u;
#define Q_CALL(A) asm volatile("s_waitcnt vmcnt(0) lgkmcnt(0)\n\t" A : "+{v[0:7]}"(lv), "+{s74}"(d74) \
               : [X] "s"(X), "{s36}"(c36), "{s37}"(c37), "{s38}"(c38), "{s39}"(c39), "{s40}"(w), "{s41}"(c41), "{s42}"(c42), "{s45}"(i), "{s92}"(TMO_OFF), BOOT_IN, [cap] "i"(SPIN_CAP), [qxw] "i"(QXW), [qxc] "i"(QXC), W_IN : VQ_CLOB, W_CLOB, S_CLOB)
  if (which == 2) { Q_CALL(QV_ASM "LQVEND:"); __builtin_amdgcn_endpgm(); }
  Q_CALL(QK_ASM "LQKEND:");
  __builtin_amdgcn_endpgm();
}

__device__ __forceinline__ void ap_role() {
  TOK_COMMON();
  const int hp = 4 * xq + ((i - 12) >> 1), ch = i & 1, a = w >> 1, rt = w & 1, h = 2 * hp + a;
  const int n = lane & 15, g = lane >> 4;
  const rsrc_t WR = mkr(p.Wo + (long)hp * 2 * DH * DM, 2u * DH * DM * 2u);
  const unsigned l16 = (unsigned)lane * 16u, ab = (unsigned)a * 24576u + l16;
  v16u lv = {l16, ab, ab + (unsigned)rt * 4096u, 49152u + (unsigned)w * 4096u + l16, 49152u + (unsigned)(w ^ 2) * 4096u + l16, l16 & ~32u, l16 >> 1,
             (unsigned)(g * 262144 + n * 16), (unsigned)(n * 32 + g * 8), (unsigned)((lane & 7) % 6) * 16u, 0u, 0u, 0u, 0u, 0u, 0u};
  v8w mk;
  const int qi = 8 * (n >> 2) + (n & 3) + 4 * rt;
#pragma unroll
  for (int k = 0; k < 8; ++k) mk[k] = 8 * g + k > qi ? 0xFF800000u : 0u;
  v16u zz = {0u, 0u, 0u, 0u, 0u, 0u, 0u, 0u, 0u, 0u, 0u, 0u, 0u, 0u, 0u, 0u};
  const unsigned tb = (unsigned)(3 * h) * PAIR;
  const unsigned c36 = tb + (rt ? PAIR + 4096u : 0u), c37 = tb + (rt ? 2u * PAIR : 4096u), c38 = tb + (rt ? 2u * PAIR + 4096u : PAIR);
  const unsigned c39 = (P_KV + (unsigned)(h >> 1)) * PAIR + (unsigned)(h & 1) * 4096u, c41 = P_FA * PAIR + (unsigned)h * 128u;
  const unsigned c42 = w == 0 ? P_FB * PAIR + (unsigned)(2 * hp + ch) * 128u : 0xF0000000u;
  const unsigned c43 = TP0 * XBLK + (unsigned)(8 * rt + (hp >> 3)) * PAIR + (unsigned)((hp & 7) * 1024 + ch * 512 + a * 256);
  unsigned d44 = (TP0 + 8u) * XBLK + (unsigned)(hp >> 1) * PAIR + (unsigned)((((hp & 1) * 4 + 2 * ch + a) * 2 + rt) * 512);
  const unsigned c45 = (unsigned)(a * 24576 + rt * 12288);
  unsigned d74 = TRC_OFF + 262144u + (unsigned)((c * 4 + w) * 66) * 8u;
  asm volatile("s_waitcnt vmcnt(0) lgkmcnt(0)\n\t" AP_ASM "LAPEND:" : "+{v[0:15]}"(lv), "+{v[24:39]}"(zz), "+{s74}"(d74), "+{s44}"(d44)
               : [X] "s"(X), "{s36}"(c36), "{s37}"(c37), "{s38}"(c38), "{s39}"(c39), "{s41}"(c41), "{s42}"(c42), "{s43}"(c43), "{s45}"(c45), "{s64}"(512 * ch + 256 * a), "{s65}"(1024 + 64 * ch + 32 * a), "{s66}"(147456 * a), "{s77}"(L ? AX1 : AX), BOOT_IN,
                 "{s92}"(TMO_OFF), [cap] "i"(SPIN_CAP), "{v[16:23]}"(mk), W_IN : VA_CLOB, W_CLOB, S_CLOB);
  __builtin_amdgcn_endpgm();
}

template <int N> __device__ __forceinline__ float rorN(float v) { return __uint_as_float((unsigned)__builtin_amdgcn_update_dpp(0, (int)__float_as_uint(v), 0x120 + N, 0xf, 0xf, false)); }
__device__ __forceinline__ void up8(v4u v, float* x) {
  x[0] = lo2f(v.x); x[1] = hi2f(v.x); x[2] = lo2f(v.y); x[3] = hi2f(v.y); x[4] = lo2f(v.z); x[5] = hi2f(v.z); x[6] = lo2f(v.w); x[7] = hi2f(v.w);
}
__device__ __forceinline__ v4u pk8(const float* x) { return (v4u){pk2(x[0], x[1]), pk2(x[2], x[3]), pk2(x[4], x[5]), pk2(x[6], x[7])}; }

__device__ __forceinline__ void rb9(float (&x)[9]) {
#pragma unroll
  for (int i = 0; i < 8; i += 2) { const unsigned u = pk2(x[i], x[i + 1]); x[i] = lo2f(u); x[i + 1] = hi2f(u); }
  x[8] = lo2f(pk2(x[8], x[8]));
}
template <int R>
__device__ __forceinline__ void lnR(const float (&x)[R][9], float (&o)[R][9]) {
  float s[2 * R], pv[R];
#pragma unroll
  for (int r = 0; r < R; ++r) {
    pv[r] = __uint_as_float(__builtin_amdgcn_readfirstlane(__float_as_uint(x[r][0])));
    const f32x2 p2 = {pv[r], pv[r]};
    f32x2 d[4];
#pragma unroll
    for (int i = 0; i < 4; ++i) d[i] = (f32x2){x[r][2 * i], x[r][2 * i + 1]} - p2;
    const float d8 = x[r][8] - pv[r];
    const f32x2 a = (d[0] + d[1]) + (d[2] + d[3]);
    const f32x2 q = __builtin_elementwise_fma(d[3], d[3], __builtin_elementwise_fma(d[2], d[2], __builtin_elementwise_fma(d[1], d[1], d[0] * d[0])));
    s[2 * r] = (a[0] + d8) + a[1];
    s[2 * r + 1] = __builtin_fmaf(d8, d8, q[0]) + q[1];
  }
#pragma unroll
  for (int k = 0; k < 2 * R; ++k) s[k] += rorN<8>(s[k]);
#pragma unroll
  for (int k = 0; k < 2 * R; ++k) s[k] += rorN<4>(s[k]);
#pragma unroll
  for (int k = 0; k < 2 * R; ++k) s[k] += rorN<2>(s[k]);
#pragma unroll
  for (int k = 0; k < 2 * R; ++k) s[k] += rorN<1>(s[k]);
#pragma unroll
  for (int r = 0; r < R; ++r) {
    auto u = __builtin_amdgcn_permlane16_swap(__float_as_uint(s[2 * r]), __float_as_uint(s[2 * r + 1]), false, false);
    s[2 * r] = __uint_as_float(u[0]) + __uint_as_float(u[1]);
  }
#pragma unroll
  for (int r = 0; r < R; ++r) {
    const unsigned u = __float_as_uint(s[2 * r]);
    const float md = (__uint_as_float(__builtin_amdgcn_readlane(u, 0)) + __uint_as_float(__builtin_amdgcn_readlane(u, 32))) * (1.0f / DM);
    const float sq = (__uint_as_float(__builtin_amdgcn_readlane(u, 16)) + __uint_as_float(__builtin_amdgcn_readlane(u, 48))) * (1.0f / DM);
    const unsigned mv = pk2(pv[r] + md, __builtin_fmaxf(__builtin_fmaf(-md, md, sq), 0.f));
    const float m = lo2f(mv), var = hi2f(mv);
    const float ve = lo2f(pk2(var + 1e-5f, 0.f));
    const float den = lo2f(pk2(__builtin_amdgcn_sqrtf(ve), 0.f));
    const float inv = __builtin_amdgcn_rcpf(den);
#pragma unroll
    for (int i = 0; i < 9; ++i) o[r][i] = (x[r][i] - m) * inv;
  }
}
__device__ __forceinline__ void ln9(const float (&x)[9], float (&o)[9]) { lnR<1>((const float (&)[1][9])x, (float (&)[1][9])o); }
__device__ __forceinline__ v4u gat8(float v) {
  const unsigned d = pk2(v, __uint_as_float((unsigned)__builtin_amdgcn_mov_dpp((int)__float_as_uint(v), 0xB1, 0xf, 0xf, true)));
  return (v4u){d, (unsigned)__builtin_amdgcn_mov_dpp((int)d, 0x102, 0xf, 0xf, true), (unsigned)__builtin_amdgcn_mov_dpp((int)d, 0x104, 0xf, 0xf, true),
               (unsigned)__builtin_amdgcn_mov_dpp((int)d, 0x106, 0xf, 0xf, true)};
}
__device__ __forceinline__ void tok_row(const MP& p, const rsrc_t X, unsigned sb1, int L, int s, int r, int lane, const v4u (&v)[18], v4u px, v4u py, unsigned px9, unsigned py9) {
  const long tok = (long)s * SEG + r;
  const int m = lane & 15, g = lane >> 4;
  const int xp = 4 * (g & 1) + 2 * (g >> 1) + (m >> 3);
  const unsigned zr = opq(0u);
  const unsigned one = (g == (m >> 2)) ? (opq(0x3F80u) << (16 * (m & 1))) : zr;
  const unsigned ax = (m & 2) ? zr : one, ay = (m & 2) ? one : zr;
  const v4u A1 = {ax, ay, zr, zr}, A2 = {zr, zr, ax, ay};
  const unsigned on2 = opq(0x3F80u) << (16 * (m & 1));
  const int q2 = (m >> 1) & 3;
  const v4u sel = {q2 == 0 ? on2 : zr, q2 == 1 ? on2 : zr, q2 == 2 ? on2 : zr, q2 == 3 ? on2 : zr};
  f32x4 d1, d2, e1;
  asm volatile("s_nop 1\n\t" MF "%0, %1, %3, 0\n\t" MF "%0, %2, %3, %0" : "=&v"(e1) : "v"(v[0]), "v"(v[1]), "v"(sel));
#pragma unroll
  for (int i = 0; i < 16; ++i) {
    if (i == 0) asm volatile("s_nop 1\n\t" MF "%0, %2, %4, 0\n\t" MF "%1, %3, %4, 0" : "=&v"(d1), "=&v"(d2) : "v"(A1), "v"(A2), "v"(v[2]));
    else asm volatile(MF "%0, %2, %4, %0\n\t" MF "%1, %3, %4, %1" : "+v"(d1), "+v"(d2) : "v"(A1), "v"(A2), "v"(v[2 + i]));
  }
  asm volatile("s_nop 7" : "+v"(d1), "+v"(d2), "+v"(e1));
  float o[9];
#pragma unroll
  for (int i = 0; i < 4; ++i) { o[i] = d1[i]; o[4 + i] = d2[i]; }
  {
    auto a02 = __builtin_amdgcn_permlane32_swap(__float_as_uint(e1[0]), __float_as_uint(e1[2]), false, false);
    auto a13 = __builtin_amdgcn_permlane32_swap(__float_as_uint(e1[1]), __float_as_uint(e1[3]), false, false);
    o[8] = m >= 8 ? __uint_as_float(a13[0]) + __uint_as_float(a13[1]) : __uint_as_float(a02[0]) + __uint_as_float(a02[1]);
  }
  float x[9];
  up8(px, x); x[8] = lo2f(px9);
#pragma unroll
  for (int i = 0; i < 9; ++i) x[i] += o[i];
  if (L == 0) {
    float y[9], ln[9];
    up8(py, y); y[8] = lo2f(py9);
    rb9(x);
    ln9(x, ln);
#pragma unroll
    for (int i = 0; i < 9; ++i) x[i] = __builtin_fmaf(ln[i], y[i], x[i]);
    rb9(x);
    st16(X, (unsigned)lane * 16u, sb1 + (P_X3 + (unsigned)(r >> 3)) * PAIR + (unsigned)(r & 7) * 1024u, pk8(x));
    const v4u x9 = gat8(x[8]);
    if ((lane & 7) == 0) st16(X, (unsigned)xp * 16u, sb1 + (P_H + 4u) * PAIR + 4096u + (unsigned)r * 128u, x9);
    ln9(x, ln);
    st16(X, hoff(r, lane), sb1, pk8(ln));
    const v4u h9 = gat8(ln[8]);
    if ((lane & 7) == 0) st16(X, hoff(r, 64 + xp), sb1, h9);
  } else {
    const rsrc_t O = mkr(p.out + tok * DM, DM * 2u);
    __builtin_amdgcn_raw_buffer_store_b128(pk8(x), O, (unsigned)lane * 16u, 0u, OAUX);
    __builtin_amdgcn_raw_buffer_store_b128(gat8(x[8]), O, (lane & 7) == 0 ? 1024u + (unsigned)xp * 16u : 0xF0000000u, 0u, OAUX);
  }
}

__device__ __forceinline__ void row_role(char* smem) {
  TOK_COMMON();
  const int rw = 4 * xq + (i - 20), r = 2 * rw + (w >> 1);
  const unsigned long long sm = die_mask(X, SG0, L, lane);
  const unsigned tm = (unsigned)die_mask(X, TP0, L, lane), dm = (unsigned)die_mask(X, 0u, L, lane);
  __attribute__((address_space(3))) unsigned* cnt = (__attribute__((address_space(3))) unsigned*)smem;
  BAR();
  if (tid < 64) cnt[tid] = 0u;
  BAR();
  for (int s = w & 1; s < NSEG; s += 2) {
    int ln_ = lane; asm volatile("" : "+v"(ln_));
    const int m = ln_ & 15, g = ln_ >> 4;
    const unsigned l16 = (unsigned)ln_ * 16u, xo = (unsigned)(8 * (4 * (g & 1) + 2 * (g >> 1) + (m >> 3)) + (m & 7)) * 2u;
    const unsigned sb = SEGB(sm, s), sb1 = sb ^ (L ? 0u : 8192u);
    if (s + ARMK < NSEG) {
      const unsigned sv = opq(SENT), sl = SLOT_AT((s + ARMK) % SRING) + ((dm >> ((s + ARMK) % SRING / 3)) & 1u) * 8192u;
      for (int j = r; j < 84; j += 32) {
#pragma unroll
        for (int k = 0; k < 4; ++k) st16(X, l16 + (unsigned)k * 1024u, sl + (unsigned)(j >> 1) * PAIR + (unsigned)(j & 1) * 4096u, (v4u){sv, sv, sv, sv});
      }
    }
    wait_all<PSL>(X, 0u, sb + P_FB * PAIR + 4096u + (unsigned)(L ? rw : r >> 3) * 128u);
    const v4u px = ld16(X, l16, sb + (P_X3 + (unsigned)(r >> 3)) * PAIR + (unsigned)(r & 7) * 1024u);
    const unsigned px9 = __builtin_amdgcn_raw_buffer_load_b16(X, xo, sb + (P_H + 4u) * PAIR + 4096u + (unsigned)r * 128u, 16);
    const rsrc_t YX = mkr(p.yT + (2 * s + (r >> 4)) * 9216, L ? 0u : 18432u);
    const v2u ya = ld8(YX, (unsigned)(ln_ * 256 + ((r ^ m) & 15) * 8), 0u), yb = ld8(YX, (unsigned)(ln_ * 256 + 128 + ((r ^ m) & 15) * 8), 0u);
    const v4u py = {ya.x, ya.y, yb.x, yb.y};
    const unsigned py9 = __builtin_amdgcn_raw_buffer_load_b16(YX, 16384u + (xo >> 3) * 128u + (((unsigned)r ^ (xo >> 4)) & 15u) * 8u + (xo & 6u), 0u, 16);
    if (s < NSEG - 1) {
      wait_all<PSP>(X, 0u, sb + P_FB * PAIR + (unsigned)r * 128u);
      __builtin_amdgcn_s_sleep(RONE);
    } else {
      wait_all<PSP>(X, 0u, sb + P_FA * PAIR + 4096u + (unsigned)r * 128u);
      wait_all<1>(X, (unsigned)(ln_ & 31) * 128u, sb + P_FB * PAIR);
    }
    if (s < NSEG - 1) wait_all<PSP>(X, (unsigned)(ln_ & 31) * 128u, sb + P_FB * PAIR);
    const unsigned j = (unsigned)s & 15u, to = TP0 * XBLK + (j << 20) + ((tm >> (j >> 1)) & 1u) * 8192u + (unsigned)r * 2u * PAIR;
    const unsigned t9 = (TP0 + 8u) * XBLK + ((tm >> 8) & 1u) * 8192u + (j << 17) + (unsigned)(((r >> 2) & 1) * 512 + (4 * (r >> 3) + (r & 3)) * 32);
    const unsigned v9 = (unsigned)(g * (int)PAIR + (m >> 1) * 1024 + (m & 1) * 16);
    v4u v[18];
#pragma unroll
    for (int k = 0; k < 2; ++k) v[k] = ld16(X, v9, t9 + (unsigned)k * 4u * PAIR);
#pragma unroll
    for (int k = 0; k < 16; ++k) v[2 + k] = ld16(X, l16 + (unsigned)(k & 3) * 1024u, to + (unsigned)(k >> 3) * PAIR + (unsigned)(k & 4) * 1024u);
    SB();
    tok_row(p, X, sb1, L, s, r, ln_, v, px, py, px9, py9);
    VM0();
    unsigned o1 = 0u;
    if (ln_ == 0) o1 = __hip_atomic_fetch_add(cnt + s, 1u, __ATOMIC_RELAXED, __HIP_MEMORY_SCOPE_WORKGROUP);
    if (__builtin_amdgcn_readfirstlane(o1) == 1u && ln_ < 2 - L) st4(X, ln_ ? sb1 : sb + 2048u, P_FB * PAIR + 4096u + (unsigned)rw * 128u, opq(1u));
  }
}

__device__ __forceinline__ void rows_task(const MP& p, const rsrc_t X, unsigned long long sm, u16* ys, int grp, int ng) {
  const int tid = threadIdx.x, lane = tid & 63, w = __builtin_amdgcn_readfirstlane(tid >> 6);
  const int m = lane & 15, g = lane >> 4;
  const int xp = 4 * (g & 1) + 2 * (g >> 1) + (m >> 3), c9 = 512 + 8 * xp + (m & 7);
  const unsigned sb = SEGB(sm, grp >> 2);
  v4u yv[5], xa[4];
  unsigned x9[4];
#pragma unroll
  for (int i = 0; i < 5; ++i) yv[i] = ld16(mkr(p.yT + (grp >> 1) * 9216, 18432u), (unsigned)(tid + 256 * i) * 16u, 0u);
#pragma unroll
  for (int rr = 0; rr < 4; ++rr) {
    const long t = grp * 8 + w + 4 * (rr & (2 * ng - 1));
    xa[rr] = *(const v4u*)&p.xin[t * DM + 8 * lane]; x9[rr] = p.xin[t * DM + c9];
  }
#pragma unroll
  for (int i = 0; i < 5; ++i) ((v4u*)ys)[tid + 256 * i] = yv[i];
  BAR();
  const unsigned o9 = (lane & 7) == 0 ? 0u : 0xF0000000u;
#pragma unroll 1
  for (int k = 0; k < ng; ++k) {
    float x[2][9], y[2][9], ln[2][9];
    const int t0 = (grp + k) * 8 + w;
#pragma unroll
    for (int rr = 0; rr < 2; ++rr) {
      const int j = (t0 + 4 * rr) & 15;
      const v2u ya = *(const v2u*)&ys[lane * 128 + (j ^ m) * 4], yb = *(const v2u*)&ys[lane * 128 + 64 + (j ^ m) * 4];
      up8(xa[rr], x[rr]); x[rr][8] = lo2f(x9[rr]); up8((v4u){ya.x, ya.y, yb.x, yb.y}, y[rr]); y[rr][8] = bf2f(ys[(c9 >> 2) * 64 + (j ^ xp) * 4 + (m & 3)]);
      xa[rr] = xa[2 + rr]; x9[rr] = x9[2 + rr];
    }
    lnR<2>(x, ln);
#pragma unroll
    for (int rr = 0; rr < 2; ++rr) {
#pragma unroll
      for (int i = 0; i < 9; ++i) x[rr][i] = __builtin_fmaf(ln[rr][i], y[rr][i], x[rr][i]);
      rb9(x[rr]);
      const int r = (t0 + 4 * rr) & 31;
      st16(X, (unsigned)lane * 16u, sb + (P_X3 + (unsigned)(r >> 3)) * PAIR + (unsigned)(r & 7) * 1024u, pk8(x[rr]));
      st16(X, (unsigned)xp * 16u + o9, sb + (P_H + 4u) * PAIR + 4096u + (unsigned)r * 128u, gat8(x[rr][8]));
    }
    lnR<2>(x, ln);
#pragma unroll
    for (int rr = 0; rr < 2; ++rr) {
      const int r = (t0 + 4 * rr) & 31;
      st16(X, hoff(r, lane), sb, pk8(ln[rr]));
      st16(X, hoff(r, 64 + xp) + o9, sb, gat8(ln[rr][8]));
    }
  }
  VM0();
  BAR();
  if (tid < ng) st4(X, (unsigned)tid * 128u, sb + P_FB * PAIR + 4096u + (unsigned)(grp & 3) * 128u, opq(1u));
}

__device__ __forceinline__ void arrive(const MP& p, int k, int grp) {
  VM0();
  BAR();
  if (threadIdx.x < 64) {
    unsigned old = 0u;
    if (threadIdx.x == 0) old = __hip_atomic_fetch_add(p.xb + MF_OFF / 4u + (k * 16 + grp) * 32, 1u, __ATOMIC_RELAXED, __HIP_MEMORY_SCOPE_AGENT);
    if (__builtin_amdgcn_readfirstlane(old) == 15u && threadIdx.x < 8) st4(mkr(p.xb, XB_BYTES), threadIdx.x * 128u, RDY_OFF(k) + (unsigned)grp * 1024u, opq(1u));
  }
}
__device__ __forceinline__ void wait_ready(const MP& p, int k) {
  if (threadIdx.x < 64) wait_all<PSL>(mkr(p.xb, XB_BYTES), (threadIdx.x % 9u) * 1024u, RDY_OFF(k) + (blockIdx.x & 7u) * 128u);
  BAR();
}
__device__ __forceinline__ void start_work(char* smem, int wk) {
  const MP p = getp();
  const int w = __builtin_amdgcn_readfirstlane(threadIdx.x >> 6);
#define NARM (ARMK - 3u)
  unsigned pf[PFM + 1];
  conv_task(p.kvT, p.ssm_k, p.ssm_D, p.yT, (u16*)(smem + w * 32768), 4 * wk + w, [&]() {
#pragma unroll
    for (int i = 0; i < PFM; ++i) {
      const int z = (i - 1) * 36864 + wk * 256 + threadIdx.x, a = z % 3;
      pf[i] = *(const unsigned*)(i == 0 ? p.Wo + (long)(z + 36864) * 64 : (a == 0 ? p.Wq : (a == 1 ? p.Wk : p.Wv)) + (long)(36863 - z / 3) * 64);
    }
  }, [&]() {
    const unsigned sv = opq(SENT);
    constexpr unsigned S16 = SLOT / 16u;
#pragma unroll
    for (unsigned k = 0; k < (NARM * S16 + 32767u) / 32768u; ++k) {
      const unsigned i = k * 32768u + wk * 256 + threadIdx.x;
      st16(mkr(p.xb, XB_BYTES), i < NARM * S16 && wk < 128 ? SLOT_AT(3u + i / S16) + (i % S16) * 16u : 0xF0000000u, 0u, (v4u){sv, sv, sv, sv});
    }
  });
#pragma unroll
  for (int i = 0; i < PFM; ++i) asm volatile("" :: "v"(pf[i]));
  arrive(p, 0, wk >> 4);
}
__device__ __forceinline__ void token_start(char* smem) {
  TOK_COMMON();
  (void)w;
  const int k = 4 * i + xq;
  if (L || (i >= 12 && i < 16)) start_work(smem, L ? 32 + k : 80 + k);
  if (i >= (L ? 12 : 20)) {
    const unsigned long long sm = die_mask(X, SG0, 0, lane);
    int g = L ? 111 - k : k - 96;
    while (g < 120) {
      if (g >= 0 && g < 64) {
        wait_ready(p, 0);
      }
      rows_task(p, X, sm, (u16*)smem, g < 0 ? g + 16 : 16 + 2 * g, g < 0 ? 1 : 2);
      g += g < 0 ? 16 : (L ? 48 : 120);
    }
  }
}
__device__ __forceinline__ void token_wg(char* smem) {
  if ((unsigned)(size_t)(__attribute__((address_space(3))) char*)smem != 0u) return;
  token_start(smem);
  const int i = ((int)blockIdx.x - NCHW) >> 3;
  if (i < 12) qkv_role();
  else if (i < 20) ap_role();
  else row_role(smem);
}

__global__ __launch_bounds__(256, 1) void k_main(MP) {
  extern __shared__ __attribute__((aligned(16))) char smem[];
  asm volatile("LKBEG:");
  if ((int)blockIdx.x < NCHW) chain_wg(smem);
  else token_wg(smem);
}

extern "C" void kernel_launch(void* const* d_in, const int* in_sizes, int n_in, void* d_out, int out_size, void* d_ws,
                              size_t ws_size, hipStream_t stream) {
  const u16* A_q = (const u16*)d_in[0];
  const u16* A_k = (const u16*)d_in[1];
  const u16* A_v = (const u16*)d_in[2];
  const u16* A_shift = (const u16*)d_in[3];
  const u16* A_ssmk = (const u16*)d_in[4];
  const u16* A_ssmD = (const u16*)d_in[5];
  const u16* B_mem0 = (const u16*)d_in[7];
  const u16* B_Wq = (const u16*)d_in[8];
  const u16* B_Wk = (const u16*)d_in[9];
  const u16* B_Wv = (const u16*)d_in[10];
  const u16* B_Wo = (const u16*)d_in[11];

  char* ws = (char*)d_ws;
  size_t off = (size_t)(-(uintptr_t)ws) & (XBLK - 1u);
  auto alloc = [&](size_t bytes) { char* q = ws + off; off += (bytes + 4095) & ~(size_t)4095; return q; };
  unsigned* xb = (unsigned*)alloc((size_t)XB_BYTES);
  u16* kvT = (u16*)alloc((size_t)DM * S_LEN * 2);
  u16* yT = (u16*)alloc((size_t)DM * S_LEN * 2);

  PreP pp;
  pp.kk = A_k; pp.vv = A_v; pp.shift_k = A_shift; pp.ssm_k = A_ssmk; pp.ssm_D = A_ssmD; pp.yT = yT; pp.kvT = kvT;
  pp.Wq = B_Wq; pp.Wk = B_Wk; pp.Wv = B_Wv; pp.Wo = B_Wo; pp.xb = xb; pp.mem0 = B_mem0;
  k_pre<<<240 + PRE_FILL + 256 + 288, 256, 0, stream>>>(pp);
  MP mp;
  mp.out = (u16*)d_out; mp.xb = xb;
  mp.xin = A_q; mp.yT = yT; mp.kvT = kvT; mp.ssm_k = A_ssmk; mp.ssm_D = A_ssmD;
  mp.Wq = B_Wq; mp.Wk = B_Wk; mp.Wv = B_Wv; mp.Wo = B_Wo;
  const int smem_bytes = 160 * 1024;
  (void)hipFuncSetAttribute((const void*)k_main, hipFuncAttributeMaxDynamicSharedMemorySize, smem_bytes);
  k_main<<<NCHW + NTOKW, 256, smem_bytes, stream>>>(mp);
}
```

```cpp
#include <hip/hip_runtime.h>
#include <stdint.h>

typedef unsigned short u16;
using bf16x8 = __attribute__((ext_vector_type(8))) short;
using f32x4 = __attribute__((ext_vector_type(4))) float;
using f32x2 = __attribute__((ext_vector_type(2))) float;
using bfx8 = __attribute__((ext_vector_type(8))) __bf16;
using bfx2 = __attribute__((ext_vector_type(2))) __bf16;
typedef unsigned v4u __attribute__((ext_vector_type(4)));
typedef unsigned v2u __attribute__((ext_vector_type(2)));
typedef short v4s __attribute__((ext_vector_type(4)));

#define S_LEN 2048
#define DM 576
#define NH 32
#define DH 128
#define HD (NH * DH)
#define SEG 32
#define NSEG 64

__device__ __forceinline__ float bf2f(u16 x) { return __uint_as_float(((unsigned)x) << 16); }
__device__ __forceinline__ unsigned pk2(float a, float b) { f32x2 v = {a, b}; bfx2 r = __builtin_convertvector(v, bfx2); return __builtin_bit_cast(unsigned, r); }
__device__ __forceinline__ float lo2f(unsigned u) { return __uint_as_float(u << 16); }
__device__ __forceinline__ float hi2f(unsigned u) { return __uint_as_float(u & 0xFFFF0000u); }

#define NFC 128
#define XBLK 2097152u
#define PAIR 16384u
#define SLOT 688128u
#define AGO 589824u
#define AGT 655360u
#define SG0 22
#define TP0 86
#define NXB 95
#define XB_BYTES (NXB * XBLK)
#define SLOT_AT(i) ((unsigned)((i) / 3) * XBLK + (unsigned)((i) % 3) * SLOT)
#define HT_OFF SLOT_AT(65)
#define CTAB_OFF (HT_OFF + 16384u)
#define MF_OFF (HT_OFF + 20480u)
#define TMO_OFF (MF_OFF + 15u * 128u)
#define RDY_OFF(k) (MF_OFF + 4096u + (k) * 10240u)
#define MF_BYTES 16384u
#define TRC_OFF SLOT_AT(64)
#define CAL_OFF (XBLK - 16384u)
#define P_KV 96u
#define P_H 112u
#define P_X3 117u
#define P_FA 121u
#define P_FB 122u
#define ARMK 8
#define NCAL 8
#define CALT 560u
#define CALM 1
#define SRING 12
#define TRING 64
__device__ __forceinline__ unsigned kv2(const unsigned (&u)[4], unsigned v, v2u w0, v2u w1) {
  const float a[4] = {hi2f(w0.y), lo2f(w0.y), hi2f(w0.x), lo2f(w0.x)}, b[4] = {hi2f(w1.y), lo2f(w1.y), hi2f(w1.x), lo2f(w1.x)};
  unsigned r = pk2(lo2f(u[0]) * a[0], hi2f(u[0]) * b[0]);
#pragma unroll
  for (int i = 1; i < 4; ++i) { const unsigned q = pk2(lo2f(u[i]) * a[i], hi2f(u[i]) * b[i]); r = pk2(lo2f(r) + lo2f(q), hi2f(r) + hi2f(q)); }
  return pk2(lo2f(r) * lo2f(v), hi2f(r) * hi2f(v));
}
struct PreP {
  const u16* kk; const u16* vv; const u16* shift_k; const u16* ssm_k; const u16* ssm_D; u16* yT; u16* kvT;
  const u16* Wq; const u16* Wk; const u16* Wv; const u16* Wo; unsigned* xb; const u16* mem0;
};
#define PRE_FILL 112
#define PFN 2
__global__ __launch_bounds__(256, 4) void k_pre(PreP p) {
  __shared__ __attribute__((aligned(16))) float sh[64 * 36];
  const int tid = threadIdx.x, b0 = blockIdx.x, b = b0 < 240 ? b0 : (b0 < 528 ? b0 + PRE_FILL + 256 : b0 - 288);
  const __amdgpu_buffer_rsrc_t rx = __builtin_amdgcn_make_buffer_rsrc((void*)p.xb, 0, XB_BYTES, 0x00020000);
  const v4u ones = {0xFFFFFFFFu, 0xFFFFFFFFu, 0xFFFFFFFFu, 0xFFFFFFFFu}, zero = {0, 0, 0, 0};
  if (b < 96) {
    if (tid >= 64) return;
    const unsigned cb = (unsigned)b * XBLK + CAL_OFF;
    __builtin_amdgcn_raw_buffer_store_b128(zero, rx, (unsigned)((tid >> 3) & 1) * 8192u + (unsigned)(tid >> 4) * 256u + (unsigned)(tid & 7) * 16u, cb, 16);
    asm volatile("s_waitcnt vmcnt(0)" ::: "memory");
    unsigned mn[2] = {~0u, ~0u};
    for (int i = 0; i < NCAL; ++i) {
      const unsigned long long c0 = __builtin_amdgcn_s_memtime();
      asm volatile("s_waitcnt lgkmcnt(0)" ::: "memory");
      const unsigned v = __builtin_amdgcn_raw_buffer_load_b32(rx, 0, cb + (unsigned)(i & 1) * 8192u + (unsigned)(i >> 1) * 256u, 16);
      asm volatile("s_waitcnt vmcnt(0)" :: "v"(v) : "memory");
      const unsigned d = (unsigned)(__builtin_amdgcn_s_memtime() - c0);
      if (i & 1) mn[1] = d < mn[1] ? d : mn[1]; else mn[0] = d < mn[0] ? d : mn[0];
      const unsigned lo = mn[0] < mn[1] ? mn[0] : mn[1], hi = mn[0] < mn[1] ? mn[1] : mn[0];
      if (i >= CALM && lo < CALT && hi > lo + 100u) break;
    }
    unsigned xcc; asm volatile("s_getreg_b32 %0, hwreg(HW_REG_XCC_ID)" : "=s"(xcc));
    const unsigned res = mn[1] < mn[0] ? 1u : 0u;
    if (tid == 0) __builtin_amdgcn_raw_buffer_store_b32(res ^ ((xcc >> 2) & 1u), rx, 0, CTAB_OFF + (unsigned)b * 4u, 16);
  } else if (b < 240) {
    const __amdgpu_buffer_rsrc_t rk = __builtin_amdgcn_make_buffer_rsrc((void*)p.kk, 0, S_LEN * DM * 2, 0x00020000);
    float (*kx)[132] = (float (*)[132])sh;
    float (*cf)[140] = (float (*)[140])(sh + 4 * 132);
    const int d0 = (b - 96) * 4, q = tid & 3, dd = (tid >> 2) & 3, t0 = (tid >> 4) * 8;
    const float Dv = bf2f(p.ssm_D[d0 + dd]);
    {
      const int s = tid >> 1, d = d0 + (tid & 1) * 2;
      unsigned u[4];
#pragma unroll
      for (int a = 0; a < 4; ++a) u[a] = __builtin_amdgcn_raw_buffer_load_b32(rk, (unsigned)((s - 3 + a) * DM + d) * 2u, 0, 0);
      const unsigned vr = *(const unsigned*)&p.vv[s * DM + d], c0 = *(const unsigned*)&p.ssm_k[(long)(d0 + (tid >> 6)) * S_LEN + (tid & 63) * 2];
      const v4u w = *(const v4u*)&p.shift_k[d * 4];
      const unsigned r = kv2(u, vr, (v2u){w.x, w.y}, (v2u){w.z, w.w});
      kx[(tid & 1) * 2][s] = lo2f(r); kx[(tid & 1) * 2 + 1][s] = hi2f(r);
      *(f32x2*)&cf[tid >> 6][8 + (tid & 63) * 2] = (f32x2){lo2f(c0), hi2f(c0)};
      if (tid < 32) cf[tid >> 3][tid & 7] = 0.f;
    }
    __syncthreads();
    float y[8];
#pragma unroll
    for (int i = 0; i < 8; ++i) y[i] = 0.f;
#pragma unroll 1
    for (int j = 8 * q; j <= t0; j += 32) {
      f32x4 kk[2], cw[4];
#pragma unroll
      for (int x = 0; x < 2; ++x) kk[x] = *(const f32x4*)&kx[dd][j + 4 * x];
#pragma unroll
      for (int x = 0; x < 4; ++x) cw[x] = *(const f32x4*)&cf[dd][t0 - j + 4 * x];
#pragma unroll
      for (int jj = 0; jj < 8; ++jj)
#pragma unroll
        for (int i = 0; i < 8; ++i) y[i] = __builtin_fmaf(cw[(8 + i - jj) >> 2][(8 + i - jj) & 3], kk[jj >> 2][jj & 3], y[i]);
    }
#pragma unroll
    for (int i = 0; i < 8; ++i) {
      y[i] += __uint_as_float((unsigned)__builtin_amdgcn_mov_dpp((int)__float_as_uint(y[i]), 0xB1, 0xf, 0xf, true));
      y[i] += __uint_as_float((unsigned)__builtin_amdgcn_mov_dpp((int)__float_as_uint(y[i]), 0x4E, 0xf, 0xf, true));
    }
    const float ya = q == 0 ? y[0] : (q == 1 ? y[2] : (q == 2 ? y[4] : y[6])), yb = q == 0 ? y[1] : (q == 1 ? y[3] : (q == 2 ? y[5] : y[7]));
    const int t = t0 + 2 * q, f = (d0 >> 3) & 15;
    const unsigned r0 = pk2(ya, yb), r1 = pk2(kx[dd][t] * Dv, kx[dd][t + 1] * Dv), o = pk2(lo2f(r0) + lo2f(r1), hi2f(r0) + hi2f(r1));
    u16* dst = p.yT + (t >> 4) * 9216 + (d0 >> 2) * 64 + dd;
    dst[((t & 15) ^ f) << 2] = (u16)(o & 0xFFFFu); dst[(((t + 1) & 15) ^ f) << 2] = (u16)(o >> 16);
  } else if (b < 240 + PRE_FILL) {
    const unsigned i0 = (unsigned)(b - 240) * 256u + tid;
    if (i0 < (SLOT - AGO) / 16u) {
      const unsigned o = AGO + i0 * 16u, s = o < AGT ? (i0 >> 11) * 4u + ((o >> 10) & 3u) : 8u, r = o & 1023u;
      const unsigned gm = s < 8u ? r >> 8 : (r >> 6) & 3u, m3 = (r >> 4) & 3u, ga = s < 8u ? (r >> 6) & 3u : r >> 8, row = 4u * (gm & 1u) + 2u * (gm >> 1) + (m3 & 1u), half = m3 >> 1;
      const unsigned* src = (const unsigned*)(p.mem0 + row * DM + 64u * s + 16u * ga + 2u * half);
      __builtin_amdgcn_raw_buffer_store_b128((v4u){src[0], src[2], src[4], src[6]}, rx, o, 0, 16);
    }
    for (unsigned i = i0; i < 131072u + MF_BYTES / 16u; i += PRE_FILL * 256)
      __builtin_amdgcn_raw_buffer_store_b128(zero, rx, i < 131072u ? (SG0 + (i >> 11)) * XBLK + P_FA * PAIR + (i & 2047u) * 16u : MF_OFF + (i & 2047u) * 16u, 0, 16);
    for (unsigned i = i0; i < 2u * SLOT / 16u; i += PRE_FILL * 256) __builtin_amdgcn_raw_buffer_store_b128(ones, rx, SLOT + i * 16u, 0, 16);
  } else if (b >= 240 + PRE_FILL + 256) {
    const __amdgpu_buffer_rsrc_t rk = __builtin_amdgcn_make_buffer_rsrc((void*)p.kk, 0, S_LEN * DM * 2, 0x00020000);
    u16 (*tile)[72] = (u16 (*)[72])sh;
    const int bid = b - 240 - PRE_FILL - 256, d0 = (bid % 9) * 64, s0 = (bid / 9) * 64;
    const int sr = (tid >> 3) * 2, d = d0 + (tid & 7) * 8;
    v4u kr[5], vr[2], w[4];
#pragma unroll
    for (int a = 0; a < 5; ++a) kr[a] = __builtin_amdgcn_raw_buffer_load_b128(rk, (unsigned)((s0 + sr - 3 + a) * DM + d) * 2u, 0, 0);
#pragma unroll
    for (int a = 0; a < 2; ++a) vr[a] = *(const v4u*)&p.vv[(long)(s0 + sr + a) * DM + d];
#pragma unroll
    for (int a = 0; a < 4; ++a) w[a] = *(const v4u*)&p.shift_k[(d + 2 * a) * 4];
    __builtin_amdgcn_sched_barrier(0);
#pragma unroll
    for (int it = 0; it < 2; ++it) {
      v4u o;
#pragma unroll
      for (int j = 0; j < 4; ++j) { const unsigned u[4] = {kr[it][j], kr[it + 1][j], kr[it + 2][j], kr[it + 3][j]}; o[j] = kv2(u, vr[it][j], (v2u){w[j].x, w[j].y}, (v2u){w[j].z, w[j].w}); }
      *(v4u*)&tile[sr + it][(tid & 7) * 8] = o;
    }
    __syncthreads();
    const int lane = tid & 63, wv = tid >> 6, g = lane >> 4, r = (lane >> 2) & 3, c = lane & 3;
    v2u o[4];
#pragma unroll
    for (int i = 0; i < 4; ++i) o[i] = __builtin_bit_cast(v2u, __builtin_amdgcn_ds_read_tr16_b64_v4i16((__attribute__((address_space(3))) v4s*)&tile[16 * g + 4 * i + r][16 * wv + 4 * c]));
    u16* dst = p.kvT + (long)(d0 + 16 * wv + (lane & 15)) * S_LEN + s0 + 16 * g;
    *(v4u*)dst = (v4u){o[0].x, o[0].y, o[1].x, o[1].y};
    *(v4u*)(dst + 8) = (v4u){o[2].x, o[2].y, o[3].x, o[3].y};
  } else {
    const int x = b & 7, k = (b - 240 - PRE_FILL) >> 3;
    unsigned long long pc; unsigned l0, l1, l2, l3; int rel;
    asm volatile("s_getpc_b64 %0\nLPRE:\n\ts_mov_b32 %1, LKBEG-LPRE\n\ts_mov_b32 %2, LCHEND-LKBEG\n\ts_mov_b32 %3, LQVEND-LKBEG\n\ts_mov_b32 %4, LQKEND-LKBEG\n\ts_mov_b32 %5, LAPEND-LKBEG"
                 : "=s"(pc), "=s"(rel), "=s"(l0), "=s"(l1), "=s"(l2), "=s"(l3));
    l0 = l0 > l1 ? l0 : l1; l2 = l2 > l3 ? l2 : l3; l0 = l0 > l2 ? l0 : l2;
    const unsigned o = (unsigned)(k * 64 + (tid & 63)) * 64u;
    unsigned a[PFN + 2];
    a[PFN] = *(const __attribute__((address_space(1))) unsigned*)(pc + rel + (o < l0 ? o : 0u));
    a[PFN + 1] = __builtin_amdgcn_raw_buffer_load_b32(rx, k == 31 ? tid * XBLK + CAL_OFF + 8064u : 0xF0000000u, 0, 16);
#pragma unroll
    for (int i = 0; i < PFN; ++i) {
      const int q = (i * 32 + k) * 256 + tid, r2 = q >> 5, gr = r2 >> 5, row = (gr / 3) * 64 + (x >> 2) * 32 + (r2 & 31), off = (q & 31) * 32, ty = gr % 3;
      a[i] = *(const unsigned*)(gr < 27 ? (ty == 0 ? p.Wq : (ty == 1 ? p.Wk : p.Wv)) + (long)row * HD + (x & 3) * 1024 + off
                                        : p.Wo + ((long)(x & 3) * DM + (gr < 36 ? gr - 27 : 0) * 64 + (x >> 2) * 32 + (r2 & 31)) * 1024 + off);
    }
#pragma unroll
    for (int i = 0; i < PFN + 2; ++i) asm volatile("" :: "v"(a[i]));
  }
}

#define CREV_STRIDE 2208
#define LWAIT() asm volatile("s_waitcnt lgkmcnt(0)" ::: "memory")
template <class E, class F>
__device__ __forceinline__ void conv_task(const u16* __restrict__ kvT, const u16* __restrict__ ssm_k, const u16* __restrict__ ssm_D, u16* yT, u16* shb, int d, E early, F fill) {
  const int lane = threadIdx.x & 63;
  u16* crev = shb + 64;
  u16* kvs = crev + 4 * CREV_STRIDE;
  const int X0 = 2048 + 63;
  v4u cv[4], kq[4];
#pragma unroll
  for (int j = 0; j < 4; ++j) { cv[j] = *(const v4u*)&ssm_k[(long)d * S_LEN + lane * 32 + 8 * j]; kq[j] = *(const v4u*)&kvT[(long)d * S_LEN + (j * 64 + lane) * 8]; }
  const float Dv = bf2f(ssm_D[d]);
  __builtin_amdgcn_sched_barrier(0);
  early();
  __builtin_amdgcn_sched_barrier(0);
  const v4u z4 = {0u, 0u, 0u, 0u};
  *(v4u*)&kvs[(lane >> 2) * 40 + (lane & 3) * 8] = z4;
  if (lane < 32) *(v4u*)&crev[(lane >> 3) * CREV_STRIDE + (lane & 7) * 8] = z4;
  if (lane < 36) *(v4u*)&crev[(lane / 9) * CREV_STRIDE + 2112 + (lane % 9) * 8] = z4;
  {
    unsigned r[18];
#pragma unroll
    for (int k = 0; k < 16; ++k) { const unsigned u = cv[3 - (k >> 2)][3 - (k & 3)]; r[k] = __builtin_amdgcn_alignbyte(u, u, 2); }
#pragma unroll
    for (int k = 0; k < 2; ++k) r[16 + k] = (unsigned)__builtin_amdgcn_update_dpp(0, (int)r[k], 0x138, 0xf, 0xf, true);
    LWAIT();
#pragma unroll
    for (int rho = 0; rho < 4; ++rho)
#pragma unroll
      for (int j = 0; j < 4; ++j) {
        v4u o;
#pragma unroll
        for (int k = 0; k < 4; ++k) { const int q = 4 * j + k + (rho >> 1); o[k] = (rho & 1) ? __builtin_amdgcn_alignbyte(r[q + 1], r[q], 2) : r[q]; }
        *(v4u*)&crev[rho * CREV_STRIDE + 2080 - 32 * lane + 8 * j] = o;
      }
    if (lane == 63) {
      *(v2u*)&crev[1 * CREV_STRIDE + 60] = (v2u){0u, r[0] << 16};
      *(v2u*)&crev[2 * CREV_STRIDE + 60] = (v2u){0u, r[0]};
      *(v2u*)&crev[3 * CREV_STRIDE + 60] = (v2u){r[0] << 16, __builtin_amdgcn_alignbyte(r[1], r[0], 2)};
    }
#pragma unroll
    for (int j = 0; j < 4; ++j) *(v4u*)&kvs[(16 + j * 16 + (lane >> 2)) * 40 + (lane & 3) * 8] = kq[j];
  }
  fill();
  LWAIT();
  const int n = lane & 15, g = lane >> 4;
  const int rho = (X0 - n) & 3;
  f32x4 acc[4][2];
#pragma unroll
  for (int q = 0; q < 8; ++q) acc[q >> 1][q & 1] = (f32x4){0.f, 0.f, 0.f, 0.f};
  {
    typedef __attribute__((address_space(3))) const v2u* l2p;
    typedef __attribute__((address_space(3))) const bf16x8* l4p;
    unsigned a0 = (unsigned)(size_t)(__attribute__((address_space(3))) u16*)&crev[rho * CREV_STRIDE + (X0 - (n - 8 * g) - rho)] - 3232u, a1 = a0 + 8u;
    unsigned b0 = (unsigned)(size_t)(__attribute__((address_space(3))) u16*)&kvs[(14 + n) * 40 + 8 * g];
    asm volatile("" : "+v"(a1));
#define AFR(m, f) ({ const unsigned o_ = 3232u - 64u * (m) - 1024u * ((f) >> 1) - 32u * ((f) & 1); const v2u lo_ = *(l2p)(size_t)(a0 + o_), hi_ = *(l2p)(size_t)(a1 + o_); \
                     __builtin_bit_cast(bf16x8, (v4u){lo_.x, lo_.y, hi_.x, hi_.y}); })
#define BFR(m, j) (*(l4p)(size_t)(b0 + 80u * (2 - (m)) + 1280u * (j)))
    bf16x8 af[2][8], bf[2][4];
#pragma unroll
    for (int f = 0; f < 8; ++f) { af[0][f] = AFR(0, f); af[1][f] = af[0][f]; __builtin_amdgcn_sched_barrier(0); }
#pragma unroll
    for (int j = 0; j < 4; ++j) { bf[0][j] = BFR(0, j); bf[1][j] = bf[0][j]; }
#pragma unroll 1
    for (int mo = 0; mo < 8; ++mo) {
#pragma unroll
      for (int m = 0; m < 2; ++m) {
        int k = 0;
#pragma unroll
        for (int a = 0; a < 4; ++a)
#pragma unroll
          for (int ct = a; ct < 4; ++ct)
#pragma unroll
            for (int ti = 0; ti < 2; ++ti) {
              acc[ct][ti] = __builtin_amdgcn_mfma_f32_16x16x32_bf16(__builtin_bit_cast(bfx8, af[m][2 * a + ti]), __builtin_bit_cast(bfx8, bf[m][ct - a]), acc[ct][ti], 0, 0, 0);
              if (k < 2) af[m ^ 1][k] = AFR(m + 1, k);
              else if (k < 6) bf[m ^ 1][k - 2] = BFR(m + 1, k - 2);
              else if (k < 12) af[m ^ 1][k - 4] = AFR(m + 1, k - 4);
              __builtin_amdgcn_sched_barrier(0);
              ++k;
            }
      }
      a0 -= 128u; a1 -= 128u; b0 -= 160u;
    }
  }
  u16* yo = crev;
  LWAIT();
#pragma unroll
  for (int q = 0; q < 8; ++q) {
    const int o = (16 * (q >> 1) + n) * 40 + 16 * (q & 1) + 4 * g;
    const v2u kx = *(const v2u*)&kvs[16 * 40 + o];
    const f32x4 y = acc[q >> 1][q & 1];
    const unsigned y0 = pk2(y[0], y[1]), y1 = pk2(y[2], y[3]), r0 = pk2(lo2f(kx.x) * Dv, hi2f(kx.x) * Dv), r1 = pk2(lo2f(kx.y) * Dv, hi2f(kx.y) * Dv);
    *(v2u*)&yo[o] = (v2u){pk2(lo2f(y0) + lo2f(r0), hi2f(y0) + hi2f(r0)), pk2(lo2f(y1) + lo2f(r1), hi2f(y1) + hi2f(r1))};
  }
  LWAIT();
  __builtin_amdgcn_s_barrier();
  asm volatile("" ::: "memory");
  {
    const int tid = threadIdx.x, f = (d >> 3) & 15;
    const u16* y0 = shb - (tid >> 6) * 16384 + 64;
#pragma unroll
    for (int k = 0; k < 4; ++k) {
      const int tile = 32 * k + (tid >> 3), t = 16 * tile + 2 * ((tid & 7) ^ (f >> 1));
      unsigned u[4];
#pragma unroll
      for (int c = 0; c < 4; ++c) u[c] = *(const unsigned*)&y0[c * 16384 + (t >> 5) * 40 + (t & 31)];
      const v2u e = {__builtin_amdgcn_perm(u[1], u[0], 0x05040100u), __builtin_amdgcn_perm(u[3], u[2], 0x05040100u)}, o = {__builtin_amdgcn_perm(u[1], u[0], 0x07060302u), __builtin_amdgcn_perm(u[3], u[2], 0x07060302u)};
      if (tile >= NFC / 16) __builtin_amdgcn_raw_buffer_store_b128((f & 1) ? (v4u){o.x, o.y, e.x, e.y} : (v4u){e.x, e.y, o.x, o.y}, __builtin_amdgcn_make_buffer_rsrc((void*)yT, 0, DM * S_LEN * 2, 0x00020000),
                                                                   tile * 18432 + (tid & 7) * 16, (d >> 2) * 128, 16);
    }
  }
}

#define NCHW 64
#define NTOKW 192
#define PSL 4
#define SENT 0xFFFFFFFFu
#define SPIN_CAP 300000u
struct MP {
  const u16* Wq; const u16* Wk; const u16* Wv; const u16* Wo; u16* out; unsigned* xb;
  const u16* xin; u16* yT; u16* kvT; const u16* ssm_k; const u16* ssm_D;
};

__device__ __forceinline__ MP getp() {
  MP r;
#if defined(__HIP_DEVICE_COMPILE__)
  const __attribute__((address_space(4))) unsigned long long* k = (const __attribute__((address_space(4))) unsigned long long*)__builtin_amdgcn_kernarg_segment_ptr();
  asm volatile("" : "+s"(k));
#define GP(T, i) ((T*)(__attribute__((address_space(1))) T*)k[i])
  r.Wq = GP(const u16, 0);
  r.Wk = GP(const u16, 1);
  r.Wv = GP(const u16, 2);
  r.Wo = GP(const u16, 3);
  r.out = GP(u16, 4);
  r.xb = GP(unsigned, 5);
  r.xin = GP(const u16, 6);
  r.yT = GP(u16, 7);
  r.kvT = GP(u16, 8);
  r.ssm_k = GP(const u16, 9);
  r.ssm_D = GP(const u16, 10);
#endif
  return r;
}
typedef __amdgpu_buffer_rsrc_t rsrc_t;
__device__ __forceinline__ rsrc_t mkr(const void* p, unsigned bytes) { return __builtin_amdgcn_make_buffer_rsrc((void*)p, 0, bytes, 0x00020000); }
__device__ __forceinline__ void st16(rsrc_t r, unsigned vo, unsigned so, v4u v) { __builtin_amdgcn_raw_buffer_store_b128(v, r, vo, so, 16); }
__device__ __forceinline__ void st4(rsrc_t r, unsigned vo, unsigned so, unsigned v) { __builtin_amdgcn_raw_buffer_store_b32(v, r, vo, so, 16); }
__device__ __forceinline__ v4u ld16(rsrc_t r, unsigned vo, unsigned so) { return __builtin_amdgcn_raw_buffer_load_b128(r, vo, so, 16); }
__device__ __forceinline__ v2u ld8(rsrc_t r, unsigned vo, unsigned so) { return __builtin_amdgcn_raw_buffer_load_b64(r, vo, so, 16); }
__device__ __forceinline__ unsigned ld4(rsrc_t r, unsigned vo, unsigned so) { return __builtin_amdgcn_raw_buffer_load_b32(r, vo, so, 16); }
__device__ __forceinline__ unsigned opq(unsigned x) { asm volatile("" : "+v"(x)); return x; }

__device__ __forceinline__ unsigned long long die_mask(rsrc_t X, unsigned first, int L, int lane) { return __ballot((ld4(X, (unsigned)lane * 4u, CTAB_OFF + first * 4u) & 1u) != (unsigned)L); }
#define SEGB(m, s) ((unsigned)(SG0 + (s)) * XBLK + (unsigned)(((m) >> (s)) & 1ull) * 8192u)
__device__ __forceinline__ bool spin_fail(unsigned& spins, rsrc_t F) {
  ++spins;
  if (spins > SPIN_CAP) { unsigned one = 0x40000000u | (blockIdx.x << 8) | threadIdx.x; asm volatile("" : "+v"(one)); if (__builtin_amdgcn_readfirstlane(ld4(F, 0, TMO_OFF)) == 0u) st4(F, 0, TMO_OFF, one); return true; }
  if ((spins & 1023u) == 0u) return __builtin_amdgcn_readfirstlane(ld4(F, 0, TMO_OFF)) != 0;
  return false;
}
__device__ __forceinline__ unsigned rtc() { return (unsigned)__builtin_amdgcn_s_memrealtime(); }
#define BAR() do { asm volatile("s_waitcnt lgkmcnt(0)" ::: "memory"); __builtin_amdgcn_s_barrier(); asm volatile("" ::: "memory"); } while (0)
#define SB() __builtin_amdgcn_sched_barrier(0)
#define MF "v_mfma_f32_16x16x32_bf16 "
#define VM0() __builtin_amdgcn_s_waitcnt(0x0F70)
#define DL1 12
#define SCH_B0 48
#define DLL1 6
#define DLL2 0
#define DLF 24
#define SCH_PEN 0
#define DLX 4
#define LATE 2
#define TKL 28
#define FIN -3
#define CH_FIN CH_S(FIN)
#define DLT 16
#define DLY 20
#define CH_DLT CH_S(DLT)
#define CH_DLY CH_S(DLY)
#define ES 6
#define ED 4
#define EB 8
#define CH_ES CH_S(ES)
#define CH_EX "%[ex]"
#define DRYL 1
#define CH_DRYL CH_S(DRYL)
#define SMAP 0x1023u
#define RO1 4
#define CH_RO1 CH_S(RO1)
#define RO3 2
#define CH_RO3 CH_S(RO3)
#define KD 303
#define KB 148
#define FU 3072
#define FD 256
#define CH_FU CH_S(FU)
#define CH_FD CH_S(FD)
#define CH_KD CH_S(KD)
#define CH_KB CH_S(KB)
#define CH_AL(n) ".p2align 5\n\t.fill " CH_S(n) ", 4, 0xbf800000\n\t"
#define ALW 6
#define ALD 5
#define CH_ALW CH_AL(ALW)
#define CH_ALD CH_AL(ALD)
#define RSL 2
#define CH_S2(x) #x
#define CH_S(x) CH_S2(x)
#define CH_TOKCHK "s_cbranch_vccz LG3_%=\n\t"
#define CH_SG0 CH_S(SG0)
#define CH_ARMD "%[armd]"
#define CH_ARMS "%[arms]"
#define CH_SLOT "688128"
#define CH_SKIP "32768"
#define CH_DLL1 CH_S(DLL1)
#define CH_DLL2 CH_S(DLL2)
#define CH_DLF CH_S(DLF)
#define CH_B0 CH_S(SCH_B0)
#define CH_PEN CH_S(SCH_PEN)
#define CH_RSL CH_S(RSL)
#define CH_DLX CH_S(DLX)
#define CH_LATE CH_S(LATE)
#define CH_DL1L "%[dl1l]"
#define CH_TKL CH_S(TKL)
#define CH_SCHW "656384"
#define CH_CAP "%[cap]"
#define CH_CTAB "%[ctab]"
#define CH_RING "%[ring]"
#define CH_TRM "%[trm]"
#define FDL 6
#define CH_FDL CH_S(FDL)
#define CH_ASM \
  "s_mov_b32 s84, " CH_CAP "\n\t" \
  "s_mov_b32 s83, " CH_CAP "\n\t" \
  "s_mov_b32 s68, 0\n\t" \
  "s_mov_b32 s74, 0\n\t" \
  "s_mov_b32 s76, 0\n\t" \
  "s_mov_b32 s77, 0\n\t" \
  "s_mov_b32 s78, 0\n\t" \
  "s_mov_b32 s79, 0\n\t" \
  "s_mov_b32 s56, 0xcccccccc\n\t" \
  "s_mov_b32 s57, 0xcccccccc\n\t" \
  "s_mov_b32 s58, 0x33333333\n\t" \
  "s_mov_b32 s59, 0x33333333\n\t" \
  "s_mov_b32 s24, 0x01010101\n\t" \
  "s_mov_b32 s25, 0\n\t" \
  "s_mov_b32 s88, 0x3f803f80\n\t" \
  "s_mov_b32 s89, 0x3f803f80\n\t" \
  "s_mov_b32 s66, s65\n\t" \
  "s_mov_b32 s47, s65\n\t" \
  "s_mov_b32 s72, 0\n\t" \
  "s_mov_b32 s73, 0\n\t" \
  "s_mov_b32 s67, 0\n\t" \
  "s_mul_i32 s60, s40, 36864\n\t" \
  "s_add_u32 s60, s60, 16384\n\t" \
  "v_add_u32 v70, s60, v69\n\t" \
  "v_bfe_i32 v68, v69, 5, 1\n\t" \
  "v_and_b32 v68, 0xaaaaaaaa, v68\n\t" \
  "v_or_b32 v68, 0x44444444, v68\n\t" \
  "v_and_b32 v73, 32, v69\n\t" \
  "v_lshl_add_u32 v73, v73, 5, v69\n\t" \
  "LLOAD_%=:\n\t" \
  "v_lshrrev_b32 v74, 2, v69\n\t" \
  "s_mov_b32 s60, " CH_CTAB "\n\t" \
  "buffer_load_dword v72, v74, s[28:31], s60 offen offset:88 sc1\n\t" \
  "buffer_load_dword v74, v74, s[28:31], s60 offen sc1\n\t" \
  "v_lshrrev_b32 v71, 8, v69\n\t" \
  "v_lshlrev_b32 v1, 6, v71\n\t" \
  "v_and_b32 v2, 0xf0, v69\n\t" \
  "v_xor_b32 v1, v1, v2\n\t" \
  "v_lshl_add_u32 v71, v71, 13, v1\n\t" \
  "s_lshl_b32 s60, s40, 17\n\t" \
  "v_add_u32 v71, s60, v71\n\t" \
  "v_lshrrev_b32 v1, 7, v69\n\t" \
  "v_and_b32 v2, 0x70, v69\n\t" \
  "s_movk_i32 s60, 1152\n\t" \
  "v_mad_u32_u24 v75, v1, s60, v2\n\t" \
  "s_lshl_b32 s54, s40, 12\n\t" \
  "s_lshl_b32 s55, s40, 7\n\t" \
  "s_mul_i32 s75, s40, 36864\n\t" \
  "s_add_u32 s75, s75, 1024\n\t" \
  "s_mov_b32 s62, 0x05040100\n\t" \
  "s_mov_b32 s63, 0x07060302\n\t" \
  "s_barrier\n\t" \
  "s_xor_b32 s61, s40, 0\n\t" \
  "s_mul_i32 s61, s61, 36864\n\t" \
  "s_add_u32 s61, s61, s55\n\t" \
  "s_add_u32 m0, s54, 0\n\t" \
  "s_add_u32 s60, s61, 0\n\t" \
  "buffer_load_dwordx4 v75, %[Wo], s60 offen lds\n\t" \
  "s_add_u32 m0, s54, 1024\n\t" \
  "s_add_u32 s60, s61, 9216\n\t" \
  "buffer_load_dwordx4 v75, %[Wo], s60 offen lds\n\t" \
  "s_add_u32 m0, s54, 2048\n\t" \
  "s_add_u32 s60, s61, 18432\n\t" \
  "buffer_load_dwordx4 v75, %[Wo], s60 offen lds\n\t" \
  "s_add_u32 m0, s54, 3072\n\t" \
  "s_add_u32 s60, s61, 27648\n\t" \
  "buffer_load_dwordx4 v75, %[Wo], s60 offen lds\n\t" \
  "s_add_u32 m0, s54, 16384\n\t" \
  "s_mov_b32 s60, 0\n\t" \
  "buffer_load_dwordx4 v71, %[Wq], s60 offen lds\n\t" \
  "s_add_u32 m0, s54, 17408\n\t" \
  "s_mov_b32 s60, 32768\n\t" \
  "buffer_load_dwordx4 v71, %[Wq], s60 offen lds\n\t" \
  "s_add_u32 m0, s54, 18432\n\t" \
  "s_mov_b32 s60, 65536\n\t" \
  "buffer_load_dwordx4 v71, %[Wq], s60 offen lds\n\t" \
  "s_add_u32 m0, s54, 19456\n\t" \
  "s_mov_b32 s60, 98304\n\t" \
  "buffer_load_dwordx4 v71, %[Wq], s60 offen lds\n\t" \
  "s_add_u32 m0, s54, 53248\n\t" \
  "s_mov_b32 s60, 0\n\t" \
  "buffer_load_dwordx4 v71, %[Wk], s60 offen lds\n\t" \
  "s_add_u32 m0, s54, 54272\n\t" \
  "s_mov_b32 s60, 32768\n\t" \
  "buffer_load_dwordx4 v71, %[Wk], s60 offen lds\n\t" \
  "s_add_u32 m0, s54, 55296\n\t" \
  "s_mov_b32 s60, 65536\n\t" \
  "buffer_load_dwordx4 v71, %[Wk], s60 offen lds\n\t" \
  "s_add_u32 m0, s54, 56320\n\t" \
  "s_mov_b32 s60, 98304\n\t" \
  "buffer_load_dwordx4 v71, %[Wk], s60 offen lds\n\t" \
  "s_xor_b32 s61, s40, 0\n\t" \
  "s_mul_i32 s61, s61, 36864\n\t" \
  "s_add_u32 s61, s61, s55\n\t" \
  "s_add_u32 m0, s54, 90112\n\t" \
  "s_add_u32 s60, s61, 512\n\t" \
  "buffer_load_dwordx4 v75, %[Wo], s60 offen lds\n\t" \
  "s_add_u32 m0, s54, 91136\n\t" \
  "s_add_u32 s60, s61, 9728\n\t" \
  "buffer_load_dwordx4 v75, %[Wo], s60 offen lds\n\t" \
  "s_add_u32 m0, s54, 92160\n\t" \
  "s_add_u32 s60, s61, 18944\n\t" \
  "buffer_load_dwordx4 v75, %[Wo], s60 offen lds\n\t" \
  "s_add_u32 m0, s54, 93184\n\t" \
  "s_add_u32 s60, s61, 28160\n\t" \
  "buffer_load_dwordx4 v75, %[Wo], s60 offen lds\n\t" \
  "s_add_u32 m0, s54, 126976\n\t" \
  "s_mov_b32 s60, 0\n\t" \
  "buffer_load_dwordx4 v71, %[Wv], s60 offen lds\n\t" \
  "s_add_u32 m0, s54, 128000\n\t" \
  "s_mov_b32 s60, 32768\n\t" \
  "buffer_load_dwordx4 v71, %[Wv], s60 offen lds\n\t" \
  "s_add_u32 m0, s54, 129024\n\t" \
  "s_mov_b32 s60, 65536\n\t" \
  "buffer_load_dwordx4 v71, %[Wv], s60 offen lds\n\t" \
  "s_add_u32 m0, s54, 130048\n\t" \
  "s_mov_b32 s60, 98304\n\t" \
  "buffer_load_dwordx4 v71, %[Wv], s60 offen lds\n\t" \
  "v_and_b32 v1, 0x300, v69\n\t" \
  "v_lshlrev_b32 v65, 3, v1\n\t" \
  "v_and_b32 v1, 0xc0, v69\n\t" \
  "v_lshl_add_u32 v65, v1, 2, v65\n\t" \
  "s_lshl_b32 s60, s40, 6\n\t" \
  "v_xor_b32 v1, s60, v1\n\t" \
  "v_and_b32 v2, 48, v69\n\t" \
  "v_add3_u32 v65, v65, v1, v2\n\t" \
  "v_lshrrev_b32 v2, 1, v2\n\t" \
  "v_sub_u32 v66, v65, v2\n\t" \
  "v_bfe_u32 v0, v69, 8, 1\n\t" \
  "v_bfe_u32 v1, v69, 9, 1\n\t" \
  "v_lshlrev_b32 v0, 2, v0\n\t" \
  "v_add_u32 v0, v0, v1\n\t" \
  "v_lshlrev_b32 v0, 9, v0\n\t" \
  "v_lshrrev_b32 v1, 1, v69\n\t" \
  "v_and_b32 v1, 0x60, v1\n\t" \
  "v_and_b32 v2, 16, v69\n\t" \
  "v_bfe_u32 v3, v69, 5, 1\n\t" \
  "v_add3_u32 v64, v0, v1, v2\n\t" \
  "v_lshl_add_u32 v64, v3, 2, v64\n\t" \
  "v_add_u32 v64, s54, v64\n\t" \
  "v_lshl_add_u32 v67, v3, 3, v0\n\t" \
  "v_bfe_u32 v1, v69, 7, 1\n\t" \
  "v_lshl_add_u32 v67, v1, 2, v67\n\t" \
  "v_bfe_u32 v1, v69, 6, 1\n\t" \
  "v_lshl_add_u32 v67, v1, 4, v67\n\t" \
  "v_bfe_u32 v1, v69, 4, 1\n\t" \
  "v_lshl_add_u32 v67, v1, 1, v67\n\t" \
  "s_lshl_b32 s60, s40, 5\n\t" \
  "v_add_u32 v67, s60, v67\n\t" \
  "v_add_u32 v78, 0, v64\n\t" \
  "v_add_u32 v79, 1024, v64\n\t" \
  "s_waitcnt vmcnt(16) lgkmcnt(0)\n\t" \
  "s_barrier\n\t" \
  "ds_read2_b32 v[80:81], v78 offset0:0 offset1:2\n\t" \
  "ds_read2_b32 v[82:83], v78 offset0:32 offset1:34\n\t" \
  "ds_read2_b32 v[84:85], v78 offset0:64 offset1:66\n\t" \
  "ds_read2_b32 v[86:87], v78 offset0:96 offset1:98\n\t" \
  "ds_read2_b32 v[88:89], v79 offset0:0 offset1:2\n\t" \
  "ds_read2_b32 v[90:91], v79 offset0:32 offset1:34\n\t" \
  "ds_read2_b32 v[92:93], v79 offset0:64 offset1:66\n\t" \
  "ds_read2_b32 v[94:95], v79 offset0:96 offset1:98\n\t" \
  "s_waitcnt lgkmcnt(6)\n\t" \
  "v_perm_b32 v112, v82, v80, s62\n\t" \
  "v_perm_b32 v113, v82, v80, s63\n\t" \
  "s_waitcnt lgkmcnt(4)\n\t" \
  "v_perm_b32 v114, v86, v84, s62\n\t" \
  "v_perm_b32 v115, v86, v84, s63\n\t" \
  "s_waitcnt lgkmcnt(2)\n\t" \
  "v_perm_b32 v116, v90, v88, s62\n\t" \
  "v_perm_b32 v117, v90, v88, s63\n\t" \
  "s_waitcnt lgkmcnt(0)\n\t" \
  "v_perm_b32 v118, v94, v92, s62\n\t" \
  "v_perm_b32 v119, v94, v92, s63\n\t" \
  "ds_write_b128 v70, v[112:115] offset:20480\n\t" \
  "ds_write_b128 v70, v[116:119] offset:21504\n\t" \
  "v_perm_b32 v120, v83, v81, s62\n\t" \
  "v_perm_b32 v121, v83, v81, s63\n\t" \
  "v_perm_b32 v122, v87, v85, s62\n\t" \
  "v_perm_b32 v123, v87, v85, s63\n\t" \
  "v_perm_b32 v124, v91, v89, s62\n\t" \
  "v_perm_b32 v125, v91, v89, s63\n\t" \
  "v_perm_b32 v126, v95, v93, s62\n\t" \
  "v_perm_b32 v127, v95, v93, s63\n\t" \
  "ds_write_b128 v70, v[120:123] offset:16384\n\t" \
  "ds_write_b128 v70, v[124:127] offset:17408\n\t" \
  "v_add_u32 v78, 16384, v65\n\t" \
  "s_waitcnt vmcnt(12) lgkmcnt(0)\n\t" \
  "s_barrier\n\t" \
  "ds_read_b64_tr_b16 v[0:1], v78 offset:0\n\t" \
  "ds_read_b64_tr_b16 v[2:3], v78 offset:1024\n\t" \
  "ds_read_b64_tr_b16 v[4:5], v78 offset:8192\n\t" \
  "ds_read_b64_tr_b16 v[6:7], v78 offset:9216\n\t" \
  "ds_read_b64_tr_b16 v[8:9], v78 offset:8\n\t" \
  "ds_read_b64_tr_b16 v[10:11], v78 offset:1032\n\t" \
  "ds_read_b64_tr_b16 v[12:13], v78 offset:8200\n\t" \
  "ds_read_b64_tr_b16 v[14:15], v78 offset:9224\n\t" \
  "s_add_u32 m0, s54, 0\n\t" \
  "s_mov_b32 s60, 524288\n\t" \
  "buffer_load_dwordx4 v71, %[Wq], s60 offen lds\n\t" \
  "s_add_u32 m0, s54, 1024\n\t" \
  "s_mov_b32 s60, 557056\n\t" \
  "buffer_load_dwordx4 v71, %[Wq], s60 offen lds\n\t" \
  "s_add_u32 m0, s54, 2048\n\t" \
  "s_mov_b32 s60, 589824\n\t" \
  "buffer_load_dwordx4 v71, %[Wq], s60 offen lds\n\t" \
  "s_add_u32 m0, s54, 3072\n\t" \
  "s_mov_b32 s60, 622592\n\t" \
  "buffer_load_dwordx4 v71, %[Wq], s60 offen lds\n\t" \
  "v_add_u32 v78, 53248, v65\n\t" \
  "s_waitcnt vmcnt(12) lgkmcnt(0)\n\t" \
  "s_barrier\n\t" \
  "ds_read_b64_tr_b16 a[0:1], v78 offset:0\n\t" \
  "ds_read_b64_tr_b16 a[2:3], v78 offset:1024\n\t" \
  "ds_read_b64_tr_b16 a[4:5], v78 offset:8192\n\t" \
  "ds_read_b64_tr_b16 a[6:7], v78 offset:9216\n\t" \
  "ds_read_b64_tr_b16 a[8:9], v78 offset:8\n\t" \
  "ds_read_b64_tr_b16 a[10:11], v78 offset:1032\n\t" \
  "ds_read_b64_tr_b16 a[12:13], v78 offset:8200\n\t" \
  "ds_read_b64_tr_b16 a[14:15], v78 offset:9224\n\t" \
  "s_xor_b32 s61, s40, 1\n\t" \
  "s_mul_i32 s61, s61, 36864\n\t" \
  "s_add_u32 s61, s61, s55\n\t" \
  "s_add_u32 m0, s54, 16384\n\t" \
  "s_add_u32 s60, s61, 0\n\t" \
  "buffer_load_dwordx4 v75, %[Wo], s60 offen lds\n\t" \
  "s_add_u32 m0, s54, 17408\n\t" \
  "s_add_u32 s60, s61, 9216\n\t" \
  "buffer_load_dwordx4 v75, %[Wo], s60 offen lds\n\t" \
  "s_add_u32 m0, s54, 18432\n\t" \
  "s_add_u32 s60, s61, 18432\n\t" \
  "buffer_load_dwordx4 v75, %[Wo], s60 offen lds\n\t" \
  "s_add_u32 m0, s54, 19456\n\t" \
  "s_add_u32 s60, s61, 27648\n\t" \
  "buffer_load_dwordx4 v75, %[Wo], s60 offen lds\n\t" \
  "v_add_u32 v78, 90112, v64\n\t" \
  "v_add_u32 v79, 91136, v64\n\t" \
  "s_waitcnt vmcnt(12) lgkmcnt(0)\n\t" \
  "s_barrier\n\t" \
  "ds_read2_b32 v[80:81], v78 offset0:0 offset1:2\n\t" \
  "ds_read2_b32 v[82:83], v78 offset0:32 offset1:34\n\t" \
  "ds_read2_b32 v[84:85], v78 offset0:64 offset1:66\n\t" \
  "ds_read2_b32 v[86:87], v78 offset0:96 offset1:98\n\t" \
  "ds_read2_b32 v[88:89], v79 offset0:0 offset1:2\n\t" \
  "ds_read2_b32 v[90:91], v79 offset0:32 offset1:34\n\t" \
  "ds_read2_b32 v[92:93], v79 offset0:64 offset1:66\n\t" \
  "ds_read2_b32 v[94:95], v79 offset0:96 offset1:98\n\t" \
  "s_add_u32 m0, s54, 53248\n\t" \
  "s_mov_b32 s60, 524288\n\t" \
  "buffer_load_dwordx4 v71, %[Wk], s60 offen lds\n\t" \
  "s_add_u32 m0, s54, 54272\n\t" \
  "s_mov_b32 s60, 557056\n\t" \
  "buffer_load_dwordx4 v71, %[Wk], s60 offen lds\n\t" \
  "s_add_u32 m0, s54, 55296\n\t" \
  "s_mov_b32 s60, 589824\n\t" \
  "buffer_load_dwordx4 v71, %[Wk], s60 offen lds\n\t" \
  "s_add_u32 m0, s54, 56320\n\t" \
  "s_mov_b32 s60, 622592\n\t" \
  "buffer_load_dwordx4 v71, %[Wk], s60 offen lds\n\t" \
  "s_waitcnt lgkmcnt(6)\n\t" \
  "v_perm_b32 v112, v82, v80, s62\n\t" \
  "v_perm_b32 v113, v82, v80, s63\n\t" \
  "s_waitcnt lgkmcnt(4)\n\t" \
  "v_perm_b32 v114, v86, v84, s62\n\t" \
  "v_perm_b32 v115, v86, v84, s63\n\t" \
  "s_waitcnt lgkmcnt(2)\n\t" \
  "v_perm_b32 v116, v90, v88, s62\n\t" \
  "v_perm_b32 v117, v90, v88, s63\n\t" \
  "s_waitcnt lgkmcnt(0)\n\t" \
  "v_perm_b32 v118, v94, v92, s62\n\t" \
  "v_perm_b32 v119, v94, v92, s63\n\t" \
  "ds_write_b128 v70, v[112:115] offset:22528\n\t" \
  "ds_write_b128 v70, v[116:119] offset:23552\n\t" \
  "v_perm_b32 v120, v83, v81, s62\n\t" \
  "v_perm_b32 v121, v83, v81, s63\n\t" \
  "v_perm_b32 v122, v87, v85, s62\n\t" \
  "v_perm_b32 v123, v87, v85, s63\n\t" \
  "v_perm_b32 v124, v91, v89, s62\n\t" \
  "v_perm_b32 v125, v91, v89, s63\n\t" \
  "v_perm_b32 v126, v95, v93, s62\n\t" \
  "v_perm_b32 v127, v95, v93, s63\n\t" \
  "ds_write_b128 v70, v[120:123] offset:18432\n\t" \
  "ds_write_b128 v70, v[124:127] offset:19456\n\t" \
  "v_add_u32 v78, 126976, v66\n\t" \
  "s_waitcnt vmcnt(12) lgkmcnt(0)\n\t" \
  "s_barrier\n\t" \
  "ds_read_b64_tr_b16 a[16:17], v78 offset:0\n\t" \
  "ds_read_b64_tr_b16 a[18:19], v78 offset:1024\n\t" \
  "ds_read_b64_tr_b16 a[20:21], v78 offset:8192\n\t" \
  "ds_read_b64_tr_b16 a[22:23], v78 offset:9216\n\t" \
  "ds_read_b64_tr_b16 a[24:25], v78 offset:32\n\t" \
  "ds_read_b64_tr_b16 a[26:27], v78 offset:1056\n\t" \
  "ds_read_b64_tr_b16 a[28:29], v78 offset:8224\n\t" \
  "ds_read_b64_tr_b16 a[30:31], v78 offset:9248\n\t" \
  "s_add_u32 m0, s54, 90112\n\t" \
  "s_mov_b32 s60, 524288\n\t" \
  "buffer_load_dwordx4 v71, %[Wv], s60 offen lds\n\t" \
  "s_add_u32 m0, s54, 91136\n\t" \
  "s_mov_b32 s60, 557056\n\t" \
  "buffer_load_dwordx4 v71, %[Wv], s60 offen lds\n\t" \
  "s_add_u32 m0, s54, 92160\n\t" \
  "s_mov_b32 s60, 589824\n\t" \
  "buffer_load_dwordx4 v71, %[Wv], s60 offen lds\n\t" \
  "s_add_u32 m0, s54, 93184\n\t" \
  "s_mov_b32 s60, 622592\n\t" \
  "buffer_load_dwordx4 v71, %[Wv], s60 offen lds\n\t" \
  "v_add_u32 v78, 0, v65\n\t" \
  "s_waitcnt vmcnt(12) lgkmcnt(0)\n\t" \
  "s_barrier\n\t" \
  "ds_read_b64_tr_b16 a[32:33], v78 offset:0\n\t" \
  "ds_read_b64_tr_b16 a[34:35], v78 offset:1024\n\t" \
  "ds_read_b64_tr_b16 a[36:37], v78 offset:8192\n\t" \
  "ds_read_b64_tr_b16 a[38:39], v78 offset:9216\n\t" \
  "ds_read_b64_tr_b16 a[40:41], v78 offset:8\n\t" \
  "ds_read_b64_tr_b16 a[42:43], v78 offset:1032\n\t" \
  "ds_read_b64_tr_b16 a[44:45], v78 offset:8200\n\t" \
  "ds_read_b64_tr_b16 a[46:47], v78 offset:9224\n\t" \
  "s_xor_b32 s61, s40, 1\n\t" \
  "s_mul_i32 s61, s61, 36864\n\t" \
  "s_add_u32 s61, s61, s55\n\t" \
  "s_add_u32 m0, s54, 126976\n\t" \
  "s_add_u32 s60, s61, 512\n\t" \
  "buffer_load_dwordx4 v75, %[Wo], s60 offen lds\n\t" \
  "s_add_u32 m0, s54, 128000\n\t" \
  "s_add_u32 s60, s61, 9728\n\t" \
  "buffer_load_dwordx4 v75, %[Wo], s60 offen lds\n\t" \
  "s_add_u32 m0, s54, 129024\n\t" \
  "s_add_u32 s60, s61, 18944\n\t" \
  "buffer_load_dwordx4 v75, %[Wo], s60 offen lds\n\t" \
  "s_add_u32 m0, s54, 130048\n\t" \
  "s_add_u32 s60, s61, 28160\n\t" \
  "buffer_load_dwordx4 v75, %[Wo], s60 offen lds\n\t" \
  "v_add_u32 v78, 16384, v64\n\t" \
  "v_add_u32 v79, 17408, v64\n\t" \
  "s_waitcnt vmcnt(12) lgkmcnt(0)\n\t" \
  "s_barrier\n\t" \
  "ds_read2_b32 v[80:81], v78 offset0:0 offset1:2\n\t" \
  "ds_read2_b32 v[82:83], v78 offset0:32 offset1:34\n\t" \
  "ds_read2_b32 v[84:85], v78 offset0:64 offset1:66\n\t" \
  "ds_read2_b32 v[86:87], v78 offset0:96 offset1:98\n\t" \
  "ds_read2_b32 v[88:89], v79 offset0:0 offset1:2\n\t" \
  "ds_read2_b32 v[90:91], v79 offset0:32 offset1:34\n\t" \
  "ds_read2_b32 v[92:93], v79 offset0:64 offset1:66\n\t" \
  "ds_read2_b32 v[94:95], v79 offset0:96 offset1:98\n\t" \
  "s_add_u32 m0, s54, 0\n\t" \
  "s_mov_b32 s60, 1048576\n\t" \
  "buffer_load_dwordx4 v71, %[Wq], s60 offen lds\n\t" \
  "s_add_u32 m0, s54, 1024\n\t" \
  "s_mov_b32 s60, 1081344\n\t" \
  "buffer_load_dwordx4 v71, %[Wq], s60 offen lds\n\t" \
  "s_add_u32 m0, s54, 2048\n\t" \
  "s_mov_b32 s60, 1114112\n\t" \
  "buffer_load_dwordx4 v71, %[Wq], s60 offen lds\n\t" \
  "s_add_u32 m0, s54, 3072\n\t" \
  "s_mov_b32 s60, 1146880\n\t" \
  "buffer_load_dwordx4 v71, %[Wq], s60 offen lds\n\t" \
  "s_waitcnt lgkmcnt(6)\n\t" \
  "v_perm_b32 v112, v82, v80, s62\n\t" \
  "v_perm_b32 v113, v82, v80, s63\n\t" \
  "s_waitcnt lgkmcnt(4)\n\t" \
  "v_perm_b32 v114, v86, v84, s62\n\t" \
  "v_perm_b32 v115, v86, v84, s63\n\t" \
  "s_waitcnt lgkmcnt(2)\n\t" \
  "v_perm_b32 v116, v90, v88, s62\n\t" \
  "v_perm_b32 v117, v90, v88, s63\n\t" \
  "s_waitcnt lgkmcnt(0)\n\t" \
  "v_perm_b32 v118, v94, v92, s62\n\t" \
  "v_perm_b32 v119, v94, v92, s63\n\t" \
  "ds_write_b128 v70, v[112:115] offset:24576\n\t" \
  "ds_write_b128 v70, v[116:119] offset:25600\n\t" \
  "v_perm_b32 v200, v83, v81, s62\n\t" \
  "v_perm_b32 v201, v83, v81, s63\n\t" \
  "v_perm_b32 v202, v87, v85, s62\n\t" \
  "v_perm_b32 v203, v87, v85, s63\n\t" \
  "v_perm_b32 v204, v91, v89, s62\n\t" \
  "v_perm_b32 v205, v91, v89, s63\n\t" \
  "v_perm_b32 v206, v95, v93, s62\n\t" \
  "v_perm_b32 v207, v95, v93, s63\n\t" \
  "v_add_u32 v78, 53248, v65\n\t" \
  "s_waitcnt vmcnt(12) lgkmcnt(0)\n\t" \
  "s_barrier\n\t" \
  "ds_read_b64_tr_b16 a[48:49], v78 offset:0\n\t" \
  "ds_read_b64_tr_b16 a[50:51], v78 offset:1024\n\t" \
  "ds_read_b64_tr_b16 a[52:53], v78 offset:8192\n\t" \
  "ds_read_b64_tr_b16 a[54:55], v78 offset:9216\n\t" \
  "ds_read_b64_tr_b16 a[56:57], v78 offset:8\n\t" \
  "ds_read_b64_tr_b16 a[58:59], v78 offset:1032\n\t" \
  "ds_read_b64_tr_b16 a[60:61], v78 offset:8200\n\t" \
  "ds_read_b64_tr_b16 a[62:63], v78 offset:9224\n\t" \
  "s_add_u32 m0, s54, 16384\n\t" \
  "s_mov_b32 s60, 1048576\n\t" \
  "buffer_load_dwordx4 v71, %[Wk], s60 offen lds\n\t" \
  "s_add_u32 m0, s54, 17408\n\t" \
  "s_mov_b32 s60, 1081344\n\t" \
  "buffer_load_dwordx4 v71, %[Wk], s60 offen lds\n\t" \
  "s_add_u32 m0, s54, 18432\n\t" \
  "s_mov_b32 s60, 1114112\n\t" \
  "buffer_load_dwordx4 v71, %[Wk], s60 offen lds\n\t" \
  "s_add_u32 m0, s54, 19456\n\t" \
  "s_mov_b32 s60, 1146880\n\t" \
  "buffer_load_dwordx4 v71, %[Wk], s60 offen lds\n\t" \
  "v_add_u32 v78, 90112, v66\n\t" \
  "s_waitcnt vmcnt(12) lgkmcnt(0)\n\t" \
  "s_barrier\n\t" \
  "ds_read_b64_tr_b16 a[64:65], v78 offset:0\n\t" \
  "ds_read_b64_tr_b16 a[66:67], v78 offset:1024\n\t" \
  "ds_read_b64_tr_b16 a[68:69], v78 offset:8192\n\t" \
  "ds_read_b64_tr_b16 a[70:71], v78 offset:9216\n\t" \
  "ds_read_b64_tr_b16 a[72:73], v78 offset:32\n\t" \
  "ds_read_b64_tr_b16 a[74:75], v78 offset:1056\n\t" \
  "ds_read_b64_tr_b16 a[76:77], v78 offset:8224\n\t" \
  "ds_read_b64_tr_b16 a[78:79], v78 offset:9248\n\t" \
  "s_xor_b32 s61, s40, 2\n\t" \
  "s_mul_i32 s61, s61, 36864\n\t" \
  "s_add_u32 s61, s61, s55\n\t" \
  "s_add_u32 m0, s54, 53248\n\t" \
  "s_add_u32 s60, s61, 0\n\t" \
  "buffer_load_dwordx4 v75, %[Wo], s60 offen lds\n\t" \
  "s_add_u32 m0, s54, 54272\n\t" \
  "s_add_u32 s60, s61, 9216\n\t" \
  "buffer_load_dwordx4 v75, %[Wo], s60 offen lds\n\t" \
  "s_add_u32 m0, s54, 55296\n\t" \
  "s_add_u32 s60, s61, 18432\n\t" \
  "buffer_load_dwordx4 v75, %[Wo], s60 offen lds\n\t" \
  "s_add_u32 m0, s54, 56320\n\t" \
  "s_add_u32 s60, s61, 27648\n\t" \
  "buffer_load_dwordx4 v75, %[Wo], s60 offen lds\n\t" \
  "v_add_u32 v78, 126976, v64\n\t" \
  "v_add_u32 v79, 128000, v64\n\t" \
  "s_waitcnt vmcnt(12) lgkmcnt(0)\n\t" \
  "s_barrier\n\t" \
  "ds_read2_b32 v[80:81], v78 offset0:0 offset1:2\n\t" \
  "ds_read2_b32 v[82:83], v78 offset0:32 offset1:34\n\t" \
  "ds_read2_b32 v[84:85], v78 offset0:64 offset1:66\n\t" \
  "ds_read2_b32 v[86:87], v78 offset0:96 offset1:98\n\t" \
  "ds_read2_b32 v[88:89], v79 offset0:0 offset1:2\n\t" \
  "ds_read2_b32 v[90:91], v79 offset0:32 offset1:34\n\t" \
  "ds_read2_b32 v[92:93], v79 offset0:64 offset1:66\n\t" \
  "ds_read2_b32 v[94:95], v79 offset0:96 offset1:98\n\t" \
  "s_add_u32 m0, s54, 90112\n\t" \
  "s_mov_b32 s60, 1048576\n\t" \
  "buffer_load_dwordx4 v71, %[Wv], s60 offen lds\n\t" \
  "s_add_u32 m0, s54, 91136\n\t" \
  "s_mov_b32 s60, 1081344\n\t" \
  "buffer_load_dwordx4 v71, %[Wv], s60 offen lds\n\t" \
  "s_add_u32 m0, s54, 92160\n\t" \
  "s_mov_b32 s60, 1114112\n\t" \
  "buffer_load_dwordx4 v71, %[Wv], s60 offen lds\n\t" \
  "s_add_u32 m0, s54, 93184\n\t" \
  "s_mov_b32 s60, 1146880\n\t" \
  "buffer_load_dwordx4 v71, %[Wv], s60 offen lds\n\t" \
  "s_waitcnt lgkmcnt(6)\n\t" \
  "v_perm_b32 v112, v82, v80, s62\n\t" \
  "v_perm_b32 v113, v82, v80, s63\n\t" \
  "s_waitcnt lgkmcnt(4)\n\t" \
  "v_perm_b32 v114, v86, v84, s62\n\t" \
  "v_perm_b32 v115, v86, v84, s63\n\t" \
  "s_waitcnt lgkmcnt(2)\n\t" \
  "v_perm_b32 v116, v90, v88, s62\n\t" \
  "v_perm_b32 v117, v90, v88, s63\n\t" \
  "s_waitcnt lgkmcnt(0)\n\t" \
  "v_perm_b32 v118, v94, v92, s62\n\t" \
  "v_perm_b32 v119, v94, v92, s63\n\t" \
  "ds_write_b128 v70, v[112:115] offset:26624\n\t" \
  "ds_write_b128 v70, v[116:119] offset:27648\n\t" \
  "v_perm_b32 v232, v83, v81, s62\n\t" \
  "v_perm_b32 v233, v83, v81, s63\n\t" \
  "v_perm_b32 v234, v87, v85, s62\n\t" \
  "v_perm_b32 v235, v87, v85, s63\n\t" \
  "v_perm_b32 v236, v91, v89, s62\n\t" \
  "v_perm_b32 v237, v91, v89, s63\n\t" \
  "v_perm_b32 v238, v95, v93, s62\n\t" \
  "v_perm_b32 v239, v95, v93, s63\n\t" \
  "v_add_u32 v78, 0, v65\n\t" \
  "s_waitcnt vmcnt(12) lgkmcnt(0)\n\t" \
  "s_barrier\n\t" \
  "ds_read_b64_tr_b16 a[80:81], v78 offset:0\n\t" \
  "ds_read_b64_tr_b16 a[82:83], v78 offset:1024\n\t" \
  "ds_read_b64_tr_b16 a[84:85], v78 offset:8192\n\t" \
  "ds_read_b64_tr_b16 a[86:87], v78 offset:9216\n\t" \
  "ds_read_b64_tr_b16 a[88:89], v78 offset:8\n\t" \
  "ds_read_b64_tr_b16 a[90:91], v78 offset:1032\n\t" \
  "ds_read_b64_tr_b16 a[92:93], v78 offset:8200\n\t" \
  "ds_read_b64_tr_b16 a[94:95], v78 offset:9224\n\t" \
  "s_add_u32 m0, s54, 126976\n\t" \
  "s_mov_b32 s60, 1572864\n\t" \
  "buffer_load_dwordx4 v71, %[Wq], s60 offen lds\n\t" \
  "s_add_u32 m0, s54, 128000\n\t" \
  "s_mov_b32 s60, 1605632\n\t" \
  "buffer_load_dwordx4 v71, %[Wq], s60 offen lds\n\t" \
  "s_add_u32 m0, s54, 129024\n\t" \
  "s_mov_b32 s60, 1638400\n\t" \
  "buffer_load_dwordx4 v71, %[Wq], s60 offen lds\n\t" \
  "s_add_u32 m0, s54, 130048\n\t" \
  "s_mov_b32 s60, 1671168\n\t" \
  "buffer_load_dwordx4 v71, %[Wq], s60 offen lds\n\t" \
  "v_add_u32 v78, 16384, v65\n\t" \
  "s_waitcnt vmcnt(12) lgkmcnt(0)\n\t" \
  "s_barrier\n\t" \
  "ds_read_b64_tr_b16 v[32:33], v78 offset:0\n\t" \
  "ds_read_b64_tr_b16 v[34:35], v78 offset:1024\n\t" \
  "ds_read_b64_tr_b16 v[36:37], v78 offset:8192\n\t" \
  "ds_read_b64_tr_b16 v[38:39], v78 offset:9216\n\t" \
  "ds_read_b64_tr_b16 a[96:97], v78 offset:8\n\t" \
  "ds_read_b64_tr_b16 a[98:99], v78 offset:1032\n\t" \
  "ds_read_b64_tr_b16 a[100:101], v78 offset:8200\n\t" \
  "ds_read_b64_tr_b16 a[102:103], v78 offset:9224\n\t" \
  "s_xor_b32 s61, s40, 2\n\t" \
  "s_mul_i32 s61, s61, 36864\n\t" \
  "s_add_u32 s61, s61, s55\n\t" \
  "s_add_u32 m0, s54, 0\n\t" \
  "s_add_u32 s60, s61, 512\n\t" \
  "buffer_load_dwordx4 v75, %[Wo], s60 offen lds\n\t" \
  "s_add_u32 m0, s54, 1024\n\t" \
  "s_add_u32 s60, s61, 9728\n\t" \
  "buffer_load_dwordx4 v75, %[Wo], s60 offen lds\n\t" \
  "s_add_u32 m0, s54, 2048\n\t" \
  "s_add_u32 s60, s61, 18944\n\t" \
  "buffer_load_dwordx4 v75, %[Wo], s60 offen lds\n\t" \
  "s_add_u32 m0, s54, 3072\n\t" \
  "s_add_u32 s60, s61, 28160\n\t" \
  "buffer_load_dwordx4 v75, %[Wo], s60 offen lds\n\t" \
  "v_add_u32 v78, 53248, v64\n\t" \
  "v_add_u32 v79, 54272, v64\n\t" \
  "s_waitcnt vmcnt(12) lgkmcnt(0)\n\t" \
  "s_barrier\n\t" \
  "ds_read2_b32 v[80:81], v78 offset0:0 offset1:2\n\t" \
  "ds_read2_b32 v[82:83], v78 offset0:32 offset1:34\n\t" \
  "ds_read2_b32 v[84:85], v78 offset0:64 offset1:66\n\t" \
  "ds_read2_b32 v[86:87], v78 offset0:96 offset1:98\n\t" \
  "ds_read2_b32 v[88:89], v79 offset0:0 offset1:2\n\t" \
  "ds_read2_b32 v[90:91], v79 offset0:32 offset1:34\n\t" \
  "ds_read2_b32 v[92:93], v79 offset0:64 offset1:66\n\t" \
  "ds_read2_b32 v[94:95], v79 offset0:96 offset1:98\n\t" \
  "s_add_u32 m0, s54, 16384\n\t" \
  "s_mov_b32 s60, 1572864\n\t" \
  "buffer_load_dwordx4 v71, %[Wk], s60 offen lds\n\t" \
  "s_add_u32 m0, s54, 17408\n\t" \
  "s_mov_b32 s60, 1605632\n\t" \
  "buffer_load_dwordx4 v71, %[Wk], s60 offen lds\n\t" \
  "s_add_u32 m0, s54, 18432\n\t" \
  "s_mov_b32 s60, 1638400\n\t" \
  "buffer_load_dwordx4 v71, %[Wk], s60 offen lds\n\t" \
  "s_add_u32 m0, s54, 19456\n\t" \
  "s_mov_b32 s60, 1671168\n\t" \
  "buffer_load_dwordx4 v71, %[Wk], s60 offen lds\n\t" \
  "s_waitcnt lgkmcnt(6)\n\t" \
  "v_perm_b32 v112, v82, v80, s62\n\t" \
  "v_perm_b32 v113, v82, v80, s63\n\t" \
  "s_waitcnt lgkmcnt(4)\n\t" \
  "v_perm_b32 v114, v86, v84, s62\n\t" \
  "v_perm_b32 v115, v86, v84, s63\n\t" \
  "s_waitcnt lgkmcnt(2)\n\t" \
  "v_perm_b32 v116, v90, v88, s62\n\t" \
  "v_perm_b32 v117, v90, v88, s63\n\t" \
  "s_waitcnt lgkmcnt(0)\n\t" \
  "v_perm_b32 v118, v94, v92, s62\n\t" \
  "v_perm_b32 v119, v94, v92, s63\n\t" \
  "ds_write_b128 v70, v[112:115] offset:28672\n\t" \
  "ds_write_b128 v70, v[116:119] offset:29696\n\t" \
  "v_perm_b32 v208, v83, v81, s62\n\t" \
  "v_perm_b32 v209, v83, v81, s63\n\t" \
  "v_perm_b32 v210, v87, v85, s62\n\t" \
  "v_perm_b32 v211, v87, v85, s63\n\t" \
  "v_perm_b32 v212, v91, v89, s62\n\t" \
  "v_perm_b32 v213, v91, v89, s63\n\t" \
  "v_perm_b32 v214, v95, v93, s62\n\t" \
  "v_perm_b32 v215, v95, v93, s63\n\t" \
  "v_add_u32 v78, 90112, v66\n\t" \
  "s_waitcnt vmcnt(12) lgkmcnt(0)\n\t" \
  "s_barrier\n\t" \
  "ds_read_b64_tr_b16 a[104:105], v78 offset:0\n\t" \
  "ds_read_b64_tr_b16 a[106:107], v78 offset:1024\n\t" \
  "ds_read_b64_tr_b16 a[108:109], v78 offset:8192\n\t" \
  "ds_read_b64_tr_b16 a[110:111], v78 offset:9216\n\t" \
  "ds_read_b64_tr_b16 a[112:113], v78 offset:32\n\t" \
  "ds_read_b64_tr_b16 a[114:115], v78 offset:1056\n\t" \
  "ds_read_b64_tr_b16 a[116:117], v78 offset:8224\n\t" \
  "ds_read_b64_tr_b16 a[118:119], v78 offset:9248\n\t" \
  "s_add_u32 m0, s54, 53248\n\t" \
  "s_mov_b32 s60, 1572864\n\t" \
  "buffer_load_dwordx4 v71, %[Wv], s60 offen lds\n\t" \
  "s_add_u32 m0, s54, 54272\n\t" \
  "s_mov_b32 s60, 1605632\n\t" \
  "buffer_load_dwordx4 v71, %[Wv], s60 offen lds\n\t" \
  "s_add_u32 m0, s54, 55296\n\t" \
  "s_mov_b32 s60, 1638400\n\t" \
  "buffer_load_dwordx4 v71, %[Wv], s60 offen lds\n\t" \
  "s_add_u32 m0, s54, 56320\n\t" \
  "s_mov_b32 s60, 1671168\n\t" \
  "buffer_load_dwordx4 v71, %[Wv], s60 offen lds\n\t" \
  "v_add_u32 v78, 126976, v65\n\t" \
  "s_waitcnt vmcnt(12) lgkmcnt(0)\n\t" \
  "s_barrier\n\t" \
  "ds_read_b64_tr_b16 a[120:121], v78 offset:0\n\t" \
  "ds_read_b64_tr_b16 a[122:123], v78 offset:1024\n\t" \
  "ds_read_b64_tr_b16 a[124:125], v78 offset:8192\n\t" \
  "ds_read_b64_tr_b16 a[126:127], v78 offset:9216\n\t" \
  "ds_read_b64_tr_b16 v[16:17], v78 offset:8\n\t" \
  "ds_read_b64_tr_b16 v[18:19], v78 offset:1032\n\t" \
  "ds_read_b64_tr_b16 v[20:21], v78 offset:8200\n\t" \
  "ds_read_b64_tr_b16 v[22:23], v78 offset:9224\n\t" \
  "s_xor_b32 s61, s40, 3\n\t" \
  "s_mul_i32 s61, s61, 36864\n\t" \
  "s_add_u32 s61, s61, s55\n\t" \
  "s_add_u32 m0, s54, 90112\n\t" \
  "s_add_u32 s60, s61, 0\n\t" \
  "buffer_load_dwordx4 v75, %[Wo], s60 offen lds\n\t" \
  "s_add_u32 m0, s54, 91136\n\t" \
  "s_add_u32 s60, s61, 9216\n\t" \
  "buffer_load_dwordx4 v75, %[Wo], s60 offen lds\n\t" \
  "s_add_u32 m0, s54, 92160\n\t" \
  "s_add_u32 s60, s61, 18432\n\t" \
  "buffer_load_dwordx4 v75, %[Wo], s60 offen lds\n\t" \
  "s_add_u32 m0, s54, 93184\n\t" \
  "s_add_u32 s60, s61, 27648\n\t" \
  "buffer_load_dwordx4 v75, %[Wo], s60 offen lds\n\t" \
  "v_add_u32 v78, 0, v64\n\t" \
  "v_add_u32 v79, 1024, v64\n\t" \
  "s_waitcnt vmcnt(12) lgkmcnt(0)\n\t" \
  "s_barrier\n\t" \
  "ds_read2_b32 v[80:81], v78 offset0:0 offset1:2\n\t" \
  "ds_read2_b32 v[82:83], v78 offset0:32 offset1:34\n\t" \
  "ds_read2_b32 v[84:85], v78 offset0:64 offset1:66\n\t" \
  "ds_read2_b32 v[86:87], v78 offset0:96 offset1:98\n\t" \
  "ds_read2_b32 v[88:89], v79 offset0:0 offset1:2\n\t" \
  "ds_read2_b32 v[90:91], v79 offset0:32 offset1:34\n\t" \
  "ds_read2_b32 v[92:93], v79 offset0:64 offset1:66\n\t" \
  "ds_read2_b32 v[94:95], v79 offset0:96 offset1:98\n\t" \
  "s_add_u32 m0, s54, 126976\n\t" \
  "s_mov_b32 s60, 2097152\n\t" \
  "buffer_load_dwordx4 v71, %[Wq], s60 offen lds\n\t" \
  "s_add_u32 m0, s54, 128000\n\t" \
  "s_mov_b32 s60, 2129920\n\t" \
  "buffer_load_dwordx4 v71, %[Wq], s60 offen lds\n\t" \
  "s_add_u32 m0, s54, 129024\n\t" \
  "s_mov_b32 s60, 2162688\n\t" \
  "buffer_load_dwordx4 v71, %[Wq], s60 offen lds\n\t" \
  "s_add_u32 m0, s54, 130048\n\t" \
  "s_mov_b32 s60, 2195456\n\t" \
  "buffer_load_dwordx4 v71, %[Wq], s60 offen lds\n\t" \
  "s_waitcnt lgkmcnt(6)\n\t" \
  "v_perm_b32 v112, v82, v80, s62\n\t" \
  "v_perm_b32 v113, v82, v80, s63\n\t" \
  "s_waitcnt lgkmcnt(4)\n\t" \
  "v_perm_b32 v114, v86, v84, s62\n\t" \
  "v_perm_b32 v115, v86, v84, s63\n\t" \
  "s_waitcnt lgkmcnt(2)\n\t" \
  "v_perm_b32 v116, v90, v88, s62\n\t" \
  "v_perm_b32 v117, v90, v88, s63\n\t" \
  "s_waitcnt lgkmcnt(0)\n\t" \
  "v_perm_b32 v118, v94, v92, s62\n\t" \
  "v_perm_b32 v119, v94, v92, s63\n\t" \
  "ds_write_b128 v70, v[112:115] offset:30720\n\t" \
  "ds_write_b128 v70, v[116:119] offset:31744\n\t" \
  "v_perm_b32 v240, v83, v81, s62\n\t" \
  "v_perm_b32 v241, v83, v81, s63\n\t" \
  "v_perm_b32 v242, v87, v85, s62\n\t" \
  "v_perm_b32 v243, v87, v85, s63\n\t" \
  "v_perm_b32 v244, v91, v89, s62\n\t" \
  "v_perm_b32 v245, v91, v89, s63\n\t" \
  "v_perm_b32 v246, v95, v93, s62\n\t" \
  "v_perm_b32 v247, v95, v93, s63\n\t" \
  "v_add_u32 v78, 16384, v65\n\t" \
  "s_waitcnt vmcnt(12) lgkmcnt(0)\n\t" \
  "s_barrier\n\t" \
  "ds_read_b64_tr_b16 a[128:129], v78 offset:0\n\t" \
  "ds_read_b64_tr_b16 a[130:131], v78 offset:1024\n\t" \
  "ds_read_b64_tr_b16 a[132:133], v78 offset:8192\n\t" \
  "ds_read_b64_tr_b16 a[134:135], v78 offset:9216\n\t" \
  "ds_read_b64_tr_b16 a[136:137], v78 offset:8\n\t" \
  "ds_read_b64_tr_b16 a[138:139], v78 offset:1032\n\t" \
  "ds_read_b64_tr_b16 a[140:141], v78 offset:8200\n\t" \
  "ds_read_b64_tr_b16 a[142:143], v78 offset:9224\n\t" \
  "s_add_u32 m0, s54, 0\n\t" \
  "s_mov_b32 s60, 2097152\n\t" \
  "buffer_load_dwordx4 v71, %[Wk], s60 offen lds\n\t" \
  "s_add_u32 m0, s54, 1024\n\t" \
  "s_mov_b32 s60, 2129920\n\t" \
  "buffer_load_dwordx4 v71, %[Wk], s60 offen lds\n\t" \
  "s_add_u32 m0, s54, 2048\n\t" \
  "s_mov_b32 s60, 2162688\n\t" \
  "buffer_load_dwordx4 v71, %[Wk], s60 offen lds\n\t" \
  "s_add_u32 m0, s54, 3072\n\t" \
  "s_mov_b32 s60, 2195456\n\t" \
  "buffer_load_dwordx4 v71, %[Wk], s60 offen lds\n\t" \
  "v_add_u32 v78, 53248, v66\n\t" \
  "s_waitcnt vmcnt(12) lgkmcnt(0)\n\t" \
  "s_barrier\n\t" \
  "ds_read_b64_tr_b16 a[144:145], v78 offset:0\n\t" \
  "ds_read_b64_tr_b16 a[146:147], v78 offset:1024\n\t" \
  "ds_read_b64_tr_b16 a[148:149], v78 offset:8192\n\t" \
  "ds_read_b64_tr_b16 a[150:151], v78 offset:9216\n\t" \
  "ds_read_b64_tr_b16 a[152:153], v78 offset:32\n\t" \
  "ds_read_b64_tr_b16 a[154:155], v78 offset:1056\n\t" \
  "ds_read_b64_tr_b16 a[156:157], v78 offset:8224\n\t" \
  "ds_read_b64_tr_b16 a[158:159], v78 offset:9248\n\t" \
  "s_xor_b32 s61, s40, 3\n\t" \
  "s_mul_i32 s61, s61, 36864\n\t" \
  "s_add_u32 s61, s61, s55\n\t" \
  "s_add_u32 m0, s54, 16384\n\t" \
  "s_add_u32 s60, s61, 512\n\t" \
  "buffer_load_dwordx4 v75, %[Wo], s60 offen lds\n\t" \
  "s_add_u32 m0, s54, 17408\n\t" \
  "s_add_u32 s60, s61, 9728\n\t" \
  "buffer_load_dwordx4 v75, %[Wo], s60 offen lds\n\t" \
  "s_add_u32 m0, s54, 18432\n\t" \
  "s_add_u32 s60, s61, 18944\n\t" \
  "buffer_load_dwordx4 v75, %[Wo], s60 offen lds\n\t" \
  "s_add_u32 m0, s54, 19456\n\t" \
  "s_add_u32 s60, s61, 28160\n\t" \
  "buffer_load_dwordx4 v75, %[Wo], s60 offen lds\n\t" \
  "v_add_u32 v78, 90112, v64\n\t" \
  "v_add_u32 v79, 91136, v64\n\t" \
  "s_waitcnt vmcnt(12) lgkmcnt(0)\n\t" \
  "s_barrier\n\t" \
  "ds_read2_b32 v[80:81], v78 offset0:0 offset1:2\n\t" \
  "ds_read2_b32 v[82:83], v78 offset0:32 offset1:34\n\t" \
  "ds_read2_b32 v[84:85], v78 offset0:64 offset1:66\n\t" \
  "ds_read2_b32 v[86:87], v78 offset0:96 offset1:98\n\t" \
  "ds_read2_b32 v[88:89], v79 offset0:0 offset1:2\n\t" \
  "ds_read2_b32 v[90:91], v79 offset0:32 offset1:34\n\t" \
  "ds_read2_b32 v[92:93], v79 offset0:64 offset1:66\n\t" \
  "ds_read2_b32 v[94:95], v79 offset0:96 offset1:98\n\t" \
  "s_add_u32 m0, s54, 53248\n\t" \
  "s_mov_b32 s60, 2097152\n\t" \
  "buffer_load_dwordx4 v71, %[Wv], s60 offen lds\n\t" \
  "s_add_u32 m0, s54, 54272\n\t" \
  "s_mov_b32 s60, 2129920\n\t" \
  "buffer_load_dwordx4 v71, %[Wv], s60 offen lds\n\t" \
  "s_add_u32 m0, s54, 55296\n\t" \
  "s_mov_b32 s60, 2162688\n\t" \
  "buffer_load_dwordx4 v71, %[Wv], s60 offen lds\n\t" \
  "s_add_u32 m0, s54, 56320\n\t" \
  "s_mov_b32 s60, 2195456\n\t" \
  "buffer_load_dwordx4 v71, %[Wv], s60 offen lds\n\t" \
  "s_waitcnt lgkmcnt(6)\n\t" \
  "v_perm_b32 v192, v82, v80, s62\n\t" \
  "v_perm_b32 v193, v82, v80, s63\n\t" \
  "s_waitcnt lgkmcnt(4)\n\t" \
  "v_perm_b32 v194, v86, v84, s62\n\t" \
  "v_perm_b32 v195, v86, v84, s63\n\t" \
  "s_waitcnt lgkmcnt(2)\n\t" \
  "v_perm_b32 v196, v90, v88, s62\n\t" \
  "v_perm_b32 v197, v90, v88, s63\n\t" \
  "s_waitcnt lgkmcnt(0)\n\t" \
  "v_perm_b32 v198, v94, v92, s62\n\t" \
  "v_perm_b32 v199, v94, v92, s63\n\t" \
  "v_perm_b32 v216, v83, v81, s62\n\t" \
  "v_perm_b32 v217, v83, v81, s63\n\t" \
  "v_perm_b32 v218, v87, v85, s62\n\t" \
  "v_perm_b32 v219, v87, v85, s63\n\t" \
  "v_perm_b32 v220, v91, v89, s62\n\t" \
  "v_perm_b32 v221, v91, v89, s63\n\t" \
  "v_perm_b32 v222, v95, v93, s62\n\t" \
  "v_perm_b32 v223, v95, v93, s63\n\t" \
  "v_add_u32 v78, 126976, v65\n\t" \
  "s_waitcnt vmcnt(12) lgkmcnt(0)\n\t" \
  "s_barrier\n\t" \
  "ds_read_b64_tr_b16 a[160:161], v78 offset:0\n\t" \
  "ds_read_b64_tr_b16 a[162:163], v78 offset:1024\n\t" \
  "ds_read_b64_tr_b16 a[164:165], v78 offset:8192\n\t" \
  "ds_read_b64_tr_b16 a[166:167], v78 offset:9216\n\t" \
  "ds_read_b64_tr_b16 a[168:169], v78 offset:8\n\t" \
  "ds_read_b64_tr_b16 a[170:171], v78 offset:1032\n\t" \
  "ds_read_b64_tr_b16 a[172:173], v78 offset:8200\n\t" \
  "ds_read_b64_tr_b16 a[174:175], v78 offset:9224\n\t" \
  "s_add_u32 m0, s54, 90112\n\t" \
  "s_mov_b32 s60, 2621440\n\t" \
  "buffer_load_dwordx4 v71, %[Wq], s60 offen lds\n\t" \
  "s_add_u32 m0, s54, 91136\n\t" \
  "s_mov_b32 s60, 2654208\n\t" \
  "buffer_load_dwordx4 v71, %[Wq], s60 offen lds\n\t" \
  "s_add_u32 m0, s54, 92160\n\t" \
  "s_mov_b32 s60, 2686976\n\t" \
  "buffer_load_dwordx4 v71, %[Wq], s60 offen lds\n\t" \
  "s_add_u32 m0, s54, 93184\n\t" \
  "s_mov_b32 s60, 2719744\n\t" \
  "buffer_load_dwordx4 v71, %[Wq], s60 offen lds\n\t" \
  "v_add_u32 v78, 0, v65\n\t" \
  "s_waitcnt vmcnt(12) lgkmcnt(0)\n\t" \
  "s_barrier\n\t" \
  "ds_read_b64_tr_b16 a[176:177], v78 offset:0\n\t" \
  "ds_read_b64_tr_b16 a[178:179], v78 offset:1024\n\t" \
  "ds_read_b64_tr_b16 a[180:181], v78 offset:8192\n\t" \
  "ds_read_b64_tr_b16 a[182:183], v78 offset:9216\n\t" \
  "ds_read_b64_tr_b16 a[184:185], v78 offset:8\n\t" \
  "ds_read_b64_tr_b16 a[186:187], v78 offset:1032\n\t" \
  "ds_read_b64_tr_b16 a[188:189], v78 offset:8200\n\t" \
  "ds_read_b64_tr_b16 a[190:191], v78 offset:9224\n\t" \
  "s_add_u32 m0, s54, 126976\n\t" \
  "s_add_u32 s60, s75, 0\n\t" \
  "buffer_load_dwordx4 v75, %[Wo], s60 offen lds\n\t" \
  "s_add_u32 m0, s54, 128000\n\t" \
  "s_add_u32 s60, s75, 9216\n\t" \
  "buffer_load_dwordx4 v75, %[Wo], s60 offen lds\n\t" \
  "s_add_u32 m0, s54, 129024\n\t" \
  "s_add_u32 s60, s75, 18432\n\t" \
  "buffer_load_dwordx4 v75, %[Wo], s60 offen lds\n\t" \
  "s_add_u32 m0, s54, 130048\n\t" \
  "s_add_u32 s60, s75, 27648\n\t" \
  "buffer_load_dwordx4 v75, %[Wo], s60 offen lds\n\t" \
  "v_add_u32 v78, 16384, v64\n\t" \
  "v_add_u32 v79, 17408, v64\n\t" \
  "s_waitcnt vmcnt(12) lgkmcnt(0)\n\t" \
  "s_barrier\n\t" \
  "ds_read2_b32 v[80:81], v78 offset0:0 offset1:2\n\t" \
  "ds_read2_b32 v[82:83], v78 offset0:32 offset1:34\n\t" \
  "ds_read2_b32 v[84:85], v78 offset0:64 offset1:66\n\t" \
  "ds_read2_b32 v[86:87], v78 offset0:96 offset1:98\n\t" \
  "ds_read2_b32 v[88:89], v79 offset0:0 offset1:2\n\t" \
  "ds_read2_b32 v[90:91], v79 offset0:32 offset1:34\n\t" \
  "ds_read2_b32 v[92:93], v79 offset0:64 offset1:66\n\t" \
  "ds_read2_b32 v[94:95], v79 offset0:96 offset1:98\n\t" \
  "s_add_u32 m0, s54, 0\n\t" \
  "s_mov_b32 s60, 2621440\n\t" \
  "buffer_load_dwordx4 v71, %[Wk], s60 offen lds\n\t" \
  "s_add_u32 m0, s54, 1024\n\t" \
  "s_mov_b32 s60, 2654208\n\t" \
  "buffer_load_dwordx4 v71, %[Wk], s60 offen lds\n\t" \
  "s_add_u32 m0, s54, 2048\n\t" \
  "s_mov_b32 s60, 2686976\n\t" \
  "buffer_load_dwordx4 v71, %[Wk], s60 offen lds\n\t" \
  "s_add_u32 m0, s54, 3072\n\t" \
  "s_mov_b32 s60, 2719744\n\t" \
  "buffer_load_dwordx4 v71, %[Wk], s60 offen lds\n\t" \
  "s_waitcnt lgkmcnt(6)\n\t" \
  "v_perm_b32 v224, v82, v80, s62\n\t" \
  "v_perm_b32 v225, v82, v80, s63\n\t" \
  "s_waitcnt lgkmcnt(4)\n\t" \
  "v_perm_b32 v226, v86, v84, s62\n\t" \
  "v_perm_b32 v227, v86, v84, s63\n\t" \
  "s_waitcnt lgkmcnt(2)\n\t" \
  "v_perm_b32 v228, v90, v88, s62\n\t" \
  "v_perm_b32 v229, v90, v88, s63\n\t" \
  "s_waitcnt lgkmcnt(0)\n\t" \
  "v_perm_b32 v230, v94, v92, s62\n\t" \
  "v_perm_b32 v231, v94, v92, s63\n\t" \
  "v_perm_b32 v120, v83, v81, s62\n\t" \
  "v_perm_b32 v121, v83, v81, s63\n\t" \
  "v_perm_b32 v122, v87, v85, s62\n\t" \
  "v_perm_b32 v123, v87, v85, s63\n\t" \
  "v_perm_b32 v124, v91, v89, s62\n\t" \
  "v_perm_b32 v125, v91, v89, s63\n\t" \
  "v_perm_b32 v126, v95, v93, s62\n\t" \
  "v_perm_b32 v127, v95, v93, s63\n\t" \
  "ds_write_b128 v70, v[120:123] offset:32768\n\t" \
  "ds_write_b128 v70, v[124:127] offset:33792\n\t" \
  "v_add_u32 v78, 53248, v66\n\t" \
  "s_waitcnt vmcnt(12) lgkmcnt(0)\n\t" \
  "s_barrier\n\t" \
  "ds_read_b64_tr_b16 a[192:193], v78 offset:0\n\t" \
  "ds_read_b64_tr_b16 a[194:195], v78 offset:1024\n\t" \
  "ds_read_b64_tr_b16 a[196:197], v78 offset:8192\n\t" \
  "ds_read_b64_tr_b16 a[198:199], v78 offset:9216\n\t" \
  "ds_read_b64_tr_b16 v[48:49], v78 offset:32\n\t" \
  "ds_read_b64_tr_b16 v[50:51], v78 offset:1056\n\t" \
  "ds_read_b64_tr_b16 v[52:53], v78 offset:8224\n\t" \
  "ds_read_b64_tr_b16 v[54:55], v78 offset:9248\n\t" \
  "s_add_u32 m0, s54, 16384\n\t" \
  "s_mov_b32 s60, 2621440\n\t" \
  "buffer_load_dwordx4 v71, %[Wv], s60 offen lds\n\t" \
  "s_add_u32 m0, s54, 17408\n\t" \
  "s_mov_b32 s60, 2654208\n\t" \
  "buffer_load_dwordx4 v71, %[Wv], s60 offen lds\n\t" \
  "s_add_u32 m0, s54, 18432\n\t" \
  "s_mov_b32 s60, 2686976\n\t" \
  "buffer_load_dwordx4 v71, %[Wv], s60 offen lds\n\t" \
  "s_add_u32 m0, s54, 19456\n\t" \
  "s_mov_b32 s60, 2719744\n\t" \
  "buffer_load_dwordx4 v71, %[Wv], s60 offen lds\n\t" \
  "v_add_u32 v78, 90112, v65\n\t" \
  "s_waitcnt vmcnt(12) lgkmcnt(0)\n\t" \
  "s_barrier\n\t" \
  "ds_read_b64_tr_b16 a[200:201], v78 offset:0\n\t" \
  "ds_read_b64_tr_b16 a[202:203], v78 offset:1024\n\t" \
  "ds_read_b64_tr_b16 a[204:205], v78 offset:8192\n\t" \
  "ds_read_b64_tr_b16 a[206:207], v78 offset:9216\n\t" \
  "ds_read_b64_tr_b16 v[24:25], v78 offset:8\n\t" \
  "ds_read_b64_tr_b16 v[26:27], v78 offset:1032\n\t" \
  "ds_read_b64_tr_b16 v[28:29], v78 offset:8200\n\t" \
  "ds_read_b64_tr_b16 v[30:31], v78 offset:9224\n\t" \
  "s_add_u32 m0, s54, 53248\n\t" \
  "s_mov_b32 s60, 3145728\n\t" \
  "buffer_load_dwordx4 v71, %[Wq], s60 offen lds\n\t" \
  "s_add_u32 m0, s54, 54272\n\t" \
  "s_mov_b32 s60, 3178496\n\t" \
  "buffer_load_dwordx4 v71, %[Wq], s60 offen lds\n\t" \
  "s_add_u32 m0, s54, 55296\n\t" \
  "s_mov_b32 s60, 3211264\n\t" \
  "buffer_load_dwordx4 v71, %[Wq], s60 offen lds\n\t" \
  "s_add_u32 m0, s54, 56320\n\t" \
  "s_mov_b32 s60, 3244032\n\t" \
  "buffer_load_dwordx4 v71, %[Wq], s60 offen lds\n\t" \
  "v_add_u32 v78, 126976, v67\n\t" \
  "s_waitcnt vmcnt(12) lgkmcnt(0)\n\t" \
  "s_barrier\n\t" \
  "ds_read_u16 v80, v78 offset:0\n\t" \
  "ds_read_u16 v81, v78 offset:128\n\t" \
  "ds_read_u16 v82, v78 offset:4096\n\t" \
  "ds_read_u16 v83, v78 offset:4224\n\t" \
  "ds_read_u16 v84, v78 offset:256\n\t" \
  "ds_read_u16 v85, v78 offset:384\n\t" \
  "ds_read_u16 v86, v78 offset:4352\n\t" \
  "ds_read_u16 v87, v78 offset:4480\n\t" \
  "ds_read_u16 v88, v78 offset:1024\n\t" \
  "ds_read_u16 v89, v78 offset:1152\n\t" \
  "ds_read_u16 v90, v78 offset:5120\n\t" \
  "ds_read_u16 v91, v78 offset:5248\n\t" \
  "ds_read_u16 v92, v78 offset:1280\n\t" \
  "ds_read_u16 v93, v78 offset:1408\n\t" \
  "ds_read_u16 v94, v78 offset:5376\n\t" \
  "ds_read_u16 v95, v78 offset:5504\n\t" \
  "ds_read_u16 v96, v78 offset:8192\n\t" \
  "ds_read_u16 v97, v78 offset:8320\n\t" \
  "ds_read_u16 v98, v78 offset:12288\n\t" \
  "ds_read_u16 v99, v78 offset:12416\n\t" \
  "ds_read_u16 v100, v78 offset:8448\n\t" \
  "ds_read_u16 v101, v78 offset:8576\n\t" \
  "ds_read_u16 v102, v78 offset:12544\n\t" \
  "ds_read_u16 v103, v78 offset:12672\n\t" \
  "ds_read_u16 v104, v78 offset:9216\n\t" \
  "ds_read_u16 v105, v78 offset:9344\n\t" \
  "ds_read_u16 v106, v78 offset:13312\n\t" \
  "ds_read_u16 v107, v78 offset:13440\n\t" \
  "ds_read_u16 v108, v78 offset:9472\n\t" \
  "ds_read_u16 v109, v78 offset:9600\n\t" \
  "ds_read_u16 v110, v78 offset:13568\n\t" \
  "ds_read_u16 v111, v78 offset:13696\n\t" \
  "s_add_u32 m0, s54, 90112\n\t" \
  "s_mov_b32 s60, 3145728\n\t" \
  "buffer_load_dwordx4 v71, %[Wk], s60 offen lds\n\t" \
  "s_add_u32 m0, s54, 91136\n\t" \
  "s_mov_b32 s60, 3178496\n\t" \
  "buffer_load_dwordx4 v71, %[Wk], s60 offen lds\n\t" \
  "s_add_u32 m0, s54, 92160\n\t" \
  "s_mov_b32 s60, 3211264\n\t" \
  "buffer_load_dwordx4 v71, %[Wk], s60 offen lds\n\t" \
  "s_add_u32 m0, s54, 93184\n\t" \
  "s_mov_b32 s60, 3244032\n\t" \
  "buffer_load_dwordx4 v71, %[Wk], s60 offen lds\n\t" \
  "s_waitcnt lgkmcnt(0)\n\t" \
  "v_lshl_or_b32 v248, v81, 16, v80\n\t" \
  "v_lshl_or_b32 v249, v83, 16, v82\n\t" \
  "v_lshl_or_b32 v250, v85, 16, v84\n\t" \
  "v_lshl_or_b32 v251, v87, 16, v86\n\t" \
  "v_lshl_or_b32 v252, v89, 16, v88\n\t" \
  "v_lshl_or_b32 v253, v91, 16, v90\n\t" \
  "v_lshl_or_b32 v254, v93, 16, v92\n\t" \
  "v_lshl_or_b32 v255, v95, 16, v94\n\t" \
  "v_lshl_or_b32 v112, v97, 16, v96\n\t" \
  "v_lshl_or_b32 v113, v99, 16, v98\n\t" \
  "v_lshl_or_b32 v114, v101, 16, v100\n\t" \
  "v_lshl_or_b32 v115, v103, 16, v102\n\t" \
  "v_lshl_or_b32 v116, v105, 16, v104\n\t" \
  "v_lshl_or_b32 v117, v107, 16, v106\n\t" \
  "v_lshl_or_b32 v118, v109, 16, v108\n\t" \
  "v_lshl_or_b32 v119, v111, 16, v110\n\t" \
  "ds_write_b128 v70, v[112:115] offset:34816\n\t" \
  "ds_write_b128 v70, v[116:119] offset:35840\n\t" \
  "v_add_u32 v78, 0, v65\n\t" \
  "s_waitcnt vmcnt(12) lgkmcnt(0)\n\t" \
  "s_barrier\n\t" \
  "ds_read_b64_tr_b16 a[208:209], v78 offset:0\n\t" \
  "ds_read_b64_tr_b16 a[210:211], v78 offset:1024\n\t" \
  "ds_read_b64_tr_b16 a[212:213], v78 offset:8192\n\t" \
  "ds_read_b64_tr_b16 a[214:215], v78 offset:9216\n\t" \
  "ds_read_b64_tr_b16 a[216:217], v78 offset:8\n\t" \
  "ds_read_b64_tr_b16 a[218:219], v78 offset:1032\n\t" \
  "ds_read_b64_tr_b16 a[220:221], v78 offset:8200\n\t" \
  "ds_read_b64_tr_b16 a[222:223], v78 offset:9224\n\t" \
  "s_add_u32 m0, s54, 126976\n\t" \
  "s_mov_b32 s60, 3145728\n\t" \
  "buffer_load_dwordx4 v71, %[Wv], s60 offen lds\n\t" \
  "s_add_u32 m0, s54, 128000\n\t" \
  "s_mov_b32 s60, 3178496\n\t" \
  "buffer_load_dwordx4 v71, %[Wv], s60 offen lds\n\t" \
  "s_add_u32 m0, s54, 129024\n\t" \
  "s_mov_b32 s60, 3211264\n\t" \
  "buffer_load_dwordx4 v71, %[Wv], s60 offen lds\n\t" \
  "s_add_u32 m0, s54, 130048\n\t" \
  "s_mov_b32 s60, 3244032\n\t" \
  "buffer_load_dwordx4 v71, %[Wv], s60 offen lds\n\t" \
  "v_add_u32 v78, 16384, v66\n\t" \
  "s_waitcnt vmcnt(12) lgkmcnt(0)\n\t" \
  "s_barrier\n\t" \
  "ds_read_b64_tr_b16 a[224:225], v78 offset:0\n\t" \
  "ds_read_b64_tr_b16 a[226:227], v78 offset:1024\n\t" \
  "ds_read_b64_tr_b16 a[228:229], v78 offset:8192\n\t" \
  "ds_read_b64_tr_b16 a[230:231], v78 offset:9216\n\t" \
  "ds_read_b64_tr_b16 a[232:233], v78 offset:32\n\t" \
  "ds_read_b64_tr_b16 a[234:235], v78 offset:1056\n\t" \
  "ds_read_b64_tr_b16 a[236:237], v78 offset:8224\n\t" \
  "ds_read_b64_tr_b16 a[238:239], v78 offset:9248\n\t" \
  "s_add_u32 m0, s54, 0\n\t" \
  "s_mov_b32 s60, 3670016\n\t" \
  "buffer_load_dwordx4 v71, %[Wq], s60 offen lds\n\t" \
  "s_add_u32 m0, s54, 1024\n\t" \
  "s_mov_b32 s60, 3702784\n\t" \
  "buffer_load_dwordx4 v71, %[Wq], s60 offen lds\n\t" \
  "s_add_u32 m0, s54, 2048\n\t" \
  "s_mov_b32 s60, 3735552\n\t" \
  "buffer_load_dwordx4 v71, %[Wq], s60 offen lds\n\t" \
  "s_add_u32 m0, s54, 3072\n\t" \
  "s_mov_b32 s60, 3768320\n\t" \
  "buffer_load_dwordx4 v71, %[Wq], s60 offen lds\n\t" \
  "v_add_u32 v78, 53248, v65\n\t" \
  "s_waitcnt vmcnt(12) lgkmcnt(0)\n\t" \
  "s_barrier\n\t" \
  "ds_read_b64_tr_b16 a[240:241], v78 offset:0\n\t" \
  "ds_read_b64_tr_b16 a[242:243], v78 offset:1024\n\t" \
  "ds_read_b64_tr_b16 a[244:245], v78 offset:8192\n\t" \
  "ds_read_b64_tr_b16 a[246:247], v78 offset:9216\n\t" \
  "ds_read_b64_tr_b16 a[248:249], v78 offset:8\n\t" \
  "ds_read_b64_tr_b16 a[250:251], v78 offset:1032\n\t" \
  "ds_read_b64_tr_b16 a[252:253], v78 offset:8200\n\t" \
  "ds_read_b64_tr_b16 a[254:255], v78 offset:9224\n\t" \
  "s_add_u32 m0, s54, 16384\n\t" \
  "s_mov_b32 s60, 3670016\n\t" \
  "buffer_load_dwordx4 v71, %[Wk], s60 offen lds\n\t" \
  "s_add_u32 m0, s54, 17408\n\t" \
  "s_mov_b32 s60, 3702784\n\t" \
  "buffer_load_dwordx4 v71, %[Wk], s60 offen lds\n\t" \
  "s_add_u32 m0, s54, 18432\n\t" \
  "s_mov_b32 s60, 3735552\n\t" \
  "buffer_load_dwordx4 v71, %[Wk], s60 offen lds\n\t" \
  "s_add_u32 m0, s54, 19456\n\t" \
  "s_mov_b32 s60, 3768320\n\t" \
  "buffer_load_dwordx4 v71, %[Wk], s60 offen lds\n\t" \
  "v_add_u32 v78, 90112, v65\n\t" \
  "s_waitcnt vmcnt(12) lgkmcnt(0)\n\t" \
  "s_barrier\n\t" \
  "ds_read_b64_tr_b16 v[80:81], v78 offset:0\n\t" \
  "ds_read_b64_tr_b16 v[82:83], v78 offset:1024\n\t" \
  "ds_read_b64_tr_b16 v[84:85], v78 offset:8192\n\t" \
  "ds_read_b64_tr_b16 v[86:87], v78 offset:9216\n\t" \
  "ds_read_b64_tr_b16 v[88:89], v78 offset:8\n\t" \
  "ds_read_b64_tr_b16 v[90:91], v78 offset:1032\n\t" \
  "ds_read_b64_tr_b16 v[92:93], v78 offset:8200\n\t" \
  "ds_read_b64_tr_b16 v[94:95], v78 offset:9224\n\t" \
  "s_add_u32 m0, s54, 53248\n\t" \
  "s_mov_b32 s60, 3670016\n\t" \
  "buffer_load_dwordx4 v71, %[Wv], s60 offen lds\n\t" \
  "s_add_u32 m0, s54, 54272\n\t" \
  "s_mov_b32 s60, 3702784\n\t" \
  "buffer_load_dwordx4 v71, %[Wv], s60 offen lds\n\t" \
  "s_add_u32 m0, s54, 55296\n\t" \
  "s_mov_b32 s60, 3735552\n\t" \
  "buffer_load_dwordx4 v71, %[Wv], s60 offen lds\n\t" \
  "s_add_u32 m0, s54, 56320\n\t" \
  "s_mov_b32 s60, 3768320\n\t" \
  "buffer_load_dwordx4 v71, %[Wv], s60 offen lds\n\t" \
  "v_add_u32 v78, 126976, v66\n\t" \
  "s_waitcnt vmcnt(12) lgkmcnt(0)\n\t" \
  "s_barrier\n\t" \
  "ds_read_b64_tr_b16 v[96:97], v78 offset:0\n\t" \
  "ds_read_b64_tr_b16 v[98:99], v78 offset:1024\n\t" \
  "ds_read_b64_tr_b16 v[100:101], v78 offset:8192\n\t" \
  "ds_read_b64_tr_b16 v[102:103], v78 offset:9216\n\t" \
  "ds_read_b64_tr_b16 v[104:105], v78 offset:32\n\t" \
  "ds_read_b64_tr_b16 v[106:107], v78 offset:1056\n\t" \
  "ds_read_b64_tr_b16 v[108:109], v78 offset:8224\n\t" \
  "ds_read_b64_tr_b16 v[110:111], v78 offset:9248\n\t" \
  "s_add_u32 m0, s54, 90112\n\t" \
  "s_mov_b32 s60, 4194304\n\t" \
  "buffer_load_dwordx4 v71, %[Wq], s60 offen lds\n\t" \
  "s_add_u32 m0, s54, 91136\n\t" \
  "s_mov_b32 s60, 4227072\n\t" \
  "buffer_load_dwordx4 v71, %[Wq], s60 offen lds\n\t" \
  "s_add_u32 m0, s54, 92160\n\t" \
  "s_mov_b32 s60, 4259840\n\t" \
  "buffer_load_dwordx4 v71, %[Wq], s60 offen lds\n\t" \
  "s_add_u32 m0, s54, 93184\n\t" \
  "s_mov_b32 s60, 4292608\n\t" \
  "buffer_load_dwordx4 v71, %[Wq], s60 offen lds\n\t" \
  "v_add_u32 v78, 0, v65\n\t" \
  "s_waitcnt vmcnt(12) lgkmcnt(0)\n\t" \
  "s_barrier\n\t" \
  "ds_read_b64_tr_b16 v[112:113], v78 offset:0\n\t" \
  "ds_read_b64_tr_b16 v[114:115], v78 offset:1024\n\t" \
  "ds_read_b64_tr_b16 v[116:117], v78 offset:8192\n\t" \
  "ds_read_b64_tr_b16 v[118:119], v78 offset:9216\n\t" \
  "ds_read_b64_tr_b16 v[120:121], v78 offset:8\n\t" \
  "ds_read_b64_tr_b16 v[122:123], v78 offset:1032\n\t" \
  "ds_read_b64_tr_b16 v[124:125], v78 offset:8200\n\t" \
  "ds_read_b64_tr_b16 v[126:127], v78 offset:9224\n\t" \
  "s_add_u32 m0, s54, 126976\n\t" \
  "s_mov_b32 s60, 4194304\n\t" \
  "buffer_load_dwordx4 v71, %[Wk], s60 offen lds\n\t" \
  "s_add_u32 m0, s54, 128000\n\t" \
  "s_mov_b32 s60, 4227072\n\t" \
  "buffer_load_dwordx4 v71, %[Wk], s60 offen lds\n\t" \
  "s_add_u32 m0, s54, 129024\n\t" \
  "s_mov_b32 s60, 4259840\n\t" \
  "buffer_load_dwordx4 v71, %[Wk], s60 offen lds\n\t" \
  "s_add_u32 m0, s54, 130048\n\t" \
  "s_mov_b32 s60, 4292608\n\t" \
  "buffer_load_dwordx4 v71, %[Wk], s60 offen lds\n\t" \
  "v_add_u32 v78, 16384, v65\n\t" \
  "s_waitcnt vmcnt(12) lgkmcnt(0)\n\t" \
  "s_barrier\n\t" \
  "ds_read_b64_tr_b16 v[128:129], v78 offset:0\n\t" \
  "ds_read_b64_tr_b16 v[130:131], v78 offset:1024\n\t" \
  "ds_read_b64_tr_b16 v[132:133], v78 offset:8192\n\t" \
  "ds_read_b64_tr_b16 v[134:135], v78 offset:9216\n\t" \
  "ds_read_b64_tr_b16 v[136:137], v78 offset:8\n\t" \
  "ds_read_b64_tr_b16 v[138:139], v78 offset:1032\n\t" \
  "ds_read_b64_tr_b16 v[140:141], v78 offset:8200\n\t" \
  "ds_read_b64_tr_b16 v[142:143], v78 offset:9224\n\t" \
  "s_add_u32 m0, s54, 0\n\t" \
  "s_mov_b32 s60, 4194304\n\t" \
  "buffer_load_dwordx4 v71, %[Wv], s60 offen lds\n\t" \
  "s_add_u32 m0, s54, 1024\n\t" \
  "s_mov_b32 s60, 4227072\n\t" \
  "buffer_load_dwordx4 v71, %[Wv], s60 offen lds\n\t" \
  "s_add_u32 m0, s54, 2048\n\t" \
  "s_mov_b32 s60, 4259840\n\t" \
  "buffer_load_dwordx4 v71, %[Wv], s60 offen lds\n\t" \
  "s_add_u32 m0, s54, 3072\n\t" \
  "s_mov_b32 s60, 4292608\n\t" \
  "buffer_load_dwordx4 v71, %[Wv], s60 offen lds\n\t" \
  "v_add_u32 v78, 53248, v66\n\t" \
  "s_waitcnt vmcnt(12) lgkmcnt(0)\n\t" \
  "s_barrier\n\t" \
  "ds_read_b64_tr_b16 v[144:145], v78 offset:0\n\t" \
  "ds_read_b64_tr_b16 v[146:147], v78 offset:1024\n\t" \
  "ds_read_b64_tr_b16 v[148:149], v78 offset:8192\n\t" \
  "ds_read_b64_tr_b16 v[150:151], v78 offset:9216\n\t" \
  "ds_read_b64_tr_b16 v[152:153], v78 offset:32\n\t" \
  "ds_read_b64_tr_b16 v[154:155], v78 offset:1056\n\t" \
  "ds_read_b64_tr_b16 v[156:157], v78 offset:8224\n\t" \
  "ds_read_b64_tr_b16 v[158:159], v78 offset:9248\n\t" \
  "v_add_u32 v78, 90112, v65\n\t" \
  "s_waitcnt vmcnt(8) lgkmcnt(0)\n\t" \
  "s_barrier\n\t" \
  "ds_read_b64_tr_b16 v[160:161], v78 offset:0\n\t" \
  "ds_read_b64_tr_b16 v[162:163], v78 offset:1024\n\t" \
  "ds_read_b64_tr_b16 v[164:165], v78 offset:8192\n\t" \
  "ds_read_b64_tr_b16 v[166:167], v78 offset:9216\n\t" \
  "ds_read_b64_tr_b16 v[168:169], v78 offset:8\n\t" \
  "ds_read_b64_tr_b16 v[170:171], v78 offset:1032\n\t" \
  "ds_read_b64_tr_b16 v[172:173], v78 offset:8200\n\t" \
  "ds_read_b64_tr_b16 v[174:175], v78 offset:9224\n\t" \
  "v_add_u32 v78, 126976, v65\n\t" \
  "s_waitcnt vmcnt(4) lgkmcnt(0)\n\t" \
  "s_barrier\n\t" \
  "ds_read_b64_tr_b16 v[40:41], v78 offset:0\n\t" \
  "ds_read_b64_tr_b16 v[42:43], v78 offset:1024\n\t" \
  "ds_read_b64_tr_b16 v[44:45], v78 offset:8192\n\t" \
  "ds_read_b64_tr_b16 v[46:47], v78 offset:9216\n\t" \
  "ds_read_b64_tr_b16 v[176:177], v78 offset:8\n\t" \
  "ds_read_b64_tr_b16 v[178:179], v78 offset:1032\n\t" \
  "ds_read_b64_tr_b16 v[180:181], v78 offset:8200\n\t" \
  "ds_read_b64_tr_b16 v[182:183], v78 offset:9224\n\t" \
  "v_add_u32 v78, 0, v66\n\t" \
  "s_waitcnt vmcnt(0) lgkmcnt(0)\n\t" \
  "s_barrier\n\t" \
  "ds_read_b64_tr_b16 v[184:185], v78 offset:0\n\t" \
  "ds_read_b64_tr_b16 v[186:187], v78 offset:1024\n\t" \
  "ds_read_b64_tr_b16 v[188:189], v78 offset:8192\n\t" \
  "ds_read_b64_tr_b16 v[190:191], v78 offset:9216\n\t" \
  "ds_read_b64_tr_b16 v[56:57], v78 offset:32\n\t" \
  "ds_read_b64_tr_b16 v[58:59], v78 offset:1056\n\t" \
  "ds_read_b64_tr_b16 v[60:61], v78 offset:8224\n\t" \
  "ds_read_b64_tr_b16 v[62:63], v78 offset:9248\n\t" \
  "s_waitcnt lgkmcnt(0)\n\t" \
  "s_barrier\n\t" \
  "ds_write_b128 v70, v[0:3] offset:0\n\t" \
  "ds_write_b128 v70, v[4:7] offset:1024\n\t" \
  "ds_write_b128 v70, v[8:11] offset:2048\n\t" \
  "ds_write_b128 v70, v[12:15] offset:3072\n\t" \
  "ds_write_b128 v70, v[16:19] offset:4096\n\t" \
  "ds_write_b128 v70, v[20:23] offset:5120\n\t" \
  "ds_write_b128 v70, v[24:27] offset:6144\n\t" \
  "ds_write_b128 v70, v[28:31] offset:7168\n\t" \
  "ds_write_b128 v70, v[32:35] offset:8192\n\t" \
  "ds_write_b128 v70, v[36:39] offset:9216\n\t" \
  "ds_write_b128 v70, v[40:43] offset:10240\n\t" \
  "ds_write_b128 v70, v[44:47] offset:11264\n\t" \
  "ds_write_b128 v70, v[48:51] offset:12288\n\t" \
  "ds_write_b128 v70, v[52:55] offset:13312\n\t" \
  "ds_write_b128 v70, v[56:59] offset:14336\n\t" \
  "ds_write_b128 v70, v[60:63] offset:15360\n\t" \
  "s_waitcnt lgkmcnt(0)\n\t" \
  "v_and_b32 v72, 1, v72\n\t" \
  "v_and_b32 v74, 1, v74\n\t" \
  "v_cmp_ne_u32 s[90:91], s32, v72\n\t" \
  "v_cmp_ne_u32 vcc, s32, v74\n\t" \
  "s_nop 1\n\t" \
  "s_mov_b32 s96, vcc_lo\n\t" \
  "s_mov_b32 s21, vcc_lo\n\t" \
  "v_and_b32 v73, 32, v69\n\t" \
  "v_lshl_add_u32 v73, v73, 5, v69\n\t" \
  "s_mov_b32 s35, s30\n\t" \
  "s_cmp_lt_u32 s32, " CH_DRYL "\n\t" \
  "s_cbranch_scc1 LINI_%=\n\t" \
  "s_mov_b32 s30, 0\n\t" \
  "s_mov_b32 s34, 0\n\t" \
  "s_cmp_eq_u32 s40, 0\n\t" \
  "s_cbranch_scc1 LW0_%=\n\t" \
  "s_barrier\n\t" \
  "s_cmp_eq_u32 s40, 1\n\t" \
  "s_cbranch_scc1 LG1_%=\n\t" \
  "s_cmp_eq_u32 s40, 2\n\t" \
  "s_cbranch_scc1 LG2_%=\n\t" \
  "s_branch LG3_%=\n\t" \
  "LINI_%=:\n\t" \
  "s_waitcnt vmcnt(0) lgkmcnt(0)\n\t" \
  "s_mov_b32 s30, s35\n\t" \
  "s_movk_i32 s34, 1000\n\t" \
  "s_mov_b32 s48, -1\n\t" \
  "s_add_u32 s97, s41, s53\n\t" \
  "s_add_u32 s98, s37, s53\n\t" \
  "s_add_u32 s82, s48, 0\n\t" \
  "s_and_b32 s82, s82, " CH_TRM "\n\t" \
  "s_lshr_b64 s[60:61], s[90:91], s82\n\t" \
  "s_and_b32 s60, s60, 1\n\t" \
  "s_lshl_b32 s60, s60, 13\n\t" \
  "s_add_u32 s82, s82, " CH_SG0 "\n\t" \
  "s_lshl_b32 s82, s82, 21\n\t" \
  "s_or_b32 s82, s82, s60\n\t" \
  "s_add_u32 s50, s82, s38\n\t" \
  "s_add_u32 s69, s82, s39\n\t" \
  "s_add_u32 s82, s48, 1\n\t" \
  "s_lshr_b64 s[60:61], s[90:91], s82\n\t" \
  "s_and_b32 s60, s60, 1\n\t" \
  "s_lshl_b32 s60, s60, 13\n\t" \
  "s_add_u32 s82, s82, " CH_SG0 "\n\t" \
  "s_lshl_b32 s82, s82, 21\n\t" \
  "s_or_b32 s82, s82, s60\n\t" \
  "s_add_u32 s52, s82, s43\n\t" \
  "s_add_u32 s82, s48, " CH_ARMD "\n\t" \
  "s_lshr_b64 s[60:61], s[90:91], s82\n\t" \
  "s_and_b32 s60, s60, 1\n\t" \
  "s_lshl_b32 s60, s60, 13\n\t" \
  "s_add_u32 s82, s82, " CH_SG0 "\n\t" \
  "s_lshl_b32 s82, s82, 21\n\t" \
  "s_or_b32 s82, s82, s60\n\t" \
  "s_add_u32 s85, s82, s45\n\t" \
  "s_cmp_lt_i32 s48, " CH_ARMS "\n\t" \
  "s_cselect_b32 s85, s27, s85\n\t" \
  "v_and_b32 v37, 0x30, v69\n\t" \
  "v_add_u32 v37, s52, v37\n\t" \
  "s_mov_b32 s48, 0\n\t" \
  "s_mov_b32 s86, 1\n\t" \
  "s_and_b32 s49, s96, 1\n\t" \
  "s_lshl_b32 s49, s49, 13\n\t" \
  "s_add_u32 s53, s49, " CH_SLOT "\n\t" \
  "s_movk_i32 s20, 2560\n\t" \
  "s_mov_b32 s22, 0\n\t" \
  "s_movk_i32 s60, 124\n\t" \
  "s_cmp_eq_u32 s40, 3\n\t" \
  "s_cselect_b32 s81, 76, s60\n\t" \
  "s_add_u32 s82, s48, 0\n\t" \
  "s_and_b32 s82, s82, " CH_TRM "\n\t" \
  "s_lshr_b64 s[60:61], s[90:91], s82\n\t" \
  "s_and_b32 s60, s60, 1\n\t" \
  "s_lshl_b32 s60, s60, 13\n\t" \
  "s_add_u32 s82, s82, " CH_SG0 "\n\t" \
  "s_lshl_b32 s82, s82, 21\n\t" \
  "s_or_b32 s82, s82, s60\n\t" \
  "s_add_u32 s50, s82, s38\n\t" \
  "s_add_u32 s69, s82, s39\n\t" \
  "s_add_u32 s82, s48, 0\n\t" \
  "s_lshr_b64 s[60:61], s[90:91], s82\n\t" \
  "s_and_b32 s60, s60, 1\n\t" \
  "s_lshl_b32 s60, s60, 13\n\t" \
  "s_add_u32 s82, s82, " CH_SG0 "\n\t" \
  "s_lshl_b32 s82, s82, 21\n\t" \
  "s_or_b32 s82, s82, s60\n\t" \
  "s_add_u32 s51, s82, s43\n\t" \
  "s_add_u32 s97, s41, s53\n\t" \
  "s_add_u32 s98, s37, s53\n\t" \
  "s_add_u32 s82, s48, 0\n\t" \
  "s_and_b32 s82, s82, " CH_TRM "\n\t" \
  "s_lshr_b64 s[60:61], s[90:91], s82\n\t" \
  "s_and_b32 s60, s60, 1\n\t" \
  "s_lshl_b32 s60, s60, 13\n\t" \
  "s_add_u32 s82, s82, " CH_SG0 "\n\t" \
  "s_lshl_b32 s82, s82, 21\n\t" \
  "s_or_b32 s82, s82, s60\n\t" \
  "s_add_u32 s50, s82, s38\n\t" \
  "s_add_u32 s69, s82, s39\n\t" \
  "s_add_u32 s82, s48, 1\n\t" \
  "s_lshr_b64 s[60:61], s[90:91], s82\n\t" \
  "s_and_b32 s60, s60, 1\n\t" \
  "s_lshl_b32 s60, s60, 13\n\t" \
  "s_add_u32 s82, s82, " CH_SG0 "\n\t" \
  "s_lshl_b32 s82, s82, 21\n\t" \
  "s_or_b32 s82, s82, s60\n\t" \
  "s_add_u32 s52, s82, s43\n\t" \
  "s_add_u32 s82, s48, " CH_ARMD "\n\t" \
  "s_lshr_b64 s[60:61], s[90:91], s82\n\t" \
  "s_and_b32 s60, s60, 1\n\t" \
  "s_lshl_b32 s60, s60, 13\n\t" \
  "s_add_u32 s82, s82, " CH_SG0 "\n\t" \
  "s_lshl_b32 s82, s82, 21\n\t" \
  "s_or_b32 s82, s82, s60\n\t" \
  "s_add_u32 s85, s82, s45\n\t" \
  "s_cmp_lt_i32 s48, " CH_ARMS "\n\t" \
  "s_cselect_b32 s85, s27, s85\n\t" \
  "s_memrealtime s[62:63]\n\t" \
  "s_waitcnt lgkmcnt(0)\n\t" \
  "s_mov_b32 s65, s62\n\t" \
  "s_sub_u32 s47, s62, 1000\n\t" \
  "s_mov_b64 vcc, -1\n\t" \
  "s_cmp_eq_u32 s40, 0\n\t" \
  "s_cbranch_scc1 LW0_%=\n\t" \
  "s_barrier\n\t" \
  "s_cmp_eq_u32 s40, 1\n\t" \
  "s_cbranch_scc1 LG1_%=\n\t" \
  "s_cmp_eq_u32 s40, 2\n\t" \
  "s_cbranch_scc1 LG2_%=\n\t" \
  "s_branch LTK_%=\n\t" \
  ".p2align 6\n\t" \
  "LW0_%=:\n\t" \
  "v_lshlrev_b32 v78, 2, v69\n\t" \
  "v_and_b32 v78, 0x300, v78\n\t" \
  "v_lshrrev_b32 v79, 8, v69\n\t" \
  "v_lshl_add_u32 v78, v79, 6, v78\n\t" \
  "v_and_b32 v79, 48, v69\n\t" \
  "v_add_u32 v78, v78, v79\n\t" \
  "s_add_u32 s98, s49, s42\n\t" \
  "s_add_u32 s97, s98, 32768\n\t" \
  "s_add_u32 s99, s98, 65536\n\t" \
  "s_barrier\n\t" \
  "s_memrealtime s[62:63]\n\t" \
  "s_waitcnt lgkmcnt(0)\n\t" \
  "s_add_u32 s62, s62, " CH_DLL2 "\n\t" \
  "s_sub_i32 s63, s62, s65\n\t" \
  "s_cmp_gt_i32 s63, " CH_DLT "\n\t" \
  "s_cselect_b32 s60, " CH_DLY ", " CH_DLX "\n\t" \
  "s_add_u32 s62, s62, s60\n\t" \
  "s_cmp_gt_i32 s63, 0\n\t" \
  "s_cselect_b32 s65, s62, s65\n\t" \
  "s_mov_b32 m0, 0\n\t" \
  "s_movk_i32 s60, 8192\n\t" \
  CH_ALW \
  "LWT_%=:\n\t" \
  "s_memrealtime s[62:63]\n\t" \
  "s_sub_u32 s60, s60, 1\n\t" \
  "s_cselect_b32 s61, 0, -1\n\t" \
  "s_waitcnt lgkmcnt(0)\n\t" \
  "s_sub_i32 s63, s62, s65\n\t" \
  "s_and_b32 s63, s63, s61\n\t" \
  "s_cmp_lt_i32 s63, " CH_FIN "\n\t" \
  "s_cbranch_scc1 LWT_%=\n\t" \
  "s_cmp_lt_i32 s63, 0\n\t" \
  "s_cbranch_scc1 LWT_%=f\n\t" \
  "LWT_%=g:\n\t" \
  "LDMA_%=:\n\t" \
  "buffer_load_dwordx4 v78, s[28:31], s98 offen offset:0 sc1 lds\n\t" \
  "buffer_load_dwordx4 v78, s[28:31], s98 offen offset:1024 sc1 lds\n\t" \
  "buffer_load_dwordx4 v78, s[28:31], s98 offen offset:2048 sc1 lds\n\t" \
  "buffer_load_dwordx4 v78, s[28:31], s98 offen offset:3072 sc1 lds\n\t" \
  "s_movk_i32 m0, 4096\n\t" \
  "s_mov_b32 s66, s62\n\t" \
  "buffer_load_dwordx4 v78, s[28:31], s97 offen offset:0 sc1 lds\n\t" \
  "buffer_load_dwordx4 v78, s[28:31], s97 offen offset:1024 sc1 lds\n\t" \
  "buffer_load_dwordx4 v78, s[28:31], s97 offen offset:2048 sc1 lds\n\t" \
  "buffer_load_dwordx4 v78, s[28:31], s97 offen offset:3072 sc1 lds\n\t" \
  "s_movk_i32 m0, 8192\n\t" \
  "s_nop 0\n\t" \
  "buffer_load_dwordx4 v69, s[28:31], s99 offen sc1 lds\n\t" \
  ".p2align 6\n\t" \
  "LG0_%=:\n\t" \
  "s_barrier\n\t" \
  "buffer_load_dwordx4 v[52:55], v69, s[28:31], s50 offen sc1\n\t" \
  "buffer_load_dwordx4 v[56:59], v69, s[28:31], s50 offen offset:1024 sc1\n\t" \
  "s_add_u32 s64, s50, 16384\n\t" \
  "s_movk_i32 m0, 11264\n\t" \
  "v_lshlrev_b32 v75, 1, v69\n\t" \
  "buffer_load_dword v75, s[28:31], s64 offen sc1 lds\n\t" \
  "s_memrealtime s[62:63]\n\t" \
  "s_waitcnt lgkmcnt(0)\n\t" \
  "s_sub_u32 s62, s62, " CH_TKL "\n\t" \
  "s_sub_i32 s63, s62, s65\n\t" \
  "s_cmp_gt_i32 s63, 0\n\t" \
  "s_cselect_b32 s62, s62, s65\n\t" \
  "s_sub_i32 s63, s62, s47\n\t" \
  "s_cmp_gt_i32 s63, " CH_LATE "\n\t" \
  "s_cselect_b32 s71, " CH_SCHW ", s46\n\t" \
  "s_add_u32 s61, s47, " CH_LATE "\n\t" \
  "s_cmp_gt_i32 s63, " CH_LATE "\n\t" \
  "s_cselect_b32 s62, s62, s61\n\t" \
  "s_add_u32 s71, s71, s53\n\t" \
  "s_add_u32 s60, s62, s81\n\t" \
  "s_add_u32 s60, s60, " CH_DL1L "\n\t" \
  "s_lshl_b32 s60, s60, 8\n\t" \
  "s_or_b32 s60, s60, " CH_B0 "\n\t" \
  "s_cmp_lt_u32 s48, " CH_ES "\n\t" \
  "s_cselect_b32 s61, " CH_EX ", 0\n\t" \
  "s_add_u32 s60, s60, s61\n\t" \
  "s_not_b32 s60, s60\n\t" \
  "v_mov_b32 v76, s60\n\t" \
  "s_lshr_b32 s60, s46, 31\n\t" \
  "s_or_b32 s60, s60, s68\n\t" \
  "s_cselect_b32 s25, 0, 1\n\t" \
  "s_mov_b64 exec, s[24:25]\n\t" \
  "buffer_atomic_umin v76, v69, s[28:31], s71 offen sc1\n\t" \
  "s_mov_b64 exec, -1\n\t" \
  "ds_read_b128 v[44:47], v70 offset:0\n\t" \
  "ds_read_b128 v[48:51], v70 offset:1024\n\t" \
  "ds_read_b128 v[60:63], v70 offset:2048\n\t" \
  "ds_read_b128 v[64:67], v70 offset:3072\n\t" \
  "v_mov_b64 v[36:37], 0\n\t" \
  "v_mov_b64 v[38:39], 0\n\t" \
  "v_mov_b64 v[40:41], 0\n\t" \
  "v_mov_b64 v[42:43], 0\n\t" \
  "v_lshrrev_b32 v71, 1, v69\n\t" \
  "v_lshrrev_b32 v72, 3, v69\n\t" \
  "v_and_b32 v72, 0x78, v72\n\t" \
  "v_and_b32 v74, 0x30, v69\n\t" \
  "v_lshl_add_u32 v72, v74, 3, v72\n\t" \
  "s_add_u32 s54, s53, s36\n\t" \
  "s_add_u32 s55, s54, 524288\n\t" \
  "s_add_u32 s54, s54, 0\n\t" \
  "s_add_u32 s75, s54, 262144\n\t" \
  "s_add_u32 s80, s53, s26\n\t" \
  "s_or_b32 s87, s53, 0x1fff80\n\t" \
  "s_and_b32 s87, s87, -128\n\t" \
  "s_add_u32 s87, s87, 0x200000\n\t" \
  ".p2align 5\n\t" \
  ".fill 2, 4, 0xbf800000\n\t" \
  "s_waitcnt vmcnt(9)\n\t" \
  "s_barrier\n\t" \
  "ds_read_b128 v[0:3], v69 offset:0\n\t" \
  "ds_read_b128 v[4:7], v69 offset:1024\n\t" \
  "ds_read_b128 v[8:11], v69 offset:2048\n\t" \
  "ds_read_b128 v[12:15], v69 offset:3072\n\t" \
  "s_waitcnt lgkmcnt(3)\n\t" \
  "v_smfmac_f32_16x16x64_bf16 v[36:39], v[0:3], v[44:51], v68\n\t" \
  "ds_read_b128 v[44:47], v70 offset:4096\n\t" \
  "v_smfmac_f32_16x16x64_bf16 v[40:43], v[0:3], v[60:67], v68\n\t" \
  "ds_read_b128 v[48:51], v70 offset:5120\n\t" \
  "s_waitcnt lgkmcnt(4)\n\t" \
  "v_smfmac_f32_16x16x64_bf16 v[36:39], v[4:7], a[32:39], v68\n\t" \
  "ds_read_b128 v[60:63], v70 offset:6144\n\t" \
  "v_smfmac_f32_16x16x64_bf16 v[40:43], v[4:7], a[40:47], v68\n\t" \
  "ds_read_b128 v[64:67], v70 offset:7168\n\t" \
  "s_waitcnt lgkmcnt(5)\n\t" \
  "v_smfmac_f32_16x16x64_bf16 v[36:39], v[8:11], a[80:87], v68\n\t" \
  "s_waitcnt vmcnt(4)\n\t" \
  "s_barrier\n\t" \
  "ds_read_b128 v[16:19], v69 offset:4096\n\t" \
  "v_smfmac_f32_16x16x64_bf16 v[40:43], v[8:11], a[88:95], v68\n\t" \
  "ds_read_b128 v[20:23], v69 offset:5120\n\t" \
  "s_waitcnt lgkmcnt(6)\n\t" \
  "v_smfmac_f32_16x16x64_bf16 v[36:39], v[12:15], a[120:127], v68\n\t" \
  "ds_read_b128 v[24:27], v69 offset:6144\n\t" \
  "s_waitcnt lgkmcnt(5)\n\t" \
  "v_smfmac_f32_16x16x64_bf16 v[40:43], v[12:15], v[44:51], v68\n\t" \
  "ds_read_b128 v[28:31], v69 offset:7168\n\t" \
  "s_waitcnt lgkmcnt(3)\n\t" \
  "v_smfmac_f32_16x16x64_bf16 v[36:39], v[16:19], a[160:167], v68\n\t" \
  "ds_read_b128 v[32:35], v69 offset:8192\n\t" \
  "v_smfmac_f32_16x16x64_bf16 v[40:43], v[16:19], a[168:175], v68\n\t" \
  "s_waitcnt lgkmcnt(3)\n\t" \
  "v_mov_b64 v[44:45], 0\n\t" \
  "v_smfmac_f32_16x16x64_bf16 v[36:39], v[20:23], a[200:207], v68\n\t" \
  "s_waitcnt lgkmcnt(2)\n\t" \
  "v_mov_b64 v[46:47], 0\n\t" \
  "v_smfmac_f32_16x16x64_bf16 v[36:39], v[24:27], a[240:247], v68\n\t" \
  "s_waitcnt lgkmcnt(1)\n\t" \
  "v_mov_b64 v[48:49], 0\n\t" \
  "v_smfmac_f32_16x16x64_bf16 v[36:39], v[28:31], v[112:119], v68\n\t" \
  "s_waitcnt lgkmcnt(0)\n\t" \
  "v_mov_b64 v[50:51], 0\n\t" \
  "v_smfmac_f32_16x16x64_bf16 v[36:39], v[32:35], v[160:167], v68\n\t" \
  "v_smfmac_f32_16x16x64_bf16 v[40:43], v[20:23], v[60:67], v68\n\t" \
  "ds_read_b128 v[60:63], v70 offset:8192\n\t" \
  "v_smfmac_f32_16x16x64_bf16 v[40:43], v[24:27], a[248:255], v68\n\t" \
  "ds_read_b128 v[64:67], v70 offset:9216\n\t" \
  "v_add_f32 v74, v36, v38\n\t" \
  "v_smfmac_f32_16x16x64_bf16 v[40:43], v[28:31], v[120:127], v68\n\t" \
  "v_add_f32 v37, v37, v39\n\t" \
  "v_smfmac_f32_16x16x64_bf16 v[40:43], v[32:35], v[168:175], v68\n\t" \
  "v_cmp_u_f32 vcc, v74, v37\n\t" \
  "v_smfmac_f32_16x16x64_bf16 v[44:47], v[0:3], a[0:7], v68\n\t" \
  "s_cbranch_vccnz LFAIL0_%=\n\t" \
  "LGO0_%=:\n\t" \
  "v_cvt_pk_bf16_f32 v74, v74, v37\n\t" \
  "v_smfmac_f32_16x16x64_bf16 v[44:47], v[4:7], a[48:55], v68\n\t" \
  "s_waitcnt lgkmcnt(0)\n\t" \
  "v_add_f32 v75, v40, v42\n\t" \
  "v_smfmac_f32_16x16x64_bf16 v[44:47], v[8:11], v[60:67], v68\n\t" \
  "ds_read_b128 v[60:63], v70 offset:10240\n\t" \
  "v_add_f32 v41, v41, v43\n\t" \
  "v_cvt_pk_bf16_f32 v75, v75, v41\n\t" \
  "v_smfmac_f32_16x16x64_bf16 v[44:47], v[12:15], a[128:135], v68\n\t" \
  "ds_write2st64_b32 v71, v74, v75 offset0:36 offset1:38\n\t" \
  "v_smfmac_f32_16x16x64_bf16 v[44:47], v[16:19], a[176:183], v68\n\t" \
  "ds_read_b128 v[64:67], v70 offset:11264\n\t" \
  "v_smfmac_f32_16x16x64_bf16 v[44:47], v[20:23], a[208:215], v68\n\t" \
  "v_smfmac_f32_16x16x64_bf16 v[44:47], v[24:27], v[80:87], v68\n\t" \
  "v_smfmac_f32_16x16x64_bf16 v[44:47], v[28:31], v[128:135], v68\n\t" \
  "s_waitcnt lgkmcnt(0)\n\t" \
  "v_smfmac_f32_16x16x64_bf16 v[44:47], v[32:35], v[60:67], v68\n\t" \
  "buffer_load_dwordx4 v[60:63], v69, s[28:31], s64 offen sc1\n\t" \
  "s_barrier\n\t" \
  "ds_read_b64_tr_b16 v[76:77], v72 offset:9216\n\t" \
  "v_smfmac_f32_16x16x64_bf16 v[48:51], v[0:3], a[8:15], v68\n\t" \
  "ds_read_b64_tr_b16 v[78:79], v72 offset:9728\n\t" \
  "v_smfmac_f32_16x16x64_bf16 v[48:51], v[4:7], a[56:63], v68\n\t" \
  "v_add_f32 v74, v44, v46\n\t" \
  "v_smfmac_f32_16x16x64_bf16 v[48:51], v[8:11], a[96:103], v68\n\t" \
  "v_add_f32 v45, v45, v47\n\t" \
  "v_smfmac_f32_16x16x64_bf16 v[48:51], v[12:15], a[136:143], v68\n\t" \
  "s_waitcnt vmcnt(4)\n\t" \
  "s_waitcnt lgkmcnt(0)\n\t" \
  "v_cvt_pk_bf16_f32 v74, v74, v45\n\t" \
  "v_mfma_f32_16x16x32_bf16 v[36:39], v[52:55], v[76:79], 0\n\t" \
  "buffer_load_dwordx4 v[64:67], v69, s[28:31], s64 offen offset:1024 sc1\n\t" \
  "s_waitcnt vmcnt(4)\n\t" \
  "v_mfma_f32_16x16x32_bf16 v[40:43], v[56:59], v[76:79], 0\n\t" \
  "v_mov_b64 v[52:53], 0\n\t" \
  "v_mov_b64 v[54:55], 0\n\t" \
  "v_smfmac_f32_16x16x64_bf16 v[48:51], v[16:19], a[184:191], v68\n\t" \
  "v_mov_b64 v[56:57], 0\n\t" \
  "v_mov_b64 v[58:59], 0\n\t" \
  "v_smfmac_f32_16x16x64_bf16 v[48:51], v[20:23], a[216:223], v68\n\t" \
  "ds_write_b32 v71, v74 offset:0\n\t" \
  "v_add_f32_dpp v36, v40, v36 quad_perm:[2,3,0,1] row_mask:0xf bank_mask:0xf\n\t" \
  "v_smfmac_f32_16x16x64_bf16 v[48:51], v[24:27], v[88:95], v68\n\t" \
  "v_add_f32_dpp v37, v41, v37 quad_perm:[2,3,0,1] row_mask:0xf bank_mask:0xf\n\t" \
  "v_smfmac_f32_16x16x64_bf16 v[48:51], v[28:31], v[136:143], v68\n\t" \
  "v_add_f32_dpp v38, v42, v38 quad_perm:[2,3,0,1] row_mask:0xf bank_mask:0xf\n\t" \
  "v_smfmac_f32_16x16x64_bf16 v[48:51], v[32:35], v[176:183], v68\n\t" \
  "v_add_f32_dpp v39, v43, v39 quad_perm:[2,3,0,1] row_mask:0xf bank_mask:0xf\n\t" \
  "v_smfmac_f32_16x16x64_bf16 v[52:55], v[0:3], a[16:23], v68\n\t" \
  "s_mov_b64 exec, s[58:59]\n\t" \
  "ds_write_b128 v69, v[36:39] offset:2048\n\t" \
  "s_mov_b64 exec, -1\n\t" \
  "v_smfmac_f32_16x16x64_bf16 v[52:55], v[4:7], a[64:71], v68\n\t" \
  "v_add_f32 v75, v48, v50\n\t" \
  "v_add_f32 v49, v49, v51\n\t" \
  "v_cvt_pk_bf16_f32 v75, v75, v49\n\t" \
  "v_smfmac_f32_16x16x64_bf16 v[56:59], v[0:3], a[24:31], v68\n\t" \
  "ds_write_b32 v71, v75 offset:512\n\t" \
  "v_smfmac_f32_16x16x64_bf16 v[56:59], v[4:7], a[72:79], v68\n\t" \
  "ds_read_b64_tr_b16 v[0:1], v72 offset:10240\n\t" \
  "ds_read_b64_tr_b16 v[2:3], v72 offset:10752\n\t" \
  "v_smfmac_f32_16x16x64_bf16 v[52:55], v[8:11], a[104:111], v68\n\t" \
  "ds_read_b128 v[36:39], v70 offset:12288\n\t" \
  "v_smfmac_f32_16x16x64_bf16 v[56:59], v[8:11], a[112:119], v68\n\t" \
  "buffer_load_dword off, s[28:31], s87 sc1 lds\n\t" \
  "ds_read_b128 v[40:43], v70 offset:13312\n\t" \
  "s_waitcnt lgkmcnt(4)\n\t" \
  "s_barrier\n\t" \
  "ds_read_b64_tr_b16 v[44:45], v72 offset:0\n\t" \
  "ds_read_b64_tr_b16 v[46:47], v72 offset:512\n\t" \
  "v_smfmac_f32_16x16x64_bf16 v[52:55], v[12:15], a[144:151], v68\n\t" \
  "ds_read_b64_tr_b16 v[48:49], v72 offset:1024\n\t" \
  "ds_read_b64_tr_b16 v[50:51], v72 offset:1536\n\t" \
  "v_smfmac_f32_16x16x64_bf16 v[56:59], v[12:15], a[152:159], v68\n\t" \
  "v_smfmac_f32_16x16x64_bf16 v[52:55], v[16:19], a[192:199], v68\n\t" \
  "ds_read_b128 v[4:7], v69 offset:2048\n\t" \
  "s_waitcnt lgkmcnt(5)\n\t" \
  "v_smfmac_f32_16x16x64_bf16 v[56:59], v[16:19], v[36:43], v68\n\t" \
  "ds_read_b128 v[8:11], v69 offset:3072\n\t" \
  "s_waitcnt lgkmcnt(4)\n\t" \
  "v_mfma_f32_16x16x32_bf16 v[12:15], v[44:47], v[76:79], 0\n\t" \
  "ds_read_b128 v[36:39], v70 offset:14336\n\t" \
  "s_waitcnt lgkmcnt(3)\n\t" \
  "v_mfma_f32_16x16x32_bf16 v[12:15], v[48:51], v[0:3], v[12:15]\n\t" \
  "ds_read_b128 v[40:43], v70 offset:15360\n\t" \
  "v_smfmac_f32_16x16x64_bf16 v[52:55], v[20:23], a[224:231], v68\n\t" \
  "s_waitcnt lgkmcnt(2)\n\t" \
  "v_add_f32 v4, v4, v8\n\t" \
  "v_add_f32 v5, v5, v9\n\t" \
  "v_add_f32 v6, v6, v10\n\t" \
  "v_cndmask_b32 v78, v13, v14, s[56:57]\n\t" \
  "v_cndmask_b32 v76, v12, v15, s[56:57]\n\t" \
  "v_add_f32 v7, v7, v11\n\t" \
  "v_max3_f32 v77, v4, v5, v6\n\t" \
  "v_add_f32_dpp v76, v78, v76 quad_perm:[2,3,0,1] row_mask:0xf bank_mask:0xf\n\t" \
  "v_max3_f32 v77, v77, v7, v76\n\t" \
  "v_smfmac_f32_16x16x64_bf16 v[56:59], v[20:23], a[232:239], v68\n\t" \
  "ds_read_b128 v[44:47], v70 offset:16384\n\t" \
  "v_max_f32_dpp v77, v77, v77 quad_perm:[2,3,0,1] row_mask:0xf bank_mask:0xf\n\t" \
  "v_mov_b32 v78, v77\n\t" \
  "v_smfmac_f32_16x16x64_bf16 v[52:55], v[24:27], v[96:103], v68\n\t" \
  "ds_read_b128 v[48:51], v70 offset:17408\n\t" \
  "v_permlane16_swap_b32 v77, v78\n\t" \
  "v_max_f32 v77, v77, v78\n\t" \
  "v_mov_b32 v78, v77\n\t" \
  "v_smfmac_f32_16x16x64_bf16 v[52:55], v[28:31], v[144:151], v68\n\t" \
  "ds_read_b128 v[16:19], v70 offset:18432\n\t" \
  "v_permlane32_swap_b32 v77, v78\n\t" \
  "v_max_f32 v77, v77, v78\n\t" \
  "v_smfmac_f32_16x16x64_bf16 v[52:55], v[32:35], v[184:191], v68\n\t" \
  "ds_read_b128 v[20:23], v70 offset:19456\n\t" \
  "v_mul_f32 v77, 0xbe0293ee, v77\n\t" \
  "v_smfmac_f32_16x16x64_bf16 v[56:59], v[24:27], v[104:111], v68\n\t" \
  "v_fmamk_f32 v4, v4, 0x3e0293ee, v77\n\t" \
  "v_smfmac_f32_16x16x64_bf16 v[56:59], v[28:31], v[152:159], v68\n\t" \
  "ds_read_b128 v[24:27], v70 offset:22528\n\t" \
  "s_waitcnt lgkmcnt(4)\n\t" \
  "v_fmamk_f32 v5, v5, 0x3e0293ee, v77\n\t" \
  "v_smfmac_f32_16x16x64_bf16 v[56:59], v[32:35], v[36:43], v68\n\t" \
  "ds_read_b128 v[28:31], v70 offset:23552\n\t" \
  "v_fmamk_f32 v6, v6, 0x3e0293ee, v77\n\t" \
  "v_fmamk_f32 v7, v7, 0x3e0293ee, v77\n\t" \
  "v_fmamk_f32 v76, v76, 0x3e0293ee, v77\n\t" \
  "v_exp_f32 v4, v4\n\t" \
  "v_exp_f32 v5, v5\n\t" \
  "v_exp_f32 v6, v6\n\t" \
  "v_exp_f32 v7, v7\n\t" \
  "v_cvt_pk_bf16_f32 v4, v4, v5\n\t" \
  "v_exp_f32 v76, v76\n\t" \
  "v_cvt_pk_bf16_f32 v5, v6, v7\n\t" \
  "v_mov_b32_dpp v12, v4 quad_perm:[0,1,0,1] row_mask:0xf bank_mask:0xf\n\t" \
  "v_mov_b32_dpp v14, v4 quad_perm:[2,3,2,3] row_mask:0xf bank_mask:0xf\n\t" \
  "v_mov_b32_dpp v13, v5 quad_perm:[0,1,0,1] row_mask:0xf bank_mask:0xf\n\t" \
  "v_mov_b32_dpp v15, v5 quad_perm:[2,3,2,3] row_mask:0xf bank_mask:0xf\n\t" \
  "v_add_f32 v52, v52, v54\n\t" \
  "v_add_f32 v53, v53, v55\n\t" \
  "s_waitcnt vmcnt(2)\n\t" \
  "v_mfma_f32_16x16x32_bf16 v[0:3], v[60:63], v[12:15], 0\n\t" \
  "ds_read_b128 v[36:39], v70 offset:20480\n\t" \
  "v_add_f32 v56, v56, v58\n\t" \
  "v_add_f32 v57, v57, v59\n\t" \
  "v_cvt_pk_bf16_f32 v52, v52, v53\n\t" \
  "v_cvt_pk_bf16_f32 v53, v56, v57\n\t" \
  "s_waitcnt vmcnt(1)\n\t" \
  "v_mfma_f32_16x16x32_bf16 v[4:7], v[64:67], v[12:15], 0\n\t" \
  "buffer_store_dwordx2 v[52:53], v71, s[28:31], s69 offen offset:2048 sc1\n\t" \
  "ds_read_b128 v[40:43], v70 offset:21504\n\t" \
  "v_mov_b64 v[56:57], s[88:89]\n\t" \
  "v_mov_b64 v[58:59], s[88:89]\n\t" \
  "v_mov_b32_dpp v78, v76 quad_perm:[0,1,0,1] row_mask:0xf bank_mask:0xf\n\t" \
  "v_mov_b32_dpp v79, v76 quad_perm:[2,3,2,3] row_mask:0xf bank_mask:0xf\n\t" \
  "v_mfma_f32_16x16x32_bf16 v[32:35], v[56:59], v[12:15], 0\n\t" \
  "ds_read_b128 v[60:63], v70 offset:24576\n\t" \
  "v_cvt_pk_bf16_f32 v11, v78, v79\n\t" \
  "v_cvt_pk_bf16_f32 v8, v78, v79\n\t" \
  "v_mov_b64 v[54:55], 0\n\t" \
  "v_mov_b32 v9, 0\n\t" \
  "v_mov_b32 v10, 0\n\t" \
  "v_mov_b64 v[14:15], 0\n\t" \
  "v_mov_b64 v[12:13], 0\n\t" \
  "v_mfma_f32_16x16x32_bf16 v[32:35], v[54:57], v[8:11], v[32:35]\n\t" \
  "s_cmp_eq_u32 s48, 63\n\t" \
  "v_mfma_f32_16x16x32_bf16 v[0:3], v[52:55], v[8:11], v[0:3]\n\t" \
  "ds_read_b128 v[64:67], v70 offset:25600\n\t" \
  "v_mov_b64 v[56:57], 0\n\t" \
  "v_mov_b64 v[58:59], 0\n\t" \
  "v_mfma_f32_16x16x32_bf16 v[4:7], v[52:55], v[10:13], v[4:7]\n\t" \
  "s_cbranch_scc1 LLAST0_%=\n\t" \
  "v_rcp_f32 v74, v32\n\t" \
  "v_mov_b32 v11, 0\n\t" \
  "v_mov_b32 v8, 0\n\t" \
  "v_pk_mul_f32 v[0:1], v[0:1], v[74:75] op_sel_hi:[1,0]\n\t" \
  "v_pk_mul_f32 v[2:3], v[2:3], v[74:75] op_sel_hi:[1,0]\n\t" \
  "v_cvt_pk_bf16_f32 v76, v0, v1\n\t" \
  "v_cvt_pk_bf16_f32 v77, v2, v3\n\t" \
  "v_pk_mul_f32 v[4:5], v[4:5], v[74:75] op_sel_hi:[1,0]\n\t" \
  "v_pk_mul_f32 v[6:7], v[6:7], v[74:75] op_sel_hi:[1,0]\n\t" \
  "v_cvt_pk_bf16_f32 v78, v4, v5\n\t" \
  "v_cvt_pk_bf16_f32 v79, v6, v7\n\t" \
  "ds_write_b128 v69, v[76:79] offset:4096\n\t" \
  "s_waitcnt lgkmcnt(2)\n\t" \
  "v_smfmac_f32_16x16x64_bf16 v[12:15], v[76:79], v[44:51], v68\n\t" \
  "ds_read_b128 v[44:47], v70 offset:26624\n\t" \
  "v_smfmac_f32_16x16x64_bf16 v[56:59], v[76:79], v[16:23], v68\n\t" \
  "ds_read_b128 v[48:51], v70 offset:27648\n\t" \
  "v_mov_b64 v[52:53], 0\n\t" \
  "v_smfmac_f32_16x16x64_bf16 v[8:11], v[76:79], v[36:43], v68\n\t" \
  "ds_read_b128 v[16:19], v70 offset:28672\n\t" \
  "v_smfmac_f32_16x16x64_bf16 v[52:55], v[76:79], v[24:31], v68\n\t" \
  "s_waitcnt lgkmcnt(3)\n\t" \
  "s_barrier\n\t" \
  "ds_read_b128 v[0:3], v69 offset:5120\n\t" \
  "ds_read_b128 v[4:7], v69 offset:6144\n\t" \
  "v_lshrrev_b32 v71, 7, v69\n\t" \
  "v_and_b32 v74, 0x70, v69\n\t" \
  "v_lshl_add_u32 v71, v71, 14, v74\n\t" \
  "v_bfe_u32 v72, v69, 2, 7\n\t" \
  "v_and_b32 v74, 0x200, v69\n\t" \
  "s_waitcnt lgkmcnt(1)\n\t" \
  "v_smfmac_f32_16x16x64_bf16 v[8:11], v[0:3], v[60:67], v68\n\t" \
  "ds_read_b128 v[20:23], v70 offset:29696\n\t" \
  "v_mov_b64 v[76:77], 0\n\t" \
  "v_smfmac_f32_16x16x64_bf16 v[12:15], v[0:3], v[200:207], v68\n\t" \
  "ds_read_b128 v[32:35], v69 offset:7168\n\t" \
  "v_mov_b64 v[78:79], 0\n\t" \
  "v_smfmac_f32_16x16x64_bf16 v[52:55], v[0:3], v[44:51], v68\n\t" \
  "ds_read_b128 v[24:27], v70 offset:30720\n\t" \
  "v_lshl_add_u32 v72, v74, 3, v72\n\t" \
  "v_smfmac_f32_16x16x64_bf16 v[56:59], v[0:3], v[232:239], v68\n\t" \
  "buffer_load_dword v75, off, s[28:31], s80 sc1\n\t" \
  "ds_read_b128 v[28:31], v70 offset:31744\n\t" \
  "s_waitcnt lgkmcnt(4)\n\t" \
  "v_smfmac_f32_16x16x64_bf16 v[12:15], v[4:7], v[208:215], v68\n\t" \
  "ds_read_b128 v[60:63], v70 offset:32768\n\t" \
  "s_waitcnt lgkmcnt(4)\n\t" \
  "v_smfmac_f32_16x16x64_bf16 v[8:11], v[4:7], v[16:23], v68\n\t" \
  "ds_read_b128 v[64:67], v70 offset:33792\n\t" \
  "s_waitcnt lgkmcnt(4)\n\t" \
  "v_smfmac_f32_16x16x64_bf16 v[8:11], v[32:35], v[192:199], v68\n\t" \
  "ds_read_b128 v[36:39], v73 offset:4096\n\t" \
  "v_smfmac_f32_16x16x64_bf16 v[12:15], v[32:35], v[216:223], v68\n\t" \
  "ds_read_b128 v[40:43], v73 offset:6144\n\t" \
  "s_waitcnt lgkmcnt(4)\n\t" \
  "v_smfmac_f32_16x16x64_bf16 v[52:55], v[4:7], v[24:31], v68\n\t" \
  "ds_read_b128 v[44:47], v70 offset:34816\n\t" \
  "v_cvt_pk_bf16_f32 v8, v8, v9\n\t" \
  "v_smfmac_f32_16x16x64_bf16 v[52:55], v[32:35], v[224:231], v68\n\t" \
  "ds_read_b128 v[48:51], v70 offset:35840\n\t" \
  "v_cvt_pk_bf16_f32 v9, v10, v11\n\t" \
  "v_smfmac_f32_16x16x64_bf16 v[56:59], v[4:7], v[240:247], v68\n\t" \
  "s_waitcnt lgkmcnt(4)\n\t" \
  "v_cvt_pk_bf16_f32 v10, v12, v13\n\t" \
  "v_smfmac_f32_16x16x64_bf16 v[56:59], v[32:35], v[60:67], v68\n\t" \
  "s_waitcnt lgkmcnt(3)\n\t" \
  "v_cvt_pk_bf16_f32 v11, v14, v15\n\t" \
  "v_smfmac_f32_16x16x64_bf16 v[76:79], v[36:39], v[248:255], v68\n\t" \
  "buffer_store_dwordx4 v[8:11], v71, s[28:31], s54 offen sc1\n\t" \
  "s_waitcnt lgkmcnt(0)\n\t" \
  "v_cvt_pk_bf16_f32 v52, v52, v53\n\t" \
  "v_smfmac_f32_16x16x64_bf16 v[76:79], v[40:43], v[44:51], v68\n\t" \
  "v_cvt_pk_bf16_f32 v53, v54, v55\n\t" \
  "v_cvt_pk_bf16_f32 v54, v56, v57\n\t" \
  "v_cvt_pk_bf16_f32 v55, v58, v59\n\t" \
  "buffer_store_dwordx4 v[52:55], v71, s[28:31], s75 offen sc1\n\t" \
  "s_nop 2\n\t" \
  "v_add_f32 v76, v76, v78\n\t" \
  "v_add_f32 v77, v77, v79\n\t" \
  "v_cvt_pk_bf16_f32 v76, v76, v77\n\t" \
  "buffer_store_dword v76, v72, s[28:31], s55 offen sc1\n\t" \
  "s_memrealtime s[70:71]\n\t" \
  "s_waitcnt vmcnt(3)\n\t" \
  "v_readfirstlane_b32 s65, v75\n\t" \
  "s_barrier\n\t" \
  "s_waitcnt lgkmcnt(0)\n\t" \
  "s_not_b32 s65, s65\n\t" \
  "s_lshl_b32 s61, s70, 8\n\t" \
  "s_andn2_b32 s64, s65, 0xff\n\t" \
  "s_sub_u32 s64, s64, s61\n\t" \
  "s_ashr_i32 s64, s64, 8\n\t" \
  "s_cmp_eq_u32 s65, 0\n\t" \
  "s_cselect_b32 s64, " CH_DLF ", s64\n\t" \
  "s_cselect_b32 s65, " CH_B0 ", s65\n\t" \
  "s_add_u32 s47, s64, s70\n\t" \
  "s_min_i32 s64, s64, s34\n\t" \
  "s_max_i32 s64, s64, " CH_DLL1 "\n\t" \
  "s_add_u32 s64, s64, s70\n\t" \
  "s_add_u32 s55, s64, " CH_FDL "\n\t" \
  "s_movk_i32 s60, 8192\n\t" \
  "FWT_%=:\n\t" \
  "s_memrealtime s[62:63]\n\t" \
  "s_sub_u32 s60, s60, 1\n\t" \
  "s_cselect_b32 s61, 0, -1\n\t" \
  "s_waitcnt lgkmcnt(0)\n\t" \
  "s_sub_i32 s63, s62, s55\n\t" \
  "s_and_b32 s63, s63, s61\n\t" \
  "s_cmp_lt_i32 s63, 0\n\t" \
  "s_cbranch_scc1 FWT_%=\n\t" \
  "s_mov_b64 exec, 1\n\t" \
  "buffer_store_dword v68, off, s[28:31], s51 offset:4064 sc1\n\t" \
  "s_mov_b64 exec, -1\n\t" \
  "s_and_b32 s65, s65, 0xff\n\t" \
  "s_add_u32 s47, s47, s65\n\t" \
  "s_add_u32 s65, s65, s64\n\t" \
  "s_sub_u32 s60, s70, s66\n\t" \
  "s_add_u32 s61, s81, 1\n\t" \
  "s_min_u32 s60, s60, s61\n\t" \
  "s_cmp_eq_u32 s68, 0\n\t" \
  "s_cselect_b32 s81, s60, s81\n\t" \
  "s_add_u32 s48, s48, 1\n\t" \
  "s_mov_b32 s49, s53\n\t" \
  "s_add_u32 s53, s53, " CH_SLOT "\n\t" \
  "s_add_u32 s60, s53, " CH_SKIP "\n\t" \
  "s_lshr_b32 s61, s96, 1\n\t" \
  "s_add_u32 s86, s86, 1\n\t" \
  "s_cmp_eq_u32 s86, 3\n\t" \
  "s_cselect_b32 s53, s60, s53\n\t" \
  "s_cselect_b32 s96, s61, s96\n\t" \
  "s_cselect_b32 s86, 0, s86\n\t" \
  "s_cmp_ge_u32 s53, " CH_RING "\n\t" \
  "s_cselect_b32 s53, 0, s53\n\t" \
  "s_cselect_b32 s96, s21, s96\n\t" \
  "s_andn2_b32 s53, s53, 0x2000\n\t" \
  "s_and_b32 s60, s96, 1\n\t" \
  "s_lshl_b32 s60, s60, 13\n\t" \
  "s_or_b32 s53, s53, s60\n\t" \
  "s_mov_b32 s68, 0\n\t" \
  "s_add_u32 s82, s48, 0\n\t" \
  "s_and_b32 s82, s82, " CH_TRM "\n\t" \
  "s_lshr_b64 s[60:61], s[90:91], s82\n\t" \
  "s_and_b32 s60, s60, 1\n\t" \
  "s_lshl_b32 s60, s60, 13\n\t" \
  "s_add_u32 s82, s82, " CH_SG0 "\n\t" \
  "s_lshl_b32 s82, s82, 21\n\t" \
  "s_or_b32 s82, s82, s60\n\t" \
  "s_add_u32 s50, s82, s38\n\t" \
  "s_add_u32 s69, s82, s39\n\t" \
  "s_add_u32 s82, s48, 0\n\t" \
  "s_lshr_b64 s[60:61], s[90:91], s82\n\t" \
  "s_and_b32 s60, s60, 1\n\t" \
  "s_lshl_b32 s60, s60, 13\n\t" \
  "s_add_u32 s82, s82, " CH_SG0 "\n\t" \
  "s_lshl_b32 s82, s82, 21\n\t" \
  "s_or_b32 s82, s82, s60\n\t" \
  "s_add_u32 s51, s82, s43\n\t" \
  "s_cmp_lg_u32 s34, 0\n\t" \
  "s_cbranch_scc1 LW0_%=\n\t" \
  "s_barrier\n\t" \
  "s_branch LINI_%=\n\t" \
  "LFAIL0_%=:\n\t" \
  "v_max_u32 v75, v0, v1\n\t" \
  "v_max3_u32 v75, v75, v2, v3\n\t" \
  "v_max3_u32 v75, v75, v4, v5\n\t" \
  "v_max3_u32 v75, v75, v6, v7\n\t" \
  "v_max3_u32 v75, v75, v8, v9\n\t" \
  "v_max3_u32 v75, v75, v10, v11\n\t" \
  "v_max3_u32 v75, v75, v12, v13\n\t" \
  "v_max3_u32 v75, v75, v14, v15\n\t" \
  "v_max3_u32 v75, v75, v16, v17\n\t" \
  "v_max3_u32 v75, v75, v18, v19\n\t" \
  "v_max3_u32 v75, v75, v20, v21\n\t" \
  "v_max3_u32 v75, v75, v22, v23\n\t" \
  "v_max3_u32 v75, v75, v24, v25\n\t" \
  "v_max3_u32 v75, v75, v26, v27\n\t" \
  "v_max3_u32 v75, v75, v28, v29\n\t" \
  "v_max3_u32 v75, v75, v30, v31\n\t" \
  "v_max3_u32 v75, v75, v32, v33\n\t" \
  "v_max3_u32 v75, v75, v34, v35\n\t" \
  "v_cmp_eq_u32 vcc, -1, v75\n\t" \
  "s_cbranch_vccz LGO0_%=\n\t" \
  "s_add_u32 s68, s68, 1\n\t" \
  "s_waitcnt lgkmcnt(0)\n\t" \
  "s_cmp_gt_u32 s68, s83\n\t" \
  "s_cbranch_scc1 LGV0_%=\n\t" \
  "s_barrier\n\t" \
  "s_memrealtime s[62:63]\n\t" \
  "s_mov_b32 m0, 0\n\t" \
  "s_waitcnt lgkmcnt(0)\n\t" \
  "s_add_u32 s65, s62, " CH_PEN "\n\t" \
  "s_branch LDMA_%=\n\t" \
  "LWT_%=f:\n\t" \
  "s_cmp_lt_i32 s63, -2\n\t" \
  "s_cbranch_scc0 LWT_%=g\n\t" \
  "s_nop 4\n\t" \
  "s_branch LWT_%=g\n\t" \
  "LGV0_%=:\n\t" \
  "s_mov_b32 s83, 0\n\t" \
  "s_lshl_b32 s60, s36, 4\n\t" \
  "s_or_b32 s60, s60, s48\n\t" \
  "s_or_b32 s60, s60, 805306368\n\t" \
  "v_mov_b32 v76, s60\n\t" \
  "v_mov_b32 v77, 0\n\t" \
  "s_mov_b64 exec, 1\n\t" \
  "buffer_atomic_cmpswap v[76:77], off, s[28:31], s92 sc1\n\t" \
  "s_mov_b64 exec, -1\n\t" \
  "s_branch LGO0_%=\n\t" \
  "LLAST0_%=:\n\t" \
  "s_waitcnt vmcnt(0) lgkmcnt(0)\n\t" \
  "s_barrier\n\t" \
  "s_mov_b64 exec, 1\n\t" \
  "buffer_store_dword v68, off, s[28:31], s51 offset:4064 sc1\n\t" \
  "s_mov_b64 exec, -1\n\t" \
  "s_branch LEND_%=\n\t" \
  ".p2align 6\n\t" \
  "LG1_%=:\n\t" \
  "s_barrier\n\t" \
  "buffer_load_dwordx4 v[52:55], v69, s[28:31], s50 offen sc1\n\t" \
  "buffer_load_dwordx4 v[56:59], v69, s[28:31], s50 offen offset:1024 sc1\n\t" \
  "s_add_u32 s64, s50, 14336\n\t" \
  "s_movk_i32 m0, 11264\n\t" \
  "v_lshlrev_b32 v75, 1, v69\n\t" \
  "buffer_load_dword v75, s[28:31], s64 offen sc1 lds\n\t" \
  "ds_read_b128 v[44:47], v70 offset:0\n\t" \
  "ds_read_b128 v[48:51], v70 offset:1024\n\t" \
  "ds_read_b128 v[60:63], v70 offset:2048\n\t" \
  "ds_read_b128 v[64:67], v70 offset:3072\n\t" \
  "v_mov_b64 v[36:37], 0\n\t" \
  "v_mov_b64 v[38:39], 0\n\t" \
  "v_mov_b64 v[40:41], 0\n\t" \
  "v_mov_b64 v[42:43], 0\n\t" \
  "v_lshrrev_b32 v71, 1, v69\n\t" \
  "v_add_u32 v71, 4, v71\n\t" \
  "v_lshrrev_b32 v72, 3, v69\n\t" \
  "v_and_b32 v72, 0x78, v72\n\t" \
  "v_and_b32 v74, 0x30, v69\n\t" \
  "v_lshl_add_u32 v72, v74, 3, v72\n\t" \
  "s_add_u32 s54, s53, s36\n\t" \
  "s_add_u32 s55, s54, 540672\n\t" \
  "s_add_u32 s54, s54, 4096\n\t" \
  "s_add_u32 s75, s54, 262144\n\t" \
  "s_add_u32 s80, s53, s26\n\t" \
  ".p2align 5\n\t" \
  ".fill 2, 4, 0xbf800000\n\t" \
  "s_waitcnt vmcnt(9)\n\t" \
  "s_barrier\n\t" \
  "ds_read_b128 v[0:3], v69 offset:0\n\t" \
  "ds_read_b128 v[4:7], v69 offset:1024\n\t" \
  "ds_read_b128 v[8:11], v69 offset:2048\n\t" \
  "ds_read_b128 v[12:15], v69 offset:3072\n\t" \
  "s_waitcnt lgkmcnt(3)\n\t" \
  "v_smfmac_f32_16x16x64_bf16 v[36:39], v[0:3], v[44:51], v68\n\t" \
  "ds_read_b128 v[44:47], v70 offset:4096\n\t" \
  "v_smfmac_f32_16x16x64_bf16 v[40:43], v[0:3], v[60:67], v68\n\t" \
  "ds_read_b128 v[48:51], v70 offset:5120\n\t" \
  "s_waitcnt lgkmcnt(4)\n\t" \
  "v_smfmac_f32_16x16x64_bf16 v[36:39], v[4:7], a[32:39], v68\n\t" \
  "ds_read_b128 v[60:63], v70 offset:6144\n\t" \
  "v_smfmac_f32_16x16x64_bf16 v[40:43], v[4:7], a[40:47], v68\n\t" \
  "ds_read_b128 v[64:67], v70 offset:7168\n\t" \
  "s_waitcnt lgkmcnt(5)\n\t" \
  "v_smfmac_f32_16x16x64_bf16 v[36:39], v[8:11], a[80:87], v68\n\t" \
  "s_barrier\n\t" \
  "ds_read_b128 v[16:19], v69 offset:4096\n\t" \
  "v_smfmac_f32_16x16x64_bf16 v[40:43], v[8:11], a[88:95], v68\n\t" \
  "ds_read_b128 v[20:23], v69 offset:5120\n\t" \
  "s_waitcnt lgkmcnt(6)\n\t" \
  "v_smfmac_f32_16x16x64_bf16 v[36:39], v[12:15], a[120:127], v68\n\t" \
  "ds_read_b128 v[24:27], v69 offset:6144\n\t" \
  "s_waitcnt lgkmcnt(5)\n\t" \
  "v_smfmac_f32_16x16x64_bf16 v[40:43], v[12:15], v[44:51], v68\n\t" \
  "ds_read_b128 v[28:31], v69 offset:7168\n\t" \
  "s_waitcnt lgkmcnt(3)\n\t" \
  "v_smfmac_f32_16x16x64_bf16 v[36:39], v[16:19], a[160:167], v68\n\t" \
  "ds_read_b128 v[32:35], v69 offset:8192\n\t" \
  "v_smfmac_f32_16x16x64_bf16 v[40:43], v[16:19], a[168:175], v68\n\t" \
  "s_waitcnt lgkmcnt(3)\n\t" \
  "v_mov_b64 v[44:45], 0\n\t" \
  "v_smfmac_f32_16x16x64_bf16 v[36:39], v[20:23], a[200:207], v68\n\t" \
  "s_waitcnt lgkmcnt(2)\n\t" \
  "v_mov_b64 v[46:47], 0\n\t" \
  "v_smfmac_f32_16x16x64_bf16 v[36:39], v[24:27], a[240:247], v68\n\t" \
  "s_waitcnt lgkmcnt(1)\n\t" \
  "v_mov_b64 v[48:49], 0\n\t" \
  "v_smfmac_f32_16x16x64_bf16 v[36:39], v[28:31], v[112:119], v68\n\t" \
  "s_waitcnt lgkmcnt(0)\n\t" \
  "v_mov_b64 v[50:51], 0\n\t" \
  "v_smfmac_f32_16x16x64_bf16 v[36:39], v[32:35], v[160:167], v68\n\t" \
  "v_smfmac_f32_16x16x64_bf16 v[40:43], v[20:23], v[60:67], v68\n\t" \
  "ds_read_b128 v[60:63], v70 offset:8192\n\t" \
  "v_smfmac_f32_16x16x64_bf16 v[40:43], v[24:27], a[248:255], v68\n\t" \
  "ds_read_b128 v[64:67], v70 offset:9216\n\t" \
  "v_add_f32 v74, v36, v38\n\t" \
  "v_smfmac_f32_16x16x64_bf16 v[40:43], v[28:31], v[120:127], v68\n\t" \
  "v_add_f32 v37, v37, v39\n\t" \
  "v_smfmac_f32_16x16x64_bf16 v[40:43], v[32:35], v[168:175], v68\n\t" \
  "v_cmp_u_f32 vcc, v74, v37\n\t" \
  "v_smfmac_f32_16x16x64_bf16 v[44:47], v[0:3], a[0:7], v68\n\t" \
  "s_cbranch_vccnz LFAIL1_%=\n\t" \
  "LGO1_%=:\n\t" \
  "v_cvt_pk_bf16_f32 v74, v74, v37\n\t" \
  "v_smfmac_f32_16x16x64_bf16 v[44:47], v[4:7], a[48:55], v68\n\t" \
  "s_waitcnt lgkmcnt(0)\n\t" \
  "v_add_f32 v75, v40, v42\n\t" \
  "v_smfmac_f32_16x16x64_bf16 v[44:47], v[8:11], v[60:67], v68\n\t" \
  "ds_read_b128 v[60:63], v70 offset:10240\n\t" \
  "v_add_f32 v41, v41, v43\n\t" \
  "v_cvt_pk_bf16_f32 v75, v75, v41\n\t" \
  "v_smfmac_f32_16x16x64_bf16 v[44:47], v[12:15], a[128:135], v68\n\t" \
  "ds_write2st64_b32 v71, v74, v75 offset0:36 offset1:38\n\t" \
  "v_smfmac_f32_16x16x64_bf16 v[44:47], v[16:19], a[176:183], v68\n\t" \
  "ds_read_b128 v[64:67], v70 offset:11264\n\t" \
  "v_smfmac_f32_16x16x64_bf16 v[44:47], v[20:23], a[208:215], v68\n\t" \
  "v_smfmac_f32_16x16x64_bf16 v[44:47], v[24:27], v[80:87], v68\n\t" \
  "v_smfmac_f32_16x16x64_bf16 v[44:47], v[28:31], v[128:135], v68\n\t" \
  "s_waitcnt lgkmcnt(0)\n\t" \
  "v_smfmac_f32_16x16x64_bf16 v[44:47], v[32:35], v[60:67], v68\n\t" \
  "s_barrier\n\t" \
  "ds_read_b64_tr_b16 v[76:77], v72 offset:9216\n\t" \
  "v_smfmac_f32_16x16x64_bf16 v[48:51], v[0:3], a[8:15], v68\n\t" \
  "buffer_load_dwordx4 v[60:63], v69, s[28:31], s64 offen sc1\n\t" \
  "ds_read_b64_tr_b16 v[78:79], v72 offset:9728\n\t" \
  "v_smfmac_f32_16x16x64_bf16 v[48:51], v[4:7], a[56:63], v68\n\t" \
  "v_add_f32 v74, v44, v46\n\t" \
  "v_smfmac_f32_16x16x64_bf16 v[48:51], v[8:11], a[96:103], v68\n\t" \
  "v_add_f32 v45, v45, v47\n\t" \
  "v_smfmac_f32_16x16x64_bf16 v[48:51], v[12:15], a[136:143], v68\n\t" \
  "s_waitcnt vmcnt(3)\n\t" \
  "s_waitcnt lgkmcnt(0)\n\t" \
  "v_cvt_pk_bf16_f32 v74, v74, v45\n\t" \
  "v_mfma_f32_16x16x32_bf16 v[36:39], v[52:55], v[76:79], 0\n\t" \
  "s_waitcnt vmcnt(2)\n\t" \
  "v_mfma_f32_16x16x32_bf16 v[40:43], v[56:59], v[76:79], 0\n\t" \
  "buffer_load_dwordx4 v[64:67], v69, s[28:31], s64 offen offset:1024 sc1\n\t" \
  "v_mov_b64 v[52:53], 0\n\t" \
  "v_mov_b64 v[54:55], 0\n\t" \
  "v_smfmac_f32_16x16x64_bf16 v[48:51], v[16:19], a[184:191], v68\n\t" \
  "v_mov_b64 v[56:57], 0\n\t" \
  "v_mov_b64 v[58:59], 0\n\t" \
  "v_smfmac_f32_16x16x64_bf16 v[48:51], v[20:23], a[216:223], v68\n\t" \
  "ds_write_b32 v71, v74 offset:0\n\t" \
  "v_add_f32_dpp v36, v40, v36 quad_perm:[2,3,0,1] row_mask:0xf bank_mask:0xf\n\t" \
  "v_smfmac_f32_16x16x64_bf16 v[48:51], v[24:27], v[88:95], v68\n\t" \
  "v_add_f32_dpp v37, v41, v37 quad_perm:[2,3,0,1] row_mask:0xf bank_mask:0xf\n\t" \
  "v_smfmac_f32_16x16x64_bf16 v[48:51], v[28:31], v[136:143], v68\n\t" \
  "v_add_f32_dpp v38, v42, v38 quad_perm:[2,3,0,1] row_mask:0xf bank_mask:0xf\n\t" \
  "v_smfmac_f32_16x16x64_bf16 v[48:51], v[32:35], v[176:183], v68\n\t" \
  "v_add_f32_dpp v39, v43, v39 quad_perm:[2,3,0,1] row_mask:0xf bank_mask:0xf\n\t" \
  "v_smfmac_f32_16x16x64_bf16 v[52:55], v[0:3], a[16:23], v68\n\t" \
  "s_mov_b64 exec, s[58:59]\n\t" \
  "ds_write_b128 v69, v[36:39] offset:2080\n\t" \
  "s_mov_b64 exec, -1\n\t" \
  "v_smfmac_f32_16x16x64_bf16 v[52:55], v[4:7], a[64:71], v68\n\t" \
  "v_add_f32 v75, v48, v50\n\t" \
  "v_add_f32 v49, v49, v51\n\t" \
  "v_cvt_pk_bf16_f32 v75, v75, v49\n\t" \
  "v_smfmac_f32_16x16x64_bf16 v[56:59], v[0:3], a[24:31], v68\n\t" \
  "ds_write_b32 v71, v75 offset:512\n\t" \
  "v_smfmac_f32_16x16x64_bf16 v[56:59], v[4:7], a[72:79], v68\n\t" \
  "ds_read_b64_tr_b16 v[0:1], v72 offset:10240\n\t" \
  "ds_read_b64_tr_b16 v[2:3], v72 offset:10752\n\t" \
  "v_smfmac_f32_16x16x64_bf16 v[52:55], v[8:11], a[104:111], v68\n\t" \
  "ds_read_b128 v[36:39], v70 offset:12288\n\t" \
  "v_smfmac_f32_16x16x64_bf16 v[56:59], v[8:11], a[112:119], v68\n\t" \
  "ds_read_b128 v[40:43], v70 offset:13312\n\t" \
  "s_waitcnt lgkmcnt(4)\n\t" \
  "s_barrier\n\t" \
  "ds_read_b64_tr_b16 v[44:45], v72 offset:0\n\t" \
  "ds_read_b64_tr_b16 v[46:47], v72 offset:512\n\t" \
  "v_smfmac_f32_16x16x64_bf16 v[52:55], v[12:15], a[144:151], v68\n\t" \
  "ds_read_b64_tr_b16 v[48:49], v72 offset:1024\n\t" \
  "ds_read_b64_tr_b16 v[50:51], v72 offset:1536\n\t" \
  "v_smfmac_f32_16x16x64_bf16 v[56:59], v[12:15], a[152:159], v68\n\t" \
  "v_smfmac_f32_16x16x64_bf16 v[52:55], v[16:19], a[192:199], v68\n\t" \
  "ds_read_b128 v[4:7], v69 offset:2048\n\t" \
  "s_waitcnt lgkmcnt(5)\n\t" \
  "v_smfmac_f32_16x16x64_bf16 v[56:59], v[16:19], v[36:43], v68\n\t" \
  "ds_read_b128 v[8:11], v69 offset:3072\n\t" \
  "s_waitcnt lgkmcnt(4)\n\t" \
  "v_mfma_f32_16x16x32_bf16 v[12:15], v[44:47], v[76:79], 0\n\t" \
  "ds_read_b128 v[36:39], v70 offset:14336\n\t" \
  "s_waitcnt lgkmcnt(3)\n\t" \
  "v_mfma_f32_16x16x32_bf16 v[12:15], v[48:51], v[0:3], v[12:15]\n\t" \
  "buffer_store_dwordx4 v[44:47], v69, s[28:31], s69 offen offset:0 sc1\n\t" \
  "ds_read_b128 v[40:43], v70 offset:15360\n\t" \
  "v_smfmac_f32_16x16x64_bf16 v[52:55], v[20:23], a[224:231], v68\n\t" \
  "s_waitcnt lgkmcnt(2)\n\t" \
  "v_add_f32 v4, v4, v8\n\t" \
  "v_add_f32 v5, v5, v9\n\t" \
  "v_add_f32 v6, v6, v10\n\t" \
  "v_cndmask_b32 v78, v13, v14, s[56:57]\n\t" \
  "v_cndmask_b32 v76, v12, v15, s[56:57]\n\t" \
  "v_add_f32 v7, v7, v11\n\t" \
  "v_max3_f32 v77, v4, v5, v6\n\t" \
  "v_add_f32_dpp v76, v78, v76 quad_perm:[2,3,0,1] row_mask:0xf bank_mask:0xf\n\t" \
  "v_max3_f32 v77, v77, v7, v76\n\t" \
  "v_smfmac_f32_16x16x64_bf16 v[56:59], v[20:23], a[232:239], v68\n\t" \
  "ds_read_b128 v[44:47], v70 offset:16384\n\t" \
  "v_max_f32_dpp v77, v77, v77 quad_perm:[2,3,0,1] row_mask:0xf bank_mask:0xf\n\t" \
  "v_mov_b32 v78, v77\n\t" \
  "v_smfmac_f32_16x16x64_bf16 v[52:55], v[24:27], v[96:103], v68\n\t" \
  "ds_read_b128 v[48:51], v70 offset:17408\n\t" \
  "v_permlane16_swap_b32 v77, v78\n\t" \
  "v_max_f32 v77, v77, v78\n\t" \
  "v_mov_b32 v78, v77\n\t" \
  "v_smfmac_f32_16x16x64_bf16 v[52:55], v[28:31], v[144:151], v68\n\t" \
  "ds_read_b128 v[16:19], v70 offset:18432\n\t" \
  "v_permlane32_swap_b32 v77, v78\n\t" \
  "v_max_f32 v77, v77, v78\n\t" \
  "v_smfmac_f32_16x16x64_bf16 v[52:55], v[32:35], v[184:191], v68\n\t" \
  "ds_read_b128 v[20:23], v70 offset:19456\n\t" \
  "v_mul_f32 v77, 0xbe0293ee, v77\n\t" \
  "v_smfmac_f32_16x16x64_bf16 v[56:59], v[24:27], v[104:111], v68\n\t" \
  "v_fmamk_f32 v4, v4, 0x3e0293ee, v77\n\t" \
  "v_smfmac_f32_16x16x64_bf16 v[56:59], v[28:31], v[152:159], v68\n\t" \
  "ds_read_b128 v[24:27], v70 offset:22528\n\t" \
  "s_waitcnt lgkmcnt(4)\n\t" \
  "v_fmamk_f32 v5, v5, 0x3e0293ee, v77\n\t" \
  "v_smfmac_f32_16x16x64_bf16 v[56:59], v[32:35], v[36:43], v68\n\t" \
  "ds_read_b128 v[28:31], v70 offset:23552\n\t" \
  "v_fmamk_f32 v6, v6, 0x3e0293ee, v77\n\t" \
  "v_fmamk_f32 v7, v7, 0x3e0293ee, v77\n\t" \
  "v_fmamk_f32 v76, v76, 0x3e0293ee, v77\n\t" \
  "v_exp_f32 v4, v4\n\t" \
  "v_exp_f32 v5, v5\n\t" \
  "v_exp_f32 v6, v6\n\t" \
  "v_exp_f32 v7, v7\n\t" \
  "v_cvt_pk_bf16_f32 v4, v4, v5\n\t" \
  "v_exp_f32 v76, v76\n\t" \
  "v_cvt_pk_bf16_f32 v5, v6, v7\n\t" \
  "v_mov_b32_dpp v12, v4 quad_perm:[0,1,0,1] row_mask:0xf bank_mask:0xf\n\t" \
  "v_mov_b32_dpp v14, v4 quad_perm:[2,3,2,3] row_mask:0xf bank_mask:0xf\n\t" \
  "v_mov_b32_dpp v13, v5 quad_perm:[0,1,0,1] row_mask:0xf bank_mask:0xf\n\t" \
  "v_mov_b32_dpp v15, v5 quad_perm:[2,3,2,3] row_mask:0xf bank_mask:0xf\n\t" \
  "v_add_f32 v52, v52, v54\n\t" \
  "v_add_f32 v53, v53, v55\n\t" \
  "s_waitcnt vmcnt(2)\n\t" \
  "v_mfma_f32_16x16x32_bf16 v[0:3], v[60:63], v[12:15], 0\n\t" \
  "ds_read_b128 v[36:39], v70 offset:20480\n\t" \
  "v_add_f32 v56, v56, v58\n\t" \
  "v_add_f32 v57, v57, v59\n\t" \
  "v_cvt_pk_bf16_f32 v52, v52, v53\n\t" \
  "v_cvt_pk_bf16_f32 v53, v56, v57\n\t" \
  "s_waitcnt vmcnt(1)\n\t" \
  "v_mfma_f32_16x16x32_bf16 v[4:7], v[64:67], v[12:15], 0\n\t" \
  "ds_read_b128 v[40:43], v70 offset:21504\n\t" \
  "v_mov_b64 v[56:57], s[88:89]\n\t" \
  "v_mov_b64 v[58:59], s[88:89]\n\t" \
  "v_mov_b32_dpp v78, v76 quad_perm:[0,1,0,1] row_mask:0xf bank_mask:0xf\n\t" \
  "v_mov_b32_dpp v79, v76 quad_perm:[2,3,2,3] row_mask:0xf bank_mask:0xf\n\t" \
  "v_mfma_f32_16x16x32_bf16 v[32:35], v[56:59], v[12:15], 0\n\t" \
  "buffer_store_dwordx2 v[52:53], v71, s[28:31], s69 offen offset:2556 sc1\n\t" \
  "ds_read_b128 v[60:63], v70 offset:24576\n\t" \
  "v_cvt_pk_bf16_f32 v11, v78, v79\n\t" \
  "v_cvt_pk_bf16_f32 v8, v78, v79\n\t" \
  "v_mov_b64 v[54:55], 0\n\t" \
  "v_mov_b32 v9, 0\n\t" \
  "v_mov_b32 v10, 0\n\t" \
  "v_mov_b64 v[14:15], 0\n\t" \
  "v_mov_b64 v[12:13], 0\n\t" \
  "v_mfma_f32_16x16x32_bf16 v[32:35], v[54:57], v[8:11], v[32:35]\n\t" \
  "s_cmp_eq_u32 s48, 63\n\t" \
  "v_mfma_f32_16x16x32_bf16 v[0:3], v[52:55], v[8:11], v[0:3]\n\t" \
  "ds_read_b128 v[64:67], v70 offset:25600\n\t" \
  "v_mov_b64 v[56:57], 0\n\t" \
  "v_mov_b64 v[58:59], 0\n\t" \
  "v_mfma_f32_16x16x32_bf16 v[4:7], v[52:55], v[10:13], v[4:7]\n\t" \
  "s_cbranch_scc1 LLAST1_%=\n\t" \
  "v_rcp_f32 v74, v32\n\t" \
  "v_mov_b32 v11, 0\n\t" \
  "v_mov_b32 v8, 0\n\t" \
  "v_pk_mul_f32 v[0:1], v[0:1], v[74:75] op_sel_hi:[1,0]\n\t" \
  "v_pk_mul_f32 v[2:3], v[2:3], v[74:75] op_sel_hi:[1,0]\n\t" \
  "v_cvt_pk_bf16_f32 v76, v0, v1\n\t" \
  "v_cvt_pk_bf16_f32 v77, v2, v3\n\t" \
  "v_pk_mul_f32 v[4:5], v[4:5], v[74:75] op_sel_hi:[1,0]\n\t" \
  "v_pk_mul_f32 v[6:7], v[6:7], v[74:75] op_sel_hi:[1,0]\n\t" \
  "v_cvt_pk_bf16_f32 v78, v4, v5\n\t" \
  "v_cvt_pk_bf16_f32 v79, v6, v7\n\t" \
  "ds_write_b128 v69, v[76:79] offset:5120\n\t" \
  "s_waitcnt lgkmcnt(2)\n\t" \
  "v_smfmac_f32_16x16x64_bf16 v[12:15], v[76:79], v[44:51], v68\n\t" \
  "ds_read_b128 v[44:47], v70 offset:26624\n\t" \
  "v_smfmac_f32_16x16x64_bf16 v[56:59], v[76:79], v[16:23], v68\n\t" \
  "ds_read_b128 v[48:51], v70 offset:27648\n\t" \
  "v_mov_b64 v[52:53], 0\n\t" \
  "v_smfmac_f32_16x16x64_bf16 v[8:11], v[76:79], v[36:43], v68\n\t" \
  "ds_read_b128 v[16:19], v70 offset:28672\n\t" \
  "v_smfmac_f32_16x16x64_bf16 v[52:55], v[76:79], v[24:31], v68\n\t" \
  "s_waitcnt lgkmcnt(3)\n\t" \
  "s_barrier\n\t" \
  "ds_read_b128 v[0:3], v69 offset:4096\n\t" \
  "ds_read_b128 v[4:7], v69 offset:7168\n\t" \
  "v_lshrrev_b32 v71, 7, v69\n\t" \
  "v_and_b32 v74, 0x70, v69\n\t" \
  "v_lshl_add_u32 v71, v71, 14, v74\n\t" \
  "v_bfe_u32 v72, v69, 2, 7\n\t" \
  "v_and_b32 v74, 0x200, v69\n\t" \
  "s_waitcnt lgkmcnt(1)\n\t" \
  "v_smfmac_f32_16x16x64_bf16 v[8:11], v[0:3], v[60:67], v68\n\t" \
  "buffer_load_dword v75, off, s[28:31], s80 sc1\n\t" \
  "ds_read_b128 v[20:23], v70 offset:29696\n\t" \
  "v_mov_b64 v[76:77], 0\n\t" \
  "v_smfmac_f32_16x16x64_bf16 v[12:15], v[0:3], v[200:207], v68\n\t" \
  "ds_read_b128 v[32:35], v69 offset:6144\n\t" \
  "v_mov_b64 v[78:79], 0\n\t" \
  "v_smfmac_f32_16x16x64_bf16 v[52:55], v[0:3], v[44:51], v68\n\t" \
  "ds_read_b128 v[24:27], v70 offset:30720\n\t" \
  "v_lshl_add_u32 v72, v74, 3, v72\n\t" \
  "v_smfmac_f32_16x16x64_bf16 v[56:59], v[0:3], v[232:239], v68\n\t" \
  "ds_read_b128 v[28:31], v70 offset:31744\n\t" \
  "s_waitcnt lgkmcnt(4)\n\t" \
  "v_smfmac_f32_16x16x64_bf16 v[12:15], v[4:7], v[208:215], v68\n\t" \
  "ds_read_b128 v[60:63], v70 offset:32768\n\t" \
  "s_waitcnt lgkmcnt(4)\n\t" \
  "v_smfmac_f32_16x16x64_bf16 v[8:11], v[4:7], v[16:23], v68\n\t" \
  "ds_read_b128 v[64:67], v70 offset:33792\n\t" \
  "s_waitcnt lgkmcnt(4)\n\t" \
  "v_smfmac_f32_16x16x64_bf16 v[8:11], v[32:35], v[192:199], v68\n\t" \
  "ds_read_b128 v[36:39], v73 offset:4096\n\t" \
  "v_smfmac_f32_16x16x64_bf16 v[12:15], v[32:35], v[216:223], v68\n\t" \
  "ds_read_b128 v[40:43], v73 offset:6144\n\t" \
  "s_waitcnt lgkmcnt(4)\n\t" \
  "v_smfmac_f32_16x16x64_bf16 v[52:55], v[4:7], v[24:31], v68\n\t" \
  "ds_read_b128 v[44:47], v70 offset:34816\n\t" \
  "v_cvt_pk_bf16_f32 v8, v8, v9\n\t" \
  "v_smfmac_f32_16x16x64_bf16 v[52:55], v[32:35], v[224:231], v68\n\t" \
  "ds_read_b128 v[48:51], v70 offset:35840\n\t" \
  "v_cvt_pk_bf16_f32 v9, v10, v11\n\t" \
  "v_smfmac_f32_16x16x64_bf16 v[56:59], v[4:7], v[240:247], v68\n\t" \
  "s_waitcnt lgkmcnt(4)\n\t" \
  "v_cvt_pk_bf16_f32 v10, v12, v13\n\t" \
  "v_smfmac_f32_16x16x64_bf16 v[56:59], v[32:35], v[60:67], v68\n\t" \
  "s_waitcnt lgkmcnt(3)\n\t" \
  "v_cvt_pk_bf16_f32 v11, v14, v15\n\t" \
  "v_smfmac_f32_16x16x64_bf16 v[76:79], v[36:39], v[248:255], v68\n\t" \
  "buffer_store_dwordx4 v[8:11], v71, s[28:31], s54 offen sc1\n\t" \
  "s_waitcnt lgkmcnt(0)\n\t" \
  "v_cvt_pk_bf16_f32 v52, v52, v53\n\t" \
  "v_smfmac_f32_16x16x64_bf16 v[76:79], v[40:43], v[44:51], v68\n\t" \
  "v_cvt_pk_bf16_f32 v53, v54, v55\n\t" \
  "v_cvt_pk_bf16_f32 v54, v56, v57\n\t" \
  "v_cvt_pk_bf16_f32 v55, v58, v59\n\t" \
  "buffer_store_dwordx4 v[52:55], v71, s[28:31], s75 offen sc1\n\t" \
  "s_nop 2\n\t" \
  "v_add_f32 v76, v76, v78\n\t" \
  "v_add_f32 v77, v77, v79\n\t" \
  "v_cvt_pk_bf16_f32 v76, v76, v77\n\t" \
  "buffer_store_dword v76, v72, s[28:31], s55 offen sc1\n\t" \
  "s_memrealtime s[70:71]\n\t" \
  "s_waitcnt vmcnt(3)\n\t" \
  "v_readfirstlane_b32 s65, v75\n\t" \
  "s_barrier\n\t" \
  "s_waitcnt lgkmcnt(0)\n\t" \
  "s_not_b32 s65, s65\n\t" \
  "s_lshl_b32 s61, s70, 8\n\t" \
  "s_andn2_b32 s64, s65, 0xff\n\t" \
  "s_sub_u32 s64, s64, s61\n\t" \
  "s_ashr_i32 s64, s64, 8\n\t" \
  "s_cmp_eq_u32 s65, 0\n\t" \
  "s_cselect_b32 s64, " CH_DLF ", s64\n\t" \
  "s_sub_i32 s64, s64, " CH_RO1 "\n\t" \
  "s_min_i32 s64, s64, s34\n\t" \
  "s_max_i32 s64, s64, " CH_DLL1 "\n\t" \
  "s_add_u32 s64, s64, s70\n\t" \
  "s_mov_b32 s74, 0\n\t" \
  "s_movk_i32 s60, 8192\n\t" \
  "RWT1_%=:\n\t" \
  "s_memrealtime s[72:73]\n\t" \
  "s_sub_u32 s60, s60, 1\n\t" \
  "s_cselect_b32 s61, 0, -1\n\t" \
  "s_waitcnt lgkmcnt(0)\n\t" \
  "s_sub_i32 s73, s72, s64\n\t" \
  "s_and_b32 s73, s73, s61\n\t" \
  "s_cmp_lt_i32 s73, " CH_FIN "\n\t" \
  "s_cbranch_scc1 RWT1_%=\n\t" \
  "s_cmp_lt_i32 s73, 0\n\t" \
  "s_cbranch_scc1 RWT1_%=f\n\t" \
  "RWT1_%=g:\n\t" \
  "RRETRY1_%=:\n\t" \
  "buffer_load_dwordx4 v[0:3], v69, s[28:31], s97 offen offset:0 sc1\n\t" \
  "buffer_load_dwordx4 v[4:7], v69, s[28:31], s97 offen offset:1024 sc1\n\t" \
  "buffer_load_dwordx4 v[8:11], v69, s[28:31], s97 offen offset:2048 sc1\n\t" \
  "buffer_load_dwordx4 v[12:15], v69, s[28:31], s97 offen offset:3072 sc1\n\t" \
  "v_lshrrev_b32 v20, 2, v69\n\t" \
  "v_and_b32 v20, 16, v20\n\t" \
  "v_mov_b32 v21, 0x3f80\n\t" \
  "v_lshlrev_b32 v21, v20, v21\n\t" \
  "v_bfe_u32 v20, v69, 4, 2\n\t" \
  "v_cmp_eq_u32 vcc, 0, v20\n\t" \
  "s_nop 1\n\t" \
  "v_cndmask_b32 v32, 0, v21, vcc\n\t" \
  "v_cmp_eq_u32 vcc, 1, v20\n\t" \
  "s_nop 1\n\t" \
  "v_cndmask_b32 v33, 0, v21, vcc\n\t" \
  "v_cmp_eq_u32 vcc, 2, v20\n\t" \
  "s_nop 1\n\t" \
  "v_cndmask_b32 v34, 0, v21, vcc\n\t" \
  "v_cmp_eq_u32 vcc, 3, v20\n\t" \
  "s_nop 1\n\t" \
  "v_cndmask_b32 v35, 0, v21, vcc\n\t" \
  "v_bfe_u32 v38, v69, 7, 1\n\t" \
  "v_bfe_u32 v20, v69, 9, 1\n\t" \
  "v_lshl_add_u32 v38, v20, 2, v38\n\t" \
  "v_lshlrev_b32 v38, 12, v38\n\t" \
  "v_and_b32 v20, 0x60, v69\n\t" \
  "v_add_u32 v38, v38, v20\n\t" \
  "v_bfe_u32 v20, v69, 8, 1\n\t" \
  "v_lshl_add_u32 v38, v20, 4, v38\n\t" \
  "v_bfe_u32 v20, v69, 4, 1\n\t" \
  "v_lshl_add_u32 v38, v20, 3, v38\n\t" \
  "v_mov_b32 v44, 0\n\t" \
  CH_ALD \
  "s_waitcnt vmcnt(3)\n\t" \
  "v_mfma_f32_16x16x32_bf16 v[40:43], v[32:35], v[0:3], 0\n\t" \
  "v_max3_u32 v44, v44, v0, v1\n\t" \
  "v_max3_u32 v44, v44, v2, v3\n\t" \
  "s_waitcnt vmcnt(2)\n\t" \
  "v_mfma_f32_16x16x32_bf16 v[40:43], v[32:35], v[4:7], v[40:43]\n\t" \
  "v_max3_u32 v44, v44, v4, v5\n\t" \
  "v_max3_u32 v44, v44, v6, v7\n\t" \
  "s_waitcnt vmcnt(1)\n\t" \
  "v_mfma_f32_16x16x32_bf16 v[40:43], v[32:35], v[8:11], v[40:43]\n\t" \
  "v_max3_u32 v44, v44, v8, v9\n\t" \
  "v_max3_u32 v44, v44, v10, v11\n\t" \
  "s_waitcnt vmcnt(0)\n\t" \
  "v_mfma_f32_16x16x32_bf16 v[40:43], v[32:35], v[12:15], v[40:43]\n\t" \
  "v_max3_u32 v44, v44, v12, v13\n\t" \
  "v_max3_u32 v44, v44, v14, v15\n\t" \
  "v_cmp_eq_u32 vcc, -1, v44\n\t" \
  "s_cbranch_vccnz RFAIL1_%=\n\t" \
  "s_nop 3\n\t" \
  "v_add_f32_dpp v40, v40, v40 row_ror:8 row_mask:0xf bank_mask:0xf\n\t" \
  "v_add_f32_dpp v41, v41, v41 row_ror:8 row_mask:0xf bank_mask:0xf\n\t" \
  "v_add_f32_dpp v42, v42, v42 row_ror:8 row_mask:0xf bank_mask:0xf\n\t" \
  "v_add_f32_dpp v43, v43, v43 row_ror:8 row_mask:0xf bank_mask:0xf\n\t" \
  "v_cvt_pk_bf16_f32 v40, v40, v41\n\t" \
  "v_cvt_pk_bf16_f32 v41, v42, v43\n\t" \
  "buffer_store_dwordx2 v[40:41], v38, s[28:31], s98 offen sc1\n\t" \
  "RDONE1_%=:\n\t" \
  "s_barrier\n\t" \
  "s_add_u32 s48, s48, 1\n\t" \
  "s_mov_b32 s49, s53\n\t" \
  "s_add_u32 s53, s53, " CH_SLOT "\n\t" \
  "s_add_u32 s60, s53, " CH_SKIP "\n\t" \
  "s_lshr_b32 s61, s96, 1\n\t" \
  "s_add_u32 s86, s86, 1\n\t" \
  "s_cmp_eq_u32 s86, 3\n\t" \
  "s_cselect_b32 s53, s60, s53\n\t" \
  "s_cselect_b32 s96, s61, s96\n\t" \
  "s_cselect_b32 s86, 0, s86\n\t" \
  "s_cmp_ge_u32 s53, " CH_RING "\n\t" \
  "s_cselect_b32 s53, 0, s53\n\t" \
  "s_cselect_b32 s96, s21, s96\n\t" \
  "s_andn2_b32 s53, s53, 0x2000\n\t" \
  "s_and_b32 s60, s96, 1\n\t" \
  "s_lshl_b32 s60, s60, 13\n\t" \
  "s_or_b32 s53, s53, s60\n\t" \
  "s_mov_b32 s68, 0\n\t" \
  "s_add_u32 s97, s41, s53\n\t" \
  "s_add_u32 s98, s37, s53\n\t" \
  "s_add_u32 s82, s48, 0\n\t" \
  "s_and_b32 s82, s82, " CH_TRM "\n\t" \
  "s_lshr_b64 s[60:61], s[90:91], s82\n\t" \
  "s_and_b32 s60, s60, 1\n\t" \
  "s_lshl_b32 s60, s60, 13\n\t" \
  "s_add_u32 s82, s82, " CH_SG0 "\n\t" \
  "s_lshl_b32 s82, s82, 21\n\t" \
  "s_or_b32 s82, s82, s60\n\t" \
  "s_add_u32 s50, s82, s38\n\t" \
  "s_add_u32 s69, s82, s39\n\t" \
  "s_cmp_eq_u32 s34, 0\n\t" \
  "s_cbranch_scc1 LINI_%=\n\t" \
  "s_branch LG1_%=\n\t" \
  "RFAIL1_%=:\n\t" \
  "s_add_u32 s74, s74, 1\n\t" \
  "s_sleep " CH_RSL "\n\t" \
  "s_waitcnt vmcnt(0)\n\t" \
  "s_cmp_gt_u32 s74, s84\n\t" \
  "s_cbranch_scc1 RG1_%=\n\t" \
  "s_and_b32 s60, s74, 1023\n\t" \
  "s_cmp_lg_u32 s60, 0\n\t" \
  "s_cbranch_scc1 RRETRY1_%=\n\t" \
  "buffer_load_dword v46, off, s[28:31], s92 sc1\n\t" \
  "s_waitcnt vmcnt(0)\n\t" \
  "v_readfirstlane_b32 s60, v46\n\t" \
  "s_nop 0\n\t" \
  "s_cmp_eq_u32 s60, 0\n\t" \
  "s_cbranch_scc1 RRETRY1_%=\n\t" \
  "RG1_%=:\n\t" \
  "s_mov_b32 s84, 0\n\t" \
  "s_lshl_b32 s60, s36, 4\n\t" \
  "s_or_b32 s60, s60, s48\n\t" \
  "s_or_b32 s60, s60, 536870912\n\t" \
  "v_mov_b32 v76, s60\n\t" \
  "v_mov_b32 v77, 0\n\t" \
  "s_mov_b64 exec, 1\n\t" \
  "buffer_atomic_cmpswap v[76:77], off, s[28:31], s92 sc1\n\t" \
  "s_mov_b64 exec, -1\n\t" \
  "s_branch RDONE1_%=\n\t" \
  "LFAIL1_%=:\n\t" \
  "v_max_u32 v75, v0, v1\n\t" \
  "v_max3_u32 v75, v75, v2, v3\n\t" \
  "v_max3_u32 v75, v75, v4, v5\n\t" \
  "v_max3_u32 v75, v75, v6, v7\n\t" \
  "v_max3_u32 v75, v75, v8, v9\n\t" \
  "v_max3_u32 v75, v75, v10, v11\n\t" \
  "v_max3_u32 v75, v75, v12, v13\n\t" \
  "v_max3_u32 v75, v75, v14, v15\n\t" \
  "v_max3_u32 v75, v75, v16, v17\n\t" \
  "v_max3_u32 v75, v75, v18, v19\n\t" \
  "v_max3_u32 v75, v75, v20, v21\n\t" \
  "v_max3_u32 v75, v75, v22, v23\n\t" \
  "v_max3_u32 v75, v75, v24, v25\n\t" \
  "v_max3_u32 v75, v75, v26, v27\n\t" \
  "v_max3_u32 v75, v75, v28, v29\n\t" \
  "v_max3_u32 v75, v75, v30, v31\n\t" \
  "v_max3_u32 v75, v75, v32, v33\n\t" \
  "v_max3_u32 v75, v75, v34, v35\n\t" \
  "v_cmp_eq_u32 vcc, -1, v75\n\t" \
  "s_cbranch_vccz LGO1_%=\n\t" \
  "s_add_u32 s68, s68, 1\n\t" \
  "s_waitcnt lgkmcnt(0)\n\t" \
  "s_cmp_gt_u32 s68, s83\n\t" \
  "s_cbranch_scc1 LGV1_%=\n\t" \
  "s_barrier\n\t" \
  "s_waitcnt vmcnt(8)\n\t" \
  "s_branch LG1_%=\n\t" \
  "RWT1_%=f:\n\t" \
  "s_cmp_lt_i32 s73, -2\n\t" \
  "s_cbranch_scc0 RWT1_%=g\n\t" \
  "s_nop 4\n\t" \
  "s_branch RWT1_%=g\n\t" \
  "LGV1_%=:\n\t" \
  "s_mov_b32 s83, 0\n\t" \
  "s_lshl_b32 s60, s36, 4\n\t" \
  "s_or_b32 s60, s60, s48\n\t" \
  "s_or_b32 s60, s60, 805306368\n\t" \
  "v_mov_b32 v76, s60\n\t" \
  "v_mov_b32 v77, 0\n\t" \
  "s_mov_b64 exec, 1\n\t" \
  "buffer_atomic_cmpswap v[76:77], off, s[28:31], s92 sc1\n\t" \
  "s_mov_b64 exec, -1\n\t" \
  "s_branch LGO1_%=\n\t" \
  "LLAST1_%=:\n\t" \
  "s_waitcnt vmcnt(0) lgkmcnt(0)\n\t" \
  "s_barrier\n\t" \
  "s_branch LEND_%=\n\t" \
  ".p2align 6\n\t" \
  "LG2_%=:\n\t" \
  "s_barrier\n\t" \
  "buffer_load_dwordx4 v[52:55], v69, s[28:31], s50 offen sc1\n\t" \
  "buffer_load_dwordx4 v[56:59], v69, s[28:31], s50 offen offset:1024 sc1\n\t" \
  "s_add_u32 s64, s50, 18432\n\t" \
  "s_movk_i32 m0, 11264\n\t" \
  "v_lshlrev_b32 v75, 1, v69\n\t" \
  "buffer_load_dword v75, s[28:31], s64 offen sc1 lds\n\t" \
  "ds_read_b128 v[44:47], v70 offset:0\n\t" \
  "ds_read_b128 v[48:51], v70 offset:1024\n\t" \
  "ds_read_b128 v[60:63], v70 offset:2048\n\t" \
  "ds_read_b128 v[64:67], v70 offset:3072\n\t" \
  "v_mov_b64 v[36:37], 0\n\t" \
  "v_mov_b64 v[38:39], 0\n\t" \
  "v_mov_b64 v[40:41], 0\n\t" \
  "v_mov_b64 v[42:43], 0\n\t" \
  "v_lshrrev_b32 v71, 1, v69\n\t" \
  "v_lshrrev_b32 v72, 3, v69\n\t" \
  "v_and_b32 v72, 0x78, v72\n\t" \
  "v_and_b32 v74, 0x30, v69\n\t" \
  "v_lshl_add_u32 v72, v74, 3, v72\n\t" \
  "s_add_u32 s54, s53, s36\n\t" \
  "s_add_u32 s55, s54, 557056\n\t" \
  "s_add_u32 s54, s54, 131072\n\t" \
  "s_add_u32 s75, s54, 262144\n\t" \
  "s_add_u32 s80, s53, s26\n\t" \
  ".p2align 5\n\t" \
  ".fill 2, 4, 0xbf800000\n\t" \
  "s_waitcnt vmcnt(9)\n\t" \
  "s_barrier\n\t" \
  "ds_read_b128 v[0:3], v69 offset:0\n\t" \
  "ds_read_b128 v[4:7], v69 offset:1024\n\t" \
  "ds_read_b128 v[8:11], v69 offset:2048\n\t" \
  "ds_read_b128 v[12:15], v69 offset:3072\n\t" \
  "s_waitcnt lgkmcnt(3)\n\t" \
  "v_smfmac_f32_16x16x64_bf16 v[36:39], v[0:3], v[44:51], v68\n\t" \
  "ds_read_b128 v[44:47], v70 offset:4096\n\t" \
  "v_smfmac_f32_16x16x64_bf16 v[40:43], v[0:3], v[60:67], v68\n\t" \
  "ds_read_b128 v[48:51], v70 offset:5120\n\t" \
  "s_waitcnt lgkmcnt(4)\n\t" \
  "v_smfmac_f32_16x16x64_bf16 v[36:39], v[4:7], a[32:39], v68\n\t" \
  "ds_read_b128 v[60:63], v70 offset:6144\n\t" \
  "v_smfmac_f32_16x16x64_bf16 v[40:43], v[4:7], a[40:47], v68\n\t" \
  "ds_read_b128 v[64:67], v70 offset:7168\n\t" \
  "s_waitcnt lgkmcnt(5)\n\t" \
  "v_smfmac_f32_16x16x64_bf16 v[36:39], v[8:11], a[80:87], v68\n\t" \
  "s_barrier\n\t" \
  "ds_read_b128 v[16:19], v69 offset:4096\n\t" \
  "v_smfmac_f32_16x16x64_bf16 v[40:43], v[8:11], a[88:95], v68\n\t" \
  "ds_read_b128 v[20:23], v69 offset:5120\n\t" \
  "s_waitcnt lgkmcnt(6)\n\t" \
  "v_smfmac_f32_16x16x64_bf16 v[36:39], v[12:15], a[120:127], v68\n\t" \
  "ds_read_b128 v[24:27], v69 offset:6144\n\t" \
  "s_waitcnt lgkmcnt(5)\n\t" \
  "v_smfmac_f32_16x16x64_bf16 v[40:43], v[12:15], v[44:51], v68\n\t" \
  "ds_read_b128 v[28:31], v69 offset:7168\n\t" \
  "s_waitcnt lgkmcnt(3)\n\t" \
  "v_smfmac_f32_16x16x64_bf16 v[36:39], v[16:19], a[160:167], v68\n\t" \
  "ds_read_b128 v[32:35], v69 offset:8192\n\t" \
  "v_smfmac_f32_16x16x64_bf16 v[40:43], v[16:19], a[168:175], v68\n\t" \
  "s_waitcnt lgkmcnt(3)\n\t" \
  "v_mov_b64 v[44:45], 0\n\t" \
  "v_smfmac_f32_16x16x64_bf16 v[36:39], v[20:23], a[200:207], v68\n\t" \
  "s_waitcnt lgkmcnt(2)\n\t" \
  "v_mov_b64 v[46:47], 0\n\t" \
  "v_smfmac_f32_16x16x64_bf16 v[36:39], v[24:27], a[240:247], v68\n\t" \
  "s_waitcnt lgkmcnt(1)\n\t" \
  "v_mov_b64 v[48:49], 0\n\t" \
  "v_smfmac_f32_16x16x64_bf16 v[36:39], v[28:31], v[112:119], v68\n\t" \
  "s_waitcnt lgkmcnt(0)\n\t" \
  "v_mov_b64 v[50:51], 0\n\t" \
  "v_smfmac_f32_16x16x64_bf16 v[36:39], v[32:35], v[160:167], v68\n\t" \
  "v_smfmac_f32_16x16x64_bf16 v[40:43], v[20:23], v[60:67], v68\n\t" \
  "ds_read_b128 v[60:63], v70 offset:8192\n\t" \
  "v_smfmac_f32_16x16x64_bf16 v[40:43], v[24:27], a[248:255], v68\n\t" \
  "ds_read_b128 v[64:67], v70 offset:9216\n\t" \
  "v_add_f32 v74, v36, v38\n\t" \
  "v_smfmac_f32_16x16x64_bf16 v[40:43], v[28:31], v[120:127], v68\n\t" \
  "v_add_f32 v37, v37, v39\n\t" \
  "v_smfmac_f32_16x16x64_bf16 v[40:43], v[32:35], v[168:175], v68\n\t" \
  "v_cmp_u_f32 vcc, v74, v37\n\t" \
  "v_smfmac_f32_16x16x64_bf16 v[44:47], v[0:3], a[0:7], v68\n\t" \
  "s_cbranch_vccnz LFAIL2_%=\n\t" \
  "LGO2_%=:\n\t" \
  "v_cvt_pk_bf16_f32 v74, v74, v37\n\t" \
  "v_smfmac_f32_16x16x64_bf16 v[44:47], v[4:7], a[48:55], v68\n\t" \
  "s_waitcnt lgkmcnt(0)\n\t" \
  "v_add_f32 v75, v40, v42\n\t" \
  "v_smfmac_f32_16x16x64_bf16 v[44:47], v[8:11], v[60:67], v68\n\t" \
  "ds_read_b128 v[60:63], v70 offset:10240\n\t" \
  "v_add_f32 v41, v41, v43\n\t" \
  "v_cvt_pk_bf16_f32 v75, v75, v41\n\t" \
  "v_smfmac_f32_16x16x64_bf16 v[44:47], v[12:15], a[128:135], v68\n\t" \
  "ds_write2st64_b32 v71, v74, v75 offset0:40 offset1:42\n\t" \
  "v_smfmac_f32_16x16x64_bf16 v[44:47], v[16:19], a[176:183], v68\n\t" \
  "ds_read_b128 v[64:67], v70 offset:11264\n\t" \
  "v_smfmac_f32_16x16x64_bf16 v[44:47], v[20:23], a[208:215], v68\n\t" \
  "v_smfmac_f32_16x16x64_bf16 v[44:47], v[24:27], v[80:87], v68\n\t" \
  "v_smfmac_f32_16x16x64_bf16 v[44:47], v[28:31], v[128:135], v68\n\t" \
  "s_waitcnt lgkmcnt(0)\n\t" \
  "v_smfmac_f32_16x16x64_bf16 v[44:47], v[32:35], v[60:67], v68\n\t" \
  "s_barrier\n\t" \
  "ds_read_b64_tr_b16 v[76:77], v72 offset:10240\n\t" \
  "v_smfmac_f32_16x16x64_bf16 v[48:51], v[0:3], a[8:15], v68\n\t" \
  "ds_read_b64_tr_b16 v[78:79], v72 offset:10752\n\t" \
  "v_smfmac_f32_16x16x64_bf16 v[48:51], v[4:7], a[56:63], v68\n\t" \
  "buffer_load_dwordx4 v[60:63], v69, s[28:31], s64 offen sc1\n\t" \
  "v_add_f32 v74, v44, v46\n\t" \
  "v_smfmac_f32_16x16x64_bf16 v[48:51], v[8:11], a[96:103], v68\n\t" \
  "v_add_f32 v45, v45, v47\n\t" \
  "v_smfmac_f32_16x16x64_bf16 v[48:51], v[12:15], a[136:143], v68\n\t" \
  "buffer_load_dwordx4 v[64:67], v69, s[28:31], s64 offen offset:1024 sc1\n\t" \
  "s_waitcnt vmcnt(4)\n\t" \
  "s_waitcnt lgkmcnt(0)\n\t" \
  "v_cvt_pk_bf16_f32 v74, v74, v45\n\t" \
  "v_mfma_f32_16x16x32_bf16 v[36:39], v[52:55], v[76:79], 0\n\t" \
  "s_waitcnt vmcnt(3)\n\t" \
  "v_mfma_f32_16x16x32_bf16 v[40:43], v[56:59], v[76:79], 0\n\t" \
  "v_mov_b64 v[52:53], 0\n\t" \
  "v_mov_b64 v[54:55], 0\n\t" \
  "v_smfmac_f32_16x16x64_bf16 v[48:51], v[16:19], a[184:191], v68\n\t" \
  "v_mov_b64 v[56:57], 0\n\t" \
  "v_mov_b64 v[58:59], 0\n\t" \
  "v_smfmac_f32_16x16x64_bf16 v[48:51], v[20:23], a[216:223], v68\n\t" \
  "ds_write_b32 v71, v74 offset:1024\n\t" \
  "v_add_f32_dpp v36, v40, v36 quad_perm:[2,3,0,1] row_mask:0xf bank_mask:0xf\n\t" \
  "v_smfmac_f32_16x16x64_bf16 v[48:51], v[24:27], v[88:95], v68\n\t" \
  "v_add_f32_dpp v37, v41, v37 quad_perm:[2,3,0,1] row_mask:0xf bank_mask:0xf\n\t" \
  "v_smfmac_f32_16x16x64_bf16 v[48:51], v[28:31], v[136:143], v68\n\t" \
  "v_add_f32_dpp v38, v42, v38 quad_perm:[2,3,0,1] row_mask:0xf bank_mask:0xf\n\t" \
  "v_smfmac_f32_16x16x64_bf16 v[48:51], v[32:35], v[176:183], v68\n\t" \
  "v_add_f32_dpp v39, v43, v39 quad_perm:[2,3,0,1] row_mask:0xf bank_mask:0xf\n\t" \
  "v_smfmac_f32_16x16x64_bf16 v[52:55], v[0:3], a[16:23], v68\n\t" \
  "s_mov_b64 exec, s[58:59]\n\t" \
  "ds_write_b128 v69, v[36:39] offset:3072\n\t" \
  "s_mov_b64 exec, -1\n\t" \
  "v_smfmac_f32_16x16x64_bf16 v[52:55], v[4:7], a[64:71], v68\n\t" \
  "v_add_f32 v75, v48, v50\n\t" \
  "v_add_f32 v49, v49, v51\n\t" \
  "v_cvt_pk_bf16_f32 v75, v75, v49\n\t" \
  "v_smfmac_f32_16x16x64_bf16 v[56:59], v[0:3], a[24:31], v68\n\t" \
  "ds_write_b32 v71, v75 offset:1536\n\t" \
  "v_smfmac_f32_16x16x64_bf16 v[56:59], v[4:7], a[72:79], v68\n\t" \
  "ds_read_b64_tr_b16 v[0:1], v72 offset:9216\n\t" \
  "ds_read_b64_tr_b16 v[2:3], v72 offset:9728\n\t" \
  "v_smfmac_f32_16x16x64_bf16 v[52:55], v[8:11], a[104:111], v68\n\t" \
  "ds_read_b128 v[36:39], v70 offset:12288\n\t" \
  "v_smfmac_f32_16x16x64_bf16 v[56:59], v[8:11], a[112:119], v68\n\t" \
  "ds_read_b128 v[40:43], v70 offset:13312\n\t" \
  "s_waitcnt lgkmcnt(4)\n\t" \
  "s_barrier\n\t" \
  "ds_read_b64_tr_b16 v[44:45], v72 offset:1024\n\t" \
  "ds_read_b64_tr_b16 v[46:47], v72 offset:1536\n\t" \
  "v_smfmac_f32_16x16x64_bf16 v[52:55], v[12:15], a[144:151], v68\n\t" \
  "ds_read_b64_tr_b16 v[48:49], v72 offset:0\n\t" \
  "ds_read_b64_tr_b16 v[50:51], v72 offset:512\n\t" \
  "v_smfmac_f32_16x16x64_bf16 v[56:59], v[12:15], a[152:159], v68\n\t" \
  "v_smfmac_f32_16x16x64_bf16 v[52:55], v[16:19], a[192:199], v68\n\t" \
  "ds_read_b128 v[4:7], v69 offset:2048\n\t" \
  "s_waitcnt lgkmcnt(5)\n\t" \
  "v_smfmac_f32_16x16x64_bf16 v[56:59], v[16:19], v[36:43], v68\n\t" \
  "ds_read_b128 v[8:11], v69 offset:3072\n\t" \
  "s_waitcnt lgkmcnt(4)\n\t" \
  "v_mfma_f32_16x16x32_bf16 v[12:15], v[44:47], v[76:79], 0\n\t" \
  "ds_read_b128 v[36:39], v70 offset:14336\n\t" \
  "s_waitcnt lgkmcnt(3)\n\t" \
  "v_mfma_f32_16x16x32_bf16 v[12:15], v[48:51], v[0:3], v[12:15]\n\t" \
  "ds_read_b128 v[40:43], v70 offset:15360\n\t" \
  "v_smfmac_f32_16x16x64_bf16 v[52:55], v[20:23], a[224:231], v68\n\t" \
  "buffer_store_dwordx4 v[44:47], v69, s[28:31], s69 offen offset:1024 sc1\n\t" \
  "s_waitcnt lgkmcnt(2)\n\t" \
  "v_add_f32 v4, v4, v8\n\t" \
  "v_add_f32 v5, v5, v9\n\t" \
  "v_add_f32 v6, v6, v10\n\t" \
  "v_cndmask_b32 v78, v13, v14, s[56:57]\n\t" \
  "v_cndmask_b32 v76, v12, v15, s[56:57]\n\t" \
  "v_add_f32 v7, v7, v11\n\t" \
  "v_max3_f32 v77, v4, v5, v6\n\t" \
  "v_add_f32_dpp v76, v78, v76 quad_perm:[2,3,0,1] row_mask:0xf bank_mask:0xf\n\t" \
  "v_max3_f32 v77, v77, v7, v76\n\t" \
  "v_smfmac_f32_16x16x64_bf16 v[56:59], v[20:23], a[232:239], v68\n\t" \
  "ds_read_b128 v[44:47], v70 offset:16384\n\t" \
  "v_max_f32_dpp v77, v77, v77 quad_perm:[2,3,0,1] row_mask:0xf bank_mask:0xf\n\t" \
  "v_mov_b32 v78, v77\n\t" \
  "v_smfmac_f32_16x16x64_bf16 v[52:55], v[24:27], v[96:103], v68\n\t" \
  "ds_read_b128 v[48:51], v70 offset:17408\n\t" \
  "v_permlane16_swap_b32 v77, v78\n\t" \
  "v_max_f32 v77, v77, v78\n\t" \
  "v_mov_b32 v78, v77\n\t" \
  "v_smfmac_f32_16x16x64_bf16 v[52:55], v[28:31], v[144:151], v68\n\t" \
  "ds_read_b128 v[16:19], v70 offset:18432\n\t" \
  "v_permlane32_swap_b32 v77, v78\n\t" \
  "v_max_f32 v77, v77, v78\n\t" \
  "v_smfmac_f32_16x16x64_bf16 v[52:55], v[32:35], v[184:191], v68\n\t" \
  "ds_read_b128 v[20:23], v70 offset:19456\n\t" \
  "v_mul_f32 v77, 0xbe0293ee, v77\n\t" \
  "v_smfmac_f32_16x16x64_bf16 v[56:59], v[24:27], v[104:111], v68\n\t" \
  "v_fmamk_f32 v4, v4, 0x3e0293ee, v77\n\t" \
  "v_smfmac_f32_16x16x64_bf16 v[56:59], v[28:31], v[152:159], v68\n\t" \
  "ds_read_b128 v[24:27], v70 offset:22528\n\t" \
  "s_waitcnt lgkmcnt(4)\n\t" \
  "v_fmamk_f32 v5, v5, 0x3e0293ee, v77\n\t" \
  "v_smfmac_f32_16x16x64_bf16 v[56:59], v[32:35], v[36:43], v68\n\t" \
  "ds_read_b128 v[28:31], v70 offset:23552\n\t" \
  "v_fmamk_f32 v6, v6, 0x3e0293ee, v77\n\t" \
  "v_fmamk_f32 v7, v7, 0x3e0293ee, v77\n\t" \
  "v_fmamk_f32 v76, v76, 0x3e0293ee, v77\n\t" \
  "v_exp_f32 v4, v4\n\t" \
  "v_exp_f32 v5, v5\n\t" \
  "v_exp_f32 v6, v6\n\t" \
  "v_exp_f32 v7, v7\n\t" \
  "v_cvt_pk_bf16_f32 v4, v4, v5\n\t" \
  "v_exp_f32 v76, v76\n\t" \
  "v_cvt_pk_bf16_f32 v5, v6, v7\n\t" \
  "v_mov_b32_dpp v12, v4 quad_perm:[0,1,0,1] row_mask:0xf bank_mask:0xf\n\t" \
  "v_mov_b32_dpp v14, v4 quad_perm:[2,3,2,3] row_mask:0xf bank_mask:0xf\n\t" \
  "v_mov_b32_dpp v13, v5 quad_perm:[0,1,0,1] row_mask:0xf bank_mask:0xf\n\t" \
  "v_mov_b32_dpp v15, v5 quad_perm:[2,3,2,3] row_mask:0xf bank_mask:0xf\n\t" \
  "v_add_f32 v52, v52, v54\n\t" \
  "v_add_f32 v53, v53, v55\n\t" \
  "s_waitcnt vmcnt(2)\n\t" \
  "v_mfma_f32_16x16x32_bf16 v[0:3], v[60:63], v[12:15], 0\n\t" \
  "ds_read_b128 v[36:39], v70 offset:20480\n\t" \
  "v_add_f32 v56, v56, v58\n\t" \
  "v_add_f32 v57, v57, v59\n\t" \
  "v_cvt_pk_bf16_f32 v52, v52, v53\n\t" \
  "v_cvt_pk_bf16_f32 v53, v56, v57\n\t" \
  "s_waitcnt vmcnt(1)\n\t" \
  "v_mfma_f32_16x16x32_bf16 v[4:7], v[64:67], v[12:15], 0\n\t" \
  "ds_read_b128 v[40:43], v70 offset:21504\n\t" \
  "v_mov_b64 v[56:57], s[88:89]\n\t" \
  "v_mov_b64 v[58:59], s[88:89]\n\t" \
  "v_mov_b32_dpp v78, v76 quad_perm:[0,1,0,1] row_mask:0xf bank_mask:0xf\n\t" \
  "v_mov_b32_dpp v79, v76 quad_perm:[2,3,2,3] row_mask:0xf bank_mask:0xf\n\t" \
  "v_mfma_f32_16x16x32_bf16 v[32:35], v[56:59], v[12:15], 0\n\t" \
  "ds_read_b128 v[60:63], v70 offset:24576\n\t" \
  "v_cvt_pk_bf16_f32 v11, v78, v79\n\t" \
  "v_cvt_pk_bf16_f32 v8, v78, v79\n\t" \
  "v_mov_b64 v[54:55], 0\n\t" \
  "v_mov_b32 v9, 0\n\t" \
  "v_mov_b32 v10, 0\n\t" \
  "v_mov_b64 v[14:15], 0\n\t" \
  "v_mov_b64 v[12:13], 0\n\t" \
  "v_mfma_f32_16x16x32_bf16 v[32:35], v[54:57], v[8:11], v[32:35]\n\t" \
  "buffer_store_dwordx2 v[52:53], v71, s[28:31], s69 offen offset:3072 sc1\n\t" \
  "s_cmp_eq_u32 s48, 63\n\t" \
  "v_mfma_f32_16x16x32_bf16 v[0:3], v[52:55], v[8:11], v[0:3]\n\t" \
  "ds_read_b128 v[64:67], v70 offset:25600\n\t" \
  "v_mov_b64 v[56:57], 0\n\t" \
  "v_mov_b64 v[58:59], 0\n\t" \
  "v_mfma_f32_16x16x32_bf16 v[4:7], v[52:55], v[10:13], v[4:7]\n\t" \
  "s_cbranch_scc1 LLAST2_%=\n\t" \
  "v_rcp_f32 v74, v32\n\t" \
  "v_mov_b32 v11, 0\n\t" \
  "v_mov_b32 v8, 0\n\t" \
  "v_pk_mul_f32 v[0:1], v[0:1], v[74:75] op_sel_hi:[1,0]\n\t" \
  "v_pk_mul_f32 v[2:3], v[2:3], v[74:75] op_sel_hi:[1,0]\n\t" \
  "v_cvt_pk_bf16_f32 v76, v0, v1\n\t" \
  "v_cvt_pk_bf16_f32 v77, v2, v3\n\t" \
  "v_pk_mul_f32 v[4:5], v[4:5], v[74:75] op_sel_hi:[1,0]\n\t" \
  "v_pk_mul_f32 v[6:7], v[6:7], v[74:75] op_sel_hi:[1,0]\n\t" \
  "v_cvt_pk_bf16_f32 v78, v4, v5\n\t" \
  "v_cvt_pk_bf16_f32 v79, v6, v7\n\t" \
  "ds_write_b128 v69, v[76:79] offset:6144\n\t" \
  "s_waitcnt lgkmcnt(2)\n\t" \
  "v_smfmac_f32_16x16x64_bf16 v[12:15], v[76:79], v[44:51], v68\n\t" \
  "ds_read_b128 v[44:47], v70 offset:26624\n\t" \
  "v_smfmac_f32_16x16x64_bf16 v[56:59], v[76:79], v[16:23], v68\n\t" \
  "ds_read_b128 v[48:51], v70 offset:27648\n\t" \
  "v_mov_b64 v[52:53], 0\n\t" \
  "v_smfmac_f32_16x16x64_bf16 v[8:11], v[76:79], v[36:43], v68\n\t" \
  "ds_read_b128 v[16:19], v70 offset:28672\n\t" \
  "v_smfmac_f32_16x16x64_bf16 v[52:55], v[76:79], v[24:31], v68\n\t" \
  "s_waitcnt lgkmcnt(3)\n\t" \
  "s_barrier\n\t" \
  "ds_read_b128 v[0:3], v69 offset:7168\n\t" \
  "ds_read_b128 v[4:7], v69 offset:4096\n\t" \
  "v_lshrrev_b32 v71, 7, v69\n\t" \
  "v_and_b32 v74, 0x70, v69\n\t" \
  "v_lshl_add_u32 v71, v71, 14, v74\n\t" \
  "v_bfe_u32 v72, v69, 2, 7\n\t" \
  "v_and_b32 v74, 0x200, v69\n\t" \
  "s_waitcnt lgkmcnt(1)\n\t" \
  "v_smfmac_f32_16x16x64_bf16 v[8:11], v[0:3], v[60:67], v68\n\t" \
  "ds_read_b128 v[20:23], v70 offset:29696\n\t" \
  "v_mov_b64 v[76:77], 0\n\t" \
  "v_smfmac_f32_16x16x64_bf16 v[12:15], v[0:3], v[200:207], v68\n\t" \
  "buffer_load_dword v75, off, s[28:31], s80 sc1\n\t" \
  "ds_read_b128 v[32:35], v69 offset:5120\n\t" \
  "v_mov_b64 v[78:79], 0\n\t" \
  "v_smfmac_f32_16x16x64_bf16 v[52:55], v[0:3], v[44:51], v68\n\t" \
  "ds_read_b128 v[24:27], v70 offset:30720\n\t" \
  "v_lshl_add_u32 v72, v74, 3, v72\n\t" \
  "v_smfmac_f32_16x16x64_bf16 v[56:59], v[0:3], v[232:239], v68\n\t" \
  "ds_read_b128 v[28:31], v70 offset:31744\n\t" \
  "s_waitcnt lgkmcnt(4)\n\t" \
  "v_smfmac_f32_16x16x64_bf16 v[12:15], v[4:7], v[208:215], v68\n\t" \
  "ds_read_b128 v[60:63], v70 offset:32768\n\t" \
  "s_waitcnt lgkmcnt(4)\n\t" \
  "v_smfmac_f32_16x16x64_bf16 v[8:11], v[4:7], v[16:23], v68\n\t" \
  "ds_read_b128 v[64:67], v70 offset:33792\n\t" \
  "s_waitcnt lgkmcnt(4)\n\t" \
  "v_smfmac_f32_16x16x64_bf16 v[8:11], v[32:35], v[192:199], v68\n\t" \
  "ds_read_b128 v[36:39], v73 offset:4096\n\t" \
  "v_smfmac_f32_16x16x64_bf16 v[12:15], v[32:35], v[216:223], v68\n\t" \
  "ds_read_b128 v[40:43], v73 offset:6144\n\t" \
  "s_waitcnt lgkmcnt(4)\n\t" \
  "v_smfmac_f32_16x16x64_bf16 v[52:55], v[4:7], v[24:31], v68\n\t" \
  "ds_read_b128 v[44:47], v70 offset:34816\n\t" \
  "v_cvt_pk_bf16_f32 v8, v8, v9\n\t" \
  "v_smfmac_f32_16x16x64_bf16 v[52:55], v[32:35], v[224:231], v68\n\t" \
  "ds_read_b128 v[48:51], v70 offset:35840\n\t" \
  "v_cvt_pk_bf16_f32 v9, v10, v11\n\t" \
  "v_smfmac_f32_16x16x64_bf16 v[56:59], v[4:7], v[240:247], v68\n\t" \
  "s_waitcnt lgkmcnt(4)\n\t" \
  "v_cvt_pk_bf16_f32 v10, v12, v13\n\t" \
  "v_smfmac_f32_16x16x64_bf16 v[56:59], v[32:35], v[60:67], v68\n\t" \
  "s_waitcnt lgkmcnt(3)\n\t" \
  "v_cvt_pk_bf16_f32 v11, v14, v15\n\t" \
  "v_smfmac_f32_16x16x64_bf16 v[76:79], v[36:39], v[248:255], v68\n\t" \
  "buffer_store_dwordx4 v[8:11], v71, s[28:31], s54 offen sc1\n\t" \
  "s_waitcnt lgkmcnt(0)\n\t" \
  "v_cvt_pk_bf16_f32 v52, v52, v53\n\t" \
  "v_smfmac_f32_16x16x64_bf16 v[76:79], v[40:43], v[44:51], v68\n\t" \
  "v_cvt_pk_bf16_f32 v53, v54, v55\n\t" \
  "v_cvt_pk_bf16_f32 v54, v56, v57\n\t" \
  "v_cvt_pk_bf16_f32 v55, v58, v59\n\t" \
  "buffer_store_dwordx4 v[52:55], v71, s[28:31], s75 offen sc1\n\t" \
  "s_nop 2\n\t" \
  "v_add_f32 v76, v76, v78\n\t" \
  "v_add_f32 v77, v77, v79\n\t" \
  "v_cvt_pk_bf16_f32 v76, v76, v77\n\t" \
  "buffer_store_dword v76, v72, s[28:31], s55 offen sc1\n\t" \
  "s_memrealtime s[70:71]\n\t" \
  "s_waitcnt vmcnt(3)\n\t" \
  "v_readfirstlane_b32 s65, v75\n\t" \
  "s_barrier\n\t" \
  "s_waitcnt lgkmcnt(0)\n\t" \
  "s_not_b32 s65, s65\n\t" \
  "s_lshl_b32 s61, s70, 8\n\t" \
  "s_andn2_b32 s64, s65, 0xff\n\t" \
  "s_sub_u32 s64, s64, s61\n\t" \
  "s_ashr_i32 s64, s64, 8\n\t" \
  "s_cmp_eq_u32 s65, 0\n\t" \
  "s_cselect_b32 s64, " CH_DLF ", s64\n\t" \
  "s_min_i32 s64, s64, s34\n\t" \
  "s_max_i32 s64, s64, " CH_DLL1 "\n\t" \
  "s_add_u32 s64, s64, s70\n\t" \
  "s_mov_b32 s74, 0\n\t" \
  "s_movk_i32 s60, 8192\n\t" \
  "RWT2_%=:\n\t" \
  "s_memrealtime s[72:73]\n\t" \
  "s_sub_u32 s60, s60, 1\n\t" \
  "s_cselect_b32 s61, 0, -1\n\t" \
  "s_waitcnt lgkmcnt(0)\n\t" \
  "s_sub_i32 s73, s72, s64\n\t" \
  "s_and_b32 s73, s73, s61\n\t" \
  "s_cmp_lt_i32 s73, " CH_FIN "\n\t" \
  "s_cbranch_scc1 RWT2_%=\n\t" \
  "s_cmp_lt_i32 s73, 0\n\t" \
  "s_cbranch_scc1 RWT2_%=f\n\t" \
  "RWT2_%=g:\n\t" \
  "RRETRY2_%=:\n\t" \
  "buffer_load_dwordx4 v[0:3], v69, s[28:31], s97 offen offset:0 sc1\n\t" \
  "buffer_load_dwordx4 v[4:7], v69, s[28:31], s97 offen offset:1024 sc1\n\t" \
  "buffer_load_dwordx4 v[8:11], v69, s[28:31], s97 offen offset:2048 sc1\n\t" \
  "buffer_load_dwordx4 v[12:15], v69, s[28:31], s97 offen offset:3072 sc1\n\t" \
  "v_lshrrev_b32 v20, 2, v69\n\t" \
  "v_and_b32 v20, 16, v20\n\t" \
  "v_mov_b32 v21, 0x3f80\n\t" \
  "v_lshlrev_b32 v21, v20, v21\n\t" \
  "v_bfe_u32 v20, v69, 4, 2\n\t" \
  "v_cmp_eq_u32 vcc, 0, v20\n\t" \
  "s_nop 1\n\t" \
  "v_cndmask_b32 v32, 0, v21, vcc\n\t" \
  "v_cmp_eq_u32 vcc, 1, v20\n\t" \
  "s_nop 1\n\t" \
  "v_cndmask_b32 v33, 0, v21, vcc\n\t" \
  "v_cmp_eq_u32 vcc, 2, v20\n\t" \
  "s_nop 1\n\t" \
  "v_cndmask_b32 v34, 0, v21, vcc\n\t" \
  "v_cmp_eq_u32 vcc, 3, v20\n\t" \
  "s_nop 1\n\t" \
  "v_cndmask_b32 v35, 0, v21, vcc\n\t" \
  "v_bfe_u32 v38, v69, 7, 1\n\t" \
  "v_bfe_u32 v20, v69, 9, 1\n\t" \
  "v_lshl_add_u32 v38, v20, 2, v38\n\t" \
  "v_lshlrev_b32 v38, 12, v38\n\t" \
  "v_and_b32 v20, 0x60, v69\n\t" \
  "v_add_u32 v38, v38, v20\n\t" \
  "v_bfe_u32 v20, v69, 8, 1\n\t" \
  "v_lshl_add_u32 v38, v20, 4, v38\n\t" \
  "v_bfe_u32 v20, v69, 4, 1\n\t" \
  "v_lshl_add_u32 v38, v20, 3, v38\n\t" \
  "v_mov_b32 v44, 0\n\t" \
  CH_ALD \
  "s_waitcnt vmcnt(3)\n\t" \
  "v_mfma_f32_16x16x32_bf16 v[40:43], v[32:35], v[0:3], 0\n\t" \
  "v_max3_u32 v44, v44, v0, v1\n\t" \
  "v_max3_u32 v44, v44, v2, v3\n\t" \
  "s_waitcnt vmcnt(2)\n\t" \
  "v_mfma_f32_16x16x32_bf16 v[40:43], v[32:35], v[4:7], v[40:43]\n\t" \
  "v_max3_u32 v44, v44, v4, v5\n\t" \
  "v_max3_u32 v44, v44, v6, v7\n\t" \
  "s_waitcnt vmcnt(1)\n\t" \
  "v_mfma_f32_16x16x32_bf16 v[40:43], v[32:35], v[8:11], v[40:43]\n\t" \
  "v_max3_u32 v44, v44, v8, v9\n\t" \
  "v_max3_u32 v44, v44, v10, v11\n\t" \
  "s_waitcnt vmcnt(0)\n\t" \
  "v_mfma_f32_16x16x32_bf16 v[40:43], v[32:35], v[12:15], v[40:43]\n\t" \
  "v_max3_u32 v44, v44, v12, v13\n\t" \
  "v_max3_u32 v44, v44, v14, v15\n\t" \
  "v_cmp_eq_u32 vcc, -1, v44\n\t" \
  "s_cbranch_vccnz RFAIL2_%=\n\t" \
  "s_nop 3\n\t" \
  "v_add_f32_dpp v40, v40, v40 row_ror:8 row_mask:0xf bank_mask:0xf\n\t" \
  "v_add_f32_dpp v41, v41, v41 row_ror:8 row_mask:0xf bank_mask:0xf\n\t" \
  "v_add_f32_dpp v42, v42, v42 row_ror:8 row_mask:0xf bank_mask:0xf\n\t" \
  "v_add_f32_dpp v43, v43, v43 row_ror:8 row_mask:0xf bank_mask:0xf\n\t" \
  "v_cvt_pk_bf16_f32 v40, v40, v41\n\t" \
  "v_cvt_pk_bf16_f32 v41, v42, v43\n\t" \
  "buffer_store_dwordx2 v[40:41], v38, s[28:31], s98 offen sc1\n\t" \
  "RDONE2_%=:\n\t" \
  "s_barrier\n\t" \
  "s_add_u32 s48, s48, 1\n\t" \
  "s_mov_b32 s49, s53\n\t" \
  "s_add_u32 s53, s53, " CH_SLOT "\n\t" \
  "s_add_u32 s60, s53, " CH_SKIP "\n\t" \
  "s_lshr_b32 s61, s96, 1\n\t" \
  "s_add_u32 s86, s86, 1\n\t" \
  "s_cmp_eq_u32 s86, 3\n\t" \
  "s_cselect_b32 s53, s60, s53\n\t" \
  "s_cselect_b32 s96, s61, s96\n\t" \
  "s_cselect_b32 s86, 0, s86\n\t" \
  "s_cmp_ge_u32 s53, " CH_RING "\n\t" \
  "s_cselect_b32 s53, 0, s53\n\t" \
  "s_cselect_b32 s96, s21, s96\n\t" \
  "s_andn2_b32 s53, s53, 0x2000\n\t" \
  "s_and_b32 s60, s96, 1\n\t" \
  "s_lshl_b32 s60, s60, 13\n\t" \
  "s_or_b32 s53, s53, s60\n\t" \
  "s_mov_b32 s68, 0\n\t" \
  "s_add_u32 s97, s41, s53\n\t" \
  "s_add_u32 s98, s37, s53\n\t" \
  "s_add_u32 s82, s48, 0\n\t" \
  "s_and_b32 s82, s82, " CH_TRM "\n\t" \
  "s_lshr_b64 s[60:61], s[90:91], s82\n\t" \
  "s_and_b32 s60, s60, 1\n\t" \
  "s_lshl_b32 s60, s60, 13\n\t" \
  "s_add_u32 s82, s82, " CH_SG0 "\n\t" \
  "s_lshl_b32 s82, s82, 21\n\t" \
  "s_or_b32 s82, s82, s60\n\t" \
  "s_add_u32 s50, s82, s38\n\t" \
  "s_add_u32 s69, s82, s39\n\t" \
  "s_cmp_eq_u32 s34, 0\n\t" \
  "s_cbranch_scc1 LINI_%=\n\t" \
  "s_branch LG2_%=\n\t" \
  "RFAIL2_%=:\n\t" \
  "s_add_u32 s74, s74, 1\n\t" \
  "s_sleep " CH_RSL "\n\t" \
  "s_waitcnt vmcnt(0)\n\t" \
  "s_cmp_gt_u32 s74, s84\n\t" \
  "s_cbranch_scc1 RG2_%=\n\t" \
  "s_and_b32 s60, s74, 1023\n\t" \
  "s_cmp_lg_u32 s60, 0\n\t" \
  "s_cbranch_scc1 RRETRY2_%=\n\t" \
  "buffer_load_dword v46, off, s[28:31], s92 sc1\n\t" \
  "s_waitcnt vmcnt(0)\n\t" \
  "v_readfirstlane_b32 s60, v46\n\t" \
  "s_nop 0\n\t" \
  "s_cmp_eq_u32 s60, 0\n\t" \
  "s_cbranch_scc1 RRETRY2_%=\n\t" \
  "RG2_%=:\n\t" \
  "s_mov_b32 s84, 0\n\t" \
  "s_lshl_b32 s60, s36, 4\n\t" \
  "s_or_b32 s60, s60, s48\n\t" \
  "s_or_b32 s60, s60, 536870912\n\t" \
  "v_mov_b32 v76, s60\n\t" \
  "v_mov_b32 v77, 0\n\t" \
  "s_mov_b64 exec, 1\n\t" \
  "buffer_atomic_cmpswap v[76:77], off, s[28:31], s92 sc1\n\t" \
  "s_mov_b64 exec, -1\n\t" \
  "s_branch RDONE2_%=\n\t" \
  "LFAIL2_%=:\n\t" \
  "v_max_u32 v75, v0, v1\n\t" \
  "v_max3_u32 v75, v75, v2, v3\n\t" \
  "v_max3_u32 v75, v75, v4, v5\n\t" \
  "v_max3_u32 v75, v75, v6, v7\n\t" \
  "v_max3_u32 v75, v75, v8, v9\n\t" \
  "v_max3_u32 v75, v75, v10, v11\n\t" \
  "v_max3_u32 v75, v75, v12, v13\n\t" \
  "v_max3_u32 v75, v75, v14, v15\n\t" \
  "v_max3_u32 v75, v75, v16, v17\n\t" \
  "v_max3_u32 v75, v75, v18, v19\n\t" \
  "v_max3_u32 v75, v75, v20, v21\n\t" \
  "v_max3_u32 v75, v75, v22, v23\n\t" \
  "v_max3_u32 v75, v75, v24, v25\n\t" \
  "v_max3_u32 v75, v75, v26, v27\n\t" \
  "v_max3_u32 v75, v75, v28, v29\n\t" \
  "v_max3_u32 v75, v75, v30, v31\n\t" \
  "v_max3_u32 v75, v75, v32, v33\n\t" \
  "v_max3_u32 v75, v75, v34, v35\n\t" \
  "v_cmp_eq_u32 vcc, -1, v75\n\t" \
  "s_cbranch_vccz LGO2_%=\n\t" \
  "s_add_u32 s68, s68, 1\n\t" \
  "s_waitcnt lgkmcnt(0)\n\t" \
  "s_cmp_gt_u32 s68, s83\n\t" \
  "s_cbranch_scc1 LGV2_%=\n\t" \
  "s_barrier\n\t" \
  "s_waitcnt vmcnt(8)\n\t" \
  "s_branch LG2_%=\n\t" \
  "RWT2_%=f:\n\t" \
  "s_cmp_lt_i32 s73, -2\n\t" \
  "s_cbranch_scc0 RWT2_%=g\n\t" \
  "s_nop 4\n\t" \
  "s_branch RWT2_%=g\n\t" \
  "LGV2_%=:\n\t" \
  "s_mov_b32 s83, 0\n\t" \
  "s_lshl_b32 s60, s36, 4\n\t" \
  "s_or_b32 s60, s60, s48\n\t" \
  "s_or_b32 s60, s60, 805306368\n\t" \
  "v_mov_b32 v76, s60\n\t" \
  "v_mov_b32 v77, 0\n\t" \
  "s_mov_b64 exec, 1\n\t" \
  "buffer_atomic_cmpswap v[76:77], off, s[28:31], s92 sc1\n\t" \
  "s_mov_b64 exec, -1\n\t" \
  "s_branch LGO2_%=\n\t" \
  "LLAST2_%=:\n\t" \
  "s_waitcnt vmcnt(0) lgkmcnt(0)\n\t" \
  "s_barrier\n\t" \
  "s_branch LEND_%=\n\t" \
  ".p2align 6\n\t" \
  "LG3_%=:\n\t" \
  "s_barrier\n\t" \
  "buffer_load_dwordx4 v[52:55], v69, s[28:31], s50 offen sc1\n\t" \
  "buffer_load_dwordx4 v[56:59], v69, s[28:31], s50 offen offset:1024 sc1\n\t" \
  "s_add_u32 s64, s50, 16384\n\t" \
  "s_movk_i32 m0, 11264\n\t" \
  "v_lshlrev_b32 v75, 1, v69\n\t" \
  "buffer_load_dword v75, s[28:31], s64 offen sc1 lds\n\t" \
  "s_lshr_b32 s61, s20, 3\n\t" \
  "s_mul_i32 s47, s61, " CH_KD "\n\t" \
  "s_lshr_b32 s47, s47, 10\n\t" \
  "s_lshl_b32 s47, s47, 8\n\t" \
  "s_mul_i32 s61, s61, " CH_KB "\n\t" \
  "s_add_u32 s61, s61, s22\n\t" \
  "s_lshr_b32 s61, s61, 10\n\t" \
  "s_min_u32 s61, s61, 100\n\t" \
  "s_or_b32 s47, s47, s61\n\t" \
  "s_cmp_lt_u32 s48, " CH_ES "\n\t" \
  "s_cselect_b32 s60, " CH_EX ", 0\n\t" \
  "s_add_u32 s47, s47, s60\n\t" \
  "s_not_b32 s47, s47\n\t" \
  "s_add_u32 s71, s53, s46\n\t" \
  "ds_read_b128 v[44:47], v70 offset:0\n\t" \
  "ds_read_b128 v[48:51], v70 offset:1024\n\t" \
  "ds_read_b128 v[60:63], v70 offset:2048\n\t" \
  "ds_read_b128 v[64:67], v70 offset:3072\n\t" \
  "v_mov_b64 v[36:37], 0\n\t" \
  "v_mov_b64 v[38:39], 0\n\t" \
  "v_mov_b64 v[40:41], 0\n\t" \
  "v_mov_b64 v[42:43], 0\n\t" \
  "v_lshrrev_b32 v71, 1, v69\n\t" \
  "v_add_u32 v71, 4, v71\n\t" \
  "v_lshrrev_b32 v72, 3, v69\n\t" \
  "v_and_b32 v72, 0x78, v72\n\t" \
  "v_and_b32 v74, 0x30, v69\n\t" \
  "v_lshl_add_u32 v72, v74, 3, v72\n\t" \
  "s_add_u32 s54, s53, s36\n\t" \
  "s_add_u32 s55, s54, 573440\n\t" \
  "s_add_u32 s54, s54, 135168\n\t" \
  "s_add_u32 s75, s54, 262144\n\t" \
  "s_add_u32 s80, s53, s26\n\t" \
  "s_or_b32 s87, s52, 0x1fff80\n\t" \
  "s_and_b32 s87, s87, -128\n\t" \
  "s_add_u32 s87, s87, 0x200000\n\t" \
  ".p2align 5\n\t" \
  ".fill 2, 4, 0xbf800000\n\t" \
  "s_waitcnt vmcnt(9)\n\t" \
  "s_barrier\n\t" \
  "ds_read_b128 v[0:3], v69 offset:0\n\t" \
  "ds_read_b128 v[4:7], v69 offset:1024\n\t" \
  "ds_read_b128 v[8:11], v69 offset:2048\n\t" \
  "ds_read_b128 v[12:15], v69 offset:3072\n\t" \
  "s_waitcnt lgkmcnt(3)\n\t" \
  "v_smfmac_f32_16x16x64_bf16 v[36:39], v[0:3], v[44:51], v68\n\t" \
  "ds_read_b128 v[44:47], v70 offset:4096\n\t" \
  "v_smfmac_f32_16x16x64_bf16 v[40:43], v[0:3], v[60:67], v68\n\t" \
  "ds_read_b128 v[48:51], v70 offset:5120\n\t" \
  "s_waitcnt lgkmcnt(4)\n\t" \
  "v_smfmac_f32_16x16x64_bf16 v[36:39], v[4:7], a[32:39], v68\n\t" \
  "ds_read_b128 v[60:63], v70 offset:6144\n\t" \
  "v_smfmac_f32_16x16x64_bf16 v[40:43], v[4:7], a[40:47], v68\n\t" \
  "ds_read_b128 v[64:67], v70 offset:7168\n\t" \
  "s_waitcnt lgkmcnt(5)\n\t" \
  "v_smfmac_f32_16x16x64_bf16 v[36:39], v[8:11], a[80:87], v68\n\t" \
  "s_barrier\n\t" \
  "ds_read_b128 v[16:19], v69 offset:4096\n\t" \
  "v_smfmac_f32_16x16x64_bf16 v[40:43], v[8:11], a[88:95], v68\n\t" \
  "ds_read_b128 v[20:23], v69 offset:5120\n\t" \
  "s_waitcnt lgkmcnt(6)\n\t" \
  "v_smfmac_f32_16x16x64_bf16 v[36:39], v[12:15], a[120:127], v68\n\t" \
  "ds_read_b128 v[24:27], v69 offset:6144\n\t" \
  "s_waitcnt lgkmcnt(5)\n\t" \
  "v_smfmac_f32_16x16x64_bf16 v[40:43], v[12:15], v[44:51], v68\n\t" \
  "ds_read_b128 v[28:31], v69 offset:7168\n\t" \
  "s_waitcnt lgkmcnt(3)\n\t" \
  "v_smfmac_f32_16x16x64_bf16 v[36:39], v[16:19], a[160:167], v68\n\t" \
  "ds_read_b128 v[32:35], v69 offset:8192\n\t" \
  "v_smfmac_f32_16x16x64_bf16 v[40:43], v[16:19], a[168:175], v68\n\t" \
  "s_waitcnt lgkmcnt(3)\n\t" \
  "v_mov_b64 v[44:45], 0\n\t" \
  "v_smfmac_f32_16x16x64_bf16 v[36:39], v[20:23], a[200:207], v68\n\t" \
  "s_waitcnt lgkmcnt(2)\n\t" \
  "v_mov_b64 v[46:47], 0\n\t" \
  "v_smfmac_f32_16x16x64_bf16 v[36:39], v[24:27], a[240:247], v68\n\t" \
  "s_waitcnt lgkmcnt(1)\n\t" \
  "v_mov_b64 v[48:49], 0\n\t" \
  "v_smfmac_f32_16x16x64_bf16 v[36:39], v[28:31], v[112:119], v68\n\t" \
  "s_waitcnt lgkmcnt(0)\n\t" \
  "v_mov_b64 v[50:51], 0\n\t" \
  "v_smfmac_f32_16x16x64_bf16 v[36:39], v[32:35], v[160:167], v68\n\t" \
  "v_smfmac_f32_16x16x64_bf16 v[40:43], v[20:23], v[60:67], v68\n\t" \
  "ds_read_b128 v[60:63], v70 offset:8192\n\t" \
  "v_smfmac_f32_16x16x64_bf16 v[40:43], v[24:27], a[248:255], v68\n\t" \
  "ds_read_b128 v[64:67], v70 offset:9216\n\t" \
  "v_add_f32 v74, v36, v38\n\t" \
  "v_smfmac_f32_16x16x64_bf16 v[40:43], v[28:31], v[120:127], v68\n\t" \
  "v_add_f32 v37, v37, v39\n\t" \
  "v_smfmac_f32_16x16x64_bf16 v[40:43], v[32:35], v[168:175], v68\n\t" \
  "v_cmp_u_f32 vcc, v74, v37\n\t" \
  "v_smfmac_f32_16x16x64_bf16 v[44:47], v[0:3], a[0:7], v68\n\t" \
  "s_cbranch_vccnz LFAIL3_%=\n\t" \
  "LGO3_%=:\n\t" \
  "v_cvt_pk_bf16_f32 v74, v74, v37\n\t" \
  "v_smfmac_f32_16x16x64_bf16 v[44:47], v[4:7], a[48:55], v68\n\t" \
  "s_waitcnt lgkmcnt(0)\n\t" \
  "v_add_f32 v75, v40, v42\n\t" \
  "v_smfmac_f32_16x16x64_bf16 v[44:47], v[8:11], v[60:67], v68\n\t" \
  "ds_read_b128 v[60:63], v70 offset:10240\n\t" \
  "v_add_f32 v41, v41, v43\n\t" \
  "v_cvt_pk_bf16_f32 v75, v75, v41\n\t" \
  "v_smfmac_f32_16x16x64_bf16 v[44:47], v[12:15], a[128:135], v68\n\t" \
  "ds_write2st64_b32 v71, v74, v75 offset0:40 offset1:42\n\t" \
  "v_smfmac_f32_16x16x64_bf16 v[44:47], v[16:19], a[176:183], v68\n\t" \
  "ds_read_b128 v[64:67], v70 offset:11264\n\t" \
  "s_memrealtime s[66:67]\n\t" \
  "v_smfmac_f32_16x16x64_bf16 v[44:47], v[20:23], a[208:215], v68\n\t" \
  "v_smfmac_f32_16x16x64_bf16 v[44:47], v[24:27], v[80:87], v68\n\t" \
  "v_smfmac_f32_16x16x64_bf16 v[44:47], v[28:31], v[128:135], v68\n\t" \
  "s_waitcnt lgkmcnt(0)\n\t" \
  "v_smfmac_f32_16x16x64_bf16 v[44:47], v[32:35], v[60:67], v68\n\t" \
  "s_barrier\n\t" \
  "ds_read_b64_tr_b16 v[76:77], v72 offset:10240\n\t" \
  "v_smfmac_f32_16x16x64_bf16 v[48:51], v[0:3], a[8:15], v68\n\t" \
  "s_lshl_b32 s60, s66, 8\n\t" \
  "s_sub_u32 s60, s47, s60\n\t" \
  "ds_read_b64_tr_b16 v[78:79], v72 offset:10752\n\t" \
  "v_mov_b32 v75, s60\n\t" \
  "v_smfmac_f32_16x16x64_bf16 v[48:51], v[4:7], a[56:63], v68\n\t" \
  "s_mov_b64 exec, s[24:25]\n\t" \
  "buffer_atomic_umin v75, v69, s[28:31], s71 offen sc1\n\t" \
  "s_mov_b64 exec, -1\n\t" \
  "v_add_f32 v74, v44, v46\n\t" \
  "v_smfmac_f32_16x16x64_bf16 v[48:51], v[8:11], a[96:103], v68\n\t" \
  "buffer_load_dwordx4 v[60:63], v69, s[28:31], s64 offen sc1\n\t" \
  "v_add_f32 v45, v45, v47\n\t" \
  "v_smfmac_f32_16x16x64_bf16 v[48:51], v[12:15], a[136:143], v68\n\t" \
  "s_waitcnt vmcnt(4)\n\t" \
  "s_waitcnt lgkmcnt(0)\n\t" \
  "v_cvt_pk_bf16_f32 v74, v74, v45\n\t" \
  "v_mfma_f32_16x16x32_bf16 v[36:39], v[52:55], v[76:79], 0\n\t" \
  "s_waitcnt vmcnt(3)\n\t" \
  "v_mfma_f32_16x16x32_bf16 v[40:43], v[56:59], v[76:79], 0\n\t" \
  "v_mov_b64 v[52:53], 0\n\t" \
  "v_mov_b64 v[54:55], 0\n\t" \
  "v_smfmac_f32_16x16x64_bf16 v[48:51], v[16:19], a[184:191], v68\n\t" \
  "buffer_load_dwordx4 v[64:67], v69, s[28:31], s64 offen offset:1024 sc1\n\t" \
  "v_mov_b64 v[56:57], 0\n\t" \
  "v_mov_b64 v[58:59], 0\n\t" \
  "v_smfmac_f32_16x16x64_bf16 v[48:51], v[20:23], a[216:223], v68\n\t" \
  "ds_write_b32 v71, v74 offset:1024\n\t" \
  "v_add_f32_dpp v36, v40, v36 quad_perm:[2,3,0,1] row_mask:0xf bank_mask:0xf\n\t" \
  "v_smfmac_f32_16x16x64_bf16 v[48:51], v[24:27], v[88:95], v68\n\t" \
  "v_add_f32_dpp v37, v41, v37 quad_perm:[2,3,0,1] row_mask:0xf bank_mask:0xf\n\t" \
  "v_smfmac_f32_16x16x64_bf16 v[48:51], v[28:31], v[136:143], v68\n\t" \
  "v_add_f32_dpp v38, v42, v38 quad_perm:[2,3,0,1] row_mask:0xf bank_mask:0xf\n\t" \
  "v_smfmac_f32_16x16x64_bf16 v[48:51], v[32:35], v[176:183], v68\n\t" \
  "v_add_f32_dpp v39, v43, v39 quad_perm:[2,3,0,1] row_mask:0xf bank_mask:0xf\n\t" \
  "v_smfmac_f32_16x16x64_bf16 v[52:55], v[0:3], a[16:23], v68\n\t" \
  "s_mov_b64 exec, s[58:59]\n\t" \
  "ds_write_b128 v69, v[36:39] offset:3104\n\t" \
  "s_mov_b64 exec, -1\n\t" \
  "v_smfmac_f32_16x16x64_bf16 v[52:55], v[4:7], a[64:71], v68\n\t" \
  "v_add_f32 v75, v48, v50\n\t" \
  "v_add_f32 v49, v49, v51\n\t" \
  "v_cvt_pk_bf16_f32 v75, v75, v49\n\t" \
  "v_smfmac_f32_16x16x64_bf16 v[56:59], v[0:3], a[24:31], v68\n\t" \
  "ds_write_b32 v71, v75 offset:1536\n\t" \
  "v_smfmac_f32_16x16x64_bf16 v[56:59], v[4:7], a[72:79], v68\n\t" \
  "ds_read_b64_tr_b16 v[0:1], v72 offset:9216\n\t" \
  "ds_read_b64_tr_b16 v[2:3], v72 offset:9728\n\t" \
  "v_smfmac_f32_16x16x64_bf16 v[52:55], v[8:11], a[104:111], v68\n\t" \
  "ds_read_b128 v[36:39], v70 offset:12288\n\t" \
  "v_smfmac_f32_16x16x64_bf16 v[56:59], v[8:11], a[112:119], v68\n\t" \
  "buffer_load_dword off, s[28:31], s87 sc1 lds\n\t" \
  "ds_read_b128 v[40:43], v70 offset:13312\n\t" \
  "s_waitcnt lgkmcnt(4)\n\t" \
  "s_barrier\n\t" \
  "ds_read_b64_tr_b16 v[44:45], v72 offset:1024\n\t" \
  "ds_read_b64_tr_b16 v[46:47], v72 offset:1536\n\t" \
  "v_smfmac_f32_16x16x64_bf16 v[52:55], v[12:15], a[144:151], v68\n\t" \
  "ds_read_b64_tr_b16 v[48:49], v72 offset:0\n\t" \
  "ds_read_b64_tr_b16 v[50:51], v72 offset:512\n\t" \
  "v_smfmac_f32_16x16x64_bf16 v[56:59], v[12:15], a[152:159], v68\n\t" \
  "v_smfmac_f32_16x16x64_bf16 v[52:55], v[16:19], a[192:199], v68\n\t" \
  "ds_read_b128 v[4:7], v69 offset:2048\n\t" \
  "s_waitcnt lgkmcnt(5)\n\t" \
  "v_smfmac_f32_16x16x64_bf16 v[56:59], v[16:19], v[36:43], v68\n\t" \
  "ds_read_b128 v[8:11], v69 offset:3072\n\t" \
  "s_waitcnt lgkmcnt(4)\n\t" \
  "v_mfma_f32_16x16x32_bf16 v[12:15], v[44:47], v[76:79], 0\n\t" \
  "ds_read_b128 v[36:39], v70 offset:14336\n\t" \
  "s_waitcnt lgkmcnt(3)\n\t" \
  "v_mfma_f32_16x16x32_bf16 v[12:15], v[48:51], v[0:3], v[12:15]\n\t" \
  "ds_read_b128 v[40:43], v70 offset:15360\n\t" \
  "v_smfmac_f32_16x16x64_bf16 v[52:55], v[20:23], a[224:231], v68\n\t" \
  "s_waitcnt lgkmcnt(2)\n\t" \
  "v_add_f32 v4, v4, v8\n\t" \
  "v_add_f32 v5, v5, v9\n\t" \
  "v_add_f32 v6, v6, v10\n\t" \
  "v_cndmask_b32 v78, v13, v14, s[56:57]\n\t" \
  "v_cndmask_b32 v76, v12, v15, s[56:57]\n\t" \
  "v_add_f32 v7, v7, v11\n\t" \
  "v_max3_f32 v77, v4, v5, v6\n\t" \
  "v_add_f32_dpp v76, v78, v76 quad_perm:[2,3,0,1] row_mask:0xf bank_mask:0xf\n\t" \
  "v_max3_f32 v77, v77, v7, v76\n\t" \
  "v_smfmac_f32_16x16x64_bf16 v[56:59], v[20:23], a[232:239], v68\n\t" \
  "ds_read_b128 v[44:47], v70 offset:16384\n\t" \
  "v_max_f32_dpp v77, v77, v77 quad_perm:[2,3,0,1] row_mask:0xf bank_mask:0xf\n\t" \
  "v_mov_b32 v78, v77\n\t" \
  "v_smfmac_f32_16x16x64_bf16 v[52:55], v[24:27], v[96:103], v68\n\t" \
  "ds_read_b128 v[48:51], v70 offset:17408\n\t" \
  "v_permlane16_swap_b32 v77, v78\n\t" \
  "v_max_f32 v77, v77, v78\n\t" \
  "v_mov_b32 v78, v77\n\t" \
  "v_smfmac_f32_16x16x64_bf16 v[52:55], v[28:31], v[144:151], v68\n\t" \
  "ds_read_b128 v[16:19], v70 offset:18432\n\t" \
  "v_permlane32_swap_b32 v77, v78\n\t" \
  "v_max_f32 v77, v77, v78\n\t" \
  "v_smfmac_f32_16x16x64_bf16 v[52:55], v[32:35], v[184:191], v68\n\t" \
  "ds_read_b128 v[20:23], v70 offset:19456\n\t" \
  "v_mul_f32 v77, 0xbe0293ee, v77\n\t" \
  "v_smfmac_f32_16x16x64_bf16 v[56:59], v[24:27], v[104:111], v68\n\t" \
  "v_fmamk_f32 v4, v4, 0x3e0293ee, v77\n\t" \
  "v_smfmac_f32_16x16x64_bf16 v[56:59], v[28:31], v[152:159], v68\n\t" \
  "ds_read_b128 v[24:27], v70 offset:22528\n\t" \
  "s_waitcnt lgkmcnt(4)\n\t" \
  "v_fmamk_f32 v5, v5, 0x3e0293ee, v77\n\t" \
  "v_smfmac_f32_16x16x64_bf16 v[56:59], v[32:35], v[36:43], v68\n\t" \
  "ds_read_b128 v[28:31], v70 offset:23552\n\t" \
  "v_fmamk_f32 v6, v6, 0x3e0293ee, v77\n\t" \
  "v_fmamk_f32 v7, v7, 0x3e0293ee, v77\n\t" \
  "v_fmamk_f32 v76, v76, 0x3e0293ee, v77\n\t" \
  "v_exp_f32 v4, v4\n\t" \
  "v_exp_f32 v5, v5\n\t" \
  "v_exp_f32 v6, v6\n\t" \
  "v_exp_f32 v7, v7\n\t" \
  "v_cvt_pk_bf16_f32 v4, v4, v5\n\t" \
  "v_exp_f32 v76, v76\n\t" \
  "v_cvt_pk_bf16_f32 v5, v6, v7\n\t" \
  "v_mov_b32_dpp v12, v4 quad_perm:[0,1,0,1] row_mask:0xf bank_mask:0xf\n\t" \
  "v_mov_b32_dpp v14, v4 quad_perm:[2,3,2,3] row_mask:0xf bank_mask:0xf\n\t" \
  "v_mov_b32_dpp v13, v5 quad_perm:[0,1,0,1] row_mask:0xf bank_mask:0xf\n\t" \
  "v_mov_b32_dpp v15, v5 quad_perm:[2,3,2,3] row_mask:0xf bank_mask:0xf\n\t" \
  "v_add_f32 v52, v52, v54\n\t" \
  "v_add_f32 v53, v53, v55\n\t" \
  "s_waitcnt vmcnt(2)\n\t" \
  "v_mfma_f32_16x16x32_bf16 v[0:3], v[60:63], v[12:15], 0\n\t" \
  "ds_read_b128 v[36:39], v70 offset:20480\n\t" \
  "v_add_f32 v56, v56, v58\n\t" \
  "v_add_f32 v57, v57, v59\n\t" \
  "v_cvt_pk_bf16_f32 v52, v52, v53\n\t" \
  "v_cvt_pk_bf16_f32 v53, v56, v57\n\t" \
  "s_waitcnt vmcnt(1)\n\t" \
  "v_mfma_f32_16x16x32_bf16 v[4:7], v[64:67], v[12:15], 0\n\t" \
  "ds_read_b128 v[40:43], v70 offset:21504\n\t" \
  "v_mov_b64 v[56:57], s[88:89]\n\t" \
  "v_mov_b64 v[58:59], s[88:89]\n\t" \
  "v_mov_b32_dpp v78, v76 quad_perm:[0,1,0,1] row_mask:0xf bank_mask:0xf\n\t" \
  "v_mov_b32_dpp v79, v76 quad_perm:[2,3,2,3] row_mask:0xf bank_mask:0xf\n\t" \
  "v_mfma_f32_16x16x32_bf16 v[32:35], v[56:59], v[12:15], 0\n\t" \
  "ds_read_b128 v[60:63], v70 offset:24576\n\t" \
  "v_cvt_pk_bf16_f32 v11, v78, v79\n\t" \
  "v_cvt_pk_bf16_f32 v8, v78, v79\n\t" \
  "v_mov_b64 v[54:55], 0\n\t" \
  "v_mov_b32 v9, 0\n\t" \
  "v_mov_b32 v10, 0\n\t" \
  "v_mov_b64 v[14:15], 0\n\t" \
  "v_mov_b64 v[12:13], 0\n\t" \
  "v_mfma_f32_16x16x32_bf16 v[32:35], v[54:57], v[8:11], v[32:35]\n\t" \
  "s_cmp_eq_u32 s48, 63\n\t" \
  "v_mfma_f32_16x16x32_bf16 v[0:3], v[52:55], v[8:11], v[0:3]\n\t" \
  "buffer_store_dwordx2 v[52:53], v71, s[28:31], s69 offen offset:3580 sc1\n\t" \
  "ds_read_b128 v[64:67], v70 offset:25600\n\t" \
  "v_mov_b64 v[56:57], 0\n\t" \
  "v_mov_b64 v[58:59], 0\n\t" \
  "v_mfma_f32_16x16x32_bf16 v[4:7], v[52:55], v[10:13], v[4:7]\n\t" \
  "s_cbranch_scc1 LLAST3_%=\n\t" \
  "v_rcp_f32 v74, v32\n\t" \
  "v_mov_b32 v11, 0\n\t" \
  "v_mov_b32 v8, 0\n\t" \
  "v_pk_mul_f32 v[0:1], v[0:1], v[74:75] op_sel_hi:[1,0]\n\t" \
  "v_pk_mul_f32 v[2:3], v[2:3], v[74:75] op_sel_hi:[1,0]\n\t" \
  "v_cvt_pk_bf16_f32 v76, v0, v1\n\t" \
  "v_cvt_pk_bf16_f32 v77, v2, v3\n\t" \
  "v_pk_mul_f32 v[4:5], v[4:5], v[74:75] op_sel_hi:[1,0]\n\t" \
  "v_pk_mul_f32 v[6:7], v[6:7], v[74:75] op_sel_hi:[1,0]\n\t" \
  "v_cvt_pk_bf16_f32 v78, v4, v5\n\t" \
  "v_cvt_pk_bf16_f32 v79, v6, v7\n\t" \
  "ds_write_b128 v69, v[76:79] offset:7168\n\t" \
  "s_waitcnt lgkmcnt(2)\n\t" \
  "v_smfmac_f32_16x16x64_bf16 v[12:15], v[76:79], v[44:51], v68\n\t" \
  "ds_read_b128 v[44:47], v70 offset:26624\n\t" \
  "v_smfmac_f32_16x16x64_bf16 v[56:59], v[76:79], v[16:23], v68\n\t" \
  "ds_read_b128 v[48:51], v70 offset:27648\n\t" \
  "v_mov_b64 v[52:53], 0\n\t" \
  "v_smfmac_f32_16x16x64_bf16 v[8:11], v[76:79], v[36:43], v68\n\t" \
  "ds_read_b128 v[16:19], v70 offset:28672\n\t" \
  "v_smfmac_f32_16x16x64_bf16 v[52:55], v[76:79], v[24:31], v68\n\t" \
  "s_waitcnt lgkmcnt(3)\n\t" \
  "s_barrier\n\t" \
  "ds_read_b128 v[0:3], v69 offset:6144\n\t" \
  "ds_read_b128 v[4:7], v69 offset:5120\n\t" \
  "v_lshrrev_b32 v71, 7, v69\n\t" \
  "v_and_b32 v74, 0x70, v69\n\t" \
  "v_lshl_add_u32 v71, v71, 14, v74\n\t" \
  "v_bfe_u32 v72, v69, 2, 7\n\t" \
  "v_and_b32 v74, 0x200, v69\n\t" \
  "s_waitcnt lgkmcnt(1)\n\t" \
  "v_smfmac_f32_16x16x64_bf16 v[8:11], v[0:3], v[60:67], v68\n\t" \
  "ds_read_b128 v[20:23], v70 offset:29696\n\t" \
  "v_mov_b64 v[76:77], 0\n\t" \
  "v_smfmac_f32_16x16x64_bf16 v[12:15], v[0:3], v[200:207], v68\n\t" \
  "ds_read_b128 v[32:35], v69 offset:4096\n\t" \
  "v_mov_b64 v[78:79], 0\n\t" \
  "v_smfmac_f32_16x16x64_bf16 v[52:55], v[0:3], v[44:51], v68\n\t" \
  "buffer_load_dword v75, off, s[28:31], s80 sc1\n\t" \
  "ds_read_b128 v[24:27], v70 offset:30720\n\t" \
  "v_lshl_add_u32 v72, v74, 3, v72\n\t" \
  "v_smfmac_f32_16x16x64_bf16 v[56:59], v[0:3], v[232:239], v68\n\t" \
  "ds_read_b128 v[28:31], v70 offset:31744\n\t" \
  "s_waitcnt lgkmcnt(4)\n\t" \
  "v_smfmac_f32_16x16x64_bf16 v[12:15], v[4:7], v[208:215], v68\n\t" \
  "ds_read_b128 v[60:63], v70 offset:32768\n\t" \
  "s_waitcnt lgkmcnt(4)\n\t" \
  "v_smfmac_f32_16x16x64_bf16 v[8:11], v[4:7], v[16:23], v68\n\t" \
  "ds_read_b128 v[64:67], v70 offset:33792\n\t" \
  "s_waitcnt lgkmcnt(4)\n\t" \
  "v_smfmac_f32_16x16x64_bf16 v[8:11], v[32:35], v[192:199], v68\n\t" \
  "ds_read_b128 v[36:39], v73 offset:4096\n\t" \
  "v_smfmac_f32_16x16x64_bf16 v[12:15], v[32:35], v[216:223], v68\n\t" \
  "ds_read_b128 v[40:43], v73 offset:6144\n\t" \
  "s_waitcnt lgkmcnt(4)\n\t" \
  "v_smfmac_f32_16x16x64_bf16 v[52:55], v[4:7], v[24:31], v68\n\t" \
  "ds_read_b128 v[44:47], v70 offset:34816\n\t" \
  "v_cvt_pk_bf16_f32 v8, v8, v9\n\t" \
  "v_smfmac_f32_16x16x64_bf16 v[52:55], v[32:35], v[224:231], v68\n\t" \
  "ds_read_b128 v[48:51], v70 offset:35840\n\t" \
  "v_cvt_pk_bf16_f32 v9, v10, v11\n\t" \
  "v_smfmac_f32_16x16x64_bf16 v[56:59], v[4:7], v[240:247], v68\n\t" \
  "s_waitcnt lgkmcnt(4)\n\t" \
  "v_cvt_pk_bf16_f32 v10, v12, v13\n\t" \
  "v_smfmac_f32_16x16x64_bf16 v[56:59], v[32:35], v[60:67], v68\n\t" \
  "s_waitcnt lgkmcnt(3)\n\t" \
  "v_cvt_pk_bf16_f32 v11, v14, v15\n\t" \
  "v_smfmac_f32_16x16x64_bf16 v[76:79], v[36:39], v[248:255], v68\n\t" \
  "buffer_store_dwordx4 v[8:11], v71, s[28:31], s54 offen sc1\n\t" \
  "s_waitcnt lgkmcnt(0)\n\t" \
  "v_cvt_pk_bf16_f32 v52, v52, v53\n\t" \
  "v_smfmac_f32_16x16x64_bf16 v[76:79], v[40:43], v[44:51], v68\n\t" \
  "v_cvt_pk_bf16_f32 v53, v54, v55\n\t" \
  "v_cvt_pk_bf16_f32 v54, v56, v57\n\t" \
  "v_cvt_pk_bf16_f32 v55, v58, v59\n\t" \
  "buffer_store_dwordx4 v[52:55], v71, s[28:31], s75 offen sc1\n\t" \
  "s_nop 2\n\t" \
  "v_add_f32 v76, v76, v78\n\t" \
  "v_add_f32 v77, v77, v79\n\t" \
  "v_cvt_pk_bf16_f32 v76, v76, v77\n\t" \
  "buffer_store_dword v76, v72, s[28:31], s55 offen sc1\n\t" \
  "s_memrealtime s[70:71]\n\t" \
  "s_waitcnt vmcnt(3)\n\t" \
  "v_readfirstlane_b32 s65, v75\n\t" \
  "s_barrier\n\t" \
  "s_waitcnt lgkmcnt(0)\n\t" \
  "s_not_b32 s65, s65\n\t" \
  "s_lshl_b32 s61, s70, 8\n\t" \
  "s_andn2_b32 s64, s65, 0xff\n\t" \
  "s_sub_u32 s64, s64, s61\n\t" \
  "s_ashr_i32 s64, s64, 8\n\t" \
  "s_cmp_eq_u32 s65, 0\n\t" \
  "s_cselect_b32 s64, " CH_DLF ", s64\n\t" \
  "s_add_i32 s64, s64, " CH_RO3 "\n\t" \
  "s_min_i32 s64, s64, s34\n\t" \
  "s_max_i32 s64, s64, " CH_DLL1 "\n\t" \
  "s_add_u32 s64, s64, s70\n\t" \
  "s_mov_b32 s74, 0\n\t" \
  "v_and_b32 v37, 0x30, v69\n\t" \
  "v_add_u32 v37, s52, v37\n\t" \
  "s_mov_b32 exec_lo, 0\n\t" \
  "v_mov_b32 v37, s85\n\t" \
  "s_mov_b32 exec_lo, -1\n\t" \
  "s_cmp_lt_i32 s41, 0\n\t" \
  "s_cbranch_scc1 LIDLE_%=\n\t" \
  "s_movk_i32 s60, 8192\n\t" \
  "RWT3_%=:\n\t" \
  "s_memrealtime s[72:73]\n\t" \
  "s_sub_u32 s60, s60, 1\n\t" \
  "s_cselect_b32 s61, 0, -1\n\t" \
  "s_waitcnt lgkmcnt(0)\n\t" \
  "s_sub_i32 s73, s72, s64\n\t" \
  "s_and_b32 s73, s73, s61\n\t" \
  "s_cmp_lt_i32 s73, " CH_FIN "\n\t" \
  "s_cbranch_scc1 RWT3_%=\n\t" \
  "s_cmp_lt_i32 s73, 0\n\t" \
  "s_cbranch_scc1 RWT3_%=f\n\t" \
  "RWT3_%=g:\n\t" \
  "RRETRY3_%=:\n\t" \
  "buffer_load_dwordx4 v[0:3], v69, s[28:31], s97 offen offset:0 sc1\n\t" \
  "buffer_load_dwordx4 v[4:7], v69, s[28:31], s97 offen offset:1024 sc1\n\t" \
  "buffer_load_dwordx4 v[8:11], v69, s[28:31], s97 offen offset:2048 sc1\n\t" \
  "buffer_load_dwordx4 v[12:15], v69, s[28:31], s97 offen offset:3072 sc1\n\t" \
  "v_lshrrev_b32 v20, 2, v69\n\t" \
  "v_and_b32 v20, 16, v20\n\t" \
  "v_mov_b32 v21, 0x3f80\n\t" \
  "v_lshlrev_b32 v21, v20, v21\n\t" \
  "v_bfe_u32 v20, v69, 4, 2\n\t" \
  "v_cmp_eq_u32 vcc, 0, v20\n\t" \
  "s_nop 1\n\t" \
  "v_cndmask_b32 v32, 0, v21, vcc\n\t" \
  "v_cmp_eq_u32 vcc, 1, v20\n\t" \
  "s_nop 1\n\t" \
  "v_cndmask_b32 v33, 0, v21, vcc\n\t" \
  "v_cmp_eq_u32 vcc, 2, v20\n\t" \
  "s_nop 1\n\t" \
  "v_cndmask_b32 v34, 0, v21, vcc\n\t" \
  "v_cmp_eq_u32 vcc, 3, v20\n\t" \
  "s_nop 1\n\t" \
  "v_cndmask_b32 v35, 0, v21, vcc\n\t" \
  "v_bfe_u32 v38, v69, 7, 1\n\t" \
  "v_bfe_u32 v20, v69, 9, 1\n\t" \
  "v_lshl_add_u32 v38, v20, 2, v38\n\t" \
  "v_lshlrev_b32 v38, 12, v38\n\t" \
  "v_and_b32 v20, 0x60, v69\n\t" \
  "v_add_u32 v38, v38, v20\n\t" \
  "v_bfe_u32 v20, v69, 8, 1\n\t" \
  "v_lshl_add_u32 v38, v20, 4, v38\n\t" \
  "v_bfe_u32 v20, v69, 4, 1\n\t" \
  "v_lshl_add_u32 v38, v20, 3, v38\n\t" \
  "v_mov_b32 v44, 0\n\t" \
  "buffer_load_dword v39, v37, s[28:31], 0 offen sc1\n\t" \
  CH_ALD \
  "s_waitcnt vmcnt(4)\n\t" \
  "v_mfma_f32_16x16x32_bf16 v[40:43], v[32:35], v[0:3], 0\n\t" \
  "v_max3_u32 v44, v44, v0, v1\n\t" \
  "v_max3_u32 v44, v44, v2, v3\n\t" \
  "s_waitcnt vmcnt(3)\n\t" \
  "v_mfma_f32_16x16x32_bf16 v[40:43], v[32:35], v[4:7], v[40:43]\n\t" \
  "v_max3_u32 v44, v44, v4, v5\n\t" \
  "v_max3_u32 v44, v44, v6, v7\n\t" \
  "s_waitcnt vmcnt(2)\n\t" \
  "v_mfma_f32_16x16x32_bf16 v[40:43], v[32:35], v[8:11], v[40:43]\n\t" \
  "v_max3_u32 v44, v44, v8, v9\n\t" \
  "v_max3_u32 v44, v44, v10, v11\n\t" \
  "s_waitcnt vmcnt(1)\n\t" \
  "v_mfma_f32_16x16x32_bf16 v[40:43], v[32:35], v[12:15], v[40:43]\n\t" \
  "v_max3_u32 v44, v44, v12, v13\n\t" \
  "v_max3_u32 v44, v44, v14, v15\n\t" \
  "v_cmp_eq_u32 vcc, -1, v44\n\t" \
  "s_cbranch_vccnz RFAIL3_%=\n\t" \
  "s_nop 3\n\t" \
  "v_add_f32_dpp v40, v40, v40 row_ror:8 row_mask:0xf bank_mask:0xf\n\t" \
  "v_add_f32_dpp v41, v41, v41 row_ror:8 row_mask:0xf bank_mask:0xf\n\t" \
  "v_add_f32_dpp v42, v42, v42 row_ror:8 row_mask:0xf bank_mask:0xf\n\t" \
  "v_add_f32_dpp v43, v43, v43 row_ror:8 row_mask:0xf bank_mask:0xf\n\t" \
  "v_cvt_pk_bf16_f32 v40, v40, v41\n\t" \
  "v_cvt_pk_bf16_f32 v41, v42, v43\n\t" \
  "buffer_store_dwordx2 v[40:41], v38, s[28:31], s98 offen sc1\n\t" \
  "RDONE3_%=:\n\t" \
  "s_barrier\n\t" \
  "s_waitcnt vmcnt(1)\n\t" \
  "v_cmp_eq_u32 vcc, 0, v39\n\t" \
  "s_sub_u32 s60, s70, s66\n\t" \
  "s_add_u32 s81, s81, 1\n\t" \
  "s_min_u32 s81, s81, s60\n\t" \
  "s_add_u32 s61, s81, 8\n\t" \
  "s_min_u32 s60, s60, s61\n\t" \
  "s_lshl_b32 s60, s60, 2\n\t" \
  "s_lshr_b32 s61, s20, 3\n\t" \
  "s_sub_u32 s20, s20, s61\n\t" \
  "s_add_u32 s20, s20, s60\n\t" \
  "s_cmp_eq_u32 s68, 0\n\t" \
  "s_cselect_b32 s60, 0, " CH_FU "\n\t" \
  "s_add_i32 s22, s22, s60\n\t" \
  "s_sub_i32 s22, s22, " CH_FD "\n\t" \
  "s_max_i32 s22, s22, 0\n\t" \
  "s_min_i32 s22, s22, 12288\n\t" \
  "s_add_u32 s48, s48, 1\n\t" \
  "s_mov_b32 s49, s53\n\t" \
  "s_add_u32 s53, s53, " CH_SLOT "\n\t" \
  "s_add_u32 s60, s53, " CH_SKIP "\n\t" \
  "s_lshr_b32 s61, s96, 1\n\t" \
  "s_add_u32 s86, s86, 1\n\t" \
  "s_cmp_eq_u32 s86, 3\n\t" \
  "s_cselect_b32 s53, s60, s53\n\t" \
  "s_cselect_b32 s96, s61, s96\n\t" \
  "s_cselect_b32 s86, 0, s86\n\t" \
  "s_cmp_ge_u32 s53, " CH_RING "\n\t" \
  "s_cselect_b32 s53, 0, s53\n\t" \
  "s_cselect_b32 s96, s21, s96\n\t" \
  "s_andn2_b32 s53, s53, 0x2000\n\t" \
  "s_and_b32 s60, s96, 1\n\t" \
  "s_lshl_b32 s60, s60, 13\n\t" \
  "s_or_b32 s53, s53, s60\n\t" \
  "s_mov_b32 s68, 0\n\t" \
  "s_add_u32 s97, s41, s53\n\t" \
  "s_add_u32 s98, s37, s53\n\t" \
  "s_add_u32 s82, s48, 0\n\t" \
  "s_and_b32 s82, s82, " CH_TRM "\n\t" \
  "s_lshr_b64 s[60:61], s[90:91], s82\n\t" \
  "s_and_b32 s60, s60, 1\n\t" \
  "s_lshl_b32 s60, s60, 13\n\t" \
  "s_add_u32 s82, s82, " CH_SG0 "\n\t" \
  "s_lshl_b32 s82, s82, 21\n\t" \
  "s_or_b32 s82, s82, s60\n\t" \
  "s_add_u32 s50, s82, s38\n\t" \
  "s_add_u32 s69, s82, s39\n\t" \
  "s_add_u32 s82, s48, 1\n\t" \
  "s_lshr_b64 s[60:61], s[90:91], s82\n\t" \
  "s_and_b32 s60, s60, 1\n\t" \
  "s_lshl_b32 s60, s60, 13\n\t" \
  "s_add_u32 s82, s82, " CH_SG0 "\n\t" \
  "s_lshl_b32 s82, s82, 21\n\t" \
  "s_or_b32 s82, s82, s60\n\t" \
  "s_add_u32 s52, s82, s43\n\t" \
  "s_add_u32 s82, s48, " CH_ARMD "\n\t" \
  "s_lshr_b64 s[60:61], s[90:91], s82\n\t" \
  "s_and_b32 s60, s60, 1\n\t" \
  "s_lshl_b32 s60, s60, 13\n\t" \
  "s_add_u32 s82, s82, " CH_SG0 "\n\t" \
  "s_lshl_b32 s82, s82, 21\n\t" \
  "s_or_b32 s82, s82, s60\n\t" \
  "s_add_u32 s85, s82, s45\n\t" \
  "s_cmp_lt_i32 s48, " CH_ARMS "\n\t" \
  "s_cselect_b32 s85, s27, s85\n\t" \
  "s_cmp_eq_u32 s34, 0\n\t" \
  "s_cbranch_scc1 LINI_%=\n\t" \
  "LTK_%=:\n\t" \
  CH_TOKCHK \
  "s_mov_b32 s61, 0\n\t" \
  "LTW_%=:\n\t" \
  "buffer_load_dword v1, v37, s[28:31], 0 offen sc1\n\t" \
  "s_waitcnt vmcnt(0)\n\t" \
  "v_cmp_eq_u32 vcc, 0, v1\n\t" \
  "s_cbranch_vccz LG3_%=\n\t" \
  "s_add_u32 s61, s61, 1\n\t" \
  "s_sleep 2\n\t" \
  "s_cmp_gt_u32 s61, s84\n\t" \
  "s_cbranch_scc1 LTG_%=\n\t" \
  "s_and_b32 s60, s61, 1023\n\t" \
  "s_cmp_lg_u32 s60, 0\n\t" \
  "s_cbranch_scc1 LTW_%=\n\t" \
  "buffer_load_dword v46, off, s[28:31], s92 sc1\n\t" \
  "s_waitcnt vmcnt(0)\n\t" \
  "v_readfirstlane_b32 s60, v46\n\t" \
  "s_nop 0\n\t" \
  "s_cmp_eq_u32 s60, 0\n\t" \
  "s_cbranch_scc1 LTW_%=\n\t" \
  "LTG_%=:\n\t" \
  "s_mov_b32 s84, 0\n\t" \
  "s_lshl_b32 s60, s36, 4\n\t" \
  "s_or_b32 s60, s60, s48\n\t" \
  "s_or_b32 s60, s60, 268435456\n\t" \
  "v_mov_b32 v76, s60\n\t" \
  "v_mov_b32 v77, 0\n\t" \
  "s_mov_b64 exec, 1\n\t" \
  "buffer_atomic_cmpswap v[76:77], off, s[28:31], s92 sc1\n\t" \
  "s_mov_b64 exec, -1\n\t" \
  "s_branch LG3_%=\n\t" \
  "LIDLE_%=:\n\t" \
  "s_add_u32 s64, s64, 8\n\t" \
  "s_movk_i32 s60, 8192\n\t" \
  "IWT_%=:\n\t" \
  "s_memrealtime s[72:73]\n\t" \
  "s_sub_u32 s60, s60, 1\n\t" \
  "s_cselect_b32 s61, 0, -1\n\t" \
  "s_waitcnt lgkmcnt(0)\n\t" \
  "s_sub_i32 s73, s72, s64\n\t" \
  "s_and_b32 s73, s73, s61\n\t" \
  "s_cmp_lt_i32 s73, 0\n\t" \
  "s_cbranch_scc1 IWT_%=\n\t" \
  "buffer_load_dword v39, v37, s[28:31], 0 offen sc1\n\t" \
  "buffer_store_dword v37, off, s[28:31], s41\n\t" \
  "s_branch RDONE3_%=\n\t" \
  "RFAIL3_%=:\n\t" \
  "s_add_u32 s74, s74, 1\n\t" \
  "s_sleep " CH_RSL "\n\t" \
  "s_waitcnt vmcnt(0)\n\t" \
  "s_cmp_gt_u32 s74, s84\n\t" \
  "s_cbranch_scc1 RG3_%=\n\t" \
  "s_and_b32 s60, s74, 1023\n\t" \
  "s_cmp_lg_u32 s60, 0\n\t" \
  "s_cbranch_scc1 RRETRY3_%=\n\t" \
  "buffer_load_dword v46, off, s[28:31], s92 sc1\n\t" \
  "s_waitcnt vmcnt(0)\n\t" \
  "v_readfirstlane_b32 s60, v46\n\t" \
  "s_nop 0\n\t" \
  "s_cmp_eq_u32 s60, 0\n\t" \
  "s_cbranch_scc1 RRETRY3_%=\n\t" \
  "RG3_%=:\n\t" \
  "s_mov_b32 s84, 0\n\t" \
  "s_lshl_b32 s60, s36, 4\n\t" \
  "s_or_b32 s60, s60, s48\n\t" \
  "s_or_b32 s60, s60, 536870912\n\t" \
  "v_mov_b32 v76, s60\n\t" \
  "v_mov_b32 v77, 0\n\t" \
  "s_mov_b64 exec, 1\n\t" \
  "buffer_atomic_cmpswap v[76:77], off, s[28:31], s92 sc1\n\t" \
  "s_mov_b64 exec, -1\n\t" \
  "s_branch RDONE3_%=\n\t" \
  "LFAIL3_%=:\n\t" \
  "v_max_u32 v75, v0, v1\n\t" \
  "v_max3_u32 v75, v75, v2, v3\n\t" \
  "v_max3_u32 v75, v75, v4, v5\n\t" \
  "v_max3_u32 v75, v75, v6, v7\n\t" \
  "v_max3_u32 v75, v75, v8, v9\n\t" \
  "v_max3_u32 v75, v75, v10, v11\n\t" \
  "v_max3_u32 v75, v75, v12, v13\n\t" \
  "v_max3_u32 v75, v75, v14, v15\n\t" \
  "v_max3_u32 v75, v75, v16, v17\n\t" \
  "v_max3_u32 v75, v75, v18, v19\n\t" \
  "v_max3_u32 v75, v75, v20, v21\n\t" \
  "v_max3_u32 v75, v75, v22, v23\n\t" \
  "v_max3_u32 v75, v75, v24, v25\n\t" \
  "v_max3_u32 v75, v75, v26, v27\n\t" \
  "v_max3_u32 v75, v75, v28, v29\n\t" \
  "v_max3_u32 v75, v75, v30, v31\n\t" \
  "v_max3_u32 v75, v75, v32, v33\n\t" \
  "v_max3_u32 v75, v75, v34, v35\n\t" \
  "v_cmp_eq_u32 vcc, -1, v75\n\t" \
  "s_cbranch_vccz LGO3_%=\n\t" \
  "s_add_u32 s68, s68, 1\n\t" \
  "s_waitcnt lgkmcnt(0)\n\t" \
  "s_cmp_gt_u32 s68, s83\n\t" \
  "s_cbranch_scc1 LGV3_%=\n\t" \
  "s_barrier\n\t" \
  "s_waitcnt vmcnt(8)\n\t" \
  "s_branch LG3_%=\n\t" \
  "RWT3_%=f:\n\t" \
  "s_cmp_lt_i32 s73, -2\n\t" \
  "s_cbranch_scc0 RWT3_%=g\n\t" \
  "s_nop 4\n\t" \
  "s_branch RWT3_%=g\n\t" \
  "LGV3_%=:\n\t" \
  "s_mov_b32 s83, 0\n\t" \
  "s_lshl_b32 s60, s36, 4\n\t" \
  "s_or_b32 s60, s60, s48\n\t" \
  "s_or_b32 s60, s60, 805306368\n\t" \
  "v_mov_b32 v76, s60\n\t" \
  "v_mov_b32 v77, 0\n\t" \
  "s_mov_b64 exec, 1\n\t" \
  "buffer_atomic_cmpswap v[76:77], off, s[28:31], s92 sc1\n\t" \
  "s_mov_b64 exec, -1\n\t" \
  "s_branch LGO3_%=\n\t" \
  "LLAST3_%=:\n\t" \
  "s_waitcnt vmcnt(0) lgkmcnt(0)\n\t" \
  "s_barrier\n\t" \
  "LEND_%=:\n\t" \
  ""
typedef unsigned v16u __attribute__((ext_vector_type(16)));
#define CH_WCLOB "a0", "a1", "a2", "a3", "a4", "a5", "a6", "a7", "a8", "a9", "a10", "a11", "a12", "a13", "a14", "a15", "a16", "a17", "a18", "a19", "a20", "a21", "a22", "a23", "a24", "a25", "a26", "a27", "a28", "a29", "a30", "a31", "a32", "a33", "a34", "a35", "a36", "a37", "a38", "a39", "a40", "a41", "a42", "a43", "a44", "a45", "a46", "a47", "a48", "a49", "a50", "a51", "a52", "a53", "a54", "a55", "a56", "a57", "a58", "a59", "a60", "a61", "a62", "a63", "a64", "a65", "a66", "a67", "a68", "a69", "a70", "a71", "a72", "a73", "a74", "a75", "a76", "a77", "a78", "a79", "a80", "a81", "a82", "a83", "a84", "a85", "a86", "a87", "a88", "a89", "a90", "a91", "a92", "a93", "a94", "a95", "a96", "a97", "a98", "a99", "a100", "a101", "a102", "a103", "a104", "a105", "a106", "a107", "a108", "a109", "a110", "a111", "a112", "a113", "a114", "a115", "a116", "a117", "a118", "a119", "a120", "a121", "a122", "a123", "a124", "a125", "a126", "a127", "a128", "a129", "a130", "a131", "a132", "a133", "a134", "a135", "a136", "a137", "a138", "a139", "a140", "a141", "a142", "a143", "a144", "a145", "a146", "a147", "a148", "a149", "a150", "a151", "a152", "a153", "a154", "a155", "a156", "a157", "a158", "a159", "a160", "a161", "a162", "a163", "a164", "a165", "a166", "a167", "a168", "a169", "a170", "a171", "a172", "a173", "a174", "a175", "a176", "a177", "a178", "a179", "a180", "a181", "a182", "a183", "a184", "a185", "a186", "a187", "a188", "a189", "a190", "a191", "a192", "a193", "a194", "a195", "a196", "a197", "a198", "a199", "a200", "a201", "a202", "a203", "a204", "a205", "a206", "a207", "a208", "a209", "a210", "a211", "a212", "a213", "a214", "a215", "a216", "a217", "a218", "a219", "a220", "a221", "a222", "a223", "a224", "a225", "a226", "a227", "a228", "a229", "a230", "a231", "a232", "a233", "a234", "a235", "a236", "a237", "a238", "a239", "a240", "a241", "a242", "a243", "a244", "a245", "a246", "a247", "a248", "a249", "a250", "a251", "a252", "a253", "a254", "a255", "v80", "v81", "v82", "v83", "v84", "v85", "v86", "v87", "v88", "v89", "v90", "v91", "v92", "v93", "v94", "v95", "v96", "v97", "v98", "v99", "v100", "v101", "v102", "v103", "v104", "v105", "v106", "v107", "v108", "v109", "v110", "v111", "v112", "v113", "v114", "v115", "v116", "v117", "v118", "v119", "v120", "v121", "v122", "v123", "v124", "v125", "v126", "v127", "v128", "v129", "v130", "v131", "v132", "v133", "v134", "v135", "v136", "v137", "v138", "v139", "v140", "v141", "v142", "v143", "v144", "v145", "v146", "v147", "v148", "v149", "v150", "v151", "v152", "v153", "v154", "v155", "v156", "v157", "v158", "v159", "v160", "v161", "v162", "v163", "v164", "v165", "v166", "v167", "v168", "v169", "v170", "v171", "v172", "v173", "v174", "v175", "v176", "v177", "v178", "v179", "v180", "v181", "v182", "v183", "v184", "v185", "v186", "v187", "v188", "v189", "v190", "v191", "v192", "v193", "v194", "v195", "v196", "v197", "v198", "v199", "v200", "v201", "v202", "v203", "v204", "v205", "v206", "v207", "v208", "v209", "v210", "v211", "v212", "v213", "v214", "v215", "v216", "v217", "v218", "v219", "v220", "v221", "v222", "v223", "v224", "v225", "v226", "v227", "v228", "v229", "v230", "v231", "v232", "v233", "v234", "v235", "v236", "v237", "v238", "v239", "v240", "v241", "v242", "v243", "v244", "v245", "v246", "v247", "v248", "v249", "v250", "v251", "v252", "v253", "v254", "v255"
#define CH_H(b) (8 * ((b) & 3) + ((b) >> 3))
#define CH_L(b) (((b) >> 2) & 1)
__device__ __forceinline__ void start_work(char* smem, int wk);
__device__ __forceinline__ void chain_wg(char* smem) {
  if (CH_L((int)blockIdx.x)) start_work(smem, CH_H((int)blockIdx.x));
  const MP p = getp();
  const int b = (int)blockIdx.x, h = CH_H(b), L = CH_L(b);
  int w = __builtin_amdgcn_readfirstlane(threadIdx.x >> 6);
  {
    unsigned id; asm volatile("s_getreg_b32 %0, hwreg(HW_REG_HW_ID, 4, 2)" : "=s"(id));
    volatile __attribute__((address_space(3))) unsigned* lw = (volatile __attribute__((address_space(3))) unsigned*)smem;
    lw[w] = 1u << id;
    BAR();
    if (__builtin_amdgcn_readfirstlane(lw[0] | lw[1] | lw[2] | lw[3]) == 15u) w = (int)((SMAP >> (4u * id)) & 3u);
  }
  unsigned lp16 = (threadIdx.x & 63u) * 16u;
  rsrc_t X = mkr(p.xb, XB_BYTES);
  if ((unsigned)(size_t)(__attribute__((address_space(3))) char*)smem != 0u) return;
  const unsigned wsz = DM * HD * 2u - (unsigned)(h * DH) * 2u;
  const rsrc_t RQ = mkr(p.Wq + h * DH, wsz), RK = mkr(p.Wk + h * DH, wsz), RV = mkr(p.Wv + h * DH, wsz), RO = mkr(p.Wo + (long)h * DH * DM, DH * DM * 2u);
  unsigned xcc; asm volatile("s_getreg_b32 %0, hwreg(HW_REG_XCC_ID)" : "=s"(xcc)); xcc &= 3u;
  unsigned d96 = 0u;
  const unsigned c43 = P_FA * PAIR + (unsigned)h * 128u + 32u;
  const unsigned h128 = (unsigned)h * 128u, uw = (unsigned)w, t = uw >> 1, T = uw & 1u, NONE = 0xF0000000u;
  const unsigned u0 = w == 3 ? (unsigned)h >> 2 : (unsigned)((h & 1) + (h >> 3) * 2 + ((h >> 1) & 3) * 8 + (w == 2 ? 32 : 0));
  const bool idle = w == 3 && (h & 3);
  const unsigned c37 = idle ? NONE : (w == 3 ? AGT + u0 * 128u : AGO + (u0 >> 5) * 32768u + (u0 & 31u) * 128u);
  const unsigned c41 = idle ? NONE : (w == 3 ? 524288u + (u0 >> 1) * 16384u + (u0 & 1u) * 4096u : (u0 & 7u) * 16384u + ((u0 >> 3) & 1u) * 4096u + (u0 >> 4) * 131072u);
  const unsigned c42 = AGO + (xcc & 1u) * 16384u + (xcc >> 1) * 4096u;
  const unsigned c46 = (w == 3 ? (h & 7) == 5 : h == 30) ? AGT + 1024u : NONE;
  const unsigned c38 = (unsigned)(3 * h + 1) * PAIR + T * 4096u + t * 2048u, c39 = (P_KV + (unsigned)(h >> 1)) * PAIR + (unsigned)(h & 1) * 4096u;
  const unsigned c45 = P_FB * PAIR + 4096u + (unsigned)(16 + (h & 15)) * 128u;
  unsigned d48 = 0u, d49 = 0u, d53 = SLOT, d86 = 1u, d65 = rtc();
  unsigned mn = 120u;
  v4u stt;
  asm volatile("s_waitcnt vmcnt(0) lgkmcnt(0)\n\t" CH_ASM "LCHEND:"
               : "+{s48}"(d48), "+{s49}"(d49), "+{s53}"(d53), "+{s65}"(d65), "+{s81}"(mn), "+{s86}"(d86), "+{s96}"(d96), "=&{s[76:79]}"(stt), "+{v69}"(lp16), "+{s[28:31]}"(X)
               : [Wq] "s"(RQ), [Wk] "s"(RK), [Wv] "s"(RV), [Wo] "s"(RO), "{s33}"(HT_OFF + 8192u + 256u + (unsigned)b * 4u), "{s36}"(h128), "{s37}"(c37), "{s40}"(w), "{s41}"(c41), "{s42}"(c42), "{s46}"(c46), "{s92}"(TMO_OFF), "{s38}"(c38), "{s39}"(c39), "{s43}"(c43), "{s45}"(c45), "{s32}"(L), "{s26}"(AGT + 1024u + xcc * 128u), "{s27}"(RDY_OFF(0) + (unsigned)((h & 7) * 1024 + (b & 7) * 128)), [ctab] "i"(CTAB_OFF), [ring] "i"(SRING / 3 * XBLK), [trm] "i"(TRING - 1), [ex] "i"(ED * 256 + EB), [dl1l] "i"(DL1 - LATE), [armd] "i"(3 - ARMK), [arms] "i"(ARMK - 3),
                 [cap] "i"(SPIN_CAP)
               : "v0", "v1", "v2", "v3", "v4", "v5", "v6", "v7", "v8", "v9", "v10", "v11", "v12", "v13", "v14", "v15", "v16", "v17", "v18", "v19",
                 "v20", "v21", "v22", "v23", "v24", "v25", "v26", "v27", "v28", "v29", "v30", "v31", "v32", "v33", "v34", "v35", "v36", "v37", "v38", "v39",
                 "v40", "v41", "v42", "v43", "v44", "v45", "v46", "v47", "v48", "v49", "v50", "v51", "v52", "v53", "v54", "v55", "v56", "v57", "v58", "v59",
                 "v60", "v61", "v62", "v63", "v64", "v65", "v66", "v67", "v68", "v70", "v71", "v72", "v73", "v74", "v75", "v76", "v77", "v78", "v79",
                 CH_WCLOB, "s20", "s21", "s22", "s34", "s35", "s24", "s25", "s90", "s91", "s47", "s87", "s50", "s51", "s52", "s69", "s82", "s85", "s54", "s75", "s80", "s55", "s56", "s57", "s58", "s59", "s60", "s61", "s62", "s63", "s64", "s66", "s67", "s68", "s70", "s71", "s72", "s73", "s74", "s88", "s89",
                 "s83", "s84", "s97", "s98", "s99", "s93", "s94", "s95", "m0", "vcc", "scc", "memory");
}
#define QLEAD 4
#define RONE 2
#define OAUX 0
#define PFM 1
#define PSP 4
#define QLEAD1 2
#define QXW -18
#define QXC QXW
#define TK_QXW "%[qxw]"
#define TK_QXC "%[qxc]"
#define PSG 16
#define TK_PSG CH_S(PSG)
#define AX 30
#define AX1 45
#define AM 22
#define TK_AM CH_S(AM)
#define AE 61
#define TK_AE CH_S(AE)
#define PSK 2
#define PSH 2
#define TK_CAP "%[cap]"
#define TK_CTAB "%[ctab]"
#define TK_RM "%[rm]"
#define TK_TRM "%[trm]"
#define TK_TRN "%[trn]"
#define TK_SG0 CH_S(SG0)
#define TK_PSK CH_S(PSK)
#define TK_PSH CH_S(PSH)
#define AP_ASM \
  "v_lshrrev_b32 v40, 8, v0\n\t" \
  "v_mul_u32_u24 v40, 4608, v40\n\t" \
  "v_and_b32 v41, 0xf0, v0\n\t" \
  "v_lshrrev_b32 v42, 3, v41\n\t" \
  "v_add3_u32 v41, v41, v40, s64\n\t" \
  "v_add3_u32 v42, v42, v40, s65\n\t" \
  "s_mov_b32 s62, 0x05040100\n\t" \
  "s_mov_b32 s63, 0x07060302\n\t" \
  "s_xor_b32 s60, s66, 0\n\t" \
  "s_add_u32 s60, s60, 0\n\t" \
  "s_add_u32 s61, s60, 18432\n\t" \
  "buffer_load_dwordx4 v[48:51], v41, %[W], s60 offen offset:0\n\t" \
  "buffer_load_dwordx4 v[52:55], v41, %[W], s60 offen offset:1152\n\t" \
  "buffer_load_dwordx4 v[56:59], v41, %[W], s60 offen offset:2304\n\t" \
  "buffer_load_dwordx4 v[60:63], v41, %[W], s60 offen offset:3456\n\t" \
  "buffer_load_dwordx4 v[64:67], v41, %[W], s61 offen offset:0\n\t" \
  "buffer_load_dwordx4 v[68:71], v41, %[W], s61 offen offset:1152\n\t" \
  "buffer_load_dwordx4 v[72:75], v41, %[W], s61 offen offset:2304\n\t" \
  "buffer_load_dwordx4 v[76:79], v41, %[W], s61 offen offset:3456\n\t" \
  "s_xor_b32 s60, s66, 0\n\t" \
  "s_add_u32 s60, s60, 36864\n\t" \
  "s_add_u32 s61, s60, 18432\n\t" \
  "buffer_load_dwordx4 v[80:83], v41, %[W], s60 offen offset:0\n\t" \
  "buffer_load_dwordx4 v[84:87], v41, %[W], s60 offen offset:1152\n\t" \
  "buffer_load_dwordx4 v[88:91], v41, %[W], s60 offen offset:2304\n\t" \
  "buffer_load_dwordx4 v[92:95], v41, %[W], s60 offen offset:3456\n\t" \
  "buffer_load_dwordx4 v[96:99], v41, %[W], s61 offen offset:0\n\t" \
  "buffer_load_dwordx4 v[100:103], v41, %[W], s61 offen offset:1152\n\t" \
  "buffer_load_dwordx4 v[104:107], v41, %[W], s61 offen offset:2304\n\t" \
  "buffer_load_dwordx4 v[108:111], v41, %[W], s61 offen offset:3456\n\t" \
  "s_xor_b32 s60, s66, 0\n\t" \
  "s_add_u32 s60, s60, 73728\n\t" \
  "s_add_u32 s61, s60, 18432\n\t" \
  "buffer_load_dwordx4 v[112:115], v41, %[W], s60 offen offset:0\n\t" \
  "buffer_load_dwordx4 v[116:119], v41, %[W], s60 offen offset:1152\n\t" \
  "buffer_load_dwordx4 v[120:123], v41, %[W], s60 offen offset:2304\n\t" \
  "buffer_load_dwordx4 v[124:127], v41, %[W], s60 offen offset:3456\n\t" \
  "buffer_load_dwordx4 v[128:131], v41, %[W], s61 offen offset:0\n\t" \
  "buffer_load_dwordx4 v[132:135], v41, %[W], s61 offen offset:1152\n\t" \
  "buffer_load_dwordx4 v[136:139], v41, %[W], s61 offen offset:2304\n\t" \
  "buffer_load_dwordx4 v[140:143], v41, %[W], s61 offen offset:3456\n\t" \
  "s_xor_b32 s60, s66, 0\n\t" \
  "s_add_u32 s60, s60, 110592\n\t" \
  "s_add_u32 s61, s60, 18432\n\t" \
  "buffer_load_dwordx4 v[144:147], v41, %[W], s60 offen offset:0\n\t" \
  "buffer_load_dwordx4 v[148:151], v41, %[W], s60 offen offset:1152\n\t" \
  "buffer_load_dwordx4 v[152:155], v41, %[W], s60 offen offset:2304\n\t" \
  "buffer_load_dwordx4 v[156:159], v41, %[W], s60 offen offset:3456\n\t" \
  "buffer_load_dwordx4 v[160:163], v41, %[W], s61 offen offset:0\n\t" \
  "buffer_load_dwordx4 v[164:167], v41, %[W], s61 offen offset:1152\n\t" \
  "buffer_load_dwordx4 v[168:171], v41, %[W], s61 offen offset:2304\n\t" \
  "buffer_load_dwordx4 v[172:175], v41, %[W], s61 offen offset:3456\n\t" \
  "s_xor_b32 s60, s66, 147456\n\t" \
  "s_add_u32 s60, s60, 0\n\t" \
  "s_add_u32 s61, s60, 18432\n\t" \
  "buffer_load_dwordx4 v[176:179], v41, %[W], s60 offen offset:0\n\t" \
  "buffer_load_dwordx4 v[180:183], v41, %[W], s60 offen offset:1152\n\t" \
  "buffer_load_dwordx4 v[184:187], v41, %[W], s60 offen offset:2304\n\t" \
  "buffer_load_dwordx4 v[188:191], v41, %[W], s60 offen offset:3456\n\t" \
  "buffer_load_dwordx4 v[192:195], v41, %[W], s61 offen offset:0\n\t" \
  "buffer_load_dwordx4 v[196:199], v41, %[W], s61 offen offset:1152\n\t" \
  "buffer_load_dwordx4 v[200:203], v41, %[W], s61 offen offset:2304\n\t" \
  "buffer_load_dwordx4 v[204:207], v41, %[W], s61 offen offset:3456\n\t" \
  "s_waitcnt vmcnt(38)\n\t" \
  "v_perm_b32 v44, v52, v48, s62\n\t" \
  "s_waitcnt vmcnt(36)\n\t" \
  "v_perm_b32 v45, v60, v56, s62\n\t" \
  "s_waitcnt vmcnt(34)\n\t" \
  "v_perm_b32 v46, v68, v64, s62\n\t" \
  "s_waitcnt vmcnt(32)\n\t" \
  "v_perm_b32 v47, v76, v72, s62\n\t" \
  "v_accvgpr_write_b32 a0, v44\n\t" \
  "v_accvgpr_write_b32 a1, v45\n\t" \
  "v_accvgpr_write_b32 a2, v46\n\t" \
  "v_accvgpr_write_b32 a3, v47\n\t" \
  "v_perm_b32 v44, v52, v48, s63\n\t" \
  "v_perm_b32 v45, v60, v56, s63\n\t" \
  "v_perm_b32 v46, v68, v64, s63\n\t" \
  "v_perm_b32 v47, v76, v72, s63\n\t" \
  "v_accvgpr_write_b32 a4, v44\n\t" \
  "v_accvgpr_write_b32 a5, v45\n\t" \
  "v_accvgpr_write_b32 a6, v46\n\t" \
  "v_accvgpr_write_b32 a7, v47\n\t" \
  "v_perm_b32 v44, v53, v49, s62\n\t" \
  "v_perm_b32 v45, v61, v57, s62\n\t" \
  "v_perm_b32 v46, v69, v65, s62\n\t" \
  "v_perm_b32 v47, v77, v73, s62\n\t" \
  "v_accvgpr_write_b32 a8, v44\n\t" \
  "v_accvgpr_write_b32 a9, v45\n\t" \
  "v_accvgpr_write_b32 a10, v46\n\t" \
  "v_accvgpr_write_b32 a11, v47\n\t" \
  "v_perm_b32 v44, v53, v49, s63\n\t" \
  "v_perm_b32 v45, v61, v57, s63\n\t" \
  "v_perm_b32 v46, v69, v65, s63\n\t" \
  "v_perm_b32 v47, v77, v73, s63\n\t" \
  "v_accvgpr_write_b32 a12, v44\n\t" \
  "v_accvgpr_write_b32 a13, v45\n\t" \
  "v_accvgpr_write_b32 a14, v46\n\t" \
  "v_accvgpr_write_b32 a15, v47\n\t" \
  "v_perm_b32 v44, v54, v50, s62\n\t" \
  "v_perm_b32 v45, v62, v58, s62\n\t" \
  "v_perm_b32 v46, v70, v66, s62\n\t" \
  "v_perm_b32 v47, v78, v74, s62\n\t" \
  "v_accvgpr_write_b32 a16, v44\n\t" \
  "v_accvgpr_write_b32 a17, v45\n\t" \
  "v_accvgpr_write_b32 a18, v46\n\t" \
  "v_accvgpr_write_b32 a19, v47\n\t" \
  "v_perm_b32 v44, v54, v50, s63\n\t" \
  "v_perm_b32 v45, v62, v58, s63\n\t" \
  "v_perm_b32 v46, v70, v66, s63\n\t" \
  "v_perm_b32 v47, v78, v74, s63\n\t" \
  "v_accvgpr_write_b32 a20, v44\n\t" \
  "v_accvgpr_write_b32 a21, v45\n\t" \
  "v_accvgpr_write_b32 a22, v46\n\t" \
  "v_accvgpr_write_b32 a23, v47\n\t" \
  "v_perm_b32 v44, v55, v51, s62\n\t" \
  "v_perm_b32 v45, v63, v59, s62\n\t" \
  "v_perm_b32 v46, v71, v67, s62\n\t" \
  "v_perm_b32 v47, v79, v75, s62\n\t" \
  "v_accvgpr_write_b32 a24, v44\n\t" \
  "v_accvgpr_write_b32 a25, v45\n\t" \
  "v_accvgpr_write_b32 a26, v46\n\t" \
  "v_accvgpr_write_b32 a27, v47\n\t" \
  "v_perm_b32 v44, v55, v51, s63\n\t" \
  "v_perm_b32 v45, v63, v59, s63\n\t" \
  "v_perm_b32 v46, v71, v67, s63\n\t" \
  "v_perm_b32 v47, v79, v75, s63\n\t" \
  "v_accvgpr_write_b32 a28, v44\n\t" \
  "v_accvgpr_write_b32 a29, v45\n\t" \
  "v_accvgpr_write_b32 a30, v46\n\t" \
  "v_accvgpr_write_b32 a31, v47\n\t" \
  "s_xor_b32 s60, s66, 147456\n\t" \
  "s_add_u32 s60, s60, 36864\n\t" \
  "s_add_u32 s61, s60, 18432\n\t" \
  "buffer_load_dwordx4 v[48:51], v41, %[W], s60 offen offset:0\n\t" \
  "buffer_load_dwordx4 v[52:55], v41, %[W], s60 offen offset:1152\n\t" \
  "buffer_load_dwordx4 v[56:59], v41, %[W], s60 offen offset:2304\n\t" \
  "buffer_load_dwordx4 v[60:63], v41, %[W], s60 offen offset:3456\n\t" \
  "buffer_load_dwordx4 v[64:67], v41, %[W], s61 offen offset:0\n\t" \
  "buffer_load_dwordx4 v[68:71], v41, %[W], s61 offen offset:1152\n\t" \
  "buffer_load_dwordx4 v[72:75], v41, %[W], s61 offen offset:2304\n\t" \
  "buffer_load_dwordx4 v[76:79], v41, %[W], s61 offen offset:3456\n\t" \
  "s_waitcnt vmcnt(38)\n\t" \
  "v_perm_b32 v44, v84, v80, s62\n\t" \
  "s_waitcnt vmcnt(36)\n\t" \
  "v_perm_b32 v45, v92, v88, s62\n\t" \
  "s_waitcnt vmcnt(34)\n\t" \
  "v_perm_b32 v46, v100, v96, s62\n\t" \
  "s_waitcnt vmcnt(32)\n\t" \
  "v_perm_b32 v47, v108, v104, s62\n\t" \
  "v_accvgpr_write_b32 a32, v44\n\t" \
  "v_accvgpr_write_b32 a33, v45\n\t" \
  "v_accvgpr_write_b32 a34, v46\n\t" \
  "v_accvgpr_write_b32 a35, v47\n\t" \
  "v_perm_b32 v44, v84, v80, s63\n\t" \
  "v_perm_b32 v45, v92, v88, s63\n\t" \
  "v_perm_b32 v46, v100, v96, s63\n\t" \
  "v_perm_b32 v47, v108, v104, s63\n\t" \
  "v_accvgpr_write_b32 a36, v44\n\t" \
  "v_accvgpr_write_b32 a37, v45\n\t" \
  "v_accvgpr_write_b32 a38, v46\n\t" \
  "v_accvgpr_write_b32 a39, v47\n\t" \
  "v_perm_b32 v44, v85, v81, s62\n\t" \
  "v_perm_b32 v45, v93, v89, s62\n\t" \
  "v_perm_b32 v46, v101, v97, s62\n\t" \
  "v_perm_b32 v47, v109, v105, s62\n\t" \
  "v_accvgpr_write_b32 a40, v44\n\t" \
  "v_accvgpr_write_b32 a41, v45\n\t" \
  "v_accvgpr_write_b32 a42, v46\n\t" \
  "v_accvgpr_write_b32 a43, v47\n\t" \
  "v_perm_b32 v44, v85, v81, s63\n\t" \
  "v_perm_b32 v45, v93, v89, s63\n\t" \
  "v_perm_b32 v46, v101, v97, s63\n\t" \
  "v_perm_b32 v47, v109, v105, s63\n\t" \
  "v_accvgpr_write_b32 a44, v44\n\t" \
  "v_accvgpr_write_b32 a45, v45\n\t" \
  "v_accvgpr_write_b32 a46, v46\n\t" \
  "v_accvgpr_write_b32 a47, v47\n\t" \
  "v_perm_b32 v44, v86, v82, s62\n\t" \
  "v_perm_b32 v45, v94, v90, s62\n\t" \
  "v_perm_b32 v46, v102, v98, s62\n\t" \
  "v_perm_b32 v47, v110, v106, s62\n\t" \
  "v_accvgpr_write_b32 a48, v44\n\t" \
  "v_accvgpr_write_b32 a49, v45\n\t" \
  "v_accvgpr_write_b32 a50, v46\n\t" \
  "v_accvgpr_write_b32 a51, v47\n\t" \
  "v_perm_b32 v44, v86, v82, s63\n\t" \
  "v_perm_b32 v45, v94, v90, s63\n\t" \
  "v_perm_b32 v46, v102, v98, s63\n\t" \
  "v_perm_b32 v47, v110, v106, s63\n\t" \
  "v_accvgpr_write_b32 a52, v44\n\t" \
  "v_accvgpr_write_b32 a53, v45\n\t" \
  "v_accvgpr_write_b32 a54, v46\n\t" \
  "v_accvgpr_write_b32 a55, v47\n\t" \
  "v_perm_b32 v44, v87, v83, s62\n\t" \
  "v_perm_b32 v45, v95, v91, s62\n\t" \
  "v_perm_b32 v46, v103, v99, s62\n\t" \
  "v_perm_b32 v47, v111, v107, s62\n\t" \
  "v_accvgpr_write_b32 a56, v44\n\t" \
  "v_accvgpr_write_b32 a57, v45\n\t" \
  "v_accvgpr_write_b32 a58, v46\n\t" \
  "v_accvgpr_write_b32 a59, v47\n\t" \
  "v_perm_b32 v44, v87, v83, s63\n\t" \
  "v_perm_b32 v45, v95, v91, s63\n\t" \
  "v_perm_b32 v46, v103, v99, s63\n\t" \
  "v_perm_b32 v47, v111, v107, s63\n\t" \
  "v_accvgpr_write_b32 a60, v44\n\t" \
  "v_accvgpr_write_b32 a61, v45\n\t" \
  "v_accvgpr_write_b32 a62, v46\n\t" \
  "v_accvgpr_write_b32 a63, v47\n\t" \
  "s_xor_b32 s60, s66, 147456\n\t" \
  "s_add_u32 s60, s60, 73728\n\t" \
  "s_add_u32 s61, s60, 18432\n\t" \
  "buffer_load_dwordx4 v[80:83], v41, %[W], s60 offen offset:0\n\t" \
  "buffer_load_dwordx4 v[84:87], v41, %[W], s60 offen offset:1152\n\t" \
  "buffer_load_dwordx4 v[88:91], v41, %[W], s60 offen offset:2304\n\t" \
  "buffer_load_dwordx4 v[92:95], v41, %[W], s60 offen offset:3456\n\t" \
  "buffer_load_dwordx4 v[96:99], v41, %[W], s61 offen offset:0\n\t" \
  "buffer_load_dwordx4 v[100:103], v41, %[W], s61 offen offset:1152\n\t" \
  "buffer_load_dwordx4 v[104:107], v41, %[W], s61 offen offset:2304\n\t" \
  "buffer_load_dwordx4 v[108:111], v41, %[W], s61 offen offset:3456\n\t" \
  "s_waitcnt vmcnt(38)\n\t" \
  "v_perm_b32 v44, v116, v112, s62\n\t" \
  "s_waitcnt vmcnt(36)\n\t" \
  "v_perm_b32 v45, v124, v120, s62\n\t" \
  "s_waitcnt vmcnt(34)\n\t" \
  "v_perm_b32 v46, v132, v128, s62\n\t" \
  "s_waitcnt vmcnt(32)\n\t" \
  "v_perm_b32 v47, v140, v136, s62\n\t" \
  "v_accvgpr_write_b32 a64, v44\n\t" \
  "v_accvgpr_write_b32 a65, v45\n\t" \
  "v_accvgpr_write_b32 a66, v46\n\t" \
  "v_accvgpr_write_b32 a67, v47\n\t" \
  "v_perm_b32 v44, v116, v112, s63\n\t" \
  "v_perm_b32 v45, v124, v120, s63\n\t" \
  "v_perm_b32 v46, v132, v128, s63\n\t" \
  "v_perm_b32 v47, v140, v136, s63\n\t" \
  "v_accvgpr_write_b32 a68, v44\n\t" \
  "v_accvgpr_write_b32 a69, v45\n\t" \
  "v_accvgpr_write_b32 a70, v46\n\t" \
  "v_accvgpr_write_b32 a71, v47\n\t" \
  "v_perm_b32 v44, v117, v113, s62\n\t" \
  "v_perm_b32 v45, v125, v121, s62\n\t" \
  "v_perm_b32 v46, v133, v129, s62\n\t" \
  "v_perm_b32 v47, v141, v137, s62\n\t" \
  "v_accvgpr_write_b32 a72, v44\n\t" \
  "v_accvgpr_write_b32 a73, v45\n\t" \
  "v_accvgpr_write_b32 a74, v46\n\t" \
  "v_accvgpr_write_b32 a75, v47\n\t" \
  "v_perm_b32 v44, v117, v113, s63\n\t" \
  "v_perm_b32 v45, v125, v121, s63\n\t" \
  "v_perm_b32 v46, v133, v129, s63\n\t" \
  "v_perm_b32 v47, v141, v137, s63\n\t" \
  "v_accvgpr_write_b32 a76, v44\n\t" \
  "v_accvgpr_write_b32 a77, v45\n\t" \
  "v_accvgpr_write_b32 a78, v46\n\t" \
  "v_accvgpr_write_b32 a79, v47\n\t" \
  "v_perm_b32 v44, v118, v114, s62\n\t" \
  "v_perm_b32 v45, v126, v122, s62\n\t" \
  "v_perm_b32 v46, v134, v130, s62\n\t" \
  "v_perm_b32 v47, v142, v138, s62\n\t" \
  "v_accvgpr_write_b32 a80, v44\n\t" \
  "v_accvgpr_write_b32 a81, v45\n\t" \
  "v_accvgpr_write_b32 a82, v46\n\t" \
  "v_accvgpr_write_b32 a83, v47\n\t" \
  "v_perm_b32 v44, v118, v114, s63\n\t" \
  "v_perm_b32 v45, v126, v122, s63\n\t" \
  "v_perm_b32 v46, v134, v130, s63\n\t" \
  "v_perm_b32 v47, v142, v138, s63\n\t" \
  "v_accvgpr_write_b32 a84, v44\n\t" \
  "v_accvgpr_write_b32 a85, v45\n\t" \
  "v_accvgpr_write_b32 a86, v46\n\t" \
  "v_accvgpr_write_b32 a87, v47\n\t" \
  "v_perm_b32 v44, v119, v115, s62\n\t" \
  "v_perm_b32 v45, v127, v123, s62\n\t" \
  "v_perm_b32 v46, v135, v131, s62\n\t" \
  "v_perm_b32 v47, v143, v139, s62\n\t" \
  "v_accvgpr_write_b32 a88, v44\n\t" \
  "v_accvgpr_write_b32 a89, v45\n\t" \
  "v_accvgpr_write_b32 a90, v46\n\t" \
  "v_accvgpr_write_b32 a91, v47\n\t" \
  "v_perm_b32 v44, v119, v115, s63\n\t" \
  "v_perm_b32 v45, v127, v123, s63\n\t" \
  "v_perm_b32 v46, v135, v131, s63\n\t" \
  "v_perm_b32 v47, v143, v139, s63\n\t" \
  "v_accvgpr_write_b32 a92, v44\n\t" \
  "v_accvgpr_write_b32 a93, v45\n\t" \
  "v_accvgpr_write_b32 a94, v46\n\t" \
  "v_accvgpr_write_b32 a95, v47\n\t" \
  "s_xor_b32 s60, s66, 147456\n\t" \
  "s_add_u32 s60, s60, 110592\n\t" \
  "s_add_u32 s61, s60, 18432\n\t" \
  "buffer_load_dwordx4 v[112:115], v41, %[W], s60 offen offset:0\n\t" \
  "buffer_load_dwordx4 v[116:119], v41, %[W], s60 offen offset:1152\n\t" \
  "buffer_load_dwordx4 v[120:123], v41, %[W], s60 offen offset:2304\n\t" \
  "buffer_load_dwordx4 v[124:127], v41, %[W], s60 offen offset:3456\n\t" \
  "buffer_load_dwordx4 v[128:131], v41, %[W], s61 offen offset:0\n\t" \
  "buffer_load_dwordx4 v[132:135], v41, %[W], s61 offen offset:1152\n\t" \
  "buffer_load_dwordx4 v[136:139], v41, %[W], s61 offen offset:2304\n\t" \
  "buffer_load_dwordx4 v[140:143], v41, %[W], s61 offen offset:3456\n\t" \
  "s_waitcnt vmcnt(38)\n\t" \
  "v_perm_b32 v44, v148, v144, s62\n\t" \
  "s_waitcnt vmcnt(36)\n\t" \
  "v_perm_b32 v45, v156, v152, s62\n\t" \
  "s_waitcnt vmcnt(34)\n\t" \
  "v_perm_b32 v46, v164, v160, s62\n\t" \
  "s_waitcnt vmcnt(32)\n\t" \
  "v_perm_b32 v47, v172, v168, s62\n\t" \
  "v_accvgpr_write_b32 a96, v44\n\t" \
  "v_accvgpr_write_b32 a97, v45\n\t" \
  "v_accvgpr_write_b32 a98, v46\n\t" \
  "v_accvgpr_write_b32 a99, v47\n\t" \
  "v_perm_b32 v44, v148, v144, s63\n\t" \
  "v_perm_b32 v45, v156, v152, s63\n\t" \
  "v_perm_b32 v46, v164, v160, s63\n\t" \
  "v_perm_b32 v47, v172, v168, s63\n\t" \
  "v_accvgpr_write_b32 a100, v44\n\t" \
  "v_accvgpr_write_b32 a101, v45\n\t" \
  "v_accvgpr_write_b32 a102, v46\n\t" \
  "v_accvgpr_write_b32 a103, v47\n\t" \
  "v_perm_b32 v44, v149, v145, s62\n\t" \
  "v_perm_b32 v45, v157, v153, s62\n\t" \
  "v_perm_b32 v46, v165, v161, s62\n\t" \
  "v_perm_b32 v47, v173, v169, s62\n\t" \
  "v_accvgpr_write_b32 a104, v44\n\t" \
  "v_accvgpr_write_b32 a105, v45\n\t" \
  "v_accvgpr_write_b32 a106, v46\n\t" \
  "v_accvgpr_write_b32 a107, v47\n\t" \
  "v_perm_b32 v44, v149, v145, s63\n\t" \
  "v_perm_b32 v45, v157, v153, s63\n\t" \
  "v_perm_b32 v46, v165, v161, s63\n\t" \
  "v_perm_b32 v47, v173, v169, s63\n\t" \
  "v_accvgpr_write_b32 a108, v44\n\t" \
  "v_accvgpr_write_b32 a109, v45\n\t" \
  "v_accvgpr_write_b32 a110, v46\n\t" \
  "v_accvgpr_write_b32 a111, v47\n\t" \
  "v_perm_b32 v44, v150, v146, s62\n\t" \
  "v_perm_b32 v45, v158, v154, s62\n\t" \
  "v_perm_b32 v46, v166, v162, s62\n\t" \
  "v_perm_b32 v47, v174, v170, s62\n\t" \
  "v_accvgpr_write_b32 a112, v44\n\t" \
  "v_accvgpr_write_b32 a113, v45\n\t" \
  "v_accvgpr_write_b32 a114, v46\n\t" \
  "v_accvgpr_write_b32 a115, v47\n\t" \
  "v_perm_b32 v44, v150, v146, s63\n\t" \
  "v_perm_b32 v45, v158, v154, s63\n\t" \
  "v_perm_b32 v46, v166, v162, s63\n\t" \
  "v_perm_b32 v47, v174, v170, s63\n\t" \
  "v_accvgpr_write_b32 a116, v44\n\t" \
  "v_accvgpr_write_b32 a117, v45\n\t" \
  "v_accvgpr_write_b32 a118, v46\n\t" \
  "v_accvgpr_write_b32 a119, v47\n\t" \
  "v_perm_b32 v44, v151, v147, s62\n\t" \
  "v_perm_b32 v45, v159, v155, s62\n\t" \
  "v_perm_b32 v46, v167, v163, s62\n\t" \
  "v_perm_b32 v47, v175, v171, s62\n\t" \
  "v_accvgpr_write_b32 a120, v44\n\t" \
  "v_accvgpr_write_b32 a121, v45\n\t" \
  "v_accvgpr_write_b32 a122, v46\n\t" \
  "v_accvgpr_write_b32 a123, v47\n\t" \
  "v_perm_b32 v44, v151, v147, s63\n\t" \
  "v_perm_b32 v45, v159, v155, s63\n\t" \
  "v_perm_b32 v46, v167, v163, s63\n\t" \
  "v_perm_b32 v47, v175, v171, s63\n\t" \
  "v_accvgpr_write_b32 a124, v44\n\t" \
  "v_accvgpr_write_b32 a125, v45\n\t" \
  "v_accvgpr_write_b32 a126, v46\n\t" \
  "v_accvgpr_write_b32 a127, v47\n\t" \
  "s_xor_b32 s60, s66, 0\n\t" \
  "s_add_u32 s60, s60, 0\n\t" \
  "s_add_u32 s61, s60, 18432\n\t" \
  "buffer_load_ushort v224, v42, %[W], s60 offen offset:0\n\t" \
  "buffer_load_ushort v144, v42, %[W], s60 offen offset:1152\n\t" \
  "buffer_load_ushort v225, v42, %[W], s60 offen offset:2304\n\t" \
  "buffer_load_ushort v145, v42, %[W], s60 offen offset:3456\n\t" \
  "buffer_load_ushort v226, v42, %[W], s61 offen offset:0\n\t" \
  "buffer_load_ushort v146, v42, %[W], s61 offen offset:1152\n\t" \
  "buffer_load_ushort v227, v42, %[W], s61 offen offset:2304\n\t" \
  "buffer_load_ushort v147, v42, %[W], s61 offen offset:3456\n\t" \
  "s_xor_b32 s60, s66, 0\n\t" \
  "s_add_u32 s60, s60, 36864\n\t" \
  "s_add_u32 s61, s60, 18432\n\t" \
  "buffer_load_ushort v228, v42, %[W], s60 offen offset:0\n\t" \
  "buffer_load_ushort v148, v42, %[W], s60 offen offset:1152\n\t" \
  "buffer_load_ushort v229, v42, %[W], s60 offen offset:2304\n\t" \
  "buffer_load_ushort v149, v42, %[W], s60 offen offset:3456\n\t" \
  "buffer_load_ushort v230, v42, %[W], s61 offen offset:0\n\t" \
  "buffer_load_ushort v150, v42, %[W], s61 offen offset:1152\n\t" \
  "buffer_load_ushort v231, v42, %[W], s61 offen offset:2304\n\t" \
  "buffer_load_ushort v151, v42, %[W], s61 offen offset:3456\n\t" \
  "s_waitcnt vmcnt(46)\n\t" \
  "v_perm_b32 v44, v180, v176, s62\n\t" \
  "s_waitcnt vmcnt(44)\n\t" \
  "v_perm_b32 v45, v188, v184, s62\n\t" \
  "s_waitcnt vmcnt(42)\n\t" \
  "v_perm_b32 v46, v196, v192, s62\n\t" \
  "s_waitcnt vmcnt(40)\n\t" \
  "v_perm_b32 v47, v204, v200, s62\n\t" \
  "v_accvgpr_write_b32 a128, v44\n\t" \
  "v_accvgpr_write_b32 a129, v45\n\t" \
  "v_accvgpr_write_b32 a130, v46\n\t" \
  "v_accvgpr_write_b32 a131, v47\n\t" \
  "v_perm_b32 v44, v180, v176, s63\n\t" \
  "v_perm_b32 v45, v188, v184, s63\n\t" \
  "v_perm_b32 v46, v196, v192, s63\n\t" \
  "v_perm_b32 v47, v204, v200, s63\n\t" \
  "v_accvgpr_write_b32 a132, v44\n\t" \
  "v_accvgpr_write_b32 a133, v45\n\t" \
  "v_accvgpr_write_b32 a134, v46\n\t" \
  "v_accvgpr_write_b32 a135, v47\n\t" \
  "v_perm_b32 v44, v181, v177, s62\n\t" \
  "v_perm_b32 v45, v189, v185, s62\n\t" \
  "v_perm_b32 v46, v197, v193, s62\n\t" \
  "v_perm_b32 v47, v205, v201, s62\n\t" \
  "v_accvgpr_write_b32 a136, v44\n\t" \
  "v_accvgpr_write_b32 a137, v45\n\t" \
  "v_accvgpr_write_b32 a138, v46\n\t" \
  "v_accvgpr_write_b32 a139, v47\n\t" \
  "v_perm_b32 v44, v181, v177, s63\n\t" \
  "v_perm_b32 v45, v189, v185, s63\n\t" \
  "v_perm_b32 v46, v197, v193, s63\n\t" \
  "v_perm_b32 v47, v205, v201, s63\n\t" \
  "v_accvgpr_write_b32 a140, v44\n\t" \
  "v_accvgpr_write_b32 a141, v45\n\t" \
  "v_accvgpr_write_b32 a142, v46\n\t" \
  "v_accvgpr_write_b32 a143, v47\n\t" \
  "v_perm_b32 v44, v182, v178, s62\n\t" \
  "v_perm_b32 v45, v190, v186, s62\n\t" \
  "v_perm_b32 v46, v198, v194, s62\n\t" \
  "v_perm_b32 v47, v206, v202, s62\n\t" \
  "v_accvgpr_write_b32 a144, v44\n\t" \
  "v_accvgpr_write_b32 a145, v45\n\t" \
  "v_accvgpr_write_b32 a146, v46\n\t" \
  "v_accvgpr_write_b32 a147, v47\n\t" \
  "v_perm_b32 v44, v182, v178, s63\n\t" \
  "v_perm_b32 v45, v190, v186, s63\n\t" \
  "v_perm_b32 v46, v198, v194, s63\n\t" \
  "v_perm_b32 v47, v206, v202, s63\n\t" \
  "v_accvgpr_write_b32 a148, v44\n\t" \
  "v_accvgpr_write_b32 a149, v45\n\t" \
  "v_accvgpr_write_b32 a150, v46\n\t" \
  "v_accvgpr_write_b32 a151, v47\n\t" \
  "v_perm_b32 v44, v183, v179, s62\n\t" \
  "v_perm_b32 v45, v191, v187, s62\n\t" \
  "v_perm_b32 v46, v199, v195, s62\n\t" \
  "v_perm_b32 v47, v207, v203, s62\n\t" \
  "v_accvgpr_write_b32 a152, v44\n\t" \
  "v_accvgpr_write_b32 a153, v45\n\t" \
  "v_accvgpr_write_b32 a154, v46\n\t" \
  "v_accvgpr_write_b32 a155, v47\n\t" \
  "v_perm_b32 v44, v183, v179, s63\n\t" \
  "v_perm_b32 v45, v191, v187, s63\n\t" \
  "v_perm_b32 v46, v199, v195, s63\n\t" \
  "v_perm_b32 v47, v207, v203, s63\n\t" \
  "v_accvgpr_write_b32 a156, v44\n\t" \
  "v_accvgpr_write_b32 a157, v45\n\t" \
  "v_accvgpr_write_b32 a158, v46\n\t" \
  "v_accvgpr_write_b32 a159, v47\n\t" \
  "s_xor_b32 s60, s66, 0\n\t" \
  "s_add_u32 s60, s60, 73728\n\t" \
  "s_add_u32 s61, s60, 18432\n\t" \
  "buffer_load_ushort v232, v42, %[W], s60 offen offset:0\n\t" \
  "buffer_load_ushort v152, v42, %[W], s60 offen offset:1152\n\t" \
  "buffer_load_ushort v233, v42, %[W], s60 offen offset:2304\n\t" \
  "buffer_load_ushort v153, v42, %[W], s60 offen offset:3456\n\t" \
  "buffer_load_ushort v234, v42, %[W], s61 offen offset:0\n\t" \
  "buffer_load_ushort v154, v42, %[W], s61 offen offset:1152\n\t" \
  "buffer_load_ushort v235, v42, %[W], s61 offen offset:2304\n\t" \
  "buffer_load_ushort v155, v42, %[W], s61 offen offset:3456\n\t" \
  "s_xor_b32 s60, s66, 0\n\t" \
  "s_add_u32 s60, s60, 110592\n\t" \
  "s_add_u32 s61, s60, 18432\n\t" \
  "buffer_load_ushort v236, v42, %[W], s60 offen offset:0\n\t" \
  "buffer_load_ushort v156, v42, %[W], s60 offen offset:1152\n\t" \
  "buffer_load_ushort v237, v42, %[W], s60 offen offset:2304\n\t" \
  "buffer_load_ushort v157, v42, %[W], s60 offen offset:3456\n\t" \
  "buffer_load_ushort v238, v42, %[W], s61 offen offset:0\n\t" \
  "buffer_load_ushort v158, v42, %[W], s61 offen offset:1152\n\t" \
  "buffer_load_ushort v239, v42, %[W], s61 offen offset:2304\n\t" \
  "buffer_load_ushort v159, v42, %[W], s61 offen offset:3456\n\t" \
  "s_waitcnt vmcnt(54)\n\t" \
  "v_perm_b32 v44, v52, v48, s62\n\t" \
  "s_waitcnt vmcnt(52)\n\t" \
  "v_perm_b32 v45, v60, v56, s62\n\t" \
  "s_waitcnt vmcnt(50)\n\t" \
  "v_perm_b32 v46, v68, v64, s62\n\t" \
  "s_waitcnt vmcnt(48)\n\t" \
  "v_perm_b32 v47, v76, v72, s62\n\t" \
  "v_accvgpr_write_b32 a160, v44\n\t" \
  "v_accvgpr_write_b32 a161, v45\n\t" \
  "v_accvgpr_write_b32 a162, v46\n\t" \
  "v_accvgpr_write_b32 a163, v47\n\t" \
  "v_perm_b32 v44, v52, v48, s63\n\t" \
  "v_perm_b32 v45, v60, v56, s63\n\t" \
  "v_perm_b32 v46, v68, v64, s63\n\t" \
  "v_perm_b32 v47, v76, v72, s63\n\t" \
  "v_accvgpr_write_b32 a164, v44\n\t" \
  "v_accvgpr_write_b32 a165, v45\n\t" \
  "v_accvgpr_write_b32 a166, v46\n\t" \
  "v_accvgpr_write_b32 a167, v47\n\t" \
  "v_perm_b32 v44, v53, v49, s62\n\t" \
  "v_perm_b32 v45, v61, v57, s62\n\t" \
  "v_perm_b32 v46, v69, v65, s62\n\t" \
  "v_perm_b32 v47, v77, v73, s62\n\t" \
  "v_accvgpr_write_b32 a168, v44\n\t" \
  "v_accvgpr_write_b32 a169, v45\n\t" \
  "v_accvgpr_write_b32 a170, v46\n\t" \
  "v_accvgpr_write_b32 a171, v47\n\t" \
  "v_perm_b32 v44, v53, v49, s63\n\t" \
  "v_perm_b32 v45, v61, v57, s63\n\t" \
  "v_perm_b32 v46, v69, v65, s63\n\t" \
  "v_perm_b32 v47, v77, v73, s63\n\t" \
  "v_accvgpr_write_b32 a172, v44\n\t" \
  "v_accvgpr_write_b32 a173, v45\n\t" \
  "v_accvgpr_write_b32 a174, v46\n\t" \
  "v_accvgpr_write_b32 a175, v47\n\t" \
  "v_perm_b32 v44, v54, v50, s62\n\t" \
  "v_perm_b32 v45, v62, v58, s62\n\t" \
  "v_perm_b32 v46, v70, v66, s62\n\t" \
  "v_perm_b32 v47, v78, v74, s62\n\t" \
  "v_accvgpr_write_b32 a176, v44\n\t" \
  "v_accvgpr_write_b32 a177, v45\n\t" \
  "v_accvgpr_write_b32 a178, v46\n\t" \
  "v_accvgpr_write_b32 a179, v47\n\t" \
  "v_perm_b32 v44, v54, v50, s63\n\t" \
  "v_perm_b32 v45, v62, v58, s63\n\t" \
  "v_perm_b32 v46, v70, v66, s63\n\t" \
  "v_perm_b32 v47, v78, v74, s63\n\t" \
  "v_accvgpr_write_b32 a180, v44\n\t" \
  "v_accvgpr_write_b32 a181, v45\n\t" \
  "v_accvgpr_write_b32 a182, v46\n\t" \
  "v_accvgpr_write_b32 a183, v47\n\t" \
  "v_perm_b32 v44, v55, v51, s62\n\t" \
  "v_perm_b32 v45, v63, v59, s62\n\t" \
  "v_perm_b32 v46, v71, v67, s62\n\t" \
  "v_perm_b32 v47, v79, v75, s62\n\t" \
  "v_accvgpr_write_b32 a184, v44\n\t" \
  "v_accvgpr_write_b32 a185, v45\n\t" \
  "v_accvgpr_write_b32 a186, v46\n\t" \
  "v_accvgpr_write_b32 a187, v47\n\t" \
  "v_perm_b32 v44, v55, v51, s63\n\t" \
  "v_perm_b32 v45, v63, v59, s63\n\t" \
  "v_perm_b32 v46, v71, v67, s63\n\t" \
  "v_perm_b32 v47, v79, v75, s63\n\t" \
  "v_accvgpr_write_b32 a188, v44\n\t" \
  "v_accvgpr_write_b32 a189, v45\n\t" \
  "v_accvgpr_write_b32 a190, v46\n\t" \
  "v_accvgpr_write_b32 a191, v47\n\t" \
  "s_xor_b32 s60, s66, 147456\n\t" \
  "s_add_u32 s60, s60, 0\n\t" \
  "s_add_u32 s61, s60, 18432\n\t" \
  "buffer_load_ushort v240, v42, %[W], s60 offen offset:0\n\t" \
  "buffer_load_ushort v160, v42, %[W], s60 offen offset:1152\n\t" \
  "buffer_load_ushort v241, v42, %[W], s60 offen offset:2304\n\t" \
  "buffer_load_ushort v161, v42, %[W], s60 offen offset:3456\n\t" \
  "buffer_load_ushort v242, v42, %[W], s61 offen offset:0\n\t" \
  "buffer_load_ushort v162, v42, %[W], s61 offen offset:1152\n\t" \
  "buffer_load_ushort v243, v42, %[W], s61 offen offset:2304\n\t" \
  "buffer_load_ushort v163, v42, %[W], s61 offen offset:3456\n\t" \
  "s_waitcnt vmcnt(54)\n\t" \
  "v_perm_b32 v44, v84, v80, s62\n\t" \
  "s_waitcnt vmcnt(52)\n\t" \
  "v_perm_b32 v45, v92, v88, s62\n\t" \
  "s_waitcnt vmcnt(50)\n\t" \
  "v_perm_b32 v46, v100, v96, s62\n\t" \
  "s_waitcnt vmcnt(48)\n\t" \
  "v_perm_b32 v47, v108, v104, s62\n\t" \
  "v_accvgpr_write_b32 a192, v44\n\t" \
  "v_accvgpr_write_b32 a193, v45\n\t" \
  "v_accvgpr_write_b32 a194, v46\n\t" \
  "v_accvgpr_write_b32 a195, v47\n\t" \
  "v_perm_b32 v44, v84, v80, s63\n\t" \
  "v_perm_b32 v45, v92, v88, s63\n\t" \
  "v_perm_b32 v46, v100, v96, s63\n\t" \
  "v_perm_b32 v47, v108, v104, s63\n\t" \
  "v_accvgpr_write_b32 a196, v44\n\t" \
  "v_accvgpr_write_b32 a197, v45\n\t" \
  "v_accvgpr_write_b32 a198, v46\n\t" \
  "v_accvgpr_write_b32 a199, v47\n\t" \
  "v_perm_b32 v44, v85, v81, s62\n\t" \
  "v_perm_b32 v45, v93, v89, s62\n\t" \
  "v_perm_b32 v46, v101, v97, s62\n\t" \
  "v_perm_b32 v47, v109, v105, s62\n\t" \
  "v_accvgpr_write_b32 a200, v44\n\t" \
  "v_accvgpr_write_b32 a201, v45\n\t" \
  "v_accvgpr_write_b32 a202, v46\n\t" \
  "v_accvgpr_write_b32 a203, v47\n\t" \
  "v_perm_b32 v44, v85, v81, s63\n\t" \
  "v_perm_b32 v45, v93, v89, s63\n\t" \
  "v_perm_b32 v46, v101, v97, s63\n\t" \
  "v_perm_b32 v47, v109, v105, s63\n\t" \
  "v_accvgpr_write_b32 a204, v44\n\t" \
  "v_accvgpr_write_b32 a205, v45\n\t" \
  "v_accvgpr_write_b32 a206, v46\n\t" \
  "v_accvgpr_write_b32 a207, v47\n\t" \
  "v_perm_b32 v44, v86, v82, s62\n\t" \
  "v_perm_b32 v45, v94, v90, s62\n\t" \
  "v_perm_b32 v46, v102, v98, s62\n\t" \
  "v_perm_b32 v47, v110, v106, s62\n\t" \
  "v_accvgpr_write_b32 a208, v44\n\t" \
  "v_accvgpr_write_b32 a209, v45\n\t" \
  "v_accvgpr_write_b32 a210, v46\n\t" \
  "v_accvgpr_write_b32 a211, v47\n\t" \
  "v_perm_b32 v44, v86, v82, s63\n\t" \
  "v_perm_b32 v45, v94, v90, s63\n\t" \
  "v_perm_b32 v46, v102, v98, s63\n\t" \
  "v_perm_b32 v47, v110, v106, s63\n\t" \
  "v_accvgpr_write_b32 a212, v44\n\t" \
  "v_accvgpr_write_b32 a213, v45\n\t" \
  "v_accvgpr_write_b32 a214, v46\n\t" \
  "v_accvgpr_write_b32 a215, v47\n\t" \
  "v_perm_b32 v44, v87, v83, s62\n\t" \
  "v_perm_b32 v45, v95, v91, s62\n\t" \
  "v_perm_b32 v46, v103, v99, s62\n\t" \
  "v_perm_b32 v47, v111, v107, s62\n\t" \
  "v_accvgpr_write_b32 a216, v44\n\t" \
  "v_accvgpr_write_b32 a217, v45\n\t" \
  "v_accvgpr_write_b32 a218, v46\n\t" \
  "v_accvgpr_write_b32 a219, v47\n\t" \
  "v_perm_b32 v44, v87, v83, s63\n\t" \
  "v_perm_b32 v45, v95, v91, s63\n\t" \
  "v_perm_b32 v46, v103, v99, s63\n\t" \
  "v_perm_b32 v47, v111, v107, s63\n\t" \
  "v_accvgpr_write_b32 a220, v44\n\t" \
  "v_accvgpr_write_b32 a221, v45\n\t" \
  "v_accvgpr_write_b32 a222, v46\n\t" \
  "v_accvgpr_write_b32 a223, v47\n\t" \
  "s_xor_b32 s60, s66, 147456\n\t" \
  "s_add_u32 s60, s60, 36864\n\t" \
  "s_add_u32 s61, s60, 18432\n\t" \
  "buffer_load_ushort v244, v42, %[W], s60 offen offset:0\n\t" \
  "buffer_load_ushort v164, v42, %[W], s60 offen offset:1152\n\t" \
  "buffer_load_ushort v245, v42, %[W], s60 offen offset:2304\n\t" \
  "buffer_load_ushort v165, v42, %[W], s60 offen offset:3456\n\t" \
  "buffer_load_ushort v246, v42, %[W], s61 offen offset:0\n\t" \
  "buffer_load_ushort v166, v42, %[W], s61 offen offset:1152\n\t" \
  "buffer_load_ushort v247, v42, %[W], s61 offen offset:2304\n\t" \
  "buffer_load_ushort v167, v42, %[W], s61 offen offset:3456\n\t" \
  "s_waitcnt vmcnt(54)\n\t" \
  "v_perm_b32 v44, v116, v112, s62\n\t" \
  "s_waitcnt vmcnt(52)\n\t" \
  "v_perm_b32 v45, v124, v120, s62\n\t" \
  "s_waitcnt vmcnt(50)\n\t" \
  "v_perm_b32 v46, v132, v128, s62\n\t" \
  "s_waitcnt vmcnt(48)\n\t" \
  "v_perm_b32 v47, v140, v136, s62\n\t" \
  "v_accvgpr_write_b32 a224, v44\n\t" \
  "v_accvgpr_write_b32 a225, v45\n\t" \
  "v_accvgpr_write_b32 a226, v46\n\t" \
  "v_accvgpr_write_b32 a227, v47\n\t" \
  "v_perm_b32 v44, v116, v112, s63\n\t" \
  "v_perm_b32 v45, v124, v120, s63\n\t" \
  "v_perm_b32 v46, v132, v128, s63\n\t" \
  "v_perm_b32 v47, v140, v136, s63\n\t" \
  "v_accvgpr_write_b32 a228, v44\n\t" \
  "v_accvgpr_write_b32 a229, v45\n\t" \
  "v_accvgpr_write_b32 a230, v46\n\t" \
  "v_accvgpr_write_b32 a231, v47\n\t" \
  "v_perm_b32 v44, v117, v113, s62\n\t" \
  "v_perm_b32 v45, v125, v121, s62\n\t" \
  "v_perm_b32 v46, v133, v129, s62\n\t" \
  "v_perm_b32 v47, v141, v137, s62\n\t" \
  "v_accvgpr_write_b32 a232, v44\n\t" \
  "v_accvgpr_write_b32 a233, v45\n\t" \
  "v_accvgpr_write_b32 a234, v46\n\t" \
  "v_accvgpr_write_b32 a235, v47\n\t" \
  "v_perm_b32 v44, v117, v113, s63\n\t" \
  "v_perm_b32 v45, v125, v121, s63\n\t" \
  "v_perm_b32 v46, v133, v129, s63\n\t" \
  "v_perm_b32 v47, v141, v137, s63\n\t" \
  "v_accvgpr_write_b32 a236, v44\n\t" \
  "v_accvgpr_write_b32 a237, v45\n\t" \
  "v_accvgpr_write_b32 a238, v46\n\t" \
  "v_accvgpr_write_b32 a239, v47\n\t" \
  "v_perm_b32 v44, v118, v114, s62\n\t" \
  "v_perm_b32 v45, v126, v122, s62\n\t" \
  "v_perm_b32 v46, v134, v130, s62\n\t" \
  "v_perm_b32 v47, v142, v138, s62\n\t" \
  "v_accvgpr_write_b32 a240, v44\n\t" \
  "v_accvgpr_write_b32 a241, v45\n\t" \
  "v_accvgpr_write_b32 a242, v46\n\t" \
  "v_accvgpr_write_b32 a243, v47\n\t" \
  "v_perm_b32 v44, v118, v114, s63\n\t" \
  "v_perm_b32 v45, v126, v122, s63\n\t" \
  "v_perm_b32 v46, v134, v130, s63\n\t" \
  "v_perm_b32 v47, v142, v138, s63\n\t" \
  "v_accvgpr_write_b32 a244, v44\n\t" \
  "v_accvgpr_write_b32 a245, v45\n\t" \
  "v_accvgpr_write_b32 a246, v46\n\t" \
  "v_accvgpr_write_b32 a247, v47\n\t" \
  "v_perm_b32 v44, v119, v115, s62\n\t" \
  "v_perm_b32 v45, v127, v123, s62\n\t" \
  "v_perm_b32 v46, v135, v131, s62\n\t" \
  "v_perm_b32 v47, v143, v139, s62\n\t" \
  "v_accvgpr_write_b32 a248, v44\n\t" \
  "v_accvgpr_write_b32 a249, v45\n\t" \
  "v_accvgpr_write_b32 a250, v46\n\t" \
  "v_accvgpr_write_b32 a251, v47\n\t" \
  "v_perm_b32 v44, v119, v115, s63\n\t" \
  "v_perm_b32 v45, v127, v123, s63\n\t" \
  "v_perm_b32 v46, v135, v131, s63\n\t" \
  "v_perm_b32 v47, v143, v139, s63\n\t" \
  "v_accvgpr_write_b32 a252, v44\n\t" \
  "v_accvgpr_write_b32 a253, v45\n\t" \
  "v_accvgpr_write_b32 a254, v46\n\t" \
  "v_accvgpr_write_b32 a255, v47\n\t" \
  "s_xor_b32 s60, s66, 147456\n\t" \
  "s_add_u32 s60, s60, 73728\n\t" \
  "s_add_u32 s61, s60, 18432\n\t" \
  "buffer_load_ushort v248, v42, %[W], s60 offen offset:0\n\t" \
  "buffer_load_ushort v168, v42, %[W], s60 offen offset:1152\n\t" \
  "buffer_load_ushort v249, v42, %[W], s60 offen offset:2304\n\t" \
  "buffer_load_ushort v169, v42, %[W], s60 offen offset:3456\n\t" \
  "buffer_load_ushort v250, v42, %[W], s61 offen offset:0\n\t" \
  "buffer_load_ushort v170, v42, %[W], s61 offen offset:1152\n\t" \
  "buffer_load_ushort v251, v42, %[W], s61 offen offset:2304\n\t" \
  "buffer_load_ushort v171, v42, %[W], s61 offen offset:3456\n\t" \
  "s_xor_b32 s60, s66, 147456\n\t" \
  "s_add_u32 s60, s60, 110592\n\t" \
  "s_add_u32 s61, s60, 18432\n\t" \
  "s_waitcnt vmcnt(40)\n\t" \
  "buffer_load_ushort v252, v42, %[W], s60 offen offset:0\n\t" \
  "buffer_load_ushort v172, v42, %[W], s60 offen offset:1152\n\t" \
  "buffer_load_ushort v253, v42, %[W], s60 offen offset:2304\n\t" \
  "buffer_load_ushort v173, v42, %[W], s60 offen offset:3456\n\t" \
  "buffer_load_ushort v254, v42, %[W], s61 offen offset:0\n\t" \
  "buffer_load_ushort v174, v42, %[W], s61 offen offset:1152\n\t" \
  "buffer_load_ushort v255, v42, %[W], s61 offen offset:2304\n\t" \
  "buffer_load_ushort v175, v42, %[W], s61 offen offset:3456\n\t" \
  "v_lshl_or_b32 v224, v144, 16, v224\n\t" \
  "v_lshl_or_b32 v225, v145, 16, v225\n\t" \
  "v_lshl_or_b32 v226, v146, 16, v226\n\t" \
  "v_lshl_or_b32 v227, v147, 16, v227\n\t" \
  "v_lshl_or_b32 v228, v148, 16, v228\n\t" \
  "v_lshl_or_b32 v229, v149, 16, v229\n\t" \
  "v_lshl_or_b32 v230, v150, 16, v230\n\t" \
  "v_lshl_or_b32 v231, v151, 16, v231\n\t" \
  "s_waitcnt vmcnt(46)\n\t" \
  "v_lshl_or_b32 v232, v152, 16, v232\n\t" \
  "s_waitcnt vmcnt(44)\n\t" \
  "v_lshl_or_b32 v233, v153, 16, v233\n\t" \
  "s_waitcnt vmcnt(42)\n\t" \
  "v_lshl_or_b32 v234, v154, 16, v234\n\t" \
  "s_waitcnt vmcnt(40)\n\t" \
  "v_lshl_or_b32 v235, v155, 16, v235\n\t" \
  "s_waitcnt vmcnt(38)\n\t" \
  "v_lshl_or_b32 v236, v156, 16, v236\n\t" \
  "s_waitcnt vmcnt(36)\n\t" \
  "v_lshl_or_b32 v237, v157, 16, v237\n\t" \
  "s_waitcnt vmcnt(34)\n\t" \
  "v_lshl_or_b32 v238, v158, 16, v238\n\t" \
  "s_waitcnt vmcnt(32)\n\t" \
  "v_lshl_or_b32 v239, v159, 16, v239\n\t" \
  "s_waitcnt vmcnt(30)\n\t" \
  "v_lshl_or_b32 v240, v160, 16, v240\n\t" \
  "s_waitcnt vmcnt(28)\n\t" \
  "v_lshl_or_b32 v241, v161, 16, v241\n\t" \
  "s_waitcnt vmcnt(26)\n\t" \
  "v_lshl_or_b32 v242, v162, 16, v242\n\t" \
  "s_waitcnt vmcnt(24)\n\t" \
  "v_lshl_or_b32 v243, v163, 16, v243\n\t" \
  "s_waitcnt vmcnt(22)\n\t" \
  "v_lshl_or_b32 v244, v164, 16, v244\n\t" \
  "s_waitcnt vmcnt(20)\n\t" \
  "v_lshl_or_b32 v245, v165, 16, v245\n\t" \
  "s_waitcnt vmcnt(18)\n\t" \
  "v_lshl_or_b32 v246, v166, 16, v246\n\t" \
  "s_waitcnt vmcnt(16)\n\t" \
  "v_lshl_or_b32 v247, v167, 16, v247\n\t" \
  "s_waitcnt vmcnt(14)\n\t" \
  "v_lshl_or_b32 v248, v168, 16, v248\n\t" \
  "s_waitcnt vmcnt(12)\n\t" \
  "v_lshl_or_b32 v249, v169, 16, v249\n\t" \
  "s_waitcnt vmcnt(10)\n\t" \
  "v_lshl_or_b32 v250, v170, 16, v250\n\t" \
  "s_waitcnt vmcnt(8)\n\t" \
  "v_lshl_or_b32 v251, v171, 16, v251\n\t" \
  "s_waitcnt vmcnt(6)\n\t" \
  "v_lshl_or_b32 v252, v172, 16, v252\n\t" \
  "s_waitcnt vmcnt(4)\n\t" \
  "v_lshl_or_b32 v253, v173, 16, v253\n\t" \
  "s_waitcnt vmcnt(2)\n\t" \
  "v_lshl_or_b32 v254, v174, 16, v254\n\t" \
  "s_waitcnt vmcnt(0)\n\t" \
  "v_lshl_or_b32 v255, v175, 16, v255\n\t" \
  "s_mov_b32 s84, " TK_CAP "\n\t" \
  "s_mov_b32 s48, 0\n\t" \
  "s_mov_b32 s49, 0xf0000000\n\t" \
  "s_mov_b32 s46, s45\n\t" \
  "v_mov_b32 v5, -1\n\t" \
  "v_lshrrev_b32 v13, 2, v0\n\t" \
  "s_mov_b32 s60, " TK_CTAB "\n\t" \
  "buffer_load_dword v10, v13, %[X], s60 offen offset:88 sc1\n\t" \
  "buffer_load_dword v13, v13, %[X], s60 offen offset:0 sc1\n\t" \
  "s_waitcnt vmcnt(0)\n\t" \
  "v_and_b32 v10, 1, v10\n\t" \
  "v_and_b32 v13, 1, v13\n\t" \
  "v_cmp_ne_u32 s[90:91], s67, v10\n\t" \
  "v_cmp_ne_u32 s[88:89], s67, v13\n\t" \
  "s_nop 1\n\t" \
  "s_mov_b32 s86, s88\n\t" \
  "v_lshrrev_b32 v13, 2, v0\n\t" \
  "s_mov_b32 s60, " TK_CTAB "\n\t" \
  "buffer_load_dword v10, v13, %[X], s60 offen offset:88 sc1\n\t" \
  "buffer_load_dword v13, v13, %[X], s60 offen offset:344 sc1\n\t" \
  "s_waitcnt vmcnt(0)\n\t" \
  "v_and_b32 v10, 1, v10\n\t" \
  "v_and_b32 v13, 1, v13\n\t" \
  "v_cmp_ne_u32 s[90:91], s67, v10\n\t" \
  "v_cmp_ne_u32 s[88:89], s67, v13\n\t" \
  "s_nop 1\n\t" \
  "s_and_b32 s60, s88, 256\n\t" \
  "s_lshl_b32 s60, s60, 5\n\t" \
  "s_add_u32 s44, s44, s60\n\t" \
  "s_add_u32 s83, s48, 0\n\t" \
  "s_lshr_b64 s[60:61], s[90:91], s83\n\t" \
  "s_and_b32 s60, s60, 1\n\t" \
  "s_lshl_b32 s60, s60, 13\n\t" \
  "s_add_u32 s83, s83, " TK_SG0 "\n\t" \
  "s_lshl_b32 s83, s83, 21\n\t" \
  "s_or_b32 s83, s83, s60\n\t" \
  "s_mov_b32 s81, s83\n\t" \
  "LCOLD_%=:\n\t" \
  "s_add_u32 s62, s83, s41\n\t" \
  "s_mov_b32 s61, 0\n\t" \
  "LPQ_%=:\n\t" \
  "buffer_load_dword v10, v9, %[X], s62 offen sc1\n\t" \
  "s_waitcnt vmcnt(0)\n\t" \
  "v_cmp_eq_u32 vcc, 0, v10\n\t" \
  "s_cbranch_vccz LPQ_%=x\n\t" \
  "s_add_u32 s61, s61, 1\n\t" \
  "s_sleep 4\n\t" \
  "s_cmp_gt_u32 s61, s84\n\t" \
  "s_cbranch_scc1 LPQ_%=g\n\t" \
  "s_and_b32 s60, s61, 1023\n\t" \
  "s_cmp_lg_u32 s60, 0\n\t" \
  "s_cbranch_scc1 LPQ_%=\n\t" \
  "buffer_load_dword v10, off, %[X], s92 sc1\n\t" \
  "s_waitcnt vmcnt(0)\n\t" \
  "v_readfirstlane_b32 s60, v10\n\t" \
  "s_nop 0\n\t" \
  "s_cmp_eq_u32 s60, 0\n\t" \
  "s_cbranch_scc1 LPQ_%=\n\t" \
  "LPQ_%=g:\n\t" \
  "s_mov_b32 s84, 0\n\t" \
  "s_or_b32 s60, s48, 1073741824\n\t" \
  "v_mov_b32 v10, s60\n\t" \
  "s_mov_b64 exec, 1\n\t" \
  "buffer_store_dword v10, off, %[X], s92 sc1\n\t" \
  "s_mov_b64 exec, -1\n\t" \
  "LPQ_%=x:\n\t" \
  "s_add_u32 m0, s46, 0\n\t" \
  "s_add_u32 s47, s81, s36\n\t" \
  "buffer_load_dwordx4 v0, %[X], s47 offen offset:0 sc1 lds\n\t" \
  "buffer_load_dwordx4 v0, %[X], s47 offen offset:1024 sc1 lds\n\t" \
  "buffer_load_dwordx4 v0, %[X], s47 offen offset:2048 sc1 lds\n\t" \
  "buffer_load_dwordx4 v0, %[X], s47 offen offset:3072 sc1 lds\n\t" \
  "s_add_u32 m0, s46, 4096\n\t" \
  "s_add_u32 s47, s81, s37\n\t" \
  "buffer_load_dwordx4 v0, %[X], s47 offen offset:0 sc1 lds\n\t" \
  "buffer_load_dwordx4 v0, %[X], s47 offen offset:1024 sc1 lds\n\t" \
  "buffer_load_dwordx4 v0, %[X], s47 offen offset:2048 sc1 lds\n\t" \
  "buffer_load_dwordx4 v0, %[X], s47 offen offset:3072 sc1 lds\n\t" \
  "s_add_u32 m0, s46, 8192\n\t" \
  "s_add_u32 s47, s81, s38\n\t" \
  "buffer_load_dwordx4 v0, %[X], s47 offen offset:0 sc1 lds\n\t" \
  "buffer_load_dwordx4 v0, %[X], s47 offen offset:1024 sc1 lds\n\t" \
  "buffer_load_dwordx4 v0, %[X], s47 offen offset:2048 sc1 lds\n\t" \
  "buffer_load_dwordx4 v0, %[X], s47 offen offset:3072 sc1 lds\n\t" \
  "s_waitcnt vmcnt(0)\n\t" \
  "LTOP_%=:\n\t" \
  "s_memrealtime s[78:79]\n\t" \
  "v_readfirstlane_b32 s76, v5\n\t" \
  "s_waitcnt lgkmcnt(0)\n\t" \
  "s_add_u32 s60, s76, 1\n\t" \
  "s_not_b32 s76, s76\n\t" \
  "s_cmp_gt_u32 s60, 1\n\t" \
  "s_cselect_b32 s61, -1, 0\n\t" \
  "s_lshl_b32 s60, s78, 8\n\t" \
  "s_sub_u32 s76, s76, s60\n\t" \
  "s_ashr_i32 s76, s76, 8\n\t" \
  "s_add_i32 s76, s76, s77\n\t" \
  "s_min_i32 s76, s76, 400\n\t" \
  "s_and_b32 s76, s76, s61\n\t" \
  "s_add_u32 s57, s78, s76\n\t" \
  "s_barrier\n\t" \
  "ds_read_b128 v[48:51], v2 offset:0\n\t" \
  "ds_read_b128 v[64:67], v1 offset:8192\n\t" \
  "ds_read_b128 v[80:83], v1 offset:12288\n\t" \
  "ds_read_b128 v[52:55], v2 offset:1024\n\t" \
  "ds_read_b128 v[68:71], v1 offset:9216\n\t" \
  "ds_read_b128 v[84:87], v1 offset:13312\n\t" \
  "ds_read_b128 v[56:59], v2 offset:2048\n\t" \
  "ds_read_b128 v[72:75], v1 offset:10240\n\t" \
  "ds_read_b128 v[88:91], v1 offset:14336\n\t" \
  "ds_read_b128 v[60:63], v2 offset:3072\n\t" \
  "ds_read_b128 v[76:79], v1 offset:11264\n\t" \
  "ds_read_b128 v[92:95], v1 offset:15360\n\t" \
  "s_mov_b32 s82, s83\n\t" \
  "s_xor_b32 s46, s46, 0x10000\n\t" \
  "s_add_u32 s58, s82, s41\n\t" \
  "s_add_u32 s58, s58, 4096\n\t" \
  "s_add_u32 s59, s81, s39\n\t" \
  "s_cmp_gt_i32 s76, 0\n\t" \
  "s_cselect_b32 s62, 0xf0000000, s58\n\t" \
  "buffer_load_dword v5, off, %[X], s62 sc1\n\t" \
  "s_cmp_gt_i32 s76, " TK_AM "\n\t" \
  "s_cselect_b32 s79, 0xf0000000, s58\n\t" \
  "s_waitcnt lgkmcnt(10)\n\t" \
  "v_mfma_f32_16x16x32_bf16 v[36:39], v[64:67], v[48:51], v[16:19]\n\t" \
  "ds_read_b128 v[96:99], v1 offset:16384\n\t" \
  "s_waitcnt lgkmcnt(10)\n\t" \
  "v_mfma_f32_16x16x32_bf16 v[40:43], v[80:83], v[48:51], v[20:23]\n\t" \
  "ds_read_b128 v[100:103], v1 offset:17408\n\t" \
  "s_waitcnt lgkmcnt(9)\n\t" \
  "v_mfma_f32_16x16x32_bf16 v[36:39], v[68:71], v[52:55], v[36:39]\n\t" \
  "ds_read_b128 v[104:107], v1 offset:18432\n\t" \
  "s_waitcnt lgkmcnt(9)\n\t" \
  "v_mfma_f32_16x16x32_bf16 v[40:43], v[84:87], v[52:55], v[40:43]\n\t" \
  "ds_read_b128 v[108:111], v1 offset:19456\n\t" \
  "s_waitcnt lgkmcnt(8)\n\t" \
  "v_mfma_f32_16x16x32_bf16 v[36:39], v[72:75], v[56:59], v[36:39]\n\t" \
  "ds_read_b128 v[112:115], v1 offset:20480\n\t" \
  "s_waitcnt lgkmcnt(8)\n\t" \
  "v_mfma_f32_16x16x32_bf16 v[40:43], v[88:91], v[56:59], v[40:43]\n\t" \
  "ds_read_b128 v[116:119], v1 offset:21504\n\t" \
  "s_waitcnt lgkmcnt(7)\n\t" \
  "v_mfma_f32_16x16x32_bf16 v[36:39], v[76:79], v[60:63], v[36:39]\n\t" \
  "ds_read_b128 v[120:123], v1 offset:22528\n\t" \
  "s_waitcnt lgkmcnt(7)\n\t" \
  "v_mfma_f32_16x16x32_bf16 v[40:43], v[92:95], v[60:63], v[40:43]\n\t" \
  "ds_read_b128 v[124:127], v1 offset:23552\n\t" \
  "s_add_u32 s83, s48, 1\n\t" \
  "s_lshr_b64 s[60:61], s[90:91], s83\n\t" \
  "s_and_b32 s60, s60, 1\n\t" \
  "s_lshl_b32 s60, s60, 13\n\t" \
  "s_add_u32 s83, s83, " TK_SG0 "\n\t" \
  "s_lshl_b32 s83, s83, 21\n\t" \
  "s_or_b32 s83, s83, s60\n\t" \
  "s_add_u32 s56, s48, 1\n\t" \
  "s_and_b32 s56, s56, " TK_TRM "\n\t" \
  "s_lshr_b64 s[60:61], s[90:91], s56\n\t" \
  "s_and_b32 s60, s60, 1\n\t" \
  "s_lshl_b32 s60, s60, 13\n\t" \
  "s_add_u32 s56, s56, " TK_SG0 "\n\t" \
  "s_lshl_b32 s56, s56, 21\n\t" \
  "s_or_b32 s56, s56, s60\n\t" \
  "v_xor_b32 v1, 0x10000, v1\n\t" \
  "v_xor_b32 v2, 0x10000, v2\n\t" \
  "v_max3_f32 v11, v36, v37, v38\n\t" \
  "v_max3_f32 v11, v11, v39, v40\n\t" \
  "v_max3_f32 v13, v41, v42, v43\n\t" \
  "v_max_f32 v11, v11, v13\n\t" \
  "v_mov_b32 v13, v11\n\t" \
  "s_nop 1\n\t" \
  "v_permlane16_swap_b32 v11, v13\n\t" \
  "v_max_f32 v11, v11, v13\n\t" \
  "v_mov_b32 v13, v11\n\t" \
  "s_nop 1\n\t" \
  "v_permlane32_swap_b32 v11, v13\n\t" \
  "v_max_f32 v11, v11, v13\n\t" \
  "v_mul_f32 v13, 0xbe0293ee, v11\n\t" \
  "v_fmamk_f32 v36, v36, 0x3e0293ee, v13\n\t" \
  "v_fmamk_f32 v37, v37, 0x3e0293ee, v13\n\t" \
  "v_fmamk_f32 v38, v38, 0x3e0293ee, v13\n\t" \
  "v_fmamk_f32 v39, v39, 0x3e0293ee, v13\n\t" \
  "v_fmamk_f32 v40, v40, 0x3e0293ee, v13\n\t" \
  "v_fmamk_f32 v41, v41, 0x3e0293ee, v13\n\t" \
  "v_fmamk_f32 v42, v42, 0x3e0293ee, v13\n\t" \
  "v_fmamk_f32 v43, v43, 0x3e0293ee, v13\n\t" \
  "s_waitcnt vmcnt(0)\n\t" \
  "v_readfirstlane_b32 s76, v5\n\t" \
  "s_barrier\n\t" \
  "s_cmp_eq_u32 s76, 0\n\t" \
  "s_cbranch_scc1 LNG_%=\n\t" \
  "buffer_load_dwordx4 v[196:199], v0, %[X], s59 offen offset:0 sc1\n\t" \
  "buffer_load_dwordx4 v[200:203], v0, %[X], s59 offen offset:1024 sc1\n\t" \
  "buffer_load_dwordx2 v[204:205], v6, %[X], s59 offen offset:2048 sc1\n\t" \
  "buffer_load_dwordx2 v[206:207], v6, %[X], s59 offen offset:2560 sc1\n\t" \
  "buffer_load_dwordx2 v[208:209], v6, %[X], s59 offen offset:3072 sc1\n\t" \
  "buffer_load_dwordx2 v[210:211], v6, %[X], s59 offen offset:3584 sc1\n\t" \
  "LNG_%=:\n\t" \
  "buffer_load_dword v5, off, %[X], s79 sc1\n\t" \
  "s_mov_b64 exec, 1\n\t" \
  "buffer_store_dword v3, off, %[X], s49 sc1\n\t" \
  "s_mov_b64 exec, -1\n\t" \
  "s_mov_b32 s49, 0xf0000000\n\t" \
  "v_exp_f32 v36, v36\n\t" \
  "v_exp_f32 v37, v37\n\t" \
  "v_exp_f32 v38, v38\n\t" \
  "v_exp_f32 v39, v39\n\t" \
  "v_exp_f32 v40, v40\n\t" \
  "v_exp_f32 v41, v41\n\t" \
  "v_exp_f32 v42, v42\n\t" \
  "v_exp_f32 v43, v43\n\t" \
  "v_cvt_pk_bf16_f32 v44, v36, v37\n\t" \
  "v_cvt_pk_bf16_f32 v45, v38, v39\n\t" \
  "v_cvt_pk_bf16_f32 v46, v40, v41\n\t" \
  "v_cvt_pk_bf16_f32 v47, v42, v43\n\t" \
  "v_add_f32 v36, v36, v37\n\t" \
  "v_add_f32 v38, v38, v39\n\t" \
  "s_waitcnt lgkmcnt(7)\n\t" \
  "v_mfma_f32_16x16x32_bf16 v[128:131], v[96:99], v[44:47], 0\n\t" \
  "v_add_f32 v40, v40, v41\n\t" \
  "s_and_b32 s62, s48, 15\n\t" \
  "s_lshr_b32 s63, s62, 1\n\t" \
  "s_waitcnt lgkmcnt(6)\n\t" \
  "v_mfma_f32_16x16x32_bf16 v[132:135], v[100:103], v[44:47], 0\n\t" \
  "v_add_f32 v42, v42, v43\n\t" \
  "s_lshr_b32 s60, s88, s63\n\t" \
  "s_and_b32 s60, s60, 1\n\t" \
  "s_waitcnt lgkmcnt(5)\n\t" \
  "v_mfma_f32_16x16x32_bf16 v[136:139], v[104:107], v[44:47], 0\n\t" \
  "v_add_f32 v36, v36, v38\n\t" \
  "s_lshl_b32 s60, s60, 13\n\t" \
  "s_lshl_b32 s61, s62, 20\n\t" \
  "s_waitcnt lgkmcnt(4)\n\t" \
  "v_mfma_f32_16x16x32_bf16 v[140:143], v[108:111], v[44:47], 0\n\t" \
  "v_add_f32 v40, v40, v42\n\t" \
  "s_add_u32 s50, s61, s60\n\t" \
  "s_add_u32 s50, s50, s43\n\t" \
  "s_waitcnt lgkmcnt(3)\n\t" \
  "v_mfma_f32_16x16x32_bf16 v[144:147], v[112:115], v[44:47], 0\n\t" \
  "v_add_f32 v12, v36, v40\n\t" \
  "s_add_u32 s51, s50, 32768\n\t" \
  "s_add_u32 s52, s51, 32768\n\t" \
  "s_waitcnt lgkmcnt(2)\n\t" \
  "v_mfma_f32_16x16x32_bf16 v[148:151], v[116:119], v[44:47], 0\n\t" \
  "s_add_u32 s53, s52, 32768\n\t" \
  "s_lshl_b32 s61, s62, 17\n\t" \
  "s_waitcnt lgkmcnt(1)\n\t" \
  "v_mfma_f32_16x16x32_bf16 v[152:155], v[120:123], v[44:47], 0\n\t" \
  "s_add_u32 s54, s61, s44\n\t" \
  "s_add_u32 s63, s83, s41\n\t" \
  "s_waitcnt lgkmcnt(0)\n\t" \
  "v_mfma_f32_16x16x32_bf16 v[156:159], v[124:127], v[44:47], 0\n\t" \
  "s_cmp_lg_u32 s76, 0\n\t" \
  "s_cbranch_scc1 LGOT_%=\n\t" \
  "s_waitcnt vmcnt(1)\n\t" \
  "v_readfirstlane_b32 s60, v5\n\t" \
  "s_nop 0\n\t" \
  "s_cmp_lg_u32 s60, 0\n\t" \
  "s_cbranch_scc1 LPK_%=x\n\t" \
  "LKW_%=:\n\t" \
  "s_memrealtime s[78:79]\n\t" \
  "s_waitcnt lgkmcnt(0)\n\t" \
  "s_sub_i32 s79, s78, s57\n\t" \
  "s_cmp_lt_i32 s79, 0\n\t" \
  "s_cbranch_scc1 LKW_%=\n\t" \
  "s_mov_b32 s61, 0\n\t" \
  "LPK_%=:\n\t" \
  "buffer_load_dword v5, off, %[X], s58 sc1\n\t" \
  "s_waitcnt vmcnt(0)\n\t" \
  "v_cmp_eq_u32 vcc, 0, v5\n\t" \
  "s_cbranch_vccz LPK_%=x\n\t" \
  "s_add_u32 s61, s61, 1\n\t" \
  "s_sleep " TK_PSK "\n\t" \
  "s_cmp_gt_u32 s61, s84\n\t" \
  "s_cbranch_scc1 LPK_%=g\n\t" \
  "s_and_b32 s60, s61, 1023\n\t" \
  "s_cmp_lg_u32 s60, 0\n\t" \
  "s_cbranch_scc1 LPK_%=\n\t" \
  "buffer_load_dword v5, off, %[X], s92 sc1\n\t" \
  "s_waitcnt vmcnt(0)\n\t" \
  "v_readfirstlane_b32 s60, v5\n\t" \
  "s_nop 0\n\t" \
  "s_cmp_eq_u32 s60, 0\n\t" \
  "s_cbranch_scc1 LPK_%=\n\t" \
  "LPK_%=g:\n\t" \
  "s_mov_b32 s84, 0\n\t" \
  "s_or_b32 s60, s48, 1342177280\n\t" \
  "v_mov_b32 v5, s60\n\t" \
  "s_mov_b64 exec, 1\n\t" \
  "buffer_store_dword v5, off, %[X], s92 sc1\n\t" \
  "s_mov_b64 exec, -1\n\t" \
  "LPK_%=x:\n\t" \
  "buffer_load_dwordx4 v[196:199], v0, %[X], s59 offen offset:0 sc1\n\t" \
  "buffer_load_dwordx4 v[200:203], v0, %[X], s59 offen offset:1024 sc1\n\t" \
  "buffer_load_dwordx2 v[204:205], v6, %[X], s59 offen offset:2048 sc1\n\t" \
  "buffer_load_dwordx2 v[206:207], v6, %[X], s59 offen offset:2560 sc1\n\t" \
  "buffer_load_dwordx2 v[208:209], v6, %[X], s59 offen offset:3072 sc1\n\t" \
  "buffer_load_dwordx2 v[210:211], v6, %[X], s59 offen offset:3584 sc1\n\t" \
  "LGOT_%=:\n\t" \
  "buffer_load_dword v10, v9, %[X], s63 offen sc1\n\t" \
  "s_add_u32 s49, s82, s42\n\t" \
  "s_waitcnt vmcnt(6)\n\t" \
  "v_mfma_f32_16x16x32_bf16 v[36:39], v[196:199], v[48:51], 0\n\t" \
  "v_mfma_f32_16x16x32_bf16 v[40:43], v[196:199], v[52:55], 0\n\t" \
  "s_waitcnt vmcnt(5)\n\t" \
  "v_mfma_f32_16x16x32_bf16 v[36:39], v[200:203], v[56:59], v[36:39]\n\t" \
  "v_mfma_f32_16x16x32_bf16 v[40:43], v[200:203], v[60:63], v[40:43]\n\t" \
  "s_nop 8\n\t" \
  "v_add_f32 v36, v36, v42\n\t" \
  "v_add_f32 v37, v37, v43\n\t" \
  "v_max3_f32 v14, v36, v37, v11\n\t" \
  "v_mov_b32 v13, v14\n\t" \
  "s_nop 1\n\t" \
  "v_permlane16_swap_b32 v14, v13\n\t" \
  "v_max_f32 v14, v14, v13\n\t" \
  "v_mov_b32 v13, v14\n\t" \
  "s_nop 1\n\t" \
  "v_permlane32_swap_b32 v14, v13\n\t" \
  "v_max_f32 v14, v14, v13\n\t" \
  "v_mul_f32 v14, 0xbe0293ee, v14\n\t" \
  "v_fmamk_f32 v11, v11, 0x3e0293ee, v14\n\t" \
  "v_fmamk_f32 v36, v36, 0x3e0293ee, v14\n\t" \
  "v_fmamk_f32 v37, v37, 0x3e0293ee, v14\n\t" \
  "v_exp_f32 v11, v11\n\t" \
  "v_exp_f32 v36, v36\n\t" \
  "v_exp_f32 v37, v37\n\t" \
  "v_fma_f32 v12, v12, v11, v36\n\t" \
  "v_add_f32 v12, v12, v37\n\t" \
  "v_mov_b32 v13, v12\n\t" \
  "s_nop 1\n\t" \
  "v_permlane16_swap_b32 v12, v13\n\t" \
  "v_add_f32 v12, v12, v13\n\t" \
  "v_mov_b32 v13, v12\n\t" \
  "s_nop 1\n\t" \
  "v_permlane32_swap_b32 v12, v13\n\t" \
  "v_add_f32 v12, v12, v13\n\t" \
  "v_rcp_f32 v12, v12\n\t" \
  "s_nop 0\n\t" \
  "v_mul_f32 v14, v11, v12\n\t" \
  "v_mul_f32 v36, v36, v12\n\t" \
  "v_mul_f32 v37, v37, v12\n\t" \
  "v_cvt_pk_bf16_f32 v26, v36, v37\n\t" \
  "v_cvt_pk_bf16_f32 v33, v36, v37\n\t" \
  "v_pk_mul_f32 v[128:129], v[128:129], v[14:15] op_sel_hi:[1,0]\n\t" \
  "v_pk_mul_f32 v[130:131], v[130:131], v[14:15] op_sel_hi:[1,0]\n\t" \
  "v_pk_mul_f32 v[132:133], v[132:133], v[14:15] op_sel_hi:[1,0]\n\t" \
  "v_pk_mul_f32 v[134:135], v[134:135], v[14:15] op_sel_hi:[1,0]\n\t" \
  "v_pk_mul_f32 v[136:137], v[136:137], v[14:15] op_sel_hi:[1,0]\n\t" \
  "v_pk_mul_f32 v[138:139], v[138:139], v[14:15] op_sel_hi:[1,0]\n\t" \
  "v_pk_mul_f32 v[140:141], v[140:141], v[14:15] op_sel_hi:[1,0]\n\t" \
  "v_pk_mul_f32 v[142:143], v[142:143], v[14:15] op_sel_hi:[1,0]\n\t" \
  "s_waitcnt vmcnt(3)\n\t" \
  "v_mfma_f32_16x16x32_bf16 v[128:131], v[204:207], v[26:29], v[128:131]\n\t" \
  "v_mul_f32 v144, v144, v14\n\t" \
  "v_mul_f32 v145, v145, v14\n\t" \
  "v_mfma_f32_16x16x32_bf16 v[132:135], v[204:207], v[32:35], v[132:135]\n\t" \
  "v_mul_f32 v146, v146, v14\n\t" \
  "v_mul_f32 v147, v147, v14\n\t" \
  "v_mfma_f32_16x16x32_bf16 v[136:139], v[204:207], v[24:27], v[136:139]\n\t" \
  "v_mul_f32 v148, v148, v14\n\t" \
  "v_mul_f32 v149, v149, v14\n\t" \
  "v_mfma_f32_16x16x32_bf16 v[140:143], v[204:207], v[30:33], v[140:143]\n\t" \
  "v_cvt_pk_bf16_f32 v64, v128, v129\n\t" \
  "v_cvt_pk_bf16_f32 v65, v130, v131\n\t" \
  "v_cvt_pk_bf16_f32 v66, v132, v133\n\t" \
  "v_cvt_pk_bf16_f32 v67, v134, v135\n\t" \
  "v_mul_f32 v150, v150, v14\n\t" \
  "v_mul_f32 v151, v151, v14\n\t" \
  "v_mfma_f32_16x16x32_bf16 v[192:195], v[224:227], v[64:67], 0\n\t" \
  "ds_write_b128 v3, v[64:67] offset:0\n\t" \
  "v_cvt_pk_bf16_f32 v68, v136, v137\n\t" \
  "v_cvt_pk_bf16_f32 v69, v138, v139\n\t" \
  "v_mfma_f32_16x16x32_bf16 v[160:163], v[64:67], a[0:3], 0\n\t" \
  "v_cvt_pk_bf16_f32 v70, v140, v141\n\t" \
  "v_cvt_pk_bf16_f32 v71, v142, v143\n\t" \
  "s_waitcnt vmcnt(1)\n\t" \
  "v_mfma_f32_16x16x32_bf16 v[144:147], v[208:211], v[26:29], v[144:147]\n\t" \
  "v_mul_f32 v152, v152, v14\n\t" \
  "v_mul_f32 v153, v153, v14\n\t" \
  "v_mfma_f32_16x16x32_bf16 v[148:151], v[208:211], v[32:35], v[148:151]\n\t" \
  "v_mul_f32 v154, v154, v14\n\t" \
  "v_mul_f32 v155, v155, v14\n\t" \
  "v_mfma_f32_16x16x32_bf16 v[164:167], v[64:67], a[4:7], 0\n\t" \
  "ds_write_b128 v3, v[68:71] offset:1024\n\t" \
  "v_mul_f32 v156, v156, v14\n\t" \
  "v_mul_f32 v157, v157, v14\n\t" \
  "v_mfma_f32_16x16x32_bf16 v[168:171], v[64:67], a[8:11], 0\n\t" \
  "v_mul_f32 v158, v158, v14\n\t" \
  "v_mul_f32 v159, v159, v14\n\t" \
  "v_mfma_f32_16x16x32_bf16 v[172:175], v[64:67], a[12:15], 0\n\t" \
  "v_cvt_pk_bf16_f32 v72, v144, v145\n\t" \
  "v_cvt_pk_bf16_f32 v73, v146, v147\n\t" \
  "v_mfma_f32_16x16x32_bf16 v[152:155], v[208:211], v[24:27], v[152:155]\n\t" \
  "v_cvt_pk_bf16_f32 v74, v148, v149\n\t" \
  "v_cvt_pk_bf16_f32 v75, v150, v151\n\t" \
  "v_mfma_f32_16x16x32_bf16 v[156:159], v[208:211], v[30:33], v[156:159]\n\t" \
  "v_mfma_f32_16x16x32_bf16 v[176:179], v[64:67], a[16:19], 0\n\t" \
  "ds_write_b128 v3, v[72:75] offset:2048\n\t" \
  "v_mfma_f32_16x16x32_bf16 v[180:183], v[64:67], a[20:23], 0\n\t" \
  "v_mfma_f32_16x16x32_bf16 v[184:187], v[64:67], a[24:27], 0\n\t" \
  "v_cvt_pk_bf16_f32 v76, v152, v153\n\t" \
  "v_cvt_pk_bf16_f32 v77, v154, v155\n\t" \
  "v_mfma_f32_16x16x32_bf16 v[188:191], v[64:67], a[28:31], 0\n\t" \
  "v_cvt_pk_bf16_f32 v78, v156, v157\n\t" \
  "v_cvt_pk_bf16_f32 v79, v158, v159\n\t" \
  "v_mfma_f32_16x16x32_bf16 v[192:195], v[228:231], v[68:71], v[192:195]\n\t" \
  "ds_write_b128 v3, v[76:79] offset:3072\n\t" \
  "v_mfma_f32_16x16x32_bf16 v[160:163], v[68:71], a[32:35], v[160:163]\n\t" \
  "s_waitcnt vmcnt(0)\n\t" \
  "v_cmp_eq_u32 vcc, 0, v10\n\t" \
  "v_mfma_f32_16x16x32_bf16 v[164:167], v[68:71], a[36:39], v[164:167]\n\t" \
  "s_cmp_eq_u64 vcc, 0\n\t" \
  "s_cselect_b32 s81, s56, 0xf0000000\n\t" \
  "s_cmp_eq_u32 s48, 63\n\t" \
  "v_mfma_f32_16x16x32_bf16 v[168:171], v[68:71], a[40:43], v[168:171]\n\t" \
  "s_cselect_b32 s81, 0xf0000000, s81\n\t" \
  "v_mfma_f32_16x16x32_bf16 v[172:175], v[68:71], a[44:47], v[172:175]\n\t" \
  "v_mfma_f32_16x16x32_bf16 v[176:179], v[68:71], a[48:51], v[176:179]\n\t" \
  "s_add_u32 m0, s46, 0\n\t" \
  "s_add_u32 s47, s81, s36\n\t" \
  "buffer_load_dwordx4 v0, %[X], s47 offen offset:0 sc1 lds\n\t" \
  "v_mfma_f32_16x16x32_bf16 v[180:183], v[68:71], a[52:55], v[180:183]\n\t" \
  "v_mfma_f32_16x16x32_bf16 v[184:187], v[68:71], a[56:59], v[184:187]\n\t" \
  "s_waitcnt lgkmcnt(0)\n\t" \
  "s_barrier\n\t" \
  "v_mfma_f32_16x16x32_bf16 v[188:191], v[68:71], a[60:63], v[188:191]\n\t" \
  "ds_read_b128 v[80:83], v4 offset:0\n\t" \
  "v_mfma_f32_16x16x32_bf16 v[192:195], v[232:235], v[72:75], v[192:195]\n\t" \
  "buffer_load_dwordx4 v0, %[X], s47 offen offset:1024 sc1 lds\n\t" \
  "v_mfma_f32_16x16x32_bf16 v[160:163], v[72:75], a[64:67], v[160:163]\n\t" \
  "ds_read_b128 v[84:87], v4 offset:1024\n\t" \
  "v_mfma_f32_16x16x32_bf16 v[164:167], v[72:75], a[68:71], v[164:167]\n\t" \
  "v_mfma_f32_16x16x32_bf16 v[168:171], v[72:75], a[72:75], v[168:171]\n\t" \
  "ds_read_b128 v[88:91], v4 offset:2048\n\t" \
  "v_mfma_f32_16x16x32_bf16 v[172:175], v[72:75], a[76:79], v[172:175]\n\t" \
  "buffer_load_dwordx4 v0, %[X], s47 offen offset:2048 sc1 lds\n\t" \
  "v_mfma_f32_16x16x32_bf16 v[176:179], v[72:75], a[80:83], v[176:179]\n\t" \
  "ds_read_b128 v[92:95], v4 offset:3072\n\t" \
  "v_mfma_f32_16x16x32_bf16 v[180:183], v[72:75], a[84:87], v[180:183]\n\t" \
  "v_mfma_f32_16x16x32_bf16 v[184:187], v[72:75], a[88:91], v[184:187]\n\t" \
  "v_mfma_f32_16x16x32_bf16 v[188:191], v[72:75], a[92:95], v[188:191]\n\t" \
  "buffer_load_dwordx4 v0, %[X], s47 offen offset:3072 sc1 lds\n\t" \
  "v_mfma_f32_16x16x32_bf16 v[192:195], v[236:239], v[76:79], v[192:195]\n\t" \
  "s_add_u32 s62, s48, 2\n\t" \
  "v_mfma_f32_16x16x32_bf16 v[160:163], v[76:79], a[96:99], v[160:163]\n\t" \
  "v_mfma_f32_16x16x32_bf16 v[164:167], v[76:79], a[100:103], v[164:167]\n\t" \
  "s_mul_i32 s63, s62, 43\n\t" \
  "s_lshr_b32 s63, s63, 9\n\t" \
  "v_mfma_f32_16x16x32_bf16 v[168:171], v[76:79], a[104:107], v[168:171]\n\t" \
  "s_add_u32 m0, s46, 4096\n\t" \
  "s_add_u32 s47, s81, s37\n\t" \
  "buffer_load_dwordx4 v0, %[X], s47 offen offset:0 sc1 lds\n\t" \
  "v_mfma_f32_16x16x32_bf16 v[172:175], v[76:79], a[108:111], v[172:175]\n\t" \
  "s_mul_i32 s63, s63, " TK_RM "\n\t" \
  "s_sub_u32 s62, s62, s63\n\t" \
  "v_mfma_f32_16x16x32_bf16 v[176:179], v[76:79], a[112:115], v[176:179]\n\t" \
  "v_mfma_f32_16x16x32_bf16 v[180:183], v[76:79], a[116:119], v[180:183]\n\t" \
  "s_mul_i32 s63, s62, 43\n\t" \
  "s_lshr_b32 s63, s63, 7\n\t" \
  "v_mfma_f32_16x16x32_bf16 v[184:187], v[76:79], a[120:123], v[184:187]\n\t" \
  "buffer_load_dwordx4 v0, %[X], s47 offen offset:1024 sc1 lds\n\t" \
  "v_mfma_f32_16x16x32_bf16 v[188:191], v[76:79], a[124:127], v[188:191]\n\t" \
  "s_mul_i32 s62, s62, 688128\n\t" \
  "s_lshl_b32 s60, s63, 15\n\t" \
  "s_waitcnt lgkmcnt(3)\n\t" \
  "v_mfma_f32_16x16x32_bf16 v[192:195], v[240:243], v[80:83], v[192:195]\n\t" \
  "v_mfma_f32_16x16x32_bf16 v[160:163], v[80:83], a[128:131], v[160:163]\n\t" \
  "s_add_u32 s62, s62, s60\n\t" \
  "s_lshr_b32 s60, s86, s63\n\t" \
  "v_mfma_f32_16x16x32_bf16 v[164:167], v[80:83], a[132:135], v[164:167]\n\t" \
  "buffer_load_dwordx4 v0, %[X], s47 offen offset:2048 sc1 lds\n\t" \
  "v_mfma_f32_16x16x32_bf16 v[168:171], v[80:83], a[136:139], v[168:171]\n\t" \
  "s_and_b32 s60, s60, 1\n\t" \
  "s_lshl_b32 s60, s60, 13\n\t" \
  "v_mfma_f32_16x16x32_bf16 v[172:175], v[80:83], a[140:143], v[172:175]\n\t" \
  "v_mfma_f32_16x16x32_bf16 v[176:179], v[80:83], a[144:147], v[176:179]\n\t" \
  "s_add_u32 s62, s62, s60\n\t" \
  "s_add_u32 s62, s62, 656896\n\t" \
  "v_mfma_f32_16x16x32_bf16 v[180:183], v[80:83], a[148:151], v[180:183]\n\t" \
  "buffer_load_dwordx4 v0, %[X], s47 offen offset:3072 sc1 lds\n\t" \
  "v_mfma_f32_16x16x32_bf16 v[184:187], v[80:83], a[152:155], v[184:187]\n\t" \
  "s_cmp_gt_u32 s48, " TK_AE "\n\t" \
  "s_cselect_b32 s62, 0xf0000000, s62\n\t" \
  "v_mfma_f32_16x16x32_bf16 v[188:191], v[80:83], a[156:159], v[188:191]\n\t" \
  "s_waitcnt lgkmcnt(2)\n\t" \
  "v_mfma_f32_16x16x32_bf16 v[192:195], v[244:247], v[84:87], v[192:195]\n\t" \
  "v_mfma_f32_16x16x32_bf16 v[160:163], v[84:87], a[160:163], v[160:163]\n\t" \
  "s_add_u32 m0, s46, 8192\n\t" \
  "s_add_u32 s47, s81, s38\n\t" \
  "buffer_load_dwordx4 v0, %[X], s47 offen offset:0 sc1 lds\n\t" \
  "v_mfma_f32_16x16x32_bf16 v[164:167], v[84:87], a[164:167], v[164:167]\n\t" \
  "v_mfma_f32_16x16x32_bf16 v[168:171], v[84:87], a[168:171], v[168:171]\n\t" \
  "v_mfma_f32_16x16x32_bf16 v[172:175], v[84:87], a[172:175], v[172:175]\n\t" \
  "v_mfma_f32_16x16x32_bf16 v[176:179], v[84:87], a[176:179], v[176:179]\n\t" \
  "buffer_load_dwordx4 v0, %[X], s47 offen offset:1024 sc1 lds\n\t" \
  "v_mfma_f32_16x16x32_bf16 v[180:183], v[84:87], a[180:183], v[180:183]\n\t" \
  "v_mfma_f32_16x16x32_bf16 v[184:187], v[84:87], a[184:187], v[184:187]\n\t" \
  "v_mfma_f32_16x16x32_bf16 v[188:191], v[84:87], a[188:191], v[188:191]\n\t" \
  "s_waitcnt lgkmcnt(1)\n\t" \
  "v_mfma_f32_16x16x32_bf16 v[192:195], v[248:251], v[88:91], v[192:195]\n\t" \
  "buffer_load_dwordx4 v0, %[X], s47 offen offset:2048 sc1 lds\n\t" \
  "v_mfma_f32_16x16x32_bf16 v[160:163], v[88:91], a[192:195], v[160:163]\n\t" \
  "v_mfma_f32_16x16x32_bf16 v[164:167], v[88:91], a[196:199], v[164:167]\n\t" \
  "v_mfma_f32_16x16x32_bf16 v[168:171], v[88:91], a[200:203], v[168:171]\n\t" \
  "v_mfma_f32_16x16x32_bf16 v[172:175], v[88:91], a[204:207], v[172:175]\n\t" \
  "buffer_load_dwordx4 v0, %[X], s47 offen offset:3072 sc1 lds\n\t" \
  "v_mfma_f32_16x16x32_bf16 v[176:179], v[88:91], a[208:211], v[176:179]\n\t" \
  "v_mfma_f32_16x16x32_bf16 v[180:183], v[88:91], a[212:215], v[180:183]\n\t" \
  "buffer_load_dword v5, off, %[X], s62 sc1\n\t" \
  "v_mfma_f32_16x16x32_bf16 v[184:187], v[88:91], a[216:219], v[184:187]\n\t" \
  "v_mfma_f32_16x16x32_bf16 v[188:191], v[88:91], a[220:223], v[188:191]\n\t" \
  "s_waitcnt lgkmcnt(0)\n\t" \
  "v_mfma_f32_16x16x32_bf16 v[192:195], v[252:255], v[92:95], v[192:195]\n\t" \
  "v_mfma_f32_16x16x32_bf16 v[160:163], v[92:95], a[224:227], v[160:163]\n\t" \
  "v_mfma_f32_16x16x32_bf16 v[164:167], v[92:95], a[228:231], v[164:167]\n\t" \
  "v_mfma_f32_16x16x32_bf16 v[168:171], v[92:95], a[232:235], v[168:171]\n\t" \
  "v_cvt_pk_bf16_f32 v112, v192, v193\n\t" \
  "v_cvt_pk_bf16_f32 v113, v194, v195\n\t" \
  "v_mfma_f32_16x16x32_bf16 v[172:175], v[92:95], a[236:239], v[172:175]\n\t" \
  "buffer_store_dwordx2 v[112:113], v8, %[X], s54 offen sc1\n\t" \
  "v_mfma_f32_16x16x32_bf16 v[176:179], v[92:95], a[240:243], v[176:179]\n\t" \
  "v_cvt_pk_bf16_f32 v96, v160, v164\n\t" \
  "v_cvt_pk_bf16_f32 v100, v161, v165\n\t" \
  "v_mfma_f32_16x16x32_bf16 v[180:183], v[92:95], a[244:247], v[180:183]\n\t" \
  "v_cvt_pk_bf16_f32 v104, v162, v166\n\t" \
  "v_cvt_pk_bf16_f32 v108, v163, v167\n\t" \
  "v_mfma_f32_16x16x32_bf16 v[184:187], v[92:95], a[248:251], v[184:187]\n\t" \
  "v_cvt_pk_bf16_f32 v97, v168, v172\n\t" \
  "v_cvt_pk_bf16_f32 v101, v169, v173\n\t" \
  "v_mfma_f32_16x16x32_bf16 v[188:191], v[92:95], a[252:255], v[188:191]\n\t" \
  "v_cvt_pk_bf16_f32 v105, v170, v174\n\t" \
  "v_cvt_pk_bf16_f32 v109, v171, v175\n\t" \
  "v_cvt_pk_bf16_f32 v98, v176, v180\n\t" \
  "v_cvt_pk_bf16_f32 v102, v177, v181\n\t" \
  "v_cvt_pk_bf16_f32 v106, v178, v182\n\t" \
  "v_cvt_pk_bf16_f32 v110, v179, v183\n\t" \
  "s_nop 1\n\t" \
  "v_cvt_pk_bf16_f32 v99, v184, v188\n\t" \
  "buffer_store_dwordx4 v[96:99], v7, %[X], s50 offen sc1\n\t" \
  "v_cvt_pk_bf16_f32 v103, v185, v189\n\t" \
  "buffer_store_dwordx4 v[100:103], v7, %[X], s51 offen sc1\n\t" \
  "v_cvt_pk_bf16_f32 v107, v186, v190\n\t" \
  "buffer_store_dwordx4 v[104:107], v7, %[X], s52 offen sc1\n\t" \
  "v_cvt_pk_bf16_f32 v111, v187, v191\n\t" \
  "buffer_store_dwordx4 v[108:111], v7, %[X], s53 offen sc1\n\t" \
  "s_cmp_eq_u32 s48, 63\n\t" \
  "s_cbranch_scc1 LEND_%=\n\t" \
  "s_add_u32 s48, s48, 1\n\t" \
  "s_cmp_eq_u32 s81, s56\n\t" \
  "s_mov_b32 s81, s56\n\t" \
  "s_cbranch_scc0 LCOLD_%=\n\t" \
  "s_waitcnt vmcnt(5)\n\t" \
  "s_branch LTOP_%=\n\t" \
  "LEND_%=:\n\t" \
  "s_waitcnt vmcnt(0)\n\t" \
  "s_barrier\n\t" \
  "s_mov_b64 exec, 1\n\t" \
  "buffer_store_dword v3, off, %[X], s49 sc1\n\t" \
  "s_mov_b64 exec, -1\n\t" \
  "s_mov_b32 s49, 0xf0000000\n\t" \
  ""
#define QK_ASM \
  "v_lshrrev_b32 v16, 7, v0\n\t" \
  "v_lshlrev_b32 v16, 13, v16\n\t" \
  "v_lshrrev_b32 v19, 2, v0\n\t" \
  "v_and_b32 v19, 64, v19\n\t" \
  "v_xor_b32 v19, v19, v0\n\t" \
  "v_and_b32 v19, 0x70, v19\n\t" \
  "v_add_u32 v16, v16, v19\n\t" \
  "s_mul_i32 s63, s40, 40960\n\t" \
  "v_and_b32 v19, 0x300, v0\n\t" \
  "v_lshlrev_b32 v17, 2, v19\n\t" \
  "v_and_b32 v19, 0xc0, v0\n\t" \
  "v_lshl_add_u32 v17, v19, 1, v17\n\t" \
  "v_and_b32 v19, 0x30, v0\n\t" \
  "v_add3_u32 v17, v17, v19, s63\n\t" \
  "v_lshrrev_b32 v19, 1, v0\n\t" \
  "v_and_b32 v19, 64, v19\n\t" \
  "v_add_u32 v17, v17, v19\n\t" \
  "v_xor_b32 v18, 64, v17\n\t" \
  "s_add_u32 m0, s63, 0\n\t" \
  "s_mov_b32 s60, 0\n\t" \
  "buffer_load_dwordx4 v16, %[W], s60 offen lds\n\t" \
  "s_add_u32 m0, s63, 1024\n\t" \
  "s_mov_b32 s60, 65536\n\t" \
  "buffer_load_dwordx4 v16, %[W], s60 offen lds\n\t" \
  "s_add_u32 m0, s63, 2048\n\t" \
  "s_mov_b32 s60, 131072\n\t" \
  "buffer_load_dwordx4 v16, %[W], s60 offen lds\n\t" \
  "s_add_u32 m0, s63, 3072\n\t" \
  "s_mov_b32 s60, 196608\n\t" \
  "buffer_load_dwordx4 v16, %[W], s60 offen lds\n\t" \
  "s_add_u32 m0, s63, 4096\n\t" \
  "s_mov_b32 s60, 262144\n\t" \
  "buffer_load_dwordx4 v16, %[W], s60 offen lds\n\t" \
  "s_add_u32 m0, s63, 5120\n\t" \
  "s_mov_b32 s60, 327680\n\t" \
  "buffer_load_dwordx4 v16, %[W], s60 offen lds\n\t" \
  "s_add_u32 m0, s63, 6144\n\t" \
  "s_mov_b32 s60, 393216\n\t" \
  "buffer_load_dwordx4 v16, %[W], s60 offen lds\n\t" \
  "s_add_u32 m0, s63, 7168\n\t" \
  "s_mov_b32 s60, 458752\n\t" \
  "buffer_load_dwordx4 v16, %[W], s60 offen lds\n\t" \
  "s_add_u32 m0, s63, 8192\n\t" \
  "s_mov_b32 s60, 524288\n\t" \
  "buffer_load_dwordx4 v16, %[W], s60 offen lds\n\t" \
  "s_add_u32 m0, s63, 9216\n\t" \
  "s_mov_b32 s60, 589824\n\t" \
  "buffer_load_dwordx4 v16, %[W], s60 offen lds\n\t" \
  "s_add_u32 m0, s63, 10240\n\t" \
  "s_mov_b32 s60, 655360\n\t" \
  "buffer_load_dwordx4 v16, %[W], s60 offen lds\n\t" \
  "s_add_u32 m0, s63, 11264\n\t" \
  "s_mov_b32 s60, 720896\n\t" \
  "buffer_load_dwordx4 v16, %[W], s60 offen lds\n\t" \
  "s_add_u32 m0, s63, 12288\n\t" \
  "s_mov_b32 s60, 786432\n\t" \
  "buffer_load_dwordx4 v16, %[W], s60 offen lds\n\t" \
  "s_add_u32 m0, s63, 13312\n\t" \
  "s_mov_b32 s60, 851968\n\t" \
  "buffer_load_dwordx4 v16, %[W], s60 offen lds\n\t" \
  "s_add_u32 m0, s63, 14336\n\t" \
  "s_mov_b32 s60, 917504\n\t" \
  "buffer_load_dwordx4 v16, %[W], s60 offen lds\n\t" \
  "s_add_u32 m0, s63, 15360\n\t" \
  "s_mov_b32 s60, 983040\n\t" \
  "buffer_load_dwordx4 v16, %[W], s60 offen lds\n\t" \
  "s_add_u32 m0, s63, 16384\n\t" \
  "s_mov_b32 s60, 1048576\n\t" \
  "buffer_load_dwordx4 v16, %[W], s60 offen lds\n\t" \
  "s_add_u32 m0, s63, 17408\n\t" \
  "s_mov_b32 s60, 1114112\n\t" \
  "buffer_load_dwordx4 v16, %[W], s60 offen lds\n\t" \
  "s_add_u32 m0, s63, 18432\n\t" \
  "s_mov_b32 s60, 1179648\n\t" \
  "buffer_load_dwordx4 v16, %[W], s60 offen lds\n\t" \
  "s_add_u32 m0, s63, 19456\n\t" \
  "s_mov_b32 s60, 1245184\n\t" \
  "buffer_load_dwordx4 v16, %[W], s60 offen lds\n\t" \
  "s_add_u32 m0, s63, 20480\n\t" \
  "s_mov_b32 s60, 1310720\n\t" \
  "buffer_load_dwordx4 v16, %[W], s60 offen lds\n\t" \
  "s_add_u32 m0, s63, 21504\n\t" \
  "s_mov_b32 s60, 1376256\n\t" \
  "buffer_load_dwordx4 v16, %[W], s60 offen lds\n\t" \
  "s_add_u32 m0, s63, 22528\n\t" \
  "s_mov_b32 s60, 1441792\n\t" \
  "buffer_load_dwordx4 v16, %[W], s60 offen lds\n\t" \
  "s_add_u32 m0, s63, 23552\n\t" \
  "s_mov_b32 s60, 1507328\n\t" \
  "buffer_load_dwordx4 v16, %[W], s60 offen lds\n\t" \
  "s_add_u32 m0, s63, 24576\n\t" \
  "s_mov_b32 s60, 1572864\n\t" \
  "buffer_load_dwordx4 v16, %[W], s60 offen lds\n\t" \
  "s_add_u32 m0, s63, 25600\n\t" \
  "s_mov_b32 s60, 1638400\n\t" \
  "buffer_load_dwordx4 v16, %[W], s60 offen lds\n\t" \
  "s_add_u32 m0, s63, 26624\n\t" \
  "s_mov_b32 s60, 1703936\n\t" \
  "buffer_load_dwordx4 v16, %[W], s60 offen lds\n\t" \
  "s_add_u32 m0, s63, 27648\n\t" \
  "s_mov_b32 s60, 1769472\n\t" \
  "buffer_load_dwordx4 v16, %[W], s60 offen lds\n\t" \
  "s_add_u32 m0, s63, 28672\n\t" \
  "s_mov_b32 s60, 1835008\n\t" \
  "buffer_load_dwordx4 v16, %[W], s60 offen lds\n\t" \
  "s_add_u32 m0, s63, 29696\n\t" \
  "s_mov_b32 s60, 1900544\n\t" \
  "buffer_load_dwordx4 v16, %[W], s60 offen lds\n\t" \
  "s_add_u32 m0, s63, 30720\n\t" \
  "s_mov_b32 s60, 1966080\n\t" \
  "buffer_load_dwordx4 v16, %[W], s60 offen lds\n\t" \
  "s_add_u32 m0, s63, 31744\n\t" \
  "s_mov_b32 s60, 2031616\n\t" \
  "buffer_load_dwordx4 v16, %[W], s60 offen lds\n\t" \
  "s_add_u32 m0, s63, 32768\n\t" \
  "s_mov_b32 s60, 2097152\n\t" \
  "buffer_load_dwordx4 v16, %[W], s60 offen lds\n\t" \
  "s_add_u32 m0, s63, 33792\n\t" \
  "s_mov_b32 s60, 2162688\n\t" \
  "buffer_load_dwordx4 v16, %[W], s60 offen lds\n\t" \
  "s_add_u32 m0, s63, 34816\n\t" \
  "s_mov_b32 s60, 2228224\n\t" \
  "buffer_load_dwordx4 v16, %[W], s60 offen lds\n\t" \
  "s_add_u32 m0, s63, 35840\n\t" \
  "s_mov_b32 s60, 2293760\n\t" \
  "buffer_load_dwordx4 v16, %[W], s60 offen lds\n\t" \
  "s_add_u32 m0, s63, 36864\n\t" \
  "s_mov_b32 s60, 2359296\n\t" \
  "buffer_load_dwordx4 v16, %[W], s60 offen lds\n\t" \
  "s_add_u32 m0, s63, 37888\n\t" \
  "s_mov_b32 s60, 2424832\n\t" \
  "buffer_load_dwordx4 v16, %[W], s60 offen lds\n\t" \
  "s_add_u32 m0, s63, 38912\n\t" \
  "s_mov_b32 s60, 2490368\n\t" \
  "buffer_load_dwordx4 v16, %[W], s60 offen lds\n\t" \
  "s_add_u32 m0, s63, 39936\n\t" \
  "s_mov_b32 s60, 2555904\n\t" \
  "buffer_load_dwordx4 v16, %[W], s60 offen lds\n\t" \
  "s_waitcnt vmcnt(32)\n\t" \
  "ds_read_b64_tr_b16 a[0:1], v17 offset:0\n\t" \
  "ds_read_b64_tr_b16 a[2:3], v17 offset:512\n\t" \
  "ds_read_b64_tr_b16 a[4:5], v17 offset:4096\n\t" \
  "ds_read_b64_tr_b16 a[6:7], v17 offset:4608\n\t" \
  "ds_read_b64_tr_b16 a[72:73], v17 offset:8\n\t" \
  "ds_read_b64_tr_b16 a[74:75], v17 offset:520\n\t" \
  "ds_read_b64_tr_b16 a[76:77], v17 offset:4104\n\t" \
  "ds_read_b64_tr_b16 a[78:79], v17 offset:4616\n\t" \
  "ds_read_b64_tr_b16 a[144:145], v18 offset:0\n\t" \
  "ds_read_b64_tr_b16 a[146:147], v18 offset:512\n\t" \
  "ds_read_b64_tr_b16 a[148:149], v18 offset:4096\n\t" \
  "ds_read_b64_tr_b16 a[150:151], v18 offset:4608\n\t" \
  "ds_read_b64_tr_b16 a[216:217], v18 offset:8\n\t" \
  "ds_read_b64_tr_b16 a[218:219], v18 offset:520\n\t" \
  "ds_read_b64_tr_b16 a[220:221], v18 offset:4104\n\t" \
  "ds_read_b64_tr_b16 a[222:223], v18 offset:4616\n\t" \
  "s_waitcnt lgkmcnt(0)\n\t" \
  "s_add_u32 m0, s63, 0\n\t" \
  "s_mov_b32 s60, 2621440\n\t" \
  "buffer_load_dwordx4 v16, %[W], s60 offen lds\n\t" \
  "s_add_u32 m0, s63, 1024\n\t" \
  "s_mov_b32 s60, 2686976\n\t" \
  "buffer_load_dwordx4 v16, %[W], s60 offen lds\n\t" \
  "s_add_u32 m0, s63, 2048\n\t" \
  "s_mov_b32 s60, 2752512\n\t" \
  "buffer_load_dwordx4 v16, %[W], s60 offen lds\n\t" \
  "s_add_u32 m0, s63, 3072\n\t" \
  "s_mov_b32 s60, 2818048\n\t" \
  "buffer_load_dwordx4 v16, %[W], s60 offen lds\n\t" \
  "s_add_u32 m0, s63, 4096\n\t" \
  "s_mov_b32 s60, 2883584\n\t" \
  "buffer_load_dwordx4 v16, %[W], s60 offen lds\n\t" \
  "s_add_u32 m0, s63, 5120\n\t" \
  "s_mov_b32 s60, 2949120\n\t" \
  "buffer_load_dwordx4 v16, %[W], s60 offen lds\n\t" \
  "s_add_u32 m0, s63, 6144\n\t" \
  "s_mov_b32 s60, 3014656\n\t" \
  "buffer_load_dwordx4 v16, %[W], s60 offen lds\n\t" \
  "s_add_u32 m0, s63, 7168\n\t" \
  "s_mov_b32 s60, 3080192\n\t" \
  "buffer_load_dwordx4 v16, %[W], s60 offen lds\n\t" \
  "s_waitcnt vmcnt(32)\n\t" \
  "ds_read_b64_tr_b16 a[8:9], v17 offset:8192\n\t" \
  "ds_read_b64_tr_b16 a[10:11], v17 offset:8704\n\t" \
  "ds_read_b64_tr_b16 a[12:13], v17 offset:12288\n\t" \
  "ds_read_b64_tr_b16 a[14:15], v17 offset:12800\n\t" \
  "ds_read_b64_tr_b16 a[80:81], v17 offset:8200\n\t" \
  "ds_read_b64_tr_b16 a[82:83], v17 offset:8712\n\t" \
  "ds_read_b64_tr_b16 a[84:85], v17 offset:12296\n\t" \
  "ds_read_b64_tr_b16 a[86:87], v17 offset:12808\n\t" \
  "ds_read_b64_tr_b16 a[152:153], v18 offset:8192\n\t" \
  "ds_read_b64_tr_b16 a[154:155], v18 offset:8704\n\t" \
  "ds_read_b64_tr_b16 a[156:157], v18 offset:12288\n\t" \
  "ds_read_b64_tr_b16 a[158:159], v18 offset:12800\n\t" \
  "ds_read_b64_tr_b16 a[224:225], v18 offset:8200\n\t" \
  "ds_read_b64_tr_b16 a[226:227], v18 offset:8712\n\t" \
  "ds_read_b64_tr_b16 a[228:229], v18 offset:12296\n\t" \
  "ds_read_b64_tr_b16 a[230:231], v18 offset:12808\n\t" \
  "s_waitcnt lgkmcnt(0)\n\t" \
  "s_add_u32 m0, s63, 8192\n\t" \
  "s_mov_b32 s60, 3145728\n\t" \
  "buffer_load_dwordx4 v16, %[W], s60 offen lds\n\t" \
  "s_add_u32 m0, s63, 9216\n\t" \
  "s_mov_b32 s60, 3211264\n\t" \
  "buffer_load_dwordx4 v16, %[W], s60 offen lds\n\t" \
  "s_add_u32 m0, s63, 10240\n\t" \
  "s_mov_b32 s60, 3276800\n\t" \
  "buffer_load_dwordx4 v16, %[W], s60 offen lds\n\t" \
  "s_add_u32 m0, s63, 11264\n\t" \
  "s_mov_b32 s60, 3342336\n\t" \
  "buffer_load_dwordx4 v16, %[W], s60 offen lds\n\t" \
  "s_add_u32 m0, s63, 12288\n\t" \
  "s_mov_b32 s60, 3407872\n\t" \
  "buffer_load_dwordx4 v16, %[W], s60 offen lds\n\t" \
  "s_add_u32 m0, s63, 13312\n\t" \
  "s_mov_b32 s60, 3473408\n\t" \
  "buffer_load_dwordx4 v16, %[W], s60 offen lds\n\t" \
  "s_add_u32 m0, s63, 14336\n\t" \
  "s_mov_b32 s60, 3538944\n\t" \
  "buffer_load_dwordx4 v16, %[W], s60 offen lds\n\t" \
  "s_add_u32 m0, s63, 15360\n\t" \
  "s_mov_b32 s60, 3604480\n\t" \
  "buffer_load_dwordx4 v16, %[W], s60 offen lds\n\t" \
  "s_waitcnt vmcnt(32)\n\t" \
  "ds_read_b64_tr_b16 a[16:17], v17 offset:16384\n\t" \
  "ds_read_b64_tr_b16 a[18:19], v17 offset:16896\n\t" \
  "ds_read_b64_tr_b16 a[20:21], v17 offset:20480\n\t" \
  "ds_read_b64_tr_b16 a[22:23], v17 offset:20992\n\t" \
  "ds_read_b64_tr_b16 a[88:89], v17 offset:16392\n\t" \
  "ds_read_b64_tr_b16 a[90:91], v17 offset:16904\n\t" \
  "ds_read_b64_tr_b16 a[92:93], v17 offset:20488\n\t" \
  "ds_read_b64_tr_b16 a[94:95], v17 offset:21000\n\t" \
  "ds_read_b64_tr_b16 a[160:161], v18 offset:16384\n\t" \
  "ds_read_b64_tr_b16 a[162:163], v18 offset:16896\n\t" \
  "ds_read_b64_tr_b16 a[164:165], v18 offset:20480\n\t" \
  "ds_read_b64_tr_b16 a[166:167], v18 offset:20992\n\t" \
  "ds_read_b64_tr_b16 a[232:233], v18 offset:16392\n\t" \
  "ds_read_b64_tr_b16 a[234:235], v18 offset:16904\n\t" \
  "ds_read_b64_tr_b16 a[236:237], v18 offset:20488\n\t" \
  "ds_read_b64_tr_b16 a[238:239], v18 offset:21000\n\t" \
  "s_waitcnt lgkmcnt(0)\n\t" \
  "s_add_u32 m0, s63, 16384\n\t" \
  "s_mov_b32 s60, 3670016\n\t" \
  "buffer_load_dwordx4 v16, %[W], s60 offen lds\n\t" \
  "s_add_u32 m0, s63, 17408\n\t" \
  "s_mov_b32 s60, 3735552\n\t" \
  "buffer_load_dwordx4 v16, %[W], s60 offen lds\n\t" \
  "s_add_u32 m0, s63, 18432\n\t" \
  "s_mov_b32 s60, 3801088\n\t" \
  "buffer_load_dwordx4 v16, %[W], s60 offen lds\n\t" \
  "s_add_u32 m0, s63, 19456\n\t" \
  "s_mov_b32 s60, 3866624\n\t" \
  "buffer_load_dwordx4 v16, %[W], s60 offen lds\n\t" \
  "s_add_u32 m0, s63, 20480\n\t" \
  "s_mov_b32 s60, 3932160\n\t" \
  "buffer_load_dwordx4 v16, %[W], s60 offen lds\n\t" \
  "s_add_u32 m0, s63, 21504\n\t" \
  "s_mov_b32 s60, 3997696\n\t" \
  "buffer_load_dwordx4 v16, %[W], s60 offen lds\n\t" \
  "s_add_u32 m0, s63, 22528\n\t" \
  "s_mov_b32 s60, 4063232\n\t" \
  "buffer_load_dwordx4 v16, %[W], s60 offen lds\n\t" \
  "s_add_u32 m0, s63, 23552\n\t" \
  "s_mov_b32 s60, 4128768\n\t" \
  "buffer_load_dwordx4 v16, %[W], s60 offen lds\n\t" \
  "s_waitcnt vmcnt(32)\n\t" \
  "ds_read_b64_tr_b16 a[24:25], v17 offset:24576\n\t" \
  "ds_read_b64_tr_b16 a[26:27], v17 offset:25088\n\t" \
  "ds_read_b64_tr_b16 a[28:29], v17 offset:28672\n\t" \
  "ds_read_b64_tr_b16 a[30:31], v17 offset:29184\n\t" \
  "ds_read_b64_tr_b16 a[96:97], v17 offset:24584\n\t" \
  "ds_read_b64_tr_b16 a[98:99], v17 offset:25096\n\t" \
  "ds_read_b64_tr_b16 a[100:101], v17 offset:28680\n\t" \
  "ds_read_b64_tr_b16 a[102:103], v17 offset:29192\n\t" \
  "ds_read_b64_tr_b16 a[168:169], v18 offset:24576\n\t" \
  "ds_read_b64_tr_b16 a[170:171], v18 offset:25088\n\t" \
  "ds_read_b64_tr_b16 a[172:173], v18 offset:28672\n\t" \
  "ds_read_b64_tr_b16 a[174:175], v18 offset:29184\n\t" \
  "ds_read_b64_tr_b16 a[240:241], v18 offset:24584\n\t" \
  "ds_read_b64_tr_b16 a[242:243], v18 offset:25096\n\t" \
  "ds_read_b64_tr_b16 a[244:245], v18 offset:28680\n\t" \
  "ds_read_b64_tr_b16 a[246:247], v18 offset:29192\n\t" \
  "s_waitcnt lgkmcnt(0)\n\t" \
  "s_add_u32 m0, s63, 24576\n\t" \
  "s_mov_b32 s60, 4194304\n\t" \
  "buffer_load_dwordx4 v16, %[W], s60 offen lds\n\t" \
  "s_add_u32 m0, s63, 25600\n\t" \
  "s_mov_b32 s60, 4259840\n\t" \
  "buffer_load_dwordx4 v16, %[W], s60 offen lds\n\t" \
  "s_add_u32 m0, s63, 26624\n\t" \
  "s_mov_b32 s60, 4325376\n\t" \
  "buffer_load_dwordx4 v16, %[W], s60 offen lds\n\t" \
  "s_add_u32 m0, s63, 27648\n\t" \
  "s_mov_b32 s60, 4390912\n\t" \
  "buffer_load_dwordx4 v16, %[W], s60 offen lds\n\t" \
  "s_add_u32 m0, s63, 28672\n\t" \
  "s_mov_b32 s60, 4456448\n\t" \
  "buffer_load_dwordx4 v16, %[W], s60 offen lds\n\t" \
  "s_add_u32 m0, s63, 29696\n\t" \
  "s_mov_b32 s60, 4521984\n\t" \
  "buffer_load_dwordx4 v16, %[W], s60 offen lds\n\t" \
  "s_add_u32 m0, s63, 30720\n\t" \
  "s_mov_b32 s60, 4587520\n\t" \
  "buffer_load_dwordx4 v16, %[W], s60 offen lds\n\t" \
  "s_add_u32 m0, s63, 31744\n\t" \
  "s_mov_b32 s60, 4653056\n\t" \
  "buffer_load_dwordx4 v16, %[W], s60 offen lds\n\t" \
  "s_waitcnt vmcnt(32)\n\t" \
  "ds_read_b64_tr_b16 a[32:33], v17 offset:32768\n\t" \
  "ds_read_b64_tr_b16 a[34:35], v17 offset:33280\n\t" \
  "ds_read_b64_tr_b16 a[36:37], v17 offset:36864\n\t" \
  "ds_read_b64_tr_b16 a[38:39], v17 offset:37376\n\t" \
  "ds_read_b64_tr_b16 a[104:105], v17 offset:32776\n\t" \
  "ds_read_b64_tr_b16 a[106:107], v17 offset:33288\n\t" \
  "ds_read_b64_tr_b16 a[108:109], v17 offset:36872\n\t" \
  "ds_read_b64_tr_b16 a[110:111], v17 offset:37384\n\t" \
  "ds_read_b64_tr_b16 a[176:177], v18 offset:32768\n\t" \
  "ds_read_b64_tr_b16 a[178:179], v18 offset:33280\n\t" \
  "ds_read_b64_tr_b16 a[180:181], v18 offset:36864\n\t" \
  "ds_read_b64_tr_b16 a[182:183], v18 offset:37376\n\t" \
  "ds_read_b64_tr_b16 a[248:249], v18 offset:32776\n\t" \
  "ds_read_b64_tr_b16 a[250:251], v18 offset:33288\n\t" \
  "ds_read_b64_tr_b16 a[252:253], v18 offset:36872\n\t" \
  "ds_read_b64_tr_b16 a[254:255], v18 offset:37384\n\t" \
  "s_waitcnt lgkmcnt(0)\n\t" \
  "s_waitcnt vmcnt(24)\n\t" \
  "ds_read_b64_tr_b16 a[40:41], v17 offset:0\n\t" \
  "ds_read_b64_tr_b16 a[42:43], v17 offset:512\n\t" \
  "ds_read_b64_tr_b16 a[44:45], v17 offset:4096\n\t" \
  "ds_read_b64_tr_b16 a[46:47], v17 offset:4608\n\t" \
  "ds_read_b64_tr_b16 a[112:113], v17 offset:8\n\t" \
  "ds_read_b64_tr_b16 a[114:115], v17 offset:520\n\t" \
  "ds_read_b64_tr_b16 a[116:117], v17 offset:4104\n\t" \
  "ds_read_b64_tr_b16 a[118:119], v17 offset:4616\n\t" \
  "ds_read_b64_tr_b16 a[184:185], v18 offset:0\n\t" \
  "ds_read_b64_tr_b16 a[186:187], v18 offset:512\n\t" \
  "ds_read_b64_tr_b16 a[188:189], v18 offset:4096\n\t" \
  "ds_read_b64_tr_b16 a[190:191], v18 offset:4608\n\t" \
  "ds_read_b64_tr_b16 v[224:225], v18 offset:8\n\t" \
  "ds_read_b64_tr_b16 v[226:227], v18 offset:520\n\t" \
  "ds_read_b64_tr_b16 v[228:229], v18 offset:4104\n\t" \
  "ds_read_b64_tr_b16 v[230:231], v18 offset:4616\n\t" \
  "s_waitcnt lgkmcnt(0)\n\t" \
  "s_waitcnt vmcnt(16)\n\t" \
  "ds_read_b64_tr_b16 a[48:49], v17 offset:8192\n\t" \
  "ds_read_b64_tr_b16 a[50:51], v17 offset:8704\n\t" \
  "ds_read_b64_tr_b16 a[52:53], v17 offset:12288\n\t" \
  "ds_read_b64_tr_b16 a[54:55], v17 offset:12800\n\t" \
  "ds_read_b64_tr_b16 a[120:121], v17 offset:8200\n\t" \
  "ds_read_b64_tr_b16 a[122:123], v17 offset:8712\n\t" \
  "ds_read_b64_tr_b16 a[124:125], v17 offset:12296\n\t" \
  "ds_read_b64_tr_b16 a[126:127], v17 offset:12808\n\t" \
  "ds_read_b64_tr_b16 a[192:193], v18 offset:8192\n\t" \
  "ds_read_b64_tr_b16 a[194:195], v18 offset:8704\n\t" \
  "ds_read_b64_tr_b16 a[196:197], v18 offset:12288\n\t" \
  "ds_read_b64_tr_b16 a[198:199], v18 offset:12800\n\t" \
  "ds_read_b64_tr_b16 v[232:233], v18 offset:8200\n\t" \
  "ds_read_b64_tr_b16 v[234:235], v18 offset:8712\n\t" \
  "ds_read_b64_tr_b16 v[236:237], v18 offset:12296\n\t" \
  "ds_read_b64_tr_b16 v[238:239], v18 offset:12808\n\t" \
  "s_waitcnt lgkmcnt(0)\n\t" \
  "s_waitcnt vmcnt(8)\n\t" \
  "ds_read_b64_tr_b16 a[56:57], v17 offset:16384\n\t" \
  "ds_read_b64_tr_b16 a[58:59], v17 offset:16896\n\t" \
  "ds_read_b64_tr_b16 a[60:61], v17 offset:20480\n\t" \
  "ds_read_b64_tr_b16 a[62:63], v17 offset:20992\n\t" \
  "ds_read_b64_tr_b16 a[128:129], v17 offset:16392\n\t" \
  "ds_read_b64_tr_b16 a[130:131], v17 offset:16904\n\t" \
  "ds_read_b64_tr_b16 a[132:133], v17 offset:20488\n\t" \
  "ds_read_b64_tr_b16 a[134:135], v17 offset:21000\n\t" \
  "ds_read_b64_tr_b16 a[200:201], v18 offset:16384\n\t" \
  "ds_read_b64_tr_b16 a[202:203], v18 offset:16896\n\t" \
  "ds_read_b64_tr_b16 a[204:205], v18 offset:20480\n\t" \
  "ds_read_b64_tr_b16 a[206:207], v18 offset:20992\n\t" \
  "ds_read_b64_tr_b16 v[240:241], v18 offset:16392\n\t" \
  "ds_read_b64_tr_b16 v[242:243], v18 offset:16904\n\t" \
  "ds_read_b64_tr_b16 v[244:245], v18 offset:20488\n\t" \
  "ds_read_b64_tr_b16 v[246:247], v18 offset:21000\n\t" \
  "s_waitcnt lgkmcnt(0)\n\t" \
  "s_waitcnt vmcnt(0)\n\t" \
  "ds_read_b64_tr_b16 a[64:65], v17 offset:24576\n\t" \
  "ds_read_b64_tr_b16 a[66:67], v17 offset:25088\n\t" \
  "ds_read_b64_tr_b16 a[68:69], v17 offset:28672\n\t" \
  "ds_read_b64_tr_b16 a[70:71], v17 offset:29184\n\t" \
  "ds_read_b64_tr_b16 a[136:137], v17 offset:24584\n\t" \
  "ds_read_b64_tr_b16 a[138:139], v17 offset:25096\n\t" \
  "ds_read_b64_tr_b16 a[140:141], v17 offset:28680\n\t" \
  "ds_read_b64_tr_b16 a[142:143], v17 offset:29192\n\t" \
  "ds_read_b64_tr_b16 a[208:209], v18 offset:24576\n\t" \
  "ds_read_b64_tr_b16 a[210:211], v18 offset:25088\n\t" \
  "ds_read_b64_tr_b16 a[212:213], v18 offset:28672\n\t" \
  "ds_read_b64_tr_b16 a[214:215], v18 offset:29184\n\t" \
  "ds_read_b64_tr_b16 v[248:249], v18 offset:24584\n\t" \
  "ds_read_b64_tr_b16 v[250:251], v18 offset:25096\n\t" \
  "ds_read_b64_tr_b16 v[252:253], v18 offset:28680\n\t" \
  "ds_read_b64_tr_b16 v[254:255], v18 offset:29192\n\t" \
  "s_waitcnt lgkmcnt(0)\n\t" \
  "s_mov_b32 s84, " TK_CAP "\n\t" \
  "s_mov_b32 s48, 0\n\t" \
  "s_mov_b32 s49, 0xf0000000\n\t" \
  "s_mov_b32 s46, s41\n\t" \
  "s_mov_b32 s56, 0\n\t" \
  "v_lshrrev_b32 v7, 2, v0\n\t" \
  "s_mov_b32 s60, " TK_CTAB "\n\t" \
  "buffer_load_dword v4, v7, %[X], s60 offen offset:88 sc1\n\t" \
  "buffer_load_dword v7, v7, %[X], s60 offen offset:0 sc1\n\t" \
  "s_waitcnt vmcnt(0)\n\t" \
  "v_and_b32 v4, 1, v4\n\t" \
  "v_and_b32 v7, 1, v7\n\t" \
  "v_cmp_ne_u32 s[90:91], s67, v4\n\t" \
  "v_cmp_ne_u32 s[88:89], s67, v7\n\t" \
  "s_nop 1\n\t" \
  "s_mov_b32 s57, 0xf0000000\n\t" \
  "s_add_u32 s83, s48, 0\n\t" \
  "s_lshr_b64 s[60:61], s[90:91], s83\n\t" \
  "s_and_b32 s60, s60, 1\n\t" \
  "s_lshl_b32 s60, s60, 13\n\t" \
  "s_add_u32 s83, s83, " TK_SG0 "\n\t" \
  "s_lshl_b32 s83, s83, 21\n\t" \
  "s_or_b32 s83, s83, s60\n\t" \
  "s_add_u32 s80, s48, " TK_TRN "\n\t" \
  "s_lshr_b64 s[60:61], s[90:91], s80\n\t" \
  "s_and_b32 s60, s60, 1\n\t" \
  "s_lshl_b32 s60, s60, 13\n\t" \
  "s_add_u32 s80, s80, " TK_SG0 "\n\t" \
  "s_lshl_b32 s80, s80, 21\n\t" \
  "s_or_b32 s80, s80, s60\n\t" \
  "s_add_u32 s62, s83, s39\n\t" \
  "s_add_u32 s80, s80, s39\n\t" \
  "s_add_u32 s80, s80, 2048\n\t" \
  "s_cmp_lt_u32 s48, " TK_TRM "\n\t" \
  "s_cselect_b32 s80, s62, s80\n\t" \
  "v_add_u32 v7, s62, v3\n\t" \
  "s_mov_b32 exec_lo, 0\n\t" \
  "v_mov_b32 v7, s80\n\t" \
  "s_mov_b32 exec_lo, -1\n\t" \
  "LCOLD_%=:\n\t" \
  "s_waitcnt vmcnt(0)\n\t" \
  "s_mov_b64 exec, 1\n\t" \
  "buffer_store_dword v6, off, %[X], s49 sc1\n\t" \
  "s_mov_b64 exec, -1\n\t" \
  "s_mov_b32 s49, 0xf0000000\n\t" \
  "s_add_u32 s55, s83, s36\n\t" \
  "s_cmp_lg_u32 s40, 0\n\t" \
  "s_cbranch_scc1 LGO_%=\n\t" \
  "s_and_b32 s62, s45, 3\n\t" \
  "s_lshl_b32 s62, s62, 7\n\t" \
  "s_add_u32 s62, s62, s83\n\t" \
  "s_add_u32 s62, s62, s39\n\t" \
  "s_mov_b32 s61, 0\n\t" \
  "LPO_%=:\n\t" \
  "buffer_load_dword v4, off, %[X], s62 sc1\n\t" \
  "s_waitcnt vmcnt(0)\n\t" \
  "v_cmp_eq_u32 vcc, 0, v4\n\t" \
  "s_cbranch_vccz LPO_%=x\n\t" \
  "s_add_u32 s61, s61, 1\n\t" \
  "s_sleep " TK_PSH "\n\t" \
  "s_cmp_gt_u32 s61, s84\n\t" \
  "s_cbranch_scc1 LPO_%=g\n\t" \
  "s_and_b32 s60, s61, 1023\n\t" \
  "s_cmp_lg_u32 s60, 0\n\t" \
  "s_cbranch_scc1 LPO_%=\n\t" \
  "buffer_load_dword v4, off, %[X], s92 sc1\n\t" \
  "s_waitcnt vmcnt(0)\n\t" \
  "v_readfirstlane_b32 s60, v4\n\t" \
  "s_nop 0\n\t" \
  "s_cmp_eq_u32 s60, 0\n\t" \
  "s_cbranch_scc1 LPO_%=\n\t" \
  "LPO_%=g:\n\t" \
  "s_mov_b32 s84, 0\n\t" \
  "s_or_b32 s60, s48, 1610612736\n\t" \
  "v_mov_b32 v4, s60\n\t" \
  "s_mov_b64 exec, 1\n\t" \
  "buffer_store_dword v4, off, %[X], s92 sc1\n\t" \
  "s_mov_b64 exec, -1\n\t" \
  "LPO_%=x:\n\t" \
  "s_mov_b32 s61, 0\n\t" \
  "LPH_%=:\n\t" \
  "buffer_load_dword v4, v7, %[X], 0 offen sc1\n\t" \
  "s_waitcnt vmcnt(0)\n\t" \
  "v_cmp_eq_u32 vcc, 0, v4\n\t" \
  "s_cbranch_vccz LPH_%=x\n\t" \
  "s_add_u32 s61, s61, 1\n\t" \
  "s_sleep " TK_PSH "\n\t" \
  "s_cmp_gt_u32 s61, s84\n\t" \
  "s_cbranch_scc1 LPH_%=g\n\t" \
  "s_and_b32 s60, s61, 1023\n\t" \
  "s_cmp_lg_u32 s60, 0\n\t" \
  "s_cbranch_scc1 LPH_%=\n\t" \
  "buffer_load_dword v4, off, %[X], s92 sc1\n\t" \
  "s_waitcnt vmcnt(0)\n\t" \
  "v_readfirstlane_b32 s60, v4\n\t" \
  "s_nop 0\n\t" \
  "s_cmp_eq_u32 s60, 0\n\t" \
  "s_cbranch_scc1 LPH_%=\n\t" \
  "LPH_%=g:\n\t" \
  "s_mov_b32 s84, 0\n\t" \
  "s_or_b32 s60, s48, 1610612736\n\t" \
  "v_mov_b32 v4, s60\n\t" \
  "s_mov_b64 exec, 1\n\t" \
  "buffer_store_dword v4, off, %[X], s92 sc1\n\t" \
  "s_mov_b64 exec, -1\n\t" \
  "LPH_%=x:\n\t" \
  "s_cmp_lt_i32 s57, 0\n\t" \
  "s_cbranch_scc1 LGO_%=\n\t" \
  "s_movk_i32 s63, 4000\n\t" \
  "s_branch LGC_%=u\n\t" \
  "LGC_%=:\n\t" \
  "s_sleep " TK_PSG "\n\t" \
  "buffer_load_dword v8, off, %[X], s57 sc1\n\t" \
  "s_waitcnt vmcnt(0)\n\t" \
  "LGC_%=u:\n\t" \
  "s_memrealtime s[52:53]\n\t" \
  "v_readfirstlane_b32 s60, v8\n\t" \
  "s_waitcnt lgkmcnt(0)\n\t" \
  "s_sub_u32 s63, s63, 1\n\t" \
  "s_cbranch_scc1 LGO_%=\n\t" \
  "s_cmp_eq_u32 s60, -1\n\t" \
  "s_cbranch_scc1 LGC_%=\n\t" \
  "s_not_b32 s60, s60\n\t" \
  "s_lshl_b32 s61, s52, 8\n\t" \
  "s_sub_u32 s60, s60, s61\n\t" \
  "s_ashr_i32 s60, s60, 8\n\t" \
  "s_sub_i32 s60, s60, " TK_QXC "\n\t" \
  "s_min_i32 s60, s60, 400\n\t" \
  "s_add_u32 s54, s52, s60\n\t" \
  "LGC_%=w:\n\t" \
  "s_sleep 1\n\t" \
  "s_memrealtime s[52:53]\n\t" \
  "s_waitcnt lgkmcnt(0)\n\t" \
  "s_sub_i32 s53, s52, s54\n\t" \
  "s_cmp_lt_i32 s53, 0\n\t" \
  "s_cbranch_scc1 LGC_%=w\n\t" \
  "LGO_%=:\n\t" \
  "s_barrier\n\t" \
  "s_add_u32 m0, s46, 0\n\t" \
  "s_add_u32 s47, s55, 0\n\t" \
  "buffer_load_dwordx4 v0, %[X], s47 offen sc1 lds\n\t" \
  "s_add_u32 m0, s46, 4096\n\t" \
  "s_add_u32 s47, s55, 4096\n\t" \
  "buffer_load_dwordx4 v0, %[X], s47 offen sc1 lds\n\t" \
  "s_add_u32 m0, s46, 8192\n\t" \
  "s_add_u32 s47, s55, 16384\n\t" \
  "buffer_load_dwordx4 v0, %[X], s47 offen sc1 lds\n\t" \
  "s_add_u32 m0, s46, 12288\n\t" \
  "s_add_u32 s47, s55, 20480\n\t" \
  "buffer_load_dwordx4 v0, %[X], s47 offen sc1 lds\n\t" \
  "s_add_u32 m0, s46, 16384\n\t" \
  "s_add_u32 s47, s55, 32768\n\t" \
  "buffer_load_dwordx4 v0, %[X], s47 offen sc1 lds\n\t" \
  "s_add_u32 m0, s46, 20480\n\t" \
  "s_add_u32 s47, s55, 36864\n\t" \
  "buffer_load_dwordx4 v0, %[X], s47 offen sc1 lds\n\t" \
  "s_add_u32 m0, s46, 24576\n\t" \
  "s_add_u32 s47, s55, 49152\n\t" \
  "buffer_load_dwordx4 v0, %[X], s47 offen sc1 lds\n\t" \
  "s_add_u32 m0, s46, 28672\n\t" \
  "s_add_u32 s47, s55, 53248\n\t" \
  "buffer_load_dwordx4 v0, %[X], s47 offen sc1 lds\n\t" \
  "s_add_u32 m0, s46, 32768\n\t" \
  "s_add_u32 s47, s55, 65536\n\t" \
  "buffer_load_dwordx4 v0, %[X], s47 offen sc1 lds\n\t" \
  "s_waitcnt vmcnt(5)\n\t" \
  "LTOP_%=:\n\t" \
  "s_barrier\n\t" \
  "ds_read_b128 v[64:67], v1 offset:0\n\t" \
  "ds_read_b128 v[68:71], v1 offset:2048\n\t" \
  "ds_read_b128 v[72:75], v2 offset:0\n\t" \
  "ds_read_b128 v[76:79], v2 offset:2048\n\t" \
  "ds_read_b128 v[80:83], v1 offset:4096\n\t" \
  "ds_read_b128 v[84:87], v1 offset:6144\n\t" \
  "s_mov_b32 s82, s83\n\t" \
  "s_xor_b32 s46, s46, 0x10000\n\t" \
  "s_waitcnt lgkmcnt(5)\n\t" \
  "v_mfma_f32_16x16x32_bf16 v[16:19], a[0:3], v[64:67], 0\n\t" \
  "ds_read_b128 v[88:91], v2 offset:4096\n\t" \
  "v_mfma_f32_16x16x32_bf16 v[20:23], a[72:75], v[64:67], 0\n\t" \
  "s_add_u32 s50, s48, 0\n\t" \
  "s_and_b32 s50, s50, " TK_TRM "\n\t" \
  "v_mfma_f32_16x16x32_bf16 v[24:27], a[144:147], v[64:67], 0\n\t" \
  "ds_read_b128 v[92:95], v2 offset:6144\n\t" \
  "v_mfma_f32_16x16x32_bf16 v[28:31], a[216:219], v[64:67], 0\n\t" \
  "s_lshr_b64 s[60:61], s[90:91], s50\n\t" \
  "s_and_b32 s60, s60, 1\n\t" \
  "s_waitcnt lgkmcnt(5)\n\t" \
  "v_mfma_f32_16x16x32_bf16 v[16:19], a[4:7], v[72:75], v[16:19]\n\t" \
  "ds_read_b128 v[96:99], v1 offset:8192\n\t" \
  "v_mfma_f32_16x16x32_bf16 v[20:23], a[76:79], v[72:75], v[20:23]\n\t" \
  "s_lshl_b32 s60, s60, 13\n\t" \
  "s_add_u32 s50, s50, " TK_SG0 "\n\t" \
  "v_mfma_f32_16x16x32_bf16 v[24:27], a[148:151], v[72:75], v[24:27]\n\t" \
  "ds_read_b128 v[100:103], v1 offset:10240\n\t" \
  "v_mfma_f32_16x16x32_bf16 v[28:31], a[220:223], v[72:75], v[28:31]\n\t" \
  "s_lshl_b32 s50, s50, 21\n\t" \
  "s_or_b32 s50, s50, s60\n\t" \
  "s_waitcnt lgkmcnt(5)\n\t" \
  "v_mfma_f32_16x16x32_bf16 v[16:19], a[8:11], v[80:83], v[16:19]\n\t" \
  "ds_read_b128 v[104:107], v2 offset:8192\n\t" \
  "v_mfma_f32_16x16x32_bf16 v[20:23], a[80:83], v[80:83], v[20:23]\n\t" \
  "s_add_u32 s50, s50, s37\n\t" \
  "s_add_u32 s51, s50, 4096\n\t" \
  "v_mfma_f32_16x16x32_bf16 v[24:27], a[152:155], v[80:83], v[24:27]\n\t" \
  "ds_read_b128 v[108:111], v2 offset:10240\n\t" \
  "v_mfma_f32_16x16x32_bf16 v[28:31], a[224:227], v[80:83], v[28:31]\n\t" \
  "s_add_u32 s83, s48, 1\n\t" \
  "s_lshr_b64 s[60:61], s[90:91], s83\n\t" \
  "s_waitcnt lgkmcnt(5)\n\t" \
  "v_mfma_f32_16x16x32_bf16 v[16:19], a[12:15], v[88:91], v[16:19]\n\t" \
  "ds_read_b128 v[112:115], v1 offset:12288\n\t" \
  "v_mfma_f32_16x16x32_bf16 v[20:23], a[84:87], v[88:91], v[20:23]\n\t" \
  "s_and_b32 s60, s60, 1\n\t" \
  "s_lshl_b32 s60, s60, 13\n\t" \
  "v_mfma_f32_16x16x32_bf16 v[24:27], a[156:159], v[88:91], v[24:27]\n\t" \
  "ds_read_b128 v[116:119], v1 offset:14336\n\t" \
  "v_mfma_f32_16x16x32_bf16 v[28:31], a[228:231], v[88:91], v[28:31]\n\t" \
  "s_add_u32 s83, s83, " TK_SG0 "\n\t" \
  "s_lshl_b32 s83, s83, 21\n\t" \
  "s_waitcnt lgkmcnt(5)\n\t" \
  "v_mfma_f32_16x16x32_bf16 v[16:19], a[16:19], v[96:99], v[16:19]\n\t" \
  "ds_read_b128 v[120:123], v2 offset:12288\n\t" \
  "v_mfma_f32_16x16x32_bf16 v[20:23], a[88:91], v[96:99], v[20:23]\n\t" \
  "s_or_b32 s83, s83, s60\n\t" \
  "s_cmp_lg_u32 s56, 0\n\t" \
  "v_mfma_f32_16x16x32_bf16 v[24:27], a[160:163], v[96:99], v[24:27]\n\t" \
  "ds_read_b128 v[124:127], v2 offset:14336\n\t" \
  "v_mfma_f32_16x16x32_bf16 v[28:31], a[232:235], v[96:99], v[28:31]\n\t" \
  "s_cbranch_scc1 LWARM_%=\n\t" \
  "s_waitcnt vmcnt(0)\n\t" \
  "LWARM_%=:\n\t" \
  "s_barrier\n\t" \
  "s_waitcnt lgkmcnt(5)\n\t" \
  "v_mfma_f32_16x16x32_bf16 v[16:19], a[20:23], v[104:107], v[16:19]\n\t" \
  "ds_read_b128 v[128:131], v1 offset:16384\n\t" \
  "v_mfma_f32_16x16x32_bf16 v[20:23], a[92:95], v[104:107], v[20:23]\n\t" \
  "v_mfma_f32_16x16x32_bf16 v[24:27], a[164:167], v[104:107], v[24:27]\n\t" \
  "ds_read_b128 v[132:135], v1 offset:18432\n\t" \
  "v_mfma_f32_16x16x32_bf16 v[28:31], a[236:239], v[104:107], v[28:31]\n\t" \
  "s_add_i32 s62, s48, s42\n\t" \
  "s_add_i32 s62, s62, 1\n\t" \
  "s_waitcnt lgkmcnt(5)\n\t" \
  "v_mfma_f32_16x16x32_bf16 v[16:19], a[24:27], v[112:115], v[16:19]\n\t" \
  "ds_read_b128 v[136:139], v2 offset:16384\n\t" \
  "v_mfma_f32_16x16x32_bf16 v[20:23], a[96:99], v[112:115], v[20:23]\n\t" \
  "s_mov_b32 s61, s62\n\t" \
  "v_mfma_f32_16x16x32_bf16 v[24:27], a[168:171], v[112:115], v[24:27]\n\t" \
  "ds_read_b128 v[140:143], v2 offset:18432\n\t" \
  "v_mfma_f32_16x16x32_bf16 v[28:31], a[240:243], v[112:115], v[28:31]\n\t" \
  "s_mul_i32 s63, s62, 43\n\t" \
  "s_lshr_b32 s63, s63, 9\n\t" \
  "s_waitcnt lgkmcnt(5)\n\t" \
  "v_mfma_f32_16x16x32_bf16 v[16:19], a[28:31], v[120:123], v[16:19]\n\t" \
  "ds_read_b128 v[144:147], v1 offset:20480\n\t" \
  "v_mfma_f32_16x16x32_bf16 v[20:23], a[100:103], v[120:123], v[20:23]\n\t" \
  "s_mul_i32 s63, s63, " TK_RM "\n\t" \
  "s_sub_u32 s62, s62, s63\n\t" \
  "v_mfma_f32_16x16x32_bf16 v[24:27], a[172:175], v[120:123], v[24:27]\n\t" \
  "ds_read_b128 v[148:151], v1 offset:22528\n\t" \
  "v_mfma_f32_16x16x32_bf16 v[28:31], a[244:247], v[120:123], v[28:31]\n\t" \
  "s_mul_i32 s63, s62, 43\n\t" \
  "s_lshr_b32 s63, s63, 7\n\t" \
  "s_waitcnt lgkmcnt(5)\n\t" \
  "v_mfma_f32_16x16x32_bf16 v[16:19], a[32:35], v[128:131], v[16:19]\n\t" \
  "ds_read_b128 v[152:155], v2 offset:20480\n\t" \
  "v_mfma_f32_16x16x32_bf16 v[20:23], a[104:107], v[128:131], v[20:23]\n\t" \
  "s_mul_i32 s57, s62, 688128\n\t" \
  "s_lshl_b32 s60, s63, 15\n\t" \
  "v_mfma_f32_16x16x32_bf16 v[24:27], a[176:179], v[128:131], v[24:27]\n\t" \
  "ds_read_b128 v[156:159], v2 offset:22528\n\t" \
  "v_mfma_f32_16x16x32_bf16 v[28:31], a[248:251], v[128:131], v[28:31]\n\t" \
  "s_add_u32 s57, s57, s60\n\t" \
  "s_lshr_b32 s60, s88, s63\n\t" \
  "s_waitcnt lgkmcnt(5)\n\t" \
  "v_mfma_f32_16x16x32_bf16 v[16:19], a[36:39], v[136:139], v[16:19]\n\t" \
  "ds_read_b128 v[160:163], v1 offset:24576\n\t" \
  "v_mfma_f32_16x16x32_bf16 v[20:23], a[108:111], v[136:139], v[20:23]\n\t" \
  "s_and_b32 s60, s60, 1\n\t" \
  "s_lshl_b32 s60, s60, 13\n\t" \
  "v_mfma_f32_16x16x32_bf16 v[24:27], a[180:183], v[136:139], v[24:27]\n\t" \
  "ds_read_b128 v[164:167], v1 offset:26624\n\t" \
  "v_mfma_f32_16x16x32_bf16 v[28:31], a[252:255], v[136:139], v[28:31]\n\t" \
  "s_add_u32 s57, s57, s60\n\t" \
  "s_add_u32 s57, s57, 656896\n\t" \
  "s_waitcnt lgkmcnt(5)\n\t" \
  "v_mfma_f32_16x16x32_bf16 v[16:19], a[40:43], v[144:147], v[16:19]\n\t" \
  "ds_read_b128 v[168:171], v2 offset:24576\n\t" \
  "v_mfma_f32_16x16x32_bf16 v[20:23], a[112:115], v[144:147], v[20:23]\n\t" \
  "s_cmp_lt_i32 s61, 1\n\t" \
  "s_cselect_b32 s57, 0xf0000000, s57\n\t" \
  "v_mfma_f32_16x16x32_bf16 v[24:27], a[184:187], v[144:147], v[24:27]\n\t" \
  "ds_read_b128 v[172:175], v2 offset:26624\n\t" \
  "v_mfma_f32_16x16x32_bf16 v[28:31], v[224:227], v[144:147], v[28:31]\n\t" \
  "s_cmp_lg_u32 s40, 0\n\t" \
  "s_cselect_b32 s57, 0xf0000000, s57\n\t" \
  "s_waitcnt lgkmcnt(5)\n\t" \
  "v_mfma_f32_16x16x32_bf16 v[16:19], a[44:47], v[152:155], v[16:19]\n\t" \
  "ds_read_b128 v[176:179], v1 offset:28672\n\t" \
  "v_mfma_f32_16x16x32_bf16 v[20:23], a[116:119], v[152:155], v[20:23]\n\t" \
  "s_add_u32 s80, s48, " TK_TRN "\n\t" \
  "s_lshr_b64 s[60:61], s[90:91], s80\n\t" \
  "s_and_b32 s60, s60, 1\n\t" \
  "v_mfma_f32_16x16x32_bf16 v[24:27], a[188:191], v[152:155], v[24:27]\n\t" \
  "ds_read_b128 v[180:183], v1 offset:30720\n\t" \
  "v_mfma_f32_16x16x32_bf16 v[28:31], v[228:231], v[152:155], v[28:31]\n\t" \
  "s_lshl_b32 s60, s60, 13\n\t" \
  "s_add_u32 s80, s80, " TK_SG0 "\n\t" \
  "s_lshl_b32 s80, s80, 21\n\t" \
  "s_waitcnt lgkmcnt(5)\n\t" \
  "v_mfma_f32_16x16x32_bf16 v[16:19], a[48:51], v[160:163], v[16:19]\n\t" \
  "ds_read_b128 v[184:187], v2 offset:28672\n\t" \
  "v_mfma_f32_16x16x32_bf16 v[20:23], a[120:123], v[160:163], v[20:23]\n\t" \
  "s_or_b32 s80, s80, s60\n\t" \
  "s_add_u32 s62, s83, s39\n\t" \
  "s_add_u32 s80, s80, s39\n\t" \
  "v_mfma_f32_16x16x32_bf16 v[24:27], a[192:195], v[160:163], v[24:27]\n\t" \
  "ds_read_b128 v[188:191], v2 offset:30720\n\t" \
  "v_mfma_f32_16x16x32_bf16 v[28:31], v[232:235], v[160:163], v[28:31]\n\t" \
  "s_add_u32 s80, s80, 2048\n\t" \
  "s_waitcnt lgkmcnt(5)\n\t" \
  "v_mfma_f32_16x16x32_bf16 v[16:19], a[52:55], v[168:171], v[16:19]\n\t" \
  "ds_read_b128 v[192:195], v1 offset:32768\n\t" \
  "v_mfma_f32_16x16x32_bf16 v[20:23], a[124:127], v[168:171], v[20:23]\n\t" \
  "v_mfma_f32_16x16x32_bf16 v[24:27], a[196:199], v[168:171], v[24:27]\n\t" \
  "ds_read_b128 v[196:199], v1 offset:34816\n\t" \
  "v_mfma_f32_16x16x32_bf16 v[28:31], v[236:239], v[168:171], v[28:31]\n\t" \
  "s_cmp_lt_u32 s48, " TK_TRM "\n\t" \
  "s_cselect_b32 s80, s62, s80\n\t" \
  "s_waitcnt lgkmcnt(5)\n\t" \
  "v_mfma_f32_16x16x32_bf16 v[16:19], a[56:59], v[176:179], v[16:19]\n\t" \
  "ds_read_b128 v[200:203], v2 offset:32768\n\t" \
  "v_mfma_f32_16x16x32_bf16 v[20:23], a[128:131], v[176:179], v[20:23]\n\t" \
  "v_add_u32 v7, s62, v3\n\t" \
  "s_mov_b32 exec_lo, 0\n\t" \
  "v_mov_b32 v7, s80\n\t" \
  "s_mov_b32 exec_lo, -1\n\t" \
  "v_mfma_f32_16x16x32_bf16 v[24:27], a[200:203], v[176:179], v[24:27]\n\t" \
  "ds_read_b128 v[204:207], v2 offset:34816\n\t" \
  "v_mfma_f32_16x16x32_bf16 v[28:31], v[240:243], v[176:179], v[28:31]\n\t" \
  "s_waitcnt lgkmcnt(5)\n\t" \
  "v_mfma_f32_16x16x32_bf16 v[16:19], a[60:63], v[184:187], v[16:19]\n\t" \
  "v_mfma_f32_16x16x32_bf16 v[20:23], a[132:135], v[184:187], v[20:23]\n\t" \
  "buffer_load_dword v4, v7, %[X], 0 offen sc1\n\t" \
  "v_mfma_f32_16x16x32_bf16 v[24:27], a[204:207], v[184:187], v[24:27]\n\t" \
  "v_mfma_f32_16x16x32_bf16 v[28:31], v[244:247], v[184:187], v[28:31]\n\t" \
  "s_waitcnt lgkmcnt(3)\n\t" \
  "v_mfma_f32_16x16x32_bf16 v[16:19], a[64:67], v[192:195], v[16:19]\n\t" \
  "v_xor_b32 v1, 0x10000, v1\n\t" \
  "v_xor_b32 v2, 0x10000, v2\n\t" \
  "v_mfma_f32_16x16x32_bf16 v[20:23], a[136:139], v[192:195], v[20:23]\n\t" \
  "v_mfma_f32_16x16x32_bf16 v[24:27], a[208:211], v[192:195], v[24:27]\n\t" \
  "v_mfma_f32_16x16x32_bf16 v[28:31], v[248:251], v[192:195], v[28:31]\n\t" \
  "s_waitcnt lgkmcnt(1)\n\t" \
  "v_mfma_f32_16x16x32_bf16 v[16:19], a[68:71], v[200:203], v[16:19]\n\t" \
  "v_mfma_f32_16x16x32_bf16 v[20:23], a[140:143], v[200:203], v[20:23]\n\t" \
  "v_mfma_f32_16x16x32_bf16 v[24:27], a[212:215], v[200:203], v[24:27]\n\t" \
  "v_mfma_f32_16x16x32_bf16 v[28:31], v[252:255], v[200:203], v[28:31]\n\t" \
  "v_mfma_f32_16x16x32_bf16 v[32:35], a[0:3], v[68:71], 0\n\t" \
  "v_cvt_pk_bf16_f32 v48, v16, v17\n\t" \
  "v_cvt_pk_bf16_f32 v49, v18, v19\n\t" \
  "v_mfma_f32_16x16x32_bf16 v[36:39], a[72:75], v[68:71], 0\n\t" \
  "v_cvt_pk_bf16_f32 v50, v20, v21\n\t" \
  "v_cvt_pk_bf16_f32 v51, v22, v23\n\t" \
  "v_mfma_f32_16x16x32_bf16 v[32:35], a[4:7], v[76:79], v[32:35]\n\t" \
  "v_cvt_pk_bf16_f32 v52, v24, v25\n\t" \
  "v_cvt_pk_bf16_f32 v53, v26, v27\n\t" \
  "v_mfma_f32_16x16x32_bf16 v[36:39], a[76:79], v[76:79], v[36:39]\n\t" \
  "v_cvt_pk_bf16_f32 v54, v28, v29\n\t" \
  "v_cvt_pk_bf16_f32 v55, v30, v31\n\t" \
  "v_mfma_f32_16x16x32_bf16 v[32:35], a[8:11], v[84:87], v[32:35]\n\t" \
  "buffer_store_dwordx4 v[48:51], v0, %[X], s50 offen offset:0 sc1\n\t" \
  "v_mfma_f32_16x16x32_bf16 v[36:39], a[80:83], v[84:87], v[36:39]\n\t" \
  "v_mfma_f32_16x16x32_bf16 v[32:35], a[12:15], v[92:95], v[32:35]\n\t" \
  "v_mfma_f32_16x16x32_bf16 v[36:39], a[84:87], v[92:95], v[36:39]\n\t" \
  "v_mfma_f32_16x16x32_bf16 v[32:35], a[16:19], v[100:103], v[32:35]\n\t" \
  "v_mfma_f32_16x16x32_bf16 v[36:39], a[88:91], v[100:103], v[36:39]\n\t" \
  "v_mfma_f32_16x16x32_bf16 v[32:35], a[20:23], v[108:111], v[32:35]\n\t" \
  "v_mfma_f32_16x16x32_bf16 v[36:39], a[92:95], v[108:111], v[36:39]\n\t" \
  "v_mfma_f32_16x16x32_bf16 v[32:35], a[24:27], v[116:119], v[32:35]\n\t" \
  "v_mfma_f32_16x16x32_bf16 v[36:39], a[96:99], v[116:119], v[36:39]\n\t" \
  "v_mfma_f32_16x16x32_bf16 v[32:35], a[28:31], v[124:127], v[32:35]\n\t" \
  "v_mfma_f32_16x16x32_bf16 v[36:39], a[100:103], v[124:127], v[36:39]\n\t" \
  "s_waitcnt vmcnt(1)\n\t" \
  "v_cmp_eq_u32 vcc, 0, v4\n\t" \
  "v_mfma_f32_16x16x32_bf16 v[32:35], a[32:35], v[132:135], v[32:35]\n\t" \
  "v_mfma_f32_16x16x32_bf16 v[36:39], a[104:107], v[132:135], v[36:39]\n\t" \
  "s_cmp_eq_u64 vcc, 0\n\t" \
  "s_cselect_b32 s60, 1, 0\n\t" \
  "v_mov_b32 v4, s60\n\t" \
  "v_mfma_f32_16x16x32_bf16 v[32:35], a[36:39], v[140:143], v[32:35]\n\t" \
  "ds_write_b32 v5, v4\n\t" \
  "v_mfma_f32_16x16x32_bf16 v[36:39], a[108:111], v[140:143], v[36:39]\n\t" \
  "v_mfma_f32_16x16x32_bf16 v[32:35], a[40:43], v[148:151], v[32:35]\n\t" \
  "v_mfma_f32_16x16x32_bf16 v[36:39], a[112:115], v[148:151], v[36:39]\n\t" \
  "buffer_store_dwordx4 v[52:55], v0, %[X], s50 offen offset:1024 sc1\n\t" \
  "v_mfma_f32_16x16x32_bf16 v[32:35], a[44:47], v[156:159], v[32:35]\n\t" \
  "v_mfma_f32_16x16x32_bf16 v[36:39], a[116:119], v[156:159], v[36:39]\n\t" \
  "s_waitcnt lgkmcnt(0)\n\t" \
  "s_barrier\n\t" \
  "v_mfma_f32_16x16x32_bf16 v[32:35], a[48:51], v[164:167], v[32:35]\n\t" \
  "ds_read_b32 v4, v6 offset:40959\n\t" \
  "v_mfma_f32_16x16x32_bf16 v[36:39], a[120:123], v[164:167], v[36:39]\n\t" \
  "v_mfma_f32_16x16x32_bf16 v[32:35], a[52:55], v[172:175], v[32:35]\n\t" \
  "v_mfma_f32_16x16x32_bf16 v[36:39], a[124:127], v[172:175], v[36:39]\n\t" \
  "v_mfma_f32_16x16x32_bf16 v[32:35], a[56:59], v[180:183], v[32:35]\n\t" \
  "v_mfma_f32_16x16x32_bf16 v[36:39], a[128:131], v[180:183], v[36:39]\n\t" \
  "s_waitcnt lgkmcnt(0)\n\t" \
  "v_readfirstlane_b32 s60, v4\n\t" \
  "s_add_u32 s55, s83, s36\n\t" \
  "v_mfma_f32_16x16x32_bf16 v[32:35], a[60:63], v[188:191], v[32:35]\n\t" \
  "s_cmp_eq_u32 s48, 63\n\t" \
  "s_cselect_b32 s56, 0, s60\n\t" \
  "v_mfma_f32_16x16x32_bf16 v[36:39], a[132:135], v[188:191], v[36:39]\n\t" \
  "s_cmp_eq_u32 s56, 0\n\t" \
  "s_cselect_b32 s55, 0xf0000000, s55\n\t" \
  "v_mfma_f32_16x16x32_bf16 v[32:35], a[64:67], v[196:199], v[32:35]\n\t" \
  "v_mfma_f32_16x16x32_bf16 v[36:39], a[136:139], v[196:199], v[36:39]\n\t" \
  "v_mfma_f32_16x16x32_bf16 v[32:35], a[68:71], v[204:207], v[32:35]\n\t" \
  "s_add_u32 m0, s46, 0\n\t" \
  "s_add_u32 s47, s55, 0\n\t" \
  "buffer_load_dwordx4 v0, %[X], s47 offen sc1 lds\n\t" \
  "v_mfma_f32_16x16x32_bf16 v[36:39], a[140:143], v[204:207], v[36:39]\n\t" \
  "v_mfma_f32_16x16x32_bf16 v[40:43], a[144:147], v[68:71], 0\n\t" \
  "v_mfma_f32_16x16x32_bf16 v[44:47], a[216:219], v[68:71], 0\n\t" \
  "v_mfma_f32_16x16x32_bf16 v[40:43], a[148:151], v[76:79], v[40:43]\n\t" \
  "v_cvt_pk_bf16_f32 v56, v32, v33\n\t" \
  "v_cvt_pk_bf16_f32 v57, v34, v35\n\t" \
  "s_add_u32 m0, s46, 4096\n\t" \
  "s_add_u32 s47, s55, 4096\n\t" \
  "buffer_load_dwordx4 v0, %[X], s47 offen sc1 lds\n\t" \
  "v_mfma_f32_16x16x32_bf16 v[44:47], a[220:223], v[76:79], v[44:47]\n\t" \
  "v_cvt_pk_bf16_f32 v58, v36, v37\n\t" \
  "v_cvt_pk_bf16_f32 v59, v38, v39\n\t" \
  "v_mfma_f32_16x16x32_bf16 v[40:43], a[152:155], v[84:87], v[40:43]\n\t" \
  "v_mfma_f32_16x16x32_bf16 v[44:47], a[224:227], v[84:87], v[44:47]\n\t" \
  "buffer_store_dwordx4 v[56:59], v0, %[X], s51 offen offset:0 sc1\n\t" \
  "v_mfma_f32_16x16x32_bf16 v[40:43], a[156:159], v[92:95], v[40:43]\n\t" \
  "s_add_u32 m0, s46, 8192\n\t" \
  "s_add_u32 s47, s55, 16384\n\t" \
  "buffer_load_dwordx4 v0, %[X], s47 offen sc1 lds\n\t" \
  "v_mfma_f32_16x16x32_bf16 v[44:47], a[228:231], v[92:95], v[44:47]\n\t" \
  "v_mfma_f32_16x16x32_bf16 v[40:43], a[160:163], v[100:103], v[40:43]\n\t" \
  "v_mfma_f32_16x16x32_bf16 v[44:47], a[232:235], v[100:103], v[44:47]\n\t" \
  "v_mfma_f32_16x16x32_bf16 v[40:43], a[164:167], v[108:111], v[40:43]\n\t" \
  "s_add_u32 m0, s46, 12288\n\t" \
  "s_add_u32 s47, s55, 20480\n\t" \
  "buffer_load_dwordx4 v0, %[X], s47 offen sc1 lds\n\t" \
  "v_mfma_f32_16x16x32_bf16 v[44:47], a[236:239], v[108:111], v[44:47]\n\t" \
  "v_mfma_f32_16x16x32_bf16 v[40:43], a[168:171], v[116:119], v[40:43]\n\t" \
  "v_mfma_f32_16x16x32_bf16 v[44:47], a[240:243], v[116:119], v[44:47]\n\t" \
  "v_mfma_f32_16x16x32_bf16 v[40:43], a[172:175], v[124:127], v[40:43]\n\t" \
  "s_add_u32 m0, s46, 16384\n\t" \
  "s_add_u32 s47, s55, 32768\n\t" \
  "buffer_load_dwordx4 v0, %[X], s47 offen sc1 lds\n\t" \
  "v_mfma_f32_16x16x32_bf16 v[44:47], a[244:247], v[124:127], v[44:47]\n\t" \
  "v_mfma_f32_16x16x32_bf16 v[40:43], a[176:179], v[132:135], v[40:43]\n\t" \
  "v_mfma_f32_16x16x32_bf16 v[44:47], a[248:251], v[132:135], v[44:47]\n\t" \
  "v_mfma_f32_16x16x32_bf16 v[40:43], a[180:183], v[140:143], v[40:43]\n\t" \
  "s_add_u32 m0, s46, 20480\n\t" \
  "s_add_u32 s47, s55, 36864\n\t" \
  "buffer_load_dwordx4 v0, %[X], s47 offen sc1 lds\n\t" \
  "v_mfma_f32_16x16x32_bf16 v[44:47], a[252:255], v[140:143], v[44:47]\n\t" \
  "v_mfma_f32_16x16x32_bf16 v[40:43], a[184:187], v[148:151], v[40:43]\n\t" \
  "v_mfma_f32_16x16x32_bf16 v[44:47], v[224:227], v[148:151], v[44:47]\n\t" \
  "v_mfma_f32_16x16x32_bf16 v[40:43], a[188:191], v[156:159], v[40:43]\n\t" \
  "s_add_u32 m0, s46, 24576\n\t" \
  "s_add_u32 s47, s55, 49152\n\t" \
  "buffer_load_dwordx4 v0, %[X], s47 offen sc1 lds\n\t" \
  "v_mfma_f32_16x16x32_bf16 v[44:47], v[228:231], v[156:159], v[44:47]\n\t" \
  "v_mfma_f32_16x16x32_bf16 v[40:43], a[192:195], v[164:167], v[40:43]\n\t" \
  "v_mfma_f32_16x16x32_bf16 v[44:47], v[232:235], v[164:167], v[44:47]\n\t" \
  "v_mfma_f32_16x16x32_bf16 v[40:43], a[196:199], v[172:175], v[40:43]\n\t" \
  "s_add_u32 m0, s46, 28672\n\t" \
  "s_add_u32 s47, s55, 53248\n\t" \
  "buffer_load_dwordx4 v0, %[X], s47 offen sc1 lds\n\t" \
  "v_mfma_f32_16x16x32_bf16 v[44:47], v[236:239], v[172:175], v[44:47]\n\t" \
  "v_mfma_f32_16x16x32_bf16 v[40:43], a[200:203], v[180:183], v[40:43]\n\t" \
  "v_mfma_f32_16x16x32_bf16 v[44:47], v[240:243], v[180:183], v[44:47]\n\t" \
  "v_mfma_f32_16x16x32_bf16 v[40:43], a[204:207], v[188:191], v[40:43]\n\t" \
  "s_add_u32 m0, s46, 32768\n\t" \
  "s_add_u32 s47, s55, 65536\n\t" \
  "buffer_load_dwordx4 v0, %[X], s47 offen sc1 lds\n\t" \
  "v_mfma_f32_16x16x32_bf16 v[44:47], v[244:247], v[188:191], v[44:47]\n\t" \
  "v_mfma_f32_16x16x32_bf16 v[40:43], a[208:211], v[196:199], v[40:43]\n\t" \
  "v_mfma_f32_16x16x32_bf16 v[44:47], v[248:251], v[196:199], v[44:47]\n\t" \
  "buffer_load_dword v8, off, %[X], s57 sc1\n\t" \
  "v_mfma_f32_16x16x32_bf16 v[40:43], a[212:215], v[204:207], v[40:43]\n\t" \
  "v_mfma_f32_16x16x32_bf16 v[44:47], v[252:255], v[204:207], v[44:47]\n\t" \
  "s_nop 4\n\t" \
  "v_cvt_pk_bf16_f32 v60, v40, v41\n\t" \
  "v_cvt_pk_bf16_f32 v61, v42, v43\n\t" \
  "s_nop 1\n\t" \
  "v_cvt_pk_bf16_f32 v62, v44, v45\n\t" \
  "v_cvt_pk_bf16_f32 v63, v46, v47\n\t" \
  "buffer_store_dwordx4 v[60:63], v0, %[X], s51 offen offset:1024 sc1\n\t" \
  "s_add_u32 s49, s82, s38\n\t" \
  "s_cmp_eq_u32 s48, 63\n\t" \
  "s_cbranch_scc1 LEND_%=\n\t" \
  "s_add_u32 s48, s48, 1\n\t" \
  "s_cmp_eq_u32 s56, 0\n\t" \
  "s_cbranch_scc1 LCOLD_%=\n\t" \
  "s_waitcnt vmcnt(0)\n\t" \
  "s_mov_b64 exec, 1\n\t" \
  "buffer_store_dword v6, off, %[X], s49 sc1\n\t" \
  "s_mov_b64 exec, -1\n\t" \
  "s_mov_b32 s49, 0xf0000000\n\t" \
  "s_cmp_lt_i32 s57, 0\n\t" \
  "s_cbranch_scc1 LTOP_%=\n\t" \
  "s_movk_i32 s63, 4000\n\t" \
  "s_branch LGW_%=u\n\t" \
  "LGW_%=:\n\t" \
  "s_sleep " TK_PSG "\n\t" \
  "buffer_load_dword v8, off, %[X], s57 sc1\n\t" \
  "s_waitcnt vmcnt(0)\n\t" \
  "LGW_%=u:\n\t" \
  "s_memrealtime s[52:53]\n\t" \
  "v_readfirstlane_b32 s60, v8\n\t" \
  "s_waitcnt lgkmcnt(0)\n\t" \
  "s_sub_u32 s63, s63, 1\n\t" \
  "s_cbranch_scc1 LTOP_%=\n\t" \
  "s_cmp_eq_u32 s60, -1\n\t" \
  "s_cbranch_scc1 LGW_%=\n\t" \
  "s_not_b32 s60, s60\n\t" \
  "s_lshl_b32 s61, s52, 8\n\t" \
  "s_sub_u32 s60, s60, s61\n\t" \
  "s_ashr_i32 s60, s60, 8\n\t" \
  "s_sub_i32 s60, s60, " TK_QXW "\n\t" \
  "s_min_i32 s60, s60, 400\n\t" \
  "s_add_u32 s54, s52, s60\n\t" \
  "LGW_%=w:\n\t" \
  "s_sleep 1\n\t" \
  "s_memrealtime s[52:53]\n\t" \
  "s_waitcnt lgkmcnt(0)\n\t" \
  "s_sub_i32 s53, s52, s54\n\t" \
  "s_cmp_lt_i32 s53, 0\n\t" \
  "s_cbranch_scc1 LGW_%=w\n\t" \
  "s_branch LTOP_%=\n\t" \
  "LEND_%=:\n\t" \
  "s_waitcnt vmcnt(0)\n\t" \
  "s_mov_b64 exec, 1\n\t" \
  "buffer_store_dword v6, off, %[X], s49 sc1\n\t" \
  "s_mov_b64 exec, -1\n\t" \
  "s_mov_b32 s49, 0xf0000000\n\t" \
  ""
#define QV_ASM \
  "v_lshrrev_b32 v16, 7, v0\n\t" \
  "v_lshlrev_b32 v16, 13, v16\n\t" \
  "v_lshrrev_b32 v19, 2, v0\n\t" \
  "v_and_b32 v19, 64, v19\n\t" \
  "v_xor_b32 v19, v19, v0\n\t" \
  "v_and_b32 v19, 0x70, v19\n\t" \
  "v_add_u32 v16, v16, v19\n\t" \
  "s_mul_i32 s63, s40, 40960\n\t" \
  "v_and_b32 v19, 0x300, v0\n\t" \
  "v_lshlrev_b32 v17, 2, v19\n\t" \
  "v_and_b32 v19, 0xc0, v0\n\t" \
  "v_lshl_add_u32 v17, v19, 1, v17\n\t" \
  "v_and_b32 v19, 0x30, v0\n\t" \
  "v_lshrrev_b32 v19, 1, v19\n\t" \
  "v_add3_u32 v17, v17, v19, s63\n\t" \
  "v_lshrrev_b32 v19, 1, v0\n\t" \
  "v_and_b32 v19, 64, v19\n\t" \
  "v_add_u32 v17, v17, v19\n\t" \
  "v_xor_b32 v18, 64, v17\n\t" \
  "s_add_u32 m0, s63, 0\n\t" \
  "s_mov_b32 s60, 0\n\t" \
  "buffer_load_dwordx4 v16, %[W], s60 offen lds\n\t" \
  "s_add_u32 m0, s63, 1024\n\t" \
  "s_mov_b32 s60, 65536\n\t" \
  "buffer_load_dwordx4 v16, %[W], s60 offen lds\n\t" \
  "s_add_u32 m0, s63, 2048\n\t" \
  "s_mov_b32 s60, 131072\n\t" \
  "buffer_load_dwordx4 v16, %[W], s60 offen lds\n\t" \
  "s_add_u32 m0, s63, 3072\n\t" \
  "s_mov_b32 s60, 196608\n\t" \
  "buffer_load_dwordx4 v16, %[W], s60 offen lds\n\t" \
  "s_add_u32 m0, s63, 4096\n\t" \
  "s_mov_b32 s60, 262144\n\t" \
  "buffer_load_dwordx4 v16, %[W], s60 offen lds\n\t" \
  "s_add_u32 m0, s63, 5120\n\t" \
  "s_mov_b32 s60, 327680\n\t" \
  "buffer_load_dwordx4 v16, %[W], s60 offen lds\n\t" \
  "s_add_u32 m0, s63, 6144\n\t" \
  "s_mov_b32 s60, 393216\n\t" \
  "buffer_load_dwordx4 v16, %[W], s60 offen lds\n\t" \
  "s_add_u32 m0, s63, 7168\n\t" \
  "s_mov_b32 s60, 458752\n\t" \
  "buffer_load_dwordx4 v16, %[W], s60 offen lds\n\t" \
  "s_add_u32 m0, s63, 8192\n\t" \
  "s_mov_b32 s60, 524288\n\t" \
  "buffer_load_dwordx4 v16, %[W], s60 offen lds\n\t" \
  "s_add_u32 m0, s63, 9216\n\t" \
  "s_mov_b32 s60, 589824\n\t" \
  "buffer_load_dwordx4 v16, %[W], s60 offen lds\n\t" \
  "s_add_u32 m0, s63, 10240\n\t" \
  "s_mov_b32 s60, 655360\n\t" \
  "buffer_load_dwordx4 v16, %[W], s60 offen lds\n\t" \
  "s_add_u32 m0, s63, 11264\n\t" \
  "s_mov_b32 s60, 720896\n\t" \
  "buffer_load_dwordx4 v16, %[W], s60 offen lds\n\t" \
  "s_add_u32 m0, s63, 12288\n\t" \
  "s_mov_b32 s60, 786432\n\t" \
  "buffer_load_dwordx4 v16, %[W], s60 offen lds\n\t" \
  "s_add_u32 m0, s63, 13312\n\t" \
  "s_mov_b32 s60, 851968\n\t" \
  "buffer_load_dwordx4 v16, %[W], s60 offen lds\n\t" \
  "s_add_u32 m0, s63, 14336\n\t" \
  "s_mov_b32 s60, 917504\n\t" \
  "buffer_load_dwordx4 v16, %[W], s60 offen lds\n\t" \
  "s_add_u32 m0, s63, 15360\n\t" \
  "s_mov_b32 s60, 983040\n\t" \
  "buffer_load_dwordx4 v16, %[W], s60 offen lds\n\t" \
  "s_add_u32 m0, s63, 16384\n\t" \
  "s_mov_b32 s60, 1048576\n\t" \
  "buffer_load_dwordx4 v16, %[W], s60 offen lds\n\t" \
  "s_add_u32 m0, s63, 17408\n\t" \
  "s_mov_b32 s60, 1114112\n\t" \
  "buffer_load_dwordx4 v16, %[W], s60 offen lds\n\t" \
  "s_add_u32 m0, s63, 18432\n\t" \
  "s_mov_b32 s60, 1179648\n\t" \
  "buffer_load_dwordx4 v16, %[W], s60 offen lds\n\t" \
  "s_add_u32 m0, s63, 19456\n\t" \
  "s_mov_b32 s60, 1245184\n\t" \
  "buffer_load_dwordx4 v16, %[W], s60 offen lds\n\t" \
  "s_add_u32 m0, s63, 20480\n\t" \
  "s_mov_b32 s60, 1310720\n\t" \
  "buffer_load_dwordx4 v16, %[W], s60 offen lds\n\t" \
  "s_add_u32 m0, s63, 21504\n\t" \
  "s_mov_b32 s60, 1376256\n\t" \
  "buffer_load_dwordx4 v16, %[W], s60 offen lds\n\t" \
  "s_add_u32 m0, s63, 22528\n\t" \
  "s_mov_b32 s60, 1441792\n\t" \
  "buffer_load_dwordx4 v16, %[W], s60 offen lds\n\t" \
  "s_add_u32 m0, s63, 23552\n\t" \
  "s_mov_b32 s60, 1507328\n\t" \
  "buffer_load_dwordx4 v16, %[W], s60 offen lds\n\t" \
  "s_add_u32 m0, s63, 24576\n\t" \
  "s_mov_b32 s60, 1572864\n\t" \
  "buffer_load_dwordx4 v16, %[W], s60 offen lds\n\t" \
  "s_add_u32 m0, s63, 25600\n\t" \
  "s_mov_b32 s60, 1638400\n\t" \
  "buffer_load_dwordx4 v16, %[W], s60 offen lds\n\t" \
  "s_add_u32 m0, s63, 26624\n\t" \
  "s_mov_b32 s60, 1703936\n\t" \
  "buffer_load_dwordx4 v16, %[W], s60 offen lds\n\t" \
  "s_add_u32 m0, s63, 27648\n\t" \
  "s_mov_b32 s60, 1769472\n\t" \
  "buffer_load_dwordx4 v16, %[W], s60 offen lds\n\t" \
  "s_add_u32 m0, s63, 28672\n\t" \
  "s_mov_b32 s60, 1835008\n\t" \
  "buffer_load_dwordx4 v16, %[W], s60 offen lds\n\t" \
  "s_add_u32 m0, s63, 29696\n\t" \
  "s_mov_b32 s60, 1900544\n\t" \
  "buffer_load_dwordx4 v16, %[W], s60 offen lds\n\t" \
  "s_add_u32 m0, s63, 30720\n\t" \
  "s_mov_b32 s60, 1966080\n\t" \
  "buffer_load_dwordx4 v16, %[W], s60 offen lds\n\t" \
  "s_add_u32 m0, s63, 31744\n\t" \
  "s_mov_b32 s60, 2031616\n\t" \
  "buffer_load_dwordx4 v16, %[W], s60 offen lds\n\t" \
  "s_add_u32 m0, s63, 32768\n\t" \
  "s_mov_b32 s60, 2097152\n\t" \
  "buffer_load_dwordx4 v16, %[W], s60 offen lds\n\t" \
  "s_add_u32 m0, s63, 33792\n\t" \
  "s_mov_b32 s60, 2162688\n\t" \
  "buffer_load_dwordx4 v16, %[W], s60 offen lds\n\t" \
  "s_add_u32 m0, s63, 34816\n\t" \
  "s_mov_b32 s60, 2228224\n\t" \
  "buffer_load_dwordx4 v16, %[W], s60 offen lds\n\t" \
  "s_add_u32 m0, s63, 35840\n\t" \
  "s_mov_b32 s60, 2293760\n\t" \
  "buffer_load_dwordx4 v16, %[W], s60 offen lds\n\t" \
  "s_add_u32 m0, s63, 36864\n\t" \
  "s_mov_b32 s60, 2359296\n\t" \
  "buffer_load_dwordx4 v16, %[W], s60 offen lds\n\t" \
  "s_add_u32 m0, s63, 37888\n\t" \
  "s_mov_b32 s60, 2424832\n\t" \
  "buffer_load_dwordx4 v16, %[W], s60 offen lds\n\t" \
  "s_add_u32 m0, s63, 38912\n\t" \
  "s_mov_b32 s60, 2490368\n\t" \
  "buffer_load_dwordx4 v16, %[W], s60 offen lds\n\t" \
  "s_add_u32 m0, s63, 39936\n\t" \
  "s_mov_b32 s60, 2555904\n\t" \
  "buffer_load_dwordx4 v16, %[W], s60 offen lds\n\t" \
  "s_waitcnt vmcnt(32)\n\t" \
  "ds_read_b64_tr_b16 a[0:1], v17 offset:0\n\t" \
  "ds_read_b64_tr_b16 a[2:3], v17 offset:512\n\t" \
  "ds_read_b64_tr_b16 a[4:5], v17 offset:4096\n\t" \
  "ds_read_b64_tr_b16 a[6:7], v17 offset:4608\n\t" \
  "ds_read_b64_tr_b16 a[72:73], v17 offset:32\n\t" \
  "ds_read_b64_tr_b16 a[74:75], v17 offset:544\n\t" \
  "ds_read_b64_tr_b16 a[76:77], v17 offset:4128\n\t" \
  "ds_read_b64_tr_b16 a[78:79], v17 offset:4640\n\t" \
  "ds_read_b64_tr_b16 a[144:145], v18 offset:0\n\t" \
  "ds_read_b64_tr_b16 a[146:147], v18 offset:512\n\t" \
  "ds_read_b64_tr_b16 a[148:149], v18 offset:4096\n\t" \
  "ds_read_b64_tr_b16 a[150:151], v18 offset:4608\n\t" \
  "ds_read_b64_tr_b16 a[216:217], v18 offset:32\n\t" \
  "ds_read_b64_tr_b16 a[218:219], v18 offset:544\n\t" \
  "ds_read_b64_tr_b16 a[220:221], v18 offset:4128\n\t" \
  "ds_read_b64_tr_b16 a[222:223], v18 offset:4640\n\t" \
  "s_waitcnt lgkmcnt(0)\n\t" \
  "s_add_u32 m0, s63, 0\n\t" \
  "s_mov_b32 s60, 2621440\n\t" \
  "buffer_load_dwordx4 v16, %[W], s60 offen lds\n\t" \
  "s_add_u32 m0, s63, 1024\n\t" \
  "s_mov_b32 s60, 2686976\n\t" \
  "buffer_load_dwordx4 v16, %[W], s60 offen lds\n\t" \
  "s_add_u32 m0, s63, 2048\n\t" \
  "s_mov_b32 s60, 2752512\n\t" \
  "buffer_load_dwordx4 v16, %[W], s60 offen lds\n\t" \
  "s_add_u32 m0, s63, 3072\n\t" \
  "s_mov_b32 s60, 2818048\n\t" \
  "buffer_load_dwordx4 v16, %[W], s60 offen lds\n\t" \
  "s_add_u32 m0, s63, 4096\n\t" \
  "s_mov_b32 s60, 2883584\n\t" \
  "buffer_load_dwordx4 v16, %[W], s60 offen lds\n\t" \
  "s_add_u32 m0, s63, 5120\n\t" \
  "s_mov_b32 s60, 2949120\n\t" \
  "buffer_load_dwordx4 v16, %[W], s60 offen lds\n\t" \
  "s_add_u32 m0, s63, 6144\n\t" \
  "s_mov_b32 s60, 3014656\n\t" \
  "buffer_load_dwordx4 v16, %[W], s60 offen lds\n\t" \
  "s_add_u32 m0, s63, 7168\n\t" \
  "s_mov_b32 s60, 3080192\n\t" \
  "buffer_load_dwordx4 v16, %[W], s60 offen lds\n\t" \
  "s_waitcnt vmcnt(32)\n\t" \
  "ds_read_b64_tr_b16 a[8:9], v17 offset:8192\n\t" \
  "ds_read_b64_tr_b16 a[10:11], v17 offset:8704\n\t" \
  "ds_read_b64_tr_b16 a[12:13], v17 offset:12288\n\t" \
  "ds_read_b64_tr_b16 a[14:15], v17 offset:12800\n\t" \
  "ds_read_b64_tr_b16 a[80:81], v17 offset:8224\n\t" \
  "ds_read_b64_tr_b16 a[82:83], v17 offset:8736\n\t" \
  "ds_read_b64_tr_b16 a[84:85], v17 offset:12320\n\t" \
  "ds_read_b64_tr_b16 a[86:87], v17 offset:12832\n\t" \
  "ds_read_b64_tr_b16 a[152:153], v18 offset:8192\n\t" \
  "ds_read_b64_tr_b16 a[154:155], v18 offset:8704\n\t" \
  "ds_read_b64_tr_b16 a[156:157], v18 offset:12288\n\t" \
  "ds_read_b64_tr_b16 a[158:159], v18 offset:12800\n\t" \
  "ds_read_b64_tr_b16 a[224:225], v18 offset:8224\n\t" \
  "ds_read_b64_tr_b16 a[226:227], v18 offset:8736\n\t" \
  "ds_read_b64_tr_b16 a[228:229], v18 offset:12320\n\t" \
  "ds_read_b64_tr_b16 a[230:231], v18 offset:12832\n\t" \
  "s_waitcnt lgkmcnt(0)\n\t" \
  "s_add_u32 m0, s63, 8192\n\t" \
  "s_mov_b32 s60, 3145728\n\t" \
  "buffer_load_dwordx4 v16, %[W], s60 offen lds\n\t" \
  "s_add_u32 m0, s63, 9216\n\t" \
  "s_mov_b32 s60, 3211264\n\t" \
  "buffer_load_dwordx4 v16, %[W], s60 offen lds\n\t" \
  "s_add_u32 m0, s63, 10240\n\t" \
  "s_mov_b32 s60, 3276800\n\t" \
  "buffer_load_dwordx4 v16, %[W], s60 offen lds\n\t" \
  "s_add_u32 m0, s63, 11264\n\t" \
  "s_mov_b32 s60, 3342336\n\t" \
  "buffer_load_dwordx4 v16, %[W], s60 offen lds\n\t" \
  "s_add_u32 m0, s63, 12288\n\t" \
  "s_mov_b32 s60, 3407872\n\t" \
  "buffer_load_dwordx4 v16, %[W], s60 offen lds\n\t" \
  "s_add_u32 m0, s63, 13312\n\t" \
  "s_mov_b32 s60, 3473408\n\t" \
  "buffer_load_dwordx4 v16, %[W], s60 offen lds\n\t" \
  "s_add_u32 m0, s63, 14336\n\t" \
  "s_mov_b32 s60, 3538944\n\t" \
  "buffer_load_dwordx4 v16, %[W], s60 offen lds\n\t" \
  "s_add_u32 m0, s63, 15360\n\t" \
  "s_mov_b32 s60, 3604480\n\t" \
  "buffer_load_dwordx4 v16, %[W], s60 offen lds\n\t" \
  "s_waitcnt vmcnt(32)\n\t" \
  "ds_read_b64_tr_b16 a[16:17], v17 offset:16384\n\t" \
  "ds_read_b64_tr_b16 a[18:19], v17 offset:16896\n\t" \
  "ds_read_b64_tr_b16 a[20:21], v17 offset:20480\n\t" \
  "ds_read_b64_tr_b16 a[22:23], v17 offset:20992\n\t" \
  "ds_read_b64_tr_b16 a[88:89], v17 offset:16416\n\t" \
  "ds_read_b64_tr_b16 a[90:91], v17 offset:16928\n\t" \
  "ds_read_b64_tr_b16 a[92:93], v17 offset:20512\n\t" \
  "ds_read_b64_tr_b16 a[94:95], v17 offset:21024\n\t" \
  "ds_read_b64_tr_b16 a[160:161], v18 offset:16384\n\t" \
  "ds_read_b64_tr_b16 a[162:163], v18 offset:16896\n\t" \
  "ds_read_b64_tr_b16 a[164:165], v18 offset:20480\n\t" \
  "ds_read_b64_tr_b16 a[166:167], v18 offset:20992\n\t" \
  "ds_read_b64_tr_b16 a[232:233], v18 offset:16416\n\t" \
  "ds_read_b64_tr_b16 a[234:235], v18 offset:16928\n\t" \
  "ds_read_b64_tr_b16 a[236:237], v18 offset:20512\n\t" \
  "ds_read_b64_tr_b16 a[238:239], v18 offset:21024\n\t" \
  "s_waitcnt lgkmcnt(0)\n\t" \
  "s_add_u32 m0, s63, 16384\n\t" \
  "s_mov_b32 s60, 3670016\n\t" \
  "buffer_load_dwordx4 v16, %[W], s60 offen lds\n\t" \
  "s_add_u32 m0, s63, 17408\n\t" \
  "s_mov_b32 s60, 3735552\n\t" \
  "buffer_load_dwordx4 v16, %[W], s60 offen lds\n\t" \
  "s_add_u32 m0, s63, 18432\n\t" \
  "s_mov_b32 s60, 3801088\n\t" \
  "buffer_load_dwordx4 v16, %[W], s60 offen lds\n\t" \
  "s_add_u32 m0, s63, 19456\n\t" \
  "s_mov_b32 s60, 3866624\n\t" \
  "buffer_load_dwordx4 v16, %[W], s60 offen lds\n\t" \
  "s_add_u32 m0, s63, 20480\n\t" \
  "s_mov_b32 s60, 3932160\n\t" \
  "buffer_load_dwordx4 v16, %[W], s60 offen lds\n\t" \
  "s_add_u32 m0, s63, 21504\n\t" \
  "s_mov_b32 s60, 3997696\n\t" \
  "buffer_load_dwordx4 v16, %[W], s60 offen lds\n\t" \
  "s_add_u32 m0, s63, 22528\n\t" \
  "s_mov_b32 s60, 4063232\n\t" \
  "buffer_load_dwordx4 v16, %[W], s60 offen lds\n\t" \
  "s_add_u32 m0, s63, 23552\n\t" \
  "s_mov_b32 s60, 4128768\n\t" \
  "buffer_load_dwordx4 v16, %[W], s60 offen lds\n\t" \
  "s_waitcnt vmcnt(32)\n\t" \
  "ds_read_b64_tr_b16 a[24:25], v17 offset:24576\n\t" \
  "ds_read_b64_tr_b16 a[26:27], v17 offset:25088\n\t" \
  "ds_read_b64_tr_b16 a[28:29], v17 offset:28672\n\t" \
  "ds_read_b64_tr_b16 a[30:31], v17 offset:29184\n\t" \
  "ds_read_b64_tr_b16 a[96:97], v17 offset:24608\n\t" \
  "ds_read_b64_tr_b16 a[98:99], v17 offset:25120\n\t" \
  "ds_read_b64_tr_b16 a[100:101], v17 offset:28704\n\t" \
  "ds_read_b64_tr_b16 a[102:103], v17 offset:29216\n\t" \
  "ds_read_b64_tr_b16 a[168:169], v18 offset:24576\n\t" \
  "ds_read_b64_tr_b16 a[170:171], v18 offset:25088\n\t" \
  "ds_read_b64_tr_b16 a[172:173], v18 offset:28672\n\t" \
  "ds_read_b64_tr_b16 a[174:175], v18 offset:29184\n\t" \
  "ds_read_b64_tr_b16 a[240:241], v18 offset:24608\n\t" \
  "ds_read_b64_tr_b16 a[242:243], v18 offset:25120\n\t" \
  "ds_read_b64_tr_b16 a[244:245], v18 offset:28704\n\t" \
  "ds_read_b64_tr_b16 a[246:247], v18 offset:29216\n\t" \
  "s_waitcnt lgkmcnt(0)\n\t" \
  "s_add_u32 m0, s63, 24576\n\t" \
  "s_mov_b32 s60, 4194304\n\t" \
  "buffer_load_dwordx4 v16, %[W], s60 offen lds\n\t" \
  "s_add_u32 m0, s63, 25600\n\t" \
  "s_mov_b32 s60, 4259840\n\t" \
  "buffer_load_dwordx4 v16, %[W], s60 offen lds\n\t" \
  "s_add_u32 m0, s63, 26624\n\t" \
  "s_mov_b32 s60, 4325376\n\t" \
  "buffer_load_dwordx4 v16, %[W], s60 offen lds\n\t" \
  "s_add_u32 m0, s63, 27648\n\t" \
  "s_mov_b32 s60, 4390912\n\t" \
  "buffer_load_dwordx4 v16, %[W], s60 offen lds\n\t" \
  "s_add_u32 m0, s63, 28672\n\t" \
  "s_mov_b32 s60, 4456448\n\t" \
  "buffer_load_dwordx4 v16, %[W], s60 offen lds\n\t" \
  "s_add_u32 m0, s63, 29696\n\t" \
  "s_mov_b32 s60, 4521984\n\t" \
  "buffer_load_dwordx4 v16, %[W], s60 offen lds\n\t" \
  "s_add_u32 m0, s63, 30720\n\t" \
  "s_mov_b32 s60, 4587520\n\t" \
  "buffer_load_dwordx4 v16, %[W], s60 offen lds\n\t" \
  "s_add_u32 m0, s63, 31744\n\t" \
  "s_mov_b32 s60, 4653056\n\t" \
  "buffer_load_dwordx4 v16, %[W], s60 offen lds\n\t" \
  "s_waitcnt vmcnt(32)\n\t" \
  "ds_read_b64_tr_b16 a[32:33], v17 offset:32768\n\t" \
  "ds_read_b64_tr_b16 a[34:35], v17 offset:33280\n\t" \
  "ds_read_b64_tr_b16 a[36:37], v17 offset:36864\n\t" \
  "ds_read_b64_tr_b16 a[38:39], v17 offset:37376\n\t" \
  "ds_read_b64_tr_b16 a[104:105], v17 offset:32800\n\t" \
  "ds_read_b64_tr_b16 a[106:107], v17 offset:33312\n\t" \
  "ds_read_b64_tr_b16 a[108:109], v17 offset:36896\n\t" \
  "ds_read_b64_tr_b16 a[110:111], v17 offset:37408\n\t" \
  "ds_read_b64_tr_b16 a[176:177], v18 offset:32768\n\t" \
  "ds_read_b64_tr_b16 a[178:179], v18 offset:33280\n\t" \
  "ds_read_b64_tr_b16 a[180:181], v18 offset:36864\n\t" \
  "ds_read_b64_tr_b16 a[182:183], v18 offset:37376\n\t" \
  "ds_read_b64_tr_b16 a[248:249], v18 offset:32800\n\t" \
  "ds_read_b64_tr_b16 a[250:251], v18 offset:33312\n\t" \
  "ds_read_b64_tr_b16 a[252:253], v18 offset:36896\n\t" \
  "ds_read_b64_tr_b16 a[254:255], v18 offset:37408\n\t" \
  "s_waitcnt lgkmcnt(0)\n\t" \
  "s_waitcnt vmcnt(24)\n\t" \
  "ds_read_b64_tr_b16 a[40:41], v17 offset:0\n\t" \
  "ds_read_b64_tr_b16 a[42:43], v17 offset:512\n\t" \
  "ds_read_b64_tr_b16 a[44:45], v17 offset:4096\n\t" \
  "ds_read_b64_tr_b16 a[46:47], v17 offset:4608\n\t" \
  "ds_read_b64_tr_b16 a[112:113], v17 offset:32\n\t" \
  "ds_read_b64_tr_b16 a[114:115], v17 offset:544\n\t" \
  "ds_read_b64_tr_b16 a[116:117], v17 offset:4128\n\t" \
  "ds_read_b64_tr_b16 a[118:119], v17 offset:4640\n\t" \
  "ds_read_b64_tr_b16 a[184:185], v18 offset:0\n\t" \
  "ds_read_b64_tr_b16 a[186:187], v18 offset:512\n\t" \
  "ds_read_b64_tr_b16 a[188:189], v18 offset:4096\n\t" \
  "ds_read_b64_tr_b16 a[190:191], v18 offset:4608\n\t" \
  "ds_read_b64_tr_b16 v[224:225], v18 offset:32\n\t" \
  "ds_read_b64_tr_b16 v[226:227], v18 offset:544\n\t" \
  "ds_read_b64_tr_b16 v[228:229], v18 offset:4128\n\t" \
  "ds_read_b64_tr_b16 v[230:231], v18 offset:4640\n\t" \
  "s_waitcnt lgkmcnt(0)\n\t" \
  "s_waitcnt vmcnt(16)\n\t" \
  "ds_read_b64_tr_b16 a[48:49], v17 offset:8192\n\t" \
  "ds_read_b64_tr_b16 a[50:51], v17 offset:8704\n\t" \
  "ds_read_b64_tr_b16 a[52:53], v17 offset:12288\n\t" \
  "ds_read_b64_tr_b16 a[54:55], v17 offset:12800\n\t" \
  "ds_read_b64_tr_b16 a[120:121], v17 offset:8224\n\t" \
  "ds_read_b64_tr_b16 a[122:123], v17 offset:8736\n\t" \
  "ds_read_b64_tr_b16 a[124:125], v17 offset:12320\n\t" \
  "ds_read_b64_tr_b16 a[126:127], v17 offset:12832\n\t" \
  "ds_read_b64_tr_b16 a[192:193], v18 offset:8192\n\t" \
  "ds_read_b64_tr_b16 a[194:195], v18 offset:8704\n\t" \
  "ds_read_b64_tr_b16 a[196:197], v18 offset:12288\n\t" \
  "ds_read_b64_tr_b16 a[198:199], v18 offset:12800\n\t" \
  "ds_read_b64_tr_b16 v[232:233], v18 offset:8224\n\t" \
  "ds_read_b64_tr_b16 v[234:235], v18 offset:8736\n\t" \
  "ds_read_b64_tr_b16 v[236:237], v18 offset:12320\n\t" \
  "ds_read_b64_tr_b16 v[238:239], v18 offset:12832\n\t" \
  "s_waitcnt lgkmcnt(0)\n\t" \
  "s_waitcnt vmcnt(8)\n\t" \
  "ds_read_b64_tr_b16 a[56:57], v17 offset:16384\n\t" \
  "ds_read_b64_tr_b16 a[58:59], v17 offset:16896\n\t" \
  "ds_read_b64_tr_b16 a[60:61], v17 offset:20480\n\t" \
  "ds_read_b64_tr_b16 a[62:63], v17 offset:20992\n\t" \
  "ds_read_b64_tr_b16 a[128:129], v17 offset:16416\n\t" \
  "ds_read_b64_tr_b16 a[130:131], v17 offset:16928\n\t" \
  "ds_read_b64_tr_b16 a[132:133], v17 offset:20512\n\t" \
  "ds_read_b64_tr_b16 a[134:135], v17 offset:21024\n\t" \
  "ds_read_b64_tr_b16 a[200:201], v18 offset:16384\n\t" \
  "ds_read_b64_tr_b16 a[202:203], v18 offset:16896\n\t" \
  "ds_read_b64_tr_b16 a[204:205], v18 offset:20480\n\t" \
  "ds_read_b64_tr_b16 a[206:207], v18 offset:20992\n\t" \
  "ds_read_b64_tr_b16 v[240:241], v18 offset:16416\n\t" \
  "ds_read_b64_tr_b16 v[242:243], v18 offset:16928\n\t" \
  "ds_read_b64_tr_b16 v[244:245], v18 offset:20512\n\t" \
  "ds_read_b64_tr_b16 v[246:247], v18 offset:21024\n\t" \
  "s_waitcnt lgkmcnt(0)\n\t" \
  "s_waitcnt vmcnt(0)\n\t" \
  "ds_read_b64_tr_b16 a[64:65], v17 offset:24576\n\t" \
  "ds_read_b64_tr_b16 a[66:67], v17 offset:25088\n\t" \
  "ds_read_b64_tr_b16 a[68:69], v17 offset:28672\n\t" \
  "ds_read_b64_tr_b16 a[70:71], v17 offset:29184\n\t" \
  "ds_read_b64_tr_b16 a[136:137], v17 offset:24608\n\t" \
  "ds_read_b64_tr_b16 a[138:139], v17 offset:25120\n\t" \
  "ds_read_b64_tr_b16 a[140:141], v17 offset:28704\n\t" \
  "ds_read_b64_tr_b16 a[142:143], v17 offset:29216\n\t" \
  "ds_read_b64_tr_b16 a[208:209], v18 offset:24576\n\t" \
  "ds_read_b64_tr_b16 a[210:211], v18 offset:25088\n\t" \
  "ds_read_b64_tr_b16 a[212:213], v18 offset:28672\n\t" \
  "ds_read_b64_tr_b16 a[214:215], v18 offset:29184\n\t" \
  "ds_read_b64_tr_b16 v[248:249], v18 offset:24608\n\t" \
  "ds_read_b64_tr_b16 v[250:251], v18 offset:25120\n\t" \
  "ds_read_b64_tr_b16 v[252:253], v18 offset:28704\n\t" \
  "ds_read_b64_tr_b16 v[254:255], v18 offset:29216\n\t" \
  "s_waitcnt lgkmcnt(0)\n\t" \
  "s_mov_b32 s84, " TK_CAP "\n\t" \
  "s_mov_b32 s48, 0\n\t" \
  "s_mov_b32 s49, 0xf0000000\n\t" \
  "s_mov_b32 s46, s41\n\t" \
  "s_mov_b32 s56, 0\n\t" \
  "v_lshrrev_b32 v7, 2, v0\n\t" \
  "s_mov_b32 s60, " TK_CTAB "\n\t" \
  "buffer_load_dword v4, v7, %[X], s60 offen offset:88 sc1\n\t" \
  "buffer_load_dword v7, v7, %[X], s60 offen offset:0 sc1\n\t" \
  "s_waitcnt vmcnt(0)\n\t" \
  "v_and_b32 v4, 1, v4\n\t" \
  "v_and_b32 v7, 1, v7\n\t" \
  "v_cmp_ne_u32 s[90:91], s67, v4\n\t" \
  "v_cmp_ne_u32 s[88:89], s67, v7\n\t" \
  "s_nop 1\n\t" \
  "s_mov_b32 s57, 0xf0000000\n\t" \
  "s_add_u32 s83, s48, 0\n\t" \
  "s_lshr_b64 s[60:61], s[90:91], s83\n\t" \
  "s_and_b32 s60, s60, 1\n\t" \
  "s_lshl_b32 s60, s60, 13\n\t" \
  "s_add_u32 s83, s83, " TK_SG0 "\n\t" \
  "s_lshl_b32 s83, s83, 21\n\t" \
  "s_or_b32 s83, s83, s60\n\t" \
  "s_add_u32 s80, s48, " TK_TRN "\n\t" \
  "s_lshr_b64 s[60:61], s[90:91], s80\n\t" \
  "s_and_b32 s60, s60, 1\n\t" \
  "s_lshl_b32 s60, s60, 13\n\t" \
  "s_add_u32 s80, s80, " TK_SG0 "\n\t" \
  "s_lshl_b32 s80, s80, 21\n\t" \
  "s_or_b32 s80, s80, s60\n\t" \
  "s_add_u32 s62, s83, s39\n\t" \
  "s_add_u32 s80, s80, s39\n\t" \
  "s_add_u32 s80, s80, 2048\n\t" \
  "s_cmp_lt_u32 s48, " TK_TRM "\n\t" \
  "s_cselect_b32 s80, s62, s80\n\t" \
  "v_add_u32 v7, s62, v3\n\t" \
  "s_mov_b32 exec_lo, 0\n\t" \
  "v_mov_b32 v7, s80\n\t" \
  "s_mov_b32 exec_lo, -1\n\t" \
  "LCOLD_%=:\n\t" \
  "s_waitcnt vmcnt(0)\n\t" \
  "s_mov_b64 exec, 1\n\t" \
  "buffer_store_dword v6, off, %[X], s49 sc1\n\t" \
  "s_mov_b64 exec, -1\n\t" \
  "s_mov_b32 s49, 0xf0000000\n\t" \
  "s_add_u32 s55, s83, s36\n\t" \
  "s_cmp_lg_u32 s40, 0\n\t" \
  "s_cbranch_scc1 LGO_%=\n\t" \
  "s_and_b32 s62, s45, 3\n\t" \
  "s_lshl_b32 s62, s62, 7\n\t" \
  "s_add_u32 s62, s62, s83\n\t" \
  "s_add_u32 s62, s62, s39\n\t" \
  "s_mov_b32 s61, 0\n\t" \
  "LPO_%=:\n\t" \
  "buffer_load_dword v4, off, %[X], s62 sc1\n\t" \
  "s_waitcnt vmcnt(0)\n\t" \
  "v_cmp_eq_u32 vcc, 0, v4\n\t" \
  "s_cbranch_vccz LPO_%=x\n\t" \
  "s_add_u32 s61, s61, 1\n\t" \
  "s_sleep " TK_PSH "\n\t" \
  "s_cmp_gt_u32 s61, s84\n\t" \
  "s_cbranch_scc1 LPO_%=g\n\t" \
  "s_and_b32 s60, s61, 1023\n\t" \
  "s_cmp_lg_u32 s60, 0\n\t" \
  "s_cbranch_scc1 LPO_%=\n\t" \
  "buffer_load_dword v4, off, %[X], s92 sc1\n\t" \
  "s_waitcnt vmcnt(0)\n\t" \
  "v_readfirstlane_b32 s60, v4\n\t" \
  "s_nop 0\n\t" \
  "s_cmp_eq_u32 s60, 0\n\t" \
  "s_cbranch_scc1 LPO_%=\n\t" \
  "LPO_%=g:\n\t" \
  "s_mov_b32 s84, 0\n\t" \
  "s_or_b32 s60, s48, 1610612736\n\t" \
  "v_mov_b32 v4, s60\n\t" \
  "s_mov_b64 exec, 1\n\t" \
  "buffer_store_dword v4, off, %[X], s92 sc1\n\t" \
  "s_mov_b64 exec, -1\n\t" \
  "LPO_%=x:\n\t" \
  "s_mov_b32 s61, 0\n\t" \
  "LPH_%=:\n\t" \
  "buffer_load_dword v4, v7, %[X], 0 offen sc1\n\t" \
  "s_waitcnt vmcnt(0)\n\t" \
  "v_cmp_eq_u32 vcc, 0, v4\n\t" \
  "s_cbranch_vccz LPH_%=x\n\t" \
  "s_add_u32 s61, s61, 1\n\t" \
  "s_sleep " TK_PSH "\n\t" \
  "s_cmp_gt_u32 s61, s84\n\t" \
  "s_cbranch_scc1 LPH_%=g\n\t" \
  "s_and_b32 s60, s61, 1023\n\t" \
  "s_cmp_lg_u32 s60, 0\n\t" \
  "s_cbranch_scc1 LPH_%=\n\t" \
  "buffer_load_dword v4, off, %[X], s92 sc1\n\t" \
  "s_waitcnt vmcnt(0)\n\t" \
  "v_readfirstlane_b32 s60, v4\n\t" \
  "s_nop 0\n\t" \
  "s_cmp_eq_u32 s60, 0\n\t" \
  "s_cbranch_scc1 LPH_%=\n\t" \
  "LPH_%=g:\n\t" \
  "s_mov_b32 s84, 0\n\t" \
  "s_or_b32 s60, s48, 1610612736\n\t" \
  "v_mov_b32 v4, s60\n\t" \
  "s_mov_b64 exec, 1\n\t" \
  "buffer_store_dword v4, off, %[X], s92 sc1\n\t" \
  "s_mov_b64 exec, -1\n\t" \
  "LPH_%=x:\n\t" \
  "s_cmp_lt_i32 s57, 0\n\t" \
  "s_cbranch_scc1 LGO_%=\n\t" \
  "s_movk_i32 s63, 4000\n\t" \
  "s_branch LGC_%=u\n\t" \
  "LGC_%=:\n\t" \
  "s_sleep " TK_PSG "\n\t" \
  "buffer_load_dword v8, off, %[X], s57 sc1\n\t" \
  "s_waitcnt vmcnt(0)\n\t" \
  "LGC_%=u:\n\t" \
  "s_memrealtime s[52:53]\n\t" \
  "v_readfirstlane_b32 s60, v8\n\t" \
  "s_waitcnt lgkmcnt(0)\n\t" \
  "s_sub_u32 s63, s63, 1\n\t" \
  "s_cbranch_scc1 LGO_%=\n\t" \
  "s_cmp_eq_u32 s60, -1\n\t" \
  "s_cbranch_scc1 LGC_%=\n\t" \
  "s_not_b32 s60, s60\n\t" \
  "s_lshl_b32 s61, s52, 8\n\t" \
  "s_sub_u32 s60, s60, s61\n\t" \
  "s_ashr_i32 s60, s60, 8\n\t" \
  "s_sub_i32 s60, s60, " TK_QXC "\n\t" \
  "s_min_i32 s60, s60, 400\n\t" \
  "s_add_u32 s54, s52, s60\n\t" \
  "LGC_%=w:\n\t" \
  "s_sleep 1\n\t" \
  "s_memrealtime s[52:53]\n\t" \
  "s_waitcnt lgkmcnt(0)\n\t" \
  "s_sub_i32 s53, s52, s54\n\t" \
  "s_cmp_lt_i32 s53, 0\n\t" \
  "s_cbranch_scc1 LGC_%=w\n\t" \
  "LGO_%=:\n\t" \
  "s_barrier\n\t" \
  "s_add_u32 m0, s46, 0\n\t" \
  "s_add_u32 s47, s55, 0\n\t" \
  "buffer_load_dwordx4 v0, %[X], s47 offen sc1 lds\n\t" \
  "s_add_u32 m0, s46, 4096\n\t" \
  "s_add_u32 s47, s55, 4096\n\t" \
  "buffer_load_dwordx4 v0, %[X], s47 offen sc1 lds\n\t" \
  "s_add_u32 m0, s46, 8192\n\t" \
  "s_add_u32 s47, s55, 16384\n\t" \
  "buffer_load_dwordx4 v0, %[X], s47 offen sc1 lds\n\t" \
  "s_add_u32 m0, s46, 12288\n\t" \
  "s_add_u32 s47, s55, 20480\n\t" \
  "buffer_load_dwordx4 v0, %[X], s47 offen sc1 lds\n\t" \
  "s_add_u32 m0, s46, 16384\n\t" \
  "s_add_u32 s47, s55, 32768\n\t" \
  "buffer_load_dwordx4 v0, %[X], s47 offen sc1 lds\n\t" \
  "s_add_u32 m0, s46, 20480\n\t" \
  "s_add_u32 s47, s55, 36864\n\t" \
  "buffer_load_dwordx4 v0, %[X], s47 offen sc1 lds\n\t" \
  "s_add_u32 m0, s46, 24576\n\t" \
  "s_add_u32 s47, s55, 49152\n\t" \
  "buffer_load_dwordx4 v0, %[X], s47 offen sc1 lds\n\t" \
  "s_add_u32 m0, s46, 28672\n\t" \
  "s_add_u32 s47, s55, 53248\n\t" \
  "buffer_load_dwordx4 v0, %[X], s47 offen sc1 lds\n\t" \
  "s_add_u32 m0, s46, 32768\n\t" \
  "s_add_u32 s47, s55, 65536\n\t" \
  "buffer_load_dwordx4 v0, %[X], s47 offen sc1 lds\n\t" \
  "s_waitcnt vmcnt(5)\n\t" \
  "LTOP_%=:\n\t" \
  "s_barrier\n\t" \
  "ds_read_b128 v[64:67], v1 offset:0\n\t" \
  "ds_read_b128 v[68:71], v1 offset:2048\n\t" \
  "ds_read_b128 v[72:75], v2 offset:0\n\t" \
  "ds_read_b128 v[76:79], v2 offset:2048\n\t" \
  "ds_read_b128 v[80:83], v1 offset:4096\n\t" \
  "ds_read_b128 v[84:87], v1 offset:6144\n\t" \
  "s_mov_b32 s82, s83\n\t" \
  "s_xor_b32 s46, s46, 0x10000\n\t" \
  "s_waitcnt lgkmcnt(5)\n\t" \
  "v_mfma_f32_16x16x32_bf16 v[16:19], v[64:67], a[0:3], 0\n\t" \
  "ds_read_b128 v[88:91], v2 offset:4096\n\t" \
  "s_waitcnt lgkmcnt(5)\n\t" \
  "v_mfma_f32_16x16x32_bf16 v[20:23], v[68:71], a[0:3], 0\n\t" \
  "s_add_u32 s50, s48, 0\n\t" \
  "s_and_b32 s50, s50, " TK_TRM "\n\t" \
  "v_mfma_f32_16x16x32_bf16 v[24:27], v[64:67], a[72:75], 0\n\t" \
  "ds_read_b128 v[92:95], v2 offset:6144\n\t" \
  "v_mfma_f32_16x16x32_bf16 v[28:31], v[68:71], a[72:75], 0\n\t" \
  "s_lshr_b64 s[60:61], s[90:91], s50\n\t" \
  "s_and_b32 s60, s60, 1\n\t" \
  "s_waitcnt lgkmcnt(5)\n\t" \
  "v_mfma_f32_16x16x32_bf16 v[16:19], v[72:75], a[4:7], v[16:19]\n\t" \
  "ds_read_b128 v[96:99], v1 offset:8192\n\t" \
  "s_waitcnt lgkmcnt(5)\n\t" \
  "v_mfma_f32_16x16x32_bf16 v[20:23], v[76:79], a[4:7], v[20:23]\n\t" \
  "s_lshl_b32 s60, s60, 13\n\t" \
  "s_add_u32 s50, s50, " TK_SG0 "\n\t" \
  "v_mfma_f32_16x16x32_bf16 v[24:27], v[72:75], a[76:79], v[24:27]\n\t" \
  "ds_read_b128 v[100:103], v1 offset:10240\n\t" \
  "v_mfma_f32_16x16x32_bf16 v[28:31], v[76:79], a[76:79], v[28:31]\n\t" \
  "s_lshl_b32 s50, s50, 21\n\t" \
  "s_or_b32 s50, s50, s60\n\t" \
  "s_waitcnt lgkmcnt(5)\n\t" \
  "v_mfma_f32_16x16x32_bf16 v[16:19], v[80:83], a[8:11], v[16:19]\n\t" \
  "ds_read_b128 v[104:107], v2 offset:8192\n\t" \
  "s_waitcnt lgkmcnt(5)\n\t" \
  "v_mfma_f32_16x16x32_bf16 v[20:23], v[84:87], a[8:11], v[20:23]\n\t" \
  "s_add_u32 s50, s50, s37\n\t" \
  "s_add_u32 s51, s50, 4096\n\t" \
  "v_mfma_f32_16x16x32_bf16 v[24:27], v[80:83], a[80:83], v[24:27]\n\t" \
  "ds_read_b128 v[108:111], v2 offset:10240\n\t" \
  "v_mfma_f32_16x16x32_bf16 v[28:31], v[84:87], a[80:83], v[28:31]\n\t" \
  "s_add_u32 s83, s48, 1\n\t" \
  "s_lshr_b64 s[60:61], s[90:91], s83\n\t" \
  "s_waitcnt lgkmcnt(5)\n\t" \
  "v_mfma_f32_16x16x32_bf16 v[16:19], v[88:91], a[12:15], v[16:19]\n\t" \
  "ds_read_b128 v[112:115], v1 offset:12288\n\t" \
  "s_waitcnt lgkmcnt(5)\n\t" \
  "v_mfma_f32_16x16x32_bf16 v[20:23], v[92:95], a[12:15], v[20:23]\n\t" \
  "s_and_b32 s60, s60, 1\n\t" \
  "s_lshl_b32 s60, s60, 13\n\t" \
  "v_mfma_f32_16x16x32_bf16 v[24:27], v[88:91], a[84:87], v[24:27]\n\t" \
  "ds_read_b128 v[116:119], v1 offset:14336\n\t" \
  "v_mfma_f32_16x16x32_bf16 v[28:31], v[92:95], a[84:87], v[28:31]\n\t" \
  "s_add_u32 s83, s83, " TK_SG0 "\n\t" \
  "s_lshl_b32 s83, s83, 21\n\t" \
  "s_waitcnt lgkmcnt(5)\n\t" \
  "v_mfma_f32_16x16x32_bf16 v[16:19], v[96:99], a[16:19], v[16:19]\n\t" \
  "ds_read_b128 v[120:123], v2 offset:12288\n\t" \
  "s_waitcnt lgkmcnt(5)\n\t" \
  "v_mfma_f32_16x16x32_bf16 v[20:23], v[100:103], a[16:19], v[20:23]\n\t" \
  "s_or_b32 s83, s83, s60\n\t" \
  "s_cmp_lg_u32 s56, 0\n\t" \
  "v_mfma_f32_16x16x32_bf16 v[24:27], v[96:99], a[88:91], v[24:27]\n\t" \
  "ds_read_b128 v[124:127], v2 offset:14336\n\t" \
  "v_mfma_f32_16x16x32_bf16 v[28:31], v[100:103], a[88:91], v[28:31]\n\t" \
  "s_cbranch_scc1 LWARM_%=\n\t" \
  "s_waitcnt vmcnt(0)\n\t" \
  "LWARM_%=:\n\t" \
  "s_barrier\n\t" \
  "s_waitcnt lgkmcnt(5)\n\t" \
  "v_mfma_f32_16x16x32_bf16 v[16:19], v[104:107], a[20:23], v[16:19]\n\t" \
  "ds_read_b128 v[128:131], v1 offset:16384\n\t" \
  "s_waitcnt lgkmcnt(5)\n\t" \
  "v_mfma_f32_16x16x32_bf16 v[20:23], v[108:111], a[20:23], v[20:23]\n\t" \
  "v_mfma_f32_16x16x32_bf16 v[24:27], v[104:107], a[92:95], v[24:27]\n\t" \
  "ds_read_b128 v[132:135], v1 offset:18432\n\t" \
  "v_mfma_f32_16x16x32_bf16 v[28:31], v[108:111], a[92:95], v[28:31]\n\t" \
  "s_add_i32 s62, s48, s42\n\t" \
  "s_add_i32 s62, s62, 1\n\t" \
  "s_waitcnt lgkmcnt(5)\n\t" \
  "v_mfma_f32_16x16x32_bf16 v[16:19], v[112:115], a[24:27], v[16:19]\n\t" \
  "ds_read_b128 v[136:139], v2 offset:16384\n\t" \
  "s_waitcnt lgkmcnt(5)\n\t" \
  "v_mfma_f32_16x16x32_bf16 v[20:23], v[116:119], a[24:27], v[20:23]\n\t" \
  "s_mov_b32 s61, s62\n\t" \
  "v_mfma_f32_16x16x32_bf16 v[24:27], v[112:115], a[96:99], v[24:27]\n\t" \
  "ds_read_b128 v[140:143], v2 offset:18432\n\t" \
  "v_mfma_f32_16x16x32_bf16 v[28:31], v[116:119], a[96:99], v[28:31]\n\t" \
  "s_mul_i32 s63, s62, 43\n\t" \
  "s_lshr_b32 s63, s63, 9\n\t" \
  "s_waitcnt lgkmcnt(5)\n\t" \
  "v_mfma_f32_16x16x32_bf16 v[16:19], v[120:123], a[28:31], v[16:19]\n\t" \
  "ds_read_b128 v[144:147], v1 offset:20480\n\t" \
  "s_waitcnt lgkmcnt(5)\n\t" \
  "v_mfma_f32_16x16x32_bf16 v[20:23], v[124:127], a[28:31], v[20:23]\n\t" \
  "s_mul_i32 s63, s63, " TK_RM "\n\t" \
  "s_sub_u32 s62, s62, s63\n\t" \
  "v_mfma_f32_16x16x32_bf16 v[24:27], v[120:123], a[100:103], v[24:27]\n\t" \
  "ds_read_b128 v[148:151], v1 offset:22528\n\t" \
  "v_mfma_f32_16x16x32_bf16 v[28:31], v[124:127], a[100:103], v[28:31]\n\t" \
  "s_mul_i32 s63, s62, 43\n\t" \
  "s_lshr_b32 s63, s63, 7\n\t" \
  "s_waitcnt lgkmcnt(5)\n\t" \
  "v_mfma_f32_16x16x32_bf16 v[16:19], v[128:131], a[32:35], v[16:19]\n\t" \
  "ds_read_b128 v[152:155], v2 offset:20480\n\t" \
  "s_waitcnt lgkmcnt(5)\n\t" \
  "v_mfma_f32_16x16x32_bf16 v[20:23], v[132:135], a[32:35], v[20:23]\n\t" \
  "s_mul_i32 s57, s62, 688128\n\t" \
  "s_lshl_b32 s60, s63, 15\n\t" \
  "v_mfma_f32_16x16x32_bf16 v[24:27], v[128:131], a[104:107], v[24:27]\n\t" \
  "ds_read_b128 v[156:159], v2 offset:22528\n\t" \
  "v_mfma_f32_16x16x32_bf16 v[28:31], v[132:135], a[104:107], v[28:31]\n\t" \
  "s_add_u32 s57, s57, s60\n\t" \
  "s_lshr_b32 s60, s88, s63\n\t" \
  "s_waitcnt lgkmcnt(5)\n\t" \
  "v_mfma_f32_16x16x32_bf16 v[16:19], v[136:139], a[36:39], v[16:19]\n\t" \
  "ds_read_b128 v[160:163], v1 offset:24576\n\t" \
  "s_waitcnt lgkmcnt(5)\n\t" \
  "v_mfma_f32_16x16x32_bf16 v[20:23], v[140:143], a[36:39], v[20:23]\n\t" \
  "s_and_b32 s60, s60, 1\n\t" \
  "s_lshl_b32 s60, s60, 13\n\t" \
  "v_mfma_f32_16x16x32_bf16 v[24:27], v[136:139], a[108:111], v[24:27]\n\t" \
  "ds_read_b128 v[164:167], v1 offset:26624\n\t" \
  "v_mfma_f32_16x16x32_bf16 v[28:31], v[140:143], a[108:111], v[28:31]\n\t" \
  "s_add_u32 s57, s57, s60\n\t" \
  "s_add_u32 s57, s57, 656896\n\t" \
  "s_waitcnt lgkmcnt(5)\n\t" \
  "v_mfma_f32_16x16x32_bf16 v[16:19], v[144:147], a[40:43], v[16:19]\n\t" \
  "ds_read_b128 v[168:171], v2 offset:24576\n\t" \
  "s_waitcnt lgkmcnt(5)\n\t" \
  "v_mfma_f32_16x16x32_bf16 v[20:23], v[148:151], a[40:43], v[20:23]\n\t" \
  "s_cmp_lt_i32 s61, 1\n\t" \
  "s_cselect_b32 s57, 0xf0000000, s57\n\t" \
  "v_mfma_f32_16x16x32_bf16 v[24:27], v[144:147], a[112:115], v[24:27]\n\t" \
  "ds_read_b128 v[172:175], v2 offset:26624\n\t" \
  "v_mfma_f32_16x16x32_bf16 v[28:31], v[148:151], a[112:115], v[28:31]\n\t" \
  "s_cmp_lg_u32 s40, 0\n\t" \
  "s_cselect_b32 s57, 0xf0000000, s57\n\t" \
  "s_waitcnt lgkmcnt(5)\n\t" \
  "v_mfma_f32_16x16x32_bf16 v[16:19], v[152:155], a[44:47], v[16:19]\n\t" \
  "ds_read_b128 v[176:179], v1 offset:28672\n\t" \
  "s_waitcnt lgkmcnt(5)\n\t" \
  "v_mfma_f32_16x16x32_bf16 v[20:23], v[156:159], a[44:47], v[20:23]\n\t" \
  "s_add_u32 s80, s48, " TK_TRN "\n\t" \
  "s_lshr_b64 s[60:61], s[90:91], s80\n\t" \
  "s_and_b32 s60, s60, 1\n\t" \
  "v_mfma_f32_16x16x32_bf16 v[24:27], v[152:155], a[116:119], v[24:27]\n\t" \
  "ds_read_b128 v[180:183], v1 offset:30720\n\t" \
  "v_mfma_f32_16x16x32_bf16 v[28:31], v[156:159], a[116:119], v[28:31]\n\t" \
  "s_lshl_b32 s60, s60, 13\n\t" \
  "s_add_u32 s80, s80, " TK_SG0 "\n\t" \
  "s_lshl_b32 s80, s80, 21\n\t" \
  "s_waitcnt lgkmcnt(5)\n\t" \
  "v_mfma_f32_16x16x32_bf16 v[16:19], v[160:163], a[48:51], v[16:19]\n\t" \
  "ds_read_b128 v[184:187], v2 offset:28672\n\t" \
  "s_waitcnt lgkmcnt(5)\n\t" \
  "v_mfma_f32_16x16x32_bf16 v[20:23], v[164:167], a[48:51], v[20:23]\n\t" \
  "s_or_b32 s80, s80, s60\n\t" \
  "s_add_u32 s62, s83, s39\n\t" \
  "s_add_u32 s80, s80, s39\n\t" \
  "v_mfma_f32_16x16x32_bf16 v[24:27], v[160:163], a[120:123], v[24:27]\n\t" \
  "ds_read_b128 v[188:191], v2 offset:30720\n\t" \
  "v_mfma_f32_16x16x32_bf16 v[28:31], v[164:167], a[120:123], v[28:31]\n\t" \
  "s_add_u32 s80, s80, 2048\n\t" \
  "s_waitcnt lgkmcnt(5)\n\t" \
  "v_mfma_f32_16x16x32_bf16 v[16:19], v[168:171], a[52:55], v[16:19]\n\t" \
  "ds_read_b128 v[192:195], v1 offset:32768\n\t" \
  "s_waitcnt lgkmcnt(5)\n\t" \
  "v_mfma_f32_16x16x32_bf16 v[20:23], v[172:175], a[52:55], v[20:23]\n\t" \
  "v_mfma_f32_16x16x32_bf16 v[24:27], v[168:171], a[124:127], v[24:27]\n\t" \
  "ds_read_b128 v[196:199], v1 offset:34816\n\t" \
  "v_mfma_f32_16x16x32_bf16 v[28:31], v[172:175], a[124:127], v[28:31]\n\t" \
  "s_cmp_lt_u32 s48, " TK_TRM "\n\t" \
  "s_cselect_b32 s80, s62, s80\n\t" \
  "s_waitcnt lgkmcnt(5)\n\t" \
  "v_mfma_f32_16x16x32_bf16 v[16:19], v[176:179], a[56:59], v[16:19]\n\t" \
  "ds_read_b128 v[200:203], v2 offset:32768\n\t" \
  "s_waitcnt lgkmcnt(5)\n\t" \
  "v_mfma_f32_16x16x32_bf16 v[20:23], v[180:183], a[56:59], v[20:23]\n\t" \
  "v_add_u32 v7, s62, v3\n\t" \
  "s_mov_b32 exec_lo, 0\n\t" \
  "v_mov_b32 v7, s80\n\t" \
  "s_mov_b32 exec_lo, -1\n\t" \
  "v_mfma_f32_16x16x32_bf16 v[24:27], v[176:179], a[128:131], v[24:27]\n\t" \
  "ds_read_b128 v[204:207], v2 offset:34816\n\t" \
  "v_mfma_f32_16x16x32_bf16 v[28:31], v[180:183], a[128:131], v[28:31]\n\t" \
  "s_waitcnt lgkmcnt(5)\n\t" \
  "v_mfma_f32_16x16x32_bf16 v[16:19], v[184:187], a[60:63], v[16:19]\n\t" \
  "s_waitcnt lgkmcnt(4)\n\t" \
  "v_mfma_f32_16x16x32_bf16 v[20:23], v[188:191], a[60:63], v[20:23]\n\t" \
  "buffer_load_dword v4, v7, %[X], 0 offen sc1\n\t" \
  "v_mfma_f32_16x16x32_bf16 v[24:27], v[184:187], a[132:135], v[24:27]\n\t" \
  "v_mfma_f32_16x16x32_bf16 v[28:31], v[188:191], a[132:135], v[28:31]\n\t" \
  "s_waitcnt lgkmcnt(3)\n\t" \
  "v_mfma_f32_16x16x32_bf16 v[16:19], v[192:195], a[64:67], v[16:19]\n\t" \
  "v_xor_b32 v1, 0x10000, v1\n\t" \
  "v_xor_b32 v2, 0x10000, v2\n\t" \
  "s_waitcnt lgkmcnt(2)\n\t" \
  "v_mfma_f32_16x16x32_bf16 v[20:23], v[196:199], a[64:67], v[20:23]\n\t" \
  "v_mfma_f32_16x16x32_bf16 v[24:27], v[192:195], a[136:139], v[24:27]\n\t" \
  "v_mfma_f32_16x16x32_bf16 v[28:31], v[196:199], a[136:139], v[28:31]\n\t" \
  "s_waitcnt lgkmcnt(1)\n\t" \
  "v_mfma_f32_16x16x32_bf16 v[16:19], v[200:203], a[68:71], v[16:19]\n\t" \
  "s_waitcnt lgkmcnt(0)\n\t" \
  "v_mfma_f32_16x16x32_bf16 v[20:23], v[204:207], a[68:71], v[20:23]\n\t" \
  "v_mfma_f32_16x16x32_bf16 v[24:27], v[200:203], a[140:143], v[24:27]\n\t" \
  "v_mfma_f32_16x16x32_bf16 v[28:31], v[204:207], a[140:143], v[28:31]\n\t" \
  "v_mfma_f32_16x16x32_bf16 v[32:35], v[64:67], a[144:147], 0\n\t" \
  "v_cvt_pk_bf16_f32 v48, v16, v17\n\t" \
  "v_cvt_pk_bf16_f32 v49, v18, v19\n\t" \
  "v_mfma_f32_16x16x32_bf16 v[36:39], v[68:71], a[144:147], 0\n\t" \
  "v_cvt_pk_bf16_f32 v50, v20, v21\n\t" \
  "v_cvt_pk_bf16_f32 v51, v22, v23\n\t" \
  "v_mfma_f32_16x16x32_bf16 v[32:35], v[72:75], a[148:151], v[32:35]\n\t" \
  "v_cvt_pk_bf16_f32 v52, v24, v25\n\t" \
  "v_cvt_pk_bf16_f32 v53, v26, v27\n\t" \
  "v_mfma_f32_16x16x32_bf16 v[36:39], v[76:79], a[148:151], v[36:39]\n\t" \
  "v_cvt_pk_bf16_f32 v54, v28, v29\n\t" \
  "v_cvt_pk_bf16_f32 v55, v30, v31\n\t" \
  "v_mfma_f32_16x16x32_bf16 v[32:35], v[80:83], a[152:155], v[32:35]\n\t" \
  "buffer_store_dwordx4 v[48:51], v0, %[X], s50 offen offset:0 sc1\n\t" \
  "v_mfma_f32_16x16x32_bf16 v[36:39], v[84:87], a[152:155], v[36:39]\n\t" \
  "v_mfma_f32_16x16x32_bf16 v[32:35], v[88:91], a[156:159], v[32:35]\n\t" \
  "v_mfma_f32_16x16x32_bf16 v[36:39], v[92:95], a[156:159], v[36:39]\n\t" \
  "v_mfma_f32_16x16x32_bf16 v[32:35], v[96:99], a[160:163], v[32:35]\n\t" \
  "v_mfma_f32_16x16x32_bf16 v[36:39], v[100:103], a[160:163], v[36:39]\n\t" \
  "v_mfma_f32_16x16x32_bf16 v[32:35], v[104:107], a[164:167], v[32:35]\n\t" \
  "v_mfma_f32_16x16x32_bf16 v[36:39], v[108:111], a[164:167], v[36:39]\n\t" \
  "v_mfma_f32_16x16x32_bf16 v[32:35], v[112:115], a[168:171], v[32:35]\n\t" \
  "v_mfma_f32_16x16x32_bf16 v[36:39], v[116:119], a[168:171], v[36:39]\n\t" \
  "v_mfma_f32_16x16x32_bf16 v[32:35], v[120:123], a[172:175], v[32:35]\n\t" \
  "v_mfma_f32_16x16x32_bf16 v[36:39], v[124:127], a[172:175], v[36:39]\n\t" \
  "s_waitcnt vmcnt(1)\n\t" \
  "v_cmp_eq_u32 vcc, 0, v4\n\t" \
  "v_mfma_f32_16x16x32_bf16 v[32:35], v[128:131], a[176:179], v[32:35]\n\t" \
  "v_mfma_f32_16x16x32_bf16 v[36:39], v[132:135], a[176:179], v[36:39]\n\t" \
  "s_cmp_eq_u64 vcc, 0\n\t" \
  "s_cselect_b32 s60, 1, 0\n\t" \
  "v_mov_b32 v4, s60\n\t" \
  "v_mfma_f32_16x16x32_bf16 v[32:35], v[136:139], a[180:183], v[32:35]\n\t" \
  "ds_write_b32 v5, v4\n\t" \
  "v_mfma_f32_16x16x32_bf16 v[36:39], v[140:143], a[180:183], v[36:39]\n\t" \
  "v_mfma_f32_16x16x32_bf16 v[32:35], v[144:147], a[184:187], v[32:35]\n\t" \
  "v_mfma_f32_16x16x32_bf16 v[36:39], v[148:151], a[184:187], v[36:39]\n\t" \
  "buffer_store_dwordx4 v[52:55], v0, %[X], s50 offen offset:1024 sc1\n\t" \
  "v_mfma_f32_16x16x32_bf16 v[32:35], v[152:155], a[188:191], v[32:35]\n\t" \
  "v_mfma_f32_16x16x32_bf16 v[36:39], v[156:159], a[188:191], v[36:39]\n\t" \
  "s_waitcnt lgkmcnt(0)\n\t" \
  "s_barrier\n\t" \
  "v_mfma_f32_16x16x32_bf16 v[32:35], v[160:163], a[192:195], v[32:35]\n\t" \
  "ds_read_b32 v4, v6 offset:40959\n\t" \
  "v_mfma_f32_16x16x32_bf16 v[36:39], v[164:167], a[192:195], v[36:39]\n\t" \
  "v_mfma_f32_16x16x32_bf16 v[32:35], v[168:171], a[196:199], v[32:35]\n\t" \
  "v_mfma_f32_16x16x32_bf16 v[36:39], v[172:175], a[196:199], v[36:39]\n\t" \
  "v_mfma_f32_16x16x32_bf16 v[32:35], v[176:179], a[200:203], v[32:35]\n\t" \
  "v_mfma_f32_16x16x32_bf16 v[36:39], v[180:183], a[200:203], v[36:39]\n\t" \
  "s_waitcnt lgkmcnt(0)\n\t" \
  "v_readfirstlane_b32 s60, v4\n\t" \
  "s_add_u32 s55, s83, s36\n\t" \
  "v_mfma_f32_16x16x32_bf16 v[32:35], v[184:187], a[204:207], v[32:35]\n\t" \
  "s_cmp_eq_u32 s48, 63\n\t" \
  "s_cselect_b32 s56, 0, s60\n\t" \
  "v_mfma_f32_16x16x32_bf16 v[36:39], v[188:191], a[204:207], v[36:39]\n\t" \
  "s_cmp_eq_u32 s56, 0\n\t" \
  "s_cselect_b32 s55, 0xf0000000, s55\n\t" \
  "v_mfma_f32_16x16x32_bf16 v[32:35], v[192:195], a[208:211], v[32:35]\n\t" \
  "v_mfma_f32_16x16x32_bf16 v[36:39], v[196:199], a[208:211], v[36:39]\n\t" \
  "v_mfma_f32_16x16x32_bf16 v[32:35], v[200:203], a[212:215], v[32:35]\n\t" \
  "s_add_u32 m0, s46, 0\n\t" \
  "s_add_u32 s47, s55, 0\n\t" \
  "buffer_load_dwordx4 v0, %[X], s47 offen sc1 lds\n\t" \
  "v_mfma_f32_16x16x32_bf16 v[36:39], v[204:207], a[212:215], v[36:39]\n\t" \
  "v_mfma_f32_16x16x32_bf16 v[40:43], v[64:67], a[216:219], 0\n\t" \
  "v_mfma_f32_16x16x32_bf16 v[44:47], v[68:71], a[216:219], 0\n\t" \
  "v_mfma_f32_16x16x32_bf16 v[40:43], v[72:75], a[220:223], v[40:43]\n\t" \
  "v_cvt_pk_bf16_f32 v56, v32, v33\n\t" \
  "v_cvt_pk_bf16_f32 v57, v34, v35\n\t" \
  "s_add_u32 m0, s46, 4096\n\t" \
  "s_add_u32 s47, s55, 4096\n\t" \
  "buffer_load_dwordx4 v0, %[X], s47 offen sc1 lds\n\t" \
  "v_mfma_f32_16x16x32_bf16 v[44:47], v[76:79], a[220:223], v[44:47]\n\t" \
  "v_cvt_pk_bf16_f32 v58, v36, v37\n\t" \
  "v_cvt_pk_bf16_f32 v59, v38, v39\n\t" \
  "v_mfma_f32_16x16x32_bf16 v[40:43], v[80:83], a[224:227], v[40:43]\n\t" \
  "v_mfma_f32_16x16x32_bf16 v[44:47], v[84:87], a[224:227], v[44:47]\n\t" \
  "buffer_store_dwordx4 v[56:59], v0, %[X], s50 offen offset:2048 sc1\n\t" \
  "v_mfma_f32_16x16x32_bf16 v[40:43], v[88:91], a[228:231], v[40:43]\n\t" \
  "s_add_u32 m0, s46, 8192\n\t" \
  "s_add_u32 s47, s55, 16384\n\t" \
  "buffer_load_dwordx4 v0, %[X], s47 offen sc1 lds\n\t" \
  "v_mfma_f32_16x16x32_bf16 v[44:47], v[92:95], a[228:231], v[44:47]\n\t" \
  "v_mfma_f32_16x16x32_bf16 v[40:43], v[96:99], a[232:235], v[40:43]\n\t" \
  "v_mfma_f32_16x16x32_bf16 v[44:47], v[100:103], a[232:235], v[44:47]\n\t" \
  "v_mfma_f32_16x16x32_bf16 v[40:43], v[104:107], a[236:239], v[40:43]\n\t" \
  "s_add_u32 m0, s46, 12288\n\t" \
  "s_add_u32 s47, s55, 20480\n\t" \
  "buffer_load_dwordx4 v0, %[X], s47 offen sc1 lds\n\t" \
  "v_mfma_f32_16x16x32_bf16 v[44:47], v[108:111], a[236:239], v[44:47]\n\t" \
  "v_mfma_f32_16x16x32_bf16 v[40:43], v[112:115], a[240:243], v[40:43]\n\t" \
  "v_mfma_f32_16x16x32_bf16 v[44:47], v[116:119], a[240:243], v[44:47]\n\t" \
  "v_mfma_f32_16x16x32_bf16 v[40:43], v[120:123], a[244:247], v[40:43]\n\t" \
  "s_add_u32 m0, s46, 16384\n\t" \
  "s_add_u32 s47, s55, 32768\n\t" \
  "buffer_load_dwordx4 v0, %[X], s47 offen sc1 lds\n\t" \
  "v_mfma_f32_16x16x32_bf16 v[44:47], v[124:127], a[244:247], v[44:47]\n\t" \
  "v_mfma_f32_16x16x32_bf16 v[40:43], v[128:131], a[248:251], v[40:43]\n\t" \
  "v_mfma_f32_16x16x32_bf16 v[44:47], v[132:135], a[248:251], v[44:47]\n\t" \
  "v_mfma_f32_16x16x32_bf16 v[40:43], v[136:139], a[252:255], v[40:43]\n\t" \
  "s_add_u32 m0, s46, 20480\n\t" \
  "s_add_u32 s47, s55, 36864\n\t" \
  "buffer_load_dwordx4 v0, %[X], s47 offen sc1 lds\n\t" \
  "v_mfma_f32_16x16x32_bf16 v[44:47], v[140:143], a[252:255], v[44:47]\n\t" \
  "v_mfma_f32_16x16x32_bf16 v[40:43], v[144:147], v[224:227], v[40:43]\n\t" \
  "v_mfma_f32_16x16x32_bf16 v[44:47], v[148:151], v[224:227], v[44:47]\n\t" \
  "v_mfma_f32_16x16x32_bf16 v[40:43], v[152:155], v[228:231], v[40:43]\n\t" \
  "s_add_u32 m0, s46, 24576\n\t" \
  "s_add_u32 s47, s55, 49152\n\t" \
  "buffer_load_dwordx4 v0, %[X], s47 offen sc1 lds\n\t" \
  "v_mfma_f32_16x16x32_bf16 v[44:47], v[156:159], v[228:231], v[44:47]\n\t" \
  "v_mfma_f32_16x16x32_bf16 v[40:43], v[160:163], v[232:235], v[40:43]\n\t" \
  "v_mfma_f32_16x16x32_bf16 v[44:47], v[164:167], v[232:235], v[44:47]\n\t" \
  "v_mfma_f32_16x16x32_bf16 v[40:43], v[168:171], v[236:239], v[40:43]\n\t" \
  "s_add_u32 m0, s46, 28672\n\t" \
  "s_add_u32 s47, s55, 53248\n\t" \
  "buffer_load_dwordx4 v0, %[X], s47 offen sc1 lds\n\t" \
  "v_mfma_f32_16x16x32_bf16 v[44:47], v[172:175], v[236:239], v[44:47]\n\t" \
  "v_mfma_f32_16x16x32_bf16 v[40:43], v[176:179], v[240:243], v[40:43]\n\t" \
  "v_mfma_f32_16x16x32_bf16 v[44:47], v[180:183], v[240:243], v[44:47]\n\t" \
  "v_mfma_f32_16x16x32_bf16 v[40:43], v[184:187], v[244:247], v[40:43]\n\t" \
  "s_add_u32 m0, s46, 32768\n\t" \
  "s_add_u32 s47, s55, 65536\n\t" \
  "buffer_load_dwordx4 v0, %[X], s47 offen sc1 lds\n\t" \
  "v_mfma_f32_16x16x32_bf16 v[44:47], v[188:191], v[244:247], v[44:47]\n\t" \
  "v_mfma_f32_16x16x32_bf16 v[40:43], v[192:195], v[248:251], v[40:43]\n\t" \
  "v_mfma_f32_16x16x32_bf16 v[44:47], v[196:199], v[248:251], v[44:47]\n\t" \
  "buffer_load_dword v8, off, %[X], s57 sc1\n\t" \
  "v_mfma_f32_16x16x32_bf16 v[40:43], v[200:203], v[252:255], v[40:43]\n\t" \
  "v_mfma_f32_16x16x32_bf16 v[44:47], v[204:207], v[252:255], v[44:47]\n\t" \
  "s_nop 4\n\t" \
  "v_cvt_pk_bf16_f32 v60, v40, v41\n\t" \
  "v_cvt_pk_bf16_f32 v61, v42, v43\n\t" \
  "s_nop 1\n\t" \
  "v_cvt_pk_bf16_f32 v62, v44, v45\n\t" \
  "v_cvt_pk_bf16_f32 v63, v46, v47\n\t" \
  "buffer_store_dwordx4 v[60:63], v0, %[X], s50 offen offset:3072 sc1\n\t" \
  "s_add_u32 s49, s82, s38\n\t" \
  "s_cmp_eq_u32 s48, 63\n\t" \
  "s_cbranch_scc1 LEND_%=\n\t" \
  "s_add_u32 s48, s48, 1\n\t" \
  "s_cmp_eq_u32 s56, 0\n\t" \
  "s_cbranch_scc1 LCOLD_%=\n\t" \
  "s_waitcnt vmcnt(0)\n\t" \
  "s_mov_b64 exec, 1\n\t" \
  "buffer_store_dword v6, off, %[X], s49 sc1\n\t" \
  "s_mov_b64 exec, -1\n\t" \
  "s_mov_b32 s49, 0xf0000000\n\t" \
  "s_cmp_lt_i32 s57, 0\n\t" \
  "s_cbranch_scc1 LTOP_%=\n\t" \
  "s_movk_i32 s63, 4000\n\t" \
  "s_branch LGW_%=u\n\t" \
  "LGW_%=:\n\t" \
  "s_sleep " TK_PSG "\n\t" \
  "buffer_load_dword v8, off, %[X], s57 sc1\n\t" \
  "s_waitcnt vmcnt(0)\n\t" \
  "LGW_%=u:\n\t" \
  "s_memrealtime s[52:53]\n\t" \
  "v_readfirstlane_b32 s60, v8\n\t" \
  "s_waitcnt lgkmcnt(0)\n\t" \
  "s_sub_u32 s63, s63, 1\n\t" \
  "s_cbranch_scc1 LTOP_%=\n\t" \
  "s_cmp_eq_u32 s60, -1\n\t" \
  "s_cbranch_scc1 LGW_%=\n\t" \
  "s_not_b32 s60, s60\n\t" \
  "s_lshl_b32 s61, s52, 8\n\t" \
  "s_sub_u32 s60, s60, s61\n\t" \
  "s_ashr_i32 s60, s60, 8\n\t" \
  "s_sub_i32 s60, s60, " TK_QXW "\n\t" \
  "s_min_i32 s60, s60, 400\n\t" \
  "s_add_u32 s54, s52, s60\n\t" \
  "LGW_%=w:\n\t" \
  "s_sleep 1\n\t" \
  "s_memrealtime s[52:53]\n\t" \
  "s_waitcnt lgkmcnt(0)\n\t" \
  "s_sub_i32 s53, s52, s54\n\t" \
  "s_cmp_lt_i32 s53, 0\n\t" \
  "s_cbranch_scc1 LGW_%=w\n\t" \
  "s_branch LTOP_%=\n\t" \
  "LEND_%=:\n\t" \
  "s_waitcnt vmcnt(0)\n\t" \
  "s_mov_b64 exec, 1\n\t" \
  "buffer_store_dword v6, off, %[X], s49 sc1\n\t" \
  "s_mov_b64 exec, -1\n\t" \
  "s_mov_b32 s49, 0xf0000000\n\t" \
  ""
__device__ __forceinline__ unsigned hoff(int r, int piece) {
  const int m = 4 * (r >> 3) + (r & 3), kp = piece >> 3;
  return (P_H + (unsigned)(kp >> 1)) * PAIR + (unsigned)((kp & 1) * 4096 + (16 * ((r >> 2) & 1) + m) * 128 + (((piece ^ m) & 7) << 4));
}
template <int SLEEP>
__device__ __forceinline__ void wait_all(rsrc_t X, unsigned vo, unsigned so) {
  unsigned spins = 0;
  while (true) {
    asm volatile("" ::: "memory");
    if (__all(ld4(X, vo, so) != 0u)) break;
    if (spin_fail(spins, X)) break;
    __builtin_amdgcn_s_sleep(SLEEP);
  }
  asm volatile("" ::: "memory");
}
#define VQ_CLOB "v8", "v16", "v17", "v18", "v19", "v20", "v21", "v22", "v23", "v24", "v25", "v26", "v27", "v28", "v29", "v30", "v31", "v32", "v33", "v34", "v35", "v36", "v37", "v38", "v39", "v40", "v41", "v42", "v43", "v44", "v45", "v46", "v47", "v48", "v49", "v50", "v51", "v52", "v53", "v54", "v55", "v56", "v57", "v58", "v59", "v60", "v61", "v62", "v63", "v64", "v65", "v66", "v67", "v68", "v69", "v70", "v71", "v72", "v73", "v74", "v75", "v76", "v77", "v78", "v79", "v80", "v81", "v82", "v83", "v84", "v85", "v86", "v87", "v88", "v89", "v90", "v91", "v92", "v93", "v94", "v95", "v96", "v97", "v98", "v99", "v100", "v101", "v102", "v103", "v104", "v105", "v106", "v107", "v108", "v109", "v110", "v111", "v112", "v113", "v114", "v115", "v116", "v117", "v118", "v119", "v120", "v121", "v122", "v123", "v124", "v125", "v126", "v127", "v128", "v129", "v130", "v131", "v132", "v133", "v134", "v135", "v136", "v137", "v138", "v139", "v140", "v141", "v142", "v143", "v144", "v145", "v146", "v147", "v148", "v149", "v150", "v151", "v152", "v153", "v154", "v155", "v156", "v157", "v158", "v159", "v160", "v161", "v162", "v163", "v164", "v165", "v166", "v167", "v168", "v169", "v170", "v171", "v172", "v173", "v174", "v175", "v176", "v177", "v178", "v179", "v180", "v181", "v182", "v183", "v184", "v185", "v186", "v187", "v188", "v189", "v190", "v191", "v192", "v193", "v194", "v195", "v196", "v197", "v198", "v199", "v200", "v201", "v202", "v203", "v204", "v205", "v206", "v207", "v220", "v221"
#define VA_CLOB "v40", "v41", "v42", "v43", "v44", "v45", "v46", "v47", "v48", "v49", "v50", "v51", "v52", "v53", "v54", "v55", "v56", "v57", "v58", "v59", "v60", "v61", "v62", "v63", "v64", "v65", "v66", "v67", "v68", "v69", "v70", "v71", "v72", "v73", "v74", "v75", "v76", "v77", "v78", "v79", "v80", "v81", "v82", "v83", "v84", "v85", "v86", "v87", "v88", "v89", "v90", "v91", "v92", "v93", "v94", "v95", "v96", "v97", "v98", "v99", "v100", "v101", "v102", "v103", "v104", "v105", "v106", "v107", "v108", "v109", "v110", "v111", "v112", "v113", "v114", "v115", "v116", "v117", "v118", "v119", "v120", "v121", "v122", "v123", "v124", "v125", "v126", "v127", "v128", "v129", "v130", "v131", "v132", "v133", "v134", "v135", "v136", "v137", "v138", "v139", "v140", "v141", "v142", "v143", "v144", "v145", "v146", "v147", "v148", "v149", "v150", "v151", "v152", "v153", "v154", "v155", "v156", "v157", "v158", "v159", "v160", "v161", "v162", "v163", "v164", "v165", "v166", "v167", "v168", "v169", "v170", "v171", "v172", "v173", "v174", "v175", "v176", "v177", "v178", "v179", "v180", "v181", "v182", "v183", "v184", "v185", "v186", "v187", "v188", "v189", "v190", "v191", "v192", "v193", "v194", "v195", "v196", "v197", "v198", "v199", "v200", "v201", "v202", "v203", "v204", "v205", "v206", "v207", "v208", "v209", "v210", "v211", "v220", "v221"
#define TOK_COMMON() \
  const MP p = getp(); \
  const int c = (int)blockIdx.x - NCHW, xq = c & 3, L = (c >> 2) & 1, i = c >> 3; \
  const int tid = threadIdx.x, lane = tid & 63, w = __builtin_amdgcn_readfirstlane(tid >> 6); \
  const rsrc_t X = mkr(p.xb, XB_BYTES)
#define W_IN [W] "s"(WR)
#define W_CLOB "a0", "a1", "a2", "a3", "a4", "a5", "a6", "a7", "a8", "a9", "a10", "a11", "a12", "a13", "a14", "a15", "a16", "a17", "a18", "a19", "a20", "a21", "a22", "a23", "a24", "a25", "a26", "a27", "a28", "a29", "a30", "a31", "a32", "a33", "a34", "a35", "a36", "a37", "a38", "a39", "a40", "a41", "a42", "a43", "a44", "a45", "a46", "a47", "a48", "a49", "a50", "a51", "a52", "a53", "a54", "a55", "a56", "a57", "a58", "a59", "a60", "a61", "a62", "a63", "a64", "a65", "a66", "a67", "a68", "a69", "a70", "a71", "a72", "a73", "a74", "a75", "a76", "a77", "a78", "a79", "a80", "a81", "a82", "a83", "a84", "a85", "a86", "a87", "a88", "a89", "a90", "a91", "a92", "a93", "a94", "a95", "a96", "a97", "a98", "a99", "a100", "a101", "a102", "a103", "a104", "a105", "a106", "a107", "a108", "a109", "a110", "a111", "a112", "a113", "a114", "a115", "a116", "a117", "a118", "a119", "a120", "a121", "a122", "a123", "a124", "a125", "a126", "a127", "a128", "a129", "a130", "a131", "a132", "a133", "a134", "a135", "a136", "a137", "a138", "a139", "a140", "a141", "a142", "a143", "a144", "a145", "a146", "a147", "a148", "a149", "a150", "a151", "a152", "a153", "a154", "a155", "a156", "a157", "a158", "a159", "a160", "a161", "a162", "a163", "a164", "a165", "a166", "a167", "a168", "a169", "a170", "a171", "a172", "a173", "a174", "a175", "a176", "a177", "a178", "a179", "a180", "a181", "a182", "a183", "a184", "a185", "a186", "a187", "a188", "a189", "a190", "a191", "a192", "a193", "a194", "a195", "a196", "a197", "a198", "a199", "a200", "a201", "a202", "a203", "a204", "a205", "a206", "a207", "a208", "a209", "a210", "a211", "a212", "a213", "a214", "a215", "a216", "a217", "a218", "a219", "a220", "a221", "a222", "a223", "a224", "a225", "a226", "a227", "a228", "a229", "a230", "a231", "a232", "a233", "a234", "a235", "a236", "a237", "a238", "a239", "a240", "a241", "a242", "a243", "a244", "a245", "a246", "a247", "a248", "a249", "a250", "a251", "a252", "a253", "a254", "a255", "v224", "v225", "v226", "v227", "v228", "v229", "v230", "v231", "v232", "v233", "v234", "v235", "v236", "v237", "v238", "v239", "v240", "v241", "v242", "v243", "v244", "v245", "v246", "v247", "v248", "v249", "v250", "v251", "v252", "v253", "v254", "v255"
#define BOOT_IN "{s67}"(L), [ctab] "i"(CTAB_OFF), [rm] "i"(SRING == 12 ? 12 : 0), [trm] "i"(TRING - 1), [trn] "i"(1 - TRING)
#define S_CLOB "s80", "s81", "s76", "s78", "s79", "s86", "s88", "s89", "s90", "s91", "s46", "s47", "s48", "s49", "s50", "s51", "s52", "s53", "s54", "s55", "s56", "s57", "s58", "s59", "s60", "s61", "s62", "s63", "s68", "s69", "s70", "s71", "s72", "s73", "s82", "s83", "s84", "m0", "vcc", "scc", "memory"
typedef unsigned v8w __attribute__((ext_vector_type(8)));

__device__ __forceinline__ void qkv_role() {
  TOK_COMMON();
  const int role = i * 4 + w, hh = 8 * xq + role / 6, part = role % 6, which = part >> 1, half = part & 1;
  const rsrc_t WR = mkr((which == 0 ? p.Wq : (which == 1 ? p.Wk : p.Wv)) + hh * DH + 64 * half, DM * HD * 2u - (unsigned)(hh * DH + 64 * half) * 2u);
  const int m = lane & 15, g = lane >> 4;
  const unsigned a0 = (unsigned)(m * 128 + ((g ^ (m & 7)) << 4));
  v8w lv = {(unsigned)lane * 16u, a0, a0 ^ 64u, (unsigned)(lane & (L ? 15 : 3)) * 128u, 0u, 40960u + 4u * (unsigned)w, 1u, 0u};
  const unsigned c36 = P_H * PAIR + (unsigned)w * 1024u, c37 = (unsigned)(3 * hh + which) * PAIR + (unsigned)half * (which == 2 ? 4096u : 2048u);
  const unsigned c38 = P_FA * PAIR + (unsigned)(hh * 128 + part * 16), c39 = w == 0 ? P_FB * PAIR + 4096u : 0xF0000000u, c41 = (unsigned)w * 1024u;
  const int c42 = L ? 1 - QLEAD1 : 1 - QLEAD;
  unsigned d74 = TRC_OFF + 262144u + (unsigned)((c * 4 + w) * 66) * 8u;
#define Q_CALL(A) asm volatile("s_waitcnt vmcnt(0) lgkmcnt(0)\n\t" A : "+{v[0:7]}"(lv), "+{s74}"(d74) \
               : [X] "s"(X), "{s36}"(c36), "{s37}"(c37), "{s38}"(c38), "{s39}"(c39), "{s40}"(w), "{s41}"(c41), "{s42}"(c42), "{s45}"(i), "{s92}"(TMO_OFF), BOOT_IN, [cap] "i"(SPIN_CAP), [qxw] "i"(QXW), [qxc] "i"(QXC), W_IN : VQ_CLOB, W_CLOB, S_CLOB)
  if (which == 2) { Q_CALL(QV_ASM "LQVEND:"); __builtin_amdgcn_endpgm(); }
  Q_CALL(QK_ASM "LQKEND:");
  __builtin_amdgcn_endpgm();
}

__device__ __forceinline__ void ap_role() {
  TOK_COMMON();
  const int hp = 4 * xq + ((i - 12) >> 1), ch = i & 1, a = w >> 1, rt = w & 1, h = 2 * hp + a;
  const int n = lane & 15, g = lane >> 4;
  const rsrc_t WR = mkr(p.Wo + (long)hp * 2 * DH * DM, 2u * DH * DM * 2u);
  const unsigned l16 = (unsigned)lane * 16u, ab = (unsigned)a * 24576u + l16;
  v16u lv = {l16, ab, ab + (unsigned)rt * 4096u, 49152u + (unsigned)w * 4096u + l16, 49152u + (unsigned)(w ^ 2) * 4096u + l16, l16 & ~32u, l16 >> 1,
             (unsigned)(g * 262144 + n * 16), (unsigned)(n * 32 + g * 8), (unsigned)((lane & 7) % 6) * 16u, 0u, 0u, 0u, 0u, 0u, 0u};
  v8w mk;
  const int qi = 8 * (n >> 2) + (n & 3) + 4 * rt;
#pragma unroll
  for (int k = 0; k < 8; ++k) mk[k] = 8 * g + k > qi ? 0xFF800000u : 0u;
  v16u zz = {0u, 0u, 0u, 0u, 0u, 0u, 0u, 0u, 0u, 0u, 0u, 0u, 0u, 0u, 0u, 0u};
  const unsigned tb = (unsigned)(3 * h) * PAIR;
  const unsigned c36 = tb + (rt ? PAIR + 4096u : 0u), c37 = tb + (rt ? 2u * PAIR : 4096u), c38 = tb + (rt ? 2u * PAIR + 4096u : PAIR);
  const unsigned c39 = (P_KV + (unsigned)(h >> 1)) * PAIR + (unsigned)(h & 1) * 4096u, c41 = P_FA * PAIR + (unsigned)h * 128u;
  const unsigned c42 = w == 0 ? P_FB * PAIR + (unsigned)(2 * hp + ch) * 128u : 0xF0000000u;
  const unsigned c43 = TP0 * XBLK + (unsigned)(8 * rt + (hp >> 3)) * PAIR + (unsigned)((hp & 7) * 1024 + ch * 512 + a * 256);
  unsigned d44 = (TP0 + 8u) * XBLK + (unsigned)(hp >> 1) * PAIR + (unsigned)((((hp & 1) * 4 + 2 * ch + a) * 2 + rt) * 512);
  const unsigned c45 = (unsigned)(a * 24576 + rt * 12288);
  unsigned d74 = TRC_OFF + 262144u + (unsigned)((c * 4 + w) * 66) * 8u;
  asm volatile("s_waitcnt vmcnt(0) lgkmcnt(0)\n\t" AP_ASM "LAPEND:" : "+{v[0:15]}"(lv), "+{v[24:39]}"(zz), "+{s74}"(d74), "+{s44}"(d44)
               : [X] "s"(X), "{s36}"(c36), "{s37}"(c37), "{s38}"(c38), "{s39}"(c39), "{s41}"(c41), "{s42}"(c42), "{s43}"(c43), "{s45}"(c45), "{s64}"(512 * ch + 256 * a), "{s65}"(1024 + 64 * ch + 32 * a), "{s66}"(147456 * a), "{s77}"(L ? AX1 : AX), BOOT_IN,
                 "{s92}"(TMO_OFF), [cap] "i"(SPIN_CAP), "{v[16:23]}"(mk), W_IN : VA_CLOB, W_CLOB, S_CLOB);
  __builtin_amdgcn_endpgm();
}

template <int N> __device__ __forceinline__ float rorN(float v) { return __uint_as_float((unsigned)__builtin_amdgcn_update_dpp(0, (int)__float_as_uint(v), 0x120 + N, 0xf, 0xf, false)); }
__device__ __forceinline__ void up8(v4u v, float* x) {
  x[0] = lo2f(v.x); x[1] = hi2f(v.x); x[2] = lo2f(v.y); x[3] = hi2f(v.y); x[4] = lo2f(v.z); x[5] = hi2f(v.z); x[6] = lo2f(v.w); x[7] = hi2f(v.w);
}
__device__ __forceinline__ v4u pk8(const float* x) { return (v4u){pk2(x[0], x[1]), pk2(x[2], x[3]), pk2(x[4], x[5]), pk2(x[6], x[7])}; }

__device__ __forceinline__ void rb9(float (&x)[9]) {
#pragma unroll
  for (int i = 0; i < 8; i += 2) { const unsigned u = pk2(x[i], x[i + 1]); x[i] = lo2f(u); x[i + 1] = hi2f(u); }
  x[8] = lo2f(pk2(x[8], x[8]));
}
template <int R>
__device__ __forceinline__ void lnR(const float (&x)[R][9], float (&o)[R][9]) {
  float s[2 * R], pv[R];
#pragma unroll
  for (int r = 0; r < R; ++r) {
    pv[r] = __uint_as_float(__builtin_amdgcn_readfirstlane(__float_as_uint(x[r][0])));
    const f32x2 p2 = {pv[r], pv[r]};
    f32x2 d[4];
#pragma unroll
    for (int i = 0; i < 4; ++i) d[i] = (f32x2){x[r][2 * i], x[r][2 * i + 1]} - p2;
    const float d8 = x[r][8] - pv[r];
    const f32x2 a = (d[0] + d[1]) + (d[2] + d[3]);
    const f32x2 q = __builtin_elementwise_fma(d[3], d[3], __builtin_elementwise_fma(d[2], d[2], __builtin_elementwise_fma(d[1], d[1], d[0] * d[0])));
    s[2 * r] = (a[0] + d8) + a[1];
    s[2 * r + 1] = __builtin_fmaf(d8, d8, q[0]) + q[1];
  }
#pragma unroll
  for (int k = 0; k < 2 * R; ++k) s[k] += rorN<8>(s[k]);
#pragma unroll
  for (int k = 0; k < 2 * R; ++k) s[k] += rorN<4>(s[k]);
#pragma unroll
  for (int k = 0; k < 2 * R; ++k) s[k] += rorN<2>(s[k]);
#pragma unroll
  for (int k = 0; k < 2 * R; ++k) s[k] += rorN<1>(s[k]);
#pragma unroll
  for (int r = 0; r < R; ++r) {
    auto u = __builtin_amdgcn_permlane16_swap(__float_as_uint(s[2 * r]), __float_as_uint(s[2 * r + 1]), false, false);
    s[2 * r] = __uint_as_float(u[0]) + __uint_as_float(u[1]);
  }
#pragma unroll
  for (int r = 0; r < R; ++r) {
    const unsigned u = __float_as_uint(s[2 * r]);
    const float md = (__uint_as_float(__builtin_amdgcn_readlane(u, 0)) + __uint_as_float(__builtin_amdgcn_readlane(u, 32))) * (1.0f / DM);
    const float sq = (__uint_as_float(__builtin_amdgcn_readlane(u, 16)) + __uint_as_float(__builtin_amdgcn_readlane(u, 48))) * (1.0f / DM);
    const unsigned mv = pk2(pv[r] + md, __builtin_fmaxf(__builtin_fmaf(-md, md, sq), 0.f));
    const float m = lo2f(mv), var = hi2f(mv);
    const float ve = lo2f(pk2(var + 1e-5f, 0.f));
    const float den = lo2f(pk2(__builtin_amdgcn_sqrtf(ve), 0.f));
    const float inv = __builtin_amdgcn_rcpf(den);
#pragma unroll
    for (int i = 0; i < 9; ++i) o[r][i] = (x[r][i] - m) * inv;
  }
}
__device__ __forceinline__ void ln9(const float (&x)[9], float (&o)[9]) { lnR<1>((const float (&)[1][9])x, (float (&)[1][9])o); }
__device__ __forceinline__ v4u gat8(float v) {
  const unsigned d = pk2(v, __uint_as_float((unsigned)__builtin_amdgcn_mov_dpp((int)__float_as_uint(v), 0xB1, 0xf, 0xf, true)));
  return (v4u){d, (unsigned)__builtin_amdgcn_mov_dpp((int)d, 0x102, 0xf, 0xf, true), (unsigned)__builtin_amdgcn_mov_dpp((int)d, 0x104, 0xf, 0xf, true),
               (unsigned)__builtin_amdgcn_mov_dpp((int)d, 0x106, 0xf, 0xf, true)};
}
__device__ __forceinline__ void tok_row(const MP& p, const rsrc_t X, unsigned sb1, int L, int s, int r, int lane, const v4u (&v)[18], v4u px, v4u py, unsigned px9, unsigned py9) {
  const long tok = (long)s * SEG + r;
  const int m = lane & 15, g = lane >> 4;
  const int xp = 4 * (g & 1) + 2 * (g >> 1) + (m >> 3);
  const unsigned zr = opq(0u);
  const unsigned one = (g == (m >> 2)) ? (opq(0x3F80u) << (16 * (m & 1))) : zr;
  const unsigned ax = (m & 2) ? zr : one, ay = (m & 2) ? one : zr;
  const v4u A1 = {ax, ay, zr, zr}, A2 = {zr, zr, ax, ay};
  const unsigned on2 = opq(0x3F80u) << (16 * (m & 1));
  const int q2 = (m >> 1) & 3;
  const v4u sel = {q2 == 0 ? on2 : zr, q2 == 1 ? on2 : zr, q2 == 2 ? on2 : zr, q2 == 3 ? on2 : zr};
  f32x4 d1, d2, e1;
  asm volatile("s_nop 1\n\t" MF "%0, %1, %3, 0\n\t" MF "%0, %2, %3, %0" : "=&v"(e1) : "v"(v[0]), "v"(v[1]), "v"(sel));
#pragma unroll
  for (int i = 0; i < 16; ++i) {
    if (i == 0) asm volatile("s_nop 1\n\t" MF "%0, %2, %4, 0\n\t" MF "%1, %3, %4, 0" : "=&v"(d1), "=&v"(d2) : "v"(A1), "v"(A2), "v"(v[2]));
    else asm volatile(MF "%0, %2, %4, %0\n\t" MF "%1, %3, %4, %1" : "+v"(d1), "+v"(d2) : "v"(A1), "v"(A2), "v"(v[2 + i]));
  }
  asm volatile("s_nop 7" : "+v"(d1), "+v"(d2), "+v"(e1));
  float o[9];
#pragma unroll
  for (int i = 0; i < 4; ++i) { o[i] = d1[i]; o[4 + i] = d2[i]; }
  {
    auto a02 = __builtin_amdgcn_permlane32_swap(__float_as_uint(e1[0]), __float_as_uint(e1[2]), false, false);
    auto a13 = __builtin_amdgcn_permlane32_swap(__float_as_uint(e1[1]), __float_as_uint(e1[3]), false, false);
    o[8] = m >= 8 ? __uint_as_float(a13[0]) + __uint_as_float(a13[1]) : __uint_as_float(a02[0]) + __uint_as_float(a02[1]);
  }
  float x[9];
  up8(px, x); x[8] = lo2f(px9);
#pragma unroll
  for (int i = 0; i < 9; ++i) x[i] += o[i];
  if (L == 0) {
    float y[9], ln[9];
    up8(py, y); y[8] = lo2f(py9);
    rb9(x);
    ln9(x, ln);
#pragma unroll
    for (int i = 0; i < 9; ++i) x[i] = __builtin_fmaf(ln[i], y[i], x[i]);
    rb9(x);
    st16(X, (unsigned)lane * 16u, sb1 + (P_X3 + (unsigned)(r >> 3)) * PAIR + (unsigned)(r & 7) * 1024u, pk8(x));
    const v4u x9 = gat8(x[8]);
    if ((lane & 7) == 0) st16(X, (unsigned)xp * 16u, sb1 + (P_H + 4u) * PAIR + 4096u + (unsigned)r * 128u, x9);
    ln9(x, ln);
    st16(X, hoff(r, lane), sb1, pk8(ln));
    const v4u h9 = gat8(ln[8]);
    if ((lane & 7) == 0) st16(X, hoff(r, 64 + xp), sb1, h9);
  } else {
    const rsrc_t O = mkr(p.out + tok * DM, DM * 2u);
    __builtin_amdgcn_raw_buffer_store_b128(pk8(x), O, (unsigned)lane * 16u, 0u, OAUX);
    __builtin_amdgcn_raw_buffer_store_b128(gat8(x[8]), O, (lane & 7) == 0 ? 1024u + (unsigned)xp * 16u : 0xF0000000u, 0u, OAUX);
  }
}

__device__ __forceinline__ void row_role(char* smem) {
  TOK_COMMON();
  const int rw = 4 * xq + (i - 20), r = 2 * rw + (w >> 1);
  const unsigned long long sm = die_mask(X, SG0, L, lane);
  const unsigned tm = (unsigned)die_mask(X, TP0, L, lane), dm = (unsigned)die_mask(X, 0u, L, lane);
  __attribute__((address_space(3))) unsigned* cnt = (__attribute__((address_space(3))) unsigned*)smem;
  BAR();
  if (tid < 64) cnt[tid] = 0u;
  BAR();
  for (int s = w & 1; s < NSEG; s += 2) {
    int ln_ = lane; asm volatile("" : "+v"(ln_));
    const int m = ln_ & 15, g = ln_ >> 4;
    const unsigned l16 = (unsigned)ln_ * 16u, xo = (unsigned)(8 * (4 * (g & 1) + 2 * (g >> 1) + (m >> 3)) + (m & 7)) * 2u;
    const unsigned sb = SEGB(sm, s), sb1 = sb ^ (L ? 0u : 8192u);
    if (s + ARMK < NSEG) {
      const unsigned sv = opq(SENT), sl = SLOT_AT((s + ARMK) % SRING) + ((dm >> ((s + ARMK) % SRING / 3)) & 1u) * 8192u;
      for (int j = r; j < 84; j += 32) {
#pragma unroll
        for (int k = 0; k < 4; ++k) st16(X, l16 + (unsigned)k * 1024u, sl + (unsigned)(j >> 1) * PAIR + (unsigned)(j & 1) * 4096u, (v4u){sv, sv, sv, sv});
      }
    }
    wait_all<PSL>(X, 0u, sb + P_FB * PAIR + 4096u + (unsigned)(L ? rw : r >> 3) * 128u);
    const v4u px = ld16(X, l16, sb + (P_X3 + (unsigned)(r >> 3)) * PAIR + (unsigned)(r & 7) * 1024u);
    const unsigned px9 = __builtin_amdgcn_raw_buffer_load_b16(X, xo, sb + (P_H + 4u) * PAIR + 4096u + (unsigned)r * 128u, 16);
    const rsrc_t YX = mkr(p.yT + (2 * s + (r >> 4)) * 9216, L ? 0u : 18432u);
    const v2u ya = ld8(YX, (unsigned)(ln_ * 256 + ((r ^ m) & 15) * 8), 0u), yb = ld8(YX, (unsigned)(ln_ * 256 + 128 + ((r ^ m) & 15) * 8), 0u);
    const v4u py = {ya.x, ya.y, yb.x, yb.y};
    const unsigned py9 = __builtin_amdgcn_raw_buffer_load_b16(YX, 16384u + (xo >> 3) * 128u + (((unsigned)r ^ (xo >> 4)) & 15u) * 8u + (xo & 6u), 0u, 16);
    if (s < NSEG - 1) {
      wait_all<PSP>(X, 0u, sb + P_FB * PAIR + (unsigned)r * 128u);
      __builtin_amdgcn_s_sleep(RONE);
    } else {
      wait_all<PSP>(X, 0u, sb + P_FA * PAIR + 4096u + (unsigned)r * 128u);
      wait_all<1>(X, (unsigned)(ln_ & 31) * 128u, sb + P_FB * PAIR);
    }
    if (s < NSEG - 1) wait_all<PSP>(X, (unsigned)(ln_ & 31) * 128u, sb + P_FB * PAIR);
    const unsigned j = (unsigned)s & 15u, to = TP0 * XBLK + (j << 20) + ((tm >> (j >> 1)) & 1u) * 8192u + (unsigned)r * 2u * PAIR;
    const unsigned t9 = (TP0 + 8u) * XBLK + ((tm >> 8) & 1u) * 8192u + (j << 17) + (unsigned)(((r >> 2) & 1) * 512 + (4 * (r >> 3) + (r & 3)) * 32);
    const unsigned v9 = (unsigned)(g * (int)PAIR + (m >> 1) * 1024 + (m & 1) * 16);
    v4u v[18];
#pragma unroll
    for (int k = 0; k < 2; ++k) v[k] = ld16(X, v9, t9 + (unsigned)k * 4u * PAIR);
#pragma unroll
    for (int k = 0; k < 16; ++k) v[2 + k] = ld16(X, l16 + (unsigned)(k & 3) * 1024u, to + (unsigned)(k >> 3) * PAIR + (unsigned)(k & 4) * 1024u);
    SB();
    tok_row(p, X, sb1, L, s, r, ln_, v, px, py, px9, py9);
    VM0();
    unsigned o1 = 0u;
    if (ln_ == 0) o1 = __hip_atomic_fetch_add(cnt + s, 1u, __ATOMIC_RELAXED, __HIP_MEMORY_SCOPE_WORKGROUP);
    if (__builtin_amdgcn_readfirstlane(o1) == 1u && ln_ < 2 - L) st4(X, ln_ ? sb1 : sb + 2048u, P_FB * PAIR + 4096u + (unsigned)rw * 128u, opq(1u));
  }
}

__device__ __forceinline__ void rows_task(const MP& p, const rsrc_t X, unsigned long long sm, u16* ys, int grp, int ng) {
  const int tid = threadIdx.x, lane = tid & 63, w = __builtin_amdgcn_readfirstlane(tid >> 6);
  const int m = lane & 15, g = lane >> 4;
  const int xp = 4 * (g & 1) + 2 * (g >> 1) + (m >> 3), c9 = 512 + 8 * xp + (m & 7);
  const unsigned sb = SEGB(sm, grp >> 2);
  v4u yv[5], xa[4];
  unsigned x9[4];
#pragma unroll
  for (int i = 0; i < 5; ++i) yv[i] = ld16(mkr(p.yT + (grp >> 1) * 9216, 18432u), (unsigned)(tid + 256 * i) * 16u, 0u);
#pragma unroll
  for (int rr = 0; rr < 4; ++rr) {
    const long t = grp * 8 + w + 4 * (rr & (2 * ng - 1));
    xa[rr] = *(const v4u*)&p.xin[t * DM + 8 * lane]; x9[rr] = p.xin[t * DM + c9];
  }
#pragma unroll
  for (int i = 0; i < 5; ++i) ((v4u*)ys)[tid + 256 * i] = yv[i];
  BAR();
  const unsigned o9 = (lane & 7) == 0 ? 0u : 0xF0000000u;
#pragma unroll 1
  for (int k = 0; k < ng; ++k) {
    float x[2][9], y[2][9], ln[2][9];
    const int t0 = (grp + k) * 8 + w;
#pragma unroll
    for (int rr = 0; rr < 2; ++rr) {
      const int j = (t0 + 4 * rr) & 15;
      const v2u ya = *(const v2u*)&ys[lane * 128 + (j ^ m) * 4], yb = *(const v2u*)&ys[lane * 128 + 64 + (j ^ m) * 4];
      up8(xa[rr], x[rr]); x[rr][8] = lo2f(x9[rr]); up8((v4u){ya.x, ya.y, yb.x, yb.y}, y[rr]); y[rr][8] = bf2f(ys[(c9 >> 2) * 64 + (j ^ xp) * 4 + (m & 3)]);
      xa[rr] = xa[2 + rr]; x9[rr] = x9[2 + rr];
    }
    lnR<2>(x, ln);
#pragma unroll
    for (int rr = 0; rr < 2; ++rr) {
#pragma unroll
      for (int i = 0; i < 9; ++i) x[rr][i] = __builtin_fmaf(ln[rr][i], y[rr][i], x[rr][i]);
      rb9(x[rr]);
      const int r = (t0 + 4 * rr) & 31;
      st16(X, (unsigned)lane * 16u, sb + (P_X3 + (unsigned)(r >> 3)) * PAIR + (unsigned)(r & 7) * 1024u, pk8(x[rr]));
      st16(X, (unsigned)xp * 16u + o9, sb + (P_H + 4u) * PAIR + 4096u + (unsigned)r * 128u, gat8(x[rr][8]));
    }
    lnR<2>(x, ln);
#pragma unroll
    for (int rr = 0; rr < 2; ++rr) {
      const int r = (t0 + 4 * rr) & 31;
      st16(X, hoff(r, lane), sb, pk8(ln[rr]));
      st16(X, hoff(r, 64 + xp) + o9, sb, gat8(ln[rr][8]));
    }
  }
  VM0();
  BAR();
  if (tid < ng) st4(X, (unsigned)tid * 128u, sb + P_FB * PAIR + 4096u + (unsigned)(grp & 3) * 128u, opq(1u));
}

__device__ __forceinline__ void arrive(const MP& p, int k, int grp) {
  VM0();
  BAR();
  if (threadIdx.x < 64) {
    unsigned old = 0u;
    if (threadIdx.x == 0) old = __hip_atomic_fetch_add(p.xb + MF_OFF / 4u + (k * 16 + grp) * 32, 1u, __ATOMIC_RELAXED, __HIP_MEMORY_SCOPE_AGENT);
    if (__builtin_amdgcn_readfirstlane(old) == 15u && threadIdx.x < 8) st4(mkr(p.xb, XB_BYTES), threadIdx.x * 128u, RDY_OFF(k) + (unsigned)grp * 1024u, opq(1u));
  }
}
__device__ __forceinline__ void wait_ready(const MP& p, int k) {
  if (threadIdx.x < 64) wait_all<PSL>(mkr(p.xb, XB_BYTES), (threadIdx.x % 9u) * 1024u, RDY_OFF(k) + (blockIdx.x & 7u) * 128u);
  BAR();
}
__device__ __forceinline__ void start_work(char* smem, int wk) {
  const MP p = getp();
  const int w = __builtin_amdgcn_readfirstlane(threadIdx.x >> 6);
#define NARM (ARMK - 3u)
  unsigned pf[PFM + 1];
  conv_task(p.kvT, p.ssm_k, p.ssm_D, p.yT, (u16*)(smem + w * 32768), 4 * wk + w, [&]() {
#pragma unroll
    for (int i = 0; i < PFM; ++i) {
      const int z = (i - 1) * 36864 + wk * 256 + threadIdx.x, a = z % 3;
      pf[i] = *(const unsigned*)(i == 0 ? p.Wo + (long)(z + 36864) * 64 : (a == 0 ? p.Wq : (a == 1 ? p.Wk : p.Wv)) + (long)(36863 - z / 3) * 64);
    }
  }, [&]() {
    const unsigned sv = opq(SENT);
    constexpr unsigned S16 = SLOT / 16u;
#pragma unroll
    for (unsigned k = 0; k < (NARM * S16 + 32767u) / 32768u; ++k) {
      const unsigned i = k * 32768u + wk * 256 + threadIdx.x;
      st16(mkr(p.xb, XB_BYTES), i < NARM * S16 && wk < 128 ? SLOT_AT(3u + i / S16) + (i % S16) * 16u : 0xF0000000u, 0u, (v4u){sv, sv, sv, sv});
    }
  });
#pragma unroll
  for (int i = 0; i < PFM; ++i) asm volatile("" :: "v"(pf[i]));
  arrive(p, 0, wk >> 4);
}
__device__ __forceinline__ void token_start(char* smem) {
  TOK_COMMON();
  (void)w;
  const int k = 4 * i + xq;
  if (L || (i >= 12 && i < 16)) start_work(smem, L ? 32 + k : 80 + k);
  if (i >= (L ? 12 : 20)) {
    const unsigned long long sm = die_mask(X, SG0, 0, lane);
    int g = L ? 111 - k : k - 96;
    while (g < 120) {
      if (g >= 0 && g < 64) {
        wait_ready(p, 0);
      }
      rows_task(p, X, sm, (u16*)smem, g < 0 ? g + 16 : 16 + 2 * g, g < 0 ? 1 : 2);
      g += g < 0 ? 16 : (L ? 48 : 120);
    }
  }
}
__device__ __forceinline__ void token_wg(char* smem) {
  if ((unsigned)(size_t)(__attribute__((address_space(3))) char*)smem != 0u) return;
  token_start(smem);
  const int i = ((int)blockIdx.x - NCHW) >> 3;
  if (i < 12) qkv_role();
  else if (i < 20) ap_role();
  else row_role(smem);
}

__global__ __launch_bounds__(256, 1) void k_main(MP) {
  extern __shared__ __attribute__((aligned(16))) char smem[];
  asm volatile("LKBEG:");
  if ((int)blockIdx.x < NCHW) chain_wg(smem);
  else token_wg(smem);
}

extern "C" void kernel_launch(void* const* d_in, const int* in_sizes, int n_in, void* d_out, int out_size, void* d_ws,
                              size_t ws_size, hipStream_t stream) {
  const u16* A_q = (const u16*)d_in[0];
  const u16* A_k = (const u16*)d_in[1];
  const u16* A_v = (const u16*)d_in[2];
  const u16* A_shift = (const u16*)d_in[3];
  const u16* A_ssmk = (const u16*)d_in[4];
  const u16* A_ssmD = (const u16*)d_in[5];
  const u16* B_mem0 = (const u16*)d_in[7];
  const u16* B_Wq = (const u16*)d_in[8];
  const u16* B_Wk = (const u16*)d_in[9];
  const u16* B_Wv = (const u16*)d_in[10];
  const u16* B_Wo = (const u16*)d_in[11];

  char* ws = (char*)d_ws;
  size_t off = (size_t)(-(uintptr_t)ws) & (XBLK - 1u);
  auto alloc = [&](size_t bytes) { char* q = ws + off; off += (bytes + 4095) & ~(size_t)4095; return q; };
  unsigned* xb = (unsigned*)alloc((size_t)XB_BYTES);
  u16* kvT = (u16*)alloc((size_t)DM * S_LEN * 2);
  u16* yT = (u16*)alloc((size_t)DM * S_LEN * 2);

  PreP pp;
  pp.kk = A_k; pp.vv = A_v; pp.shift_k = A_shift; pp.ssm_k = A_ssmk; pp.ssm_D = A_ssmD; pp.yT = yT; pp.kvT = kvT;
  pp.Wq = B_Wq; pp.Wk = B_Wk; pp.Wv = B_Wv; pp.Wo = B_Wo; pp.xb = xb; pp.mem0 = B_mem0;
  k_pre<<<240 + PRE_FILL + 256 + 288, 256, 0, stream>>>(pp);
  MP mp;
  mp.out = (u16*)d_out; mp.xb = xb;
  mp.xin = A_q; mp.yT = yT; mp.kvT = kvT; mp.ssm_k = A_ssmk; mp.ssm_D = A_ssmD;
  mp.Wq = B_Wq; mp.Wk = B_Wk; mp.Wv = B_Wv; mp.Wo = B_Wo;
  const int smem_bytes = 160 * 1024;
  (void)hipFuncSetAttribute((const void*)k_main, hipFuncAttributeMaxDynamicSharedMemorySize, smem_bytes);
  k_main<<<NCHW + NTOKW, 256, smem_bytes, stream>>>(mp);
}
```

```cpp
#include <hip/hip_runtime.h>
#include <stdint.h>

typedef unsigned short u16;
using bf16x8 = __attribute__((ext_vector_type(8))) short;
using f32x4 = __attribute__((ext_vector_type(4))) float;
using f32x2 = __attribute__((ext_vector_type(2))) float;
using bfx8 = __attribute__((ext_vector_type(8))) __bf16;
using bfx2 = __attribute__((ext_vector_type(2))) __bf16;
typedef unsigned v4u __attribute__((ext_vector_type(4)));
typedef unsigned v2u __attribute__((ext_vector_type(2)));
typedef short v4s __attribute__((ext_vector_type(4)));

#define S_LEN 2048
#define DM 576
#define NH 32
#define DH 128
#define HD (NH * DH)
#define SEG 32
#define NSEG 64

__device__ __forceinline__ float bf2f(u16 x) { return __uint_as_float(((unsigned)x) << 16); }
__device__ __forceinline__ unsigned pk2(float a, float b) { f32x2 v = {a, b}; bfx2 r = __builtin_convertvector(v, bfx2); return __builtin_bit_cast(unsigned, r); }
__device__ __forceinline__ float lo2f(unsigned u) { return __uint_as_float(u << 16); }
__device__ __forceinline__ float hi2f(unsigned u) { return __uint_as_float(u & 0xFFFF0000u); }

#define NFC 128
#define XBLK 2097152u
#define PAIR 16384u
#define SLOT 688128u
#define AGO 589824u
#define AGT 655360u
#define SG0 22
#define TP0 86
#define NXB 95
#define XB_BYTES (NXB * XBLK)
#define SLOT_AT(i) ((unsigned)((i) / 3) * XBLK + (unsigned)((i) % 3) * SLOT)
#define HT_OFF SLOT_AT(65)
#define CTAB_OFF (HT_OFF + 16384u)
#define MF_OFF (HT_OFF + 20480u)
#define TMO_OFF (MF_OFF + 15u * 128u)
#define RDY_OFF(k) (MF_OFF + 4096u + (k) * 10240u)
#define MF_BYTES 16384u
#define TRC_OFF SLOT_AT(64)
#define CAL_OFF (XBLK - 16384u)
#define P_KV 96u
#define P_H 112u
#define P_X3 117u
#define P_FA 121u
#define P_FB 122u
#define ARMK 8
#define NCAL 8
#define CALT 560u
#define CALM 1
#define SRING 12
#define TRING 64
#define KPM 0
__device__ __forceinline__ unsigned kv2(const unsigned (&u)[4], unsigned v, v2u w0, v2u w1) {
  const float a[4] = {hi2f(w0.y), lo2f(w0.y), hi2f(w0.x), lo2f(w0.x)}, b[4] = {hi2f(w1.y), lo2f(w1.y), hi2f(w1.x), lo2f(w1.x)};
  unsigned r = pk2(lo2f(u[0]) * a[0], hi2f(u[0]) * b[0]);
#pragma unroll
  for (int i = 1; i < 4; ++i) { const unsigned q = pk2(lo2f(u[i]) * a[i], hi2f(u[i]) * b[i]); r = pk2(lo2f(r) + lo2f(q), hi2f(r) + hi2f(q)); }
  return pk2(lo2f(r) * lo2f(v), hi2f(r) * hi2f(v));
}
struct PreP {
  const u16* kk; const u16* vv; const u16* shift_k; const u16* ssm_k; const u16* ssm_D; u16* yT; u16* kvT;
  const u16* Wq; const u16* Wk; const u16* Wv; const u16* Wo; unsigned* xb; const u16* mem0;
};
#define PRE_FILL 112
#define PFN 2
__global__ __launch_bounds__(256, 4) void k_pre(PreP p) {
  __shared__ __attribute__((aligned(16))) float sh[64 * 36];
  const int tid = threadIdx.x, b0 = blockIdx.x, b = b0 < 240 ? b0 : (b0 < 528 ? b0 + PRE_FILL + 256 : b0 - 288);
  const __amdgpu_buffer_rsrc_t rx = __builtin_amdgcn_make_buffer_rsrc((void*)p.xb, 0, XB_BYTES, 0x00020000);
  const v4u ones = {0xFFFFFFFFu, 0xFFFFFFFFu, 0xFFFFFFFFu, 0xFFFFFFFFu}, zero = {0, 0, 0, 0};
  if (b < 96) {
    if (tid >= 64 || (KPM & 16)) return;
    const unsigned cb = (unsigned)b * XBLK + CAL_OFF;
    __builtin_amdgcn_raw_buffer_store_b128(zero, rx, (unsigned)((tid >> 3) & 1) * 8192u + (unsigned)(tid >> 4) * 256u + (unsigned)(tid & 7) * 16u, cb, 16);
    asm volatile("s_waitcnt vmcnt(0)" ::: "memory");
    unsigned mn[2] = {~0u, ~0u};
    for (int i = 0; i < NCAL; ++i) {
      const unsigned long long c0 = __builtin_amdgcn_s_memtime();
      asm volatile("s_waitcnt lgkmcnt(0)" ::: "memory");
      const unsigned v = __builtin_amdgcn_raw_buffer_load_b32(rx, 0, cb + (unsigned)(i & 1) * 8192u + (unsigned)(i >> 1) * 256u, 16);
      asm volatile("s_waitcnt vmcnt(0)" :: "v"(v) : "memory");
      const unsigned d = (unsigned)(__builtin_amdgcn_s_memtime() - c0);
      if (i & 1) mn[1] = d < mn[1] ? d : mn[1]; else mn[0] = d < mn[0] ? d : mn[0];
      const unsigned lo = mn[0] < mn[1] ? mn[0] : mn[1], hi = mn[0] < mn[1] ? mn[1] : mn[0];
      if (i >= CALM && lo < CALT && hi > lo + 100u) break;
    }
    unsigned xcc; asm volatile("s_getreg_b32 %0, hwreg(HW_REG_XCC_ID)" : "=s"(xcc));
    const unsigned res = mn[1] < mn[0] ? 1u : 0u;
    if (tid == 0) __builtin_amdgcn_raw_buffer_store_b32(res ^ ((xcc >> 2) & 1u), rx, 0, CTAB_OFF + (unsigned)b * 4u, 16);
  } else if (b < 240) {
    if (KPM & 1) return;
    const __amdgpu_buffer_rsrc_t rk = __builtin_amdgcn_make_buffer_rsrc((void*)p.kk, 0, S_LEN * DM * 2, 0x00020000);
    float (*kx)[132] = (float (*)[132])sh;
    float (*cf)[140] = (float (*)[140])(sh + 4 * 132);
    const int d0 = (b - 96) * 4, q = tid & 3, dd = (tid >> 2) & 3, t0 = (tid >> 4) * 8;
    const float Dv = bf2f(p.ssm_D[d0 + dd]);
    {
      const int s = tid >> 1, d = d0 + (tid & 1) * 2;
      unsigned u[4];
#pragma unroll
      for (int a = 0; a < 4; ++a) u[a] = __builtin_amdgcn_raw_buffer_load_b32(rk, (unsigned)((s - 3 + a) * DM + d) * 2u, 0, 0);
      const unsigned vr = *(const unsigned*)&p.vv[s * DM + d], c0 = *(const unsigned*)&p.ssm_k[(long)(d0 + (tid >> 6)) * S_LEN + (tid & 63) * 2];
      const v4u w = *(const v4u*)&p.shift_k[d * 4];
      const unsigned r = kv2(u, vr, (v2u){w.x, w.y}, (v2u){w.z, w.w});
      kx[(tid & 1) * 2][s] = lo2f(r); kx[(tid & 1) * 2 + 1][s] = hi2f(r);
      *(f32x2*)&cf[tid >> 6][8 + (tid & 63) * 2] = (f32x2){lo2f(c0), hi2f(c0)};
      if (tid < 32) cf[tid >> 3][tid & 7] = 0.f;
    }
    __syncthreads();
    float y[8];
#pragma unroll
    for (int i = 0; i < 8; ++i) y[i] = 0.f;
#pragma unroll 1
    for (int j = 8 * q; j <= t0; j += 32) {
      f32x4 kk[2], cw[4];
#pragma unroll
      for (int x = 0; x < 2; ++x) kk[x] = *(const f32x4*)&kx[dd][j + 4 * x];
#pragma unroll
      for (int x = 0; x < 4; ++x) cw[x] = *(const f32x4*)&cf[dd][t0 - j + 4 * x];
#pragma unroll
      for (int jj = 0; jj < 8; ++jj)
#pragma unroll
        for (int i = 0; i < 8; ++i) y[i] = __builtin_fmaf(cw[(8 + i - jj) >> 2][(8 + i - jj) & 3], kk[jj >> 2][jj & 3], y[i]);
    }
#pragma unroll
    for (int i = 0; i < 8; ++i) {
      y[i] += __uint_as_float((unsigned)__builtin_amdgcn_mov_dpp((int)__float_as_uint(y[i]), 0xB1, 0xf, 0xf, true));
      y[i] += __uint_as_float((unsigned)__builtin_amdgcn_mov_dpp((int)__float_as_uint(y[i]), 0x4E, 0xf, 0xf, true));
    }
    const float ya = q == 0 ? y[0] : (q == 1 ? y[2] : (q == 2 ? y[4] : y[6])), yb = q == 0 ? y[1] : (q == 1 ? y[3] : (q == 2 ? y[5] : y[7]));
    const int t = t0 + 2 * q, f = (d0 >> 3) & 15;
    const unsigned r0 = pk2(ya, yb), r1 = pk2(kx[dd][t] * Dv, kx[dd][t + 1] * Dv), o = pk2(lo2f(r0) + lo2f(r1), hi2f(r0) + hi2f(r1));
    u16* dst = p.yT + (t >> 4) * 9216 + (d0 >> 2) * 64 + dd;
    dst[((t & 15) ^ f) << 2] = (u16)(o & 0xFFFFu); dst[(((t + 1) & 15) ^ f) << 2] = (u16)(o >> 16);
  } else if (b < 240 + PRE_FILL) {
    if (KPM & 32) return;
    const unsigned i0 = (unsigned)(b - 240) * 256u + tid;
    if (i0 < (SLOT - AGO) / 16u) {
      const unsigned o = AGO + i0 * 16u, s = o < AGT ? (i0 >> 11) * 4u + ((o >> 10) & 3u) : 8u, r = o & 1023u;
      const unsigned gm = s < 8u ? r >> 8 : (r >> 6) & 3u, m3 = (r >> 4) & 3u, ga = s < 8u ? (r >> 6) & 3u : r >> 8, row = 4u * (gm & 1u) + 2u * (gm >> 1) + (m3 & 1u), half = m3 >> 1;
      const unsigned* src = (const unsigned*)(p.mem0 + row * DM + 64u * s + 16u * ga + 2u * half);
      __builtin_amdgcn_raw_buffer_store_b128((v4u){src[0], src[2], src[4], src[6]}, rx, o, 0, 16);
    }
    for (unsigned i = i0; i < 131072u + MF_BYTES / 16u; i += PRE_FILL * 256)
      __builtin_amdgcn_raw_buffer_store_b128(zero, rx, i < 131072u ? (SG0 + (i >> 11)) * XBLK + P_FA * PAIR + (i & 2047u) * 16u : MF_OFF + (i & 2047u) * 16u, 0, 16);
    for (unsigned i = i0; i < 2u * SLOT / 16u; i += PRE_FILL * 256) __builtin_amdgcn_raw_buffer_store_b128(ones, rx, SLOT + i * 16u, 0, 16);
  } else if (b >= 240 + PRE_FILL + 256) {
    if (KPM & 2) return;
    const __amdgpu_buffer_rsrc_t rk = __builtin_amdgcn_make_buffer_rsrc((void*)p.kk, 0, S_LEN * DM * 2, 0x00020000);
    u16 (*tile)[72] = (u16 (*)[72])sh;
    const int bid = b - 240 - PRE_FILL - 256, d0 = (bid % 9) * 64, s0 = (bid / 9) * 64;
    const int sr = (tid >> 3) * 2, d = d0 + (tid & 7) * 8;
    v4u kr[5], vr[2], w[4];
#pragma unroll
    for (int a = 0; a < 5; ++a) kr[a] = __builtin_amdgcn_raw_buffer_load_b128(rk, (unsigned)((s0 + sr - 3 + a) * DM + d) * 2u, 0, 0);
#pragma unroll
    for (int a = 0; a < 2; ++a) vr[a] = *(const v4u*)&p.vv[(long)(s0 + sr + a) * DM + d];
#pragma unroll
    for (int a = 0; a < 4; ++a) w[a] = *(const v4u*)&p.shift_k[(d + 2 * a) * 4];
    __builtin_amdgcn_sched_barrier(0);
#pragma unroll
    for (int it = 0; it < 2; ++it) {
      v4u o;
#pragma unroll
      for (int j = 0; j < 4; ++j) { const unsigned u[4] = {kr[it][j], kr[it + 1][j], kr[it + 2][j], kr[it + 3][j]}; o[j] = kv2(u, vr[it][j], (v2u){w[j].x, w[j].y}, (v2u){w[j].z, w[j].w}); }
      *(v4u*)&tile[sr + it][(tid & 7) * 8] = o;
    }
    __syncthreads();
    const int lane = tid & 63, wv = tid >> 6, g = lane >> 4, r = (lane >> 2) & 3, c = lane & 3;
    v2u o[4];
#pragma unroll
    for (int i = 0; i < 4; ++i) o[i] = __builtin_bit_cast(v2u, __builtin_amdgcn_ds_read_tr16_b64_v4i16((__attribute__((address_space(3))) v4s*)&tile[16 * g + 4 * i + r][16 * wv + 4 * c]));
    u16* dst = p.kvT + (long)(d0 + 16 * wv + (lane & 15)) * S_LEN + s0 + 16 * g;
    *(v4u*)dst = (v4u){o[0].x, o[0].y, o[1].x, o[1].y};
    *(v4u*)(dst + 8) = (v4u){o[2].x, o[2].y, o[3].x, o[3].y};
  } else {
    if (KPM & 4) return;
    const int x = b & 7, k = (b - 240 - PRE_FILL) >> 3;
    unsigned long long pc; unsigned l0, l1, l2, l3; int rel;
    asm volatile("s_getpc_b64 %0\nLPRE:\n\ts_mov_b32 %1, LKBEG-LPRE\n\ts_mov_b32 %2, LCHEND-LKBEG\n\ts_mov_b32 %3, LQVEND-LKBEG\n\ts_mov_b32 %4, LQKEND-LKBEG\n\ts_mov_b32 %5, LAPEND-LKBEG"
                 : "=s"(pc), "=s"(rel), "=s"(l0), "=s"(l1), "=s"(l2), "=s"(l3));
    l0 = l0 > l1 ? l0 : l1; l2 = l2 > l3 ? l2 : l3; l0 = l0 > l2 ? l0 : l2;
    const unsigned o = (unsigned)(k * 64 + (tid & 63)) * 64u;
    unsigned a[PFN + 2];
    a[PFN] = *(const __attribute__((address_space(1))) unsigned*)(pc + rel + (o < l0 ? o : 0u));
    a[PFN + 1] = __builtin_amdgcn_raw_buffer_load_b32(rx, k == 31 ? tid * XBLK + CAL_OFF + 8064u : 0xF0000000u, 0, 16);
#pragma unroll
    for (int i = 0; i < PFN; ++i) {
      const int q = (i * 32 + k) * 256 + tid, r2 = q >> 5, gr = r2 >> 5, row = (gr / 3) * 64 + (x >> 2) * 32 + (r2 & 31), off = (q & 31) * 32, ty = gr % 3;
      a[i] = *(const unsigned*)(gr < 27 ? (ty == 0 ? p.Wq : (ty == 1 ? p.Wk : p.Wv)) + (long)row * HD + (x & 3) * 1024 + off
                                        : p.Wo + ((long)(x & 3) * DM + (gr < 36 ? gr - 27 : 0) * 64 + (x >> 2) * 32 + (r2 & 31)) * 1024 + off);
    }
#pragma unroll
    for (int i = 0; i < PFN + 2; ++i) asm volatile("" :: "v"(a[i]));
  }
}

#define CREV_STRIDE 2208
#define SP(k)
#define LWAIT() asm volatile("s_waitcnt lgkmcnt(0)" ::: "memory")
template <class E, class F>
__device__ __forceinline__ void conv_task(const u16* __restrict__ kvT, const u16* __restrict__ ssm_k, const u16* __restrict__ ssm_D, u16* yT, u16* shb, int d, E early, F fill) {
  const int lane = threadIdx.x & 63;
  u16* crev = shb + 64;
  u16* kvs = crev + 4 * CREV_STRIDE;
  const int X0 = 2048 + 63;
  SP(0);
  v4u cv[4], kq[4];
#pragma unroll
  for (int j = 0; j < 4; ++j) { cv[j] = *(const v4u*)&ssm_k[(long)d * S_LEN + lane * 32 + 8 * j]; kq[j] = *(const v4u*)&kvT[(long)d * S_LEN + (j * 64 + lane) * 8]; }
  const float Dv = bf2f(ssm_D[d]);
  __builtin_amdgcn_sched_barrier(0);
  early();
  __builtin_amdgcn_sched_barrier(0);
  const v4u z4 = {0u, 0u, 0u, 0u};
  *(v4u*)&kvs[(lane >> 2) * 40 + (lane & 3) * 8] = z4;
  if (lane < 32) *(v4u*)&crev[(lane >> 3) * CREV_STRIDE + (lane & 7) * 8] = z4;
  if (lane < 36) *(v4u*)&crev[(lane / 9) * CREV_STRIDE + 2112 + (lane % 9) * 8] = z4;
  {
    unsigned r[18];
#pragma unroll
    for (int k = 0; k < 16; ++k) { const unsigned u = cv[3 - (k >> 2)][3 - (k & 3)]; r[k] = __builtin_amdgcn_alignbyte(u, u, 2); }
#pragma unroll
    for (int k = 0; k < 2; ++k) r[16 + k] = (unsigned)__builtin_amdgcn_update_dpp(0, (int)r[k], 0x138, 0xf, 0xf, true);
    LWAIT();
#pragma unroll
    for (int rho = 0; rho < 4; ++rho)
#pragma unroll
      for (int j = 0; j < 4; ++j) {
        v4u o;
#pragma unroll
        for (int k = 0; k < 4; ++k) { const int q = 4 * j + k + (rho >> 1); o[k] = (rho & 1) ? __builtin_amdgcn_alignbyte(r[q + 1], r[q], 2) : r[q]; }
        *(v4u*)&crev[rho * CREV_STRIDE + 2080 - 32 * lane + 8 * j] = o;
      }
    if (lane == 63) {
      *(v2u*)&crev[1 * CREV_STRIDE + 60] = (v2u){0u, r[0] << 16};
      *(v2u*)&crev[2 * CREV_STRIDE + 60] = (v2u){0u, r[0]};
      *(v2u*)&crev[3 * CREV_STRIDE + 60] = (v2u){r[0] << 16, __builtin_amdgcn_alignbyte(r[1], r[0], 2)};
    }
#pragma unroll
    for (int j = 0; j < 4; ++j) *(v4u*)&kvs[(16 + j * 16 + (lane >> 2)) * 40 + (lane & 3) * 8] = kq[j];
  }
  fill();
  LWAIT();
  SP(1);
  const int n = lane & 15, g = lane >> 4;
  const int rho = (X0 - n) & 3;
  f32x4 acc[4][2];
#pragma unroll
  for (int q = 0; q < 8; ++q) acc[q >> 1][q & 1] = (f32x4){0.f, 0.f, 0.f, 0.f};
  {
    typedef __attribute__((address_space(3))) const v2u* l2p;
    typedef __attribute__((address_space(3))) const bf16x8* l4p;
    unsigned a0 = (unsigned)(size_t)(__attribute__((address_space(3))) u16*)&crev[rho * CREV_STRIDE + (X0 - (n - 8 * g) - rho)] - 3232u, a1 = a0 + 8u;
    unsigned b0 = (unsigned)(size_t)(__attribute__((address_space(3))) u16*)&kvs[(14 + n) * 40 + 8 * g];
    asm volatile("" : "+v"(a1));
#define AFR(m, f) ({ const unsigned o_ = 3232u - 64u * (m) - 1024u * ((f) >> 1) - 32u * ((f) & 1); const v2u lo_ = *(l2p)(size_t)(a0 + o_), hi_ = *(l2p)(size_t)(a1 + o_); \
                     __builtin_bit_cast(bf16x8, (v4u){lo_.x, lo_.y, hi_.x, hi_.y}); })
#define BFR(m, j) (*(l4p)(size_t)(b0 + 80u * (2 - (m)) + 1280u * (j)))
    bf16x8 af[2][8], bf[2][4];
#pragma unroll
    for (int f = 0; f < 8; ++f) { af[0][f] = AFR(0, f); af[1][f] = af[0][f]; __builtin_amdgcn_sched_barrier(0); }
#pragma unroll
    for (int j = 0; j < 4; ++j) { bf[0][j] = BFR(0, j); bf[1][j] = bf[0][j]; }
#pragma unroll 1
    for (int mo = 0; mo < 8; ++mo) {
#pragma unroll
      for (int m = 0; m < 2; ++m) {
        int k = 0;
#pragma unroll
        for (int a = 0; a < 4; ++a)
#pragma unroll
          for (int ct = a; ct < 4; ++ct)
#pragma unroll
            for (int ti = 0; ti < 2; ++ti) {
              acc[ct][ti] = __builtin_amdgcn_mfma_f32_16x16x32_bf16(__builtin_bit_cast(bfx8, af[m][2 * a + ti]), __builtin_bit_cast(bfx8, bf[m][ct - a]), acc[ct][ti], 0, 0, 0);
              if (k < 2) af[m ^ 1][k] = AFR(m + 1, k);
              else if (k < 6) bf[m ^ 1][k - 2] = BFR(m + 1, k - 2);
              else if (k < 12) af[m ^ 1][k - 4] = AFR(m + 1, k - 4);
              __builtin_amdgcn_sched_barrier(0);
              ++k;
            }
      }
      a0 -= 128u; a1 -= 128u; b0 -= 160u;
    }
  }
  SP(2);
  u16* yo = crev;
  LWAIT();
#pragma unroll
  for (int q = 0; q < 8; ++q) {
    const int o = (16 * (q >> 1) + n) * 40 + 16 * (q & 1) + 4 * g;
    const v2u kx = *(const v2u*)&kvs[16 * 40 + o];
    const f32x4 y = acc[q >> 1][q & 1];
    const unsigned y0 = pk2(y[0], y[1]), y1 = pk2(y[2], y[3]), r0 = pk2(lo2f(kx.x) * Dv, hi2f(kx.x) * Dv), r1 = pk2(lo2f(kx.y) * Dv, hi2f(kx.y) * Dv);
    *(v2u*)&yo[o] = (v2u){pk2(lo2f(y0) + lo2f(r0), hi2f(y0) + hi2f(r0)), pk2(lo2f(y1) + lo2f(r1), hi2f(y1) + hi2f(r1))};
  }
  LWAIT();
  __builtin_amdgcn_s_barrier();
  asm volatile("" ::: "memory");
  {
    const int tid = threadIdx.x, f = (d >> 3) & 15;
    const u16* y0 = shb - (tid >> 6) * 16384 + 64;
#pragma unroll
    for (int k = 0; k < 4; ++k) {
      const int tile = 32 * k + (tid >> 3), t = 16 * tile + 2 * ((tid & 7) ^ (f >> 1));
      unsigned u[4];
#pragma unroll
      for (int c = 0; c < 4; ++c) u[c] = *(const unsigned*)&y0[c * 16384 + (t >> 5) * 40 + (t & 31)];
      const v2u e = {__builtin_amdgcn_perm(u[1], u[0], 0x05040100u), __builtin_amdgcn_perm(u[3], u[2], 0x05040100u)}, o = {__builtin_amdgcn_perm(u[1], u[0], 0x07060302u), __builtin_amdgcn_perm(u[3], u[2], 0x07060302u)};
      if (tile >= NFC / 16) __builtin_amdgcn_raw_buffer_store_b128((f & 1) ? (v4u){o.x, o.y, e.x, e.y} : (v4u){e.x, e.y, o.x, o.y}, __builtin_amdgcn_make_buffer_rsrc((void*)yT, 0, DM * S_LEN * 2, 0x00020000),
                                                                   tile * 18432 + (tid & 7) * 16, (d >> 2) * 128, 16);
    }
  }
}

#define NCHW 64
#define NTOKW 192
#define PSL 4
#define SENT 0xFFFFFFFFu
#define SPIN_CAP 300000u
struct MP {
  const u16* Wq; const u16* Wk; const u16* Wv; const u16* Wo; u16* out; unsigned* xb;
  const u16* xin; u16* yT; u16* kvT; const u16* ssm_k; const u16* ssm_D;
};

__device__ __forceinline__ MP getp() {
  MP r;
#if defined(__HIP_DEVICE_COMPILE__)
  const __attribute__((address_space(4))) unsigned long long* k = (const __attribute__((address_space(4))) unsigned long long*)__builtin_amdgcn_kernarg_segment_ptr();
  asm volatile("" : "+s"(k));
#define GP(T, i) ((T*)(__attribute__((address_space(1))) T*)k[i])
  r.Wq = GP(const u16, 0);
  r.Wk = GP(const u16, 1);
  r.Wv = GP(const u16, 2);
  r.Wo = GP(const u16, 3);
  r.out = GP(u16, 4);
  r.xb = GP(unsigned, 5);
  r.xin = GP(const u16, 6);
  r.yT = GP(u16, 7);
  r.kvT = GP(u16, 8);
  r.ssm_k = GP(const u16, 9);
  r.ssm_D = GP(const u16, 10);
#endif
  return r;
}
typedef __amdgpu_buffer_rsrc_t rsrc_t;
__device__ __forceinline__ rsrc_t mkr(const void* p, unsigned bytes) { return __builtin_amdgcn_make_buffer_rsrc((void*)p, 0, bytes, 0x00020000); }
__device__ __forceinline__ void st16(rsrc_t r, unsigned vo, unsigned so, v4u v) { __builtin_amdgcn_raw_buffer_store_b128(v, r, vo, so, 16); }
__device__ __forceinline__ void st4(rsrc_t r, unsigned vo, unsigned so, unsigned v) { __builtin_amdgcn_raw_buffer_store_b32(v, r, vo, so, 16); }
__device__ __forceinline__ v4u ld16(rsrc_t r, unsigned vo, unsigned so) { return __builtin_amdgcn_raw_buffer_load_b128(r, vo, so, 16); }
__device__ __forceinline__ v2u ld8(rsrc_t r, unsigned vo, unsigned so) { return __builtin_amdgcn_raw_buffer_load_b64(r, vo, so, 16); }
__device__ __forceinline__ unsigned ld4(rsrc_t r, unsigned vo, unsigned so) { return __builtin_amdgcn_raw_buffer_load_b32(r, vo, so, 16); }
__device__ __forceinline__ unsigned opq(unsigned x) { asm volatile("" : "+v"(x)); return x; }

__device__ __forceinline__ unsigned long long die_mask(rsrc_t X, unsigned first, int L, int lane) { return __ballot((ld4(X, (unsigned)lane * 4u, CTAB_OFF + first * 4u) & 1u) != (unsigned)L); }
#define SEGB(m, s) ((unsigned)(SG0 + (s)) * XBLK + (unsigned)(((m) >> (s)) & 1ull) * 8192u)
__device__ __forceinline__ bool spin_fail(unsigned& spins, rsrc_t F) {
  ++spins;
  if (spins > SPIN_CAP) { unsigned one = 0x40000000u | (blockIdx.x << 8) | threadIdx.x; asm volatile("" : "+v"(one)); if (__builtin_amdgcn_readfirstlane(ld4(F, 0, TMO_OFF)) == 0u) st4(F, 0, TMO_OFF, one); return true; }
  if ((spins & 1023u) == 0u) return __builtin_amdgcn_readfirstlane(ld4(F, 0, TMO_OFF)) != 0;
  return false;
}
__device__ __forceinline__ unsigned rtc() { return (unsigned)__builtin_amdgcn_s_memrealtime(); }
#define BAR() do { asm volatile("s_waitcnt lgkmcnt(0)" ::: "memory"); __builtin_amdgcn_s_barrier(); asm volatile("" ::: "memory"); } while (0)
#define SB() __builtin_amdgcn_sched_barrier(0)
#define MF "v_mfma_f32_16x16x32_bf16 "
#define VM0() __builtin_amdgcn_s_waitcnt(0x0F70)
#define DL1 12
#define SCH_B0 48
#define DLL1 6
#define DLL2 0
#define DLF 24
#define SCH_PEN 0
#define DLX 4
#define LATE 2
#define TKL 28
#define UP 1
#define DN 100
#define FIN -3
#define CH_FIN CH_S(FIN)
#define DLT 16
#define DLY 20
#define CH_DLT CH_S(DLT)
#define CH_DLY CH_S(DLY)
#define ES 6
#define ED 4
#define EB 8
#define CH_ES CH_S(ES)
#define CH_EX "%[ex]"
#define DRYL 1
#define CH_DRYL CH_S(DRYL)
#define SMAP 0x1023u
#define RO1 4
#define CH_HNP
#define CH_RO1 CH_S(RO1)
#define RO3 2
#define CH_RO3 CH_S(RO3)
#define KD 300
#define KB 150
#define B3 0
#define CH_MS "0"
#define CH_KD CH_S(KD)
#define CH_KB CH_S(KB)
#define CH_B3 CH_S(B3)
#define RSL 2
#define CH_S2(x) #x
#define CH_S(x) CH_S2(x)
#define CH_ST "s_memtime s[62:63]\n\ts_waitcnt lgkmcnt(0)\n\ts_sub_u32 s67, s62, s67\n\t"
#define CH_BASE "s_memtime s[94:95]\n\ts_waitcnt lgkmcnt(0)\n\ts_mov_b32 s67, s94\n\t"
#define CH_DPC
#define CH_TOKCHK "s_cbranch_vccz LG3_%=\n\t"
#define CH_SEGC
#define CH_SG0 CH_S(SG0)
#define CH_ARMD "%[armd]"
#define CH_ARMS "%[arms]"
#define CH_SLOT "688128"
#define CH_SKIP "32768"
#define CH_DL1 CH_S(DL1)
#define CH_DLL1 CH_S(DLL1)
#define CH_DLL2 CH_S(DLL2)
#define CH_DLF CH_S(DLF)
#define CH_B0 CH_S(SCH_B0)
#define CH_PEN CH_S(SCH_PEN)
#define CH_RSL CH_S(RSL)
#define CH_UP CH_S(UP)
#define CH_DN CH_S(DN)
#define CH_DLX CH_S(DLX)
#define CH_LATE CH_S(LATE)
#define CH_DL1L "%[dl1l]"
#define CH_TKL CH_S(TKL)
#define CH_SCHW "656384"
#define CH_CAP "%[cap]"
#define CH_CTAB "%[ctab]"
#define CH_RING "%[ring]"
#define CH_TRM "%[trm]"
#define CH_COW
#define CH_INW "s_sub_u32 s93, s48, 10\n\ts_cmp_lt_u32 s93, 50\n\t"
#define CH_ST1
#define CH_ST2
#define FDL 6
#define CH_FDL CH_S(FDL)
#define CH_ST3
#define CH_ST4
#define CH_TRC
#define CH_TG2
#define CH_TG5
#define CH_STV
#define CH_STW
#define CH_DP0
#define CH_DP1
#define CH_DP2
#define CH_DP3
#define CH_DP4
#define CH_DP5
#define CH_DP6
#define CH_DP7
#define CH_DP8
#define CH_DP9
#define CH_DP10
#define CH_DP11
#define CH_DP12
#define CH_DP13
#define CH_DP14
#define CH_DP15
#define CH_DP16
#define CH_DP17
#define CH_OPS {64, 80, 128, 144, 1, 17, 65, 81, 129, 145, 2, 18, 82, 130, 146, 3, 67, 83, 131, 147, 4, 20, 68, 84, 132, 5, 69, 85, 133, 149, 6, 22, 70, 86, 134, 150, 7, 23, 71, 87, 135, 151, 8, 24, 88, 136, 195, 209, 210, 211, 227, 241, 242, 256, 0, 16, 19, 21, 66, 72, 148, 152, 208, 240, 192, 224, 193, 225, 194, 226, 243, 257}
#define CH_ASM \
  "s_mov_b32 s84, " CH_CAP "\n\t" \
  "s_mov_b32 s83, " CH_CAP "\n\t" \
  "s_mov_b32 s68, 0\n\t" \
  "s_mov_b32 s74, 0\n\t" \
  "s_mov_b32 s76, 0\n\t" \
  "s_mov_b32 s77, 0\n\t" \
  "s_mov_b32 s78, 0\n\t" \
  "s_mov_b32 s79, 0\n\t" \
  "s_mov_b32 s56, 0xcccccccc\n\t" \
  "s_mov_b32 s57, 0xcccccccc\n\t" \
  "s_mov_b32 s58, 0x33333333\n\t" \
  "s_mov_b32 s59, 0x33333333\n\t" \
  "s_mov_b32 s24, 0x01010101\n\t" \
  "s_mov_b32 s25, 0\n\t" \
  "s_mov_b32 s88, 0x3f803f80\n\t" \
  "s_mov_b32 s89, 0x3f803f80\n\t" \
  "s_mov_b32 s66, s65\n\t" \
  "s_mov_b32 s47, s65\n\t" \
  "s_mov_b32 s72, 0\n\t" \
  "s_mov_b32 s73, 0\n\t" \
  "s_mov_b32 s67, 0\n\t" \
  "s_mul_i32 s60, s40, 36864\n\t" \
  "s_add_u32 s60, s60, 16384\n\t" \
  "v_add_u32 v70, s60, v69\n\t" \
  "v_bfe_i32 v68, v69, 5, 1\n\t" \
  "v_and_b32 v68, 0xaaaaaaaa, v68\n\t" \
  "v_or_b32 v68, 0x44444444, v68\n\t" \
  "v_and_b32 v73, 32, v69\n\t" \
  "v_lshl_add_u32 v73, v73, 5, v69\n\t" \
  "LLOAD_%=:\n\t" \
  "v_lshrrev_b32 v74, 2, v69\n\t" \
  "s_mov_b32 s60, " CH_CTAB "\n\t" \
  "buffer_load_dword v72, v74, s[28:31], s60 offen offset:88 sc1\n\t" \
  "buffer_load_dword v74, v74, s[28:31], s60 offen sc1\n\t" \
  "v_lshrrev_b32 v71, 8, v69\n\t" \
  "v_lshlrev_b32 v1, 6, v71\n\t" \
  "v_and_b32 v2, 0xf0, v69\n\t" \
  "v_xor_b32 v1, v1, v2\n\t" \
  "v_lshl_add_u32 v71, v71, 13, v1\n\t" \
  "s_lshl_b32 s60, s40, 17\n\t" \
  "v_add_u32 v71, s60, v71\n\t" \
  "v_lshrrev_b32 v1, 7, v69\n\t" \
  "v_and_b32 v2, 0x70, v69\n\t" \
  "s_movk_i32 s60, 1152\n\t" \
  "v_mad_u32_u24 v75, v1, s60, v2\n\t" \
  "s_lshl_b32 s54, s40, 12\n\t" \
  "s_lshl_b32 s55, s40, 7\n\t" \
  "s_mul_i32 s75, s40, 36864\n\t" \
  "s_add_u32 s75, s75, 1024\n\t" \
  "s_mov_b32 s62, 0x05040100\n\t" \
  "s_mov_b32 s63, 0x07060302\n\t" \
  "s_barrier\n\t" \
  "s_xor_b32 s61, s40, 0\n\t" \
  "s_mul_i32 s61, s61, 36864\n\t" \
  "s_add_u32 s61, s61, s55\n\t" \
  "s_add_u32 m0, s54, 0\n\t" \
  "s_add_u32 s60, s61, 0\n\t" \
  "buffer_load_dwordx4 v75, %[Wo], s60 offen lds\n\t" \
  "s_add_u32 m0, s54, 1024\n\t" \
  "s_add_u32 s60, s61, 9216\n\t" \
  "buffer_load_dwordx4 v75, %[Wo], s60 offen lds\n\t" \
  "s_add_u32 m0, s54, 2048\n\t" \
  "s_add_u32 s60, s61, 18432\n\t" \
  "buffer_load_dwordx4 v75, %[Wo], s60 offen lds\n\t" \
  "s_add_u32 m0, s54, 3072\n\t" \
  "s_add_u32 s60, s61, 27648\n\t" \
  "buffer_load_dwordx4 v75, %[Wo], s60 offen lds\n\t" \
  "s_add_u32 m0, s54, 16384\n\t" \
  "s_mov_b32 s60, 0\n\t" \
  "buffer_load_dwordx4 v71, %[Wq], s60 offen lds\n\t" \
  "s_add_u32 m0, s54, 17408\n\t" \
  "s_mov_b32 s60, 32768\n\t" \
  "buffer_load_dwordx4 v71, %[Wq], s60 offen lds\n\t" \
  "s_add_u32 m0, s54, 18432\n\t" \
  "s_mov_b32 s60, 65536\n\t" \
  "buffer_load_dwordx4 v71, %[Wq], s60 offen lds\n\t" \
  "s_add_u32 m0, s54, 19456\n\t" \
  "s_mov_b32 s60, 98304\n\t" \
  "buffer_load_dwordx4 v71, %[Wq], s60 offen lds\n\t" \
  "s_add_u32 m0, s54, 53248\n\t" \
  "s_mov_b32 s60, 0\n\t" \
  "buffer_load_dwordx4 v71, %[Wk], s60 offen lds\n\t" \
  "s_add_u32 m0, s54, 54272\n\t" \
  "s_mov_b32 s60, 32768\n\t" \
  "buffer_load_dwordx4 v71, %[Wk], s60 offen lds\n\t" \
  "s_add_u32 m0, s54, 55296\n\t" \
  "s_mov_b32 s60, 65536\n\t" \
  "buffer_load_dwordx4 v71, %[Wk], s60 offen lds\n\t" \
  "s_add_u32 m0, s54, 56320\n\t" \
  "s_mov_b32 s60, 98304\n\t" \
  "buffer_load_dwordx4 v71, %[Wk], s60 offen lds\n\t" \
  "s_xor_b32 s61, s40, 0\n\t" \
  "s_mul_i32 s61, s61, 36864\n\t" \
  "s_add_u32 s61, s61, s55\n\t" \
  "s_add_u32 m0, s54, 90112\n\t" \
  "s_add_u32 s60, s61, 512\n\t" \
  "buffer_load_dwordx4 v75, %[Wo], s60 offen lds\n\t" \
  "s_add_u32 m0, s54, 91136\n\t" \
  "s_add_u32 s60, s61, 9728\n\t" \
  "buffer_load_dwordx4 v75, %[Wo], s60 offen lds\n\t" \
  "s_add_u32 m0, s54, 92160\n\t" \
  "s_add_u32 s60, s61, 18944\n\t" \
  "buffer_load_dwordx4 v75, %[Wo], s60 offen lds\n\t" \
  "s_add_u32 m0, s54, 93184\n\t" \
  "s_add_u32 s60, s61, 28160\n\t" \
  "buffer_load_dwordx4 v75, %[Wo], s60 offen lds\n\t" \
  "s_add_u32 m0, s54, 126976\n\t" \
  "s_mov_b32 s60, 0\n\t" \
  "buffer_load_dwordx4 v71, %[Wv], s60 offen lds\n\t" \
  "s_add_u32 m0, s54, 128000\n\t" \
  "s_mov_b32 s60, 32768\n\t" \
  "buffer_load_dwordx4 v71, %[Wv], s60 offen lds\n\t" \
  "s_add_u32 m0, s54, 129024\n\t" \
  "s_mov_b32 s60, 65536\n\t" \
  "buffer_load_dwordx4 v71, %[Wv], s60 offen lds\n\t" \
  "s_add_u32 m0, s54, 130048\n\t" \
  "s_mov_b32 s60, 98304\n\t" \
  "buffer_load_dwordx4 v71, %[Wv], s60 offen lds\n\t" \
  "v_and_b32 v1, 0x300, v69\n\t" \
  "v_lshlrev_b32 v65, 3, v1\n\t" \
  "v_and_b32 v1, 0xc0, v69\n\t" \
  "v_lshl_add_u32 v65, v1, 2, v65\n\t" \
  "s_lshl_b32 s60, s40, 6\n\t" \
  "v_xor_b32 v1, s60, v1\n\t" \
  "v_and_b32 v2, 48, v69\n\t" \
  "v_add3_u32 v65, v65, v1, v2\n\t" \
  "v_lshrrev_b32 v2, 1, v2\n\t" \
  "v_sub_u32 v66, v65, v2\n\t" \
  "v_bfe_u32 v0, v69, 8, 1\n\t" \
  "v_bfe_u32 v1, v69, 9, 1\n\t" \
  "v_lshlrev_b32 v0, 2, v0\n\t" \
  "v_add_u32 v0, v0, v1\n\t" \
  "v_lshlrev_b32 v0, 9, v0\n\t" \
  "v_lshrrev_b32 v1, 1, v69\n\t" \
  "v_and_b32 v1, 0x60, v1\n\t" \
  "v_and_b32 v2, 16, v69\n\t" \
  "v_bfe_u32 v3, v69, 5, 1\n\t" \
  "v_add3_u32 v64, v0, v1, v2\n\t" \
  "v_lshl_add_u32 v64, v3, 2, v64\n\t" \
  "v_add_u32 v64, s54, v64\n\t" \
  "v_lshl_add_u32 v67, v3, 3, v0\n\t" \
  "v_bfe_u32 v1, v69, 7, 1\n\t" \
  "v_lshl_add_u32 v67, v1, 2, v67\n\t" \
  "v_bfe_u32 v1, v69, 6, 1\n\t" \
  "v_lshl_add_u32 v67, v1, 4, v67\n\t" \
  "v_bfe_u32 v1, v69, 4, 1\n\t" \
  "v_lshl_add_u32 v67, v1, 1, v67\n\t" \
  "s_lshl_b32 s60, s40, 5\n\t" \
  "v_add_u32 v67, s60, v67\n\t" \
  "v_add_u32 v78, 0, v64\n\t" \
  "v_add_u32 v79, 1024, v64\n\t" \
  "s_waitcnt vmcnt(16) lgkmcnt(0)\n\t" \
  "s_barrier\n\t" \
  "ds_read2_b32 v[80:81], v78 offset0:0 offset1:2\n\t" \
  "ds_read2_b32 v[82:83], v78 offset0:32 offset1:34\n\t" \
  "ds_read2_b32 v[84:85], v78 offset0:64 offset1:66\n\t" \
  "ds_read2_b32 v[86:87], v78 offset0:96 offset1:98\n\t" \
  "ds_read2_b32 v[88:89], v79 offset0:0 offset1:2\n\t" \
  "ds_read2_b32 v[90:91], v79 offset0:32 offset1:34\n\t" \
  "ds_read2_b32 v[92:93], v79 offset0:64 offset1:66\n\t" \
  "ds_read2_b32 v[94:95], v79 offset0:96 offset1:98\n\t" \
  "s_waitcnt lgkmcnt(6)\n\t" \
  "v_perm_b32 v112, v82, v80, s62\n\t" \
  "v_perm_b32 v113, v82, v80, s63\n\t" \
  "s_waitcnt lgkmcnt(4)\n\t" \
  "v_perm_b32 v114, v86, v84, s62\n\t" \
  "v_perm_b32 v115, v86, v84, s63\n\t" \
  "s_waitcnt lgkmcnt(2)\n\t" \
  "v_perm_b32 v116, v90, v88, s62\n\t" \
  "v_perm_b32 v117, v90, v88, s63\n\t" \
  "s_waitcnt lgkmcnt(0)\n\t" \
  "v_perm_b32 v118, v94, v92, s62\n\t" \
  "v_perm_b32 v119, v94, v92, s63\n\t" \
  "ds_write_b128 v70, v[112:115] offset:20480\n\t" \
  "ds_write_b128 v70, v[116:119] offset:21504\n\t" \
  "v_perm_b32 v120, v83, v81, s62\n\t" \
  "v_perm_b32 v121, v83, v81, s63\n\t" \
  "v_perm_b32 v122, v87, v85, s62\n\t" \
  "v_perm_b32 v123, v87, v85, s63\n\t" \
  "v_perm_b32 v124, v91, v89, s62\n\t" \
  "v_perm_b32 v125, v91, v89, s63\n\t" \
  "v_perm_b32 v126, v95, v93, s62\n\t" \
  "v_perm_b32 v127, v95, v93, s63\n\t" \
  "ds_write_b128 v70, v[120:123] offset:16384\n\t" \
  "ds_write_b128 v70, v[124:127] offset:17408\n\t" \
  "v_add_u32 v78, 16384, v65\n\t" \
  "s_waitcnt vmcnt(12) lgkmcnt(0)\n\t" \
  "s_barrier\n\t" \
  "ds_read_b64_tr_b16 v[0:1], v78 offset:0\n\t" \
  "ds_read_b64_tr_b16 v[2:3], v78 offset:1024\n\t" \
  "ds_read_b64_tr_b16 v[4:5], v78 offset:8192\n\t" \
  "ds_read_b64_tr_b16 v[6:7], v78 offset:9216\n\t" \
  "ds_read_b64_tr_b16 v[8:9], v78 offset:8\n\t" \
  "ds_read_b64_tr_b16 v[10:11], v78 offset:1032\n\t" \
  "ds_read_b64_tr_b16 v[12:13], v78 offset:8200\n\t" \
  "ds_read_b64_tr_b16 v[14:15], v78 offset:9224\n\t" \
  "s_add_u32 m0, s54, 0\n\t" \
  "s_mov_b32 s60, 524288\n\t" \
  "buffer_load_dwordx4 v71, %[Wq], s60 offen lds\n\t" \
  "s_add_u32 m0, s54, 1024\n\t" \
  "s_mov_b32 s60, 557056\n\t" \
  "buffer_load_dwordx4 v71, %[Wq], s60 offen lds\n\t" \
  "s_add_u32 m0, s54, 2048\n\t" \
  "s_mov_b32 s60, 589824\n\t" \
  "buffer_load_dwordx4 v71, %[Wq], s60 offen lds\n\t" \
  "s_add_u32 m0, s54, 3072\n\t" \
  "s_mov_b32 s60, 622592\n\t" \
  "buffer_load_dwordx4 v71, %[Wq], s60 offen lds\n\t" \
  "v_add_u32 v78, 53248, v65\n\t" \
  "s_waitcnt vmcnt(12) lgkmcnt(0)\n\t" \
  "s_barrier\n\t" \
  "ds_read_b64_tr_b16 a[0:1], v78 offset:0\n\t" \
  "ds_read_b64_tr_b16 a[2:3], v78 offset:1024\n\t" \
  "ds_read_b64_tr_b16 a[4:5], v78 offset:8192\n\t" \
  "ds_read_b64_tr_b16 a[6:7], v78 offset:9216\n\t" \
  "ds_read_b64_tr_b16 a[8:9], v78 offset:8\n\t" \
  "ds_read_b64_tr_b16 a[10:11], v78 offset:1032\n\t" \
  "ds_read_b64_tr_b16 a[12:13], v78 offset:8200\n\t" \
  "ds_read_b64_tr_b16 a[14:15], v78 offset:9224\n\t" \
  "s_xor_b32 s61, s40, 1\n\t" \
  "s_mul_i32 s61, s61, 36864\n\t" \
  "s_add_u32 s61, s61, s55\n\t" \
  "s_add_u32 m0, s54, 16384\n\t" \
  "s_add_u32 s60, s61, 0\n\t" \
  "buffer_load_dwordx4 v75, %[Wo], s60 offen lds\n\t" \
  "s_add_u32 m0, s54, 17408\n\t" \
  "s_add_u32 s60, s61, 9216\n\t" \
  "buffer_load_dwordx4 v75, %[Wo], s60 offen lds\n\t" \
  "s_add_u32 m0, s54, 18432\n\t" \
  "s_add_u32 s60, s61, 18432\n\t" \
  "buffer_load_dwordx4 v75, %[Wo], s60 offen lds\n\t" \
  "s_add_u32 m0, s54, 19456\n\t" \
  "s_add_u32 s60, s61, 27648\n\t" \
  "buffer_load_dwordx4 v75, %[Wo], s60 offen lds\n\t" \
  "v_add_u32 v78, 90112, v64\n\t" \
  "v_add_u32 v79, 91136, v64\n\t" \
  "s_waitcnt vmcnt(12) lgkmcnt(0)\n\t" \
  "s_barrier\n\t" \
  "ds_read2_b32 v[80:81], v78 offset0:0 offset1:2\n\t" \
  "ds_read2_b32 v[82:83], v78 offset0:32 offset1:34\n\t" \
  "ds_read2_b32 v[84:85], v78 offset0:64 offset1:66\n\t" \
  "ds_read2_b32 v[86:87], v78 offset0:96 offset1:98\n\t" \
  "ds_read2_b32 v[88:89], v79 offset0:0 offset1:2\n\t" \
  "ds_read2_b32 v[90:91], v79 offset0:32 offset1:34\n\t" \
  "ds_read2_b32 v[92:93], v79 offset0:64 offset1:66\n\t" \
  "ds_read2_b32 v[94:95], v79 offset0:96 offset1:98\n\t" \
  "s_add_u32 m0, s54, 53248\n\t" \
  "s_mov_b32 s60, 524288\n\t" \
  "buffer_load_dwordx4 v71, %[Wk], s60 offen lds\n\t" \
  "s_add_u32 m0, s54, 54272\n\t" \
  "s_mov_b32 s60, 557056\n\t" \
  "buffer_load_dwordx4 v71, %[Wk], s60 offen lds\n\t" \
  "s_add_u32 m0, s54, 55296\n\t" \
  "s_mov_b32 s60, 589824\n\t" \
  "buffer_load_dwordx4 v71, %[Wk], s60 offen lds\n\t" \
  "s_add_u32 m0, s54, 56320\n\t" \
  "s_mov_b32 s60, 622592\n\t" \
  "buffer_load_dwordx4 v71, %[Wk], s60 offen lds\n\t" \
  "s_waitcnt lgkmcnt(6)\n\t" \
  "v_perm_b32 v112, v82, v80, s62\n\t" \
  "v_perm_b32 v113, v82, v80, s63\n\t" \
  "s_waitcnt lgkmcnt(4)\n\t" \
  "v_perm_b32 v114, v86, v84, s62\n\t" \
  "v_perm_b32 v115, v86, v84, s63\n\t" \
  "s_waitcnt lgkmcnt(2)\n\t" \
  "v_perm_b32 v116, v90, v88, s62\n\t" \
  "v_perm_b32 v117, v90, v88, s63\n\t" \
  "s_waitcnt lgkmcnt(0)\n\t" \
  "v_perm_b32 v118, v94, v92, s62\n\t" \
  "v_perm_b32 v119, v94, v92, s63\n\t" \
  "ds_write_b128 v70, v[112:115] offset:22528\n\t" \
  "ds_write_b128 v70, v[116:119] offset:23552\n\t" \
  "v_perm_b32 v120, v83, v81, s62\n\t" \
  "v_perm_b32 v121, v83, v81, s63\n\t" \
  "v_perm_b32 v122, v87, v85, s62\n\t" \
  "v_perm_b32 v123, v87, v85, s63\n\t" \
  "v_perm_b32 v124, v91, v89, s62\n\t" \
  "v_perm_b32 v125, v91, v89, s63\n\t" \
  "v_perm_b32 v126, v95, v93, s62\n\t" \
  "v_perm_b32 v127, v95, v93, s63\n\t" \
  "ds_write_b128 v70, v[120:123] offset:18432\n\t" \
  "ds_write_b128 v70, v[124:127] offset:19456\n\t" \
  "v_add_u32 v78, 126976, v66\n\t" \
  "s_waitcnt vmcnt(12) lgkmcnt(0)\n\t" \
  "s_barrier\n\t" \
  "ds_read_b64_tr_b16 a[16:17], v78 offset:0\n\t" \
  "ds_read_b64_tr_b16 a[18:19], v78 offset:1024\n\t" \
  "ds_read_b64_tr_b16 a[20:21], v78 offset:8192\n\t" \
  "ds_read_b64_tr_b16 a[22:23], v78 offset:9216\n\t" \
  "ds_read_b64_tr_b16 a[24:25], v78 offset:32\n\t" \
  "ds_read_b64_tr_b16 a[26:27], v78 offset:1056\n\t" \
  "ds_read_b64_tr_b16 a[28:29], v78 offset:8224\n\t" \
  "ds_read_b64_tr_b16 a[30:31], v78 offset:9248\n\t" \
  "s_add_u32 m0, s54, 90112\n\t" \
  "s_mov_b32 s60, 524288\n\t" \
  "buffer_load_dwordx4 v71, %[Wv], s60 offen lds\n\t" \
  "s_add_u32 m0, s54, 91136\n\t" \
  "s_mov_b32 s60, 557056\n\t" \
  "buffer_load_dwordx4 v71, %[Wv], s60 offen lds\n\t" \
  "s_add_u32 m0, s54, 92160\n\t" \
  "s_mov_b32 s60, 589824\n\t" \
  "buffer_load_dwordx4 v71, %[Wv], s60 offen lds\n\t" \
  "s_add_u32 m0, s54, 93184\n\t" \
  "s_mov_b32 s60, 622592\n\t" \
  "buffer_load_dwordx4 v71, %[Wv], s60 offen lds\n\t" \
  "v_add_u32 v78, 0, v65\n\t" \
  "s_waitcnt vmcnt(12) lgkmcnt(0)\n\t" \
  "s_barrier\n\t" \
  "ds_read_b64_tr_b16 a[32:33], v78 offset:0\n\t" \
  "ds_read_b64_tr_b16 a[34:35], v78 offset:1024\n\t" \
  "ds_read_b64_tr_b16 a[36:37], v78 offset:8192\n\t" \
  "ds_read_b64_tr_b16 a[38:39], v78 offset:9216\n\t" \
  "ds_read_b64_tr_b16 a[40:41], v78 offset:8\n\t" \
  "ds_read_b64_tr_b16 a[42:43], v78 offset:1032\n\t" \
  "ds_read_b64_tr_b16 a[44:45], v78 offset:8200\n\t" \
  "ds_read_b64_tr_b16 a[46:47], v78 offset:9224\n\t" \
  "s_xor_b32 s61, s40, 1\n\t" \
  "s_mul_i32 s61, s61, 36864\n\t" \
  "s_add_u32 s61, s61, s55\n\t" \
  "s_add_u32 m0, s54, 126976\n\t" \
  "s_add_u32 s60, s61, 512\n\t" \
  "buffer_load_dwordx4 v75, %[Wo], s60 offen lds\n\t" \
  "s_add_u32 m0, s54, 128000\n\t" \
  "s_add_u32 s60, s61, 9728\n\t" \
  "buffer_load_dwordx4 v75, %[Wo], s60 offen lds\n\t" \
  "s_add_u32 m0, s54, 129024\n\t" \
  "s_add_u32 s60, s61, 18944\n\t" \
  "buffer_load_dwordx4 v75, %[Wo], s60 offen lds\n\t" \
  "s_add_u32 m0, s54, 130048\n\t" \
  "s_add_u32 s60, s61, 28160\n\t" \
  "buffer_load_dwordx4 v75, %[Wo], s60 offen lds\n\t" \
  "v_add_u32 v78, 16384, v64\n\t" \
  "v_add_u32 v79, 17408, v64\n\t" \
  "s_waitcnt vmcnt(12) lgkmcnt(0)\n\t" \
  "s_barrier\n\t" \
  "ds_read2_b32 v[80:81], v78 offset0:0 offset1:2\n\t" \
  "ds_read2_b32 v[82:83], v78 offset0:32 offset1:34\n\t" \
  "ds_read2_b32 v[84:85], v78 offset0:64 offset1:66\n\t" \
  "ds_read2_b32 v[86:87], v78 offset0:96 offset1:98\n\t" \
  "ds_read2_b32 v[88:89], v79 offset0:0 offset1:2\n\t" \
  "ds_read2_b32 v[90:91], v79 offset0:32 offset1:34\n\t" \
  "ds_read2_b32 v[92:93], v79 offset0:64 offset1:66\n\t" \
  "ds_read2_b32 v[94:95], v79 offset0:96 offset1:98\n\t" \
  "s_add_u32 m0, s54, 0\n\t" \
  "s_mov_b32 s60, 1048576\n\t" \
  "buffer_load_dwordx4 v71, %[Wq], s60 offen lds\n\t" \
  "s_add_u32 m0, s54, 1024\n\t" \
  "s_mov_b32 s60, 1081344\n\t" \
  "buffer_load_dwordx4 v71, %[Wq], s60 offen lds\n\t" \
  "s_add_u32 m0, s54, 2048\n\t" \
  "s_mov_b32 s60, 1114112\n\t" \
  "buffer_load_dwordx4 v71, %[Wq], s60 offen lds\n\t" \
  "s_add_u32 m0, s54, 3072\n\t" \
  "s_mov_b32 s60, 1146880\n\t" \
  "buffer_load_dwordx4 v71, %[Wq], s60 offen lds\n\t" \
  "s_waitcnt lgkmcnt(6)\n\t" \
  "v_perm_b32 v112, v82, v80, s62\n\t" \
  "v_perm_b32 v113, v82, v80, s63\n\t" \
  "s_waitcnt lgkmcnt(4)\n\t" \
  "v_perm_b32 v114, v86, v84, s62\n\t" \
  "v_perm_b32 v115, v86, v84, s63\n\t" \
  "s_waitcnt lgkmcnt(2)\n\t" \
  "v_perm_b32 v116, v90, v88, s62\n\t" \
  "v_perm_b32 v117, v90, v88, s63\n\t" \
  "s_waitcnt lgkmcnt(0)\n\t" \
  "v_perm_b32 v118, v94, v92, s62\n\t" \
  "v_perm_b32 v119, v94, v92, s63\n\t" \
  "ds_write_b128 v70, v[112:115] offset:24576\n\t" \
  "ds_write_b128 v70, v[116:119] offset:25600\n\t" \
  "v_perm_b32 v200, v83, v81, s62\n\t" \
  "v_perm_b32 v201, v83, v81, s63\n\t" \
  "v_perm_b32 v202, v87, v85, s62\n\t" \
  "v_perm_b32 v203, v87, v85, s63\n\t" \
  "v_perm_b32 v204, v91, v89, s62\n\t" \
  "v_perm_b32 v205, v91, v89, s63\n\t" \
  "v_perm_b32 v206, v95, v93, s62\n\t" \
  "v_perm_b32 v207, v95, v93, s63\n\t" \
  "v_add_u32 v78, 53248, v65\n\t" \
  "s_waitcnt vmcnt(12) lgkmcnt(0)\n\t" \
  "s_barrier\n\t" \
  "ds_read_b64_tr_b16 a[48:49], v78 offset:0\n\t" \
  "ds_read_b64_tr_b16 a[50:51], v78 offset:1024\n\t" \
  "ds_read_b64_tr_b16 a[52:53], v78 offset:8192\n\t" \
  "ds_read_b64_tr_b16 a[54:55], v78 offset:9216\n\t" \
  "ds_read_b64_tr_b16 a[56:57], v78 offset:8\n\t" \
  "ds_read_b64_tr_b16 a[58:59], v78 offset:1032\n\t" \
  "ds_read_b64_tr_b16 a[60:61], v78 offset:8200\n\t" \
  "ds_read_b64_tr_b16 a[62:63], v78 offset:9224\n\t" \
  "s_add_u32 m0, s54, 16384\n\t" \
  "s_mov_b32 s60, 1048576\n\t" \
  "buffer_load_dwordx4 v71, %[Wk], s60 offen lds\n\t" \
  "s_add_u32 m0, s54, 17408\n\t" \
  "s_mov_b32 s60, 1081344\n\t" \
  "buffer_load_dwordx4 v71, %[Wk], s60 offen lds\n\t" \
  "s_add_u32 m0, s54, 18432\n\t" \
  "s_mov_b32 s60, 1114112\n\t" \
  "buffer_load_dwordx4 v71, %[Wk], s60 offen lds\n\t" \
  "s_add_u32 m0, s54, 19456\n\t" \
  "s_mov_b32 s60, 1146880\n\t" \
  "buffer_load_dwordx4 v71, %[Wk], s60 offen lds\n\t" \
  "v_add_u32 v78, 90112, v66\n\t" \
  "s_waitcnt vmcnt(12) lgkmcnt(0)\n\t" \
  "s_barrier\n\t" \
  "ds_read_b64_tr_b16 a[64:65], v78 offset:0\n\t" \
  "ds_read_b64_tr_b16 a[66:67], v78 offset:1024\n\t" \
  "ds_read_b64_tr_b16 a[68:69], v78 offset:8192\n\t" \
  "ds_read_b64_tr_b16 a[70:71], v78 offset:9216\n\t" \
  "ds_read_b64_tr_b16 a[72:73], v78 offset:32\n\t" \
  "ds_read_b64_tr_b16 a[74:75], v78 offset:1056\n\t" \
  "ds_read_b64_tr_b16 a[76:77], v78 offset:8224\n\t" \
  "ds_read_b64_tr_b16 a[78:79], v78 offset:9248\n\t" \
  "s_xor_b32 s61, s40, 2\n\t" \
  "s_mul_i32 s61, s61, 36864\n\t" \
  "s_add_u32 s61, s61, s55\n\t" \
  "s_add_u32 m0, s54, 53248\n\t" \
  "s_add_u32 s60, s61, 0\n\t" \
  "buffer_load_dwordx4 v75, %[Wo], s60 offen lds\n\t" \
  "s_add_u32 m0, s54, 54272\n\t" \
  "s_add_u32 s60, s61, 9216\n\t" \
  "buffer_load_dwordx4 v75, %[Wo], s60 offen lds\n\t" \
  "s_add_u32 m0, s54, 55296\n\t" \
  "s_add_u32 s60, s61, 18432\n\t" \
  "buffer_load_dwordx4 v75, %[Wo], s60 offen lds\n\t" \
  "s_add_u32 m0, s54, 56320\n\t" \
  "s_add_u32 s60, s61, 27648\n\t" \
  "buffer_load_dwordx4 v75, %[Wo], s60 offen lds\n\t" \
  "v_add_u32 v78, 126976, v64\n\t" \
  "v_add_u32 v79, 128000, v64\n\t" \
  "s_waitcnt vmcnt(12) lgkmcnt(0)\n\t" \
  "s_barrier\n\t" \
  "ds_read2_b32 v[80:81], v78 offset0:0 offset1:2\n\t" \
  "ds_read2_b32 v[82:83], v78 offset0:32 offset1:34\n\t" \
  "ds_read2_b32 v[84:85], v78 offset0:64 offset1:66\n\t" \
  "ds_read2_b32 v[86:87], v78 offset0:96 offset1:98\n\t" \
  "ds_read2_b32 v[88:89], v79 offset0:0 offset1:2\n\t" \
  "ds_read2_b32 v[90:91], v79 offset0:32 offset1:34\n\t" \
  "ds_read2_b32 v[92:93], v79 offset0:64 offset1:66\n\t" \
  "ds_read2_b32 v[94:95], v79 offset0:96 offset1:98\n\t" \
  "s_add_u32 m0, s54, 90112\n\t" \
  "s_mov_b32 s60, 1048576\n\t" \
  "buffer_load_dwordx4 v71, %[Wv], s60 offen lds\n\t" \
  "s_add_u32 m0, s54, 91136\n\t" \
  "s_mov_b32 s60, 1081344\n\t" \
  "buffer_load_dwordx4 v71, %[Wv], s60 offen lds\n\t" \
  "s_add_u32 m0, s54, 92160\n\t" \
  "s_mov_b32 s60, 1114112\n\t" \
  "buffer_load_dwordx4 v71, %[Wv], s60 offen lds\n\t" \
  "s_add_u32 m0, s54, 93184\n\t" \
  "s_mov_b32 s60, 1146880\n\t" \
  "buffer_load_dwordx4 v71, %[Wv], s60 offen lds\n\t" \
  "s_waitcnt lgkmcnt(6)\n\t" \
  "v_perm_b32 v112, v82, v80, s62\n\t" \
  "v_perm_b32 v113, v82, v80, s63\n\t" \
  "s_waitcnt lgkmcnt(4)\n\t" \
  "v_perm_b32 v114, v86, v84, s62\n\t" \
  "v_perm_b32 v115, v86, v84, s63\n\t" \
  "s_waitcnt lgkmcnt(2)\n\t" \
  "v_perm_b32 v116, v90, v88, s62\n\t" \
  "v_perm_b32 v117, v90, v88, s63\n\t" \
  "s_waitcnt lgkmcnt(0)\n\t" \
  "v_perm_b32 v118, v94, v92, s62\n\t" \
  "v_perm_b32 v119, v94, v92, s63\n\t" \
  "ds_write_b128 v70, v[112:115] offset:26624\n\t" \
  "ds_write_b128 v70, v[116:119] offset:27648\n\t" \
  "v_perm_b32 v232, v83, v81, s62\n\t" \
  "v_perm_b32 v233, v83, v81, s63\n\t" \
  "v_perm_b32 v234, v87, v85, s62\n\t" \
  "v_perm_b32 v235, v87, v85, s63\n\t" \
  "v_perm_b32 v236, v91, v89, s62\n\t" \
  "v_perm_b32 v237, v91, v89, s63\n\t" \
  "v_perm_b32 v238, v95, v93, s62\n\t" \
  "v_perm_b32 v239, v95, v93, s63\n\t" \
  "v_add_u32 v78, 0, v65\n\t" \
  "s_waitcnt vmcnt(12) lgkmcnt(0)\n\t" \
  "s_barrier\n\t" \
  "ds_read_b64_tr_b16 a[80:81], v78 offset:0\n\t" \
  "ds_read_b64_tr_b16 a[82:83], v78 offset:1024\n\t" \
  "ds_read_b64_tr_b16 a[84:85], v78 offset:8192\n\t" \
  "ds_read_b64_tr_b16 a[86:87], v78 offset:9216\n\t" \
  "ds_read_b64_tr_b16 a[88:89], v78 offset:8\n\t" \
  "ds_read_b64_tr_b16 a[90:91], v78 offset:1032\n\t" \
  "ds_read_b64_tr_b16 a[92:93], v78 offset:8200\n\t" \
  "ds_read_b64_tr_b16 a[94:95], v78 offset:9224\n\t" \
  "s_add_u32 m0, s54, 126976\n\t" \
  "s_mov_b32 s60, 1572864\n\t" \
  "buffer_load_dwordx4 v71, %[Wq], s60 offen lds\n\t" \
  "s_add_u32 m0, s54, 128000\n\t" \
  "s_mov_b32 s60, 1605632\n\t" \
  "buffer_load_dwordx4 v71, %[Wq], s60 offen lds\n\t" \
  "s_add_u32 m0, s54, 129024\n\t" \
  "s_mov_b32 s60, 1638400\n\t" \
  "buffer_load_dwordx4 v71, %[Wq], s60 offen lds\n\t" \
  "s_add_u32 m0, s54, 130048\n\t" \
  "s_mov_b32 s60, 1671168\n\t" \
  "buffer_load_dwordx4 v71, %[Wq], s60 offen lds\n\t" \
  "v_add_u32 v78, 16384, v65\n\t" \
  "s_waitcnt vmcnt(12) lgkmcnt(0)\n\t" \
  "s_barrier\n\t" \
  "ds_read_b64_tr_b16 v[32:33], v78 offset:0\n\t" \
  "ds_read_b64_tr_b16 v[34:35], v78 offset:1024\n\t" \
  "ds_read_b64_tr_b16 v[36:37], v78 offset:8192\n\t" \
  "ds_read_b64_tr_b16 v[38:39], v78 offset:9216\n\t" \
  "ds_read_b64_tr_b16 a[96:97], v78 offset:8\n\t" \
  "ds_read_b64_tr_b16 a[98:99], v78 offset:1032\n\t" \
  "ds_read_b64_tr_b16 a[100:101], v78 offset:8200\n\t" \
  "ds_read_b64_tr_b16 a[102:103], v78 offset:9224\n\t" \
  "s_xor_b32 s61, s40, 2\n\t" \
  "s_mul_i32 s61, s61, 36864\n\t" \
  "s_add_u32 s61, s61, s55\n\t" \
  "s_add_u32 m0, s54, 0\n\t" \
  "s_add_u32 s60, s61, 512\n\t" \
  "buffer_load_dwordx4 v75, %[Wo], s60 offen lds\n\t" \
  "s_add_u32 m0, s54, 1024\n\t" \
  "s_add_u32 s60, s61, 9728\n\t" \
  "buffer_load_dwordx4 v75, %[Wo], s60 offen lds\n\t" \
  "s_add_u32 m0, s54, 2048\n\t" \
  "s_add_u32 s60, s61, 18944\n\t" \
  "buffer_load_dwordx4 v75, %[Wo], s60 offen lds\n\t" \
  "s_add_u32 m0, s54, 3072\n\t" \
  "s_add_u32 s60, s61, 28160\n\t" \
  "buffer_load_dwordx4 v75, %[Wo], s60 offen lds\n\t" \
  "v_add_u32 v78, 53248, v64\n\t" \
  "v_add_u32 v79, 54272, v64\n\t" \
  "s_waitcnt vmcnt(12) lgkmcnt(0)\n\t" \
  "s_barrier\n\t" \
  "ds_read2_b32 v[80:81], v78 offset0:0 offset1:2\n\t" \
  "ds_read2_b32 v[82:83], v78 offset0:32 offset1:34\n\t" \
  "ds_read2_b32 v[84:85], v78 offset0:64 offset1:66\n\t" \
  "ds_read2_b32 v[86:87], v78 offset0:96 offset1:98\n\t" \
  "ds_read2_b32 v[88:89], v79 offset0:0 offset1:2\n\t" \
  "ds_read2_b32 v[90:91], v79 offset0:32 offset1:34\n\t" \
  "ds_read2_b32 v[92:93], v79 offset0:64 offset1:66\n\t" \
  "ds_read2_b32 v[94:95], v79 offset0:96 offset1:98\n\t" \
  "s_add_u32 m0, s54, 16384\n\t" \
  "s_mov_b32 s60, 1572864\n\t" \
  "buffer_load_dwordx4 v71, %[Wk], s60 offen lds\n\t" \
  "s_add_u32 m0, s54, 17408\n\t" \
  "s_mov_b32 s60, 1605632\n\t" \
  "buffer_load_dwordx4 v71, %[Wk], s60 offen lds\n\t" \
  "s_add_u32 m0, s54, 18432\n\t" \
  "s_mov_b32 s60, 1638400\n\t" \
  "buffer_load_dwordx4 v71, %[Wk], s60 offen lds\n\t" \
  "s_add_u32 m0, s54, 19456\n\t" \
  "s_mov_b32 s60, 1671168\n\t" \
  "buffer_load_dwordx4 v71, %[Wk], s60 offen lds\n\t" \
  "s_waitcnt lgkmcnt(6)\n\t" \
  "v_perm_b32 v112, v82, v80, s62\n\t" \
  "v_perm_b32 v113, v82, v80, s63\n\t" \
  "s_waitcnt lgkmcnt(4)\n\t" \
  "v_perm_b32 v114, v86, v84, s62\n\t" \
  "v_perm_b32 v115, v86, v84, s63\n\t" \
  "s_waitcnt lgkmcnt(2)\n\t" \
  "v_perm_b32 v116, v90, v88, s62\n\t" \
  "v_perm_b32 v117, v90, v88, s63\n\t" \
  "s_waitcnt lgkmcnt(0)\n\t" \
  "v_perm_b32 v118, v94, v92, s62\n\t" \
  "v_perm_b32 v119, v94, v92, s63\n\t" \
  "ds_write_b128 v70, v[112:115] offset:28672\n\t" \
  "ds_write_b128 v70, v[116:119] offset:29696\n\t" \
  "v_perm_b32 v208, v83, v81, s62\n\t" \
  "v_perm_b32 v209, v83, v81, s63\n\t" \
  "v_perm_b32 v210, v87, v85, s62\n\t" \
  "v_perm_b32 v211, v87, v85, s63\n\t" \
  "v_perm_b32 v212, v91, v89, s62\n\t" \
  "v_perm_b32 v213, v91, v89, s63\n\t" \
  "v_perm_b32 v214, v95, v93, s62\n\t" \
  "v_perm_b32 v215, v95, v93, s63\n\t" \
  "v_add_u32 v78, 90112, v66\n\t" \
  "s_waitcnt vmcnt(12) lgkmcnt(0)\n\t" \
  "s_barrier\n\t" \
  "ds_read_b64_tr_b16 a[104:105], v78 offset:0\n\t" \
  "ds_read_b64_tr_b16 a[106:107], v78 offset:1024\n\t" \
  "ds_read_b64_tr_b16 a[108:109], v78 offset:8192\n\t" \
  "ds_read_b64_tr_b16 a[110:111], v78 offset:9216\n\t" \
  "ds_read_b64_tr_b16 a[112:113], v78 offset:32\n\t" \
  "ds_read_b64_tr_b16 a[114:115], v78 offset:1056\n\t" \
  "ds_read_b64_tr_b16 a[116:117], v78 offset:8224\n\t" \
  "ds_read_b64_tr_b16 a[118:119], v78 offset:9248\n\t" \
  "s_add_u32 m0, s54, 53248\n\t" \
  "s_mov_b32 s60, 1572864\n\t" \
  "buffer_load_dwordx4 v71, %[Wv], s60 offen lds\n\t" \
  "s_add_u32 m0, s54, 54272\n\t" \
  "s_mov_b32 s60, 1605632\n\t" \
  "buffer_load_dwordx4 v71, %[Wv], s60 offen lds\n\t" \
  "s_add_u32 m0, s54, 55296\n\t" \
  "s_mov_b32 s60, 1638400\n\t" \
  "buffer_load_dwordx4 v71, %[Wv], s60 offen lds\n\t" \
  "s_add_u32 m0, s54, 56320\n\t" \
  "s_mov_b32 s60, 1671168\n\t" \
  "buffer_load_dwordx4 v71, %[Wv], s60 offen lds\n\t" \
  "v_add_u32 v78, 126976, v65\n\t" \
  "s_waitcnt vmcnt(12) lgkmcnt(0)\n\t" \
  "s_barrier\n\t" \
  "ds_read_b64_tr_b16 a[120:121], v78 offset:0\n\t" \
  "ds_read_b64_tr_b16 a[122:123], v78 offset:1024\n\t" \
  "ds_read_b64_tr_b16 a[124:125], v78 offset:8192\n\t" \
  "ds_read_b64_tr_b16 a[126:127], v78 offset:9216\n\t" \
  "ds_read_b64_tr_b16 v[16:17], v78 offset:8\n\t" \
  "ds_read_b64_tr_b16 v[18:19], v78 offset:1032\n\t" \
  "ds_read_b64_tr_b16 v[20:21], v78 offset:8200\n\t" \
  "ds_read_b64_tr_b16 v[22:23], v78 offset:9224\n\t" \
  "s_xor_b32 s61, s40, 3\n\t" \
  "s_mul_i32 s61, s61, 36864\n\t" \
  "s_add_u32 s61, s61, s55\n\t" \
  "s_add_u32 m0, s54, 90112\n\t" \
  "s_add_u32 s60, s61, 0\n\t" \
  "buffer_load_dwordx4 v75, %[Wo], s60 offen lds\n\t" \
  "s_add_u32 m0, s54, 91136\n\t" \
  "s_add_u32 s60, s61, 9216\n\t" \
  "buffer_load_dwordx4 v75, %[Wo], s60 offen lds\n\t" \
  "s_add_u32 m0, s54, 92160\n\t" \
  "s_add_u32 s60, s61, 18432\n\t" \
  "buffer_load_dwordx4 v75, %[Wo], s60 offen lds\n\t" \
  "s_add_u32 m0, s54, 93184\n\t" \
  "s_add_u32 s60, s61, 27648\n\t" \
  "buffer_load_dwordx4 v75, %[Wo], s60 offen lds\n\t" \
  "v_add_u32 v78, 0, v64\n\t" \
  "v_add_u32 v79, 1024, v64\n\t" \
  "s_waitcnt vmcnt(12) lgkmcnt(0)\n\t" \
  "s_barrier\n\t" \
  "ds_read2_b32 v[80:81], v78 offset0:0 offset1:2\n\t" \
  "ds_read2_b32 v[82:83], v78 offset0:32 offset1:34\n\t" \
  "ds_read2_b32 v[84:85], v78 offset0:64 offset1:66\n\t" \
  "ds_read2_b32 v[86:87], v78 offset0:96 offset1:98\n\t" \
  "ds_read2_b32 v[88:89], v79 offset0:0 offset1:2\n\t" \
  "ds_read2_b32 v[90:91], v79 offset0:32 offset1:34\n\t" \
  "ds_read2_b32 v[92:93], v79 offset0:64 offset1:66\n\t" \
  "ds_read2_b32 v[94:95], v79 offset0:96 offset1:98\n\t" \
  "s_add_u32 m0, s54, 126976\n\t" \
  "s_mov_b32 s60, 2097152\n\t" \
  "buffer_load_dwordx4 v71, %[Wq], s60 offen lds\n\t" \
  "s_add_u32 m0, s54, 128000\n\t" \
  "s_mov_b32 s60, 2129920\n\t" \
  "buffer_load_dwordx4 v71, %[Wq], s60 offen lds\n\t" \
  "s_add_u32 m0, s54, 129024\n\t" \
  "s_mov_b32 s60, 2162688\n\t" \
  "buffer_load_dwordx4 v71, %[Wq], s60 offen lds\n\t" \
  "s_add_u32 m0, s54, 130048\n\t" \
  "s_mov_b32 s60, 2195456\n\t" \
  "buffer_load_dwordx4 v71, %[Wq], s60 offen lds\n\t" \
  "s_waitcnt lgkmcnt(6)\n\t" \
  "v_perm_b32 v112, v82, v80, s62\n\t" \
  "v_perm_b32 v113, v82, v80, s63\n\t" \
  "s_waitcnt lgkmcnt(4)\n\t" \
  "v_perm_b32 v114, v86, v84, s62\n\t" \
  "v_perm_b32 v115, v86, v84, s63\n\t" \
  "s_waitcnt lgkmcnt(2)\n\t" \
  "v_perm_b32 v116, v90, v88, s62\n\t" \
  "v_perm_b32 v117, v90, v88, s63\n\t" \
  "s_waitcnt lgkmcnt(0)\n\t" \
  "v_perm_b32 v118, v94, v92, s62\n\t" \
  "v_perm_b32 v119, v94, v92, s63\n\t" \
  "ds_write_b128 v70, v[112:115] offset:30720\n\t" \
  "ds_write_b128 v70, v[116:119] offset:31744\n\t" \
  "v_perm_b32 v240, v83, v81, s62\n\t" \
  "v_perm_b32 v241, v83, v81, s63\n\t" \
  "v_perm_b32 v242, v87, v85, s62\n\t" \
  "v_perm_b32 v243, v87, v85, s63\n\t" \
  "v_perm_b32 v244, v91, v89, s62\n\t" \
  "v_perm_b32 v245, v91, v89, s63\n\t" \
  "v_perm_b32 v246, v95, v93, s62\n\t" \
  "v_perm_b32 v247, v95, v93, s63\n\t" \
  "v_add_u32 v78, 16384, v65\n\t" \
  "s_waitcnt vmcnt(12) lgkmcnt(0)\n\t" \
  "s_barrier\n\t" \
  "ds_read_b64_tr_b16 a[128:129], v78 offset:0\n\t" \
  "ds_read_b64_tr_b16 a[130:131], v78 offset:1024\n\t" \
  "ds_read_b64_tr_b16 a[132:133], v78 offset:8192\n\t" \
  "ds_read_b64_tr_b16 a[134:135], v78 offset:9216\n\t" \
  "ds_read_b64_tr_b16 a[136:137], v78 offset:8\n\t" \
  "ds_read_b64_tr_b16 a[138:139], v78 offset:1032\n\t" \
  "ds_read_b64_tr_b16 a[140:141], v78 offset:8200\n\t" \
  "ds_read_b64_tr_b16 a[142:143], v78 offset:9224\n\t" \
  "s_add_u32 m0, s54, 0\n\t" \
  "s_mov_b32 s60, 2097152\n\t" \
  "buffer_load_dwordx4 v71, %[Wk], s60 offen lds\n\t" \
  "s_add_u32 m0, s54, 1024\n\t" \
  "s_mov_b32 s60, 2129920\n\t" \
  "buffer_load_dwordx4 v71, %[Wk], s60 offen lds\n\t" \
  "s_add_u32 m0, s54, 2048\n\t" \
  "s_mov_b32 s60, 2162688\n\t" \
  "buffer_load_dwordx4 v71, %[Wk], s60 offen lds\n\t" \
  "s_add_u32 m0, s54, 3072\n\t" \
  "s_mov_b32 s60, 2195456\n\t" \
  "buffer_load_dwordx4 v71, %[Wk], s60 offen lds\n\t" \
  "v_add_u32 v78, 53248, v66\n\t" \
  "s_waitcnt vmcnt(12) lgkmcnt(0)\n\t" \
  "s_barrier\n\t" \
  "ds_read_b64_tr_b16 a[144:145], v78 offset:0\n\t" \
  "ds_read_b64_tr_b16 a[146:147], v78 offset:1024\n\t" \
  "ds_read_b64_tr_b16 a[148:149], v78 offset:8192\n\t" \
  "ds_read_b64_tr_b16 a[150:151], v78 offset:9216\n\t" \
  "ds_read_b64_tr_b16 a[152:153], v78 offset:32\n\t" \
  "ds_read_b64_tr_b16 a[154:155], v78 offset:1056\n\t" \
  "ds_read_b64_tr_b16 a[156:157], v78 offset:8224\n\t" \
  "ds_read_b64_tr_b16 a[158:159], v78 offset:9248\n\t" \
  "s_xor_b32 s61, s40, 3\n\t" \
  "s_mul_i32 s61, s61, 36864\n\t" \
  "s_add_u32 s61, s61, s55\n\t" \
  "s_add_u32 m0, s54, 16384\n\t" \
  "s_add_u32 s60, s61, 512\n\t" \
  "buffer_load_dwordx4 v75, %[Wo], s60 offen lds\n\t" \
  "s_add_u32 m0, s54, 17408\n\t" \
  "s_add_u32 s60, s61, 9728\n\t" \
  "buffer_load_dwordx4 v75, %[Wo], s60 offen lds\n\t" \
  "s_add_u32 m0, s54, 18432\n\t" \
  "s_add_u32 s60, s61, 18944\n\t" \
  "buffer_load_dwordx4 v75, %[Wo], s60 offen lds\n\t" \
  "s_add_u32 m0, s54, 19456\n\t" \
  "s_add_u32 s60, s61, 28160\n\t" \
  "buffer_load_dwordx4 v75, %[Wo], s60 offen lds\n\t" \
  "v_add_u32 v78, 90112, v64\n\t" \
  "v_add_u32 v79, 91136, v64\n\t" \
  "s_waitcnt vmcnt(12) lgkmcnt(0)\n\t" \
  "s_barrier\n\t" \
  "ds_read2_b32 v[80:81], v78 offset0:0 offset1:2\n\t" \
  "ds_read2_b32 v[82:83], v78 offset0:32 offset1:34\n\t" \
  "ds_read2_b32 v[84:85], v78 offset0:64 offset1:66\n\t" \
  "ds_read2_b32 v[86:87], v78 offset0:96 offset1:98\n\t" \
  "ds_read2_b32 v[88:89], v79 offset0:0 offset1:2\n\t" \
  "ds_read2_b32 v[90:91], v79 offset0:32 offset1:34\n\t" \
  "ds_read2_b32 v[92:93], v79 offset0:64 offset1:66\n\t" \
  "ds_read2_b32 v[94:95], v79 offset0:96 offset1:98\n\t" \
  "s_add_u32 m0, s54, 53248\n\t" \
  "s_mov_b32 s60, 2097152\n\t" \
  "buffer_load_dwordx4 v71, %[Wv], s60 offen lds\n\t" \
  "s_add_u32 m0, s54, 54272\n\t" \
  "s_mov_b32 s60, 2129920\n\t" \
  "buffer_load_dwordx4 v71, %[Wv], s60 offen lds\n\t" \
  "s_add_u32 m0, s54, 55296\n\t" \
  "s_mov_b32 s60, 2162688\n\t" \
  "buffer_load_dwordx4 v71, %[Wv], s60 offen lds\n\t" \
  "s_add_u32 m0, s54, 56320\n\t" \
  "s_mov_b32 s60, 2195456\n\t" \
  "buffer_load_dwordx4 v71, %[Wv], s60 offen lds\n\t" \
  "s_waitcnt lgkmcnt(6)\n\t" \
  "v_perm_b32 v192, v82, v80, s62\n\t" \
  "v_perm_b32 v193, v82, v80, s63\n\t" \
  "s_waitcnt lgkmcnt(4)\n\t" \
  "v_perm_b32 v194, v86, v84, s62\n\t" \
  "v_perm_b32 v195, v86, v84, s63\n\t" \
  "s_waitcnt lgkmcnt(2)\n\t" \
  "v_perm_b32 v196, v90, v88, s62\n\t" \
  "v_perm_b32 v197, v90, v88, s63\n\t" \
  "s_waitcnt lgkmcnt(0)\n\t" \
  "v_perm_b32 v198, v94, v92, s62\n\t" \
  "v_perm_b32 v199, v94, v92, s63\n\t" \
  "v_perm_b32 v216, v83, v81, s62\n\t" \
  "v_perm_b32 v217, v83, v81, s63\n\t" \
  "v_perm_b32 v218, v87, v85, s62\n\t" \
  "v_perm_b32 v219, v87, v85, s63\n\t" \
  "v_perm_b32 v220, v91, v89, s62\n\t" \
  "v_perm_b32 v221, v91, v89, s63\n\t" \
  "v_perm_b32 v222, v95, v93, s62\n\t" \
  "v_perm_b32 v223, v95, v93, s63\n\t" \
  "v_add_u32 v78, 126976, v65\n\t" \
  "s_waitcnt vmcnt(12) lgkmcnt(0)\n\t" \
  "s_barrier\n\t" \
  "ds_read_b64_tr_b16 a[160:161], v78 offset:0\n\t" \
  "ds_read_b64_tr_b16 a[162:163], v78 offset:1024\n\t" \
  "ds_read_b64_tr_b16 a[164:165], v78 offset:8192\n\t" \
  "ds_read_b64_tr_b16 a[166:167], v78 offset:9216\n\t" \
  "ds_read_b64_tr_b16 a[168:169], v78 offset:8\n\t" \
  "ds_read_b64_tr_b16 a[170:171], v78 offset:1032\n\t" \
  "ds_read_b64_tr_b16 a[172:173], v78 offset:8200\n\t" \
  "ds_read_b64_tr_b16 a[174:175], v78 offset:9224\n\t" \
  "s_add_u32 m0, s54, 90112\n\t" \
  "s_mov_b32 s60, 2621440\n\t" \
  "buffer_load_dwordx4 v71, %[Wq], s60 offen lds\n\t" \
  "s_add_u32 m0, s54, 91136\n\t" \
  "s_mov_b32 s60, 2654208\n\t" \
  "buffer_load_dwordx4 v71, %[Wq], s60 offen lds\n\t" \
  "s_add_u32 m0, s54, 92160\n\t" \
  "s_mov_b32 s60, 2686976\n\t" \
  "buffer_load_dwordx4 v71, %[Wq], s60 offen lds\n\t" \
  "s_add_u32 m0, s54, 93184\n\t" \
  "s_mov_b32 s60, 2719744\n\t" \
  "buffer_load_dwordx4 v71, %[Wq], s60 offen lds\n\t" \
  "v_add_u32 v78, 0, v65\n\t" \
  "s_waitcnt vmcnt(12) lgkmcnt(0)\n\t" \
  "s_barrier\n\t" \
  "ds_read_b64_tr_b16 a[176:177], v78 offset:0\n\t" \
  "ds_read_b64_tr_b16 a[178:179], v78 offset:1024\n\t" \
  "ds_read_b64_tr_b16 a[180:181], v78 offset:8192\n\t" \
  "ds_read_b64_tr_b16 a[182:183], v78 offset:9216\n\t" \
  "ds_read_b64_tr_b16 a[184:185], v78 offset:8\n\t" \
  "ds_read_b64_tr_b16 a[186:187], v78 offset:1032\n\t" \
  "ds_read_b64_tr_b16 a[188:189], v78 offset:8200\n\t" \
  "ds_read_b64_tr_b16 a[190:191], v78 offset:9224\n\t" \
  "s_add_u32 m0, s54, 126976\n\t" \
  "s_add_u32 s60, s75, 0\n\t" \
  "buffer_load_dwordx4 v75, %[Wo], s60 offen lds\n\t" \
  "s_add_u32 m0, s54, 128000\n\t" \
  "s_add_u32 s60, s75, 9216\n\t" \
  "buffer_load_dwordx4 v75, %[Wo], s60 offen lds\n\t" \
  "s_add_u32 m0, s54, 129024\n\t" \
  "s_add_u32 s60, s75, 18432\n\t" \
  "buffer_load_dwordx4 v75, %[Wo], s60 offen lds\n\t" \
  "s_add_u32 m0, s54, 130048\n\t" \
  "s_add_u32 s60, s75, 27648\n\t" \
  "buffer_load_dwordx4 v75, %[Wo], s60 offen lds\n\t" \
  "v_add_u32 v78, 16384, v64\n\t" \
  "v_add_u32 v79, 17408, v64\n\t" \
  "s_waitcnt vmcnt(12) lgkmcnt(0)\n\t" \
  "s_barrier\n\t" \
  "ds_read2_b32 v[80:81], v78 offset0:0 offset1:2\n\t" \
  "ds_read2_b32 v[82:83], v78 offset0:32 offset1:34\n\t" \
  "ds_read2_b32 v[84:85], v78 offset0:64 offset1:66\n\t" \
  "ds_read2_b32 v[86:87], v78 offset0:96 offset1:98\n\t" \
  "ds_read2_b32 v[88:89], v79 offset0:0 offset1:2\n\t" \
  "ds_read2_b32 v[90:91], v79 offset0:32 offset1:34\n\t" \
  "ds_read2_b32 v[92:93], v79 offset0:64 offset1:66\n\t" \
  "ds_read2_b32 v[94:95], v79 offset0:96 offset1:98\n\t" \
  "s_add_u32 m0, s54, 0\n\t" \
  "s_mov_b32 s60, 2621440\n\t" \
  "buffer_load_dwordx4 v71, %[Wk], s60 offen lds\n\t" \
  "s_add_u32 m0, s54, 1024\n\t" \
  "s_mov_b32 s60, 2654208\n\t" \
  "buffer_load_dwordx4 v71, %[Wk], s60 offen lds\n\t" \
  "s_add_u32 m0, s54, 2048\n\t" \
  "s_mov_b32 s60, 2686976\n\t" \
  "buffer_load_dwordx4 v71, %[Wk], s60 offen lds\n\t" \
  "s_add_u32 m0, s54, 3072\n\t" \
  "s_mov_b32 s60, 2719744\n\t" \
  "buffer_load_dwordx4 v71, %[Wk], s60 offen lds\n\t" \
  "s_waitcnt lgkmcnt(6)\n\t" \
  "v_perm_b32 v224, v82, v80, s62\n\t" \
  "v_perm_b32 v225, v82, v80, s63\n\t" \
  "s_waitcnt lgkmcnt(4)\n\t" \
  "v_perm_b32 v226, v86, v84, s62\n\t" \
  "v_perm_b32 v227, v86, v84, s63\n\t" \
  "s_waitcnt lgkmcnt(2)\n\t" \
  "v_perm_b32 v228, v90, v88, s62\n\t" \
  "v_perm_b32 v229, v90, v88, s63\n\t" \
  "s_waitcnt lgkmcnt(0)\n\t" \
  "v_perm_b32 v230, v94, v92, s62\n\t" \
  "v_perm_b32 v231, v94, v92, s63\n\t" \
  "v_perm_b32 v120, v83, v81, s62\n\t" \
  "v_perm_b32 v121, v83, v81, s63\n\t" \
  "v_perm_b32 v122, v87, v85, s62\n\t" \
  "v_perm_b32 v123, v87, v85, s63\n\t" \
  "v_perm_b32 v124, v91, v89, s62\n\t" \
  "v_perm_b32 v125, v91, v89, s63\n\t" \
  "v_perm_b32 v126, v95, v93, s62\n\t" \
  "v_perm_b32 v127, v95, v93, s63\n\t" \
  "ds_write_b128 v70, v[120:123] offset:32768\n\t" \
  "ds_write_b128 v70, v[124:127] offset:33792\n\t" \
  "v_add_u32 v78, 53248, v66\n\t" \
  "s_waitcnt vmcnt(12) lgkmcnt(0)\n\t" \
  "s_barrier\n\t" \
  "ds_read_b64_tr_b16 a[192:193], v78 offset:0\n\t" \
  "ds_read_b64_tr_b16 a[194:195], v78 offset:1024\n\t" \
  "ds_read_b64_tr_b16 a[196:197], v78 offset:8192\n\t" \
  "ds_read_b64_tr_b16 a[198:199], v78 offset:9216\n\t" \
  "ds_read_b64_tr_b16 v[48:49], v78 offset:32\n\t" \
  "ds_read_b64_tr_b16 v[50:51], v78 offset:1056\n\t" \
  "ds_read_b64_tr_b16 v[52:53], v78 offset:8224\n\t" \
  "ds_read_b64_tr_b16 v[54:55], v78 offset:9248\n\t" \
  "s_add_u32 m0, s54, 16384\n\t" \
  "s_mov_b32 s60, 2621440\n\t" \
  "buffer_load_dwordx4 v71, %[Wv], s60 offen lds\n\t" \
  "s_add_u32 m0, s54, 17408\n\t" \
  "s_mov_b32 s60, 2654208\n\t" \
  "buffer_load_dwordx4 v71, %[Wv], s60 offen lds\n\t" \
  "s_add_u32 m0, s54, 18432\n\t" \
  "s_mov_b32 s60, 2686976\n\t" \
  "buffer_load_dwordx4 v71, %[Wv], s60 offen lds\n\t" \
  "s_add_u32 m0, s54, 19456\n\t" \
  "s_mov_b32 s60, 2719744\n\t" \
  "buffer_load_dwordx4 v71, %[Wv], s60 offen lds\n\t" \
  "v_add_u32 v78, 90112, v65\n\t" \
  "s_waitcnt vmcnt(12) lgkmcnt(0)\n\t" \
  "s_barrier\n\t" \
  "ds_read_b64_tr_b16 a[200:201], v78 offset:0\n\t" \
  "ds_read_b64_tr_b16 a[202:203], v78 offset:1024\n\t" \
  "ds_read_b64_tr_b16 a[204:205], v78 offset:8192\n\t" \
  "ds_read_b64_tr_b16 a[206:207], v78 offset:9216\n\t" \
  "ds_read_b64_tr_b16 v[24:25], v78 offset:8\n\t" \
  "ds_read_b64_tr_b16 v[26:27], v78 offset:1032\n\t" \
  "ds_read_b64_tr_b16 v[28:29], v78 offset:8200\n\t" \
  "ds_read_b64_tr_b16 v[30:31], v78 offset:9224\n\t" \
  "s_add_u32 m0, s54, 53248\n\t" \
  "s_mov_b32 s60, 3145728\n\t" \
  "buffer_load_dwordx4 v71, %[Wq], s60 offen lds\n\t" \
  "s_add_u32 m0, s54, 54272\n\t" \
  "s_mov_b32 s60, 3178496\n\t" \
  "buffer_load_dwordx4 v71, %[Wq], s60 offen lds\n\t" \
  "s_add_u32 m0, s54, 55296\n\t" \
  "s_mov_b32 s60, 3211264\n\t" \
  "buffer_load_dwordx4 v71, %[Wq], s60 offen lds\n\t" \
  "s_add_u32 m0, s54, 56320\n\t" \
  "s_mov_b32 s60, 3244032\n\t" \
  "buffer_load_dwordx4 v71, %[Wq], s60 offen lds\n\t" \
  "v_add_u32 v78, 126976, v67\n\t" \
  "s_waitcnt vmcnt(12) lgkmcnt(0)\n\t" \
  "s_barrier\n\t" \
  "ds_read_u16 v80, v78 offset:0\n\t" \
  "ds_read_u16 v81, v78 offset:128\n\t" \
  "ds_read_u16 v82, v78 offset:4096\n\t" \
  "ds_read_u16 v83, v78 offset:4224\n\t" \
  "ds_read_u16 v84, v78 offset:256\n\t" \
  "ds_read_u16 v85, v78 offset:384\n\t" \
  "ds_read_u16 v86, v78 offset:4352\n\t" \
  "ds_read_u16 v87, v78 offset:4480\n\t" \
  "ds_read_u16 v88, v78 offset:1024\n\t" \
  "ds_read_u16 v89, v78 offset:1152\n\t" \
  "ds_read_u16 v90, v78 offset:5120\n\t" \
  "ds_read_u16 v91, v78 offset:5248\n\t" \
  "ds_read_u16 v92, v78 offset:1280\n\t" \
  "ds_read_u16 v93, v78 offset:1408\n\t" \
  "ds_read_u16 v94, v78 offset:5376\n\t" \
  "ds_read_u16 v95, v78 offset:5504\n\t" \
  "ds_read_u16 v96, v78 offset:8192\n\t" \
  "ds_read_u16 v97, v78 offset:8320\n\t" \
  "ds_read_u16 v98, v78 offset:12288\n\t" \
  "ds_read_u16 v99, v78 offset:12416\n\t" \
  "ds_read_u16 v100, v78 offset:8448\n\t" \
  "ds_read_u16 v101, v78 offset:8576\n\t" \
  "ds_read_u16 v102, v78 offset:12544\n\t" \
  "ds_read_u16 v103, v78 offset:12672\n\t" \
  "ds_read_u16 v104, v78 offset:9216\n\t" \
  "ds_read_u16 v105, v78 offset:9344\n\t" \
  "ds_read_u16 v106, v78 offset:13312\n\t" \
  "ds_read_u16 v107, v78 offset:13440\n\t" \
  "ds_read_u16 v108, v78 offset:9472\n\t" \
  "ds_read_u16 v109, v78 offset:9600\n\t" \
  "ds_read_u16 v110, v78 offset:13568\n\t" \
  "ds_read_u16 v111, v78 offset:13696\n\t" \
  "s_add_u32 m0, s54, 90112\n\t" \
  "s_mov_b32 s60, 3145728\n\t" \
  "buffer_load_dwordx4 v71, %[Wk], s60 offen lds\n\t" \
  "s_add_u32 m0, s54, 91136\n\t" \
  "s_mov_b32 s60, 3178496\n\t" \
  "buffer_load_dwordx4 v71, %[Wk], s60 offen lds\n\t" \
  "s_add_u32 m0, s54, 92160\n\t" \
  "s_mov_b32 s60, 3211264\n\t" \
  "buffer_load_dwordx4 v71, %[Wk], s60 offen lds\n\t" \
  "s_add_u32 m0, s54, 93184\n\t" \
  "s_mov_b32 s60, 3244032\n\t" \
  "buffer_load_dwordx4 v71, %[Wk], s60 offen lds\n\t" \
  "s_waitcnt lgkmcnt(0)\n\t" \
  "v_lshl_or_b32 v248, v81, 16, v80\n\t" \
  "v_lshl_or_b32 v249, v83, 16, v82\n\t" \
  "v_lshl_or_b32 v250, v85, 16, v84\n\t" \
  "v_lshl_or_b32 v251, v87, 16, v86\n\t" \
  "v_lshl_or_b32 v252, v89, 16, v88\n\t" \
  "v_lshl_or_b32 v253, v91, 16, v90\n\t" \
  "v_lshl_or_b32 v254, v93, 16, v92\n\t" \
  "v_lshl_or_b32 v255, v95, 16, v94\n\t" \
  "v_lshl_or_b32 v112, v97, 16, v96\n\t" \
  "v_lshl_or_b32 v113, v99, 16, v98\n\t" \
  "v_lshl_or_b32 v114, v101, 16, v100\n\t" \
  "v_lshl_or_b32 v115, v103, 16, v102\n\t" \
  "v_lshl_or_b32 v116, v105, 16, v104\n\t" \
  "v_lshl_or_b32 v117, v107, 16, v106\n\t" \
  "v_lshl_or_b32 v118, v109, 16, v108\n\t" \
  "v_lshl_or_b32 v119, v111, 16, v110\n\t" \
  "ds_write_b128 v70, v[112:115] offset:34816\n\t" \
  "ds_write_b128 v70, v[116:119] offset:35840\n\t" \
  "v_add_u32 v78, 0, v65\n\t" \
  "s_waitcnt vmcnt(12) lgkmcnt(0)\n\t" \
  "s_barrier\n\t" \
  "ds_read_b64_tr_b16 a[208:209], v78 offset:0\n\t" \
  "ds_read_b64_tr_b16 a[210:211], v78 offset:1024\n\t" \
  "ds_read_b64_tr_b16 a[212:213], v78 offset:8192\n\t" \
  "ds_read_b64_tr_b16 a[214:215], v78 offset:9216\n\t" \
  "ds_read_b64_tr_b16 a[216:217], v78 offset:8\n\t" \
  "ds_read_b64_tr_b16 a[218:219], v78 offset:1032\n\t" \
  "ds_read_b64_tr_b16 a[220:221], v78 offset:8200\n\t" \
  "ds_read_b64_tr_b16 a[222:223], v78 offset:9224\n\t" \
  "s_add_u32 m0, s54, 126976\n\t" \
  "s_mov_b32 s60, 3145728\n\t" \
  "buffer_load_dwordx4 v71, %[Wv], s60 offen lds\n\t" \
  "s_add_u32 m0, s54, 128000\n\t" \
  "s_mov_b32 s60, 3178496\n\t" \
  "buffer_load_dwordx4 v71, %[Wv], s60 offen lds\n\t" \
  "s_add_u32 m0, s54, 129024\n\t" \
  "s_mov_b32 s60, 3211264\n\t" \
  "buffer_load_dwordx4 v71, %[Wv], s60 offen lds\n\t" \
  "s_add_u32 m0, s54, 130048\n\t" \
  "s_mov_b32 s60, 3244032\n\t" \
  "buffer_load_dwordx4 v71, %[Wv], s60 offen lds\n\t" \
  "v_add_u32 v78, 16384, v66\n\t" \
  "s_waitcnt vmcnt(12) lgkmcnt(0)\n\t" \
  "s_barrier\n\t" \
  "ds_read_b64_tr_b16 a[224:225], v78 offset:0\n\t" \
  "ds_read_b64_tr_b16 a[226:227], v78 offset:1024\n\t" \
  "ds_read_b64_tr_b16 a[228:229], v78 offset:8192\n\t" \
  "ds_read_b64_tr_b16 a[230:231], v78 offset:9216\n\t" \
  "ds_read_b64_tr_b16 a[232:233], v78 offset:32\n\t" \
  "ds_read_b64_tr_b16 a[234:235], v78 offset:1056\n\t" \
  "ds_read_b64_tr_b16 a[236:237], v78 offset:8224\n\t" \
  "ds_read_b64_tr_b16 a[238:239], v78 offset:9248\n\t" \
  "s_add_u32 m0, s54, 0\n\t" \
  "s_mov_b32 s60, 3670016\n\t" \
  "buffer_load_dwordx4 v71, %[Wq], s60 offen lds\n\t" \
  "s_add_u32 m0, s54, 1024\n\t" \
  "s_mov_b32 s60, 3702784\n\t" \
  "buffer_load_dwordx4 v71, %[Wq], s60 offen lds\n\t" \
  "s_add_u32 m0, s54, 2048\n\t" \
  "s_mov_b32 s60, 3735552\n\t" \
  "buffer_load_dwordx4 v71, %[Wq], s60 offen lds\n\t" \
  "s_add_u32 m0, s54, 3072\n\t" \
  "s_mov_b32 s60, 3768320\n\t" \
  "buffer_load_dwordx4 v71, %[Wq], s60 offen lds\n\t" \
  "v_add_u32 v78, 53248, v65\n\t" \
  "s_waitcnt vmcnt(12) lgkmcnt(0)\n\t" \
  "s_barrier\n\t" \
  "ds_read_b64_tr_b16 a[240:241], v78 offset:0\n\t" \
  "ds_read_b64_tr_b16 a[242:243], v78 offset:1024\n\t" \
  "ds_read_b64_tr_b16 a[244:245], v78 offset:8192\n\t" \
  "ds_read_b64_tr_b16 a[246:247], v78 offset:9216\n\t" \
  "ds_read_b64_tr_b16 a[248:249], v78 offset:8\n\t" \
  "ds_read_b64_tr_b16 a[250:251], v78 offset:1032\n\t" \
  "ds_read_b64_tr_b16 a[252:253], v78 offset:8200\n\t" \
  "ds_read_b64_tr_b16 a[254:255], v78 offset:9224\n\t" \
  "s_add_u32 m0, s54, 16384\n\t" \
  "s_mov_b32 s60, 3670016\n\t" \
  "buffer_load_dwordx4 v71, %[Wk], s60 offen lds\n\t" \
  "s_add_u32 m0, s54, 17408\n\t" \
  "s_mov_b32 s60, 3702784\n\t" \
  "buffer_load_dwordx4 v71, %[Wk], s60 offen lds\n\t" \
  "s_add_u32 m0, s54, 18432\n\t" \
  "s_mov_b32 s60, 3735552\n\t" \
  "buffer_load_dwordx4 v71, %[Wk], s60 offen lds\n\t" \
  "s_add_u32 m0, s54, 19456\n\t" \
  "s_mov_b32 s60, 3768320\n\t" \
  "buffer_load_dwordx4 v71, %[Wk], s60 offen lds\n\t" \
  "v_add_u32 v78, 90112, v65\n\t" \
  "s_waitcnt vmcnt(12) lgkmcnt(0)\n\t" \
  "s_barrier\n\t" \
  "ds_read_b64_tr_b16 v[80:81], v78 offset:0\n\t" \
  "ds_read_b64_tr_b16 v[82:83], v78 offset:1024\n\t" \
  "ds_read_b64_tr_b16 v[84:85], v78 offset:8192\n\t" \
  "ds_read_b64_tr_b16 v[86:87], v78 offset:9216\n\t" \
  "ds_read_b64_tr_b16 v[88:89], v78 offset:8\n\t" \
  "ds_read_b64_tr_b16 v[90:91], v78 offset:1032\n\t" \
  "ds_read_b64_tr_b16 v[92:93], v78 offset:8200\n\t" \
  "ds_read_b64_tr_b16 v[94:95], v78 offset:9224\n\t" \
  "s_add_u32 m0, s54, 53248\n\t" \
  "s_mov_b32 s60, 3670016\n\t" \
  "buffer_load_dwordx4 v71, %[Wv], s60 offen lds\n\t" \
  "s_add_u32 m0, s54, 54272\n\t" \
  "s_mov_b32 s60, 3702784\n\t" \
  "buffer_load_dwordx4 v71, %[Wv], s60 offen lds\n\t" \
  "s_add_u32 m0, s54, 55296\n\t" \
  "s_mov_b32 s60, 3735552\n\t" \
  "buffer_load_dwordx4 v71, %[Wv], s60 offen lds\n\t" \
  "s_add_u32 m0, s54, 56320\n\t" \
  "s_mov_b32 s60, 3768320\n\t" \
  "buffer_load_dwordx4 v71, %[Wv], s60 offen lds\n\t" \
  "v_add_u32 v78, 126976, v66\n\t" \
  "s_waitcnt vmcnt(12) lgkmcnt(0)\n\t" \
  "s_barrier\n\t" \
  "ds_read_b64_tr_b16 v[96:97], v78 offset:0\n\t" \
  "ds_read_b64_tr_b16 v[98:99], v78 offset:1024\n\t" \
  "ds_read_b64_tr_b16 v[100:101], v78 offset:8192\n\t" \
  "ds_read_b64_tr_b16 v[102:103], v78 offset:9216\n\t" \
  "ds_read_b64_tr_b16 v[104:105], v78 offset:32\n\t" \
  "ds_read_b64_tr_b16 v[106:107], v78 offset:1056\n\t" \
  "ds_read_b64_tr_b16 v[108:109], v78 offset:8224\n\t" \
  "ds_read_b64_tr_b16 v[110:111], v78 offset:9248\n\t" \
  "s_add_u32 m0, s54, 90112\n\t" \
  "s_mov_b32 s60, 4194304\n\t" \
  "buffer_load_dwordx4 v71, %[Wq], s60 offen lds\n\t" \
  "s_add_u32 m0, s54, 91136\n\t" \
  "s_mov_b32 s60, 4227072\n\t" \
  "buffer_load_dwordx4 v71, %[Wq], s60 offen lds\n\t" \
  "s_add_u32 m0, s54, 92160\n\t" \
  "s_mov_b32 s60, 4259840\n\t" \
  "buffer_load_dwordx4 v71, %[Wq], s60 offen lds\n\t" \
  "s_add_u32 m0, s54, 93184\n\t" \
  "s_mov_b32 s60, 4292608\n\t" \
  "buffer_load_dwordx4 v71, %[Wq], s60 offen lds\n\t" \
  "v_add_u32 v78, 0, v65\n\t" \
  "s_waitcnt vmcnt(12) lgkmcnt(0)\n\t" \
  "s_barrier\n\t" \
  "ds_read_b64_tr_b16 v[112:113], v78 offset:0\n\t" \
  "ds_read_b64_tr_b16 v[114:115], v78 offset:1024\n\t" \
  "ds_read_b64_tr_b16 v[116:117], v78 offset:8192\n\t" \
  "ds_read_b64_tr_b16 v[118:119], v78 offset:9216\n\t" \
  "ds_read_b64_tr_b16 v[120:121], v78 offset:8\n\t" \
  "ds_read_b64_tr_b16 v[122:123], v78 offset:1032\n\t" \
  "ds_read_b64_tr_b16 v[124:125], v78 offset:8200\n\t" \
  "ds_read_b64_tr_b16 v[126:127], v78 offset:9224\n\t" \
  "s_add_u32 m0, s54, 126976\n\t" \
  "s_mov_b32 s60, 4194304\n\t" \
  "buffer_load_dwordx4 v71, %[Wk], s60 offen lds\n\t" \
  "s_add_u32 m0, s54, 128000\n\t" \
  "s_mov_b32 s60, 4227072\n\t" \
  "buffer_load_dwordx4 v71, %[Wk], s60 offen lds\n\t" \
  "s_add_u32 m0, s54, 129024\n\t" \
  "s_mov_b32 s60, 4259840\n\t" \
  "buffer_load_dwordx4 v71, %[Wk], s60 offen lds\n\t" \
  "s_add_u32 m0, s54, 130048\n\t" \
  "s_mov_b32 s60, 4292608\n\t" \
  "buffer_load_dwordx4 v71, %[Wk], s60 offen lds\n\t" \
  "v_add_u32 v78, 16384, v65\n\t" \
  "s_waitcnt vmcnt(12) lgkmcnt(0)\n\t" \
  "s_barrier\n\t" \
  "ds_read_b64_tr_b16 v[128:129], v78 offset:0\n\t" \
  "ds_read_b64_tr_b16 v[130:131], v78 offset:1024\n\t" \
  "ds_read_b64_tr_b16 v[132:133], v78 offset:8192\n\t" \
  "ds_read_b64_tr_b16 v[134:135], v78 offset:9216\n\t" \
  "ds_read_b64_tr_b16 v[136:137], v78 offset:8\n\t" \
  "ds_read_b64_tr_b16 v[138:139], v78 offset:1032\n\t" \
  "ds_read_b64_tr_b16 v[140:141], v78 offset:8200\n\t" \
  "ds_read_b64_tr_b16 v[142:143], v78 offset:9224\n\t" \
  "s_add_u32 m0, s54, 0\n\t" \
  "s_mov_b32 s60, 4194304\n\t" \
  "buffer_load_dwordx4 v71, %[Wv], s60 offen lds\n\t" \
  "s_add_u32 m0, s54, 1024\n\t" \
  "s_mov_b32 s60, 4227072\n\t" \
  "buffer_load_dwordx4 v71, %[Wv], s60 offen lds\n\t" \
  "s_add_u32 m0, s54, 2048\n\t" \
  "s_mov_b32 s60, 4259840\n\t" \
  "buffer_load_dwordx4 v71, %[Wv], s60 offen lds\n\t" \
  "s_add_u32 m0, s54, 3072\n\t" \
  "s_mov_b32 s60, 4292608\n\t" \
  "buffer_load_dwordx4 v71, %[Wv], s60 offen lds\n\t" \
  "v_add_u32 v78, 53248, v66\n\t" \
  "s_waitcnt vmcnt(12) lgkmcnt(0)\n\t" \
  "s_barrier\n\t" \
  "ds_read_b64_tr_b16 v[144:145], v78 offset:0\n\t" \
  "ds_read_b64_tr_b16 v[146:147], v78 offset:1024\n\t" \
  "ds_read_b64_tr_b16 v[148:149], v78 offset:8192\n\t" \
  "ds_read_b64_tr_b16 v[150:151], v78 offset:9216\n\t" \
  "ds_read_b64_tr_b16 v[152:153], v78 offset:32\n\t" \
  "ds_read_b64_tr_b16 v[154:155], v78 offset:1056\n\t" \
  "ds_read_b64_tr_b16 v[156:157], v78 offset:8224\n\t" \
  "ds_read_b64_tr_b16 v[158:159], v78 offset:9248\n\t" \
  "v_add_u32 v78, 90112, v65\n\t" \
  "s_waitcnt vmcnt(8) lgkmcnt(0)\n\t" \
  "s_barrier\n\t" \
  "ds_read_b64_tr_b16 v[160:161], v78 offset:0\n\t" \
  "ds_read_b64_tr_b16 v[162:163], v78 offset:1024\n\t" \
  "ds_read_b64_tr_b16 v[164:165], v78 offset:8192\n\t" \
  "ds_read_b64_tr_b16 v[166:167], v78 offset:9216\n\t" \
  "ds_read_b64_tr_b16 v[168:169], v78 offset:8\n\t" \
  "ds_read_b64_tr_b16 v[170:171], v78 offset:1032\n\t" \
  "ds_read_b64_tr_b16 v[172:173], v78 offset:8200\n\t" \
  "ds_read_b64_tr_b16 v[174:175], v78 offset:9224\n\t" \
  "v_add_u32 v78, 126976, v65\n\t" \
  "s_waitcnt vmcnt(4) lgkmcnt(0)\n\t" \
  "s_barrier\n\t" \
  "ds_read_b64_tr_b16 v[40:41], v78 offset:0\n\t" \
  "ds_read_b64_tr_b16 v[42:43], v78 offset:1024\n\t" \
  "ds_read_b64_tr_b16 v[44:45], v78 offset:8192\n\t" \
  "ds_read_b64_tr_b16 v[46:47], v78 offset:9216\n\t" \
  "ds_read_b64_tr_b16 v[176:177], v78 offset:8\n\t" \
  "ds_read_b64_tr_b16 v[178:179], v78 offset:1032\n\t" \
  "ds_read_b64_tr_b16 v[180:181], v78 offset:8200\n\t" \
  "ds_read_b64_tr_b16 v[182:183], v78 offset:9224\n\t" \
  "v_add_u32 v78, 0, v66\n\t" \
  "s_waitcnt vmcnt(0) lgkmcnt(0)\n\t" \
  "s_barrier\n\t" \
  "ds_read_b64_tr_b16 v[184:185], v78 offset:0\n\t" \
  "ds_read_b64_tr_b16 v[186:187], v78 offset:1024\n\t" \
  "ds_read_b64_tr_b16 v[188:189], v78 offset:8192\n\t" \
  "ds_read_b64_tr_b16 v[190:191], v78 offset:9216\n\t" \
  "ds_read_b64_tr_b16 v[56:57], v78 offset:32\n\t" \
  "ds_read_b64_tr_b16 v[58:59], v78 offset:1056\n\t" \
  "ds_read_b64_tr_b16 v[60:61], v78 offset:8224\n\t" \
  "ds_read_b64_tr_b16 v[62:63], v78 offset:9248\n\t" \
  CH_STV \
  "s_waitcnt lgkmcnt(0)\n\t" \
  "s_barrier\n\t" \
  "ds_write_b128 v70, v[0:3] offset:0\n\t" \
  "ds_write_b128 v70, v[4:7] offset:1024\n\t" \
  "ds_write_b128 v70, v[8:11] offset:2048\n\t" \
  "ds_write_b128 v70, v[12:15] offset:3072\n\t" \
  "ds_write_b128 v70, v[16:19] offset:4096\n\t" \
  "ds_write_b128 v70, v[20:23] offset:5120\n\t" \
  "ds_write_b128 v70, v[24:27] offset:6144\n\t" \
  "ds_write_b128 v70, v[28:31] offset:7168\n\t" \
  "ds_write_b128 v70, v[32:35] offset:8192\n\t" \
  "ds_write_b128 v70, v[36:39] offset:9216\n\t" \
  "ds_write_b128 v70, v[40:43] offset:10240\n\t" \
  "ds_write_b128 v70, v[44:47] offset:11264\n\t" \
  "ds_write_b128 v70, v[48:51] offset:12288\n\t" \
  "ds_write_b128 v70, v[52:55] offset:13312\n\t" \
  "ds_write_b128 v70, v[56:59] offset:14336\n\t" \
  "ds_write_b128 v70, v[60:63] offset:15360\n\t" \
  "s_waitcnt lgkmcnt(0)\n\t" \
  CH_STW \
  CH_COW \
  "v_and_b32 v72, 1, v72\n\t" \
  "v_and_b32 v74, 1, v74\n\t" \
  "v_cmp_ne_u32 s[90:91], s32, v72\n\t" \
  "v_cmp_ne_u32 vcc, s32, v74\n\t" \
  "s_nop 1\n\t" \
  "s_mov_b32 s96, vcc_lo\n\t" \
  "s_mov_b32 s21, vcc_lo\n\t" \
  "v_and_b32 v73, 32, v69\n\t" \
  "v_lshl_add_u32 v73, v73, 5, v69\n\t" \
  "s_mov_b32 s35, s30\n\t" \
  "s_cmp_lt_u32 s32, " CH_DRYL "\n\t" \
  "s_cbranch_scc1 LINI_%=\n\t" \
  "s_mov_b32 s30, 0\n\t" \
  "s_mov_b32 s34, 0\n\t" \
  "s_cmp_eq_u32 s40, 0\n\t" \
  "s_cbranch_scc1 LW0_%=\n\t" \
  "s_barrier\n\t" \
  "s_cmp_eq_u32 s40, 1\n\t" \
  "s_cbranch_scc1 LG1_%=\n\t" \
  "s_cmp_eq_u32 s40, 2\n\t" \
  "s_cbranch_scc1 LG2_%=\n\t" \
  "s_branch LG3_%=\n\t" \
  "LINI_%=:\n\t" \
  "s_waitcnt vmcnt(0) lgkmcnt(0)\n\t" \
  "s_mov_b32 s30, s35\n\t" \
  "s_movk_i32 s34, 1000\n\t" \
  "s_mov_b32 s48, -1\n\t" \
  "s_add_u32 s97, s41, s53\n\t" \
  "s_add_u32 s98, s37, s53\n\t" \
  "s_add_u32 s82, s48, 0\n\t" \
  "s_and_b32 s82, s82, " CH_TRM "\n\t" \
  CH_SEGC \
  "s_lshr_b64 s[60:61], s[90:91], s82\n\t" \
  "s_and_b32 s60, s60, 1\n\t" \
  "s_lshl_b32 s60, s60, 13\n\t" \
  "s_add_u32 s82, s82, " CH_SG0 "\n\t" \
  "s_lshl_b32 s82, s82, 21\n\t" \
  "s_or_b32 s82, s82, s60\n\t" \
  "s_add_u32 s50, s82, s38\n\t" \
  "s_add_u32 s69, s82, s39\n\t" \
  "s_add_u32 s82, s48, 1\n\t" \
  CH_SEGC \
  "s_lshr_b64 s[60:61], s[90:91], s82\n\t" \
  "s_and_b32 s60, s60, 1\n\t" \
  "s_lshl_b32 s60, s60, 13\n\t" \
  "s_add_u32 s82, s82, " CH_SG0 "\n\t" \
  "s_lshl_b32 s82, s82, 21\n\t" \
  "s_or_b32 s82, s82, s60\n\t" \
  "s_add_u32 s52, s82, s43\n\t" \
  "s_add_u32 s82, s48, " CH_ARMD "\n\t" \
  CH_SEGC \
  "s_lshr_b64 s[60:61], s[90:91], s82\n\t" \
  "s_and_b32 s60, s60, 1\n\t" \
  "s_lshl_b32 s60, s60, 13\n\t" \
  "s_add_u32 s82, s82, " CH_SG0 "\n\t" \
  "s_lshl_b32 s82, s82, 21\n\t" \
  "s_or_b32 s82, s82, s60\n\t" \
  "s_add_u32 s85, s82, s45\n\t" \
  "s_cmp_lt_i32 s48, " CH_ARMS "\n\t" \
  "s_cselect_b32 s85, s27, s85\n\t" \
  "v_and_b32 v37, 0x30, v69\n\t" \
  "v_add_u32 v37, s52, v37\n\t" \
  "s_mov_b32 s48, 0\n\t" \
  "s_mov_b32 s86, 1\n\t" \
  "s_and_b32 s49, s96, 1\n\t" \
  "s_lshl_b32 s49, s49, 13\n\t" \
  "s_add_u32 s53, s49, " CH_SLOT "\n\t" \
  "s_movk_i32 s20, 2560\n\t" \
  "s_movk_i32 s60, 124\n\t" \
  "s_cmp_eq_u32 s40, 3\n\t" \
  "s_cselect_b32 s81, 76, s60\n\t" \
  "s_add_u32 s82, s48, 0\n\t" \
  "s_and_b32 s82, s82, " CH_TRM "\n\t" \
  CH_SEGC \
  "s_lshr_b64 s[60:61], s[90:91], s82\n\t" \
  "s_and_b32 s60, s60, 1\n\t" \
  "s_lshl_b32 s60, s60, 13\n\t" \
  "s_add_u32 s82, s82, " CH_SG0 "\n\t" \
  "s_lshl_b32 s82, s82, 21\n\t" \
  "s_or_b32 s82, s82, s60\n\t" \
  "s_add_u32 s50, s82, s38\n\t" \
  "s_add_u32 s69, s82, s39\n\t" \
  "s_add_u32 s82, s48, 0\n\t" \
  CH_SEGC \
  "s_lshr_b64 s[60:61], s[90:91], s82\n\t" \
  "s_and_b32 s60, s60, 1\n\t" \
  "s_lshl_b32 s60, s60, 13\n\t" \
  "s_add_u32 s82, s82, " CH_SG0 "\n\t" \
  "s_lshl_b32 s82, s82, 21\n\t" \
  "s_or_b32 s82, s82, s60\n\t" \
  "s_add_u32 s51, s82, s43\n\t" \
  "s_add_u32 s97, s41, s53\n\t" \
  "s_add_u32 s98, s37, s53\n\t" \
  "s_add_u32 s82, s48, 0\n\t" \
  "s_and_b32 s82, s82, " CH_TRM "\n\t" \
  CH_SEGC \
  "s_lshr_b64 s[60:61], s[90:91], s82\n\t" \
  "s_and_b32 s60, s60, 1\n\t" \
  "s_lshl_b32 s60, s60, 13\n\t" \
  "s_add_u32 s82, s82, " CH_SG0 "\n\t" \
  "s_lshl_b32 s82, s82, 21\n\t" \
  "s_or_b32 s82, s82, s60\n\t" \
  "s_add_u32 s50, s82, s38\n\t" \
  "s_add_u32 s69, s82, s39\n\t" \
  "s_add_u32 s82, s48, 1\n\t" \
  CH_SEGC \
  "s_lshr_b64 s[60:61], s[90:91], s82\n\t" \
  "s_and_b32 s60, s60, 1\n\t" \
  "s_lshl_b32 s60, s60, 13\n\t" \
  "s_add_u32 s82, s82, " CH_SG0 "\n\t" \
  "s_lshl_b32 s82, s82, 21\n\t" \
  "s_or_b32 s82, s82, s60\n\t" \
  "s_add_u32 s52, s82, s43\n\t" \
  "s_add_u32 s82, s48, " CH_ARMD "\n\t" \
  CH_SEGC \
  "s_lshr_b64 s[60:61], s[90:91], s82\n\t" \
  "s_and_b32 s60, s60, 1\n\t" \
  "s_lshl_b32 s60, s60, 13\n\t" \
  "s_add_u32 s82, s82, " CH_SG0 "\n\t" \
  "s_lshl_b32 s82, s82, 21\n\t" \
  "s_or_b32 s82, s82, s60\n\t" \
  "s_add_u32 s85, s82, s45\n\t" \
  "s_cmp_lt_i32 s48, " CH_ARMS "\n\t" \
  "s_cselect_b32 s85, s27, s85\n\t" \
  "s_memrealtime s[62:63]\n\t" \
  "s_waitcnt lgkmcnt(0)\n\t" \
  "s_mov_b32 s65, s62\n\t" \
  "s_sub_u32 s47, s62, 1000\n\t" \
  "s_mov_b64 vcc, -1\n\t" \
  "s_cmp_eq_u32 s40, 0\n\t" \
  "s_cbranch_scc1 LW0_%=\n\t" \
  "s_barrier\n\t" \
  "s_cmp_eq_u32 s40, 1\n\t" \
  "s_cbranch_scc1 LG1_%=\n\t" \
  "s_cmp_eq_u32 s40, 2\n\t" \
  "s_cbranch_scc1 LG2_%=\n\t" \
  "s_branch LTK_%=\n\t" \
  ".p2align 6\n\t" \
  "LW0_%=:\n\t" \
  "v_lshlrev_b32 v78, 2, v69\n\t" \
  "v_and_b32 v78, 0x300, v78\n\t" \
  "v_lshrrev_b32 v79, 8, v69\n\t" \
  "v_lshl_add_u32 v78, v79, 6, v78\n\t" \
  "v_and_b32 v79, 48, v69\n\t" \
  "v_add_u32 v78, v78, v79\n\t" \
  "s_add_u32 s98, s49, s42\n\t" \
  "s_add_u32 s97, s98, 32768\n\t" \
  "s_add_u32 s99, s98, 65536\n\t" \
  "s_barrier\n\t" \
  "s_memrealtime s[62:63]\n\t" \
  "s_waitcnt lgkmcnt(0)\n\t" \
  "s_add_u32 s62, s62, " CH_DLL2 "\n\t" \
  "s_sub_i32 s63, s62, s65\n\t" \
  "s_cmp_gt_i32 s63, " CH_DLT "\n\t" \
  "s_cselect_b32 s60, " CH_DLY ", " CH_DLX "\n\t" \
  "s_add_u32 s62, s62, s60\n\t" \
  "s_cmp_gt_i32 s63, 0\n\t" \
  "s_cselect_b32 s65, s62, s65\n\t" \
  "s_mov_b32 m0, 0\n\t" \
  "s_movk_i32 s60, 8192\n\t" \
  "LWT_%=:\n\t" \
  "s_memrealtime s[62:63]\n\t" \
  "s_sub_u32 s60, s60, 1\n\t" \
  "s_cselect_b32 s61, 0, -1\n\t" \
  "s_waitcnt lgkmcnt(0)\n\t" \
  "s_sub_i32 s63, s62, s65\n\t" \
  "s_and_b32 s63, s63, s61\n\t" \
  "s_cmp_lt_i32 s63, " CH_FIN "\n\t" \
  "s_cbranch_scc1 LWT_%=\n\t" \
  "s_cmp_lt_i32 s63, 0\n\t" \
  "s_cbranch_scc1 LWT_%=f\n\t" \
  "LWT_%=g:\n\t" \
  "LDMA_%=:\n\t" \
  CH_DPC \
  "buffer_load_dwordx4 v78, s[28:31], s98 offen offset:0 sc1 lds\n\t" \
  "buffer_load_dwordx4 v78, s[28:31], s98 offen offset:1024 sc1 lds\n\t" \
  "buffer_load_dwordx4 v78, s[28:31], s98 offen offset:2048 sc1 lds\n\t" \
  "buffer_load_dwordx4 v78, s[28:31], s98 offen offset:3072 sc1 lds\n\t" \
  "s_movk_i32 m0, 4096\n\t" \
  "s_mov_b32 s66, s62\n\t" \
  CH_HNP \
  "buffer_load_dwordx4 v78, s[28:31], s97 offen offset:0 sc1 lds\n\t" \
  "buffer_load_dwordx4 v78, s[28:31], s97 offen offset:1024 sc1 lds\n\t" \
  "buffer_load_dwordx4 v78, s[28:31], s97 offen offset:2048 sc1 lds\n\t" \
  "buffer_load_dwordx4 v78, s[28:31], s97 offen offset:3072 sc1 lds\n\t" \
  "s_movk_i32 m0, 8192\n\t" \
  "s_nop 0\n\t" \
  "buffer_load_dwordx4 v69, s[28:31], s99 offen sc1 lds\n\t" \
  CH_ST1 \
  ".p2align 6\n\t" \
  "LG0_%=:\n\t" \
  "s_barrier\n\t" \
  "buffer_load_dwordx4 v[52:55], v69, s[28:31], s50 offen sc1\n\t" \
  "buffer_load_dwordx4 v[56:59], v69, s[28:31], s50 offen offset:1024 sc1\n\t" \
  "s_add_u32 s64, s50, 16384\n\t" \
  "s_movk_i32 m0, 11264\n\t" \
  "v_lshlrev_b32 v75, 1, v69\n\t" \
  "buffer_load_dword v75, s[28:31], s64 offen sc1 lds\n\t" \
  "s_memrealtime s[62:63]\n\t" \
  "s_waitcnt lgkmcnt(0)\n\t" \
  "s_sub_u32 s62, s62, " CH_TKL "\n\t" \
  "s_sub_i32 s63, s62, s65\n\t" \
  "s_cmp_gt_i32 s63, 0\n\t" \
  "s_cselect_b32 s62, s62, s65\n\t" \
  "s_sub_i32 s63, s62, s47\n\t" \
  "s_cmp_gt_i32 s63, " CH_LATE "\n\t" \
  "s_cselect_b32 s71, " CH_SCHW ", s46\n\t" \
  "s_add_u32 s61, s47, " CH_LATE "\n\t" \
  "s_cmp_gt_i32 s63, " CH_LATE "\n\t" \
  "s_cselect_b32 s62, s62, s61\n\t" \
  "s_add_u32 s71, s71, s53\n\t" \
  "s_add_u32 s60, s62, s81\n\t" \
  "s_add_u32 s60, s60, " CH_DL1L "\n\t" \
  "s_lshl_b32 s60, s60, 8\n\t" \
  "s_or_b32 s60, s60, " CH_B0 "\n\t" \
  "s_cmp_lt_u32 s48, " CH_ES "\n\t" \
  "s_cselect_b32 s61, " CH_EX ", 0\n\t" \
  "s_add_u32 s60, s60, s61\n\t" \
  "s_not_b32 s60, s60\n\t" \
  "v_mov_b32 v76, s60\n\t" \
  "s_lshr_b32 s60, s46, 31\n\t" \
  "s_or_b32 s60, s60, s68\n\t" \
  "s_cselect_b32 s25, 0, 1\n\t" \
  "s_mov_b64 exec, s[24:25]\n\t" \
  "buffer_atomic_umin v76, v69, s[28:31], s71 offen sc1\n\t" \
  "s_mov_b64 exec, -1\n\t" \
  "ds_read_b128 v[44:47], v70 offset:0\n\t" \
  "ds_read_b128 v[48:51], v70 offset:1024\n\t" \
  "ds_read_b128 v[60:63], v70 offset:2048\n\t" \
  "ds_read_b128 v[64:67], v70 offset:3072\n\t" \
  "v_mov_b64 v[36:37], 0\n\t" \
  "v_mov_b64 v[38:39], 0\n\t" \
  "v_mov_b64 v[40:41], 0\n\t" \
  "v_mov_b64 v[42:43], 0\n\t" \
  "v_lshrrev_b32 v71, 1, v69\n\t" \
  "v_lshrrev_b32 v72, 3, v69\n\t" \
  "v_and_b32 v72, 0x78, v72\n\t" \
  "v_and_b32 v74, 0x30, v69\n\t" \
  "v_lshl_add_u32 v72, v74, 3, v72\n\t" \
  "s_add_u32 s54, s53, s36\n\t" \
  "s_add_u32 s55, s54, 524288\n\t" \
  "s_add_u32 s54, s54, 0\n\t" \
  "s_add_u32 s75, s54, 262144\n\t" \
  "s_add_u32 s80, s53, s26\n\t" \
  "s_or_b32 s87, s53, 0x1fff80\n\t" \
  "s_and_b32 s87, s87, -128\n\t" \
  "s_add_u32 s87, s87, 0x200000\n\t" \
  "s_waitcnt vmcnt(9)\n\t" \
  "s_barrier\n\t" \
  CH_DP0 \
  "ds_read_b128 v[0:3], v69 offset:0\n\t" \
  "ds_read_b128 v[4:7], v69 offset:1024\n\t" \
  "ds_read_b128 v[8:11], v69 offset:2048\n\t" \
  "ds_read_b128 v[12:15], v69 offset:3072\n\t" \
  "s_waitcnt lgkmcnt(3)\n\t" \
  "v_smfmac_f32_16x16x64_bf16 v[36:39], v[0:3], v[44:51], v68\n\t" \
  "ds_read_b128 v[44:47], v70 offset:4096\n\t" \
  "v_smfmac_f32_16x16x64_bf16 v[40:43], v[0:3], v[60:67], v68\n\t" \
  "ds_read_b128 v[48:51], v70 offset:5120\n\t" \
  "s_waitcnt lgkmcnt(4)\n\t" \
  "v_smfmac_f32_16x16x64_bf16 v[36:39], v[4:7], a[32:39], v68\n\t" \
  "ds_read_b128 v[60:63], v70 offset:6144\n\t" \
  "v_smfmac_f32_16x16x64_bf16 v[40:43], v[4:7], a[40:47], v68\n\t" \
  "ds_read_b128 v[64:67], v70 offset:7168\n\t" \
  "s_waitcnt lgkmcnt(5)\n\t" \
  "v_smfmac_f32_16x16x64_bf16 v[36:39], v[8:11], a[80:87], v68\n\t" \
  "s_waitcnt vmcnt(4)\n\t" \
  "s_barrier\n\t" \
  CH_DP1 \
  CH_TG2 \
  "ds_read_b128 v[16:19], v69 offset:4096\n\t" \
  "v_smfmac_f32_16x16x64_bf16 v[40:43], v[8:11], a[88:95], v68\n\t" \
  "ds_read_b128 v[20:23], v69 offset:5120\n\t" \
  "s_waitcnt lgkmcnt(6)\n\t" \
  "v_smfmac_f32_16x16x64_bf16 v[36:39], v[12:15], a[120:127], v68\n\t" \
  "ds_read_b128 v[24:27], v69 offset:6144\n\t" \
  "s_waitcnt lgkmcnt(5)\n\t" \
  "v_smfmac_f32_16x16x64_bf16 v[40:43], v[12:15], v[44:51], v68\n\t" \
  "ds_read_b128 v[28:31], v69 offset:7168\n\t" \
  "s_waitcnt lgkmcnt(3)\n\t" \
  "v_smfmac_f32_16x16x64_bf16 v[36:39], v[16:19], a[160:167], v68\n\t" \
  "ds_read_b128 v[32:35], v69 offset:8192\n\t" \
  "v_smfmac_f32_16x16x64_bf16 v[40:43], v[16:19], a[168:175], v68\n\t" \
  "s_waitcnt lgkmcnt(3)\n\t" \
  "v_mov_b64 v[44:45], 0\n\t" \
  "v_smfmac_f32_16x16x64_bf16 v[36:39], v[20:23], a[200:207], v68\n\t" \
  "s_waitcnt lgkmcnt(2)\n\t" \
  "v_mov_b64 v[46:47], 0\n\t" \
  "v_smfmac_f32_16x16x64_bf16 v[36:39], v[24:27], a[240:247], v68\n\t" \
  "s_waitcnt lgkmcnt(1)\n\t" \
  "v_mov_b64 v[48:49], 0\n\t" \
  "v_smfmac_f32_16x16x64_bf16 v[36:39], v[28:31], v[112:119], v68\n\t" \
  "s_waitcnt lgkmcnt(0)\n\t" \
  "v_mov_b64 v[50:51], 0\n\t" \
  "v_smfmac_f32_16x16x64_bf16 v[36:39], v[32:35], v[160:167], v68\n\t" \
  "v_smfmac_f32_16x16x64_bf16 v[40:43], v[20:23], v[60:67], v68\n\t" \
  "ds_read_b128 v[60:63], v70 offset:8192\n\t" \
  "v_smfmac_f32_16x16x64_bf16 v[40:43], v[24:27], a[248:255], v68\n\t" \
  "ds_read_b128 v[64:67], v70 offset:9216\n\t" \
  "v_add_f32 v74, v36, v38\n\t" \
  "v_smfmac_f32_16x16x64_bf16 v[40:43], v[28:31], v[120:127], v68\n\t" \
  "v_add_f32 v37, v37, v39\n\t" \
  "v_smfmac_f32_16x16x64_bf16 v[40:43], v[32:35], v[168:175], v68\n\t" \
  "v_cmp_u_f32 vcc, v74, v37\n\t" \
  CH_DP2 \
  "v_smfmac_f32_16x16x64_bf16 v[44:47], v[0:3], a[0:7], v68\n\t" \
  "s_cbranch_vccnz LFAIL0_%=\n\t" \
  "LGO0_%=:\n\t" \
  "v_cvt_pk_bf16_f32 v74, v74, v37\n\t" \
  "v_smfmac_f32_16x16x64_bf16 v[44:47], v[4:7], a[48:55], v68\n\t" \
  "s_waitcnt lgkmcnt(0)\n\t" \
  "v_add_f32 v75, v40, v42\n\t" \
  "v_smfmac_f32_16x16x64_bf16 v[44:47], v[8:11], v[60:67], v68\n\t" \
  "ds_read_b128 v[60:63], v70 offset:10240\n\t" \
  "v_add_f32 v41, v41, v43\n\t" \
  "v_cvt_pk_bf16_f32 v75, v75, v41\n\t" \
  "v_smfmac_f32_16x16x64_bf16 v[44:47], v[12:15], a[128:135], v68\n\t" \
  "ds_write2st64_b32 v71, v74, v75 offset0:36 offset1:38\n\t" \
  "v_smfmac_f32_16x16x64_bf16 v[44:47], v[16:19], a[176:183], v68\n\t" \
  "ds_read_b128 v[64:67], v70 offset:11264\n\t" \
  "v_smfmac_f32_16x16x64_bf16 v[44:47], v[20:23], a[208:215], v68\n\t" \
  "v_smfmac_f32_16x16x64_bf16 v[44:47], v[24:27], v[80:87], v68\n\t" \
  "v_smfmac_f32_16x16x64_bf16 v[44:47], v[28:31], v[128:135], v68\n\t" \
  "s_waitcnt lgkmcnt(0)\n\t" \
  "v_smfmac_f32_16x16x64_bf16 v[44:47], v[32:35], v[60:67], v68\n\t" \
  "buffer_load_dwordx4 v[60:63], v69, s[28:31], s64 offen sc1\n\t" \
  "s_barrier\n\t" \
  "ds_read_b64_tr_b16 v[76:77], v72 offset:9216\n\t" \
  CH_DP3 \
  "v_smfmac_f32_16x16x64_bf16 v[48:51], v[0:3], a[8:15], v68\n\t" \
  "ds_read_b64_tr_b16 v[78:79], v72 offset:9728\n\t" \
  "v_smfmac_f32_16x16x64_bf16 v[48:51], v[4:7], a[56:63], v68\n\t" \
  "v_add_f32 v74, v44, v46\n\t" \
  "v_smfmac_f32_16x16x64_bf16 v[48:51], v[8:11], a[96:103], v68\n\t" \
  "v_add_f32 v45, v45, v47\n\t" \
  "v_smfmac_f32_16x16x64_bf16 v[48:51], v[12:15], a[136:143], v68\n\t" \
  "s_waitcnt vmcnt(4)\n\t" \
  "s_waitcnt lgkmcnt(0)\n\t" \
  "v_cvt_pk_bf16_f32 v74, v74, v45\n\t" \
  "v_mfma_f32_16x16x32_bf16 v[36:39], v[52:55], v[76:79], 0\n\t" \
  "buffer_load_dwordx4 v[64:67], v69, s[28:31], s64 offen offset:1024 sc1\n\t" \
  "s_waitcnt vmcnt(4)\n\t" \
  "v_mfma_f32_16x16x32_bf16 v[40:43], v[56:59], v[76:79], 0\n\t" \
  "v_mov_b64 v[52:53], 0\n\t" \
  "v_mov_b64 v[54:55], 0\n\t" \
  "v_smfmac_f32_16x16x64_bf16 v[48:51], v[16:19], a[184:191], v68\n\t" \
  "v_mov_b64 v[56:57], 0\n\t" \
  "v_mov_b64 v[58:59], 0\n\t" \
  "v_smfmac_f32_16x16x64_bf16 v[48:51], v[20:23], a[216:223], v68\n\t" \
  "ds_write_b32 v71, v74 offset:0\n\t" \
  "v_add_f32_dpp v36, v40, v36 quad_perm:[2,3,0,1] row_mask:0xf bank_mask:0xf\n\t" \
  "v_smfmac_f32_16x16x64_bf16 v[48:51], v[24:27], v[88:95], v68\n\t" \
  "v_add_f32_dpp v37, v41, v37 quad_perm:[2,3,0,1] row_mask:0xf bank_mask:0xf\n\t" \
  "v_smfmac_f32_16x16x64_bf16 v[48:51], v[28:31], v[136:143], v68\n\t" \
  "v_add_f32_dpp v38, v42, v38 quad_perm:[2,3,0,1] row_mask:0xf bank_mask:0xf\n\t" \
  "v_smfmac_f32_16x16x64_bf16 v[48:51], v[32:35], v[176:183], v68\n\t" \
  "v_add_f32_dpp v39, v43, v39 quad_perm:[2,3,0,1] row_mask:0xf bank_mask:0xf\n\t" \
  CH_DP4 \
  "v_smfmac_f32_16x16x64_bf16 v[52:55], v[0:3], a[16:23], v68\n\t" \
  "s_mov_b64 exec, s[58:59]\n\t" \
  "ds_write_b128 v69, v[36:39] offset:2048\n\t" \
  "s_mov_b64 exec, -1\n\t" \
  "v_smfmac_f32_16x16x64_bf16 v[52:55], v[4:7], a[64:71], v68\n\t" \
  "v_add_f32 v75, v48, v50\n\t" \
  "v_add_f32 v49, v49, v51\n\t" \
  "v_cvt_pk_bf16_f32 v75, v75, v49\n\t" \
  "v_smfmac_f32_16x16x64_bf16 v[56:59], v[0:3], a[24:31], v68\n\t" \
  "ds_write_b32 v71, v75 offset:512\n\t" \
  "v_smfmac_f32_16x16x64_bf16 v[56:59], v[4:7], a[72:79], v68\n\t" \
  "ds_read_b64_tr_b16 v[0:1], v72 offset:10240\n\t" \
  "ds_read_b64_tr_b16 v[2:3], v72 offset:10752\n\t" \
  "v_smfmac_f32_16x16x64_bf16 v[52:55], v[8:11], a[104:111], v68\n\t" \
  "ds_read_b128 v[36:39], v70 offset:12288\n\t" \
  "v_smfmac_f32_16x16x64_bf16 v[56:59], v[8:11], a[112:119], v68\n\t" \
  "buffer_load_dword off, s[28:31], s87 sc1 lds\n\t" \
  "ds_read_b128 v[40:43], v70 offset:13312\n\t" \
  "v_smfmac_f32_16x16x64_bf16 v[52:55], v[12:15], a[144:151], v68\n\t" \
  "s_waitcnt lgkmcnt(4)\n\t" \
  "s_barrier\n\t" \
  CH_DP5 \
  CH_TG5 \
  "ds_read_b64_tr_b16 v[44:45], v72 offset:0\n\t" \
  "ds_read_b64_tr_b16 v[46:47], v72 offset:512\n\t" \
  "v_smfmac_f32_16x16x64_bf16 v[56:59], v[12:15], a[152:159], v68\n\t" \
  "ds_read_b64_tr_b16 v[48:49], v72 offset:1024\n\t" \
  "ds_read_b64_tr_b16 v[50:51], v72 offset:1536\n\t" \
  "v_smfmac_f32_16x16x64_bf16 v[52:55], v[16:19], a[192:199], v68\n\t" \
  "ds_read_b128 v[4:7], v69 offset:2048\n\t" \
  "s_waitcnt lgkmcnt(5)\n\t" \
  "v_smfmac_f32_16x16x64_bf16 v[56:59], v[16:19], v[36:43], v68\n\t" \
  "ds_read_b128 v[8:11], v69 offset:3072\n\t" \
  "s_waitcnt lgkmcnt(4)\n\t" \
  "v_mfma_f32_16x16x32_bf16 v[12:15], v[44:47], v[76:79], 0\n\t" \
  "ds_read_b128 v[36:39], v70 offset:14336\n\t" \
  "s_waitcnt lgkmcnt(3)\n\t" \
  "v_mfma_f32_16x16x32_bf16 v[12:15], v[48:51], v[0:3], v[12:15]\n\t" \
  "ds_read_b128 v[40:43], v70 offset:15360\n\t" \
  "v_smfmac_f32_16x16x64_bf16 v[52:55], v[20:23], a[224:231], v68\n\t" \
  "s_waitcnt lgkmcnt(2)\n\t" \
  "v_add_f32 v4, v4, v8\n\t" \
  "v_add_f32 v5, v5, v9\n\t" \
  "v_add_f32 v6, v6, v10\n\t" \
  CH_DP6 \
  "v_cndmask_b32 v78, v13, v14, s[56:57]\n\t" \
  "v_cndmask_b32 v76, v12, v15, s[56:57]\n\t" \
  "v_add_f32 v7, v7, v11\n\t" \
  "v_max3_f32 v77, v4, v5, v6\n\t" \
  "v_add_f32_dpp v76, v78, v76 quad_perm:[2,3,0,1] row_mask:0xf bank_mask:0xf\n\t" \
  "v_max3_f32 v77, v77, v7, v76\n\t" \
  "v_smfmac_f32_16x16x64_bf16 v[56:59], v[20:23], a[232:239], v68\n\t" \
  "ds_read_b128 v[44:47], v70 offset:16384\n\t" \
  "v_max_f32_dpp v77, v77, v77 quad_perm:[2,3,0,1] row_mask:0xf bank_mask:0xf\n\t" \
  "v_mov_b32 v78, v77\n\t" \
  "v_smfmac_f32_16x16x64_bf16 v[52:55], v[24:27], v[96:103], v68\n\t" \
  "ds_read_b128 v[48:51], v70 offset:17408\n\t" \
  "v_permlane16_swap_b32 v77, v78\n\t" \
  "v_max_f32 v77, v77, v78\n\t" \
  "v_mov_b32 v78, v77\n\t" \
  "v_smfmac_f32_16x16x64_bf16 v[52:55], v[28:31], v[144:151], v68\n\t" \
  "ds_read_b128 v[16:19], v70 offset:18432\n\t" \
  "v_permlane32_swap_b32 v77, v78\n\t" \
  "v_max_f32 v77, v77, v78\n\t" \
  "v_smfmac_f32_16x16x64_bf16 v[52:55], v[32:35], v[184:191], v68\n\t" \
  "ds_read_b128 v[20:23], v70 offset:19456\n\t" \
  "v_mul_f32 v77, 0xbe0293ee, v77\n\t" \
  CH_DP7 \
  "v_smfmac_f32_16x16x64_bf16 v[56:59], v[24:27], v[104:111], v68\n\t" \
  "v_fmamk_f32 v4, v4, 0x3e0293ee, v77\n\t" \
  "v_smfmac_f32_16x16x64_bf16 v[56:59], v[28:31], v[152:159], v68\n\t" \
  "ds_read_b128 v[24:27], v70 offset:22528\n\t" \
  "s_waitcnt lgkmcnt(4)\n\t" \
  "v_fmamk_f32 v5, v5, 0x3e0293ee, v77\n\t" \
  "v_smfmac_f32_16x16x64_bf16 v[56:59], v[32:35], v[36:43], v68\n\t" \
  "ds_read_b128 v[28:31], v70 offset:23552\n\t" \
  "v_fmamk_f32 v6, v6, 0x3e0293ee, v77\n\t" \
  "v_fmamk_f32 v7, v7, 0x3e0293ee, v77\n\t" \
  "v_fmamk_f32 v76, v76, 0x3e0293ee, v77\n\t" \
  "v_exp_f32 v4, v4\n\t" \
  "v_exp_f32 v5, v5\n\t" \
  "v_exp_f32 v6, v6\n\t" \
  "v_exp_f32 v7, v7\n\t" \
  "v_cvt_pk_bf16_f32 v4, v4, v5\n\t" \
  "v_exp_f32 v76, v76\n\t" \
  "v_cvt_pk_bf16_f32 v5, v6, v7\n\t" \
  "v_mov_b32_dpp v12, v4 quad_perm:[0,1,0,1] row_mask:0xf bank_mask:0xf\n\t" \
  "v_mov_b32_dpp v14, v4 quad_perm:[2,3,2,3] row_mask:0xf bank_mask:0xf\n\t" \
  "v_mov_b32_dpp v13, v5 quad_perm:[0,1,0,1] row_mask:0xf bank_mask:0xf\n\t" \
  "v_mov_b32_dpp v15, v5 quad_perm:[2,3,2,3] row_mask:0xf bank_mask:0xf\n\t" \
  "v_add_f32 v52, v52, v54\n\t" \
  "v_add_f32 v53, v53, v55\n\t" \
  "s_waitcnt vmcnt(2)\n\t" \
  CH_DP8 \
  "v_mfma_f32_16x16x32_bf16 v[0:3], v[60:63], v[12:15], 0\n\t" \
  "ds_read_b128 v[36:39], v70 offset:20480\n\t" \
  "v_add_f32 v56, v56, v58\n\t" \
  "v_add_f32 v57, v57, v59\n\t" \
  "v_cvt_pk_bf16_f32 v52, v52, v53\n\t" \
  "v_cvt_pk_bf16_f32 v53, v56, v57\n\t" \
  "s_waitcnt vmcnt(1)\n\t" \
  "v_mfma_f32_16x16x32_bf16 v[4:7], v[64:67], v[12:15], 0\n\t" \
  "buffer_store_dwordx2 v[52:53], v71, s[28:31], s69 offen offset:2048 sc1\n\t" \
  "ds_read_b128 v[40:43], v70 offset:21504\n\t" \
  "v_mov_b64 v[56:57], s[88:89]\n\t" \
  "v_mov_b64 v[58:59], s[88:89]\n\t" \
  "v_mov_b32_dpp v78, v76 quad_perm:[0,1,0,1] row_mask:0xf bank_mask:0xf\n\t" \
  "v_mov_b32_dpp v79, v76 quad_perm:[2,3,2,3] row_mask:0xf bank_mask:0xf\n\t" \
  "v_mfma_f32_16x16x32_bf16 v[32:35], v[56:59], v[12:15], 0\n\t" \
  "ds_read_b128 v[60:63], v70 offset:24576\n\t" \
  "v_cvt_pk_bf16_f32 v11, v78, v79\n\t" \
  "v_cvt_pk_bf16_f32 v8, v78, v79\n\t" \
  "v_mov_b64 v[54:55], 0\n\t" \
  "v_mov_b32 v9, 0\n\t" \
  "v_mov_b32 v10, 0\n\t" \
  "v_mov_b64 v[14:15], 0\n\t" \
  "s_nop 0\n\t" \
  "v_mfma_f32_16x16x32_bf16 v[32:35], v[54:57], v[8:11], v[32:35]\n\t" \
  "s_cmp_eq_u32 s48, 63\n\t" \
  "v_mfma_f32_16x16x32_bf16 v[0:3], v[52:55], v[8:11], v[0:3]\n\t" \
  "ds_read_b128 v[64:67], v70 offset:25600\n\t" \
  "v_mov_b64 v[56:57], 0\n\t" \
  "v_mov_b64 v[58:59], 0\n\t" \
  "v_mfma_f32_16x16x32_bf16 v[4:7], v[52:55], v[10:13], v[4:7]\n\t" \
  "s_cbranch_scc1 LLAST0_%=\n\t" \
  "v_rcp_f32 v74, v32\n\t" \
  "v_mov_b64 v[12:13], 0\n\t" \
  "v_mov_b32 v8, 0\n\t" \
  "v_pk_mul_f32 v[0:1], v[0:1], v[74:75] op_sel_hi:[1,0]\n\t" \
  "v_pk_mul_f32 v[2:3], v[2:3], v[74:75] op_sel_hi:[1,0]\n\t" \
  "v_cvt_pk_bf16_f32 v76, v0, v1\n\t" \
  "v_cvt_pk_bf16_f32 v77, v2, v3\n\t" \
  "v_pk_mul_f32 v[4:5], v[4:5], v[74:75] op_sel_hi:[1,0]\n\t" \
  "v_pk_mul_f32 v[6:7], v[6:7], v[74:75] op_sel_hi:[1,0]\n\t" \
  "v_cvt_pk_bf16_f32 v78, v4, v5\n\t" \
  "v_cvt_pk_bf16_f32 v79, v6, v7\n\t" \
  "ds_write_b128 v69, v[76:79] offset:4096\n\t" \
  CH_DP9 \
  "s_waitcnt lgkmcnt(2)\n\t" \
  "v_smfmac_f32_16x16x64_bf16 v[12:15], v[76:79], v[44:51], v68\n\t" \
  "ds_read_b128 v[44:47], v70 offset:26624\n\t" \
  "v_mov_b32 v11, 0\n\t" \
  "v_smfmac_f32_16x16x64_bf16 v[56:59], v[76:79], v[16:23], v68\n\t" \
  "ds_read_b128 v[48:51], v70 offset:27648\n\t" \
  "v_mov_b64 v[52:53], 0\n\t" \
  "v_smfmac_f32_16x16x64_bf16 v[8:11], v[76:79], v[36:43], v68\n\t" \
  "ds_read_b128 v[16:19], v70 offset:28672\n\t" \
  "v_smfmac_f32_16x16x64_bf16 v[52:55], v[76:79], v[24:31], v68\n\t" \
  "s_waitcnt lgkmcnt(3)\n\t" \
  "s_barrier\n\t" \
  CH_DP10 \
  "ds_read_b128 v[0:3], v69 offset:5120\n\t" \
  "ds_read_b128 v[4:7], v69 offset:6144\n\t" \
  "v_lshrrev_b32 v71, 7, v69\n\t" \
  "v_and_b32 v74, 0x70, v69\n\t" \
  "v_lshl_add_u32 v71, v71, 14, v74\n\t" \
  "v_bfe_u32 v72, v69, 2, 7\n\t" \
  "v_and_b32 v74, 0x200, v69\n\t" \
  "s_waitcnt lgkmcnt(1)\n\t" \
  "v_smfmac_f32_16x16x64_bf16 v[8:11], v[0:3], v[60:67], v68\n\t" \
  "ds_read_b128 v[20:23], v70 offset:29696\n\t" \
  "v_mov_b64 v[76:77], 0\n\t" \
  "v_smfmac_f32_16x16x64_bf16 v[12:15], v[0:3], v[200:207], v68\n\t" \
  "ds_read_b128 v[32:35], v69 offset:7168\n\t" \
  "v_mov_b64 v[78:79], 0\n\t" \
  "v_smfmac_f32_16x16x64_bf16 v[52:55], v[0:3], v[44:51], v68\n\t" \
  "ds_read_b128 v[24:27], v70 offset:30720\n\t" \
  "v_lshl_add_u32 v72, v74, 3, v72\n\t" \
  "v_smfmac_f32_16x16x64_bf16 v[56:59], v[0:3], v[232:239], v68\n\t" \
  "buffer_load_dword v75, off, s[28:31], s80 sc1\n\t" \
  "ds_read_b128 v[28:31], v70 offset:31744\n\t" \
  "s_waitcnt lgkmcnt(4)\n\t" \
  "v_smfmac_f32_16x16x64_bf16 v[12:15], v[4:7], v[208:215], v68\n\t" \
  "ds_read_b128 v[60:63], v70 offset:32768\n\t" \
  "s_waitcnt lgkmcnt(4)\n\t" \
  "v_smfmac_f32_16x16x64_bf16 v[8:11], v[4:7], v[16:23], v68\n\t" \
  "ds_read_b128 v[64:67], v70 offset:33792\n\t" \
  "s_waitcnt lgkmcnt(4)\n\t" \
  "v_smfmac_f32_16x16x64_bf16 v[8:11], v[32:35], v[192:199], v68\n\t" \
  "ds_read_b128 v[36:39], v73 offset:4096\n\t" \
  "v_smfmac_f32_16x16x64_bf16 v[12:15], v[32:35], v[216:223], v68\n\t" \
  "ds_read_b128 v[40:43], v73 offset:6144\n\t" \
  "s_waitcnt lgkmcnt(4)\n\t" \
  "v_smfmac_f32_16x16x64_bf16 v[52:55], v[4:7], v[24:31], v68\n\t" \
  "ds_read_b128 v[44:47], v70 offset:34816\n\t" \
  "v_cvt_pk_bf16_f32 v8, v8, v9\n\t" \
  "v_smfmac_f32_16x16x64_bf16 v[52:55], v[32:35], v[224:231], v68\n\t" \
  "ds_read_b128 v[48:51], v70 offset:35840\n\t" \
  "v_cvt_pk_bf16_f32 v9, v10, v11\n\t" \
  "v_smfmac_f32_16x16x64_bf16 v[56:59], v[4:7], v[240:247], v68\n\t" \
  "s_waitcnt lgkmcnt(4)\n\t" \
  "v_cvt_pk_bf16_f32 v10, v12, v13\n\t" \
  "v_smfmac_f32_16x16x64_bf16 v[56:59], v[32:35], v[60:67], v68\n\t" \
  "s_waitcnt lgkmcnt(3)\n\t" \
  "v_cvt_pk_bf16_f32 v11, v14, v15\n\t" \
  "v_smfmac_f32_16x16x64_bf16 v[76:79], v[36:39], v[248:255], v68\n\t" \
  "buffer_store_dwordx4 v[8:11], v71, s[28:31], s54 offen sc1\n\t" \
  "s_waitcnt lgkmcnt(0)\n\t" \
  CH_DP11 \
  "v_cvt_pk_bf16_f32 v52, v52, v53\n\t" \
  "v_smfmac_f32_16x16x64_bf16 v[76:79], v[40:43], v[44:51], v68\n\t" \
  "v_cvt_pk_bf16_f32 v53, v54, v55\n\t" \
  "v_cvt_pk_bf16_f32 v54, v56, v57\n\t" \
  "v_cvt_pk_bf16_f32 v55, v58, v59\n\t" \
  "buffer_store_dwordx4 v[52:55], v71, s[28:31], s75 offen sc1\n\t" \
  "s_nop 2\n\t" \
  "v_add_f32 v76, v76, v78\n\t" \
  "v_add_f32 v77, v77, v79\n\t" \
  "v_cvt_pk_bf16_f32 v76, v76, v77\n\t" \
  "buffer_store_dword v76, v72, s[28:31], s55 offen sc1\n\t" \
  CH_DP12 \
  "s_memrealtime s[70:71]\n\t" \
  "s_waitcnt vmcnt(3)\n\t" \
  "v_readfirstlane_b32 s65, v75\n\t" \
  "s_barrier\n\t" \
  "s_waitcnt lgkmcnt(0)\n\t" \
  "s_not_b32 s65, s65\n\t" \
  "s_lshl_b32 s61, s70, 8\n\t" \
  "s_andn2_b32 s64, s65, 0xff\n\t" \
  "s_sub_u32 s64, s64, s61\n\t" \
  "s_ashr_i32 s64, s64, 8\n\t" \
  "s_cmp_eq_u32 s65, 0\n\t" \
  "s_cselect_b32 s64, " CH_DLF ", s64\n\t" \
  "s_cselect_b32 s65, " CH_B0 ", s65\n\t" \
  "s_add_u32 s47, s64, s70\n\t" \
  "s_min_i32 s64, s64, s34\n\t" \
  "s_max_i32 s64, s64, " CH_DLL1 "\n\t" \
  "s_add_u32 s64, s64, s70\n\t" \
  CH_ST2 \
  "s_add_u32 s55, s64, " CH_FDL "\n\t" \
  "s_movk_i32 s60, 8192\n\t" \
  "FWT_%=:\n\t" \
  "s_memrealtime s[62:63]\n\t" \
  "s_sub_u32 s60, s60, 1\n\t" \
  "s_cselect_b32 s61, 0, -1\n\t" \
  "s_waitcnt lgkmcnt(0)\n\t" \
  "s_sub_i32 s63, s62, s55\n\t" \
  "s_and_b32 s63, s63, s61\n\t" \
  "s_cmp_lt_i32 s63, 0\n\t" \
  "s_cbranch_scc1 FWT_%=\n\t" \
  "s_mov_b64 exec, 1\n\t" \
  "buffer_store_dword v68, off, s[28:31], s51 offset:4064 sc1\n\t" \
  "s_mov_b64 exec, -1\n\t" \
  "s_and_b32 s65, s65, 0xff\n\t" \
  "s_add_u32 s47, s47, s65\n\t" \
  "s_add_u32 s65, s65, s64\n\t" \
  "s_sub_u32 s60, s70, s66\n\t" \
  "s_add_u32 s61, s81, " CH_UP "\n\t" \
  "s_min_u32 s60, s60, s61\n\t" \
  "s_sub_u32 s61, s81, " CH_DN "\n\t" \
  "s_max_u32 s60, s60, s61\n\t" \
  "s_cmp_eq_u32 s68, 0\n\t" \
  "s_cselect_b32 s81, s60, s81\n\t" \
  CH_TRC \
  "s_add_u32 s48, s48, 1\n\t" \
  "s_mov_b32 s49, s53\n\t" \
  "s_add_u32 s53, s53, " CH_SLOT "\n\t" \
  "s_add_u32 s60, s53, " CH_SKIP "\n\t" \
  "s_lshr_b32 s61, s96, 1\n\t" \
  "s_add_u32 s86, s86, 1\n\t" \
  "s_cmp_eq_u32 s86, 3\n\t" \
  "s_cselect_b32 s53, s60, s53\n\t" \
  "s_cselect_b32 s96, s61, s96\n\t" \
  "s_cselect_b32 s86, 0, s86\n\t" \
  "s_cmp_ge_u32 s53, " CH_RING "\n\t" \
  "s_cselect_b32 s53, 0, s53\n\t" \
  "s_cselect_b32 s96, s21, s96\n\t" \
  "s_andn2_b32 s53, s53, 0x2000\n\t" \
  "s_and_b32 s60, s96, 1\n\t" \
  "s_lshl_b32 s60, s60, 13\n\t" \
  "s_or_b32 s53, s53, s60\n\t" \
  "s_mov_b32 s68, 0\n\t" \
  "s_add_u32 s82, s48, 0\n\t" \
  "s_and_b32 s82, s82, " CH_TRM "\n\t" \
  CH_SEGC \
  "s_lshr_b64 s[60:61], s[90:91], s82\n\t" \
  "s_and_b32 s60, s60, 1\n\t" \
  "s_lshl_b32 s60, s60, 13\n\t" \
  "s_add_u32 s82, s82, " CH_SG0 "\n\t" \
  "s_lshl_b32 s82, s82, 21\n\t" \
  "s_or_b32 s82, s82, s60\n\t" \
  "s_add_u32 s50, s82, s38\n\t" \
  "s_add_u32 s69, s82, s39\n\t" \
  "s_add_u32 s82, s48, 0\n\t" \
  CH_SEGC \
  "s_lshr_b64 s[60:61], s[90:91], s82\n\t" \
  "s_and_b32 s60, s60, 1\n\t" \
  "s_lshl_b32 s60, s60, 13\n\t" \
  "s_add_u32 s82, s82, " CH_SG0 "\n\t" \
  "s_lshl_b32 s82, s82, 21\n\t" \
  "s_or_b32 s82, s82, s60\n\t" \
  "s_add_u32 s51, s82, s43\n\t" \
  "s_cmp_lg_u32 s34, 0\n\t" \
  "s_cbranch_scc1 LW0_%=\n\t" \
  "s_barrier\n\t" \
  "s_branch LINI_%=\n\t" \
  "LFAIL0_%=:\n\t" \
  "v_max_u32 v75, v0, v1\n\t" \
  "v_max3_u32 v75, v75, v2, v3\n\t" \
  "v_max3_u32 v75, v75, v4, v5\n\t" \
  "v_max3_u32 v75, v75, v6, v7\n\t" \
  "v_max3_u32 v75, v75, v8, v9\n\t" \
  "v_max3_u32 v75, v75, v10, v11\n\t" \
  "v_max3_u32 v75, v75, v12, v13\n\t" \
  "v_max3_u32 v75, v75, v14, v15\n\t" \
  "v_max3_u32 v75, v75, v16, v17\n\t" \
  "v_max3_u32 v75, v75, v18, v19\n\t" \
  "v_max3_u32 v75, v75, v20, v21\n\t" \
  "v_max3_u32 v75, v75, v22, v23\n\t" \
  "v_max3_u32 v75, v75, v24, v25\n\t" \
  "v_max3_u32 v75, v75, v26, v27\n\t" \
  "v_max3_u32 v75, v75, v28, v29\n\t" \
  "v_max3_u32 v75, v75, v30, v31\n\t" \
  "v_max3_u32 v75, v75, v32, v33\n\t" \
  "v_max3_u32 v75, v75, v34, v35\n\t" \
  "v_cmp_eq_u32 vcc, -1, v75\n\t" \
  "s_cbranch_vccz LGO0_%=\n\t" \
  "s_add_u32 s68, s68, 1\n\t" \
  "s_waitcnt lgkmcnt(0)\n\t" \
  "s_cmp_gt_u32 s68, s83\n\t" \
  "s_cbranch_scc1 LGV0_%=\n\t" \
  "s_barrier\n\t" \
  "s_memrealtime s[62:63]\n\t" \
  "s_mov_b32 m0, 0\n\t" \
  "s_waitcnt lgkmcnt(0)\n\t" \
  "s_add_u32 s65, s62, " CH_PEN "\n\t" \
  "s_branch LDMA_%=\n\t" \
  "LWT_%=f:\n\t" \
  "s_cmp_lt_i32 s63, -2\n\t" \
  "s_cbranch_scc0 LWT_%=g\n\t" \
  "s_nop 4\n\t" \
  "s_branch LWT_%=g\n\t" \
  "LGV0_%=:\n\t" \
  "s_mov_b32 s83, 0\n\t" \
  "s_lshl_b32 s60, s36, 4\n\t" \
  "s_or_b32 s60, s60, s48\n\t" \
  "s_or_b32 s60, s60, 805306368\n\t" \
  "v_mov_b32 v76, s60\n\t" \
  "v_mov_b32 v77, 0\n\t" \
  "s_mov_b64 exec, 1\n\t" \
  "buffer_atomic_cmpswap v[76:77], off, s[28:31], s92 sc1\n\t" \
  "s_mov_b64 exec, -1\n\t" \
  "s_branch LGO0_%=\n\t" \
  "LLAST0_%=:\n\t" \
  "s_waitcnt vmcnt(0) lgkmcnt(0)\n\t" \
  "s_barrier\n\t" \
  "s_mov_b64 exec, 1\n\t" \
  "buffer_store_dword v68, off, s[28:31], s51 offset:4064 sc1\n\t" \
  "s_mov_b64 exec, -1\n\t" \
  "s_branch LEND_%=\n\t" \
  ".p2align 6\n\t" \
  "LG1_%=:\n\t" \
  "s_barrier\n\t" \
  "buffer_load_dwordx4 v[52:55], v69, s[28:31], s50 offen sc1\n\t" \
  "buffer_load_dwordx4 v[56:59], v69, s[28:31], s50 offen offset:1024 sc1\n\t" \
  "s_add_u32 s64, s50, 14336\n\t" \
  "s_movk_i32 m0, 11264\n\t" \
  "v_lshlrev_b32 v75, 1, v69\n\t" \
  "buffer_load_dword v75, s[28:31], s64 offen sc1 lds\n\t" \
  "ds_read_b128 v[44:47], v70 offset:0\n\t" \
  "ds_read_b128 v[48:51], v70 offset:1024\n\t" \
  "ds_read_b128 v[60:63], v70 offset:2048\n\t" \
  "ds_read_b128 v[64:67], v70 offset:3072\n\t" \
  "v_mov_b64 v[36:37], 0\n\t" \
  "v_mov_b64 v[38:39], 0\n\t" \
  "v_mov_b64 v[40:41], 0\n\t" \
  "v_mov_b64 v[42:43], 0\n\t" \
  "v_lshrrev_b32 v71, 1, v69\n\t" \
  "v_add_u32 v71, 4, v71\n\t" \
  "v_lshrrev_b32 v72, 3, v69\n\t" \
  "v_and_b32 v72, 0x78, v72\n\t" \
  "v_and_b32 v74, 0x30, v69\n\t" \
  "v_lshl_add_u32 v72, v74, 3, v72\n\t" \
  "s_add_u32 s54, s53, s36\n\t" \
  "s_add_u32 s55, s54, 540672\n\t" \
  "s_add_u32 s54, s54, 4096\n\t" \
  "s_add_u32 s75, s54, 262144\n\t" \
  "s_add_u32 s80, s53, s26\n\t" \
  "s_barrier\n\t" \
  CH_DP0 \
  "ds_read_b128 v[0:3], v69 offset:0\n\t" \
  "ds_read_b128 v[4:7], v69 offset:1024\n\t" \
  "ds_read_b128 v[8:11], v69 offset:2048\n\t" \
  "ds_read_b128 v[12:15], v69 offset:3072\n\t" \
  "s_waitcnt lgkmcnt(3)\n\t" \
  "v_smfmac_f32_16x16x64_bf16 v[36:39], v[0:3], v[44:51], v68\n\t" \
  "ds_read_b128 v[44:47], v70 offset:4096\n\t" \
  "v_smfmac_f32_16x16x64_bf16 v[40:43], v[0:3], v[60:67], v68\n\t" \
  "ds_read_b128 v[48:51], v70 offset:5120\n\t" \
  "s_waitcnt lgkmcnt(4)\n\t" \
  "v_smfmac_f32_16x16x64_bf16 v[36:39], v[4:7], a[32:39], v68\n\t" \
  "ds_read_b128 v[60:63], v70 offset:6144\n\t" \
  "v_smfmac_f32_16x16x64_bf16 v[40:43], v[4:7], a[40:47], v68\n\t" \
  "ds_read_b128 v[64:67], v70 offset:7168\n\t" \
  "s_waitcnt lgkmcnt(5)\n\t" \
  "v_smfmac_f32_16x16x64_bf16 v[36:39], v[8:11], a[80:87], v68\n\t" \
  "s_barrier\n\t" \
  CH_DP1 \
  "ds_read_b128 v[16:19], v69 offset:4096\n\t" \
  "v_smfmac_f32_16x16x64_bf16 v[40:43], v[8:11], a[88:95], v68\n\t" \
  "ds_read_b128 v[20:23], v69 offset:5120\n\t" \
  "s_waitcnt lgkmcnt(6)\n\t" \
  "v_smfmac_f32_16x16x64_bf16 v[36:39], v[12:15], a[120:127], v68\n\t" \
  "ds_read_b128 v[24:27], v69 offset:6144\n\t" \
  "s_waitcnt lgkmcnt(5)\n\t" \
  "v_smfmac_f32_16x16x64_bf16 v[40:43], v[12:15], v[44:51], v68\n\t" \
  "ds_read_b128 v[28:31], v69 offset:7168\n\t" \
  "s_waitcnt lgkmcnt(3)\n\t" \
  "v_smfmac_f32_16x16x64_bf16 v[36:39], v[16:19], a[160:167], v68\n\t" \
  "ds_read_b128 v[32:35], v69 offset:8192\n\t" \
  "v_smfmac_f32_16x16x64_bf16 v[40:43], v[16:19], a[168:175], v68\n\t" \
  "s_waitcnt lgkmcnt(3)\n\t" \
  "v_mov_b64 v[44:45], 0\n\t" \
  "v_smfmac_f32_16x16x64_bf16 v[36:39], v[20:23], a[200:207], v68\n\t" \
  "s_waitcnt lgkmcnt(2)\n\t" \
  "v_mov_b64 v[46:47], 0\n\t" \
  "v_smfmac_f32_16x16x64_bf16 v[36:39], v[24:27], a[240:247], v68\n\t" \
  "s_waitcnt lgkmcnt(1)\n\t" \
  "v_mov_b64 v[48:49], 0\n\t" \
  "v_smfmac_f32_16x16x64_bf16 v[36:39], v[28:31], v[112:119], v68\n\t" \
  "s_waitcnt lgkmcnt(0)\n\t" \
  "v_mov_b64 v[50:51], 0\n\t" \
  "v_smfmac_f32_16x16x64_bf16 v[36:39], v[32:35], v[160:167], v68\n\t" \
  "v_smfmac_f32_16x16x64_bf16 v[40:43], v[20:23], v[60:67], v68\n\t" \
  "ds_read_b128 v[60:63], v70 offset:8192\n\t" \
  "v_smfmac_f32_16x16x64_bf16 v[40:43], v[24:27], a[248:255], v68\n\t" \
  "ds_read_b128 v[64:67], v70 offset:9216\n\t" \
  "v_add_f32 v74, v36, v38\n\t" \
  "v_smfmac_f32_16x16x64_bf16 v[40:43], v[28:31], v[120:127], v68\n\t" \
  "v_add_f32 v37, v37, v39\n\t" \
  "v_smfmac_f32_16x16x64_bf16 v[40:43], v[32:35], v[168:175], v68\n\t" \
  "v_cmp_u_f32 vcc, v74, v37\n\t" \
  CH_DP2 \
  "v_smfmac_f32_16x16x64_bf16 v[44:47], v[0:3], a[0:7], v68\n\t" \
  "s_cbranch_vccnz LFAIL1_%=\n\t" \
  "LGO1_%=:\n\t" \
  "v_cvt_pk_bf16_f32 v74, v74, v37\n\t" \
  "v_smfmac_f32_16x16x64_bf16 v[44:47], v[4:7], a[48:55], v68\n\t" \
  "s_waitcnt lgkmcnt(0)\n\t" \
  "v_add_f32 v75, v40, v42\n\t" \
  "v_smfmac_f32_16x16x64_bf16 v[44:47], v[8:11], v[60:67], v68\n\t" \
  "ds_read_b128 v[60:63], v70 offset:10240\n\t" \
  "v_add_f32 v41, v41, v43\n\t" \
  "v_cvt_pk_bf16_f32 v75, v75, v41\n\t" \
  "v_smfmac_f32_16x16x64_bf16 v[44:47], v[12:15], a[128:135], v68\n\t" \
  "ds_write2st64_b32 v71, v74, v75 offset0:36 offset1:38\n\t" \
  "v_smfmac_f32_16x16x64_bf16 v[44:47], v[16:19], a[176:183], v68\n\t" \
  "ds_read_b128 v[64:67], v70 offset:11264\n\t" \
  "v_smfmac_f32_16x16x64_bf16 v[44:47], v[20:23], a[208:215], v68\n\t" \
  "v_smfmac_f32_16x16x64_bf16 v[44:47], v[24:27], v[80:87], v68\n\t" \
  "v_smfmac_f32_16x16x64_bf16 v[44:47], v[28:31], v[128:135], v68\n\t" \
  "s_waitcnt lgkmcnt(0)\n\t" \
  "v_smfmac_f32_16x16x64_bf16 v[44:47], v[32:35], v[60:67], v68\n\t" \
  "s_barrier\n\t" \
  "ds_read_b64_tr_b16 v[76:77], v72 offset:9216\n\t" \
  CH_DP3 \
  "v_smfmac_f32_16x16x64_bf16 v[48:51], v[0:3], a[8:15], v68\n\t" \
  "buffer_load_dwordx4 v[60:63], v69, s[28:31], s64 offen sc1\n\t" \
  "ds_read_b64_tr_b16 v[78:79], v72 offset:9728\n\t" \
  "v_smfmac_f32_16x16x64_bf16 v[48:51], v[4:7], a[56:63], v68\n\t" \
  "v_add_f32 v74, v44, v46\n\t" \
  "v_smfmac_f32_16x16x64_bf16 v[48:51], v[8:11], a[96:103], v68\n\t" \
  "v_add_f32 v45, v45, v47\n\t" \
  "v_smfmac_f32_16x16x64_bf16 v[48:51], v[12:15], a[136:143], v68\n\t" \
  "s_waitcnt vmcnt(3)\n\t" \
  "s_waitcnt lgkmcnt(0)\n\t" \
  "v_cvt_pk_bf16_f32 v74, v74, v45\n\t" \
  "v_mfma_f32_16x16x32_bf16 v[36:39], v[52:55], v[76:79], 0\n\t" \
  "s_waitcnt vmcnt(2)\n\t" \
  "v_mfma_f32_16x16x32_bf16 v[40:43], v[56:59], v[76:79], 0\n\t" \
  "buffer_load_dwordx4 v[64:67], v69, s[28:31], s64 offen offset:1024 sc1\n\t" \
  "v_mov_b64 v[52:53], 0\n\t" \
  "v_mov_b64 v[54:55], 0\n\t" \
  "v_smfmac_f32_16x16x64_bf16 v[48:51], v[16:19], a[184:191], v68\n\t" \
  "v_mov_b64 v[56:57], 0\n\t" \
  "v_mov_b64 v[58:59], 0\n\t" \
  "v_smfmac_f32_16x16x64_bf16 v[48:51], v[20:23], a[216:223], v68\n\t" \
  "ds_write_b32 v71, v74 offset:0\n\t" \
  "v_add_f32_dpp v36, v40, v36 quad_perm:[2,3,0,1] row_mask:0xf bank_mask:0xf\n\t" \
  "v_smfmac_f32_16x16x64_bf16 v[48:51], v[24:27], v[88:95], v68\n\t" \
  "v_add_f32_dpp v37, v41, v37 quad_perm:[2,3,0,1] row_mask:0xf bank_mask:0xf\n\t" \
  "v_smfmac_f32_16x16x64_bf16 v[48:51], v[28:31], v[136:143], v68\n\t" \
  "v_add_f32_dpp v38, v42, v38 quad_perm:[2,3,0,1] row_mask:0xf bank_mask:0xf\n\t" \
  "v_smfmac_f32_16x16x64_bf16 v[48:51], v[32:35], v[176:183], v68\n\t" \
  "v_add_f32_dpp v39, v43, v39 quad_perm:[2,3,0,1] row_mask:0xf bank_mask:0xf\n\t" \
  CH_DP4 \
  "v_smfmac_f32_16x16x64_bf16 v[52:55], v[0:3], a[16:23], v68\n\t" \
  "s_mov_b64 exec, s[58:59]\n\t" \
  "ds_write_b128 v69, v[36:39] offset:2080\n\t" \
  "s_mov_b64 exec, -1\n\t" \
  "v_smfmac_f32_16x16x64_bf16 v[52:55], v[4:7], a[64:71], v68\n\t" \
  "v_add_f32 v75, v48, v50\n\t" \
  "v_add_f32 v49, v49, v51\n\t" \
  "v_cvt_pk_bf16_f32 v75, v75, v49\n\t" \
  "v_smfmac_f32_16x16x64_bf16 v[56:59], v[0:3], a[24:31], v68\n\t" \
  "ds_write_b32 v71, v75 offset:512\n\t" \
  "v_smfmac_f32_16x16x64_bf16 v[56:59], v[4:7], a[72:79], v68\n\t" \
  "ds_read_b64_tr_b16 v[0:1], v72 offset:10240\n\t" \
  "ds_read_b64_tr_b16 v[2:3], v72 offset:10752\n\t" \
  "v_smfmac_f32_16x16x64_bf16 v[52:55], v[8:11], a[104:111], v68\n\t" \
  "ds_read_b128 v[36:39], v70 offset:12288\n\t" \
  "v_smfmac_f32_16x16x64_bf16 v[56:59], v[8:11], a[112:119], v68\n\t" \
  "ds_read_b128 v[40:43], v70 offset:13312\n\t" \
  "v_smfmac_f32_16x16x64_bf16 v[52:55], v[12:15], a[144:151], v68\n\t" \
  "s_waitcnt lgkmcnt(4)\n\t" \
  "s_barrier\n\t" \
  CH_DP5 \
  "ds_read_b64_tr_b16 v[44:45], v72 offset:0\n\t" \
  "ds_read_b64_tr_b16 v[46:47], v72 offset:512\n\t" \
  "v_smfmac_f32_16x16x64_bf16 v[56:59], v[12:15], a[152:159], v68\n\t" \
  "ds_read_b64_tr_b16 v[48:49], v72 offset:1024\n\t" \
  "ds_read_b64_tr_b16 v[50:51], v72 offset:1536\n\t" \
  "v_smfmac_f32_16x16x64_bf16 v[52:55], v[16:19], a[192:199], v68\n\t" \
  "ds_read_b128 v[4:7], v69 offset:2048\n\t" \
  "s_waitcnt lgkmcnt(5)\n\t" \
  "v_smfmac_f32_16x16x64_bf16 v[56:59], v[16:19], v[36:43], v68\n\t" \
  "ds_read_b128 v[8:11], v69 offset:3072\n\t" \
  "s_waitcnt lgkmcnt(4)\n\t" \
  "v_mfma_f32_16x16x32_bf16 v[12:15], v[44:47], v[76:79], 0\n\t" \
  "ds_read_b128 v[36:39], v70 offset:14336\n\t" \
  "s_waitcnt lgkmcnt(3)\n\t" \
  "v_mfma_f32_16x16x32_bf16 v[12:15], v[48:51], v[0:3], v[12:15]\n\t" \
  "buffer_store_dwordx4 v[44:47], v69, s[28:31], s69 offen offset:0 sc1\n\t" \
  "ds_read_b128 v[40:43], v70 offset:15360\n\t" \
  "v_smfmac_f32_16x16x64_bf16 v[52:55], v[20:23], a[224:231], v68\n\t" \
  "s_waitcnt lgkmcnt(2)\n\t" \
  "v_add_f32 v4, v4, v8\n\t" \
  "v_add_f32 v5, v5, v9\n\t" \
  "v_add_f32 v6, v6, v10\n\t" \
  CH_DP6 \
  "v_cndmask_b32 v78, v13, v14, s[56:57]\n\t" \
  "v_cndmask_b32 v76, v12, v15, s[56:57]\n\t" \
  "v_add_f32 v7, v7, v11\n\t" \
  "v_max3_f32 v77, v4, v5, v6\n\t" \
  "v_add_f32_dpp v76, v78, v76 quad_perm:[2,3,0,1] row_mask:0xf bank_mask:0xf\n\t" \
  "v_max3_f32 v77, v77, v7, v76\n\t" \
  "v_smfmac_f32_16x16x64_bf16 v[56:59], v[20:23], a[232:239], v68\n\t" \
  "ds_read_b128 v[44:47], v70 offset:16384\n\t" \
  "v_max_f32_dpp v77, v77, v77 quad_perm:[2,3,0,1] row_mask:0xf bank_mask:0xf\n\t" \
  "v_mov_b32 v78, v77\n\t" \
  "v_smfmac_f32_16x16x64_bf16 v[52:55], v[24:27], v[96:103], v68\n\t" \
  "ds_read_b128 v[48:51], v70 offset:17408\n\t" \
  "v_permlane16_swap_b32 v77, v78\n\t" \
  "v_max_f32 v77, v77, v78\n\t" \
  "v_mov_b32 v78, v77\n\t" \
  "v_smfmac_f32_16x16x64_bf16 v[52:55], v[28:31], v[144:151], v68\n\t" \
  "ds_read_b128 v[16:19], v70 offset:18432\n\t" \
  "v_permlane32_swap_b32 v77, v78\n\t" \
  "v_max_f32 v77, v77, v78\n\t" \
  "v_smfmac_f32_16x16x64_bf16 v[52:55], v[32:35], v[184:191], v68\n\t" \
  "ds_read_b128 v[20:23], v70 offset:19456\n\t" \
  "v_mul_f32 v77, 0xbe0293ee, v77\n\t" \
  CH_DP7 \
  "v_smfmac_f32_16x16x64_bf16 v[56:59], v[24:27], v[104:111], v68\n\t" \
  "v_fmamk_f32 v4, v4, 0x3e0293ee, v77\n\t" \
  "v_smfmac_f32_16x16x64_bf16 v[56:59], v[28:31], v[152:159], v68\n\t" \
  "ds_read_b128 v[24:27], v70 offset:22528\n\t" \
  "s_waitcnt lgkmcnt(4)\n\t" \
  "v_fmamk_f32 v5, v5, 0x3e0293ee, v77\n\t" \
  "v_smfmac_f32_16x16x64_bf16 v[56:59], v[32:35], v[36:43], v68\n\t" \
  "ds_read_b128 v[28:31], v70 offset:23552\n\t" \
  "v_fmamk_f32 v6, v6, 0x3e0293ee, v77\n\t" \
  "v_fmamk_f32 v7, v7, 0x3e0293ee, v77\n\t" \
  "v_fmamk_f32 v76, v76, 0x3e0293ee, v77\n\t" \
  "v_exp_f32 v4, v4\n\t" \
  "v_exp_f32 v5, v5\n\t" \
  "v_exp_f32 v6, v6\n\t" \
  "v_exp_f32 v7, v7\n\t" \
  "v_cvt_pk_bf16_f32 v4, v4, v5\n\t" \
  "v_exp_f32 v76, v76\n\t" \
  "v_cvt_pk_bf16_f32 v5, v6, v7\n\t" \
  "v_mov_b32_dpp v12, v4 quad_perm:[0,1,0,1] row_mask:0xf bank_mask:0xf\n\t" \
  "v_mov_b32_dpp v14, v4 quad_perm:[2,3,2,3] row_mask:0xf bank_mask:0xf\n\t" \
  "v_mov_b32_dpp v13, v5 quad_perm:[0,1,0,1] row_mask:0xf bank_mask:0xf\n\t" \
  "v_mov_b32_dpp v15, v5 quad_perm:[2,3,2,3] row_mask:0xf bank_mask:0xf\n\t" \
  "v_add_f32 v52, v52, v54\n\t" \
  "v_add_f32 v53, v53, v55\n\t" \
  "s_waitcnt vmcnt(2)\n\t" \
  CH_DP8 \
  "v_mfma_f32_16x16x32_bf16 v[0:3], v[60:63], v[12:15], 0\n\t" \
  "ds_read_b128 v[36:39], v70 offset:20480\n\t" \
  "v_add_f32 v56, v56, v58\n\t" \
  "v_add_f32 v57, v57, v59\n\t" \
  "v_cvt_pk_bf16_f32 v52, v52, v53\n\t" \
  "v_cvt_pk_bf16_f32 v53, v56, v57\n\t" \
  "s_waitcnt vmcnt(1)\n\t" \
  "v_mfma_f32_16x16x32_bf16 v[4:7], v[64:67], v[12:15], 0\n\t" \
  "ds_read_b128 v[40:43], v70 offset:21504\n\t" \
  "v_mov_b64 v[56:57], s[88:89]\n\t" \
  "v_mov_b64 v[58:59], s[88:89]\n\t" \
  "v_mov_b32_dpp v78, v76 quad_perm:[0,1,0,1] row_mask:0xf bank_mask:0xf\n\t" \
  "v_mov_b32_dpp v79, v76 quad_perm:[2,3,2,3] row_mask:0xf bank_mask:0xf\n\t" \
  "v_mfma_f32_16x16x32_bf16 v[32:35], v[56:59], v[12:15], 0\n\t" \
  "buffer_store_dwordx2 v[52:53], v71, s[28:31], s69 offen offset:2556 sc1\n\t" \
  "ds_read_b128 v[60:63], v70 offset:24576\n\t" \
  "v_cvt_pk_bf16_f32 v11, v78, v79\n\t" \
  "v_cvt_pk_bf16_f32 v8, v78, v79\n\t" \
  "v_mov_b64 v[54:55], 0\n\t" \
  "v_mov_b32 v9, 0\n\t" \
  "v_mov_b32 v10, 0\n\t" \
  "v_mov_b64 v[14:15], 0\n\t" \
  "s_nop 0\n\t" \
  "v_mfma_f32_16x16x32_bf16 v[32:35], v[54:57], v[8:11], v[32:35]\n\t" \
  "s_cmp_eq_u32 s48, 63\n\t" \
  "v_mfma_f32_16x16x32_bf16 v[0:3], v[52:55], v[8:11], v[0:3]\n\t" \
  "ds_read_b128 v[64:67], v70 offset:25600\n\t" \
  "v_mov_b64 v[56:57], 0\n\t" \
  "v_mov_b64 v[58:59], 0\n\t" \
  "v_mfma_f32_16x16x32_bf16 v[4:7], v[52:55], v[10:13], v[4:7]\n\t" \
  "s_cbranch_scc1 LLAST1_%=\n\t" \
  "v_rcp_f32 v74, v32\n\t" \
  "v_mov_b64 v[12:13], 0\n\t" \
  "v_mov_b32 v8, 0\n\t" \
  "v_pk_mul_f32 v[0:1], v[0:1], v[74:75] op_sel_hi:[1,0]\n\t" \
  "v_pk_mul_f32 v[2:3], v[2:3], v[74:75] op_sel_hi:[1,0]\n\t" \
  "v_cvt_pk_bf16_f32 v76, v0, v1\n\t" \
  "v_cvt_pk_bf16_f32 v77, v2, v3\n\t" \
  "v_pk_mul_f32 v[4:5], v[4:5], v[74:75] op_sel_hi:[1,0]\n\t" \
  "v_pk_mul_f32 v[6:7], v[6:7], v[74:75] op_sel_hi:[1,0]\n\t" \
  "v_cvt_pk_bf16_f32 v78, v4, v5\n\t" \
  "v_cvt_pk_bf16_f32 v79, v6, v7\n\t" \
  "ds_write_b128 v69, v[76:79] offset:5120\n\t" \
  CH_DP9 \
  "s_waitcnt lgkmcnt(2)\n\t" \
  "v_smfmac_f32_16x16x64_bf16 v[12:15], v[76:79], v[44:51], v68\n\t" \
  "ds_read_b128 v[44:47], v70 offset:26624\n\t" \
  "v_mov_b32 v11, 0\n\t" \
  "v_smfmac_f32_16x16x64_bf16 v[56:59], v[76:79], v[16:23], v68\n\t" \
  "ds_read_b128 v[48:51], v70 offset:27648\n\t" \
  "v_mov_b64 v[52:53], 0\n\t" \
  "v_smfmac_f32_16x16x64_bf16 v[8:11], v[76:79], v[36:43], v68\n\t" \
  "ds_read_b128 v[16:19], v70 offset:28672\n\t" \
  "v_smfmac_f32_16x16x64_bf16 v[52:55], v[76:79], v[24:31], v68\n\t" \
  "s_waitcnt lgkmcnt(3)\n\t" \
  "s_barrier\n\t" \
  CH_DP10 \
  "ds_read_b128 v[0:3], v69 offset:4096\n\t" \
  "ds_read_b128 v[4:7], v69 offset:7168\n\t" \
  "v_lshrrev_b32 v71, 7, v69\n\t" \
  "v_and_b32 v74, 0x70, v69\n\t" \
  "v_lshl_add_u32 v71, v71, 14, v74\n\t" \
  "v_bfe_u32 v72, v69, 2, 7\n\t" \
  "v_and_b32 v74, 0x200, v69\n\t" \
  "s_waitcnt lgkmcnt(1)\n\t" \
  "v_smfmac_f32_16x16x64_bf16 v[8:11], v[0:3], v[60:67], v68\n\t" \
  "buffer_load_dword v75, off, s[28:31], s80 sc1\n\t" \
  "ds_read_b128 v[20:23], v70 offset:29696\n\t" \
  "v_mov_b64 v[76:77], 0\n\t" \
  "v_smfmac_f32_16x16x64_bf16 v[12:15], v[0:3], v[200:207], v68\n\t" \
  "ds_read_b128 v[32:35], v69 offset:6144\n\t" \
  "v_mov_b64 v[78:79], 0\n\t" \
  "v_smfmac_f32_16x16x64_bf16 v[52:55], v[0:3], v[44:51], v68\n\t" \
  "ds_read_b128 v[24:27], v70 offset:30720\n\t" \
  "v_lshl_add_u32 v72, v74, 3, v72\n\t" \
  "v_smfmac_f32_16x16x64_bf16 v[56:59], v[0:3], v[232:239], v68\n\t" \
  "ds_read_b128 v[28:31], v70 offset:31744\n\t" \
  "s_waitcnt lgkmcnt(4)\n\t" \
  "v_smfmac_f32_16x16x64_bf16 v[12:15], v[4:7], v[208:215], v68\n\t" \
  "ds_read_b128 v[60:63], v70 offset:32768\n\t" \
  "s_waitcnt lgkmcnt(4)\n\t" \
  "v_smfmac_f32_16x16x64_bf16 v[8:11], v[4:7], v[16:23], v68\n\t" \
  "ds_read_b128 v[64:67], v70 offset:33792\n\t" \
  "s_waitcnt lgkmcnt(4)\n\t" \
  "v_smfmac_f32_16x16x64_bf16 v[8:11], v[32:35], v[192:199], v68\n\t" \
  "ds_read_b128 v[36:39], v73 offset:4096\n\t" \
  "v_smfmac_f32_16x16x64_bf16 v[12:15], v[32:35], v[216:223], v68\n\t" \
  "ds_read_b128 v[40:43], v73 offset:6144\n\t" \
  "s_waitcnt lgkmcnt(4)\n\t" \
  "v_smfmac_f32_16x16x64_bf16 v[52:55], v[4:7], v[24:31], v68\n\t" \
  "ds_read_b128 v[44:47], v70 offset:34816\n\t" \
  "v_cvt_pk_bf16_f32 v8, v8, v9\n\t" \
  "v_smfmac_f32_16x16x64_bf16 v[52:55], v[32:35], v[224:231], v68\n\t" \
  "ds_read_b128 v[48:51], v70 offset:35840\n\t" \
  "v_cvt_pk_bf16_f32 v9, v10, v11\n\t" \
  "v_smfmac_f32_16x16x64_bf16 v[56:59], v[4:7], v[240:247], v68\n\t" \
  "s_waitcnt lgkmcnt(4)\n\t" \
  "v_cvt_pk_bf16_f32 v10, v12, v13\n\t" \
  "v_smfmac_f32_16x16x64_bf16 v[56:59], v[32:35], v[60:67], v68\n\t" \
  "s_waitcnt lgkmcnt(3)\n\t" \
  "v_cvt_pk_bf16_f32 v11, v14, v15\n\t" \
  "v_smfmac_f32_16x16x64_bf16 v[76:79], v[36:39], v[248:255], v68\n\t" \
  "buffer_store_dwordx4 v[8:11], v71, s[28:31], s54 offen sc1\n\t" \
  "s_waitcnt lgkmcnt(0)\n\t" \
  CH_DP11 \
  "v_cvt_pk_bf16_f32 v52, v52, v53\n\t" \
  "v_smfmac_f32_16x16x64_bf16 v[76:79], v[40:43], v[44:51], v68\n\t" \
  "v_cvt_pk_bf16_f32 v53, v54, v55\n\t" \
  "v_cvt_pk_bf16_f32 v54, v56, v57\n\t" \
  "v_cvt_pk_bf16_f32 v55, v58, v59\n\t" \
  "buffer_store_dwordx4 v[52:55], v71, s[28:31], s75 offen sc1\n\t" \
  "s_nop 2\n\t" \
  "v_add_f32 v76, v76, v78\n\t" \
  "v_add_f32 v77, v77, v79\n\t" \
  "v_cvt_pk_bf16_f32 v76, v76, v77\n\t" \
  "buffer_store_dword v76, v72, s[28:31], s55 offen sc1\n\t" \
  CH_DP12 \
  "s_memrealtime s[70:71]\n\t" \
  "s_waitcnt vmcnt(3)\n\t" \
  "v_readfirstlane_b32 s65, v75\n\t" \
  "s_barrier\n\t" \
  "s_waitcnt lgkmcnt(0)\n\t" \
  "s_not_b32 s65, s65\n\t" \
  "s_lshl_b32 s61, s70, 8\n\t" \
  "s_andn2_b32 s64, s65, 0xff\n\t" \
  "s_sub_u32 s64, s64, s61\n\t" \
  "s_ashr_i32 s64, s64, 8\n\t" \
  "s_cmp_eq_u32 s65, 0\n\t" \
  "s_cselect_b32 s64, " CH_DLF ", s64\n\t" \
  "s_sub_i32 s64, s64, " CH_RO1 "\n\t" \
  "s_min_i32 s64, s64, s34\n\t" \
  "s_max_i32 s64, s64, " CH_DLL1 "\n\t" \
  "s_add_u32 s64, s64, s70\n\t" \
  CH_ST2 \
  "s_mov_b32 s74, 0\n\t" \
  "s_movk_i32 s60, 8192\n\t" \
  "RWT1_%=:\n\t" \
  "s_memrealtime s[72:73]\n\t" \
  "s_sub_u32 s60, s60, 1\n\t" \
  "s_cselect_b32 s61, 0, -1\n\t" \
  "s_waitcnt lgkmcnt(0)\n\t" \
  "s_sub_i32 s73, s72, s64\n\t" \
  "s_and_b32 s73, s73, s61\n\t" \
  "s_cmp_lt_i32 s73, " CH_FIN "\n\t" \
  "s_cbranch_scc1 RWT1_%=\n\t" \
  "s_cmp_lt_i32 s73, 0\n\t" \
  "s_cbranch_scc1 RWT1_%=f\n\t" \
  "RWT1_%=g:\n\t" \
  "RRETRY1_%=:\n\t" \
  CH_DP13 \
  "buffer_load_dwordx4 v[0:3], v69, s[28:31], s97 offen offset:0 sc1\n\t" \
  "buffer_load_dwordx4 v[4:7], v69, s[28:31], s97 offen offset:1024 sc1\n\t" \
  "buffer_load_dwordx4 v[8:11], v69, s[28:31], s97 offen offset:2048 sc1\n\t" \
  "buffer_load_dwordx4 v[12:15], v69, s[28:31], s97 offen offset:3072 sc1\n\t" \
  "v_lshrrev_b32 v20, 2, v69\n\t" \
  "v_and_b32 v20, 16, v20\n\t" \
  "v_mov_b32 v21, 0x3f80\n\t" \
  "v_lshlrev_b32 v21, v20, v21\n\t" \
  "v_bfe_u32 v20, v69, 4, 2\n\t" \
  "v_cmp_eq_u32 vcc, 0, v20\n\t" \
  "s_nop 1\n\t" \
  "v_cndmask_b32 v32, 0, v21, vcc\n\t" \
  "v_cmp_eq_u32 vcc, 1, v20\n\t" \
  "s_nop 1\n\t" \
  "v_cndmask_b32 v33, 0, v21, vcc\n\t" \
  "v_cmp_eq_u32 vcc, 2, v20\n\t" \
  "s_nop 1\n\t" \
  "v_cndmask_b32 v34, 0, v21, vcc\n\t" \
  "v_cmp_eq_u32 vcc, 3, v20\n\t" \
  "s_nop 1\n\t" \
  "v_cndmask_b32 v35, 0, v21, vcc\n\t" \
  "v_bfe_u32 v38, v69, 7, 1\n\t" \
  "v_bfe_u32 v20, v69, 9, 1\n\t" \
  "v_lshl_add_u32 v38, v20, 2, v38\n\t" \
  "v_lshlrev_b32 v38, 12, v38\n\t" \
  "v_and_b32 v20, 0x60, v69\n\t" \
  "v_add_u32 v38, v38, v20\n\t" \
  "v_bfe_u32 v20, v69, 8, 1\n\t" \
  "v_lshl_add_u32 v38, v20, 4, v38\n\t" \
  "v_bfe_u32 v20, v69, 4, 1\n\t" \
  "v_lshl_add_u32 v38, v20, 3, v38\n\t" \
  "v_mov_b32 v44, 0\n\t" \
  CH_DP17 \
  "s_waitcnt vmcnt(3)\n\t" \
  CH_DP14 \
  "v_mfma_f32_16x16x32_bf16 v[40:43], v[32:35], v[0:3], 0\n\t" \
  "v_max3_u32 v44, v44, v0, v1\n\t" \
  "v_max3_u32 v44, v44, v2, v3\n\t" \
  "s_waitcnt vmcnt(2)\n\t" \
  "v_mfma_f32_16x16x32_bf16 v[40:43], v[32:35], v[4:7], v[40:43]\n\t" \
  "v_max3_u32 v44, v44, v4, v5\n\t" \
  "v_max3_u32 v44, v44, v6, v7\n\t" \
  "s_waitcnt vmcnt(1)\n\t" \
  "v_mfma_f32_16x16x32_bf16 v[40:43], v[32:35], v[8:11], v[40:43]\n\t" \
  "v_max3_u32 v44, v44, v8, v9\n\t" \
  "v_max3_u32 v44, v44, v10, v11\n\t" \
  "s_waitcnt vmcnt(0)\n\t" \
  CH_DP15 \
  "v_mfma_f32_16x16x32_bf16 v[40:43], v[32:35], v[12:15], v[40:43]\n\t" \
  "v_max3_u32 v44, v44, v12, v13\n\t" \
  "v_max3_u32 v44, v44, v14, v15\n\t" \
  "v_cmp_eq_u32 vcc, -1, v44\n\t" \
  "s_cbranch_vccnz RFAIL1_%=\n\t" \
  "s_nop 3\n\t" \
  "v_add_f32_dpp v40, v40, v40 row_ror:8 row_mask:0xf bank_mask:0xf\n\t" \
  "v_add_f32_dpp v41, v41, v41 row_ror:8 row_mask:0xf bank_mask:0xf\n\t" \
  "v_add_f32_dpp v42, v42, v42 row_ror:8 row_mask:0xf bank_mask:0xf\n\t" \
  "v_add_f32_dpp v43, v43, v43 row_ror:8 row_mask:0xf bank_mask:0xf\n\t" \
  "v_cvt_pk_bf16_f32 v40, v40, v41\n\t" \
  "v_cvt_pk_bf16_f32 v41, v42, v43\n\t" \
  "buffer_store_dwordx2 v[40:41], v38, s[28:31], s98 offen sc1\n\t" \
  "RDONE1_%=:\n\t" \
  CH_DP16 \
  CH_ST4 \
  "s_barrier\n\t" \
  CH_ST3 \
  CH_TRC \
  "s_add_u32 s48, s48, 1\n\t" \
  "s_mov_b32 s49, s53\n\t" \
  "s_add_u32 s53, s53, " CH_SLOT "\n\t" \
  "s_add_u32 s60, s53, " CH_SKIP "\n\t" \
  "s_lshr_b32 s61, s96, 1\n\t" \
  "s_add_u32 s86, s86, 1\n\t" \
  "s_cmp_eq_u32 s86, 3\n\t" \
  "s_cselect_b32 s53, s60, s53\n\t" \
  "s_cselect_b32 s96, s61, s96\n\t" \
  "s_cselect_b32 s86, 0, s86\n\t" \
  "s_cmp_ge_u32 s53, " CH_RING "\n\t" \
  "s_cselect_b32 s53, 0, s53\n\t" \
  "s_cselect_b32 s96, s21, s96\n\t" \
  "s_andn2_b32 s53, s53, 0x2000\n\t" \
  "s_and_b32 s60, s96, 1\n\t" \
  "s_lshl_b32 s60, s60, 13\n\t" \
  "s_or_b32 s53, s53, s60\n\t" \
  "s_mov_b32 s68, 0\n\t" \
  "s_add_u32 s97, s41, s53\n\t" \
  "s_add_u32 s98, s37, s53\n\t" \
  "s_add_u32 s82, s48, 0\n\t" \
  "s_and_b32 s82, s82, " CH_TRM "\n\t" \
  CH_SEGC \
  "s_lshr_b64 s[60:61], s[90:91], s82\n\t" \
  "s_and_b32 s60, s60, 1\n\t" \
  "s_lshl_b32 s60, s60, 13\n\t" \
  "s_add_u32 s82, s82, " CH_SG0 "\n\t" \
  "s_lshl_b32 s82, s82, 21\n\t" \
  "s_or_b32 s82, s82, s60\n\t" \
  "s_add_u32 s50, s82, s38\n\t" \
  "s_add_u32 s69, s82, s39\n\t" \
  "s_cmp_eq_u32 s34, 0\n\t" \
  "s_cbranch_scc1 LINI_%=\n\t" \
  "s_branch LG1_%=\n\t" \
  "RFAIL1_%=:\n\t" \
  "s_add_u32 s74, s74, 1\n\t" \
  "s_sleep " CH_RSL "\n\t" \
  "s_waitcnt vmcnt(0)\n\t" \
  "s_cmp_gt_u32 s74, s84\n\t" \
  "s_cbranch_scc1 RG1_%=\n\t" \
  "s_and_b32 s60, s74, 1023\n\t" \
  "s_cmp_lg_u32 s60, 0\n\t" \
  "s_cbranch_scc1 RRETRY1_%=\n\t" \
  "buffer_load_dword v46, off, s[28:31], s92 sc1\n\t" \
  "s_waitcnt vmcnt(0)\n\t" \
  "v_readfirstlane_b32 s60, v46\n\t" \
  "s_nop 0\n\t" \
  "s_cmp_eq_u32 s60, 0\n\t" \
  "s_cbranch_scc1 RRETRY1_%=\n\t" \
  "RG1_%=:\n\t" \
  "s_mov_b32 s84, 0\n\t" \
  "s_lshl_b32 s60, s36, 4\n\t" \
  "s_or_b32 s60, s60, s48\n\t" \
  "s_or_b32 s60, s60, 536870912\n\t" \
  "v_mov_b32 v76, s60\n\t" \
  "v_mov_b32 v77, 0\n\t" \
  "s_mov_b64 exec, 1\n\t" \
  "buffer_atomic_cmpswap v[76:77], off, s[28:31], s92 sc1\n\t" \
  "s_mov_b64 exec, -1\n\t" \
  "s_branch RDONE1_%=\n\t" \
  "LFAIL1_%=:\n\t" \
  "v_max_u32 v75, v0, v1\n\t" \
  "v_max3_u32 v75, v75, v2, v3\n\t" \
  "v_max3_u32 v75, v75, v4, v5\n\t" \
  "v_max3_u32 v75, v75, v6, v7\n\t" \
  "v_max3_u32 v75, v75, v8, v9\n\t" \
  "v_max3_u32 v75, v75, v10, v11\n\t" \
  "v_max3_u32 v75, v75, v12, v13\n\t" \
  "v_max3_u32 v75, v75, v14, v15\n\t" \
  "v_max3_u32 v75, v75, v16, v17\n\t" \
  "v_max3_u32 v75, v75, v18, v19\n\t" \
  "v_max3_u32 v75, v75, v20, v21\n\t" \
  "v_max3_u32 v75, v75, v22, v23\n\t" \
  "v_max3_u32 v75, v75, v24, v25\n\t" \
  "v_max3_u32 v75, v75, v26, v27\n\t" \
  "v_max3_u32 v75, v75, v28, v29\n\t" \
  "v_max3_u32 v75, v75, v30, v31\n\t" \
  "v_max3_u32 v75, v75, v32, v33\n\t" \
  "v_max3_u32 v75, v75, v34, v35\n\t" \
  "v_cmp_eq_u32 vcc, -1, v75\n\t" \
  "s_cbranch_vccz LGO1_%=\n\t" \
  "s_add_u32 s68, s68, 1\n\t" \
  "s_waitcnt lgkmcnt(0)\n\t" \
  "s_cmp_gt_u32 s68, s83\n\t" \
  "s_cbranch_scc1 LGV1_%=\n\t" \
  "s_barrier\n\t" \
  "s_waitcnt vmcnt(8)\n\t" \
  "s_branch LG1_%=\n\t" \
  "RWT1_%=f:\n\t" \
  "s_cmp_lt_i32 s73, -2\n\t" \
  "s_cbranch_scc0 RWT1_%=g\n\t" \
  "s_nop 4\n\t" \
  "s_branch RWT1_%=g\n\t" \
  "LGV1_%=:\n\t" \
  "s_mov_b32 s83, 0\n\t" \
  "s_lshl_b32 s60, s36, 4\n\t" \
  "s_or_b32 s60, s60, s48\n\t" \
  "s_or_b32 s60, s60, 805306368\n\t" \
  "v_mov_b32 v76, s60\n\t" \
  "v_mov_b32 v77, 0\n\t" \
  "s_mov_b64 exec, 1\n\t" \
  "buffer_atomic_cmpswap v[76:77], off, s[28:31], s92 sc1\n\t" \
  "s_mov_b64 exec, -1\n\t" \
  "s_branch LGO1_%=\n\t" \
  "LLAST1_%=:\n\t" \
  "s_waitcnt vmcnt(0) lgkmcnt(0)\n\t" \
  "s_barrier\n\t" \
  "s_branch LEND_%=\n\t" \
  ".p2align 6\n\t" \
  "LG2_%=:\n\t" \
  "s_barrier\n\t" \
  "buffer_load_dwordx4 v[52:55], v69, s[28:31], s50 offen sc1\n\t" \
  "buffer_load_dwordx4 v[56:59], v69, s[28:31], s50 offen offset:1024 sc1\n\t" \
  "s_add_u32 s64, s50, 18432\n\t" \
  "s_movk_i32 m0, 11264\n\t" \
  "v_lshlrev_b32 v75, 1, v69\n\t" \
  "buffer_load_dword v75, s[28:31], s64 offen sc1 lds\n\t" \
  "ds_read_b128 v[44:47], v70 offset:0\n\t" \
  "ds_read_b128 v[48:51], v70 offset:1024\n\t" \
  "ds_read_b128 v[60:63], v70 offset:2048\n\t" \
  "ds_read_b128 v[64:67], v70 offset:3072\n\t" \
  "v_mov_b64 v[36:37], 0\n\t" \
  "v_mov_b64 v[38:39], 0\n\t" \
  "v_mov_b64 v[40:41], 0\n\t" \
  "v_mov_b64 v[42:43], 0\n\t" \
  "v_lshrrev_b32 v71, 1, v69\n\t" \
  "v_lshrrev_b32 v72, 3, v69\n\t" \
  "v_and_b32 v72, 0x78, v72\n\t" \
  "v_and_b32 v74, 0x30, v69\n\t" \
  "v_lshl_add_u32 v72, v74, 3, v72\n\t" \
  "s_add_u32 s54, s53, s36\n\t" \
  "s_add_u32 s55, s54, 557056\n\t" \
  "s_add_u32 s54, s54, 131072\n\t" \
  "s_add_u32 s75, s54, 262144\n\t" \
  "s_add_u32 s80, s53, s26\n\t" \
  "s_barrier\n\t" \
  CH_DP0 \
  "ds_read_b128 v[0:3], v69 offset:0\n\t" \
  "ds_read_b128 v[4:7], v69 offset:1024\n\t" \
  "ds_read_b128 v[8:11], v69 offset:2048\n\t" \
  "ds_read_b128 v[12:15], v69 offset:3072\n\t" \
  "s_waitcnt lgkmcnt(3)\n\t" \
  "v_smfmac_f32_16x16x64_bf16 v[36:39], v[0:3], v[44:51], v68\n\t" \
  "ds_read_b128 v[44:47], v70 offset:4096\n\t" \
  "v_smfmac_f32_16x16x64_bf16 v[40:43], v[0:3], v[60:67], v68\n\t" \
  "ds_read_b128 v[48:51], v70 offset:5120\n\t" \
  "s_waitcnt lgkmcnt(4)\n\t" \
  "v_smfmac_f32_16x16x64_bf16 v[36:39], v[4:7], a[32:39], v68\n\t" \
  "ds_read_b128 v[60:63], v70 offset:6144\n\t" \
  "v_smfmac_f32_16x16x64_bf16 v[40:43], v[4:7], a[40:47], v68\n\t" \
  "ds_read_b128 v[64:67], v70 offset:7168\n\t" \
  "s_waitcnt lgkmcnt(5)\n\t" \
  "v_smfmac_f32_16x16x64_bf16 v[36:39], v[8:11], a[80:87], v68\n\t" \
  "s_barrier\n\t" \
  CH_DP1 \
  "ds_read_b128 v[16:19], v69 offset:4096\n\t" \
  "v_smfmac_f32_16x16x64_bf16 v[40:43], v[8:11], a[88:95], v68\n\t" \
  "ds_read_b128 v[20:23], v69 offset:5120\n\t" \
  "s_waitcnt lgkmcnt(6)\n\t" \
  "v_smfmac_f32_16x16x64_bf16 v[36:39], v[12:15], a[120:127], v68\n\t" \
  "ds_read_b128 v[24:27], v69 offset:6144\n\t" \
  "s_waitcnt lgkmcnt(5)\n\t" \
  "v_smfmac_f32_16x16x64_bf16 v[40:43], v[12:15], v[44:51], v68\n\t" \
  "ds_read_b128 v[28:31], v69 offset:7168\n\t" \
  "s_waitcnt lgkmcnt(3)\n\t" \
  "v_smfmac_f32_16x16x64_bf16 v[36:39], v[16:19], a[160:167], v68\n\t" \
  "ds_read_b128 v[32:35], v69 offset:8192\n\t" \
  "v_smfmac_f32_16x16x64_bf16 v[40:43], v[16:19], a[168:175], v68\n\t" \
  "s_waitcnt lgkmcnt(3)\n\t" \
  "v_mov_b64 v[44:45], 0\n\t" \
  "v_smfmac_f32_16x16x64_bf16 v[36:39], v[20:23], a[200:207], v68\n\t" \
  "s_waitcnt lgkmcnt(2)\n\t" \
  "v_mov_b64 v[46:47], 0\n\t" \
  "v_smfmac_f32_16x16x64_bf16 v[36:39], v[24:27], a[240:247], v68\n\t" \
  "s_waitcnt lgkmcnt(1)\n\t" \
  "v_mov_b64 v[48:49], 0\n\t" \
  "v_smfmac_f32_16x16x64_bf16 v[36:39], v[28:31], v[112:119], v68\n\t" \
  "s_waitcnt lgkmcnt(0)\n\t" \
  "v_mov_b64 v[50:51], 0\n\t" \
  "v_smfmac_f32_16x16x64_bf16 v[36:39], v[32:35], v[160:167], v68\n\t" \
  "v_smfmac_f32_16x16x64_bf16 v[40:43], v[20:23], v[60:67], v68\n\t" \
  "ds_read_b128 v[60:63], v70 offset:8192\n\t" \
  "v_smfmac_f32_16x16x64_bf16 v[40:43], v[24:27], a[248:255], v68\n\t" \
  "ds_read_b128 v[64:67], v70 offset:9216\n\t" \
  "v_add_f32 v74, v36, v38\n\t" \
  "v_smfmac_f32_16x16x64_bf16 v[40:43], v[28:31], v[120:127], v68\n\t" \
  "v_add_f32 v37, v37, v39\n\t" \
  "v_smfmac_f32_16x16x64_bf16 v[40:43], v[32:35], v[168:175], v68\n\t" \
  "v_cmp_u_f32 vcc, v74, v37\n\t" \
  CH_DP2 \
  "v_smfmac_f32_16x16x64_bf16 v[44:47], v[0:3], a[0:7], v68\n\t" \
  "s_cbranch_vccnz LFAIL2_%=\n\t" \
  "LGO2_%=:\n\t" \
  "v_cvt_pk_bf16_f32 v74, v74, v37\n\t" \
  "v_smfmac_f32_16x16x64_bf16 v[44:47], v[4:7], a[48:55], v68\n\t" \
  "s_waitcnt lgkmcnt(0)\n\t" \
  "v_add_f32 v75, v40, v42\n\t" \
  "v_smfmac_f32_16x16x64_bf16 v[44:47], v[8:11], v[60:67], v68\n\t" \
  "ds_read_b128 v[60:63], v70 offset:10240\n\t" \
  "v_add_f32 v41, v41, v43\n\t" \
  "v_cvt_pk_bf16_f32 v75, v75, v41\n\t" \
  "v_smfmac_f32_16x16x64_bf16 v[44:47], v[12:15], a[128:135], v68\n\t" \
  "ds_write2st64_b32 v71, v74, v75 offset0:40 offset1:42\n\t" \
  "v_smfmac_f32_16x16x64_bf16 v[44:47], v[16:19], a[176:183], v68\n\t" \
  "ds_read_b128 v[64:67], v70 offset:11264\n\t" \
  "v_smfmac_f32_16x16x64_bf16 v[44:47], v[20:23], a[208:215], v68\n\t" \
  "v_smfmac_f32_16x16x64_bf16 v[44:47], v[24:27], v[80:87], v68\n\t" \
  "v_smfmac_f32_16x16x64_bf16 v[44:47], v[28:31], v[128:135], v68\n\t" \
  "s_waitcnt lgkmcnt(0)\n\t" \
  "v_smfmac_f32_16x16x64_bf16 v[44:47], v[32:35], v[60:67], v68\n\t" \
  "s_barrier\n\t" \
  "ds_read_b64_tr_b16 v[76:77], v72 offset:10240\n\t" \
  CH_DP3 \
  "v_smfmac_f32_16x16x64_bf16 v[48:51], v[0:3], a[8:15], v68\n\t" \
  "ds_read_b64_tr_b16 v[78:79], v72 offset:10752\n\t" \
  "v_smfmac_f32_16x16x64_bf16 v[48:51], v[4:7], a[56:63], v68\n\t" \
  "buffer_load_dwordx4 v[60:63], v69, s[28:31], s64 offen sc1\n\t" \
  "v_add_f32 v74, v44, v46\n\t" \
  "v_smfmac_f32_16x16x64_bf16 v[48:51], v[8:11], a[96:103], v68\n\t" \
  "v_add_f32 v45, v45, v47\n\t" \
  "v_smfmac_f32_16x16x64_bf16 v[48:51], v[12:15], a[136:143], v68\n\t" \
  "buffer_load_dwordx4 v[64:67], v69, s[28:31], s64 offen offset:1024 sc1\n\t" \
  "s_waitcnt vmcnt(4)\n\t" \
  "s_waitcnt lgkmcnt(0)\n\t" \
  "v_cvt_pk_bf16_f32 v74, v74, v45\n\t" \
  "v_mfma_f32_16x16x32_bf16 v[36:39], v[52:55], v[76:79], 0\n\t" \
  "s_waitcnt vmcnt(3)\n\t" \
  "v_mfma_f32_16x16x32_bf16 v[40:43], v[56:59], v[76:79], 0\n\t" \
  "v_mov_b64 v[52:53], 0\n\t" \
  "v_mov_b64 v[54:55], 0\n\t" \
  "v_smfmac_f32_16x16x64_bf16 v[48:51], v[16:19], a[184:191], v68\n\t" \
  "v_mov_b64 v[56:57], 0\n\t" \
  "v_mov_b64 v[58:59], 0\n\t" \
  "v_smfmac_f32_16x16x64_bf16 v[48:51], v[20:23], a[216:223], v68\n\t" \
  "ds_write_b32 v71, v74 offset:1024\n\t" \
  "v_add_f32_dpp v36, v40, v36 quad_perm:[2,3,0,1] row_mask:0xf bank_mask:0xf\n\t" \
  "v_smfmac_f32_16x16x64_bf16 v[48:51], v[24:27], v[88:95], v68\n\t" \
  "v_add_f32_dpp v37, v41, v37 quad_perm:[2,3,0,1] row_mask:0xf bank_mask:0xf\n\t" \
  "v_smfmac_f32_16x16x64_bf16 v[48:51], v[28:31], v[136:143], v68\n\t" \
  "v_add_f32_dpp v38, v42, v38 quad_perm:[2,3,0,1] row_mask:0xf bank_mask:0xf\n\t" \
  "v_smfmac_f32_16x16x64_bf16 v[48:51], v[32:35], v[176:183], v68\n\t" \
  "v_add_f32_dpp v39, v43, v39 quad_perm:[2,3,0,1] row_mask:0xf bank_mask:0xf\n\t" \
  CH_DP4 \
  "v_smfmac_f32_16x16x64_bf16 v[52:55], v[0:3], a[16:23], v68\n\t" \
  "s_mov_b64 exec, s[58:59]\n\t" \
  "ds_write_b128 v69, v[36:39] offset:3072\n\t" \
  "s_mov_b64 exec, -1\n\t" \
  "v_smfmac_f32_16x16x64_bf16 v[52:55], v[4:7], a[64:71], v68\n\t" \
  "v_add_f32 v75, v48, v50\n\t" \
  "v_add_f32 v49, v49, v51\n\t" \
  "v_cvt_pk_bf16_f32 v75, v75, v49\n\t" \
  "v_smfmac_f32_16x16x64_bf16 v[56:59], v[0:3], a[24:31], v68\n\t" \
  "ds_write_b32 v71, v75 offset:1536\n\t" \
  "v_smfmac_f32_16x16x64_bf16 v[56:59], v[4:7], a[72:79], v68\n\t" \
  "ds_read_b64_tr_b16 v[0:1], v72 offset:9216\n\t" \
  "ds_read_b64_tr_b16 v[2:3], v72 offset:9728\n\t" \
  "v_smfmac_f32_16x16x64_bf16 v[52:55], v[8:11], a[104:111], v68\n\t" \
  "ds_read_b128 v[36:39], v70 offset:12288\n\t" \
  "v_smfmac_f32_16x16x64_bf16 v[56:59], v[8:11], a[112:119], v68\n\t" \
  "ds_read_b128 v[40:43], v70 offset:13312\n\t" \
  "v_smfmac_f32_16x16x64_bf16 v[52:55], v[12:15], a[144:151], v68\n\t" \
  "s_waitcnt lgkmcnt(4)\n\t" \
  "s_barrier\n\t" \
  CH_DP5 \
  "ds_read_b64_tr_b16 v[44:45], v72 offset:1024\n\t" \
  "ds_read_b64_tr_b16 v[46:47], v72 offset:1536\n\t" \
  "v_smfmac_f32_16x16x64_bf16 v[56:59], v[12:15], a[152:159], v68\n\t" \
  "ds_read_b64_tr_b16 v[48:49], v72 offset:0\n\t" \
  "ds_read_b64_tr_b16 v[50:51], v72 offset:512\n\t" \
  "v_smfmac_f32_16x16x64_bf16 v[52:55], v[16:19], a[192:199], v68\n\t" \
  "ds_read_b128 v[4:7], v69 offset:2048\n\t" \
  "s_waitcnt lgkmcnt(5)\n\t" \
  "v_smfmac_f32_16x16x64_bf16 v[56:59], v[16:19], v[36:43], v68\n\t" \
  "ds_read_b128 v[8:11], v69 offset:3072\n\t" \
  "s_waitcnt lgkmcnt(4)\n\t" \
  "v_mfma_f32_16x16x32_bf16 v[12:15], v[44:47], v[76:79], 0\n\t" \
  "ds_read_b128 v[36:39], v70 offset:14336\n\t" \
  "s_waitcnt lgkmcnt(3)\n\t" \
  "v_mfma_f32_16x16x32_bf16 v[12:15], v[48:51], v[0:3], v[12:15]\n\t" \
  "ds_read_b128 v[40:43], v70 offset:15360\n\t" \
  "v_smfmac_f32_16x16x64_bf16 v[52:55], v[20:23], a[224:231], v68\n\t" \
  "buffer_store_dwordx4 v[44:47], v69, s[28:31], s69 offen offset:1024 sc1\n\t" \
  "s_waitcnt lgkmcnt(2)\n\t" \
  "v_add_f32 v4, v4, v8\n\t" \
  "v_add_f32 v5, v5, v9\n\t" \
  "v_add_f32 v6, v6, v10\n\t" \
  CH_DP6 \
  "v_cndmask_b32 v78, v13, v14, s[56:57]\n\t" \
  "v_cndmask_b32 v76, v12, v15, s[56:57]\n\t" \
  "v_add_f32 v7, v7, v11\n\t" \
  "v_max3_f32 v77, v4, v5, v6\n\t" \
  "v_add_f32_dpp v76, v78, v76 quad_perm:[2,3,0,1] row_mask:0xf bank_mask:0xf\n\t" \
  "v_max3_f32 v77, v77, v7, v76\n\t" \
  "v_smfmac_f32_16x16x64_bf16 v[56:59], v[20:23], a[232:239], v68\n\t" \
  "ds_read_b128 v[44:47], v70 offset:16384\n\t" \
  "v_max_f32_dpp v77, v77, v77 quad_perm:[2,3,0,1] row_mask:0xf bank_mask:0xf\n\t" \
  "v_mov_b32 v78, v77\n\t" \
  "v_smfmac_f32_16x16x64_bf16 v[52:55], v[24:27], v[96:103], v68\n\t" \
  "ds_read_b128 v[48:51], v70 offset:17408\n\t" \
  "v_permlane16_swap_b32 v77, v78\n\t" \
  "v_max_f32 v77, v77, v78\n\t" \
  "v_mov_b32 v78, v77\n\t" \
  "v_smfmac_f32_16x16x64_bf16 v[52:55], v[28:31], v[144:151], v68\n\t" \
  "ds_read_b128 v[16:19], v70 offset:18432\n\t" \
  "v_permlane32_swap_b32 v77, v78\n\t" \
  "v_max_f32 v77, v77, v78\n\t" \
  "v_smfmac_f32_16x16x64_bf16 v[52:55], v[32:35], v[184:191], v68\n\t" \
  "ds_read_b128 v[20:23], v70 offset:19456\n\t" \
  "v_mul_f32 v77, 0xbe0293ee, v77\n\t" \
  CH_DP7 \
  "v_smfmac_f32_16x16x64_bf16 v[56:59], v[24:27], v[104:111], v68\n\t" \
  "v_fmamk_f32 v4, v4, 0x3e0293ee, v77\n\t" \
  "v_smfmac_f32_16x16x64_bf16 v[56:59], v[28:31], v[152:159], v68\n\t" \
  "ds_read_b128 v[24:27], v70 offset:22528\n\t" \
  "s_waitcnt lgkmcnt(4)\n\t" \
  "v_fmamk_f32 v5, v5, 0x3e0293ee, v77\n\t" \
  "v_smfmac_f32_16x16x64_bf16 v[56:59], v[32:35], v[36:43], v68\n\t" \
  "ds_read_b128 v[28:31], v70 offset:23552\n\t" \
  "v_fmamk_f32 v6, v6, 0x3e0293ee, v77\n\t" \
  "v_fmamk_f32 v7, v7, 0x3e0293ee, v77\n\t" \
  "v_fmamk_f32 v76, v76, 0x3e0293ee, v77\n\t" \
  "v_exp_f32 v4, v4\n\t" \
  "v_exp_f32 v5, v5\n\t" \
  "v_exp_f32 v6, v6\n\t" \
  "v_exp_f32 v7, v7\n\t" \
  "v_cvt_pk_bf16_f32 v4, v4, v5\n\t" \
  "v_exp_f32 v76, v76\n\t" \
  "v_cvt_pk_bf16_f32 v5, v6, v7\n\t" \
  "v_mov_b32_dpp v12, v4 quad_perm:[0,1,0,1] row_mask:0xf bank_mask:0xf\n\t" \
  "v_mov_b32_dpp v14, v4 quad_perm:[2,3,2,3] row_mask:0xf bank_mask:0xf\n\t" \
  "v_mov_b32_dpp v13, v5 quad_perm:[0,1,0,1] row_mask:0xf bank_mask:0xf\n\t" \
  "v_mov_b32_dpp v15, v5 quad_perm:[2,3,2,3] row_mask:0xf bank_mask:0xf\n\t" \
  "v_add_f32 v52, v52, v54\n\t" \
  "v_add_f32 v53, v53, v55\n\t" \
  "s_waitcnt vmcnt(2)\n\t" \
  CH_DP8 \
  "v_mfma_f32_16x16x32_bf16 v[0:3], v[60:63], v[12:15], 0\n\t" \
  "ds_read_b128 v[36:39], v70 offset:20480\n\t" \
  "v_add_f32 v56, v56, v58\n\t" \
  "v_add_f32 v57, v57, v59\n\t" \
  "v_cvt_pk_bf16_f32 v52, v52, v53\n\t" \
  "v_cvt_pk_bf16_f32 v53, v56, v57\n\t" \
  "s_waitcnt vmcnt(1)\n\t" \
  "v_mfma_f32_16x16x32_bf16 v[4:7], v[64:67], v[12:15], 0\n\t" \
  "ds_read_b128 v[40:43], v70 offset:21504\n\t" \
  "v_mov_b64 v[56:57], s[88:89]\n\t" \
  "v_mov_b64 v[58:59], s[88:89]\n\t" \
  "v_mov_b32_dpp v78, v76 quad_perm:[0,1,0,1] row_mask:0xf bank_mask:0xf\n\t" \
  "v_mov_b32_dpp v79, v76 quad_perm:[2,3,2,3] row_mask:0xf bank_mask:0xf\n\t" \
  "v_mfma_f32_16x16x32_bf16 v[32:35], v[56:59], v[12:15], 0\n\t" \
  "ds_read_b128 v[60:63], v70 offset:24576\n\t" \
  "v_cvt_pk_bf16_f32 v11, v78, v79\n\t" \
  "v_cvt_pk_bf16_f32 v8, v78, v79\n\t" \
  "v_mov_b64 v[54:55], 0\n\t" \
  "v_mov_b32 v9, 0\n\t" \
  "v_mov_b32 v10, 0\n\t" \
  "v_mov_b64 v[14:15], 0\n\t" \
  "s_nop 0\n\t" \
  "v_mfma_f32_16x16x32_bf16 v[32:35], v[54:57], v[8:11], v[32:35]\n\t" \
  "buffer_store_dwordx2 v[52:53], v71, s[28:31], s69 offen offset:3072 sc1\n\t" \
  "s_cmp_eq_u32 s48, 63\n\t" \
  "v_mfma_f32_16x16x32_bf16 v[0:3], v[52:55], v[8:11], v[0:3]\n\t" \
  "ds_read_b128 v[64:67], v70 offset:25600\n\t" \
  "v_mov_b64 v[56:57], 0\n\t" \
  "v_mov_b64 v[58:59], 0\n\t" \
  "v_mfma_f32_16x16x32_bf16 v[4:7], v[52:55], v[10:13], v[4:7]\n\t" \
  "s_cbranch_scc1 LLAST2_%=\n\t" \
  "v_rcp_f32 v74, v32\n\t" \
  "v_mov_b64 v[12:13], 0\n\t" \
  "v_mov_b32 v8, 0\n\t" \
  "v_pk_mul_f32 v[0:1], v[0:1], v[74:75] op_sel_hi:[1,0]\n\t" \
  "v_pk_mul_f32 v[2:3], v[2:3], v[74:75] op_sel_hi:[1,0]\n\t" \
  "v_cvt_pk_bf16_f32 v76, v0, v1\n\t" \
  "v_cvt_pk_bf16_f32 v77, v2, v3\n\t" \
  "v_pk_mul_f32 v[4:5], v[4:5], v[74:75] op_sel_hi:[1,0]\n\t" \
  "v_pk_mul_f32 v[6:7], v[6:7], v[74:75] op_sel_hi:[1,0]\n\t" \
  "v_cvt_pk_bf16_f32 v78, v4, v5\n\t" \
  "v_cvt_pk_bf16_f32 v79, v6, v7\n\t" \
  "ds_write_b128 v69, v[76:79] offset:6144\n\t" \
  CH_DP9 \
  "s_waitcnt lgkmcnt(2)\n\t" \
  "v_smfmac_f32_16x16x64_bf16 v[12:15], v[76:79], v[44:51], v68\n\t" \
  "ds_read_b128 v[44:47], v70 offset:26624\n\t" \
  "v_mov_b32 v11, 0\n\t" \
  "v_smfmac_f32_16x16x64_bf16 v[56:59], v[76:79], v[16:23], v68\n\t" \
  "ds_read_b128 v[48:51], v70 offset:27648\n\t" \
  "v_mov_b64 v[52:53], 0\n\t" \
  "v_smfmac_f32_16x16x64_bf16 v[8:11], v[76:79], v[36:43], v68\n\t" \
  "ds_read_b128 v[16:19], v70 offset:28672\n\t" \
  "v_smfmac_f32_16x16x64_bf16 v[52:55], v[76:79], v[24:31], v68\n\t" \
  "s_waitcnt lgkmcnt(3)\n\t" \
  "s_barrier\n\t" \
  CH_DP10 \
  "ds_read_b128 v[0:3], v69 offset:7168\n\t" \
  "ds_read_b128 v[4:7], v69 offset:4096\n\t" \
  "v_lshrrev_b32 v71, 7, v69\n\t" \
  "v_and_b32 v74, 0x70, v69\n\t" \
  "v_lshl_add_u32 v71, v71, 14, v74\n\t" \
  "v_bfe_u32 v72, v69, 2, 7\n\t" \
  "v_and_b32 v74, 0x200, v69\n\t" \
  "s_waitcnt lgkmcnt(1)\n\t" \
  "v_smfmac_f32_16x16x64_bf16 v[8:11], v[0:3], v[60:67], v68\n\t" \
  "ds_read_b128 v[20:23], v70 offset:29696\n\t" \
  "v_mov_b64 v[76:77], 0\n\t" \
  "v_smfmac_f32_16x16x64_bf16 v[12:15], v[0:3], v[200:207], v68\n\t" \
  "buffer_load_dword v75, off, s[28:31], s80 sc1\n\t" \
  "ds_read_b128 v[32:35], v69 offset:5120\n\t" \
  "v_mov_b64 v[78:79], 0\n\t" \
  "v_smfmac_f32_16x16x64_bf16 v[52:55], v[0:3], v[44:51], v68\n\t" \
  "ds_read_b128 v[24:27], v70 offset:30720\n\t" \
  "v_lshl_add_u32 v72, v74, 3, v72\n\t" \
  "v_smfmac_f32_16x16x64_bf16 v[56:59], v[0:3], v[232:239], v68\n\t" \
  "ds_read_b128 v[28:31], v70 offset:31744\n\t" \
  "s_waitcnt lgkmcnt(4)\n\t" \
  "v_smfmac_f32_16x16x64_bf16 v[12:15], v[4:7], v[208:215], v68\n\t" \
  "ds_read_b128 v[60:63], v70 offset:32768\n\t" \
  "s_waitcnt lgkmcnt(4)\n\t" \
  "v_smfmac_f32_16x16x64_bf16 v[8:11], v[4:7], v[16:23], v68\n\t" \
  "ds_read_b128 v[64:67], v70 offset:33792\n\t" \
  "s_waitcnt lgkmcnt(4)\n\t" \
  "v_smfmac_f32_16x16x64_bf16 v[8:11], v[32:35], v[192:199], v68\n\t" \
  "ds_read_b128 v[36:39], v73 offset:4096\n\t" \
  "v_smfmac_f32_16x16x64_bf16 v[12:15], v[32:35], v[216:223], v68\n\t" \
  "ds_read_b128 v[40:43], v73 offset:6144\n\t" \
  "s_waitcnt lgkmcnt(4)\n\t" \
  "v_smfmac_f32_16x16x64_bf16 v[52:55], v[4:7], v[24:31], v68\n\t" \
  "ds_read_b128 v[44:47], v70 offset:34816\n\t" \
  "v_cvt_pk_bf16_f32 v8, v8, v9\n\t" \
  "v_smfmac_f32_16x16x64_bf16 v[52:55], v[32:35], v[224:231], v68\n\t" \
  "ds_read_b128 v[48:51], v70 offset:35840\n\t" \
  "v_cvt_pk_bf16_f32 v9, v10, v11\n\t" \
  "v_smfmac_f32_16x16x64_bf16 v[56:59], v[4:7], v[240:247], v68\n\t" \
  "s_waitcnt lgkmcnt(4)\n\t" \
  "v_cvt_pk_bf16_f32 v10, v12, v13\n\t" \
  "v_smfmac_f32_16x16x64_bf16 v[56:59], v[32:35], v[60:67], v68\n\t" \
  "s_waitcnt lgkmcnt(3)\n\t" \
  "v_cvt_pk_bf16_f32 v11, v14, v15\n\t" \
  "v_smfmac_f32_16x16x64_bf16 v[76:79], v[36:39], v[248:255], v68\n\t" \
  "buffer_store_dwordx4 v[8:11], v71, s[28:31], s54 offen sc1\n\t" \
  "s_waitcnt lgkmcnt(0)\n\t" \
  CH_DP11 \
  "v_cvt_pk_bf16_f32 v52, v52, v53\n\t" \
  "v_smfmac_f32_16x16x64_bf16 v[76:79], v[40:43], v[44:51], v68\n\t" \
  "v_cvt_pk_bf16_f32 v53, v54, v55\n\t" \
  "v_cvt_pk_bf16_f32 v54, v56, v57\n\t" \
  "v_cvt_pk_bf16_f32 v55, v58, v59\n\t" \
  "buffer_store_dwordx4 v[52:55], v71, s[28:31], s75 offen sc1\n\t" \
  "s_nop 2\n\t" \
  "v_add_f32 v76, v76, v78\n\t" \
  "v_add_f32 v77, v77, v79\n\t" \
  "v_cvt_pk_bf16_f32 v76, v76, v77\n\t" \
  "buffer_store_dword v76, v72, s[28:31], s55 offen sc1\n\t" \
  CH_DP12 \
  "s_memrealtime s[70:71]\n\t" \
  "s_waitcnt vmcnt(3)\n\t" \
  "v_readfirstlane_b32 s65, v75\n\t" \
  "s_barrier\n\t" \
  "s_waitcnt lgkmcnt(0)\n\t" \
  "s_not_b32 s65, s65\n\t" \
  "s_lshl_b32 s61, s70, 8\n\t" \
  "s_andn2_b32 s64, s65, 0xff\n\t" \
  "s_sub_u32 s64, s64, s61\n\t" \
  "s_ashr_i32 s64, s64, 8\n\t" \
  "s_cmp_eq_u32 s65, 0\n\t" \
  "s_cselect_b32 s64, " CH_DLF ", s64\n\t" \
  "s_min_i32 s64, s64, s34\n\t" \
  "s_max_i32 s64, s64, " CH_DLL1 "\n\t" \
  "s_add_u32 s64, s64, s70\n\t" \
  CH_ST2 \
  "s_mov_b32 s74, 0\n\t" \
  "s_movk_i32 s60, 8192\n\t" \
  "RWT2_%=:\n\t" \
  "s_memrealtime s[72:73]\n\t" \
  "s_sub_u32 s60, s60, 1\n\t" \
  "s_cselect_b32 s61, 0, -1\n\t" \
  "s_waitcnt lgkmcnt(0)\n\t" \
  "s_sub_i32 s73, s72, s64\n\t" \
  "s_and_b32 s73, s73, s61\n\t" \
  "s_cmp_lt_i32 s73, " CH_FIN "\n\t" \
  "s_cbranch_scc1 RWT2_%=\n\t" \
  "s_cmp_lt_i32 s73, 0\n\t" \
  "s_cbranch_scc1 RWT2_%=f\n\t" \
  "RWT2_%=g:\n\t" \
  "RRETRY2_%=:\n\t" \
  CH_DP13 \
  "buffer_load_dwordx4 v[0:3], v69, s[28:31], s97 offen offset:0 sc1\n\t" \
  "buffer_load_dwordx4 v[4:7], v69, s[28:31], s97 offen offset:1024 sc1\n\t" \
  "buffer_load_dwordx4 v[8:11], v69, s[28:31], s97 offen offset:2048 sc1\n\t" \
  "buffer_load_dwordx4 v[12:15], v69, s[28:31], s97 offen offset:3072 sc1\n\t" \
  "v_lshrrev_b32 v20, 2, v69\n\t" \
  "v_and_b32 v20, 16, v20\n\t" \
  "v_mov_b32 v21, 0x3f80\n\t" \
  "v_lshlrev_b32 v21, v20, v21\n\t" \
  "v_bfe_u32 v20, v69, 4, 2\n\t" \
  "v_cmp_eq_u32 vcc, 0, v20\n\t" \
  "s_nop 1\n\t" \
  "v_cndmask_b32 v32, 0, v21, vcc\n\t" \
  "v_cmp_eq_u32 vcc, 1, v20\n\t" \
  "s_nop 1\n\t" \
  "v_cndmask_b32 v33, 0, v21, vcc\n\t" \
  "v_cmp_eq_u32 vcc, 2, v20\n\t" \
  "s_nop 1\n\t" \
  "v_cndmask_b32 v34, 0, v21, vcc\n\t" \
  "v_cmp_eq_u32 vcc, 3, v20\n\t" \
  "s_nop 1\n\t" \
  "v_cndmask_b32 v35, 0, v21, vcc\n\t" \
  "v_bfe_u32 v38, v69, 7, 1\n\t" \
  "v_bfe_u32 v20, v69, 9, 1\n\t" \
  "v_lshl_add_u32 v38, v20, 2, v38\n\t" \
  "v_lshlrev_b32 v38, 12, v38\n\t" \
  "v_and_b32 v20, 0x60, v69\n\t" \
  "v_add_u32 v38, v38, v20\n\t" \
  "v_bfe_u32 v20, v69, 8, 1\n\t" \
  "v_lshl_add_u32 v38, v20, 4, v38\n\t" \
  "v_bfe_u32 v20, v69, 4, 1\n\t" \
  "v_lshl_add_u32 v38, v20, 3, v38\n\t" \
  "v_mov_b32 v44, 0\n\t" \
  CH_DP17 \
  "s_waitcnt vmcnt(3)\n\t" \
  CH_DP14 \
  "v_mfma_f32_16x16x32_bf16 v[40:43], v[32:35], v[0:3], 0\n\t" \
  "v_max3_u32 v44, v44, v0, v1\n\t" \
  "v_max3_u32 v44, v44, v2, v3\n\t" \
  "s_waitcnt vmcnt(2)\n\t" \
  "v_mfma_f32_16x16x32_bf16 v[40:43], v[32:35], v[4:7], v[40:43]\n\t" \
  "v_max3_u32 v44, v44, v4, v5\n\t" \
  "v_max3_u32 v44, v44, v6, v7\n\t" \
  "s_waitcnt vmcnt(1)\n\t" \
  "v_mfma_f32_16x16x32_bf16 v[40:43], v[32:35], v[8:11], v[40:43]\n\t" \
  "v_max3_u32 v44, v44, v8, v9\n\t" \
  "v_max3_u32 v44, v44, v10, v11\n\t" \
  "s_waitcnt vmcnt(0)\n\t" \
  CH_DP15 \
  "v_mfma_f32_16x16x32_bf16 v[40:43], v[32:35], v[12:15], v[40:43]\n\t" \
  "v_max3_u32 v44, v44, v12, v13\n\t" \
  "v_max3_u32 v44, v44, v14, v15\n\t" \
  "v_cmp_eq_u32 vcc, -1, v44\n\t" \
  "s_cbranch_vccnz RFAIL2_%=\n\t" \
  "s_nop 3\n\t" \
  "v_add_f32_dpp v40, v40, v40 row_ror:8 row_mask:0xf bank_mask:0xf\n\t" \
  "v_add_f32_dpp v41, v41, v41 row_ror:8 row_mask:0xf bank_mask:0xf\n\t" \
  "v_add_f32_dpp v42, v42, v42 row_ror:8 row_mask:0xf bank_mask:0xf\n\t" \
  "v_add_f32_dpp v43, v43, v43 row_ror:8 row_mask:0xf bank_mask:0xf\n\t" \
  "v_cvt_pk_bf16_f32 v40, v40, v41\n\t" \
  "v_cvt_pk_bf16_f32 v41, v42, v43\n\t" \
  "buffer_store_dwordx2 v[40:41], v38, s[28:31], s98 offen sc1\n\t" \
  "RDONE2_%=:\n\t" \
  CH_DP16 \
  CH_ST4 \
  "s_barrier\n\t" \
  CH_ST3 \
  CH_TRC \
  "s_add_u32 s48, s48, 1\n\t" \
  "s_mov_b32 s49, s53\n\t" \
  "s_add_u32 s53, s53, " CH_SLOT "\n\t" \
  "s_add_u32 s60, s53, " CH_SKIP "\n\t" \
  "s_lshr_b32 s61, s96, 1\n\t" \
  "s_add_u32 s86, s86, 1\n\t" \
  "s_cmp_eq_u32 s86, 3\n\t" \
  "s_cselect_b32 s53, s60, s53\n\t" \
  "s_cselect_b32 s96, s61, s96\n\t" \
  "s_cselect_b32 s86, 0, s86\n\t" \
  "s_cmp_ge_u32 s53, " CH_RING "\n\t" \
  "s_cselect_b32 s53, 0, s53\n\t" \
  "s_cselect_b32 s96, s21, s96\n\t" \
  "s_andn2_b32 s53, s53, 0x2000\n\t" \
  "s_and_b32 s60, s96, 1\n\t" \
  "s_lshl_b32 s60, s60, 13\n\t" \
  "s_or_b32 s53, s53, s60\n\t" \
  "s_mov_b32 s68, 0\n\t" \
  "s_add_u32 s97, s41, s53\n\t" \
  "s_add_u32 s98, s37, s53\n\t" \
  "s_add_u32 s82, s48, 0\n\t" \
  "s_and_b32 s82, s82, " CH_TRM "\n\t" \
  CH_SEGC \
  "s_lshr_b64 s[60:61], s[90:91], s82\n\t" \
  "s_and_b32 s60, s60, 1\n\t" \
  "s_lshl_b32 s60, s60, 13\n\t" \
  "s_add_u32 s82, s82, " CH_SG0 "\n\t" \
  "s_lshl_b32 s82, s82, 21\n\t" \
  "s_or_b32 s82, s82, s60\n\t" \
  "s_add_u32 s50, s82, s38\n\t" \
  "s_add_u32 s69, s82, s39\n\t" \
  "s_cmp_eq_u32 s34, 0\n\t" \
  "s_cbranch_scc1 LINI_%=\n\t" \
  "s_branch LG2_%=\n\t" \
  "RFAIL2_%=:\n\t" \
  "s_add_u32 s74, s74, 1\n\t" \
  "s_sleep " CH_RSL "\n\t" \
  "s_waitcnt vmcnt(0)\n\t" \
  "s_cmp_gt_u32 s74, s84\n\t" \
  "s_cbranch_scc1 RG2_%=\n\t" \
  "s_and_b32 s60, s74, 1023\n\t" \
  "s_cmp_lg_u32 s60, 0\n\t" \
  "s_cbranch_scc1 RRETRY2_%=\n\t" \
  "buffer_load_dword v46, off, s[28:31], s92 sc1\n\t" \
  "s_waitcnt vmcnt(0)\n\t" \
  "v_readfirstlane_b32 s60, v46\n\t" \
  "s_nop 0\n\t" \
  "s_cmp_eq_u32 s60, 0\n\t" \
  "s_cbranch_scc1 RRETRY2_%=\n\t" \
  "RG2_%=:\n\t" \
  "s_mov_b32 s84, 0\n\t" \
  "s_lshl_b32 s60, s36, 4\n\t" \
  "s_or_b32 s60, s60, s48\n\t" \
  "s_or_b32 s60, s60, 536870912\n\t" \
  "v_mov_b32 v76, s60\n\t" \
  "v_mov_b32 v77, 0\n\t" \
  "s_mov_b64 exec, 1\n\t" \
  "buffer_atomic_cmpswap v[76:77], off, s[28:31], s92 sc1\n\t" \
  "s_mov_b64 exec, -1\n\t" \
  "s_branch RDONE2_%=\n\t" \
  "LFAIL2_%=:\n\t" \
  "v_max_u32 v75, v0, v1\n\t" \
  "v_max3_u32 v75, v75, v2, v3\n\t" \
  "v_max3_u32 v75, v75, v4, v5\n\t" \
  "v_max3_u32 v75, v75, v6, v7\n\t" \
  "v_max3_u32 v75, v75, v8, v9\n\t" \
  "v_max3_u32 v75, v75, v10, v11\n\t" \
  "v_max3_u32 v75, v75, v12, v13\n\t" \
  "v_max3_u32 v75, v75, v14, v15\n\t" \
  "v_max3_u32 v75, v75, v16, v17\n\t" \
  "v_max3_u32 v75, v75, v18, v19\n\t" \
  "v_max3_u32 v75, v75, v20, v21\n\t" \
  "v_max3_u32 v75, v75, v22, v23\n\t" \
  "v_max3_u32 v75, v75, v24, v25\n\t" \
  "v_max3_u32 v75, v75, v26, v27\n\t" \
  "v_max3_u32 v75, v75, v28, v29\n\t" \
  "v_max3_u32 v75, v75, v30, v31\n\t" \
  "v_max3_u32 v75, v75, v32, v33\n\t" \
  "v_max3_u32 v75, v75, v34, v35\n\t" \
  "v_cmp_eq_u32 vcc, -1, v75\n\t" \
  "s_cbranch_vccz LGO2_%=\n\t" \
  "s_add_u32 s68, s68, 1\n\t" \
  "s_waitcnt lgkmcnt(0)\n\t" \
  "s_cmp_gt_u32 s68, s83\n\t" \
  "s_cbranch_scc1 LGV2_%=\n\t" \
  "s_barrier\n\t" \
  "s_waitcnt vmcnt(8)\n\t" \
  "s_branch LG2_%=\n\t" \
  "RWT2_%=f:\n\t" \
  "s_cmp_lt_i32 s73, -2\n\t" \
  "s_cbranch_scc0 RWT2_%=g\n\t" \
  "s_nop 4\n\t" \
  "s_branch RWT2_%=g\n\t" \
  "LGV2_%=:\n\t" \
  "s_mov_b32 s83, 0\n\t" \
  "s_lshl_b32 s60, s36, 4\n\t" \
  "s_or_b32 s60, s60, s48\n\t" \
  "s_or_b32 s60, s60, 805306368\n\t" \
  "v_mov_b32 v76, s60\n\t" \
  "v_mov_b32 v77, 0\n\t" \
  "s_mov_b64 exec, 1\n\t" \
  "buffer_atomic_cmpswap v[76:77], off, s[28:31], s92 sc1\n\t" \
  "s_mov_b64 exec, -1\n\t" \
  "s_branch LGO2_%=\n\t" \
  "LLAST2_%=:\n\t" \
  "s_waitcnt vmcnt(0) lgkmcnt(0)\n\t" \
  "s_barrier\n\t" \
  "s_branch LEND_%=\n\t" \
  ".p2align 6\n\t" \
  "LG3_%=:\n\t" \
  "s_barrier\n\t" \
  "buffer_load_dwordx4 v[52:55], v69, s[28:31], s50 offen sc1\n\t" \
  "buffer_load_dwordx4 v[56:59], v69, s[28:31], s50 offen offset:1024 sc1\n\t" \
  "s_add_u32 s64, s50, 16384\n\t" \
  "s_movk_i32 m0, 11264\n\t" \
  "v_lshlrev_b32 v75, 1, v69\n\t" \
  "buffer_load_dword v75, s[28:31], s64 offen sc1 lds\n\t" \
  "s_lshr_b32 s61, s20, 3\n\t" \
  "s_mul_i32 s47, s61, " CH_KD "\n\t" \
  "s_lshr_b32 s47, s47, 10\n\t" \
  "s_add_u32 s47, s47, " CH_MS "\n\t" \
  "s_lshl_b32 s47, s47, 8\n\t" \
  "s_mul_i32 s61, s61, " CH_KB "\n\t" \
  "s_lshr_b32 s61, s61, 10\n\t" \
  "s_add_u32 s61, s61, " CH_B3 "\n\t" \
  "s_min_u32 s61, s61, 100\n\t" \
  "s_or_b32 s47, s47, s61\n\t" \
  "s_cmp_lt_u32 s48, " CH_ES "\n\t" \
  "s_cselect_b32 s60, " CH_EX ", 0\n\t" \
  "s_add_u32 s47, s47, s60\n\t" \
  "s_not_b32 s47, s47\n\t" \
  "s_add_u32 s71, s53, s46\n\t" \
  "ds_read_b128 v[44:47], v70 offset:0\n\t" \
  "ds_read_b128 v[48:51], v70 offset:1024\n\t" \
  "ds_read_b128 v[60:63], v70 offset:2048\n\t" \
  "ds_read_b128 v[64:67], v70 offset:3072\n\t" \
  "v_mov_b64 v[36:37], 0\n\t" \
  "v_mov_b64 v[38:39], 0\n\t" \
  "v_mov_b64 v[40:41], 0\n\t" \
  "v_mov_b64 v[42:43], 0\n\t" \
  "v_lshrrev_b32 v71, 1, v69\n\t" \
  "v_add_u32 v71, 4, v71\n\t" \
  "v_lshrrev_b32 v72, 3, v69\n\t" \
  "v_and_b32 v72, 0x78, v72\n\t" \
  "v_and_b32 v74, 0x30, v69\n\t" \
  "v_lshl_add_u32 v72, v74, 3, v72\n\t" \
  "s_add_u32 s54, s53, s36\n\t" \
  "s_add_u32 s55, s54, 573440\n\t" \
  "s_add_u32 s54, s54, 135168\n\t" \
  "s_add_u32 s75, s54, 262144\n\t" \
  "s_add_u32 s80, s53, s26\n\t" \
  "s_or_b32 s87, s52, 0x1fff80\n\t" \
  "s_and_b32 s87, s87, -128\n\t" \
  "s_add_u32 s87, s87, 0x200000\n\t" \
  "s_barrier\n\t" \
  CH_DP0 \
  "ds_read_b128 v[0:3], v69 offset:0\n\t" \
  "ds_read_b128 v[4:7], v69 offset:1024\n\t" \
  "ds_read_b128 v[8:11], v69 offset:2048\n\t" \
  "ds_read_b128 v[12:15], v69 offset:3072\n\t" \
  "s_waitcnt lgkmcnt(3)\n\t" \
  "v_smfmac_f32_16x16x64_bf16 v[36:39], v[0:3], v[44:51], v68\n\t" \
  "ds_read_b128 v[44:47], v70 offset:4096\n\t" \
  "v_smfmac_f32_16x16x64_bf16 v[40:43], v[0:3], v[60:67], v68\n\t" \
  "ds_read_b128 v[48:51], v70 offset:5120\n\t" \
  "s_waitcnt lgkmcnt(4)\n\t" \
  "v_smfmac_f32_16x16x64_bf16 v[36:39], v[4:7], a[32:39], v68\n\t" \
  "ds_read_b128 v[60:63], v70 offset:6144\n\t" \
  "v_smfmac_f32_16x16x64_bf16 v[40:43], v[4:7], a[40:47], v68\n\t" \
  "ds_read_b128 v[64:67], v70 offset:7168\n\t" \
  "s_waitcnt lgkmcnt(5)\n\t" \
  "v_smfmac_f32_16x16x64_bf16 v[36:39], v[8:11], a[80:87], v68\n\t" \
  "s_barrier\n\t" \
  CH_DP1 \
  "ds_read_b128 v[16:19], v69 offset:4096\n\t" \
  "v_smfmac_f32_16x16x64_bf16 v[40:43], v[8:11], a[88:95], v68\n\t" \
  "ds_read_b128 v[20:23], v69 offset:5120\n\t" \
  "s_waitcnt lgkmcnt(6)\n\t" \
  "v_smfmac_f32_16x16x64_bf16 v[36:39], v[12:15], a[120:127], v68\n\t" \
  "ds_read_b128 v[24:27], v69 offset:6144\n\t" \
  "s_waitcnt lgkmcnt(5)\n\t" \
  "v_smfmac_f32_16x16x64_bf16 v[40:43], v[12:15], v[44:51], v68\n\t" \
  "ds_read_b128 v[28:31], v69 offset:7168\n\t" \
  "s_waitcnt lgkmcnt(3)\n\t" \
  "v_smfmac_f32_16x16x64_bf16 v[36:39], v[16:19], a[160:167], v68\n\t" \
  "ds_read_b128 v[32:35], v69 offset:8192\n\t" \
  "v_smfmac_f32_16x16x64_bf16 v[40:43], v[16:19], a[168:175], v68\n\t" \
  "s_waitcnt lgkmcnt(3)\n\t" \
  "v_mov_b64 v[44:45], 0\n\t" \
  "v_smfmac_f32_16x16x64_bf16 v[36:39], v[20:23], a[200:207], v68\n\t" \
  "s_waitcnt lgkmcnt(2)\n\t" \
  "v_mov_b64 v[46:47], 0\n\t" \
  "v_smfmac_f32_16x16x64_bf16 v[36:39], v[24:27], a[240:247], v68\n\t" \
  "s_waitcnt lgkmcnt(1)\n\t" \
  "v_mov_b64 v[48:49], 0\n\t" \
  "v_smfmac_f32_16x16x64_bf16 v[36:39], v[28:31], v[112:119], v68\n\t" \
  "s_waitcnt lgkmcnt(0)\n\t" \
  "v_mov_b64 v[50:51], 0\n\t" \
  "v_smfmac_f32_16x16x64_bf16 v[36:39], v[32:35], v[160:167], v68\n\t" \
  "v_smfmac_f32_16x16x64_bf16 v[40:43], v[20:23], v[60:67], v68\n\t" \
  "ds_read_b128 v[60:63], v70 offset:8192\n\t" \
  "v_smfmac_f32_16x16x64_bf16 v[40:43], v[24:27], a[248:255], v68\n\t" \
  "ds_read_b128 v[64:67], v70 offset:9216\n\t" \
  "v_add_f32 v74, v36, v38\n\t" \
  "v_smfmac_f32_16x16x64_bf16 v[40:43], v[28:31], v[120:127], v68\n\t" \
  "v_add_f32 v37, v37, v39\n\t" \
  "v_smfmac_f32_16x16x64_bf16 v[40:43], v[32:35], v[168:175], v68\n\t" \
  "v_cmp_u_f32 vcc, v74, v37\n\t" \
  CH_DP2 \
  "v_smfmac_f32_16x16x64_bf16 v[44:47], v[0:3], a[0:7], v68\n\t" \
  "s_cbranch_vccnz LFAIL3_%=\n\t" \
  "LGO3_%=:\n\t" \
  "v_cvt_pk_bf16_f32 v74, v74, v37\n\t" \
  "v_smfmac_f32_16x16x64_bf16 v[44:47], v[4:7], a[48:55], v68\n\t" \
  "s_waitcnt lgkmcnt(0)\n\t" \
  "v_add_f32 v75, v40, v42\n\t" \
  "v_smfmac_f32_16x16x64_bf16 v[44:47], v[8:11], v[60:67], v68\n\t" \
  "ds_read_b128 v[60:63], v70 offset:10240\n\t" \
  "v_add_f32 v41, v41, v43\n\t" \
  "v_cvt_pk_bf16_f32 v75, v75, v41\n\t" \
  "v_smfmac_f32_16x16x64_bf16 v[44:47], v[12:15], a[128:135], v68\n\t" \
  "ds_write2st64_b32 v71, v74, v75 offset0:40 offset1:42\n\t" \
  "v_smfmac_f32_16x16x64_bf16 v[44:47], v[16:19], a[176:183], v68\n\t" \
  "ds_read_b128 v[64:67], v70 offset:11264\n\t" \
  "s_memrealtime s[66:67]\n\t" \
  "v_smfmac_f32_16x16x64_bf16 v[44:47], v[20:23], a[208:215], v68\n\t" \
  "v_smfmac_f32_16x16x64_bf16 v[44:47], v[24:27], v[80:87], v68\n\t" \
  "v_smfmac_f32_16x16x64_bf16 v[44:47], v[28:31], v[128:135], v68\n\t" \
  "s_waitcnt lgkmcnt(0)\n\t" \
  "v_smfmac_f32_16x16x64_bf16 v[44:47], v[32:35], v[60:67], v68\n\t" \
  "s_barrier\n\t" \
  "ds_read_b64_tr_b16 v[76:77], v72 offset:10240\n\t" \
  CH_DP3 \
  "v_smfmac_f32_16x16x64_bf16 v[48:51], v[0:3], a[8:15], v68\n\t" \
  "s_lshl_b32 s60, s66, 8\n\t" \
  "s_sub_u32 s60, s47, s60\n\t" \
  "ds_read_b64_tr_b16 v[78:79], v72 offset:10752\n\t" \
  "v_mov_b32 v75, s60\n\t" \
  "v_smfmac_f32_16x16x64_bf16 v[48:51], v[4:7], a[56:63], v68\n\t" \
  "s_mov_b64 exec, s[24:25]\n\t" \
  "buffer_atomic_umin v75, v69, s[28:31], s71 offen sc1\n\t" \
  "s_mov_b64 exec, -1\n\t" \
  "v_add_f32 v74, v44, v46\n\t" \
  "v_smfmac_f32_16x16x64_bf16 v[48:51], v[8:11], a[96:103], v68\n\t" \
  "buffer_load_dwordx4 v[60:63], v69, s[28:31], s64 offen sc1\n\t" \
  "v_add_f32 v45, v45, v47\n\t" \
  "v_smfmac_f32_16x16x64_bf16 v[48:51], v[12:15], a[136:143], v68\n\t" \
  "s_waitcnt vmcnt(4)\n\t" \
  "s_waitcnt lgkmcnt(0)\n\t" \
  "v_cvt_pk_bf16_f32 v74, v74, v45\n\t" \
  "v_mfma_f32_16x16x32_bf16 v[36:39], v[52:55], v[76:79], 0\n\t" \
  "s_waitcnt vmcnt(3)\n\t" \
  "v_mfma_f32_16x16x32_bf16 v[40:43], v[56:59], v[76:79], 0\n\t" \
  "v_mov_b64 v[52:53], 0\n\t" \
  "v_mov_b64 v[54:55], 0\n\t" \
  "v_smfmac_f32_16x16x64_bf16 v[48:51], v[16:19], a[184:191], v68\n\t" \
  "buffer_load_dwordx4 v[64:67], v69, s[28:31], s64 offen offset:1024 sc1\n\t" \
  "v_mov_b64 v[56:57], 0\n\t" \
  "v_mov_b64 v[58:59], 0\n\t" \
  "v_smfmac_f32_16x16x64_bf16 v[48:51], v[20:23], a[216:223], v68\n\t" \
  "ds_write_b32 v71, v74 offset:1024\n\t" \
  "v_add_f32_dpp v36, v40, v36 quad_perm:[2,3,0,1] row_mask:0xf bank_mask:0xf\n\t" \
  "v_smfmac_f32_16x16x64_bf16 v[48:51], v[24:27], v[88:95], v68\n\t" \
  "v_add_f32_dpp v37, v41, v37 quad_perm:[2,3,0,1] row_mask:0xf bank_mask:0xf\n\t" \
  "v_smfmac_f32_16x16x64_bf16 v[48:51], v[28:31], v[136:143], v68\n\t" \
  "v_add_f32_dpp v38, v42, v38 quad_perm:[2,3,0,1] row_mask:0xf bank_mask:0xf\n\t" \
  "v_smfmac_f32_16x16x64_bf16 v[48:51], v[32:35], v[176:183], v68\n\t" \
  "v_add_f32_dpp v39, v43, v39 quad_perm:[2,3,0,1] row_mask:0xf bank_mask:0xf\n\t" \
  CH_DP4 \
  "v_smfmac_f32_16x16x64_bf16 v[52:55], v[0:3], a[16:23], v68\n\t" \
  "s_mov_b64 exec, s[58:59]\n\t" \
  "ds_write_b128 v69, v[36:39] offset:3104\n\t" \
  "s_mov_b64 exec, -1\n\t" \
  "v_smfmac_f32_16x16x64_bf16 v[52:55], v[4:7], a[64:71], v68\n\t" \
  "v_add_f32 v75, v48, v50\n\t" \
  "v_add_f32 v49, v49, v51\n\t" \
  "v_cvt_pk_bf16_f32 v75, v75, v49\n\t" \
  "v_smfmac_f32_16x16x64_bf16 v[56:59], v[0:3], a[24:31], v68\n\t" \
  "ds_write_b32 v71, v75 offset:1536\n\t" \
  "v_smfmac_f32_16x16x64_bf16 v[56:59], v[4:7], a[72:79], v68\n\t" \
  "ds_read_b64_tr_b16 v[0:1], v72 offset:9216\n\t" \
  "ds_read_b64_tr_b16 v[2:3], v72 offset:9728\n\t" \
  "v_smfmac_f32_16x16x64_bf16 v[52:55], v[8:11], a[104:111], v68\n\t" \
  "ds_read_b128 v[36:39], v70 offset:12288\n\t" \
  "v_smfmac_f32_16x16x64_bf16 v[56:59], v[8:11], a[112:119], v68\n\t" \
  "buffer_load_dword off, s[28:31], s87 sc1 lds\n\t" \
  "ds_read_b128 v[40:43], v70 offset:13312\n\t" \
  "v_smfmac_f32_16x16x64_bf16 v[52:55], v[12:15], a[144:151], v68\n\t" \
  "s_waitcnt lgkmcnt(4)\n\t" \
  "s_barrier\n\t" \
  CH_DP5 \
  "ds_read_b64_tr_b16 v[44:45], v72 offset:1024\n\t" \
  "ds_read_b64_tr_b16 v[46:47], v72 offset:1536\n\t" \
  "v_smfmac_f32_16x16x64_bf16 v[56:59], v[12:15], a[152:159], v68\n\t" \
  "ds_read_b64_tr_b16 v[48:49], v72 offset:0\n\t" \
  "ds_read_b64_tr_b16 v[50:51], v72 offset:512\n\t" \
  "v_smfmac_f32_16x16x64_bf16 v[52:55], v[16:19], a[192:199], v68\n\t" \
  "ds_read_b128 v[4:7], v69 offset:2048\n\t" \
  "s_waitcnt lgkmcnt(5)\n\t" \
  "v_smfmac_f32_16x16x64_bf16 v[56:59], v[16:19], v[36:43], v68\n\t" \
  "ds_read_b128 v[8:11], v69 offset:3072\n\t" \
  "s_waitcnt lgkmcnt(4)\n\t" \
  "v_mfma_f32_16x16x32_bf16 v[12:15], v[44:47], v[76:79], 0\n\t" \
  "ds_read_b128 v[36:39], v70 offset:14336\n\t" \
  "s_waitcnt lgkmcnt(3)\n\t" \
  "v_mfma_f32_16x16x32_bf16 v[12:15], v[48:51], v[0:3], v[12:15]\n\t" \
  "ds_read_b128 v[40:43], v70 offset:15360\n\t" \
  "v_smfmac_f32_16x16x64_bf16 v[52:55], v[20:23], a[224:231], v68\n\t" \
  "s_waitcnt lgkmcnt(2)\n\t" \
  "v_add_f32 v4, v4, v8\n\t" \
  "v_add_f32 v5, v5, v9\n\t" \
  "v_add_f32 v6, v6, v10\n\t" \
  CH_DP6 \
  "v_cndmask_b32 v78, v13, v14, s[56:57]\n\t" \
  "v_cndmask_b32 v76, v12, v15, s[56:57]\n\t" \
  "v_add_f32 v7, v7, v11\n\t" \
  "v_max3_f32 v77, v4, v5, v6\n\t" \
  "v_add_f32_dpp v76, v78, v76 quad_perm:[2,3,0,1] row_mask:0xf bank_mask:0xf\n\t" \
  "v_max3_f32 v77, v77, v7, v76\n\t" \
  "v_smfmac_f32_16x16x64_bf16 v[56:59], v[20:23], a[232:239], v68\n\t" \
  "ds_read_b128 v[44:47], v70 offset:16384\n\t" \
  "v_max_f32_dpp v77, v77, v77 quad_perm:[2,3,0,1] row_mask:0xf bank_mask:0xf\n\t" \
  "v_mov_b32 v78, v77\n\t" \
  "v_smfmac_f32_16x16x64_bf16 v[52:55], v[24:27], v[96:103], v68\n\t" \
  "ds_read_b128 v[48:51], v70 offset:17408\n\t" \
  "v_permlane16_swap_b32 v77, v78\n\t" \
  "v_max_f32 v77, v77, v78\n\t" \
  "v_mov_b32 v78, v77\n\t" \
  "v_smfmac_f32_16x16x64_bf16 v[52:55], v[28:31], v[144:151], v68\n\t" \
  "ds_read_b128 v[16:19], v70 offset:18432\n\t" \
  "v_permlane32_swap_b32 v77, v78\n\t" \
  "v_max_f32 v77, v77, v78\n\t" \
  "v_smfmac_f32_16x16x64_bf16 v[52:55], v[32:35], v[184:191], v68\n\t" \
  "ds_read_b128 v[20:23], v70 offset:19456\n\t" \
  "v_mul_f32 v77, 0xbe0293ee, v77\n\t" \
  CH_DP7 \
  "v_smfmac_f32_16x16x64_bf16 v[56:59], v[24:27], v[104:111], v68\n\t" \
  "v_fmamk_f32 v4, v4, 0x3e0293ee, v77\n\t" \
  "v_smfmac_f32_16x16x64_bf16 v[56:59], v[28:31], v[152:159], v68\n\t" \
  "ds_read_b128 v[24:27], v70 offset:22528\n\t" \
  "s_waitcnt lgkmcnt(4)\n\t" \
  "v_fmamk_f32 v5, v5, 0x3e0293ee, v77\n\t" \
  "v_smfmac_f32_16x16x64_bf16 v[56:59], v[32:35], v[36:43], v68\n\t" \
  "ds_read_b128 v[28:31], v70 offset:23552\n\t" \
  "v_fmamk_f32 v6, v6, 0x3e0293ee, v77\n\t" \
  "v_fmamk_f32 v7, v7, 0x3e0293ee, v77\n\t" \
  "v_fmamk_f32 v76, v76, 0x3e0293ee, v77\n\t" \
  "v_exp_f32 v4, v4\n\t" \
  "v_exp_f32 v5, v5\n\t" \
  "v_exp_f32 v6, v6\n\t" \
  "v_exp_f32 v7, v7\n\t" \
  "v_cvt_pk_bf16_f32 v4, v4, v5\n\t" \
  "v_exp_f32 v76, v76\n\t" \
  "v_cvt_pk_bf16_f32 v5, v6, v7\n\t" \
  "v_mov_b32_dpp v12, v4 quad_perm:[0,1,0,1] row_mask:0xf bank_mask:0xf\n\t" \
  "v_mov_b32_dpp v14, v4 quad_perm:[2,3,2,3] row_mask:0xf bank_mask:0xf\n\t" \
  "v_mov_b32_dpp v13, v5 quad_perm:[0,1,0,1] row_mask:0xf bank_mask:0xf\n\t" \
  "v_mov_b32_dpp v15, v5 quad_perm:[2,3,2,3] row_mask:0xf bank_mask:0xf\n\t" \
  "v_add_f32 v52, v52, v54\n\t" \
  "v_add_f32 v53, v53, v55\n\t" \
  "s_waitcnt vmcnt(2)\n\t" \
  CH_DP8 \
  "v_mfma_f32_16x16x32_bf16 v[0:3], v[60:63], v[12:15], 0\n\t" \
  "ds_read_b128 v[36:39], v70 offset:20480\n\t" \
  "v_add_f32 v56, v56, v58\n\t" \
  "v_add_f32 v57, v57, v59\n\t" \
  "v_cvt_pk_bf16_f32 v52, v52, v53\n\t" \
  "v_cvt_pk_bf16_f32 v53, v56, v57\n\t" \
  "s_waitcnt vmcnt(1)\n\t" \
  "v_mfma_f32_16x16x32_bf16 v[4:7], v[64:67], v[12:15], 0\n\t" \
  "ds_read_b128 v[40:43], v70 offset:21504\n\t" \
  "v_mov_b64 v[56:57], s[88:89]\n\t" \
  "v_mov_b64 v[58:59], s[88:89]\n\t" \
  "v_mov_b32_dpp v78, v76 quad_perm:[0,1,0,1] row_mask:0xf bank_mask:0xf\n\t" \
  "v_mov_b32_dpp v79, v76 quad_perm:[2,3,2,3] row_mask:0xf bank_mask:0xf\n\t" \
  "v_mfma_f32_16x16x32_bf16 v[32:35], v[56:59], v[12:15], 0\n\t" \
  "ds_read_b128 v[60:63], v70 offset:24576\n\t" \
  "v_cvt_pk_bf16_f32 v11, v78, v79\n\t" \
  "v_cvt_pk_bf16_f32 v8, v78, v79\n\t" \
  "v_mov_b64 v[54:55], 0\n\t" \
  "v_mov_b32 v9, 0\n\t" \
  "v_mov_b32 v10, 0\n\t" \
  "v_mov_b64 v[14:15], 0\n\t" \
  "s_nop 0\n\t" \
  "v_mfma_f32_16x16x32_bf16 v[32:35], v[54:57], v[8:11], v[32:35]\n\t" \
  "s_cmp_eq_u32 s48, 63\n\t" \
  "v_mfma_f32_16x16x32_bf16 v[0:3], v[52:55], v[8:11], v[0:3]\n\t" \
  "buffer_store_dwordx2 v[52:53], v71, s[28:31], s69 offen offset:3580 sc1\n\t" \
  "ds_read_b128 v[64:67], v70 offset:25600\n\t" \
  "v_mov_b64 v[56:57], 0\n\t" \
  "v_mov_b64 v[58:59], 0\n\t" \
  "v_mfma_f32_16x16x32_bf16 v[4:7], v[52:55], v[10:13], v[4:7]\n\t" \
  "s_cbranch_scc1 LLAST3_%=\n\t" \
  "v_rcp_f32 v74, v32\n\t" \
  "v_mov_b64 v[12:13], 0\n\t" \
  "v_mov_b32 v8, 0\n\t" \
  "v_pk_mul_f32 v[0:1], v[0:1], v[74:75] op_sel_hi:[1,0]\n\t" \
  "v_pk_mul_f32 v[2:3], v[2:3], v[74:75] op_sel_hi:[1,0]\n\t" \
  "v_cvt_pk_bf16_f32 v76, v0, v1\n\t" \
  "v_cvt_pk_bf16_f32 v77, v2, v3\n\t" \
  "v_pk_mul_f32 v[4:5], v[4:5], v[74:75] op_sel_hi:[1,0]\n\t" \
  "v_pk_mul_f32 v[6:7], v[6:7], v[74:75] op_sel_hi:[1,0]\n\t" \
  "v_cvt_pk_bf16_f32 v78, v4, v5\n\t" \
  "v_cvt_pk_bf16_f32 v79, v6, v7\n\t" \
  "ds_write_b128 v69, v[76:79] offset:7168\n\t" \
  CH_DP9 \
  "s_waitcnt lgkmcnt(2)\n\t" \
  "v_smfmac_f32_16x16x64_bf16 v[12:15], v[76:79], v[44:51], v68\n\t" \
  "ds_read_b128 v[44:47], v70 offset:26624\n\t" \
  "v_mov_b32 v11, 0\n\t" \
  "v_smfmac_f32_16x16x64_bf16 v[56:59], v[76:79], v[16:23], v68\n\t" \
  "ds_read_b128 v[48:51], v70 offset:27648\n\t" \
  "v_mov_b64 v[52:53], 0\n\t" \
  "v_smfmac_f32_16x16x64_bf16 v[8:11], v[76:79], v[36:43], v68\n\t" \
  "ds_read_b128 v[16:19], v70 offset:28672\n\t" \
  "v_smfmac_f32_16x16x64_bf16 v[52:55], v[76:79], v[24:31], v68\n\t" \
  "s_waitcnt lgkmcnt(3)\n\t" \
  "s_barrier\n\t" \
  CH_DP10 \
  "ds_read_b128 v[0:3], v69 offset:6144\n\t" \
  "ds_read_b128 v[4:7], v69 offset:5120\n\t" \
  "v_lshrrev_b32 v71, 7, v69\n\t" \
  "v_and_b32 v74, 0x70, v69\n\t" \
  "v_lshl_add_u32 v71, v71, 14, v74\n\t" \
  "v_bfe_u32 v72, v69, 2, 7\n\t" \
  "v_and_b32 v74, 0x200, v69\n\t" \
  "s_waitcnt lgkmcnt(1)\n\t" \
  "v_smfmac_f32_16x16x64_bf16 v[8:11], v[0:3], v[60:67], v68\n\t" \
  "ds_read_b128 v[20:23], v70 offset:29696\n\t" \
  "v_mov_b64 v[76:77], 0\n\t" \
  "v_smfmac_f32_16x16x64_bf16 v[12:15], v[0:3], v[200:207], v68\n\t" \
  "ds_read_b128 v[32:35], v69 offset:4096\n\t" \
  "v_mov_b64 v[78:79], 0\n\t" \
  "v_smfmac_f32_16x16x64_bf16 v[52:55], v[0:3], v[44:51], v68\n\t" \
  "buffer_load_dword v75, off, s[28:31], s80 sc1\n\t" \
  "ds_read_b128 v[24:27], v70 offset:30720\n\t" \
  "v_lshl_add_u32 v72, v74, 3, v72\n\t" \
  "v_smfmac_f32_16x16x64_bf16 v[56:59], v[0:3], v[232:239], v68\n\t" \
  "ds_read_b128 v[28:31], v70 offset:31744\n\t" \
  "s_waitcnt lgkmcnt(4)\n\t" \
  "v_smfmac_f32_16x16x64_bf16 v[12:15], v[4:7], v[208:215], v68\n\t" \
  "ds_read_b128 v[60:63], v70 offset:32768\n\t" \
  "s_waitcnt lgkmcnt(4)\n\t" \
  "v_smfmac_f32_16x16x64_bf16 v[8:11], v[4:7], v[16:23], v68\n\t" \
  "ds_read_b128 v[64:67], v70 offset:33792\n\t" \
  "s_waitcnt lgkmcnt(4)\n\t" \
  "v_smfmac_f32_16x16x64_bf16 v[8:11], v[32:35], v[192:199], v68\n\t" \
  "ds_read_b128 v[36:39], v73 offset:4096\n\t" \
  "v_smfmac_f32_16x16x64_bf16 v[12:15], v[32:35], v[216:223], v68\n\t" \
  "ds_read_b128 v[40:43], v73 offset:6144\n\t" \
  "s_waitcnt lgkmcnt(4)\n\t" \
  "v_smfmac_f32_16x16x64_bf16 v[52:55], v[4:7], v[24:31], v68\n\t" \
  "ds_read_b128 v[44:47], v70 offset:34816\n\t" \
  "v_cvt_pk_bf16_f32 v8, v8, v9\n\t" \
  "v_smfmac_f32_16x16x64_bf16 v[52:55], v[32:35], v[224:231], v68\n\t" \
  "ds_read_b128 v[48:51], v70 offset:35840\n\t" \
  "v_cvt_pk_bf16_f32 v9, v10, v11\n\t" \
  "v_smfmac_f32_16x16x64_bf16 v[56:59], v[4:7], v[240:247], v68\n\t" \
  "s_waitcnt lgkmcnt(4)\n\t" \
  "v_cvt_pk_bf16_f32 v10, v12, v13\n\t" \
  "v_smfmac_f32_16x16x64_bf16 v[56:59], v[32:35], v[60:67], v68\n\t" \
  "s_waitcnt lgkmcnt(3)\n\t" \
  "v_cvt_pk_bf16_f32 v11, v14, v15\n\t" \
  "v_smfmac_f32_16x16x64_bf16 v[76:79], v[36:39], v[248:255], v68\n\t" \
  "buffer_store_dwordx4 v[8:11], v71, s[28:31], s54 offen sc1\n\t" \
  "s_waitcnt lgkmcnt(0)\n\t" \
  CH_DP11 \
  "v_cvt_pk_bf16_f32 v52, v52, v53\n\t" \
  "v_smfmac_f32_16x16x64_bf16 v[76:79], v[40:43], v[44:51], v68\n\t" \
  "v_cvt_pk_bf16_f32 v53, v54, v55\n\t" \
  "v_cvt_pk_bf16_f32 v54, v56, v57\n\t" \
  "v_cvt_pk_bf16_f32 v55, v58, v59\n\t" \
  "buffer_store_dwordx4 v[52:55], v71, s[28:31], s75 offen sc1\n\t" \
  "s_nop 2\n\t" \
  "v_add_f32 v76, v76, v78\n\t" \
  "v_add_f32 v77, v77, v79\n\t" \
  "v_cvt_pk_bf16_f32 v76, v76, v77\n\t" \
  "buffer_store_dword v76, v72, s[28:31], s55 offen sc1\n\t" \
  CH_DP12 \
  "s_memrealtime s[70:71]\n\t" \
  "s_waitcnt vmcnt(3)\n\t" \
  "v_readfirstlane_b32 s65, v75\n\t" \
  "s_barrier\n\t" \
  "s_waitcnt lgkmcnt(0)\n\t" \
  "s_not_b32 s65, s65\n\t" \
  "s_lshl_b32 s61, s70, 8\n\t" \
  "s_andn2_b32 s64, s65, 0xff\n\t" \
  "s_sub_u32 s64, s64, s61\n\t" \
  "s_ashr_i32 s64, s64, 8\n\t" \
  "s_cmp_eq_u32 s65, 0\n\t" \
  "s_cselect_b32 s64, " CH_DLF ", s64\n\t" \
  "s_add_i32 s64, s64, " CH_RO3 "\n\t" \
  "s_min_i32 s64, s64, s34\n\t" \
  "s_max_i32 s64, s64, " CH_DLL1 "\n\t" \
  "s_add_u32 s64, s64, s70\n\t" \
  CH_ST2 \
  "s_mov_b32 s74, 0\n\t" \
  "v_and_b32 v37, 0x30, v69\n\t" \
  "v_add_u32 v37, s52, v37\n\t" \
  "s_mov_b32 exec_lo, 0\n\t" \
  "v_mov_b32 v37, s85\n\t" \
  "s_mov_b32 exec_lo, -1\n\t" \
  "s_cmp_lt_i32 s41, 0\n\t" \
  "s_cbranch_scc1 LIDLE_%=\n\t" \
  "s_movk_i32 s60, 8192\n\t" \
  "RWT3_%=:\n\t" \
  "s_memrealtime s[72:73]\n\t" \
  "s_sub_u32 s60, s60, 1\n\t" \
  "s_cselect_b32 s61, 0, -1\n\t" \
  "s_waitcnt lgkmcnt(0)\n\t" \
  "s_sub_i32 s73, s72, s64\n\t" \
  "s_and_b32 s73, s73, s61\n\t" \
  "s_cmp_lt_i32 s73, " CH_FIN "\n\t" \
  "s_cbranch_scc1 RWT3_%=\n\t" \
  "s_cmp_lt_i32 s73, 0\n\t" \
  "s_cbranch_scc1 RWT3_%=f\n\t" \
  "RWT3_%=g:\n\t" \
  "RRETRY3_%=:\n\t" \
  CH_DP13 \
  "buffer_load_dwordx4 v[0:3], v69, s[28:31], s97 offen offset:0 sc1\n\t" \
  "buffer_load_dwordx4 v[4:7], v69, s[28:31], s97 offen offset:1024 sc1\n\t" \
  "buffer_load_dwordx4 v[8:11], v69, s[28:31], s97 offen offset:2048 sc1\n\t" \
  "buffer_load_dwordx4 v[12:15], v69, s[28:31], s97 offen offset:3072 sc1\n\t" \
  "v_lshrrev_b32 v20, 2, v69\n\t" \
  "v_and_b32 v20, 16, v20\n\t" \
  "v_mov_b32 v21, 0x3f80\n\t" \
  "v_lshlrev_b32 v21, v20, v21\n\t" \
  "v_bfe_u32 v20, v69, 4, 2\n\t" \
  "v_cmp_eq_u32 vcc, 0, v20\n\t" \
  "s_nop 1\n\t" \
  "v_cndmask_b32 v32, 0, v21, vcc\n\t" \
  "v_cmp_eq_u32 vcc, 1, v20\n\t" \
  "s_nop 1\n\t" \
  "v_cndmask_b32 v33, 0, v21, vcc\n\t" \
  "v_cmp_eq_u32 vcc, 2, v20\n\t" \
  "s_nop 1\n\t" \
  "v_cndmask_b32 v34, 0, v21, vcc\n\t" \
  "v_cmp_eq_u32 vcc, 3, v20\n\t" \
  "s_nop 1\n\t" \
  "v_cndmask_b32 v35, 0, v21, vcc\n\t" \
  "v_bfe_u32 v38, v69, 7, 1\n\t" \
  "v_bfe_u32 v20, v69, 9, 1\n\t" \
  "v_lshl_add_u32 v38, v20, 2, v38\n\t" \
  "v_lshlrev_b32 v38, 12, v38\n\t" \
  "v_and_b32 v20, 0x60, v69\n\t" \
  "v_add_u32 v38, v38, v20\n\t" \
  "v_bfe_u32 v20, v69, 8, 1\n\t" \
  "v_lshl_add_u32 v38, v20, 4, v38\n\t" \
  "v_bfe_u32 v20, v69, 4, 1\n\t" \
  "v_lshl_add_u32 v38, v20, 3, v38\n\t" \
  "v_mov_b32 v44, 0\n\t" \
  "buffer_load_dword v39, v37, s[28:31], 0 offen sc1\n\t" \
  CH_DP17 \
  "s_waitcnt vmcnt(4)\n\t" \
  CH_DP14 \
  "v_mfma_f32_16x16x32_bf16 v[40:43], v[32:35], v[0:3], 0\n\t" \
  "v_max3_u32 v44, v44, v0, v1\n\t" \
  "v_max3_u32 v44, v44, v2, v3\n\t" \
  "s_waitcnt vmcnt(3)\n\t" \
  "v_mfma_f32_16x16x32_bf16 v[40:43], v[32:35], v[4:7], v[40:43]\n\t" \
  "v_max3_u32 v44, v44, v4, v5\n\t" \
  "v_max3_u32 v44, v44, v6, v7\n\t" \
  "s_waitcnt vmcnt(2)\n\t" \
  "v_mfma_f32_16x16x32_bf16 v[40:43], v[32:35], v[8:11], v[40:43]\n\t" \
  "v_max3_u32 v44, v44, v8, v9\n\t" \
  "v_max3_u32 v44, v44, v10, v11\n\t" \
  "s_waitcnt vmcnt(1)\n\t" \
  CH_DP15 \
  "v_mfma_f32_16x16x32_bf16 v[40:43], v[32:35], v[12:15], v[40:43]\n\t" \
  "v_max3_u32 v44, v44, v12, v13\n\t" \
  "v_max3_u32 v44, v44, v14, v15\n\t" \
  "v_cmp_eq_u32 vcc, -1, v44\n\t" \
  "s_cbranch_vccnz RFAIL3_%=\n\t" \
  "s_nop 3\n\t" \
  "v_add_f32_dpp v40, v40, v40 row_ror:8 row_mask:0xf bank_mask:0xf\n\t" \
  "v_add_f32_dpp v41, v41, v41 row_ror:8 row_mask:0xf bank_mask:0xf\n\t" \
  "v_add_f32_dpp v42, v42, v42 row_ror:8 row_mask:0xf bank_mask:0xf\n\t" \
  "v_add_f32_dpp v43, v43, v43 row_ror:8 row_mask:0xf bank_mask:0xf\n\t" \
  "v_cvt_pk_bf16_f32 v40, v40, v41\n\t" \
  "v_cvt_pk_bf16_f32 v41, v42, v43\n\t" \
  "buffer_store_dwordx2 v[40:41], v38, s[28:31], s98 offen sc1\n\t" \
  "RDONE3_%=:\n\t" \
  CH_DP16 \
  CH_ST4 \
  "s_barrier\n\t" \
  CH_ST3 \
  "s_waitcnt vmcnt(1)\n\t" \
  "v_cmp_eq_u32 vcc, 0, v39\n\t" \
  "s_sub_u32 s60, s70, s66\n\t" \
  "s_add_u32 s81, s81, 1\n\t" \
  "s_min_u32 s81, s81, s60\n\t" \
  "s_add_u32 s61, s81, 8\n\t" \
  "s_min_u32 s60, s60, s61\n\t" \
  "s_lshl_b32 s60, s60, 2\n\t" \
  "s_lshr_b32 s61, s20, 3\n\t" \
  "s_sub_u32 s20, s20, s61\n\t" \
  "s_add_u32 s20, s20, s60\n\t" \
  CH_TRC \
  "s_add_u32 s48, s48, 1\n\t" \
  "s_mov_b32 s49, s53\n\t" \
  "s_add_u32 s53, s53, " CH_SLOT "\n\t" \
  "s_add_u32 s60, s53, " CH_SKIP "\n\t" \
  "s_lshr_b32 s61, s96, 1\n\t" \
  "s_add_u32 s86, s86, 1\n\t" \
  "s_cmp_eq_u32 s86, 3\n\t" \
  "s_cselect_b32 s53, s60, s53\n\t" \
  "s_cselect_b32 s96, s61, s96\n\t" \
  "s_cselect_b32 s86, 0, s86\n\t" \
  "s_cmp_ge_u32 s53, " CH_RING "\n\t" \
  "s_cselect_b32 s53, 0, s53\n\t" \
  "s_cselect_b32 s96, s21, s96\n\t" \
  "s_andn2_b32 s53, s53, 0x2000\n\t" \
  "s_and_b32 s60, s96, 1\n\t" \
  "s_lshl_b32 s60, s60, 13\n\t" \
  "s_or_b32 s53, s53, s60\n\t" \
  "s_mov_b32 s68, 0\n\t" \
  "s_add_u32 s97, s41, s53\n\t" \
  "s_add_u32 s98, s37, s53\n\t" \
  "s_add_u32 s82, s48, 0\n\t" \
  "s_and_b32 s82, s82, " CH_TRM "\n\t" \
  CH_SEGC \
  "s_lshr_b64 s[60:61], s[90:91], s82\n\t" \
  "s_and_b32 s60, s60, 1\n\t" \
  "s_lshl_b32 s60, s60, 13\n\t" \
  "s_add_u32 s82, s82, " CH_SG0 "\n\t" \
  "s_lshl_b32 s82, s82, 21\n\t" \
  "s_or_b32 s82, s82, s60\n\t" \
  "s_add_u32 s50, s82, s38\n\t" \
  "s_add_u32 s69, s82, s39\n\t" \
  "s_add_u32 s82, s48, 1\n\t" \
  CH_SEGC \
  "s_lshr_b64 s[60:61], s[90:91], s82\n\t" \
  "s_and_b32 s60, s60, 1\n\t" \
  "s_lshl_b32 s60, s60, 13\n\t" \
  "s_add_u32 s82, s82, " CH_SG0 "\n\t" \
  "s_lshl_b32 s82, s82, 21\n\t" \
  "s_or_b32 s82, s82, s60\n\t" \
  "s_add_u32 s52, s82, s43\n\t" \
  "s_add_u32 s82, s48, " CH_ARMD "\n\t" \
  CH_SEGC \
  "s_lshr_b64 s[60:61], s[90:91], s82\n\t" \
  "s_and_b32 s60, s60, 1\n\t" \
  "s_lshl_b32 s60, s60, 13\n\t" \
  "s_add_u32 s82, s82, " CH_SG0 "\n\t" \
  "s_lshl_b32 s82, s82, 21\n\t" \
  "s_or_b32 s82, s82, s60\n\t" \
  "s_add_u32 s85, s82, s45\n\t" \
  "s_cmp_lt_i32 s48, " CH_ARMS "\n\t" \
  "s_cselect_b32 s85, s27, s85\n\t" \
  "s_cmp_eq_u32 s34, 0\n\t" \
  "s_cbranch_scc1 LINI_%=\n\t" \
  "LTK_%=:\n\t" \
  CH_TOKCHK \
  "s_mov_b32 s61, 0\n\t" \
  "LTW_%=:\n\t" \
  "buffer_load_dword v1, v37, s[28:31], 0 offen sc1\n\t" \
  "s_waitcnt vmcnt(0)\n\t" \
  "v_cmp_eq_u32 vcc, 0, v1\n\t" \
  "s_cbranch_vccz LG3_%=\n\t" \
  "s_add_u32 s61, s61, 1\n\t" \
  "s_sleep 2\n\t" \
  "s_cmp_gt_u32 s61, s84\n\t" \
  "s_cbranch_scc1 LTG_%=\n\t" \
  "s_and_b32 s60, s61, 1023\n\t" \
  "s_cmp_lg_u32 s60, 0\n\t" \
  "s_cbranch_scc1 LTW_%=\n\t" \
  "buffer_load_dword v46, off, s[28:31], s92 sc1\n\t" \
  "s_waitcnt vmcnt(0)\n\t" \
  "v_readfirstlane_b32 s60, v46\n\t" \
  "s_nop 0\n\t" \
  "s_cmp_eq_u32 s60, 0\n\t" \
  "s_cbranch_scc1 LTW_%=\n\t" \
  "LTG_%=:\n\t" \
  "s_mov_b32 s84, 0\n\t" \
  "s_lshl_b32 s60, s36, 4\n\t" \
  "s_or_b32 s60, s60, s48\n\t" \
  "s_or_b32 s60, s60, 268435456\n\t" \
  "v_mov_b32 v76, s60\n\t" \
  "v_mov_b32 v77, 0\n\t" \
  "s_mov_b64 exec, 1\n\t" \
  "buffer_atomic_cmpswap v[76:77], off, s[28:31], s92 sc1\n\t" \
  "s_mov_b64 exec, -1\n\t" \
  "s_branch LG3_%=\n\t" \
  "LIDLE_%=:\n\t" \
  "s_add_u32 s64, s64, 8\n\t" \
  "s_movk_i32 s60, 8192\n\t" \
  "IWT_%=:\n\t" \
  "s_memrealtime s[72:73]\n\t" \
  "s_sub_u32 s60, s60, 1\n\t" \
  "s_cselect_b32 s61, 0, -1\n\t" \
  "s_waitcnt lgkmcnt(0)\n\t" \
  "s_sub_i32 s73, s72, s64\n\t" \
  "s_and_b32 s73, s73, s61\n\t" \
  "s_cmp_lt_i32 s73, 0\n\t" \
  "s_cbranch_scc1 IWT_%=\n\t" \
  "buffer_load_dword v39, v37, s[28:31], 0 offen sc1\n\t" \
  "buffer_store_dword v37, off, s[28:31], s41\n\t" \
  "s_branch RDONE3_%=\n\t" \
  "RFAIL3_%=:\n\t" \
  "s_add_u32 s74, s74, 1\n\t" \
  "s_sleep " CH_RSL "\n\t" \
  "s_waitcnt vmcnt(0)\n\t" \
  "s_cmp_gt_u32 s74, s84\n\t" \
  "s_cbranch_scc1 RG3_%=\n\t" \
  "s_and_b32 s60, s74, 1023\n\t" \
  "s_cmp_lg_u32 s60, 0\n\t" \
  "s_cbranch_scc1 RRETRY3_%=\n\t" \
  "buffer_load_dword v46, off, s[28:31], s92 sc1\n\t" \
  "s_waitcnt vmcnt(0)\n\t" \
  "v_readfirstlane_b32 s60, v46\n\t" \
  "s_nop 0\n\t" \
  "s_cmp_eq_u32 s60, 0\n\t" \
  "s_cbranch_scc1 RRETRY3_%=\n\t" \
  "RG3_%=:\n\t" \
  "s_mov_b32 s84, 0\n\t" \
  "s_lshl_b32 s60, s36, 4\n\t" \
  "s_or_b32 s60, s60, s48\n\t" \
  "s_or_b32 s60, s60, 536870912\n\t" \
  "v_mov_b32 v76, s60\n\t" \
  "v_mov_b32 v77, 0\n\t" \
  "s_mov_b64 exec, 1\n\t" \
  "buffer_atomic_cmpswap v[76:77], off, s[28:31], s92 sc1\n\t" \
  "s_mov_b64 exec, -1\n\t" \
  "s_branch RDONE3_%=\n\t" \
  "LFAIL3_%=:\n\t" \
  "v_max_u32 v75, v0, v1\n\t" \
  "v_max3_u32 v75, v75, v2, v3\n\t" \
  "v_max3_u32 v75, v75, v4, v5\n\t" \
  "v_max3_u32 v75, v75, v6, v7\n\t" \
  "v_max3_u32 v75, v75, v8, v9\n\t" \
  "v_max3_u32 v75, v75, v10, v11\n\t" \
  "v_max3_u32 v75, v75, v12, v13\n\t" \
  "v_max3_u32 v75, v75, v14, v15\n\t" \
  "v_max3_u32 v75, v75, v16, v17\n\t" \
  "v_max3_u32 v75, v75, v18, v19\n\t" \
  "v_max3_u32 v75, v75, v20, v21\n\t" \
  "v_max3_u32 v75, v75, v22, v23\n\t" \
  "v_max3_u32 v75, v75, v24, v25\n\t" \
  "v_max3_u32 v75, v75, v26, v27\n\t" \
  "v_max3_u32 v75, v75, v28, v29\n\t" \
  "v_max3_u32 v75, v75, v30, v31\n\t" \
  "v_max3_u32 v75, v75, v32, v33\n\t" \
  "v_max3_u32 v75, v75, v34, v35\n\t" \
  "v_cmp_eq_u32 vcc, -1, v75\n\t" \
  "s_cbranch_vccz LGO3_%=\n\t" \
  "s_add_u32 s68, s68, 1\n\t" \
  "s_waitcnt lgkmcnt(0)\n\t" \
  "s_cmp_gt_u32 s68, s83\n\t" \
  "s_cbranch_scc1 LGV3_%=\n\t" \
  "s_barrier\n\t" \
  "s_waitcnt vmcnt(8)\n\t" \
  "s_branch LG3_%=\n\t" \
  "RWT3_%=f:\n\t" \
  "s_cmp_lt_i32 s73, -2\n\t" \
  "s_cbranch_scc0 RWT3_%=g\n\t" \
  "s_nop 4\n\t" \
  "s_branch RWT3_%=g\n\t" \
  "LGV3_%=:\n\t" \
  "s_mov_b32 s83, 0\n\t" \
  "s_lshl_b32 s60, s36, 4\n\t" \
  "s_or_b32 s60, s60, s48\n\t" \
  "s_or_b32 s60, s60, 805306368\n\t" \
  "v_mov_b32 v76, s60\n\t" \
  "v_mov_b32 v77, 0\n\t" \
  "s_mov_b64 exec, 1\n\t" \
  "buffer_atomic_cmpswap v[76:77], off, s[28:31], s92 sc1\n\t" \
  "s_mov_b64 exec, -1\n\t" \
  "s_branch LGO3_%=\n\t" \
  "LLAST3_%=:\n\t" \
  "s_waitcnt vmcnt(0) lgkmcnt(0)\n\t" \
  "s_barrier\n\t" \
  "LEND_%=:\n\t" \
  ""
typedef unsigned v16u __attribute__((ext_vector_type(16)));
#define CH_WCLOB "a0", "a1", "a2", "a3", "a4", "a5", "a6", "a7", "a8", "a9", "a10", "a11", "a12", "a13", "a14", "a15", "a16", "a17", "a18", "a19", "a20", "a21", "a22", "a23", "a24", "a25", "a26", "a27", "a28", "a29", "a30", "a31", "a32", "a33", "a34", "a35", "a36", "a37", "a38", "a39", "a40", "a41", "a42", "a43", "a44", "a45", "a46", "a47", "a48", "a49", "a50", "a51", "a52", "a53", "a54", "a55", "a56", "a57", "a58", "a59", "a60", "a61", "a62", "a63", "a64", "a65", "a66", "a67", "a68", "a69", "a70", "a71", "a72", "a73", "a74", "a75", "a76", "a77", "a78", "a79", "a80", "a81", "a82", "a83", "a84", "a85", "a86", "a87", "a88", "a89", "a90", "a91", "a92", "a93", "a94", "a95", "a96", "a97", "a98", "a99", "a100", "a101", "a102", "a103", "a104", "a105", "a106", "a107", "a108", "a109", "a110", "a111", "a112", "a113", "a114", "a115", "a116", "a117", "a118", "a119", "a120", "a121", "a122", "a123", "a124", "a125", "a126", "a127", "a128", "a129", "a130", "a131", "a132", "a133", "a134", "a135", "a136", "a137", "a138", "a139", "a140", "a141", "a142", "a143", "a144", "a145", "a146", "a147", "a148", "a149", "a150", "a151", "a152", "a153", "a154", "a155", "a156", "a157", "a158", "a159", "a160", "a161", "a162", "a163", "a164", "a165", "a166", "a167", "a168", "a169", "a170", "a171", "a172", "a173", "a174", "a175", "a176", "a177", "a178", "a179", "a180", "a181", "a182", "a183", "a184", "a185", "a186", "a187", "a188", "a189", "a190", "a191", "a192", "a193", "a194", "a195", "a196", "a197", "a198", "a199", "a200", "a201", "a202", "a203", "a204", "a205", "a206", "a207", "a208", "a209", "a210", "a211", "a212", "a213", "a214", "a215", "a216", "a217", "a218", "a219", "a220", "a221", "a222", "a223", "a224", "a225", "a226", "a227", "a228", "a229", "a230", "a231", "a232", "a233", "a234", "a235", "a236", "a237", "a238", "a239", "a240", "a241", "a242", "a243", "a244", "a245", "a246", "a247", "a248", "a249", "a250", "a251", "a252", "a253", "a254", "a255", "v80", "v81", "v82", "v83", "v84", "v85", "v86", "v87", "v88", "v89", "v90", "v91", "v92", "v93", "v94", "v95", "v96", "v97", "v98", "v99", "v100", "v101", "v102", "v103", "v104", "v105", "v106", "v107", "v108", "v109", "v110", "v111", "v112", "v113", "v114", "v115", "v116", "v117", "v118", "v119", "v120", "v121", "v122", "v123", "v124", "v125", "v126", "v127", "v128", "v129", "v130", "v131", "v132", "v133", "v134", "v135", "v136", "v137", "v138", "v139", "v140", "v141", "v142", "v143", "v144", "v145", "v146", "v147", "v148", "v149", "v150", "v151", "v152", "v153", "v154", "v155", "v156", "v157", "v158", "v159", "v160", "v161", "v162", "v163", "v164", "v165", "v166", "v167", "v168", "v169", "v170", "v171", "v172", "v173", "v174", "v175", "v176", "v177", "v178", "v179", "v180", "v181", "v182", "v183", "v184", "v185", "v186", "v187", "v188", "v189", "v190", "v191", "v192", "v193", "v194", "v195", "v196", "v197", "v198", "v199", "v200", "v201", "v202", "v203", "v204", "v205", "v206", "v207", "v208", "v209", "v210", "v211", "v212", "v213", "v214", "v215", "v216", "v217", "v218", "v219", "v220", "v221", "v222", "v223", "v224", "v225", "v226", "v227", "v228", "v229", "v230", "v231", "v232", "v233", "v234", "v235", "v236", "v237", "v238", "v239", "v240", "v241", "v242", "v243", "v244", "v245", "v246", "v247", "v248", "v249", "v250", "v251", "v252", "v253", "v254", "v255"
#define CH_H(b) (8 * ((b) & 3) + ((b) >> 3))
#define CH_L(b) (((b) >> 2) & 1)
__device__ __forceinline__ void start_work(char* smem, int wk);
__device__ __forceinline__ void chain_wg(char* smem) {
  if (CH_L((int)blockIdx.x)) start_work(smem, CH_H((int)blockIdx.x));
  const MP p = getp();
  const int b = (int)blockIdx.x, h = CH_H(b), L = CH_L(b);
  int w = __builtin_amdgcn_readfirstlane(threadIdx.x >> 6);
  {
    unsigned id; asm volatile("s_getreg_b32 %0, hwreg(HW_REG_HW_ID, 4, 2)" : "=s"(id));
    volatile __attribute__((address_space(3))) unsigned* lw = (volatile __attribute__((address_space(3))) unsigned*)smem;
    lw[w] = 1u << id;
    BAR();
    if (__builtin_amdgcn_readfirstlane(lw[0] | lw[1] | lw[2] | lw[3]) == 15u) w = (int)((SMAP >> (4u * id)) & 3u);
  }
  unsigned lp16 = (threadIdx.x & 63u) * 16u;
  rsrc_t X = mkr(p.xb, XB_BYTES);
  if ((unsigned)(size_t)(__attribute__((address_space(3))) char*)smem != 0u) return;
  const unsigned wsz = DM * HD * 2u - (unsigned)(h * DH) * 2u;
  const rsrc_t RQ = mkr(p.Wq + h * DH, wsz), RK = mkr(p.Wk + h * DH, wsz), RV = mkr(p.Wv + h * DH, wsz), RO = mkr(p.Wo + (long)h * DH * DM, DH * DM * 2u);
  unsigned xcc; asm volatile("s_getreg_b32 %0, hwreg(HW_REG_XCC_ID)" : "=s"(xcc)); xcc &= 3u;
  unsigned d96 = 0u;
  const unsigned c43 = P_FA * PAIR + (unsigned)h * 128u + 32u;
  const unsigned h128 = (unsigned)h * 128u, uw = (unsigned)w, t = uw >> 1, T = uw & 1u, NONE = 0xF0000000u;
  const unsigned u0 = w == 3 ? (unsigned)h >> 2 : (unsigned)((h & 1) + (h >> 3) * 2 + ((h >> 1) & 3) * 8 + (w == 2 ? 32 : 0));
  const bool idle = w == 3 && (h & 3);
  const unsigned c37 = idle ? NONE : (w == 3 ? AGT + u0 * 128u : AGO + (u0 >> 5) * 32768u + (u0 & 31u) * 128u);
  const unsigned c41 = idle ? NONE : (w == 3 ? 524288u + (u0 >> 1) * 16384u + (u0 & 1u) * 4096u : (u0 & 7u) * 16384u + ((u0 >> 3) & 1u) * 4096u + (u0 >> 4) * 131072u);
  const unsigned c42 = AGO + (xcc & 1u) * 16384u + (xcc >> 1) * 4096u;
  const unsigned c46 = (w == 3 ? (h & 7) == 5 : h == 30) ? AGT + 1024u : NONE;
  const unsigned c38 = (unsigned)(3 * h + 1) * PAIR + T * 4096u + t * 2048u, c39 = (P_KV + (unsigned)(h >> 1)) * PAIR + (unsigned)(h & 1) * 4096u;
  const unsigned c45 = P_FB * PAIR + 4096u + (unsigned)(16 + (h & 15)) * 128u;
  unsigned d48 = 0u, d49 = 0u, d53 = SLOT, d86 = 1u, d65 = rtc();
  unsigned mn = 120u;
  v4u stt;
  asm volatile("s_waitcnt vmcnt(0) lgkmcnt(0)\n\t" CH_ASM "LCHEND:"
               : "+{s48}"(d48), "+{s49}"(d49), "+{s53}"(d53), "+{s65}"(d65), "+{s81}"(mn), "+{s86}"(d86), "+{s96}"(d96), "=&{s[76:79]}"(stt), "+{v69}"(lp16), "+{s[28:31]}"(X)
               : [Wq] "s"(RQ), [Wk] "s"(RK), [Wv] "s"(RV), [Wo] "s"(RO), "{s33}"(HT_OFF + 8192u + 256u + (unsigned)b * 4u), "{s36}"(h128), "{s37}"(c37), "{s40}"(w), "{s41}"(c41), "{s42}"(c42), "{s46}"(c46), "{s92}"(TMO_OFF), "{s38}"(c38), "{s39}"(c39), "{s43}"(c43), "{s45}"(c45), "{s32}"(L), "{s26}"(AGT + 1024u + xcc * 128u), "{s27}"(RDY_OFF(0) + (unsigned)((h & 7) * 1024 + (b & 7) * 128)), [ctab] "i"(CTAB_OFF), [ring] "i"(SRING / 3 * XBLK), [trm] "i"(TRING - 1), [ex] "i"(ED * 256 + EB), [dl1l] "i"(DL1 - LATE), [armd] "i"(3 - ARMK), [arms] "i"(ARMK - 3),
                 [cap] "i"(SPIN_CAP)
               : "v0", "v1", "v2", "v3", "v4", "v5", "v6", "v7", "v8", "v9", "v10", "v11", "v12", "v13", "v14", "v15", "v16", "v17", "v18", "v19",
                 "v20", "v21", "v22", "v23", "v24", "v25", "v26", "v27", "v28", "v29", "v30", "v31", "v32", "v33", "v34", "v35", "v36", "v37", "v38", "v39",
                 "v40", "v41", "v42", "v43", "v44", "v45", "v46", "v47", "v48", "v49", "v50", "v51", "v52", "v53", "v54", "v55", "v56", "v57", "v58", "v59",
                 "v60", "v61", "v62", "v63", "v64", "v65", "v66", "v67", "v68", "v70", "v71", "v72", "v73", "v74", "v75", "v76", "v77", "v78", "v79",
                 CH_WCLOB, "s20", "s21", "s34", "s35", "s24", "s25", "s90", "s91", "s47", "s87", "s50", "s51", "s52", "s69", "s82", "s85", "s54", "s75", "s80", "s55", "s56", "s57", "s58", "s59", "s60", "s61", "s62", "s63", "s64", "s66", "s67", "s68", "s70", "s71", "s72", "s73", "s74", "s88", "s89",
                 "s83", "s84", "s97", "s98", "s99", "s93", "s94", "s95", "m0", "vcc", "scc", "memory");
}
#define XQ 0u
#define XQL 0u
#define XK 0u
#define XT 0u
#define XA 0u
#define XAL 0u
#define XRL 0u
#define QLEAD 4
#define RONE 2
#define OAUX 0
#define PFM 1
#define PSP 4
#define PSR 8
#define RDLY 40
#define QLEAD1 2
#define QXW -18
#define QXC QXW
#define TK_QXW "%[qxw]"
#define TK_QXC "%[qxc]"
#define PSG 16
#define TK_PSG CH_S(PSG)
#define AX 30
#define AX1 45
#define AM 22
#define TK_AM CH_S(AM)
#define PSK 2
#define PSH 2
#define TK_CAP "%[cap]"
#define TK_CTAB "%[ctab]"
#define TK_RM "%[rm]"
#define TK_TRM "%[trm]"
#define TK_TRN "%[trn]"
#define TK_SG0 CH_S(SG0)
#define TK_PSK CH_S(PSK)
#define TK_PSH CH_S(PSH)
#define TK_PA0
#define TK_PA1
#define TK_PA2
#define TK_PQ0
#define TK_PQ1
#define TK_PQ2
#define TT(s, i)
#define TK_AC(n)
#define TK_ACE
#define TK_AC0 TK_AC(212)
#define TK_AC1 TK_AC(213)
#define TK_AC2 TK_AC(214)
#define TK_AC3 TK_AC(215)
#define TK_AC4 TK_AC(216)
#define TK_AC5 TK_AC(217)
#define TK_AC6 TK_AC(218)
#define TK_AC7 TK_AC(219)
#define TK_AC8 TK_AC(220)
#define AP_ASM \
  "v_lshrrev_b32 v40, 8, v0\n\t" \
  "v_mul_u32_u24 v40, 4608, v40\n\t" \
  "v_and_b32 v41, 0xf0, v0\n\t" \
  "v_lshrrev_b32 v42, 3, v41\n\t" \
  "v_add3_u32 v41, v41, v40, s64\n\t" \
  "v_add3_u32 v42, v42, v40, s65\n\t" \
  "s_mov_b32 s62, 0x05040100\n\t" \
  "s_mov_b32 s63, 0x07060302\n\t" \
  "s_xor_b32 s60, s66, 0\n\t" \
  "s_add_u32 s60, s60, 0\n\t" \
  "s_add_u32 s61, s60, 18432\n\t" \
  "buffer_load_dwordx4 v[48:51], v41, %[W], s60 offen offset:0\n\t" \
  "buffer_load_dwordx4 v[52:55], v41, %[W], s60 offen offset:1152\n\t" \
  "buffer_load_dwordx4 v[56:59], v41, %[W], s60 offen offset:2304\n\t" \
  "buffer_load_dwordx4 v[60:63], v41, %[W], s60 offen offset:3456\n\t" \
  "buffer_load_dwordx4 v[64:67], v41, %[W], s61 offen offset:0\n\t" \
  "buffer_load_dwordx4 v[68:71], v41, %[W], s61 offen offset:1152\n\t" \
  "buffer_load_dwordx4 v[72:75], v41, %[W], s61 offen offset:2304\n\t" \
  "buffer_load_dwordx4 v[76:79], v41, %[W], s61 offen offset:3456\n\t" \
  "s_xor_b32 s60, s66, 0\n\t" \
  "s_add_u32 s60, s60, 36864\n\t" \
  "s_add_u32 s61, s60, 18432\n\t" \
  "buffer_load_dwordx4 v[80:83], v41, %[W], s60 offen offset:0\n\t" \
  "buffer_load_dwordx4 v[84:87], v41, %[W], s60 offen offset:1152\n\t" \
  "buffer_load_dwordx4 v[88:91], v41, %[W], s60 offen offset:2304\n\t" \
  "buffer_load_dwordx4 v[92:95], v41, %[W], s60 offen offset:3456\n\t" \
  "buffer_load_dwordx4 v[96:99], v41, %[W], s61 offen offset:0\n\t" \
  "buffer_load_dwordx4 v[100:103], v41, %[W], s61 offen offset:1152\n\t" \
  "buffer_load_dwordx4 v[104:107], v41, %[W], s61 offen offset:2304\n\t" \
  "buffer_load_dwordx4 v[108:111], v41, %[W], s61 offen offset:3456\n\t" \
  "s_xor_b32 s60, s66, 0\n\t" \
  "s_add_u32 s60, s60, 73728\n\t" \
  "s_add_u32 s61, s60, 18432\n\t" \
  "buffer_load_dwordx4 v[112:115], v41, %[W], s60 offen offset:0\n\t" \
  "buffer_load_dwordx4 v[116:119], v41, %[W], s60 offen offset:1152\n\t" \
  "buffer_load_dwordx4 v[120:123], v41, %[W], s60 offen offset:2304\n\t" \
  "buffer_load_dwordx4 v[124:127], v41, %[W], s60 offen offset:3456\n\t" \
  "buffer_load_dwordx4 v[128:131], v41, %[W], s61 offen offset:0\n\t" \
  "buffer_load_dwordx4 v[132:135], v41, %[W], s61 offen offset:1152\n\t" \
  "buffer_load_dwordx4 v[136:139], v41, %[W], s61 offen offset:2304\n\t" \
  "buffer_load_dwordx4 v[140:143], v41, %[W], s61 offen offset:3456\n\t" \
  "s_xor_b32 s60, s66, 0\n\t" \
  "s_add_u32 s60, s60, 110592\n\t" \
  "s_add_u32 s61, s60, 18432\n\t" \
  "buffer_load_dwordx4 v[144:147], v41, %[W], s60 offen offset:0\n\t" \
  "buffer_load_dwordx4 v[148:151], v41, %[W], s60 offen offset:1152\n\t" \
  "buffer_load_dwordx4 v[152:155], v41, %[W], s60 offen offset:2304\n\t" \
  "buffer_load_dwordx4 v[156:159], v41, %[W], s60 offen offset:3456\n\t" \
  "buffer_load_dwordx4 v[160:163], v41, %[W], s61 offen offset:0\n\t" \
  "buffer_load_dwordx4 v[164:167], v41, %[W], s61 offen offset:1152\n\t" \
  "buffer_load_dwordx4 v[168:171], v41, %[W], s61 offen offset:2304\n\t" \
  "buffer_load_dwordx4 v[172:175], v41, %[W], s61 offen offset:3456\n\t" \
  "s_xor_b32 s60, s66, 147456\n\t" \
  "s_add_u32 s60, s60, 0\n\t" \
  "s_add_u32 s61, s60, 18432\n\t" \
  "buffer_load_dwordx4 v[176:179], v41, %[W], s60 offen offset:0\n\t" \
  "buffer_load_dwordx4 v[180:183], v41, %[W], s60 offen offset:1152\n\t" \
  "buffer_load_dwordx4 v[184:187], v41, %[W], s60 offen offset:2304\n\t" \
  "buffer_load_dwordx4 v[188:191], v41, %[W], s60 offen offset:3456\n\t" \
  "buffer_load_dwordx4 v[192:195], v41, %[W], s61 offen offset:0\n\t" \
  "buffer_load_dwordx4 v[196:199], v41, %[W], s61 offen offset:1152\n\t" \
  "buffer_load_dwordx4 v[200:203], v41, %[W], s61 offen offset:2304\n\t" \
  "buffer_load_dwordx4 v[204:207], v41, %[W], s61 offen offset:3456\n\t" \
  "s_waitcnt vmcnt(38)\n\t" \
  "v_perm_b32 v44, v52, v48, s62\n\t" \
  "s_waitcnt vmcnt(36)\n\t" \
  "v_perm_b32 v45, v60, v56, s62\n\t" \
  "s_waitcnt vmcnt(34)\n\t" \
  "v_perm_b32 v46, v68, v64, s62\n\t" \
  "s_waitcnt vmcnt(32)\n\t" \
  "v_perm_b32 v47, v76, v72, s62\n\t" \
  "v_accvgpr_write_b32 a0, v44\n\t" \
  "v_accvgpr_write_b32 a1, v45\n\t" \
  "v_accvgpr_write_b32 a2, v46\n\t" \
  "v_accvgpr_write_b32 a3, v47\n\t" \
  "v_perm_b32 v44, v52, v48, s63\n\t" \
  "v_perm_b32 v45, v60, v56, s63\n\t" \
  "v_perm_b32 v46, v68, v64, s63\n\t" \
  "v_perm_b32 v47, v76, v72, s63\n\t" \
  "v_accvgpr_write_b32 a4, v44\n\t" \
  "v_accvgpr_write_b32 a5, v45\n\t" \
  "v_accvgpr_write_b32 a6, v46\n\t" \
  "v_accvgpr_write_b32 a7, v47\n\t" \
  "v_perm_b32 v44, v53, v49, s62\n\t" \
  "v_perm_b32 v45, v61, v57, s62\n\t" \
  "v_perm_b32 v46, v69, v65, s62\n\t" \
  "v_perm_b32 v47, v77, v73, s62\n\t" \
  "v_accvgpr_write_b32 a8, v44\n\t" \
  "v_accvgpr_write_b32 a9, v45\n\t" \
  "v_accvgpr_write_b32 a10, v46\n\t" \
  "v_accvgpr_write_b32 a11, v47\n\t" \
  "v_perm_b32 v44, v53, v49, s63\n\t" \
  "v_perm_b32 v45, v61, v57, s63\n\t" \
  "v_perm_b32 v46, v69, v65, s63\n\t" \
  "v_perm_b32 v47, v77, v73, s63\n\t" \
  "v_accvgpr_write_b32 a12, v44\n\t" \
  "v_accvgpr_write_b32 a13, v45\n\t" \
  "v_accvgpr_write_b32 a14, v46\n\t" \
  "v_accvgpr_write_b32 a15, v47\n\t" \
  "v_perm_b32 v44, v54, v50, s62\n\t" \
  "v_perm_b32 v45, v62, v58, s62\n\t" \
  "v_perm_b32 v46, v70, v66, s62\n\t" \
  "v_perm_b32 v47, v78, v74, s62\n\t" \
  "v_accvgpr_write_b32 a16, v44\n\t" \
  "v_accvgpr_write_b32 a17, v45\n\t" \
  "v_accvgpr_write_b32 a18, v46\n\t" \
  "v_accvgpr_write_b32 a19, v47\n\t" \
  "v_perm_b32 v44, v54, v50, s63\n\t" \
  "v_perm_b32 v45, v62, v58, s63\n\t" \
  "v_perm_b32 v46, v70, v66, s63\n\t" \
  "v_perm_b32 v47, v78, v74, s63\n\t" \
  "v_accvgpr_write_b32 a20, v44\n\t" \
  "v_accvgpr_write_b32 a21, v45\n\t" \
  "v_accvgpr_write_b32 a22, v46\n\t" \
  "v_accvgpr_write_b32 a23, v47\n\t" \
  "v_perm_b32 v44, v55, v51, s62\n\t" \
  "v_perm_b32 v45, v63, v59, s62\n\t" \
  "v_perm_b32 v46, v71, v67, s62\n\t" \
  "v_perm_b32 v47, v79, v75, s62\n\t" \
  "v_accvgpr_write_b32 a24, v44\n\t" \
  "v_accvgpr_write_b32 a25, v45\n\t" \
  "v_accvgpr_write_b32 a26, v46\n\t" \
  "v_accvgpr_write_b32 a27, v47\n\t" \
  "v_perm_b32 v44, v55, v51, s63\n\t" \
  "v_perm_b32 v45, v63, v59, s63\n\t" \
  "v_perm_b32 v46, v71, v67, s63\n\t" \
  "v_perm_b32 v47, v79, v75, s63\n\t" \
  "v_accvgpr_write_b32 a28, v44\n\t" \
  "v_accvgpr_write_b32 a29, v45\n\t" \
  "v_accvgpr_write_b32 a30, v46\n\t" \
  "v_accvgpr_write_b32 a31, v47\n\t" \
  "s_xor_b32 s60, s66, 147456\n\t" \
  "s_add_u32 s60, s60, 36864\n\t" \
  "s_add_u32 s61, s60, 18432\n\t" \
  "buffer_load_dwordx4 v[48:51], v41, %[W], s60 offen offset:0\n\t" \
  "buffer_load_dwordx4 v[52:55], v41, %[W], s60 offen offset:1152\n\t" \
  "buffer_load_dwordx4 v[56:59], v41, %[W], s60 offen offset:2304\n\t" \
  "buffer_load_dwordx4 v[60:63], v41, %[W], s60 offen offset:3456\n\t" \
  "buffer_load_dwordx4 v[64:67], v41, %[W], s61 offen offset:0\n\t" \
  "buffer_load_dwordx4 v[68:71], v41, %[W], s61 offen offset:1152\n\t" \
  "buffer_load_dwordx4 v[72:75], v41, %[W], s61 offen offset:2304\n\t" \
  "buffer_load_dwordx4 v[76:79], v41, %[W], s61 offen offset:3456\n\t" \
  "s_waitcnt vmcnt(38)\n\t" \
  "v_perm_b32 v44, v84, v80, s62\n\t" \
  "s_waitcnt vmcnt(36)\n\t" \
  "v_perm_b32 v45, v92, v88, s62\n\t" \
  "s_waitcnt vmcnt(34)\n\t" \
  "v_perm_b32 v46, v100, v96, s62\n\t" \
  "s_waitcnt vmcnt(32)\n\t" \
  "v_perm_b32 v47, v108, v104, s62\n\t" \
  "v_accvgpr_write_b32 a32, v44\n\t" \
  "v_accvgpr_write_b32 a33, v45\n\t" \
  "v_accvgpr_write_b32 a34, v46\n\t" \
  "v_accvgpr_write_b32 a35, v47\n\t" \
  "v_perm_b32 v44, v84, v80, s63\n\t" \
  "v_perm_b32 v45, v92, v88, s63\n\t" \
  "v_perm_b32 v46, v100, v96, s63\n\t" \
  "v_perm_b32 v47, v108, v104, s63\n\t" \
  "v_accvgpr_write_b32 a36, v44\n\t" \
  "v_accvgpr_write_b32 a37, v45\n\t" \
  "v_accvgpr_write_b32 a38, v46\n\t" \
  "v_accvgpr_write_b32 a39, v47\n\t" \
  "v_perm_b32 v44, v85, v81, s62\n\t" \
  "v_perm_b32 v45, v93, v89, s62\n\t" \
  "v_perm_b32 v46, v101, v97, s62\n\t" \
  "v_perm_b32 v47, v109, v105, s62\n\t" \
  "v_accvgpr_write_b32 a40, v44\n\t" \
  "v_accvgpr_write_b32 a41, v45\n\t" \
  "v_accvgpr_write_b32 a42, v46\n\t" \
  "v_accvgpr_write_b32 a43, v47\n\t" \
  "v_perm_b32 v44, v85, v81, s63\n\t" \
  "v_perm_b32 v45, v93, v89, s63\n\t" \
  "v_perm_b32 v46, v101, v97, s63\n\t" \
  "v_perm_b32 v47, v109, v105, s63\n\t" \
  "v_accvgpr_write_b32 a44, v44\n\t" \
  "v_accvgpr_write_b32 a45, v45\n\t" \
  "v_accvgpr_write_b32 a46, v46\n\t" \
  "v_accvgpr_write_b32 a47, v47\n\t" \
  "v_perm_b32 v44, v86, v82, s62\n\t" \
  "v_perm_b32 v45, v94, v90, s62\n\t" \
  "v_perm_b32 v46, v102, v98, s62\n\t" \
  "v_perm_b32 v47, v110, v106, s62\n\t" \
  "v_accvgpr_write_b32 a48, v44\n\t" \
  "v_accvgpr_write_b32 a49, v45\n\t" \
  "v_accvgpr_write_b32 a50, v46\n\t" \
  "v_accvgpr_write_b32 a51, v47\n\t" \
  "v_perm_b32 v44, v86, v82, s63\n\t" \
  "v_perm_b32 v45, v94, v90, s63\n\t" \
  "v_perm_b32 v46, v102, v98, s63\n\t" \
  "v_perm_b32 v47, v110, v106, s63\n\t" \
  "v_accvgpr_write_b32 a52, v44\n\t" \
  "v_accvgpr_write_b32 a53, v45\n\t" \
  "v_accvgpr_write_b32 a54, v46\n\t" \
  "v_accvgpr_write_b32 a55, v47\n\t" \
  "v_perm_b32 v44, v87, v83, s62\n\t" \
  "v_perm_b32 v45, v95, v91, s62\n\t" \
  "v_perm_b32 v46, v103, v99, s62\n\t" \
  "v_perm_b32 v47, v111, v107, s62\n\t" \
  "v_accvgpr_write_b32 a56, v44\n\t" \
  "v_accvgpr_write_b32 a57, v45\n\t" \
  "v_accvgpr_write_b32 a58, v46\n\t" \
  "v_accvgpr_write_b32 a59, v47\n\t" \
  "v_perm_b32 v44, v87, v83, s63\n\t" \
  "v_perm_b32 v45, v95, v91, s63\n\t" \
  "v_perm_b32 v46, v103, v99, s63\n\t" \
  "v_perm_b32 v47, v111, v107, s63\n\t" \
  "v_accvgpr_write_b32 a60, v44\n\t" \
  "v_accvgpr_write_b32 a61, v45\n\t" \
  "v_accvgpr_write_b32 a62, v46\n\t" \
  "v_accvgpr_write_b32 a63, v47\n\t" \
  "s_xor_b32 s60, s66, 147456\n\t" \
  "s_add_u32 s60, s60, 73728\n\t" \
  "s_add_u32 s61, s60, 18432\n\t" \
  "buffer_load_dwordx4 v[80:83], v41, %[W], s60 offen offset:0\n\t" \
  "buffer_load_dwordx4 v[84:87], v41, %[W], s60 offen offset:1152\n\t" \
  "buffer_load_dwordx4 v[88:91], v41, %[W], s60 offen offset:2304\n\t" \
  "buffer_load_dwordx4 v[92:95], v41, %[W], s60 offen offset:3456\n\t" \
  "buffer_load_dwordx4 v[96:99], v41, %[W], s61 offen offset:0\n\t" \
  "buffer_load_dwordx4 v[100:103], v41, %[W], s61 offen offset:1152\n\t" \
  "buffer_load_dwordx4 v[104:107], v41, %[W], s61 offen offset:2304\n\t" \
  "buffer_load_dwordx4 v[108:111], v41, %[W], s61 offen offset:3456\n\t" \
  "s_waitcnt vmcnt(38)\n\t" \
  "v_perm_b32 v44, v116, v112, s62\n\t" \
  "s_waitcnt vmcnt(36)\n\t" \
  "v_perm_b32 v45, v124, v120, s62\n\t" \
  "s_waitcnt vmcnt(34)\n\t" \
  "v_perm_b32 v46, v132, v128, s62\n\t" \
  "s_waitcnt vmcnt(32)\n\t" \
  "v_perm_b32 v47, v140, v136, s62\n\t" \
  "v_accvgpr_write_b32 a64, v44\n\t" \
  "v_accvgpr_write_b32 a65, v45\n\t" \
  "v_accvgpr_write_b32 a66, v46\n\t" \
  "v_accvgpr_write_b32 a67, v47\n\t" \
  "v_perm_b32 v44, v116, v112, s63\n\t" \
  "v_perm_b32 v45, v124, v120, s63\n\t" \
  "v_perm_b32 v46, v132, v128, s63\n\t" \
  "v_perm_b32 v47, v140, v136, s63\n\t" \
  "v_accvgpr_write_b32 a68, v44\n\t" \
  "v_accvgpr_write_b32 a69, v45\n\t" \
  "v_accvgpr_write_b32 a70, v46\n\t" \
  "v_accvgpr_write_b32 a71, v47\n\t" \
  "v_perm_b32 v44, v117, v113, s62\n\t" \
  "v_perm_b32 v45, v125, v121, s62\n\t" \
  "v_perm_b32 v46, v133, v129, s62\n\t" \
  "v_perm_b32 v47, v141, v137, s62\n\t" \
  "v_accvgpr_write_b32 a72, v44\n\t" \
  "v_accvgpr_write_b32 a73, v45\n\t" \
  "v_accvgpr_write_b32 a74, v46\n\t" \
  "v_accvgpr_write_b32 a75, v47\n\t" \
  "v_perm_b32 v44, v117, v113, s63\n\t" \
  "v_perm_b32 v45, v125, v121, s63\n\t" \
  "v_perm_b32 v46, v133, v129, s63\n\t" \
  "v_perm_b32 v47, v141, v137, s63\n\t" \
  "v_accvgpr_write_b32 a76, v44\n\t" \
  "v_accvgpr_write_b32 a77, v45\n\t" \
  "v_accvgpr_write_b32 a78, v46\n\t" \
  "v_accvgpr_write_b32 a79, v47\n\t" \
  "v_perm_b32 v44, v118, v114, s62\n\t" \
  "v_perm_b32 v45, v126, v122, s62\n\t" \
  "v_perm_b32 v46, v134, v130, s62\n\t" \
  "v_perm_b32 v47, v142, v138, s62\n\t" \
  "v_accvgpr_write_b32 a80, v44\n\t" \
  "v_accvgpr_write_b32 a81, v45\n\t" \
  "v_accvgpr_write_b32 a82, v46\n\t" \
  "v_accvgpr_write_b32 a83, v47\n\t" \
  "v_perm_b32 v44, v118, v114, s63\n\t" \
  "v_perm_b32 v45, v126, v122, s63\n\t" \
  "v_perm_b32 v46, v134, v130, s63\n\t" \
  "v_perm_b32 v47, v142, v138, s63\n\t" \
  "v_accvgpr_write_b32 a84, v44\n\t" \
  "v_accvgpr_write_b32 a85, v45\n\t" \
  "v_accvgpr_write_b32 a86, v46\n\t" \
  "v_accvgpr_write_b32 a87, v47\n\t" \
  "v_perm_b32 v44, v119, v115, s62\n\t" \
  "v_perm_b32 v45, v127, v123, s62\n\t" \
  "v_perm_b32 v46, v135, v131, s62\n\t" \
  "v_perm_b32 v47, v143, v139, s62\n\t" \
  "v_accvgpr_write_b32 a88, v44\n\t" \
  "v_accvgpr_write_b32 a89, v45\n\t" \
  "v_accvgpr_write_b32 a90, v46\n\t" \
  "v_accvgpr_write_b32 a91, v47\n\t" \
  "v_perm_b32 v44, v119, v115, s63\n\t" \
  "v_perm_b32 v45, v127, v123, s63\n\t" \
  "v_perm_b32 v46, v135, v131, s63\n\t" \
  "v_perm_b32 v47, v143, v139, s63\n\t" \
  "v_accvgpr_write_b32 a92, v44\n\t" \
  "v_accvgpr_write_b32 a93, v45\n\t" \
  "v_accvgpr_write_b32 a94, v46\n\t" \
  "v_accvgpr_write_b32 a95, v47\n\t" \
  "s_xor_b32 s60, s66, 147456\n\t" \
  "s_add_u32 s60, s60, 110592\n\t" \
  "s_add_u32 s61, s60, 18432\n\t" \
  "buffer_load_dwordx4 v[112:115], v41, %[W], s60 offen offset:0\n\t" \
  "buffer_load_dwordx4 v[116:119], v41, %[W], s60 offen offset:1152\n\t" \
  "buffer_load_dwordx4 v[120:123], v41, %[W], s60 offen offset:2304\n\t" \
  "buffer_load_dwordx4 v[124:127], v41, %[W], s60 offen offset:3456\n\t" \
  "buffer_load_dwordx4 v[128:131], v41, %[W], s61 offen offset:0\n\t" \
  "buffer_load_dwordx4 v[132:135], v41, %[W], s61 offen offset:1152\n\t" \
  "buffer_load_dwordx4 v[136:139], v41, %[W], s61 offen offset:2304\n\t" \
  "buffer_load_dwordx4 v[140:143], v41, %[W], s61 offen offset:3456\n\t" \
  "s_waitcnt vmcnt(38)\n\t" \
  "v_perm_b32 v44, v148, v144, s62\n\t" \
  "s_waitcnt vmcnt(36)\n\t" \
  "v_perm_b32 v45, v156, v152, s62\n\t" \
  "s_waitcnt vmcnt(34)\n\t" \
  "v_perm_b32 v46, v164, v160, s62\n\t" \
  "s_waitcnt vmcnt(32)\n\t" \
  "v_perm_b32 v47, v172, v168, s62\n\t" \
  "v_accvgpr_write_b32 a96, v44\n\t" \
  "v_accvgpr_write_b32 a97, v45\n\t" \
  "v_accvgpr_write_b32 a98, v46\n\t" \
  "v_accvgpr_write_b32 a99, v47\n\t" \
  "v_perm_b32 v44, v148, v144, s63\n\t" \
  "v_perm_b32 v45, v156, v152, s63\n\t" \
  "v_perm_b32 v46, v164, v160, s63\n\t" \
  "v_perm_b32 v47, v172, v168, s63\n\t" \
  "v_accvgpr_write_b32 a100, v44\n\t" \
  "v_accvgpr_write_b32 a101, v45\n\t" \
  "v_accvgpr_write_b32 a102, v46\n\t" \
  "v_accvgpr_write_b32 a103, v47\n\t" \
  "v_perm_b32 v44, v149, v145, s62\n\t" \
  "v_perm_b32 v45, v157, v153, s62\n\t" \
  "v_perm_b32 v46, v165, v161, s62\n\t" \
  "v_perm_b32 v47, v173, v169, s62\n\t" \
  "v_accvgpr_write_b32 a104, v44\n\t" \
  "v_accvgpr_write_b32 a105, v45\n\t" \
  "v_accvgpr_write_b32 a106, v46\n\t" \
  "v_accvgpr_write_b32 a107, v47\n\t" \
  "v_perm_b32 v44, v149, v145, s63\n\t" \
  "v_perm_b32 v45, v157, v153, s63\n\t" \
  "v_perm_b32 v46, v165, v161, s63\n\t" \
  "v_perm_b32 v47, v173, v169, s63\n\t" \
  "v_accvgpr_write_b32 a108, v44\n\t" \
  "v_accvgpr_write_b32 a109, v45\n\t" \
  "v_accvgpr_write_b32 a110, v46\n\t" \
  "v_accvgpr_write_b32 a111, v47\n\t" \
  "v_perm_b32 v44, v150, v146, s62\n\t" \
  "v_perm_b32 v45, v158, v154, s62\n\t" \
  "v_perm_b32 v46, v166, v162, s62\n\t" \
  "v_perm_b32 v47, v174, v170, s62\n\t" \
  "v_accvgpr_write_b32 a112, v44\n\t" \
  "v_accvgpr_write_b32 a113, v45\n\t" \
  "v_accvgpr_write_b32 a114, v46\n\t" \
  "v_accvgpr_write_b32 a115, v47\n\t" \
  "v_perm_b32 v44, v150, v146, s63\n\t" \
  "v_perm_b32 v45, v158, v154, s63\n\t" \
  "v_perm_b32 v46, v166, v162, s63\n\t" \
  "v_perm_b32 v47, v174, v170, s63\n\t" \
  "v_accvgpr_write_b32 a116, v44\n\t" \
  "v_accvgpr_write_b32 a117, v45\n\t" \
  "v_accvgpr_write_b32 a118, v46\n\t" \
  "v_accvgpr_write_b32 a119, v47\n\t" \
  "v_perm_b32 v44, v151, v147, s62\n\t" \
  "v_perm_b32 v45, v159, v155, s62\n\t" \
  "v_perm_b32 v46, v167, v163, s62\n\t" \
  "v_perm_b32 v47, v175, v171, s62\n\t" \
  "v_accvgpr_write_b32 a120, v44\n\t" \
  "v_accvgpr_write_b32 a121, v45\n\t" \
  "v_accvgpr_write_b32 a122, v46\n\t" \
  "v_accvgpr_write_b32 a123, v47\n\t" \
  "v_perm_b32 v44, v151, v147, s63\n\t" \
  "v_perm_b32 v45, v159, v155, s63\n\t" \
  "v_perm_b32 v46, v167, v163, s63\n\t" \
  "v_perm_b32 v47, v175, v171, s63\n\t" \
  "v_accvgpr_write_b32 a124, v44\n\t" \
  "v_accvgpr_write_b32 a125, v45\n\t" \
  "v_accvgpr_write_b32 a126, v46\n\t" \
  "v_accvgpr_write_b32 a127, v47\n\t" \
  "s_xor_b32 s60, s66, 0\n\t" \
  "s_add_u32 s60, s60, 0\n\t" \
  "s_add_u32 s61, s60, 18432\n\t" \
  "buffer_load_ushort v224, v42, %[W], s60 offen offset:0\n\t" \
  "buffer_load_ushort v144, v42, %[W], s60 offen offset:1152\n\t" \
  "buffer_load_ushort v225, v42, %[W], s60 offen offset:2304\n\t" \
  "buffer_load_ushort v145, v42, %[W], s60 offen offset:3456\n\t" \
  "buffer_load_ushort v226, v42, %[W], s61 offen offset:0\n\t" \
  "buffer_load_ushort v146, v42, %[W], s61 offen offset:1152\n\t" \
  "buffer_load_ushort v227, v42, %[W], s61 offen offset:2304\n\t" \
  "buffer_load_ushort v147, v42, %[W], s61 offen offset:3456\n\t" \
  "s_xor_b32 s60, s66, 0\n\t" \
  "s_add_u32 s60, s60, 36864\n\t" \
  "s_add_u32 s61, s60, 18432\n\t" \
  "buffer_load_ushort v228, v42, %[W], s60 offen offset:0\n\t" \
  "buffer_load_ushort v148, v42, %[W], s60 offen offset:1152\n\t" \
  "buffer_load_ushort v229, v42, %[W], s60 offen offset:2304\n\t" \
  "buffer_load_ushort v149, v42, %[W], s60 offen offset:3456\n\t" \
  "buffer_load_ushort v230, v42, %[W], s61 offen offset:0\n\t" \
  "buffer_load_ushort v150, v42, %[W], s61 offen offset:1152\n\t" \
  "buffer_load_ushort v231, v42, %[W], s61 offen offset:2304\n\t" \
  "buffer_load_ushort v151, v42, %[W], s61 offen offset:3456\n\t" \
  "s_waitcnt vmcnt(46)\n\t" \
  "v_perm_b32 v44, v180, v176, s62\n\t" \
  "s_waitcnt vmcnt(44)\n\t" \
  "v_perm_b32 v45, v188, v184, s62\n\t" \
  "s_waitcnt vmcnt(42)\n\t" \
  "v_perm_b32 v46, v196, v192, s62\n\t" \
  "s_waitcnt vmcnt(40)\n\t" \
  "v_perm_b32 v47, v204, v200, s62\n\t" \
  "v_accvgpr_write_b32 a128, v44\n\t" \
  "v_accvgpr_write_b32 a129, v45\n\t" \
  "v_accvgpr_write_b32 a130, v46\n\t" \
  "v_accvgpr_write_b32 a131, v47\n\t" \
  "v_perm_b32 v44, v180, v176, s63\n\t" \
  "v_perm_b32 v45, v188, v184, s63\n\t" \
  "v_perm_b32 v46, v196, v192, s63\n\t" \
  "v_perm_b32 v47, v204, v200, s63\n\t" \
  "v_accvgpr_write_b32 a132, v44\n\t" \
  "v_accvgpr_write_b32 a133, v45\n\t" \
  "v_accvgpr_write_b32 a134, v46\n\t" \
  "v_accvgpr_write_b32 a135, v47\n\t" \
  "v_perm_b32 v44, v181, v177, s62\n\t" \
  "v_perm_b32 v45, v189, v185, s62\n\t" \
  "v_perm_b32 v46, v197, v193, s62\n\t" \
  "v_perm_b32 v47, v205, v201, s62\n\t" \
  "v_accvgpr_write_b32 a136, v44\n\t" \
  "v_accvgpr_write_b32 a137, v45\n\t" \
  "v_accvgpr_write_b32 a138, v46\n\t" \
  "v_accvgpr_write_b32 a139, v47\n\t" \
  "v_perm_b32 v44, v181, v177, s63\n\t" \
  "v_perm_b32 v45, v189, v185, s63\n\t" \
  "v_perm_b32 v46, v197, v193, s63\n\t" \
  "v_perm_b32 v47, v205, v201, s63\n\t" \
  "v_accvgpr_write_b32 a140, v44\n\t" \
  "v_accvgpr_write_b32 a141, v45\n\t" \
  "v_accvgpr_write_b32 a142, v46\n\t" \
  "v_accvgpr_write_b32 a143, v47\n\t" \
  "v_perm_b32 v44, v182, v178, s62\n\t" \
  "v_perm_b32 v45, v190, v186, s62\n\t" \
  "v_perm_b32 v46, v198, v194, s62\n\t" \
  "v_perm_b32 v47, v206, v202, s62\n\t" \
  "v_accvgpr_write_b32 a144, v44\n\t" \
  "v_accvgpr_write_b32 a145, v45\n\t" \
  "v_accvgpr_write_b32 a146, v46\n\t" \
  "v_accvgpr_write_b32 a147, v47\n\t" \
  "v_perm_b32 v44, v182, v178, s63\n\t" \
  "v_perm_b32 v45, v190, v186, s63\n\t" \
  "v_perm_b32 v46, v198, v194, s63\n\t" \
  "v_perm_b32 v47, v206, v202, s63\n\t" \
  "v_accvgpr_write_b32 a148, v44\n\t" \
  "v_accvgpr_write_b32 a149, v45\n\t" \
  "v_accvgpr_write_b32 a150, v46\n\t" \
  "v_accvgpr_write_b32 a151, v47\n\t" \
  "v_perm_b32 v44, v183, v179, s62\n\t" \
  "v_perm_b32 v45, v191, v187, s62\n\t" \
  "v_perm_b32 v46, v199, v195, s62\n\t" \
  "v_perm_b32 v47, v207, v203, s62\n\t" \
  "v_accvgpr_write_b32 a152, v44\n\t" \
  "v_accvgpr_write_b32 a153, v45\n\t" \
  "v_accvgpr_write_b32 a154, v46\n\t" \
  "v_accvgpr_write_b32 a155, v47\n\t" \
  "v_perm_b32 v44, v183, v179, s63\n\t" \
  "v_perm_b32 v45, v191, v187, s63\n\t" \
  "v_perm_b32 v46, v199, v195, s63\n\t" \
  "v_perm_b32 v47, v207, v203, s63\n\t" \
  "v_accvgpr_write_b32 a156, v44\n\t" \
  "v_accvgpr_write_b32 a157, v45\n\t" \
  "v_accvgpr_write_b32 a158, v46\n\t" \
  "v_accvgpr_write_b32 a159, v47\n\t" \
  "s_xor_b32 s60, s66, 0\n\t" \
  "s_add_u32 s60, s60, 73728\n\t" \
  "s_add_u32 s61, s60, 18432\n\t" \
  "buffer_load_ushort v232, v42, %[W], s60 offen offset:0\n\t" \
  "buffer_load_ushort v152, v42, %[W], s60 offen offset:1152\n\t" \
  "buffer_load_ushort v233, v42, %[W], s60 offen offset:2304\n\t" \
  "buffer_load_ushort v153, v42, %[W], s60 offen offset:3456\n\t" \
  "buffer_load_ushort v234, v42, %[W], s61 offen offset:0\n\t" \
  "buffer_load_ushort v154, v42, %[W], s61 offen offset:1152\n\t" \
  "buffer_load_ushort v235, v42, %[W], s61 offen offset:2304\n\t" \
  "buffer_load_ushort v155, v42, %[W], s61 offen offset:3456\n\t" \
  "s_xor_b32 s60, s66, 0\n\t" \
  "s_add_u32 s60, s60, 110592\n\t" \
  "s_add_u32 s61, s60, 18432\n\t" \
  "buffer_load_ushort v236, v42, %[W], s60 offen offset:0\n\t" \
  "buffer_load_ushort v156, v42, %[W], s60 offen offset:1152\n\t" \
  "buffer_load_ushort v237, v42, %[W], s60 offen offset:2304\n\t" \
  "buffer_load_ushort v157, v42, %[W], s60 offen offset:3456\n\t" \
  "buffer_load_ushort v238, v42, %[W], s61 offen offset:0\n\t" \
  "buffer_load_ushort v158, v42, %[W], s61 offen offset:1152\n\t" \
  "buffer_load_ushort v239, v42, %[W], s61 offen offset:2304\n\t" \
  "buffer_load_ushort v159, v42, %[W], s61 offen offset:3456\n\t" \
  "s_waitcnt vmcnt(54)\n\t" \
  "v_perm_b32 v44, v52, v48, s62\n\t" \
  "s_waitcnt vmcnt(52)\n\t" \
  "v_perm_b32 v45, v60, v56, s62\n\t" \
  "s_waitcnt vmcnt(50)\n\t" \
  "v_perm_b32 v46, v68, v64, s62\n\t" \
  "s_waitcnt vmcnt(48)\n\t" \
  "v_perm_b32 v47, v76, v72, s62\n\t" \
  "v_accvgpr_write_b32 a160, v44\n\t" \
  "v_accvgpr_write_b32 a161, v45\n\t" \
  "v_accvgpr_write_b32 a162, v46\n\t" \
  "v_accvgpr_write_b32 a163, v47\n\t" \
  "v_perm_b32 v44, v52, v48, s63\n\t" \
  "v_perm_b32 v45, v60, v56, s63\n\t" \
  "v_perm_b32 v46, v68, v64, s63\n\t" \
  "v_perm_b32 v47, v76, v72, s63\n\t" \
  "v_accvgpr_write_b32 a164, v44\n\t" \
  "v_accvgpr_write_b32 a165, v45\n\t" \
  "v_accvgpr_write_b32 a166, v46\n\t" \
  "v_accvgpr_write_b32 a167, v47\n\t" \
  "v_perm_b32 v44, v53, v49, s62\n\t" \
  "v_perm_b32 v45, v61, v57, s62\n\t" \
  "v_perm_b32 v46, v69, v65, s62\n\t" \
  "v_perm_b32 v47, v77, v73, s62\n\t" \
  "v_accvgpr_write_b32 a168, v44\n\t" \
  "v_accvgpr_write_b32 a169, v45\n\t" \
  "v_accvgpr_write_b32 a170, v46\n\t" \
  "v_accvgpr_write_b32 a171, v47\n\t" \
  "v_perm_b32 v44, v53, v49, s63\n\t" \
  "v_perm_b32 v45, v61, v57, s63\n\t" \
  "v_perm_b32 v46, v69, v65, s63\n\t" \
  "v_perm_b32 v47, v77, v73, s63\n\t" \
  "v_accvgpr_write_b32 a172, v44\n\t" \
  "v_accvgpr_write_b32 a173, v45\n\t" \
  "v_accvgpr_write_b32 a174, v46\n\t" \
  "v_accvgpr_write_b32 a175, v47\n\t" \
  "v_perm_b32 v44, v54, v50, s62\n\t" \
  "v_perm_b32 v45, v62, v58, s62\n\t" \
  "v_perm_b32 v46, v70, v66, s62\n\t" \
  "v_perm_b32 v47, v78, v74, s62\n\t" \
  "v_accvgpr_write_b32 a176, v44\n\t" \
  "v_accvgpr_write_b32 a177, v45\n\t" \
  "v_accvgpr_write_b32 a178, v46\n\t" \
  "v_accvgpr_write_b32 a179, v47\n\t" \
  "v_perm_b32 v44, v54, v50, s63\n\t" \
  "v_perm_b32 v45, v62, v58, s63\n\t" \
  "v_perm_b32 v46, v70, v66, s63\n\t" \
  "v_perm_b32 v47, v78, v74, s63\n\t" \
  "v_accvgpr_write_b32 a180, v44\n\t" \
  "v_accvgpr_write_b32 a181, v45\n\t" \
  "v_accvgpr_write_b32 a182, v46\n\t" \
  "v_accvgpr_write_b32 a183, v47\n\t" \
  "v_perm_b32 v44, v55, v51, s62\n\t" \
  "v_perm_b32 v45, v63, v59, s62\n\t" \
  "v_perm_b32 v46, v71, v67, s62\n\t" \
  "v_perm_b32 v47, v79, v75, s62\n\t" \
  "v_accvgpr_write_b32 a184, v44\n\t" \
  "v_accvgpr_write_b32 a185, v45\n\t" \
  "v_accvgpr_write_b32 a186, v46\n\t" \
  "v_accvgpr_write_b32 a187, v47\n\t" \
  "v_perm_b32 v44, v55, v51, s63\n\t" \
  "v_perm_b32 v45, v63, v59, s63\n\t" \
  "v_perm_b32 v46, v71, v67, s63\n\t" \
  "v_perm_b32 v47, v79, v75, s63\n\t" \
  "v_accvgpr_write_b32 a188, v44\n\t" \
  "v_accvgpr_write_b32 a189, v45\n\t" \
  "v_accvgpr_write_b32 a190, v46\n\t" \
  "v_accvgpr_write_b32 a191, v47\n\t" \
  "s_xor_b32 s60, s66, 147456\n\t" \
  "s_add_u32 s60, s60, 0\n\t" \
  "s_add_u32 s61, s60, 18432\n\t" \
  "buffer_load_ushort v240, v42, %[W], s60 offen offset:0\n\t" \
  "buffer_load_ushort v160, v42, %[W], s60 offen offset:1152\n\t" \
  "buffer_load_ushort v241, v42, %[W], s60 offen offset:2304\n\t" \
  "buffer_load_ushort v161, v42, %[W], s60 offen offset:3456\n\t" \
  "buffer_load_ushort v242, v42, %[W], s61 offen offset:0\n\t" \
  "buffer_load_ushort v162, v42, %[W], s61 offen offset:1152\n\t" \
  "buffer_load_ushort v243, v42, %[W], s61 offen offset:2304\n\t" \
  "buffer_load_ushort v163, v42, %[W], s61 offen offset:3456\n\t" \
  "s_waitcnt vmcnt(54)\n\t" \
  "v_perm_b32 v44, v84, v80, s62\n\t" \
  "s_waitcnt vmcnt(52)\n\t" \
  "v_perm_b32 v45, v92, v88, s62\n\t" \
  "s_waitcnt vmcnt(50)\n\t" \
  "v_perm_b32 v46, v100, v96, s62\n\t" \
  "s_waitcnt vmcnt(48)\n\t" \
  "v_perm_b32 v47, v108, v104, s62\n\t" \
  "v_accvgpr_write_b32 a192, v44\n\t" \
  "v_accvgpr_write_b32 a193, v45\n\t" \
  "v_accvgpr_write_b32 a194, v46\n\t" \
  "v_accvgpr_write_b32 a195, v47\n\t" \
  "v_perm_b32 v44, v84, v80, s63\n\t" \
  "v_perm_b32 v45, v92, v88, s63\n\t" \
  "v_perm_b32 v46, v100, v96, s63\n\t" \
  "v_perm_b32 v47, v108, v104, s63\n\t" \
  "v_accvgpr_write_b32 a196, v44\n\t" \
  "v_accvgpr_write_b32 a197, v45\n\t" \
  "v_accvgpr_write_b32 a198, v46\n\t" \
  "v_accvgpr_write_b32 a199, v47\n\t" \
  "v_perm_b32 v44, v85, v81, s62\n\t" \
  "v_perm_b32 v45, v93, v89, s62\n\t" \
  "v_perm_b32 v46, v101, v97, s62\n\t" \
  "v_perm_b32 v47, v109, v105, s62\n\t" \
  "v_accvgpr_write_b32 a200, v44\n\t" \
  "v_accvgpr_write_b32 a201, v45\n\t" \
  "v_accvgpr_write_b32 a202, v46\n\t" \
  "v_accvgpr_write_b32 a203, v47\n\t" \
  "v_perm_b32 v44, v85, v81, s63\n\t" \
  "v_perm_b32 v45, v93, v89, s63\n\t" \
  "v_perm_b32 v46, v101, v97, s63\n\t" \
  "v_perm_b32 v47, v109, v105, s63\n\t" \
  "v_accvgpr_write_b32 a204, v44\n\t" \
  "v_accvgpr_write_b32 a205, v45\n\t" \
  "v_accvgpr_write_b32 a206, v46\n\t" \
  "v_accvgpr_write_b32 a207, v47\n\t" \
  "v_perm_b32 v44, v86, v82, s62\n\t" \
  "v_perm_b32 v45, v94, v90, s62\n\t" \
  "v_perm_b32 v46, v102, v98, s62\n\t" \
  "v_perm_b32 v47, v110, v106, s62\n\t" \
  "v_accvgpr_write_b32 a208, v44\n\t" \
  "v_accvgpr_write_b32 a209, v45\n\t" \
  "v_accvgpr_write_b32 a210, v46\n\t" \
  "v_accvgpr_write_b32 a211, v47\n\t" \
  "v_perm_b32 v44, v86, v82, s63\n\t" \
  "v_perm_b32 v45, v94, v90, s63\n\t" \
  "v_perm_b32 v46, v102, v98, s63\n\t" \
  "v_perm_b32 v47, v110, v106, s63\n\t" \
  "v_accvgpr_write_b32 a212, v44\n\t" \
  "v_accvgpr_write_b32 a213, v45\n\t" \
  "v_accvgpr_write_b32 a214, v46\n\t" \
  "v_accvgpr_write_b32 a215, v47\n\t" \
  "v_perm_b32 v44, v87, v83, s62\n\t" \
  "v_perm_b32 v45, v95, v91, s62\n\t" \
  "v_perm_b32 v46, v103, v99, s62\n\t" \
  "v_perm_b32 v47, v111, v107, s62\n\t" \
  "v_accvgpr_write_b32 a216, v44\n\t" \
  "v_accvgpr_write_b32 a217, v45\n\t" \
  "v_accvgpr_write_b32 a218, v46\n\t" \
  "v_accvgpr_write_b32 a219, v47\n\t" \
  "v_perm_b32 v44, v87, v83, s63\n\t" \
  "v_perm_b32 v45, v95, v91, s63\n\t" \
  "v_perm_b32 v46, v103, v99, s63\n\t" \
  "v_perm_b32 v47, v111, v107, s63\n\t" \
  "v_accvgpr_write_b32 a220, v44\n\t" \
  "v_accvgpr_write_b32 a221, v45\n\t" \
  "v_accvgpr_write_b32 a222, v46\n\t" \
  "v_accvgpr_write_b32 a223, v47\n\t" \
  "s_xor_b32 s60, s66, 147456\n\t" \
  "s_add_u32 s60, s60, 36864\n\t" \
  "s_add_u32 s61, s60, 18432\n\t" \
  "buffer_load_ushort v244, v42, %[W], s60 offen offset:0\n\t" \
  "buffer_load_ushort v164, v42, %[W], s60 offen offset:1152\n\t" \
  "buffer_load_ushort v245, v42, %[W], s60 offen offset:2304\n\t" \
  "buffer_load_ushort v165, v42, %[W], s60 offen offset:3456\n\t" \
  "buffer_load_ushort v246, v42, %[W], s61 offen offset:0\n\t" \
  "buffer_load_ushort v166, v42, %[W], s61 offen offset:1152\n\t" \
  "buffer_load_ushort v247, v42, %[W], s61 offen offset:2304\n\t" \
  "buffer_load_ushort v167, v42, %[W], s61 offen offset:3456\n\t" \
  "s_waitcnt vmcnt(54)\n\t" \
  "v_perm_b32 v44, v116, v112, s62\n\t" \
  "s_waitcnt vmcnt(52)\n\t" \
  "v_perm_b32 v45, v124, v120, s62\n\t" \
  "s_waitcnt vmcnt(50)\n\t" \
  "v_perm_b32 v46, v132, v128, s62\n\t" \
  "s_waitcnt vmcnt(48)\n\t" \
  "v_perm_b32 v47, v140, v136, s62\n\t" \
  "v_accvgpr_write_b32 a224, v44\n\t" \
  "v_accvgpr_write_b32 a225, v45\n\t" \
  "v_accvgpr_write_b32 a226, v46\n\t" \
  "v_accvgpr_write_b32 a227, v47\n\t" \
  "v_perm_b32 v44, v116, v112, s63\n\t" \
  "v_perm_b32 v45, v124, v120, s63\n\t" \
  "v_perm_b32 v46, v132, v128, s63\n\t" \
  "v_perm_b32 v47, v140, v136, s63\n\t" \
  "v_accvgpr_write_b32 a228, v44\n\t" \
  "v_accvgpr_write_b32 a229, v45\n\t" \
  "v_accvgpr_write_b32 a230, v46\n\t" \
  "v_accvgpr_write_b32 a231, v47\n\t" \
  "v_perm_b32 v44, v117, v113, s62\n\t" \
  "v_perm_b32 v45, v125, v121, s62\n\t" \
  "v_perm_b32 v46, v133, v129, s62\n\t" \
  "v_perm_b32 v47, v141, v137, s62\n\t" \
  "v_accvgpr_write_b32 a232, v44\n\t" \
  "v_accvgpr_write_b32 a233, v45\n\t" \
  "v_accvgpr_write_b32 a234, v46\n\t" \
  "v_accvgpr_write_b32 a235, v47\n\t" \
  "v_perm_b32 v44, v117, v113, s63\n\t" \
  "v_perm_b32 v45, v125, v121, s63\n\t" \
  "v_perm_b32 v46, v133, v129, s63\n\t" \
  "v_perm_b32 v47, v141, v137, s63\n\t" \
  "v_accvgpr_write_b32 a236, v44\n\t" \
  "v_accvgpr_write_b32 a237, v45\n\t" \
  "v_accvgpr_write_b32 a238, v46\n\t" \
  "v_accvgpr_write_b32 a239, v47\n\t" \
  "v_perm_b32 v44, v118, v114, s62\n\t" \
  "v_perm_b32 v45, v126, v122, s62\n\t" \
  "v_perm_b32 v46, v134, v130, s62\n\t" \
  "v_perm_b32 v47, v142, v138, s62\n\t" \
  "v_accvgpr_write_b32 a240, v44\n\t" \
  "v_accvgpr_write_b32 a241, v45\n\t" \
  "v_accvgpr_write_b32 a242, v46\n\t" \
  "v_accvgpr_write_b32 a243, v47\n\t" \
  "v_perm_b32 v44, v118, v114, s63\n\t" \
  "v_perm_b32 v45, v126, v122, s63\n\t" \
  "v_perm_b32 v46, v134, v130, s63\n\t" \
  "v_perm_b32 v47, v142, v138, s63\n\t" \
  "v_accvgpr_write_b32 a244, v44\n\t" \
  "v_accvgpr_write_b32 a245, v45\n\t" \
  "v_accvgpr_write_b32 a246, v46\n\t" \
  "v_accvgpr_write_b32 a247, v47\n\t" \
  "v_perm_b32 v44, v119, v115, s62\n\t" \
  "v_perm_b32 v45, v127, v123, s62\n\t" \
  "v_perm_b32 v46, v135, v131, s62\n\t" \
  "v_perm_b32 v47, v143, v139, s62\n\t" \
  "v_accvgpr_write_b32 a248, v44\n\t" \
  "v_accvgpr_write_b32 a249, v45\n\t" \
  "v_accvgpr_write_b32 a250, v46\n\t" \
  "v_accvgpr_write_b32 a251, v47\n\t" \
  "v_perm_b32 v44, v119, v115, s63\n\t" \
  "v_perm_b32 v45, v127, v123, s63\n\t" \
  "v_perm_b32 v46, v135, v131, s63\n\t" \
  "v_perm_b32 v47, v143, v139, s63\n\t" \
  "v_accvgpr_write_b32 a252, v44\n\t" \
  "v_accvgpr_write_b32 a253, v45\n\t" \
  "v_accvgpr_write_b32 a254, v46\n\t" \
  "v_accvgpr_write_b32 a255, v47\n\t" \
  "s_xor_b32 s60, s66, 147456\n\t" \
  "s_add_u32 s60, s60, 73728\n\t" \
  "s_add_u32 s61, s60, 18432\n\t" \
  "buffer_load_ushort v248, v42, %[W], s60 offen offset:0\n\t" \
  "buffer_load_ushort v168, v42, %[W], s60 offen offset:1152\n\t" \
  "buffer_load_ushort v249, v42, %[W], s60 offen offset:2304\n\t" \
  "buffer_load_ushort v169, v42, %[W], s60 offen offset:3456\n\t" \
  "buffer_load_ushort v250, v42, %[W], s61 offen offset:0\n\t" \
  "buffer_load_ushort v170, v42, %[W], s61 offen offset:1152\n\t" \
  "buffer_load_ushort v251, v42, %[W], s61 offen offset:2304\n\t" \
  "buffer_load_ushort v171, v42, %[W], s61 offen offset:3456\n\t" \
  "s_xor_b32 s60, s66, 147456\n\t" \
  "s_add_u32 s60, s60, 110592\n\t" \
  "s_add_u32 s61, s60, 18432\n\t" \
  "s_waitcnt vmcnt(40)\n\t" \
  "buffer_load_ushort v252, v42, %[W], s60 offen offset:0\n\t" \
  "buffer_load_ushort v172, v42, %[W], s60 offen offset:1152\n\t" \
  "buffer_load_ushort v253, v42, %[W], s60 offen offset:2304\n\t" \
  "buffer_load_ushort v173, v42, %[W], s60 offen offset:3456\n\t" \
  "buffer_load_ushort v254, v42, %[W], s61 offen offset:0\n\t" \
  "buffer_load_ushort v174, v42, %[W], s61 offen offset:1152\n\t" \
  "buffer_load_ushort v255, v42, %[W], s61 offen offset:2304\n\t" \
  "buffer_load_ushort v175, v42, %[W], s61 offen offset:3456\n\t" \
  "v_lshl_or_b32 v224, v144, 16, v224\n\t" \
  "v_lshl_or_b32 v225, v145, 16, v225\n\t" \
  "v_lshl_or_b32 v226, v146, 16, v226\n\t" \
  "v_lshl_or_b32 v227, v147, 16, v227\n\t" \
  "v_lshl_or_b32 v228, v148, 16, v228\n\t" \
  "v_lshl_or_b32 v229, v149, 16, v229\n\t" \
  "v_lshl_or_b32 v230, v150, 16, v230\n\t" \
  "v_lshl_or_b32 v231, v151, 16, v231\n\t" \
  "s_waitcnt vmcnt(46)\n\t" \
  "v_lshl_or_b32 v232, v152, 16, v232\n\t" \
  "s_waitcnt vmcnt(44)\n\t" \
  "v_lshl_or_b32 v233, v153, 16, v233\n\t" \
  "s_waitcnt vmcnt(42)\n\t" \
  "v_lshl_or_b32 v234, v154, 16, v234\n\t" \
  "s_waitcnt vmcnt(40)\n\t" \
  "v_lshl_or_b32 v235, v155, 16, v235\n\t" \
  "s_waitcnt vmcnt(38)\n\t" \
  "v_lshl_or_b32 v236, v156, 16, v236\n\t" \
  "s_waitcnt vmcnt(36)\n\t" \
  "v_lshl_or_b32 v237, v157, 16, v237\n\t" \
  "s_waitcnt vmcnt(34)\n\t" \
  "v_lshl_or_b32 v238, v158, 16, v238\n\t" \
  "s_waitcnt vmcnt(32)\n\t" \
  "v_lshl_or_b32 v239, v159, 16, v239\n\t" \
  "s_waitcnt vmcnt(30)\n\t" \
  "v_lshl_or_b32 v240, v160, 16, v240\n\t" \
  "s_waitcnt vmcnt(28)\n\t" \
  "v_lshl_or_b32 v241, v161, 16, v241\n\t" \
  "s_waitcnt vmcnt(26)\n\t" \
  "v_lshl_or_b32 v242, v162, 16, v242\n\t" \
  "s_waitcnt vmcnt(24)\n\t" \
  "v_lshl_or_b32 v243, v163, 16, v243\n\t" \
  "s_waitcnt vmcnt(22)\n\t" \
  "v_lshl_or_b32 v244, v164, 16, v244\n\t" \
  "s_waitcnt vmcnt(20)\n\t" \
  "v_lshl_or_b32 v245, v165, 16, v245\n\t" \
  "s_waitcnt vmcnt(18)\n\t" \
  "v_lshl_or_b32 v246, v166, 16, v246\n\t" \
  "s_waitcnt vmcnt(16)\n\t" \
  "v_lshl_or_b32 v247, v167, 16, v247\n\t" \
  "s_waitcnt vmcnt(14)\n\t" \
  "v_lshl_or_b32 v248, v168, 16, v248\n\t" \
  "s_waitcnt vmcnt(12)\n\t" \
  "v_lshl_or_b32 v249, v169, 16, v249\n\t" \
  "s_waitcnt vmcnt(10)\n\t" \
  "v_lshl_or_b32 v250, v170, 16, v250\n\t" \
  "s_waitcnt vmcnt(8)\n\t" \
  "v_lshl_or_b32 v251, v171, 16, v251\n\t" \
  "s_waitcnt vmcnt(6)\n\t" \
  "v_lshl_or_b32 v252, v172, 16, v252\n\t" \
  "s_waitcnt vmcnt(4)\n\t" \
  "v_lshl_or_b32 v253, v173, 16, v253\n\t" \
  "s_waitcnt vmcnt(2)\n\t" \
  "v_lshl_or_b32 v254, v174, 16, v254\n\t" \
  "s_waitcnt vmcnt(0)\n\t" \
  "v_lshl_or_b32 v255, v175, 16, v255\n\t" \
  "s_mov_b32 s84, " TK_CAP "\n\t" \
  "s_mov_b32 s48, 0\n\t" \
  "s_mov_b32 s49, 0xf0000000\n\t" \
  "s_mov_b32 s46, s45\n\t" \
  "v_mov_b32 v5, -1\n\t" \
  "v_lshrrev_b32 v13, 2, v0\n\t" \
  "s_mov_b32 s60, " TK_CTAB "\n\t" \
  "buffer_load_dword v10, v13, %[X], s60 offen offset:88 sc1\n\t" \
  "buffer_load_dword v13, v13, %[X], s60 offen offset:0 sc1\n\t" \
  "s_waitcnt vmcnt(0)\n\t" \
  "v_and_b32 v10, 1, v10\n\t" \
  "v_and_b32 v13, 1, v13\n\t" \
  "v_cmp_ne_u32 s[90:91], s67, v10\n\t" \
  "v_cmp_ne_u32 s[88:89], s67, v13\n\t" \
  "s_nop 1\n\t" \
  "s_mov_b32 s86, s88\n\t" \
  "v_lshrrev_b32 v13, 2, v0\n\t" \
  "s_mov_b32 s60, " TK_CTAB "\n\t" \
  "buffer_load_dword v10, v13, %[X], s60 offen offset:88 sc1\n\t" \
  "buffer_load_dword v13, v13, %[X], s60 offen offset:344 sc1\n\t" \
  "s_waitcnt vmcnt(0)\n\t" \
  "v_and_b32 v10, 1, v10\n\t" \
  "v_and_b32 v13, 1, v13\n\t" \
  "v_cmp_ne_u32 s[90:91], s67, v10\n\t" \
  "v_cmp_ne_u32 s[88:89], s67, v13\n\t" \
  "s_nop 1\n\t" \
  "s_and_b32 s60, s88, 256\n\t" \
  "s_lshl_b32 s60, s60, 5\n\t" \
  "s_add_u32 s44, s44, s60\n\t" \
  "s_add_u32 s83, s48, 0\n\t" \
  "s_lshr_b64 s[60:61], s[90:91], s83\n\t" \
  "s_and_b32 s60, s60, 1\n\t" \
  "s_lshl_b32 s60, s60, 13\n\t" \
  "s_add_u32 s83, s83, " TK_SG0 "\n\t" \
  "s_lshl_b32 s83, s83, 21\n\t" \
  "s_or_b32 s83, s83, s60\n\t" \
  "s_mov_b32 s81, s83\n\t" \
  "LCOLD_%=:\n\t" \
  "s_add_u32 s62, s83, s41\n\t" \
  "s_mov_b32 s61, 0\n\t" \
  "LPQ_%=:\n\t" \
  "buffer_load_dword v10, v9, %[X], s62 offen sc1\n\t" \
  "s_waitcnt vmcnt(0)\n\t" \
  "v_cmp_eq_u32 vcc, 0, v10\n\t" \
  "s_cbranch_vccz LPQ_%=x\n\t" \
  "s_add_u32 s61, s61, 1\n\t" \
  "s_sleep 4\n\t" \
  "s_cmp_gt_u32 s61, s84\n\t" \
  "s_cbranch_scc1 LPQ_%=g\n\t" \
  "s_and_b32 s60, s61, 1023\n\t" \
  "s_cmp_lg_u32 s60, 0\n\t" \
  "s_cbranch_scc1 LPQ_%=\n\t" \
  "buffer_load_dword v10, off, %[X], s92 sc1\n\t" \
  "s_waitcnt vmcnt(0)\n\t" \
  "v_readfirstlane_b32 s60, v10\n\t" \
  "s_nop 0\n\t" \
  "s_cmp_eq_u32 s60, 0\n\t" \
  "s_cbranch_scc1 LPQ_%=\n\t" \
  "LPQ_%=g:\n\t" \
  "s_mov_b32 s84, 0\n\t" \
  "s_or_b32 s60, s48, 1073741824\n\t" \
  "v_mov_b32 v10, s60\n\t" \
  "s_mov_b64 exec, 1\n\t" \
  "buffer_store_dword v10, off, %[X], s92 sc1\n\t" \
  "s_mov_b64 exec, -1\n\t" \
  "LPQ_%=x:\n\t" \
  "s_add_u32 m0, s46, 0\n\t" \
  "s_add_u32 s47, s81, s36\n\t" \
  "buffer_load_dwordx4 v0, %[X], s47 offen offset:0 sc1 lds\n\t" \
  "buffer_load_dwordx4 v0, %[X], s47 offen offset:1024 sc1 lds\n\t" \
  "buffer_load_dwordx4 v0, %[X], s47 offen offset:2048 sc1 lds\n\t" \
  "buffer_load_dwordx4 v0, %[X], s47 offen offset:3072 sc1 lds\n\t" \
  "s_add_u32 m0, s46, 4096\n\t" \
  "s_add_u32 s47, s81, s37\n\t" \
  "buffer_load_dwordx4 v0, %[X], s47 offen offset:0 sc1 lds\n\t" \
  "buffer_load_dwordx4 v0, %[X], s47 offen offset:1024 sc1 lds\n\t" \
  "buffer_load_dwordx4 v0, %[X], s47 offen offset:2048 sc1 lds\n\t" \
  "buffer_load_dwordx4 v0, %[X], s47 offen offset:3072 sc1 lds\n\t" \
  "s_add_u32 m0, s46, 8192\n\t" \
  "s_add_u32 s47, s81, s38\n\t" \
  "buffer_load_dwordx4 v0, %[X], s47 offen offset:0 sc1 lds\n\t" \
  "buffer_load_dwordx4 v0, %[X], s47 offen offset:1024 sc1 lds\n\t" \
  "buffer_load_dwordx4 v0, %[X], s47 offen offset:2048 sc1 lds\n\t" \
  "buffer_load_dwordx4 v0, %[X], s47 offen offset:3072 sc1 lds\n\t" \
  "s_waitcnt vmcnt(0)\n\t" \
  "LTOP_%=:\n\t" \
  "s_memrealtime s[78:79]\n\t" \
  "v_readfirstlane_b32 s76, v5\n\t" \
  "s_waitcnt lgkmcnt(0)\n\t" \
  "s_add_u32 s60, s76, 1\n\t" \
  "s_not_b32 s76, s76\n\t" \
  "s_cmp_gt_u32 s60, 1\n\t" \
  "s_cselect_b32 s61, -1, 0\n\t" \
  "s_lshl_b32 s60, s78, 8\n\t" \
  "s_sub_u32 s76, s76, s60\n\t" \
  "s_ashr_i32 s76, s76, 8\n\t" \
  "s_add_i32 s76, s76, s77\n\t" \
  "s_min_i32 s76, s76, 400\n\t" \
  "s_and_b32 s76, s76, s61\n\t" \
  "s_add_u32 s57, s78, s76\n\t" \
  TK_AC0 \
  "s_barrier\n\t" \
  TK_AC1 \
  "ds_read_b128 v[48:51], v2 offset:0\n\t" \
  "ds_read_b128 v[64:67], v1 offset:8192\n\t" \
  "ds_read_b128 v[80:83], v1 offset:12288\n\t" \
  "ds_read_b128 v[52:55], v2 offset:1024\n\t" \
  "ds_read_b128 v[68:71], v1 offset:9216\n\t" \
  "ds_read_b128 v[84:87], v1 offset:13312\n\t" \
  "ds_read_b128 v[56:59], v2 offset:2048\n\t" \
  "ds_read_b128 v[72:75], v1 offset:10240\n\t" \
  "ds_read_b128 v[88:91], v1 offset:14336\n\t" \
  "ds_read_b128 v[60:63], v2 offset:3072\n\t" \
  "ds_read_b128 v[76:79], v1 offset:11264\n\t" \
  "ds_read_b128 v[92:95], v1 offset:15360\n\t" \
  "s_mov_b32 s82, s83\n\t" \
  "s_xor_b32 s46, s46, 0x10000\n\t" \
  "s_add_u32 s58, s82, s41\n\t" \
  "s_add_u32 s58, s58, 4096\n\t" \
  "s_add_u32 s59, s81, s39\n\t" \
  "s_cmp_gt_i32 s76, 0\n\t" \
  "s_cselect_b32 s62, 0xf0000000, s58\n\t" \
  "buffer_load_dword v5, off, %[X], s62 sc1\n\t" \
  "s_cmp_gt_i32 s76, " TK_AM "\n\t" \
  "s_cselect_b32 s79, 0xf0000000, s58\n\t" \
  "s_waitcnt lgkmcnt(10)\n\t" \
  "v_mfma_f32_16x16x32_bf16 v[36:39], v[64:67], v[48:51], v[16:19]\n\t" \
  "ds_read_b128 v[96:99], v1 offset:16384\n\t" \
  "s_waitcnt lgkmcnt(10)\n\t" \
  "v_mfma_f32_16x16x32_bf16 v[40:43], v[80:83], v[48:51], v[20:23]\n\t" \
  "ds_read_b128 v[100:103], v1 offset:17408\n\t" \
  "s_waitcnt lgkmcnt(9)\n\t" \
  "v_mfma_f32_16x16x32_bf16 v[36:39], v[68:71], v[52:55], v[36:39]\n\t" \
  "ds_read_b128 v[104:107], v1 offset:18432\n\t" \
  "s_waitcnt lgkmcnt(9)\n\t" \
  "v_mfma_f32_16x16x32_bf16 v[40:43], v[84:87], v[52:55], v[40:43]\n\t" \
  "ds_read_b128 v[108:111], v1 offset:19456\n\t" \
  "s_waitcnt lgkmcnt(8)\n\t" \
  "v_mfma_f32_16x16x32_bf16 v[36:39], v[72:75], v[56:59], v[36:39]\n\t" \
  "ds_read_b128 v[112:115], v1 offset:20480\n\t" \
  "s_waitcnt lgkmcnt(8)\n\t" \
  "v_mfma_f32_16x16x32_bf16 v[40:43], v[88:91], v[56:59], v[40:43]\n\t" \
  "ds_read_b128 v[116:119], v1 offset:21504\n\t" \
  "s_waitcnt lgkmcnt(7)\n\t" \
  "v_mfma_f32_16x16x32_bf16 v[36:39], v[76:79], v[60:63], v[36:39]\n\t" \
  "ds_read_b128 v[120:123], v1 offset:22528\n\t" \
  "s_waitcnt lgkmcnt(7)\n\t" \
  "v_mfma_f32_16x16x32_bf16 v[40:43], v[92:95], v[60:63], v[40:43]\n\t" \
  "ds_read_b128 v[124:127], v1 offset:23552\n\t" \
  "s_add_u32 s83, s48, 1\n\t" \
  "s_lshr_b64 s[60:61], s[90:91], s83\n\t" \
  "s_and_b32 s60, s60, 1\n\t" \
  "s_lshl_b32 s60, s60, 13\n\t" \
  "s_add_u32 s83, s83, " TK_SG0 "\n\t" \
  "s_lshl_b32 s83, s83, 21\n\t" \
  "s_or_b32 s83, s83, s60\n\t" \
  "s_add_u32 s56, s48, 1\n\t" \
  "s_and_b32 s56, s56, " TK_TRM "\n\t" \
  "s_lshr_b64 s[60:61], s[90:91], s56\n\t" \
  "s_and_b32 s60, s60, 1\n\t" \
  "s_lshl_b32 s60, s60, 13\n\t" \
  "s_add_u32 s56, s56, " TK_SG0 "\n\t" \
  "s_lshl_b32 s56, s56, 21\n\t" \
  "s_or_b32 s56, s56, s60\n\t" \
  "v_xor_b32 v1, 0x10000, v1\n\t" \
  "v_xor_b32 v2, 0x10000, v2\n\t" \
  "v_max3_f32 v11, v36, v37, v38\n\t" \
  "v_max3_f32 v11, v11, v39, v40\n\t" \
  "v_max3_f32 v13, v41, v42, v43\n\t" \
  "v_max_f32 v11, v11, v13\n\t" \
  "v_mov_b32 v13, v11\n\t" \
  "s_nop 1\n\t" \
  "v_permlane16_swap_b32 v11, v13\n\t" \
  "v_max_f32 v11, v11, v13\n\t" \
  "v_mov_b32 v13, v11\n\t" \
  "s_nop 1\n\t" \
  "v_permlane32_swap_b32 v11, v13\n\t" \
  "v_max_f32 v11, v11, v13\n\t" \
  "v_mul_f32 v13, 0xbe0293ee, v11\n\t" \
  "v_fmamk_f32 v36, v36, 0x3e0293ee, v13\n\t" \
  "v_fmamk_f32 v37, v37, 0x3e0293ee, v13\n\t" \
  "v_fmamk_f32 v38, v38, 0x3e0293ee, v13\n\t" \
  "v_fmamk_f32 v39, v39, 0x3e0293ee, v13\n\t" \
  "v_fmamk_f32 v40, v40, 0x3e0293ee, v13\n\t" \
  "v_fmamk_f32 v41, v41, 0x3e0293ee, v13\n\t" \
  "v_fmamk_f32 v42, v42, 0x3e0293ee, v13\n\t" \
  "v_fmamk_f32 v43, v43, 0x3e0293ee, v13\n\t" \
  "s_waitcnt vmcnt(0)\n\t" \
  "v_readfirstlane_b32 s76, v5\n\t" \
  "s_barrier\n\t" \
  "s_cmp_eq_u32 s76, 0\n\t" \
  "s_cbranch_scc1 LNG_%=\n\t" \
  "buffer_load_dwordx4 v[196:199], v0, %[X], s59 offen offset:0 sc1\n\t" \
  "buffer_load_dwordx4 v[200:203], v0, %[X], s59 offen offset:1024 sc1\n\t" \
  "buffer_load_dwordx2 v[204:205], v6, %[X], s59 offen offset:2048 sc1\n\t" \
  "buffer_load_dwordx2 v[206:207], v6, %[X], s59 offen offset:2560 sc1\n\t" \
  "buffer_load_dwordx2 v[208:209], v6, %[X], s59 offen offset:3072 sc1\n\t" \
  "buffer_load_dwordx2 v[210:211], v6, %[X], s59 offen offset:3584 sc1\n\t" \
  "LNG_%=:\n\t" \
  "buffer_load_dword v5, off, %[X], s79 sc1\n\t" \
  "s_mov_b64 exec, 1\n\t" \
  "buffer_store_dword v3, off, %[X], s49 sc1\n\t" \
  "s_mov_b64 exec, -1\n\t" \
  "s_mov_b32 s49, 0xf0000000\n\t" \
  "v_exp_f32 v36, v36\n\t" \
  "v_exp_f32 v37, v37\n\t" \
  "v_exp_f32 v38, v38\n\t" \
  "v_exp_f32 v39, v39\n\t" \
  "v_exp_f32 v40, v40\n\t" \
  "v_exp_f32 v41, v41\n\t" \
  "v_exp_f32 v42, v42\n\t" \
  "v_exp_f32 v43, v43\n\t" \
  "v_cvt_pk_bf16_f32 v44, v36, v37\n\t" \
  "v_cvt_pk_bf16_f32 v45, v38, v39\n\t" \
  "v_cvt_pk_bf16_f32 v46, v40, v41\n\t" \
  "v_cvt_pk_bf16_f32 v47, v42, v43\n\t" \
  "v_add_f32 v36, v36, v37\n\t" \
  "v_add_f32 v38, v38, v39\n\t" \
  "s_waitcnt lgkmcnt(7)\n\t" \
  "v_mfma_f32_16x16x32_bf16 v[128:131], v[96:99], v[44:47], 0\n\t" \
  "v_add_f32 v40, v40, v41\n\t" \
  "s_and_b32 s62, s48, 15\n\t" \
  "s_lshr_b32 s63, s62, 1\n\t" \
  "s_waitcnt lgkmcnt(6)\n\t" \
  "v_mfma_f32_16x16x32_bf16 v[132:135], v[100:103], v[44:47], 0\n\t" \
  "v_add_f32 v42, v42, v43\n\t" \
  "s_lshr_b32 s60, s88, s63\n\t" \
  "s_and_b32 s60, s60, 1\n\t" \
  "s_waitcnt lgkmcnt(5)\n\t" \
  "v_mfma_f32_16x16x32_bf16 v[136:139], v[104:107], v[44:47], 0\n\t" \
  "v_add_f32 v36, v36, v38\n\t" \
  "s_lshl_b32 s60, s60, 13\n\t" \
  "s_lshl_b32 s61, s62, 20\n\t" \
  "s_waitcnt lgkmcnt(4)\n\t" \
  "v_mfma_f32_16x16x32_bf16 v[140:143], v[108:111], v[44:47], 0\n\t" \
  "v_add_f32 v40, v40, v42\n\t" \
  "s_add_u32 s50, s61, s60\n\t" \
  "s_add_u32 s50, s50, s43\n\t" \
  "s_waitcnt lgkmcnt(3)\n\t" \
  "v_mfma_f32_16x16x32_bf16 v[144:147], v[112:115], v[44:47], 0\n\t" \
  "v_add_f32 v12, v36, v40\n\t" \
  "s_add_u32 s51, s50, 32768\n\t" \
  "s_add_u32 s52, s51, 32768\n\t" \
  "s_waitcnt lgkmcnt(2)\n\t" \
  "v_mfma_f32_16x16x32_bf16 v[148:151], v[116:119], v[44:47], 0\n\t" \
  "s_add_u32 s53, s52, 32768\n\t" \
  "s_lshl_b32 s61, s62, 17\n\t" \
  "s_waitcnt lgkmcnt(1)\n\t" \
  "v_mfma_f32_16x16x32_bf16 v[152:155], v[120:123], v[44:47], 0\n\t" \
  "s_add_u32 s54, s61, s44\n\t" \
  "s_add_u32 s63, s83, s41\n\t" \
  "s_waitcnt lgkmcnt(0)\n\t" \
  "v_mfma_f32_16x16x32_bf16 v[156:159], v[124:127], v[44:47], 0\n\t" \
  TK_AC2 \
  TK_AC3 \
  "s_cmp_lg_u32 s76, 0\n\t" \
  "s_cbranch_scc1 LGOT_%=\n\t" \
  "s_waitcnt vmcnt(1)\n\t" \
  "v_readfirstlane_b32 s60, v5\n\t" \
  "s_nop 0\n\t" \
  "s_cmp_lg_u32 s60, 0\n\t" \
  "s_cbranch_scc1 LPK_%=x\n\t" \
  "LKW_%=:\n\t" \
  "s_memrealtime s[78:79]\n\t" \
  "s_waitcnt lgkmcnt(0)\n\t" \
  "s_sub_i32 s79, s78, s57\n\t" \
  "s_cmp_lt_i32 s79, 0\n\t" \
  "s_cbranch_scc1 LKW_%=\n\t" \
  "s_mov_b32 s61, 0\n\t" \
  "LPK_%=:\n\t" \
  "buffer_load_dword v5, off, %[X], s58 sc1\n\t" \
  "s_waitcnt vmcnt(0)\n\t" \
  "v_cmp_eq_u32 vcc, 0, v5\n\t" \
  "s_cbranch_vccz LPK_%=x\n\t" \
  "s_add_u32 s61, s61, 1\n\t" \
  "s_sleep " TK_PSK "\n\t" \
  "s_cmp_gt_u32 s61, s84\n\t" \
  "s_cbranch_scc1 LPK_%=g\n\t" \
  "s_and_b32 s60, s61, 1023\n\t" \
  "s_cmp_lg_u32 s60, 0\n\t" \
  "s_cbranch_scc1 LPK_%=\n\t" \
  "buffer_load_dword v5, off, %[X], s92 sc1\n\t" \
  "s_waitcnt vmcnt(0)\n\t" \
  "v_readfirstlane_b32 s60, v5\n\t" \
  "s_nop 0\n\t" \
  "s_cmp_eq_u32 s60, 0\n\t" \
  "s_cbranch_scc1 LPK_%=\n\t" \
  "LPK_%=g:\n\t" \
  "s_mov_b32 s84, 0\n\t" \
  "s_or_b32 s60, s48, 1342177280\n\t" \
  "v_mov_b32 v5, s60\n\t" \
  "s_mov_b64 exec, 1\n\t" \
  "buffer_store_dword v5, off, %[X], s92 sc1\n\t" \
  "s_mov_b64 exec, -1\n\t" \
  "LPK_%=x:\n\t" \
  "buffer_load_dwordx4 v[196:199], v0, %[X], s59 offen offset:0 sc1\n\t" \
  "buffer_load_dwordx4 v[200:203], v0, %[X], s59 offen offset:1024 sc1\n\t" \
  "buffer_load_dwordx2 v[204:205], v6, %[X], s59 offen offset:2048 sc1\n\t" \
  "buffer_load_dwordx2 v[206:207], v6, %[X], s59 offen offset:2560 sc1\n\t" \
  "buffer_load_dwordx2 v[208:209], v6, %[X], s59 offen offset:3072 sc1\n\t" \
  "buffer_load_dwordx2 v[210:211], v6, %[X], s59 offen offset:3584 sc1\n\t" \
  "LGOT_%=:\n\t" \
  TK_PA0 \
  TK_PA1 \
  TK_AC4 \
  "buffer_load_dword v10, v9, %[X], s63 offen sc1\n\t" \
  "s_add_u32 s49, s82, s42\n\t" \
  "s_waitcnt vmcnt(6)\n\t" \
  "v_mfma_f32_16x16x32_bf16 v[36:39], v[196:199], v[48:51], 0\n\t" \
  "v_mfma_f32_16x16x32_bf16 v[40:43], v[196:199], v[52:55], 0\n\t" \
  "s_waitcnt vmcnt(5)\n\t" \
  "v_mfma_f32_16x16x32_bf16 v[36:39], v[200:203], v[56:59], v[36:39]\n\t" \
  "v_mfma_f32_16x16x32_bf16 v[40:43], v[200:203], v[60:63], v[40:43]\n\t" \
  TK_AC5 \
  "s_nop 8\n\t" \
  "v_add_f32 v36, v36, v42\n\t" \
  "v_add_f32 v37, v37, v43\n\t" \
  "v_max3_f32 v14, v36, v37, v11\n\t" \
  "v_mov_b32 v13, v14\n\t" \
  "s_nop 1\n\t" \
  "v_permlane16_swap_b32 v14, v13\n\t" \
  "v_max_f32 v14, v14, v13\n\t" \
  "v_mov_b32 v13, v14\n\t" \
  "s_nop 1\n\t" \
  "v_permlane32_swap_b32 v14, v13\n\t" \
  "v_max_f32 v14, v14, v13\n\t" \
  "v_mul_f32 v14, 0xbe0293ee, v14\n\t" \
  "v_fmamk_f32 v11, v11, 0x3e0293ee, v14\n\t" \
  "v_fmamk_f32 v36, v36, 0x3e0293ee, v14\n\t" \
  "v_fmamk_f32 v37, v37, 0x3e0293ee, v14\n\t" \
  "v_exp_f32 v11, v11\n\t" \
  "v_exp_f32 v36, v36\n\t" \
  "v_exp_f32 v37, v37\n\t" \
  "v_fma_f32 v12, v12, v11, v36\n\t" \
  "v_add_f32 v12, v12, v37\n\t" \
  "v_mov_b32 v13, v12\n\t" \
  "s_nop 1\n\t" \
  "v_permlane16_swap_b32 v12, v13\n\t" \
  "v_add_f32 v12, v12, v13\n\t" \
  "v_mov_b32 v13, v12\n\t" \
  "s_nop 1\n\t" \
  "v_permlane32_swap_b32 v12, v13\n\t" \
  "v_add_f32 v12, v12, v13\n\t" \
  "v_rcp_f32 v12, v12\n\t" \
  "s_nop 0\n\t" \
  "v_mul_f32 v14, v11, v12\n\t" \
  "v_mul_f32 v36, v36, v12\n\t" \
  "v_mul_f32 v37, v37, v12\n\t" \
  "v_cvt_pk_bf16_f32 v26, v36, v37\n\t" \
  "v_cvt_pk_bf16_f32 v33, v36, v37\n\t" \
  "v_pk_mul_f32 v[128:129], v[128:129], v[14:15] op_sel_hi:[1,0]\n\t" \
  "v_pk_mul_f32 v[130:131], v[130:131], v[14:15] op_sel_hi:[1,0]\n\t" \
  "v_pk_mul_f32 v[132:133], v[132:133], v[14:15] op_sel_hi:[1,0]\n\t" \
  "v_pk_mul_f32 v[134:135], v[134:135], v[14:15] op_sel_hi:[1,0]\n\t" \
  "v_pk_mul_f32 v[136:137], v[136:137], v[14:15] op_sel_hi:[1,0]\n\t" \
  "v_pk_mul_f32 v[138:139], v[138:139], v[14:15] op_sel_hi:[1,0]\n\t" \
  "v_pk_mul_f32 v[140:141], v[140:141], v[14:15] op_sel_hi:[1,0]\n\t" \
  "v_pk_mul_f32 v[142:143], v[142:143], v[14:15] op_sel_hi:[1,0]\n\t" \
  "s_waitcnt vmcnt(3)\n\t" \
  "v_mfma_f32_16x16x32_bf16 v[128:131], v[204:207], v[26:29], v[128:131]\n\t" \
  "v_mul_f32 v144, v144, v14\n\t" \
  "v_mul_f32 v145, v145, v14\n\t" \
  "v_mfma_f32_16x16x32_bf16 v[132:135], v[204:207], v[32:35], v[132:135]\n\t" \
  "v_mul_f32 v146, v146, v14\n\t" \
  "v_mul_f32 v147, v147, v14\n\t" \
  "v_mfma_f32_16x16x32_bf16 v[136:139], v[204:207], v[24:27], v[136:139]\n\t" \
  "v_mul_f32 v148, v148, v14\n\t" \
  "v_mul_f32 v149, v149, v14\n\t" \
  "v_mfma_f32_16x16x32_bf16 v[140:143], v[204:207], v[30:33], v[140:143]\n\t" \
  "v_cvt_pk_bf16_f32 v64, v128, v129\n\t" \
  "v_cvt_pk_bf16_f32 v65, v130, v131\n\t" \
  "v_cvt_pk_bf16_f32 v66, v132, v133\n\t" \
  "v_cvt_pk_bf16_f32 v67, v134, v135\n\t" \
  "v_mul_f32 v150, v150, v14\n\t" \
  "v_mul_f32 v151, v151, v14\n\t" \
  TK_AC6 \
  "v_mfma_f32_16x16x32_bf16 v[192:195], v[224:227], v[64:67], 0\n\t" \
  "ds_write_b128 v3, v[64:67] offset:0\n\t" \
  "v_cvt_pk_bf16_f32 v68, v136, v137\n\t" \
  "v_cvt_pk_bf16_f32 v69, v138, v139\n\t" \
  "v_mfma_f32_16x16x32_bf16 v[160:163], v[64:67], a[0:3], 0\n\t" \
  "v_cvt_pk_bf16_f32 v70, v140, v141\n\t" \
  "v_cvt_pk_bf16_f32 v71, v142, v143\n\t" \
  "s_waitcnt vmcnt(1)\n\t" \
  "v_mfma_f32_16x16x32_bf16 v[144:147], v[208:211], v[26:29], v[144:147]\n\t" \
  "v_mul_f32 v152, v152, v14\n\t" \
  "v_mul_f32 v153, v153, v14\n\t" \
  "v_mfma_f32_16x16x32_bf16 v[148:151], v[208:211], v[32:35], v[148:151]\n\t" \
  "v_mul_f32 v154, v154, v14\n\t" \
  "v_mul_f32 v155, v155, v14\n\t" \
  "v_mfma_f32_16x16x32_bf16 v[164:167], v[64:67], a[4:7], 0\n\t" \
  "ds_write_b128 v3, v[68:71] offset:1024\n\t" \
  "v_mul_f32 v156, v156, v14\n\t" \
  "v_mul_f32 v157, v157, v14\n\t" \
  "v_mfma_f32_16x16x32_bf16 v[168:171], v[64:67], a[8:11], 0\n\t" \
  "v_mul_f32 v158, v158, v14\n\t" \
  "v_mul_f32 v159, v159, v14\n\t" \
  "v_mfma_f32_16x16x32_bf16 v[172:175], v[64:67], a[12:15], 0\n\t" \
  "v_cvt_pk_bf16_f32 v72, v144, v145\n\t" \
  "v_cvt_pk_bf16_f32 v73, v146, v147\n\t" \
  "v_mfma_f32_16x16x32_bf16 v[152:155], v[208:211], v[24:27], v[152:155]\n\t" \
  "v_cvt_pk_bf16_f32 v74, v148, v149\n\t" \
  "v_cvt_pk_bf16_f32 v75, v150, v151\n\t" \
  "v_mfma_f32_16x16x32_bf16 v[156:159], v[208:211], v[30:33], v[156:159]\n\t" \
  "v_mfma_f32_16x16x32_bf16 v[176:179], v[64:67], a[16:19], 0\n\t" \
  "ds_write_b128 v3, v[72:75] offset:2048\n\t" \
  "v_mfma_f32_16x16x32_bf16 v[180:183], v[64:67], a[20:23], 0\n\t" \
  "v_mfma_f32_16x16x32_bf16 v[184:187], v[64:67], a[24:27], 0\n\t" \
  "v_cvt_pk_bf16_f32 v76, v152, v153\n\t" \
  "v_cvt_pk_bf16_f32 v77, v154, v155\n\t" \
  "v_mfma_f32_16x16x32_bf16 v[188:191], v[64:67], a[28:31], 0\n\t" \
  "v_cvt_pk_bf16_f32 v78, v156, v157\n\t" \
  "v_cvt_pk_bf16_f32 v79, v158, v159\n\t" \
  "v_mfma_f32_16x16x32_bf16 v[192:195], v[228:231], v[68:71], v[192:195]\n\t" \
  "ds_write_b128 v3, v[76:79] offset:3072\n\t" \
  "v_mfma_f32_16x16x32_bf16 v[160:163], v[68:71], a[32:35], v[160:163]\n\t" \
  "s_waitcnt vmcnt(0)\n\t" \
  "v_cmp_eq_u32 vcc, 0, v10\n\t" \
  "v_mfma_f32_16x16x32_bf16 v[164:167], v[68:71], a[36:39], v[164:167]\n\t" \
  "s_cmp_eq_u64 vcc, 0\n\t" \
  "s_cselect_b32 s81, s56, 0xf0000000\n\t" \
  "s_cmp_eq_u32 s48, 63\n\t" \
  "v_mfma_f32_16x16x32_bf16 v[168:171], v[68:71], a[40:43], v[168:171]\n\t" \
  "s_cselect_b32 s81, 0xf0000000, s81\n\t" \
  "v_mfma_f32_16x16x32_bf16 v[172:175], v[68:71], a[44:47], v[172:175]\n\t" \
  "v_mfma_f32_16x16x32_bf16 v[176:179], v[68:71], a[48:51], v[176:179]\n\t" \
  "s_add_u32 m0, s46, 0\n\t" \
  "s_add_u32 s47, s81, s36\n\t" \
  "buffer_load_dwordx4 v0, %[X], s47 offen offset:0 sc1 lds\n\t" \
  "v_mfma_f32_16x16x32_bf16 v[180:183], v[68:71], a[52:55], v[180:183]\n\t" \
  "v_mfma_f32_16x16x32_bf16 v[184:187], v[68:71], a[56:59], v[184:187]\n\t" \
  "s_waitcnt lgkmcnt(0)\n\t" \
  "s_barrier\n\t" \
  "v_mfma_f32_16x16x32_bf16 v[188:191], v[68:71], a[60:63], v[188:191]\n\t" \
  "ds_read_b128 v[80:83], v4 offset:0\n\t" \
  "v_mfma_f32_16x16x32_bf16 v[192:195], v[232:235], v[72:75], v[192:195]\n\t" \
  "buffer_load_dwordx4 v0, %[X], s47 offen offset:1024 sc1 lds\n\t" \
  "v_mfma_f32_16x16x32_bf16 v[160:163], v[72:75], a[64:67], v[160:163]\n\t" \
  "ds_read_b128 v[84:87], v4 offset:1024\n\t" \
  "v_mfma_f32_16x16x32_bf16 v[164:167], v[72:75], a[68:71], v[164:167]\n\t" \
  "v_mfma_f32_16x16x32_bf16 v[168:171], v[72:75], a[72:75], v[168:171]\n\t" \
  "ds_read_b128 v[88:91], v4 offset:2048\n\t" \
  "v_mfma_f32_16x16x32_bf16 v[172:175], v[72:75], a[76:79], v[172:175]\n\t" \
  "buffer_load_dwordx4 v0, %[X], s47 offen offset:2048 sc1 lds\n\t" \
  "v_mfma_f32_16x16x32_bf16 v[176:179], v[72:75], a[80:83], v[176:179]\n\t" \
  "ds_read_b128 v[92:95], v4 offset:3072\n\t" \
  "v_mfma_f32_16x16x32_bf16 v[180:183], v[72:75], a[84:87], v[180:183]\n\t" \
  "v_mfma_f32_16x16x32_bf16 v[184:187], v[72:75], a[88:91], v[184:187]\n\t" \
  "v_mfma_f32_16x16x32_bf16 v[188:191], v[72:75], a[92:95], v[188:191]\n\t" \
  "buffer_load_dwordx4 v0, %[X], s47 offen offset:3072 sc1 lds\n\t" \
  "v_mfma_f32_16x16x32_bf16 v[192:195], v[236:239], v[76:79], v[192:195]\n\t" \
  "s_add_u32 s62, s48, 2\n\t" \
  "v_mfma_f32_16x16x32_bf16 v[160:163], v[76:79], a[96:99], v[160:163]\n\t" \
  "v_mfma_f32_16x16x32_bf16 v[164:167], v[76:79], a[100:103], v[164:167]\n\t" \
  "s_mul_i32 s63, s62, 43\n\t" \
  "s_lshr_b32 s63, s63, 9\n\t" \
  "v_mfma_f32_16x16x32_bf16 v[168:171], v[76:79], a[104:107], v[168:171]\n\t" \
  "s_add_u32 m0, s46, 4096\n\t" \
  "s_add_u32 s47, s81, s37\n\t" \
  "buffer_load_dwordx4 v0, %[X], s47 offen offset:0 sc1 lds\n\t" \
  "v_mfma_f32_16x16x32_bf16 v[172:175], v[76:79], a[108:111], v[172:175]\n\t" \
  "s_mul_i32 s63, s63, " TK_RM "\n\t" \
  "s_sub_u32 s62, s62, s63\n\t" \
  "v_mfma_f32_16x16x32_bf16 v[176:179], v[76:79], a[112:115], v[176:179]\n\t" \
  "v_mfma_f32_16x16x32_bf16 v[180:183], v[76:79], a[116:119], v[180:183]\n\t" \
  "s_mul_i32 s63, s62, 43\n\t" \
  "s_lshr_b32 s63, s63, 7\n\t" \
  "v_mfma_f32_16x16x32_bf16 v[184:187], v[76:79], a[120:123], v[184:187]\n\t" \
  "buffer_load_dwordx4 v0, %[X], s47 offen offset:1024 sc1 lds\n\t" \
  "v_mfma_f32_16x16x32_bf16 v[188:191], v[76:79], a[124:127], v[188:191]\n\t" \
  "s_mul_i32 s62, s62, 688128\n\t" \
  "s_lshl_b32 s60, s63, 15\n\t" \
  "s_waitcnt lgkmcnt(3)\n\t" \
  "v_mfma_f32_16x16x32_bf16 v[192:195], v[240:243], v[80:83], v[192:195]\n\t" \
  "v_mfma_f32_16x16x32_bf16 v[160:163], v[80:83], a[128:131], v[160:163]\n\t" \
  "s_add_u32 s62, s62, s60\n\t" \
  "s_lshr_b32 s60, s86, s63\n\t" \
  "v_mfma_f32_16x16x32_bf16 v[164:167], v[80:83], a[132:135], v[164:167]\n\t" \
  "buffer_load_dwordx4 v0, %[X], s47 offen offset:2048 sc1 lds\n\t" \
  "v_mfma_f32_16x16x32_bf16 v[168:171], v[80:83], a[136:139], v[168:171]\n\t" \
  "s_and_b32 s60, s60, 1\n\t" \
  "s_lshl_b32 s60, s60, 13\n\t" \
  "v_mfma_f32_16x16x32_bf16 v[172:175], v[80:83], a[140:143], v[172:175]\n\t" \
  "v_mfma_f32_16x16x32_bf16 v[176:179], v[80:83], a[144:147], v[176:179]\n\t" \
  "s_add_u32 s62, s62, s60\n\t" \
  "s_add_u32 s62, s62, 656896\n\t" \
  "v_mfma_f32_16x16x32_bf16 v[180:183], v[80:83], a[148:151], v[180:183]\n\t" \
  "buffer_load_dwordx4 v0, %[X], s47 offen offset:3072 sc1 lds\n\t" \
  "v_mfma_f32_16x16x32_bf16 v[184:187], v[80:83], a[152:155], v[184:187]\n\t" \
  "s_cmp_gt_u32 s48, 61\n\t" \
  "s_cselect_b32 s62, 0xf0000000, s62\n\t" \
  "v_mfma_f32_16x16x32_bf16 v[188:191], v[80:83], a[156:159], v[188:191]\n\t" \
  "s_waitcnt lgkmcnt(2)\n\t" \
  "v_mfma_f32_16x16x32_bf16 v[192:195], v[244:247], v[84:87], v[192:195]\n\t" \
  "v_mfma_f32_16x16x32_bf16 v[160:163], v[84:87], a[160:163], v[160:163]\n\t" \
  "s_add_u32 m0, s46, 8192\n\t" \
  "s_add_u32 s47, s81, s38\n\t" \
  "buffer_load_dwordx4 v0, %[X], s47 offen offset:0 sc1 lds\n\t" \
  "v_mfma_f32_16x16x32_bf16 v[164:167], v[84:87], a[164:167], v[164:167]\n\t" \
  "v_mfma_f32_16x16x32_bf16 v[168:171], v[84:87], a[168:171], v[168:171]\n\t" \
  "v_mfma_f32_16x16x32_bf16 v[172:175], v[84:87], a[172:175], v[172:175]\n\t" \
  "v_mfma_f32_16x16x32_bf16 v[176:179], v[84:87], a[176:179], v[176:179]\n\t" \
  "buffer_load_dwordx4 v0, %[X], s47 offen offset:1024 sc1 lds\n\t" \
  "v_mfma_f32_16x16x32_bf16 v[180:183], v[84:87], a[180:183], v[180:183]\n\t" \
  "v_mfma_f32_16x16x32_bf16 v[184:187], v[84:87], a[184:187], v[184:187]\n\t" \
  "v_mfma_f32_16x16x32_bf16 v[188:191], v[84:87], a[188:191], v[188:191]\n\t" \
  "s_waitcnt lgkmcnt(1)\n\t" \
  "v_mfma_f32_16x16x32_bf16 v[192:195], v[248:251], v[88:91], v[192:195]\n\t" \
  "buffer_load_dwordx4 v0, %[X], s47 offen offset:2048 sc1 lds\n\t" \
  "v_mfma_f32_16x16x32_bf16 v[160:163], v[88:91], a[192:195], v[160:163]\n\t" \
  "v_mfma_f32_16x16x32_bf16 v[164:167], v[88:91], a[196:199], v[164:167]\n\t" \
  "v_mfma_f32_16x16x32_bf16 v[168:171], v[88:91], a[200:203], v[168:171]\n\t" \
  "v_mfma_f32_16x16x32_bf16 v[172:175], v[88:91], a[204:207], v[172:175]\n\t" \
  "buffer_load_dwordx4 v0, %[X], s47 offen offset:3072 sc1 lds\n\t" \
  "v_mfma_f32_16x16x32_bf16 v[176:179], v[88:91], a[208:211], v[176:179]\n\t" \
  "v_mfma_f32_16x16x32_bf16 v[180:183], v[88:91], a[212:215], v[180:183]\n\t" \
  "buffer_load_dword v5, off, %[X], s62 sc1\n\t" \
  "v_mfma_f32_16x16x32_bf16 v[184:187], v[88:91], a[216:219], v[184:187]\n\t" \
  "v_mfma_f32_16x16x32_bf16 v[188:191], v[88:91], a[220:223], v[188:191]\n\t" \
  "s_waitcnt lgkmcnt(0)\n\t" \
  "v_mfma_f32_16x16x32_bf16 v[192:195], v[252:255], v[92:95], v[192:195]\n\t" \
  "v_mfma_f32_16x16x32_bf16 v[160:163], v[92:95], a[224:227], v[160:163]\n\t" \
  "v_mfma_f32_16x16x32_bf16 v[164:167], v[92:95], a[228:231], v[164:167]\n\t" \
  "v_mfma_f32_16x16x32_bf16 v[168:171], v[92:95], a[232:235], v[168:171]\n\t" \
  "v_cvt_pk_bf16_f32 v112, v192, v193\n\t" \
  "v_cvt_pk_bf16_f32 v113, v194, v195\n\t" \
  "v_mfma_f32_16x16x32_bf16 v[172:175], v[92:95], a[236:239], v[172:175]\n\t" \
  "buffer_store_dwordx2 v[112:113], v8, %[X], s54 offen sc1\n\t" \
  "v_mfma_f32_16x16x32_bf16 v[176:179], v[92:95], a[240:243], v[176:179]\n\t" \
  "v_cvt_pk_bf16_f32 v96, v160, v164\n\t" \
  "v_cvt_pk_bf16_f32 v100, v161, v165\n\t" \
  "v_mfma_f32_16x16x32_bf16 v[180:183], v[92:95], a[244:247], v[180:183]\n\t" \
  "v_cvt_pk_bf16_f32 v104, v162, v166\n\t" \
  "v_cvt_pk_bf16_f32 v108, v163, v167\n\t" \
  "v_mfma_f32_16x16x32_bf16 v[184:187], v[92:95], a[248:251], v[184:187]\n\t" \
  "v_cvt_pk_bf16_f32 v97, v168, v172\n\t" \
  "v_cvt_pk_bf16_f32 v101, v169, v173\n\t" \
  "v_mfma_f32_16x16x32_bf16 v[188:191], v[92:95], a[252:255], v[188:191]\n\t" \
  "v_cvt_pk_bf16_f32 v105, v170, v174\n\t" \
  "v_cvt_pk_bf16_f32 v109, v171, v175\n\t" \
  "v_cvt_pk_bf16_f32 v98, v176, v180\n\t" \
  "v_cvt_pk_bf16_f32 v102, v177, v181\n\t" \
  "v_cvt_pk_bf16_f32 v106, v178, v182\n\t" \
  "v_cvt_pk_bf16_f32 v110, v179, v183\n\t" \
  TK_AC7 \
  "s_nop 1\n\t" \
  "v_cvt_pk_bf16_f32 v99, v184, v188\n\t" \
  "buffer_store_dwordx4 v[96:99], v7, %[X], s50 offen sc1\n\t" \
  "v_cvt_pk_bf16_f32 v103, v185, v189\n\t" \
  "buffer_store_dwordx4 v[100:103], v7, %[X], s51 offen sc1\n\t" \
  "v_cvt_pk_bf16_f32 v107, v186, v190\n\t" \
  "buffer_store_dwordx4 v[104:107], v7, %[X], s52 offen sc1\n\t" \
  "v_cvt_pk_bf16_f32 v111, v187, v191\n\t" \
  "buffer_store_dwordx4 v[108:111], v7, %[X], s53 offen sc1\n\t" \
  TK_PA2 \
  TK_AC8 \
  "s_cmp_eq_u32 s48, 63\n\t" \
  "s_cbranch_scc1 LEND_%=\n\t" \
  "s_add_u32 s48, s48, 1\n\t" \
  "s_cmp_eq_u32 s81, s56\n\t" \
  "s_mov_b32 s81, s56\n\t" \
  "s_cbranch_scc0 LCOLD_%=\n\t" \
  "s_waitcnt vmcnt(5)\n\t" \
  "s_branch LTOP_%=\n\t" \
  "LEND_%=:\n\t" \
  "s_waitcnt vmcnt(0)\n\t" \
  "s_barrier\n\t" \
  "s_mov_b64 exec, 1\n\t" \
  "buffer_store_dword v3, off, %[X], s49 sc1\n\t" \
  "s_mov_b64 exec, -1\n\t" \
  "s_mov_b32 s49, 0xf0000000\n\t" \
  TK_PA0 \
  TK_ACE \
  ""
#define QK_ASM \
  "v_lshrrev_b32 v16, 7, v0\n\t" \
  "v_lshlrev_b32 v16, 13, v16\n\t" \
  "v_lshrrev_b32 v19, 2, v0\n\t" \
  "v_and_b32 v19, 64, v19\n\t" \
  "v_xor_b32 v19, v19, v0\n\t" \
  "v_and_b32 v19, 0x70, v19\n\t" \
  "v_add_u32 v16, v16, v19\n\t" \
  "s_mul_i32 s63, s40, 40960\n\t" \
  "v_and_b32 v19, 0x300, v0\n\t" \
  "v_lshlrev_b32 v17, 2, v19\n\t" \
  "v_and_b32 v19, 0xc0, v0\n\t" \
  "v_lshl_add_u32 v17, v19, 1, v17\n\t" \
  "v_and_b32 v19, 0x30, v0\n\t" \
  "v_add3_u32 v17, v17, v19, s63\n\t" \
  "v_lshrrev_b32 v19, 1, v0\n\t" \
  "v_and_b32 v19, 64, v19\n\t" \
  "v_add_u32 v17, v17, v19\n\t" \
  "v_xor_b32 v18, 64, v17\n\t" \
  "s_add_u32 m0, s63, 0\n\t" \
  "s_mov_b32 s60, 0\n\t" \
  "buffer_load_dwordx4 v16, %[W], s60 offen lds\n\t" \
  "s_add_u32 m0, s63, 1024\n\t" \
  "s_mov_b32 s60, 65536\n\t" \
  "buffer_load_dwordx4 v16, %[W], s60 offen lds\n\t" \
  "s_add_u32 m0, s63, 2048\n\t" \
  "s_mov_b32 s60, 131072\n\t" \
  "buffer_load_dwordx4 v16, %[W], s60 offen lds\n\t" \
  "s_add_u32 m0, s63, 3072\n\t" \
  "s_mov_b32 s60, 196608\n\t" \
  "buffer_load_dwordx4 v16, %[W], s60 offen lds\n\t" \
  "s_add_u32 m0, s63, 4096\n\t" \
  "s_mov_b32 s60, 262144\n\t" \
  "buffer_load_dwordx4 v16, %[W], s60 offen lds\n\t" \
  "s_add_u32 m0, s63, 5120\n\t" \
  "s_mov_b32 s60, 327680\n\t" \
  "buffer_load_dwordx4 v16, %[W], s60 offen lds\n\t" \
  "s_add_u32 m0, s63, 6144\n\t" \
  "s_mov_b32 s60, 393216\n\t" \
  "buffer_load_dwordx4 v16, %[W], s60 offen lds\n\t" \
  "s_add_u32 m0, s63, 7168\n\t" \
  "s_mov_b32 s60, 458752\n\t" \
  "buffer_load_dwordx4 v16, %[W], s60 offen lds\n\t" \
  "s_add_u32 m0, s63, 8192\n\t" \
  "s_mov_b32 s60, 524288\n\t" \
  "buffer_load_dwordx4 v16, %[W], s60 offen lds\n\t" \
  "s_add_u32 m0, s63, 9216\n\t" \
  "s_mov_b32 s60, 589824\n\t" \
  "buffer_load_dwordx4 v16, %[W], s60 offen lds\n\t" \
  "s_add_u32 m0, s63, 10240\n\t" \
  "s_mov_b32 s60, 655360\n\t" \
  "buffer_load_dwordx4 v16, %[W], s60 offen lds\n\t" \
  "s_add_u32 m0, s63, 11264\n\t" \
  "s_mov_b32 s60, 720896\n\t" \
  "buffer_load_dwordx4 v16, %[W], s60 offen lds\n\t" \
  "s_add_u32 m0, s63, 12288\n\t" \
  "s_mov_b32 s60, 786432\n\t" \
  "buffer_load_dwordx4 v16, %[W], s60 offen lds\n\t" \
  "s_add_u32 m0, s63, 13312\n\t" \
  "s_mov_b32 s60, 851968\n\t" \
  "buffer_load_dwordx4 v16, %[W], s60 offen lds\n\t" \
  "s_add_u32 m0, s63, 14336\n\t" \
  "s_mov_b32 s60, 917504\n\t" \
  "buffer_load_dwordx4 v16, %[W], s60 offen lds\n\t" \
  "s_add_u32 m0, s63, 15360\n\t" \
  "s_mov_b32 s60, 983040\n\t" \
  "buffer_load_dwordx4 v16, %[W], s60 offen lds\n\t" \
  "s_add_u32 m0, s63, 16384\n\t" \
  "s_mov_b32 s60, 1048576\n\t" \
  "buffer_load_dwordx4 v16, %[W], s60 offen lds\n\t" \
  "s_add_u32 m0, s63, 17408\n\t" \
  "s_mov_b32 s60, 1114112\n\t" \
  "buffer_load_dwordx4 v16, %[W], s60 offen lds\n\t" \
  "s_add_u32 m0, s63, 18432\n\t" \
  "s_mov_b32 s60, 1179648\n\t" \
  "buffer_load_dwordx4 v16, %[W], s60 offen lds\n\t" \
  "s_add_u32 m0, s63, 19456\n\t" \
  "s_mov_b32 s60, 1245184\n\t" \
  "buffer_load_dwordx4 v16, %[W], s60 offen lds\n\t" \
  "s_add_u32 m0, s63, 20480\n\t" \
  "s_mov_b32 s60, 1310720\n\t" \
  "buffer_load_dwordx4 v16, %[W], s60 offen lds\n\t" \
  "s_add_u32 m0, s63, 21504\n\t" \
  "s_mov_b32 s60, 1376256\n\t" \
  "buffer_load_dwordx4 v16, %[W], s60 offen lds\n\t" \
  "s_add_u32 m0, s63, 22528\n\t" \
  "s_mov_b32 s60, 1441792\n\t" \
  "buffer_load_dwordx4 v16, %[W], s60 offen lds\n\t" \
  "s_add_u32 m0, s63, 23552\n\t" \
  "s_mov_b32 s60, 1507328\n\t" \
  "buffer_load_dwordx4 v16, %[W], s60 offen lds\n\t" \
  "s_add_u32 m0, s63, 24576\n\t" \
  "s_mov_b32 s60, 1572864\n\t" \
  "buffer_load_dwordx4 v16, %[W], s60 offen lds\n\t" \
  "s_add_u32 m0, s63, 25600\n\t" \
  "s_mov_b32 s60, 1638400\n\t" \
  "buffer_load_dwordx4 v16, %[W], s60 offen lds\n\t" \
  "s_add_u32 m0, s63, 26624\n\t" \
  "s_mov_b32 s60, 1703936\n\t" \
  "buffer_load_dwordx4 v16, %[W], s60 offen lds\n\t" \
  "s_add_u32 m0, s63, 27648\n\t" \
  "s_mov_b32 s60, 1769472\n\t" \
  "buffer_load_dwordx4 v16, %[W], s60 offen lds\n\t" \
  "s_add_u32 m0, s63, 28672\n\t" \
  "s_mov_b32 s60, 1835008\n\t" \
  "buffer_load_dwordx4 v16, %[W], s60 offen lds\n\t" \
  "s_add_u32 m0, s63, 29696\n\t" \
  "s_mov_b32 s60, 1900544\n\t" \
  "buffer_load_dwordx4 v16, %[W], s60 offen lds\n\t" \
  "s_add_u32 m0, s63, 30720\n\t" \
  "s_mov_b32 s60, 1966080\n\t" \
  "buffer_load_dwordx4 v16, %[W], s60 offen lds\n\t" \
  "s_add_u32 m0, s63, 31744\n\t" \
  "s_mov_b32 s60, 2031616\n\t" \
  "buffer_load_dwordx4 v16, %[W], s60 offen lds\n\t" \
  "s_add_u32 m0, s63, 32768\n\t" \
  "s_mov_b32 s60, 2097152\n\t" \
  "buffer_load_dwordx4 v16, %[W], s60 offen lds\n\t" \
  "s_add_u32 m0, s63, 33792\n\t" \
  "s_mov_b32 s60, 2162688\n\t" \
  "buffer_load_dwordx4 v16, %[W], s60 offen lds\n\t" \
  "s_add_u32 m0, s63, 34816\n\t" \
  "s_mov_b32 s60, 2228224\n\t" \
  "buffer_load_dwordx4 v16, %[W], s60 offen lds\n\t" \
  "s_add_u32 m0, s63, 35840\n\t" \
  "s_mov_b32 s60, 2293760\n\t" \
  "buffer_load_dwordx4 v16, %[W], s60 offen lds\n\t" \
  "s_add_u32 m0, s63, 36864\n\t" \
  "s_mov_b32 s60, 2359296\n\t" \
  "buffer_load_dwordx4 v16, %[W], s60 offen lds\n\t" \
  "s_add_u32 m0, s63, 37888\n\t" \
  "s_mov_b32 s60, 2424832\n\t" \
  "buffer_load_dwordx4 v16, %[W], s60 offen lds\n\t" \
  "s_add_u32 m0, s63, 38912\n\t" \
  "s_mov_b32 s60, 2490368\n\t" \
  "buffer_load_dwordx4 v16, %[W], s60 offen lds\n\t" \
  "s_add_u32 m0, s63, 39936\n\t" \
  "s_mov_b32 s60, 2555904\n\t" \
  "buffer_load_dwordx4 v16, %[W], s60 offen lds\n\t" \
  "s_waitcnt vmcnt(32)\n\t" \
  "ds_read_b64_tr_b16 a[0:1], v17 offset:0\n\t" \
  "ds_read_b64_tr_b16 a[2:3], v17 offset:512\n\t" \
  "ds_read_b64_tr_b16 a[4:5], v17 offset:4096\n\t" \
  "ds_read_b64_tr_b16 a[6:7], v17 offset:4608\n\t" \
  "ds_read_b64_tr_b16 a[72:73], v17 offset:8\n\t" \
  "ds_read_b64_tr_b16 a[74:75], v17 offset:520\n\t" \
  "ds_read_b64_tr_b16 a[76:77], v17 offset:4104\n\t" \
  "ds_read_b64_tr_b16 a[78:79], v17 offset:4616\n\t" \
  "ds_read_b64_tr_b16 a[144:145], v18 offset:0\n\t" \
  "ds_read_b64_tr_b16 a[146:147], v18 offset:512\n\t" \
  "ds_read_b64_tr_b16 a[148:149], v18 offset:4096\n\t" \
  "ds_read_b64_tr_b16 a[150:151], v18 offset:4608\n\t" \
  "ds_read_b64_tr_b16 a[216:217], v18 offset:8\n\t" \
  "ds_read_b64_tr_b16 a[218:219], v18 offset:520\n\t" \
  "ds_read_b64_tr_b16 a[220:221], v18 offset:4104\n\t" \
  "ds_read_b64_tr_b16 a[222:223], v18 offset:4616\n\t" \
  "s_waitcnt lgkmcnt(0)\n\t" \
  "s_add_u32 m0, s63, 0\n\t" \
  "s_mov_b32 s60, 2621440\n\t" \
  "buffer_load_dwordx4 v16, %[W], s60 offen lds\n\t" \
  "s_add_u32 m0, s63, 1024\n\t" \
  "s_mov_b32 s60, 2686976\n\t" \
  "buffer_load_dwordx4 v16, %[W], s60 offen lds\n\t" \
  "s_add_u32 m0, s63, 2048\n\t" \
  "s_mov_b32 s60, 2752512\n\t" \
  "buffer_load_dwordx4 v16, %[W], s60 offen lds\n\t" \
  "s_add_u32 m0, s63, 3072\n\t" \
  "s_mov_b32 s60, 2818048\n\t" \
  "buffer_load_dwordx4 v16, %[W], s60 offen lds\n\t" \
  "s_add_u32 m0, s63, 4096\n\t" \
  "s_mov_b32 s60, 2883584\n\t" \
  "buffer_load_dwordx4 v16, %[W], s60 offen lds\n\t" \
  "s_add_u32 m0, s63, 5120\n\t" \
  "s_mov_b32 s60, 2949120\n\t" \
  "buffer_load_dwordx4 v16, %[W], s60 offen lds\n\t" \
  "s_add_u32 m0, s63, 6144\n\t" \
  "s_mov_b32 s60, 3014656\n\t" \
  "buffer_load_dwordx4 v16, %[W], s60 offen lds\n\t" \
  "s_add_u32 m0, s63, 7168\n\t" \
  "s_mov_b32 s60, 3080192\n\t" \
  "buffer_load_dwordx4 v16, %[W], s60 offen lds\n\t" \
  "s_waitcnt vmcnt(32)\n\t" \
  "ds_read_b64_tr_b16 a[8:9], v17 offset:8192\n\t" \
  "ds_read_b64_tr_b16 a[10:11], v17 offset:8704\n\t" \
  "ds_read_b64_tr_b16 a[12:13], v17 offset:12288\n\t" \
  "ds_read_b64_tr_b16 a[14:15], v17 offset:12800\n\t" \
  "ds_read_b64_tr_b16 a[80:81], v17 offset:8200\n\t" \
  "ds_read_b64_tr_b16 a[82:83], v17 offset:8712\n\t" \
  "ds_read_b64_tr_b16 a[84:85], v17 offset:12296\n\t" \
  "ds_read_b64_tr_b16 a[86:87], v17 offset:12808\n\t" \
  "ds_read_b64_tr_b16 a[152:153], v18 offset:8192\n\t" \
  "ds_read_b64_tr_b16 a[154:155], v18 offset:8704\n\t" \
  "ds_read_b64_tr_b16 a[156:157], v18 offset:12288\n\t" \
  "ds_read_b64_tr_b16 a[158:159], v18 offset:12800\n\t" \
  "ds_read_b64_tr_b16 a[224:225], v18 offset:8200\n\t" \
  "ds_read_b64_tr_b16 a[226:227], v18 offset:8712\n\t" \
  "ds_read_b64_tr_b16 a[228:229], v18 offset:12296\n\t" \
  "ds_read_b64_tr_b16 a[230:231], v18 offset:12808\n\t" \
  "s_waitcnt lgkmcnt(0)\n\t" \
  "s_add_u32 m0, s63, 8192\n\t" \
  "s_mov_b32 s60, 3145728\n\t" \
  "buffer_load_dwordx4 v16, %[W], s60 offen lds\n\t" \
  "s_add_u32 m0, s63, 9216\n\t" \
  "s_mov_b32 s60, 3211264\n\t" \
  "buffer_load_dwordx4 v16, %[W], s60 offen lds\n\t" \
  "s_add_u32 m0, s63, 10240\n\t" \
  "s_mov_b32 s60, 3276800\n\t" \
  "buffer_load_dwordx4 v16, %[W], s60 offen lds\n\t" \
  "s_add_u32 m0, s63, 11264\n\t" \
  "s_mov_b32 s60, 3342336\n\t" \
  "buffer_load_dwordx4 v16, %[W], s60 offen lds\n\t" \
  "s_add_u32 m0, s63, 12288\n\t" \
  "s_mov_b32 s60, 3407872\n\t" \
  "buffer_load_dwordx4 v16, %[W], s60 offen lds\n\t" \
  "s_add_u32 m0, s63, 13312\n\t" \
  "s_mov_b32 s60, 3473408\n\t" \
  "buffer_load_dwordx4 v16, %[W], s60 offen lds\n\t" \
  "s_add_u32 m0, s63, 14336\n\t" \
  "s_mov_b32 s60, 3538944\n\t" \
  "buffer_load_dwordx4 v16, %[W], s60 offen lds\n\t" \
  "s_add_u32 m0, s63, 15360\n\t" \
  "s_mov_b32 s60, 3604480\n\t" \
  "buffer_load_dwordx4 v16, %[W], s60 offen lds\n\t" \
  "s_waitcnt vmcnt(32)\n\t" \
  "ds_read_b64_tr_b16 a[16:17], v17 offset:16384\n\t" \
  "ds_read_b64_tr_b16 a[18:19], v17 offset:16896\n\t" \
  "ds_read_b64_tr_b16 a[20:21], v17 offset:20480\n\t" \
  "ds_read_b64_tr_b16 a[22:23], v17 offset:20992\n\t" \
  "ds_read_b64_tr_b16 a[88:89], v17 offset:16392\n\t" \
  "ds_read_b64_tr_b16 a[90:91], v17 offset:16904\n\t" \
  "ds_read_b64_tr_b16 a[92:93], v17 offset:20488\n\t" \
  "ds_read_b64_tr_b16 a[94:95], v17 offset:21000\n\t" \
  "ds_read_b64_tr_b16 a[160:161], v18 offset:16384\n\t" \
  "ds_read_b64_tr_b16 a[162:163], v18 offset:16896\n\t" \
  "ds_read_b64_tr_b16 a[164:165], v18 offset:20480\n\t" \
  "ds_read_b64_tr_b16 a[166:167], v18 offset:20992\n\t" \
  "ds_read_b64_tr_b16 a[232:233], v18 offset:16392\n\t" \
  "ds_read_b64_tr_b16 a[234:235], v18 offset:16904\n\t" \
  "ds_read_b64_tr_b16 a[236:237], v18 offset:20488\n\t" \
  "ds_read_b64_tr_b16 a[238:239], v18 offset:21000\n\t" \
  "s_waitcnt lgkmcnt(0)\n\t" \
  "s_add_u32 m0, s63, 16384\n\t" \
  "s_mov_b32 s60, 3670016\n\t" \
  "buffer_load_dwordx4 v16, %[W], s60 offen lds\n\t" \
  "s_add_u32 m0, s63, 17408\n\t" \
  "s_mov_b32 s60, 3735552\n\t" \
  "buffer_load_dwordx4 v16, %[W], s60 offen lds\n\t" \
  "s_add_u32 m0, s63, 18432\n\t" \
  "s_mov_b32 s60, 3801088\n\t" \
  "buffer_load_dwordx4 v16, %[W], s60 offen lds\n\t" \
  "s_add_u32 m0, s63, 19456\n\t" \
  "s_mov_b32 s60, 3866624\n\t" \
  "buffer_load_dwordx4 v16, %[W], s60 offen lds\n\t" \
  "s_add_u32 m0, s63, 20480\n\t" \
  "s_mov_b32 s60, 3932160\n\t" \
  "buffer_load_dwordx4 v16, %[W], s60 offen lds\n\t" \
  "s_add_u32 m0, s63, 21504\n\t" \
  "s_mov_b32 s60, 3997696\n\t" \
  "buffer_load_dwordx4 v16, %[W], s60 offen lds\n\t" \
  "s_add_u32 m0, s63, 22528\n\t" \
  "s_mov_b32 s60, 4063232\n\t" \
  "buffer_load_dwordx4 v16, %[W], s60 offen lds\n\t" \
  "s_add_u32 m0, s63, 23552\n\t" \
  "s_mov_b32 s60, 4128768\n\t" \
  "buffer_load_dwordx4 v16, %[W], s60 offen lds\n\t" \
  "s_waitcnt vmcnt(32)\n\t" \
  "ds_read_b64_tr_b16 a[24:25], v17 offset:24576\n\t" \
  "ds_read_b64_tr_b16 a[26:27], v17 offset:25088\n\t" \
  "ds_read_b64_tr_b16 a[28:29], v17 offset:28672\n\t" \
  "ds_read_b64_tr_b16 a[30:31], v17 offset:29184\n\t" \
  "ds_read_b64_tr_b16 a[96:97], v17 offset:24584\n\t" \
  "ds_read_b64_tr_b16 a[98:99], v17 offset:25096\n\t" \
  "ds_read_b64_tr_b16 a[100:101], v17 offset:28680\n\t" \
  "ds_read_b64_tr_b16 a[102:103], v17 offset:29192\n\t" \
  "ds_read_b64_tr_b16 a[168:169], v18 offset:24576\n\t" \
  "ds_read_b64_tr_b16 a[170:171], v18 offset:25088\n\t" \
  "ds_read_b64_tr_b16 a[172:173], v18 offset:28672\n\t" \
  "ds_read_b64_tr_b16 a[174:175], v18 offset:29184\n\t" \
  "ds_read_b64_tr_b16 a[240:241], v18 offset:24584\n\t" \
  "ds_read_b64_tr_b16 a[242:243], v18 offset:25096\n\t" \
  "ds_read_b64_tr_b16 a[244:245], v18 offset:28680\n\t" \
  "ds_read_b64_tr_b16 a[246:247], v18 offset:29192\n\t" \
  "s_waitcnt lgkmcnt(0)\n\t" \
  "s_add_u32 m0, s63, 24576\n\t" \
  "s_mov_b32 s60, 4194304\n\t" \
  "buffer_load_dwordx4 v16, %[W], s60 offen lds\n\t" \
  "s_add_u32 m0, s63, 25600\n\t" \
  "s_mov_b32 s60, 4259840\n\t" \
  "buffer_load_dwordx4 v16, %[W], s60 offen lds\n\t" \
  "s_add_u32 m0, s63, 26624\n\t" \
  "s_mov_b32 s60, 4325376\n\t" \
  "buffer_load_dwordx4 v16, %[W], s60 offen lds\n\t" \
  "s_add_u32 m0, s63, 27648\n\t" \
  "s_mov_b32 s60, 4390912\n\t" \
  "buffer_load_dwordx4 v16, %[W], s60 offen lds\n\t" \
  "s_add_u32 m0, s63, 28672\n\t" \
  "s_mov_b32 s60, 4456448\n\t" \
  "buffer_load_dwordx4 v16, %[W], s60 offen lds\n\t" \
  "s_add_u32 m0, s63, 29696\n\t" \
  "s_mov_b32 s60, 4521984\n\t" \
  "buffer_load_dwordx4 v16, %[W], s60 offen lds\n\t" \
  "s_add_u32 m0, s63, 30720\n\t" \
  "s_mov_b32 s60, 4587520\n\t" \
  "buffer_load_dwordx4 v16, %[W], s60 offen lds\n\t" \
  "s_add_u32 m0, s63, 31744\n\t" \
  "s_mov_b32 s60, 4653056\n\t" \
  "buffer_load_dwordx4 v16, %[W], s60 offen lds\n\t" \
  "s_waitcnt vmcnt(32)\n\t" \
  "ds_read_b64_tr_b16 a[32:33], v17 offset:32768\n\t" \
  "ds_read_b64_tr_b16 a[34:35], v17 offset:33280\n\t" \
  "ds_read_b64_tr_b16 a[36:37], v17 offset:36864\n\t" \
  "ds_read_b64_tr_b16 a[38:39], v17 offset:37376\n\t" \
  "ds_read_b64_tr_b16 a[104:105], v17 offset:32776\n\t" \
  "ds_read_b64_tr_b16 a[106:107], v17 offset:33288\n\t" \
  "ds_read_b64_tr_b16 a[108:109], v17 offset:36872\n\t" \
  "ds_read_b64_tr_b16 a[110:111], v17 offset:37384\n\t" \
  "ds_read_b64_tr_b16 a[176:177], v18 offset:32768\n\t" \
  "ds_read_b64_tr_b16 a[178:179], v18 offset:33280\n\t" \
  "ds_read_b64_tr_b16 a[180:181], v18 offset:36864\n\t" \
  "ds_read_b64_tr_b16 a[182:183], v18 offset:37376\n\t" \
  "ds_read_b64_tr_b16 a[248:249], v18 offset:32776\n\t" \
  "ds_read_b64_tr_b16 a[250:251], v18 offset:33288\n\t" \
  "ds_read_b64_tr_b16 a[252:253], v18 offset:36872\n\t" \
  "ds_read_b64_tr_b16 a[254:255], v18 offset:37384\n\t" \
  "s_waitcnt lgkmcnt(0)\n\t" \
  "s_waitcnt vmcnt(24)\n\t" \
  "ds_read_b64_tr_b16 a[40:41], v17 offset:0\n\t" \
  "ds_read_b64_tr_b16 a[42:43], v17 offset:512\n\t" \
  "ds_read_b64_tr_b16 a[44:45], v17 offset:4096\n\t" \
  "ds_read_b64_tr_b16 a[46:47], v17 offset:4608\n\t" \
  "ds_read_b64_tr_b16 a[112:113], v17 offset:8\n\t" \
  "ds_read_b64_tr_b16 a[114:115], v17 offset:520\n\t" \
  "ds_read_b64_tr_b16 a[116:117], v17 offset:4104\n\t" \
  "ds_read_b64_tr_b16 a[118:119], v17 offset:4616\n\t" \
  "ds_read_b64_tr_b16 a[184:185], v18 offset:0\n\t" \
  "ds_read_b64_tr_b16 a[186:187], v18 offset:512\n\t" \
  "ds_read_b64_tr_b16 a[188:189], v18 offset:4096\n\t" \
  "ds_read_b64_tr_b16 a[190:191], v18 offset:4608\n\t" \
  "ds_read_b64_tr_b16 v[224:225], v18 offset:8\n\t" \
  "ds_read_b64_tr_b16 v[226:227], v18 offset:520\n\t" \
  "ds_read_b64_tr_b16 v[228:229], v18 offset:4104\n\t" \
  "ds_read_b64_tr_b16 v[230:231], v18 offset:4616\n\t" \
  "s_waitcnt lgkmcnt(0)\n\t" \
  "s_waitcnt vmcnt(16)\n\t" \
  "ds_read_b64_tr_b16 a[48:49], v17 offset:8192\n\t" \
  "ds_read_b64_tr_b16 a[50:51], v17 offset:8704\n\t" \
  "ds_read_b64_tr_b16 a[52:53], v17 offset:12288\n\t" \
  "ds_read_b64_tr_b16 a[54:55], v17 offset:12800\n\t" \
  "ds_read_b64_tr_b16 a[120:121], v17 offset:8200\n\t" \
  "ds_read_b64_tr_b16 a[122:123], v17 offset:8712\n\t" \
  "ds_read_b64_tr_b16 a[124:125], v17 offset:12296\n\t" \
  "ds_read_b64_tr_b16 a[126:127], v17 offset:12808\n\t" \
  "ds_read_b64_tr_b16 a[192:193], v18 offset:8192\n\t" \
  "ds_read_b64_tr_b16 a[194:195], v18 offset:8704\n\t" \
  "ds_read_b64_tr_b16 a[196:197], v18 offset:12288\n\t" \
  "ds_read_b64_tr_b16 a[198:199], v18 offset:12800\n\t" \
  "ds_read_b64_tr_b16 v[232:233], v18 offset:8200\n\t" \
  "ds_read_b64_tr_b16 v[234:235], v18 offset:8712\n\t" \
  "ds_read_b64_tr_b16 v[236:237], v18 offset:12296\n\t" \
  "ds_read_b64_tr_b16 v[238:239], v18 offset:12808\n\t" \
  "s_waitcnt lgkmcnt(0)\n\t" \
  "s_waitcnt vmcnt(8)\n\t" \
  "ds_read_b64_tr_b16 a[56:57], v17 offset:16384\n\t" \
  "ds_read_b64_tr_b16 a[58:59], v17 offset:16896\n\t" \
  "ds_read_b64_tr_b16 a[60:61], v17 offset:20480\n\t" \
  "ds_read_b64_tr_b16 a[62:63], v17 offset:20992\n\t" \
  "ds_read_b64_tr_b16 a[128:129], v17 offset:16392\n\t" \
  "ds_read_b64_tr_b16 a[130:131], v17 offset:16904\n\t" \
  "ds_read_b64_tr_b16 a[132:133], v17 offset:20488\n\t" \
  "ds_read_b64_tr_b16 a[134:135], v17 offset:21000\n\t" \
  "ds_read_b64_tr_b16 a[200:201], v18 offset:16384\n\t" \
  "ds_read_b64_tr_b16 a[202:203], v18 offset:16896\n\t" \
  "ds_read_b64_tr_b16 a[204:205], v18 offset:20480\n\t" \
  "ds_read_b64_tr_b16 a[206:207], v18 offset:20992\n\t" \
  "ds_read_b64_tr_b16 v[240:241], v18 offset:16392\n\t" \
  "ds_read_b64_tr_b16 v[242:243], v18 offset:16904\n\t" \
  "ds_read_b64_tr_b16 v[244:245], v18 offset:20488\n\t" \
  "ds_read_b64_tr_b16 v[246:247], v18 offset:21000\n\t" \
  "s_waitcnt lgkmcnt(0)\n\t" \
  "s_waitcnt vmcnt(0)\n\t" \
  "ds_read_b64_tr_b16 a[64:65], v17 offset:24576\n\t" \
  "ds_read_b64_tr_b16 a[66:67], v17 offset:25088\n\t" \
  "ds_read_b64_tr_b16 a[68:69], v17 offset:28672\n\t" \
  "ds_read_b64_tr_b16 a[70:71], v17 offset:29184\n\t" \
  "ds_read_b64_tr_b16 a[136:137], v17 offset:24584\n\t" \
  "ds_read_b64_tr_b16 a[138:139], v17 offset:25096\n\t" \
  "ds_read_b64_tr_b16 a[140:141], v17 offset:28680\n\t" \
  "ds_read_b64_tr_b16 a[142:143], v17 offset:29192\n\t" \
  "ds_read_b64_tr_b16 a[208:209], v18 offset:24576\n\t" \
  "ds_read_b64_tr_b16 a[210:211], v18 offset:25088\n\t" \
  "ds_read_b64_tr_b16 a[212:213], v18 offset:28672\n\t" \
  "ds_read_b64_tr_b16 a[214:215], v18 offset:29184\n\t" \
  "ds_read_b64_tr_b16 v[248:249], v18 offset:24584\n\t" \
  "ds_read_b64_tr_b16 v[250:251], v18 offset:25096\n\t" \
  "ds_read_b64_tr_b16 v[252:253], v18 offset:28680\n\t" \
  "ds_read_b64_tr_b16 v[254:255], v18 offset:29192\n\t" \
  "s_waitcnt lgkmcnt(0)\n\t" \
  "s_mov_b32 s84, " TK_CAP "\n\t" \
  "s_mov_b32 s48, 0\n\t" \
  "s_mov_b32 s49, 0xf0000000\n\t" \
  "s_mov_b32 s46, s41\n\t" \
  "s_mov_b32 s56, 0\n\t" \
  "v_lshrrev_b32 v7, 2, v0\n\t" \
  "s_mov_b32 s60, " TK_CTAB "\n\t" \
  "buffer_load_dword v4, v7, %[X], s60 offen offset:88 sc1\n\t" \
  "buffer_load_dword v7, v7, %[X], s60 offen offset:0 sc1\n\t" \
  "s_waitcnt vmcnt(0)\n\t" \
  "v_and_b32 v4, 1, v4\n\t" \
  "v_and_b32 v7, 1, v7\n\t" \
  "v_cmp_ne_u32 s[90:91], s67, v4\n\t" \
  "v_cmp_ne_u32 s[88:89], s67, v7\n\t" \
  "s_nop 1\n\t" \
  "s_mov_b32 s57, 0xf0000000\n\t" \
  "s_add_u32 s83, s48, 0\n\t" \
  "s_lshr_b64 s[60:61], s[90:91], s83\n\t" \
  "s_and_b32 s60, s60, 1\n\t" \
  "s_lshl_b32 s60, s60, 13\n\t" \
  "s_add_u32 s83, s83, " TK_SG0 "\n\t" \
  "s_lshl_b32 s83, s83, 21\n\t" \
  "s_or_b32 s83, s83, s60\n\t" \
  "s_add_u32 s80, s48, " TK_TRN "\n\t" \
  "s_lshr_b64 s[60:61], s[90:91], s80\n\t" \
  "s_and_b32 s60, s60, 1\n\t" \
  "s_lshl_b32 s60, s60, 13\n\t" \
  "s_add_u32 s80, s80, " TK_SG0 "\n\t" \
  "s_lshl_b32 s80, s80, 21\n\t" \
  "s_or_b32 s80, s80, s60\n\t" \
  "s_add_u32 s62, s83, s39\n\t" \
  "s_add_u32 s80, s80, s39\n\t" \
  "s_add_u32 s80, s80, 2048\n\t" \
  "s_cmp_lt_u32 s48, " TK_TRM "\n\t" \
  "s_cselect_b32 s80, s62, s80\n\t" \
  "v_add_u32 v7, s62, v3\n\t" \
  "s_mov_b32 exec_lo, 0\n\t" \
  "v_mov_b32 v7, s80\n\t" \
  "s_mov_b32 exec_lo, -1\n\t" \
  "LCOLD_%=:\n\t" \
  "s_waitcnt vmcnt(0)\n\t" \
  "s_mov_b64 exec, 1\n\t" \
  "buffer_store_dword v6, off, %[X], s49 sc1\n\t" \
  "s_mov_b64 exec, -1\n\t" \
  "s_mov_b32 s49, 0xf0000000\n\t" \
  "s_add_u32 s55, s83, s36\n\t" \
  "s_cmp_lg_u32 s40, 0\n\t" \
  "s_cbranch_scc1 LGO_%=\n\t" \
  "s_and_b32 s62, s45, 3\n\t" \
  "s_lshl_b32 s62, s62, 7\n\t" \
  "s_add_u32 s62, s62, s83\n\t" \
  "s_add_u32 s62, s62, s39\n\t" \
  "s_mov_b32 s61, 0\n\t" \
  "LPO_%=:\n\t" \
  "buffer_load_dword v4, off, %[X], s62 sc1\n\t" \
  "s_waitcnt vmcnt(0)\n\t" \
  "v_cmp_eq_u32 vcc, 0, v4\n\t" \
  "s_cbranch_vccz LPO_%=x\n\t" \
  "s_add_u32 s61, s61, 1\n\t" \
  "s_sleep " TK_PSH "\n\t" \
  "s_cmp_gt_u32 s61, s84\n\t" \
  "s_cbranch_scc1 LPO_%=g\n\t" \
  "s_and_b32 s60, s61, 1023\n\t" \
  "s_cmp_lg_u32 s60, 0\n\t" \
  "s_cbranch_scc1 LPO_%=\n\t" \
  "buffer_load_dword v4, off, %[X], s92 sc1\n\t" \
  "s_waitcnt vmcnt(0)\n\t" \
  "v_readfirstlane_b32 s60, v4\n\t" \
  "s_nop 0\n\t" \
  "s_cmp_eq_u32 s60, 0\n\t" \
  "s_cbranch_scc1 LPO_%=\n\t" \
  "LPO_%=g:\n\t" \
  "s_mov_b32 s84, 0\n\t" \
  "s_or_b32 s60, s48, 1610612736\n\t" \
  "v_mov_b32 v4, s60\n\t" \
  "s_mov_b64 exec, 1\n\t" \
  "buffer_store_dword v4, off, %[X], s92 sc1\n\t" \
  "s_mov_b64 exec, -1\n\t" \
  "LPO_%=x:\n\t" \
  "s_mov_b32 s61, 0\n\t" \
  "LPH_%=:\n\t" \
  "buffer_load_dword v4, v7, %[X], 0 offen sc1\n\t" \
  "s_waitcnt vmcnt(0)\n\t" \
  "v_cmp_eq_u32 vcc, 0, v4\n\t" \
  "s_cbranch_vccz LPH_%=x\n\t" \
  "s_add_u32 s61, s61, 1\n\t" \
  "s_sleep " TK_PSH "\n\t" \
  "s_cmp_gt_u32 s61, s84\n\t" \
  "s_cbranch_scc1 LPH_%=g\n\t" \
  "s_and_b32 s60, s61, 1023\n\t" \
  "s_cmp_lg_u32 s60, 0\n\t" \
  "s_cbranch_scc1 LPH_%=\n\t" \
  "buffer_load_dword v4, off, %[X], s92 sc1\n\t" \
  "s_waitcnt vmcnt(0)\n\t" \
  "v_readfirstlane_b32 s60, v4\n\t" \
  "s_nop 0\n\t" \
  "s_cmp_eq_u32 s60, 0\n\t" \
  "s_cbranch_scc1 LPH_%=\n\t" \
  "LPH_%=g:\n\t" \
  "s_mov_b32 s84, 0\n\t" \
  "s_or_b32 s60, s48, 1610612736\n\t" \
  "v_mov_b32 v4, s60\n\t" \
  "s_mov_b64 exec, 1\n\t" \
  "buffer_store_dword v4, off, %[X], s92 sc1\n\t" \
  "s_mov_b64 exec, -1\n\t" \
  "LPH_%=x:\n\t" \
  "s_cmp_lt_i32 s57, 0\n\t" \
  "s_cbranch_scc1 LGO_%=\n\t" \
  "s_movk_i32 s63, 4000\n\t" \
  "s_branch LGC_%=u\n\t" \
  "LGC_%=:\n\t" \
  "s_sleep " TK_PSG "\n\t" \
  "buffer_load_dword v8, off, %[X], s57 sc1\n\t" \
  "s_waitcnt vmcnt(0)\n\t" \
  "LGC_%=u:\n\t" \
  "s_memrealtime s[52:53]\n\t" \
  "v_readfirstlane_b32 s60, v8\n\t" \
  "s_waitcnt lgkmcnt(0)\n\t" \
  "s_sub_u32 s63, s63, 1\n\t" \
  "s_cbranch_scc1 LGO_%=\n\t" \
  "s_cmp_eq_u32 s60, -1\n\t" \
  "s_cbranch_scc1 LGC_%=\n\t" \
  "s_not_b32 s60, s60\n\t" \
  "s_lshl_b32 s61, s52, 8\n\t" \
  "s_sub_u32 s60, s60, s61\n\t" \
  "s_ashr_i32 s60, s60, 8\n\t" \
  "s_sub_i32 s60, s60, " TK_QXC "\n\t" \
  "s_min_i32 s60, s60, 400\n\t" \
  "s_add_u32 s54, s52, s60\n\t" \
  "LGC_%=w:\n\t" \
  "s_sleep 1\n\t" \
  "s_memrealtime s[52:53]\n\t" \
  "s_waitcnt lgkmcnt(0)\n\t" \
  "s_sub_i32 s53, s52, s54\n\t" \
  "s_cmp_lt_i32 s53, 0\n\t" \
  "s_cbranch_scc1 LGC_%=w\n\t" \
  "LGO_%=:\n\t" \
  "s_barrier\n\t" \
  "s_add_u32 m0, s46, 0\n\t" \
  "s_add_u32 s47, s55, 0\n\t" \
  "buffer_load_dwordx4 v0, %[X], s47 offen sc1 lds\n\t" \
  "s_add_u32 m0, s46, 4096\n\t" \
  "s_add_u32 s47, s55, 4096\n\t" \
  "buffer_load_dwordx4 v0, %[X], s47 offen sc1 lds\n\t" \
  "s_add_u32 m0, s46, 8192\n\t" \
  "s_add_u32 s47, s55, 16384\n\t" \
  "buffer_load_dwordx4 v0, %[X], s47 offen sc1 lds\n\t" \
  "s_add_u32 m0, s46, 12288\n\t" \
  "s_add_u32 s47, s55, 20480\n\t" \
  "buffer_load_dwordx4 v0, %[X], s47 offen sc1 lds\n\t" \
  "s_add_u32 m0, s46, 16384\n\t" \
  "s_add_u32 s47, s55, 32768\n\t" \
  "buffer_load_dwordx4 v0, %[X], s47 offen sc1 lds\n\t" \
  "s_add_u32 m0, s46, 20480\n\t" \
  "s_add_u32 s47, s55, 36864\n\t" \
  "buffer_load_dwordx4 v0, %[X], s47 offen sc1 lds\n\t" \
  "s_add_u32 m0, s46, 24576\n\t" \
  "s_add_u32 s47, s55, 49152\n\t" \
  "buffer_load_dwordx4 v0, %[X], s47 offen sc1 lds\n\t" \
  "s_add_u32 m0, s46, 28672\n\t" \
  "s_add_u32 s47, s55, 53248\n\t" \
  "buffer_load_dwordx4 v0, %[X], s47 offen sc1 lds\n\t" \
  "s_add_u32 m0, s46, 32768\n\t" \
  "s_add_u32 s47, s55, 65536\n\t" \
  "buffer_load_dwordx4 v0, %[X], s47 offen sc1 lds\n\t" \
  "s_waitcnt vmcnt(5)\n\t" \
  "LTOP_%=:\n\t" \
  "s_barrier\n\t" \
  TK_PQ1 \
  "ds_read_b128 v[64:67], v1 offset:0\n\t" \
  "ds_read_b128 v[68:71], v1 offset:2048\n\t" \
  "ds_read_b128 v[72:75], v2 offset:0\n\t" \
  "ds_read_b128 v[76:79], v2 offset:2048\n\t" \
  "ds_read_b128 v[80:83], v1 offset:4096\n\t" \
  "ds_read_b128 v[84:87], v1 offset:6144\n\t" \
  "s_mov_b32 s82, s83\n\t" \
  "s_xor_b32 s46, s46, 0x10000\n\t" \
  "s_waitcnt lgkmcnt(5)\n\t" \
  "v_mfma_f32_16x16x32_bf16 v[16:19], a[0:3], v[64:67], 0\n\t" \
  "ds_read_b128 v[88:91], v2 offset:4096\n\t" \
  "v_mfma_f32_16x16x32_bf16 v[20:23], a[72:75], v[64:67], 0\n\t" \
  "s_add_u32 s50, s48, 0\n\t" \
  "s_and_b32 s50, s50, " TK_TRM "\n\t" \
  "v_mfma_f32_16x16x32_bf16 v[24:27], a[144:147], v[64:67], 0\n\t" \
  "ds_read_b128 v[92:95], v2 offset:6144\n\t" \
  "v_mfma_f32_16x16x32_bf16 v[28:31], a[216:219], v[64:67], 0\n\t" \
  "s_lshr_b64 s[60:61], s[90:91], s50\n\t" \
  "s_and_b32 s60, s60, 1\n\t" \
  "s_waitcnt lgkmcnt(5)\n\t" \
  "v_mfma_f32_16x16x32_bf16 v[16:19], a[4:7], v[72:75], v[16:19]\n\t" \
  "ds_read_b128 v[96:99], v1 offset:8192\n\t" \
  "v_mfma_f32_16x16x32_bf16 v[20:23], a[76:79], v[72:75], v[20:23]\n\t" \
  "s_lshl_b32 s60, s60, 13\n\t" \
  "s_add_u32 s50, s50, " TK_SG0 "\n\t" \
  "v_mfma_f32_16x16x32_bf16 v[24:27], a[148:151], v[72:75], v[24:27]\n\t" \
  "ds_read_b128 v[100:103], v1 offset:10240\n\t" \
  "v_mfma_f32_16x16x32_bf16 v[28:31], a[220:223], v[72:75], v[28:31]\n\t" \
  "s_lshl_b32 s50, s50, 21\n\t" \
  "s_or_b32 s50, s50, s60\n\t" \
  "s_waitcnt lgkmcnt(5)\n\t" \
  "v_mfma_f32_16x16x32_bf16 v[16:19], a[8:11], v[80:83], v[16:19]\n\t" \
  "ds_read_b128 v[104:107], v2 offset:8192\n\t" \
  "v_mfma_f32_16x16x32_bf16 v[20:23], a[80:83], v[80:83], v[20:23]\n\t" \
  "s_add_u32 s50, s50, s37\n\t" \
  "s_add_u32 s51, s50, 4096\n\t" \
  "v_mfma_f32_16x16x32_bf16 v[24:27], a[152:155], v[80:83], v[24:27]\n\t" \
  "ds_read_b128 v[108:111], v2 offset:10240\n\t" \
  "v_mfma_f32_16x16x32_bf16 v[28:31], a[224:227], v[80:83], v[28:31]\n\t" \
  "s_add_u32 s83, s48, 1\n\t" \
  "s_lshr_b64 s[60:61], s[90:91], s83\n\t" \
  "s_waitcnt lgkmcnt(5)\n\t" \
  "v_mfma_f32_16x16x32_bf16 v[16:19], a[12:15], v[88:91], v[16:19]\n\t" \
  "ds_read_b128 v[112:115], v1 offset:12288\n\t" \
  "v_mfma_f32_16x16x32_bf16 v[20:23], a[84:87], v[88:91], v[20:23]\n\t" \
  "s_and_b32 s60, s60, 1\n\t" \
  "s_lshl_b32 s60, s60, 13\n\t" \
  "v_mfma_f32_16x16x32_bf16 v[24:27], a[156:159], v[88:91], v[24:27]\n\t" \
  "ds_read_b128 v[116:119], v1 offset:14336\n\t" \
  "v_mfma_f32_16x16x32_bf16 v[28:31], a[228:231], v[88:91], v[28:31]\n\t" \
  "s_add_u32 s83, s83, " TK_SG0 "\n\t" \
  "s_lshl_b32 s83, s83, 21\n\t" \
  "s_waitcnt lgkmcnt(5)\n\t" \
  "v_mfma_f32_16x16x32_bf16 v[16:19], a[16:19], v[96:99], v[16:19]\n\t" \
  "ds_read_b128 v[120:123], v2 offset:12288\n\t" \
  "v_mfma_f32_16x16x32_bf16 v[20:23], a[88:91], v[96:99], v[20:23]\n\t" \
  "s_or_b32 s83, s83, s60\n\t" \
  "s_cmp_lg_u32 s56, 0\n\t" \
  "v_mfma_f32_16x16x32_bf16 v[24:27], a[160:163], v[96:99], v[24:27]\n\t" \
  "ds_read_b128 v[124:127], v2 offset:14336\n\t" \
  "v_mfma_f32_16x16x32_bf16 v[28:31], a[232:235], v[96:99], v[28:31]\n\t" \
  "s_cbranch_scc1 LWARM_%=\n\t" \
  "s_waitcnt vmcnt(0)\n\t" \
  "LWARM_%=:\n\t" \
  "s_barrier\n\t" \
  "s_waitcnt lgkmcnt(5)\n\t" \
  "v_mfma_f32_16x16x32_bf16 v[16:19], a[20:23], v[104:107], v[16:19]\n\t" \
  "ds_read_b128 v[128:131], v1 offset:16384\n\t" \
  "v_mfma_f32_16x16x32_bf16 v[20:23], a[92:95], v[104:107], v[20:23]\n\t" \
  "v_mfma_f32_16x16x32_bf16 v[24:27], a[164:167], v[104:107], v[24:27]\n\t" \
  "ds_read_b128 v[132:135], v1 offset:18432\n\t" \
  "v_mfma_f32_16x16x32_bf16 v[28:31], a[236:239], v[104:107], v[28:31]\n\t" \
  "s_add_i32 s62, s48, s42\n\t" \
  "s_add_i32 s62, s62, 1\n\t" \
  "s_waitcnt lgkmcnt(5)\n\t" \
  "v_mfma_f32_16x16x32_bf16 v[16:19], a[24:27], v[112:115], v[16:19]\n\t" \
  "ds_read_b128 v[136:139], v2 offset:16384\n\t" \
  "v_mfma_f32_16x16x32_bf16 v[20:23], a[96:99], v[112:115], v[20:23]\n\t" \
  "s_mov_b32 s61, s62\n\t" \
  "v_mfma_f32_16x16x32_bf16 v[24:27], a[168:171], v[112:115], v[24:27]\n\t" \
  "ds_read_b128 v[140:143], v2 offset:18432\n\t" \
  "v_mfma_f32_16x16x32_bf16 v[28:31], a[240:243], v[112:115], v[28:31]\n\t" \
  "s_mul_i32 s63, s62, 43\n\t" \
  "s_lshr_b32 s63, s63, 9\n\t" \
  "s_waitcnt lgkmcnt(5)\n\t" \
  "v_mfma_f32_16x16x32_bf16 v[16:19], a[28:31], v[120:123], v[16:19]\n\t" \
  "ds_read_b128 v[144:147], v1 offset:20480\n\t" \
  "v_mfma_f32_16x16x32_bf16 v[20:23], a[100:103], v[120:123], v[20:23]\n\t" \
  "s_mul_i32 s63, s63, " TK_RM "\n\t" \
  "s_sub_u32 s62, s62, s63\n\t" \
  "v_mfma_f32_16x16x32_bf16 v[24:27], a[172:175], v[120:123], v[24:27]\n\t" \
  "ds_read_b128 v[148:151], v1 offset:22528\n\t" \
  "v_mfma_f32_16x16x32_bf16 v[28:31], a[244:247], v[120:123], v[28:31]\n\t" \
  "s_mul_i32 s63, s62, 43\n\t" \
  "s_lshr_b32 s63, s63, 7\n\t" \
  "s_waitcnt lgkmcnt(5)\n\t" \
  "v_mfma_f32_16x16x32_bf16 v[16:19], a[32:35], v[128:131], v[16:19]\n\t" \
  "ds_read_b128 v[152:155], v2 offset:20480\n\t" \
  "v_mfma_f32_16x16x32_bf16 v[20:23], a[104:107], v[128:131], v[20:23]\n\t" \
  "s_mul_i32 s57, s62, 688128\n\t" \
  "s_lshl_b32 s60, s63, 15\n\t" \
  "v_mfma_f32_16x16x32_bf16 v[24:27], a[176:179], v[128:131], v[24:27]\n\t" \
  "ds_read_b128 v[156:159], v2 offset:22528\n\t" \
  "v_mfma_f32_16x16x32_bf16 v[28:31], a[248:251], v[128:131], v[28:31]\n\t" \
  "s_add_u32 s57, s57, s60\n\t" \
  "s_lshr_b32 s60, s88, s63\n\t" \
  "s_waitcnt lgkmcnt(5)\n\t" \
  "v_mfma_f32_16x16x32_bf16 v[16:19], a[36:39], v[136:139], v[16:19]\n\t" \
  "ds_read_b128 v[160:163], v1 offset:24576\n\t" \
  "v_mfma_f32_16x16x32_bf16 v[20:23], a[108:111], v[136:139], v[20:23]\n\t" \
  "s_and_b32 s60, s60, 1\n\t" \
  "s_lshl_b32 s60, s60, 13\n\t" \
  "v_mfma_f32_16x16x32_bf16 v[24:27], a[180:183], v[136:139], v[24:27]\n\t" \
  "ds_read_b128 v[164:167], v1 offset:26624\n\t" \
  "v_mfma_f32_16x16x32_bf16 v[28:31], a[252:255], v[136:139], v[28:31]\n\t" \
  "s_add_u32 s57, s57, s60\n\t" \
  "s_add_u32 s57, s57, 656896\n\t" \
  "s_waitcnt lgkmcnt(5)\n\t" \
  "v_mfma_f32_16x16x32_bf16 v[16:19], a[40:43], v[144:147], v[16:19]\n\t" \
  "ds_read_b128 v[168:171], v2 offset:24576\n\t" \
  "v_mfma_f32_16x16x32_bf16 v[20:23], a[112:115], v[144:147], v[20:23]\n\t" \
  "s_cmp_lt_i32 s61, 1\n\t" \
  "s_cselect_b32 s57, 0xf0000000, s57\n\t" \
  "v_mfma_f32_16x16x32_bf16 v[24:27], a[184:187], v[144:147], v[24:27]\n\t" \
  "ds_read_b128 v[172:175], v2 offset:26624\n\t" \
  "v_mfma_f32_16x16x32_bf16 v[28:31], v[224:227], v[144:147], v[28:31]\n\t" \
  "s_cmp_lg_u32 s40, 0\n\t" \
  "s_cselect_b32 s57, 0xf0000000, s57\n\t" \
  "s_waitcnt lgkmcnt(5)\n\t" \
  "v_mfma_f32_16x16x32_bf16 v[16:19], a[44:47], v[152:155], v[16:19]\n\t" \
  "ds_read_b128 v[176:179], v1 offset:28672\n\t" \
  "v_mfma_f32_16x16x32_bf16 v[20:23], a[116:119], v[152:155], v[20:23]\n\t" \
  "s_add_u32 s80, s48, " TK_TRN "\n\t" \
  "s_lshr_b64 s[60:61], s[90:91], s80\n\t" \
  "s_and_b32 s60, s60, 1\n\t" \
  "v_mfma_f32_16x16x32_bf16 v[24:27], a[188:191], v[152:155], v[24:27]\n\t" \
  "ds_read_b128 v[180:183], v1 offset:30720\n\t" \
  "v_mfma_f32_16x16x32_bf16 v[28:31], v[228:231], v[152:155], v[28:31]\n\t" \
  "s_lshl_b32 s60, s60, 13\n\t" \
  "s_add_u32 s80, s80, " TK_SG0 "\n\t" \
  "s_lshl_b32 s80, s80, 21\n\t" \
  "s_waitcnt lgkmcnt(5)\n\t" \
  "v_mfma_f32_16x16x32_bf16 v[16:19], a[48:51], v[160:163], v[16:19]\n\t" \
  "ds_read_b128 v[184:187], v2 offset:28672\n\t" \
  "v_mfma_f32_16x16x32_bf16 v[20:23], a[120:123], v[160:163], v[20:23]\n\t" \
  "s_or_b32 s80, s80, s60\n\t" \
  "s_add_u32 s62, s83, s39\n\t" \
  "s_add_u32 s80, s80, s39\n\t" \
  TK_PQ0 \
  "v_mfma_f32_16x16x32_bf16 v[24:27], a[192:195], v[160:163], v[24:27]\n\t" \
  "ds_read_b128 v[188:191], v2 offset:30720\n\t" \
  "v_mfma_f32_16x16x32_bf16 v[28:31], v[232:235], v[160:163], v[28:31]\n\t" \
  "s_add_u32 s80, s80, 2048\n\t" \
  "s_waitcnt lgkmcnt(5)\n\t" \
  "v_mfma_f32_16x16x32_bf16 v[16:19], a[52:55], v[168:171], v[16:19]\n\t" \
  "ds_read_b128 v[192:195], v1 offset:32768\n\t" \
  "v_mfma_f32_16x16x32_bf16 v[20:23], a[124:127], v[168:171], v[20:23]\n\t" \
  "v_mfma_f32_16x16x32_bf16 v[24:27], a[196:199], v[168:171], v[24:27]\n\t" \
  "ds_read_b128 v[196:199], v1 offset:34816\n\t" \
  "v_mfma_f32_16x16x32_bf16 v[28:31], v[236:239], v[168:171], v[28:31]\n\t" \
  "s_cmp_lt_u32 s48, " TK_TRM "\n\t" \
  "s_cselect_b32 s80, s62, s80\n\t" \
  "s_waitcnt lgkmcnt(5)\n\t" \
  "v_mfma_f32_16x16x32_bf16 v[16:19], a[56:59], v[176:179], v[16:19]\n\t" \
  "ds_read_b128 v[200:203], v2 offset:32768\n\t" \
  "v_mfma_f32_16x16x32_bf16 v[20:23], a[128:131], v[176:179], v[20:23]\n\t" \
  "v_add_u32 v7, s62, v3\n\t" \
  "s_mov_b32 exec_lo, 0\n\t" \
  "v_mov_b32 v7, s80\n\t" \
  "s_mov_b32 exec_lo, -1\n\t" \
  "v_mfma_f32_16x16x32_bf16 v[24:27], a[200:203], v[176:179], v[24:27]\n\t" \
  "ds_read_b128 v[204:207], v2 offset:34816\n\t" \
  "v_mfma_f32_16x16x32_bf16 v[28:31], v[240:243], v[176:179], v[28:31]\n\t" \
  "s_waitcnt lgkmcnt(5)\n\t" \
  "v_mfma_f32_16x16x32_bf16 v[16:19], a[60:63], v[184:187], v[16:19]\n\t" \
  "v_mfma_f32_16x16x32_bf16 v[20:23], a[132:135], v[184:187], v[20:23]\n\t" \
  "buffer_load_dword v4, v7, %[X], 0 offen sc1\n\t" \
  "v_mfma_f32_16x16x32_bf16 v[24:27], a[204:207], v[184:187], v[24:27]\n\t" \
  "v_mfma_f32_16x16x32_bf16 v[28:31], v[244:247], v[184:187], v[28:31]\n\t" \
  "s_waitcnt lgkmcnt(3)\n\t" \
  "v_mfma_f32_16x16x32_bf16 v[16:19], a[64:67], v[192:195], v[16:19]\n\t" \
  "v_xor_b32 v1, 0x10000, v1\n\t" \
  "v_xor_b32 v2, 0x10000, v2\n\t" \
  "v_mfma_f32_16x16x32_bf16 v[20:23], a[136:139], v[192:195], v[20:23]\n\t" \
  "v_mfma_f32_16x16x32_bf16 v[24:27], a[208:211], v[192:195], v[24:27]\n\t" \
  "v_mfma_f32_16x16x32_bf16 v[28:31], v[248:251], v[192:195], v[28:31]\n\t" \
  "s_waitcnt lgkmcnt(1)\n\t" \
  "v_mfma_f32_16x16x32_bf16 v[16:19], a[68:71], v[200:203], v[16:19]\n\t" \
  "v_mfma_f32_16x16x32_bf16 v[20:23], a[140:143], v[200:203], v[20:23]\n\t" \
  "v_mfma_f32_16x16x32_bf16 v[24:27], a[212:215], v[200:203], v[24:27]\n\t" \
  "v_mfma_f32_16x16x32_bf16 v[28:31], v[252:255], v[200:203], v[28:31]\n\t" \
  "v_mfma_f32_16x16x32_bf16 v[32:35], a[0:3], v[68:71], 0\n\t" \
  "v_cvt_pk_bf16_f32 v48, v16, v17\n\t" \
  "v_cvt_pk_bf16_f32 v49, v18, v19\n\t" \
  "v_mfma_f32_16x16x32_bf16 v[36:39], a[72:75], v[68:71], 0\n\t" \
  "v_cvt_pk_bf16_f32 v50, v20, v21\n\t" \
  "v_cvt_pk_bf16_f32 v51, v22, v23\n\t" \
  "v_mfma_f32_16x16x32_bf16 v[32:35], a[4:7], v[76:79], v[32:35]\n\t" \
  "v_cvt_pk_bf16_f32 v52, v24, v25\n\t" \
  "v_cvt_pk_bf16_f32 v53, v26, v27\n\t" \
  "v_mfma_f32_16x16x32_bf16 v[36:39], a[76:79], v[76:79], v[36:39]\n\t" \
  "v_cvt_pk_bf16_f32 v54, v28, v29\n\t" \
  "v_cvt_pk_bf16_f32 v55, v30, v31\n\t" \
  "v_mfma_f32_16x16x32_bf16 v[32:35], a[8:11], v[84:87], v[32:35]\n\t" \
  "buffer_store_dwordx4 v[48:51], v0, %[X], s50 offen offset:0 sc1\n\t" \
  "v_mfma_f32_16x16x32_bf16 v[36:39], a[80:83], v[84:87], v[36:39]\n\t" \
  "v_mfma_f32_16x16x32_bf16 v[32:35], a[12:15], v[92:95], v[32:35]\n\t" \
  "v_mfma_f32_16x16x32_bf16 v[36:39], a[84:87], v[92:95], v[36:39]\n\t" \
  "v_mfma_f32_16x16x32_bf16 v[32:35], a[16:19], v[100:103], v[32:35]\n\t" \
  "v_mfma_f32_16x16x32_bf16 v[36:39], a[88:91], v[100:103], v[36:39]\n\t" \
  "v_mfma_f32_16x16x32_bf16 v[32:35], a[20:23], v[108:111], v[32:35]\n\t" \
  "v_mfma_f32_16x16x32_bf16 v[36:39], a[92:95], v[108:111], v[36:39]\n\t" \
  "v_mfma_f32_16x16x32_bf16 v[32:35], a[24:27], v[116:119], v[32:35]\n\t" \
  "v_mfma_f32_16x16x32_bf16 v[36:39], a[96:99], v[116:119], v[36:39]\n\t" \
  "v_mfma_f32_16x16x32_bf16 v[32:35], a[28:31], v[124:127], v[32:35]\n\t" \
  "v_mfma_f32_16x16x32_bf16 v[36:39], a[100:103], v[124:127], v[36:39]\n\t" \
  "s_waitcnt vmcnt(1)\n\t" \
  "v_cmp_eq_u32 vcc, 0, v4\n\t" \
  "v_mfma_f32_16x16x32_bf16 v[32:35], a[32:35], v[132:135], v[32:35]\n\t" \
  "v_mfma_f32_16x16x32_bf16 v[36:39], a[104:107], v[132:135], v[36:39]\n\t" \
  "s_cmp_eq_u64 vcc, 0\n\t" \
  "s_cselect_b32 s60, 1, 0\n\t" \
  "v_mov_b32 v4, s60\n\t" \
  "v_mfma_f32_16x16x32_bf16 v[32:35], a[36:39], v[140:143], v[32:35]\n\t" \
  "ds_write_b32 v5, v4\n\t" \
  "v_mfma_f32_16x16x32_bf16 v[36:39], a[108:111], v[140:143], v[36:39]\n\t" \
  "v_mfma_f32_16x16x32_bf16 v[32:35], a[40:43], v[148:151], v[32:35]\n\t" \
  "v_mfma_f32_16x16x32_bf16 v[36:39], a[112:115], v[148:151], v[36:39]\n\t" \
  "buffer_store_dwordx4 v[52:55], v0, %[X], s50 offen offset:1024 sc1\n\t" \
  "v_mfma_f32_16x16x32_bf16 v[32:35], a[44:47], v[156:159], v[32:35]\n\t" \
  "v_mfma_f32_16x16x32_bf16 v[36:39], a[116:119], v[156:159], v[36:39]\n\t" \
  "s_waitcnt lgkmcnt(0)\n\t" \
  "s_barrier\n\t" \
  "v_mfma_f32_16x16x32_bf16 v[32:35], a[48:51], v[164:167], v[32:35]\n\t" \
  "ds_read_b32 v4, v6 offset:40959\n\t" \
  "v_mfma_f32_16x16x32_bf16 v[36:39], a[120:123], v[164:167], v[36:39]\n\t" \
  "v_mfma_f32_16x16x32_bf16 v[32:35], a[52:55], v[172:175], v[32:35]\n\t" \
  "v_mfma_f32_16x16x32_bf16 v[36:39], a[124:127], v[172:175], v[36:39]\n\t" \
  "v_mfma_f32_16x16x32_bf16 v[32:35], a[56:59], v[180:183], v[32:35]\n\t" \
  "v_mfma_f32_16x16x32_bf16 v[36:39], a[128:131], v[180:183], v[36:39]\n\t" \
  "s_waitcnt lgkmcnt(0)\n\t" \
  "v_readfirstlane_b32 s60, v4\n\t" \
  "s_add_u32 s55, s83, s36\n\t" \
  "v_mfma_f32_16x16x32_bf16 v[32:35], a[60:63], v[188:191], v[32:35]\n\t" \
  "s_cmp_eq_u32 s48, 63\n\t" \
  "s_cselect_b32 s56, 0, s60\n\t" \
  "v_mfma_f32_16x16x32_bf16 v[36:39], a[132:135], v[188:191], v[36:39]\n\t" \
  "s_cmp_eq_u32 s56, 0\n\t" \
  "s_cselect_b32 s55, 0xf0000000, s55\n\t" \
  "v_mfma_f32_16x16x32_bf16 v[32:35], a[64:67], v[196:199], v[32:35]\n\t" \
  "v_mfma_f32_16x16x32_bf16 v[36:39], a[136:139], v[196:199], v[36:39]\n\t" \
  "v_mfma_f32_16x16x32_bf16 v[32:35], a[68:71], v[204:207], v[32:35]\n\t" \
  "s_add_u32 m0, s46, 0\n\t" \
  "s_add_u32 s47, s55, 0\n\t" \
  "buffer_load_dwordx4 v0, %[X], s47 offen sc1 lds\n\t" \
  "v_mfma_f32_16x16x32_bf16 v[36:39], a[140:143], v[204:207], v[36:39]\n\t" \
  "v_mfma_f32_16x16x32_bf16 v[40:43], a[144:147], v[68:71], 0\n\t" \
  "v_mfma_f32_16x16x32_bf16 v[44:47], a[216:219], v[68:71], 0\n\t" \
  "v_mfma_f32_16x16x32_bf16 v[40:43], a[148:151], v[76:79], v[40:43]\n\t" \
  "v_cvt_pk_bf16_f32 v56, v32, v33\n\t" \
  "v_cvt_pk_bf16_f32 v57, v34, v35\n\t" \
  "s_add_u32 m0, s46, 4096\n\t" \
  "s_add_u32 s47, s55, 4096\n\t" \
  "buffer_load_dwordx4 v0, %[X], s47 offen sc1 lds\n\t" \
  "v_mfma_f32_16x16x32_bf16 v[44:47], a[220:223], v[76:79], v[44:47]\n\t" \
  "v_cvt_pk_bf16_f32 v58, v36, v37\n\t" \
  "v_cvt_pk_bf16_f32 v59, v38, v39\n\t" \
  "v_mfma_f32_16x16x32_bf16 v[40:43], a[152:155], v[84:87], v[40:43]\n\t" \
  "v_mfma_f32_16x16x32_bf16 v[44:47], a[224:227], v[84:87], v[44:47]\n\t" \
  "buffer_store_dwordx4 v[56:59], v0, %[X], s51 offen offset:0 sc1\n\t" \
  "v_mfma_f32_16x16x32_bf16 v[40:43], a[156:159], v[92:95], v[40:43]\n\t" \
  "s_add_u32 m0, s46, 8192\n\t" \
  "s_add_u32 s47, s55, 16384\n\t" \
  "buffer_load_dwordx4 v0, %[X], s47 offen sc1 lds\n\t" \
  "v_mfma_f32_16x16x32_bf16 v[44:47], a[228:231], v[92:95], v[44:47]\n\t" \
  "v_mfma_f32_16x16x32_bf16 v[40:43], a[160:163], v[100:103], v[40:43]\n\t" \
  "v_mfma_f32_16x16x32_bf16 v[44:47], a[232:235], v[100:103], v[44:47]\n\t" \
  "v_mfma_f32_16x16x32_bf16 v[40:43], a[164:167], v[108:111], v[40:43]\n\t" \
  "s_add_u32 m0, s46, 12288\n\t" \
  "s_add_u32 s47, s55, 20480\n\t" \
  "buffer_load_dwordx4 v0, %[X], s47 offen sc1 lds\n\t" \
  "v_mfma_f32_16x16x32_bf16 v[44:47], a[236:239], v[108:111], v[44:47]\n\t" \
  "v_mfma_f32_16x16x32_bf16 v[40:43], a[168:171], v[116:119], v[40:43]\n\t" \
  "v_mfma_f32_16x16x32_bf16 v[44:47], a[240:243], v[116:119], v[44:47]\n\t" \
  "v_mfma_f32_16x16x32_bf16 v[40:43], a[172:175], v[124:127], v[40:43]\n\t" \
  "s_add_u32 m0, s46, 16384\n\t" \
  "s_add_u32 s47, s55, 32768\n\t" \
  "buffer_load_dwordx4 v0, %[X], s47 offen sc1 lds\n\t" \
  "v_mfma_f32_16x16x32_bf16 v[44:47], a[244:247], v[124:127], v[44:47]\n\t" \
  "v_mfma_f32_16x16x32_bf16 v[40:43], a[176:179], v[132:135], v[40:43]\n\t" \
  "v_mfma_f32_16x16x32_bf16 v[44:47], a[248:251], v[132:135], v[44:47]\n\t" \
  "v_mfma_f32_16x16x32_bf16 v[40:43], a[180:183], v[140:143], v[40:43]\n\t" \
  "s_add_u32 m0, s46, 20480\n\t" \
  "s_add_u32 s47, s55, 36864\n\t" \
  "buffer_load_dwordx4 v0, %[X], s47 offen sc1 lds\n\t" \
  "v_mfma_f32_16x16x32_bf16 v[44:47], a[252:255], v[140:143], v[44:47]\n\t" \
  "v_mfma_f32_16x16x32_bf16 v[40:43], a[184:187], v[148:151], v[40:43]\n\t" \
  "v_mfma_f32_16x16x32_bf16 v[44:47], v[224:227], v[148:151], v[44:47]\n\t" \
  "v_mfma_f32_16x16x32_bf16 v[40:43], a[188:191], v[156:159], v[40:43]\n\t" \
  "s_add_u32 m0, s46, 24576\n\t" \
  "s_add_u32 s47, s55, 49152\n\t" \
  "buffer_load_dwordx4 v0, %[X], s47 offen sc1 lds\n\t" \
  "v_mfma_f32_16x16x32_bf16 v[44:47], v[228:231], v[156:159], v[44:47]\n\t" \
  "v_mfma_f32_16x16x32_bf16 v[40:43], a[192:195], v[164:167], v[40:43]\n\t" \
  "v_mfma_f32_16x16x32_bf16 v[44:47], v[232:235], v[164:167], v[44:47]\n\t" \
  "v_mfma_f32_16x16x32_bf16 v[40:43], a[196:199], v[172:175], v[40:43]\n\t" \
  "s_add_u32 m0, s46, 28672\n\t" \
  "s_add_u32 s47, s55, 53248\n\t" \
  "buffer_load_dwordx4 v0, %[X], s47 offen sc1 lds\n\t" \
  "v_mfma_f32_16x16x32_bf16 v[44:47], v[236:239], v[172:175], v[44:47]\n\t" \
  "v_mfma_f32_16x16x32_bf16 v[40:43], a[200:203], v[180:183], v[40:43]\n\t" \
  "v_mfma_f32_16x16x32_bf16 v[44:47], v[240:243], v[180:183], v[44:47]\n\t" \
  "v_mfma_f32_16x16x32_bf16 v[40:43], a[204:207], v[188:191], v[40:43]\n\t" \
  "s_add_u32 m0, s46, 32768\n\t" \
  "s_add_u32 s47, s55, 65536\n\t" \
  "buffer_load_dwordx4 v0, %[X], s47 offen sc1 lds\n\t" \
  "v_mfma_f32_16x16x32_bf16 v[44:47], v[244:247], v[188:191], v[44:47]\n\t" \
  "v_mfma_f32_16x16x32_bf16 v[40:43], a[208:211], v[196:199], v[40:43]\n\t" \
  "v_mfma_f32_16x16x32_bf16 v[44:47], v[248:251], v[196:199], v[44:47]\n\t" \
  "buffer_load_dword v8, off, %[X], s57 sc1\n\t" \
  "v_mfma_f32_16x16x32_bf16 v[40:43], a[212:215], v[204:207], v[40:43]\n\t" \
  "v_mfma_f32_16x16x32_bf16 v[44:47], v[252:255], v[204:207], v[44:47]\n\t" \
  "s_nop 4\n\t" \
  "v_cvt_pk_bf16_f32 v60, v40, v41\n\t" \
  "v_cvt_pk_bf16_f32 v61, v42, v43\n\t" \
  "s_nop 1\n\t" \
  "v_cvt_pk_bf16_f32 v62, v44, v45\n\t" \
  "v_cvt_pk_bf16_f32 v63, v46, v47\n\t" \
  "buffer_store_dwordx4 v[60:63], v0, %[X], s51 offen offset:1024 sc1\n\t" \
  TK_PQ2 \
  "s_add_u32 s49, s82, s38\n\t" \
  "s_cmp_eq_u32 s48, 63\n\t" \
  "s_cbranch_scc1 LEND_%=\n\t" \
  "s_add_u32 s48, s48, 1\n\t" \
  "s_cmp_eq_u32 s56, 0\n\t" \
  "s_cbranch_scc1 LCOLD_%=\n\t" \
  "s_waitcnt vmcnt(0)\n\t" \
  "s_mov_b64 exec, 1\n\t" \
  "buffer_store_dword v6, off, %[X], s49 sc1\n\t" \
  "s_mov_b64 exec, -1\n\t" \
  "s_mov_b32 s49, 0xf0000000\n\t" \
  "s_cmp_lt_i32 s57, 0\n\t" \
  "s_cbranch_scc1 LTOP_%=\n\t" \
  "s_movk_i32 s63, 4000\n\t" \
  "s_branch LGW_%=u\n\t" \
  "LGW_%=:\n\t" \
  "s_sleep " TK_PSG "\n\t" \
  "buffer_load_dword v8, off, %[X], s57 sc1\n\t" \
  "s_waitcnt vmcnt(0)\n\t" \
  "LGW_%=u:\n\t" \
  "s_memrealtime s[52:53]\n\t" \
  "v_readfirstlane_b32 s60, v8\n\t" \
  "s_waitcnt lgkmcnt(0)\n\t" \
  "s_sub_u32 s63, s63, 1\n\t" \
  "s_cbranch_scc1 LTOP_%=\n\t" \
  "s_cmp_eq_u32 s60, -1\n\t" \
  "s_cbranch_scc1 LGW_%=\n\t" \
  "s_not_b32 s60, s60\n\t" \
  "s_lshl_b32 s61, s52, 8\n\t" \
  "s_sub_u32 s60, s60, s61\n\t" \
  "s_ashr_i32 s60, s60, 8\n\t" \
  "s_sub_i32 s60, s60, " TK_QXW "\n\t" \
  "s_min_i32 s60, s60, 400\n\t" \
  "s_add_u32 s54, s52, s60\n\t" \
  "LGW_%=w:\n\t" \
  "s_sleep 1\n\t" \
  "s_memrealtime s[52:53]\n\t" \
  "s_waitcnt lgkmcnt(0)\n\t" \
  "s_sub_i32 s53, s52, s54\n\t" \
  "s_cmp_lt_i32 s53, 0\n\t" \
  "s_cbranch_scc1 LGW_%=w\n\t" \
  "s_branch LTOP_%=\n\t" \
  "LEND_%=:\n\t" \
  "s_waitcnt vmcnt(0)\n\t" \
  "s_mov_b64 exec, 1\n\t" \
  "buffer_store_dword v6, off, %[X], s49 sc1\n\t" \
  "s_mov_b64 exec, -1\n\t" \
  "s_mov_b32 s49, 0xf0000000\n\t" \
  TK_PQ0 \
  ""
#define QV_ASM \
  "v_lshrrev_b32 v16, 7, v0\n\t" \
  "v_lshlrev_b32 v16, 13, v16\n\t" \
  "v_lshrrev_b32 v19, 2, v0\n\t" \
  "v_and_b32 v19, 64, v19\n\t" \
  "v_xor_b32 v19, v19, v0\n\t" \
  "v_and_b32 v19, 0x70, v19\n\t" \
  "v_add_u32 v16, v16, v19\n\t" \
  "s_mul_i32 s63, s40, 40960\n\t" \
  "v_and_b32 v19, 0x300, v0\n\t" \
  "v_lshlrev_b32 v17, 2, v19\n\t" \
  "v_and_b32 v19, 0xc0, v0\n\t" \
  "v_lshl_add_u32 v17, v19, 1, v17\n\t" \
  "v_and_b32 v19, 0x30, v0\n\t" \
  "v_lshrrev_b32 v19, 1, v19\n\t" \
  "v_add3_u32 v17, v17, v19, s63\n\t" \
  "v_lshrrev_b32 v19, 1, v0\n\t" \
  "v_and_b32 v19, 64, v19\n\t" \
  "v_add_u32 v17, v17, v19\n\t" \
  "v_xor_b32 v18, 64, v17\n\t" \
  "s_add_u32 m0, s63, 0\n\t" \
  "s_mov_b32 s60, 0\n\t" \
  "buffer_load_dwordx4 v16, %[W], s60 offen lds\n\t" \
  "s_add_u32 m0, s63, 1024\n\t" \
  "s_mov_b32 s60, 65536\n\t" \
  "buffer_load_dwordx4 v16, %[W], s60 offen lds\n\t" \
  "s_add_u32 m0, s63, 2048\n\t" \
  "s_mov_b32 s60, 131072\n\t" \
  "buffer_load_dwordx4 v16, %[W], s60 offen lds\n\t" \
  "s_add_u32 m0, s63, 3072\n\t" \
  "s_mov_b32 s60, 196608\n\t" \
  "buffer_load_dwordx4 v16, %[W], s60 offen lds\n\t" \
  "s_add_u32 m0, s63, 4096\n\t" \
  "s_mov_b32 s60, 262144\n\t" \
  "buffer_load_dwordx4 v16, %[W], s60 offen lds\n\t" \
  "s_add_u32 m0, s63, 5120\n\t" \
  "s_mov_b32 s60, 327680\n\t" \
  "buffer_load_dwordx4 v16, %[W], s60 offen lds\n\t" \
  "s_add_u32 m0, s63, 6144\n\t" \
  "s_mov_b32 s60, 393216\n\t" \
  "buffer_load_dwordx4 v16, %[W], s60 offen lds\n\t" \
  "s_add_u32 m0, s63, 7168\n\t" \
  "s_mov_b32 s60, 458752\n\t" \
  "buffer_load_dwordx4 v16, %[W], s60 offen lds\n\t" \
  "s_add_u32 m0, s63, 8192\n\t" \
  "s_mov_b32 s60, 524288\n\t" \
  "buffer_load_dwordx4 v16, %[W], s60 offen lds\n\t" \
  "s_add_u32 m0, s63, 9216\n\t" \
  "s_mov_b32 s60, 589824\n\t" \
  "buffer_load_dwordx4 v16, %[W], s60 offen lds\n\t" \
  "s_add_u32 m0, s63, 10240\n\t" \
  "s_mov_b32 s60, 655360\n\t" \
  "buffer_load_dwordx4 v16, %[W], s60 offen lds\n\t" \
  "s_add_u32 m0, s63, 11264\n\t" \
  "s_mov_b32 s60, 720896\n\t" \
  "buffer_load_dwordx4 v16, %[W], s60 offen lds\n\t" \
  "s_add_u32 m0, s63, 12288\n\t" \
  "s_mov_b32 s60, 786432\n\t" \
  "buffer_load_dwordx4 v16, %[W], s60 offen lds\n\t" \
  "s_add_u32 m0, s63, 13312\n\t" \
  "s_mov_b32 s60, 851968\n\t" \
  "buffer_load_dwordx4 v16, %[W], s60 offen lds\n\t" \
  "s_add_u32 m0, s63, 14336\n\t" \
  "s_mov_b32 s60, 917504\n\t" \
  "buffer_load_dwordx4 v16, %[W], s60 offen lds\n\t" \
  "s_add_u32 m0, s63, 15360\n\t" \
  "s_mov_b32 s60, 983040\n\t" \
  "buffer_load_dwordx4 v16, %[W], s60 offen lds\n\t" \
  "s_add_u32 m0, s63, 16384\n\t" \
  "s_mov_b32 s60, 1048576\n\t" \
  "buffer_load_dwordx4 v16, %[W], s60 offen lds\n\t" \
  "s_add_u32 m0, s63, 17408\n\t" \
  "s_mov_b32 s60, 1114112\n\t" \
  "buffer_load_dwordx4 v16, %[W], s60 offen lds\n\t" \
  "s_add_u32 m0, s63, 18432\n\t" \
  "s_mov_b32 s60, 1179648\n\t" \
  "buffer_load_dwordx4 v16, %[W], s60 offen lds\n\t" \
  "s_add_u32 m0, s63, 19456\n\t" \
  "s_mov_b32 s60, 1245184\n\t" \
  "buffer_load_dwordx4 v16, %[W], s60 offen lds\n\t" \
  "s_add_u32 m0, s63, 20480\n\t" \
  "s_mov_b32 s60, 1310720\n\t" \
  "buffer_load_dwordx4 v16, %[W], s60 offen lds\n\t" \
  "s_add_u32 m0, s63, 21504\n\t" \
  "s_mov_b32 s60, 1376256\n\t" \
  "buffer_load_dwordx4 v16, %[W], s60 offen lds\n\t" \
  "s_add_u32 m0, s63, 22528\n\t" \
  "s_mov_b32 s60, 1441792\n\t" \
  "buffer_load_dwordx4 v16, %[W], s60 offen lds\n\t" \
  "s_add_u32 m0, s63, 23552\n\t" \
  "s_mov_b32 s60, 1507328\n\t" \
  "buffer_load_dwordx4 v16, %[W], s60 offen lds\n\t" \
  "s_add_u32 m0, s63, 24576\n\t" \
  "s_mov_b32 s60, 1572864\n\t" \
  "buffer_load_dwordx4 v16, %[W], s60 offen lds\n\t" \
  "s_add_u32 m0, s63, 25600\n\t" \
  "s_mov_b32 s60, 1638400\n\t" \
  "buffer_load_dwordx4 v16, %[W], s60 offen lds\n\t" \
  "s_add_u32 m0, s63, 26624\n\t" \
  "s_mov_b32 s60, 1703936\n\t" \
  "buffer_load_dwordx4 v16, %[W], s60 offen lds\n\t" \
  "s_add_u32 m0, s63, 27648\n\t" \
  "s_mov_b32 s60, 1769472\n\t" \
  "buffer_load_dwordx4 v16, %[W], s60 offen lds\n\t" \
  "s_add_u32 m0, s63, 28672\n\t" \
  "s_mov_b32 s60, 1835008\n\t" \
  "buffer_load_dwordx4 v16, %[W], s60 offen lds\n\t" \
  "s_add_u32 m0, s63, 29696\n\t" \
  "s_mov_b32 s60, 1900544\n\t" \
  "buffer_load_dwordx4 v16, %[W], s60 offen lds\n\t" \
  "s_add_u32 m0, s63, 30720\n\t" \
  "s_mov_b32 s60, 1966080\n\t" \
  "buffer_load_dwordx4 v16, %[W], s60 offen lds\n\t" \
  "s_add_u32 m0, s63, 31744\n\t" \
  "s_mov_b32 s60, 2031616\n\t" \
  "buffer_load_dwordx4 v16, %[W], s60 offen lds\n\t" \
  "s_add_u32 m0, s63, 32768\n\t" \
  "s_mov_b32 s60, 2097152\n\t" \
  "buffer_load_dwordx4 v16, %[W], s60 offen lds\n\t" \
  "s_add_u32 m0, s63, 33792\n\t" \
  "s_mov_b32 s60, 2162688\n\t" \
  "buffer_load_dwordx4 v16, %[W], s60 offen lds\n\t" \
  "s_add_u32 m0, s63, 34816\n\t" \
  "s_mov_b32 s60, 2228224\n\t" \
  "buffer_load_dwordx4 v16, %[W], s60 offen lds\n\t" \
  "s_add_u32 m0, s63, 35840\n\t" \
  "s_mov_b32 s60, 2293760\n\t" \
  "buffer_load_dwordx4 v16, %[W], s60 offen lds\n\t" \
  "s_add_u32 m0, s63, 36864\n\t" \
  "s_mov_b32 s60, 2359296\n\t" \
  "buffer_load_dwordx4 v16, %[W], s60 offen lds\n\t" \
  "s_add_u32 m0, s63, 37888\n\t" \
  "s_mov_b32 s60, 2424832\n\t" \
  "buffer_load_dwordx4 v16, %[W], s60 offen lds\n\t" \
  "s_add_u32 m0, s63, 38912\n\t" \
  "s_mov_b32 s60, 2490368\n\t" \
  "buffer_load_dwordx4 v16, %[W], s60 offen lds\n\t" \
  "s_add_u32 m0, s63, 39936\n\t" \
  "s_mov_b32 s60, 2555904\n\t" \
  "buffer_load_dwordx4 v16, %[W], s60 offen lds\n\t" \
  "s_waitcnt vmcnt(32)\n\t" \
  "ds_read_b64_tr_b16 a[0:1], v17 offset:0\n\t" \
  "ds_read_b64_tr_b16 a[2:3], v17 offset:512\n\t" \
  "ds_read_b64_tr_b16 a[4:5], v17 offset:4096\n\t" \
  "ds_read_b64_tr_b16 a[6:7], v17 offset:4608\n\t" \
  "ds_read_b64_tr_b16 a[72:73], v17 offset:32\n\t" \
  "ds_read_b64_tr_b16 a[74:75], v17 offset:544\n\t" \
  "ds_read_b64_tr_b16 a[76:77], v17 offset:4128\n\t" \
  "ds_read_b64_tr_b16 a[78:79], v17 offset:4640\n\t" \
  "ds_read_b64_tr_b16 a[144:145], v18 offset:0\n\t" \
  "ds_read_b64_tr_b16 a[146:147], v18 offset:512\n\t" \
  "ds_read_b64_tr_b16 a[148:149], v18 offset:4096\n\t" \
  "ds_read_b64_tr_b16 a[150:151], v18 offset:4608\n\t" \
  "ds_read_b64_tr_b16 a[216:217], v18 offset:32\n\t" \
  "ds_read_b64_tr_b16 a[218:219], v18 offset:544\n\t" \
  "ds_read_b64_tr_b16 a[220:221], v18 offset:4128\n\t" \
  "ds_read_b64_tr_b16 a[222:223], v18 offset:4640\n\t" \
  "s_waitcnt lgkmcnt(0)\n\t" \
  "s_add_u32 m0, s63, 0\n\t" \
  "s_mov_b32 s60, 2621440\n\t" \
  "buffer_load_dwordx4 v16, %[W], s60 offen lds\n\t" \
  "s_add_u32 m0, s63, 1024\n\t" \
  "s_mov_b32 s60, 2686976\n\t" \
  "buffer_load_dwordx4 v16, %[W], s60 offen lds\n\t" \
  "s_add_u32 m0, s63, 2048\n\t" \
  "s_mov_b32 s60, 2752512\n\t" \
  "buffer_load_dwordx4 v16, %[W], s60 offen lds\n\t" \
  "s_add_u32 m0, s63, 3072\n\t" \
  "s_mov_b32 s60, 2818048\n\t" \
  "buffer_load_dwordx4 v16, %[W], s60 offen lds\n\t" \
  "s_add_u32 m0, s63, 4096\n\t" \
  "s_mov_b32 s60, 2883584\n\t" \
  "buffer_load_dwordx4 v16, %[W], s60 offen lds\n\t" \
  "s_add_u32 m0, s63, 5120\n\t" \
  "s_mov_b32 s60, 2949120\n\t" \
  "buffer_load_dwordx4 v16, %[W], s60 offen lds\n\t" \
  "s_add_u32 m0, s63, 6144\n\t" \
  "s_mov_b32 s60, 3014656\n\t" \
  "buffer_load_dwordx4 v16, %[W], s60 offen lds\n\t" \
  "s_add_u32 m0, s63, 7168\n\t" \
  "s_mov_b32 s60, 3080192\n\t" \
  "buffer_load_dwordx4 v16, %[W], s60 offen lds\n\t" \
  "s_waitcnt vmcnt(32)\n\t" \
  "ds_read_b64_tr_b16 a[8:9], v17 offset:8192\n\t" \
  "ds_read_b64_tr_b16 a[10:11], v17 offset:8704\n\t" \
  "ds_read_b64_tr_b16 a[12:13], v17 offset:12288\n\t" \
  "ds_read_b64_tr_b16 a[14:15], v17 offset:12800\n\t" \
  "ds_read_b64_tr_b16 a[80:81], v17 offset:8224\n\t" \
  "ds_read_b64_tr_b16 a[82:83], v17 offset:8736\n\t" \
  "ds_read_b64_tr_b16 a[84:85], v17 offset:12320\n\t" \
  "ds_read_b64_tr_b16 a[86:87], v17 offset:12832\n\t" \
  "ds_read_b64_tr_b16 a[152:153], v18 offset:8192\n\t" \
  "ds_read_b64_tr_b16 a[154:155], v18 offset:8704\n\t" \
  "ds_read_b64_tr_b16 a[156:157], v18 offset:12288\n\t" \
  "ds_read_b64_tr_b16 a[158:159], v18 offset:12800\n\t" \
  "ds_read_b64_tr_b16 a[224:225], v18 offset:8224\n\t" \
  "ds_read_b64_tr_b16 a[226:227], v18 offset:8736\n\t" \
  "ds_read_b64_tr_b16 a[228:229], v18 offset:12320\n\t" \
  "ds_read_b64_tr_b16 a[230:231], v18 offset:12832\n\t" \
  "s_waitcnt lgkmcnt(0)\n\t" \
  "s_add_u32 m0, s63, 8192\n\t" \
  "s_mov_b32 s60, 3145728\n\t" \
  "buffer_load_dwordx4 v16, %[W], s60 offen lds\n\t" \
  "s_add_u32 m0, s63, 9216\n\t" \
  "s_mov_b32 s60, 3211264\n\t" \
  "buffer_load_dwordx4 v16, %[W], s60 offen lds\n\t" \
  "s_add_u32 m0, s63, 10240\n\t" \
  "s_mov_b32 s60, 3276800\n\t" \
  "buffer_load_dwordx4 v16, %[W], s60 offen lds\n\t" \
  "s_add_u32 m0, s63, 11264\n\t" \
  "s_mov_b32 s60, 3342336\n\t" \
  "buffer_load_dwordx4 v16, %[W], s60 offen lds\n\t" \
  "s_add_u32 m0, s63, 12288\n\t" \
  "s_mov_b32 s60, 3407872\n\t" \
  "buffer_load_dwordx4 v16, %[W], s60 offen lds\n\t" \
  "s_add_u32 m0, s63, 13312\n\t" \
  "s_mov_b32 s60, 3473408\n\t" \
  "buffer_load_dwordx4 v16, %[W], s60 offen lds\n\t" \
  "s_add_u32 m0, s63, 14336\n\t" \
  "s_mov_b32 s60, 3538944\n\t" \
  "buffer_load_dwordx4 v16, %[W], s60 offen lds\n\t" \
  "s_add_u32 m0, s63, 15360\n\t" \
  "s_mov_b32 s60, 3604480\n\t" \
  "buffer_load_dwordx4 v16, %[W], s60 offen lds\n\t" \
  "s_waitcnt vmcnt(32)\n\t" \
  "ds_read_b64_tr_b16 a[16:17], v17 offset:16384\n\t" \
  "ds_read_b64_tr_b16 a[18:19], v17 offset:16896\n\t" \
  "ds_read_b64_tr_b16 a[20:21], v17 offset:20480\n\t" \
  "ds_read_b64_tr_b16 a[22:23], v17 offset:20992\n\t" \
  "ds_read_b64_tr_b16 a[88:89], v17 offset:16416\n\t" \
  "ds_read_b64_tr_b16 a[90:91], v17 offset:16928\n\t" \
  "ds_read_b64_tr_b16 a[92:93], v17 offset:20512\n\t" \
  "ds_read_b64_tr_b16 a[94:95], v17 offset:21024\n\t" \
  "ds_read_b64_tr_b16 a[160:161], v18 offset:16384\n\t" \
  "ds_read_b64_tr_b16 a[162:163], v18 offset:16896\n\t" \
  "ds_read_b64_tr_b16 a[164:165], v18 offset:20480\n\t" \
  "ds_read_b64_tr_b16 a[166:167], v18 offset:20992\n\t" \
  "ds_read_b64_tr_b16 a[232:233], v18 offset:16416\n\t" \
  "ds_read_b64_tr_b16 a[234:235], v18 offset:16928\n\t" \
  "ds_read_b64_tr_b16 a[236:237], v18 offset:20512\n\t" \
  "ds_read_b64_tr_b16 a[238:239], v18 offset:21024\n\t" \
  "s_waitcnt lgkmcnt(0)\n\t" \
  "s_add_u32 m0, s63, 16384\n\t" \
  "s_mov_b32 s60, 3670016\n\t" \
  "buffer_load_dwordx4 v16, %[W], s60 offen lds\n\t" \
  "s_add_u32 m0, s63, 17408\n\t" \
  "s_mov_b32 s60, 3735552\n\t" \
  "buffer_load_dwordx4 v16, %[W], s60 offen lds\n\t" \
  "s_add_u32 m0, s63, 18432\n\t" \
  "s_mov_b32 s60, 3801088\n\t" \
  "buffer_load_dwordx4 v16, %[W], s60 offen lds\n\t" \
  "s_add_u32 m0, s63, 19456\n\t" \
  "s_mov_b32 s60, 3866624\n\t" \
  "buffer_load_dwordx4 v16, %[W], s60 offen lds\n\t" \
  "s_add_u32 m0, s63, 20480\n\t" \
  "s_mov_b32 s60, 3932160\n\t" \
  "buffer_load_dwordx4 v16, %[W], s60 offen lds\n\t" \
  "s_add_u32 m0, s63, 21504\n\t" \
  "s_mov_b32 s60, 3997696\n\t" \
  "buffer_load_dwordx4 v16, %[W], s60 offen lds\n\t" \
  "s_add_u32 m0, s63, 22528\n\t" \
  "s_mov_b32 s60, 4063232\n\t" \
  "buffer_load_dwordx4 v16, %[W], s60 offen lds\n\t" \
  "s_add_u32 m0, s63, 23552\n\t" \
  "s_mov_b32 s60, 4128768\n\t" \
  "buffer_load_dwordx4 v16, %[W], s60 offen lds\n\t" \
  "s_waitcnt vmcnt(32)\n\t" \
  "ds_read_b64_tr_b16 a[24:25], v17 offset:24576\n\t" \
  "ds_read_b64_tr_b16 a[26:27], v17 offset:25088\n\t" \
  "ds_read_b64_tr_b16 a[28:29], v17 offset:28672\n\t" \
  "ds_read_b64_tr_b16 a[30:31], v17 offset:29184\n\t" \
  "ds_read_b64_tr_b16 a[96:97], v17 offset:24608\n\t" \
  "ds_read_b64_tr_b16 a[98:99], v17 offset:25120\n\t" \
  "ds_read_b64_tr_b16 a[100:101], v17 offset:28704\n\t" \
  "ds_read_b64_tr_b16 a[102:103], v17 offset:29216\n\t" \
  "ds_read_b64_tr_b16 a[168:169], v18 offset:24576\n\t" \
  "ds_read_b64_tr_b16 a[170:171], v18 offset:25088\n\t" \
  "ds_read_b64_tr_b16 a[172:173], v18 offset:28672\n\t" \
  "ds_read_b64_tr_b16 a[174:175], v18 offset:29184\n\t" \
  "ds_read_b64_tr_b16 a[240:241], v18 offset:24608\n\t" \
  "ds_read_b64_tr_b16 a[242:243], v18 offset:25120\n\t" \
  "ds_read_b64_tr_b16 a[244:245], v18 offset:28704\n\t" \
  "ds_read_b64_tr_b16 a[246:247], v18 offset:29216\n\t" \
  "s_waitcnt lgkmcnt(0)\n\t" \
  "s_add_u32 m0, s63, 24576\n\t" \
  "s_mov_b32 s60, 4194304\n\t" \
  "buffer_load_dwordx4 v16, %[W], s60 offen lds\n\t" \
  "s_add_u32 m0, s63, 25600\n\t" \
  "s_mov_b32 s60, 4259840\n\t" \
  "buffer_load_dwordx4 v16, %[W], s60 offen lds\n\t" \
  "s_add_u32 m0, s63, 26624\n\t" \
  "s_mov_b32 s60, 4325376\n\t" \
  "buffer_load_dwordx4 v16, %[W], s60 offen lds\n\t" \
  "s_add_u32 m0, s63, 27648\n\t" \
  "s_mov_b32 s60, 4390912\n\t" \
  "buffer_load_dwordx4 v16, %[W], s60 offen lds\n\t" \
  "s_add_u32 m0, s63, 28672\n\t" \
  "s_mov_b32 s60, 4456448\n\t" \
  "buffer_load_dwordx4 v16, %[W], s60 offen lds\n\t" \
  "s_add_u32 m0, s63, 29696\n\t" \
  "s_mov_b32 s60, 4521984\n\t" \
  "buffer_load_dwordx4 v16, %[W], s60 offen lds\n\t" \
  "s_add_u32 m0, s63, 30720\n\t" \
  "s_mov_b32 s60, 4587520\n\t" \
  "buffer_load_dwordx4 v16, %[W], s60 offen lds\n\t" \
  "s_add_u32 m0, s63, 31744\n\t" \
  "s_mov_b32 s60, 4653056\n\t" \
  "buffer_load_dwordx4 v16, %[W], s60 offen lds\n\t" \
  "s_waitcnt vmcnt(32)\n\t" \
  "ds_read_b64_tr_b16 a[32:33], v17 offset:32768\n\t" \
  "ds_read_b64_tr_b16 a[34:35], v17 offset:33280\n\t" \
  "ds_read_b64_tr_b16 a[36:37], v17 offset:36864\n\t" \
  "ds_read_b64_tr_b16 a[38:39], v17 offset:37376\n\t" \
  "ds_read_b64_tr_b16 a[104:105], v17 offset:32800\n\t" \
  "ds_read_b64_tr_b16 a[106:107], v17 offset:33312\n\t" \
  "ds_read_b64_tr_b16 a[108:109], v17 offset:36896\n\t" \
  "ds_read_b64_tr_b16 a[110:111], v17 offset:37408\n\t" \
  "ds_read_b64_tr_b16 a[176:177], v18 offset:32768\n\t" \
  "ds_read_b64_tr_b16 a[178:179], v18 offset:33280\n\t" \
  "ds_read_b64_tr_b16 a[180:181], v18 offset:36864\n\t" \
  "ds_read_b64_tr_b16 a[182:183], v18 offset:37376\n\t" \
  "ds_read_b64_tr_b16 a[248:249], v18 offset:32800\n\t" \
  "ds_read_b64_tr_b16 a[250:251], v18 offset:33312\n\t" \
  "ds_read_b64_tr_b16 a[252:253], v18 offset:36896\n\t" \
  "ds_read_b64_tr_b16 a[254:255], v18 offset:37408\n\t" \
  "s_waitcnt lgkmcnt(0)\n\t" \
  "s_waitcnt vmcnt(24)\n\t" \
  "ds_read_b64_tr_b16 a[40:41], v17 offset:0\n\t" \
  "ds_read_b64_tr_b16 a[42:43], v17 offset:512\n\t" \
  "ds_read_b64_tr_b16 a[44:45], v17 offset:4096\n\t" \
  "ds_read_b64_tr_b16 a[46:47], v17 offset:4608\n\t" \
  "ds_read_b64_tr_b16 a[112:113], v17 offset:32\n\t" \
  "ds_read_b64_tr_b16 a[114:115], v17 offset:544\n\t" \
  "ds_read_b64_tr_b16 a[116:117], v17 offset:4128\n\t" \
  "ds_read_b64_tr_b16 a[118:119], v17 offset:4640\n\t" \
  "ds_read_b64_tr_b16 a[184:185], v18 offset:0\n\t" \
  "ds_read_b64_tr_b16 a[186:187], v18 offset:512\n\t" \
  "ds_read_b64_tr_b16 a[188:189], v18 offset:4096\n\t" \
  "ds_read_b64_tr_b16 a[190:191], v18 offset:4608\n\t" \
  "ds_read_b64_tr_b16 v[224:225], v18 offset:32\n\t" \
  "ds_read_b64_tr_b16 v[226:227], v18 offset:544\n\t" \
  "ds_read_b64_tr_b16 v[228:229], v18 offset:4128\n\t" \
  "ds_read_b64_tr_b16 v[230:231], v18 offset:4640\n\t" \
  "s_waitcnt lgkmcnt(0)\n\t" \
  "s_waitcnt vmcnt(16)\n\t" \
  "ds_read_b64_tr_b16 a[48:49], v17 offset:8192\n\t" \
  "ds_read_b64_tr_b16 a[50:51], v17 offset:8704\n\t" \
  "ds_read_b64_tr_b16 a[52:53], v17 offset:12288\n\t" \
  "ds_read_b64_tr_b16 a[54:55], v17 offset:12800\n\t" \
  "ds_read_b64_tr_b16 a[120:121], v17 offset:8224\n\t" \
  "ds_read_b64_tr_b16 a[122:123], v17 offset:8736\n\t" \
  "ds_read_b64_tr_b16 a[124:125], v17 offset:12320\n\t" \
  "ds_read_b64_tr_b16 a[126:127], v17 offset:12832\n\t" \
  "ds_read_b64_tr_b16 a[192:193], v18 offset:8192\n\t" \
  "ds_read_b64_tr_b16 a[194:195], v18 offset:8704\n\t" \
  "ds_read_b64_tr_b16 a[196:197], v18 offset:12288\n\t" \
  "ds_read_b64_tr_b16 a[198:199], v18 offset:12800\n\t" \
  "ds_read_b64_tr_b16 v[232:233], v18 offset:8224\n\t" \
  "ds_read_b64_tr_b16 v[234:235], v18 offset:8736\n\t" \
  "ds_read_b64_tr_b16 v[236:237], v18 offset:12320\n\t" \
  "ds_read_b64_tr_b16 v[238:239], v18 offset:12832\n\t" \
  "s_waitcnt lgkmcnt(0)\n\t" \
  "s_waitcnt vmcnt(8)\n\t" \
  "ds_read_b64_tr_b16 a[56:57], v17 offset:16384\n\t" \
  "ds_read_b64_tr_b16 a[58:59], v17 offset:16896\n\t" \
  "ds_read_b64_tr_b16 a[60:61], v17 offset:20480\n\t" \
  "ds_read_b64_tr_b16 a[62:63], v17 offset:20992\n\t" \
  "ds_read_b64_tr_b16 a[128:129], v17 offset:16416\n\t" \
  "ds_read_b64_tr_b16 a[130:131], v17 offset:16928\n\t" \
  "ds_read_b64_tr_b16 a[132:133], v17 offset:20512\n\t" \
  "ds_read_b64_tr_b16 a[134:135], v17 offset:21024\n\t" \
  "ds_read_b64_tr_b16 a[200:201], v18 offset:16384\n\t" \
  "ds_read_b64_tr_b16 a[202:203], v18 offset:16896\n\t" \
  "ds_read_b64_tr_b16 a[204:205], v18 offset:20480\n\t" \
  "ds_read_b64_tr_b16 a[206:207], v18 offset:20992\n\t" \
  "ds_read_b64_tr_b16 v[240:241], v18 offset:16416\n\t" \
  "ds_read_b64_tr_b16 v[242:243], v18 offset:16928\n\t" \
  "ds_read_b64_tr_b16 v[244:245], v18 offset:20512\n\t" \
  "ds_read_b64_tr_b16 v[246:247], v18 offset:21024\n\t" \
  "s_waitcnt lgkmcnt(0)\n\t" \
  "s_waitcnt vmcnt(0)\n\t" \
  "ds_read_b64_tr_b16 a[64:65], v17 offset:24576\n\t" \
  "ds_read_b64_tr_b16 a[66:67], v17 offset:25088\n\t" \
  "ds_read_b64_tr_b16 a[68:69], v17 offset:28672\n\t" \
  "ds_read_b64_tr_b16 a[70:71], v17 offset:29184\n\t" \
  "ds_read_b64_tr_b16 a[136:137], v17 offset:24608\n\t" \
  "ds_read_b64_tr_b16 a[138:139], v17 offset:25120\n\t" \
  "ds_read_b64_tr_b16 a[140:141], v17 offset:28704\n\t" \
  "ds_read_b64_tr_b16 a[142:143], v17 offset:29216\n\t" \
  "ds_read_b64_tr_b16 a[208:209], v18 offset:24576\n\t" \
  "ds_read_b64_tr_b16 a[210:211], v18 offset:25088\n\t" \
  "ds_read_b64_tr_b16 a[212:213], v18 offset:28672\n\t" \
  "ds_read_b64_tr_b16 a[214:215], v18 offset:29184\n\t" \
  "ds_read_b64_tr_b16 v[248:249], v18 offset:24608\n\t" \
  "ds_read_b64_tr_b16 v[250:251], v18 offset:25120\n\t" \
  "ds_read_b64_tr_b16 v[252:253], v18 offset:28704\n\t" \
  "ds_read_b64_tr_b16 v[254:255], v18 offset:29216\n\t" \
  "s_waitcnt lgkmcnt(0)\n\t" \
  "s_mov_b32 s84, " TK_CAP "\n\t" \
  "s_mov_b32 s48, 0\n\t" \
  "s_mov_b32 s49, 0xf0000000\n\t" \
  "s_mov_b32 s46, s41\n\t" \
  "s_mov_b32 s56, 0\n\t" \
  "v_lshrrev_b32 v7, 2, v0\n\t" \
  "s_mov_b32 s60, " TK_CTAB "\n\t" \
  "buffer_load_dword v4, v7, %[X], s60 offen offset:88 sc1\n\t" \
  "buffer_load_dword v7, v7, %[X], s60 offen offset:0 sc1\n\t" \
  "s_waitcnt vmcnt(0)\n\t" \
  "v_and_b32 v4, 1, v4\n\t" \
  "v_and_b32 v7, 1, v7\n\t" \
  "v_cmp_ne_u32 s[90:91], s67, v4\n\t" \
  "v_cmp_ne_u32 s[88:89], s67, v7\n\t" \
  "s_nop 1\n\t" \
  "s_mov_b32 s57, 0xf0000000\n\t" \
  "s_add_u32 s83, s48, 0\n\t" \
  "s_lshr_b64 s[60:61], s[90:91], s83\n\t" \
  "s_and_b32 s60, s60, 1\n\t" \
  "s_lshl_b32 s60, s60, 13\n\t" \
  "s_add_u32 s83, s83, " TK_SG0 "\n\t" \
  "s_lshl_b32 s83, s83, 21\n\t" \
  "s_or_b32 s83, s83, s60\n\t" \
  "s_add_u32 s80, s48, " TK_TRN "\n\t" \
  "s_lshr_b64 s[60:61], s[90:91], s80\n\t" \
  "s_and_b32 s60, s60, 1\n\t" \
  "s_lshl_b32 s60, s60, 13\n\t" \
  "s_add_u32 s80, s80, " TK_SG0 "\n\t" \
  "s_lshl_b32 s80, s80, 21\n\t" \
  "s_or_b32 s80, s80, s60\n\t" \
  "s_add_u32 s62, s83, s39\n\t" \
  "s_add_u32 s80, s80, s39\n\t" \
  "s_add_u32 s80, s80, 2048\n\t" \
  "s_cmp_lt_u32 s48, " TK_TRM "\n\t" \
  "s_cselect_b32 s80, s62, s80\n\t" \
  "v_add_u32 v7, s62, v3\n\t" \
  "s_mov_b32 exec_lo, 0\n\t" \
  "v_mov_b32 v7, s80\n\t" \
  "s_mov_b32 exec_lo, -1\n\t" \
  "LCOLD_%=:\n\t" \
  "s_waitcnt vmcnt(0)\n\t" \
  "s_mov_b64 exec, 1\n\t" \
  "buffer_store_dword v6, off, %[X], s49 sc1\n\t" \
  "s_mov_b64 exec, -1\n\t" \
  "s_mov_b32 s49, 0xf0000000\n\t" \
  "s_add_u32 s55, s83, s36\n\t" \
  "s_cmp_lg_u32 s40, 0\n\t" \
  "s_cbranch_scc1 LGO_%=\n\t" \
  "s_and_b32 s62, s45, 3\n\t" \
  "s_lshl_b32 s62, s62, 7\n\t" \
  "s_add_u32 s62, s62, s83\n\t" \
  "s_add_u32 s62, s62, s39\n\t" \
  "s_mov_b32 s61, 0\n\t" \
  "LPO_%=:\n\t" \
  "buffer_load_dword v4, off, %[X], s62 sc1\n\t" \
  "s_waitcnt vmcnt(0)\n\t" \
  "v_cmp_eq_u32 vcc, 0, v4\n\t" \
  "s_cbranch_vccz LPO_%=x\n\t" \
  "s_add_u32 s61, s61, 1\n\t" \
  "s_sleep " TK_PSH "\n\t" \
  "s_cmp_gt_u32 s61, s84\n\t" \
  "s_cbranch_scc1 LPO_%=g\n\t" \
  "s_and_b32 s60, s61, 1023\n\t" \
  "s_cmp_lg_u32 s60, 0\n\t" \
  "s_cbranch_scc1 LPO_%=\n\t" \
  "buffer_load_dword v4, off, %[X], s92 sc1\n\t" \
  "s_waitcnt vmcnt(0)\n\t" \
  "v_readfirstlane_b32 s60, v4\n\t" \
  "s_nop 0\n\t" \
  "s_cmp_eq_u32 s60, 0\n\t" \
  "s_cbranch_scc1 LPO_%=\n\t" \
  "LPO_%=g:\n\t" \
  "s_mov_b32 s84, 0\n\t" \
  "s_or_b32 s60, s48, 1610612736\n\t" \
  "v_mov_b32 v4, s60\n\t" \
  "s_mov_b64 exec, 1\n\t" \
  "buffer_store_dword v4, off, %[X], s92 sc1\n\t" \
  "s_mov_b64 exec, -1\n\t" \
  "LPO_%=x:\n\t" \
  "s_mov_b32 s61, 0\n\t" \
  "LPH_%=:\n\t" \
  "buffer_load_dword v4, v7, %[X], 0 offen sc1\n\t" \
  "s_waitcnt vmcnt(0)\n\t" \
  "v_cmp_eq_u32 vcc, 0, v4\n\t" \
  "s_cbranch_vccz LPH_%=x\n\t" \
  "s_add_u32 s61, s61, 1\n\t" \
  "s_sleep " TK_PSH "\n\t" \
  "s_cmp_gt_u32 s61, s84\n\t" \
  "s_cbranch_scc1 LPH_%=g\n\t" \
  "s_and_b32 s60, s61, 1023\n\t" \
  "s_cmp_lg_u32 s60, 0\n\t" \
  "s_cbranch_scc1 LPH_%=\n\t" \
  "buffer_load_dword v4, off, %[X], s92 sc1\n\t" \
  "s_waitcnt vmcnt(0)\n\t" \
  "v_readfirstlane_b32 s60, v4\n\t" \
  "s_nop 0\n\t" \
  "s_cmp_eq_u32 s60, 0\n\t" \
  "s_cbranch_scc1 LPH_%=\n\t" \
  "LPH_%=g:\n\t" \
  "s_mov_b32 s84, 0\n\t" \
  "s_or_b32 s60, s48, 1610612736\n\t" \
  "v_mov_b32 v4, s60\n\t" \
  "s_mov_b64 exec, 1\n\t" \
  "buffer_store_dword v4, off, %[X], s92 sc1\n\t" \
  "s_mov_b64 exec, -1\n\t" \
  "LPH_%=x:\n\t" \
  "s_cmp_lt_i32 s57, 0\n\t" \
  "s_cbranch_scc1 LGO_%=\n\t" \
  "s_movk_i32 s63, 4000\n\t" \
  "s_branch LGC_%=u\n\t" \
  "LGC_%=:\n\t" \
  "s_sleep " TK_PSG "\n\t" \
  "buffer_load_dword v8, off, %[X], s57 sc1\n\t" \
  "s_waitcnt vmcnt(0)\n\t" \
  "LGC_%=u:\n\t" \
  "s_memrealtime s[52:53]\n\t" \
  "v_readfirstlane_b32 s60, v8\n\t" \
  "s_waitcnt lgkmcnt(0)\n\t" \
  "s_sub_u32 s63, s63, 1\n\t" \
  "s_cbranch_scc1 LGO_%=\n\t" \
  "s_cmp_eq_u32 s60, -1\n\t" \
  "s_cbranch_scc1 LGC_%=\n\t" \
  "s_not_b32 s60, s60\n\t" \
  "s_lshl_b32 s61, s52, 8\n\t" \
  "s_sub_u32 s60, s60, s61\n\t" \
  "s_ashr_i32 s60, s60, 8\n\t" \
  "s_sub_i32 s60, s60, " TK_QXC "\n\t" \
  "s_min_i32 s60, s60, 400\n\t" \
  "s_add_u32 s54, s52, s60\n\t" \
  "LGC_%=w:\n\t" \
  "s_sleep 1\n\t" \
  "s_memrealtime s[52:53]\n\t" \
  "s_waitcnt lgkmcnt(0)\n\t" \
  "s_sub_i32 s53, s52, s54\n\t" \
  "s_cmp_lt_i32 s53, 0\n\t" \
  "s_cbranch_scc1 LGC_%=w\n\t" \
  "LGO_%=:\n\t" \
  "s_barrier\n\t" \
  "s_add_u32 m0, s46, 0\n\t" \
  "s_add_u32 s47, s55, 0\n\t" \
  "buffer_load_dwordx4 v0, %[X], s47 offen sc1 lds\n\t" \
  "s_add_u32 m0, s46, 4096\n\t" \
  "s_add_u32 s47, s55, 4096\n\t" \
  "buffer_load_dwordx4 v0, %[X], s47 offen sc1 lds\n\t" \
  "s_add_u32 m0, s46, 8192\n\t" \
  "s_add_u32 s47, s55, 16384\n\t" \
  "buffer_load_dwordx4 v0, %[X], s47 offen sc1 lds\n\t" \
  "s_add_u32 m0, s46, 12288\n\t" \
  "s_add_u32 s47, s55, 20480\n\t" \
  "buffer_load_dwordx4 v0, %[X], s47 offen sc1 lds\n\t" \
  "s_add_u32 m0, s46, 16384\n\t" \
  "s_add_u32 s47, s55, 32768\n\t" \
  "buffer_load_dwordx4 v0, %[X], s47 offen sc1 lds\n\t" \
  "s_add_u32 m0, s46, 20480\n\t" \
  "s_add_u32 s47, s55, 36864\n\t" \
  "buffer_load_dwordx4 v0, %[X], s47 offen sc1 lds\n\t" \
  "s_add_u32 m0, s46, 24576\n\t" \
  "s_add_u32 s47, s55, 49152\n\t" \
  "buffer_load_dwordx4 v0, %[X], s47 offen sc1 lds\n\t" \
  "s_add_u32 m0, s46, 28672\n\t" \
  "s_add_u32 s47, s55, 53248\n\t" \
  "buffer_load_dwordx4 v0, %[X], s47 offen sc1 lds\n\t" \
  "s_add_u32 m0, s46, 32768\n\t" \
  "s_add_u32 s47, s55, 65536\n\t" \
  "buffer_load_dwordx4 v0, %[X], s47 offen sc1 lds\n\t" \
  "s_waitcnt vmcnt(5)\n\t" \
  "LTOP_%=:\n\t" \
  "s_barrier\n\t" \
  TK_PQ1 \
  "ds_read_b128 v[64:67], v1 offset:0\n\t" \
  "ds_read_b128 v[68:71], v1 offset:2048\n\t" \
  "ds_read_b128 v[72:75], v2 offset:0\n\t" \
  "ds_read_b128 v[76:79], v2 offset:2048\n\t" \
  "ds_read_b128 v[80:83], v1 offset:4096\n\t" \
  "ds_read_b128 v[84:87], v1 offset:6144\n\t" \
  "s_mov_b32 s82, s83\n\t" \
  "s_xor_b32 s46, s46, 0x10000\n\t" \
  "s_waitcnt lgkmcnt(5)\n\t" \
  "v_mfma_f32_16x16x32_bf16 v[16:19], v[64:67], a[0:3], 0\n\t" \
  "ds_read_b128 v[88:91], v2 offset:4096\n\t" \
  "s_waitcnt lgkmcnt(5)\n\t" \
  "v_mfma_f32_16x16x32_bf16 v[20:23], v[68:71], a[0:3], 0\n\t" \
  "s_add_u32 s50, s48, 0\n\t" \
  "s_and_b32 s50, s50, " TK_TRM "\n\t" \
  "v_mfma_f32_16x16x32_bf16 v[24:27], v[64:67], a[72:75], 0\n\t" \
  "ds_read_b128 v[92:95], v2 offset:6144\n\t" \
  "v_mfma_f32_16x16x32_bf16 v[28:31], v[68:71], a[72:75], 0\n\t" \
  "s_lshr_b64 s[60:61], s[90:91], s50\n\t" \
  "s_and_b32 s60, s60, 1\n\t" \
  "s_waitcnt lgkmcnt(5)\n\t" \
  "v_mfma_f32_16x16x32_bf16 v[16:19], v[72:75], a[4:7], v[16:19]\n\t" \
  "ds_read_b128 v[96:99], v1 offset:8192\n\t" \
  "s_waitcnt lgkmcnt(5)\n\t" \
  "v_mfma_f32_16x16x32_bf16 v[20:23], v[76:79], a[4:7], v[20:23]\n\t" \
  "s_lshl_b32 s60, s60, 13\n\t" \
  "s_add_u32 s50, s50, " TK_SG0 "\n\t" \
  "v_mfma_f32_16x16x32_bf16 v[24:27], v[72:75], a[76:79], v[24:27]\n\t" \
  "ds_read_b128 v[100:103], v1 offset:10240\n\t" \
  "v_mfma_f32_16x16x32_bf16 v[28:31], v[76:79], a[76:79], v[28:31]\n\t" \
  "s_lshl_b32 s50, s50, 21\n\t" \
  "s_or_b32 s50, s50, s60\n\t" \
  "s_waitcnt lgkmcnt(5)\n\t" \
  "v_mfma_f32_16x16x32_bf16 v[16:19], v[80:83], a[8:11], v[16:19]\n\t" \
  "ds_read_b128 v[104:107], v2 offset:8192\n\t" \
  "s_waitcnt lgkmcnt(5)\n\t" \
  "v_mfma_f32_16x16x32_bf16 v[20:23], v[84:87], a[8:11], v[20:23]\n\t" \
  "s_add_u32 s50, s50, s37\n\t" \
  "s_add_u32 s51, s50, 4096\n\t" \
  "v_mfma_f32_16x16x32_bf16 v[24:27], v[80:83], a[80:83], v[24:27]\n\t" \
  "ds_read_b128 v[108:111], v2 offset:10240\n\t" \
  "v_mfma_f32_16x16x32_bf16 v[28:31], v[84:87], a[80:83], v[28:31]\n\t" \
  "s_add_u32 s83, s48, 1\n\t" \
  "s_lshr_b64 s[60:61], s[90:91], s83\n\t" \
  "s_waitcnt lgkmcnt(5)\n\t" \
  "v_mfma_f32_16x16x32_bf16 v[16:19], v[88:91], a[12:15], v[16:19]\n\t" \
  "ds_read_b128 v[112:115], v1 offset:12288\n\t" \
  "s_waitcnt lgkmcnt(5)\n\t" \
  "v_mfma_f32_16x16x32_bf16 v[20:23], v[92:95], a[12:15], v[20:23]\n\t" \
  "s_and_b32 s60, s60, 1\n\t" \
  "s_lshl_b32 s60, s60, 13\n\t" \
  "v_mfma_f32_16x16x32_bf16 v[24:27], v[88:91], a[84:87], v[24:27]\n\t" \
  "ds_read_b128 v[116:119], v1 offset:14336\n\t" \
  "v_mfma_f32_16x16x32_bf16 v[28:31], v[92:95], a[84:87], v[28:31]\n\t" \
  "s_add_u32 s83, s83, " TK_SG0 "\n\t" \
  "s_lshl_b32 s83, s83, 21\n\t" \
  "s_waitcnt lgkmcnt(5)\n\t" \
  "v_mfma_f32_16x16x32_bf16 v[16:19], v[96:99], a[16:19], v[16:19]\n\t" \
  "ds_read_b128 v[120:123], v2 offset:12288\n\t" \
  "s_waitcnt lgkmcnt(5)\n\t" \
  "v_mfma_f32_16x16x32_bf16 v[20:23], v[100:103], a[16:19], v[20:23]\n\t" \
  "s_or_b32 s83, s83, s60\n\t" \
  "s_cmp_lg_u32 s56, 0\n\t" \
  "v_mfma_f32_16x16x32_bf16 v[24:27], v[96:99], a[88:91], v[24:27]\n\t" \
  "ds_read_b128 v[124:127], v2 offset:14336\n\t" \
  "v_mfma_f32_16x16x32_bf16 v[28:31], v[100:103], a[88:91], v[28:31]\n\t" \
  "s_cbranch_scc1 LWARM_%=\n\t" \
  "s_waitcnt vmcnt(0)\n\t" \
  "LWARM_%=:\n\t" \
  "s_barrier\n\t" \
  "s_waitcnt lgkmcnt(5)\n\t" \
  "v_mfma_f32_16x16x32_bf16 v[16:19], v[104:107], a[20:23], v[16:19]\n\t" \
  "ds_read_b128 v[128:131], v1 offset:16384\n\t" \
  "s_waitcnt lgkmcnt(5)\n\t" \
  "v_mfma_f32_16x16x32_bf16 v[20:23], v[108:111], a[20:23], v[20:23]\n\t" \
  "v_mfma_f32_16x16x32_bf16 v[24:27], v[104:107], a[92:95], v[24:27]\n\t" \
  "ds_read_b128 v[132:135], v1 offset:18432\n\t" \
  "v_mfma_f32_16x16x32_bf16 v[28:31], v[108:111], a[92:95], v[28:31]\n\t" \
  "s_add_i32 s62, s48, s42\n\t" \
  "s_add_i32 s62, s62, 1\n\t" \
  "s_waitcnt lgkmcnt(5)\n\t" \
  "v_mfma_f32_16x16x32_bf16 v[16:19], v[112:115], a[24:27], v[16:19]\n\t" \
  "ds_read_b128 v[136:139], v2 offset:16384\n\t" \
  "s_waitcnt lgkmcnt(5)\n\t" \
  "v_mfma_f32_16x16x32_bf16 v[20:23], v[116:119], a[24:27], v[20:23]\n\t" \
  "s_mov_b32 s61, s62\n\t" \
  "v_mfma_f32_16x16x32_bf16 v[24:27], v[112:115], a[96:99], v[24:27]\n\t" \
  "ds_read_b128 v[140:143], v2 offset:18432\n\t" \
  "v_mfma_f32_16x16x32_bf16 v[28:31], v[116:119], a[96:99], v[28:31]\n\t" \
  "s_mul_i32 s63, s62, 43\n\t" \
  "s_lshr_b32 s63, s63, 9\n\t" \
  "s_waitcnt lgkmcnt(5)\n\t" \
  "v_mfma_f32_16x16x32_bf16 v[16:19], v[120:123], a[28:31], v[16:19]\n\t" \
  "ds_read_b128 v[144:147], v1 offset:20480\n\t" \
  "s_waitcnt lgkmcnt(5)\n\t" \
  "v_mfma_f32_16x16x32_bf16 v[20:23], v[124:127], a[28:31], v[20:23]\n\t" \
  "s_mul_i32 s63, s63, " TK_RM "\n\t" \
  "s_sub_u32 s62, s62, s63\n\t" \
  "v_mfma_f32_16x16x32_bf16 v[24:27], v[120:123], a[100:103], v[24:27]\n\t" \
  "ds_read_b128 v[148:151], v1 offset:22528\n\t" \
  "v_mfma_f32_16x16x32_bf16 v[28:31], v[124:127], a[100:103], v[28:31]\n\t" \
  "s_mul_i32 s63, s62, 43\n\t" \
  "s_lshr_b32 s63, s63, 7\n\t" \
  "s_waitcnt lgkmcnt(5)\n\t" \
  "v_mfma_f32_16x16x32_bf16 v[16:19], v[128:131], a[32:35], v[16:19]\n\t" \
  "ds_read_b128 v[152:155], v2 offset:20480\n\t" \
  "s_waitcnt lgkmcnt(5)\n\t" \
  "v_mfma_f32_16x16x32_bf16 v[20:23], v[132:135], a[32:35], v[20:23]\n\t" \
  "s_mul_i32 s57, s62, 688128\n\t" \
  "s_lshl_b32 s60, s63, 15\n\t" \
  "v_mfma_f32_16x16x32_bf16 v[24:27], v[128:131], a[104:107], v[24:27]\n\t" \
  "ds_read_b128 v[156:159], v2 offset:22528\n\t" \
  "v_mfma_f32_16x16x32_bf16 v[28:31], v[132:135], a[104:107], v[28:31]\n\t" \
  "s_add_u32 s57, s57, s60\n\t" \
  "s_lshr_b32 s60, s88, s63\n\t" \
  "s_waitcnt lgkmcnt(5)\n\t" \
  "v_mfma_f32_16x16x32_bf16 v[16:19], v[136:139], a[36:39], v[16:19]\n\t" \
  "ds_read_b128 v[160:163], v1 offset:24576\n\t" \
  "s_waitcnt lgkmcnt(5)\n\t" \
  "v_mfma_f32_16x16x32_bf16 v[20:23], v[140:143], a[36:39], v[20:23]\n\t" \
  "s_and_b32 s60, s60, 1\n\t" \
  "s_lshl_b32 s60, s60, 13\n\t" \
  "v_mfma_f32_16x16x32_bf16 v[24:27], v[136:139], a[108:111], v[24:27]\n\t" \
  "ds_read_b128 v[164:167], v1 offset:26624\n\t" \
  "v_mfma_f32_16x16x32_bf16 v[28:31], v[140:143], a[108:111], v[28:31]\n\t" \
  "s_add_u32 s57, s57, s60\n\t" \
  "s_add_u32 s57, s57, 656896\n\t" \
  "s_waitcnt lgkmcnt(5)\n\t" \
  "v_mfma_f32_16x16x32_bf16 v[16:19], v[144:147], a[40:43], v[16:19]\n\t" \
  "ds_read_b128 v[168:171], v2 offset:24576\n\t" \
  "s_waitcnt lgkmcnt(5)\n\t" \
  "v_mfma_f32_16x16x32_bf16 v[20:23], v[148:151], a[40:43], v[20:23]\n\t" \
  "s_cmp_lt_i32 s61, 1\n\t" \
  "s_cselect_b32 s57, 0xf0000000, s57\n\t" \
  "v_mfma_f32_16x16x32_bf16 v[24:27], v[144:147], a[112:115], v[24:27]\n\t" \
  "ds_read_b128 v[172:175], v2 offset:26624\n\t" \
  "v_mfma_f32_16x16x32_bf16 v[28:31], v[148:151], a[112:115], v[28:31]\n\t" \
  "s_cmp_lg_u32 s40, 0\n\t" \
  "s_cselect_b32 s57, 0xf0000000, s57\n\t" \
  "s_waitcnt lgkmcnt(5)\n\t" \
  "v_mfma_f32_16x16x32_bf16 v[16:19], v[152:155], a[44:47], v[16:19]\n\t" \
  "ds_read_b128 v[176:179], v1 offset:28672\n\t" \
  "s_waitcnt lgkmcnt(5)\n\t" \
  "v_mfma_f32_16x16x32_bf16 v[20:23], v[156:159], a[44:47], v[20:23]\n\t" \
  "s_add_u32 s80, s48, " TK_TRN "\n\t" \
  "s_lshr_b64 s[60:61], s[90:91], s80\n\t" \
  "s_and_b32 s60, s60, 1\n\t" \
  "v_mfma_f32_16x16x32_bf16 v[24:27], v[152:155], a[116:119], v[24:27]\n\t" \
  "ds_read_b128 v[180:183], v1 offset:30720\n\t" \
  "v_mfma_f32_16x16x32_bf16 v[28:31], v[156:159], a[116:119], v[28:31]\n\t" \
  "s_lshl_b32 s60, s60, 13\n\t" \
  "s_add_u32 s80, s80, " TK_SG0 "\n\t" \
  "s_lshl_b32 s80, s80, 21\n\t" \
  "s_waitcnt lgkmcnt(5)\n\t" \
  "v_mfma_f32_16x16x32_bf16 v[16:19], v[160:163], a[48:51], v[16:19]\n\t" \
  "ds_read_b128 v[184:187], v2 offset:28672\n\t" \
  "s_waitcnt lgkmcnt(5)\n\t" \
  "v_mfma_f32_16x16x32_bf16 v[20:23], v[164:167], a[48:51], v[20:23]\n\t" \
  "s_or_b32 s80, s80, s60\n\t" \
  "s_add_u32 s62, s83, s39\n\t" \
  "s_add_u32 s80, s80, s39\n\t" \
  TK_PQ0 \
  "v_mfma_f32_16x16x32_bf16 v[24:27], v[160:163], a[120:123], v[24:27]\n\t" \
  "ds_read_b128 v[188:191], v2 offset:30720\n\t" \
  "v_mfma_f32_16x16x32_bf16 v[28:31], v[164:167], a[120:123], v[28:31]\n\t" \
  "s_add_u32 s80, s80, 2048\n\t" \
  "s_waitcnt lgkmcnt(5)\n\t" \
  "v_mfma_f32_16x16x32_bf16 v[16:19], v[168:171], a[52:55], v[16:19]\n\t" \
  "ds_read_b128 v[192:195], v1 offset:32768\n\t" \
  "s_waitcnt lgkmcnt(5)\n\t" \
  "v_mfma_f32_16x16x32_bf16 v[20:23], v[172:175], a[52:55], v[20:23]\n\t" \
  "v_mfma_f32_16x16x32_bf16 v[24:27], v[168:171], a[124:127], v[24:27]\n\t" \
  "ds_read_b128 v[196:199], v1 offset:34816\n\t" \
  "v_mfma_f32_16x16x32_bf16 v[28:31], v[172:175], a[124:127], v[28:31]\n\t" \
  "s_cmp_lt_u32 s48, " TK_TRM "\n\t" \
  "s_cselect_b32 s80, s62, s80\n\t" \
  "s_waitcnt lgkmcnt(5)\n\t" \
  "v_mfma_f32_16x16x32_bf16 v[16:19], v[176:179], a[56:59], v[16:19]\n\t" \
  "ds_read_b128 v[200:203], v2 offset:32768\n\t" \
  "s_waitcnt lgkmcnt(5)\n\t" \
  "v_mfma_f32_16x16x32_bf16 v[20:23], v[180:183], a[56:59], v[20:23]\n\t" \
  "v_add_u32 v7, s62, v3\n\t" \
  "s_mov_b32 exec_lo, 0\n\t" \
  "v_mov_b32 v7, s80\n\t" \
  "s_mov_b32 exec_lo, -1\n\t" \
  "v_mfma_f32_16x16x32_bf16 v[24:27], v[176:179], a[128:131], v[24:27]\n\t" \
  "ds_read_b128 v[204:207], v2 offset:34816\n\t" \
  "v_mfma_f32_16x16x32_bf16 v[28:31], v[180:183], a[128:131], v[28:31]\n\t" \
  "s_waitcnt lgkmcnt(5)\n\t" \
  "v_mfma_f32_16x16x32_bf16 v[16:19], v[184:187], a[60:63], v[16:19]\n\t" \
  "s_waitcnt lgkmcnt(4)\n\t" \
  "v_mfma_f32_16x16x32_bf16 v[20:23], v[188:191], a[60:63], v[20:23]\n\t" \
  "buffer_load_dword v4, v7, %[X], 0 offen sc1\n\t" \
  "v_mfma_f32_16x16x32_bf16 v[24:27], v[184:187], a[132:135], v[24:27]\n\t" \
  "v_mfma_f32_16x16x32_bf16 v[28:31], v[188:191], a[132:135], v[28:31]\n\t" \
  "s_waitcnt lgkmcnt(3)\n\t" \
  "v_mfma_f32_16x16x32_bf16 v[16:19], v[192:195], a[64:67], v[16:19]\n\t" \
  "v_xor_b32 v1, 0x10000, v1\n\t" \
  "v_xor_b32 v2, 0x10000, v2\n\t" \
  "s_waitcnt lgkmcnt(2)\n\t" \
  "v_mfma_f32_16x16x32_bf16 v[20:23], v[196:199], a[64:67], v[20:23]\n\t" \
  "v_mfma_f32_16x16x32_bf16 v[24:27], v[192:195], a[136:139], v[24:27]\n\t" \
  "v_mfma_f32_16x16x32_bf16 v[28:31], v[196:199], a[136:139], v[28:31]\n\t" \
  "s_waitcnt lgkmcnt(1)\n\t" \
  "v_mfma_f32_16x16x32_bf16 v[16:19], v[200:203], a[68:71], v[16:19]\n\t" \
  "s_waitcnt lgkmcnt(0)\n\t" \
  "v_mfma_f32_16x16x32_bf16 v[20:23], v[204:207], a[68:71], v[20:23]\n\t" \
  "v_mfma_f32_16x16x32_bf16 v[24:27], v[200:203], a[140:143], v[24:27]\n\t" \
  "v_mfma_f32_16x16x32_bf16 v[28:31], v[204:207], a[140:143], v[28:31]\n\t" \
  "v_mfma_f32_16x16x32_bf16 v[32:35], v[64:67], a[144:147], 0\n\t" \
  "v_cvt_pk_bf16_f32 v48, v16, v17\n\t" \
  "v_cvt_pk_bf16_f32 v49, v18, v19\n\t" \
  "v_mfma_f32_16x16x32_bf16 v[36:39], v[68:71], a[144:147], 0\n\t" \
  "v_cvt_pk_bf16_f32 v50, v20, v21\n\t" \
  "v_cvt_pk_bf16_f32 v51, v22, v23\n\t" \
  "v_mfma_f32_16x16x32_bf16 v[32:35], v[72:75], a[148:151], v[32:35]\n\t" \
  "v_cvt_pk_bf16_f32 v52, v24, v25\n\t" \
  "v_cvt_pk_bf16_f32 v53, v26, v27\n\t" \
  "v_mfma_f32_16x16x32_bf16 v[36:39], v[76:79], a[148:151], v[36:39]\n\t" \
  "v_cvt_pk_bf16_f32 v54, v28, v29\n\t" \
  "v_cvt_pk_bf16_f32 v55, v30, v31\n\t" \
  "v_mfma_f32_16x16x32_bf16 v[32:35], v[80:83], a[152:155], v[32:35]\n\t" \
  "buffer_store_dwordx4 v[48:51], v0, %[X], s50 offen offset:0 sc1\n\t" \
  "v_mfma_f32_16x16x32_bf16 v[36:39], v[84:87], a[152:155], v[36:39]\n\t" \
  "v_mfma_f32_16x16x32_bf16 v[32:35], v[88:91], a[156:159], v[32:35]\n\t" \
  "v_mfma_f32_16x16x32_bf16 v[36:39], v[92:95], a[156:159], v[36:39]\n\t" \
  "v_mfma_f32_16x16x32_bf16 v[32:35], v[96:99], a[160:163], v[32:35]\n\t" \
  "v_mfma_f32_16x16x32_bf16 v[36:39], v[100:103], a[160:163], v[36:39]\n\t" \
  "v_mfma_f32_16x16x32_bf16 v[32:35], v[104:107], a[164:167], v[32:35]\n\t" \
  "v_mfma_f32_16x16x32_bf16 v[36:39], v[108:111], a[164:167], v[36:39]\n\t" \
  "v_mfma_f32_16x16x32_bf16 v[32:35], v[112:115], a[168:171], v[32:35]\n\t" \
  "v_mfma_f32_16x16x32_bf16 v[36:39], v[116:119], a[168:171], v[36:39]\n\t" \
  "v_mfma_f32_16x16x32_bf16 v[32:35], v[120:123], a[172:175], v[32:35]\n\t" \
  "v_mfma_f32_16x16x32_bf16 v[36:39], v[124:127], a[172:175], v[36:39]\n\t" \
  "s_waitcnt vmcnt(1)\n\t" \
  "v_cmp_eq_u32 vcc, 0, v4\n\t" \
  "v_mfma_f32_16x16x32_bf16 v[32:35], v[128:131], a[176:179], v[32:35]\n\t" \
  "v_mfma_f32_16x16x32_bf16 v[36:39], v[132:135], a[176:179], v[36:39]\n\t" \
  "s_cmp_eq_u64 vcc, 0\n\t" \
  "s_cselect_b32 s60, 1, 0\n\t" \
  "v_mov_b32 v4, s60\n\t" \
  "v_mfma_f32_16x16x32_bf16 v[32:35], v[136:139], a[180:183], v[32:35]\n\t" \
  "ds_write_b32 v5, v4\n\t" \
  "v_mfma_f32_16x16x32_bf16 v[36:39], v[140:143], a[180:183], v[36:39]\n\t" \
  "v_mfma_f32_16x16x32_bf16 v[32:35], v[144:147], a[184:187], v[32:35]\n\t" \
  "v_mfma_f32_16x16x32_bf16 v[36:39], v[148:151], a[184:187], v[36:39]\n\t" \
  "buffer_store_dwordx4 v[52:55], v0, %[X], s50 offen offset:1024 sc1\n\t" \
  "v_mfma_f32_16x16x32_bf16 v[32:35], v[152:155], a[188:191], v[32:35]\n\t" \
  "v_mfma_f32_16x16x32_bf16 v[36:39], v[156:159], a[188:191], v[36:39]\n\t" \
  "s_waitcnt lgkmcnt(0)\n\t" \
  "s_barrier\n\t" \
  "v_mfma_f32_16x16x32_bf16 v[32:35], v[160:163], a[192:195], v[32:35]\n\t" \
  "ds_read_b32 v4, v6 offset:40959\n\t" \
  "v_mfma_f32_16x16x32_bf16 v[36:39], v[164:167], a[192:195], v[36:39]\n\t" \
  "v_mfma_f32_16x16x32_bf16 v[32:35], v[168:171], a[196:199], v[32:35]\n\t" \
  "v_mfma_f32_16x16x32_bf16 v[36:39], v[172:175], a[196:199], v[36:39]\n\t" \
  "v_mfma_f32_16x16x32_bf16 v[32:35], v[176:179], a[200:203], v[32:35]\n\t" \
  "v_mfma_f32_16x16x32_bf16 v[36:39], v[180:183], a[200:203], v[36:39]\n\t" \
  "s_waitcnt lgkmcnt(0)\n\t" \
  "v_readfirstlane_b32 s60, v4\n\t" \
  "s_add_u32 s55, s83, s36\n\t" \
  "v_mfma_f32_16x16x32_bf16 v[32:35], v[184:187], a[204:207], v[32:35]\n\t" \
  "s_cmp_eq_u32 s48, 63\n\t" \
  "s_cselect_b32 s56, 0, s60\n\t" \
  "v_mfma_f32_16x16x32_bf16 v[36:39], v[188:191], a[204:207], v[36:39]\n\t" \
  "s_cmp_eq_u32 s56, 0\n\t" \
  "s_cselect_b32 s55, 0xf0000000, s55\n\t" \
  "v_mfma_f32_16x16x32_bf16 v[32:35], v[192:195], a[208:211], v[32:35]\n\t" \
  "v_mfma_f32_16x16x32_bf16 v[36:39], v[196:199], a[208:211], v[36:39]\n\t" \
  "v_mfma_f32_16x16x32_bf16 v[32:35], v[200:203], a[212:215], v[32:35]\n\t" \
  "s_add_u32 m0, s46, 0\n\t" \
  "s_add_u32 s47, s55, 0\n\t" \
  "buffer_load_dwordx4 v0, %[X], s47 offen sc1 lds\n\t" \
  "v_mfma_f32_16x16x32_bf16 v[36:39], v[204:207], a[212:215], v[36:39]\n\t" \
  "v_mfma_f32_16x16x32_bf16 v[40:43], v[64:67], a[216:219], 0\n\t" \
  "v_mfma_f32_16x16x32_bf16 v[44:47], v[68:71], a[216:219], 0\n\t" \
  "v_mfma_f32_16x16x32_bf16 v[40:43], v[72:75], a[220:223], v[40:43]\n\t" \
  "v_cvt_pk_bf16_f32 v56, v32, v33\n\t" \
  "v_cvt_pk_bf16_f32 v57, v34, v35\n\t" \
  "s_add_u32 m0, s46, 4096\n\t" \
  "s_add_u32 s47, s55, 4096\n\t" \
  "buffer_load_dwordx4 v0, %[X], s47 offen sc1 lds\n\t" \
  "v_mfma_f32_16x16x32_bf16 v[44:47], v[76:79], a[220:223], v[44:47]\n\t" \
  "v_cvt_pk_bf16_f32 v58, v36, v37\n\t" \
  "v_cvt_pk_bf16_f32 v59, v38, v39\n\t" \
  "v_mfma_f32_16x16x32_bf16 v[40:43], v[80:83], a[224:227], v[40:43]\n\t" \
  "v_mfma_f32_16x16x32_bf16 v[44:47], v[84:87], a[224:227], v[44:47]\n\t" \
  "buffer_store_dwordx4 v[56:59], v0, %[X], s50 offen offset:2048 sc1\n\t" \
  "v_mfma_f32_16x16x32_bf16 v[40:43], v[88:91], a[228:231], v[40:43]\n\t" \
  "s_add_u32 m0, s46, 8192\n\t" \
  "s_add_u32 s47, s55, 16384\n\t" \
  "buffer_load_dwordx4 v0, %[X], s47 offen sc1 lds\n\t" \
  "v_mfma_f32_16x16x32_bf16 v[44:47], v[92:95], a[228:231], v[44:47]\n\t" \
  "v_mfma_f32_16x16x32_bf16 v[40:43], v[96:99], a[232:235], v[40:43]\n\t" \
  "v_mfma_f32_16x16x32_bf16 v[44:47], v[100:103], a[232:235], v[44:47]\n\t" \
  "v_mfma_f32_16x16x32_bf16 v[40:43], v[104:107], a[236:239], v[40:43]\n\t" \
  "s_add_u32 m0, s46, 12288\n\t" \
  "s_add_u32 s47, s55, 20480\n\t" \
  "buffer_load_dwordx4 v0, %[X], s47 offen sc1 lds\n\t" \
  "v_mfma_f32_16x16x32_bf16 v[44:47], v[108:111], a[236:239], v[44:47]\n\t" \
  "v_mfma_f32_16x16x32_bf16 v[40:43], v[112:115], a[240:243], v[40:43]\n\t" \
  "v_mfma_f32_16x16x32_bf16 v[44:47], v[116:119], a[240:243], v[44:47]\n\t" \
  "v_mfma_f32_16x16x32_bf16 v[40:43], v[120:123], a[244:247], v[40:43]\n\t" \
  "s_add_u32 m0, s46, 16384\n\t" \
  "s_add_u32 s47, s55, 32768\n\t" \
  "buffer_load_dwordx4 v0, %[X], s47 offen sc1 lds\n\t" \
  "v_mfma_f32_16x16x32_bf16 v[44:47], v[124:127], a[244:247], v[44:47]\n\t" \
  "v_mfma_f32_16x16x32_bf16 v[40:43], v[128:131], a[248:251], v[40:43]\n\t" \
  "v_mfma_f32_16x16x32_bf16 v[44:47], v[132:135], a[248:251], v[44:47]\n\t" \
  "v_mfma_f32_16x16x32_bf16 v[40:43], v[136:139], a[252:255], v[40:43]\n\t" \
  "s_add_u32 m0, s46, 20480\n\t" \
  "s_add_u32 s47, s55, 36864\n\t" \
  "buffer_load_dwordx4 v0, %[X], s47 offen sc1 lds\n\t" \
  "v_mfma_f32_16x16x32_bf16 v[44:47], v[140:143], a[252:255], v[44:47]\n\t" \
  "v_mfma_f32_16x16x32_bf16 v[40:43], v[144:147], v[224:227], v[40:43]\n\t" \
  "v_mfma_f32_16x16x32_bf16 v[44:47], v[148:151], v[224:227], v[44:47]\n\t" \
  "v_mfma_f32_16x16x32_bf16 v[40:43], v[152:155], v[228:231], v[40:43]\n\t" \
  "s_add_u32 m0, s46, 24576\n\t" \
  "s_add_u32 s47, s55, 49152\n\t" \
  "buffer_load_dwordx4 v0, %[X], s47 offen sc1 lds\n\t" \
  "v_mfma_f32_16x16x32_bf16 v[44:47], v[156:159], v[228:231], v[44:47]\n\t" \
  "v_mfma_f32_16x16x32_bf16 v[40:43], v[160:163], v[232:235], v[40:43]\n\t" \
  "v_mfma_f32_16x16x32_bf16 v[44:47], v[164:167], v[232:235], v[44:47]\n\t" \
  "v_mfma_f32_16x16x32_bf16 v[40:43], v[168:171], v[236:239], v[40:43]\n\t" \
  "s_add_u32 m0, s46, 28672\n\t" \
  "s_add_u32 s47, s55, 53248\n\t" \
  "buffer_load_dwordx4 v0, %[X], s47 offen sc1 lds\n\t" \
  "v_mfma_f32_16x16x32_bf16 v[44:47], v[172:175], v[236:239], v[44:47]\n\t" \
  "v_mfma_f32_16x16x32_bf16 v[40:43], v[176:179], v[240:243], v[40:43]\n\t" \
  "v_mfma_f32_16x16x32_bf16 v[44:47], v[180:183], v[240:243], v[44:47]\n\t" \
  "v_mfma_f32_16x16x32_bf16 v[40:43], v[184:187], v[244:247], v[40:43]\n\t" \
  "s_add_u32 m0, s46, 32768\n\t" \
  "s_add_u32 s47, s55, 65536\n\t" \
  "buffer_load_dwordx4 v0, %[X], s47 offen sc1 lds\n\t" \
  "v_mfma_f32_16x16x32_bf16 v[44:47], v[188:191], v[244:247], v[44:47]\n\t" \
  "v_mfma_f32_16x16x32_bf16 v[40:43], v[192:195], v[248:251], v[40:43]\n\t" \
  "v_mfma_f32_16x16x32_bf16 v[44:47], v[196:199], v[248:251], v[44:47]\n\t" \
  "buffer_load_dword v8, off, %[X], s57 sc1\n\t" \
  "v_mfma_f32_16x16x32_bf16 v[40:43], v[200:203], v[252:255], v[40:43]\n\t" \
  "v_mfma_f32_16x16x32_bf16 v[44:47], v[204:207], v[252:255], v[44:47]\n\t" \
  "s_nop 4\n\t" \
  "v_cvt_pk_bf16_f32 v60, v40, v41\n\t" \
  "v_cvt_pk_bf16_f32 v61, v42, v43\n\t" \
  "s_nop 1\n\t" \
  "v_cvt_pk_bf16_f32 v62, v44, v45\n\t" \
  "v_cvt_pk_bf16_f32 v63, v46, v47\n\t" \
  "buffer_store_dwordx4 v[60:63], v0, %[X], s50 offen offset:3072 sc1\n\t" \
  TK_PQ2 \
  "s_add_u32 s49, s82, s38\n\t" \
  "s_cmp_eq_u32 s48, 63\n\t" \
  "s_cbranch_scc1 LEND_%=\n\t" \
  "s_add_u32 s48, s48, 1\n\t" \
  "s_cmp_eq_u32 s56, 0\n\t" \
  "s_cbranch_scc1 LCOLD_%=\n\t" \
  "s_waitcnt vmcnt(0)\n\t" \
  "s_mov_b64 exec, 1\n\t" \
  "buffer_store_dword v6, off, %[X], s49 sc1\n\t" \
  "s_mov_b64 exec, -1\n\t" \
  "s_mov_b32 s49, 0xf0000000\n\t" \
  "s_cmp_lt_i32 s57, 0\n\t" \
  "s_cbranch_scc1 LTOP_%=\n\t" \
  "s_movk_i32 s63, 4000\n\t" \
  "s_branch LGW_%=u\n\t" \
  "LGW_%=:\n\t" \
  "s_sleep " TK_PSG "\n\t" \
  "buffer_load_dword v8, off, %[X], s57 sc1\n\t" \
  "s_waitcnt vmcnt(0)\n\t" \
  "LGW_%=u:\n\t" \
  "s_memrealtime s[52:53]\n\t" \
  "v_readfirstlane_b32 s60, v8\n\t" \
  "s_waitcnt lgkmcnt(0)\n\t" \
  "s_sub_u32 s63, s63, 1\n\t" \
  "s_cbranch_scc1 LTOP_%=\n\t" \
  "s_cmp_eq_u32 s60, -1\n\t" \
  "s_cbranch_scc1 LGW_%=\n\t" \
  "s_not_b32 s60, s60\n\t" \
  "s_lshl_b32 s61, s52, 8\n\t" \
  "s_sub_u32 s60, s60, s61\n\t" \
  "s_ashr_i32 s60, s60, 8\n\t" \
  "s_sub_i32 s60, s60, " TK_QXW "\n\t" \
  "s_min_i32 s60, s60, 400\n\t" \
  "s_add_u32 s54, s52, s60\n\t" \
  "LGW_%=w:\n\t" \
  "s_sleep 1\n\t" \
  "s_memrealtime s[52:53]\n\t" \
  "s_waitcnt lgkmcnt(0)\n\t" \
  "s_sub_i32 s53, s52, s54\n\t" \
  "s_cmp_lt_i32 s53, 0\n\t" \
  "s_cbranch_scc1 LGW_%=w\n\t" \
  "s_branch LTOP_%=\n\t" \
  "LEND_%=:\n\t" \
  "s_waitcnt vmcnt(0)\n\t" \
  "s_mov_b64 exec, 1\n\t" \
  "buffer_store_dword v6, off, %[X], s49 sc1\n\t" \
  "s_mov_b64 exec, -1\n\t" \
  "s_mov_b32 s49, 0xf0000000\n\t" \
  TK_PQ0 \
  ""
__device__ __forceinline__ unsigned hoff(int r, int piece) {
  const int m = 4 * (r >> 3) + (r & 3), kp = piece >> 3;
  return (P_H + (unsigned)(kp >> 1)) * PAIR + (unsigned)((kp & 1) * 4096 + (16 * ((r >> 2) & 1) + m) * 128 + (((piece ^ m) & 7) << 4));
}
template <int SLEEP>
__device__ __forceinline__ void wait_all(rsrc_t X, unsigned vo, unsigned so) {
  unsigned spins = 0;
  while (true) {
    asm volatile("" ::: "memory");
    if (__all(ld4(X, vo, so) != 0u)) break;
    if (spin_fail(spins, X)) break;
    __builtin_amdgcn_s_sleep(SLEEP);
  }
  asm volatile("" ::: "memory");
}
#define VQ_CLOB "v8", "v16", "v17", "v18", "v19", "v20", "v21", "v22", "v23", "v24", "v25", "v26", "v27", "v28", "v29", "v30", "v31", "v32", "v33", "v34", "v35", "v36", "v37", "v38", "v39", "v40", "v41", "v42", "v43", "v44", "v45", "v46", "v47", "v48", "v49", "v50", "v51", "v52", "v53", "v54", "v55", "v56", "v57", "v58", "v59", "v60", "v61", "v62", "v63", "v64", "v65", "v66", "v67", "v68", "v69", "v70", "v71", "v72", "v73", "v74", "v75", "v76", "v77", "v78", "v79", "v80", "v81", "v82", "v83", "v84", "v85", "v86", "v87", "v88", "v89", "v90", "v91", "v92", "v93", "v94", "v95", "v96", "v97", "v98", "v99", "v100", "v101", "v102", "v103", "v104", "v105", "v106", "v107", "v108", "v109", "v110", "v111", "v112", "v113", "v114", "v115", "v116", "v117", "v118", "v119", "v120", "v121", "v122", "v123", "v124", "v125", "v126", "v127", "v128", "v129", "v130", "v131", "v132", "v133", "v134", "v135", "v136", "v137", "v138", "v139", "v140", "v141", "v142", "v143", "v144", "v145", "v146", "v147", "v148", "v149", "v150", "v151", "v152", "v153", "v154", "v155", "v156", "v157", "v158", "v159", "v160", "v161", "v162", "v163", "v164", "v165", "v166", "v167", "v168", "v169", "v170", "v171", "v172", "v173", "v174", "v175", "v176", "v177", "v178", "v179", "v180", "v181", "v182", "v183", "v184", "v185", "v186", "v187", "v188", "v189", "v190", "v191", "v192", "v193", "v194", "v195", "v196", "v197", "v198", "v199", "v200", "v201", "v202", "v203", "v204", "v205", "v206", "v207", "v220", "v221"
#define VA_CLOB "v40", "v41", "v42", "v43", "v44", "v45", "v46", "v47", "v48", "v49", "v50", "v51", "v52", "v53", "v54", "v55", "v56", "v57", "v58", "v59", "v60", "v61", "v62", "v63", "v64", "v65", "v66", "v67", "v68", "v69", "v70", "v71", "v72", "v73", "v74", "v75", "v76", "v77", "v78", "v79", "v80", "v81", "v82", "v83", "v84", "v85", "v86", "v87", "v88", "v89", "v90", "v91", "v92", "v93", "v94", "v95", "v96", "v97", "v98", "v99", "v100", "v101", "v102", "v103", "v104", "v105", "v106", "v107", "v108", "v109", "v110", "v111", "v112", "v113", "v114", "v115", "v116", "v117", "v118", "v119", "v120", "v121", "v122", "v123", "v124", "v125", "v126", "v127", "v128", "v129", "v130", "v131", "v132", "v133", "v134", "v135", "v136", "v137", "v138", "v139", "v140", "v141", "v142", "v143", "v144", "v145", "v146", "v147", "v148", "v149", "v150", "v151", "v152", "v153", "v154", "v155", "v156", "v157", "v158", "v159", "v160", "v161", "v162", "v163", "v164", "v165", "v166", "v167", "v168", "v169", "v170", "v171", "v172", "v173", "v174", "v175", "v176", "v177", "v178", "v179", "v180", "v181", "v182", "v183", "v184", "v185", "v186", "v187", "v188", "v189", "v190", "v191", "v192", "v193", "v194", "v195", "v196", "v197", "v198", "v199", "v200", "v201", "v202", "v203", "v204", "v205", "v206", "v207", "v208", "v209", "v210", "v211", "v220", "v221"
#define TOK_COMMON() \
  const MP p = getp(); \
  const int c = (int)blockIdx.x - NCHW, xq = c & 3, L = (c >> 2) & 1, i = c >> 3; \
  const int tid = threadIdx.x, lane = tid & 63, w = __builtin_amdgcn_readfirstlane(tid >> 6); \
  const rsrc_t X = mkr(p.xb, XB_BYTES)
#define W_IN [W] "s"(WR)
#define W_CLOB "a0", "a1", "a2", "a3", "a4", "a5", "a6", "a7", "a8", "a9", "a10", "a11", "a12", "a13", "a14", "a15", "a16", "a17", "a18", "a19", "a20", "a21", "a22", "a23", "a24", "a25", "a26", "a27", "a28", "a29", "a30", "a31", "a32", "a33", "a34", "a35", "a36", "a37", "a38", "a39", "a40", "a41", "a42", "a43", "a44", "a45", "a46", "a47", "a48", "a49", "a50", "a51", "a52", "a53", "a54", "a55", "a56", "a57", "a58", "a59", "a60", "a61", "a62", "a63", "a64", "a65", "a66", "a67", "a68", "a69", "a70", "a71", "a72", "a73", "a74", "a75", "a76", "a77", "a78", "a79", "a80", "a81", "a82", "a83", "a84", "a85", "a86", "a87", "a88", "a89", "a90", "a91", "a92", "a93", "a94", "a95", "a96", "a97", "a98", "a99", "a100", "a101", "a102", "a103", "a104", "a105", "a106", "a107", "a108", "a109", "a110", "a111", "a112", "a113", "a114", "a115", "a116", "a117", "a118", "a119", "a120", "a121", "a122", "a123", "a124", "a125", "a126", "a127", "a128", "a129", "a130", "a131", "a132", "a133", "a134", "a135", "a136", "a137", "a138", "a139", "a140", "a141", "a142", "a143", "a144", "a145", "a146", "a147", "a148", "a149", "a150", "a151", "a152", "a153", "a154", "a155", "a156", "a157", "a158", "a159", "a160", "a161", "a162", "a163", "a164", "a165", "a166", "a167", "a168", "a169", "a170", "a171", "a172", "a173", "a174", "a175", "a176", "a177", "a178", "a179", "a180", "a181", "a182", "a183", "a184", "a185", "a186", "a187", "a188", "a189", "a190", "a191", "a192", "a193", "a194", "a195", "a196", "a197", "a198", "a199", "a200", "a201", "a202", "a203", "a204", "a205", "a206", "a207", "a208", "a209", "a210", "a211", "a212", "a213", "a214", "a215", "a216", "a217", "a218", "a219", "a220", "a221", "a222", "a223", "a224", "a225", "a226", "a227", "a228", "a229", "a230", "a231", "a232", "a233", "a234", "a235", "a236", "a237", "a238", "a239", "a240", "a241", "a242", "a243", "a244", "a245", "a246", "a247", "a248", "a249", "a250", "a251", "a252", "a253", "a254", "a255", "v224", "v225", "v226", "v227", "v228", "v229", "v230", "v231", "v232", "v233", "v234", "v235", "v236", "v237", "v238", "v239", "v240", "v241", "v242", "v243", "v244", "v245", "v246", "v247", "v248", "v249", "v250", "v251", "v252", "v253", "v254", "v255"
#define BOOT_IN "{s67}"(L), [ctab] "i"(CTAB_OFF), [rm] "i"(SRING == 12 ? 12 : 0), [trm] "i"(TRING - 1), [trn] "i"(1 - TRING)
#define S_CLOB "s80", "s81", "s76", "s78", "s79", "s86", "s88", "s89", "s90", "s91", "s46", "s47", "s48", "s49", "s50", "s51", "s52", "s53", "s54", "s55", "s56", "s57", "s58", "s59", "s60", "s61", "s62", "s63", "s68", "s69", "s70", "s71", "s72", "s73", "s82", "s83", "s84", "m0", "vcc", "scc", "memory"
typedef unsigned v8w __attribute__((ext_vector_type(8)));

__device__ __forceinline__ void qkv_role() {
  TOK_COMMON();
  const int role = i * 4 + w, hh = 8 * xq + role / 6, part = role % 6, which = part >> 1, half = part & 1;
  const rsrc_t WR = mkr((which == 0 ? p.Wq : (which == 1 ? p.Wk : p.Wv)) + hh * DH + 64 * half, DM * HD * 2u - (unsigned)(hh * DH + 64 * half) * 2u);
  const int m = lane & 15, g = lane >> 4;
  const unsigned a0 = (unsigned)(m * 128 + ((g ^ (m & 7)) << 4));
  v8w lv = {(unsigned)lane * 16u, a0, a0 ^ 64u, (unsigned)(lane & (L ? 15 : 3)) * 128u, 0u, 40960u + 4u * (unsigned)w, 1u, 0u};
  const unsigned c36 = XQL + P_H * PAIR + (unsigned)w * 1024u, c37 = XQ + (unsigned)(3 * hh + which) * PAIR + (unsigned)half * (which == 2 ? 4096u : 2048u);
  const unsigned c38 = P_FA * PAIR + (unsigned)(hh * 128 + part * 16), c39 = w == 0 ? P_FB * PAIR + 4096u : 0xF0000000u, c41 = (unsigned)w * 1024u;
  const int c42 = L ? 1 - QLEAD1 : 1 - QLEAD;
  unsigned d74 = TRC_OFF + 262144u + (unsigned)((c * 4 + w) * 66) * 8u;
  TT(64, 0);
#define Q_CALL(A) asm volatile("s_waitcnt vmcnt(0) lgkmcnt(0)\n\t" A : "+{v[0:7]}"(lv), "+{s74}"(d74) \
               : [X] "s"(X), "{s36}"(c36), "{s37}"(c37), "{s38}"(c38), "{s39}"(c39), "{s40}"(w), "{s41}"(c41), "{s42}"(c42), "{s45}"(i), "{s92}"(TMO_OFF), BOOT_IN, [cap] "i"(SPIN_CAP), [qxw] "i"(QXW), [qxc] "i"(QXC), W_IN : VQ_CLOB, W_CLOB, S_CLOB)
  if (which == 2) { Q_CALL(QV_ASM "LQVEND:"); __builtin_amdgcn_endpgm(); }
  Q_CALL(QK_ASM "LQKEND:");
  __builtin_amdgcn_endpgm();
}

__device__ __forceinline__ void ap_role() {
  TOK_COMMON();
  const int hp = 4 * xq + ((i - 12) >> 1), ch = i & 1, a = w >> 1, rt = w & 1, h = 2 * hp + a;
  const int n = lane & 15, g = lane >> 4;
  const rsrc_t WR = mkr(p.Wo + (long)hp * 2 * DH * DM, 2u * DH * DM * 2u);
  const unsigned l16 = (unsigned)lane * 16u, ab = (unsigned)a * 24576u + l16;
  v16u lv = {l16, ab, ab + (unsigned)rt * 4096u, 49152u + (unsigned)w * 4096u + l16, 49152u + (unsigned)(w ^ 2) * 4096u + l16, l16 & ~32u, l16 >> 1,
             (unsigned)(g * 262144 + n * 16), (unsigned)(n * 32 + g * 8), (unsigned)((lane & 7) % 6) * 16u, 0u, 0u, 0u, 0u, 0u, 0u};
  v8w mk;
  const int qi = 8 * (n >> 2) + (n & 3) + 4 * rt;
#pragma unroll
  for (int k = 0; k < 8; ++k) mk[k] = 8 * g + k > qi ? 0xFF800000u : 0u;
  v16u zz = {0u, 0u, 0u, 0u, 0u, 0u, 0u, 0u, 0u, 0u, 0u, 0u, 0u, 0u, 0u, 0u};
  const unsigned tb = XAL + (unsigned)(3 * h) * PAIR;
  const unsigned c36 = tb + (rt ? PAIR + 4096u : 0u), c37 = tb + (rt ? 2u * PAIR : 4096u), c38 = tb + (rt ? 2u * PAIR + 4096u : PAIR);
  const unsigned c39 = XK + (P_KV + (unsigned)(h >> 1)) * PAIR + (unsigned)(h & 1) * 4096u, c41 = P_FA * PAIR + (unsigned)h * 128u;
  const unsigned c42 = w == 0 ? P_FB * PAIR + (unsigned)(2 * hp + ch) * 128u : 0xF0000000u;
  const unsigned c43 = XA + TP0 * XBLK + (unsigned)(8 * rt + (hp >> 3)) * PAIR + (unsigned)((hp & 7) * 1024 + ch * 512 + a * 256);
  unsigned d44 = XT + (TP0 + 8u) * XBLK + (unsigned)(hp >> 1) * PAIR + (unsigned)((((hp & 1) * 4 + 2 * ch + a) * 2 + rt) * 512);
  const unsigned c45 = (unsigned)(a * 24576 + rt * 12288);
  unsigned d74 = TRC_OFF + 262144u + (unsigned)((c * 4 + w) * 66) * 8u;
  asm volatile("s_waitcnt vmcnt(0) lgkmcnt(0)\n\t" AP_ASM "LAPEND:" : "+{v[0:15]}"(lv), "+{v[24:39]}"(zz), "+{s74}"(d74), "+{s44}"(d44)
               : [X] "s"(X), "{s36}"(c36), "{s37}"(c37), "{s38}"(c38), "{s39}"(c39), "{s41}"(c41), "{s42}"(c42), "{s43}"(c43), "{s45}"(c45), "{s64}"(512 * ch + 256 * a), "{s65}"(1024 + 64 * ch + 32 * a), "{s66}"(147456 * a), "{s77}"(L ? AX1 : AX), BOOT_IN,
                 "{s92}"(TMO_OFF), [cap] "i"(SPIN_CAP), "{v[16:23]}"(mk), W_IN : VA_CLOB, W_CLOB, S_CLOB);
  __builtin_amdgcn_endpgm();
}

template <int N> __device__ __forceinline__ float rorN(float v) { return __uint_as_float((unsigned)__builtin_amdgcn_update_dpp(0, (int)__float_as_uint(v), 0x120 + N, 0xf, 0xf, false)); }
__device__ __forceinline__ void up8(v4u v, float* x) {
  x[0] = lo2f(v.x); x[1] = hi2f(v.x); x[2] = lo2f(v.y); x[3] = hi2f(v.y); x[4] = lo2f(v.z); x[5] = hi2f(v.z); x[6] = lo2f(v.w); x[7] = hi2f(v.w);
}
__device__ __forceinline__ v4u pk8(const float* x) { return (v4u){pk2(x[0], x[1]), pk2(x[2], x[3]), pk2(x[4], x[5]), pk2(x[6], x[7])}; }

__device__ __forceinline__ void rb9(float (&x)[9]) {
#pragma unroll
  for (int i = 0; i < 8; i += 2) { const unsigned u = pk2(x[i], x[i + 1]); x[i] = lo2f(u); x[i + 1] = hi2f(u); }
  x[8] = lo2f(pk2(x[8], x[8]));
}
template <int R>
__device__ __forceinline__ void lnR(const float (&x)[R][9], float (&o)[R][9]) {
  float s[2 * R], pv[R];
#pragma unroll
  for (int r = 0; r < R; ++r) {
    pv[r] = __uint_as_float(__builtin_amdgcn_readfirstlane(__float_as_uint(x[r][0])));
    const f32x2 p2 = {pv[r], pv[r]};
    f32x2 d[4];
#pragma unroll
    for (int i = 0; i < 4; ++i) d[i] = (f32x2){x[r][2 * i], x[r][2 * i + 1]} - p2;
    const float d8 = x[r][8] - pv[r];
    const f32x2 a = (d[0] + d[1]) + (d[2] + d[3]);
    const f32x2 q = __builtin_elementwise_fma(d[3], d[3], __builtin_elementwise_fma(d[2], d[2], __builtin_elementwise_fma(d[1], d[1], d[0] * d[0])));
    s[2 * r] = (a[0] + d8) + a[1];
    s[2 * r + 1] = __builtin_fmaf(d8, d8, q[0]) + q[1];
  }
#pragma unroll
  for (int k = 0; k < 2 * R; ++k) s[k] += rorN<8>(s[k]);
#pragma unroll
  for (int k = 0; k < 2 * R; ++k) s[k] += rorN<4>(s[k]);
#pragma unroll
  for (int k = 0; k < 2 * R; ++k) s[k] += rorN<2>(s[k]);
#pragma unroll
  for (int k = 0; k < 2 * R; ++k) s[k] += rorN<1>(s[k]);
#pragma unroll
  for (int r = 0; r < R; ++r) {
    auto u = __builtin_amdgcn_permlane16_swap(__float_as_uint(s[2 * r]), __float_as_uint(s[2 * r + 1]), false, false);
    s[2 * r] = __uint_as_float(u[0]) + __uint_as_float(u[1]);
  }
#pragma unroll
  for (int r = 0; r < R; ++r) {
    const unsigned u = __float_as_uint(s[2 * r]);
    const float md = (__uint_as_float(__builtin_amdgcn_readlane(u, 0)) + __uint_as_float(__builtin_amdgcn_readlane(u, 32))) * (1.0f / DM);
    const float sq = (__uint_as_float(__builtin_amdgcn_readlane(u, 16)) + __uint_as_float(__builtin_amdgcn_readlane(u, 48))) * (1.0f / DM);
    const unsigned mv = pk2(pv[r] + md, __builtin_fmaxf(__builtin_fmaf(-md, md, sq), 0.f));
    const float m = lo2f(mv), var = hi2f(mv);
    const float ve = lo2f(pk2(var + 1e-5f, 0.f));
    const float den = lo2f(pk2(__builtin_amdgcn_sqrtf(ve), 0.f));
    const float inv = __builtin_amdgcn_rcpf(den);
#pragma unroll
    for (int i = 0; i < 9; ++i) o[r][i] = (x[r][i] - m) * inv;
  }
}
__device__ __forceinline__ void ln9(const float (&x)[9], float (&o)[9]) { lnR<1>((const float (&)[1][9])x, (float (&)[1][9])o); }
__device__ __forceinline__ v4u gat8(float v) {
  const unsigned d = pk2(v, __uint_as_float((unsigned)__builtin_amdgcn_mov_dpp((int)__float_as_uint(v), 0xB1, 0xf, 0xf, true)));
  return (v4u){d, (unsigned)__builtin_amdgcn_mov_dpp((int)d, 0x102, 0xf, 0xf, true), (unsigned)__builtin_amdgcn_mov_dpp((int)d, 0x104, 0xf, 0xf, true),
               (unsigned)__builtin_amdgcn_mov_dpp((int)d, 0x106, 0xf, 0xf, true)};
}
__device__ __forceinline__ void tok_row(const MP& p, const rsrc_t X, unsigned sb1, int L, int s, int r, int lane, const v4u (&v)[18], v4u px, v4u py, unsigned px9, unsigned py9) {
  const long tok = (long)s * SEG + r;
  const int m = lane & 15, g = lane >> 4;
  const int xp = 4 * (g & 1) + 2 * (g >> 1) + (m >> 3);
  const unsigned zr = opq(0u);
  const unsigned one = (g == (m >> 2)) ? (opq(0x3F80u) << (16 * (m & 1))) : zr;
  const unsigned ax = (m & 2) ? zr : one, ay = (m & 2) ? one : zr;
  const v4u A1 = {ax, ay, zr, zr}, A2 = {zr, zr, ax, ay};
  const unsigned on2 = opq(0x3F80u) << (16 * (m & 1));
  const int q2 = (m >> 1) & 3;
  const v4u sel = {q2 == 0 ? on2 : zr, q2 == 1 ? on2 : zr, q2 == 2 ? on2 : zr, q2 == 3 ? on2 : zr};
  f32x4 d1, d2, e1;
  asm volatile("s_nop 1\n\t" MF "%0, %1, %3, 0\n\t" MF "%0, %2, %3, %0" : "=&v"(e1) : "v"(v[0]), "v"(v[1]), "v"(sel));
#pragma unroll
  for (int i = 0; i < 16; ++i) {
    if (i == 0) asm volatile("s_nop 1\n\t" MF "%0, %2, %4, 0\n\t" MF "%1, %3, %4, 0" : "=&v"(d1), "=&v"(d2) : "v"(A1), "v"(A2), "v"(v[2]));
    else asm volatile(MF "%0, %2, %4, %0\n\t" MF "%1, %3, %4, %1" : "+v"(d1), "+v"(d2) : "v"(A1), "v"(A2), "v"(v[2 + i]));
  }
  asm volatile("s_nop 7" : "+v"(d1), "+v"(d2), "+v"(e1));
  float o[9];
#pragma unroll
  for (int i = 0; i < 4; ++i) { o[i] = d1[i]; o[4 + i] = d2[i]; }
  {
    auto a02 = __builtin_amdgcn_permlane32_swap(__float_as_uint(e1[0]), __float_as_uint(e1[2]), false, false);
    auto a13 = __builtin_amdgcn_permlane32_swap(__float_as_uint(e1[1]), __float_as_uint(e1[3]), false, false);
    o[8] = m >= 8 ? __uint_as_float(a13[0]) + __uint_as_float(a13[1]) : __uint_as_float(a02[0]) + __uint_as_float(a02[1]);
  }
  float x[9];
  up8(px, x); x[8] = lo2f(px9);
#pragma unroll
  for (int i = 0; i < 9; ++i) x[i] += o[i];
  if (L == 0) {
    float y[9], ln[9];
    up8(py, y); y[8] = lo2f(py9);
    rb9(x);
    ln9(x, ln);
#pragma unroll
    for (int i = 0; i < 9; ++i) x[i] = __builtin_fmaf(ln[i], y[i], x[i]);
    rb9(x);
    st16(X, (unsigned)lane * 16u, sb1 + (P_X3 + (unsigned)(r >> 3)) * PAIR + (unsigned)(r & 7) * 1024u, pk8(x));
    const v4u x9 = gat8(x[8]);
    if ((lane & 7) == 0) st16(X, (unsigned)xp * 16u, sb1 + (P_H + 4u) * PAIR + 4096u + (unsigned)r * 128u, x9);
    ln9(x, ln);
    st16(X, hoff(r, lane), sb1, pk8(ln));
    const v4u h9 = gat8(ln[8]);
    if ((lane & 7) == 0) st16(X, hoff(r, 64 + xp), sb1, h9);
  } else {
    const rsrc_t O = mkr(p.out + tok * DM, DM * 2u);
    __builtin_amdgcn_raw_buffer_store_b128(pk8(x), O, (unsigned)lane * 16u, 0u, OAUX);
    __builtin_amdgcn_raw_buffer_store_b128(gat8(x[8]), O, (lane & 7) == 0 ? 1024u + (unsigned)xp * 16u : 0xF0000000u, 0u, OAUX);
  }
}

__device__ __forceinline__ void row_role(char* smem) {
  TOK_COMMON();
  const int rw = 4 * xq + (i - 20), r = 2 * rw + (w >> 1);
  const unsigned long long sm = die_mask(X, SG0, L, lane);
  const unsigned tm = (unsigned)die_mask(X, TP0, L, lane), dm = (unsigned)die_mask(X, 0u, L, lane);
  __attribute__((address_space(3))) unsigned* cnt = (__attribute__((address_space(3))) unsigned*)smem;
  BAR();
  if (tid < 64) cnt[tid] = 0u;
  BAR();
#define ANCT 25000
  for (int s = w & 1; s < NSEG; s += 2) {
    int ln_ = lane; asm volatile("" : "+v"(ln_));
    const int m = ln_ & 15, g = ln_ >> 4;
    const unsigned l16 = (unsigned)ln_ * 16u, xo = (unsigned)(8 * (4 * (g & 1) + 2 * (g >> 1) + (m >> 3)) + (m & 7)) * 2u;
    const unsigned sb = SEGB(sm, s), sb1 = sb ^ (L ? 0u : 8192u);
    if (s + ARMK < NSEG) {
      const unsigned sv = opq(SENT), sl = SLOT_AT((s + ARMK) % SRING) + ((dm >> ((s + ARMK) % SRING / 3)) & 1u) * 8192u;
      for (int j = r; j < 84; j += 32) {
#pragma unroll
        for (int k = 0; k < 4; ++k) st16(X, l16 + (unsigned)k * 1024u, sl + (unsigned)(j >> 1) * PAIR + (unsigned)(j & 1) * 4096u, (v4u){sv, sv, sv, sv});
      }
    }
    wait_all<PSL>(X, 0u, sb + P_FB * PAIR + 4096u + (unsigned)(L ? rw : r >> 3) * 128u);
    const v4u px = ld16(X, l16, sb + (P_X3 + (unsigned)(r >> 3)) * PAIR + (unsigned)(r & 7) * 1024u);
    const unsigned px9 = __builtin_amdgcn_raw_buffer_load_b16(X, xo, sb + (P_H + 4u) * PAIR + 4096u + (unsigned)r * 128u, 16);
    const rsrc_t YX = mkr(p.yT + (2 * s + (r >> 4)) * 9216, L ? 0u : 18432u);
    const v2u ya = ld8(YX, (unsigned)(ln_ * 256 + ((r ^ m) & 15) * 8), 0u), yb = ld8(YX, (unsigned)(ln_ * 256 + 128 + ((r ^ m) & 15) * 8), 0u);
    const v4u py = {ya.x, ya.y, yb.x, yb.y};
    const unsigned py9 = __builtin_amdgcn_raw_buffer_load_b16(YX, 16384u + (xo >> 3) * 128u + (((unsigned)r ^ (xo >> 4)) & 15u) * 8u + (xo & 6u), 0u, 16);
    wait_all<PSP>(X, 0u, sb + P_FB * PAIR + (unsigned)r * 128u);
    __builtin_amdgcn_s_sleep(RONE);
    wait_all<PSP>(X, (unsigned)(ln_ & 31) * 128u, sb + P_FB * PAIR);
    TT(s, 0);
    const unsigned j = (unsigned)s & 15u, to = XRL + TP0 * XBLK + (j << 20) + ((tm >> (j >> 1)) & 1u) * 8192u + (unsigned)r * 2u * PAIR;
    const unsigned t9 = (TP0 + 8u) * XBLK + ((tm >> 8) & 1u) * 8192u + (j << 17) + (unsigned)(((r >> 2) & 1) * 512 + (4 * (r >> 3) + (r & 3)) * 32);
    const unsigned v9 = (unsigned)(g * (int)PAIR + (m >> 1) * 1024 + (m & 1) * 16);
    v4u v[18];
#pragma unroll
    for (int k = 0; k < 2; ++k) v[k] = ld16(X, v9, t9 + (unsigned)k * 4u * PAIR);
#pragma unroll
    for (int k = 0; k < 16; ++k) v[2 + k] = ld16(X, l16 + (unsigned)(k & 3) * 1024u, to + (unsigned)(k >> 3) * PAIR + (unsigned)(k & 4) * 1024u);
    SB();
    tok_row(p, X, sb1, L, s, r, ln_, v, px, py, px9, py9);
    VM0();
    unsigned o1 = 0u;
    if (ln_ == 0) o1 = __hip_atomic_fetch_add(cnt + s, 1u, __ATOMIC_RELAXED, __HIP_MEMORY_SCOPE_WORKGROUP);
    if (__builtin_amdgcn_readfirstlane(o1) == 1u && ln_ < 2 - L) st4(X, ln_ ? sb1 : sb + 2048u, P_FB * PAIR + 4096u + (unsigned)rw * 128u, opq(1u));
    TT(s, 1);
  }
}

__device__ __forceinline__ void rows_task(const MP& p, const rsrc_t X, unsigned long long sm, u16* ys, int grp, int ng) {
  const int tid = threadIdx.x, lane = tid & 63, w = __builtin_amdgcn_readfirstlane(tid >> 6);
  const int m = lane & 15, g = lane >> 4;
  const int xp = 4 * (g & 1) + 2 * (g >> 1) + (m >> 3), c9 = 512 + 8 * xp + (m & 7);
  const unsigned sb = SEGB(sm, grp >> 2);
  v4u yv[5], xa[4];
  unsigned x9[4];
#pragma unroll
  for (int i = 0; i < 5; ++i) yv[i] = ld16(mkr(p.yT + (grp >> 1) * 9216, 18432u), (unsigned)(tid + 256 * i) * 16u, 0u);
#pragma unroll
  for (int rr = 0; rr < 4; ++rr) {
    const long t = grp * 8 + w + 4 * (rr & (2 * ng - 1));
    xa[rr] = *(const v4u*)&p.xin[t * DM + 8 * lane]; x9[rr] = p.xin[t * DM + c9];
  }
#pragma unroll
  for (int i = 0; i < 5; ++i) ((v4u*)ys)[tid + 256 * i] = yv[i];
  BAR();
  SP(13 - 3 * ng);
  const unsigned o9 = (lane & 7) == 0 ? 0u : 0xF0000000u;
#pragma unroll 1
  for (int k = 0; k < ng; ++k) {
    float x[2][9], y[2][9], ln[2][9];
    const int t0 = (grp + k) * 8 + w;
#pragma unroll
    for (int rr = 0; rr < 2; ++rr) {
      const int j = (t0 + 4 * rr) & 15;
      const v2u ya = *(const v2u*)&ys[lane * 128 + (j ^ m) * 4], yb = *(const v2u*)&ys[lane * 128 + 64 + (j ^ m) * 4];
      up8(xa[rr], x[rr]); x[rr][8] = lo2f(x9[rr]); up8((v4u){ya.x, ya.y, yb.x, yb.y}, y[rr]); y[rr][8] = bf2f(ys[(c9 >> 2) * 64 + (j ^ xp) * 4 + (m & 3)]);
      xa[rr] = xa[2 + rr]; x9[rr] = x9[2 + rr];
    }
    lnR<2>(x, ln);
#pragma unroll
    for (int rr = 0; rr < 2; ++rr) {
#pragma unroll
      for (int i = 0; i < 9; ++i) x[rr][i] = __builtin_fmaf(ln[rr][i], y[rr][i], x[rr][i]);
      rb9(x[rr]);
      const int r = (t0 + 4 * rr) & 31;
      st16(X, (unsigned)lane * 16u, sb + (P_X3 + (unsigned)(r >> 3)) * PAIR + (unsigned)(r & 7) * 1024u, pk8(x[rr]));
      st16(X, (unsigned)xp * 16u + o9, sb + (P_H + 4u) * PAIR + 4096u + (unsigned)r * 128u, gat8(x[rr][8]));
    }
    lnR<2>(x, ln);
#pragma unroll
    for (int rr = 0; rr < 2; ++rr) {
      const int r = (t0 + 4 * rr) & 31;
      st16(X, hoff(r, lane), sb, pk8(ln[rr]));
      st16(X, hoff(r, 64 + xp) + o9, sb, gat8(ln[rr][8]));
    }
  }
  SP(14 - 3 * ng);
  VM0();
  BAR();
  SP(15 - 3 * ng);
  if (tid < ng) st4(X, (unsigned)tid * 128u, sb + P_FB * PAIR + 4096u + (unsigned)(grp & 3) * 128u, opq(1u));
}

__device__ __forceinline__ void arrive(const MP& p, int k, int grp) {
  VM0();
  BAR();
  if (threadIdx.x < 64) {
    unsigned old = 0u;
    if (threadIdx.x == 0) old = __hip_atomic_fetch_add(p.xb + MF_OFF / 4u + (k * 16 + grp) * 32, 1u, __ATOMIC_RELAXED, __HIP_MEMORY_SCOPE_AGENT);
    if (__builtin_amdgcn_readfirstlane(old) == 15u && threadIdx.x < 8) st4(mkr(p.xb, XB_BYTES), threadIdx.x * 128u, RDY_OFF(k) + (unsigned)grp * 1024u, opq(1u));
  }
}
__device__ __forceinline__ void wait_ready(const MP& p, int k) {
  if (threadIdx.x < 64) wait_all<PSL>(mkr(p.xb, XB_BYTES), (threadIdx.x % 9u) * 1024u, RDY_OFF(k) + (blockIdx.x & 7u) * 128u);
  BAR();
}
__device__ __forceinline__ void start_work(char* smem, int wk) {
  const MP p = getp();
  const int w = __builtin_amdgcn_readfirstlane(threadIdx.x >> 6);
#define NARM (ARMK - 3u)
  unsigned pf[PFM + 1];
  conv_task(p.kvT, p.ssm_k, p.ssm_D, p.yT, (u16*)(smem + w * 32768), 4 * wk + w, [&]() {
#pragma unroll
    for (int i = 0; i < PFM; ++i) {
      const int z = (i - 1) * 36864 + wk * 256 + threadIdx.x, a = z % 3;
      pf[i] = *(const unsigned*)(i == 0 ? p.Wo + (long)(z + 36864) * 64 : (a == 0 ? p.Wq : (a == 1 ? p.Wk : p.Wv)) + (long)(36863 - z / 3) * 64);
    }
  }, [&]() {
    const unsigned sv = opq(SENT);
    constexpr unsigned S16 = SLOT / 16u;
#pragma unroll
    for (unsigned k = 0; k < (NARM * S16 + 32767u) / 32768u; ++k) {
      const unsigned i = k * 32768u + wk * 256 + threadIdx.x;
      st16(mkr(p.xb, XB_BYTES), i < NARM * S16 && wk < 128 ? SLOT_AT(3u + i / S16) + (i % S16) * 16u : 0xF0000000u, 0u, (v4u){sv, sv, sv, sv});
    }
  });
#pragma unroll
  for (int i = 0; i < PFM; ++i) asm volatile("" :: "v"(pf[i]));
  SP(3);
  arrive(p, 0, wk >> 4);
  SP(4);
}
__device__ __forceinline__ void token_start(char* smem) {
  TOK_COMMON();
  (void)w;
  const int k = 4 * i + xq;
  if (L || (i >= 12 && i < 16)) start_work(smem, L ? 32 + k : 80 + k);
  if (i >= (L ? 12 : 20)) {
    const unsigned long long sm = die_mask(X, SG0, 0, lane);
    int g = L ? 111 - k : k - 96;
    while (g < 120) {
      if (g >= 0 && g < 64) {
        SP(5);
        wait_ready(p, 0);
        SP(6);
      }
      rows_task(p, X, sm, (u16*)smem, g < 0 ? g + 16 : 16 + 2 * g, g < 0 ? 1 : 2);
      g += g < 0 ? 16 : (L ? 48 : 120);
    }
  }
}
__device__ __forceinline__ void token_wg(char* smem) {
  if ((unsigned)(size_t)(__attribute__((address_space(3))) char*)smem != 0u) return;
  token_start(smem);
  const int i = ((int)blockIdx.x - NCHW) >> 3;
  if (i < 12) qkv_role();
  else if (i < 20) ap_role();
  else row_role(smem);
}

__global__ __launch_bounds__(256, 1) void k_main(MP) {
  extern __shared__ __attribute__((aligned(16))) char smem[];
  asm volatile("LKBEG:");
  SP(13);
  if ((int)blockIdx.x < NCHW) chain_wg(smem);
  else token_wg(smem);
}

extern "C" void kernel_launch(void* const* d_in, const int* in_sizes, int n_in, void* d_out, int out_size, void* d_ws,
                              size_t ws_size, hipStream_t stream) {
  const u16* A_q = (const u16*)d_in[0];
  const u16* A_k = (const u16*)d_in[1];
  const u16* A_v = (const u16*)d_in[2];
  const u16* A_shift = (const u16*)d_in[3];
  const u16* A_ssmk = (const u16*)d_in[4];
  const u16* A_ssmD = (const u16*)d_in[5];
  const u16* B_mem0 = (const u16*)d_in[7];
  const u16* B_Wq = (const u16*)d_in[8];
  const u16* B_Wk = (const u16*)d_in[9];
  const u16* B_Wv = (const u16*)d_in[10];
  const u16* B_Wo = (const u16*)d_in[11];

  char* ws = (char*)d_ws;
  size_t off = (size_t)(-(uintptr_t)ws) & (XBLK - 1u);
  auto alloc = [&](size_t bytes) { char* q = ws + off; off += (bytes + 4095) & ~(size_t)4095; return q; };
  unsigned* xb = (unsigned*)alloc((size_t)XB_BYTES);
  u16* kvT = (u16*)alloc((size_t)DM * S_LEN * 2);
  u16* yT = (u16*)alloc((size_t)DM * S_LEN * 2);

  PreP pp;
  pp.kk = A_k; pp.vv = A_v; pp.shift_k = A_shift; pp.ssm_k = A_ssmk; pp.ssm_D = A_ssmD; pp.yT = yT; pp.kvT = kvT;
  pp.Wq = B_Wq; pp.Wk = B_Wk; pp.Wv = B_Wv; pp.Wo = B_Wo; pp.xb = xb; pp.mem0 = B_mem0;
  k_pre<<<240 + PRE_FILL + 256 + 288, 256, 0, stream>>>(pp);
  MP mp;
  mp.out = (u16*)d_out; mp.xb = xb;
  mp.xin = A_q; mp.yT = yT; mp.kvT = kvT; mp.ssm_k = A_ssmk; mp.ssm_D = A_ssmD;
  mp.Wq = B_Wq; mp.Wk = B_Wk; mp.Wv = B_Wv; mp.Wo = B_Wo;
  const int smem_bytes = 160 * 1024;
  (void)hipFuncSetAttribute((const void*)k_main, hipFuncAttributeMaxDynamicSharedMemorySize, smem_bytes);
  k_main<<<NCHW + NTOKW, 256, smem_bytes, stream>>>(mp);
}
```

```cpp
#include <hip/hip_runtime.h>
#include <stdint.h>

typedef unsigned short u16;
using bf16x8 = __attribute__((ext_vector_type(8))) short;
using f32x4 = __attribute__((ext_vector_type(4))) float;
using f32x2 = __attribute__((ext_vector_type(2))) float;
using bfx8 = __attribute__((ext_vector_type(8))) __bf16;
using bfx2 = __attribute__((ext_vector_type(2))) __bf16;
typedef unsigned v4u __attribute__((ext_vector_type(4)));
typedef unsigned v2u __attribute__((ext_vector_type(2)));
typedef short v4s __attribute__((ext_vector_type(4)));

#define S_LEN 2048
#define DM 576
#define NH 32
#define DH 128
#define HD (NH * DH)
#define SEG 32
#define NSEG 64

__device__ __forceinline__ float bf2f(u16 x) { return __uint_as_float(((unsigned)x) << 16); }
__device__ __forceinline__ unsigned pk2(float a, float b) { f32x2 v = {a, b}; bfx2 r = __builtin_convertvector(v, bfx2); return __builtin_bit_cast(unsigned, r); }
__device__ __forceinline__ float lo2f(unsigned u) { return __uint_as_float(u << 16); }
__device__ __forceinline__ float hi2f(unsigned u) { return __uint_as_float(u & 0xFFFF0000u); }

#define NFC 128
#define XBLK 2097152u
#define PAIR 16384u
#define SLOT 688128u
#define AGO 589824u
#define AGT 655360u
#define SG0 22
#define TP0 86
#define NXB 95
#define XB_BYTES (NXB * XBLK)
#define SLOT_AT(i) ((unsigned)((i) / 3) * XBLK + (unsigned)((i) % 3) * SLOT)
#define HT_OFF SLOT_AT(65)
#define CTAB_OFF (HT_OFF + 16384u)
#define MF_OFF (HT_OFF + 20480u)
#define TMO_OFF (MF_OFF + 15u * 128u)
#define RDY_OFF(k) (MF_OFF + 4096u + (k) * 10240u)
#define MF_BYTES 16384u
#define TRC_OFF SLOT_AT(64)
#define CAL_OFF (XBLK - 16384u)
#define P_KV 96u
#define P_H 112u
#define P_X3 117u
#define P_FA 121u
#define P_FB 122u
#define ARMK 8
#define NCAL 8
#define CALT 560u
#define CALM 1
#define SRING 12
#define TRING 64
__device__ __forceinline__ unsigned kv2(const unsigned (&u)[4], unsigned v, v2u w0, v2u w1) {
  const float a[4] = {hi2f(w0.y), lo2f(w0.y), hi2f(w0.x), lo2f(w0.x)}, b[4] = {hi2f(w1.y), lo2f(w1.y), hi2f(w1.x), lo2f(w1.x)};
  unsigned r = pk2(lo2f(u[0]) * a[0], hi2f(u[0]) * b[0]);
#pragma unroll
  for (int i = 1; i < 4; ++i) { const unsigned q = pk2(lo2f(u[i]) * a[i], hi2f(u[i]) * b[i]); r = pk2(lo2f(r) + lo2f(q), hi2f(r) + hi2f(q)); }
  return pk2(lo2f(r) * lo2f(v), hi2f(r) * hi2f(v));
}
struct PreP {
  const u16* kk; const u16* vv; const u16* shift_k; const u16* ssm_k; const u16* ssm_D; u16* yT; u16* kvT;
  const u16* Wq; const u16* Wk; const u16* Wv; const u16* Wo; unsigned* xb; const u16* mem0;
};
#define PRE_FILL 112
#define PFN 2
__global__ __launch_bounds__(256, 4) void k_pre(PreP p) {
  __shared__ __attribute__((aligned(16))) float sh[64 * 36];
  const int tid = threadIdx.x, b0 = blockIdx.x, b = b0 < 240 ? b0 : (b0 < 528 ? b0 + PRE_FILL + 256 : b0 - 288);
  const __amdgpu_buffer_rsrc_t rx = __builtin_amdgcn_make_buffer_rsrc((void*)p.xb, 0, XB_BYTES, 0x00020000);
  const v4u ones = {0xFFFFFFFFu, 0xFFFFFFFFu, 0xFFFFFFFFu, 0xFFFFFFFFu}, zero = {0, 0, 0, 0};
  if (b < 96) {
    if (tid >= 64) return;
    const unsigned cb = (unsigned)b * XBLK + CAL_OFF;
    __builtin_amdgcn_raw_buffer_store_b128(zero, rx, (unsigned)((tid >> 3) & 1) * 8192u + (unsigned)(tid >> 4) * 256u + (unsigned)(tid & 7) * 16u, cb, 16);
    asm volatile("s_waitcnt vmcnt(0)" ::: "memory");
    unsigned mn[2] = {~0u, ~0u};
    for (int i = 0; i < NCAL; ++i) {
      const unsigned long long c0 = __builtin_amdgcn_s_memtime();
      asm volatile("s_waitcnt lgkmcnt(0)" ::: "memory");
      const unsigned v = __builtin_amdgcn_raw_buffer_load_b32(rx, 0, cb + (unsigned)(i & 1) * 8192u + (unsigned)(i >> 1) * 256u, 16);
      asm volatile("s_waitcnt vmcnt(0)" :: "v"(v) : "memory");
      const unsigned d = (unsigned)(__builtin_amdgcn_s_memtime() - c0);
      if (i & 1) mn[1] = d < mn[1] ? d : mn[1]; else mn[0] = d < mn[0] ? d : mn[0];
      const unsigned lo = mn[0] < mn[1] ? mn[0] : mn[1], hi = mn[0] < mn[1] ? mn[1] : mn[0];
      if (i >= CALM && lo < CALT && hi > lo + 100u) break;
    }
    unsigned xcc; asm volatile("s_getreg_b32 %0, hwreg(HW_REG_XCC_ID)" : "=s"(xcc));
    const unsigned res = mn[1] < mn[0] ? 1u : 0u;
    if (tid == 0) __builtin_amdgcn_raw_buffer_store_b32(res ^ ((xcc >> 2) & 1u), rx, 0, CTAB_OFF + (unsigned)b * 4u, 16);
  } else if (b < 240) {
    const __amdgpu_buffer_rsrc_t rk = __builtin_amdgcn_make_buffer_rsrc((void*)p.kk, 0, S_LEN * DM * 2, 0x00020000);
    float (*kx)[132] = (float (*)[132])sh;
    float (*cf)[140] = (float (*)[140])(sh + 4 * 132);
    const int d0 = (b - 96) * 4, q = tid & 3, dd = (tid >> 2) & 3, t0 = (tid >> 4) * 8;
    const float Dv = bf2f(p.ssm_D[d0 + dd]);
    {
      const int s = tid >> 1, d = d0 + (tid & 1) * 2;
      unsigned u[4];
#pragma unroll
      for (int a = 0; a < 4; ++a) u[a] = __builtin_amdgcn_raw_buffer_load_b32(rk, (unsigned)((s - 3 + a) * DM + d) * 2u, 0, 0);
      const unsigned vr = *(const unsigned*)&p.vv[s * DM + d], c0 = *(const unsigned*)&p.ssm_k[(long)(d0 + (tid >> 6)) * S_LEN + (tid & 63) * 2];
      const v4u w = *(const v4u*)&p.shift_k[d * 4];
      const unsigned r = kv2(u, vr, (v2u){w.x, w.y}, (v2u){w.z, w.w});
      kx[(tid & 1) * 2][s] = lo2f(r); kx[(tid & 1) * 2 + 1][s] = hi2f(r);
      *(f32x2*)&cf[tid >> 6][8 + (tid & 63) * 2] = (f32x2){lo2f(c0), hi2f(c0)};
      if (tid < 32) cf[tid >> 3][tid & 7] = 0.f;
    }
    __syncthreads();
    float y[8];
#pragma unroll
    for (int i = 0; i < 8; ++i) y[i] = 0.f;
#pragma unroll 1
    for (int j = 8 * q; j <= t0; j += 32) {
      f32x4 kk[2], cw[4];
#pragma unroll
      for (int x = 0; x < 2; ++x) kk[x] = *(const f32x4*)&kx[dd][j + 4 * x];
#pragma unroll
      for (int x = 0; x < 4; ++x) cw[x] = *(const f32x4*)&cf[dd][t0 - j + 4 * x];
#pragma unroll
      for (int jj = 0; jj < 8; ++jj)
#pragma unroll
        for (int i = 0; i < 8; ++i) y[i] = __builtin_fmaf(cw[(8 + i - jj) >> 2][(8 + i - jj) & 3], kk[jj >> 2][jj & 3], y[i]);
    }
#pragma unroll
    for (int i = 0; i < 8; ++i) {
      y[i] += __uint_as_float((unsigned)__builtin_amdgcn_mov_dpp((int)__float_as_uint(y[i]), 0xB1, 0xf, 0xf, true));
      y[i] += __uint_as_float((unsigned)__builtin_amdgcn_mov_dpp((int)__float_as_uint(y[i]), 0x4E, 0xf, 0xf, true));
    }
    const float ya = q == 0 ? y[0] : (q == 1 ? y[2] : (q == 2 ? y[4] : y[6])), yb = q == 0 ? y[1] : (q == 1 ? y[3] : (q == 2 ? y[5] : y[7]));
    const int t = t0 + 2 * q, f = (d0 >> 3) & 15;
    const unsigned r0 = pk2(ya, yb), r1 = pk2(kx[dd][t] * Dv, kx[dd][t + 1] * Dv), o = pk2(lo2f(r0) + lo2f(r1), hi2f(r0) + hi2f(r1));
    u16* dst = p.yT + (t >> 4) * 9216 + (d0 >> 2) * 64 + dd;
    dst[((t & 15) ^ f) << 2] = (u16)(o & 0xFFFFu); dst[(((t + 1) & 15) ^ f) << 2] = (u16)(o >> 16);
  } else if (b < 240 + PRE_FILL) {
    const unsigned i0 = (unsigned)(b - 240) * 256u + tid;
    if (i0 < (SLOT - AGO) / 16u) {
      const unsigned o = AGO + i0 * 16u, s = o < AGT ? (i0 >> 11) * 4u + ((o >> 10) & 3u) : 8u, r = o & 1023u;
      const unsigned gm = s < 8u ? r >> 8 : (r >> 6) & 3u, m3 = (r >> 4) & 3u, ga = s < 8u ? (r >> 6) & 3u : r >> 8, row = 4u * (gm & 1u) + 2u * (gm >> 1) + (m3 & 1u), half = m3 >> 1;
      const unsigned* src = (const unsigned*)(p.mem0 + row * DM + 64u * s + 16u * ga + 2u * half);
      __builtin_amdgcn_raw_buffer_store_b128((v4u){src[0], src[2], src[4], src[6]}, rx, o, 0, 16);
    }
    for (unsigned i = i0; i < 131072u + MF_BYTES / 16u; i += PRE_FILL * 256)
      __builtin_amdgcn_raw_buffer_store_b128(zero, rx, i < 131072u ? (SG0 + (i >> 11)) * XBLK + P_FA * PAIR + (i & 2047u) * 16u : MF_OFF + (i & 2047u) * 16u, 0, 16);
    for (unsigned i = i0; i < 2u * SLOT / 16u; i += PRE_FILL * 256) __builtin_amdgcn_raw_buffer_store_b128(ones, rx, SLOT + i * 16u, 0, 16);
  } else if (b >= 240 + PRE_FILL + 256) {
    const __amdgpu_buffer_rsrc_t rk = __builtin_amdgcn_make_buffer_rsrc((void*)p.kk, 0, S_LEN * DM * 2, 0x00020000);
    u16 (*tile)[72] = (u16 (*)[72])sh;
    const int bid = b - 240 - PRE_FILL - 256, d0 = (bid % 9) * 64, s0 = (bid / 9) * 64;
    const int sr = (tid >> 3) * 2, d = d0 + (tid & 7) * 8;
    v4u kr[5], vr[2], w[4];
#pragma unroll
    for (int a = 0; a < 5; ++a) kr[a] = __builtin_amdgcn_raw_buffer_load_b128(rk, (unsigned)((s0 + sr - 3 + a) * DM + d) * 2u, 0, 0);
#pragma unroll
    for (int a = 0; a < 2; ++a) vr[a] = *(const v4u*)&p.vv[(long)(s0 + sr + a) * DM + d];
#pragma unroll
    for (int a = 0; a < 4; ++a) w[a] = *(const v4u*)&p.shift_k[(d + 2 * a) * 4];
    __builtin_amdgcn_sched_barrier(0);
#pragma unroll
    for (int it = 0; it < 2; ++it) {
      v4u o;
#pragma unroll
      for (int j = 0; j < 4; ++j) { const unsigned u[4] = {kr[it][j], kr[it + 1][j], kr[it + 2][j], kr[it + 3][j]}; o[j] = kv2(u, vr[it][j], (v2u){w[j].x, w[j].y}, (v2u){w[j].z, w[j].w}); }
      *(v4u*)&tile[sr + it][(tid & 7) * 8] = o;
    }
    __syncthreads();
    const int lane = tid & 63, wv = tid >> 6, g = lane >> 4, r = (lane >> 2) & 3, c = lane & 3;
    v2u o[4];
#pragma unroll
    for (int i = 0; i < 4; ++i) o[i] = __builtin_bit_cast(v2u, __builtin_amdgcn_ds_read_tr16_b64_v4i16((__attribute__((address_space(3))) v4s*)&tile[16 * g + 4 * i + r][16 * wv + 4 * c]));
    u16* dst = p.kvT + (long)(d0 + 16 * wv + (lane & 15)) * S_LEN + s0 + 16 * g;
    *(v4u*)dst = (v4u){o[0].x, o[0].y, o[1].x, o[1].y};
    *(v4u*)(dst + 8) = (v4u){o[2].x, o[2].y, o[3].x, o[3].y};
  } else {
    const int x = b & 7, k = (b - 240 - PRE_FILL) >> 3;
    unsigned long long pc; unsigned l0, l1, l2, l3; int rel;
    asm volatile("s_getpc_b64 %0\nLPRE:\n\ts_mov_b32 %1, LKBEG-LPRE\n\ts_mov_b32 %2, LCHEND-LKBEG\n\ts_mov_b32 %3, LQVEND-LKBEG\n\ts_mov_b32 %4, LQKEND-LKBEG\n\ts_mov_b32 %5, LAPEND-LKBEG"
                 : "=s"(pc), "=s"(rel), "=s"(l0), "=s"(l1), "=s"(l2), "=s"(l3));
    l0 = l0 > l1 ? l0 : l1; l2 = l2 > l3 ? l2 : l3; l0 = l0 > l2 ? l0 : l2;
    const unsigned o = (unsigned)(k * 64 + (tid & 63)) * 64u;
    unsigned a[PFN + 2];
    a[PFN] = *(const __attribute__((address_space(1))) unsigned*)(pc + rel + (o < l0 ? o : 0u));
    a[PFN + 1] = __builtin_amdgcn_raw_buffer_load_b32(rx, k == 31 ? tid * XBLK + CAL_OFF + 8064u : 0xF0000000u, 0, 16);
#pragma unroll
    for (int i = 0; i < PFN; ++i) {
      const int q = (i * 32 + k) * 256 + tid, r2 = q >> 5, gr = r2 >> 5, row = (gr / 3) * 64 + (x >> 2) * 32 + (r2 & 31), off = (q & 31) * 32, ty = gr % 3;
      a[i] = *(const unsigned*)(gr < 27 ? (ty == 0 ? p.Wq : (ty == 1 ? p.Wk : p.Wv)) + (long)row * HD + (x & 3) * 1024 + off
                                        : p.Wo + ((long)(x & 3) * DM + (gr < 36 ? gr - 27 : 0) * 64 + (x >> 2) * 32 + (r2 & 31)) * 1024 + off);
    }
#pragma unroll
    for (int i = 0; i < PFN + 2; ++i) asm volatile("" :: "v"(a[i]));
  }
}

#define CREV_STRIDE 2208
#define LWAIT() asm volatile("s_waitcnt lgkmcnt(0)" ::: "memory")
template <class E, class F>
__device__ __forceinline__ void conv_task(const u16* __restrict__ kvT, const u16* __restrict__ ssm_k, const u16* __restrict__ ssm_D, u16* yT, u16* shb, int d, E early, F fill) {
  const int lane = threadIdx.x & 63;
  u16* crev = shb + 64;
  u16* kvs = crev + 4 * CREV_STRIDE;
  const int X0 = 2048 + 63;
  v4u cv[4], kq[4];
#pragma unroll
  for (int j = 0; j < 4; ++j) { cv[j] = *(const v4u*)&ssm_k[(long)d * S_LEN + lane * 32 + 8 * j]; kq[j] = *(const v4u*)&kvT[(long)d * S_LEN + (j * 64 + lane) * 8]; }
  const float Dv = bf2f(ssm_D[d]);
  __builtin_amdgcn_sched_barrier(0);
  early();
  __builtin_amdgcn_sched_barrier(0);
  const v4u z4 = {0u, 0u, 0u, 0u};
  *(v4u*)&kvs[(lane >> 2) * 40 + (lane & 3) * 8] = z4;
  if (lane < 32) *(v4u*)&crev[(lane >> 3) * CREV_STRIDE + (lane & 7) * 8] = z4;
  if (lane < 36) *(v4u*)&crev[(lane / 9) * CREV_STRIDE + 2112 + (lane % 9) * 8] = z4;
  {
    unsigned r[18];
#pragma unroll
    for (int k = 0; k < 16; ++k) { const unsigned u = cv[3 - (k >> 2)][3 - (k & 3)]; r[k] = __builtin_amdgcn_alignbyte(u, u, 2); }
#pragma unroll
    for (int k = 0; k < 2; ++k) r[16 + k] = (unsigned)__builtin_amdgcn_update_dpp(0, (int)r[k], 0x138, 0xf, 0xf, true);
    LWAIT();
#pragma unroll
    for (int rho = 0; rho < 4; ++rho)
#pragma unroll
      for (int j = 0; j < 4; ++j) {
        v4u o;
#pragma unroll
        for (int k = 0; k < 4; ++k) { const int q = 4 * j + k + (rho >> 1); o[k] = (rho & 1) ? __builtin_amdgcn_alignbyte(r[q + 1], r[q], 2) : r[q]; }
        *(v4u*)&crev[rho * CREV_STRIDE + 2080 - 32 * lane + 8 * j] = o;
      }
    if (lane == 63) {
      *(v2u*)&crev[1 * CREV_STRIDE + 60] = (v2u){0u, r[0] << 16};
      *(v2u*)&crev[2 * CREV_STRIDE + 60] = (v2u){0u, r[0]};
      *(v2u*)&crev[3 * CREV_STRIDE + 60] = (v2u){r[0] << 16, __builtin_amdgcn_alignbyte(r[1], r[0], 2)};
    }
#pragma unroll
    for (int j = 0; j < 4; ++j) *(v4u*)&kvs[(16 + j * 16 + (lane >> 2)) * 40 + (lane & 3) * 8] = kq[j];
  }
  fill();
  LWAIT();
  const int n = lane & 15, g = lane >> 4;
  const int rho = (X0 - n) & 3;
  f32x4 acc[4][2];
#pragma unroll
  for (int q = 0; q < 8; ++q) acc[q >> 1][q & 1] = (f32x4){0.f, 0.f, 0.f, 0.f};
  {
    typedef __attribute__((address_space(3))) const v2u* l2p;
    typedef __attribute__((address_space(3))) const bf16x8* l4p;
    unsigned a0 = (unsigned)(size_t)(__attribute__((address_space(3))) u16*)&crev[rho * CREV_STRIDE + (X0 - (n - 8 * g) - rho)] - 3232u, a1 = a0 + 8u;
    unsigned b0 = (unsigned)(size_t)(__attribute__((address_space(3))) u16*)&kvs[(14 + n) * 40 + 8 * g];
    asm volatile("" : "+v"(a1));
#define AFR(m, f) ({ const unsigned o_ = 3232u - 64u * (m) - 1024u * ((f) >> 1) - 32u * ((f) & 1); const v2u lo_ = *(l2p)(size_t)(a0 + o_), hi_ = *(l2p)(size_t)(a1 + o_); \
                     __builtin_bit_cast(bf16x8, (v4u){lo_.x, lo_.y, hi_.x, hi_.y}); })
#define BFR(m, j) (*(l4p)(size_t)(b0 + 80u * (2 - (m)) + 1280u * (j)))
    bf16x8 af[2][8], bf[2][4];
#pragma unroll
    for (int f = 0; f < 8; ++f) { af[0][f] = AFR(0, f); af[1][f] = af[0][f]; __builtin_amdgcn_sched_barrier(0); }
#pragma unroll
    for (int j = 0; j < 4; ++j) { bf[0][j] = BFR(0, j); bf[1][j] = bf[0][j]; }
#pragma unroll 1
    for (int mo = 0; mo < 8; ++mo) {
#pragma unroll
      for (int m = 0; m < 2; ++m) {
        int k = 0;
#pragma unroll
        for (int a = 0; a < 4; ++a)
#pragma unroll
          for (int ct = a; ct < 4; ++ct)
#pragma unroll
            for (int ti = 0; ti < 2; ++ti) {
              acc[ct][ti] = __builtin_amdgcn_mfma_f32_16x16x32_bf16(__builtin_bit_cast(bfx8, af[m][2 * a + ti]), __builtin_bit_cast(bfx8, bf[m][ct - a]), acc[ct][ti], 0, 0, 0);
              if (k < 2) af[m ^ 1][k] = AFR(m + 1, k);
              else if (k < 6) bf[m ^ 1][k - 2] = BFR(m + 1, k - 2);
              else if (k < 12) af[m ^ 1][k - 4] = AFR(m + 1, k - 4);
              __builtin_amdgcn_sched_barrier(0);
              ++k;
            }
      }
      a0 -= 128u; a1 -= 128u; b0 -= 160u;
    }
  }
  u16* yo = crev;
  LWAIT();
#pragma unroll
  for (int q = 0; q < 8; ++q) {
    const int o = (16 * (q >> 1) + n) * 40 + 16 * (q & 1) + 4 * g;
    const v2u kx = *(const v2u*)&kvs[16 * 40 + o];
    const f32x4 y = acc[q >> 1][q & 1];
    const unsigned y0 = pk2(y[0], y[1]), y1 = pk2(y[2], y[3]), r0 = pk2(lo2f(kx.x) * Dv, hi2f(kx.x) * Dv), r1 = pk2(lo2f(kx.y) * Dv, hi2f(kx.y) * Dv);
    *(v2u*)&yo[o] = (v2u){pk2(lo2f(y0) + lo2f(r0), hi2f(y0) + hi2f(r0)), pk2(lo2f(y1) + lo2f(r1), hi2f(y1) + hi2f(r1))};
  }
  LWAIT();
  __builtin_amdgcn_s_barrier();
  asm volatile("" ::: "memory");
  {
    const int tid = threadIdx.x, f = (d >> 3) & 15;
    const u16* y0 = shb - (tid >> 6) * 16384 + 64;
#pragma unroll
    for (int k = 0; k < 4; ++k) {
      const int tile = 32 * k + (tid >> 3), t = 16 * tile + 2 * ((tid & 7) ^ (f >> 1));
      unsigned u[4];
#pragma unroll
      for (int c = 0; c < 4; ++c) u[c] = *(const unsigned*)&y0[c * 16384 + (t >> 5) * 40 + (t & 31)];
      const v2u e = {__builtin_amdgcn_perm(u[1], u[0], 0x05040100u), __builtin_amdgcn_perm(u[3], u[2], 0x05040100u)}, o = {__builtin_amdgcn_perm(u[1], u[0], 0x07060302u), __builtin_amdgcn_perm(u[3], u[2], 0x07060302u)};
      if (tile >= NFC / 16) __builtin_amdgcn_raw_buffer_store_b128((f & 1) ? (v4u){o.x, o.y, e.x, e.y} : (v4u){e.x, e.y, o.x, o.y}, __builtin_amdgcn_make_buffer_rsrc((void*)yT, 0, DM * S_LEN * 2, 0x00020000),
                                                                   tile * 18432 + (tid & 7) * 16, (d >> 2) * 128, 16);
    }
  }
}

#define NCHW 64
#define NTOKW 192
#define PSL 4
#define SENT 0xFFFFFFFFu
#define SPIN_CAP 300000u
struct MP {
  const u16* Wq; const u16* Wk; const u16* Wv; const u16* Wo; u16* out; unsigned* xb;
  const u16* xin; u16* yT; u16* kvT; const u16* ssm_k; const u16* ssm_D;
};

__device__ __forceinline__ MP getp() {
  MP r;
#if defined(__HIP_DEVICE_COMPILE__)
  const __attribute__((address_space(4))) unsigned long long* k = (const __attribute__((address_space(4))) unsigned long long*)__builtin_amdgcn_kernarg_segment_ptr();
  asm volatile("" : "+s"(k));
#define GP(T, i) ((T*)(__attribute__((address_space(1))) T*)k[i])
  r.Wq = GP(const u16, 0);
  r.Wk = GP(const u16, 1);
  r.Wv = GP(const u16, 2);
  r.Wo = GP(const u16, 3);
  r.out = GP(u16, 4);
  r.xb = GP(unsigned, 5);
  r.xin = GP(const u16, 6);
  r.yT = GP(u16, 7);
  r.kvT = GP(u16, 8);
  r.ssm_k = GP(const u16, 9);
  r.ssm_D = GP(const u16, 10);
#endif
  return r;
}
typedef __amdgpu_buffer_rsrc_t rsrc_t;
__device__ __forceinline__ rsrc_t mkr(const void* p, unsigned bytes) { return __builtin_amdgcn_make_buffer_rsrc((void*)p, 0, bytes, 0x00020000); }
__device__ __forceinline__ void st16(rsrc_t r, unsigned vo, unsigned so, v4u v) { __builtin_amdgcn_raw_buffer_store_b128(v, r, vo, so, 16); }
__device__ __forceinline__ void st4(rsrc_t r, unsigned vo, unsigned so, unsigned v) { __builtin_amdgcn_raw_buffer_store_b32(v, r, vo, so, 16); }
__device__ __forceinline__ v4u ld16(rsrc_t r, unsigned vo, unsigned so) { return __builtin_amdgcn_raw_buffer_load_b128(r, vo, so, 16); }
__device__ __forceinline__ v2u ld8(rsrc_t r, unsigned vo, unsigned so) { return __builtin_amdgcn_raw_buffer_load_b64(r, vo, so, 16); }
__device__ __forceinline__ unsigned ld4(rsrc_t r, unsigned vo, unsigned so) { return __builtin_amdgcn_raw_buffer_load_b32(r, vo, so, 16); }
__device__ __forceinline__ unsigned opq(unsigned x) { asm volatile("" : "+v"(x)); return x; }

__device__ __forceinline__ unsigned long long die_mask(rsrc_t X, unsigned first, int L, int lane) { return __ballot((ld4(X, (unsigned)lane * 4u, CTAB_OFF + first * 4u) & 1u) != (unsigned)L); }
#define SEGB(m, s) ((unsigned)(SG0 + (s)) * XBLK + (unsigned)(((m) >> (s)) & 1ull) * 8192u)
__device__ __forceinline__ bool spin_fail(unsigned& spins, rsrc_t F) {
  ++spins;
  if (spins > SPIN_CAP) { unsigned one = 0x40000000u | (blockIdx.x << 8) | threadIdx.x; asm volatile("" : "+v"(one)); if (__builtin_amdgcn_readfirstlane(ld4(F, 0, TMO_OFF)) == 0u) st4(F, 0, TMO_OFF, one); return true; }
  if ((spins & 1023u) == 0u) return __builtin_amdgcn_readfirstlane(ld4(F, 0, TMO_OFF)) != 0;
  return false;
}
__device__ __forceinline__ unsigned rtc() { return (unsigned)__builtin_amdgcn_s_memrealtime(); }
#define BAR() do { asm volatile("s_waitcnt lgkmcnt(0)" ::: "memory"); __builtin_amdgcn_s_barrier(); asm volatile("" ::: "memory"); } while (0)
#define SB() __builtin_amdgcn_sched_barrier(0)
#define MF "v_mfma_f32_16x16x32_bf16 "
#define VM0() __builtin_amdgcn_s_waitcnt(0x0F70)
#define DL1 12
#define SCH_B0 48
#define DLL1 6
#define DLL2 0
#define DLF 24
#define SCH_PEN 0
#define DLX 4
#define LATE 2
#define TKL 28
#define FIN -3
#define CH_FIN CH_S(FIN)
#define DLT 16
#define DLY 20
#define CH_DLT CH_S(DLT)
#define CH_DLY CH_S(DLY)
#define ES 6
#define ED 4
#define EB 8
#define CH_ES CH_S(ES)
#define CH_EX "%[ex]"
#define DRYL 1
#define CH_DRYL CH_S(DRYL)
#define SMAP 0x1023u
#define RO1 4
#define CH_RO1 CH_S(RO1)
#define RO3 2
#define CH_RO3 CH_S(RO3)
#define KD 303
#define KB 146
#define FU 3072
#define FD 256
#define CH_FU CH_S(FU)
#define CH_FD CH_S(FD)
#define CH_KD CH_S(KD)
#define CH_KB CH_S(KB)
#define CH_AL(n) ".p2align 5\n\t.fill " CH_S(n) ", 4, 0xbf800000\n\t"
#define ALW 6
#define ALD 5
#define CH_ALW CH_AL(ALW)
#define CH_ALD CH_AL(ALD)
#define RSL 2
#define CH_S2(x) #x
#define CH_S(x) CH_S2(x)
#define CH_TOKCHK "s_cbranch_vccz LG3_%=\n\t"
#define CH_SG0 CH_S(SG0)
#define CH_ARMD "%[armd]"
#define CH_ARMS "%[arms]"
#define CH_SLOT "688128"
#define CH_SKIP "32768"
#define CH_DLL1 CH_S(DLL1)
#define CH_DLL2 CH_S(DLL2)
#define CH_DLF CH_S(DLF)
#define CH_B0 CH_S(SCH_B0)
#define CH_PEN CH_S(SCH_PEN)
#define CH_RSL CH_S(RSL)
#define CH_DLX CH_S(DLX)
#define CH_LATE CH_S(LATE)
#define CH_DL1L "%[dl1l]"
#define CH_TKL CH_S(TKL)
#define CH_SCHW "656384"
#define CH_CAP "%[cap]"
#define CH_CTAB "%[ctab]"
#define CH_RING "%[ring]"
#define CH_TRM "%[trm]"
#define FDL 6
#define CH_FDL CH_S(FDL)
#define CH_ASM \
  "s_mov_b32 s84, " CH_CAP "\n\t" \
  "s_mov_b32 s83, " CH_CAP "\n\t" \
  "s_mov_b32 s68, 0\n\t" \
  "s_mov_b32 s74, 0\n\t" \
  "s_mov_b32 s76, 0\n\t" \
  "s_mov_b32 s77, 0\n\t" \
  "s_mov_b32 s78, 0\n\t" \
  "s_mov_b32 s79, 0\n\t" \
  "s_mov_b32 s56, 0xcccccccc\n\t" \
  "s_mov_b32 s57, 0xcccccccc\n\t" \
  "s_mov_b32 s58, 0x33333333\n\t" \
  "s_mov_b32 s59, 0x33333333\n\t" \
  "s_mov_b32 s24, 0x01010101\n\t" \
  "s_mov_b32 s25, 0\n\t" \
  "s_mov_b32 s88, 0x3f803f80\n\t" \
  "s_mov_b32 s89, 0x3f803f80\n\t" \
  "s_mov_b32 s66, s65\n\t" \
  "s_mov_b32 s47, s65\n\t" \
  "s_mov_b32 s72, 0\n\t" \
  "s_mov_b32 s73, 0\n\t" \
  "s_mov_b32 s67, 0\n\t" \
  "s_mul_i32 s60, s40, 36864\n\t" \
  "s_add_u32 s60, s60, 16384\n\t" \
  "v_add_u32 v70, s60, v69\n\t" \
  "v_bfe_i32 v68, v69, 5, 1\n\t" \
  "v_and_b32 v68, 0xaaaaaaaa, v68\n\t" \
  "v_or_b32 v68, 0x44444444, v68\n\t" \
  "v_and_b32 v73, 32, v69\n\t" \
  "v_lshl_add_u32 v73, v73, 5, v69\n\t" \
  "LLOAD_%=:\n\t" \
  "v_lshrrev_b32 v74, 2, v69\n\t" \
  "s_mov_b32 s60, " CH_CTAB "\n\t" \
  "buffer_load_dword v72, v74, s[28:31], s60 offen offset:88 sc1\n\t" \
  "buffer_load_dword v74, v74, s[28:31], s60 offen sc1\n\t" \
  "v_lshrrev_b32 v71, 8, v69\n\t" \
  "v_lshlrev_b32 v1, 6, v71\n\t" \
  "v_and_b32 v2, 0xf0, v69\n\t" \
  "v_xor_b32 v1, v1, v2\n\t" \
  "v_lshl_add_u32 v71, v71, 13, v1\n\t" \
  "s_lshl_b32 s60, s40, 17\n\t" \
  "v_add_u32 v71, s60, v71\n\t" \
  "v_lshrrev_b32 v1, 7, v69\n\t" \
  "v_and_b32 v2, 0x70, v69\n\t" \
  "s_movk_i32 s60, 1152\n\t" \
  "v_mad_u32_u24 v75, v1, s60, v2\n\t" \
  "s_lshl_b32 s54, s40, 12\n\t" \
  "s_lshl_b32 s55, s40, 7\n\t" \
  "s_mul_i32 s75, s40, 36864\n\t" \
  "s_add_u32 s75, s75, 1024\n\t" \
  "s_mov_b32 s62, 0x05040100\n\t" \
  "s_mov_b32 s63, 0x07060302\n\t" \
  "s_barrier\n\t" \
  "s_xor_b32 s61, s40, 0\n\t" \
  "s_mul_i32 s61, s61, 36864\n\t" \
  "s_add_u32 s61, s61, s55\n\t" \
  "s_add_u32 m0, s54, 0\n\t" \
  "s_add_u32 s60, s61, 0\n\t" \
  "buffer_load_dwordx4 v75, %[Wo], s60 offen lds\n\t" \
  "s_add_u32 m0, s54, 1024\n\t" \
  "s_add_u32 s60, s61, 9216\n\t" \
  "buffer_load_dwordx4 v75, %[Wo], s60 offen lds\n\t" \
  "s_add_u32 m0, s54, 2048\n\t" \
  "s_add_u32 s60, s61, 18432\n\t" \
  "buffer_load_dwordx4 v75, %[Wo], s60 offen lds\n\t" \
  "s_add_u32 m0, s54, 3072\n\t" \
  "s_add_u32 s60, s61, 27648\n\t" \
  "buffer_load_dwordx4 v75, %[Wo], s60 offen lds\n\t" \
  "s_add_u32 m0, s54, 16384\n\t" \
  "s_mov_b32 s60, 0\n\t" \
  "buffer_load_dwordx4 v71, %[Wq], s60 offen lds\n\t" \
  "s_add_u32 m0, s54, 17408\n\t" \
  "s_mov_b32 s60, 32768\n\t" \
  "buffer_load_dwordx4 v71, %[Wq], s60 offen lds\n\t" \
  "s_add_u32 m0, s54, 18432\n\t" \
  "s_mov_b32 s60, 65536\n\t" \
  "buffer_load_dwordx4 v71, %[Wq], s60 offen lds\n\t" \
  "s_add_u32 m0, s54, 19456\n\t" \
  "s_mov_b32 s60, 98304\n\t" \
  "buffer_load_dwordx4 v71, %[Wq], s60 offen lds\n\t" \
  "s_add_u32 m0, s54, 53248\n\t" \
  "s_mov_b32 s60, 0\n\t" \
  "buffer_load_dwordx4 v71, %[Wk], s60 offen lds\n\t" \
  "s_add_u32 m0, s54, 54272\n\t" \
  "s_mov_b32 s60, 32768\n\t" \
  "buffer_load_dwordx4 v71, %[Wk], s60 offen lds\n\t" \
  "s_add_u32 m0, s54, 55296\n\t" \
  "s_mov_b32 s60, 65536\n\t" \
  "buffer_load_dwordx4 v71, %[Wk], s60 offen lds\n\t" \
  "s_add_u32 m0, s54, 56320\n\t" \
  "s_mov_b32 s60, 98304\n\t" \
  "buffer_load_dwordx4 v71, %[Wk], s60 offen lds\n\t" \
  "s_xor_b32 s61, s40, 0\n\t" \
  "s_mul_i32 s61, s61, 36864\n\t" \
  "s_add_u32 s61, s61, s55\n\t" \
  "s_add_u32 m0, s54, 90112\n\t" \
  "s_add_u32 s60, s61, 512\n\t" \
  "buffer_load_dwordx4 v75, %[Wo], s60 offen lds\n\t" \
  "s_add_u32 m0, s54, 91136\n\t" \
  "s_add_u32 s60, s61, 9728\n\t" \
  "buffer_load_dwordx4 v75, %[Wo], s60 offen lds\n\t" \
  "s_add_u32 m0, s54, 92160\n\t" \
  "s_add_u32 s60, s61, 18944\n\t" \
  "buffer_load_dwordx4 v75, %[Wo], s60 offen lds\n\t" \
  "s_add_u32 m0, s54, 93184\n\t" \
  "s_add_u32 s60, s61, 28160\n\t" \
  "buffer_load_dwordx4 v75, %[Wo], s60 offen lds\n\t" \
  "s_add_u32 m0, s54, 126976\n\t" \
  "s_mov_b32 s60, 0\n\t" \
  "buffer_load_dwordx4 v71, %[Wv], s60 offen lds\n\t" \
  "s_add_u32 m0, s54, 128000\n\t" \
  "s_mov_b32 s60, 32768\n\t" \
  "buffer_load_dwordx4 v71, %[Wv], s60 offen lds\n\t" \
  "s_add_u32 m0, s54, 129024\n\t" \
  "s_mov_b32 s60, 65536\n\t" \
  "buffer_load_dwordx4 v71, %[Wv], s60 offen lds\n\t" \
  "s_add_u32 m0, s54, 130048\n\t" \
  "s_mov_b32 s60, 98304\n\t" \
  "buffer_load_dwordx4 v71, %[Wv], s60 offen lds\n\t" \
  "v_and_b32 v1, 0x300, v69\n\t" \
  "v_lshlrev_b32 v65, 3, v1\n\t" \
  "v_and_b32 v1, 0xc0, v69\n\t" \
  "v_lshl_add_u32 v65, v1, 2, v65\n\t" \
  "s_lshl_b32 s60, s40, 6\n\t" \
  "v_xor_b32 v1, s60, v1\n\t" \
  "v_and_b32 v2, 48, v69\n\t" \
  "v_add3_u32 v65, v65, v1, v2\n\t" \
  "v_lshrrev_b32 v2, 1, v2\n\t" \
  "v_sub_u32 v66, v65, v2\n\t" \
  "v_bfe_u32 v0, v69, 8, 1\n\t" \
  "v_bfe_u32 v1, v69, 9, 1\n\t" \
  "v_lshlrev_b32 v0, 2, v0\n\t" \
  "v_add_u32 v0, v0, v1\n\t" \
  "v_lshlrev_b32 v0, 9, v0\n\t" \
  "v_lshrrev_b32 v1, 1, v69\n\t" \
  "v_and_b32 v1, 0x60, v1\n\t" \
  "v_and_b32 v2, 16, v69\n\t" \
  "v_bfe_u32 v3, v69, 5, 1\n\t" \
  "v_add3_u32 v64, v0, v1, v2\n\t" \
  "v_lshl_add_u32 v64, v3, 2, v64\n\t" \
  "v_add_u32 v64, s54, v64\n\t" \
  "v_lshl_add_u32 v67, v3, 3, v0\n\t" \
  "v_bfe_u32 v1, v69, 7, 1\n\t" \
  "v_lshl_add_u32 v67, v1, 2, v67\n\t" \
  "v_bfe_u32 v1, v69, 6, 1\n\t" \
  "v_lshl_add_u32 v67, v1, 4, v67\n\t" \
  "v_bfe_u32 v1, v69, 4, 1\n\t" \
  "v_lshl_add_u32 v67, v1, 1, v67\n\t" \
  "s_lshl_b32 s60, s40, 5\n\t" \
  "v_add_u32 v67, s60, v67\n\t" \
  "v_add_u32 v78, 0, v64\n\t" \
  "v_add_u32 v79, 1024, v64\n\t" \
  "s_waitcnt vmcnt(16) lgkmcnt(0)\n\t" \
  "s_barrier\n\t" \
  "ds_read2_b32 v[80:81], v78 offset0:0 offset1:2\n\t" \
  "ds_read2_b32 v[82:83], v78 offset0:32 offset1:34\n\t" \
  "ds_read2_b32 v[84:85], v78 offset0:64 offset1:66\n\t" \
  "ds_read2_b32 v[86:87], v78 offset0:96 offset1:98\n\t" \
  "ds_read2_b32 v[88:89], v79 offset0:0 offset1:2\n\t" \
  "ds_read2_b32 v[90:91], v79 offset0:32 offset1:34\n\t" \
  "ds_read2_b32 v[92:93], v79 offset0:64 offset1:66\n\t" \
  "ds_read2_b32 v[94:95], v79 offset0:96 offset1:98\n\t" \
  "s_waitcnt lgkmcnt(6)\n\t" \
  "v_perm_b32 v112, v82, v80, s62\n\t" \
  "v_perm_b32 v113, v82, v80, s63\n\t" \
  "s_waitcnt lgkmcnt(4)\n\t" \
  "v_perm_b32 v114, v86, v84, s62\n\t" \
  "v_perm_b32 v115, v86, v84, s63\n\t" \
  "s_waitcnt lgkmcnt(2)\n\t" \
  "v_perm_b32 v116, v90, v88, s62\n\t" \
  "v_perm_b32 v117, v90, v88, s63\n\t" \
  "s_waitcnt lgkmcnt(0)\n\t" \
  "v_perm_b32 v118, v94, v92, s62\n\t" \
  "v_perm_b32 v119, v94, v92, s63\n\t" \
  "ds_write_b128 v70, v[112:115] offset:20480\n\t" \
  "ds_write_b128 v70, v[116:119] offset:21504\n\t" \
  "v_perm_b32 v120, v83, v81, s62\n\t" \
  "v_perm_b32 v121, v83, v81, s63\n\t" \
  "v_perm_b32 v122, v87, v85, s62\n\t" \
  "v_perm_b32 v123, v87, v85, s63\n\t" \
  "v_perm_b32 v124, v91, v89, s62\n\t" \
  "v_perm_b32 v125, v91, v89, s63\n\t" \
  "v_perm_b32 v126, v95, v93, s62\n\t" \
  "v_perm_b32 v127, v95, v93, s63\n\t" \
  "ds_write_b128 v70, v[120:123] offset:16384\n\t" \
  "ds_write_b128 v70, v[124:127] offset:17408\n\t" \
  "v_add_u32 v78, 16384, v65\n\t" \
  "s_waitcnt vmcnt(12) lgkmcnt(0)\n\t" \
  "s_barrier\n\t" \
  "ds_read_b64_tr_b16 v[0:1], v78 offset:0\n\t" \
  "ds_read_b64_tr_b16 v[2:3], v78 offset:1024\n\t" \
  "ds_read_b64_tr_b16 v[4:5], v78 offset:8192\n\t" \
  "ds_read_b64_tr_b16 v[6:7], v78 offset:9216\n\t" \
  "ds_read_b64_tr_b16 v[8:9], v78 offset:8\n\t" \
  "ds_read_b64_tr_b16 v[10:11], v78 offset:1032\n\t" \
  "ds_read_b64_tr_b16 v[12:13], v78 offset:8200\n\t" \
  "ds_read_b64_tr_b16 v[14:15], v78 offset:9224\n\t" \
  "s_add_u32 m0, s54, 0\n\t" \
  "s_mov_b32 s60, 524288\n\t" \
  "buffer_load_dwordx4 v71, %[Wq], s60 offen lds\n\t" \
  "s_add_u32 m0, s54, 1024\n\t" \
  "s_mov_b32 s60, 557056\n\t" \
  "buffer_load_dwordx4 v71, %[Wq], s60 offen lds\n\t" \
  "s_add_u32 m0, s54, 2048\n\t" \
  "s_mov_b32 s60, 589824\n\t" \
  "buffer_load_dwordx4 v71, %[Wq], s60 offen lds\n\t" \
  "s_add_u32 m0, s54, 3072\n\t" \
  "s_mov_b32 s60, 622592\n\t" \
  "buffer_load_dwordx4 v71, %[Wq], s60 offen lds\n\t" \
  "v_add_u32 v78, 53248, v65\n\t" \
  "s_waitcnt vmcnt(12) lgkmcnt(0)\n\t" \
  "s_barrier\n\t" \
  "ds_read_b64_tr_b16 a[0:1], v78 offset:0\n\t" \
  "ds_read_b64_tr_b16 a[2:3], v78 offset:1024\n\t" \
  "ds_read_b64_tr_b16 a[4:5], v78 offset:8192\n\t" \
  "ds_read_b64_tr_b16 a[6:7], v78 offset:9216\n\t" \
  "ds_read_b64_tr_b16 a[8:9], v78 offset:8\n\t" \
  "ds_read_b64_tr_b16 a[10:11], v78 offset:1032\n\t" \
  "ds_read_b64_tr_b16 a[12:13], v78 offset:8200\n\t" \
  "ds_read_b64_tr_b16 a[14:15], v78 offset:9224\n\t" \
  "s_xor_b32 s61, s40, 1\n\t" \
  "s_mul_i32 s61, s61, 36864\n\t" \
  "s_add_u32 s61, s61, s55\n\t" \
  "s_add_u32 m0, s54, 16384\n\t" \
  "s_add_u32 s60, s61, 0\n\t" \
  "buffer_load_dwordx4 v75, %[Wo], s60 offen lds\n\t" \
  "s_add_u32 m0, s54, 17408\n\t" \
  "s_add_u32 s60, s61, 9216\n\t" \
  "buffer_load_dwordx4 v75, %[Wo], s60 offen lds\n\t" \
  "s_add_u32 m0, s54, 18432\n\t" \
  "s_add_u32 s60, s61, 18432\n\t" \
  "buffer_load_dwordx4 v75, %[Wo], s60 offen lds\n\t" \
  "s_add_u32 m0, s54, 19456\n\t" \
  "s_add_u32 s60, s61, 27648\n\t" \
  "buffer_load_dwordx4 v75, %[Wo], s60 offen lds\n\t" \
  "v_add_u32 v78, 90112, v64\n\t" \
  "v_add_u32 v79, 91136, v64\n\t" \
  "s_waitcnt vmcnt(12) lgkmcnt(0)\n\t" \
  "s_barrier\n\t" \
  "ds_read2_b32 v[80:81], v78 offset0:0 offset1:2\n\t" \
  "ds_read2_b32 v[82:83], v78 offset0:32 offset1:34\n\t" \
  "ds_read2_b32 v[84:85], v78 offset0:64 offset1:66\n\t" \
  "ds_read2_b32 v[86:87], v78 offset0:96 offset1:98\n\t" \
  "ds_read2_b32 v[88:89], v79 offset0:0 offset1:2\n\t" \
  "ds_read2_b32 v[90:91], v79 offset0:32 offset1:34\n\t" \
  "ds_read2_b32 v[92:93], v79 offset0:64 offset1:66\n\t" \
  "ds_read2_b32 v[94:95], v79 offset0:96 offset1:98\n\t" \
  "s_add_u32 m0, s54, 53248\n\t" \
  "s_mov_b32 s60, 524288\n\t" \
  "buffer_load_dwordx4 v71, %[Wk], s60 offen lds\n\t" \
  "s_add_u32 m0, s54, 54272\n\t" \
  "s_mov_b32 s60, 557056\n\t" \
  "buffer_load_dwordx4 v71, %[Wk], s60 offen lds\n\t" \
  "s_add_u32 m0, s54, 55296\n\t" \
  "s_mov_b32 s60, 589824\n\t" \
  "buffer_load_dwordx4 v71, %[Wk], s60 offen lds\n\t" \
  "s_add_u32 m0, s54, 56320\n\t" \
  "s_mov_b32 s60, 622592\n\t" \
  "buffer_load_dwordx4 v71, %[Wk], s60 offen lds\n\t" \
  "s_waitcnt lgkmcnt(6)\n\t" \
  "v_perm_b32 v112, v82, v80, s62\n\t" \
  "v_perm_b32 v113, v82, v80, s63\n\t" \
  "s_waitcnt lgkmcnt(4)\n\t" \
  "v_perm_b32 v114, v86, v84, s62\n\t" \
  "v_perm_b32 v115, v86, v84, s63\n\t" \
  "s_waitcnt lgkmcnt(2)\n\t" \
  "v_perm_b32 v116, v90, v88, s62\n\t" \
  "v_perm_b32 v117, v90, v88, s63\n\t" \
  "s_waitcnt lgkmcnt(0)\n\t" \
  "v_perm_b32 v118, v94, v92, s62\n\t" \
  "v_perm_b32 v119, v94, v92, s63\n\t" \
  "ds_write_b128 v70, v[112:115] offset:22528\n\t" \
  "ds_write_b128 v70, v[116:119] offset:23552\n\t" \
  "v_perm_b32 v120, v83, v81, s62\n\t" \
  "v_perm_b32 v121, v83, v81, s63\n\t" \
  "v_perm_b32 v122, v87, v85, s62\n\t" \
  "v_perm_b32 v123, v87, v85, s63\n\t" \
  "v_perm_b32 v124, v91, v89, s62\n\t" \
  "v_perm_b32 v125, v91, v89, s63\n\t" \
  "v_perm_b32 v126, v95, v93, s62\n\t" \
  "v_perm_b32 v127, v95, v93, s63\n\t" \
  "ds_write_b128 v70, v[120:123] offset:18432\n\t" \
  "ds_write_b128 v70, v[124:127] offset:19456\n\t" \
  "v_add_u32 v78, 126976, v66\n\t" \
  "s_waitcnt vmcnt(12) lgkmcnt(0)\n\t" \
  "s_barrier\n\t" \
  "ds_read_b64_tr_b16 a[16:17], v78 offset:0\n\t" \
  "ds_read_b64_tr_b16 a[18:19], v78 offset:1024\n\t" \
  "ds_read_b64_tr_b16 a[20:21], v78 offset:8192\n\t" \
  "ds_read_b64_tr_b16 a[22:23], v78 offset:9216\n\t" \
  "ds_read_b64_tr_b16 a[24:25], v78 offset:32\n\t" \
  "ds_read_b64_tr_b16 a[26:27], v78 offset:1056\n\t" \
  "ds_read_b64_tr_b16 a[28:29], v78 offset:8224\n\t" \
  "ds_read_b64_tr_b16 a[30:31], v78 offset:9248\n\t" \
  "s_add_u32 m0, s54, 90112\n\t" \
  "s_mov_b32 s60, 524288\n\t" \
  "buffer_load_dwordx4 v71, %[Wv], s60 offen lds\n\t" \
  "s_add_u32 m0, s54, 91136\n\t" \
  "s_mov_b32 s60, 557056\n\t" \
  "buffer_load_dwordx4 v71, %[Wv], s60 offen lds\n\t" \
  "s_add_u32 m0, s54, 92160\n\t" \
  "s_mov_b32 s60, 589824\n\t" \
  "buffer_load_dwordx4 v71, %[Wv], s60 offen lds\n\t" \
  "s_add_u32 m0, s54, 93184\n\t" \
  "s_mov_b32 s60, 622592\n\t" \
  "buffer_load_dwordx4 v71, %[Wv], s60 offen lds\n\t" \
  "v_add_u32 v78, 0, v65\n\t" \
  "s_waitcnt vmcnt(12) lgkmcnt(0)\n\t" \
  "s_barrier\n\t" \
  "ds_read_b64_tr_b16 a[32:33], v78 offset:0\n\t" \
  "ds_read_b64_tr_b16 a[34:35], v78 offset:1024\n\t" \
  "ds_read_b64_tr_b16 a[36:37], v78 offset:8192\n\t" \
  "ds_read_b64_tr_b16 a[38:39], v78 offset:9216\n\t" \
  "ds_read_b64_tr_b16 a[40:41], v78 offset:8\n\t" \
  "ds_read_b64_tr_b16 a[42:43], v78 offset:1032\n\t" \
  "ds_read_b64_tr_b16 a[44:45], v78 offset:8200\n\t" \
  "ds_read_b64_tr_b16 a[46:47], v78 offset:9224\n\t" \
  "s_xor_b32 s61, s40, 1\n\t" \
  "s_mul_i32 s61, s61, 36864\n\t" \
  "s_add_u32 s61, s61, s55\n\t" \
  "s_add_u32 m0, s54, 126976\n\t" \
  "s_add_u32 s60, s61, 512\n\t" \
  "buffer_load_dwordx4 v75, %[Wo], s60 offen lds\n\t" \
  "s_add_u32 m0, s54, 128000\n\t" \
  "s_add_u32 s60, s61, 9728\n\t" \
  "buffer_load_dwordx4 v75, %[Wo], s60 offen lds\n\t" \
  "s_add_u32 m0, s54, 129024\n\t" \
  "s_add_u32 s60, s61, 18944\n\t" \
  "buffer_load_dwordx4 v75, %[Wo], s60 offen lds\n\t" \
  "s_add_u32 m0, s54, 130048\n\t" \
  "s_add_u32 s60, s61, 28160\n\t" \
  "buffer_load_dwordx4 v75, %[Wo], s60 offen lds\n\t" \
  "v_add_u32 v78, 16384, v64\n\t" \
  "v_add_u32 v79, 17408, v64\n\t" \
  "s_waitcnt vmcnt(12) lgkmcnt(0)\n\t" \
  "s_barrier\n\t" \
  "ds_read2_b32 v[80:81], v78 offset0:0 offset1:2\n\t" \
  "ds_read2_b32 v[82:83], v78 offset0:32 offset1:34\n\t" \
  "ds_read2_b32 v[84:85], v78 offset0:64 offset1:66\n\t" \
  "ds_read2_b32 v[86:87], v78 offset0:96 offset1:98\n\t" \
  "ds_read2_b32 v[88:89], v79 offset0:0 offset1:2\n\t" \
  "ds_read2_b32 v[90:91], v79 offset0:32 offset1:34\n\t" \
  "ds_read2_b32 v[92:93], v79 offset0:64 offset1:66\n\t" \
  "ds_read2_b32 v[94:95], v79 offset0:96 offset1:98\n\t" \
  "s_add_u32 m0, s54, 0\n\t" \
  "s_mov_b32 s60, 1048576\n\t" \
  "buffer_load_dwordx4 v71, %[Wq], s60 offen lds\n\t" \
  "s_add_u32 m0, s54, 1024\n\t" \
  "s_mov_b32 s60, 1081344\n\t" \
  "buffer_load_dwordx4 v71, %[Wq], s60 offen lds\n\t" \
  "s_add_u32 m0, s54, 2048\n\t" \
  "s_mov_b32 s60, 1114112\n\t" \
  "buffer_load_dwordx4 v71, %[Wq], s60 offen lds\n\t" \
  "s_add_u32 m0, s54, 3072\n\t" \
  "s_mov_b32 s60, 1146880\n\t" \
  "buffer_load_dwordx4 v71, %[Wq], s60 offen lds\n\t" \
  "s_waitcnt lgkmcnt(6)\n\t" \
  "v_perm_b32 v112, v82, v80, s62\n\t" \
  "v_perm_b32 v113, v82, v80, s63\n\t" \
  "s_waitcnt lgkmcnt(4)\n\t" \
  "v_perm_b32 v114, v86, v84, s62\n\t" \
  "v_perm_b32 v115, v86, v84, s63\n\t" \
  "s_waitcnt lgkmcnt(2)\n\t" \
  "v_perm_b32 v116, v90, v88, s62\n\t" \
  "v_perm_b32 v117, v90, v88, s63\n\t" \
  "s_waitcnt lgkmcnt(0)\n\t" \
  "v_perm_b32 v118, v94, v92, s62\n\t" \
  "v_perm_b32 v119, v94, v92, s63\n\t" \
  "ds_write_b128 v70, v[112:115] offset:24576\n\t" \
  "ds_write_b128 v70, v[116:119] offset:25600\n\t" \
  "v_perm_b32 v200, v83, v81, s62\n\t" \
  "v_perm_b32 v201, v83, v81, s63\n\t" \
  "v_perm_b32 v202, v87, v85, s62\n\t" \
  "v_perm_b32 v203, v87, v85, s63\n\t" \
  "v_perm_b32 v204, v91, v89, s62\n\t" \
  "v_perm_b32 v205, v91, v89, s63\n\t" \
  "v_perm_b32 v206, v95, v93, s62\n\t" \
  "v_perm_b32 v207, v95, v93, s63\n\t" \
  "v_add_u32 v78, 53248, v65\n\t" \
  "s_waitcnt vmcnt(12) lgkmcnt(0)\n\t" \
  "s_barrier\n\t" \
  "ds_read_b64_tr_b16 a[48:49], v78 offset:0\n\t" \
  "ds_read_b64_tr_b16 a[50:51], v78 offset:1024\n\t" \
  "ds_read_b64_tr_b16 a[52:53], v78 offset:8192\n\t" \
  "ds_read_b64_tr_b16 a[54:55], v78 offset:9216\n\t" \
  "ds_read_b64_tr_b16 a[56:57], v78 offset:8\n\t" \
  "ds_read_b64_tr_b16 a[58:59], v78 offset:1032\n\t" \
  "ds_read_b64_tr_b16 a[60:61], v78 offset:8200\n\t" \
  "ds_read_b64_tr_b16 a[62:63], v78 offset:9224\n\t" \
  "s_add_u32 m0, s54, 16384\n\t" \
  "s_mov_b32 s60, 1048576\n\t" \
  "buffer_load_dwordx4 v71, %[Wk], s60 offen lds\n\t" \
  "s_add_u32 m0, s54, 17408\n\t" \
  "s_mov_b32 s60, 1081344\n\t" \
  "buffer_load_dwordx4 v71, %[Wk], s60 offen lds\n\t" \
  "s_add_u32 m0, s54, 18432\n\t" \
  "s_mov_b32 s60, 1114112\n\t" \
  "buffer_load_dwordx4 v71, %[Wk], s60 offen lds\n\t" \
  "s_add_u32 m0, s54, 19456\n\t" \
  "s_mov_b32 s60, 1146880\n\t" \
  "buffer_load_dwordx4 v71, %[Wk], s60 offen lds\n\t" \
  "v_add_u32 v78, 90112, v66\n\t" \
  "s_waitcnt vmcnt(12) lgkmcnt(0)\n\t" \
  "s_barrier\n\t" \
  "ds_read_b64_tr_b16 a[64:65], v78 offset:0\n\t" \
  "ds_read_b64_tr_b16 a[66:67], v78 offset:1024\n\t" \
  "ds_read_b64_tr_b16 a[68:69], v78 offset:8192\n\t" \
  "ds_read_b64_tr_b16 a[70:71], v78 offset:9216\n\t" \
  "ds_read_b64_tr_b16 a[72:73], v78 offset:32\n\t" \
  "ds_read_b64_tr_b16 a[74:75], v78 offset:1056\n\t" \
  "ds_read_b64_tr_b16 a[76:77], v78 offset:8224\n\t" \
  "ds_read_b64_tr_b16 a[78:79], v78 offset:9248\n\t" \
  "s_xor_b32 s61, s40, 2\n\t" \
  "s_mul_i32 s61, s61, 36864\n\t" \
  "s_add_u32 s61, s61, s55\n\t" \
  "s_add_u32 m0, s54, 53248\n\t" \
  "s_add_u32 s60, s61, 0\n\t" \
  "buffer_load_dwordx4 v75, %[Wo], s60 offen lds\n\t" \
  "s_add_u32 m0, s54, 54272\n\t" \
  "s_add_u32 s60, s61, 9216\n\t" \
  "buffer_load_dwordx4 v75, %[Wo], s60 offen lds\n\t" \
  "s_add_u32 m0, s54, 55296\n\t" \
  "s_add_u32 s60, s61, 18432\n\t" \
  "buffer_load_dwordx4 v75, %[Wo], s60 offen lds\n\t" \
  "s_add_u32 m0, s54, 56320\n\t" \
  "s_add_u32 s60, s61, 27648\n\t" \
  "buffer_load_dwordx4 v75, %[Wo], s60 offen lds\n\t" \
  "v_add_u32 v78, 126976, v64\n\t" \
  "v_add_u32 v79, 128000, v64\n\t" \
  "s_waitcnt vmcnt(12) lgkmcnt(0)\n\t" \
  "s_barrier\n\t" \
  "ds_read2_b32 v[80:81], v78 offset0:0 offset1:2\n\t" \
  "ds_read2_b32 v[82:83], v78 offset0:32 offset1:34\n\t" \
  "ds_read2_b32 v[84:85], v78 offset0:64 offset1:66\n\t" \
  "ds_read2_b32 v[86:87], v78 offset0:96 offset1:98\n\t" \
  "ds_read2_b32 v[88:89], v79 offset0:0 offset1:2\n\t" \
  "ds_read2_b32 v[90:91], v79 offset0:32 offset1:34\n\t" \
  "ds_read2_b32 v[92:93], v79 offset0:64 offset1:66\n\t" \
  "ds_read2_b32 v[94:95], v79 offset0:96 offset1:98\n\t" \
  "s_add_u32 m0, s54, 90112\n\t" \
  "s_mov_b32 s60, 1048576\n\t" \
  "buffer_load_dwordx4 v71, %[Wv], s60 offen lds\n\t" \
  "s_add_u32 m0, s54, 91136\n\t" \
  "s_mov_b32 s60, 1081344\n\t" \
  "buffer_load_dwordx4 v71, %[Wv], s60 offen lds\n\t" \
  "s_add_u32 m0, s54, 92160\n\t" \
  "s_mov_b32 s60, 1114112\n\t" \
  "buffer_load_dwordx4 v71, %[Wv], s60 offen lds\n\t" \
  "s_add_u32 m0, s54, 93184\n\t" \
  "s_mov_b32 s60, 1146880\n\t" \
  "buffer_load_dwordx4 v71, %[Wv], s60 offen lds\n\t" \
  "s_waitcnt lgkmcnt(6)\n\t" \
  "v_perm_b32 v112, v82, v80, s62\n\t" \
  "v_perm_b32 v113, v82, v80, s63\n\t" \
  "s_waitcnt lgkmcnt(4)\n\t" \
  "v_perm_b32 v114, v86, v84, s62\n\t" \
  "v_perm_b32 v115, v86, v84, s63\n\t" \
  "s_waitcnt lgkmcnt(2)\n\t" \
  "v_perm_b32 v116, v90, v88, s62\n\t" \
  "v_perm_b32 v117, v90, v88, s63\n\t" \
  "s_waitcnt lgkmcnt(0)\n\t" \
  "v_perm_b32 v118, v94, v92, s62\n\t" \
  "v_perm_b32 v119, v94, v92, s63\n\t" \
  "ds_write_b128 v70, v[112:115] offset:26624\n\t" \
  "ds_write_b128 v70, v[116:119] offset:27648\n\t" \
  "v_perm_b32 v232, v83, v81, s62\n\t" \
  "v_perm_b32 v233, v83, v81, s63\n\t" \
  "v_perm_b32 v234, v87, v85, s62\n\t" \
  "v_perm_b32 v235, v87, v85, s63\n\t" \
  "v_perm_b32 v236, v91, v89, s62\n\t" \
  "v_perm_b32 v237, v91, v89, s63\n\t" \
  "v_perm_b32 v238, v95, v93, s62\n\t" \
  "v_perm_b32 v239, v95, v93, s63\n\t" \
  "v_add_u32 v78, 0, v65\n\t" \
  "s_waitcnt vmcnt(12) lgkmcnt(0)\n\t" \
  "s_barrier\n\t" \
  "ds_read_b64_tr_b16 a[80:81], v78 offset:0\n\t" \
  "ds_read_b64_tr_b16 a[82:83], v78 offset:1024\n\t" \
  "ds_read_b64_tr_b16 a[84:85], v78 offset:8192\n\t" \
  "ds_read_b64_tr_b16 a[86:87], v78 offset:9216\n\t" \
  "ds_read_b64_tr_b16 a[88:89], v78 offset:8\n\t" \
  "ds_read_b64_tr_b16 a[90:91], v78 offset:1032\n\t" \
  "ds_read_b64_tr_b16 a[92:93], v78 offset:8200\n\t" \
  "ds_read_b64_tr_b16 a[94:95], v78 offset:9224\n\t" \
  "s_add_u32 m0, s54, 126976\n\t" \
  "s_mov_b32 s60, 1572864\n\t" \
  "buffer_load_dwordx4 v71, %[Wq], s60 offen lds\n\t" \
  "s_add_u32 m0, s54, 128000\n\t" \
  "s_mov_b32 s60, 1605632\n\t" \
  "buffer_load_dwordx4 v71, %[Wq], s60 offen lds\n\t" \
  "s_add_u32 m0, s54, 129024\n\t" \
  "s_mov_b32 s60, 1638400\n\t" \
  "buffer_load_dwordx4 v71, %[Wq], s60 offen lds\n\t" \
  "s_add_u32 m0, s54, 130048\n\t" \
  "s_mov_b32 s60, 1671168\n\t" \
  "buffer_load_dwordx4 v71, %[Wq], s60 offen lds\n\t" \
  "v_add_u32 v78, 16384, v65\n\t" \
  "s_waitcnt vmcnt(12) lgkmcnt(0)\n\t" \
  "s_barrier\n\t" \
  "ds_read_b64_tr_b16 v[32:33], v78 offset:0\n\t" \
  "ds_read_b64_tr_b16 v[34:35], v78 offset:1024\n\t" \
  "ds_read_b64_tr_b16 v[36:37], v78 offset:8192\n\t" \
  "ds_read_b64_tr_b16 v[38:39], v78 offset:9216\n\t" \
  "ds_read_b64_tr_b16 a[96:97], v78 offset:8\n\t" \
  "ds_read_b64_tr_b16 a[98:99], v78 offset:1032\n\t" \
  "ds_read_b64_tr_b16 a[100:101], v78 offset:8200\n\t" \
  "ds_read_b64_tr_b16 a[102:103], v78 offset:9224\n\t" \
  "s_xor_b32 s61, s40, 2\n\t" \
  "s_mul_i32 s61, s61, 36864\n\t" \
  "s_add_u32 s61, s61, s55\n\t" \
  "s_add_u32 m0, s54, 0\n\t" \
  "s_add_u32 s60, s61, 512\n\t" \
  "buffer_load_dwordx4 v75, %[Wo], s60 offen lds\n\t" \
  "s_add_u32 m0, s54, 1024\n\t" \
  "s_add_u32 s60, s61, 9728\n\t" \
  "buffer_load_dwordx4 v75, %[Wo], s60 offen lds\n\t" \
  "s_add_u32 m0, s54, 2048\n\t" \
  "s_add_u32 s60, s61, 18944\n\t" \
  "buffer_load_dwordx4 v75, %[Wo], s60 offen lds\n\t" \
  "s_add_u32 m0, s54, 3072\n\t" \
  "s_add_u32 s60, s61, 28160\n\t" \
  "buffer_load_dwordx4 v75, %[Wo], s60 offen lds\n\t" \
  "v_add_u32 v78, 53248, v64\n\t" \
  "v_add_u32 v79, 54272, v64\n\t" \
  "s_waitcnt vmcnt(12) lgkmcnt(0)\n\t" \
  "s_barrier\n\t" \
  "ds_read2_b32 v[80:81], v78 offset0:0 offset1:2\n\t" \
  "ds_read2_b32 v[82:83], v78 offset0:32 offset1:34\n\t" \
  "ds_read2_b32 v[84:85], v78 offset0:64 offset1:66\n\t" \
  "ds_read2_b32 v[86:87], v78 offset0:96 offset1:98\n\t" \
  "ds_read2_b32 v[88:89], v79 offset0:0 offset1:2\n\t" \
  "ds_read2_b32 v[90:91], v79 offset0:32 offset1:34\n\t" \
  "ds_read2_b32 v[92:93], v79 offset0:64 offset1:66\n\t" \
  "ds_read2_b32 v[94:95], v79 offset0:96 offset1:98\n\t" \
  "s_add_u32 m0, s54, 16384\n\t" \
  "s_mov_b32 s60, 1572864\n\t" \
  "buffer_load_dwordx4 v71, %[Wk], s60 offen lds\n\t" \
  "s_add_u32 m0, s54, 17408\n\t" \
  "s_mov_b32 s60, 1605632\n\t" \
  "buffer_load_dwordx4 v71, %[Wk], s60 offen lds\n\t" \
  "s_add_u32 m0, s54, 18432\n\t" \
  "s_mov_b32 s60, 1638400\n\t" \
  "buffer_load_dwordx4 v71, %[Wk], s60 offen lds\n\t" \
  "s_add_u32 m0, s54, 19456\n\t" \
  "s_mov_b32 s60, 1671168\n\t" \
  "buffer_load_dwordx4 v71, %[Wk], s60 offen lds\n\t" \
  "s_waitcnt lgkmcnt(6)\n\t" \
  "v_perm_b32 v112, v82, v80, s62\n\t" \
  "v_perm_b32 v113, v82, v80, s63\n\t" \
  "s_waitcnt lgkmcnt(4)\n\t" \
  "v_perm_b32 v114, v86, v84, s62\n\t" \
  "v_perm_b32 v115, v86, v84, s63\n\t" \
  "s_waitcnt lgkmcnt(2)\n\t" \
  "v_perm_b32 v116, v90, v88, s62\n\t" \
  "v_perm_b32 v117, v90, v88, s63\n\t" \
  "s_waitcnt lgkmcnt(0)\n\t" \
  "v_perm_b32 v118, v94, v92, s62\n\t" \
  "v_perm_b32 v119, v94, v92, s63\n\t" \
  "ds_write_b128 v70, v[112:115] offset:28672\n\t" \
  "ds_write_b128 v70, v[116:119] offset:29696\n\t" \
  "v_perm_b32 v208, v83, v81, s62\n\t" \
  "v_perm_b32 v209, v83, v81, s63\n\t" \
  "v_perm_b32 v210, v87, v85, s62\n\t" \
  "v_perm_b32 v211, v87, v85, s63\n\t" \
  "v_perm_b32 v212, v91, v89, s62\n\t" \
  "v_perm_b32 v213, v91, v89, s63\n\t" \
  "v_perm_b32 v214, v95, v93, s62\n\t" \
  "v_perm_b32 v215, v95, v93, s63\n\t" \
  "v_add_u32 v78, 90112, v66\n\t" \
  "s_waitcnt vmcnt(12) lgkmcnt(0)\n\t" \
  "s_barrier\n\t" \
  "ds_read_b64_tr_b16 a[104:105], v78 offset:0\n\t" \
  "ds_read_b64_tr_b16 a[106:107], v78 offset:1024\n\t" \
  "ds_read_b64_tr_b16 a[108:109], v78 offset:8192\n\t" \
  "ds_read_b64_tr_b16 a[110:111], v78 offset:9216\n\t" \
  "ds_read_b64_tr_b16 a[112:113], v78 offset:32\n\t" \
  "ds_read_b64_tr_b16 a[114:115], v78 offset:1056\n\t" \
  "ds_read_b64_tr_b16 a[116:117], v78 offset:8224\n\t" \
  "ds_read_b64_tr_b16 a[118:119], v78 offset:9248\n\t" \
  "s_add_u32 m0, s54, 53248\n\t" \
  "s_mov_b32 s60, 1572864\n\t" \
  "buffer_load_dwordx4 v71, %[Wv], s60 offen lds\n\t" \
  "s_add_u32 m0, s54, 54272\n\t" \
  "s_mov_b32 s60, 1605632\n\t" \
  "buffer_load_dwordx4 v71, %[Wv], s60 offen lds\n\t" \
  "s_add_u32 m0, s54, 55296\n\t" \
  "s_mov_b32 s60, 1638400\n\t" \
  "buffer_load_dwordx4 v71, %[Wv], s60 offen lds\n\t" \
  "s_add_u32 m0, s54, 56320\n\t" \
  "s_mov_b32 s60, 1671168\n\t" \
  "buffer_load_dwordx4 v71, %[Wv], s60 offen lds\n\t" \
  "v_add_u32 v78, 126976, v65\n\t" \
  "s_waitcnt vmcnt(12) lgkmcnt(0)\n\t" \
  "s_barrier\n\t" \
  "ds_read_b64_tr_b16 a[120:121], v78 offset:0\n\t" \
  "ds_read_b64_tr_b16 a[122:123], v78 offset:1024\n\t" \
  "ds_read_b64_tr_b16 a[124:125], v78 offset:8192\n\t" \
  "ds_read_b64_tr_b16 a[126:127], v78 offset:9216\n\t" \
  "ds_read_b64_tr_b16 v[16:17], v78 offset:8\n\t" \
  "ds_read_b64_tr_b16 v[18:19], v78 offset:1032\n\t" \
  "ds_read_b64_tr_b16 v[20:21], v78 offset:8200\n\t" \
  "ds_read_b64_tr_b16 v[22:23], v78 offset:9224\n\t" \
  "s_xor_b32 s61, s40, 3\n\t" \
  "s_mul_i32 s61, s61, 36864\n\t" \
  "s_add_u32 s61, s61, s55\n\t" \
  "s_add_u32 m0, s54, 90112\n\t" \
  "s_add_u32 s60, s61, 0\n\t" \
  "buffer_load_dwordx4 v75, %[Wo], s60 offen lds\n\t" \
  "s_add_u32 m0, s54, 91136\n\t" \
  "s_add_u32 s60, s61, 9216\n\t" \
  "buffer_load_dwordx4 v75, %[Wo], s60 offen lds\n\t" \
  "s_add_u32 m0, s54, 92160\n\t" \
  "s_add_u32 s60, s61, 18432\n\t" \
  "buffer_load_dwordx4 v75, %[Wo], s60 offen lds\n\t" \
  "s_add_u32 m0, s54, 93184\n\t" \
  "s_add_u32 s60, s61, 27648\n\t" \
  "buffer_load_dwordx4 v75, %[Wo], s60 offen lds\n\t" \
  "v_add_u32 v78, 0, v64\n\t" \
  "v_add_u32 v79, 1024, v64\n\t" \
  "s_waitcnt vmcnt(12) lgkmcnt(0)\n\t" \
  "s_barrier\n\t" \
  "ds_read2_b32 v[80:81], v78 offset0:0 offset1:2\n\t" \
  "ds_read2_b32 v[82:83], v78 offset0:32 offset1:34\n\t" \
  "ds_read2_b32 v[84:85], v78 offset0:64 offset1:66\n\t" \
  "ds_read2_b32 v[86:87], v78 offset0:96 offset1:98\n\t" \
  "ds_read2_b32 v[88:89], v79 offset0:0 offset1:2\n\t" \
  "ds_read2_b32 v[90:91], v79 offset0:32 offset1:34\n\t" \
  "ds_read2_b32 v[92:93], v79 offset0:64 offset1:66\n\t" \
  "ds_read2_b32 v[94:95], v79 offset0:96 offset1:98\n\t" \
  "s_add_u32 m0, s54, 126976\n\t" \
  "s_mov_b32 s60, 2097152\n\t" \
  "buffer_load_dwordx4 v71, %[Wq], s60 offen lds\n\t" \
  "s_add_u32 m0, s54, 128000\n\t" \
  "s_mov_b32 s60, 2129920\n\t" \
  "buffer_load_dwordx4 v71, %[Wq], s60 offen lds\n\t" \
  "s_add_u32 m0, s54, 129024\n\t" \
  "s_mov_b32 s60, 2162688\n\t" \
  "buffer_load_dwordx4 v71, %[Wq], s60 offen lds\n\t" \
  "s_add_u32 m0, s54, 130048\n\t" \
  "s_mov_b32 s60, 2195456\n\t" \
  "buffer_load_dwordx4 v71, %[Wq], s60 offen lds\n\t" \
  "s_waitcnt lgkmcnt(6)\n\t" \
  "v_perm_b32 v112, v82, v80, s62\n\t" \
  "v_perm_b32 v113, v82, v80, s63\n\t" \
  "s_waitcnt lgkmcnt(4)\n\t" \
  "v_perm_b32 v114, v86, v84, s62\n\t" \
  "v_perm_b32 v115, v86, v84, s63\n\t" \
  "s_waitcnt lgkmcnt(2)\n\t" \
  "v_perm_b32 v116, v90, v88, s62\n\t" \
  "v_perm_b32 v117, v90, v88, s63\n\t" \
  "s_waitcnt lgkmcnt(0)\n\t" \
  "v_perm_b32 v118, v94, v92, s62\n\t" \
  "v_perm_b32 v119, v94, v92, s63\n\t" \
  "ds_write_b128 v70, v[112:115] offset:30720\n\t" \
  "ds_write_b128 v70, v[116:119] offset:31744\n\t" \
  "v_perm_b32 v240, v83, v81, s62\n\t" \
  "v_perm_b32 v241, v83, v81, s63\n\t" \
  "v_perm_b32 v242, v87, v85, s62\n\t" \
  "v_perm_b32 v243, v87, v85, s63\n\t" \
  "v_perm_b32 v244, v91, v89, s62\n\t" \
  "v_perm_b32 v245, v91, v89, s63\n\t" \
  "v_perm_b32 v246, v95, v93, s62\n\t" \
  "v_perm_b32 v247, v95, v93, s63\n\t" \
  "v_add_u32 v78, 16384, v65\n\t" \
  "s_waitcnt vmcnt(12) lgkmcnt(0)\n\t" \
  "s_barrier\n\t" \
  "ds_read_b64_tr_b16 a[128:129], v78 offset:0\n\t" \
  "ds_read_b64_tr_b16 a[130:131], v78 offset:1024\n\t" \
  "ds_read_b64_tr_b16 a[132:133], v78 offset:8192\n\t" \
  "ds_read_b64_tr_b16 a[134:135], v78 offset:9216\n\t" \
  "ds_read_b64_tr_b16 a[136:137], v78 offset:8\n\t" \
  "ds_read_b64_tr_b16 a[138:139], v78 offset:1032\n\t" \
  "ds_read_b64_tr_b16 a[140:141], v78 offset:8200\n\t" \
  "ds_read_b64_tr_b16 a[142:143], v78 offset:9224\n\t" \
  "s_add_u32 m0, s54, 0\n\t" \
  "s_mov_b32 s60, 2097152\n\t" \
  "buffer_load_dwordx4 v71, %[Wk], s60 offen lds\n\t" \
  "s_add_u32 m0, s54, 1024\n\t" \
  "s_mov_b32 s60, 2129920\n\t" \
  "buffer_load_dwordx4 v71, %[Wk], s60 offen lds\n\t" \
  "s_add_u32 m0, s54, 2048\n\t" \
  "s_mov_b32 s60, 2162688\n\t" \
  "buffer_load_dwordx4 v71, %[Wk], s60 offen lds\n\t" \
  "s_add_u32 m0, s54, 3072\n\t" \
  "s_mov_b32 s60, 2195456\n\t" \
  "buffer_load_dwordx4 v71, %[Wk], s60 offen lds\n\t" \
  "v_add_u32 v78, 53248, v66\n\t" \
  "s_waitcnt vmcnt(12) lgkmcnt(0)\n\t" \
  "s_barrier\n\t" \
  "ds_read_b64_tr_b16 a[144:145], v78 offset:0\n\t" \
  "ds_read_b64_tr_b16 a[146:147], v78 offset:1024\n\t" \
  "ds_read_b64_tr_b16 a[148:149], v78 offset:8192\n\t" \
  "ds_read_b64_tr_b16 a[150:151], v78 offset:9216\n\t" \
  "ds_read_b64_tr_b16 a[152:153], v78 offset:32\n\t" \
  "ds_read_b64_tr_b16 a[154:155], v78 offset:1056\n\t" \
  "ds_read_b64_tr_b16 a[156:157], v78 offset:8224\n\t" \
  "ds_read_b64_tr_b16 a[158:159], v78 offset:9248\n\t" \
  "s_xor_b32 s61, s40, 3\n\t" \
  "s_mul_i32 s61, s61, 36864\n\t" \
  "s_add_u32 s61, s61, s55\n\t" \
  "s_add_u32 m0, s54, 16384\n\t" \
  "s_add_u32 s60, s61, 512\n\t" \
  "buffer_load_dwordx4 v75, %[Wo], s60 offen lds\n\t" \
  "s_add_u32 m0, s54, 17408\n\t" \
  "s_add_u32 s60, s61, 9728\n\t" \
  "buffer_load_dwordx4 v75, %[Wo], s60 offen lds\n\t" \
  "s_add_u32 m0, s54, 18432\n\t" \
  "s_add_u32 s60, s61, 18944\n\t" \
  "buffer_load_dwordx4 v75, %[Wo], s60 offen lds\n\t" \
  "s_add_u32 m0, s54, 19456\n\t" \
  "s_add_u32 s60, s61, 28160\n\t" \
  "buffer_load_dwordx4 v75, %[Wo], s60 offen lds\n\t" \
  "v_add_u32 v78, 90112, v64\n\t" \
  "v_add_u32 v79, 91136, v64\n\t" \
  "s_waitcnt vmcnt(12) lgkmcnt(0)\n\t" \
  "s_barrier\n\t" \
  "ds_read2_b32 v[80:81], v78 offset0:0 offset1:2\n\t" \
  "ds_read2_b32 v[82:83], v78 offset0:32 offset1:34\n\t" \
  "ds_read2_b32 v[84:85], v78 offset0:64 offset1:66\n\t" \
  "ds_read2_b32 v[86:87], v78 offset0:96 offset1:98\n\t" \
  "ds_read2_b32 v[88:89], v79 offset0:0 offset1:2\n\t" \
  "ds_read2_b32 v[90:91], v79 offset0:32 offset1:34\n\t" \
  "ds_read2_b32 v[92:93], v79 offset0:64 offset1:66\n\t" \
  "ds_read2_b32 v[94:95], v79 offset0:96 offset1:98\n\t" \
  "s_add_u32 m0, s54, 53248\n\t" \
  "s_mov_b32 s60, 2097152\n\t" \
  "buffer_load_dwordx4 v71, %[Wv], s60 offen lds\n\t" \
  "s_add_u32 m0, s54, 54272\n\t" \
  "s_mov_b32 s60, 2129920\n\t" \
  "buffer_load_dwordx4 v71, %[Wv], s60 offen lds\n\t" \
  "s_add_u32 m0, s54, 55296\n\t" \
  "s_mov_b32 s60, 2162688\n\t" \
  "buffer_load_dwordx4 v71, %[Wv], s60 offen lds\n\t" \
  "s_add_u32 m0, s54, 56320\n\t" \
  "s_mov_b32 s60, 2195456\n\t" \
  "buffer_load_dwordx4 v71, %[Wv], s60 offen lds\n\t" \
  "s_waitcnt lgkmcnt(6)\n\t" \
  "v_perm_b32 v192, v82, v80, s62\n\t" \
  "v_perm_b32 v193, v82, v80, s63\n\t" \
  "s_waitcnt lgkmcnt(4)\n\t" \
  "v_perm_b32 v194, v86, v84, s62\n\t" \
  "v_perm_b32 v195, v86, v84, s63\n\t" \
  "s_waitcnt lgkmcnt(2)\n\t" \
  "v_perm_b32 v196, v90, v88, s62\n\t" \
  "v_perm_b32 v197, v90, v88, s63\n\t" \
  "s_waitcnt lgkmcnt(0)\n\t" \
  "v_perm_b32 v198, v94, v92, s62\n\t" \
  "v_perm_b32 v199, v94, v92, s63\n\t" \
  "v_perm_b32 v216, v83, v81, s62\n\t" \
  "v_perm_b32 v217, v83, v81, s63\n\t" \
  "v_perm_b32 v218, v87, v85, s62\n\t" \
  "v_perm_b32 v219, v87, v85, s63\n\t" \
  "v_perm_b32 v220, v91, v89, s62\n\t" \
  "v_perm_b32 v221, v91, v89, s63\n\t" \
  "v_perm_b32 v222, v95, v93, s62\n\t" \
  "v_perm_b32 v223, v95, v93, s63\n\t" \
  "v_add_u32 v78, 126976, v65\n\t" \
  "s_waitcnt vmcnt(12) lgkmcnt(0)\n\t" \
  "s_barrier\n\t" \
  "ds_read_b64_tr_b16 a[160:161], v78 offset:0\n\t" \
  "ds_read_b64_tr_b16 a[162:163], v78 offset:1024\n\t" \
  "ds_read_b64_tr_b16 a[164:165], v78 offset:8192\n\t" \
  "ds_read_b64_tr_b16 a[166:167], v78 offset:9216\n\t" \
  "ds_read_b64_tr_b16 a[168:169], v78 offset:8\n\t" \
  "ds_read_b64_tr_b16 a[170:171], v78 offset:1032\n\t" \
  "ds_read_b64_tr_b16 a[172:173], v78 offset:8200\n\t" \
  "ds_read_b64_tr_b16 a[174:175], v78 offset:9224\n\t" \
  "s_add_u32 m0, s54, 90112\n\t" \
  "s_mov_b32 s60, 2621440\n\t" \
  "buffer_load_dwordx4 v71, %[Wq], s60 offen lds\n\t" \
  "s_add_u32 m0, s54, 91136\n\t" \
  "s_mov_b32 s60, 2654208\n\t" \
  "buffer_load_dwordx4 v71, %[Wq], s60 offen lds\n\t" \
  "s_add_u32 m0, s54, 92160\n\t" \
  "s_mov_b32 s60, 2686976\n\t" \
  "buffer_load_dwordx4 v71, %[Wq], s60 offen lds\n\t" \
  "s_add_u32 m0, s54, 93184\n\t" \
  "s_mov_b32 s60, 2719744\n\t" \
  "buffer_load_dwordx4 v71, %[Wq], s60 offen lds\n\t" \
  "v_add_u32 v78, 0, v65\n\t" \
  "s_waitcnt vmcnt(12) lgkmcnt(0)\n\t" \
  "s_barrier\n\t" \
  "ds_read_b64_tr_b16 a[176:177], v78 offset:0\n\t" \
  "ds_read_b64_tr_b16 a[178:179], v78 offset:1024\n\t" \
  "ds_read_b64_tr_b16 a[180:181], v78 offset:8192\n\t" \
  "ds_read_b64_tr_b16 a[182:183], v78 offset:9216\n\t" \
  "ds_read_b64_tr_b16 a[184:185], v78 offset:8\n\t" \
  "ds_read_b64_tr_b16 a[186:187], v78 offset:1032\n\t" \
  "ds_read_b64_tr_b16 a[188:189], v78 offset:8200\n\t" \
  "ds_read_b64_tr_b16 a[190:191], v78 offset:9224\n\t" \
  "s_add_u32 m0, s54, 126976\n\t" \
  "s_add_u32 s60, s75, 0\n\t" \
  "buffer_load_dwordx4 v75, %[Wo], s60 offen lds\n\t" \
  "s_add_u32 m0, s54, 128000\n\t" \
  "s_add_u32 s60, s75, 9216\n\t" \
  "buffer_load_dwordx4 v75, %[Wo], s60 offen lds\n\t" \
  "s_add_u32 m0, s54, 129024\n\t" \
  "s_add_u32 s60, s75, 18432\n\t" \
  "buffer_load_dwordx4 v75, %[Wo], s60 offen lds\n\t" \
  "s_add_u32 m0, s54, 130048\n\t" \
  "s_add_u32 s60, s75, 27648\n\t" \
  "buffer_load_dwordx4 v75, %[Wo], s60 offen lds\n\t" \
  "v_add_u32 v78, 16384, v64\n\t" \
  "v_add_u32 v79, 17408, v64\n\t" \
  "s_waitcnt vmcnt(12) lgkmcnt(0)\n\t" \
  "s_barrier\n\t" \
  "ds_read2_b32 v[80:81], v78 offset0:0 offset1:2\n\t" \
  "ds_read2_b32 v[82:83], v78 offset0:32 offset1:34\n\t" \
  "ds_read2_b32 v[84:85], v78 offset0:64 offset1:66\n\t" \
  "ds_read2_b32 v[86:87], v78 offset0:96 offset1:98\n\t" \
  "ds_read2_b32 v[88:89], v79 offset0:0 offset1:2\n\t" \
  "ds_read2_b32 v[90:91], v79 offset0:32 offset1:34\n\t" \
  "ds_read2_b32 v[92:93], v79 offset0:64 offset1:66\n\t" \
  "ds_read2_b32 v[94:95], v79 offset0:96 offset1:98\n\t" \
  "s_add_u32 m0, s54, 0\n\t" \
  "s_mov_b32 s60, 2621440\n\t" \
  "buffer_load_dwordx4 v71, %[Wk], s60 offen lds\n\t" \
  "s_add_u32 m0, s54, 1024\n\t" \
  "s_mov_b32 s60, 2654208\n\t" \
  "buffer_load_dwordx4 v71, %[Wk], s60 offen lds\n\t" \
  "s_add_u32 m0, s54, 2048\n\t" \
  "s_mov_b32 s60, 2686976\n\t" \
  "buffer_load_dwordx4 v71, %[Wk], s60 offen lds\n\t" \
  "s_add_u32 m0, s54, 3072\n\t" \
  "s_mov_b32 s60, 2719744\n\t" \
  "buffer_load_dwordx4 v71, %[Wk], s60 offen lds\n\t" \
  "s_waitcnt lgkmcnt(6)\n\t" \
  "v_perm_b32 v224, v82, v80, s62\n\t" \
  "v_perm_b32 v225, v82, v80, s63\n\t" \
  "s_waitcnt lgkmcnt(4)\n\t" \
  "v_perm_b32 v226, v86, v84, s62\n\t" \
  "v_perm_b32 v227, v86, v84, s63\n\t" \
  "s_waitcnt lgkmcnt(2)\n\t" \
  "v_perm_b32 v228, v90, v88, s62\n\t" \
  "v_perm_b32 v229, v90, v88, s63\n\t" \
  "s_waitcnt lgkmcnt(0)\n\t" \
  "v_perm_b32 v230, v94, v92, s62\n\t" \
  "v_perm_b32 v231, v94, v92, s63\n\t" \
  "v_perm_b32 v120, v83, v81, s62\n\t" \
  "v_perm_b32 v121, v83, v81, s63\n\t" \
  "v_perm_b32 v122, v87, v85, s62\n\t" \
  "v_perm_b32 v123, v87, v85, s63\n\t" \
  "v_perm_b32 v124, v91, v89, s62\n\t" \
  "v_perm_b32 v125, v91, v89, s63\n\t" \
  "v_perm_b32 v126, v95, v93, s62\n\t" \
  "v_perm_b32 v127, v95, v93, s63\n\t" \
  "ds_write_b128 v70, v[120:123] offset:32768\n\t" \
  "ds_write_b128 v70, v[124:127] offset:33792\n\t" \
  "v_add_u32 v78, 53248, v66\n\t" \
  "s_waitcnt vmcnt(12) lgkmcnt(0)\n\t" \
  "s_barrier\n\t" \
  "ds_read_b64_tr_b16 a[192:193], v78 offset:0\n\t" \
  "ds_read_b64_tr_b16 a[194:195], v78 offset:1024\n\t" \
  "ds_read_b64_tr_b16 a[196:197], v78 offset:8192\n\t" \
  "ds_read_b64_tr_b16 a[198:199], v78 offset:9216\n\t" \
  "ds_read_b64_tr_b16 v[48:49], v78 offset:32\n\t" \
  "ds_read_b64_tr_b16 v[50:51], v78 offset:1056\n\t" \
  "ds_read_b64_tr_b16 v[52:53], v78 offset:8224\n\t" \
  "ds_read_b64_tr_b16 v[54:55], v78 offset:9248\n\t" \
  "s_add_u32 m0, s54, 16384\n\t" \
  "s_mov_b32 s60, 2621440\n\t" \
  "buffer_load_dwordx4 v71, %[Wv], s60 offen lds\n\t" \
  "s_add_u32 m0, s54, 17408\n\t" \
  "s_mov_b32 s60, 2654208\n\t" \
  "buffer_load_dwordx4 v71, %[Wv], s60 offen lds\n\t" \
  "s_add_u32 m0, s54, 18432\n\t" \
  "s_mov_b32 s60, 2686976\n\t" \
  "buffer_load_dwordx4 v71, %[Wv], s60 offen lds\n\t" \
  "s_add_u32 m0, s54, 19456\n\t" \
  "s_mov_b32 s60, 2719744\n\t" \
  "buffer_load_dwordx4 v71, %[Wv], s60 offen lds\n\t" \
  "v_add_u32 v78, 90112, v65\n\t" \
  "s_waitcnt vmcnt(12) lgkmcnt(0)\n\t" \
  "s_barrier\n\t" \
  "ds_read_b64_tr_b16 a[200:201], v78 offset:0\n\t" \
  "ds_read_b64_tr_b16 a[202:203], v78 offset:1024\n\t" \
  "ds_read_b64_tr_b16 a[204:205], v78 offset:8192\n\t" \
  "ds_read_b64_tr_b16 a[206:207], v78 offset:9216\n\t" \
  "ds_read_b64_tr_b16 v[24:25], v78 offset:8\n\t" \
  "ds_read_b64_tr_b16 v[26:27], v78 offset:1032\n\t" \
  "ds_read_b64_tr_b16 v[28:29], v78 offset:8200\n\t" \
  "ds_read_b64_tr_b16 v[30:31], v78 offset:9224\n\t" \
  "s_add_u32 m0, s54, 53248\n\t" \
  "s_mov_b32 s60, 3145728\n\t" \
  "buffer_load_dwordx4 v71, %[Wq], s60 offen lds\n\t" \
  "s_add_u32 m0, s54, 54272\n\t" \
  "s_mov_b32 s60, 3178496\n\t" \
  "buffer_load_dwordx4 v71, %[Wq], s60 offen lds\n\t" \
  "s_add_u32 m0, s54, 55296\n\t" \
  "s_mov_b32 s60, 3211264\n\t" \
  "buffer_load_dwordx4 v71, %[Wq], s60 offen lds\n\t" \
  "s_add_u32 m0, s54, 56320\n\t" \
  "s_mov_b32 s60, 3244032\n\t" \
  "buffer_load_dwordx4 v71, %[Wq], s60 offen lds\n\t" \
  "v_add_u32 v78, 126976, v67\n\t" \
  "s_waitcnt vmcnt(12) lgkmcnt(0)\n\t" \
  "s_barrier\n\t" \
  "ds_read_u16 v80, v78 offset:0\n\t" \
  "ds_read_u16 v81, v78 offset:128\n\t" \
  "ds_read_u16 v82, v78 offset:4096\n\t" \
  "ds_read_u16 v83, v78 offset:4224\n\t" \
  "ds_read_u16 v84, v78 offset:256\n\t" \
  "ds_read_u16 v85, v78 offset:384\n\t" \
  "ds_read_u16 v86, v78 offset:4352\n\t" \
  "ds_read_u16 v87, v78 offset:4480\n\t" \
  "ds_read_u16 v88, v78 offset:1024\n\t" \
  "ds_read_u16 v89, v78 offset:1152\n\t" \
  "ds_read_u16 v90, v78 offset:5120\n\t" \
  "ds_read_u16 v91, v78 offset:5248\n\t" \
  "ds_read_u16 v92, v78 offset:1280\n\t" \
  "ds_read_u16 v93, v78 offset:1408\n\t" \
  "ds_read_u16 v94, v78 offset:5376\n\t" \
  "ds_read_u16 v95, v78 offset:5504\n\t" \
  "ds_read_u16 v96, v78 offset:8192\n\t" \
  "ds_read_u16 v97, v78 offset:8320\n\t" \
  "ds_read_u16 v98, v78 offset:12288\n\t" \
  "ds_read_u16 v99, v78 offset:12416\n\t" \
  "ds_read_u16 v100, v78 offset:8448\n\t" \
  "ds_read_u16 v101, v78 offset:8576\n\t" \
  "ds_read_u16 v102, v78 offset:12544\n\t" \
  "ds_read_u16 v103, v78 offset:12672\n\t" \
  "ds_read_u16 v104, v78 offset:9216\n\t" \
  "ds_read_u16 v105, v78 offset:9344\n\t" \
  "ds_read_u16 v106, v78 offset:13312\n\t" \
  "ds_read_u16 v107, v78 offset:13440\n\t" \
  "ds_read_u16 v108, v78 offset:9472\n\t" \
  "ds_read_u16 v109, v78 offset:9600\n\t" \
  "ds_read_u16 v110, v78 offset:13568\n\t" \
  "ds_read_u16 v111, v78 offset:13696\n\t" \
  "s_add_u32 m0, s54, 90112\n\t" \
  "s_mov_b32 s60, 3145728\n\t" \
  "buffer_load_dwordx4 v71, %[Wk], s60 offen lds\n\t" \
  "s_add_u32 m0, s54, 91136\n\t" \
  "s_mov_b32 s60, 3178496\n\t" \
  "buffer_load_dwordx4 v71, %[Wk], s60 offen lds\n\t" \
  "s_add_u32 m0, s54, 92160\n\t" \
  "s_mov_b32 s60, 3211264\n\t" \
  "buffer_load_dwordx4 v71, %[Wk], s60 offen lds\n\t" \
  "s_add_u32 m0, s54, 93184\n\t" \
  "s_mov_b32 s60, 3244032\n\t" \
  "buffer_load_dwordx4 v71, %[Wk], s60 offen lds\n\t" \
  "s_waitcnt lgkmcnt(0)\n\t" \
  "v_lshl_or_b32 v248, v81, 16, v80\n\t" \
  "v_lshl_or_b32 v249, v83, 16, v82\n\t" \
  "v_lshl_or_b32 v250, v85, 16, v84\n\t" \
  "v_lshl_or_b32 v251, v87, 16, v86\n\t" \
  "v_lshl_or_b32 v252, v89, 16, v88\n\t" \
  "v_lshl_or_b32 v253, v91, 16, v90\n\t" \
  "v_lshl_or_b32 v254, v93, 16, v92\n\t" \
  "v_lshl_or_b32 v255, v95, 16, v94\n\t" \
  "v_lshl_or_b32 v112, v97, 16, v96\n\t" \
  "v_lshl_or_b32 v113, v99, 16, v98\n\t" \
  "v_lshl_or_b32 v114, v101, 16, v100\n\t" \
  "v_lshl_or_b32 v115, v103, 16, v102\n\t" \
  "v_lshl_or_b32 v116, v105, 16, v104\n\t" \
  "v_lshl_or_b32 v117, v107, 16, v106\n\t" \
  "v_lshl_or_b32 v118, v109, 16, v108\n\t" \
  "v_lshl_or_b32 v119, v111, 16, v110\n\t" \
  "ds_write_b128 v70, v[112:115] offset:34816\n\t" \
  "ds_write_b128 v70, v[116:119] offset:35840\n\t" \
  "v_add_u32 v78, 0, v65\n\t" \
  "s_waitcnt vmcnt(12) lgkmcnt(0)\n\t" \
  "s_barrier\n\t" \
  "ds_read_b64_tr_b16 a[208:209], v78 offset:0\n\t" \
  "ds_read_b64_tr_b16 a[210:211], v78 offset:1024\n\t" \
  "ds_read_b64_tr_b16 a[212:213], v78 offset:8192\n\t" \
  "ds_read_b64_tr_b16 a[214:215], v78 offset:9216\n\t" \
  "ds_read_b64_tr_b16 a[216:217], v78 offset:8\n\t" \
  "ds_read_b64_tr_b16 a[218:219], v78 offset:1032\n\t" \
  "ds_read_b64_tr_b16 a[220:221], v78 offset:8200\n\t" \
  "ds_read_b64_tr_b16 a[222:223], v78 offset:9224\n\t" \
  "s_add_u32 m0, s54, 126976\n\t" \
  "s_mov_b32 s60, 3145728\n\t" \
  "buffer_load_dwordx4 v71, %[Wv], s60 offen lds\n\t" \
  "s_add_u32 m0, s54, 128000\n\t" \
  "s_mov_b32 s60, 3178496\n\t" \
  "buffer_load_dwordx4 v71, %[Wv], s60 offen lds\n\t" \
  "s_add_u32 m0, s54, 129024\n\t" \
  "s_mov_b32 s60, 3211264\n\t" \
  "buffer_load_dwordx4 v71, %[Wv], s60 offen lds\n\t" \
  "s_add_u32 m0, s54, 130048\n\t" \
  "s_mov_b32 s60, 3244032\n\t" \
  "buffer_load_dwordx4 v71, %[Wv], s60 offen lds\n\t" \
  "v_add_u32 v78, 16384, v66\n\t" \
  "s_waitcnt vmcnt(12) lgkmcnt(0)\n\t" \
  "s_barrier\n\t" \
  "ds_read_b64_tr_b16 a[224:225], v78 offset:0\n\t" \
  "ds_read_b64_tr_b16 a[226:227], v78 offset:1024\n\t" \
  "ds_read_b64_tr_b16 a[228:229], v78 offset:8192\n\t" \
  "ds_read_b64_tr_b16 a[230:231], v78 offset:9216\n\t" \
  "ds_read_b64_tr_b16 a[232:233], v78 offset:32\n\t" \
  "ds_read_b64_tr_b16 a[234:235], v78 offset:1056\n\t" \
  "ds_read_b64_tr_b16 a[236:237], v78 offset:8224\n\t" \
  "ds_read_b64_tr_b16 a[238:239], v78 offset:9248\n\t" \
  "s_add_u32 m0, s54, 0\n\t" \
  "s_mov_b32 s60, 3670016\n\t" \
  "buffer_load_dwordx4 v71, %[Wq], s60 offen lds\n\t" \
  "s_add_u32 m0, s54, 1024\n\t" \
  "s_mov_b32 s60, 3702784\n\t" \
  "buffer_load_dwordx4 v71, %[Wq], s60 offen lds\n\t" \
  "s_add_u32 m0, s54, 2048\n\t" \
  "s_mov_b32 s60, 3735552\n\t" \
  "buffer_load_dwordx4 v71, %[Wq], s60 offen lds\n\t" \
  "s_add_u32 m0, s54, 3072\n\t" \
  "s_mov_b32 s60, 3768320\n\t" \
  "buffer_load_dwordx4 v71, %[Wq], s60 offen lds\n\t" \
  "v_add_u32 v78, 53248, v65\n\t" \
  "s_waitcnt vmcnt(12) lgkmcnt(0)\n\t" \
  "s_barrier\n\t" \
  "ds_read_b64_tr_b16 a[240:241], v78 offset:0\n\t" \
  "ds_read_b64_tr_b16 a[242:243], v78 offset:1024\n\t" \
  "ds_read_b64_tr_b16 a[244:245], v78 offset:8192\n\t" \
  "ds_read_b64_tr_b16 a[246:247], v78 offset:9216\n\t" \
  "ds_read_b64_tr_b16 a[248:249], v78 offset:8\n\t" \
  "ds_read_b64_tr_b16 a[250:251], v78 offset:1032\n\t" \
  "ds_read_b64_tr_b16 a[252:253], v78 offset:8200\n\t" \
  "ds_read_b64_tr_b16 a[254:255], v78 offset:9224\n\t" \
  "s_add_u32 m0, s54, 16384\n\t" \
  "s_mov_b32 s60, 3670016\n\t" \
  "buffer_load_dwordx4 v71, %[Wk], s60 offen lds\n\t" \
  "s_add_u32 m0, s54, 17408\n\t" \
  "s_mov_b32 s60, 3702784\n\t" \
  "buffer_load_dwordx4 v71, %[Wk], s60 offen lds\n\t" \
  "s_add_u32 m0, s54, 18432\n\t" \
  "s_mov_b32 s60, 3735552\n\t" \
  "buffer_load_dwordx4 v71, %[Wk], s60 offen lds\n\t" \
  "s_add_u32 m0, s54, 19456\n\t" \
  "s_mov_b32 s60, 3768320\n\t" \
  "buffer_load_dwordx4 v71, %[Wk], s60 offen lds\n\t" \
  "v_add_u32 v78, 90112, v65\n\t" \
  "s_waitcnt vmcnt(12) lgkmcnt(0)\n\t" \
  "s_barrier\n\t" \
  "ds_read_b64_tr_b16 v[80:81], v78 offset:0\n\t" \
  "ds_read_b64_tr_b16 v[82:83], v78 offset:1024\n\t" \
  "ds_read_b64_tr_b16 v[84:85], v78 offset:8192\n\t" \
  "ds_read_b64_tr_b16 v[86:87], v78 offset:9216\n\t" \
  "ds_read_b64_tr_b16 v[88:89], v78 offset:8\n\t" \
  "ds_read_b64_tr_b16 v[90:91], v78 offset:1032\n\t" \
  "ds_read_b64_tr_b16 v[92:93], v78 offset:8200\n\t" \
  "ds_read_b64_tr_b16 v[94:95], v78 offset:9224\n\t" \
  "s_add_u32 m0, s54, 53248\n\t" \
  "s_mov_b32 s60, 3670016\n\t" \
  "buffer_load_dwordx4 v71, %[Wv], s60 offen lds\n\t" \
  "s_add_u32 m0, s54, 54272\n\t" \
  "s_mov_b32 s60, 3702784\n\t" \
  "buffer_load_dwordx4 v71, %[Wv], s60 offen lds\n\t" \
  "s_add_u32 m0, s54, 55296\n\t" \
  "s_mov_b32 s60, 3735552\n\t" \
  "buffer_load_dwordx4 v71, %[Wv], s60 offen lds\n\t" \
  "s_add_u32 m0, s54, 56320\n\t" \
  "s_mov_b32 s60, 3768320\n\t" \
  "buffer_load_dwordx4 v71, %[Wv], s60 offen lds\n\t" \
  "v_add_u32 v78, 126976, v66\n\t" \
  "s_waitcnt vmcnt(12) lgkmcnt(0)\n\t" \
  "s_barrier\n\t" \
  "ds_read_b64_tr_b16 v[96:97], v78 offset:0\n\t" \
  "ds_read_b64_tr_b16 v[98:99], v78 offset:1024\n\t" \
  "ds_read_b64_tr_b16 v[100:101], v78 offset:8192\n\t" \
  "ds_read_b64_tr_b16 v[102:103], v78 offset:9216\n\t" \
  "ds_read_b64_tr_b16 v[104:105], v78 offset:32\n\t" \
  "ds_read_b64_tr_b16 v[106:107], v78 offset:1056\n\t" \
  "ds_read_b64_tr_b16 v[108:109], v78 offset:8224\n\t" \
  "ds_read_b64_tr_b16 v[110:111], v78 offset:9248\n\t" \
  "s_add_u32 m0, s54, 90112\n\t" \
  "s_mov_b32 s60, 4194304\n\t" \
  "buffer_load_dwordx4 v71, %[Wq], s60 offen lds\n\t" \
  "s_add_u32 m0, s54, 91136\n\t" \
  "s_mov_b32 s60, 4227072\n\t" \
  "buffer_load_dwordx4 v71, %[Wq], s60 offen lds\n\t" \
  "s_add_u32 m0, s54, 92160\n\t" \
  "s_mov_b32 s60, 4259840\n\t" \
  "buffer_load_dwordx4 v71, %[Wq], s60 offen lds\n\t" \
  "s_add_u32 m0, s54, 93184\n\t" \
  "s_mov_b32 s60, 4292608\n\t" \
  "buffer_load_dwordx4 v71, %[Wq], s60 offen lds\n\t" \
  "v_add_u32 v78, 0, v65\n\t" \
  "s_waitcnt vmcnt(12) lgkmcnt(0)\n\t" \
  "s_barrier\n\t" \
  "ds_read_b64_tr_b16 v[112:113], v78 offset:0\n\t" \
  "ds_read_b64_tr_b16 v[114:115], v78 offset:1024\n\t" \
  "ds_read_b64_tr_b16 v[116:117], v78 offset:8192\n\t" \
  "ds_read_b64_tr_b16 v[118:119], v78 offset:9216\n\t" \
  "ds_read_b64_tr_b16 v[120:121], v78 offset:8\n\t" \
  "ds_read_b64_tr_b16 v[122:123], v78 offset:1032\n\t" \
  "ds_read_b64_tr_b16 v[124:125], v78 offset:8200\n\t" \
  "ds_read_b64_tr_b16 v[126:127], v78 offset:9224\n\t" \
  "s_add_u32 m0, s54, 126976\n\t" \
  "s_mov_b32 s60, 4194304\n\t" \
  "buffer_load_dwordx4 v71, %[Wk], s60 offen lds\n\t" \
  "s_add_u32 m0, s54, 128000\n\t" \
  "s_mov_b32 s60, 4227072\n\t" \
  "buffer_load_dwordx4 v71, %[Wk], s60 offen lds\n\t" \
  "s_add_u32 m0, s54, 129024\n\t" \
  "s_mov_b32 s60, 4259840\n\t" \
  "buffer_load_dwordx4 v71, %[Wk], s60 offen lds\n\t" \
  "s_add_u32 m0, s54, 130048\n\t" \
  "s_mov_b32 s60, 4292608\n\t" \
  "buffer_load_dwordx4 v71, %[Wk], s60 offen lds\n\t" \
  "v_add_u32 v78, 16384, v65\n\t" \
  "s_waitcnt vmcnt(12) lgkmcnt(0)\n\t" \
  "s_barrier\n\t" \
  "ds_read_b64_tr_b16 v[128:129], v78 offset:0\n\t" \
  "ds_read_b64_tr_b16 v[130:131], v78 offset:1024\n\t" \
  "ds_read_b64_tr_b16 v[132:133], v78 offset:8192\n\t" \
  "ds_read_b64_tr_b16 v[134:135], v78 offset:9216\n\t" \
  "ds_read_b64_tr_b16 v[136:137], v78 offset:8\n\t" \
  "ds_read_b64_tr_b16 v[138:139], v78 offset:1032\n\t" \
  "ds_read_b64_tr_b16 v[140:141], v78 offset:8200\n\t" \
  "ds_read_b64_tr_b16 v[142:143], v78 offset:9224\n\t" \
  "s_add_u32 m0, s54, 0\n\t" \
  "s_mov_b32 s60, 4194304\n\t" \
  "buffer_load_dwordx4 v71, %[Wv], s60 offen lds\n\t" \
  "s_add_u32 m0, s54, 1024\n\t" \
  "s_mov_b32 s60, 4227072\n\t" \
  "buffer_load_dwordx4 v71, %[Wv], s60 offen lds\n\t" \
  "s_add_u32 m0, s54, 2048\n\t" \
  "s_mov_b32 s60, 4259840\n\t" \
  "buffer_load_dwordx4 v71, %[Wv], s60 offen lds\n\t" \
  "s_add_u32 m0, s54, 3072\n\t" \
  "s_mov_b32 s60, 4292608\n\t" \
  "buffer_load_dwordx4 v71, %[Wv], s60 offen lds\n\t" \
  "v_add_u32 v78, 53248, v66\n\t" \
  "s_waitcnt vmcnt(12) lgkmcnt(0)\n\t" \
  "s_barrier\n\t" \
  "ds_read_b64_tr_b16 v[144:145], v78 offset:0\n\t" \
  "ds_read_b64_tr_b16 v[146:147], v78 offset:1024\n\t" \
  "ds_read_b64_tr_b16 v[148:149], v78 offset:8192\n\t" \
  "ds_read_b64_tr_b16 v[150:151], v78 offset:9216\n\t" \
  "ds_read_b64_tr_b16 v[152:153], v78 offset:32\n\t" \
  "ds_read_b64_tr_b16 v[154:155], v78 offset:1056\n\t" \
  "ds_read_b64_tr_b16 v[156:157], v78 offset:8224\n\t" \
  "ds_read_b64_tr_b16 v[158:159], v78 offset:9248\n\t" \
  "v_add_u32 v78, 90112, v65\n\t" \
  "s_waitcnt vmcnt(8) lgkmcnt(0)\n\t" \
  "s_barrier\n\t" \
  "ds_read_b64_tr_b16 v[160:161], v78 offset:0\n\t" \
  "ds_read_b64_tr_b16 v[162:163], v78 offset:1024\n\t" \
  "ds_read_b64_tr_b16 v[164:165], v78 offset:8192\n\t" \
  "ds_read_b64_tr_b16 v[166:167], v78 offset:9216\n\t" \
  "ds_read_b64_tr_b16 v[168:169], v78 offset:8\n\t" \
  "ds_read_b64_tr_b16 v[170:171], v78 offset:1032\n\t" \
  "ds_read_b64_tr_b16 v[172:173], v78 offset:8200\n\t" \
  "ds_read_b64_tr_b16 v[174:175], v78 offset:9224\n\t" \
  "v_add_u32 v78, 126976, v65\n\t" \
  "s_waitcnt vmcnt(4) lgkmcnt(0)\n\t" \
  "s_barrier\n\t" \
  "ds_read_b64_tr_b16 v[40:41], v78 offset:0\n\t" \
  "ds_read_b64_tr_b16 v[42:43], v78 offset:1024\n\t" \
  "ds_read_b64_tr_b16 v[44:45], v78 offset:8192\n\t" \
  "ds_read_b64_tr_b16 v[46:47], v78 offset:9216\n\t" \
  "ds_read_b64_tr_b16 v[176:177], v78 offset:8\n\t" \
  "ds_read_b64_tr_b16 v[178:179], v78 offset:1032\n\t" \
  "ds_read_b64_tr_b16 v[180:181], v78 offset:8200\n\t" \
  "ds_read_b64_tr_b16 v[182:183], v78 offset:9224\n\t" \
  "v_add_u32 v78, 0, v66\n\t" \
  "s_waitcnt vmcnt(0) lgkmcnt(0)\n\t" \
  "s_barrier\n\t" \
  "ds_read_b64_tr_b16 v[184:185], v78 offset:0\n\t" \
  "ds_read_b64_tr_b16 v[186:187], v78 offset:1024\n\t" \
  "ds_read_b64_tr_b16 v[188:189], v78 offset:8192\n\t" \
  "ds_read_b64_tr_b16 v[190:191], v78 offset:9216\n\t" \
  "ds_read_b64_tr_b16 v[56:57], v78 offset:32\n\t" \
  "ds_read_b64_tr_b16 v[58:59], v78 offset:1056\n\t" \
  "ds_read_b64_tr_b16 v[60:61], v78 offset:8224\n\t" \
  "ds_read_b64_tr_b16 v[62:63], v78 offset:9248\n\t" \
  "s_waitcnt lgkmcnt(0)\n\t" \
  "s_barrier\n\t" \
  "ds_write_b128 v70, v[0:3] offset:0\n\t" \
  "ds_write_b128 v70, v[4:7] offset:1024\n\t" \
  "ds_write_b128 v70, v[8:11] offset:2048\n\t" \
  "ds_write_b128 v70, v[12:15] offset:3072\n\t" \
  "ds_write_b128 v70, v[16:19] offset:4096\n\t" \
  "ds_write_b128 v70, v[20:23] offset:5120\n\t" \
  "ds_write_b128 v70, v[24:27] offset:6144\n\t" \
  "ds_write_b128 v70, v[28:31] offset:7168\n\t" \
  "ds_write_b128 v70, v[32:35] offset:8192\n\t" \
  "ds_write_b128 v70, v[36:39] offset:9216\n\t" \
  "ds_write_b128 v70, v[40:43] offset:10240\n\t" \
  "ds_write_b128 v70, v[44:47] offset:11264\n\t" \
  "ds_write_b128 v70, v[48:51] offset:12288\n\t" \
  "ds_write_b128 v70, v[52:55] offset:13312\n\t" \
  "ds_write_b128 v70, v[56:59] offset:14336\n\t" \
  "ds_write_b128 v70, v[60:63] offset:15360\n\t" \
  "s_waitcnt lgkmcnt(0)\n\t" \
  "v_and_b32 v72, 1, v72\n\t" \
  "v_and_b32 v74, 1, v74\n\t" \
  "v_cmp_ne_u32 s[90:91], s32, v72\n\t" \
  "v_cmp_ne_u32 vcc, s32, v74\n\t" \
  "s_nop 1\n\t" \
  "s_mov_b32 s96, vcc_lo\n\t" \
  "s_mov_b32 s21, vcc_lo\n\t" \
  "v_and_b32 v73, 32, v69\n\t" \
  "v_lshl_add_u32 v73, v73, 5, v69\n\t" \
  "s_mov_b32 s35, s30\n\t" \
  "s_cmp_lt_u32 s32, " CH_DRYL "\n\t" \
  "s_cbranch_scc1 LINI_%=\n\t" \
  "s_mov_b32 s30, 0\n\t" \
  "s_mov_b32 s34, 0\n\t" \
  "s_cmp_eq_u32 s40, 0\n\t" \
  "s_cbranch_scc1 LW0_%=\n\t" \
  "s_barrier\n\t" \
  "s_cmp_eq_u32 s40, 1\n\t" \
  "s_cbranch_scc1 LG1_%=\n\t" \
  "s_cmp_eq_u32 s40, 2\n\t" \
  "s_cbranch_scc1 LG2_%=\n\t" \
  "s_branch LG3_%=\n\t" \
  "LINI_%=:\n\t" \
  "s_waitcnt vmcnt(0) lgkmcnt(0)\n\t" \
  "s_mov_b32 s30, s35\n\t" \
  "s_movk_i32 s34, 1000\n\t" \
  "s_mov_b32 s48, -1\n\t" \
  "s_add_u32 s97, s41, s53\n\t" \
  "s_add_u32 s98, s37, s53\n\t" \
  "s_add_u32 s82, s48, 0\n\t" \
  "s_and_b32 s82, s82, " CH_TRM "\n\t" \
  "s_lshr_b64 s[60:61], s[90:91], s82\n\t" \
  "s_and_b32 s60, s60, 1\n\t" \
  "s_lshl_b32 s60, s60, 13\n\t" \
  "s_add_u32 s82, s82, " CH_SG0 "\n\t" \
  "s_lshl_b32 s82, s82, 21\n\t" \
  "s_or_b32 s82, s82, s60\n\t" \
  "s_add_u32 s50, s82, s38\n\t" \
  "s_add_u32 s69, s82, s39\n\t" \
  "s_add_u32 s82, s48, 1\n\t" \
  "s_lshr_b64 s[60:61], s[90:91], s82\n\t" \
  "s_and_b32 s60, s60, 1\n\t" \
  "s_lshl_b32 s60, s60, 13\n\t" \
  "s_add_u32 s82, s82, " CH_SG0 "\n\t" \
  "s_lshl_b32 s82, s82, 21\n\t" \
  "s_or_b32 s82, s82, s60\n\t" \
  "s_add_u32 s52, s82, s43\n\t" \
  "s_add_u32 s82, s48, " CH_ARMD "\n\t" \
  "s_lshr_b64 s[60:61], s[90:91], s82\n\t" \
  "s_and_b32 s60, s60, 1\n\t" \
  "s_lshl_b32 s60, s60, 13\n\t" \
  "s_add_u32 s82, s82, " CH_SG0 "\n\t" \
  "s_lshl_b32 s82, s82, 21\n\t" \
  "s_or_b32 s82, s82, s60\n\t" \
  "s_add_u32 s85, s82, s45\n\t" \
  "s_cmp_lt_i32 s48, " CH_ARMS "\n\t" \
  "s_cselect_b32 s85, s27, s85\n\t" \
  "v_and_b32 v37, 0x30, v69\n\t" \
  "v_add_u32 v37, s52, v37\n\t" \
  "s_mov_b32 s48, 0\n\t" \
  "s_mov_b32 s86, 1\n\t" \
  "s_and_b32 s49, s96, 1\n\t" \
  "s_lshl_b32 s49, s49, 13\n\t" \
  "s_add_u32 s53, s49, " CH_SLOT "\n\t" \
  "s_movk_i32 s20, 2560\n\t" \
  "s_mov_b32 s22, 0\n\t" \
  "s_movk_i32 s60, 124\n\t" \
  "s_cmp_eq_u32 s40, 3\n\t" \
  "s_cselect_b32 s81, 76, s60\n\t" \
  "s_add_u32 s82, s48, 0\n\t" \
  "s_and_b32 s82, s82, " CH_TRM "\n\t" \
  "s_lshr_b64 s[60:61], s[90:91], s82\n\t" \
  "s_and_b32 s60, s60, 1\n\t" \
  "s_lshl_b32 s60, s60, 13\n\t" \
  "s_add_u32 s82, s82, " CH_SG0 "\n\t" \
  "s_lshl_b32 s82, s82, 21\n\t" \
  "s_or_b32 s82, s82, s60\n\t" \
  "s_add_u32 s50, s82, s38\n\t" \
  "s_add_u32 s69, s82, s39\n\t" \
  "s_add_u32 s82, s48, 0\n\t" \
  "s_lshr_b64 s[60:61], s[90:91], s82\n\t" \
  "s_and_b32 s60, s60, 1\n\t" \
  "s_lshl_b32 s60, s60, 13\n\t" \
  "s_add_u32 s82, s82, " CH_SG0 "\n\t" \
  "s_lshl_b32 s82, s82, 21\n\t" \
  "s_or_b32 s82, s82, s60\n\t" \
  "s_add_u32 s51, s82, s43\n\t" \
  "s_add_u32 s97, s41, s53\n\t" \
  "s_add_u32 s98, s37, s53\n\t" \
  "s_add_u32 s82, s48, 0\n\t" \
  "s_and_b32 s82, s82, " CH_TRM "\n\t" \
  "s_lshr_b64 s[60:61], s[90:91], s82\n\t" \
  "s_and_b32 s60, s60, 1\n\t" \
  "s_lshl_b32 s60, s60, 13\n\t" \
  "s_add_u32 s82, s82, " CH_SG0 "\n\t" \
  "s_lshl_b32 s82, s82, 21\n\t" \
  "s_or_b32 s82, s82, s60\n\t" \
  "s_add_u32 s50, s82, s38\n\t" \
  "s_add_u32 s69, s82, s39\n\t" \
  "s_add_u32 s82, s48, 1\n\t" \
  "s_lshr_b64 s[60:61], s[90:91], s82\n\t" \
  "s_and_b32 s60, s60, 1\n\t" \
  "s_lshl_b32 s60, s60, 13\n\t" \
  "s_add_u32 s82, s82, " CH_SG0 "\n\t" \
  "s_lshl_b32 s82, s82, 21\n\t" \
  "s_or_b32 s82, s82, s60\n\t" \
  "s_add_u32 s52, s82, s43\n\t" \
  "s_add_u32 s82, s48, " CH_ARMD "\n\t" \
  "s_lshr_b64 s[60:61], s[90:91], s82\n\t" \
  "s_and_b32 s60, s60, 1\n\t" \
  "s_lshl_b32 s60, s60, 13\n\t" \
  "s_add_u32 s82, s82, " CH_SG0 "\n\t" \
  "s_lshl_b32 s82, s82, 21\n\t" \
  "s_or_b32 s82, s82, s60\n\t" \
  "s_add_u32 s85, s82, s45\n\t" \
  "s_cmp_lt_i32 s48, " CH_ARMS "\n\t" \
  "s_cselect_b32 s85, s27, s85\n\t" \
  "s_memrealtime s[62:63]\n\t" \
  "s_waitcnt lgkmcnt(0)\n\t" \
  "s_mov_b32 s65, s62\n\t" \
  "s_sub_u32 s47, s62, 1000\n\t" \
  "s_mov_b64 vcc, -1\n\t" \
  "s_cmp_eq_u32 s40, 0\n\t" \
  "s_cbranch_scc1 LW0_%=\n\t" \
  "s_barrier\n\t" \
  "s_cmp_eq_u32 s40, 1\n\t" \
  "s_cbranch_scc1 LG1_%=\n\t" \
  "s_cmp_eq_u32 s40, 2\n\t" \
  "s_cbranch_scc1 LG2_%=\n\t" \
  "s_branch LTK_%=\n\t" \
  ".p2align 6\n\t" \
  "LW0_%=:\n\t" \
  "v_lshlrev_b32 v78, 2, v69\n\t" \
  "v_and_b32 v78, 0x300, v78\n\t" \
  "v_lshrrev_b32 v79, 8, v69\n\t" \
  "v_lshl_add_u32 v78, v79, 6, v78\n\t" \
  "v_and_b32 v79, 48, v69\n\t" \
  "v_add_u32 v78, v78, v79\n\t" \
  "s_add_u32 s98, s49, s42\n\t" \
  "s_add_u32 s97, s98, 32768\n\t" \
  "s_add_u32 s99, s98, 65536\n\t" \
  "s_barrier\n\t" \
  "s_memrealtime s[62:63]\n\t" \
  "s_waitcnt lgkmcnt(0)\n\t" \
  "s_add_u32 s62, s62, " CH_DLL2 "\n\t" \
  "s_sub_i32 s63, s62, s65\n\t" \
  "s_cmp_gt_i32 s63, " CH_DLT "\n\t" \
  "s_cselect_b32 s60, " CH_DLY ", " CH_DLX "\n\t" \
  "s_add_u32 s62, s62, s60\n\t" \
  "s_cmp_gt_i32 s63, 0\n\t" \
  "s_cselect_b32 s65, s62, s65\n\t" \
  "s_mov_b32 m0, 0\n\t" \
  "s_movk_i32 s60, 8192\n\t" \
  CH_ALW \
  "LWT_%=:\n\t" \
  "s_memrealtime s[62:63]\n\t" \
  "s_sub_u32 s60, s60, 1\n\t" \
  "s_cselect_b32 s61, 0, -1\n\t" \
  "s_waitcnt lgkmcnt(0)\n\t" \
  "s_sub_i32 s63, s62, s65\n\t" \
  "s_and_b32 s63, s63, s61\n\t" \
  "s_cmp_lt_i32 s63, " CH_FIN "\n\t" \
  "s_cbranch_scc1 LWT_%=\n\t" \
  "s_cmp_lt_i32 s63, 0\n\t" \
  "s_cbranch_scc1 LWT_%=f\n\t" \
  "LWT_%=g:\n\t" \
  "LDMA_%=:\n\t" \
  "buffer_load_dwordx4 v78, s[28:31], s98 offen offset:0 sc1 lds\n\t" \
  "buffer_load_dwordx4 v78, s[28:31], s98 offen offset:1024 sc1 lds\n\t" \
  "buffer_load_dwordx4 v78, s[28:31], s98 offen offset:2048 sc1 lds\n\t" \
  "buffer_load_dwordx4 v78, s[28:31], s98 offen offset:3072 sc1 lds\n\t" \
  "s_movk_i32 m0, 4096\n\t" \
  "s_mov_b32 s66, s62\n\t" \
  "buffer_load_dwordx4 v78, s[28:31], s97 offen offset:0 sc1 lds\n\t" \
  "buffer_load_dwordx4 v78, s[28:31], s97 offen offset:1024 sc1 lds\n\t" \
  "buffer_load_dwordx4 v78, s[28:31], s97 offen offset:2048 sc1 lds\n\t" \
  "buffer_load_dwordx4 v78, s[28:31], s97 offen offset:3072 sc1 lds\n\t" \
  "s_movk_i32 m0, 8192\n\t" \
  "s_nop 0\n\t" \
  "buffer_load_dwordx4 v69, s[28:31], s99 offen sc1 lds\n\t" \
  ".p2align 6\n\t" \
  "LG0_%=:\n\t" \
  "s_barrier\n\t" \
  "buffer_load_dwordx4 v[52:55], v69, s[28:31], s50 offen sc1\n\t" \
  "buffer_load_dwordx4 v[56:59], v69, s[28:31], s50 offen offset:1024 sc1\n\t" \
  "s_add_u32 s64, s50, 16384\n\t" \
  "s_movk_i32 m0, 11264\n\t" \
  "v_lshlrev_b32 v75, 1, v69\n\t" \
  "buffer_load_dword v75, s[28:31], s64 offen sc1 lds\n\t" \
  "s_memrealtime s[62:63]\n\t" \
  "s_waitcnt lgkmcnt(0)\n\t" \
  "s_sub_u32 s62, s62, " CH_TKL "\n\t" \
  "s_sub_i32 s63, s62, s65\n\t" \
  "s_cmp_gt_i32 s63, 0\n\t" \
  "s_cselect_b32 s62, s62, s65\n\t" \
  "s_sub_i32 s63, s62, s47\n\t" \
  "s_cmp_gt_i32 s63, " CH_LATE "\n\t" \
  "s_cselect_b32 s71, " CH_SCHW ", s46\n\t" \
  "s_add_u32 s61, s47, " CH_LATE "\n\t" \
  "s_cmp_gt_i32 s63, " CH_LATE "\n\t" \
  "s_cselect_b32 s62, s62, s61\n\t" \
  "s_add_u32 s71, s71, s53\n\t" \
  "s_add_u32 s60, s62, s81\n\t" \
  "s_add_u32 s60, s60, " CH_DL1L "\n\t" \
  "s_lshl_b32 s60, s60, 8\n\t" \
  "s_or_b32 s60, s60, " CH_B0 "\n\t" \
  "s_cmp_lt_u32 s48, " CH_ES "\n\t" \
  "s_cselect_b32 s61, " CH_EX ", 0\n\t" \
  "s_add_u32 s60, s60, s61\n\t" \
  "s_not_b32 s60, s60\n\t" \
  "v_mov_b32 v76, s60\n\t" \
  "s_lshr_b32 s60, s46, 31\n\t" \
  "s_or_b32 s60, s60, s68\n\t" \
  "s_cselect_b32 s25, 0, 1\n\t" \
  "s_mov_b64 exec, s[24:25]\n\t" \
  "buffer_atomic_umin v76, v69, s[28:31], s71 offen sc1\n\t" \
  "s_mov_b64 exec, -1\n\t" \
  "ds_read_b128 v[44:47], v70 offset:0\n\t" \
  "ds_read_b128 v[48:51], v70 offset:1024\n\t" \
  "ds_read_b128 v[60:63], v70 offset:2048\n\t" \
  "ds_read_b128 v[64:67], v70 offset:3072\n\t" \
  "v_mov_b64 v[36:37], 0\n\t" \
  "v_mov_b64 v[38:39], 0\n\t" \
  "v_mov_b64 v[40:41], 0\n\t" \
  "v_mov_b64 v[42:43], 0\n\t" \
  "v_lshrrev_b32 v71, 1, v69\n\t" \
  "v_lshrrev_b32 v72, 3, v69\n\t" \
  "v_and_b32 v72, 0x78, v72\n\t" \
  "v_and_b32 v74, 0x30, v69\n\t" \
  "v_lshl_add_u32 v72, v74, 3, v72\n\t" \
  "s_add_u32 s54, s53, s36\n\t" \
  "s_add_u32 s55, s54, 524288\n\t" \
  "s_add_u32 s54, s54, 0\n\t" \
  "s_add_u32 s75, s54, 262144\n\t" \
  "s_add_u32 s80, s53, s26\n\t" \
  "s_or_b32 s87, s53, 0x1fff80\n\t" \
  "s_and_b32 s87, s87, -128\n\t" \
  "s_add_u32 s87, s87, 0x200000\n\t" \
  ".p2align 5\n\t" \
  ".fill 2, 4, 0xbf800000\n\t" \
  "s_waitcnt vmcnt(9)\n\t" \
  "s_barrier\n\t" \
  "ds_read_b128 v[0:3], v69 offset:0\n\t" \
  "ds_read_b128 v[4:7], v69 offset:1024\n\t" \
  "ds_read_b128 v[8:11], v69 offset:2048\n\t" \
  "ds_read_b128 v[12:15], v69 offset:3072\n\t" \
  "s_waitcnt lgkmcnt(3)\n\t" \
  "v_smfmac_f32_16x16x64_bf16 v[36:39], v[0:3], v[44:51], v68\n\t" \
  "ds_read_b128 v[44:47], v70 offset:4096\n\t" \
  "v_smfmac_f32_16x16x64_bf16 v[40:43], v[0:3], v[60:67], v68\n\t" \
  "ds_read_b128 v[48:51], v70 offset:5120\n\t" \
  "s_waitcnt lgkmcnt(4)\n\t" \
  "v_smfmac_f32_16x16x64_bf16 v[36:39], v[4:7], a[32:39], v68\n\t" \
  "ds_read_b128 v[60:63], v70 offset:6144\n\t" \
  "v_smfmac_f32_16x16x64_bf16 v[40:43], v[4:7], a[40:47], v68\n\t" \
  "ds_read_b128 v[64:67], v70 offset:7168\n\t" \
  "s_waitcnt lgkmcnt(5)\n\t" \
  "v_smfmac_f32_16x16x64_bf16 v[36:39], v[8:11], a[80:87], v68\n\t" \
  "s_waitcnt vmcnt(4)\n\t" \
  "s_barrier\n\t" \
  "ds_read_b128 v[16:19], v69 offset:4096\n\t" \
  "v_smfmac_f32_16x16x64_bf16 v[40:43], v[8:11], a[88:95], v68\n\t" \
  "ds_read_b128 v[20:23], v69 offset:5120\n\t" \
  "s_waitcnt lgkmcnt(6)\n\t" \
  "v_smfmac_f32_16x16x64_bf16 v[36:39], v[12:15], a[120:127], v68\n\t" \
  "ds_read_b128 v[24:27], v69 offset:6144\n\t" \
  "s_waitcnt lgkmcnt(5)\n\t" \
  "v_smfmac_f32_16x16x64_bf16 v[40:43], v[12:15], v[44:51], v68\n\t" \
  "ds_read_b128 v[28:31], v69 offset:7168\n\t" \
  "s_waitcnt lgkmcnt(3)\n\t" \
  "v_smfmac_f32_16x16x64_bf16 v[36:39], v[16:19], a[160:167], v68\n\t" \
  "ds_read_b128 v[32:35], v69 offset:8192\n\t" \
  "v_smfmac_f32_16x16x64_bf16 v[40:43], v[16:19], a[168:175], v68\n\t" \
  "s_waitcnt lgkmcnt(3)\n\t" \
  "v_mov_b64 v[44:45], 0\n\t" \
  "v_smfmac_f32_16x16x64_bf16 v[36:39], v[20:23], a[200:207], v68\n\t" \
  "s_waitcnt lgkmcnt(2)\n\t" \
  "v_mov_b64 v[46:47], 0\n\t" \
  "v_smfmac_f32_16x16x64_bf16 v[36:39], v[24:27], a[240:247], v68\n\t" \
  "s_waitcnt lgkmcnt(1)\n\t" \
  "v_mov_b64 v[48:49], 0\n\t" \
  "v_smfmac_f32_16x16x64_bf16 v[36:39], v[28:31], v[112:119], v68\n\t" \
  "s_waitcnt lgkmcnt(0)\n\t" \
  "v_mov_b64 v[50:51], 0\n\t" \
  "v_smfmac_f32_16x16x64_bf16 v[36:39], v[32:35], v[160:167], v68\n\t" \
  "v_smfmac_f32_16x16x64_bf16 v[40:43], v[20:23], v[60:67], v68\n\t" \
  "ds_read_b128 v[60:63], v70 offset:8192\n\t" \
  "v_smfmac_f32_16x16x64_bf16 v[40:43], v[24:27], a[248:255], v68\n\t" \
  "ds_read_b128 v[64:67], v70 offset:9216\n\t" \
  "v_add_f32 v74, v36, v38\n\t" \
  "v_smfmac_f32_16x16x64_bf16 v[40:43], v[28:31], v[120:127], v68\n\t" \
  "v_add_f32 v37, v37, v39\n\t" \
  "v_smfmac_f32_16x16x64_bf16 v[40:43], v[32:35], v[168:175], v68\n\t" \
  "v_cmp_u_f32 vcc, v74, v37\n\t" \
  "v_smfmac_f32_16x16x64_bf16 v[44:47], v[0:3], a[0:7], v68\n\t" \
  "s_cbranch_vccnz LFAIL0_%=\n\t" \
  "LGO0_%=:\n\t" \
  "v_cvt_pk_bf16_f32 v74, v74, v37\n\t" \
  "v_smfmac_f32_16x16x64_bf16 v[44:47], v[4:7], a[48:55], v68\n\t" \
  "s_waitcnt lgkmcnt(0)\n\t" \
  "v_add_f32 v75, v40, v42\n\t" \
  "v_smfmac_f32_16x16x64_bf16 v[44:47], v[8:11], v[60:67], v68\n\t" \
  "ds_read_b128 v[60:63], v70 offset:10240\n\t" \
  "v_add_f32 v41, v41, v43\n\t" \
  "v_cvt_pk_bf16_f32 v75, v75, v41\n\t" \
  "v_smfmac_f32_16x16x64_bf16 v[44:47], v[12:15], a[128:135], v68\n\t" \
  "ds_write2st64_b32 v71, v74, v75 offset0:36 offset1:38\n\t" \
  "v_smfmac_f32_16x16x64_bf16 v[44:47], v[16:19], a[176:183], v68\n\t" \
  "ds_read_b128 v[64:67], v70 offset:11264\n\t" \
  "v_smfmac_f32_16x16x64_bf16 v[44:47], v[20:23], a[208:215], v68\n\t" \
  "v_smfmac_f32_16x16x64_bf16 v[44:47], v[24:27], v[80:87], v68\n\t" \
  "v_smfmac_f32_16x16x64_bf16 v[44:47], v[28:31], v[128:135], v68\n\t" \
  "s_waitcnt lgkmcnt(0)\n\t" \
  "v_smfmac_f32_16x16x64_bf16 v[44:47], v[32:35], v[60:67], v68\n\t" \
  "buffer_load_dwordx4 v[60:63], v69, s[28:31], s64 offen sc1\n\t" \
  "s_barrier\n\t" \
  "ds_read_b64_tr_b16 v[76:77], v72 offset:9216\n\t" \
  "v_smfmac_f32_16x16x64_bf16 v[48:51], v[0:3], a[8:15], v68\n\t" \
  "ds_read_b64_tr_b16 v[78:79], v72 offset:9728\n\t" \
  "v_smfmac_f32_16x16x64_bf16 v[48:51], v[4:7], a[56:63], v68\n\t" \
  "v_add_f32 v74, v44, v46\n\t" \
  "v_smfmac_f32_16x16x64_bf16 v[48:51], v[8:11], a[96:103], v68\n\t" \
  "v_add_f32 v45, v45, v47\n\t" \
  "v_smfmac_f32_16x16x64_bf16 v[48:51], v[12:15], a[136:143], v68\n\t" \
  "s_waitcnt vmcnt(4)\n\t" \
  "s_waitcnt lgkmcnt(0)\n\t" \
  "v_cvt_pk_bf16_f32 v74, v74, v45\n\t" \
  "v_mfma_f32_16x16x32_bf16 v[36:39], v[52:55], v[76:79], 0\n\t" \
  "buffer_load_dwordx4 v[64:67], v69, s[28:31], s64 offen offset:1024 sc1\n\t" \
  "s_waitcnt vmcnt(4)\n\t" \
  "v_mfma_f32_16x16x32_bf16 v[40:43], v[56:59], v[76:79], 0\n\t" \
  "v_mov_b64 v[52:53], 0\n\t" \
  "v_mov_b64 v[54:55], 0\n\t" \
  "v_smfmac_f32_16x16x64_bf16 v[48:51], v[16:19], a[184:191], v68\n\t" \
  "v_mov_b64 v[56:57], 0\n\t" \
  "v_mov_b64 v[58:59], 0\n\t" \
  "v_smfmac_f32_16x16x64_bf16 v[48:51], v[20:23], a[216:223], v68\n\t" \
  "ds_write_b32 v71, v74 offset:0\n\t" \
  "v_add_f32_dpp v36, v40, v36 quad_perm:[2,3,0,1] row_mask:0xf bank_mask:0xf\n\t" \
  "v_smfmac_f32_16x16x64_bf16 v[48:51], v[24:27], v[88:95], v68\n\t" \
  "v_add_f32_dpp v37, v41, v37 quad_perm:[2,3,0,1] row_mask:0xf bank_mask:0xf\n\t" \
  "v_smfmac_f32_16x16x64_bf16 v[48:51], v[28:31], v[136:143], v68\n\t" \
  "v_add_f32_dpp v38, v42, v38 quad_perm:[2,3,0,1] row_mask:0xf bank_mask:0xf\n\t" \
  "v_smfmac_f32_16x16x64_bf16 v[48:51], v[32:35], v[176:183], v68\n\t" \
  "v_add_f32_dpp v39, v43, v39 quad_perm:[2,3,0,1] row_mask:0xf bank_mask:0xf\n\t" \
  "v_smfmac_f32_16x16x64_bf16 v[52:55], v[0:3], a[16:23], v68\n\t" \
  "s_mov_b64 exec, s[58:59]\n\t" \
  "ds_write_b128 v69, v[36:39] offset:2048\n\t" \
  "s_mov_b64 exec, -1\n\t" \
  "v_smfmac_f32_16x16x64_bf16 v[52:55], v[4:7], a[64:71], v68\n\t" \
  "v_add_f32 v75, v48, v50\n\t" \
  "v_add_f32 v49, v49, v51\n\t" \
  "v_cvt_pk_bf16_f32 v75, v75, v49\n\t" \
  "v_smfmac_f32_16x16x64_bf16 v[56:59], v[0:3], a[24:31], v68\n\t" \
  "ds_write_b32 v71, v75 offset:512\n\t" \
  "v_smfmac_f32_16x16x64_bf16 v[56:59], v[4:7], a[72:79], v68\n\t" \
  "ds_read_b64_tr_b16 v[0:1], v72 offset:10240\n\t" \
  "ds_read_b64_tr_b16 v[2:3], v72 offset:10752\n\t" \
  "v_smfmac_f32_16x16x64_bf16 v[52:55], v[8:11], a[104:111], v68\n\t" \
  "ds_read_b128 v[36:39], v70 offset:12288\n\t" \
  "v_smfmac_f32_16x16x64_bf16 v[56:59], v[8:11], a[112:119], v68\n\t" \
  "buffer_load_dword off, s[28:31], s87 sc1 lds\n\t" \
  "ds_read_b128 v[40:43], v70 offset:13312\n\t" \
  "s_waitcnt lgkmcnt(4)\n\t" \
  "s_barrier\n\t" \
  "ds_read_b64_tr_b16 v[44:45], v72 offset:0\n\t" \
  "ds_read_b64_tr_b16 v[46:47], v72 offset:512\n\t" \
  "v_smfmac_f32_16x16x64_bf16 v[52:55], v[12:15], a[144:151], v68\n\t" \
  "ds_read_b64_tr_b16 v[48:49], v72 offset:1024\n\t" \
  "ds_read_b64_tr_b16 v[50:51], v72 offset:1536\n\t" \
  "v_smfmac_f32_16x16x64_bf16 v[56:59], v[12:15], a[152:159], v68\n\t" \
  "v_smfmac_f32_16x16x64_bf16 v[52:55], v[16:19], a[192:199], v68\n\t" \
  "ds_read_b128 v[4:7], v69 offset:2048\n\t" \
  "s_waitcnt lgkmcnt(5)\n\t" \
  "v_smfmac_f32_16x16x64_bf16 v[56:59], v[16:19], v[36:43], v68\n\t" \
  "ds_read_b128 v[8:11], v69 offset:3072\n\t" \
  "s_waitcnt lgkmcnt(4)\n\t" \
  "v_mfma_f32_16x16x32_bf16 v[12:15], v[44:47], v[76:79], 0\n\t" \
  "ds_read_b128 v[36:39], v70 offset:14336\n\t" \
  "s_waitcnt lgkmcnt(3)\n\t" \
  "v_mfma_f32_16x16x32_bf16 v[12:15], v[48:51], v[0:3], v[12:15]\n\t" \
  "ds_read_b128 v[40:43], v70 offset:15360\n\t" \
  "v_smfmac_f32_16x16x64_bf16 v[52:55], v[20:23], a[224:231], v68\n\t" \
  "s_waitcnt lgkmcnt(2)\n\t" \
  "v_add_f32 v4, v4, v8\n\t" \
  "v_add_f32 v5, v5, v9\n\t" \
  "v_add_f32 v6, v6, v10\n\t" \
  "v_cndmask_b32 v78, v13, v14, s[56:57]\n\t" \
  "v_cndmask_b32 v76, v12, v15, s[56:57]\n\t" \
  "v_add_f32 v7, v7, v11\n\t" \
  "v_max3_f32 v77, v4, v5, v6\n\t" \
  "v_add_f32_dpp v76, v78, v76 quad_perm:[2,3,0,1] row_mask:0xf bank_mask:0xf\n\t" \
  "v_max3_f32 v77, v77, v7, v76\n\t" \
  "v_smfmac_f32_16x16x64_bf16 v[56:59], v[20:23], a[232:239], v68\n\t" \
  "ds_read_b128 v[44:47], v70 offset:16384\n\t" \
  "v_max_f32_dpp v77, v77, v77 quad_perm:[2,3,0,1] row_mask:0xf bank_mask:0xf\n\t" \
  "v_mov_b32 v78, v77\n\t" \
  "v_smfmac_f32_16x16x64_bf16 v[52:55], v[24:27], v[96:103], v68\n\t" \
  "ds_read_b128 v[48:51], v70 offset:17408\n\t" \
  "v_permlane16_swap_b32 v77, v78\n\t" \
  "v_max_f32 v77, v77, v78\n\t" \
  "v_mov_b32 v78, v77\n\t" \
  "v_smfmac_f32_16x16x64_bf16 v[52:55], v[28:31], v[144:151], v68\n\t" \
  "ds_read_b128 v[16:19], v70 offset:18432\n\t" \
  "v_permlane32_swap_b32 v77, v78\n\t" \
  "v_max_f32 v77, v77, v78\n\t" \
  "v_smfmac_f32_16x16x64_bf16 v[52:55], v[32:35], v[184:191], v68\n\t" \
  "ds_read_b128 v[20:23], v70 offset:19456\n\t" \
  "v_mul_f32 v77, 0xbe0293ee, v77\n\t" \
  "v_smfmac_f32_16x16x64_bf16 v[56:59], v[24:27], v[104:111], v68\n\t" \
  "v_fmamk_f32 v4, v4, 0x3e0293ee, v77\n\t" \
  "v_smfmac_f32_16x16x64_bf16 v[56:59], v[28:31], v[152:159], v68\n\t" \
  "ds_read_b128 v[24:27], v70 offset:22528\n\t" \
  "s_waitcnt lgkmcnt(4)\n\t" \
  "v_fmamk_f32 v5, v5, 0x3e0293ee, v77\n\t" \
  "v_smfmac_f32_16x16x64_bf16 v[56:59], v[32:35], v[36:43], v68\n\t" \
  "ds_read_b128 v[28:31], v70 offset:23552\n\t" \
  "v_fmamk_f32 v6, v6, 0x3e0293ee, v77\n\t" \
  "v_fmamk_f32 v7, v7, 0x3e0293ee, v77\n\t" \
  "v_fmamk_f32 v76, v76, 0x3e0293ee, v77\n\t" \
  "v_exp_f32 v4, v4\n\t" \
  "v_exp_f32 v5, v5\n\t" \
  "v_exp_f32 v6, v6\n\t" \
  "v_exp_f32 v7, v7\n\t" \
  "v_cvt_pk_bf16_f32 v4, v4, v5\n\t" \
  "v_exp_f32 v76, v76\n\t" \
  "v_cvt_pk_bf16_f32 v5, v6, v7\n\t" \
  "v_mov_b32_dpp v12, v4 quad_perm:[0,1,0,1] row_mask:0xf bank_mask:0xf\n\t" \
  "v_mov_b32_dpp v14, v4 quad_perm:[2,3,2,3] row_mask:0xf bank_mask:0xf\n\t" \
  "v_mov_b32_dpp v13, v5 quad_perm:[0,1,0,1] row_mask:0xf bank_mask:0xf\n\t" \
  "v_mov_b32_dpp v15, v5 quad_perm:[2,3,2,3] row_mask:0xf bank_mask:0xf\n\t" \
  "v_add_f32 v52, v52, v54\n\t" \
  "v_add_f32 v53, v53, v55\n\t" \
  "s_waitcnt vmcnt(2)\n\t" \
  "v_mfma_f32_16x16x32_bf16 v[0:3], v[60:63], v[12:15], 0\n\t" \
  "ds_read_b128 v[36:39], v70 offset:20480\n\t" \
  "v_add_f32 v56, v56, v58\n\t" \
  "v_add_f32 v57, v57, v59\n\t" \
  "v_cvt_pk_bf16_f32 v52, v52, v53\n\t" \
  "v_cvt_pk_bf16_f32 v53, v56, v57\n\t" \
  "s_waitcnt vmcnt(1)\n\t" \
  "v_mfma_f32_16x16x32_bf16 v[4:7], v[64:67], v[12:15], 0\n\t" \
  "buffer_store_dwordx2 v[52:53], v71, s[28:31], s69 offen offset:2048 sc1\n\t" \
  "ds_read_b128 v[40:43], v70 offset:21504\n\t" \
  "v_mov_b64 v[56:57], s[88:89]\n\t" \
  "v_mov_b64 v[58:59], s[88:89]\n\t" \
  "v_mov_b32_dpp v78, v76 quad_perm:[0,1,0,1] row_mask:0xf bank_mask:0xf\n\t" \
  "v_mov_b32_dpp v79, v76 quad_perm:[2,3,2,3] row_mask:0xf bank_mask:0xf\n\t" \
  "v_mfma_f32_16x16x32_bf16 v[32:35], v[56:59], v[12:15], 0\n\t" \
  "ds_read_b128 v[60:63], v70 offset:24576\n\t" \
  "v_cvt_pk_bf16_f32 v11, v78, v79\n\t" \
  "v_cvt_pk_bf16_f32 v8, v78, v79\n\t" \
  "v_mov_b64 v[54:55], 0\n\t" \
  "v_mov_b32 v9, 0\n\t" \
  "v_mov_b32 v10, 0\n\t" \
  "v_mov_b64 v[14:15], 0\n\t" \
  "v_mov_b64 v[12:13], 0\n\t" \
  "v_mfma_f32_16x16x32_bf16 v[32:35], v[54:57], v[8:11], v[32:35]\n\t" \
  "s_cmp_eq_u32 s48, 63\n\t" \
  "v_mfma_f32_16x16x32_bf16 v[0:3], v[52:55], v[8:11], v[0:3]\n\t" \
  "ds_read_b128 v[64:67], v70 offset:25600\n\t" \
  "v_mov_b64 v[56:57], 0\n\t" \
  "v_mov_b64 v[58:59], 0\n\t" \
  "v_mfma_f32_16x16x32_bf16 v[4:7], v[52:55], v[10:13], v[4:7]\n\t" \
  "s_cbranch_scc1 LLAST0_%=\n\t" \
  "v_rcp_f32 v74, v32\n\t" \
  "v_mov_b32 v11, 0\n\t" \
  "v_mov_b32 v8, 0\n\t" \
  "v_pk_mul_f32 v[0:1], v[0:1], v[74:75] op_sel_hi:[1,0]\n\t" \
  "v_pk_mul_f32 v[2:3], v[2:3], v[74:75] op_sel_hi:[1,0]\n\t" \
  "v_cvt_pk_bf16_f32 v76, v0, v1\n\t" \
  "v_cvt_pk_bf16_f32 v77, v2, v3\n\t" \
  "v_pk_mul_f32 v[4:5], v[4:5], v[74:75] op_sel_hi:[1,0]\n\t" \
  "v_pk_mul_f32 v[6:7], v[6:7], v[74:75] op_sel_hi:[1,0]\n\t" \
  "v_cvt_pk_bf16_f32 v78, v4, v5\n\t" \
  "v_cvt_pk_bf16_f32 v79, v6, v7\n\t" \
  "ds_write_b128 v69, v[76:79] offset:4096\n\t" \
  "s_waitcnt lgkmcnt(2)\n\t" \
  "v_smfmac_f32_16x16x64_bf16 v[12:15], v[76:79], v[44:51], v68\n\t" \
  "ds_read_b128 v[44:47], v70 offset:26624\n\t" \
  "v_smfmac_f32_16x16x64_bf16 v[56:59], v[76:79], v[16:23], v68\n\t" \
  "ds_read_b128 v[48:51], v70 offset:27648\n\t" \
  "v_mov_b64 v[52:53], 0\n\t" \
  "v_smfmac_f32_16x16x64_bf16 v[8:11], v[76:79], v[36:43], v68\n\t" \
  "ds_read_b128 v[16:19], v70 offset:28672\n\t" \
  "v_smfmac_f32_16x16x64_bf16 v[52:55], v[76:79], v[24:31], v68\n\t" \
  "s_waitcnt lgkmcnt(3)\n\t" \
  "s_barrier\n\t" \
  "ds_read_b128 v[0:3], v69 offset:5120\n\t" \
  "ds_read_b128 v[4:7], v69 offset:6144\n\t" \
  "v_lshrrev_b32 v71, 7, v69\n\t" \
  "v_and_b32 v74, 0x70, v69\n\t" \
  "v_lshl_add_u32 v71, v71, 14, v74\n\t" \
  "v_bfe_u32 v72, v69, 2, 7\n\t" \
  "v_and_b32 v74, 0x200, v69\n\t" \
  "s_waitcnt lgkmcnt(1)\n\t" \
  "v_smfmac_f32_16x16x64_bf16 v[8:11], v[0:3], v[60:67], v68\n\t" \
  "ds_read_b128 v[20:23], v70 offset:29696\n\t" \
  "v_mov_b64 v[76:77], 0\n\t" \
  "v_smfmac_f32_16x16x64_bf16 v[12:15], v[0:3], v[200:207], v68\n\t" \
  "ds_read_b128 v[32:35], v69 offset:7168\n\t" \
  "v_mov_b64 v[78:79], 0\n\t" \
  "v_smfmac_f32_16x16x64_bf16 v[52:55], v[0:3], v[44:51], v68\n\t" \
  "ds_read_b128 v[24:27], v70 offset:30720\n\t" \
  "v_lshl_add_u32 v72, v74, 3, v72\n\t" \
  "v_smfmac_f32_16x16x64_bf16 v[56:59], v[0:3], v[232:239], v68\n\t" \
  "buffer_load_dword v75, off, s[28:31], s80 sc1\n\t" \
  "ds_read_b128 v[28:31], v70 offset:31744\n\t" \
  "s_waitcnt lgkmcnt(4)\n\t" \
  "v_smfmac_f32_16x16x64_bf16 v[12:15], v[4:7], v[208:215], v68\n\t" \
  "ds_read_b128 v[60:63], v70 offset:32768\n\t" \
  "s_waitcnt lgkmcnt(4)\n\t" \
  "v_smfmac_f32_16x16x64_bf16 v[8:11], v[4:7], v[16:23], v68\n\t" \
  "ds_read_b128 v[64:67], v70 offset:33792\n\t" \
  "s_waitcnt lgkmcnt(4)\n\t" \
  "v_smfmac_f32_16x16x64_bf16 v[8:11], v[32:35], v[192:199], v68\n\t" \
  "ds_read_b128 v[36:39], v73 offset:4096\n\t" \
  "v_smfmac_f32_16x16x64_bf16 v[12:15], v[32:35], v[216:223], v68\n\t" \
  "ds_read_b128 v[40:43], v73 offset:6144\n\t" \
  "s_waitcnt lgkmcnt(4)\n\t" \
  "v_smfmac_f32_16x16x64_bf16 v[52:55], v[4:7], v[24:31], v68\n\t" \
  "ds_read_b128 v[44:47], v70 offset:34816\n\t" \
  "v_cvt_pk_bf16_f32 v8, v8, v9\n\t" \
  "v_smfmac_f32_16x16x64_bf16 v[52:55], v[32:35], v[224:231], v68\n\t" \
  "ds_read_b128 v[48:51], v70 offset:35840\n\t" \
  "v_cvt_pk_bf16_f32 v9, v10, v11\n\t" \
  "v_smfmac_f32_16x16x64_bf16 v[56:59], v[4:7], v[240:247], v68\n\t" \
  "s_waitcnt lgkmcnt(4)\n\t" \
  "v_cvt_pk_bf16_f32 v10, v12, v13\n\t" \
  "v_smfmac_f32_16x16x64_bf16 v[56:59], v[32:35], v[60:67], v68\n\t" \
  "s_waitcnt lgkmcnt(3)\n\t" \
  "v_cvt_pk_bf16_f32 v11, v14, v15\n\t" \
  "v_smfmac_f32_16x16x64_bf16 v[76:79], v[36:39], v[248:255], v68\n\t" \
  "buffer_store_dwordx4 v[8:11], v71, s[28:31], s54 offen sc1\n\t" \
  "s_waitcnt lgkmcnt(0)\n\t" \
  "v_cvt_pk_bf16_f32 v52, v52, v53\n\t" \
  "v_smfmac_f32_16x16x64_bf16 v[76:79], v[40:43], v[44:51], v68\n\t" \
  "v_cvt_pk_bf16_f32 v53, v54, v55\n\t" \
  "v_cvt_pk_bf16_f32 v54, v56, v57\n\t" \
  "v_cvt_pk_bf16_f32 v55, v58, v59\n\t" \
  "buffer_store_dwordx4 v[52:55], v71, s[28:31], s75 offen sc1\n\t" \
  "s_nop 2\n\t" \
  "v_add_f32 v76, v76, v78\n\t" \
  "v_add_f32 v77, v77, v79\n\t" \
  "v_cvt_pk_bf16_f32 v76, v76, v77\n\t" \
  "buffer_store_dword v76, v72, s[28:31], s55 offen sc1\n\t" \
  "s_memrealtime s[70:71]\n\t" \
  "s_waitcnt vmcnt(3)\n\t" \
  "v_readfirstlane_b32 s65, v75\n\t" \
  "s_barrier\n\t" \
  "s_waitcnt lgkmcnt(0)\n\t" \
  "s_not_b32 s65, s65\n\t" \
  "s_lshl_b32 s61, s70, 8\n\t" \
  "s_andn2_b32 s64, s65, 0xff\n\t" \
  "s_sub_u32 s64, s64, s61\n\t" \
  "s_ashr_i32 s64, s64, 8\n\t" \
  "s_cmp_eq_u32 s65, 0\n\t" \
  "s_cselect_b32 s64, " CH_DLF ", s64\n\t" \
  "s_cselect_b32 s65, " CH_B0 ", s65\n\t" \
  "s_add_u32 s47, s64, s70\n\t" \
  "s_min_i32 s64, s64, s34\n\t" \
  "s_max_i32 s64, s64, " CH_DLL1 "\n\t" \
  "s_add_u32 s64, s64, s70\n\t" \
  "s_add_u32 s55, s64, " CH_FDL "\n\t" \
  "s_movk_i32 s60, 8192\n\t" \
  "FWT_%=:\n\t" \
  "s_memrealtime s[62:63]\n\t" \
  "s_sub_u32 s60, s60, 1\n\t" \
  "s_cselect_b32 s61, 0, -1\n\t" \
  "s_waitcnt lgkmcnt(0)\n\t" \
  "s_sub_i32 s63, s62, s55\n\t" \
  "s_and_b32 s63, s63, s61\n\t" \
  "s_cmp_lt_i32 s63, 0\n\t" \
  "s_cbranch_scc1 FWT_%=\n\t" \
  "s_mov_b64 exec, 1\n\t" \
  "buffer_store_dword v68, off, s[28:31], s51 offset:4064 sc1\n\t" \
  "s_mov_b64 exec, -1\n\t" \
  "s_and_b32 s65, s65, 0xff\n\t" \
  "s_add_u32 s47, s47, s65\n\t" \
  "s_add_u32 s65, s65, s64\n\t" \
  "s_sub_u32 s60, s70, s66\n\t" \
  "s_add_u32 s61, s81, 1\n\t" \
  "s_min_u32 s60, s60, s61\n\t" \
  "s_cmp_eq_u32 s68, 0\n\t" \
  "s_cselect_b32 s81, s60, s81\n\t" \
  "s_add_u32 s48, s48, 1\n\t" \
  "s_mov_b32 s49, s53\n\t" \
  "s_add_u32 s53, s53, " CH_SLOT "\n\t" \
  "s_add_u32 s60, s53, " CH_SKIP "\n\t" \
  "s_lshr_b32 s61, s96, 1\n\t" \
  "s_add_u32 s86, s86, 1\n\t" \
  "s_cmp_eq_u32 s86, 3\n\t" \
  "s_cselect_b32 s53, s60, s53\n\t" \
  "s_cselect_b32 s96, s61, s96\n\t" \
  "s_cselect_b32 s86, 0, s86\n\t" \
  "s_cmp_ge_u32 s53, " CH_RING "\n\t" \
  "s_cselect_b32 s53, 0, s53\n\t" \
  "s_cselect_b32 s96, s21, s96\n\t" \
  "s_andn2_b32 s53, s53, 0x2000\n\t" \
  "s_and_b32 s60, s96, 1\n\t" \
  "s_lshl_b32 s60, s60, 13\n\t" \
  "s_or_b32 s53, s53, s60\n\t" \
  "s_mov_b32 s68, 0\n\t" \
  "s_add_u32 s82, s48, 0\n\t" \
  "s_and_b32 s82, s82, " CH_TRM "\n\t" \
  "s_lshr_b64 s[60:61], s[90:91], s82\n\t" \
  "s_and_b32 s60, s60, 1\n\t" \
  "s_lshl_b32 s60, s60, 13\n\t" \
  "s_add_u32 s82, s82, " CH_SG0 "\n\t" \
  "s_lshl_b32 s82, s82, 21\n\t" \
  "s_or_b32 s82, s82, s60\n\t" \
  "s_add_u32 s50, s82, s38\n\t" \
  "s_add_u32 s69, s82, s39\n\t" \
  "s_add_u32 s82, s48, 0\n\t" \
  "s_lshr_b64 s[60:61], s[90:91], s82\n\t" \
  "s_and_b32 s60, s60, 1\n\t" \
  "s_lshl_b32 s60, s60, 13\n\t" \
  "s_add_u32 s82, s82, " CH_SG0 "\n\t" \
  "s_lshl_b32 s82, s82, 21\n\t" \
  "s_or_b32 s82, s82, s60\n\t" \
  "s_add_u32 s51, s82, s43\n\t" \
  "s_cmp_lg_u32 s34, 0\n\t" \
  "s_cbranch_scc1 LW0_%=\n\t" \
  "s_barrier\n\t" \
  "s_branch LINI_%=\n\t" \
  "LFAIL0_%=:\n\t" \
  "v_max_u32 v75, v0, v1\n\t" \
  "v_max3_u32 v75, v75, v2, v3\n\t" \
  "v_max3_u32 v75, v75, v4, v5\n\t" \
  "v_max3_u32 v75, v75, v6, v7\n\t" \
  "v_max3_u32 v75, v75, v8, v9\n\t" \
  "v_max3_u32 v75, v75, v10, v11\n\t" \
  "v_max3_u32 v75, v75, v12, v13\n\t" \
  "v_max3_u32 v75, v75, v14, v15\n\t" \
  "v_max3_u32 v75, v75, v16, v17\n\t" \
  "v_max3_u32 v75, v75, v18, v19\n\t" \
  "v_max3_u32 v75, v75, v20, v21\n\t" \
  "v_max3_u32 v75, v75, v22, v23\n\t" \
  "v_max3_u32 v75, v75, v24, v25\n\t" \
  "v_max3_u32 v75, v75, v26, v27\n\t" \
  "v_max3_u32 v75, v75, v28, v29\n\t" \
  "v_max3_u32 v75, v75, v30, v31\n\t" \
  "v_max3_u32 v75, v75, v32, v33\n\t" \
  "v_max3_u32 v75, v75, v34, v35\n\t" \
  "v_cmp_eq_u32 vcc, -1, v75\n\t" \
  "s_cbranch_vccz LGO0_%=\n\t" \
  "s_add_u32 s68, s68, 1\n\t" \
  "s_waitcnt lgkmcnt(0)\n\t" \
  "s_cmp_gt_u32 s68, s83\n\t" \
  "s_cbranch_scc1 LGV0_%=\n\t" \
  "s_barrier\n\t" \
  "s_memrealtime s[62:63]\n\t" \
  "s_mov_b32 m0, 0\n\t" \
  "s_waitcnt lgkmcnt(0)\n\t" \
  "s_add_u32 s65, s62, " CH_PEN "\n\t" \
  "s_branch LDMA_%=\n\t" \
  "LWT_%=f:\n\t" \
  "s_cmp_lt_i32 s63, -2\n\t" \
  "s_cbranch_scc0 LWT_%=g\n\t" \
  "s_nop 4\n\t" \
  "s_branch LWT_%=g\n\t" \
  "LGV0_%=:\n\t" \
  "s_mov_b32 s83, 0\n\t" \
  "s_lshl_b32 s60, s36, 4\n\t" \
  "s_or_b32 s60, s60, s48\n\t" \
  "s_or_b32 s60, s60, 805306368\n\t" \
  "v_mov_b32 v76, s60\n\t" \
  "v_mov_b32 v77, 0\n\t" \
  "s_mov_b64 exec, 1\n\t" \
  "buffer_atomic_cmpswap v[76:77], off, s[28:31], s92 sc1\n\t" \
  "s_mov_b64 exec, -1\n\t" \
  "s_branch LGO0_%=\n\t" \
  "LLAST0_%=:\n\t" \
  "s_waitcnt vmcnt(0) lgkmcnt(0)\n\t" \
  "s_barrier\n\t" \
  "s_mov_b64 exec, 1\n\t" \
  "buffer_store_dword v68, off, s[28:31], s51 offset:4064 sc1\n\t" \
  "s_mov_b64 exec, -1\n\t" \
  "s_branch LEND_%=\n\t" \
  ".p2align 6\n\t" \
  "LG1_%=:\n\t" \
  "s_barrier\n\t" \
  "buffer_load_dwordx4 v[52:55], v69, s[28:31], s50 offen sc1\n\t" \
  "buffer_load_dwordx4 v[56:59], v69, s[28:31], s50 offen offset:1024 sc1\n\t" \
  "s_add_u32 s64, s50, 14336\n\t" \
  "s_movk_i32 m0, 11264\n\t" \
  "v_lshlrev_b32 v75, 1, v69\n\t" \
  "buffer_load_dword v75, s[28:31], s64 offen sc1 lds\n\t" \
  "ds_read_b128 v[44:47], v70 offset:0\n\t" \
  "ds_read_b128 v[48:51], v70 offset:1024\n\t" \
  "ds_read_b128 v[60:63], v70 offset:2048\n\t" \
  "ds_read_b128 v[64:67], v70 offset:3072\n\t" \
  "v_mov_b64 v[36:37], 0\n\t" \
  "v_mov_b64 v[38:39], 0\n\t" \
  "v_mov_b64 v[40:41], 0\n\t" \
  "v_mov_b64 v[42:43], 0\n\t" \
  "v_lshrrev_b32 v71, 1, v69\n\t" \
  "v_add_u32 v71, 4, v71\n\t" \
  "v_lshrrev_b32 v72, 3, v69\n\t" \
  "v_and_b32 v72, 0x78, v72\n\t" \
  "v_and_b32 v74, 0x30, v69\n\t" \
  "v_lshl_add_u32 v72, v74, 3, v72\n\t" \
  "s_add_u32 s54, s53, s36\n\t" \
  "s_add_u32 s55, s54, 540672\n\t" \
  "s_add_u32 s54, s54, 4096\n\t" \
  "s_add_u32 s75, s54, 262144\n\t" \
  "s_add_u32 s80, s53, s26\n\t" \
  ".p2align 5\n\t" \
  ".fill 2, 4, 0xbf800000\n\t" \
  "s_waitcnt vmcnt(9)\n\t" \
  "s_barrier\n\t" \
  "ds_read_b128 v[0:3], v69 offset:0\n\t" \
  "ds_read_b128 v[4:7], v69 offset:1024\n\t" \
  "ds_read_b128 v[8:11], v69 offset:2048\n\t" \
  "ds_read_b128 v[12:15], v69 offset:3072\n\t" \
  "s_waitcnt lgkmcnt(3)\n\t" \
  "v_smfmac_f32_16x16x64_bf16 v[36:39], v[0:3], v[44:51], v68\n\t" \
  "ds_read_b128 v[44:47], v70 offset:4096\n\t" \
  "v_smfmac_f32_16x16x64_bf16 v[40:43], v[0:3], v[60:67], v68\n\t" \
  "ds_read_b128 v[48:51], v70 offset:5120\n\t" \
  "s_waitcnt lgkmcnt(4)\n\t" \
  "v_smfmac_f32_16x16x64_bf16 v[36:39], v[4:7], a[32:39], v68\n\t" \
  "ds_read_b128 v[60:63], v70 offset:6144\n\t" \
  "v_smfmac_f32_16x16x64_bf16 v[40:43], v[4:7], a[40:47], v68\n\t" \
  "ds_read_b128 v[64:67], v70 offset:7168\n\t" \
  "s_waitcnt lgkmcnt(5)\n\t" \
  "v_smfmac_f32_16x16x64_bf16 v[36:39], v[8:11], a[80:87], v68\n\t" \
  "s_barrier\n\t" \
  "ds_read_b128 v[16:19], v69 offset:4096\n\t" \
  "v_smfmac_f32_16x16x64_bf16 v[40:43], v[8:11], a[88:95], v68\n\t" \
  "ds_read_b128 v[20:23], v69 offset:5120\n\t" \
  "s_waitcnt lgkmcnt(6)\n\t" \
  "v_smfmac_f32_16x16x64_bf16 v[36:39], v[12:15], a[120:127], v68\n\t" \
  "ds_read_b128 v[24:27], v69 offset:6144\n\t" \
  "s_waitcnt lgkmcnt(5)\n\t" \
  "v_smfmac_f32_16x16x64_bf16 v[40:43], v[12:15], v[44:51], v68\n\t" \
  "ds_read_b128 v[28:31], v69 offset:7168\n\t" \
  "s_waitcnt lgkmcnt(3)\n\t" \
  "v_smfmac_f32_16x16x64_bf16 v[36:39], v[16:19], a[160:167], v68\n\t" \
  "ds_read_b128 v[32:35], v69 offset:8192\n\t" \
  "v_smfmac_f32_16x16x64_bf16 v[40:43], v[16:19], a[168:175], v68\n\t" \
  "s_waitcnt lgkmcnt(3)\n\t" \
  "v_mov_b64 v[44:45], 0\n\t" \
  "v_smfmac_f32_16x16x64_bf16 v[36:39], v[20:23], a[200:207], v68\n\t" \
  "s_waitcnt lgkmcnt(2)\n\t" \
  "v_mov_b64 v[46:47], 0\n\t" \
  "v_smfmac_f32_16x16x64_bf16 v[36:39], v[24:27], a[240:247], v68\n\t" \
  "s_waitcnt lgkmcnt(1)\n\t" \
  "v_mov_b64 v[48:49], 0\n\t" \
  "v_smfmac_f32_16x16x64_bf16 v[36:39], v[28:31], v[112:119], v68\n\t" \
  "s_waitcnt lgkmcnt(0)\n\t" \
  "v_mov_b64 v[50:51], 0\n\t" \
  "v_smfmac_f32_16x16x64_bf16 v[36:39], v[32:35], v[160:167], v68\n\t" \
  "v_smfmac_f32_16x16x64_bf16 v[40:43], v[20:23], v[60:67], v68\n\t" \
  "ds_read_b128 v[60:63], v70 offset:8192\n\t" \
  "v_smfmac_f32_16x16x64_bf16 v[40:43], v[24:27], a[248:255], v68\n\t" \
  "ds_read_b128 v[64:67], v70 offset:9216\n\t" \
  "v_add_f32 v74, v36, v38\n\t" \
  "v_smfmac_f32_16x16x64_bf16 v[40:43], v[28:31], v[120:127], v68\n\t" \
  "v_add_f32 v37, v37, v39\n\t" \
  "v_smfmac_f32_16x16x64_bf16 v[40:43], v[32:35], v[168:175], v68\n\t" \
  "v_cmp_u_f32 vcc, v74, v37\n\t" \
  "v_smfmac_f32_16x16x64_bf16 v[44:47], v[0:3], a[0:7], v68\n\t" \
  "s_cbranch_vccnz LFAIL1_%=\n\t" \
  "LGO1_%=:\n\t" \
  "v_cvt_pk_bf16_f32 v74, v74, v37\n\t" \
  "v_smfmac_f32_16x16x64_bf16 v[44:47], v[4:7], a[48:55], v68\n\t" \
  "s_waitcnt lgkmcnt(0)\n\t" \
  "v_add_f32 v75, v40, v42\n\t" \
  "v_smfmac_f32_16x16x64_bf16 v[44:47], v[8:11], v[60:67], v68\n\t" \
  "ds_read_b128 v[60:63], v70 offset:10240\n\t" \
  "v_add_f32 v41, v41, v43\n\t" \
  "v_cvt_pk_bf16_f32 v75, v75, v41\n\t" \
  "v_smfmac_f32_16x16x64_bf16 v[44:47], v[12:15], a[128:135], v68\n\t" \
  "ds_write2st64_b32 v71, v74, v75 offset0:36 offset1:38\n\t" \
  "v_smfmac_f32_16x16x64_bf16 v[44:47], v[16:19], a[176:183], v68\n\t" \
  "ds_read_b128 v[64:67], v70 offset:11264\n\t" \
  "v_smfmac_f32_16x16x64_bf16 v[44:47], v[20:23], a[208:215], v68\n\t" \
  "v_smfmac_f32_16x16x64_bf16 v[44:47], v[24:27], v[80:87], v68\n\t" \
  "v_smfmac_f32_16x16x64_bf16 v[44:47], v[28:31], v[128:135], v68\n\t" \
  "s_waitcnt lgkmcnt(0)\n\t" \
  "v_smfmac_f32_16x16x64_bf16 v[44:47], v[32:35], v[60:67], v68\n\t" \
  "s_barrier\n\t" \
  "ds_read_b64_tr_b16 v[76:77], v72 offset:9216\n\t" \
  "v_smfmac_f32_16x16x64_bf16 v[48:51], v[0:3], a[8:15], v68\n\t" \
  "buffer_load_dwordx4 v[60:63], v69, s[28:31], s64 offen sc1\n\t" \
  "ds_read_b64_tr_b16 v[78:79], v72 offset:9728\n\t" \
  "v_smfmac_f32_16x16x64_bf16 v[48:51], v[4:7], a[56:63], v68\n\t" \
  "v_add_f32 v74, v44, v46\n\t" \
  "v_smfmac_f32_16x16x64_bf16 v[48:51], v[8:11], a[96:103], v68\n\t" \
  "v_add_f32 v45, v45, v47\n\t" \
  "v_smfmac_f32_16x16x64_bf16 v[48:51], v[12:15], a[136:143], v68\n\t" \
  "s_waitcnt vmcnt(3)\n\t" \
  "s_waitcnt lgkmcnt(0)\n\t" \
  "v_cvt_pk_bf16_f32 v74, v74, v45\n\t" \
  "v_mfma_f32_16x16x32_bf16 v[36:39], v[52:55], v[76:79], 0\n\t" \
  "s_waitcnt vmcnt(2)\n\t" \
  "v_mfma_f32_16x16x32_bf16 v[40:43], v[56:59], v[76:79], 0\n\t" \
  "buffer_load_dwordx4 v[64:67], v69, s[28:31], s64 offen offset:1024 sc1\n\t" \
  "v_mov_b64 v[52:53], 0\n\t" \
  "v_mov_b64 v[54:55], 0\n\t" \
  "v_smfmac_f32_16x16x64_bf16 v[48:51], v[16:19], a[184:191], v68\n\t" \
  "v_mov_b64 v[56:57], 0\n\t" \
  "v_mov_b64 v[58:59], 0\n\t" \
  "v_smfmac_f32_16x16x64_bf16 v[48:51], v[20:23], a[216:223], v68\n\t" \
  "ds_write_b32 v71, v74 offset:0\n\t" \
  "v_add_f32_dpp v36, v40, v36 quad_perm:[2,3,0,1] row_mask:0xf bank_mask:0xf\n\t" \
  "v_smfmac_f32_16x16x64_bf16 v[48:51], v[24:27], v[88:95], v68\n\t" \
  "v_add_f32_dpp v37, v41, v37 quad_perm:[2,3,0,1] row_mask:0xf bank_mask:0xf\n\t" \
  "v_smfmac_f32_16x16x64_bf16 v[48:51], v[28:31], v[136:143], v68\n\t" \
  "v_add_f32_dpp v38, v42, v38 quad_perm:[2,3,0,1] row_mask:0xf bank_mask:0xf\n\t" \
  "v_smfmac_f32_16x16x64_bf16 v[48:51], v[32:35], v[176:183], v68\n\t" \
  "v_add_f32_dpp v39, v43, v39 quad_perm:[2,3,0,1] row_mask:0xf bank_mask:0xf\n\t" \
  "v_smfmac_f32_16x16x64_bf16 v[52:55], v[0:3], a[16:23], v68\n\t" \
  "s_mov_b64 exec, s[58:59]\n\t" \
  "ds_write_b128 v69, v[36:39] offset:2080\n\t" \
  "s_mov_b64 exec, -1\n\t" \
  "v_smfmac_f32_16x16x64_bf16 v[52:55], v[4:7], a[64:71], v68\n\t" \
  "v_add_f32 v75, v48, v50\n\t" \
  "v_add_f32 v49, v49, v51\n\t" \
  "v_cvt_pk_bf16_f32 v75, v75, v49\n\t" \
  "v_smfmac_f32_16x16x64_bf16 v[56:59], v[0:3], a[24:31], v68\n\t" \
  "ds_write_b32 v71, v75 offset:512\n\t" \
  "v_smfmac_f32_16x16x64_bf16 v[56:59], v[4:7], a[72:79], v68\n\t" \
  "ds_read_b64_tr_b16 v[0:1], v72 offset:10240\n\t" \
  "ds_read_b64_tr_b16 v[2:3], v72 offset:10752\n\t" \
  "v_smfmac_f32_16x16x64_bf16 v[52:55], v[8:11], a[104:111], v68\n\t" \
  "ds_read_b128 v[36:39], v70 offset:12288\n\t" \
  "v_smfmac_f32_16x16x64_bf16 v[56:59], v[8:11], a[112:119], v68\n\t" \
  "ds_read_b128 v[40:43], v70 offset:13312\n\t" \
  "s_waitcnt lgkmcnt(4)\n\t" \
  "s_barrier\n\t" \
  "ds_read_b64_tr_b16 v[44:45], v72 offset:0\n\t" \
  "ds_read_b64_tr_b16 v[46:47], v72 offset:512\n\t" \
  "v_smfmac_f32_16x16x64_bf16 v[52:55], v[12:15], a[144:151], v68\n\t" \
  "ds_read_b64_tr_b16 v[48:49], v72 offset:1024\n\t" \
  "ds_read_b64_tr_b16 v[50:51], v72 offset:1536\n\t" \
  "v_smfmac_f32_16x16x64_bf16 v[56:59], v[12:15], a[152:159], v68\n\t" \
  "v_smfmac_f32_16x16x64_bf16 v[52:55], v[16:19], a[192:199], v68\n\t" \
  "ds_read_b128 v[4:7], v69 offset:2048\n\t" \
  "s_waitcnt lgkmcnt(5)\n\t" \
  "v_smfmac_f32_16x16x64_bf16 v[56:59], v[16:19], v[36:43], v68\n\t" \
  "ds_read_b128 v[8:11], v69 offset:3072\n\t" \
  "s_waitcnt lgkmcnt(4)\n\t" \
  "v_mfma_f32_16x16x32_bf16 v[12:15], v[44:47], v[76:79], 0\n\t" \
  "ds_read_b128 v[36:39], v70 offset:14336\n\t" \
  "s_waitcnt lgkmcnt(3)\n\t" \
  "v_mfma_f32_16x16x32_bf16 v[12:15], v[48:51], v[0:3], v[12:15]\n\t" \
  "buffer_store_dwordx4 v[44:47], v69, s[28:31], s69 offen offset:0 sc1\n\t" \
  "ds_read_b128 v[40:43], v70 offset:15360\n\t" \
  "v_smfmac_f32_16x16x64_bf16 v[52:55], v[20:23], a[224:231], v68\n\t" \
  "s_waitcnt lgkmcnt(2)\n\t" \
  "v_add_f32 v4, v4, v8\n\t" \
  "v_add_f32 v5, v5, v9\n\t" \
  "v_add_f32 v6, v6, v10\n\t" \
  "v_cndmask_b32 v78, v13, v14, s[56:57]\n\t" \
  "v_cndmask_b32 v76, v12, v15, s[56:57]\n\t" \
  "v_add_f32 v7, v7, v11\n\t" \
  "v_max3_f32 v77, v4, v5, v6\n\t" \
  "v_add_f32_dpp v76, v78, v76 quad_perm:[2,3,0,1] row_mask:0xf bank_mask:0xf\n\t" \
  "v_max3_f32 v77, v77, v7, v76\n\t" \
  "v_smfmac_f32_16x16x64_bf16 v[56:59], v[20:23], a[232:239], v68\n\t" \
  "ds_read_b128 v[44:47], v70 offset:16384\n\t" \
  "v_max_f32_dpp v77, v77, v77 quad_perm:[2,3,0,1] row_mask:0xf bank_mask:0xf\n\t" \
  "v_mov_b32 v78, v77\n\t" \
  "v_smfmac_f32_16x16x64_bf16 v[52:55], v[24:27], v[96:103], v68\n\t" \
  "ds_read_b128 v[48:51], v70 offset:17408\n\t" \
  "v_permlane16_swap_b32 v77, v78\n\t" \
  "v_max_f32 v77, v77, v78\n\t" \
  "v_mov_b32 v78, v77\n\t" \
  "v_smfmac_f32_16x16x64_bf16 v[52:55], v[28:31], v[144:151], v68\n\t" \
  "ds_read_b128 v[16:19], v70 offset:18432\n\t" \
  "v_permlane32_swap_b32 v77, v78\n\t" \
  "v_max_f32 v77, v77, v78\n\t" \
  "v_smfmac_f32_16x16x64_bf16 v[52:55], v[32:35], v[184:191], v68\n\t" \
  "ds_read_b128 v[20:23], v70 offset:19456\n\t" \
  "v_mul_f32 v77, 0xbe0293ee, v77\n\t" \
  "v_smfmac_f32_16x16x64_bf16 v[56:59], v[24:27], v[104:111], v68\n\t" \
  "v_fmamk_f32 v4, v4, 0x3e0293ee, v77\n\t" \
  "v_smfmac_f32_16x16x64_bf16 v[56:59], v[28:31], v[152:159], v68\n\t" \
  "ds_read_b128 v[24:27], v70 offset:22528\n\t" \
  "s_waitcnt lgkmcnt(4)\n\t" \
  "v_fmamk_f32 v5, v5, 0x3e0293ee, v77\n\t" \
  "v_smfmac_f32_16x16x64_bf16 v[56:59], v[32:35], v[36:43], v68\n\t" \
  "ds_read_b128 v[28:31], v70 offset:23552\n\t" \
  "v_fmamk_f32 v6, v6, 0x3e0293ee, v77\n\t" \
  "v_fmamk_f32 v7, v7, 0x3e0293ee, v77\n\t" \
  "v_fmamk_f32 v76, v76, 0x3e0293ee, v77\n\t" \
  "v_exp_f32 v4, v4\n\t" \
  "v_exp_f32 v5, v5\n\t" \
  "v_exp_f32 v6, v6\n\t" \
  "v_exp_f32 v7, v7\n\t" \
  "v_cvt_pk_bf16_f32 v4, v4, v5\n\t" \
  "v_exp_f32 v76, v76\n\t" \
  "v_cvt_pk_bf16_f32 v5, v6, v7\n\t" \
  "v_mov_b32_dpp v12, v4 quad_perm:[0,1,0,1] row_mask:0xf bank_mask:0xf\n\t" \
  "v_mov_b32_dpp v14, v4 quad_perm:[2,3,2,3] row_mask:0xf bank_mask:0xf\n\t" \
  "v_mov_b32_dpp v13, v5 quad_perm:[0,1,0,1] row_mask:0xf bank_mask:0xf\n\t" \
  "v_mov_b32_dpp v15, v5 quad_perm:[2,3,2,3] row_mask:0xf bank_mask:0xf\n\t" \
  "v_add_f32 v52, v52, v54\n\t" \
  "v_add_f32 v53, v53, v55\n\t" \
  "s_waitcnt vmcnt(2)\n\t" \
  "v_mfma_f32_16x16x32_bf16 v[0:3], v[60:63], v[12:15], 0\n\t" \
  "ds_read_b128 v[36:39], v70 offset:20480\n\t" \
  "v_add_f32 v56, v56, v58\n\t" \
  "v_add_f32 v57, v57, v59\n\t" \
  "v_cvt_pk_bf16_f32 v52, v52, v53\n\t" \
  "v_cvt_pk_bf16_f32 v53, v56, v57\n\t" \
  "s_waitcnt vmcnt(1)\n\t" \
  "v_mfma_f32_16x16x32_bf16 v[4:7], v[64:67], v[12:15], 0\n\t" \
  "ds_read_b128 v[40:43], v70 offset:21504\n\t" \
  "v_mov_b64 v[56:57], s[88:89]\n\t" \
  "v_mov_b64 v[58:59], s[88:89]\n\t" \
  "v_mov_b32_dpp v78, v76 quad_perm:[0,1,0,1] row_mask:0xf bank_mask:0xf\n\t" \
  "v_mov_b32_dpp v79, v76 quad_perm:[2,3,2,3] row_mask:0xf bank_mask:0xf\n\t" \
  "v_mfma_f32_16x16x32_bf16 v[32:35], v[56:59], v[12:15], 0\n\t" \
  "buffer_store_dwordx2 v[52:53], v71, s[28:31], s69 offen offset:2556 sc1\n\t" \
  "ds_read_b128 v[60:63], v70 offset:24576\n\t" \
  "v_cvt_pk_bf16_f32 v11, v78, v79\n\t" \
  "v_cvt_pk_bf16_f32 v8, v78, v79\n\t" \
  "v_mov_b64 v[54:55], 0\n\t" \
  "v_mov_b32 v9, 0\n\t" \
  "v_mov_b32 v10, 0\n\t" \
  "v_mov_b64 v[14:15], 0\n\t" \
  "v_mov_b64 v[12:13], 0\n\t" \
  "v_mfma_f32_16x16x32_bf16 v[32:35], v[54:57], v[8:11], v[32:35]\n\t" \
  "s_cmp_eq_u32 s48, 63\n\t" \
  "v_mfma_f32_16x16x32_bf16 v[0:3], v[52:55], v[8:11], v[0:3]\n\t" \
  "ds_read_b128 v[64:67], v70 offset:25600\n\t" \
  "v_mov_b64 v[56:57], 0\n\t" \
  "v_mov_b64 v[58:59], 0\n\t" \
  "v_mfma_f32_16x16x32_bf16 v[4:7], v[52:55], v[10:13], v[4:7]\n\t" \
  "s_cbranch_scc1 LLAST1_%=\n\t" \
  "v_rcp_f32 v74, v32\n\t" \
  "v_mov_b32 v11, 0\n\t" \
  "v_mov_b32 v8, 0\n\t" \
  "v_pk_mul_f32 v[0:1], v[0:1], v[74:75] op_sel_hi:[1,0]\n\t" \
  "v_pk_mul_f32 v[2:3], v[2:3], v[74:75] op_sel_hi:[1,0]\n\t" \
  "v_cvt_pk_bf16_f32 v76, v0, v1\n\t" \
  "v_cvt_pk_bf16_f32 v77, v2, v3\n\t" \
  "v_pk_mul_f32 v[4:5], v[4:5], v[74:75] op_sel_hi:[1,0]\n\t" \
  "v_pk_mul_f32 v[6:7], v[6:7], v[74:75] op_sel_hi:[1,0]\n\t" \
  "v_cvt_pk_bf16_f32 v78, v4, v5\n\t" \
  "v_cvt_pk_bf16_f32 v79, v6, v7\n\t" \
  "ds_write_b128 v69, v[76:79] offset:5120\n\t" \
  "s_waitcnt lgkmcnt(2)\n\t" \
  "v_smfmac_f32_16x16x64_bf16 v[12:15], v[76:79], v[44:51], v68\n\t" \
  "ds_read_b128 v[44:47], v70 offset:26624\n\t" \
  "v_smfmac_f32_16x16x64_bf16 v[56:59], v[76:79], v[16:23], v68\n\t" \
  "ds_read_b128 v[48:51], v70 offset:27648\n\t" \
  "v_mov_b64 v[52:53], 0\n\t" \
  "v_smfmac_f32_16x16x64_bf16 v[8:11], v[76:79], v[36:43], v68\n\t" \
  "ds_read_b128 v[16:19], v70 offset:28672\n\t" \
  "v_smfmac_f32_16x16x64_bf16 v[52:55], v[76:79], v[24:31], v68\n\t" \
  "s_waitcnt lgkmcnt(3)\n\t" \
  "s_barrier\n\t" \
  "ds_read_b128 v[0:3], v69 offset:4096\n\t" \
  "ds_read_b128 v[4:7], v69 offset:7168\n\t" \
  "v_lshrrev_b32 v71, 7, v69\n\t" \
  "v_and_b32 v74, 0x70, v69\n\t" \
  "v_lshl_add_u32 v71, v71, 14, v74\n\t" \
  "v_bfe_u32 v72, v69, 2, 7\n\t" \
  "v_and_b32 v74, 0x200, v69\n\t" \
  "s_waitcnt lgkmcnt(1)\n\t" \
  "v_smfmac_f32_16x16x64_bf16 v[8:11], v[0:3], v[60:67], v68\n\t" \
  "buffer_load_dword v75, off, s[28:31], s80 sc1\n\t" \
  "ds_read_b128 v[20:23], v70 offset:29696\n\t" \
  "v_mov_b64 v[76:77], 0\n\t" \
  "v_smfmac_f32_16x16x64_bf16 v[12:15], v[0:3], v[200:207], v68\n\t" \
  "ds_read_b128 v[32:35], v69 offset:6144\n\t" \
  "v_mov_b64 v[78:79], 0\n\t" \
  "v_smfmac_f32_16x16x64_bf16 v[52:55], v[0:3], v[44:51], v68\n\t" \
  "ds_read_b128 v[24:27], v70 offset:30720\n\t" \
  "v_lshl_add_u32 v72, v74, 3, v72\n\t" \
  "v_smfmac_f32_16x16x64_bf16 v[56:59], v[0:3], v[232:239], v68\n\t" \
  "ds_read_b128 v[28:31], v70 offset:31744\n\t" \
  "s_waitcnt lgkmcnt(4)\n\t" \
  "v_smfmac_f32_16x16x64_bf16 v[12:15], v[4:7], v[208:215], v68\n\t" \
  "ds_read_b128 v[60:63], v70 offset:32768\n\t" \
  "s_waitcnt lgkmcnt(4)\n\t" \
  "v_smfmac_f32_16x16x64_bf16 v[8:11], v[4:7], v[16:23], v68\n\t" \
  "ds_read_b128 v[64:67], v70 offset:33792\n\t" \
  "s_waitcnt lgkmcnt(4)\n\t" \
  "v_smfmac_f32_16x16x64_bf16 v[8:11], v[32:35], v[192:199], v68\n\t" \
  "ds_read_b128 v[36:39], v73 offset:4096\n\t" \
  "v_smfmac_f32_16x16x64_bf16 v[12:15], v[32:35], v[216:223], v68\n\t" \
  "ds_read_b128 v[40:43], v73 offset:6144\n\t" \
  "s_waitcnt lgkmcnt(4)\n\t" \
  "v_smfmac_f32_16x16x64_bf16 v[52:55], v[4:7], v[24:31], v68\n\t" \
  "ds_read_b128 v[44:47], v70 offset:34816\n\t" \
  "v_cvt_pk_bf16_f32 v8, v8, v9\n\t" \
  "v_smfmac_f32_16x16x64_bf16 v[52:55], v[32:35], v[224:231], v68\n\t" \
  "ds_read_b128 v[48:51], v70 offset:35840\n\t" \
  "v_cvt_pk_bf16_f32 v9, v10, v11\n\t" \
  "v_smfmac_f32_16x16x64_bf16 v[56:59], v[4:7], v[240:247], v68\n\t" \
  "s_waitcnt lgkmcnt(4)\n\t" \
  "v_cvt_pk_bf16_f32 v10, v12, v13\n\t" \
  "v_smfmac_f32_16x16x64_bf16 v[56:59], v[32:35], v[60:67], v68\n\t" \
  "s_waitcnt lgkmcnt(3)\n\t" \
  "v_cvt_pk_bf16_f32 v11, v14, v15\n\t" \
  "v_smfmac_f32_16x16x64_bf16 v[76:79], v[36:39], v[248:255], v68\n\t" \
  "buffer_store_dwordx4 v[8:11], v71, s[28:31], s54 offen sc1\n\t" \
  "s_waitcnt lgkmcnt(0)\n\t" \
  "v_cvt_pk_bf16_f32 v52, v52, v53\n\t" \
  "v_smfmac_f32_16x16x64_bf16 v[76:79], v[40:43], v[44:51], v68\n\t" \
  "v_cvt_pk_bf16_f32 v53, v54, v55\n\t" \
  "v_cvt_pk_bf16_f32 v54, v56, v57\n\t" \
  "v_cvt_pk_bf16_f32 v55, v58, v59\n\t" \
  "buffer_store_dwordx4 v[52:55], v71, s[28:31], s75 offen sc1\n\t" \
  "s_nop 2\n\t" \
  "v_add_f32 v76, v76, v78\n\t" \
  "v_add_f32 v77, v77, v79\n\t" \
  "v_cvt_pk_bf16_f32 v76, v76, v77\n\t" \
  "buffer_store_dword v76, v72, s[28:31], s55 offen sc1\n\t" \
  "s_memrealtime s[70:71]\n\t" \
  "s_waitcnt vmcnt(3)\n\t" \
  "v_readfirstlane_b32 s65, v75\n\t" \
  "s_barrier\n\t" \
  "s_waitcnt lgkmcnt(0)\n\t" \
  "s_not_b32 s65, s65\n\t" \
  "s_lshl_b32 s61, s70, 8\n\t" \
  "s_andn2_b32 s64, s65, 0xff\n\t" \
  "s_sub_u32 s64, s64, s61\n\t" \
  "s_ashr_i32 s64, s64, 8\n\t" \
  "s_cmp_eq_u32 s65, 0\n\t" \
  "s_cselect_b32 s64, " CH_DLF ", s64\n\t" \
  "s_sub_i32 s64, s64, " CH_RO1 "\n\t" \
  "s_min_i32 s64, s64, s34\n\t" \
  "s_max_i32 s64, s64, " CH_DLL1 "\n\t" \
  "s_add_u32 s64, s64, s70\n\t" \
  "s_mov_b32 s74, 0\n\t" \
  "s_movk_i32 s60, 8192\n\t" \
  "RWT1_%=:\n\t" \
  "s_memrealtime s[72:73]\n\t" \
  "s_sub_u32 s60, s60, 1\n\t" \
  "s_cselect_b32 s61, 0, -1\n\t" \
  "s_waitcnt lgkmcnt(0)\n\t" \
  "s_sub_i32 s73, s72, s64\n\t" \
  "s_and_b32 s73, s73, s61\n\t" \
  "s_cmp_lt_i32 s73, " CH_FIN "\n\t" \
  "s_cbranch_scc1 RWT1_%=\n\t" \
  "s_cmp_lt_i32 s73, 0\n\t" \
  "s_cbranch_scc1 RWT1_%=f\n\t" \
  "RWT1_%=g:\n\t" \
  "RRETRY1_%=:\n\t" \
  "buffer_load_dwordx4 v[0:3], v69, s[28:31], s97 offen offset:0 sc1\n\t" \
  "buffer_load_dwordx4 v[4:7], v69, s[28:31], s97 offen offset:1024 sc1\n\t" \
  "buffer_load_dwordx4 v[8:11], v69, s[28:31], s97 offen offset:2048 sc1\n\t" \
  "buffer_load_dwordx4 v[12:15], v69, s[28:31], s97 offen offset:3072 sc1\n\t" \
  "v_lshrrev_b32 v20, 2, v69\n\t" \
  "v_and_b32 v20, 16, v20\n\t" \
  "v_mov_b32 v21, 0x3f80\n\t" \
  "v_lshlrev_b32 v21, v20, v21\n\t" \
  "v_bfe_u32 v20, v69, 4, 2\n\t" \
  "v_cmp_eq_u32 vcc, 0, v20\n\t" \
  "s_nop 1\n\t" \
  "v_cndmask_b32 v32, 0, v21, vcc\n\t" \
  "v_cmp_eq_u32 vcc, 1, v20\n\t" \
  "s_nop 1\n\t" \
  "v_cndmask_b32 v33, 0, v21, vcc\n\t" \
  "v_cmp_eq_u32 vcc, 2, v20\n\t" \
  "s_nop 1\n\t" \
  "v_cndmask_b32 v34, 0, v21, vcc\n\t" \
  "v_cmp_eq_u32 vcc, 3, v20\n\t" \
  "s_nop 1\n\t" \
  "v_cndmask_b32 v35, 0, v21, vcc\n\t" \
  "v_bfe_u32 v38, v69, 7, 1\n\t" \
  "v_bfe_u32 v20, v69, 9, 1\n\t" \
  "v_lshl_add_u32 v38, v20, 2, v38\n\t" \
  "v_lshlrev_b32 v38, 12, v38\n\t" \
  "v_and_b32 v20, 0x60, v69\n\t" \
  "v_add_u32 v38, v38, v20\n\t" \
  "v_bfe_u32 v20, v69, 8, 1\n\t" \
  "v_lshl_add_u32 v38, v20, 4, v38\n\t" \
  "v_bfe_u32 v20, v69, 4, 1\n\t" \
  "v_lshl_add_u32 v38, v20, 3, v38\n\t" \
  "v_mov_b32 v44, 0\n\t" \
  CH_ALD \
  "s_waitcnt vmcnt(3)\n\t" \
  "v_mfma_f32_16x16x32_bf16 v[40:43], v[32:35], v[0:3], 0\n\t" \
  "v_max3_u32 v44, v44, v0, v1\n\t" \
  "v_max3_u32 v44, v44, v2, v3\n\t" \
  "s_waitcnt vmcnt(2)\n\t" \
  "v_mfma_f32_16x16x32_bf16 v[40:43], v[32:35], v[4:7], v[40:43]\n\t" \
  "v_max3_u32 v44, v44, v4, v5\n\t" \
  "v_max3_u32 v44, v44, v6, v7\n\t" \
  "s_waitcnt vmcnt(1)\n\t" \
  "v_mfma_f32_16x16x32_bf16 v[40:43], v[32:35], v[8:11], v[40:43]\n\t" \
  "v_max3_u32 v44, v44, v8, v9\n\t" \
  "v_max3_u32 v44, v44, v10, v11\n\t" \
  "s_waitcnt vmcnt(0)\n\t" \
  "v_mfma_f32_16x16x32_bf16 v[40:43], v[32:35], v[12:15], v[40:43]\n\t" \
  "v_max3_u32 v44, v44, v12, v13\n\t" \
  "v_max3_u32 v44, v44, v14, v15\n\t" \
  "v_cmp_eq_u32 vcc, -1, v44\n\t" \
  "s_cbranch_vccnz RFAIL1_%=\n\t" \
  "s_nop 3\n\t" \
  "v_add_f32_dpp v40, v40, v40 row_ror:8 row_mask:0xf bank_mask:0xf\n\t" \
  "v_add_f32_dpp v41, v41, v41 row_ror:8 row_mask:0xf bank_mask:0xf\n\t" \
  "v_add_f32_dpp v42, v42, v42 row_ror:8 row_mask:0xf bank_mask:0xf\n\t" \
  "v_add_f32_dpp v43, v43, v43 row_ror:8 row_mask:0xf bank_mask:0xf\n\t" \
  "v_cvt_pk_bf16_f32 v40, v40, v41\n\t" \
  "v_cvt_pk_bf16_f32 v41, v42, v43\n\t" \
  "buffer_store_dwordx2 v[40:41], v38, s[28:31], s98 offen sc1\n\t" \
  "RDONE1_%=:\n\t" \
  "s_barrier\n\t" \
  "s_add_u32 s48, s48, 1\n\t" \
  "s_mov_b32 s49, s53\n\t" \
  "s_add_u32 s53, s53, " CH_SLOT "\n\t" \
  "s_add_u32 s60, s53, " CH_SKIP "\n\t" \
  "s_lshr_b32 s61, s96, 1\n\t" \
  "s_add_u32 s86, s86, 1\n\t" \
  "s_cmp_eq_u32 s86, 3\n\t" \
  "s_cselect_b32 s53, s60, s53\n\t" \
  "s_cselect_b32 s96, s61, s96\n\t" \
  "s_cselect_b32 s86, 0, s86\n\t" \
  "s_cmp_ge_u32 s53, " CH_RING "\n\t" \
  "s_cselect_b32 s53, 0, s53\n\t" \
  "s_cselect_b32 s96, s21, s96\n\t" \
  "s_andn2_b32 s53, s53, 0x2000\n\t" \
  "s_and_b32 s60, s96, 1\n\t" \
  "s_lshl_b32 s60, s60, 13\n\t" \
  "s_or_b32 s53, s53, s60\n\t" \
  "s_mov_b32 s68, 0\n\t" \
  "s_add_u32 s97, s41, s53\n\t" \
  "s_add_u32 s98, s37, s53\n\t" \
  "s_add_u32 s82, s48, 0\n\t" \
  "s_and_b32 s82, s82, " CH_TRM "\n\t" \
  "s_lshr_b64 s[60:61], s[90:91], s82\n\t" \
  "s_and_b32 s60, s60, 1\n\t" \
  "s_lshl_b32 s60, s60, 13\n\t" \
  "s_add_u32 s82, s82, " CH_SG0 "\n\t" \
  "s_lshl_b32 s82, s82, 21\n\t" \
  "s_or_b32 s82, s82, s60\n\t" \
  "s_add_u32 s50, s82, s38\n\t" \
  "s_add_u32 s69, s82, s39\n\t" \
  "s_cmp_eq_u32 s34, 0\n\t" \
  "s_cbranch_scc1 LINI_%=\n\t" \
  "s_branch LG1_%=\n\t" \
  "RFAIL1_%=:\n\t" \
  "s_add_u32 s74, s74, 1\n\t" \
  "s_sleep " CH_RSL "\n\t" \
  "s_waitcnt vmcnt(0)\n\t" \
  "s_cmp_gt_u32 s74, s84\n\t" \
  "s_cbranch_scc1 RG1_%=\n\t" \
  "s_and_b32 s60, s74, 1023\n\t" \
  "s_cmp_lg_u32 s60, 0\n\t" \
  "s_cbranch_scc1 RRETRY1_%=\n\t" \
  "buffer_load_dword v46, off, s[28:31], s92 sc1\n\t" \
  "s_waitcnt vmcnt(0)\n\t" \
  "v_readfirstlane_b32 s60, v46\n\t" \
  "s_nop 0\n\t" \
  "s_cmp_eq_u32 s60, 0\n\t" \
  "s_cbranch_scc1 RRETRY1_%=\n\t" \
  "RG1_%=:\n\t" \
  "s_mov_b32 s84, 0\n\t" \
  "s_lshl_b32 s60, s36, 4\n\t" \
  "s_or_b32 s60, s60, s48\n\t" \
  "s_or_b32 s60, s60, 536870912\n\t" \
  "v_mov_b32 v76, s60\n\t" \
  "v_mov_b32 v77, 0\n\t" \
  "s_mov_b64 exec, 1\n\t" \
  "buffer_atomic_cmpswap v[76:77], off, s[28:31], s92 sc1\n\t" \
  "s_mov_b64 exec, -1\n\t" \
  "s_branch RDONE1_%=\n\t" \
  "LFAIL1_%=:\n\t" \
  "v_max_u32 v75, v0, v1\n\t" \
  "v_max3_u32 v75, v75, v2, v3\n\t" \
  "v_max3_u32 v75, v75, v4, v5\n\t" \
  "v_max3_u32 v75, v75, v6, v7\n\t" \
  "v_max3_u32 v75, v75, v8, v9\n\t" \
  "v_max3_u32 v75, v75, v10, v11\n\t" \
  "v_max3_u32 v75, v75, v12, v13\n\t" \
  "v_max3_u32 v75, v75, v14, v15\n\t" \
  "v_max3_u32 v75, v75, v16, v17\n\t" \
  "v_max3_u32 v75, v75, v18, v19\n\t" \
  "v_max3_u32 v75, v75, v20, v21\n\t" \
  "v_max3_u32 v75, v75, v22, v23\n\t" \
  "v_max3_u32 v75, v75, v24, v25\n\t" \
  "v_max3_u32 v75, v75, v26, v27\n\t" \
  "v_max3_u32 v75, v75, v28, v29\n\t" \
  "v_max3_u32 v75, v75, v30, v31\n\t" \
  "v_max3_u32 v75, v75, v32, v33\n\t" \
  "v_max3_u32 v75, v75, v34, v35\n\t" \
  "v_cmp_eq_u32 vcc, -1, v75\n\t" \
  "s_cbranch_vccz LGO1_%=\n\t" \
  "s_add_u32 s68, s68, 1\n\t" \
  "s_waitcnt lgkmcnt(0)\n\t" \
  "s_cmp_gt_u32 s68, s83\n\t" \
  "s_cbranch_scc1 LGV1_%=\n\t" \
  "s_barrier\n\t" \
  "s_waitcnt vmcnt(8)\n\t" \
  "s_branch LG1_%=\n\t" \
  "RWT1_%=f:\n\t" \
  "s_cmp_lt_i32 s73, -2\n\t" \
  "s_cbranch_scc0 RWT1_%=g\n\t" \
  "s_nop 4\n\t" \
  "s_branch RWT1_%=g\n\t" \
  "LGV1_%=:\n\t" \
  "s_mov_b32 s83, 0\n\t" \
  "s_lshl_b32 s60, s36, 4\n\t" \
  "s_or_b32 s60, s60, s48\n\t" \
  "s_or_b32 s60, s60, 805306368\n\t" \
  "v_mov_b32 v76, s60\n\t" \
  "v_mov_b32 v77, 0\n\t" \
  "s_mov_b64 exec, 1\n\t" \
  "buffer_atomic_cmpswap v[76:77], off, s[28:31], s92 sc1\n\t" \
  "s_mov_b64 exec, -1\n\t" \
  "s_branch LGO1_%=\n\t" \
  "LLAST1_%=:\n\t" \
  "s_waitcnt vmcnt(0) lgkmcnt(0)\n\t" \
  "s_barrier\n\t" \
  "s_branch LEND_%=\n\t" \
  ".p2align 6\n\t" \
  "LG2_%=:\n\t" \
  "s_barrier\n\t" \
  "buffer_load_dwordx4 v[52:55], v69, s[28:31], s50 offen sc1\n\t" \
  "buffer_load_dwordx4 v[56:59], v69, s[28:31], s50 offen offset:1024 sc1\n\t" \
  "s_add_u32 s64, s50, 18432\n\t" \
  "s_movk_i32 m0, 11264\n\t" \
  "v_lshlrev_b32 v75, 1, v69\n\t" \
  "buffer_load_dword v75, s[28:31], s64 offen sc1 lds\n\t" \
  "ds_read_b128 v[44:47], v70 offset:0\n\t" \
  "ds_read_b128 v[48:51], v70 offset:1024\n\t" \
  "ds_read_b128 v[60:63], v70 offset:2048\n\t" \
  "ds_read_b128 v[64:67], v70 offset:3072\n\t" \
  "v_mov_b64 v[36:37], 0\n\t" \
  "v_mov_b64 v[38:39], 0\n\t" \
  "v_mov_b64 v[40:41], 0\n\t" \
  "v_mov_b64 v[42:43], 0\n\t" \
  "v_lshrrev_b32 v71, 1, v69\n\t" \
  "v_lshrrev_b32 v72, 3, v69\n\t" \
  "v_and_b32 v72, 0x78, v72\n\t" \
  "v_and_b32 v74, 0x30, v69\n\t" \
  "v_lshl_add_u32 v72, v74, 3, v72\n\t" \
  "s_add_u32 s54, s53, s36\n\t" \
  "s_add_u32 s55, s54, 557056\n\t" \
  "s_add_u32 s54, s54, 131072\n\t" \
  "s_add_u32 s75, s54, 262144\n\t" \
  "s_add_u32 s80, s53, s26\n\t" \
  ".p2align 5\n\t" \
  ".fill 2, 4, 0xbf800000\n\t" \
  "s_waitcnt vmcnt(9)\n\t" \
  "s_barrier\n\t" \
  "ds_read_b128 v[0:3], v69 offset:0\n\t" \
  "ds_read_b128 v[4:7], v69 offset:1024\n\t" \
  "ds_read_b128 v[8:11], v69 offset:2048\n\t" \
  "ds_read_b128 v[12:15], v69 offset:3072\n\t" \
  "s_waitcnt lgkmcnt(3)\n\t" \
  "v_smfmac_f32_16x16x64_bf16 v[36:39], v[0:3], v[44:51], v68\n\t" \
  "ds_read_b128 v[44:47], v70 offset:4096\n\t" \
  "v_smfmac_f32_16x16x64_bf16 v[40:43], v[0:3], v[60:67], v68\n\t" \
  "ds_read_b128 v[48:51], v70 offset:5120\n\t" \
  "s_waitcnt lgkmcnt(4)\n\t" \
  "v_smfmac_f32_16x16x64_bf16 v[36:39], v[4:7], a[32:39], v68\n\t" \
  "ds_read_b128 v[60:63], v70 offset:6144\n\t" \
  "v_smfmac_f32_16x16x64_bf16 v[40:43], v[4:7], a[40:47], v68\n\t" \
  "ds_read_b128 v[64:67], v70 offset:7168\n\t" \
  "s_waitcnt lgkmcnt(5)\n\t" \
  "v_smfmac_f32_16x16x64_bf16 v[36:39], v[8:11], a[80:87], v68\n\t" \
  "s_barrier\n\t" \
  "ds_read_b128 v[16:19], v69 offset:4096\n\t" \
  "v_smfmac_f32_16x16x64_bf16 v[40:43], v[8:11], a[88:95], v68\n\t" \
  "ds_read_b128 v[20:23], v69 offset:5120\n\t" \
  "s_waitcnt lgkmcnt(6)\n\t" \
  "v_smfmac_f32_16x16x64_bf16 v[36:39], v[12:15], a[120:127], v68\n\t" \
  "ds_read_b128 v[24:27], v69 offset:6144\n\t" \
  "s_waitcnt lgkmcnt(5)\n\t" \
  "v_smfmac_f32_16x16x64_bf16 v[40:43], v[12:15], v[44:51], v68\n\t" \
  "ds_read_b128 v[28:31], v69 offset:7168\n\t" \
  "s_waitcnt lgkmcnt(3)\n\t" \
  "v_smfmac_f32_16x16x64_bf16 v[36:39], v[16:19], a[160:167], v68\n\t" \
  "ds_read_b128 v[32:35], v69 offset:8192\n\t" \
  "v_smfmac_f32_16x16x64_bf16 v[40:43], v[16:19], a[168:175], v68\n\t" \
  "s_waitcnt lgkmcnt(3)\n\t" \
  "v_mov_b64 v[44:45], 0\n\t" \
  "v_smfmac_f32_16x16x64_bf16 v[36:39], v[20:23], a[200:207], v68\n\t" \
  "s_waitcnt lgkmcnt(2)\n\t" \
  "v_mov_b64 v[46:47], 0\n\t" \
  "v_smfmac_f32_16x16x64_bf16 v[36:39], v[24:27], a[240:247], v68\n\t" \
  "s_waitcnt lgkmcnt(1)\n\t" \
  "v_mov_b64 v[48:49], 0\n\t" \
  "v_smfmac_f32_16x16x64_bf16 v[36:39], v[28:31], v[112:119], v68\n\t" \
  "s_waitcnt lgkmcnt(0)\n\t" \
  "v_mov_b64 v[50:51], 0\n\t" \
  "v_smfmac_f32_16x16x64_bf16 v[36:39], v[32:35], v[160:167], v68\n\t" \
  "v_smfmac_f32_16x16x64_bf16 v[40:43], v[20:23], v[60:67], v68\n\t" \
  "ds_read_b128 v[60:63], v70 offset:8192\n\t" \
  "v_smfmac_f32_16x16x64_bf16 v[40:43], v[24:27], a[248:255], v68\n\t" \
  "ds_read_b128 v[64:67], v70 offset:9216\n\t" \
  "v_add_f32 v74, v36, v38\n\t" \
  "v_smfmac_f32_16x16x64_bf16 v[40:43], v[28:31], v[120:127], v68\n\t" \
  "v_add_f32 v37, v37, v39\n\t" \
  "v_smfmac_f32_16x16x64_bf16 v[40:43], v[32:35], v[168:175], v68\n\t" \
  "v_cmp_u_f32 vcc, v74, v37\n\t" \
  "v_smfmac_f32_16x16x64_bf16 v[44:47], v[0:3], a[0:7], v68\n\t" \
  "s_cbranch_vccnz LFAIL2_%=\n\t" \
  "LGO2_%=:\n\t" \
  "v_cvt_pk_bf16_f32 v74, v74, v37\n\t" \
  "v_smfmac_f32_16x16x64_bf16 v[44:47], v[4:7], a[48:55], v68\n\t" \
  "s_waitcnt lgkmcnt(0)\n\t" \
  "v_add_f32 v75, v40, v42\n\t" \
  "v_smfmac_f32_16x16x64_bf16 v[44:47], v[8:11], v[60:67], v68\n\t" \
  "ds_read_b128 v[60:63], v70 offset:10240\n\t" \
  "v_add_f32 v41, v41, v43\n\t" \
  "v_cvt_pk_bf16_f32 v75, v75, v41\n\t" \
  "v_smfmac_f32_16x16x64_bf16 v[44:47], v[12:15], a[128:135], v68\n\t" \
  "ds_write2st64_b32 v71, v74, v75 offset0:40 offset1:42\n\t" \
  "v_smfmac_f32_16x16x64_bf16 v[44:47], v[16:19], a[176:183], v68\n\t" \
  "ds_read_b128 v[64:67], v70 offset:11264\n\t" \
  "v_smfmac_f32_16x16x64_bf16 v[44:47], v[20:23], a[208:215], v68\n\t" \
  "v_smfmac_f32_16x16x64_bf16 v[44:47], v[24:27], v[80:87], v68\n\t" \
  "v_smfmac_f32_16x16x64_bf16 v[44:47], v[28:31], v[128:135], v68\n\t" \
  "s_waitcnt lgkmcnt(0)\n\t" \
  "v_smfmac_f32_16x16x64_bf16 v[44:47], v[32:35], v[60:67], v68\n\t" \
  "s_barrier\n\t" \
  "ds_read_b64_tr_b16 v[76:77], v72 offset:10240\n\t" \
  "v_smfmac_f32_16x16x64_bf16 v[48:51], v[0:3], a[8:15], v68\n\t" \
  "ds_read_b64_tr_b16 v[78:79], v72 offset:10752\n\t" \
  "v_smfmac_f32_16x16x64_bf16 v[48:51], v[4:7], a[56:63], v68\n\t" \
  "buffer_load_dwordx4 v[60:63], v69, s[28:31], s64 offen sc1\n\t" \
  "v_add_f32 v74, v44, v46\n\t" \
  "v_smfmac_f32_16x16x64_bf16 v[48:51], v[8:11], a[96:103], v68\n\t" \
  "v_add_f32 v45, v45, v47\n\t" \
  "v_smfmac_f32_16x16x64_bf16 v[48:51], v[12:15], a[136:143], v68\n\t" \
  "buffer_load_dwordx4 v[64:67], v69, s[28:31], s64 offen offset:1024 sc1\n\t" \
  "s_waitcnt vmcnt(4)\n\t" \
  "s_waitcnt lgkmcnt(0)\n\t" \
  "v_cvt_pk_bf16_f32 v74, v74, v45\n\t" \
  "v_mfma_f32_16x16x32_bf16 v[36:39], v[52:55], v[76:79], 0\n\t" \
  "s_waitcnt vmcnt(3)\n\t" \
  "v_mfma_f32_16x16x32_bf16 v[40:43], v[56:59], v[76:79], 0\n\t" \
  "v_mov_b64 v[52:53], 0\n\t" \
  "v_mov_b64 v[54:55], 0\n\t" \
  "v_smfmac_f32_16x16x64_bf16 v[48:51], v[16:19], a[184:191], v68\n\t" \
  "v_mov_b64 v[56:57], 0\n\t" \
  "v_mov_b64 v[58:59], 0\n\t" \
  "v_smfmac_f32_16x16x64_bf16 v[48:51], v[20:23], a[216:223], v68\n\t" \
  "ds_write_b32 v71, v74 offset:1024\n\t" \
  "v_add_f32_dpp v36, v40, v36 quad_perm:[2,3,0,1] row_mask:0xf bank_mask:0xf\n\t" \
  "v_smfmac_f32_16x16x64_bf16 v[48:51], v[24:27], v[88:95], v68\n\t" \
  "v_add_f32_dpp v37, v41, v37 quad_perm:[2,3,0,1] row_mask:0xf bank_mask:0xf\n\t" \
  "v_smfmac_f32_16x16x64_bf16 v[48:51], v[28:31], v[136:143], v68\n\t" \
  "v_add_f32_dpp v38, v42, v38 quad_perm:[2,3,0,1] row_mask:0xf bank_mask:0xf\n\t" \
  "v_smfmac_f32_16x16x64_bf16 v[48:51], v[32:35], v[176:183], v68\n\t" \
  "v_add_f32_dpp v39, v43, v39 quad_perm:[2,3,0,1] row_mask:0xf bank_mask:0xf\n\t" \
  "v_smfmac_f32_16x16x64_bf16 v[52:55], v[0:3], a[16:23], v68\n\t" \
  "s_mov_b64 exec, s[58:59]\n\t" \
  "ds_write_b128 v69, v[36:39] offset:3072\n\t" \
  "s_mov_b64 exec, -1\n\t" \
  "v_smfmac_f32_16x16x64_bf16 v[52:55], v[4:7], a[64:71], v68\n\t" \
  "v_add_f32 v75, v48, v50\n\t" \
  "v_add_f32 v49, v49, v51\n\t" \
  "v_cvt_pk_bf16_f32 v75, v75, v49\n\t" \
  "v_smfmac_f32_16x16x64_bf16 v[56:59], v[0:3], a[24:31], v68\n\t" \
  "ds_write_b32 v71, v75 offset:1536\n\t" \
  "v_smfmac_f32_16x16x64_bf16 v[56:59], v[4:7], a[72:79], v68\n\t" \
  "ds_read_b64_tr_b16 v[0:1], v72 offset:9216\n\t" \
  "ds_read_b64_tr_b16 v[2:3], v72 offset:9728\n\t" \
  "v_smfmac_f32_16x16x64_bf16 v[52:55], v[8:11], a[104:111], v68\n\t" \
  "ds_read_b128 v[36:39], v70 offset:12288\n\t" \
  "v_smfmac_f32_16x16x64_bf16 v[56:59], v[8:11], a[112:119], v68\n\t" \
  "ds_read_b128 v[40:43], v70 offset:13312\n\t" \
  "s_waitcnt lgkmcnt(4)\n\t" \
  "s_barrier\n\t" \
  "ds_read_b64_tr_b16 v[44:45], v72 offset:1024\n\t" \
  "ds_read_b64_tr_b16 v[46:47], v72 offset:1536\n\t" \
  "v_smfmac_f32_16x16x64_bf16 v[52:55], v[12:15], a[144:151], v68\n\t" \
  "ds_read_b64_tr_b16 v[48:49], v72 offset:0\n\t" \
  "ds_read_b64_tr_b16 v[50:51], v72 offset:512\n\t" \
  "v_smfmac_f32_16x16x64_bf16 v[56:59], v[12:15], a[152:159], v68\n\t" \
  "v_smfmac_f32_16x16x64_bf16 v[52:55], v[16:19], a[192:199], v68\n\t" \
  "ds_read_b128 v[4:7], v69 offset:2048\n\t" \
  "s_waitcnt lgkmcnt(5)\n\t" \
  "v_smfmac_f32_16x16x64_bf16 v[56:59], v[16:19], v[36:43], v68\n\t" \
  "ds_read_b128 v[8:11], v69 offset:3072\n\t" \
  "s_waitcnt lgkmcnt(4)\n\t" \
  "v_mfma_f32_16x16x32_bf16 v[12:15], v[44:47], v[76:79], 0\n\t" \
  "ds_read_b128 v[36:39], v70 offset:14336\n\t" \
  "s_waitcnt lgkmcnt(3)\n\t" \
  "v_mfma_f32_16x16x32_bf16 v[12:15], v[48:51], v[0:3], v[12:15]\n\t" \
  "ds_read_b128 v[40:43], v70 offset:15360\n\t" \
  "v_smfmac_f32_16x16x64_bf16 v[52:55], v[20:23], a[224:231], v68\n\t" \
  "buffer_store_dwordx4 v[44:47], v69, s[28:31], s69 offen offset:1024 sc1\n\t" \
  "s_waitcnt lgkmcnt(2)\n\t" \
  "v_add_f32 v4, v4, v8\n\t" \
  "v_add_f32 v5, v5, v9\n\t" \
  "v_add_f32 v6, v6, v10\n\t" \
  "v_cndmask_b32 v78, v13, v14, s[56:57]\n\t" \
  "v_cndmask_b32 v76, v12, v15, s[56:57]\n\t" \
  "v_add_f32 v7, v7, v11\n\t" \
  "v_max3_f32 v77, v4, v5, v6\n\t" \
  "v_add_f32_dpp v76, v78, v76 quad_perm:[2,3,0,1] row_mask:0xf bank_mask:0xf\n\t" \
  "v_max3_f32 v77, v77, v7, v76\n\t" \
  "v_smfmac_f32_16x16x64_bf16 v[56:59], v[20:23], a[232:239], v68\n\t" \
  "ds_read_b128 v[44:47], v70 offset:16384\n\t" \
  "v_max_f32_dpp v77, v77, v77 quad_perm:[2,3,0,1] row_mask:0xf bank_mask:0xf\n\t" \
  "v_mov_b32 v78, v77\n\t" \
  "v_smfmac_f32_16x16x64_bf16 v[52:55], v[24:27], v[96:103], v68\n\t" \
  "ds_read_b128 v[48:51], v70 offset:17408\n\t" \
  "v_permlane16_swap_b32 v77, v78\n\t" \
  "v_max_f32 v77, v77, v78\n\t" \
  "v_mov_b32 v78, v77\n\t" \
  "v_smfmac_f32_16x16x64_bf16 v[52:55], v[28:31], v[144:151], v68\n\t" \
  "ds_read_b128 v[16:19], v70 offset:18432\n\t" \
  "v_permlane32_swap_b32 v77, v78\n\t" \
  "v_max_f32 v77, v77, v78\n\t" \
  "v_smfmac_f32_16x16x64_bf16 v[52:55], v[32:35], v[184:191], v68\n\t" \
  "ds_read_b128 v[20:23], v70 offset:19456\n\t" \
  "v_mul_f32 v77, 0xbe0293ee, v77\n\t" \
  "v_smfmac_f32_16x16x64_bf16 v[56:59], v[24:27], v[104:111], v68\n\t" \
  "v_fmamk_f32 v4, v4, 0x3e0293ee, v77\n\t" \
  "v_smfmac_f32_16x16x64_bf16 v[56:59], v[28:31], v[152:159], v68\n\t" \
  "ds_read_b128 v[24:27], v70 offset:22528\n\t" \
  "s_waitcnt lgkmcnt(4)\n\t" \
  "v_fmamk_f32 v5, v5, 0x3e0293ee, v77\n\t" \
  "v_smfmac_f32_16x16x64_bf16 v[56:59], v[32:35], v[36:43], v68\n\t" \
  "ds_read_b128 v[28:31], v70 offset:23552\n\t" \
  "v_fmamk_f32 v6, v6, 0x3e0293ee, v77\n\t" \
  "v_fmamk_f32 v7, v7, 0x3e0293ee, v77\n\t" \
  "v_fmamk_f32 v76, v76, 0x3e0293ee, v77\n\t" \
  "v_exp_f32 v4, v4\n\t" \
  "v_exp_f32 v5, v5\n\t" \
  "v_exp_f32 v6, v6\n\t" \
  "v_exp_f32 v7, v7\n\t" \
  "v_cvt_pk_bf16_f32 v4, v4, v5\n\t" \
  "v_exp_f32 v76, v76\n\t" \
  "v_cvt_pk_bf16_f32 v5, v6, v7\n\t" \
  "v_mov_b32_dpp v12, v4 quad_perm:[0,1,0,1] row_mask:0xf bank_mask:0xf\n\t" \
  "v_mov_b32_dpp v14, v4 quad_perm:[2,3,2,3] row_mask:0xf bank_mask:0xf\n\t" \
  "v_mov_b32_dpp v13, v5 quad_perm:[0,1,0,1] row_mask:0xf bank_mask:0xf\n\t" \
  "v_mov_b32_dpp v15, v5 quad_perm:[2,3,2,3] row_mask:0xf bank_mask:0xf\n\t" \
  "v_add_f32 v52, v52, v54\n\t" \
  "v_add_f32 v53, v53, v55\n\t" \
  "s_waitcnt vmcnt(2)\n\t" \
  "v_mfma_f32_16x16x32_bf16 v[0:3], v[60:63], v[12:15], 0\n\t" \
  "ds_read_b128 v[36:39], v70 offset:20480\n\t" \
  "v_add_f32 v56, v56, v58\n\t" \
  "v_add_f32 v57, v57, v59\n\t" \
  "v_cvt_pk_bf16_f32 v52, v52, v53\n\t" \
  "v_cvt_pk_bf16_f32 v53, v56, v57\n\t" \
  "s_waitcnt vmcnt(1)\n\t" \
  "v_mfma_f32_16x16x32_bf16 v[4:7], v[64:67], v[12:15], 0\n\t" \
  "ds_read_b128 v[40:43], v70 offset:21504\n\t" \
  "v_mov_b64 v[56:57], s[88:89]\n\t" \
  "v_mov_b64 v[58:59], s[88:89]\n\t" \
  "v_mov_b32_dpp v78, v76 quad_perm:[0,1,0,1] row_mask:0xf bank_mask:0xf\n\t" \
  "v_mov_b32_dpp v79, v76 quad_perm:[2,3,2,3] row_mask:0xf bank_mask:0xf\n\t" \
  "v_mfma_f32_16x16x32_bf16 v[32:35], v[56:59], v[12:15], 0\n\t" \
  "ds_read_b128 v[60:63], v70 offset:24576\n\t" \
  "v_cvt_pk_bf16_f32 v11, v78, v79\n\t" \
  "v_cvt_pk_bf16_f32 v8, v78, v79\n\t" \
  "v_mov_b64 v[54:55], 0\n\t" \
  "v_mov_b32 v9, 0\n\t" \
  "v_mov_b32 v10, 0\n\t" \
  "v_mov_b64 v[14:15], 0\n\t" \
  "v_mov_b64 v[12:13], 0\n\t" \
  "v_mfma_f32_16x16x32_bf16 v[32:35], v[54:57], v[8:11], v[32:35]\n\t" \
  "buffer_store_dwordx2 v[52:53], v71, s[28:31], s69 offen offset:3072 sc1\n\t" \
  "s_cmp_eq_u32 s48, 63\n\t" \
  "v_mfma_f32_16x16x32_bf16 v[0:3], v[52:55], v[8:11], v[0:3]\n\t" \
  "ds_read_b128 v[64:67], v70 offset:25600\n\t" \
  "v_mov_b64 v[56:57], 0\n\t" \
  "v_mov_b64 v[58:59], 0\n\t" \
  "v_mfma_f32_16x16x32_bf16 v[4:7], v[52:55], v[10:13], v[4:7]\n\t" \
  "s_cbranch_scc1 LLAST2_%=\n\t" \
  "v_rcp_f32 v74, v32\n\t" \
  "v_mov_b32 v11, 0\n\t" \
  "v_mov_b32 v8, 0\n\t" \
  "v_pk_mul_f32 v[0:1], v[0:1], v[74:75] op_sel_hi:[1,0]\n\t" \
  "v_pk_mul_f32 v[2:3], v[2:3], v[74:75] op_sel_hi:[1,0]\n\t" \
  "v_cvt_pk_bf16_f32 v76, v0, v1\n\t" \
  "v_cvt_pk_bf16_f32 v77, v2, v3\n\t" \
  "v_pk_mul_f32 v[4:5], v[4:5], v[74:75] op_sel_hi:[1,0]\n\t" \
  "v_pk_mul_f32 v[6:7], v[6:7], v[74:75] op_sel_hi:[1,0]\n\t" \
  "v_cvt_pk_bf16_f32 v78, v4, v5\n\t" \
  "v_cvt_pk_bf16_f32 v79, v6, v7\n\t" \
  "ds_write_b128 v69, v[76:79] offset:6144\n\t" \
  "s_waitcnt lgkmcnt(2)\n\t" \
  "v_smfmac_f32_16x16x64_bf16 v[12:15], v[76:79], v[44:51], v68\n\t" \
  "ds_read_b128 v[44:47], v70 offset:26624\n\t" \
  "v_smfmac_f32_16x16x64_bf16 v[56:59], v[76:79], v[16:23], v68\n\t" \
  "ds_read_b128 v[48:51], v70 offset:27648\n\t" \
  "v_mov_b64 v[52:53], 0\n\t" \
  "v_smfmac_f32_16x16x64_bf16 v[8:11], v[76:79], v[36:43], v68\n\t" \
  "ds_read_b128 v[16:19], v70 offset:28672\n\t" \
  "v_smfmac_f32_16x16x64_bf16 v[52:55], v[76:79], v[24:31], v68\n\t" \
  "s_waitcnt lgkmcnt(3)\n\t" \
  "s_barrier\n\t" \
  "ds_read_b128 v[0:3], v69 offset:7168\n\t" \
  "ds_read_b128 v[4:7], v69 offset:4096\n\t" \
  "v_lshrrev_b32 v71, 7, v69\n\t" \
  "v_and_b32 v74, 0x70, v69\n\t" \
  "v_lshl_add_u32 v71, v71, 14, v74\n\t" \
  "v_bfe_u32 v72, v69, 2, 7\n\t" \
  "v_and_b32 v74, 0x200, v69\n\t" \
  "s_waitcnt lgkmcnt(1)\n\t" \
  "v_smfmac_f32_16x16x64_bf16 v[8:11], v[0:3], v[60:67], v68\n\t" \
  "ds_read_b128 v[20:23], v70 offset:29696\n\t" \
  "v_mov_b64 v[76:77], 0\n\t" \
  "v_smfmac_f32_16x16x64_bf16 v[12:15], v[0:3], v[200:207], v68\n\t" \
  "buffer_load_dword v75, off, s[28:31], s80 sc1\n\t" \
  "ds_read_b128 v[32:35], v69 offset:5120\n\t" \
  "v_mov_b64 v[78:79], 0\n\t" \
  "v_smfmac_f32_16x16x64_bf16 v[52:55], v[0:3], v[44:51], v68\n\t" \
  "ds_read_b128 v[24:27], v70 offset:30720\n\t" \
  "v_lshl_add_u32 v72, v74, 3, v72\n\t" \
  "v_smfmac_f32_16x16x64_bf16 v[56:59], v[0:3], v[232:239], v68\n\t" \
  "ds_read_b128 v[28:31], v70 offset:31744\n\t" \
  "s_waitcnt lgkmcnt(4)\n\t" \
  "v_smfmac_f32_16x16x64_bf16 v[12:15], v[4:7], v[208:215], v68\n\t" \
  "ds_read_b128 v[60:63], v70 offset:32768\n\t" \
  "s_waitcnt lgkmcnt(4)\n\t" \
  "v_smfmac_f32_16x16x64_bf16 v[8:11], v[4:7], v[16:23], v68\n\t" \
  "ds_read_b128 v[64:67], v70 offset:33792\n\t" \
  "s_waitcnt lgkmcnt(4)\n\t" \
  "v_smfmac_f32_16x16x64_bf16 v[8:11], v[32:35], v[192:199], v68\n\t" \
  "ds_read_b128 v[36:39], v73 offset:4096\n\t" \
  "v_smfmac_f32_16x16x64_bf16 v[12:15], v[32:35], v[216:223], v68\n\t" \
  "ds_read_b128 v[40:43], v73 offset:6144\n\t" \
  "s_waitcnt lgkmcnt(4)\n\t" \
  "v_smfmac_f32_16x16x64_bf16 v[52:55], v[4:7], v[24:31], v68\n\t" \
  "ds_read_b128 v[44:47], v70 offset:34816\n\t" \
  "v_cvt_pk_bf16_f32 v8, v8, v9\n\t" \
  "v_smfmac_f32_16x16x64_bf16 v[52:55], v[32:35], v[224:231], v68\n\t" \
  "ds_read_b128 v[48:51], v70 offset:35840\n\t" \
  "v_cvt_pk_bf16_f32 v9, v10, v11\n\t" \
  "v_smfmac_f32_16x16x64_bf16 v[56:59], v[4:7], v[240:247], v68\n\t" \
  "s_waitcnt lgkmcnt(4)\n\t" \
  "v_cvt_pk_bf16_f32 v10, v12, v13\n\t" \
  "v_smfmac_f32_16x16x64_bf16 v[56:59], v[32:35], v[60:67], v68\n\t" \
  "s_waitcnt lgkmcnt(3)\n\t" \
  "v_cvt_pk_bf16_f32 v11, v14, v15\n\t" \
  "v_smfmac_f32_16x16x64_bf16 v[76:79], v[36:39], v[248:255], v68\n\t" \
  "buffer_store_dwordx4 v[8:11], v71, s[28:31], s54 offen sc1\n\t" \
  "s_waitcnt lgkmcnt(0)\n\t" \
  "v_cvt_pk_bf16_f32 v52, v52, v53\n\t" \
  "v_smfmac_f32_16x16x64_bf16 v[76:79], v[40:43], v[44:51], v68\n\t" \
  "v_cvt_pk_bf16_f32 v53, v54, v55\n\t" \
  "v_cvt_pk_bf16_f32 v54, v56, v57\n\t" \
  "v_cvt_pk_bf16_f32 v55, v58, v59\n\t" \
  "buffer_store_dwordx4 v[52:55], v71, s[28:31], s75 offen sc1\n\t" \
  "s_nop 2\n\t" \
  "v_add_f32 v76, v76, v78\n\t" \
  "v_add_f32 v77, v77, v79\n\t" \
  "v_cvt_pk_bf16_f32 v76, v76, v77\n\t" \
  "buffer_store_dword v76, v72, s[28:31], s55 offen sc1\n\t" \
  "s_memrealtime s[70:71]\n\t" \
  "s_waitcnt vmcnt(3)\n\t" \
  "v_readfirstlane_b32 s65, v75\n\t" \
  "s_barrier\n\t" \
  "s_waitcnt lgkmcnt(0)\n\t" \
  "s_not_b32 s65, s65\n\t" \
  "s_lshl_b32 s61, s70, 8\n\t" \
  "s_andn2_b32 s64, s65, 0xff\n\t" \
  "s_sub_u32 s64, s64, s61\n\t" \
  "s_ashr_i32 s64, s64, 8\n\t" \
  "s_cmp_eq_u32 s65, 0\n\t" \
  "s_cselect_b32 s64, " CH_DLF ", s64\n\t" \
  "s_min_i32 s64, s64, s34\n\t" \
  "s_max_i32 s64, s64, " CH_DLL1 "\n\t" \
  "s_add_u32 s64, s64, s70\n\t" \
  "s_mov_b32 s74, 0\n\t" \
  "s_movk_i32 s60, 8192\n\t" \
  "RWT2_%=:\n\t" \
  "s_memrealtime s[72:73]\n\t" \
  "s_sub_u32 s60, s60, 1\n\t" \
  "s_cselect_b32 s61, 0, -1\n\t" \
  "s_waitcnt lgkmcnt(0)\n\t" \
  "s_sub_i32 s73, s72, s64\n\t" \
  "s_and_b32 s73, s73, s61\n\t" \
  "s_cmp_lt_i32 s73, " CH_FIN "\n\t" \
  "s_cbranch_scc1 RWT2_%=\n\t" \
  "s_cmp_lt_i32 s73, 0\n\t" \
  "s_cbranch_scc1 RWT2_%=f\n\t" \
  "RWT2_%=g:\n\t" \
  "RRETRY2_%=:\n\t" \
  "buffer_load_dwordx4 v[0:3], v69, s[28:31], s97 offen offset:0 sc1\n\t" \
  "buffer_load_dwordx4 v[4:7], v69, s[28:31], s97 offen offset:1024 sc1\n\t" \
  "buffer_load_dwordx4 v[8:11], v69, s[28:31], s97 offen offset:2048 sc1\n\t" \
  "buffer_load_dwordx4 v[12:15], v69, s[28:31], s97 offen offset:3072 sc1\n\t" \
  "v_lshrrev_b32 v20, 2, v69\n\t" \
  "v_and_b32 v20, 16, v20\n\t" \
  "v_mov_b32 v21, 0x3f80\n\t" \
  "v_lshlrev_b32 v21, v20, v21\n\t" \
  "v_bfe_u32 v20, v69, 4, 2\n\t" \
  "v_cmp_eq_u32 vcc, 0, v20\n\t" \
  "s_nop 1\n\t" \
  "v_cndmask_b32 v32, 0, v21, vcc\n\t" \
  "v_cmp_eq_u32 vcc, 1, v20\n\t" \
  "s_nop 1\n\t" \
  "v_cndmask_b32 v33, 0, v21, vcc\n\t" \
  "v_cmp_eq_u32 vcc, 2, v20\n\t" \
  "s_nop 1\n\t" \
  "v_cndmask_b32 v34, 0, v21, vcc\n\t" \
  "v_cmp_eq_u32 vcc, 3, v20\n\t" \
  "s_nop 1\n\t" \
  "v_cndmask_b32 v35, 0, v21, vcc\n\t" \
  "v_bfe_u32 v38, v69, 7, 1\n\t" \
  "v_bfe_u32 v20, v69, 9, 1\n\t" \
  "v_lshl_add_u32 v38, v20, 2, v38\n\t" \
  "v_lshlrev_b32 v38, 12, v38\n\t" \
  "v_and_b32 v20, 0x60, v69\n\t" \
  "v_add_u32 v38, v38, v20\n\t" \
  "v_bfe_u32 v20, v69, 8, 1\n\t" \
  "v_lshl_add_u32 v38, v20, 4, v38\n\t" \
  "v_bfe_u32 v20, v69, 4, 1\n\t" \
  "v_lshl_add_u32 v38, v20, 3, v38\n\t" \
  "v_mov_b32 v44, 0\n\t" \
  CH_ALD \
  "s_waitcnt vmcnt(3)\n\t" \
  "v_mfma_f32_16x16x32_bf16 v[40:43], v[32:35], v[0:3], 0\n\t" \
  "v_max3_u32 v44, v44, v0, v1\n\t" \
  "v_max3_u32 v44, v44, v2, v3\n\t" \
  "s_waitcnt vmcnt(2)\n\t" \
  "v_mfma_f32_16x16x32_bf16 v[40:43], v[32:35], v[4:7], v[40:43]\n\t" \
  "v_max3_u32 v44, v44, v4, v5\n\t" \
  "v_max3_u32 v44, v44, v6, v7\n\t" \
  "s_waitcnt vmcnt(1)\n\t" \
  "v_mfma_f32_16x16x32_bf16 v[40:43], v[32:35], v[8:11], v[40:43]\n\t" \
  "v_max3_u32 v44, v44, v8, v9\n\t" \
  "v_max3_u32 v44, v44, v10, v11\n\t" \
  "s_waitcnt vmcnt(0)\n\t" \
  "v_mfma_f32_16x16x32_bf16 v[40:43], v[32:35], v[12:15], v[40:43]\n\t" \
  "v_max3_u32 v44, v44, v12, v13\n\t" \
  "v_max3_u32 v44, v44, v14, v15\n\t" \
  "v_cmp_eq_u32 vcc, -1, v44\n\t" \
  "s_cbranch_vccnz RFAIL2_%=\n\t" \
  "s_nop 3\n\t" \
  "v_add_f32_dpp v40, v40, v40 row_ror:8 row_mask:0xf bank_mask:0xf\n\t" \
  "v_add_f32_dpp v41, v41, v41 row_ror:8 row_mask:0xf bank_mask:0xf\n\t" \
  "v_add_f32_dpp v42, v42, v42 row_ror:8 row_mask:0xf bank_mask:0xf\n\t" \
  "v_add_f32_dpp v43, v43, v43 row_ror:8 row_mask:0xf bank_mask:0xf\n\t" \
  "v_cvt_pk_bf16_f32 v40, v40, v41\n\t" \
  "v_cvt_pk_bf16_f32 v41, v42, v43\n\t" \
  "buffer_store_dwordx2 v[40:41], v38, s[28:31], s98 offen sc1\n\t" \
  "RDONE2_%=:\n\t" \
  "s_barrier\n\t" \
  "s_add_u32 s48, s48, 1\n\t" \
  "s_mov_b32 s49, s53\n\t" \
  "s_add_u32 s53, s53, " CH_SLOT "\n\t" \
  "s_add_u32 s60, s53, " CH_SKIP "\n\t" \
  "s_lshr_b32 s61, s96, 1\n\t" \
  "s_add_u32 s86, s86, 1\n\t" \
  "s_cmp_eq_u32 s86, 3\n\t" \
  "s_cselect_b32 s53, s60, s53\n\t" \
  "s_cselect_b32 s96, s61, s96\n\t" \
  "s_cselect_b32 s86, 0, s86\n\t" \
  "s_cmp_ge_u32 s53, " CH_RING "\n\t" \
  "s_cselect_b32 s53, 0, s53\n\t" \
  "s_cselect_b32 s96, s21, s96\n\t" \
  "s_andn2_b32 s53, s53, 0x2000\n\t" \
  "s_and_b32 s60, s96, 1\n\t" \
  "s_lshl_b32 s60, s60, 13\n\t" \
  "s_or_b32 s53, s53, s60\n\t" \
  "s_mov_b32 s68, 0\n\t" \
  "s_add_u32 s97, s41, s53\n\t" \
  "s_add_u32 s98, s37, s53\n\t" \
  "s_add_u32 s82, s48, 0\n\t" \
  "s_and_b32 s82, s82, " CH_TRM "\n\t" \
  "s_lshr_b64 s[60:61], s[90:91], s82\n\t" \
  "s_and_b32 s60, s60, 1\n\t" \
  "s_lshl_b32 s60, s60, 13\n\t" \
  "s_add_u32 s82, s82, " CH_SG0 "\n\t" \
  "s_lshl_b32 s82, s82, 21\n\t" \
  "s_or_b32 s82, s82, s60\n\t" \
  "s_add_u32 s50, s82, s38\n\t" \
  "s_add_u32 s69, s82, s39\n\t" \
  "s_cmp_eq_u32 s34, 0\n\t" \
  "s_cbranch_scc1 LINI_%=\n\t" \
  "s_branch LG2_%=\n\t" \
  "RFAIL2_%=:\n\t" \
  "s_add_u32 s74, s74, 1\n\t" \
  "s_sleep " CH_RSL "\n\t" \
  "s_waitcnt vmcnt(0)\n\t" \
  "s_cmp_gt_u32 s74, s84\n\t" \
  "s_cbranch_scc1 RG2_%=\n\t" \
  "s_and_b32 s60, s74, 1023\n\t" \
  "s_cmp_lg_u32 s60, 0\n\t" \
  "s_cbranch_scc1 RRETRY2_%=\n\t" \
  "buffer_load_dword v46, off, s[28:31], s92 sc1\n\t" \
  "s_waitcnt vmcnt(0)\n\t" \
  "v_readfirstlane_b32 s60, v46\n\t" \
  "s_nop 0\n\t" \
  "s_cmp_eq_u32 s60, 0\n\t" \
  "s_cbranch_scc1 RRETRY2_%=\n\t" \
  "RG2_%=:\n\t" \
  "s_mov_b32 s84, 0\n\t" \
  "s_lshl_b32 s60, s36, 4\n\t" \
  "s_or_b32 s60, s60, s48\n\t" \
  "s_or_b32 s60, s60, 536870912\n\t" \
  "v_mov_b32 v76, s60\n\t" \
  "v_mov_b32 v77, 0\n\t" \
  "s_mov_b64 exec, 1\n\t" \
  "buffer_atomic_cmpswap v[76:77], off, s[28:31], s92 sc1\n\t" \
  "s_mov_b64 exec, -1\n\t" \
  "s_branch RDONE2_%=\n\t" \
  "LFAIL2_%=:\n\t" \
  "v_max_u32 v75, v0, v1\n\t" \
  "v_max3_u32 v75, v75, v2, v3\n\t" \
  "v_max3_u32 v75, v75, v4, v5\n\t" \
  "v_max3_u32 v75, v75, v6, v7\n\t" \
  "v_max3_u32 v75, v75, v8, v9\n\t" \
  "v_max3_u32 v75, v75, v10, v11\n\t" \
  "v_max3_u32 v75, v75, v12, v13\n\t" \
  "v_max3_u32 v75, v75, v14, v15\n\t" \
  "v_max3_u32 v75, v75, v16, v17\n\t" \
  "v_max3_u32 v75, v75, v18, v19\n\t" \
  "v_max3_u32 v75, v75, v20, v21\n\t" \
  "v_max3_u32 v75, v75, v22, v23\n\t" \
  "v_max3_u32 v75, v75, v24, v25\n\t" \
  "v_max3_u32 v75, v75, v26, v27\n\t" \
  "v_max3_u32 v75, v75, v28, v29\n\t" \
  "v_max3_u32 v75, v75, v30, v31\n\t" \
  "v_max3_u32 v75, v75, v32, v33\n\t" \
  "v_max3_u32 v75, v75, v34, v35\n\t" \
  "v_cmp_eq_u32 vcc, -1, v75\n\t" \
  "s_cbranch_vccz LGO2_%=\n\t" \
  "s_add_u32 s68, s68, 1\n\t" \
  "s_waitcnt lgkmcnt(0)\n\t" \
  "s_cmp_gt_u32 s68, s83\n\t" \
  "s_cbranch_scc1 LGV2_%=\n\t" \
  "s_barrier\n\t" \
  "s_waitcnt vmcnt(8)\n\t" \
  "s_branch LG2_%=\n\t" \
  "RWT2_%=f:\n\t" \
  "s_cmp_lt_i32 s73, -2\n\t" \
  "s_cbranch_scc0 RWT2_%=g\n\t" \
  "s_nop 4\n\t" \
  "s_branch RWT2_%=g\n\t" \
  "LGV2_%=:\n\t" \
  "s_mov_b32 s83, 0\n\t" \
  "s_lshl_b32 s60, s36, 4\n\t" \
  "s_or_b32 s60, s60, s48\n\t" \
  "s_or_b32 s60, s60, 805306368\n\t" \
  "v_mov_b32 v76, s60\n\t" \
  "v_mov_b32 v77, 0\n\t" \
  "s_mov_b64 exec, 1\n\t" \
  "buffer_atomic_cmpswap v[76:77], off, s[28:31], s92 sc1\n\t" \
  "s_mov_b64 exec, -1\n\t" \
  "s_branch LGO2_%=\n\t" \
  "LLAST2_%=:\n\t" \
  "s_waitcnt vmcnt(0) lgkmcnt(0)\n\t" \
  "s_barrier\n\t" \
  "s_branch LEND_%=\n\t" \
  ".p2align 6\n\t" \
  "LG3_%=:\n\t" \
  "s_barrier\n\t" \
  "buffer_load_dwordx4 v[52:55], v69, s[28:31], s50 offen sc1\n\t" \
  "buffer_load_dwordx4 v[56:59], v69, s[28:31], s50 offen offset:1024 sc1\n\t" \
  "s_add_u32 s64, s50, 16384\n\t" \
  "s_movk_i32 m0, 11264\n\t" \
  "v_lshlrev_b32 v75, 1, v69\n\t" \
  "buffer_load_dword v75, s[28:31], s64 offen sc1 lds\n\t" \
  "s_lshr_b32 s61, s20, 3\n\t" \
  "s_mul_i32 s47, s61, " CH_KD "\n\t" \
  "s_lshr_b32 s47, s47, 10\n\t" \
  "s_lshl_b32 s47, s47, 8\n\t" \
  "s_mul_i32 s61, s61, " CH_KB "\n\t" \
  "s_add_u32 s61, s61, s22\n\t" \
  "s_lshr_b32 s61, s61, 10\n\t" \
  "s_min_u32 s61, s61, 100\n\t" \
  "s_or_b32 s47, s47, s61\n\t" \
  "s_cmp_lt_u32 s48, " CH_ES "\n\t" \
  "s_cselect_b32 s60, " CH_EX ", 0\n\t" \
  "s_add_u32 s47, s47, s60\n\t" \
  "s_not_b32 s47, s47\n\t" \
  "s_add_u32 s71, s53, s46\n\t" \
  "ds_read_b128 v[44:47], v70 offset:0\n\t" \
  "ds_read_b128 v[48:51], v70 offset:1024\n\t" \
  "ds_read_b128 v[60:63], v70 offset:2048\n\t" \
  "ds_read_b128 v[64:67], v70 offset:3072\n\t" \
  "v_mov_b64 v[36:37], 0\n\t" \
  "v_mov_b64 v[38:39], 0\n\t" \
  "v_mov_b64 v[40:41], 0\n\t" \
  "v_mov_b64 v[42:43], 0\n\t" \
  "v_lshrrev_b32 v71, 1, v69\n\t" \
  "v_add_u32 v71, 4, v71\n\t" \
  "v_lshrrev_b32 v72, 3, v69\n\t" \
  "v_and_b32 v72, 0x78, v72\n\t" \
  "v_and_b32 v74, 0x30, v69\n\t" \
  "v_lshl_add_u32 v72, v74, 3, v72\n\t" \
  "s_add_u32 s54, s53, s36\n\t" \
  "s_add_u32 s55, s54, 573440\n\t" \
  "s_add_u32 s54, s54, 135168\n\t" \
  "s_add_u32 s75, s54, 262144\n\t" \
  "s_add_u32 s80, s53, s26\n\t" \
  "s_or_b32 s87, s52, 0x1fff80\n\t" \
  "s_and_b32 s87, s87, -128\n\t" \
  "s_add_u32 s87, s87, 0x200000\n\t" \
  ".p2align 5\n\t" \
  ".fill 2, 4, 0xbf800000\n\t" \
  "s_waitcnt vmcnt(9)\n\t" \
  "s_barrier\n\t" \
  "ds_read_b128 v[0:3], v69 offset:0\n\t" \
  "ds_read_b128 v[4:7], v69 offset:1024\n\t" \
  "ds_read_b128 v[8:11], v69 offset:2048\n\t" \
  "ds_read_b128 v[12:15], v69 offset:3072\n\t" \
  "s_waitcnt lgkmcnt(3)\n\t" \
  "v_smfmac_f32_16x16x64_bf16 v[36:39], v[0:3], v[44:51], v68\n\t" \
  "ds_read_b128 v[44:47], v70 offset:4096\n\t" \
  "v_smfmac_f32_16x16x64_bf16 v[40:43], v[0:3], v[60:67], v68\n\t" \
  "ds_read_b128 v[48:51], v70 offset:5120\n\t" \
  "s_waitcnt lgkmcnt(4)\n\t" \
  "v_smfmac_f32_16x16x64_bf16 v[36:39], v[4:7], a[32:39], v68\n\t" \
  "ds_read_b128 v[60:63], v70 offset:6144\n\t" \
  "v_smfmac_f32_16x16x64_bf16 v[40:43], v[4:7], a[40:47], v68\n\t" \
  "ds_read_b128 v[64:67], v70 offset:7168\n\t" \
  "s_waitcnt lgkmcnt(5)\n\t" \
  "v_smfmac_f32_16x16x64_bf16 v[36:39], v[8:11], a[80:87], v68\n\t" \
  "s_barrier\n\t" \
  "ds_read_b128 v[16:19], v69 offset:4096\n\t" \
  "v_smfmac_f32_16x16x64_bf16 v[40:43], v[8:11], a[88:95], v68\n\t" \
  "ds_read_b128 v[20:23], v69 offset:5120\n\t" \
  "s_waitcnt lgkmcnt(6)\n\t" \
  "v_smfmac_f32_16x16x64_bf16 v[36:39], v[12:15], a[120:127], v68\n\t" \
  "ds_read_b128 v[24:27], v69 offset:6144\n\t" \
  "s_waitcnt lgkmcnt(5)\n\t" \
  "v_smfmac_f32_16x16x64_bf16 v[40:43], v[12:15], v[44:51], v68\n\t" \
  "ds_read_b128 v[28:31], v69 offset:7168\n\t" \
  "s_waitcnt lgkmcnt(3)\n\t" \
  "v_smfmac_f32_16x16x64_bf16 v[36:39], v[16:19], a[160:167], v68\n\t" \
  "ds_read_b128 v[32:35], v69 offset:8192\n\t" \
  "v_smfmac_f32_16x16x64_bf16 v[40:43], v[16:19], a[168:175], v68\n\t" \
  "s_waitcnt lgkmcnt(3)\n\t" \
  "v_mov_b64 v[44:45], 0\n\t" \
  "v_smfmac_f32_16x16x64_bf16 v[36:39], v[20:23], a[200:207], v68\n\t" \
  "s_waitcnt lgkmcnt(2)\n\t" \
  "v_mov_b64 v[46:47], 0\n\t" \
  "v_smfmac_f32_16x16x64_bf16 v[36:39], v[24:27], a[240:247], v68\n\t" \
  "s_waitcnt lgkmcnt(1)\n\t" \
  "v_mov_b64 v[48:49], 0\n\t" \
  "v_smfmac_f32_16x16x64_bf16 v[36:39], v[28:31], v[112:119], v68\n\t" \
  "s_waitcnt lgkmcnt(0)\n\t" \
  "v_mov_b64 v[50:51], 0\n\t" \
  "v_smfmac_f32_16x16x64_bf16 v[36:39], v[32:35], v[160:167], v68\n\t" \
  "v_smfmac_f32_16x16x64_bf16 v[40:43], v[20:23], v[60:67], v68\n\t" \
  "ds_read_b128 v[60:63], v70 offset:8192\n\t" \
  "v_smfmac_f32_16x16x64_bf16 v[40:43], v[24:27], a[248:255], v68\n\t" \
  "ds_read_b128 v[64:67], v70 offset:9216\n\t" \
  "v_add_f32 v74, v36, v38\n\t" \
  "v_smfmac_f32_16x16x64_bf16 v[40:43], v[28:31], v[120:127], v68\n\t" \
  "v_add_f32 v37, v37, v39\n\t" \
  "v_smfmac_f32_16x16x64_bf16 v[40:43], v[32:35], v[168:175], v68\n\t" \
  "v_cmp_u_f32 vcc, v74, v37\n\t" \
  "v_smfmac_f32_16x16x64_bf16 v[44:47], v[0:3], a[0:7], v68\n\t" \
  "s_cbranch_vccnz LFAIL3_%=\n\t" \
  "LGO3_%=:\n\t" \
  "v_cvt_pk_bf16_f32 v74, v74, v37\n\t" \
  "v_smfmac_f32_16x16x64_bf16 v[44:47], v[4:7], a[48:55], v68\n\t" \
  "s_waitcnt lgkmcnt(0)\n\t" \
  "v_add_f32 v75, v40, v42\n\t" \
  "v_smfmac_f32_16x16x64_bf16 v[44:47], v[8:11], v[60:67], v68\n\t" \
  "ds_read_b128 v[60:63], v70 offset:10240\n\t" \
  "v_add_f32 v41, v41, v43\n\t" \
  "v_cvt_pk_bf16_f32 v75, v75, v41\n\t" \
  "v_smfmac_f32_16x16x64_bf16 v[44:47], v[12:15], a[128:135], v68\n\t" \
  "ds_write2st64_b32 v71, v74, v75 offset0:40 offset1:42\n\t" \
  "v_smfmac_f32_16x16x64_bf16 v[44:47], v[16:19], a[176:183], v68\n\t" \
  "ds_read_b128 v[64:67], v70 offset:11264\n\t" \
  "s_memrealtime s[66:67]\n\t" \
  "v_smfmac_f32_16x16x64_bf16 v[44:47], v[20:23], a[208:215], v68\n\t" \
  "v_smfmac_f32_16x16x64_bf16 v[44:47], v[24:27], v[80:87], v68\n\t" \
  "v_smfmac_f32_16x16x64_bf16 v[44:47], v[28:31], v[128:135], v68\n\t" \
  "s_waitcnt lgkmcnt(0)\n\t" \
  "v_smfmac_f32_16x16x64_bf16 v[44:47], v[32:35], v[60:67], v68\n\t" \
  "s_barrier\n\t" \
  "ds_read_b64_tr_b16 v[76:77], v72 offset:10240\n\t" \
  "v_smfmac_f32_16x16x64_bf16 v[48:51], v[0:3], a[8:15], v68\n\t" \
  "s_lshl_b32 s60, s66, 8\n\t" \
  "s_sub_u32 s60, s47, s60\n\t" \
  "ds_read_b64_tr_b16 v[78:79], v72 offset:10752\n\t" \
  "v_mov_b32 v75, s60\n\t" \
  "v_smfmac_f32_16x16x64_bf16 v[48:51], v[4:7], a[56:63], v68\n\t" \
  "s_mov_b64 exec, s[24:25]\n\t" \
  "buffer_atomic_umin v75, v69, s[28:31], s71 offen sc1\n\t" \
  "s_mov_b64 exec, -1\n\t" \
  "v_add_f32 v74, v44, v46\n\t" \
  "v_smfmac_f32_16x16x64_bf16 v[48:51], v[8:11], a[96:103], v68\n\t" \
  "buffer_load_dwordx4 v[60:63], v69, s[28:31], s64 offen sc1\n\t" \
  "v_add_f32 v45, v45, v47\n\t" \
  "v_smfmac_f32_16x16x64_bf16 v[48:51], v[12:15], a[136:143], v68\n\t" \
  "s_waitcnt vmcnt(4)\n\t" \
  "s_waitcnt lgkmcnt(0)\n\t" \
  "v_cvt_pk_bf16_f32 v74, v74, v45\n\t" \
  "v_mfma_f32_16x16x32_bf16 v[36:39], v[52:55], v[76:79], 0\n\t" \
  "s_waitcnt vmcnt(3)\n\t" \
  "v_mfma_f32_16x16x32_bf16 v[40:43], v[56:59], v[76:79], 0\n\t" \
  "v_mov_b64 v[52:53], 0\n\t" \
  "v_mov_b64 v[54:55], 0\n\t" \
  "v_smfmac_f32_16x16x64_bf16 v[48:51], v[16:19], a[184:191], v68\n\t" \
  "buffer_load_dwordx4 v[64:67], v69, s[28:31], s64 offen offset:1024 sc1\n\t" \
  "v_mov_b64 v[56:57], 0\n\t" \
  "v_mov_b64 v[58:59], 0\n\t" \
  "v_smfmac_f32_16x16x64_bf16 v[48:51], v[20:23], a[216:223], v68\n\t" \
  "ds_write_b32 v71, v74 offset:1024\n\t" \
  "v_add_f32_dpp v36, v40, v36 quad_perm:[2,3,0,1] row_mask:0xf bank_mask:0xf\n\t" \
  "v_smfmac_f32_16x16x64_bf16 v[48:51], v[24:27], v[88:95], v68\n\t" \
  "v_add_f32_dpp v37, v41, v37 quad_perm:[2,3,0,1] row_mask:0xf bank_mask:0xf\n\t" \
  "v_smfmac_f32_16x16x64_bf16 v[48:51], v[28:31], v[136:143], v68\n\t" \
  "v_add_f32_dpp v38, v42, v38 quad_perm:[2,3,0,1] row_mask:0xf bank_mask:0xf\n\t" \
  "v_smfmac_f32_16x16x64_bf16 v[48:51], v[32:35], v[176:183], v68\n\t" \
  "v_add_f32_dpp v39, v43, v39 quad_perm:[2,3,0,1] row_mask:0xf bank_mask:0xf\n\t" \
  "v_smfmac_f32_16x16x64_bf16 v[52:55], v[0:3], a[16:23], v68\n\t" \
  "s_mov_b64 exec, s[58:59]\n\t" \
  "ds_write_b128 v69, v[36:39] offset:3104\n\t" \
  "s_mov_b64 exec, -1\n\t" \
  "v_smfmac_f32_16x16x64_bf16 v[52:55], v[4:7], a[64:71], v68\n\t" \
  "v_add_f32 v75, v48, v50\n\t" \
  "v_add_f32 v49, v49, v51\n\t" \
  "v_cvt_pk_bf16_f32 v75, v75, v49\n\t" \
  "v_smfmac_f32_16x16x64_bf16 v[56:59], v[0:3], a[24:31], v68\n\t" \
  "ds_write_b32 v71, v75 offset:1536\n\t" \
  "v_smfmac_f32_16x16x64_bf16 v[56:59], v[4:7], a[72:79], v68\n\t" \
  "ds_read_b64_tr_b16 v[0:1], v72 offset:9216\n\t" \
  "ds_read_b64_tr_b16 v[2:3], v72 offset:9728\n\t" \
  "v_smfmac_f32_16x16x64_bf16 v[52:55], v[8:11], a[104:111], v68\n\t" \
  "ds_read_b128 v[36:39], v70 offset:12288\n\t" \
  "v_smfmac_f32_16x16x64_bf16 v[56:59], v[8:11], a[112:119], v68\n\t" \
  "buffer_load_dword off, s[28:31], s87 sc1 lds\n\t" \
  "ds_read_b128 v[40:43], v70 offset:13312\n\t" \
  "s_waitcnt lgkmcnt(4)\n\t" \
  "s_barrier\n\t" \
  "ds_read_b64_tr_b16 v[44:45], v72 offset:1024\n\t" \
  "ds_read_b64_tr_b16 v[46:47], v72 offset:1536\n\t" \
  "v_smfmac_f32_16x16x64_bf16 v[52:55], v[12:15], a[144:151], v68\n\t" \
  "ds_read_b64_tr_b16 v[48:49], v72 offset:0\n\t" \
  "ds_read_b64_tr_b16 v[50:51], v72 offset:512\n\t" \
  "v_smfmac_f32_16x16x64_bf16 v[56:59], v[12:15], a[152:159], v68\n\t" \
  "v_smfmac_f32_16x16x64_bf16 v[52:55], v[16:19], a[192:199], v68\n\t" \
  "ds_read_b128 v[4:7], v69 offset:2048\n\t" \
  "s_waitcnt lgkmcnt(5)\n\t" \
  "v_smfmac_f32_16x16x64_bf16 v[56:59], v[16:19], v[36:43], v68\n\t" \
  "ds_read_b128 v[8:11], v69 offset:3072\n\t" \
  "s_waitcnt lgkmcnt(4)\n\t" \
  "v_mfma_f32_16x16x32_bf16 v[12:15], v[44:47], v[76:79], 0\n\t" \
  "ds_read_b128 v[36:39], v70 offset:14336\n\t" \
  "s_waitcnt lgkmcnt(3)\n\t" \
  "v_mfma_f32_16x16x32_bf16 v[12:15], v[48:51], v[0:3], v[12:15]\n\t" \
  "ds_read_b128 v[40:43], v70 offset:15360\n\t" \
  "v_smfmac_f32_16x16x64_bf16 v[52:55], v[20:23], a[224:231], v68\n\t" \
  "s_waitcnt lgkmcnt(2)\n\t" \
  "v_add_f32 v4, v4, v8\n\t" \
  "v_add_f32 v5, v5, v9\n\t" \
  "v_add_f32 v6, v6, v10\n\t" \
  "v_cndmask_b32 v78, v13, v14, s[56:57]\n\t" \
  "v_cndmask_b32 v76, v12, v15, s[56:57]\n\t" \
  "v_add_f32 v7, v7, v11\n\t" \
  "v_max3_f32 v77, v4, v5, v6\n\t" \
  "v_add_f32_dpp v76, v78, v76 quad_perm:[2,3,0,1] row_mask:0xf bank_mask:0xf\n\t" \
  "v_max3_f32 v77, v77, v7, v76\n\t" \
  "v_smfmac_f32_16x16x64_bf16 v[56:59], v[20:23], a[232:239], v68\n\t" \
  "ds_read_b128 v[44:47], v70 offset:16384\n\t" \
  "v_max_f32_dpp v77, v77, v77 quad_perm:[2,3,0,1] row_mask:0xf bank_mask:0xf\n\t" \
  "v_mov_b32 v78, v77\n\t" \
  "v_smfmac_f32_16x16x64_bf16 v[52:55], v[24:27], v[96:103], v68\n\t" \
  "ds_read_b128 v[48:51], v70 offset:17408\n\t" \
  "v_permlane16_swap_b32 v77, v78\n\t" \
  "v_max_f32 v77, v77, v78\n\t" \
  "v_mov_b32 v78, v77\n\t" \
  "v_smfmac_f32_16x16x64_bf16 v[52:55], v[28:31], v[144:151], v68\n\t" \
  "ds_read_b128 v[16:19], v70 offset:18432\n\t" \
  "v_permlane32_swap_b32 v77, v78\n\t" \
  "v_max_f32 v77, v77, v78\n\t" \
  "v_smfmac_f32_16x16x64_bf16 v[52:55], v[32:35], v[184:191], v68\n\t" \
  "ds_read_b128 v[20:23], v70 offset:19456\n\t" \
  "v_mul_f32 v77, 0xbe0293ee, v77\n\t" \
  "v_smfmac_f32_16x16x64_bf16 v[56:59], v[24:27], v[104:111], v68\n\t" \
  "v_fmamk_f32 v4, v4, 0x3e0293ee, v77\n\t" \
  "v_smfmac_f32_16x16x64_bf16 v[56:59], v[28:31], v[152:159], v68\n\t" \
  "ds_read_b128 v[24:27], v70 offset:22528\n\t" \
  "s_waitcnt lgkmcnt(4)\n\t" \
  "v_fmamk_f32 v5, v5, 0x3e0293ee, v77\n\t" \
  "v_smfmac_f32_16x16x64_bf16 v[56:59], v[32:35], v[36:43], v68\n\t" \
  "ds_read_b128 v[28:31], v70 offset:23552\n\t" \
  "v_fmamk_f32 v6, v6, 0x3e0293ee, v77\n\t" \
  "v_fmamk_f32 v7, v7, 0x3e0293ee, v77\n\t" \
  "v_fmamk_f32 v76, v76, 0x3e0293ee, v77\n\t" \
  "v_exp_f32 v4, v4\n\t" \
  "v_exp_f32 v5, v5\n\t" \
  "v_exp_f32 v6, v6\n\t" \
  "v_exp_f32 v7, v7\n\t" \
  "v_cvt_pk_bf16_f32 v4, v4, v5\n\t" \
  "v_exp_f32 v76, v76\n\t" \
  "v_cvt_pk_bf16_f32 v5, v6, v7\n\t" \
  "v_mov_b32_dpp v12, v4 quad_perm:[0,1,0,1] row_mask:0xf bank_mask:0xf\n\t" \
  "v_mov_b32_dpp v14, v4 quad_perm:[2,3,2,3] row_mask:0xf bank_mask:0xf\n\t" \
  "v_mov_b32_dpp v13, v5 quad_perm:[0,1,0,1] row_mask:0xf bank_mask:0xf\n\t" \
  "v_mov_b32_dpp v15, v5 quad_perm:[2,3,2,3] row_mask:0xf bank_mask:0xf\n\t" \
  "v_add_f32 v52, v52, v54\n\t" \
  "v_add_f32 v53, v53, v55\n\t" \
  "s_waitcnt vmcnt(2)\n\t" \
  "v_mfma_f32_16x16x32_bf16 v[0:3], v[60:63], v[12:15], 0\n\t" \
  "ds_read_b128 v[36:39], v70 offset:20480\n\t" \
  "v_add_f32 v56, v56, v58\n\t" \
  "v_add_f32 v57, v57, v59\n\t" \
  "v_cvt_pk_bf16_f32 v52, v52, v53\n\t" \
  "v_cvt_pk_bf16_f32 v53, v56, v57\n\t" \
  "s_waitcnt vmcnt(1)\n\t" \
  "v_mfma_f32_16x16x32_bf16 v[4:7], v[64:67], v[12:15], 0\n\t" \
  "ds_read_b128 v[40:43], v70 offset:21504\n\t" \
  "v_mov_b64 v[56:57], s[88:89]\n\t" \
  "v_mov_b64 v[58:59], s[88:89]\n\t" \
  "v_mov_b32_dpp v78, v76 quad_perm:[0,1,0,1] row_mask:0xf bank_mask:0xf\n\t" \
  "v_mov_b32_dpp v79, v76 quad_perm:[2,3,2,3] row_mask:0xf bank_mask:0xf\n\t" \
  "v_mfma_f32_16x16x32_bf16 v[32:35], v[56:59], v[12:15], 0\n\t" \
  "ds_read_b128 v[60:63], v70 offset:24576\n\t" \
  "v_cvt_pk_bf16_f32 v11, v78, v79\n\t" \
  "v_cvt_pk_bf16_f32 v8, v78, v79\n\t" \
  "v_mov_b64 v[54:55], 0\n\t" \
  "v_mov_b32 v9, 0\n\t" \
  "v_mov_b32 v10, 0\n\t" \
  "v_mov_b64 v[14:15], 0\n\t" \
  "v_mov_b64 v[12:13], 0\n\t" \
  "v_mfma_f32_16x16x32_bf16 v[32:35], v[54:57], v[8:11], v[32:35]\n\t" \
  "s_cmp_eq_u32 s48, 63\n\t" \
  "v_mfma_f32_16x16x32_bf16 v[0:3], v[52:55], v[8:11], v[0:3]\n\t" \
  "buffer_store_dwordx2 v[52:53], v71, s[28:31], s69 offen offset:3580 sc1\n\t" \
  "ds_read_b128 v[64:67], v70 offset:25600\n\t" \
  "v_mov_b64 v[56:57], 0\n\t" \
  "v_mov_b64 v[58:59], 0\n\t" \
  "v_mfma_f32_16x16x32_bf16 v[4:7], v[52:55], v[10:13], v[4:7]\n\t" \
  "s_cbranch_scc1 LLAST3_%=\n\t" \
  "v_rcp_f32 v74, v32\n\t" \
  "v_mov_b32 v11, 0\n\t" \
  "v_mov_b32 v8, 0\n\t" \
  "v_pk_mul_f32 v[0:1], v[0:1], v[74:75] op_sel_hi:[1,0]\n\t" \
  "v_pk_mul_f32 v[2:3], v[2:3], v[74:75] op_sel_hi:[1,0]\n\t" \
  "v_cvt_pk_bf16_f32 v76, v0, v1\n\t" \
  "v_cvt_pk_bf16_f32 v77, v2, v3\n\t" \
  "v_pk_mul_f32 v[4:5], v[4:5], v[74:75] op_sel_hi:[1,0]\n\t" \
  "v_pk_mul_f32 v[6:7], v[6:7], v[74:75] op_sel_hi:[1,0]\n\t" \
  "v_cvt_pk_bf16_f32 v78, v4, v5\n\t" \
  "v_cvt_pk_bf16_f32 v79, v6, v7\n\t" \
  "ds_write_b128 v69, v[76:79] offset:7168\n\t" \
  "s_waitcnt lgkmcnt(2)\n\t" \
  "v_smfmac_f32_16x16x64_bf16 v[12:15], v[76:79], v[44:51], v68\n\t" \
  "ds_read_b128 v[44:47], v70 offset:26624\n\t" \
  "v_smfmac_f32_16x16x64_bf16 v[56:59], v[76:79], v[16:23], v68\n\t" \
  "ds_read_b128 v[48:51], v70 offset:27648\n\t" \
  "v_mov_b64 v[52:53], 0\n\t" \
  "v_smfmac_f32_16x16x64_bf16 v[8:11], v[76:79], v[36:43], v68\n\t" \
  "ds_read_b128 v[16:19], v70 offset:28672\n\t" \
  "v_smfmac_f32_16x16x64_bf16 v[52:55], v[76:79], v[24:31], v68\n\t" \
  "s_waitcnt lgkmcnt(3)\n\t" \
  "s_barrier\n\t" \
  "ds_read_b128 v[0:3], v69 offset:6144\n\t" \
  "ds_read_b128 v[4:7], v69 offset:5120\n\t" \
  "v_lshrrev_b32 v71, 7, v69\n\t" \
  "v_and_b32 v74, 0x70, v69\n\t" \
  "v_lshl_add_u32 v71, v71, 14, v74\n\t" \
  "v_bfe_u32 v72, v69, 2, 7\n\t" \
  "v_and_b32 v74, 0x200, v69\n\t" \
  "s_waitcnt lgkmcnt(1)\n\t" \
  "v_smfmac_f32_16x16x64_bf16 v[8:11], v[0:3], v[60:67], v68\n\t" \
  "ds_read_b128 v[20:23], v70 offset:29696\n\t" \
  "v_mov_b64 v[76:77], 0\n\t" \
  "v_smfmac_f32_16x16x64_bf16 v[12:15], v[0:3], v[200:207], v68\n\t" \
  "ds_read_b128 v[32:35], v69 offset:4096\n\t" \
  "v_mov_b64 v[78:79], 0\n\t" \
  "v_smfmac_f32_16x16x64_bf16 v[52:55], v[0:3], v[44:51], v68\n\t" \
  "buffer_load_dword v75, off, s[28:31], s80 sc1\n\t" \
  "ds_read_b128 v[24:27], v70 offset:30720\n\t" \
  "v_lshl_add_u32 v72, v74, 3, v72\n\t" \
  "v_smfmac_f32_16x16x64_bf16 v[56:59], v[0:3], v[232:239], v68\n\t" \
  "ds_read_b128 v[28:31], v70 offset:31744\n\t" \
  "s_waitcnt lgkmcnt(4)\n\t" \
  "v_smfmac_f32_16x16x64_bf16 v[12:15], v[4:7], v[208:215], v68\n\t" \
  "ds_read_b128 v[60:63], v70 offset:32768\n\t" \
  "s_waitcnt lgkmcnt(4)\n\t" \
  "v_smfmac_f32_16x16x64_bf16 v[8:11], v[4:7], v[16:23], v68\n\t" \
  "ds_read_b128 v[64:67], v70 offset:33792\n\t" \
  "s_waitcnt lgkmcnt(4)\n\t" \
  "v_smfmac_f32_16x16x64_bf16 v[8:11], v[32:35], v[192:199], v68\n\t" \
  "ds_read_b128 v[36:39], v73 offset:4096\n\t" \
  "v_smfmac_f32_16x16x64_bf16 v[12:15], v[32:35], v[216:223], v68\n\t" \
  "ds_read_b128 v[40:43], v73 offset:6144\n\t" \
  "s_waitcnt lgkmcnt(4)\n\t" \
  "v_smfmac_f32_16x16x64_bf16 v[52:55], v[4:7], v[24:31], v68\n\t" \
  "ds_read_b128 v[44:47], v70 offset:34816\n\t" \
  "v_cvt_pk_bf16_f32 v8, v8, v9\n\t" \
  "v_smfmac_f32_16x16x64_bf16 v[52:55], v[32:35], v[224:231], v68\n\t" \
  "ds_read_b128 v[48:51], v70 offset:35840\n\t" \
  "v_cvt_pk_bf16_f32 v9, v10, v11\n\t" \
  "v_smfmac_f32_16x16x64_bf16 v[56:59], v[4:7], v[240:247], v68\n\t" \
  "s_waitcnt lgkmcnt(4)\n\t" \
  "v_cvt_pk_bf16_f32 v10, v12, v13\n\t" \
  "v_smfmac_f32_16x16x64_bf16 v[56:59], v[32:35], v[60:67], v68\n\t" \
  "s_waitcnt lgkmcnt(3)\n\t" \
  "v_cvt_pk_bf16_f32 v11, v14, v15\n\t" \
  "v_smfmac_f32_16x16x64_bf16 v[76:79], v[36:39], v[248:255], v68\n\t" \
  "buffer_store_dwordx4 v[8:11], v71, s[28:31], s54 offen sc1\n\t" \
  "s_waitcnt lgkmcnt(0)\n\t" \
  "v_cvt_pk_bf16_f32 v52, v52, v53\n\t" \
  "v_smfmac_f32_16x16x64_bf16 v[76:79], v[40:43], v[44:51], v68\n\t" \
  "v_cvt_pk_bf16_f32 v53, v54, v55\n\t" \
  "v_cvt_pk_bf16_f32 v54, v56, v57\n\t" \
  "v_cvt_pk_bf16_f32 v55, v58, v59\n\t" \
  "buffer_store_dwordx4 v[52:55], v71, s[28:31], s75 offen sc1\n\t" \
  "s_nop 2\n\t" \
  "v_add_f32 v76, v76, v78\n\t" \
  "v_add_f32 v77, v77, v79\n\t" \
  "v_cvt_pk_bf16_f32 v76, v76, v77\n\t" \
  "buffer_store_dword v76, v72, s[28:31], s55 offen sc1\n\t" \
  "s_memrealtime s[70:71]\n\t" \
  "s_waitcnt vmcnt(3)\n\t" \
  "v_readfirstlane_b32 s65, v75\n\t" \
  "s_barrier\n\t" \
  "s_waitcnt lgkmcnt(0)\n\t" \
  "s_not_b32 s65, s65\n\t" \
  "s_lshl_b32 s61, s70, 8\n\t" \
  "s_andn2_b32 s64, s65, 0xff\n\t" \
  "s_sub_u32 s64, s64, s61\n\t" \
  "s_ashr_i32 s64, s64, 8\n\t" \
  "s_cmp_eq_u32 s65, 0\n\t" \
  "s_cselect_b32 s64, " CH_DLF ", s64\n\t" \
  "s_add_i32 s64, s64, " CH_RO3 "\n\t" \
  "s_min_i32 s64, s64, s34\n\t" \
  "s_max_i32 s64, s64, " CH_DLL1 "\n\t" \
  "s_add_u32 s64, s64, s70\n\t" \
  "s_mov_b32 s74, 0\n\t" \
  "v_and_b32 v37, 0x30, v69\n\t" \
  "v_add_u32 v37, s52, v37\n\t" \
  "s_mov_b32 exec_lo, 0\n\t" \
  "v_mov_b32 v37, s85\n\t" \
  "s_mov_b32 exec_lo, -1\n\t" \
  "s_cmp_lt_i32 s41, 0\n\t" \
  "s_cbranch_scc1 LIDLE_%=\n\t" \
  "s_movk_i32 s60, 8192\n\t" \
  "RWT3_%=:\n\t" \
  "s_memrealtime s[72:73]\n\t" \
  "s_sub_u32 s60, s60, 1\n\t" \
  "s_cselect_b32 s61, 0, -1\n\t" \
  "s_waitcnt lgkmcnt(0)\n\t" \
  "s_sub_i32 s73, s72, s64\n\t" \
  "s_and_b32 s73, s73, s61\n\t" \
  "s_cmp_lt_i32 s73, " CH_FIN "\n\t" \
  "s_cbranch_scc1 RWT3_%=\n\t" \
  "s_cmp_lt_i32 s73, 0\n\t" \
  "s_cbranch_scc1 RWT3_%=f\n\t" \
  "RWT3_%=g:\n\t" \
  "RRETRY3_%=:\n\t" \
  "buffer_load_dwordx4 v[0:3], v69, s[28:31], s97 offen offset:0 sc1\n\t" \
  "buffer_load_dwordx4 v[4:7], v69, s[28:31], s97 offen offset:1024 sc1\n\t" \
  "buffer_load_dwordx4 v[8:11], v69, s[28:31], s97 offen offset:2048 sc1\n\t" \
  "buffer_load_dwordx4 v[12:15], v69, s[28:31], s97 offen offset:3072 sc1\n\t" \
  "v_lshrrev_b32 v20, 2, v69\n\t" \
  "v_and_b32 v20, 16, v20\n\t" \
  "v_mov_b32 v21, 0x3f80\n\t" \
  "v_lshlrev_b32 v21, v20, v21\n\t" \
  "v_bfe_u32 v20, v69, 4, 2\n\t" \
  "v_cmp_eq_u32 vcc, 0, v20\n\t" \
  "s_nop 1\n\t" \
  "v_cndmask_b32 v32, 0, v21, vcc\n\t" \
  "v_cmp_eq_u32 vcc, 1, v20\n\t" \
  "s_nop 1\n\t" \
  "v_cndmask_b32 v33, 0, v21, vcc\n\t" \
  "v_cmp_eq_u32 vcc, 2, v20\n\t" \
  "s_nop 1\n\t" \
  "v_cndmask_b32 v34, 0, v21, vcc\n\t" \
  "v_cmp_eq_u32 vcc, 3, v20\n\t" \
  "s_nop 1\n\t" \
  "v_cndmask_b32 v35, 0, v21, vcc\n\t" \
  "v_bfe_u32 v38, v69, 7, 1\n\t" \
  "v_bfe_u32 v20, v69, 9, 1\n\t" \
  "v_lshl_add_u32 v38, v20, 2, v38\n\t" \
  "v_lshlrev_b32 v38, 12, v38\n\t" \
  "v_and_b32 v20, 0x60, v69\n\t" \
  "v_add_u32 v38, v38, v20\n\t" \
  "v_bfe_u32 v20, v69, 8, 1\n\t" \
  "v_lshl_add_u32 v38, v20, 4, v38\n\t" \
  "v_bfe_u32 v20, v69, 4, 1\n\t" \
  "v_lshl_add_u32 v38, v20, 3, v38\n\t" \
  "v_mov_b32 v44, 0\n\t" \
  "buffer_load_dword v39, v37, s[28:31], 0 offen sc1\n\t" \
  CH_ALD \
  "s_waitcnt vmcnt(4)\n\t" \
  "v_mfma_f32_16x16x32_bf16 v[40:43], v[32:35], v[0:3], 0\n\t" \
  "v_max3_u32 v44, v44, v0, v1\n\t" \
  "v_max3_u32 v44, v44, v2, v3\n\t" \
  "s_waitcnt vmcnt(3)\n\t" \
  "v_mfma_f32_16x16x32_bf16 v[40:43], v[32:35], v[4:7], v[40:43]\n\t" \
  "v_max3_u32 v44, v44, v4, v5\n\t" \
  "v_max3_u32 v44, v44, v6, v7\n\t" \
  "s_waitcnt vmcnt(2)\n\t" \
  "v_mfma_f32_16x16x32_bf16 v[40:43], v[32:35], v[8:11], v[40:43]\n\t" \
  "v_max3_u32 v44, v44, v8, v9\n\t" \
  "v_max3_u32 v44, v44, v10, v11\n\t" \
  "s_waitcnt vmcnt(1)\n\t" \
  "v_mfma_f32_16x16x32_bf16 v[40:43], v[32:35], v[12:15], v[40:43]\n\t" \
  "v_max3_u32 v44, v44, v12, v13\n\t" \
  "v_max3_u32 v44, v44, v14, v15\n\t" \
  "v_cmp_eq_u32 vcc, -1, v44\n\t" \
  "s_cbranch_vccnz RFAIL3_%=\n\t" \
  "s_nop 3\n\t" \
  "v_add_f32_dpp v40, v40, v40 row_ror:8 row_mask:0xf bank_mask:0xf\n\t" \
  "v_add_f32_dpp v41, v41, v41 row_ror:8 row_mask:0xf bank_mask:0xf\n\t" \
  "v_add_f32_dpp v42, v42, v42 row_ror:8 row_mask:0xf bank_mask:0xf\n\t" \
  "v_add_f32_dpp v43, v43, v43 row_ror:8 row_mask:0xf bank_mask:0xf\n\t" \
  "v_cvt_pk_bf16_f32 v40, v40, v41\n\t" \
  "v_cvt_pk_bf16_f32 v41, v42, v43\n\t" \
  "buffer_store_dwordx2 v[40:41], v38, s[28:31], s98 offen sc1\n\t" \
  "RDONE3_%=:\n\t" \
  "s_barrier\n\t" \
  "s_waitcnt vmcnt(1)\n\t" \
  "v_cmp_eq_u32 vcc, 0, v39\n\t" \
  "s_sub_u32 s60, s70, s66\n\t" \
  "s_add_u32 s81, s81, 1\n\t" \
  "s_min_u32 s81, s81, s60\n\t" \
  "s_add_u32 s61, s81, 8\n\t" \
  "s_min_u32 s60, s60, s61\n\t" \
  "s_lshl_b32 s60, s60, 2\n\t" \
  "s_lshr_b32 s61, s20, 3\n\t" \
  "s_sub_u32 s20, s20, s61\n\t" \
  "s_add_u32 s20, s20, s60\n\t" \
  "s_cmp_eq_u32 s68, 0\n\t" \
  "s_cselect_b32 s60, 0, " CH_FU "\n\t" \
  "s_add_i32 s22, s22, s60\n\t" \
  "s_sub_i32 s22, s22, " CH_FD "\n\t" \
  "s_max_i32 s22, s22, 0\n\t" \
  "s_min_i32 s22, s22, 12288\n\t" \
  "s_add_u32 s48, s48, 1\n\t" \
  "s_mov_b32 s49, s53\n\t" \
  "s_add_u32 s53, s53, " CH_SLOT "\n\t" \
  "s_add_u32 s60, s53, " CH_SKIP "\n\t" \
  "s_lshr_b32 s61, s96, 1\n\t" \
  "s_add_u32 s86, s86, 1\n\t" \
  "s_cmp_eq_u32 s86, 3\n\t" \
  "s_cselect_b32 s53, s60, s53\n\t" \
  "s_cselect_b32 s96, s61, s96\n\t" \
  "s_cselect_b32 s86, 0, s86\n\t" \
  "s_cmp_ge_u32 s53, " CH_RING "\n\t" \
  "s_cselect_b32 s53, 0, s53\n\t" \
  "s_cselect_b32 s96, s21, s96\n\t" \
  "s_andn2_b32 s53, s53, 0x2000\n\t" \
  "s_and_b32 s60, s96, 1\n\t" \
  "s_lshl_b32 s60, s60, 13\n\t" \
  "s_or_b32 s53, s53, s60\n\t" \
  "s_mov_b32 s68, 0\n\t" \
  "s_add_u32 s97, s41, s53\n\t" \
  "s_add_u32 s98, s37, s53\n\t" \
  "s_add_u32 s82, s48, 0\n\t" \
  "s_and_b32 s82, s82, " CH_TRM "\n\t" \
  "s_lshr_b64 s[60:61], s[90:91], s82\n\t" \
  "s_and_b32 s60, s60, 1\n\t" \
  "s_lshl_b32 s60, s60, 13\n\t" \
  "s_add_u32 s82, s82, " CH_SG0 "\n\t" \
  "s_lshl_b32 s82, s82, 21\n\t" \
  "s_or_b32 s82, s82, s60\n\t" \
  "s_add_u32 s50, s82, s38\n\t" \
  "s_add_u32 s69, s82, s39\n\t" \
  "s_add_u32 s82, s48, 1\n\t" \
  "s_lshr_b64 s[60:61], s[90:91], s82\n\t" \
  "s_and_b32 s60, s60, 1\n\t" \
  "s_lshl_b32 s60, s60, 13\n\t" \
  "s_add_u32 s82, s82, " CH_SG0 "\n\t" \
  "s_lshl_b32 s82, s82, 21\n\t" \
  "s_or_b32 s82, s82, s60\n\t" \
  "s_add_u32 s52, s82, s43\n\t" \
  "s_add_u32 s82, s48, " CH_ARMD "\n\t" \
  "s_lshr_b64 s[60:61], s[90:91], s82\n\t" \
  "s_and_b32 s60, s60, 1\n\t" \
  "s_lshl_b32 s60, s60, 13\n\t" \
  "s_add_u32 s82, s82, " CH_SG0 "\n\t" \
  "s_lshl_b32 s82, s82, 21\n\t" \
  "s_or_b32 s82, s82, s60\n\t" \
  "s_add_u32 s85, s82, s45\n\t" \
  "s_cmp_lt_i32 s48, " CH_ARMS "\n\t" \
  "s_cselect_b32 s85, s27, s85\n\t" \
  "s_cmp_eq_u32 s34, 0\n\t" \
  "s_cbranch_scc1 LINI_%=\n\t" \
  "LTK_%=:\n\t" \
  CH_TOKCHK \
  "s_mov_b32 s61, 0\n\t" \
  "LTW_%=:\n\t" \
  "buffer_load_dword v1, v37, s[28:31], 0 offen sc1\n\t" \
  "s_waitcnt vmcnt(0)\n\t" \
  "v_cmp_eq_u32 vcc, 0, v1\n\t" \
  "s_cbranch_vccz LG3_%=\n\t" \
  "s_add_u32 s61, s61, 1\n\t" \
  "s_sleep 2\n\t" \
  "s_cmp_gt_u32 s61, s84\n\t" \
  "s_cbranch_scc1 LTG_%=\n\t" \
  "s_and_b32 s60, s61, 1023\n\t" \
  "s_cmp_lg_u32 s60, 0\n\t" \
  "s_cbranch_scc1 LTW_%=\n\t" \
  "buffer_load_dword v46, off, s[28:31], s92 sc1\n\t" \
  "s_waitcnt vmcnt(0)\n\t" \
  "v_readfirstlane_b32 s60, v46\n\t" \
  "s_nop 0\n\t" \
  "s_cmp_eq_u32 s60, 0\n\t" \
  "s_cbranch_scc1 LTW_%=\n\t" \
  "LTG_%=:\n\t" \
  "s_mov_b32 s84, 0\n\t" \
  "s_lshl_b32 s60, s36, 4\n\t" \
  "s_or_b32 s60, s60, s48\n\t" \
  "s_or_b32 s60, s60, 268435456\n\t" \
  "v_mov_b32 v76, s60\n\t" \
  "v_mov_b32 v77, 0\n\t" \
  "s_mov_b64 exec, 1\n\t" \
  "buffer_atomic_cmpswap v[76:77], off, s[28:31], s92 sc1\n\t" \
  "s_mov_b64 exec, -1\n\t" \
  "s_branch LG3_%=\n\t" \
  "LIDLE_%=:\n\t" \
  "s_add_u32 s64, s64, 8\n\t" \
  "s_movk_i32 s60, 8192\n\t" \
  "IWT_%=:\n\t" \
  "s_memrealtime s[72:73]\n\t" \
  "s_sub_u32 s60, s60, 1\n\t" \
  "s_cselect_b32 s61, 0, -1\n\t" \
  "s_waitcnt lgkmcnt(0)\n\t" \
  "s_sub_i32 s73, s72, s64\n\t" \
  "s_and_b32 s73, s73, s61\n\t" \
  "s_cmp_lt_i32 s73, 0\n\t" \
  "s_cbranch_scc1 IWT_%=\n\t" \
  "buffer_load_dword v39, v37, s[28:31], 0 offen sc1\n\t" \
  "buffer_store_dword v37, off, s[28:31], s41\n\t" \
  "s_branch RDONE3_%=\n\t" \
  "RFAIL3_%=:\n\t" \
  "s_add_u32 s74, s74, 1\n\t" \
  "s_sleep " CH_RSL "\n\t" \
  "s_waitcnt vmcnt(0)\n\t" \
  "s_cmp_gt_u32 s74, s84\n\t" \
  "s_cbranch_scc1 RG3_%=\n\t" \
  "s_and_b32 s60, s74, 1023\n\t" \
  "s_cmp_lg_u32 s60, 0\n\t" \
  "s_cbranch_scc1 RRETRY3_%=\n\t" \
  "buffer_load_dword v46, off, s[28:31], s92 sc1\n\t" \
  "s_waitcnt vmcnt(0)\n\t" \
  "v_readfirstlane_b32 s60, v46\n\t" \
  "s_nop 0\n\t" \
  "s_cmp_eq_u32 s60, 0\n\t" \
  "s_cbranch_scc1 RRETRY3_%=\n\t" \
  "RG3_%=:\n\t" \
  "s_mov_b32 s84, 0\n\t" \
  "s_lshl_b32 s60, s36, 4\n\t" \
  "s_or_b32 s60, s60, s48\n\t" \
  "s_or_b32 s60, s60, 536870912\n\t" \
  "v_mov_b32 v76, s60\n\t" \
  "v_mov_b32 v77, 0\n\t" \
  "s_mov_b64 exec, 1\n\t" \
  "buffer_atomic_cmpswap v[76:77], off, s[28:31], s92 sc1\n\t" \
  "s_mov_b64 exec, -1\n\t" \
  "s_branch RDONE3_%=\n\t" \
  "LFAIL3_%=:\n\t" \
  "v_max_u32 v75, v0, v1\n\t" \
  "v_max3_u32 v75, v75, v2, v3\n\t" \
  "v_max3_u32 v75, v75, v4, v5\n\t" \
  "v_max3_u32 v75, v75, v6, v7\n\t" \
  "v_max3_u32 v75, v75, v8, v9\n\t" \
  "v_max3_u32 v75, v75, v10, v11\n\t" \
  "v_max3_u32 v75, v75, v12, v13\n\t" \
  "v_max3_u32 v75, v75, v14, v15\n\t" \
  "v_max3_u32 v75, v75, v16, v17\n\t" \
  "v_max3_u32 v75, v75, v18, v19\n\t" \
  "v_max3_u32 v75, v75, v20, v21\n\t" \
  "v_max3_u32 v75, v75, v22, v23\n\t" \
  "v_max3_u32 v75, v75, v24, v25\n\t" \
  "v_max3_u32 v75, v75, v26, v27\n\t" \
  "v_max3_u32 v75, v75, v28, v29\n\t" \
  "v_max3_u32 v75, v75, v30, v31\n\t" \
  "v_max3_u32 v75, v75, v32, v33\n\t" \
  "v_max3_u32 v75, v75, v34, v35\n\t" \
  "v_cmp_eq_u32 vcc, -1, v75\n\t" \
  "s_cbranch_vccz LGO3_%=\n\t" \
  "s_add_u32 s68, s68, 1\n\t" \
  "s_waitcnt lgkmcnt(0)\n\t" \
  "s_cmp_gt_u32 s68, s83\n\t" \
  "s_cbranch_scc1 LGV3_%=\n\t" \
  "s_barrier\n\t" \
  "s_waitcnt vmcnt(8)\n\t" \
  "s_branch LG3_%=\n\t" \
  "RWT3_%=f:\n\t" \
  "s_cmp_lt_i32 s73, -2\n\t" \
  "s_cbranch_scc0 RWT3_%=g\n\t" \
  "s_nop 4\n\t" \
  "s_branch RWT3_%=g\n\t" \
  "LGV3_%=:\n\t" \
  "s_mov_b32 s83, 0\n\t" \
  "s_lshl_b32 s60, s36, 4\n\t" \
  "s_or_b32 s60, s60, s48\n\t" \
  "s_or_b32 s60, s60, 805306368\n\t" \
  "v_mov_b32 v76, s60\n\t" \
  "v_mov_b32 v77, 0\n\t" \
  "s_mov_b64 exec, 1\n\t" \
  "buffer_atomic_cmpswap v[76:77], off, s[28:31], s92 sc1\n\t" \
  "s_mov_b64 exec, -1\n\t" \
  "s_branch LGO3_%=\n\t" \
  "LLAST3_%=:\n\t" \
  "s_waitcnt vmcnt(0) lgkmcnt(0)\n\t" \
  "s_barrier\n\t" \
  "LEND_%=:\n\t" \
  ""
typedef unsigned v16u __attribute__((ext_vector_type(16)));
#define CH_WCLOB "a0", "a1", "a2", "a3", "a4", "a5", "a6", "a7", "a8", "a9", "a10", "a11", "a12", "a13", "a14", "a15", "a16", "a17", "a18", "a19", "a20", "a21", "a22", "a23", "a24", "a25", "a26", "a27", "a28", "a29", "a30", "a31", "a32", "a33", "a34", "a35", "a36", "a37", "a38", "a39", "a40", "a41", "a42", "a43", "a44", "a45", "a46", "a47", "a48", "a49", "a50", "a51", "a52", "a53", "a54", "a55", "a56", "a57", "a58", "a59", "a60", "a61", "a62", "a63", "a64", "a65", "a66", "a67", "a68", "a69", "a70", "a71", "a72", "a73", "a74", "a75", "a76", "a77", "a78", "a79", "a80", "a81", "a82", "a83", "a84", "a85", "a86", "a87", "a88", "a89", "a90", "a91", "a92", "a93", "a94", "a95", "a96", "a97", "a98", "a99", "a100", "a101", "a102", "a103", "a104", "a105", "a106", "a107", "a108", "a109", "a110", "a111", "a112", "a113", "a114", "a115", "a116", "a117", "a118", "a119", "a120", "a121", "a122", "a123", "a124", "a125", "a126", "a127", "a128", "a129", "a130", "a131", "a132", "a133", "a134", "a135", "a136", "a137", "a138", "a139", "a140", "a141", "a142", "a143", "a144", "a145", "a146", "a147", "a148", "a149", "a150", "a151", "a152", "a153", "a154", "a155", "a156", "a157", "a158", "a159", "a160", "a161", "a162", "a163", "a164", "a165", "a166", "a167", "a168", "a169", "a170", "a171", "a172", "a173", "a174", "a175", "a176", "a177", "a178", "a179", "a180", "a181", "a182", "a183", "a184", "a185", "a186", "a187", "a188", "a189", "a190", "a191", "a192", "a193", "a194", "a195", "a196", "a197", "a198", "a199", "a200", "a201", "a202", "a203", "a204", "a205", "a206", "a207", "a208", "a209", "a210", "a211", "a212", "a213", "a214", "a215", "a216", "a217", "a218", "a219", "a220", "a221", "a222", "a223", "a224", "a225", "a226", "a227", "a228", "a229", "a230", "a231", "a232", "a233", "a234", "a235", "a236", "a237", "a238", "a239", "a240", "a241", "a242", "a243", "a244", "a245", "a246", "a247", "a248", "a249", "a250", "a251", "a252", "a253", "a254", "a255", "v80", "v81", "v82", "v83", "v84", "v85", "v86", "v87", "v88", "v89", "v90", "v91", "v92", "v93", "v94", "v95", "v96", "v97", "v98", "v99", "v100", "v101", "v102", "v103", "v104", "v105", "v106", "v107", "v108", "v109", "v110", "v111", "v112", "v113", "v114", "v115", "v116", "v117", "v118", "v119", "v120", "v121", "v122", "v123", "v124", "v125", "v126", "v127", "v128", "v129", "v130", "v131", "v132", "v133", "v134", "v135", "v136", "v137", "v138", "v139", "v140", "v141", "v142", "v143", "v144", "v145", "v146", "v147", "v148", "v149", "v150", "v151", "v152", "v153", "v154", "v155", "v156", "v157", "v158", "v159", "v160", "v161", "v162", "v163", "v164", "v165", "v166", "v167", "v168", "v169", "v170", "v171", "v172", "v173", "v174", "v175", "v176", "v177", "v178", "v179", "v180", "v181", "v182", "v183", "v184", "v185", "v186", "v187", "v188", "v189", "v190", "v191", "v192", "v193", "v194", "v195", "v196", "v197", "v198", "v199", "v200", "v201", "v202", "v203", "v204", "v205", "v206", "v207", "v208", "v209", "v210", "v211", "v212", "v213", "v214", "v215", "v216", "v217", "v218", "v219", "v220", "v221", "v222", "v223", "v224", "v225", "v226", "v227", "v228", "v229", "v230", "v231", "v232", "v233", "v234", "v235", "v236", "v237", "v238", "v239", "v240", "v241", "v242", "v243", "v244", "v245", "v246", "v247", "v248", "v249", "v250", "v251", "v252", "v253", "v254", "v255"
#define CH_H(b) (8 * ((b) & 3) + ((b) >> 3))
#define CH_L(b) (((b) >> 2) & 1)
__device__ __forceinline__ void start_work(char* smem, int wk);
__device__ __forceinline__ void chain_wg(char* smem) {
  if (CH_L((int)blockIdx.x)) start_work(smem, CH_H((int)blockIdx.x));
  const MP p = getp();
  const int b = (int)blockIdx.x, h = CH_H(b), L = CH_L(b);
  int w = __builtin_amdgcn_readfirstlane(threadIdx.x >> 6);
  {
    unsigned id; asm volatile("s_getreg_b32 %0, hwreg(HW_REG_HW_ID, 4, 2)" : "=s"(id));
    volatile __attribute__((address_space(3))) unsigned* lw = (volatile __attribute__((address_space(3))) unsigned*)smem;
    lw[w] = 1u << id;
    BAR();
    if (__builtin_amdgcn_readfirstlane(lw[0] | lw[1] | lw[2] | lw[3]) == 15u) w = (int)((SMAP >> (4u * id)) & 3u);
  }
  unsigned lp16 = (threadIdx.x & 63u) * 16u;
  rsrc_t X = mkr(p.xb, XB_BYTES);
  if ((unsigned)(size_t)(__attribute__((address_space(3))) char*)smem != 0u) return;
  const unsigned wsz = DM * HD * 2u - (unsigned)(h * DH) * 2u;
  const rsrc_t RQ = mkr(p.Wq + h * DH, wsz), RK = mkr(p.Wk + h * DH, wsz), RV = mkr(p.Wv + h * DH, wsz), RO = mkr(p.Wo + (long)h * DH * DM, DH * DM * 2u);
  unsigned xcc; asm volatile("s_getreg_b32 %0, hwreg(HW_REG_XCC_ID)" : "=s"(xcc)); xcc &= 3u;
  unsigned d96 = 0u;
  const unsigned c43 = P_FA * PAIR + (unsigned)h * 128u + 32u;
  const unsigned h128 = (unsigned)h * 128u, uw = (unsigned)w, t = uw >> 1, T = uw & 1u, NONE = 0xF0000000u;
  const unsigned u0 = w == 3 ? (unsigned)h >> 2 : (unsigned)((h & 1) + (h >> 3) * 2 + ((h >> 1) & 3) * 8 + (w == 2 ? 32 : 0));
  const bool idle = w == 3 && (h & 3);
  const unsigned c37 = idle ? NONE : (w == 3 ? AGT + u0 * 128u : AGO + (u0 >> 5) * 32768u + (u0 & 31u) * 128u);
  const unsigned c41 = idle ? NONE : (w == 3 ? 524288u + (u0 >> 1) * 16384u + (u0 & 1u) * 4096u : (u0 & 7u) * 16384u + ((u0 >> 3) & 1u) * 4096u + (u0 >> 4) * 131072u);
  const unsigned c42 = AGO + (xcc & 1u) * 16384u + (xcc >> 1) * 4096u;
  const unsigned c46 = (w == 3 ? (h & 7) == 5 : h == 30) ? AGT + 1024u : NONE;
  const unsigned c38 = (unsigned)(3 * h + 1) * PAIR + T * 4096u + t * 2048u, c39 = (P_KV + (unsigned)(h >> 1)) * PAIR + (unsigned)(h & 1) * 4096u;
  const unsigned c45 = P_FB * PAIR + 4096u + (unsigned)(16 + (h & 15)) * 128u;
  unsigned d48 = 0u, d49 = 0u, d53 = SLOT, d86 = 1u, d65 = rtc();
  unsigned mn = 120u;
  v4u stt;
  asm volatile("s_waitcnt vmcnt(0) lgkmcnt(0)\n\t" CH_ASM "LCHEND:"
               : "+{s48}"(d48), "+{s49}"(d49), "+{s53}"(d53), "+{s65}"(d65), "+{s81}"(mn), "+{s86}"(d86), "+{s96}"(d96), "=&{s[76:79]}"(stt), "+{v69}"(lp16), "+{s[28:31]}"(X)
               : [Wq] "s"(RQ), [Wk] "s"(RK), [Wv] "s"(RV), [Wo] "s"(RO), "{s33}"(HT_OFF + 8192u + 256u + (unsigned)b * 4u), "{s36}"(h128), "{s37}"(c37), "{s40}"(w), "{s41}"(c41), "{s42}"(c42), "{s46}"(c46), "{s92}"(TMO_OFF), "{s38}"(c38), "{s39}"(c39), "{s43}"(c43), "{s45}"(c45), "{s32}"(L), "{s26}"(AGT + 1024u + xcc * 128u), "{s27}"(RDY_OFF(0) + (unsigned)((h & 7) * 1024 + (b & 7) * 128)), [ctab] "i"(CTAB_OFF), [ring] "i"(SRING / 3 * XBLK), [trm] "i"(TRING - 1), [ex] "i"(ED * 256 + EB), [dl1l] "i"(DL1 - LATE), [armd] "i"(3 - ARMK), [arms] "i"(ARMK - 3),
                 [cap] "i"(SPIN_CAP)
               : "v0", "v1", "v2", "v3", "v4", "v5", "v6", "v7", "v8", "v9", "v10", "v11", "v12", "v13", "v14", "v15", "v16", "v17", "v18", "v19",
                 "v20", "v21", "v22", "v23", "v24", "v25", "v26", "v27", "v28", "v29", "v30", "v31", "v32", "v33", "v34", "v35", "v36", "v37", "v38", "v39",
                 "v40", "v41", "v42", "v43", "v44", "v45", "v46", "v47", "v48", "v49", "v50", "v51", "v52", "v53", "v54", "v55", "v56", "v57", "v58", "v59",
                 "v60", "v61", "v62", "v63", "v64", "v65", "v66", "v67", "v68", "v70", "v71", "v72", "v73", "v74", "v75", "v76", "v77", "v78", "v79",
                 CH_WCLOB, "s20", "s21", "s22", "s34", "s35", "s24", "s25", "s90", "s91", "s47", "s87", "s50", "s51", "s52", "s69", "s82", "s85", "s54", "s75", "s80", "s55", "s56", "s57", "s58", "s59", "s60", "s61", "s62", "s63", "s64", "s66", "s67", "s68", "s70", "s71", "s72", "s73", "s74", "s88", "s89",
                 "s83", "s84", "s97", "s98", "s99", "s93", "s94", "s95", "m0", "vcc", "scc", "memory");
}
#define QLEAD 4
#define RONE 2
#define OAUX 0
#define PFM 1
#define ARMD0 28
#define PSP 4
#define QLEAD1 2
#define QXW -18
#define QXC QXW
#define TK_QXW "%[qxw]"
#define TK_QXC "%[qxc]"
#define PSG 16
#define TK_PSG CH_S(PSG)
#define AX 24
#define AX1 24
#define AM 22
#define TK_AM CH_S(AM)
#define AE 61
#define TK_AE CH_S(AE)
#define PSK 2
#define PSH 2
#define TK_CAP "%[cap]"
#define TK_CTAB "%[ctab]"
#define TK_RM "%[rm]"
#define TK_TRM "%[trm]"
#define TK_TRN "%[trn]"
#define TK_SG0 CH_S(SG0)
#define TK_PSK CH_S(PSK)
#define TK_PSH CH_S(PSH)
#define AP_ASM \
  "v_lshrrev_b32 v40, 8, v0\n\t" \
  "v_mul_u32_u24 v40, 4608, v40\n\t" \
  "v_and_b32 v41, 0xf0, v0\n\t" \
  "v_lshrrev_b32 v42, 3, v41\n\t" \
  "v_add3_u32 v41, v41, v40, s64\n\t" \
  "v_add3_u32 v42, v42, v40, s65\n\t" \
  "s_mov_b32 s62, 0x05040100\n\t" \
  "s_mov_b32 s63, 0x07060302\n\t" \
  "s_xor_b32 s60, s66, 0\n\t" \
  "s_mul_i32 s60, s60, 36864\n\t" \
  "s_add_u32 s61, s60, 18432\n\t" \
  "buffer_load_dwordx4 v[48:51], v41, %[W], s60 offen offset:0\n\t" \
  "buffer_load_dwordx4 v[52:55], v41, %[W], s60 offen offset:1152\n\t" \
  "buffer_load_dwordx4 v[56:59], v41, %[W], s60 offen offset:2304\n\t" \
  "buffer_load_dwordx4 v[60:63], v41, %[W], s60 offen offset:3456\n\t" \
  "buffer_load_dwordx4 v[64:67], v41, %[W], s61 offen offset:0\n\t" \
  "buffer_load_dwordx4 v[68:71], v41, %[W], s61 offen offset:1152\n\t" \
  "buffer_load_dwordx4 v[72:75], v41, %[W], s61 offen offset:2304\n\t" \
  "buffer_load_dwordx4 v[76:79], v41, %[W], s61 offen offset:3456\n\t" \
  "s_xor_b32 s60, s66, 1\n\t" \
  "s_mul_i32 s60, s60, 36864\n\t" \
  "s_add_u32 s61, s60, 18432\n\t" \
  "buffer_load_dwordx4 v[80:83], v41, %[W], s60 offen offset:0\n\t" \
  "buffer_load_dwordx4 v[84:87], v41, %[W], s60 offen offset:1152\n\t" \
  "buffer_load_dwordx4 v[88:91], v41, %[W], s60 offen offset:2304\n\t" \
  "buffer_load_dwordx4 v[92:95], v41, %[W], s60 offen offset:3456\n\t" \
  "buffer_load_dwordx4 v[96:99], v41, %[W], s61 offen offset:0\n\t" \
  "buffer_load_dwordx4 v[100:103], v41, %[W], s61 offen offset:1152\n\t" \
  "buffer_load_dwordx4 v[104:107], v41, %[W], s61 offen offset:2304\n\t" \
  "buffer_load_dwordx4 v[108:111], v41, %[W], s61 offen offset:3456\n\t" \
  "s_xor_b32 s60, s66, 2\n\t" \
  "s_mul_i32 s60, s60, 36864\n\t" \
  "s_add_u32 s61, s60, 18432\n\t" \
  "buffer_load_dwordx4 v[112:115], v41, %[W], s60 offen offset:0\n\t" \
  "buffer_load_dwordx4 v[116:119], v41, %[W], s60 offen offset:1152\n\t" \
  "buffer_load_dwordx4 v[120:123], v41, %[W], s60 offen offset:2304\n\t" \
  "buffer_load_dwordx4 v[124:127], v41, %[W], s60 offen offset:3456\n\t" \
  "buffer_load_dwordx4 v[128:131], v41, %[W], s61 offen offset:0\n\t" \
  "buffer_load_dwordx4 v[132:135], v41, %[W], s61 offen offset:1152\n\t" \
  "buffer_load_dwordx4 v[136:139], v41, %[W], s61 offen offset:2304\n\t" \
  "buffer_load_dwordx4 v[140:143], v41, %[W], s61 offen offset:3456\n\t" \
  "s_xor_b32 s60, s66, 3\n\t" \
  "s_mul_i32 s60, s60, 36864\n\t" \
  "s_add_u32 s61, s60, 18432\n\t" \
  "buffer_load_dwordx4 v[144:147], v41, %[W], s60 offen offset:0\n\t" \
  "buffer_load_dwordx4 v[148:151], v41, %[W], s60 offen offset:1152\n\t" \
  "buffer_load_dwordx4 v[152:155], v41, %[W], s60 offen offset:2304\n\t" \
  "buffer_load_dwordx4 v[156:159], v41, %[W], s60 offen offset:3456\n\t" \
  "buffer_load_dwordx4 v[160:163], v41, %[W], s61 offen offset:0\n\t" \
  "buffer_load_dwordx4 v[164:167], v41, %[W], s61 offen offset:1152\n\t" \
  "buffer_load_dwordx4 v[168:171], v41, %[W], s61 offen offset:2304\n\t" \
  "buffer_load_dwordx4 v[172:175], v41, %[W], s61 offen offset:3456\n\t" \
  "s_xor_b32 s60, s66, 4\n\t" \
  "s_mul_i32 s60, s60, 36864\n\t" \
  "s_add_u32 s61, s60, 18432\n\t" \
  "buffer_load_dwordx4 v[176:179], v41, %[W], s60 offen offset:0\n\t" \
  "buffer_load_dwordx4 v[180:183], v41, %[W], s60 offen offset:1152\n\t" \
  "buffer_load_dwordx4 v[184:187], v41, %[W], s60 offen offset:2304\n\t" \
  "buffer_load_dwordx4 v[188:191], v41, %[W], s60 offen offset:3456\n\t" \
  "buffer_load_dwordx4 v[192:195], v41, %[W], s61 offen offset:0\n\t" \
  "buffer_load_dwordx4 v[196:199], v41, %[W], s61 offen offset:1152\n\t" \
  "buffer_load_dwordx4 v[200:203], v41, %[W], s61 offen offset:2304\n\t" \
  "buffer_load_dwordx4 v[204:207], v41, %[W], s61 offen offset:3456\n\t" \
  "s_waitcnt vmcnt(38)\n\t" \
  "v_perm_b32 v44, v52, v48, s62\n\t" \
  "s_waitcnt vmcnt(36)\n\t" \
  "v_perm_b32 v45, v60, v56, s62\n\t" \
  "s_waitcnt vmcnt(34)\n\t" \
  "v_perm_b32 v46, v68, v64, s62\n\t" \
  "s_waitcnt vmcnt(32)\n\t" \
  "v_perm_b32 v47, v76, v72, s62\n\t" \
  "v_accvgpr_write_b32 a0, v44\n\t" \
  "v_accvgpr_write_b32 a1, v45\n\t" \
  "v_accvgpr_write_b32 a2, v46\n\t" \
  "v_accvgpr_write_b32 a3, v47\n\t" \
  "v_perm_b32 v44, v52, v48, s63\n\t" \
  "v_perm_b32 v45, v60, v56, s63\n\t" \
  "v_perm_b32 v46, v68, v64, s63\n\t" \
  "v_perm_b32 v47, v76, v72, s63\n\t" \
  "v_accvgpr_write_b32 a4, v44\n\t" \
  "v_accvgpr_write_b32 a5, v45\n\t" \
  "v_accvgpr_write_b32 a6, v46\n\t" \
  "v_accvgpr_write_b32 a7, v47\n\t" \
  "v_perm_b32 v44, v53, v49, s62\n\t" \
  "v_perm_b32 v45, v61, v57, s62\n\t" \
  "v_perm_b32 v46, v69, v65, s62\n\t" \
  "v_perm_b32 v47, v77, v73, s62\n\t" \
  "v_accvgpr_write_b32 a8, v44\n\t" \
  "v_accvgpr_write_b32 a9, v45\n\t" \
  "v_accvgpr_write_b32 a10, v46\n\t" \
  "v_accvgpr_write_b32 a11, v47\n\t" \
  "v_perm_b32 v44, v53, v49, s63\n\t" \
  "v_perm_b32 v45, v61, v57, s63\n\t" \
  "v_perm_b32 v46, v69, v65, s63\n\t" \
  "v_perm_b32 v47, v77, v73, s63\n\t" \
  "v_accvgpr_write_b32 a12, v44\n\t" \
  "v_accvgpr_write_b32 a13, v45\n\t" \
  "v_accvgpr_write_b32 a14, v46\n\t" \
  "v_accvgpr_write_b32 a15, v47\n\t" \
  "v_perm_b32 v44, v54, v50, s62\n\t" \
  "v_perm_b32 v45, v62, v58, s62\n\t" \
  "v_perm_b32 v46, v70, v66, s62\n\t" \
  "v_perm_b32 v47, v78, v74, s62\n\t" \
  "v_accvgpr_write_b32 a16, v44\n\t" \
  "v_accvgpr_write_b32 a17, v45\n\t" \
  "v_accvgpr_write_b32 a18, v46\n\t" \
  "v_accvgpr_write_b32 a19, v47\n\t" \
  "v_perm_b32 v44, v54, v50, s63\n\t" \
  "v_perm_b32 v45, v62, v58, s63\n\t" \
  "v_perm_b32 v46, v70, v66, s63\n\t" \
  "v_perm_b32 v47, v78, v74, s63\n\t" \
  "v_accvgpr_write_b32 a20, v44\n\t" \
  "v_accvgpr_write_b32 a21, v45\n\t" \
  "v_accvgpr_write_b32 a22, v46\n\t" \
  "v_accvgpr_write_b32 a23, v47\n\t" \
  "v_perm_b32 v44, v55, v51, s62\n\t" \
  "v_perm_b32 v45, v63, v59, s62\n\t" \
  "v_perm_b32 v46, v71, v67, s62\n\t" \
  "v_perm_b32 v47, v79, v75, s62\n\t" \
  "v_accvgpr_write_b32 a24, v44\n\t" \
  "v_accvgpr_write_b32 a25, v45\n\t" \
  "v_accvgpr_write_b32 a26, v46\n\t" \
  "v_accvgpr_write_b32 a27, v47\n\t" \
  "v_perm_b32 v44, v55, v51, s63\n\t" \
  "v_perm_b32 v45, v63, v59, s63\n\t" \
  "v_perm_b32 v46, v71, v67, s63\n\t" \
  "v_perm_b32 v47, v79, v75, s63\n\t" \
  "v_accvgpr_write_b32 a28, v44\n\t" \
  "v_accvgpr_write_b32 a29, v45\n\t" \
  "v_accvgpr_write_b32 a30, v46\n\t" \
  "v_accvgpr_write_b32 a31, v47\n\t" \
  "s_xor_b32 s60, s66, 5\n\t" \
  "s_mul_i32 s60, s60, 36864\n\t" \
  "s_add_u32 s61, s60, 18432\n\t" \
  "buffer_load_dwordx4 v[48:51], v41, %[W], s60 offen offset:0\n\t" \
  "buffer_load_dwordx4 v[52:55], v41, %[W], s60 offen offset:1152\n\t" \
  "buffer_load_dwordx4 v[56:59], v41, %[W], s60 offen offset:2304\n\t" \
  "buffer_load_dwordx4 v[60:63], v41, %[W], s60 offen offset:3456\n\t" \
  "buffer_load_dwordx4 v[64:67], v41, %[W], s61 offen offset:0\n\t" \
  "buffer_load_dwordx4 v[68:71], v41, %[W], s61 offen offset:1152\n\t" \
  "buffer_load_dwordx4 v[72:75], v41, %[W], s61 offen offset:2304\n\t" \
  "buffer_load_dwordx4 v[76:79], v41, %[W], s61 offen offset:3456\n\t" \
  "s_waitcnt vmcnt(38)\n\t" \
  "v_perm_b32 v44, v84, v80, s62\n\t" \
  "s_waitcnt vmcnt(36)\n\t" \
  "v_perm_b32 v45, v92, v88, s62\n\t" \
  "s_waitcnt vmcnt(34)\n\t" \
  "v_perm_b32 v46, v100, v96, s62\n\t" \
  "s_waitcnt vmcnt(32)\n\t" \
  "v_perm_b32 v47, v108, v104, s62\n\t" \
  "v_accvgpr_write_b32 a32, v44\n\t" \
  "v_accvgpr_write_b32 a33, v45\n\t" \
  "v_accvgpr_write_b32 a34, v46\n\t" \
  "v_accvgpr_write_b32 a35, v47\n\t" \
  "v_perm_b32 v44, v84, v80, s63\n\t" \
  "v_perm_b32 v45, v92, v88, s63\n\t" \
  "v_perm_b32 v46, v100, v96, s63\n\t" \
  "v_perm_b32 v47, v108, v104, s63\n\t" \
  "v_accvgpr_write_b32 a36, v44\n\t" \
  "v_accvgpr_write_b32 a37, v45\n\t" \
  "v_accvgpr_write_b32 a38, v46\n\t" \
  "v_accvgpr_write_b32 a39, v47\n\t" \
  "v_perm_b32 v44, v85, v81, s62\n\t" \
  "v_perm_b32 v45, v93, v89, s62\n\t" \
  "v_perm_b32 v46, v101, v97, s62\n\t" \
  "v_perm_b32 v47, v109, v105, s62\n\t" \
  "v_accvgpr_write_b32 a40, v44\n\t" \
  "v_accvgpr_write_b32 a41, v45\n\t" \
  "v_accvgpr_write_b32 a42, v46\n\t" \
  "v_accvgpr_write_b32 a43, v47\n\t" \
  "v_perm_b32 v44, v85, v81, s63\n\t" \
  "v_perm_b32 v45, v93, v89, s63\n\t" \
  "v_perm_b32 v46, v101, v97, s63\n\t" \
  "v_perm_b32 v47, v109, v105, s63\n\t" \
  "v_accvgpr_write_b32 a44, v44\n\t" \
  "v_accvgpr_write_b32 a45, v45\n\t" \
  "v_accvgpr_write_b32 a46, v46\n\t" \
  "v_accvgpr_write_b32 a47, v47\n\t" \
  "v_perm_b32 v44, v86, v82, s62\n\t" \
  "v_perm_b32 v45, v94, v90, s62\n\t" \
  "v_perm_b32 v46, v102, v98, s62\n\t" \
  "v_perm_b32 v47, v110, v106, s62\n\t" \
  "v_accvgpr_write_b32 a48, v44\n\t" \
  "v_accvgpr_write_b32 a49, v45\n\t" \
  "v_accvgpr_write_b32 a50, v46\n\t" \
  "v_accvgpr_write_b32 a51, v47\n\t" \
  "v_perm_b32 v44, v86, v82, s63\n\t" \
  "v_perm_b32 v45, v94, v90, s63\n\t" \
  "v_perm_b32 v46, v102, v98, s63\n\t" \
  "v_perm_b32 v47, v110, v106, s63\n\t" \
  "v_accvgpr_write_b32 a52, v44\n\t" \
  "v_accvgpr_write_b32 a53, v45\n\t" \
  "v_accvgpr_write_b32 a54, v46\n\t" \
  "v_accvgpr_write_b32 a55, v47\n\t" \
  "v_perm_b32 v44, v87, v83, s62\n\t" \
  "v_perm_b32 v45, v95, v91, s62\n\t" \
  "v_perm_b32 v46, v103, v99, s62\n\t" \
  "v_perm_b32 v47, v111, v107, s62\n\t" \
  "v_accvgpr_write_b32 a56, v44\n\t" \
  "v_accvgpr_write_b32 a57, v45\n\t" \
  "v_accvgpr_write_b32 a58, v46\n\t" \
  "v_accvgpr_write_b32 a59, v47\n\t" \
  "v_perm_b32 v44, v87, v83, s63\n\t" \
  "v_perm_b32 v45, v95, v91, s63\n\t" \
  "v_perm_b32 v46, v103, v99, s63\n\t" \
  "v_perm_b32 v47, v111, v107, s63\n\t" \
  "v_accvgpr_write_b32 a60, v44\n\t" \
  "v_accvgpr_write_b32 a61, v45\n\t" \
  "v_accvgpr_write_b32 a62, v46\n\t" \
  "v_accvgpr_write_b32 a63, v47\n\t" \
  "s_xor_b32 s60, s66, 6\n\t" \
  "s_mul_i32 s60, s60, 36864\n\t" \
  "s_add_u32 s61, s60, 18432\n\t" \
  "buffer_load_dwordx4 v[80:83], v41, %[W], s60 offen offset:0\n\t" \
  "buffer_load_dwordx4 v[84:87], v41, %[W], s60 offen offset:1152\n\t" \
  "buffer_load_dwordx4 v[88:91], v41, %[W], s60 offen offset:2304\n\t" \
  "buffer_load_dwordx4 v[92:95], v41, %[W], s60 offen offset:3456\n\t" \
  "buffer_load_dwordx4 v[96:99], v41, %[W], s61 offen offset:0\n\t" \
  "buffer_load_dwordx4 v[100:103], v41, %[W], s61 offen offset:1152\n\t" \
  "buffer_load_dwordx4 v[104:107], v41, %[W], s61 offen offset:2304\n\t" \
  "buffer_load_dwordx4 v[108:111], v41, %[W], s61 offen offset:3456\n\t" \
  "s_waitcnt vmcnt(38)\n\t" \
  "v_perm_b32 v44, v116, v112, s62\n\t" \
  "s_waitcnt vmcnt(36)\n\t" \
  "v_perm_b32 v45, v124, v120, s62\n\t" \
  "s_waitcnt vmcnt(34)\n\t" \
  "v_perm_b32 v46, v132, v128, s62\n\t" \
  "s_waitcnt vmcnt(32)\n\t" \
  "v_perm_b32 v47, v140, v136, s62\n\t" \
  "v_accvgpr_write_b32 a64, v44\n\t" \
  "v_accvgpr_write_b32 a65, v45\n\t" \
  "v_accvgpr_write_b32 a66, v46\n\t" \
  "v_accvgpr_write_b32 a67, v47\n\t" \
  "v_perm_b32 v44, v116, v112, s63\n\t" \
  "v_perm_b32 v45, v124, v120, s63\n\t" \
  "v_perm_b32 v46, v132, v128, s63\n\t" \
  "v_perm_b32 v47, v140, v136, s63\n\t" \
  "v_accvgpr_write_b32 a68, v44\n\t" \
  "v_accvgpr_write_b32 a69, v45\n\t" \
  "v_accvgpr_write_b32 a70, v46\n\t" \
  "v_accvgpr_write_b32 a71, v47\n\t" \
  "v_perm_b32 v44, v117, v113, s62\n\t" \
  "v_perm_b32 v45, v125, v121, s62\n\t" \
  "v_perm_b32 v46, v133, v129, s62\n\t" \
  "v_perm_b32 v47, v141, v137, s62\n\t" \
  "v_accvgpr_write_b32 a72, v44\n\t" \
  "v_accvgpr_write_b32 a73, v45\n\t" \
  "v_accvgpr_write_b32 a74, v46\n\t" \
  "v_accvgpr_write_b32 a75, v47\n\t" \
  "v_perm_b32 v44, v117, v113, s63\n\t" \
  "v_perm_b32 v45, v125, v121, s63\n\t" \
  "v_perm_b32 v46, v133, v129, s63\n\t" \
  "v_perm_b32 v47, v141, v137, s63\n\t" \
  "v_accvgpr_write_b32 a76, v44\n\t" \
  "v_accvgpr_write_b32 a77, v45\n\t" \
  "v_accvgpr_write_b32 a78, v46\n\t" \
  "v_accvgpr_write_b32 a79, v47\n\t" \
  "v_perm_b32 v44, v118, v114, s62\n\t" \
  "v_perm_b32 v45, v126, v122, s62\n\t" \
  "v_perm_b32 v46, v134, v130, s62\n\t" \
  "v_perm_b32 v47, v142, v138, s62\n\t" \
  "v_accvgpr_write_b32 a80, v44\n\t" \
  "v_accvgpr_write_b32 a81, v45\n\t" \
  "v_accvgpr_write_b32 a82, v46\n\t" \
  "v_accvgpr_write_b32 a83, v47\n\t" \
  "v_perm_b32 v44, v118, v114, s63\n\t" \
  "v_perm_b32 v45, v126, v122, s63\n\t" \
  "v_perm_b32 v46, v134, v130, s63\n\t" \
  "v_perm_b32 v47, v142, v138, s63\n\t" \
  "v_accvgpr_write_b32 a84, v44\n\t" \
  "v_accvgpr_write_b32 a85, v45\n\t" \
  "v_accvgpr_write_b32 a86, v46\n\t" \
  "v_accvgpr_write_b32 a87, v47\n\t" \
  "v_perm_b32 v44, v119, v115, s62\n\t" \
  "v_perm_b32 v45, v127, v123, s62\n\t" \
  "v_perm_b32 v46, v135, v131, s62\n\t" \
  "v_perm_b32 v47, v143, v139, s62\n\t" \
  "v_accvgpr_write_b32 a88, v44\n\t" \
  "v_accvgpr_write_b32 a89, v45\n\t" \
  "v_accvgpr_write_b32 a90, v46\n\t" \
  "v_accvgpr_write_b32 a91, v47\n\t" \
  "v_perm_b32 v44, v119, v115, s63\n\t" \
  "v_perm_b32 v45, v127, v123, s63\n\t" \
  "v_perm_b32 v46, v135, v131, s63\n\t" \
  "v_perm_b32 v47, v143, v139, s63\n\t" \
  "v_accvgpr_write_b32 a92, v44\n\t" \
  "v_accvgpr_write_b32 a93, v45\n\t" \
  "v_accvgpr_write_b32 a94, v46\n\t" \
  "v_accvgpr_write_b32 a95, v47\n\t" \
  "s_xor_b32 s60, s66, 7\n\t" \
  "s_mul_i32 s60, s60, 36864\n\t" \
  "s_add_u32 s61, s60, 18432\n\t" \
  "buffer_load_dwordx4 v[112:115], v41, %[W], s60 offen offset:0\n\t" \
  "buffer_load_dwordx4 v[116:119], v41, %[W], s60 offen offset:1152\n\t" \
  "buffer_load_dwordx4 v[120:123], v41, %[W], s60 offen offset:2304\n\t" \
  "buffer_load_dwordx4 v[124:127], v41, %[W], s60 offen offset:3456\n\t" \
  "buffer_load_dwordx4 v[128:131], v41, %[W], s61 offen offset:0\n\t" \
  "buffer_load_dwordx4 v[132:135], v41, %[W], s61 offen offset:1152\n\t" \
  "buffer_load_dwordx4 v[136:139], v41, %[W], s61 offen offset:2304\n\t" \
  "buffer_load_dwordx4 v[140:143], v41, %[W], s61 offen offset:3456\n\t" \
  "s_waitcnt vmcnt(38)\n\t" \
  "v_perm_b32 v44, v148, v144, s62\n\t" \
  "s_waitcnt vmcnt(36)\n\t" \
  "v_perm_b32 v45, v156, v152, s62\n\t" \
  "s_waitcnt vmcnt(34)\n\t" \
  "v_perm_b32 v46, v164, v160, s62\n\t" \
  "s_waitcnt vmcnt(32)\n\t" \
  "v_perm_b32 v47, v172, v168, s62\n\t" \
  "v_accvgpr_write_b32 a96, v44\n\t" \
  "v_accvgpr_write_b32 a97, v45\n\t" \
  "v_accvgpr_write_b32 a98, v46\n\t" \
  "v_accvgpr_write_b32 a99, v47\n\t" \
  "v_perm_b32 v44, v148, v144, s63\n\t" \
  "v_perm_b32 v45, v156, v152, s63\n\t" \
  "v_perm_b32 v46, v164, v160, s63\n\t" \
  "v_perm_b32 v47, v172, v168, s63\n\t" \
  "v_accvgpr_write_b32 a100, v44\n\t" \
  "v_accvgpr_write_b32 a101, v45\n\t" \
  "v_accvgpr_write_b32 a102, v46\n\t" \
  "v_accvgpr_write_b32 a103, v47\n\t" \
  "v_perm_b32 v44, v149, v145, s62\n\t" \
  "v_perm_b32 v45, v157, v153, s62\n\t" \
  "v_perm_b32 v46, v165, v161, s62\n\t" \
  "v_perm_b32 v47, v173, v169, s62\n\t" \
  "v_accvgpr_write_b32 a104, v44\n\t" \
  "v_accvgpr_write_b32 a105, v45\n\t" \
  "v_accvgpr_write_b32 a106, v46\n\t" \
  "v_accvgpr_write_b32 a107, v47\n\t" \
  "v_perm_b32 v44, v149, v145, s63\n\t" \
  "v_perm_b32 v45, v157, v153, s63\n\t" \
  "v_perm_b32 v46, v165, v161, s63\n\t" \
  "v_perm_b32 v47, v173, v169, s63\n\t" \
  "v_accvgpr_write_b32 a108, v44\n\t" \
  "v_accvgpr_write_b32 a109, v45\n\t" \
  "v_accvgpr_write_b32 a110, v46\n\t" \
  "v_accvgpr_write_b32 a111, v47\n\t" \
  "v_perm_b32 v44, v150, v146, s62\n\t" \
  "v_perm_b32 v45, v158, v154, s62\n\t" \
  "v_perm_b32 v46, v166, v162, s62\n\t" \
  "v_perm_b32 v47, v174, v170, s62\n\t" \
  "v_accvgpr_write_b32 a112, v44\n\t" \
  "v_accvgpr_write_b32 a113, v45\n\t" \
  "v_accvgpr_write_b32 a114, v46\n\t" \
  "v_accvgpr_write_b32 a115, v47\n\t" \
  "v_perm_b32 v44, v150, v146, s63\n\t" \
  "v_perm_b32 v45, v158, v154, s63\n\t" \
  "v_perm_b32 v46, v166, v162, s63\n\t" \
  "v_perm_b32 v47, v174, v170, s63\n\t" \
  "v_accvgpr_write_b32 a116, v44\n\t" \
  "v_accvgpr_write_b32 a117, v45\n\t" \
  "v_accvgpr_write_b32 a118, v46\n\t" \
  "v_accvgpr_write_b32 a119, v47\n\t" \
  "v_perm_b32 v44, v151, v147, s62\n\t" \
  "v_perm_b32 v45, v159, v155, s62\n\t" \
  "v_perm_b32 v46, v167, v163, s62\n\t" \
  "v_perm_b32 v47, v175, v171, s62\n\t" \
  "v_accvgpr_write_b32 a120, v44\n\t" \
  "v_accvgpr_write_b32 a121, v45\n\t" \
  "v_accvgpr_write_b32 a122, v46\n\t" \
  "v_accvgpr_write_b32 a123, v47\n\t" \
  "v_perm_b32 v44, v151, v147, s63\n\t" \
  "v_perm_b32 v45, v159, v155, s63\n\t" \
  "v_perm_b32 v46, v167, v163, s63\n\t" \
  "v_perm_b32 v47, v175, v171, s63\n\t" \
  "v_accvgpr_write_b32 a124, v44\n\t" \
  "v_accvgpr_write_b32 a125, v45\n\t" \
  "v_accvgpr_write_b32 a126, v46\n\t" \
  "v_accvgpr_write_b32 a127, v47\n\t" \
  "s_xor_b32 s60, s66, 0\n\t" \
  "s_mul_i32 s60, s60, 36864\n\t" \
  "s_add_u32 s61, s60, 18432\n\t" \
  "buffer_load_ushort v224, v42, %[W], s60 offen offset:0\n\t" \
  "buffer_load_ushort v144, v42, %[W], s60 offen offset:1152\n\t" \
  "buffer_load_ushort v225, v42, %[W], s60 offen offset:2304\n\t" \
  "buffer_load_ushort v145, v42, %[W], s60 offen offset:3456\n\t" \
  "buffer_load_ushort v226, v42, %[W], s61 offen offset:0\n\t" \
  "buffer_load_ushort v146, v42, %[W], s61 offen offset:1152\n\t" \
  "buffer_load_ushort v227, v42, %[W], s61 offen offset:2304\n\t" \
  "buffer_load_ushort v147, v42, %[W], s61 offen offset:3456\n\t" \
  "s_xor_b32 s60, s66, 1\n\t" \
  "s_mul_i32 s60, s60, 36864\n\t" \
  "s_add_u32 s61, s60, 18432\n\t" \
  "buffer_load_ushort v228, v42, %[W], s60 offen offset:0\n\t" \
  "buffer_load_ushort v148, v42, %[W], s60 offen offset:1152\n\t" \
  "buffer_load_ushort v229, v42, %[W], s60 offen offset:2304\n\t" \
  "buffer_load_ushort v149, v42, %[W], s60 offen offset:3456\n\t" \
  "buffer_load_ushort v230, v42, %[W], s61 offen offset:0\n\t" \
  "buffer_load_ushort v150, v42, %[W], s61 offen offset:1152\n\t" \
  "buffer_load_ushort v231, v42, %[W], s61 offen offset:2304\n\t" \
  "buffer_load_ushort v151, v42, %[W], s61 offen offset:3456\n\t" \
  "s_waitcnt vmcnt(46)\n\t" \
  "v_perm_b32 v44, v180, v176, s62\n\t" \
  "s_waitcnt vmcnt(44)\n\t" \
  "v_perm_b32 v45, v188, v184, s62\n\t" \
  "s_waitcnt vmcnt(42)\n\t" \
  "v_perm_b32 v46, v196, v192, s62\n\t" \
  "s_waitcnt vmcnt(40)\n\t" \
  "v_perm_b32 v47, v204, v200, s62\n\t" \
  "v_accvgpr_write_b32 a128, v44\n\t" \
  "v_accvgpr_write_b32 a129, v45\n\t" \
  "v_accvgpr_write_b32 a130, v46\n\t" \
  "v_accvgpr_write_b32 a131, v47\n\t" \
  "v_perm_b32 v44, v180, v176, s63\n\t" \
  "v_perm_b32 v45, v188, v184, s63\n\t" \
  "v_perm_b32 v46, v196, v192, s63\n\t" \
  "v_perm_b32 v47, v204, v200, s63\n\t" \
  "v_accvgpr_write_b32 a132, v44\n\t" \
  "v_accvgpr_write_b32 a133, v45\n\t" \
  "v_accvgpr_write_b32 a134, v46\n\t" \
  "v_accvgpr_write_b32 a135, v47\n\t" \
  "v_perm_b32 v44, v181, v177, s62\n\t" \
  "v_perm_b32 v45, v189, v185, s62\n\t" \
  "v_perm_b32 v46, v197, v193, s62\n\t" \
  "v_perm_b32 v47, v205, v201, s62\n\t" \
  "v_accvgpr_write_b32 a136, v44\n\t" \
  "v_accvgpr_write_b32 a137, v45\n\t" \
  "v_accvgpr_write_b32 a138, v46\n\t" \
  "v_accvgpr_write_b32 a139, v47\n\t" \
  "v_perm_b32 v44, v181, v177, s63\n\t" \
  "v_perm_b32 v45, v189, v185, s63\n\t" \
  "v_perm_b32 v46, v197, v193, s63\n\t" \
  "v_perm_b32 v47, v205, v201, s63\n\t" \
  "v_accvgpr_write_b32 a140, v44\n\t" \
  "v_accvgpr_write_b32 a141, v45\n\t" \
  "v_accvgpr_write_b32 a142, v46\n\t" \
  "v_accvgpr_write_b32 a143, v47\n\t" \
  "v_perm_b32 v44, v182, v178, s62\n\t" \
  "v_perm_b32 v45, v190, v186, s62\n\t" \
  "v_perm_b32 v46, v198, v194, s62\n\t" \
  "v_perm_b32 v47, v206, v202, s62\n\t" \
  "v_accvgpr_write_b32 a144, v44\n\t" \
  "v_accvgpr_write_b32 a145, v45\n\t" \
  "v_accvgpr_write_b32 a146, v46\n\t" \
  "v_accvgpr_write_b32 a147, v47\n\t" \
  "v_perm_b32 v44, v182, v178, s63\n\t" \
  "v_perm_b32 v45, v190, v186, s63\n\t" \
  "v_perm_b32 v46, v198, v194, s63\n\t" \
  "v_perm_b32 v47, v206, v202, s63\n\t" \
  "v_accvgpr_write_b32 a148, v44\n\t" \
  "v_accvgpr_write_b32 a149, v45\n\t" \
  "v_accvgpr_write_b32 a150, v46\n\t" \
  "v_accvgpr_write_b32 a151, v47\n\t" \
  "v_perm_b32 v44, v183, v179, s62\n\t" \
  "v_perm_b32 v45, v191, v187, s62\n\t" \
  "v_perm_b32 v46, v199, v195, s62\n\t" \
  "v_perm_b32 v47, v207, v203, s62\n\t" \
  "v_accvgpr_write_b32 a152, v44\n\t" \
  "v_accvgpr_write_b32 a153, v45\n\t" \
  "v_accvgpr_write_b32 a154, v46\n\t" \
  "v_accvgpr_write_b32 a155, v47\n\t" \
  "v_perm_b32 v44, v183, v179, s63\n\t" \
  "v_perm_b32 v45, v191, v187, s63\n\t" \
  "v_perm_b32 v46, v199, v195, s63\n\t" \
  "v_perm_b32 v47, v207, v203, s63\n\t" \
  "v_accvgpr_write_b32 a156, v44\n\t" \
  "v_accvgpr_write_b32 a157, v45\n\t" \
  "v_accvgpr_write_b32 a158, v46\n\t" \
  "v_accvgpr_write_b32 a159, v47\n\t" \
  "s_xor_b32 s60, s66, 2\n\t" \
  "s_mul_i32 s60, s60, 36864\n\t" \
  "s_add_u32 s61, s60, 18432\n\t" \
  "buffer_load_ushort v232, v42, %[W], s60 offen offset:0\n\t" \
  "buffer_load_ushort v152, v42, %[W], s60 offen offset:1152\n\t" \
  "buffer_load_ushort v233, v42, %[W], s60 offen offset:2304\n\t" \
  "buffer_load_ushort v153, v42, %[W], s60 offen offset:3456\n\t" \
  "buffer_load_ushort v234, v42, %[W], s61 offen offset:0\n\t" \
  "buffer_load_ushort v154, v42, %[W], s61 offen offset:1152\n\t" \
  "buffer_load_ushort v235, v42, %[W], s61 offen offset:2304\n\t" \
  "buffer_load_ushort v155, v42, %[W], s61 offen offset:3456\n\t" \
  "s_xor_b32 s60, s66, 3\n\t" \
  "s_mul_i32 s60, s60, 36864\n\t" \
  "s_add_u32 s61, s60, 18432\n\t" \
  "buffer_load_ushort v236, v42, %[W], s60 offen offset:0\n\t" \
  "buffer_load_ushort v156, v42, %[W], s60 offen offset:1152\n\t" \
  "buffer_load_ushort v237, v42, %[W], s60 offen offset:2304\n\t" \
  "buffer_load_ushort v157, v42, %[W], s60 offen offset:3456\n\t" \
  "buffer_load_ushort v238, v42, %[W], s61 offen offset:0\n\t" \
  "buffer_load_ushort v158, v42, %[W], s61 offen offset:1152\n\t" \
  "buffer_load_ushort v239, v42, %[W], s61 offen offset:2304\n\t" \
  "buffer_load_ushort v159, v42, %[W], s61 offen offset:3456\n\t" \
  "s_waitcnt vmcnt(54)\n\t" \
  "v_perm_b32 v44, v52, v48, s62\n\t" \
  "s_waitcnt vmcnt(52)\n\t" \
  "v_perm_b32 v45, v60, v56, s62\n\t" \
  "s_waitcnt vmcnt(50)\n\t" \
  "v_perm_b32 v46, v68, v64, s62\n\t" \
  "s_waitcnt vmcnt(48)\n\t" \
  "v_perm_b32 v47, v76, v72, s62\n\t" \
  "v_accvgpr_write_b32 a160, v44\n\t" \
  "v_accvgpr_write_b32 a161, v45\n\t" \
  "v_accvgpr_write_b32 a162, v46\n\t" \
  "v_accvgpr_write_b32 a163, v47\n\t" \
  "v_perm_b32 v44, v52, v48, s63\n\t" \
  "v_perm_b32 v45, v60, v56, s63\n\t" \
  "v_perm_b32 v46, v68, v64, s63\n\t" \
  "v_perm_b32 v47, v76, v72, s63\n\t" \
  "v_accvgpr_write_b32 a164, v44\n\t" \
  "v_accvgpr_write_b32 a165, v45\n\t" \
  "v_accvgpr_write_b32 a166, v46\n\t" \
  "v_accvgpr_write_b32 a167, v47\n\t" \
  "v_perm_b32 v44, v53, v49, s62\n\t" \
  "v_perm_b32 v45, v61, v57, s62\n\t" \
  "v_perm_b32 v46, v69, v65, s62\n\t" \
  "v_perm_b32 v47, v77, v73, s62\n\t" \
  "v_accvgpr_write_b32 a168, v44\n\t" \
  "v_accvgpr_write_b32 a169, v45\n\t" \
  "v_accvgpr_write_b32 a170, v46\n\t" \
  "v_accvgpr_write_b32 a171, v47\n\t" \
  "v_perm_b32 v44, v53, v49, s63\n\t" \
  "v_perm_b32 v45, v61, v57, s63\n\t" \
  "v_perm_b32 v46, v69, v65, s63\n\t" \
  "v_perm_b32 v47, v77, v73, s63\n\t" \
  "v_accvgpr_write_b32 a172, v44\n\t" \
  "v_accvgpr_write_b32 a173, v45\n\t" \
  "v_accvgpr_write_b32 a174, v46\n\t" \
  "v_accvgpr_write_b32 a175, v47\n\t" \
  "v_perm_b32 v44, v54, v50, s62\n\t" \
  "v_perm_b32 v45, v62, v58, s62\n\t" \
  "v_perm_b32 v46, v70, v66, s62\n\t" \
  "v_perm_b32 v47, v78, v74, s62\n\t" \
  "v_accvgpr_write_b32 a176, v44\n\t" \
  "v_accvgpr_write_b32 a177, v45\n\t" \
  "v_accvgpr_write_b32 a178, v46\n\t" \
  "v_accvgpr_write_b32 a179, v47\n\t" \
  "v_perm_b32 v44, v54, v50, s63\n\t" \
  "v_perm_b32 v45, v62, v58, s63\n\t" \
  "v_perm_b32 v46, v70, v66, s63\n\t" \
  "v_perm_b32 v47, v78, v74, s63\n\t" \
  "v_accvgpr_write_b32 a180, v44\n\t" \
  "v_accvgpr_write_b32 a181, v45\n\t" \
  "v_accvgpr_write_b32 a182, v46\n\t" \
  "v_accvgpr_write_b32 a183, v47\n\t" \
  "v_perm_b32 v44, v55, v51, s62\n\t" \
  "v_perm_b32 v45, v63, v59, s62\n\t" \
  "v_perm_b32 v46, v71, v67, s62\n\t" \
  "v_perm_b32 v47, v79, v75, s62\n\t" \
  "v_accvgpr_write_b32 a184, v44\n\t" \
  "v_accvgpr_write_b32 a185, v45\n\t" \
  "v_accvgpr_write_b32 a186, v46\n\t" \
  "v_accvgpr_write_b32 a187, v47\n\t" \
  "v_perm_b32 v44, v55, v51, s63\n\t" \
  "v_perm_b32 v45, v63, v59, s63\n\t" \
  "v_perm_b32 v46, v71, v67, s63\n\t" \
  "v_perm_b32 v47, v79, v75, s63\n\t" \
  "v_accvgpr_write_b32 a188, v44\n\t" \
  "v_accvgpr_write_b32 a189, v45\n\t" \
  "v_accvgpr_write_b32 a190, v46\n\t" \
  "v_accvgpr_write_b32 a191, v47\n\t" \
  "s_xor_b32 s60, s66, 4\n\t" \
  "s_mul_i32 s60, s60, 36864\n\t" \
  "s_add_u32 s61, s60, 18432\n\t" \
  "buffer_load_ushort v240, v42, %[W], s60 offen offset:0\n\t" \
  "buffer_load_ushort v160, v42, %[W], s60 offen offset:1152\n\t" \
  "buffer_load_ushort v241, v42, %[W], s60 offen offset:2304\n\t" \
  "buffer_load_ushort v161, v42, %[W], s60 offen offset:3456\n\t" \
  "buffer_load_ushort v242, v42, %[W], s61 offen offset:0\n\t" \
  "buffer_load_ushort v162, v42, %[W], s61 offen offset:1152\n\t" \
  "buffer_load_ushort v243, v42, %[W], s61 offen offset:2304\n\t" \
  "buffer_load_ushort v163, v42, %[W], s61 offen offset:3456\n\t" \
  "s_waitcnt vmcnt(54)\n\t" \
  "v_perm_b32 v44, v84, v80, s62\n\t" \
  "s_waitcnt vmcnt(52)\n\t" \
  "v_perm_b32 v45, v92, v88, s62\n\t" \
  "s_waitcnt vmcnt(50)\n\t" \
  "v_perm_b32 v46, v100, v96, s62\n\t" \
  "s_waitcnt vmcnt(48)\n\t" \
  "v_perm_b32 v47, v108, v104, s62\n\t" \
  "v_accvgpr_write_b32 a192, v44\n\t" \
  "v_accvgpr_write_b32 a193, v45\n\t" \
  "v_accvgpr_write_b32 a194, v46\n\t" \
  "v_accvgpr_write_b32 a195, v47\n\t" \
  "v_perm_b32 v44, v84, v80, s63\n\t" \
  "v_perm_b32 v45, v92, v88, s63\n\t" \
  "v_perm_b32 v46, v100, v96, s63\n\t" \
  "v_perm_b32 v47, v108, v104, s63\n\t" \
  "v_accvgpr_write_b32 a196, v44\n\t" \
  "v_accvgpr_write_b32 a197, v45\n\t" \
  "v_accvgpr_write_b32 a198, v46\n\t" \
  "v_accvgpr_write_b32 a199, v47\n\t" \
  "v_perm_b32 v44, v85, v81, s62\n\t" \
  "v_perm_b32 v45, v93, v89, s62\n\t" \
  "v_perm_b32 v46, v101, v97, s62\n\t" \
  "v_perm_b32 v47, v109, v105, s62\n\t" \
  "v_accvgpr_write_b32 a200, v44\n\t" \
  "v_accvgpr_write_b32 a201, v45\n\t" \
  "v_accvgpr_write_b32 a202, v46\n\t" \
  "v_accvgpr_write_b32 a203, v47\n\t" \
  "v_perm_b32 v44, v85, v81, s63\n\t" \
  "v_perm_b32 v45, v93, v89, s63\n\t" \
  "v_perm_b32 v46, v101, v97, s63\n\t" \
  "v_perm_b32 v47, v109, v105, s63\n\t" \
  "v_accvgpr_write_b32 a204, v44\n\t" \
  "v_accvgpr_write_b32 a205, v45\n\t" \
  "v_accvgpr_write_b32 a206, v46\n\t" \
  "v_accvgpr_write_b32 a207, v47\n\t" \
  "v_perm_b32 v44, v86, v82, s62\n\t" \
  "v_perm_b32 v45, v94, v90, s62\n\t" \
  "v_perm_b32 v46, v102, v98, s62\n\t" \
  "v_perm_b32 v47, v110, v106, s62\n\t" \
  "v_accvgpr_write_b32 a208, v44\n\t" \
  "v_accvgpr_write_b32 a209, v45\n\t" \
  "v_accvgpr_write_b32 a210, v46\n\t" \
  "v_accvgpr_write_b32 a211, v47\n\t" \
  "v_perm_b32 v44, v86, v82, s63\n\t" \
  "v_perm_b32 v45, v94, v90, s63\n\t" \
  "v_perm_b32 v46, v102, v98, s63\n\t" \
  "v_perm_b32 v47, v110, v106, s63\n\t" \
  "v_accvgpr_write_b32 a212, v44\n\t" \
  "v_accvgpr_write_b32 a213, v45\n\t" \
  "v_accvgpr_write_b32 a214, v46\n\t" \
  "v_accvgpr_write_b32 a215, v47\n\t" \
  "v_perm_b32 v44, v87, v83, s62\n\t" \
  "v_perm_b32 v45, v95, v91, s62\n\t" \
  "v_perm_b32 v46, v103, v99, s62\n\t" \
  "v_perm_b32 v47, v111, v107, s62\n\t" \
  "v_accvgpr_write_b32 a216, v44\n\t" \
  "v_accvgpr_write_b32 a217, v45\n\t" \
  "v_accvgpr_write_b32 a218, v46\n\t" \
  "v_accvgpr_write_b32 a219, v47\n\t" \
  "v_perm_b32 v44, v87, v83, s63\n\t" \
  "v_perm_b32 v45, v95, v91, s63\n\t" \
  "v_perm_b32 v46, v103, v99, s63\n\t" \
  "v_perm_b32 v47, v111, v107, s63\n\t" \
  "v_accvgpr_write_b32 a220, v44\n\t" \
  "v_accvgpr_write_b32 a221, v45\n\t" \
  "v_accvgpr_write_b32 a222, v46\n\t" \
  "v_accvgpr_write_b32 a223, v47\n\t" \
  "s_xor_b32 s60, s66, 5\n\t" \
  "s_mul_i32 s60, s60, 36864\n\t" \
  "s_add_u32 s61, s60, 18432\n\t" \
  "buffer_load_ushort v244, v42, %[W], s60 offen offset:0\n\t" \
  "buffer_load_ushort v164, v42, %[W], s60 offen offset:1152\n\t" \
  "buffer_load_ushort v245, v42, %[W], s60 offen offset:2304\n\t" \
  "buffer_load_ushort v165, v42, %[W], s60 offen offset:3456\n\t" \
  "buffer_load_ushort v246, v42, %[W], s61 offen offset:0\n\t" \
  "buffer_load_ushort v166, v42, %[W], s61 offen offset:1152\n\t" \
  "buffer_load_ushort v247, v42, %[W], s61 offen offset:2304\n\t" \
  "buffer_load_ushort v167, v42, %[W], s61 offen offset:3456\n\t" \
  "s_waitcnt vmcnt(54)\n\t" \
  "v_perm_b32 v44, v116, v112, s62\n\t" \
  "s_waitcnt vmcnt(52)\n\t" \
  "v_perm_b32 v45, v124, v120, s62\n\t" \
  "s_waitcnt vmcnt(50)\n\t" \
  "v_perm_b32 v46, v132, v128, s62\n\t" \
  "s_waitcnt vmcnt(48)\n\t" \
  "v_perm_b32 v47, v140, v136, s62\n\t" \
  "v_accvgpr_write_b32 a224, v44\n\t" \
  "v_accvgpr_write_b32 a225, v45\n\t" \
  "v_accvgpr_write_b32 a226, v46\n\t" \
  "v_accvgpr_write_b32 a227, v47\n\t" \
  "v_perm_b32 v44, v116, v112, s63\n\t" \
  "v_perm_b32 v45, v124, v120, s63\n\t" \
  "v_perm_b32 v46, v132, v128, s63\n\t" \
  "v_perm_b32 v47, v140, v136, s63\n\t" \
  "v_accvgpr_write_b32 a228, v44\n\t" \
  "v_accvgpr_write_b32 a229, v45\n\t" \
  "v_accvgpr_write_b32 a230, v46\n\t" \
  "v_accvgpr_write_b32 a231, v47\n\t" \
  "v_perm_b32 v44, v117, v113, s62\n\t" \
  "v_perm_b32 v45, v125, v121, s62\n\t" \
  "v_perm_b32 v46, v133, v129, s62\n\t" \
  "v_perm_b32 v47, v141, v137, s62\n\t" \
  "v_accvgpr_write_b32 a232, v44\n\t" \
  "v_accvgpr_write_b32 a233, v45\n\t" \
  "v_accvgpr_write_b32 a234, v46\n\t" \
  "v_accvgpr_write_b32 a235, v47\n\t" \
  "v_perm_b32 v44, v117, v113, s63\n\t" \
  "v_perm_b32 v45, v125, v121, s63\n\t" \
  "v_perm_b32 v46, v133, v129, s63\n\t" \
  "v_perm_b32 v47, v141, v137, s63\n\t" \
  "v_accvgpr_write_b32 a236, v44\n\t" \
  "v_accvgpr_write_b32 a237, v45\n\t" \
  "v_accvgpr_write_b32 a238, v46\n\t" \
  "v_accvgpr_write_b32 a239, v47\n\t" \
  "v_perm_b32 v44, v118, v114, s62\n\t" \
  "v_perm_b32 v45, v126, v122, s62\n\t" \
  "v_perm_b32 v46, v134, v130, s62\n\t" \
  "v_perm_b32 v47, v142, v138, s62\n\t" \
  "v_accvgpr_write_b32 a240, v44\n\t" \
  "v_accvgpr_write_b32 a241, v45\n\t" \
  "v_accvgpr_write_b32 a242, v46\n\t" \
  "v_accvgpr_write_b32 a243, v47\n\t" \
  "v_perm_b32 v44, v118, v114, s63\n\t" \
  "v_perm_b32 v45, v126, v122, s63\n\t" \
  "v_perm_b32 v46, v134, v130, s63\n\t" \
  "v_perm_b32 v47, v142, v138, s63\n\t" \
  "v_accvgpr_write_b32 a244, v44\n\t" \
  "v_accvgpr_write_b32 a245, v45\n\t" \
  "v_accvgpr_write_b32 a246, v46\n\t" \
  "v_accvgpr_write_b32 a247, v47\n\t" \
  "v_perm_b32 v44, v119, v115, s62\n\t" \
  "v_perm_b32 v45, v127, v123, s62\n\t" \
  "v_perm_b32 v46, v135, v131, s62\n\t" \
  "v_perm_b32 v47, v143, v139, s62\n\t" \
  "v_accvgpr_write_b32 a248, v44\n\t" \
  "v_accvgpr_write_b32 a249, v45\n\t" \
  "v_accvgpr_write_b32 a250, v46\n\t" \
  "v_accvgpr_write_b32 a251, v47\n\t" \
  "v_perm_b32 v44, v119, v115, s63\n\t" \
  "v_perm_b32 v45, v127, v123, s63\n\t" \
  "v_perm_b32 v46, v135, v131, s63\n\t" \
  "v_perm_b32 v47, v143, v139, s63\n\t" \
  "v_accvgpr_write_b32 a252, v44\n\t" \
  "v_accvgpr_write_b32 a253, v45\n\t" \
  "v_accvgpr_write_b32 a254, v46\n\t" \
  "v_accvgpr_write_b32 a255, v47\n\t" \
  "s_xor_b32 s60, s66, 6\n\t" \
  "s_mul_i32 s60, s60, 36864\n\t" \
  "s_add_u32 s61, s60, 18432\n\t" \
  "buffer_load_ushort v248, v42, %[W], s60 offen offset:0\n\t" \
  "buffer_load_ushort v168, v42, %[W], s60 offen offset:1152\n\t" \
  "buffer_load_ushort v249, v42, %[W], s60 offen offset:2304\n\t" \
  "buffer_load_ushort v169, v42, %[W], s60 offen offset:3456\n\t" \
  "buffer_load_ushort v250, v42, %[W], s61 offen offset:0\n\t" \
  "buffer_load_ushort v170, v42, %[W], s61 offen offset:1152\n\t" \
  "buffer_load_ushort v251, v42, %[W], s61 offen offset:2304\n\t" \
  "buffer_load_ushort v171, v42, %[W], s61 offen offset:3456\n\t" \
  "s_xor_b32 s60, s66, 7\n\t" \
  "s_mul_i32 s60, s60, 36864\n\t" \
  "s_add_u32 s61, s60, 18432\n\t" \
  "s_waitcnt vmcnt(40)\n\t" \
  "buffer_load_ushort v252, v42, %[W], s60 offen offset:0\n\t" \
  "buffer_load_ushort v172, v42, %[W], s60 offen offset:1152\n\t" \
  "buffer_load_ushort v253, v42, %[W], s60 offen offset:2304\n\t" \
  "buffer_load_ushort v173, v42, %[W], s60 offen offset:3456\n\t" \
  "buffer_load_ushort v254, v42, %[W], s61 offen offset:0\n\t" \
  "buffer_load_ushort v174, v42, %[W], s61 offen offset:1152\n\t" \
  "buffer_load_ushort v255, v42, %[W], s61 offen offset:2304\n\t" \
  "buffer_load_ushort v175, v42, %[W], s61 offen offset:3456\n\t" \
  "v_lshl_or_b32 v224, v144, 16, v224\n\t" \
  "v_lshl_or_b32 v225, v145, 16, v225\n\t" \
  "v_lshl_or_b32 v226, v146, 16, v226\n\t" \
  "v_lshl_or_b32 v227, v147, 16, v227\n\t" \
  "v_lshl_or_b32 v228, v148, 16, v228\n\t" \
  "v_lshl_or_b32 v229, v149, 16, v229\n\t" \
  "v_lshl_or_b32 v230, v150, 16, v230\n\t" \
  "v_lshl_or_b32 v231, v151, 16, v231\n\t" \
  "s_waitcnt vmcnt(46)\n\t" \
  "v_lshl_or_b32 v232, v152, 16, v232\n\t" \
  "s_waitcnt vmcnt(44)\n\t" \
  "v_lshl_or_b32 v233, v153, 16, v233\n\t" \
  "s_waitcnt vmcnt(42)\n\t" \
  "v_lshl_or_b32 v234, v154, 16, v234\n\t" \
  "s_waitcnt vmcnt(40)\n\t" \
  "v_lshl_or_b32 v235, v155, 16, v235\n\t" \
  "s_waitcnt vmcnt(38)\n\t" \
  "v_lshl_or_b32 v236, v156, 16, v236\n\t" \
  "s_waitcnt vmcnt(36)\n\t" \
  "v_lshl_or_b32 v237, v157, 16, v237\n\t" \
  "s_waitcnt vmcnt(34)\n\t" \
  "v_lshl_or_b32 v238, v158, 16, v238\n\t" \
  "s_waitcnt vmcnt(32)\n\t" \
  "v_lshl_or_b32 v239, v159, 16, v239\n\t" \
  "s_waitcnt vmcnt(30)\n\t" \
  "v_lshl_or_b32 v240, v160, 16, v240\n\t" \
  "s_waitcnt vmcnt(28)\n\t" \
  "v_lshl_or_b32 v241, v161, 16, v241\n\t" \
  "s_waitcnt vmcnt(26)\n\t" \
  "v_lshl_or_b32 v242, v162, 16, v242\n\t" \
  "s_waitcnt vmcnt(24)\n\t" \
  "v_lshl_or_b32 v243, v163, 16, v243\n\t" \
  "s_waitcnt vmcnt(22)\n\t" \
  "v_lshl_or_b32 v244, v164, 16, v244\n\t" \
  "s_waitcnt vmcnt(20)\n\t" \
  "v_lshl_or_b32 v245, v165, 16, v245\n\t" \
  "s_waitcnt vmcnt(18)\n\t" \
  "v_lshl_or_b32 v246, v166, 16, v246\n\t" \
  "s_waitcnt vmcnt(16)\n\t" \
  "v_lshl_or_b32 v247, v167, 16, v247\n\t" \
  "s_waitcnt vmcnt(14)\n\t" \
  "v_lshl_or_b32 v248, v168, 16, v248\n\t" \
  "s_waitcnt vmcnt(12)\n\t" \
  "v_lshl_or_b32 v249, v169, 16, v249\n\t" \
  "s_waitcnt vmcnt(10)\n\t" \
  "v_lshl_or_b32 v250, v170, 16, v250\n\t" \
  "s_waitcnt vmcnt(8)\n\t" \
  "v_lshl_or_b32 v251, v171, 16, v251\n\t" \
  "s_waitcnt vmcnt(6)\n\t" \
  "v_lshl_or_b32 v252, v172, 16, v252\n\t" \
  "s_waitcnt vmcnt(4)\n\t" \
  "v_lshl_or_b32 v253, v173, 16, v253\n\t" \
  "s_waitcnt vmcnt(2)\n\t" \
  "v_lshl_or_b32 v254, v174, 16, v254\n\t" \
  "s_waitcnt vmcnt(0)\n\t" \
  "v_lshl_or_b32 v255, v175, 16, v255\n\t" \
  "s_mov_b32 s84, " TK_CAP "\n\t" \
  "s_mov_b32 s48, 0\n\t" \
  "s_mov_b32 s49, 0xf0000000\n\t" \
  "s_mov_b32 s46, 0\n\t" \
  "v_mov_b32 v5, -1\n\t" \
  "v_lshrrev_b32 v13, 2, v0\n\t" \
  "s_mov_b32 s60, " TK_CTAB "\n\t" \
  "buffer_load_dword v10, v13, %[X], s60 offen offset:88 sc1\n\t" \
  "buffer_load_dword v13, v13, %[X], s60 offen offset:0 sc1\n\t" \
  "s_waitcnt vmcnt(0)\n\t" \
  "v_and_b32 v10, 1, v10\n\t" \
  "v_and_b32 v13, 1, v13\n\t" \
  "v_cmp_ne_u32 s[90:91], s67, v10\n\t" \
  "v_cmp_ne_u32 s[88:89], s67, v13\n\t" \
  "s_nop 1\n\t" \
  "s_mov_b32 s86, s88\n\t" \
  "v_lshrrev_b32 v13, 2, v0\n\t" \
  "s_mov_b32 s60, " TK_CTAB "\n\t" \
  "buffer_load_dword v10, v13, %[X], s60 offen offset:88 sc1\n\t" \
  "buffer_load_dword v13, v13, %[X], s60 offen offset:344 sc1\n\t" \
  "s_waitcnt vmcnt(0)\n\t" \
  "v_and_b32 v10, 1, v10\n\t" \
  "v_and_b32 v13, 1, v13\n\t" \
  "v_cmp_ne_u32 s[90:91], s67, v10\n\t" \
  "v_cmp_ne_u32 s[88:89], s67, v13\n\t" \
  "s_nop 1\n\t" \
  "s_and_b32 s60, s88, 256\n\t" \
  "s_lshl_b32 s60, s60, 5\n\t" \
  "s_add_u32 s44, s44, s60\n\t" \
  "s_add_u32 s87, s45, 4096\n\t" \
  "s_add_u32 s89, s45, 12288\n\t" \
  "s_add_u32 s83, s48, 0\n\t" \
  "s_lshr_b64 s[60:61], s[90:91], s83\n\t" \
  "s_and_b32 s60, s60, 1\n\t" \
  "s_lshl_b32 s60, s60, 13\n\t" \
  "s_add_u32 s83, s83, " TK_SG0 "\n\t" \
  "s_lshl_b32 s83, s83, 21\n\t" \
  "s_or_b32 s83, s83, s60\n\t" \
  "s_mov_b32 s81, s83\n\t" \
  "LCOLD_%=:\n\t" \
  "s_add_u32 s62, s83, s41\n\t" \
  "s_mov_b32 s61, 0\n\t" \
  "LPQ_%=:\n\t" \
  "buffer_load_dword v10, v9, %[X], s62 offen sc1\n\t" \
  "s_waitcnt vmcnt(0)\n\t" \
  "v_cmp_eq_u32 vcc, 0, v10\n\t" \
  "s_cbranch_vccz LPQ_%=x\n\t" \
  "s_add_u32 s61, s61, 1\n\t" \
  "s_sleep 4\n\t" \
  "s_cmp_gt_u32 s61, s84\n\t" \
  "s_cbranch_scc1 LPQ_%=g\n\t" \
  "s_and_b32 s60, s61, 1023\n\t" \
  "s_cmp_lg_u32 s60, 0\n\t" \
  "s_cbranch_scc1 LPQ_%=\n\t" \
  "buffer_load_dword v10, off, %[X], s92 sc1\n\t" \
  "s_waitcnt vmcnt(0)\n\t" \
  "v_readfirstlane_b32 s60, v10\n\t" \
  "s_nop 0\n\t" \
  "s_cmp_eq_u32 s60, 0\n\t" \
  "s_cbranch_scc1 LPQ_%=\n\t" \
  "LPQ_%=g:\n\t" \
  "s_mov_b32 s84, 0\n\t" \
  "s_or_b32 s60, s48, 1073741824\n\t" \
  "v_mov_b32 v10, s60\n\t" \
  "s_mov_b64 exec, 1\n\t" \
  "buffer_store_dword v10, off, %[X], s92 sc1\n\t" \
  "s_mov_b64 exec, -1\n\t" \
  "LPQ_%=x:\n\t" \
  "s_add_u32 m0, s46, s85\n\t" \
  "s_add_u32 s47, s81, s36\n\t" \
  "buffer_load_dwordx4 v0, %[X], s47 offen offset:0 sc1 lds\n\t" \
  "buffer_load_dwordx4 v0, %[X], s47 offen offset:1024 sc1 lds\n\t" \
  "s_add_u32 m0, s46, s87\n\t" \
  "s_add_u32 s47, s81, s37\n\t" \
  "buffer_load_dwordx4 v0, %[X], s47 offen offset:0 sc1 lds\n\t" \
  "buffer_load_dwordx4 v0, %[X], s47 offen offset:1024 sc1 lds\n\t" \
  "buffer_load_dwordx4 v0, %[X], s47 offen offset:2048 sc1 lds\n\t" \
  "buffer_load_dwordx4 v0, %[X], s47 offen offset:3072 sc1 lds\n\t" \
  "s_add_u32 m0, s46, s89\n\t" \
  "s_add_u32 s47, s81, s38\n\t" \
  "buffer_load_dwordx4 v0, %[X], s47 offen offset:0 sc1 lds\n\t" \
  "buffer_load_dwordx4 v0, %[X], s47 offen offset:1024 sc1 lds\n\t" \
  "buffer_load_dwordx4 v0, %[X], s47 offen offset:2048 sc1 lds\n\t" \
  "buffer_load_dwordx4 v0, %[X], s47 offen offset:3072 sc1 lds\n\t" \
  "s_waitcnt vmcnt(0)\n\t" \
  "LTOP_%=:\n\t" \
  "s_memrealtime s[78:79]\n\t" \
  "v_readfirstlane_b32 s76, v5\n\t" \
  "s_waitcnt lgkmcnt(0)\n\t" \
  "s_add_u32 s60, s76, 1\n\t" \
  "s_not_b32 s76, s76\n\t" \
  "s_cmp_gt_u32 s60, 1\n\t" \
  "s_cselect_b32 s61, -1, 0\n\t" \
  "s_lshl_b32 s60, s78, 8\n\t" \
  "s_sub_u32 s76, s76, s60\n\t" \
  "s_ashr_i32 s76, s76, 8\n\t" \
  "s_add_i32 s76, s76, s77\n\t" \
  "s_min_i32 s76, s76, 400\n\t" \
  "s_and_b32 s76, s76, s61\n\t" \
  "s_add_u32 s57, s78, s76\n\t" \
  "s_barrier\n\t" \
  "ds_read_b128 v[48:51], v1 offset:0\n\t" \
  "ds_read_b128 v[64:67], v1 offset:4096\n\t" \
  "ds_read_b128 v[80:83], v1 offset:8192\n\t" \
  "ds_read_b128 v[52:55], v1 offset:1024\n\t" \
  "ds_read_b128 v[68:71], v1 offset:5120\n\t" \
  "ds_read_b128 v[84:87], v1 offset:9216\n\t" \
  "ds_read_b128 v[56:59], v1 offset:2048\n\t" \
  "ds_read_b128 v[72:75], v1 offset:6144\n\t" \
  "ds_read_b128 v[88:91], v1 offset:10240\n\t" \
  "ds_read_b128 v[60:63], v1 offset:3072\n\t" \
  "ds_read_b128 v[76:79], v1 offset:7168\n\t" \
  "ds_read_b128 v[92:95], v1 offset:11264\n\t" \
  "s_mov_b32 s82, s83\n\t" \
  "s_xor_b32 s46, s46, 0x10000\n\t" \
  "s_add_u32 s58, s82, s41\n\t" \
  "s_add_u32 s58, s58, 4096\n\t" \
  "s_add_u32 s59, s81, s39\n\t" \
  "s_cmp_gt_i32 s76, 0\n\t" \
  "s_cselect_b32 s62, 0xf0000000, s58\n\t" \
  "buffer_load_dword v5, off, %[X], s62 sc1\n\t" \
  "s_cmp_gt_i32 s76, " TK_AM "\n\t" \
  "s_cselect_b32 s79, 0xf0000000, s58\n\t" \
  "s_waitcnt lgkmcnt(10)\n\t" \
  "v_mfma_f32_16x16x32_bf16 v[36:39], v[64:67], v[48:51], v[16:19]\n\t" \
  "ds_read_b128 v[96:99], v2 offset:0\n\t" \
  "s_waitcnt lgkmcnt(10)\n\t" \
  "v_mfma_f32_16x16x32_bf16 v[40:43], v[80:83], v[48:51], v[20:23]\n\t" \
  "s_waitcnt lgkmcnt(8)\n\t" \
  "v_mfma_f32_16x16x32_bf16 v[36:39], v[68:71], v[52:55], v[36:39]\n\t" \
  "ds_read_b128 v[100:103], v2 offset:1024\n\t" \
  "s_waitcnt lgkmcnt(8)\n\t" \
  "v_mfma_f32_16x16x32_bf16 v[40:43], v[84:87], v[52:55], v[40:43]\n\t" \
  "s_waitcnt lgkmcnt(6)\n\t" \
  "v_mfma_f32_16x16x32_bf16 v[36:39], v[72:75], v[56:59], v[36:39]\n\t" \
  "ds_read_b128 v[104:107], v2 offset:2048\n\t" \
  "s_waitcnt lgkmcnt(6)\n\t" \
  "v_mfma_f32_16x16x32_bf16 v[40:43], v[88:91], v[56:59], v[40:43]\n\t" \
  "s_waitcnt lgkmcnt(4)\n\t" \
  "v_mfma_f32_16x16x32_bf16 v[36:39], v[76:79], v[60:63], v[36:39]\n\t" \
  "ds_read_b128 v[108:111], v2 offset:3072\n\t" \
  "s_waitcnt lgkmcnt(4)\n\t" \
  "v_mfma_f32_16x16x32_bf16 v[40:43], v[92:95], v[60:63], v[40:43]\n\t" \
  "s_add_u32 s83, s48, 1\n\t" \
  "s_lshr_b64 s[60:61], s[90:91], s83\n\t" \
  "s_and_b32 s60, s60, 1\n\t" \
  "s_lshl_b32 s60, s60, 13\n\t" \
  "s_add_u32 s83, s83, " TK_SG0 "\n\t" \
  "s_lshl_b32 s83, s83, 21\n\t" \
  "s_or_b32 s83, s83, s60\n\t" \
  "s_add_u32 s56, s48, 1\n\t" \
  "s_and_b32 s56, s56, " TK_TRM "\n\t" \
  "s_lshr_b64 s[60:61], s[90:91], s56\n\t" \
  "s_and_b32 s60, s60, 1\n\t" \
  "s_lshl_b32 s60, s60, 13\n\t" \
  "s_add_u32 s56, s56, " TK_SG0 "\n\t" \
  "s_lshl_b32 s56, s56, 21\n\t" \
  "s_or_b32 s56, s56, s60\n\t" \
  "v_xor_b32 v1, 0x10000, v1\n\t" \
  "v_xor_b32 v2, 0x10000, v2\n\t" \
  "v_max3_f32 v11, v36, v37, v38\n\t" \
  "v_max3_f32 v11, v11, v39, v40\n\t" \
  "v_max3_f32 v13, v41, v42, v43\n\t" \
  "v_max_f32 v11, v11, v13\n\t" \
  "v_mov_b32 v13, v11\n\t" \
  "s_nop 1\n\t" \
  "v_permlane16_swap_b32 v11, v13\n\t" \
  "v_max_f32 v11, v11, v13\n\t" \
  "v_mov_b32 v13, v11\n\t" \
  "s_nop 1\n\t" \
  "v_permlane32_swap_b32 v11, v13\n\t" \
  "v_max_f32 v11, v11, v13\n\t" \
  "v_mul_f32 v13, 0xbe0293ee, v11\n\t" \
  "v_fmamk_f32 v36, v36, 0x3e0293ee, v13\n\t" \
  "v_fmamk_f32 v37, v37, 0x3e0293ee, v13\n\t" \
  "v_fmamk_f32 v38, v38, 0x3e0293ee, v13\n\t" \
  "v_fmamk_f32 v39, v39, 0x3e0293ee, v13\n\t" \
  "v_fmamk_f32 v40, v40, 0x3e0293ee, v13\n\t" \
  "v_fmamk_f32 v41, v41, 0x3e0293ee, v13\n\t" \
  "v_fmamk_f32 v42, v42, 0x3e0293ee, v13\n\t" \
  "v_fmamk_f32 v43, v43, 0x3e0293ee, v13\n\t" \
  "s_waitcnt vmcnt(0)\n\t" \
  "v_readfirstlane_b32 s76, v5\n\t" \
  "s_barrier\n\t" \
  "s_cmp_eq_u32 s76, 0\n\t" \
  "s_cbranch_scc1 LNG_%=\n\t" \
  "buffer_load_dwordx4 v[196:199], v0, %[X], s59 offen offset:0 sc1\n\t" \
  "buffer_load_dwordx4 v[200:203], v0, %[X], s59 offen offset:1024 sc1\n\t" \
  "buffer_load_dwordx2 v[204:205], v6, %[X], s59 offen offset:2048 sc1\n\t" \
  "buffer_load_dwordx2 v[206:207], v6, %[X], s59 offen offset:2560 sc1\n\t" \
  "LNG_%=:\n\t" \
  "buffer_load_dword v5, off, %[X], s79 sc1\n\t" \
  "s_mov_b64 exec, 1\n\t" \
  "buffer_store_dword v3, off, %[X], s49 sc1\n\t" \
  "s_mov_b64 exec, -1\n\t" \
  "s_mov_b32 s49, 0xf0000000\n\t" \
  "v_exp_f32 v36, v36\n\t" \
  "v_exp_f32 v37, v37\n\t" \
  "v_exp_f32 v38, v38\n\t" \
  "v_exp_f32 v39, v39\n\t" \
  "v_exp_f32 v40, v40\n\t" \
  "v_exp_f32 v41, v41\n\t" \
  "v_exp_f32 v42, v42\n\t" \
  "v_exp_f32 v43, v43\n\t" \
  "v_cvt_pk_bf16_f32 v44, v36, v37\n\t" \
  "v_cvt_pk_bf16_f32 v45, v38, v39\n\t" \
  "v_cvt_pk_bf16_f32 v46, v40, v41\n\t" \
  "v_cvt_pk_bf16_f32 v47, v42, v43\n\t" \
  "v_add_f32 v36, v36, v37\n\t" \
  "v_add_f32 v38, v38, v39\n\t" \
  "s_waitcnt lgkmcnt(3)\n\t" \
  "v_mfma_f32_16x16x32_bf16 v[128:131], v[96:99], v[44:47], 0\n\t" \
  "v_add_f32 v40, v40, v41\n\t" \
  "v_add_f32 v42, v42, v43\n\t" \
  "s_and_b32 s62, s48, 15\n\t" \
  "s_lshr_b32 s63, s62, 1\n\t" \
  "s_lshr_b32 s60, s88, s63\n\t" \
  "s_and_b32 s60, s60, 1\n\t" \
  "s_waitcnt lgkmcnt(2)\n\t" \
  "v_mfma_f32_16x16x32_bf16 v[132:135], v[100:103], v[44:47], 0\n\t" \
  "v_add_f32 v36, v36, v38\n\t" \
  "v_add_f32 v40, v40, v42\n\t" \
  "s_lshl_b32 s60, s60, 13\n\t" \
  "s_lshl_b32 s61, s62, 20\n\t" \
  "s_add_u32 s50, s61, s60\n\t" \
  "s_add_u32 s50, s50, s43\n\t" \
  "s_waitcnt lgkmcnt(1)\n\t" \
  "v_mfma_f32_16x16x32_bf16 v[136:139], v[104:107], v[44:47], 0\n\t" \
  "v_add_f32 v12, v36, v40\n\t" \
  "s_add_u32 s51, s50, 32768\n\t" \
  "s_add_u32 s52, s51, 32768\n\t" \
  "s_add_u32 s53, s52, 32768\n\t" \
  "s_lshl_b32 s61, s62, 17\n\t" \
  "s_waitcnt lgkmcnt(0)\n\t" \
  "v_mfma_f32_16x16x32_bf16 v[140:143], v[108:111], v[44:47], 0\n\t" \
  "s_add_u32 s54, s61, s44\n\t" \
  "s_add_u32 s63, s83, s41\n\t" \
  "s_cmp_lg_u32 s76, 0\n\t" \
  "s_cbranch_scc1 LGOT_%=\n\t" \
  "s_waitcnt vmcnt(1)\n\t" \
  "v_readfirstlane_b32 s60, v5\n\t" \
  "s_nop 0\n\t" \
  "s_cmp_lg_u32 s60, 0\n\t" \
  "s_cbranch_scc1 LPK_%=x\n\t" \
  "LKW_%=:\n\t" \
  "s_memrealtime s[78:79]\n\t" \
  "s_waitcnt lgkmcnt(0)\n\t" \
  "s_sub_i32 s79, s78, s57\n\t" \
  "s_cmp_lt_i32 s79, 0\n\t" \
  "s_cbranch_scc1 LKW_%=\n\t" \
  "s_mov_b32 s61, 0\n\t" \
  "LPK_%=:\n\t" \
  "buffer_load_dword v5, off, %[X], s58 sc1\n\t" \
  "s_waitcnt vmcnt(0)\n\t" \
  "v_cmp_eq_u32 vcc, 0, v5\n\t" \
  "s_cbranch_vccz LPK_%=x\n\t" \
  "s_add_u32 s61, s61, 1\n\t" \
  "s_sleep " TK_PSK "\n\t" \
  "s_cmp_gt_u32 s61, s84\n\t" \
  "s_cbranch_scc1 LPK_%=g\n\t" \
  "s_and_b32 s60, s61, 1023\n\t" \
  "s_cmp_lg_u32 s60, 0\n\t" \
  "s_cbranch_scc1 LPK_%=\n\t" \
  "buffer_load_dword v5, off, %[X], s92 sc1\n\t" \
  "s_waitcnt vmcnt(0)\n\t" \
  "v_readfirstlane_b32 s60, v5\n\t" \
  "s_nop 0\n\t" \
  "s_cmp_eq_u32 s60, 0\n\t" \
  "s_cbranch_scc1 LPK_%=\n\t" \
  "LPK_%=g:\n\t" \
  "s_mov_b32 s84, 0\n\t" \
  "s_or_b32 s60, s48, 1342177280\n\t" \
  "v_mov_b32 v5, s60\n\t" \
  "s_mov_b64 exec, 1\n\t" \
  "buffer_store_dword v5, off, %[X], s92 sc1\n\t" \
  "s_mov_b64 exec, -1\n\t" \
  "LPK_%=x:\n\t" \
  "buffer_load_dwordx4 v[196:199], v0, %[X], s59 offen offset:0 sc1\n\t" \
  "buffer_load_dwordx4 v[200:203], v0, %[X], s59 offen offset:1024 sc1\n\t" \
  "buffer_load_dwordx2 v[204:205], v6, %[X], s59 offen offset:2048 sc1\n\t" \
  "buffer_load_dwordx2 v[206:207], v6, %[X], s59 offen offset:2560 sc1\n\t" \
  "LGOT_%=:\n\t" \
  "buffer_load_dword v10, v9, %[X], s63 offen sc1\n\t" \
  "s_add_u32 s49, s82, s42\n\t" \
  "s_waitcnt vmcnt(4)\n\t" \
  "v_mfma_f32_16x16x32_bf16 v[36:39], v[196:199], v[48:51], 0\n\t" \
  "v_mfma_f32_16x16x32_bf16 v[40:43], v[196:199], v[52:55], 0\n\t" \
  "s_waitcnt vmcnt(3)\n\t" \
  "v_mfma_f32_16x16x32_bf16 v[36:39], v[200:203], v[56:59], v[36:39]\n\t" \
  "v_mfma_f32_16x16x32_bf16 v[40:43], v[200:203], v[60:63], v[40:43]\n\t" \
  "s_nop 8\n\t" \
  "v_add_f32 v36, v36, v42\n\t" \
  "v_add_f32 v37, v37, v43\n\t" \
  "v_max3_f32 v14, v36, v37, v11\n\t" \
  "v_mov_b32 v13, v14\n\t" \
  "s_nop 1\n\t" \
  "v_permlane16_swap_b32 v14, v13\n\t" \
  "v_max_f32 v14, v14, v13\n\t" \
  "v_mov_b32 v13, v14\n\t" \
  "s_nop 1\n\t" \
  "v_permlane32_swap_b32 v14, v13\n\t" \
  "v_max_f32 v14, v14, v13\n\t" \
  "v_mul_f32 v14, 0xbe0293ee, v14\n\t" \
  "v_fmamk_f32 v11, v11, 0x3e0293ee, v14\n\t" \
  "v_fmamk_f32 v36, v36, 0x3e0293ee, v14\n\t" \
  "v_fmamk_f32 v37, v37, 0x3e0293ee, v14\n\t" \
  "v_exp_f32 v11, v11\n\t" \
  "v_exp_f32 v36, v36\n\t" \
  "v_exp_f32 v37, v37\n\t" \
  "v_fma_f32 v12, v12, v11, v36\n\t" \
  "v_add_f32 v12, v12, v37\n\t" \
  "v_mov_b32 v13, v12\n\t" \
  "s_nop 1\n\t" \
  "v_permlane16_swap_b32 v12, v13\n\t" \
  "v_add_f32 v12, v12, v13\n\t" \
  "v_mov_b32 v13, v12\n\t" \
  "s_nop 1\n\t" \
  "v_permlane32_swap_b32 v12, v13\n\t" \
  "v_add_f32 v12, v12, v13\n\t" \
  "v_rcp_f32 v12, v12\n\t" \
  "s_nop 0\n\t" \
  "v_mul_f32 v14, v11, v12\n\t" \
  "v_mul_f32 v36, v36, v12\n\t" \
  "v_mul_f32 v37, v37, v12\n\t" \
  "v_cvt_pk_bf16_f32 v26, v36, v37\n\t" \
  "v_cvt_pk_bf16_f32 v33, v36, v37\n\t" \
  "v_pk_mul_f32 v[128:129], v[128:129], v[14:15] op_sel_hi:[1,0]\n\t" \
  "v_pk_mul_f32 v[130:131], v[130:131], v[14:15] op_sel_hi:[1,0]\n\t" \
  "v_pk_mul_f32 v[132:133], v[132:133], v[14:15] op_sel_hi:[1,0]\n\t" \
  "v_pk_mul_f32 v[134:135], v[134:135], v[14:15] op_sel_hi:[1,0]\n\t" \
  "v_pk_mul_f32 v[136:137], v[136:137], v[14:15] op_sel_hi:[1,0]\n\t" \
  "v_pk_mul_f32 v[138:139], v[138:139], v[14:15] op_sel_hi:[1,0]\n\t" \
  "v_pk_mul_f32 v[140:141], v[140:141], v[14:15] op_sel_hi:[1,0]\n\t" \
  "v_pk_mul_f32 v[142:143], v[142:143], v[14:15] op_sel_hi:[1,0]\n\t" \
  "s_waitcnt vmcnt(1)\n\t" \
  "v_mfma_f32_16x16x32_bf16 v[128:131], v[204:207], v[26:29], v[128:131]\n\t" \
  "v_mfma_f32_16x16x32_bf16 v[132:135], v[204:207], v[32:35], v[132:135]\n\t" \
  "v_mfma_f32_16x16x32_bf16 v[136:139], v[204:207], v[24:27], v[136:139]\n\t" \
  "v_mfma_f32_16x16x32_bf16 v[140:143], v[204:207], v[30:33], v[140:143]\n\t" \
  "v_cvt_pk_bf16_f32 v64, v128, v129\n\t" \
  "v_cvt_pk_bf16_f32 v65, v130, v131\n\t" \
  "v_cvt_pk_bf16_f32 v66, v132, v133\n\t" \
  "v_cvt_pk_bf16_f32 v67, v134, v135\n\t" \
  "s_nop 1\n\t" \
  "v_mfma_f32_16x16x32_bf16 v[192:195], v[224:227], v[64:67], 0\n\t" \
  "ds_write_b128 v3, v[64:67] offset:0\n\t" \
  "v_cvt_pk_bf16_f32 v68, v136, v137\n\t" \
  "v_cvt_pk_bf16_f32 v69, v138, v139\n\t" \
  "v_mfma_f32_16x16x32_bf16 v[160:163], v[64:67], a[0:3], 0\n\t" \
  "v_cvt_pk_bf16_f32 v70, v140, v141\n\t" \
  "v_cvt_pk_bf16_f32 v71, v142, v143\n\t" \
  "v_mfma_f32_16x16x32_bf16 v[164:167], v[64:67], a[4:7], 0\n\t" \
  "ds_write_b128 v3, v[68:71] offset:1024\n\t" \
  "v_mfma_f32_16x16x32_bf16 v[168:171], v[64:67], a[8:11], 0\n\t" \
  "v_xor_b32 v13, 0x1000, v3\n\t" \
  "v_xor_b32 v15, 0x1800, v3\n\t" \
  "v_mfma_f32_16x16x32_bf16 v[172:175], v[64:67], a[12:15], 0\n\t" \
  "v_mfma_f32_16x16x32_bf16 v[176:179], v[64:67], a[16:19], 0\n\t" \
  "v_mfma_f32_16x16x32_bf16 v[180:183], v[64:67], a[20:23], 0\n\t" \
  "v_mfma_f32_16x16x32_bf16 v[184:187], v[64:67], a[24:27], 0\n\t" \
  "s_waitcnt lgkmcnt(0)\n\t" \
  "s_barrier\n\t" \
  "v_mfma_f32_16x16x32_bf16 v[188:191], v[64:67], a[28:31], 0\n\t" \
  "ds_read_b128 v[72:75], v4 offset:0\n\t" \
  "v_mfma_f32_16x16x32_bf16 v[192:195], v[228:231], v[68:71], v[192:195]\n\t" \
  "ds_read_b128 v[76:79], v4 offset:1024\n\t" \
  "v_mfma_f32_16x16x32_bf16 v[160:163], v[68:71], a[32:35], v[160:163]\n\t" \
  "s_waitcnt vmcnt(0)\n\t" \
  "v_cmp_eq_u32 vcc, 0, v10\n\t" \
  "v_mfma_f32_16x16x32_bf16 v[164:167], v[68:71], a[36:39], v[164:167]\n\t" \
  "s_cmp_eq_u64 vcc, 0\n\t" \
  "s_cselect_b32 s81, s56, 0xf0000000\n\t" \
  "s_cmp_eq_u32 s48, 63\n\t" \
  "v_mfma_f32_16x16x32_bf16 v[168:171], v[68:71], a[40:43], v[168:171]\n\t" \
  "s_cselect_b32 s81, 0xf0000000, s81\n\t" \
  "v_mfma_f32_16x16x32_bf16 v[172:175], v[68:71], a[44:47], v[172:175]\n\t" \
  "ds_read_b128 v[80:83], v13 offset:0\n\t" \
  "v_mfma_f32_16x16x32_bf16 v[176:179], v[68:71], a[48:51], v[176:179]\n\t" \
  "v_mfma_f32_16x16x32_bf16 v[180:183], v[68:71], a[52:55], v[180:183]\n\t" \
  "ds_read_b128 v[84:87], v13 offset:1024\n\t" \
  "v_mfma_f32_16x16x32_bf16 v[184:187], v[68:71], a[56:59], v[184:187]\n\t" \
  "ds_read_b128 v[88:91], v15 offset:0\n\t" \
  "v_mfma_f32_16x16x32_bf16 v[188:191], v[68:71], a[60:63], v[188:191]\n\t" \
  "ds_read_b128 v[92:95], v15 offset:1024\n\t" \
  "s_waitcnt lgkmcnt(5)\n\t" \
  "v_mfma_f32_16x16x32_bf16 v[192:195], v[232:235], v[72:75], v[192:195]\n\t" \
  "s_add_u32 m0, s46, s85\n\t" \
  "s_add_u32 s47, s81, s36\n\t" \
  "buffer_load_dwordx4 v0, %[X], s47 offen offset:0 sc1 lds\n\t" \
  "s_waitcnt lgkmcnt(4)\n\t" \
  "v_mfma_f32_16x16x32_bf16 v[192:195], v[236:239], v[76:79], v[192:195]\n\t" \
  "v_mfma_f32_16x16x32_bf16 v[160:163], v[72:75], a[64:67], v[160:163]\n\t" \
  "v_mfma_f32_16x16x32_bf16 v[160:163], v[76:79], a[96:99], v[160:163]\n\t" \
  "v_mfma_f32_16x16x32_bf16 v[164:167], v[72:75], a[68:71], v[164:167]\n\t" \
  "buffer_load_dwordx4 v0, %[X], s47 offen offset:1024 sc1 lds\n\t" \
  "v_mfma_f32_16x16x32_bf16 v[164:167], v[76:79], a[100:103], v[164:167]\n\t" \
  "v_mfma_f32_16x16x32_bf16 v[168:171], v[72:75], a[72:75], v[168:171]\n\t" \
  "v_mfma_f32_16x16x32_bf16 v[168:171], v[76:79], a[104:107], v[168:171]\n\t" \
  "v_mfma_f32_16x16x32_bf16 v[172:175], v[72:75], a[76:79], v[172:175]\n\t" \
  "s_add_u32 m0, s46, s87\n\t" \
  "s_add_u32 s47, s81, s37\n\t" \
  "buffer_load_dwordx4 v0, %[X], s47 offen offset:0 sc1 lds\n\t" \
  "v_mfma_f32_16x16x32_bf16 v[172:175], v[76:79], a[108:111], v[172:175]\n\t" \
  "s_add_u32 s62, s48, 2\n\t" \
  "v_mfma_f32_16x16x32_bf16 v[176:179], v[72:75], a[80:83], v[176:179]\n\t" \
  "v_mfma_f32_16x16x32_bf16 v[176:179], v[76:79], a[112:115], v[176:179]\n\t" \
  "s_mul_i32 s63, s62, 43\n\t" \
  "s_lshr_b32 s63, s63, 9\n\t" \
  "v_mfma_f32_16x16x32_bf16 v[180:183], v[72:75], a[84:87], v[180:183]\n\t" \
  "buffer_load_dwordx4 v0, %[X], s47 offen offset:1024 sc1 lds\n\t" \
  "v_mfma_f32_16x16x32_bf16 v[180:183], v[76:79], a[116:119], v[180:183]\n\t" \
  "s_mul_i32 s63, s63, " TK_RM "\n\t" \
  "s_sub_u32 s62, s62, s63\n\t" \
  "v_mfma_f32_16x16x32_bf16 v[184:187], v[72:75], a[88:91], v[184:187]\n\t" \
  "v_mfma_f32_16x16x32_bf16 v[184:187], v[76:79], a[120:123], v[184:187]\n\t" \
  "s_mul_i32 s63, s62, 43\n\t" \
  "s_lshr_b32 s63, s63, 7\n\t" \
  "v_mfma_f32_16x16x32_bf16 v[188:191], v[72:75], a[92:95], v[188:191]\n\t" \
  "buffer_load_dwordx4 v0, %[X], s47 offen offset:2048 sc1 lds\n\t" \
  "v_mfma_f32_16x16x32_bf16 v[188:191], v[76:79], a[124:127], v[188:191]\n\t" \
  "s_mul_i32 s62, s62, 688128\n\t" \
  "s_lshl_b32 s60, s63, 15\n\t" \
  "s_waitcnt lgkmcnt(3)\n\t" \
  "v_mfma_f32_16x16x32_bf16 v[192:195], v[240:243], v[80:83], v[192:195]\n\t" \
  "s_waitcnt lgkmcnt(2)\n\t" \
  "v_mfma_f32_16x16x32_bf16 v[192:195], v[244:247], v[84:87], v[192:195]\n\t" \
  "s_add_u32 s62, s62, s60\n\t" \
  "s_lshr_b32 s60, s86, s63\n\t" \
  "s_waitcnt lgkmcnt(1)\n\t" \
  "v_mfma_f32_16x16x32_bf16 v[192:195], v[248:251], v[88:91], v[192:195]\n\t" \
  "buffer_load_dwordx4 v0, %[X], s47 offen offset:3072 sc1 lds\n\t" \
  "s_waitcnt lgkmcnt(0)\n\t" \
  "v_mfma_f32_16x16x32_bf16 v[192:195], v[252:255], v[92:95], v[192:195]\n\t" \
  "s_and_b32 s60, s60, 1\n\t" \
  "s_lshl_b32 s60, s60, 13\n\t" \
  "v_mfma_f32_16x16x32_bf16 v[160:163], v[80:83], a[128:131], v[160:163]\n\t" \
  "v_mfma_f32_16x16x32_bf16 v[160:163], v[84:87], a[160:163], v[160:163]\n\t" \
  "s_add_u32 s62, s62, s60\n\t" \
  "s_add_u32 s62, s62, 656896\n\t" \
  "v_mfma_f32_16x16x32_bf16 v[160:163], v[88:91], a[192:195], v[160:163]\n\t" \
  "s_add_u32 m0, s46, s89\n\t" \
  "s_add_u32 s47, s81, s38\n\t" \
  "buffer_load_dwordx4 v0, %[X], s47 offen offset:0 sc1 lds\n\t" \
  "v_mfma_f32_16x16x32_bf16 v[160:163], v[92:95], a[224:227], v[160:163]\n\t" \
  "s_cmp_gt_u32 s48, " TK_AE "\n\t" \
  "s_cselect_b32 s62, 0xf0000000, s62\n\t" \
  "v_cvt_pk_bf16_f32 v112, v192, v193\n\t" \
  "v_cvt_pk_bf16_f32 v113, v194, v195\n\t" \
  "v_mfma_f32_16x16x32_bf16 v[164:167], v[80:83], a[132:135], v[164:167]\n\t" \
  "v_mfma_f32_16x16x32_bf16 v[164:167], v[84:87], a[164:167], v[164:167]\n\t" \
  "v_mfma_f32_16x16x32_bf16 v[164:167], v[88:91], a[196:199], v[164:167]\n\t" \
  "buffer_load_dwordx4 v0, %[X], s47 offen offset:1024 sc1 lds\n\t" \
  "v_mfma_f32_16x16x32_bf16 v[164:167], v[92:95], a[228:231], v[164:167]\n\t" \
  "v_mfma_f32_16x16x32_bf16 v[168:171], v[80:83], a[136:139], v[168:171]\n\t" \
  "v_mfma_f32_16x16x32_bf16 v[168:171], v[84:87], a[168:171], v[168:171]\n\t" \
  "v_mfma_f32_16x16x32_bf16 v[168:171], v[88:91], a[200:203], v[168:171]\n\t" \
  "buffer_load_dwordx4 v0, %[X], s47 offen offset:2048 sc1 lds\n\t" \
  "v_mfma_f32_16x16x32_bf16 v[168:171], v[92:95], a[232:235], v[168:171]\n\t" \
  "v_cvt_pk_bf16_f32 v96, v160, v164\n\t" \
  "v_cvt_pk_bf16_f32 v100, v161, v165\n\t" \
  "v_mfma_f32_16x16x32_bf16 v[172:175], v[80:83], a[140:143], v[172:175]\n\t" \
  "v_cvt_pk_bf16_f32 v104, v162, v166\n\t" \
  "v_cvt_pk_bf16_f32 v108, v163, v167\n\t" \
  "v_mfma_f32_16x16x32_bf16 v[172:175], v[84:87], a[172:175], v[172:175]\n\t" \
  "v_mfma_f32_16x16x32_bf16 v[172:175], v[88:91], a[204:207], v[172:175]\n\t" \
  "buffer_load_dwordx4 v0, %[X], s47 offen offset:3072 sc1 lds\n\t" \
  "v_mfma_f32_16x16x32_bf16 v[172:175], v[92:95], a[236:239], v[172:175]\n\t" \
  "v_mfma_f32_16x16x32_bf16 v[176:179], v[80:83], a[144:147], v[176:179]\n\t" \
  "v_mfma_f32_16x16x32_bf16 v[176:179], v[84:87], a[176:179], v[176:179]\n\t" \
  "v_mfma_f32_16x16x32_bf16 v[176:179], v[88:91], a[208:211], v[176:179]\n\t" \
  "v_mfma_f32_16x16x32_bf16 v[176:179], v[92:95], a[240:243], v[176:179]\n\t" \
  "v_cvt_pk_bf16_f32 v97, v168, v172\n\t" \
  "v_cvt_pk_bf16_f32 v101, v169, v173\n\t" \
  "v_mfma_f32_16x16x32_bf16 v[180:183], v[80:83], a[148:151], v[180:183]\n\t" \
  "buffer_load_dword v5, off, %[X], s62 sc1\n\t" \
  "v_cvt_pk_bf16_f32 v105, v170, v174\n\t" \
  "v_cvt_pk_bf16_f32 v109, v171, v175\n\t" \
  "v_mfma_f32_16x16x32_bf16 v[180:183], v[84:87], a[180:183], v[180:183]\n\t" \
  "v_mfma_f32_16x16x32_bf16 v[180:183], v[88:91], a[212:215], v[180:183]\n\t" \
  "v_mfma_f32_16x16x32_bf16 v[180:183], v[92:95], a[244:247], v[180:183]\n\t" \
  "v_mfma_f32_16x16x32_bf16 v[184:187], v[80:83], a[152:155], v[184:187]\n\t" \
  "v_mfma_f32_16x16x32_bf16 v[184:187], v[84:87], a[184:187], v[184:187]\n\t" \
  "buffer_store_dwordx2 v[112:113], v8, %[X], s54 offen sc1\n\t" \
  "v_mfma_f32_16x16x32_bf16 v[184:187], v[88:91], a[216:219], v[184:187]\n\t" \
  "v_mfma_f32_16x16x32_bf16 v[184:187], v[92:95], a[248:251], v[184:187]\n\t" \
  "v_cvt_pk_bf16_f32 v98, v176, v180\n\t" \
  "v_cvt_pk_bf16_f32 v102, v177, v181\n\t" \
  "v_mfma_f32_16x16x32_bf16 v[188:191], v[80:83], a[156:159], v[188:191]\n\t" \
  "v_cvt_pk_bf16_f32 v106, v178, v182\n\t" \
  "v_cvt_pk_bf16_f32 v110, v179, v183\n\t" \
  "v_mfma_f32_16x16x32_bf16 v[188:191], v[84:87], a[188:191], v[188:191]\n\t" \
  "v_mfma_f32_16x16x32_bf16 v[188:191], v[88:91], a[220:223], v[188:191]\n\t" \
  "v_mfma_f32_16x16x32_bf16 v[188:191], v[92:95], a[252:255], v[188:191]\n\t" \
  "s_nop 8\n\t" \
  "v_cvt_pk_bf16_f32 v99, v184, v188\n\t" \
  "buffer_store_dwordx4 v[96:99], v7, %[X], s50 offen sc1\n\t" \
  "v_cvt_pk_bf16_f32 v103, v185, v189\n\t" \
  "buffer_store_dwordx4 v[100:103], v7, %[X], s51 offen sc1\n\t" \
  "v_cvt_pk_bf16_f32 v107, v186, v190\n\t" \
  "buffer_store_dwordx4 v[104:107], v7, %[X], s52 offen sc1\n\t" \
  "v_cvt_pk_bf16_f32 v111, v187, v191\n\t" \
  "buffer_store_dwordx4 v[108:111], v7, %[X], s53 offen sc1\n\t" \
  "s_cmp_eq_u32 s48, 63\n\t" \
  "s_cbranch_scc1 LEND_%=\n\t" \
  "s_add_u32 s48, s48, 1\n\t" \
  "s_cmp_eq_u32 s81, s56\n\t" \
  "s_mov_b32 s81, s56\n\t" \
  "s_cbranch_scc0 LCOLD_%=\n\t" \
  "s_waitcnt vmcnt(5)\n\t" \
  "s_branch LTOP_%=\n\t" \
  "LEND_%=:\n\t" \
  "s_waitcnt vmcnt(0)\n\t" \
  "s_barrier\n\t" \
  "s_mov_b64 exec, 1\n\t" \
  "buffer_store_dword v3, off, %[X], s49 sc1\n\t" \
  "s_mov_b64 exec, -1\n\t" \
  "s_mov_b32 s49, 0xf0000000\n\t" \
  ""
#define QK_ASM \
  "v_lshrrev_b32 v16, 7, v0\n\t" \
  "v_lshlrev_b32 v16, 13, v16\n\t" \
  "v_lshrrev_b32 v19, 2, v0\n\t" \
  "v_and_b32 v19, 64, v19\n\t" \
  "v_xor_b32 v19, v19, v0\n\t" \
  "v_and_b32 v19, 0x70, v19\n\t" \
  "v_add_u32 v16, v16, v19\n\t" \
  "s_mul_i32 s63, s40, 40960\n\t" \
  "v_and_b32 v19, 0x300, v0\n\t" \
  "v_lshlrev_b32 v17, 2, v19\n\t" \
  "v_and_b32 v19, 0xc0, v0\n\t" \
  "v_lshl_add_u32 v17, v19, 1, v17\n\t" \
  "v_and_b32 v19, 0x30, v0\n\t" \
  "v_add3_u32 v17, v17, v19, s63\n\t" \
  "v_lshrrev_b32 v19, 1, v0\n\t" \
  "v_and_b32 v19, 64, v19\n\t" \
  "v_add_u32 v17, v17, v19\n\t" \
  "v_xor_b32 v18, 64, v17\n\t" \
  "s_add_u32 m0, s63, 0\n\t" \
  "s_mov_b32 s60, 0\n\t" \
  "buffer_load_dwordx4 v16, %[W], s60 offen lds\n\t" \
  "s_add_u32 m0, s63, 1024\n\t" \
  "s_mov_b32 s60, 65536\n\t" \
  "buffer_load_dwordx4 v16, %[W], s60 offen lds\n\t" \
  "s_add_u32 m0, s63, 2048\n\t" \
  "s_mov_b32 s60, 131072\n\t" \
  "buffer_load_dwordx4 v16, %[W], s60 offen lds\n\t" \
  "s_add_u32 m0, s63, 3072\n\t" \
  "s_mov_b32 s60, 196608\n\t" \
  "buffer_load_dwordx4 v16, %[W], s60 offen lds\n\t" \
  "s_add_u32 m0, s63, 4096\n\t" \
  "s_mov_b32 s60, 262144\n\t" \
  "buffer_load_dwordx4 v16, %[W], s60 offen lds\n\t" \
  "s_add_u32 m0, s63, 5120\n\t" \
  "s_mov_b32 s60, 327680\n\t" \
  "buffer_load_dwordx4 v16, %[W], s60 offen lds\n\t" \
  "s_add_u32 m0, s63, 6144\n\t" \
  "s_mov_b32 s60, 393216\n\t" \
  "buffer_load_dwordx4 v16, %[W], s60 offen lds\n\t" \
  "s_add_u32 m0, s63, 7168\n\t" \
  "s_mov_b32 s60, 458752\n\t" \
  "buffer_load_dwordx4 v16, %[W], s60 offen lds\n\t" \
  "s_add_u32 m0, s63, 8192\n\t" \
  "s_mov_b32 s60, 524288\n\t" \
  "buffer_load_dwordx4 v16, %[W], s60 offen lds\n\t" \
  "s_add_u32 m0, s63, 9216\n\t" \
  "s_mov_b32 s60, 589824\n\t" \
  "buffer_load_dwordx4 v16, %[W], s60 offen lds\n\t" \
  "s_add_u32 m0, s63, 10240\n\t" \
  "s_mov_b32 s60, 655360\n\t" \
  "buffer_load_dwordx4 v16, %[W], s60 offen lds\n\t" \
  "s_add_u32 m0, s63, 11264\n\t" \
  "s_mov_b32 s60, 720896\n\t" \
  "buffer_load_dwordx4 v16, %[W], s60 offen lds\n\t" \
  "s_add_u32 m0, s63, 12288\n\t" \
  "s_mov_b32 s60, 786432\n\t" \
  "buffer_load_dwordx4 v16, %[W], s60 offen lds\n\t" \
  "s_add_u32 m0, s63, 13312\n\t" \
  "s_mov_b32 s60, 851968\n\t" \
  "buffer_load_dwordx4 v16, %[W], s60 offen lds\n\t" \
  "s_add_u32 m0, s63, 14336\n\t" \
  "s_mov_b32 s60, 917504\n\t" \
  "buffer_load_dwordx4 v16, %[W], s60 offen lds\n\t" \
  "s_add_u32 m0, s63, 15360\n\t" \
  "s_mov_b32 s60, 983040\n\t" \
  "buffer_load_dwordx4 v16, %[W], s60 offen lds\n\t" \
  "s_add_u32 m0, s63, 16384\n\t" \
  "s_mov_b32 s60, 1048576\n\t" \
  "buffer_load_dwordx4 v16, %[W], s60 offen lds\n\t" \
  "s_add_u32 m0, s63, 17408\n\t" \
  "s_mov_b32 s60, 1114112\n\t" \
  "buffer_load_dwordx4 v16, %[W], s60 offen lds\n\t" \
  "s_add_u32 m0, s63, 18432\n\t" \
  "s_mov_b32 s60, 1179648\n\t" \
  "buffer_load_dwordx4 v16, %[W], s60 offen lds\n\t" \
  "s_add_u32 m0, s63, 19456\n\t" \
  "s_mov_b32 s60, 1245184\n\t" \
  "buffer_load_dwordx4 v16, %[W], s60 offen lds\n\t" \
  "s_add_u32 m0, s63, 20480\n\t" \
  "s_mov_b32 s60, 1310720\n\t" \
  "buffer_load_dwordx4 v16, %[W], s60 offen lds\n\t" \
  "s_add_u32 m0, s63, 21504\n\t" \
  "s_mov_b32 s60, 1376256\n\t" \
  "buffer_load_dwordx4 v16, %[W], s60 offen lds\n\t" \
  "s_add_u32 m0, s63, 22528\n\t" \
  "s_mov_b32 s60, 1441792\n\t" \
  "buffer_load_dwordx4 v16, %[W], s60 offen lds\n\t" \
  "s_add_u32 m0, s63, 23552\n\t" \
  "s_mov_b32 s60, 1507328\n\t" \
  "buffer_load_dwordx4 v16, %[W], s60 offen lds\n\t" \
  "s_add_u32 m0, s63, 24576\n\t" \
  "s_mov_b32 s60, 1572864\n\t" \
  "buffer_load_dwordx4 v16, %[W], s60 offen lds\n\t" \
  "s_add_u32 m0, s63, 25600\n\t" \
  "s_mov_b32 s60, 1638400\n\t" \
  "buffer_load_dwordx4 v16, %[W], s60 offen lds\n\t" \
  "s_add_u32 m0, s63, 26624\n\t" \
  "s_mov_b32 s60, 1703936\n\t" \
  "buffer_load_dwordx4 v16, %[W], s60 offen lds\n\t" \
  "s_add_u32 m0, s63, 27648\n\t" \
  "s_mov_b32 s60, 1769472\n\t" \
  "buffer_load_dwordx4 v16, %[W], s60 offen lds\n\t" \
  "s_add_u32 m0, s63, 28672\n\t" \
  "s_mov_b32 s60, 1835008\n\t" \
  "buffer_load_dwordx4 v16, %[W], s60 offen lds\n\t" \
  "s_add_u32 m0, s63, 29696\n\t" \
  "s_mov_b32 s60, 1900544\n\t" \
  "buffer_load_dwordx4 v16, %[W], s60 offen lds\n\t" \
  "s_add_u32 m0, s63, 30720\n\t" \
  "s_mov_b32 s60, 1966080\n\t" \
  "buffer_load_dwordx4 v16, %[W], s60 offen lds\n\t" \
  "s_add_u32 m0, s63, 31744\n\t" \
  "s_mov_b32 s60, 2031616\n\t" \
  "buffer_load_dwordx4 v16, %[W], s60 offen lds\n\t" \
  "s_add_u32 m0, s63, 32768\n\t" \
  "s_mov_b32 s60, 2097152\n\t" \
  "buffer_load_dwordx4 v16, %[W], s60 offen lds\n\t" \
  "s_add_u32 m0, s63, 33792\n\t" \
  "s_mov_b32 s60, 2162688\n\t" \
  "buffer_load_dwordx4 v16, %[W], s60 offen lds\n\t" \
  "s_add_u32 m0, s63, 34816\n\t" \
  "s_mov_b32 s60, 2228224\n\t" \
  "buffer_load_dwordx4 v16, %[W], s60 offen lds\n\t" \
  "s_add_u32 m0, s63, 35840\n\t" \
  "s_mov_b32 s60, 2293760\n\t" \
  "buffer_load_dwordx4 v16, %[W], s60 offen lds\n\t" \
  "s_add_u32 m0, s63, 36864\n\t" \
  "s_mov_b32 s60, 2359296\n\t" \
  "buffer_load_dwordx4 v16, %[W], s60 offen lds\n\t" \
  "s_add_u32 m0, s63, 37888\n\t" \
  "s_mov_b32 s60, 2424832\n\t" \
  "buffer_load_dwordx4 v16, %[W], s60 offen lds\n\t" \
  "s_add_u32 m0, s63, 38912\n\t" \
  "s_mov_b32 s60, 2490368\n\t" \
  "buffer_load_dwordx4 v16, %[W], s60 offen lds\n\t" \
  "s_add_u32 m0, s63, 39936\n\t" \
  "s_mov_b32 s60, 2555904\n\t" \
  "buffer_load_dwordx4 v16, %[W], s60 offen lds\n\t" \
  "s_waitcnt vmcnt(32)\n\t" \
  "ds_read_b64_tr_b16 a[0:1], v17 offset:0\n\t" \
  "ds_read_b64_tr_b16 a[2:3], v17 offset:512\n\t" \
  "ds_read_b64_tr_b16 a[4:5], v17 offset:4096\n\t" \
  "ds_read_b64_tr_b16 a[6:7], v17 offset:4608\n\t" \
  "ds_read_b64_tr_b16 a[72:73], v17 offset:8\n\t" \
  "ds_read_b64_tr_b16 a[74:75], v17 offset:520\n\t" \
  "ds_read_b64_tr_b16 a[76:77], v17 offset:4104\n\t" \
  "ds_read_b64_tr_b16 a[78:79], v17 offset:4616\n\t" \
  "ds_read_b64_tr_b16 a[144:145], v18 offset:0\n\t" \
  "ds_read_b64_tr_b16 a[146:147], v18 offset:512\n\t" \
  "ds_read_b64_tr_b16 a[148:149], v18 offset:4096\n\t" \
  "ds_read_b64_tr_b16 a[150:151], v18 offset:4608\n\t" \
  "ds_read_b64_tr_b16 a[216:217], v18 offset:8\n\t" \
  "ds_read_b64_tr_b16 a[218:219], v18 offset:520\n\t" \
  "ds_read_b64_tr_b16 a[220:221], v18 offset:4104\n\t" \
  "ds_read_b64_tr_b16 a[222:223], v18 offset:4616\n\t" \
  "s_waitcnt lgkmcnt(0)\n\t" \
  "s_add_u32 m0, s63, 0\n\t" \
  "s_mov_b32 s60, 2621440\n\t" \
  "buffer_load_dwordx4 v16, %[W], s60 offen lds\n\t" \
  "s_add_u32 m0, s63, 1024\n\t" \
  "s_mov_b32 s60, 2686976\n\t" \
  "buffer_load_dwordx4 v16, %[W], s60 offen lds\n\t" \
  "s_add_u32 m0, s63, 2048\n\t" \
  "s_mov_b32 s60, 2752512\n\t" \
  "buffer_load_dwordx4 v16, %[W], s60 offen lds\n\t" \
  "s_add_u32 m0, s63, 3072\n\t" \
  "s_mov_b32 s60, 2818048\n\t" \
  "buffer_load_dwordx4 v16, %[W], s60 offen lds\n\t" \
  "s_add_u32 m0, s63, 4096\n\t" \
  "s_mov_b32 s60, 2883584\n\t" \
  "buffer_load_dwordx4 v16, %[W], s60 offen lds\n\t" \
  "s_add_u32 m0, s63, 5120\n\t" \
  "s_mov_b32 s60, 2949120\n\t" \
  "buffer_load_dwordx4 v16, %[W], s60 offen lds\n\t" \
  "s_add_u32 m0, s63, 6144\n\t" \
  "s_mov_b32 s60, 3014656\n\t" \
  "buffer_load_dwordx4 v16, %[W], s60 offen lds\n\t" \
  "s_add_u32 m0, s63, 7168\n\t" \
  "s_mov_b32 s60, 3080192\n\t" \
  "buffer_load_dwordx4 v16, %[W], s60 offen lds\n\t" \
  "s_waitcnt vmcnt(32)\n\t" \
  "ds_read_b64_tr_b16 a[8:9], v17 offset:8192\n\t" \
  "ds_read_b64_tr_b16 a[10:11], v17 offset:8704\n\t" \
  "ds_read_b64_tr_b16 a[12:13], v17 offset:12288\n\t" \
  "ds_read_b64_tr_b16 a[14:15], v17 offset:12800\n\t" \
  "ds_read_b64_tr_b16 a[80:81], v17 offset:8200\n\t" \
  "ds_read_b64_tr_b16 a[82:83], v17 offset:8712\n\t" \
  "ds_read_b64_tr_b16 a[84:85], v17 offset:12296\n\t" \
  "ds_read_b64_tr_b16 a[86:87], v17 offset:12808\n\t" \
  "ds_read_b64_tr_b16 a[152:153], v18 offset:8192\n\t" \
  "ds_read_b64_tr_b16 a[154:155], v18 offset:8704\n\t" \
  "ds_read_b64_tr_b16 a[156:157], v18 offset:12288\n\t" \
  "ds_read_b64_tr_b16 a[158:159], v18 offset:12800\n\t" \
  "ds_read_b64_tr_b16 a[224:225], v18 offset:8200\n\t" \
  "ds_read_b64_tr_b16 a[226:227], v18 offset:8712\n\t" \
  "ds_read_b64_tr_b16 a[228:229], v18 offset:12296\n\t" \
  "ds_read_b64_tr_b16 a[230:231], v18 offset:12808\n\t" \
  "s_waitcnt lgkmcnt(0)\n\t" \
  "s_add_u32 m0, s63, 8192\n\t" \
  "s_mov_b32 s60, 3145728\n\t" \
  "buffer_load_dwordx4 v16, %[W], s60 offen lds\n\t" \
  "s_add_u32 m0, s63, 9216\n\t" \
  "s_mov_b32 s60, 3211264\n\t" \
  "buffer_load_dwordx4 v16, %[W], s60 offen lds\n\t" \
  "s_add_u32 m0, s63, 10240\n\t" \
  "s_mov_b32 s60, 3276800\n\t" \
  "buffer_load_dwordx4 v16, %[W], s60 offen lds\n\t" \
  "s_add_u32 m0, s63, 11264\n\t" \
  "s_mov_b32 s60, 3342336\n\t" \
  "buffer_load_dwordx4 v16, %[W], s60 offen lds\n\t" \
  "s_add_u32 m0, s63, 12288\n\t" \
  "s_mov_b32 s60, 3407872\n\t" \
  "buffer_load_dwordx4 v16, %[W], s60 offen lds\n\t" \
  "s_add_u32 m0, s63, 13312\n\t" \
  "s_mov_b32 s60, 3473408\n\t" \
  "buffer_load_dwordx4 v16, %[W], s60 offen lds\n\t" \
  "s_add_u32 m0, s63, 14336\n\t" \
  "s_mov_b32 s60, 3538944\n\t" \
  "buffer_load_dwordx4 v16, %[W], s60 offen lds\n\t" \
  "s_add_u32 m0, s63, 15360\n\t" \
  "s_mov_b32 s60, 3604480\n\t" \
  "buffer_load_dwordx4 v16, %[W], s60 offen lds\n\t" \
  "s_waitcnt vmcnt(32)\n\t" \
  "ds_read_b64_tr_b16 a[16:17], v17 offset:16384\n\t" \
  "ds_read_b64_tr_b16 a[18:19], v17 offset:16896\n\t" \
  "ds_read_b64_tr_b16 a[20:21], v17 offset:20480\n\t" \
  "ds_read_b64_tr_b16 a[22:23], v17 offset:20992\n\t" \
  "ds_read_b64_tr_b16 a[88:89], v17 offset:16392\n\t" \
  "ds_read_b64_tr_b16 a[90:91], v17 offset:16904\n\t" \
  "ds_read_b64_tr_b16 a[92:93], v17 offset:20488\n\t" \
  "ds_read_b64_tr_b16 a[94:95], v17 offset:21000\n\t" \
  "ds_read_b64_tr_b16 a[160:161], v18 offset:16384\n\t" \
  "ds_read_b64_tr_b16 a[162:163], v18 offset:16896\n\t" \
  "ds_read_b64_tr_b16 a[164:165], v18 offset:20480\n\t" \
  "ds_read_b64_tr_b16 a[166:167], v18 offset:20992\n\t" \
  "ds_read_b64_tr_b16 a[232:233], v18 offset:16392\n\t" \
  "ds_read_b64_tr_b16 a[234:235], v18 offset:16904\n\t" \
  "ds_read_b64_tr_b16 a[236:237], v18 offset:20488\n\t" \
  "ds_read_b64_tr_b16 a[238:239], v18 offset:21000\n\t" \
  "s_waitcnt lgkmcnt(0)\n\t" \
  "s_add_u32 m0, s63, 16384\n\t" \
  "s_mov_b32 s60, 3670016\n\t" \
  "buffer_load_dwordx4 v16, %[W], s60 offen lds\n\t" \
  "s_add_u32 m0, s63, 17408\n\t" \
  "s_mov_b32 s60, 3735552\n\t" \
  "buffer_load_dwordx4 v16, %[W], s60 offen lds\n\t" \
  "s_add_u32 m0, s63, 18432\n\t" \
  "s_mov_b32 s60, 3801088\n\t" \
  "buffer_load_dwordx4 v16, %[W], s60 offen lds\n\t" \
  "s_add_u32 m0, s63, 19456\n\t" \
  "s_mov_b32 s60, 3866624\n\t" \
  "buffer_load_dwordx4 v16, %[W], s60 offen lds\n\t" \
  "s_add_u32 m0, s63, 20480\n\t" \
  "s_mov_b32 s60, 3932160\n\t" \
  "buffer_load_dwordx4 v16, %[W], s60 offen lds\n\t" \
  "s_add_u32 m0, s63, 21504\n\t" \
  "s_mov_b32 s60, 3997696\n\t" \
  "buffer_load_dwordx4 v16, %[W], s60 offen lds\n\t" \
  "s_add_u32 m0, s63, 22528\n\t" \
  "s_mov_b32 s60, 4063232\n\t" \
  "buffer_load_dwordx4 v16, %[W], s60 offen lds\n\t" \
  "s_add_u32 m0, s63, 23552\n\t" \
  "s_mov_b32 s60, 4128768\n\t" \
  "buffer_load_dwordx4 v16, %[W], s60 offen lds\n\t" \
  "s_waitcnt vmcnt(32)\n\t" \
  "ds_read_b64_tr_b16 a[24:25], v17 offset:24576\n\t" \
  "ds_read_b64_tr_b16 a[26:27], v17 offset:25088\n\t" \
  "ds_read_b64_tr_b16 a[28:29], v17 offset:28672\n\t" \
  "ds_read_b64_tr_b16 a[30:31], v17 offset:29184\n\t" \
  "ds_read_b64_tr_b16 a[96:97], v17 offset:24584\n\t" \
  "ds_read_b64_tr_b16 a[98:99], v17 offset:25096\n\t" \
  "ds_read_b64_tr_b16 a[100:101], v17 offset:28680\n\t" \
  "ds_read_b64_tr_b16 a[102:103], v17 offset:29192\n\t" \
  "ds_read_b64_tr_b16 a[168:169], v18 offset:24576\n\t" \
  "ds_read_b64_tr_b16 a[170:171], v18 offset:25088\n\t" \
  "ds_read_b64_tr_b16 a[172:173], v18 offset:28672\n\t" \
  "ds_read_b64_tr_b16 a[174:175], v18 offset:29184\n\t" \
  "ds_read_b64_tr_b16 a[240:241], v18 offset:24584\n\t" \
  "ds_read_b64_tr_b16 a[242:243], v18 offset:25096\n\t" \
  "ds_read_b64_tr_b16 a[244:245], v18 offset:28680\n\t" \
  "ds_read_b64_tr_b16 a[246:247], v18 offset:29192\n\t" \
  "s_waitcnt lgkmcnt(0)\n\t" \
  "s_add_u32 m0, s63, 24576\n\t" \
  "s_mov_b32 s60, 4194304\n\t" \
  "buffer_load_dwordx4 v16, %[W], s60 offen lds\n\t" \
  "s_add_u32 m0, s63, 25600\n\t" \
  "s_mov_b32 s60, 4259840\n\t" \
  "buffer_load_dwordx4 v16, %[W], s60 offen lds\n\t" \
  "s_add_u32 m0, s63, 26624\n\t" \
  "s_mov_b32 s60, 4325376\n\t" \
  "buffer_load_dwordx4 v16, %[W], s60 offen lds\n\t" \
  "s_add_u32 m0, s63, 27648\n\t" \
  "s_mov_b32 s60, 4390912\n\t" \
  "buffer_load_dwordx4 v16, %[W], s60 offen lds\n\t" \
  "s_add_u32 m0, s63, 28672\n\t" \
  "s_mov_b32 s60, 4456448\n\t" \
  "buffer_load_dwordx4 v16, %[W], s60 offen lds\n\t" \
  "s_add_u32 m0, s63, 29696\n\t" \
  "s_mov_b32 s60, 4521984\n\t" \
  "buffer_load_dwordx4 v16, %[W], s60 offen lds\n\t" \
  "s_add_u32 m0, s63, 30720\n\t" \
  "s_mov_b32 s60, 4587520\n\t" \
  "buffer_load_dwordx4 v16, %[W], s60 offen lds\n\t" \
  "s_add_u32 m0, s63, 31744\n\t" \
  "s_mov_b32 s60, 4653056\n\t" \
  "buffer_load_dwordx4 v16, %[W], s60 offen lds\n\t" \
  "s_waitcnt vmcnt(32)\n\t" \
  "ds_read_b64_tr_b16 a[32:33], v17 offset:32768\n\t" \
  "ds_read_b64_tr_b16 a[34:35], v17 offset:33280\n\t" \
  "ds_read_b64_tr_b16 a[36:37], v17 offset:36864\n\t" \
  "ds_read_b64_tr_b16 a[38:39], v17 offset:37376\n\t" \
  "ds_read_b64_tr_b16 a[104:105], v17 offset:32776\n\t" \
  "ds_read_b64_tr_b16 a[106:107], v17 offset:33288\n\t" \
  "ds_read_b64_tr_b16 a[108:109], v17 offset:36872\n\t" \
  "ds_read_b64_tr_b16 a[110:111], v17 offset:37384\n\t" \
  "ds_read_b64_tr_b16 a[176:177], v18 offset:32768\n\t" \
  "ds_read_b64_tr_b16 a[178:179], v18 offset:33280\n\t" \
  "ds_read_b64_tr_b16 a[180:181], v18 offset:36864\n\t" \
  "ds_read_b64_tr_b16 a[182:183], v18 offset:37376\n\t" \
  "ds_read_b64_tr_b16 a[248:249], v18 offset:32776\n\t" \
  "ds_read_b64_tr_b16 a[250:251], v18 offset:33288\n\t" \
  "ds_read_b64_tr_b16 a[252:253], v18 offset:36872\n\t" \
  "ds_read_b64_tr_b16 a[254:255], v18 offset:37384\n\t" \
  "s_waitcnt lgkmcnt(0)\n\t" \
  "s_waitcnt vmcnt(24)\n\t" \
  "ds_read_b64_tr_b16 a[40:41], v17 offset:0\n\t" \
  "ds_read_b64_tr_b16 a[42:43], v17 offset:512\n\t" \
  "ds_read_b64_tr_b16 a[44:45], v17 offset:4096\n\t" \
  "ds_read_b64_tr_b16 a[46:47], v17 offset:4608\n\t" \
  "ds_read_b64_tr_b16 a[112:113], v17 offset:8\n\t" \
  "ds_read_b64_tr_b16 a[114:115], v17 offset:520\n\t" \
  "ds_read_b64_tr_b16 a[116:117], v17 offset:4104\n\t" \
  "ds_read_b64_tr_b16 a[118:119], v17 offset:4616\n\t" \
  "ds_read_b64_tr_b16 a[184:185], v18 offset:0\n\t" \
  "ds_read_b64_tr_b16 a[186:187], v18 offset:512\n\t" \
  "ds_read_b64_tr_b16 a[188:189], v18 offset:4096\n\t" \
  "ds_read_b64_tr_b16 a[190:191], v18 offset:4608\n\t" \
  "ds_read_b64_tr_b16 v[224:225], v18 offset:8\n\t" \
  "ds_read_b64_tr_b16 v[226:227], v18 offset:520\n\t" \
  "ds_read_b64_tr_b16 v[228:229], v18 offset:4104\n\t" \
  "ds_read_b64_tr_b16 v[230:231], v18 offset:4616\n\t" \
  "s_waitcnt lgkmcnt(0)\n\t" \
  "s_waitcnt vmcnt(16)\n\t" \
  "ds_read_b64_tr_b16 a[48:49], v17 offset:8192\n\t" \
  "ds_read_b64_tr_b16 a[50:51], v17 offset:8704\n\t" \
  "ds_read_b64_tr_b16 a[52:53], v17 offset:12288\n\t" \
  "ds_read_b64_tr_b16 a[54:55], v17 offset:12800\n\t" \
  "ds_read_b64_tr_b16 a[120:121], v17 offset:8200\n\t" \
  "ds_read_b64_tr_b16 a[122:123], v17 offset:8712\n\t" \
  "ds_read_b64_tr_b16 a[124:125], v17 offset:12296\n\t" \
  "ds_read_b64_tr_b16 a[126:127], v17 offset:12808\n\t" \
  "ds_read_b64_tr_b16 a[192:193], v18 offset:8192\n\t" \
  "ds_read_b64_tr_b16 a[194:195], v18 offset:8704\n\t" \
  "ds_read_b64_tr_b16 a[196:197], v18 offset:12288\n\t" \
  "ds_read_b64_tr_b16 a[198:199], v18 offset:12800\n\t" \
  "ds_read_b64_tr_b16 v[232:233], v18 offset:8200\n\t" \
  "ds_read_b64_tr_b16 v[234:235], v18 offset:8712\n\t" \
  "ds_read_b64_tr_b16 v[236:237], v18 offset:12296\n\t" \
  "ds_read_b64_tr_b16 v[238:239], v18 offset:12808\n\t" \
  "s_waitcnt lgkmcnt(0)\n\t" \
  "s_waitcnt vmcnt(8)\n\t" \
  "ds_read_b64_tr_b16 a[56:57], v17 offset:16384\n\t" \
  "ds_read_b64_tr_b16 a[58:59], v17 offset:16896\n\t" \
  "ds_read_b64_tr_b16 a[60:61], v17 offset:20480\n\t" \
  "ds_read_b64_tr_b16 a[62:63], v17 offset:20992\n\t" \
  "ds_read_b64_tr_b16 a[128:129], v17 offset:16392\n\t" \
  "ds_read_b64_tr_b16 a[130:131], v17 offset:16904\n\t" \
  "ds_read_b64_tr_b16 a[132:133], v17 offset:20488\n\t" \
  "ds_read_b64_tr_b16 a[134:135], v17 offset:21000\n\t" \
  "ds_read_b64_tr_b16 a[200:201], v18 offset:16384\n\t" \
  "ds_read_b64_tr_b16 a[202:203], v18 offset:16896\n\t" \
  "ds_read_b64_tr_b16 a[204:205], v18 offset:20480\n\t" \
  "ds_read_b64_tr_b16 a[206:207], v18 offset:20992\n\t" \
  "ds_read_b64_tr_b16 v[240:241], v18 offset:16392\n\t" \
  "ds_read_b64_tr_b16 v[242:243], v18 offset:16904\n\t" \
  "ds_read_b64_tr_b16 v[244:245], v18 offset:20488\n\t" \
  "ds_read_b64_tr_b16 v[246:247], v18 offset:21000\n\t" \
  "s_waitcnt lgkmcnt(0)\n\t" \
  "s_waitcnt vmcnt(0)\n\t" \
  "ds_read_b64_tr_b16 a[64:65], v17 offset:24576\n\t" \
  "ds_read_b64_tr_b16 a[66:67], v17 offset:25088\n\t" \
  "ds_read_b64_tr_b16 a[68:69], v17 offset:28672\n\t" \
  "ds_read_b64_tr_b16 a[70:71], v17 offset:29184\n\t" \
  "ds_read_b64_tr_b16 a[136:137], v17 offset:24584\n\t" \
  "ds_read_b64_tr_b16 a[138:139], v17 offset:25096\n\t" \
  "ds_read_b64_tr_b16 a[140:141], v17 offset:28680\n\t" \
  "ds_read_b64_tr_b16 a[142:143], v17 offset:29192\n\t" \
  "ds_read_b64_tr_b16 a[208:209], v18 offset:24576\n\t" \
  "ds_read_b64_tr_b16 a[210:211], v18 offset:25088\n\t" \
  "ds_read_b64_tr_b16 a[212:213], v18 offset:28672\n\t" \
  "ds_read_b64_tr_b16 a[214:215], v18 offset:29184\n\t" \
  "ds_read_b64_tr_b16 v[248:249], v18 offset:24584\n\t" \
  "ds_read_b64_tr_b16 v[250:251], v18 offset:25096\n\t" \
  "ds_read_b64_tr_b16 v[252:253], v18 offset:28680\n\t" \
  "ds_read_b64_tr_b16 v[254:255], v18 offset:29192\n\t" \
  "s_waitcnt lgkmcnt(0)\n\t" \
  "s_mov_b32 s84, " TK_CAP "\n\t" \
  "s_mov_b32 s48, 0\n\t" \
  "s_mov_b32 s49, 0xf0000000\n\t" \
  "s_mov_b32 s46, s41\n\t" \
  "s_mov_b32 s56, 0\n\t" \
  "v_lshrrev_b32 v7, 2, v0\n\t" \
  "s_mov_b32 s60, " TK_CTAB "\n\t" \
  "buffer_load_dword v4, v7, %[X], s60 offen offset:88 sc1\n\t" \
  "buffer_load_dword v7, v7, %[X], s60 offen offset:0 sc1\n\t" \
  "s_waitcnt vmcnt(0)\n\t" \
  "v_and_b32 v4, 1, v4\n\t" \
  "v_and_b32 v7, 1, v7\n\t" \
  "v_cmp_ne_u32 s[90:91], s67, v4\n\t" \
  "v_cmp_ne_u32 s[88:89], s67, v7\n\t" \
  "s_nop 1\n\t" \
  "s_mov_b32 s57, 0xf0000000\n\t" \
  "s_add_u32 s83, s48, 0\n\t" \
  "s_lshr_b64 s[60:61], s[90:91], s83\n\t" \
  "s_and_b32 s60, s60, 1\n\t" \
  "s_lshl_b32 s60, s60, 13\n\t" \
  "s_add_u32 s83, s83, " TK_SG0 "\n\t" \
  "s_lshl_b32 s83, s83, 21\n\t" \
  "s_or_b32 s83, s83, s60\n\t" \
  "s_add_u32 s80, s48, " TK_TRN "\n\t" \
  "s_lshr_b64 s[60:61], s[90:91], s80\n\t" \
  "s_and_b32 s60, s60, 1\n\t" \
  "s_lshl_b32 s60, s60, 13\n\t" \
  "s_add_u32 s80, s80, " TK_SG0 "\n\t" \
  "s_lshl_b32 s80, s80, 21\n\t" \
  "s_or_b32 s80, s80, s60\n\t" \
  "s_add_u32 s62, s83, s39\n\t" \
  "s_add_u32 s80, s80, s39\n\t" \
  "s_add_u32 s80, s80, 2048\n\t" \
  "s_cmp_lt_u32 s48, " TK_TRM "\n\t" \
  "s_cselect_b32 s80, s62, s80\n\t" \
  "v_add_u32 v7, s62, v3\n\t" \
  "s_mov_b32 exec_lo, 0\n\t" \
  "v_mov_b32 v7, s80\n\t" \
  "s_mov_b32 exec_lo, -1\n\t" \
  "LCOLD_%=:\n\t" \
  "s_waitcnt vmcnt(0)\n\t" \
  "s_mov_b64 exec, 1\n\t" \
  "buffer_store_dword v6, off, %[X], s49 sc1\n\t" \
  "s_mov_b64 exec, -1\n\t" \
  "s_mov_b32 s49, 0xf0000000\n\t" \
  "s_add_u32 s55, s83, s36\n\t" \
  "s_cmp_lg_u32 s40, 0\n\t" \
  "s_cbranch_scc1 LGO_%=\n\t" \
  "s_and_b32 s62, s45, 3\n\t" \
  "s_lshl_b32 s62, s62, 7\n\t" \
  "s_add_u32 s62, s62, s83\n\t" \
  "s_add_u32 s62, s62, s39\n\t" \
  "s_mov_b32 s61, 0\n\t" \
  "LPO_%=:\n\t" \
  "buffer_load_dword v4, off, %[X], s62 sc1\n\t" \
  "s_waitcnt vmcnt(0)\n\t" \
  "v_cmp_eq_u32 vcc, 0, v4\n\t" \
  "s_cbranch_vccz LPO_%=x\n\t" \
  "s_add_u32 s61, s61, 1\n\t" \
  "s_sleep " TK_PSH "\n\t" \
  "s_cmp_gt_u32 s61, s84\n\t" \
  "s_cbranch_scc1 LPO_%=g\n\t" \
  "s_and_b32 s60, s61, 1023\n\t" \
  "s_cmp_lg_u32 s60, 0\n\t" \
  "s_cbranch_scc1 LPO_%=\n\t" \
  "buffer_load_dword v4, off, %[X], s92 sc1\n\t" \
  "s_waitcnt vmcnt(0)\n\t" \
  "v_readfirstlane_b32 s60, v4\n\t" \
  "s_nop 0\n\t" \
  "s_cmp_eq_u32 s60, 0\n\t" \
  "s_cbranch_scc1 LPO_%=\n\t" \
  "LPO_%=g:\n\t" \
  "s_mov_b32 s84, 0\n\t" \
  "s_or_b32 s60, s48, 1610612736\n\t" \
  "v_mov_b32 v4, s60\n\t" \
  "s_mov_b64 exec, 1\n\t" \
  "buffer_store_dword v4, off, %[X], s92 sc1\n\t" \
  "s_mov_b64 exec, -1\n\t" \
  "LPO_%=x:\n\t" \
  "s_mov_b32 s61, 0\n\t" \
  "LPH_%=:\n\t" \
  "buffer_load_dword v4, v7, %[X], 0 offen sc1\n\t" \
  "s_waitcnt vmcnt(0)\n\t" \
  "v_cmp_eq_u32 vcc, 0, v4\n\t" \
  "s_cbranch_vccz LPH_%=x\n\t" \
  "s_add_u32 s61, s61, 1\n\t" \
  "s_sleep " TK_PSH "\n\t" \
  "s_cmp_gt_u32 s61, s84\n\t" \
  "s_cbranch_scc1 LPH_%=g\n\t" \
  "s_and_b32 s60, s61, 1023\n\t" \
  "s_cmp_lg_u32 s60, 0\n\t" \
  "s_cbranch_scc1 LPH_%=\n\t" \
  "buffer_load_dword v4, off, %[X], s92 sc1\n\t" \
  "s_waitcnt vmcnt(0)\n\t" \
  "v_readfirstlane_b32 s60, v4\n\t" \
  "s_nop 0\n\t" \
  "s_cmp_eq_u32 s60, 0\n\t" \
  "s_cbranch_scc1 LPH_%=\n\t" \
  "LPH_%=g:\n\t" \
  "s_mov_b32 s84, 0\n\t" \
  "s_or_b32 s60, s48, 1610612736\n\t" \
  "v_mov_b32 v4, s60\n\t" \
  "s_mov_b64 exec, 1\n\t" \
  "buffer_store_dword v4, off, %[X], s92 sc1\n\t" \
  "s_mov_b64 exec, -1\n\t" \
  "LPH_%=x:\n\t" \
  "s_cmp_lt_i32 s57, 0\n\t" \
  "s_cbranch_scc1 LGO_%=\n\t" \
  "s_movk_i32 s63, 4000\n\t" \
  "s_branch LGC_%=u\n\t" \
  "LGC_%=:\n\t" \
  "s_sleep " TK_PSG "\n\t" \
  "buffer_load_dword v8, off, %[X], s57 sc1\n\t" \
  "s_waitcnt vmcnt(0)\n\t" \
  "LGC_%=u:\n\t" \
  "s_memrealtime s[52:53]\n\t" \
  "v_readfirstlane_b32 s60, v8\n\t" \
  "s_waitcnt lgkmcnt(0)\n\t" \
  "s_sub_u32 s63, s63, 1\n\t" \
  "s_cbranch_scc1 LGO_%=\n\t" \
  "s_cmp_eq_u32 s60, -1\n\t" \
  "s_cbranch_scc1 LGC_%=\n\t" \
  "s_not_b32 s60, s60\n\t" \
  "s_lshl_b32 s61, s52, 8\n\t" \
  "s_sub_u32 s60, s60, s61\n\t" \
  "s_ashr_i32 s60, s60, 8\n\t" \
  "s_sub_i32 s60, s60, " TK_QXC "\n\t" \
  "s_min_i32 s60, s60, 400\n\t" \
  "s_add_u32 s54, s52, s60\n\t" \
  "LGC_%=w:\n\t" \
  "s_sleep 1\n\t" \
  "s_memrealtime s[52:53]\n\t" \
  "s_waitcnt lgkmcnt(0)\n\t" \
  "s_sub_i32 s53, s52, s54\n\t" \
  "s_cmp_lt_i32 s53, 0\n\t" \
  "s_cbranch_scc1 LGC_%=w\n\t" \
  "LGO_%=:\n\t" \
  "s_barrier\n\t" \
  "s_add_u32 m0, s46, 0\n\t" \
  "s_add_u32 s47, s55, 0\n\t" \
  "buffer_load_dwordx4 v0, %[X], s47 offen sc1 lds\n\t" \
  "s_add_u32 m0, s46, 4096\n\t" \
  "s_add_u32 s47, s55, 4096\n\t" \
  "buffer_load_dwordx4 v0, %[X], s47 offen sc1 lds\n\t" \
  "s_add_u32 m0, s46, 8192\n\t" \
  "s_add_u32 s47, s55, 16384\n\t" \
  "buffer_load_dwordx4 v0, %[X], s47 offen sc1 lds\n\t" \
  "s_add_u32 m0, s46, 12288\n\t" \
  "s_add_u32 s47, s55, 20480\n\t" \
  "buffer_load_dwordx4 v0, %[X], s47 offen sc1 lds\n\t" \
  "s_add_u32 m0, s46, 16384\n\t" \
  "s_add_u32 s47, s55, 32768\n\t" \
  "buffer_load_dwordx4 v0, %[X], s47 offen sc1 lds\n\t" \
  "s_add_u32 m0, s46, 20480\n\t" \
  "s_add_u32 s47, s55, 36864\n\t" \
  "buffer_load_dwordx4 v0, %[X], s47 offen sc1 lds\n\t" \
  "s_add_u32 m0, s46, 24576\n\t" \
  "s_add_u32 s47, s55, 49152\n\t" \
  "buffer_load_dwordx4 v0, %[X], s47 offen sc1 lds\n\t" \
  "s_add_u32 m0, s46, 28672\n\t" \
  "s_add_u32 s47, s55, 53248\n\t" \
  "buffer_load_dwordx4 v0, %[X], s47 offen sc1 lds\n\t" \
  "s_add_u32 m0, s46, 32768\n\t" \
  "s_add_u32 s47, s55, 65536\n\t" \
  "buffer_load_dwordx4 v0, %[X], s47 offen sc1 lds\n\t" \
  "s_waitcnt vmcnt(5)\n\t" \
  "LTOP_%=:\n\t" \
  "s_barrier\n\t" \
  "ds_read_b128 v[64:67], v1 offset:0\n\t" \
  "ds_read_b128 v[68:71], v1 offset:2048\n\t" \
  "ds_read_b128 v[72:75], v2 offset:0\n\t" \
  "ds_read_b128 v[76:79], v2 offset:2048\n\t" \
  "ds_read_b128 v[80:83], v1 offset:4096\n\t" \
  "ds_read_b128 v[84:87], v1 offset:6144\n\t" \
  "s_mov_b32 s82, s83\n\t" \
  "s_xor_b32 s46, s46, 0x10000\n\t" \
  "s_waitcnt lgkmcnt(5)\n\t" \
  "v_mfma_f32_16x16x32_bf16 v[16:19], a[0:3], v[64:67], 0\n\t" \
  "ds_read_b128 v[88:91], v2 offset:4096\n\t" \
  "s_waitcnt lgkmcnt(4)\n\t" \
  "v_mfma_f32_16x16x32_bf16 v[16:19], a[4:7], v[72:75], v[16:19]\n\t" \
  "s_add_u32 s50, s48, 0\n\t" \
  "s_and_b32 s50, s50, " TK_TRM "\n\t" \
  "v_mfma_f32_16x16x32_bf16 v[20:23], a[72:75], v[64:67], 0\n\t" \
  "ds_read_b128 v[92:95], v2 offset:6144\n\t" \
  "v_mfma_f32_16x16x32_bf16 v[20:23], a[76:79], v[72:75], v[20:23]\n\t" \
  "s_lshr_b64 s[60:61], s[90:91], s50\n\t" \
  "s_and_b32 s60, s60, 1\n\t" \
  "v_mfma_f32_16x16x32_bf16 v[24:27], a[144:147], v[64:67], 0\n\t" \
  "ds_read_b128 v[96:99], v1 offset:8192\n\t" \
  "v_mfma_f32_16x16x32_bf16 v[24:27], a[148:151], v[72:75], v[24:27]\n\t" \
  "s_lshl_b32 s60, s60, 13\n\t" \
  "s_add_u32 s50, s50, " TK_SG0 "\n\t" \
  "v_mfma_f32_16x16x32_bf16 v[28:31], a[216:219], v[64:67], 0\n\t" \
  "ds_read_b128 v[100:103], v1 offset:10240\n\t" \
  "v_mfma_f32_16x16x32_bf16 v[28:31], a[220:223], v[72:75], v[28:31]\n\t" \
  "s_lshl_b32 s50, s50, 21\n\t" \
  "s_or_b32 s50, s50, s60\n\t" \
  "s_waitcnt lgkmcnt(5)\n\t" \
  "v_mfma_f32_16x16x32_bf16 v[16:19], a[8:11], v[80:83], v[16:19]\n\t" \
  "ds_read_b128 v[104:107], v2 offset:8192\n\t" \
  "s_waitcnt lgkmcnt(4)\n\t" \
  "v_mfma_f32_16x16x32_bf16 v[16:19], a[12:15], v[88:91], v[16:19]\n\t" \
  "s_add_u32 s50, s50, s37\n\t" \
  "s_add_u32 s51, s50, 4096\n\t" \
  "v_mfma_f32_16x16x32_bf16 v[20:23], a[80:83], v[80:83], v[20:23]\n\t" \
  "ds_read_b128 v[108:111], v2 offset:10240\n\t" \
  "v_mfma_f32_16x16x32_bf16 v[20:23], a[84:87], v[88:91], v[20:23]\n\t" \
  "s_add_u32 s83, s48, 1\n\t" \
  "s_lshr_b64 s[60:61], s[90:91], s83\n\t" \
  "v_mfma_f32_16x16x32_bf16 v[24:27], a[152:155], v[80:83], v[24:27]\n\t" \
  "ds_read_b128 v[112:115], v1 offset:12288\n\t" \
  "v_mfma_f32_16x16x32_bf16 v[24:27], a[156:159], v[88:91], v[24:27]\n\t" \
  "s_and_b32 s60, s60, 1\n\t" \
  "s_lshl_b32 s60, s60, 13\n\t" \
  "v_mfma_f32_16x16x32_bf16 v[28:31], a[224:227], v[80:83], v[28:31]\n\t" \
  "ds_read_b128 v[116:119], v1 offset:14336\n\t" \
  "v_mfma_f32_16x16x32_bf16 v[28:31], a[228:231], v[88:91], v[28:31]\n\t" \
  "s_add_u32 s83, s83, " TK_SG0 "\n\t" \
  "s_lshl_b32 s83, s83, 21\n\t" \
  "s_waitcnt lgkmcnt(5)\n\t" \
  "v_mfma_f32_16x16x32_bf16 v[16:19], a[16:19], v[96:99], v[16:19]\n\t" \
  "ds_read_b128 v[120:123], v2 offset:12288\n\t" \
  "s_waitcnt lgkmcnt(4)\n\t" \
  "v_mfma_f32_16x16x32_bf16 v[16:19], a[20:23], v[104:107], v[16:19]\n\t" \
  "s_or_b32 s83, s83, s60\n\t" \
  "s_cmp_lg_u32 s56, 0\n\t" \
  "v_mfma_f32_16x16x32_bf16 v[20:23], a[88:91], v[96:99], v[20:23]\n\t" \
  "ds_read_b128 v[124:127], v2 offset:14336\n\t" \
  "v_mfma_f32_16x16x32_bf16 v[20:23], a[92:95], v[104:107], v[20:23]\n\t" \
  "s_cbranch_scc1 LWARM_%=\n\t" \
  "s_waitcnt vmcnt(0)\n\t" \
  "LWARM_%=:\n\t" \
  "s_barrier\n\t" \
  "v_mfma_f32_16x16x32_bf16 v[24:27], a[160:163], v[96:99], v[24:27]\n\t" \
  "ds_read_b128 v[128:131], v1 offset:16384\n\t" \
  "v_mfma_f32_16x16x32_bf16 v[24:27], a[164:167], v[104:107], v[24:27]\n\t" \
  "v_mfma_f32_16x16x32_bf16 v[28:31], a[232:235], v[96:99], v[28:31]\n\t" \
  "ds_read_b128 v[132:135], v1 offset:18432\n\t" \
  "v_mfma_f32_16x16x32_bf16 v[28:31], a[236:239], v[104:107], v[28:31]\n\t" \
  "s_add_i32 s62, s48, s42\n\t" \
  "s_add_i32 s62, s62, 1\n\t" \
  "s_waitcnt lgkmcnt(5)\n\t" \
  "v_mfma_f32_16x16x32_bf16 v[16:19], a[24:27], v[112:115], v[16:19]\n\t" \
  "ds_read_b128 v[136:139], v2 offset:16384\n\t" \
  "s_waitcnt lgkmcnt(4)\n\t" \
  "v_mfma_f32_16x16x32_bf16 v[16:19], a[28:31], v[120:123], v[16:19]\n\t" \
  "s_mov_b32 s61, s62\n\t" \
  "v_mfma_f32_16x16x32_bf16 v[20:23], a[96:99], v[112:115], v[20:23]\n\t" \
  "ds_read_b128 v[140:143], v2 offset:18432\n\t" \
  "v_mfma_f32_16x16x32_bf16 v[20:23], a[100:103], v[120:123], v[20:23]\n\t" \
  "s_mul_i32 s63, s62, 43\n\t" \
  "s_lshr_b32 s63, s63, 9\n\t" \
  "v_mfma_f32_16x16x32_bf16 v[24:27], a[168:171], v[112:115], v[24:27]\n\t" \
  "ds_read_b128 v[144:147], v1 offset:20480\n\t" \
  "v_mfma_f32_16x16x32_bf16 v[24:27], a[172:175], v[120:123], v[24:27]\n\t" \
  "s_mul_i32 s63, s63, " TK_RM "\n\t" \
  "s_sub_u32 s62, s62, s63\n\t" \
  "v_mfma_f32_16x16x32_bf16 v[28:31], a[240:243], v[112:115], v[28:31]\n\t" \
  "ds_read_b128 v[148:151], v1 offset:22528\n\t" \
  "v_mfma_f32_16x16x32_bf16 v[28:31], a[244:247], v[120:123], v[28:31]\n\t" \
  "s_mul_i32 s63, s62, 43\n\t" \
  "s_lshr_b32 s63, s63, 7\n\t" \
  "s_waitcnt lgkmcnt(5)\n\t" \
  "v_mfma_f32_16x16x32_bf16 v[16:19], a[32:35], v[128:131], v[16:19]\n\t" \
  "ds_read_b128 v[152:155], v2 offset:20480\n\t" \
  "s_waitcnt lgkmcnt(4)\n\t" \
  "v_mfma_f32_16x16x32_bf16 v[16:19], a[36:39], v[136:139], v[16:19]\n\t" \
  "s_mul_i32 s57, s62, 688128\n\t" \
  "s_lshl_b32 s60, s63, 15\n\t" \
  "v_mfma_f32_16x16x32_bf16 v[20:23], a[104:107], v[128:131], v[20:23]\n\t" \
  "ds_read_b128 v[156:159], v2 offset:22528\n\t" \
  "v_mfma_f32_16x16x32_bf16 v[20:23], a[108:111], v[136:139], v[20:23]\n\t" \
  "s_add_u32 s57, s57, s60\n\t" \
  "s_lshr_b32 s60, s88, s63\n\t" \
  "v_mfma_f32_16x16x32_bf16 v[24:27], a[176:179], v[128:131], v[24:27]\n\t" \
  "ds_read_b128 v[160:163], v1 offset:24576\n\t" \
  "v_mfma_f32_16x16x32_bf16 v[24:27], a[180:183], v[136:139], v[24:27]\n\t" \
  "s_and_b32 s60, s60, 1\n\t" \
  "s_lshl_b32 s60, s60, 13\n\t" \
  "v_mfma_f32_16x16x32_bf16 v[28:31], a[248:251], v[128:131], v[28:31]\n\t" \
  "ds_read_b128 v[164:167], v1 offset:26624\n\t" \
  "v_mfma_f32_16x16x32_bf16 v[28:31], a[252:255], v[136:139], v[28:31]\n\t" \
  "s_add_u32 s57, s57, s60\n\t" \
  "s_add_u32 s57, s57, 656896\n\t" \
  "s_waitcnt lgkmcnt(5)\n\t" \
  "v_mfma_f32_16x16x32_bf16 v[16:19], a[40:43], v[144:147], v[16:19]\n\t" \
  "ds_read_b128 v[168:171], v2 offset:24576\n\t" \
  "s_waitcnt lgkmcnt(4)\n\t" \
  "v_mfma_f32_16x16x32_bf16 v[16:19], a[44:47], v[152:155], v[16:19]\n\t" \
  "s_cmp_lt_i32 s61, 1\n\t" \
  "s_cselect_b32 s57, 0xf0000000, s57\n\t" \
  "v_mfma_f32_16x16x32_bf16 v[20:23], a[112:115], v[144:147], v[20:23]\n\t" \
  "ds_read_b128 v[172:175], v2 offset:26624\n\t" \
  "v_mfma_f32_16x16x32_bf16 v[20:23], a[116:119], v[152:155], v[20:23]\n\t" \
  "s_cmp_lg_u32 s40, 0\n\t" \
  "s_cselect_b32 s57, 0xf0000000, s57\n\t" \
  "v_mfma_f32_16x16x32_bf16 v[24:27], a[184:187], v[144:147], v[24:27]\n\t" \
  "ds_read_b128 v[176:179], v1 offset:28672\n\t" \
  "v_mfma_f32_16x16x32_bf16 v[24:27], a[188:191], v[152:155], v[24:27]\n\t" \
  "s_add_u32 s80, s48, " TK_TRN "\n\t" \
  "s_lshr_b64 s[60:61], s[90:91], s80\n\t" \
  "s_and_b32 s60, s60, 1\n\t" \
  "v_mfma_f32_16x16x32_bf16 v[28:31], v[224:227], v[144:147], v[28:31]\n\t" \
  "ds_read_b128 v[180:183], v1 offset:30720\n\t" \
  "v_mfma_f32_16x16x32_bf16 v[28:31], v[228:231], v[152:155], v[28:31]\n\t" \
  "s_lshl_b32 s60, s60, 13\n\t" \
  "s_add_u32 s80, s80, " TK_SG0 "\n\t" \
  "s_lshl_b32 s80, s80, 21\n\t" \
  "s_waitcnt lgkmcnt(5)\n\t" \
  "v_mfma_f32_16x16x32_bf16 v[16:19], a[48:51], v[160:163], v[16:19]\n\t" \
  "ds_read_b128 v[184:187], v2 offset:28672\n\t" \
  "s_waitcnt lgkmcnt(4)\n\t" \
  "v_mfma_f32_16x16x32_bf16 v[16:19], a[52:55], v[168:171], v[16:19]\n\t" \
  "s_or_b32 s80, s80, s60\n\t" \
  "s_add_u32 s62, s83, s39\n\t" \
  "s_add_u32 s80, s80, s39\n\t" \
  "v_mfma_f32_16x16x32_bf16 v[20:23], a[120:123], v[160:163], v[20:23]\n\t" \
  "ds_read_b128 v[188:191], v2 offset:30720\n\t" \
  "v_mfma_f32_16x16x32_bf16 v[20:23], a[124:127], v[168:171], v[20:23]\n\t" \
  "s_add_u32 s80, s80, 2048\n\t" \
  "v_mfma_f32_16x16x32_bf16 v[24:27], a[192:195], v[160:163], v[24:27]\n\t" \
  "ds_read_b128 v[192:195], v1 offset:32768\n\t" \
  "v_mfma_f32_16x16x32_bf16 v[24:27], a[196:199], v[168:171], v[24:27]\n\t" \
  "v_mfma_f32_16x16x32_bf16 v[28:31], v[232:235], v[160:163], v[28:31]\n\t" \
  "ds_read_b128 v[196:199], v1 offset:34816\n\t" \
  "v_mfma_f32_16x16x32_bf16 v[28:31], v[236:239], v[168:171], v[28:31]\n\t" \
  "s_cmp_lt_u32 s48, " TK_TRM "\n\t" \
  "s_cselect_b32 s80, s62, s80\n\t" \
  "s_waitcnt lgkmcnt(5)\n\t" \
  "v_mfma_f32_16x16x32_bf16 v[16:19], a[56:59], v[176:179], v[16:19]\n\t" \
  "ds_read_b128 v[200:203], v2 offset:32768\n\t" \
  "s_waitcnt lgkmcnt(4)\n\t" \
  "v_mfma_f32_16x16x32_bf16 v[16:19], a[60:63], v[184:187], v[16:19]\n\t" \
  "v_add_u32 v7, s62, v3\n\t" \
  "s_mov_b32 exec_lo, 0\n\t" \
  "v_mov_b32 v7, s80\n\t" \
  "s_mov_b32 exec_lo, -1\n\t" \
  "v_mfma_f32_16x16x32_bf16 v[20:23], a[128:131], v[176:179], v[20:23]\n\t" \
  "ds_read_b128 v[204:207], v2 offset:34816\n\t" \
  "v_mfma_f32_16x16x32_bf16 v[20:23], a[132:135], v[184:187], v[20:23]\n\t" \
  "v_mfma_f32_16x16x32_bf16 v[24:27], a[200:203], v[176:179], v[24:27]\n\t" \
  "v_mfma_f32_16x16x32_bf16 v[24:27], a[204:207], v[184:187], v[24:27]\n\t" \
  "buffer_load_dword v4, v7, %[X], 0 offen sc1\n\t" \
  "v_mfma_f32_16x16x32_bf16 v[28:31], v[240:243], v[176:179], v[28:31]\n\t" \
  "v_mfma_f32_16x16x32_bf16 v[28:31], v[244:247], v[184:187], v[28:31]\n\t" \
  "s_waitcnt lgkmcnt(3)\n\t" \
  "v_mfma_f32_16x16x32_bf16 v[16:19], a[64:67], v[192:195], v[16:19]\n\t" \
  "v_xor_b32 v1, 0x10000, v1\n\t" \
  "v_xor_b32 v2, 0x10000, v2\n\t" \
  "s_waitcnt lgkmcnt(1)\n\t" \
  "v_mfma_f32_16x16x32_bf16 v[16:19], a[68:71], v[200:203], v[16:19]\n\t" \
  "v_mfma_f32_16x16x32_bf16 v[20:23], a[136:139], v[192:195], v[20:23]\n\t" \
  "v_mfma_f32_16x16x32_bf16 v[20:23], a[140:143], v[200:203], v[20:23]\n\t" \
  "v_mfma_f32_16x16x32_bf16 v[24:27], a[208:211], v[192:195], v[24:27]\n\t" \
  "v_mfma_f32_16x16x32_bf16 v[24:27], a[212:215], v[200:203], v[24:27]\n\t" \
  "v_mfma_f32_16x16x32_bf16 v[28:31], v[248:251], v[192:195], v[28:31]\n\t" \
  "v_mfma_f32_16x16x32_bf16 v[28:31], v[252:255], v[200:203], v[28:31]\n\t" \
  "v_mfma_f32_16x16x32_bf16 v[32:35], a[0:3], v[68:71], 0\n\t" \
  "v_cvt_pk_bf16_f32 v48, v16, v17\n\t" \
  "v_cvt_pk_bf16_f32 v49, v18, v19\n\t" \
  "v_mfma_f32_16x16x32_bf16 v[32:35], a[4:7], v[76:79], v[32:35]\n\t" \
  "v_cvt_pk_bf16_f32 v50, v20, v21\n\t" \
  "v_cvt_pk_bf16_f32 v51, v22, v23\n\t" \
  "v_mfma_f32_16x16x32_bf16 v[32:35], a[8:11], v[84:87], v[32:35]\n\t" \
  "v_cvt_pk_bf16_f32 v52, v24, v25\n\t" \
  "v_cvt_pk_bf16_f32 v53, v26, v27\n\t" \
  "v_mfma_f32_16x16x32_bf16 v[32:35], a[12:15], v[92:95], v[32:35]\n\t" \
  "v_cvt_pk_bf16_f32 v54, v28, v29\n\t" \
  "v_cvt_pk_bf16_f32 v55, v30, v31\n\t" \
  "v_mfma_f32_16x16x32_bf16 v[32:35], a[16:19], v[100:103], v[32:35]\n\t" \
  "buffer_store_dwordx4 v[48:51], v0, %[X], s50 offen offset:0 sc1\n\t" \
  "v_mfma_f32_16x16x32_bf16 v[32:35], a[20:23], v[108:111], v[32:35]\n\t" \
  "v_mfma_f32_16x16x32_bf16 v[32:35], a[24:27], v[116:119], v[32:35]\n\t" \
  "v_mfma_f32_16x16x32_bf16 v[32:35], a[28:31], v[124:127], v[32:35]\n\t" \
  "v_mfma_f32_16x16x32_bf16 v[32:35], a[32:35], v[132:135], v[32:35]\n\t" \
  "v_mfma_f32_16x16x32_bf16 v[32:35], a[36:39], v[140:143], v[32:35]\n\t" \
  "v_mfma_f32_16x16x32_bf16 v[32:35], a[40:43], v[148:151], v[32:35]\n\t" \
  "v_mfma_f32_16x16x32_bf16 v[32:35], a[44:47], v[156:159], v[32:35]\n\t" \
  "v_mfma_f32_16x16x32_bf16 v[32:35], a[48:51], v[164:167], v[32:35]\n\t" \
  "v_mfma_f32_16x16x32_bf16 v[32:35], a[52:55], v[172:175], v[32:35]\n\t" \
  "v_mfma_f32_16x16x32_bf16 v[32:35], a[56:59], v[180:183], v[32:35]\n\t" \
  "v_mfma_f32_16x16x32_bf16 v[32:35], a[60:63], v[188:191], v[32:35]\n\t" \
  "s_waitcnt vmcnt(1)\n\t" \
  "v_cmp_eq_u32 vcc, 0, v4\n\t" \
  "v_mfma_f32_16x16x32_bf16 v[32:35], a[64:67], v[196:199], v[32:35]\n\t" \
  "s_waitcnt lgkmcnt(0)\n\t" \
  "v_mfma_f32_16x16x32_bf16 v[32:35], a[68:71], v[204:207], v[32:35]\n\t" \
  "s_cmp_eq_u64 vcc, 0\n\t" \
  "s_cselect_b32 s60, 1, 0\n\t" \
  "v_mov_b32 v4, s60\n\t" \
  "v_mfma_f32_16x16x32_bf16 v[36:39], a[72:75], v[68:71], 0\n\t" \
  "ds_write_b32 v5, v4\n\t" \
  "v_mfma_f32_16x16x32_bf16 v[36:39], a[76:79], v[76:79], v[36:39]\n\t" \
  "v_mfma_f32_16x16x32_bf16 v[36:39], a[80:83], v[84:87], v[36:39]\n\t" \
  "v_cvt_pk_bf16_f32 v56, v32, v33\n\t" \
  "v_cvt_pk_bf16_f32 v57, v34, v35\n\t" \
  "v_mfma_f32_16x16x32_bf16 v[36:39], a[84:87], v[92:95], v[36:39]\n\t" \
  "buffer_store_dwordx4 v[52:55], v0, %[X], s50 offen offset:1024 sc1\n\t" \
  "v_mfma_f32_16x16x32_bf16 v[36:39], a[88:91], v[100:103], v[36:39]\n\t" \
  "v_mfma_f32_16x16x32_bf16 v[36:39], a[92:95], v[108:111], v[36:39]\n\t" \
  "s_waitcnt lgkmcnt(0)\n\t" \
  "s_barrier\n\t" \
  "v_mfma_f32_16x16x32_bf16 v[36:39], a[96:99], v[116:119], v[36:39]\n\t" \
  "ds_read_b32 v4, v6 offset:40959\n\t" \
  "v_mfma_f32_16x16x32_bf16 v[36:39], a[100:103], v[124:127], v[36:39]\n\t" \
  "v_mfma_f32_16x16x32_bf16 v[36:39], a[104:107], v[132:135], v[36:39]\n\t" \
  "v_mfma_f32_16x16x32_bf16 v[36:39], a[108:111], v[140:143], v[36:39]\n\t" \
  "v_mfma_f32_16x16x32_bf16 v[36:39], a[112:115], v[148:151], v[36:39]\n\t" \
  "v_mfma_f32_16x16x32_bf16 v[36:39], a[116:119], v[156:159], v[36:39]\n\t" \
  "s_waitcnt lgkmcnt(0)\n\t" \
  "v_readfirstlane_b32 s60, v4\n\t" \
  "s_add_u32 s55, s83, s36\n\t" \
  "v_mfma_f32_16x16x32_bf16 v[36:39], a[120:123], v[164:167], v[36:39]\n\t" \
  "s_cmp_eq_u32 s48, 63\n\t" \
  "s_cselect_b32 s56, 0, s60\n\t" \
  "v_mfma_f32_16x16x32_bf16 v[36:39], a[124:127], v[172:175], v[36:39]\n\t" \
  "s_cmp_eq_u32 s56, 0\n\t" \
  "s_cselect_b32 s55, 0xf0000000, s55\n\t" \
  "v_mfma_f32_16x16x32_bf16 v[36:39], a[128:131], v[180:183], v[36:39]\n\t" \
  "v_mfma_f32_16x16x32_bf16 v[36:39], a[132:135], v[188:191], v[36:39]\n\t" \
  "v_mfma_f32_16x16x32_bf16 v[36:39], a[136:139], v[196:199], v[36:39]\n\t" \
  "s_add_u32 m0, s46, 0\n\t" \
  "s_add_u32 s47, s55, 0\n\t" \
  "buffer_load_dwordx4 v0, %[X], s47 offen sc1 lds\n\t" \
  "v_mfma_f32_16x16x32_bf16 v[36:39], a[140:143], v[204:207], v[36:39]\n\t" \
  "v_mfma_f32_16x16x32_bf16 v[40:43], a[144:147], v[68:71], 0\n\t" \
  "v_mfma_f32_16x16x32_bf16 v[40:43], a[148:151], v[76:79], v[40:43]\n\t" \
  "v_mfma_f32_16x16x32_bf16 v[40:43], a[152:155], v[84:87], v[40:43]\n\t" \
  "s_add_u32 m0, s46, 4096\n\t" \
  "s_add_u32 s47, s55, 4096\n\t" \
  "buffer_load_dwordx4 v0, %[X], s47 offen sc1 lds\n\t" \
  "v_mfma_f32_16x16x32_bf16 v[40:43], a[156:159], v[92:95], v[40:43]\n\t" \
  "v_cvt_pk_bf16_f32 v58, v36, v37\n\t" \
  "v_cvt_pk_bf16_f32 v59, v38, v39\n\t" \
  "v_mfma_f32_16x16x32_bf16 v[40:43], a[160:163], v[100:103], v[40:43]\n\t" \
  "v_mfma_f32_16x16x32_bf16 v[40:43], a[164:167], v[108:111], v[40:43]\n\t" \
  "buffer_store_dwordx4 v[56:59], v0, %[X], s51 offen offset:0 sc1\n\t" \
  "v_mfma_f32_16x16x32_bf16 v[40:43], a[168:171], v[116:119], v[40:43]\n\t" \
  "s_add_u32 m0, s46, 8192\n\t" \
  "s_add_u32 s47, s55, 16384\n\t" \
  "buffer_load_dwordx4 v0, %[X], s47 offen sc1 lds\n\t" \
  "v_mfma_f32_16x16x32_bf16 v[40:43], a[172:175], v[124:127], v[40:43]\n\t" \
  "v_mfma_f32_16x16x32_bf16 v[40:43], a[176:179], v[132:135], v[40:43]\n\t" \
  "v_mfma_f32_16x16x32_bf16 v[40:43], a[180:183], v[140:143], v[40:43]\n\t" \
  "v_mfma_f32_16x16x32_bf16 v[40:43], a[184:187], v[148:151], v[40:43]\n\t" \
  "s_add_u32 m0, s46, 12288\n\t" \
  "s_add_u32 s47, s55, 20480\n\t" \
  "buffer_load_dwordx4 v0, %[X], s47 offen sc1 lds\n\t" \
  "v_mfma_f32_16x16x32_bf16 v[40:43], a[188:191], v[156:159], v[40:43]\n\t" \
  "v_mfma_f32_16x16x32_bf16 v[40:43], a[192:195], v[164:167], v[40:43]\n\t" \
  "v_mfma_f32_16x16x32_bf16 v[40:43], a[196:199], v[172:175], v[40:43]\n\t" \
  "v_mfma_f32_16x16x32_bf16 v[40:43], a[200:203], v[180:183], v[40:43]\n\t" \
  "s_add_u32 m0, s46, 16384\n\t" \
  "s_add_u32 s47, s55, 32768\n\t" \
  "buffer_load_dwordx4 v0, %[X], s47 offen sc1 lds\n\t" \
  "v_mfma_f32_16x16x32_bf16 v[40:43], a[204:207], v[188:191], v[40:43]\n\t" \
  "v_mfma_f32_16x16x32_bf16 v[40:43], a[208:211], v[196:199], v[40:43]\n\t" \
  "v_mfma_f32_16x16x32_bf16 v[40:43], a[212:215], v[204:207], v[40:43]\n\t" \
  "v_mfma_f32_16x16x32_bf16 v[44:47], a[216:219], v[68:71], 0\n\t" \
  "s_add_u32 m0, s46, 20480\n\t" \
  "s_add_u32 s47, s55, 36864\n\t" \
  "buffer_load_dwordx4 v0, %[X], s47 offen sc1 lds\n\t" \
  "v_mfma_f32_16x16x32_bf16 v[44:47], a[220:223], v[76:79], v[44:47]\n\t" \
  "v_mfma_f32_16x16x32_bf16 v[44:47], a[224:227], v[84:87], v[44:47]\n\t" \
  "v_mfma_f32_16x16x32_bf16 v[44:47], a[228:231], v[92:95], v[44:47]\n\t" \
  "v_cvt_pk_bf16_f32 v60, v40, v41\n\t" \
  "v_cvt_pk_bf16_f32 v61, v42, v43\n\t" \
  "v_mfma_f32_16x16x32_bf16 v[44:47], a[232:235], v[100:103], v[44:47]\n\t" \
  "s_add_u32 m0, s46, 24576\n\t" \
  "s_add_u32 s47, s55, 49152\n\t" \
  "buffer_load_dwordx4 v0, %[X], s47 offen sc1 lds\n\t" \
  "v_mfma_f32_16x16x32_bf16 v[44:47], a[236:239], v[108:111], v[44:47]\n\t" \
  "v_mfma_f32_16x16x32_bf16 v[44:47], a[240:243], v[116:119], v[44:47]\n\t" \
  "v_mfma_f32_16x16x32_bf16 v[44:47], a[244:247], v[124:127], v[44:47]\n\t" \
  "v_mfma_f32_16x16x32_bf16 v[44:47], a[248:251], v[132:135], v[44:47]\n\t" \
  "s_add_u32 m0, s46, 28672\n\t" \
  "s_add_u32 s47, s55, 53248\n\t" \
  "buffer_load_dwordx4 v0, %[X], s47 offen sc1 lds\n\t" \
  "v_mfma_f32_16x16x32_bf16 v[44:47], a[252:255], v[140:143], v[44:47]\n\t" \
  "v_mfma_f32_16x16x32_bf16 v[44:47], v[224:227], v[148:151], v[44:47]\n\t" \
  "v_mfma_f32_16x16x32_bf16 v[44:47], v[228:231], v[156:159], v[44:47]\n\t" \
  "v_mfma_f32_16x16x32_bf16 v[44:47], v[232:235], v[164:167], v[44:47]\n\t" \
  "s_add_u32 m0, s46, 32768\n\t" \
  "s_add_u32 s47, s55, 65536\n\t" \
  "buffer_load_dwordx4 v0, %[X], s47 offen sc1 lds\n\t" \
  "v_mfma_f32_16x16x32_bf16 v[44:47], v[236:239], v[172:175], v[44:47]\n\t" \
  "v_mfma_f32_16x16x32_bf16 v[44:47], v[240:243], v[180:183], v[44:47]\n\t" \
  "v_mfma_f32_16x16x32_bf16 v[44:47], v[244:247], v[188:191], v[44:47]\n\t" \
  "buffer_load_dword v8, off, %[X], s57 sc1\n\t" \
  "v_mfma_f32_16x16x32_bf16 v[44:47], v[248:251], v[196:199], v[44:47]\n\t" \
  "v_mfma_f32_16x16x32_bf16 v[44:47], v[252:255], v[204:207], v[44:47]\n\t" \
  "s_nop 8\n\t" \
  "v_cvt_pk_bf16_f32 v62, v44, v45\n\t" \
  "v_cvt_pk_bf16_f32 v63, v46, v47\n\t" \
  "buffer_store_dwordx4 v[60:63], v0, %[X], s51 offen offset:1024 sc1\n\t" \
  "s_add_u32 s49, s82, s38\n\t" \
  "s_cmp_eq_u32 s48, 63\n\t" \
  "s_cbranch_scc1 LEND_%=\n\t" \
  "s_add_u32 s48, s48, 1\n\t" \
  "s_cmp_eq_u32 s56, 0\n\t" \
  "s_cbranch_scc1 LCOLD_%=\n\t" \
  "s_waitcnt vmcnt(0)\n\t" \
  "s_mov_b64 exec, 1\n\t" \
  "buffer_store_dword v6, off, %[X], s49 sc1\n\t" \
  "s_mov_b64 exec, -1\n\t" \
  "s_mov_b32 s49, 0xf0000000\n\t" \
  "s_cmp_lt_i32 s57, 0\n\t" \
  "s_cbranch_scc1 LTOP_%=\n\t" \
  "s_movk_i32 s63, 4000\n\t" \
  "s_branch LGW_%=u\n\t" \
  "LGW_%=:\n\t" \
  "s_sleep " TK_PSG "\n\t" \
  "buffer_load_dword v8, off, %[X], s57 sc1\n\t" \
  "s_waitcnt vmcnt(0)\n\t" \
  "LGW_%=u:\n\t" \
  "s_memrealtime s[52:53]\n\t" \
  "v_readfirstlane_b32 s60, v8\n\t" \
  "s_waitcnt lgkmcnt(0)\n\t" \
  "s_sub_u32 s63, s63, 1\n\t" \
  "s_cbranch_scc1 LTOP_%=\n\t" \
  "s_cmp_eq_u32 s60, -1\n\t" \
  "s_cbranch_scc1 LGW_%=\n\t" \
  "s_not_b32 s60, s60\n\t" \
  "s_lshl_b32 s61, s52, 8\n\t" \
  "s_sub_u32 s60, s60, s61\n\t" \
  "s_ashr_i32 s60, s60, 8\n\t" \
  "s_sub_i32 s60, s60, " TK_QXW "\n\t" \
  "s_min_i32 s60, s60, 400\n\t" \
  "s_add_u32 s54, s52, s60\n\t" \
  "LGW_%=w:\n\t" \
  "s_sleep 1\n\t" \
  "s_memrealtime s[52:53]\n\t" \
  "s_waitcnt lgkmcnt(0)\n\t" \
  "s_sub_i32 s53, s52, s54\n\t" \
  "s_cmp_lt_i32 s53, 0\n\t" \
  "s_cbranch_scc1 LGW_%=w\n\t" \
  "s_branch LTOP_%=\n\t" \
  "LEND_%=:\n\t" \
  "s_waitcnt vmcnt(0)\n\t" \
  "s_mov_b64 exec, 1\n\t" \
  "buffer_store_dword v6, off, %[X], s49 sc1\n\t" \
  "s_mov_b64 exec, -1\n\t" \
  "s_mov_b32 s49, 0xf0000000\n\t" \
  ""
#define QV_ASM \
  "v_lshrrev_b32 v16, 7, v0\n\t" \
  "v_lshlrev_b32 v16, 13, v16\n\t" \
  "v_lshrrev_b32 v19, 2, v0\n\t" \
  "v_and_b32 v19, 64, v19\n\t" \
  "v_xor_b32 v19, v19, v0\n\t" \
  "v_and_b32 v19, 0x70, v19\n\t" \
  "v_add_u32 v16, v16, v19\n\t" \
  "s_mul_i32 s63, s40, 40960\n\t" \
  "v_and_b32 v19, 0x300, v0\n\t" \
  "v_lshlrev_b32 v17, 2, v19\n\t" \
  "v_and_b32 v19, 0xc0, v0\n\t" \
  "v_lshl_add_u32 v17, v19, 1, v17\n\t" \
  "v_and_b32 v19, 0x30, v0\n\t" \
  "v_lshrrev_b32 v19, 1, v19\n\t" \
  "v_add3_u32 v17, v17, v19, s63\n\t" \
  "v_lshrrev_b32 v19, 1, v0\n\t" \
  "v_and_b32 v19, 64, v19\n\t" \
  "v_add_u32 v17, v17, v19\n\t" \
  "v_xor_b32 v18, 64, v17\n\t" \
  "s_add_u32 m0, s63, 0\n\t" \
  "s_mov_b32 s60, 0\n\t" \
  "buffer_load_dwordx4 v16, %[W], s60 offen lds\n\t" \
  "s_add_u32 m0, s63, 1024\n\t" \
  "s_mov_b32 s60, 65536\n\t" \
  "buffer_load_dwordx4 v16, %[W], s60 offen lds\n\t" \
  "s_add_u32 m0, s63, 2048\n\t" \
  "s_mov_b32 s60, 131072\n\t" \
  "buffer_load_dwordx4 v16, %[W], s60 offen lds\n\t" \
  "s_add_u32 m0, s63, 3072\n\t" \
  "s_mov_b32 s60, 196608\n\t" \
  "buffer_load_dwordx4 v16, %[W], s60 offen lds\n\t" \
  "s_add_u32 m0, s63, 4096\n\t" \
  "s_mov_b32 s60, 262144\n\t" \
  "buffer_load_dwordx4 v16, %[W], s60 offen lds\n\t" \
  "s_add_u32 m0, s63, 5120\n\t" \
  "s_mov_b32 s60, 327680\n\t" \
  "buffer_load_dwordx4 v16, %[W], s60 offen lds\n\t" \
  "s_add_u32 m0, s63, 6144\n\t" \
  "s_mov_b32 s60, 393216\n\t" \
  "buffer_load_dwordx4 v16, %[W], s60 offen lds\n\t" \
  "s_add_u32 m0, s63, 7168\n\t" \
  "s_mov_b32 s60, 458752\n\t" \
  "buffer_load_dwordx4 v16, %[W], s60 offen lds\n\t" \
  "s_add_u32 m0, s63, 8192\n\t" \
  "s_mov_b32 s60, 524288\n\t" \
  "buffer_load_dwordx4 v16, %[W], s60 offen lds\n\t" \
  "s_add_u32 m0, s63, 9216\n\t" \
  "s_mov_b32 s60, 589824\n\t" \
  "buffer_load_dwordx4 v16, %[W], s60 offen lds\n\t" \
  "s_add_u32 m0, s63, 10240\n\t" \
  "s_mov_b32 s60, 655360\n\t" \
  "buffer_load_dwordx4 v16, %[W], s60 offen lds\n\t" \
  "s_add_u32 m0, s63, 11264\n\t" \
  "s_mov_b32 s60, 720896\n\t" \
  "buffer_load_dwordx4 v16, %[W], s60 offen lds\n\t" \
  "s_add_u32 m0, s63, 12288\n\t" \
  "s_mov_b32 s60, 786432\n\t" \
  "buffer_load_dwordx4 v16, %[W], s60 offen lds\n\t" \
  "s_add_u32 m0, s63, 13312\n\t" \
  "s_mov_b32 s60, 851968\n\t" \
  "buffer_load_dwordx4 v16, %[W], s60 offen lds\n\t" \
  "s_add_u32 m0, s63, 14336\n\t" \
  "s_mov_b32 s60, 917504\n\t" \
  "buffer_load_dwordx4 v16, %[W], s60 offen lds\n\t" \
  "s_add_u32 m0, s63, 15360\n\t" \
  "s_mov_b32 s60, 983040\n\t" \
  "buffer_load_dwordx4 v16, %[W], s60 offen lds\n\t" \
  "s_add_u32 m0, s63, 16384\n\t" \
  "s_mov_b32 s60, 1048576\n\t" \
  "buffer_load_dwordx4 v16, %[W], s60 offen lds\n\t" \
  "s_add_u32 m0, s63, 17408\n\t" \
  "s_mov_b32 s60, 1114112\n\t" \
  "buffer_load_dwordx4 v16, %[W], s60 offen lds\n\t" \
  "s_add_u32 m0, s63, 18432\n\t" \
  "s_mov_b32 s60, 1179648\n\t" \
  "buffer_load_dwordx4 v16, %[W], s60 offen lds\n\t" \
  "s_add_u32 m0, s63, 19456\n\t" \
  "s_mov_b32 s60, 1245184\n\t" \
  "buffer_load_dwordx4 v16, %[W], s60 offen lds\n\t" \
  "s_add_u32 m0, s63, 20480\n\t" \
  "s_mov_b32 s60, 1310720\n\t" \
  "buffer_load_dwordx4 v16, %[W], s60 offen lds\n\t" \
  "s_add_u32 m0, s63, 21504\n\t" \
  "s_mov_b32 s60, 1376256\n\t" \
  "buffer_load_dwordx4 v16, %[W], s60 offen lds\n\t" \
  "s_add_u32 m0, s63, 22528\n\t" \
  "s_mov_b32 s60, 1441792\n\t" \
  "buffer_load_dwordx4 v16, %[W], s60 offen lds\n\t" \
  "s_add_u32 m0, s63, 23552\n\t" \
  "s_mov_b32 s60, 1507328\n\t" \
  "buffer_load_dwordx4 v16, %[W], s60 offen lds\n\t" \
  "s_add_u32 m0, s63, 24576\n\t" \
  "s_mov_b32 s60, 1572864\n\t" \
  "buffer_load_dwordx4 v16, %[W], s60 offen lds\n\t" \
  "s_add_u32 m0, s63, 25600\n\t" \
  "s_mov_b32 s60, 1638400\n\t" \
  "buffer_load_dwordx4 v16, %[W], s60 offen lds\n\t" \
  "s_add_u32 m0, s63, 26624\n\t" \
  "s_mov_b32 s60, 1703936\n\t" \
  "buffer_load_dwordx4 v16, %[W], s60 offen lds\n\t" \
  "s_add_u32 m0, s63, 27648\n\t" \
  "s_mov_b32 s60, 1769472\n\t" \
  "buffer_load_dwordx4 v16, %[W], s60 offen lds\n\t" \
  "s_add_u32 m0, s63, 28672\n\t" \
  "s_mov_b32 s60, 1835008\n\t" \
  "buffer_load_dwordx4 v16, %[W], s60 offen lds\n\t" \
  "s_add_u32 m0, s63, 29696\n\t" \
  "s_mov_b32 s60, 1900544\n\t" \
  "buffer_load_dwordx4 v16, %[W], s60 offen lds\n\t" \
  "s_add_u32 m0, s63, 30720\n\t" \
  "s_mov_b32 s60, 1966080\n\t" \
  "buffer_load_dwordx4 v16, %[W], s60 offen lds\n\t" \
  "s_add_u32 m0, s63, 31744\n\t" \
  "s_mov_b32 s60, 2031616\n\t" \
  "buffer_load_dwordx4 v16, %[W], s60 offen lds\n\t" \
  "s_add_u32 m0, s63, 32768\n\t" \
  "s_mov_b32 s60, 2097152\n\t" \
  "buffer_load_dwordx4 v16, %[W], s60 offen lds\n\t" \
  "s_add_u32 m0, s63, 33792\n\t" \
  "s_mov_b32 s60, 2162688\n\t" \
  "buffer_load_dwordx4 v16, %[W], s60 offen lds\n\t" \
  "s_add_u32 m0, s63, 34816\n\t" \
  "s_mov_b32 s60, 2228224\n\t" \
  "buffer_load_dwordx4 v16, %[W], s60 offen lds\n\t" \
  "s_add_u32 m0, s63, 35840\n\t" \
  "s_mov_b32 s60, 2293760\n\t" \
  "buffer_load_dwordx4 v16, %[W], s60 offen lds\n\t" \
  "s_add_u32 m0, s63, 36864\n\t" \
  "s_mov_b32 s60, 2359296\n\t" \
  "buffer_load_dwordx4 v16, %[W], s60 offen lds\n\t" \
  "s_add_u32 m0, s63, 37888\n\t" \
  "s_mov_b32 s60, 2424832\n\t" \
  "buffer_load_dwordx4 v16, %[W], s60 offen lds\n\t" \
  "s_add_u32 m0, s63, 38912\n\t" \
  "s_mov_b32 s60, 2490368\n\t" \
  "buffer_load_dwordx4 v16, %[W], s60 offen lds\n\t" \
  "s_add_u32 m0, s63, 39936\n\t" \
  "s_mov_b32 s60, 2555904\n\t" \
  "buffer_load_dwordx4 v16, %[W], s60 offen lds\n\t" \
  "s_waitcnt vmcnt(32)\n\t" \
  "ds_read_b64_tr_b16 a[0:1], v17 offset:0\n\t" \
  "ds_read_b64_tr_b16 a[2:3], v17 offset:512\n\t" \
  "ds_read_b64_tr_b16 a[4:5], v17 offset:4096\n\t" \
  "ds_read_b64_tr_b16 a[6:7], v17 offset:4608\n\t" \
  "ds_read_b64_tr_b16 a[72:73], v17 offset:32\n\t" \
  "ds_read_b64_tr_b16 a[74:75], v17 offset:544\n\t" \
  "ds_read_b64_tr_b16 a[76:77], v17 offset:4128\n\t" \
  "ds_read_b64_tr_b16 a[78:79], v17 offset:4640\n\t" \
  "ds_read_b64_tr_b16 a[144:145], v18 offset:0\n\t" \
  "ds_read_b64_tr_b16 a[146:147], v18 offset:512\n\t" \
  "ds_read_b64_tr_b16 a[148:149], v18 offset:4096\n\t" \
  "ds_read_b64_tr_b16 a[150:151], v18 offset:4608\n\t" \
  "ds_read_b64_tr_b16 a[216:217], v18 offset:32\n\t" \
  "ds_read_b64_tr_b16 a[218:219], v18 offset:544\n\t" \
  "ds_read_b64_tr_b16 a[220:221], v18 offset:4128\n\t" \
  "ds_read_b64_tr_b16 a[222:223], v18 offset:4640\n\t" \
  "s_waitcnt lgkmcnt(0)\n\t" \
  "s_add_u32 m0, s63, 0\n\t" \
  "s_mov_b32 s60, 2621440\n\t" \
  "buffer_load_dwordx4 v16, %[W], s60 offen lds\n\t" \
  "s_add_u32 m0, s63, 1024\n\t" \
  "s_mov_b32 s60, 2686976\n\t" \
  "buffer_load_dwordx4 v16, %[W], s60 offen lds\n\t" \
  "s_add_u32 m0, s63, 2048\n\t" \
  "s_mov_b32 s60, 2752512\n\t" \
  "buffer_load_dwordx4 v16, %[W], s60 offen lds\n\t" \
  "s_add_u32 m0, s63, 3072\n\t" \
  "s_mov_b32 s60, 2818048\n\t" \
  "buffer_load_dwordx4 v16, %[W], s60 offen lds\n\t" \
  "s_add_u32 m0, s63, 4096\n\t" \
  "s_mov_b32 s60, 2883584\n\t" \
  "buffer_load_dwordx4 v16, %[W], s60 offen lds\n\t" \
  "s_add_u32 m0, s63, 5120\n\t" \
  "s_mov_b32 s60, 2949120\n\t" \
  "buffer_load_dwordx4 v16, %[W], s60 offen lds\n\t" \
  "s_add_u32 m0, s63, 6144\n\t" \
  "s_mov_b32 s60, 3014656\n\t" \
  "buffer_load_dwordx4 v16, %[W], s60 offen lds\n\t" \
  "s_add_u32 m0, s63, 7168\n\t" \
  "s_mov_b32 s60, 3080192\n\t" \
  "buffer_load_dwordx4 v16, %[W], s60 offen lds\n\t" \
  "s_waitcnt vmcnt(32)\n\t" \
  "ds_read_b64_tr_b16 a[8:9], v17 offset:8192\n\t" \
  "ds_read_b64_tr_b16 a[10:11], v17 offset:8704\n\t" \
  "ds_read_b64_tr_b16 a[12:13], v17 offset:12288\n\t" \
  "ds_read_b64_tr_b16 a[14:15], v17 offset:12800\n\t" \
  "ds_read_b64_tr_b16 a[80:81], v17 offset:8224\n\t" \
  "ds_read_b64_tr_b16 a[82:83], v17 offset:8736\n\t" \
  "ds_read_b64_tr_b16 a[84:85], v17 offset:12320\n\t" \
  "ds_read_b64_tr_b16 a[86:87], v17 offset:12832\n\t" \
  "ds_read_b64_tr_b16 a[152:153], v18 offset:8192\n\t" \
  "ds_read_b64_tr_b16 a[154:155], v18 offset:8704\n\t" \
  "ds_read_b64_tr_b16 a[156:157], v18 offset:12288\n\t" \
  "ds_read_b64_tr_b16 a[158:159], v18 offset:12800\n\t" \
  "ds_read_b64_tr_b16 a[224:225], v18 offset:8224\n\t" \
  "ds_read_b64_tr_b16 a[226:227], v18 offset:8736\n\t" \
  "ds_read_b64_tr_b16 a[228:229], v18 offset:12320\n\t" \
  "ds_read_b64_tr_b16 a[230:231], v18 offset:12832\n\t" \
  "s_waitcnt lgkmcnt(0)\n\t" \
  "s_add_u32 m0, s63, 8192\n\t" \
  "s_mov_b32 s60, 3145728\n\t" \
  "buffer_load_dwordx4 v16, %[W], s60 offen lds\n\t" \
  "s_add_u32 m0, s63, 9216\n\t" \
  "s_mov_b32 s60, 3211264\n\t" \
  "buffer_load_dwordx4 v16, %[W], s60 offen lds\n\t" \
  "s_add_u32 m0, s63, 10240\n\t" \
  "s_mov_b32 s60, 3276800\n\t" \
  "buffer_load_dwordx4 v16, %[W], s60 offen lds\n\t" \
  "s_add_u32 m0, s63, 11264\n\t" \
  "s_mov_b32 s60, 3342336\n\t" \
  "buffer_load_dwordx4 v16, %[W], s60 offen lds\n\t" \
  "s_add_u32 m0, s63, 12288\n\t" \
  "s_mov_b32 s60, 3407872\n\t" \
  "buffer_load_dwordx4 v16, %[W], s60 offen lds\n\t" \
  "s_add_u32 m0, s63, 13312\n\t" \
  "s_mov_b32 s60, 3473408\n\t" \
  "buffer_load_dwordx4 v16, %[W], s60 offen lds\n\t" \
  "s_add_u32 m0, s63, 14336\n\t" \
  "s_mov_b32 s60, 3538944\n\t" \
  "buffer_load_dwordx4 v16, %[W], s60 offen lds\n\t" \
  "s_add_u32 m0, s63, 15360\n\t" \
  "s_mov_b32 s60, 3604480\n\t" \
  "buffer_load_dwordx4 v16, %[W], s60 offen lds\n\t" \
  "s_waitcnt vmcnt(32)\n\t" \
  "ds_read_b64_tr_b16 a[16:17], v17 offset:16384\n\t" \
  "ds_read_b64_tr_b16 a[18:19], v17 offset:16896\n\t" \
  "ds_read_b64_tr_b16 a[20:21], v17 offset:20480\n\t" \
  "ds_read_b64_tr_b16 a[22:23], v17 offset:20992\n\t" \
  "ds_read_b64_tr_b16 a[88:89], v17 offset:16416\n\t" \
  "ds_read_b64_tr_b16 a[90:91], v17 offset:16928\n\t" \
  "ds_read_b64_tr_b16 a[92:93], v17 offset:20512\n\t" \
  "ds_read_b64_tr_b16 a[94:95], v17 offset:21024\n\t" \
  "ds_read_b64_tr_b16 a[160:161], v18 offset:16384\n\t" \
  "ds_read_b64_tr_b16 a[162:163], v18 offset:16896\n\t" \
  "ds_read_b64_tr_b16 a[164:165], v18 offset:20480\n\t" \
  "ds_read_b64_tr_b16 a[166:167], v18 offset:20992\n\t" \
  "ds_read_b64_tr_b16 a[232:233], v18 offset:16416\n\t" \
  "ds_read_b64_tr_b16 a[234:235], v18 offset:16928\n\t" \
  "ds_read_b64_tr_b16 a[236:237], v18 offset:20512\n\t" \
  "ds_read_b64_tr_b16 a[238:239], v18 offset:21024\n\t" \
  "s_waitcnt lgkmcnt(0)\n\t" \
  "s_add_u32 m0, s63, 16384\n\t" \
  "s_mov_b32 s60, 3670016\n\t" \
  "buffer_load_dwordx4 v16, %[W], s60 offen lds\n\t" \
  "s_add_u32 m0, s63, 17408\n\t" \
  "s_mov_b32 s60, 3735552\n\t" \
  "buffer_load_dwordx4 v16, %[W], s60 offen lds\n\t" \
  "s_add_u32 m0, s63, 18432\n\t" \
  "s_mov_b32 s60, 3801088\n\t" \
  "buffer_load_dwordx4 v16, %[W], s60 offen lds\n\t" \
  "s_add_u32 m0, s63, 19456\n\t" \
  "s_mov_b32 s60, 3866624\n\t" \
  "buffer_load_dwordx4 v16, %[W], s60 offen lds\n\t" \
  "s_add_u32 m0, s63, 20480\n\t" \
  "s_mov_b32 s60, 3932160\n\t" \
  "buffer_load_dwordx4 v16, %[W], s60 offen lds\n\t" \
  "s_add_u32 m0, s63, 21504\n\t" \
  "s_mov_b32 s60, 3997696\n\t" \
  "buffer_load_dwordx4 v16, %[W], s60 offen lds\n\t" \
  "s_add_u32 m0, s63, 22528\n\t" \
  "s_mov_b32 s60, 4063232\n\t" \
  "buffer_load_dwordx4 v16, %[W], s60 offen lds\n\t" \
  "s_add_u32 m0, s63, 23552\n\t" \
  "s_mov_b32 s60, 4128768\n\t" \
  "buffer_load_dwordx4 v16, %[W], s60 offen lds\n\t" \
  "s_waitcnt vmcnt(32)\n\t" \
  "ds_read_b64_tr_b16 a[24:25], v17 offset:24576\n\t" \
  "ds_read_b64_tr_b16 a[26:27], v17 offset:25088\n\t" \
  "ds_read_b64_tr_b16 a[28:29], v17 offset:28672\n\t" \
  "ds_read_b64_tr_b16 a[30:31], v17 offset:29184\n\t" \
  "ds_read_b64_tr_b16 a[96:97], v17 offset:24608\n\t" \
  "ds_read_b64_tr_b16 a[98:99], v17 offset:25120\n\t" \
  "ds_read_b64_tr_b16 a[100:101], v17 offset:28704\n\t" \
  "ds_read_b64_tr_b16 a[102:103], v17 offset:29216\n\t" \
  "ds_read_b64_tr_b16 a[168:169], v18 offset:24576\n\t" \
  "ds_read_b64_tr_b16 a[170:171], v18 offset:25088\n\t" \
  "ds_read_b64_tr_b16 a[172:173], v18 offset:28672\n\t" \
  "ds_read_b64_tr_b16 a[174:175], v18 offset:29184\n\t" \
  "ds_read_b64_tr_b16 a[240:241], v18 offset:24608\n\t" \
  "ds_read_b64_tr_b16 a[242:243], v18 offset:25120\n\t" \
  "ds_read_b64_tr_b16 a[244:245], v18 offset:28704\n\t" \
  "ds_read_b64_tr_b16 a[246:247], v18 offset:29216\n\t" \
  "s_waitcnt lgkmcnt(0)\n\t" \
  "s_add_u32 m0, s63, 24576\n\t" \
  "s_mov_b32 s60, 4194304\n\t" \
  "buffer_load_dwordx4 v16, %[W], s60 offen lds\n\t" \
  "s_add_u32 m0, s63, 25600\n\t" \
  "s_mov_b32 s60, 4259840\n\t" \
  "buffer_load_dwordx4 v16, %[W], s60 offen lds\n\t" \
  "s_add_u32 m0, s63, 26624\n\t" \
  "s_mov_b32 s60, 4325376\n\t" \
  "buffer_load_dwordx4 v16, %[W], s60 offen lds\n\t" \
  "s_add_u32 m0, s63, 27648\n\t" \
  "s_mov_b32 s60, 4390912\n\t" \
  "buffer_load_dwordx4 v16, %[W], s60 offen lds\n\t" \
  "s_add_u32 m0, s63, 28672\n\t" \
  "s_mov_b32 s60, 4456448\n\t" \
  "buffer_load_dwordx4 v16, %[W], s60 offen lds\n\t" \
  "s_add_u32 m0, s63, 29696\n\t" \
  "s_mov_b32 s60, 4521984\n\t" \
  "buffer_load_dwordx4 v16, %[W], s60 offen lds\n\t" \
  "s_add_u32 m0, s63, 30720\n\t" \
  "s_mov_b32 s60, 4587520\n\t" \
  "buffer_load_dwordx4 v16, %[W], s60 offen lds\n\t" \
  "s_add_u32 m0, s63, 31744\n\t" \
  "s_mov_b32 s60, 4653056\n\t" \
  "buffer_load_dwordx4 v16, %[W], s60 offen lds\n\t" \
  "s_waitcnt vmcnt(32)\n\t" \
  "ds_read_b64_tr_b16 a[32:33], v17 offset:32768\n\t" \
  "ds_read_b64_tr_b16 a[34:35], v17 offset:33280\n\t" \
  "ds_read_b64_tr_b16 a[36:37], v17 offset:36864\n\t" \
  "ds_read_b64_tr_b16 a[38:39], v17 offset:37376\n\t" \
  "ds_read_b64_tr_b16 a[104:105], v17 offset:32800\n\t" \
  "ds_read_b64_tr_b16 a[106:107], v17 offset:33312\n\t" \
  "ds_read_b64_tr_b16 a[108:109], v17 offset:36896\n\t" \
  "ds_read_b64_tr_b16 a[110:111], v17 offset:37408\n\t" \
  "ds_read_b64_tr_b16 a[176:177], v18 offset:32768\n\t" \
  "ds_read_b64_tr_b16 a[178:179], v18 offset:33280\n\t" \
  "ds_read_b64_tr_b16 a[180:181], v18 offset:36864\n\t" \
  "ds_read_b64_tr_b16 a[182:183], v18 offset:37376\n\t" \
  "ds_read_b64_tr_b16 a[248:249], v18 offset:32800\n\t" \
  "ds_read_b64_tr_b16 a[250:251], v18 offset:33312\n\t" \
  "ds_read_b64_tr_b16 a[252:253], v18 offset:36896\n\t" \
  "ds_read_b64_tr_b16 a[254:255], v18 offset:37408\n\t" \
  "s_waitcnt lgkmcnt(0)\n\t" \
  "s_waitcnt vmcnt(24)\n\t" \
  "ds_read_b64_tr_b16 a[40:41], v17 offset:0\n\t" \
  "ds_read_b64_tr_b16 a[42:43], v17 offset:512\n\t" \
  "ds_read_b64_tr_b16 a[44:45], v17 offset:4096\n\t" \
  "ds_read_b64_tr_b16 a[46:47], v17 offset:4608\n\t" \
  "ds_read_b64_tr_b16 a[112:113], v17 offset:32\n\t" \
  "ds_read_b64_tr_b16 a[114:115], v17 offset:544\n\t" \
  "ds_read_b64_tr_b16 a[116:117], v17 offset:4128\n\t" \
  "ds_read_b64_tr_b16 a[118:119], v17 offset:4640\n\t" \
  "ds_read_b64_tr_b16 a[184:185], v18 offset:0\n\t" \
  "ds_read_b64_tr_b16 a[186:187], v18 offset:512\n\t" \
  "ds_read_b64_tr_b16 a[188:189], v18 offset:4096\n\t" \
  "ds_read_b64_tr_b16 a[190:191], v18 offset:4608\n\t" \
  "ds_read_b64_tr_b16 v[224:225], v18 offset:32\n\t" \
  "ds_read_b64_tr_b16 v[226:227], v18 offset:544\n\t" \
  "ds_read_b64_tr_b16 v[228:229], v18 offset:4128\n\t" \
  "ds_read_b64_tr_b16 v[230:231], v18 offset:4640\n\t" \
  "s_waitcnt lgkmcnt(0)\n\t" \
  "s_waitcnt vmcnt(16)\n\t" \
  "ds_read_b64_tr_b16 a[48:49], v17 offset:8192\n\t" \
  "ds_read_b64_tr_b16 a[50:51], v17 offset:8704\n\t" \
  "ds_read_b64_tr_b16 a[52:53], v17 offset:12288\n\t" \
  "ds_read_b64_tr_b16 a[54:55], v17 offset:12800\n\t" \
  "ds_read_b64_tr_b16 a[120:121], v17 offset:8224\n\t" \
  "ds_read_b64_tr_b16 a[122:123], v17 offset:8736\n\t" \
  "ds_read_b64_tr_b16 a[124:125], v17 offset:12320\n\t" \
  "ds_read_b64_tr_b16 a[126:127], v17 offset:12832\n\t" \
  "ds_read_b64_tr_b16 a[192:193], v18 offset:8192\n\t" \
  "ds_read_b64_tr_b16 a[194:195], v18 offset:8704\n\t" \
  "ds_read_b64_tr_b16 a[196:197], v18 offset:12288\n\t" \
  "ds_read_b64_tr_b16 a[198:199], v18 offset:12800\n\t" \
  "ds_read_b64_tr_b16 v[232:233], v18 offset:8224\n\t" \
  "ds_read_b64_tr_b16 v[234:235], v18 offset:8736\n\t" \
  "ds_read_b64_tr_b16 v[236:237], v18 offset:12320\n\t" \
  "ds_read_b64_tr_b16 v[238:239], v18 offset:12832\n\t" \
  "s_waitcnt lgkmcnt(0)\n\t" \
  "s_waitcnt vmcnt(8)\n\t" \
  "ds_read_b64_tr_b16 a[56:57], v17 offset:16384\n\t" \
  "ds_read_b64_tr_b16 a[58:59], v17 offset:16896\n\t" \
  "ds_read_b64_tr_b16 a[60:61], v17 offset:20480\n\t" \
  "ds_read_b64_tr_b16 a[62:63], v17 offset:20992\n\t" \
  "ds_read_b64_tr_b16 a[128:129], v17 offset:16416\n\t" \
  "ds_read_b64_tr_b16 a[130:131], v17 offset:16928\n\t" \
  "ds_read_b64_tr_b16 a[132:133], v17 offset:20512\n\t" \
  "ds_read_b64_tr_b16 a[134:135], v17 offset:21024\n\t" \
  "ds_read_b64_tr_b16 a[200:201], v18 offset:16384\n\t" \
  "ds_read_b64_tr_b16 a[202:203], v18 offset:16896\n\t" \
  "ds_read_b64_tr_b16 a[204:205], v18 offset:20480\n\t" \
  "ds_read_b64_tr_b16 a[206:207], v18 offset:20992\n\t" \
  "ds_read_b64_tr_b16 v[240:241], v18 offset:16416\n\t" \
  "ds_read_b64_tr_b16 v[242:243], v18 offset:16928\n\t" \
  "ds_read_b64_tr_b16 v[244:245], v18 offset:20512\n\t" \
  "ds_read_b64_tr_b16 v[246:247], v18 offset:21024\n\t" \
  "s_waitcnt lgkmcnt(0)\n\t" \
  "s_waitcnt vmcnt(0)\n\t" \
  "ds_read_b64_tr_b16 a[64:65], v17 offset:24576\n\t" \
  "ds_read_b64_tr_b16 a[66:67], v17 offset:25088\n\t" \
  "ds_read_b64_tr_b16 a[68:69], v17 offset:28672\n\t" \
  "ds_read_b64_tr_b16 a[70:71], v17 offset:29184\n\t" \
  "ds_read_b64_tr_b16 a[136:137], v17 offset:24608\n\t" \
  "ds_read_b64_tr_b16 a[138:139], v17 offset:25120\n\t" \
  "ds_read_b64_tr_b16 a[140:141], v17 offset:28704\n\t" \
  "ds_read_b64_tr_b16 a[142:143], v17 offset:29216\n\t" \
  "ds_read_b64_tr_b16 a[208:209], v18 offset:24576\n\t" \
  "ds_read_b64_tr_b16 a[210:211], v18 offset:25088\n\t" \
  "ds_read_b64_tr_b16 a[212:213], v18 offset:28672\n\t" \
  "ds_read_b64_tr_b16 a[214:215], v18 offset:29184\n\t" \
  "ds_read_b64_tr_b16 v[248:249], v18 offset:24608\n\t" \
  "ds_read_b64_tr_b16 v[250:251], v18 offset:25120\n\t" \
  "ds_read_b64_tr_b16 v[252:253], v18 offset:28704\n\t" \
  "ds_read_b64_tr_b16 v[254:255], v18 offset:29216\n\t" \
  "s_waitcnt lgkmcnt(0)\n\t" \
  "s_mov_b32 s84, " TK_CAP "\n\t" \
  "s_mov_b32 s48, 0\n\t" \
  "s_mov_b32 s49, 0xf0000000\n\t" \
  "s_mov_b32 s46, s41\n\t" \
  "s_mov_b32 s56, 0\n\t" \
  "v_lshrrev_b32 v7, 2, v0\n\t" \
  "s_mov_b32 s60, " TK_CTAB "\n\t" \
  "buffer_load_dword v4, v7, %[X], s60 offen offset:88 sc1\n\t" \
  "buffer_load_dword v7, v7, %[X], s60 offen offset:0 sc1\n\t" \
  "s_waitcnt vmcnt(0)\n\t" \
  "v_and_b32 v4, 1, v4\n\t" \
  "v_and_b32 v7, 1, v7\n\t" \
  "v_cmp_ne_u32 s[90:91], s67, v4\n\t" \
  "v_cmp_ne_u32 s[88:89], s67, v7\n\t" \
  "s_nop 1\n\t" \
  "s_mov_b32 s57, 0xf0000000\n\t" \
  "s_add_u32 s83, s48, 0\n\t" \
  "s_lshr_b64 s[60:61], s[90:91], s83\n\t" \
  "s_and_b32 s60, s60, 1\n\t" \
  "s_lshl_b32 s60, s60, 13\n\t" \
  "s_add_u32 s83, s83, " TK_SG0 "\n\t" \
  "s_lshl_b32 s83, s83, 21\n\t" \
  "s_or_b32 s83, s83, s60\n\t" \
  "s_add_u32 s80, s48, " TK_TRN "\n\t" \
  "s_lshr_b64 s[60:61], s[90:91], s80\n\t" \
  "s_and_b32 s60, s60, 1\n\t" \
  "s_lshl_b32 s60, s60, 13\n\t" \
  "s_add_u32 s80, s80, " TK_SG0 "\n\t" \
  "s_lshl_b32 s80, s80, 21\n\t" \
  "s_or_b32 s80, s80, s60\n\t" \
  "s_add_u32 s62, s83, s39\n\t" \
  "s_add_u32 s80, s80, s39\n\t" \
  "s_add_u32 s80, s80, 2048\n\t" \
  "s_cmp_lt_u32 s48, " TK_TRM "\n\t" \
  "s_cselect_b32 s80, s62, s80\n\t" \
  "v_add_u32 v7, s62, v3\n\t" \
  "s_mov_b32 exec_lo, 0\n\t" \
  "v_mov_b32 v7, s80\n\t" \
  "s_mov_b32 exec_lo, -1\n\t" \
  "LCOLD_%=:\n\t" \
  "s_waitcnt vmcnt(0)\n\t" \
  "s_mov_b64 exec, 1\n\t" \
  "buffer_store_dword v6, off, %[X], s49 sc1\n\t" \
  "s_mov_b64 exec, -1\n\t" \
  "s_mov_b32 s49, 0xf0000000\n\t" \
  "s_add_u32 s55, s83, s36\n\t" \
  "s_cmp_lg_u32 s40, 0\n\t" \
  "s_cbranch_scc1 LGO_%=\n\t" \
  "s_and_b32 s62, s45, 3\n\t" \
  "s_lshl_b32 s62, s62, 7\n\t" \
  "s_add_u32 s62, s62, s83\n\t" \
  "s_add_u32 s62, s62, s39\n\t" \
  "s_mov_b32 s61, 0\n\t" \
  "LPO_%=:\n\t" \
  "buffer_load_dword v4, off, %[X], s62 sc1\n\t" \
  "s_waitcnt vmcnt(0)\n\t" \
  "v_cmp_eq_u32 vcc, 0, v4\n\t" \
  "s_cbranch_vccz LPO_%=x\n\t" \
  "s_add_u32 s61, s61, 1\n\t" \
  "s_sleep " TK_PSH "\n\t" \
  "s_cmp_gt_u32 s61, s84\n\t" \
  "s_cbranch_scc1 LPO_%=g\n\t" \
  "s_and_b32 s60, s61, 1023\n\t" \
  "s_cmp_lg_u32 s60, 0\n\t" \
  "s_cbranch_scc1 LPO_%=\n\t" \
  "buffer_load_dword v4, off, %[X], s92 sc1\n\t" \
  "s_waitcnt vmcnt(0)\n\t" \
  "v_readfirstlane_b32 s60, v4\n\t" \
  "s_nop 0\n\t" \
  "s_cmp_eq_u32 s60, 0\n\t" \
  "s_cbranch_scc1 LPO_%=\n\t" \
  "LPO_%=g:\n\t" \
  "s_mov_b32 s84, 0\n\t" \
  "s_or_b32 s60, s48, 1610612736\n\t" \
  "v_mov_b32 v4, s60\n\t" \
  "s_mov_b64 exec, 1\n\t" \
  "buffer_store_dword v4, off, %[X], s92 sc1\n\t" \
  "s_mov_b64 exec, -1\n\t" \
  "LPO_%=x:\n\t" \
  "s_mov_b32 s61, 0\n\t" \
  "LPH_%=:\n\t" \
  "buffer_load_dword v4, v7, %[X], 0 offen sc1\n\t" \
  "s_waitcnt vmcnt(0)\n\t" \
  "v_cmp_eq_u32 vcc, 0, v4\n\t" \
  "s_cbranch_vccz LPH_%=x\n\t" \
  "s_add_u32 s61, s61, 1\n\t" \
  "s_sleep " TK_PSH "\n\t" \
  "s_cmp_gt_u32 s61, s84\n\t" \
  "s_cbranch_scc1 LPH_%=g\n\t" \
  "s_and_b32 s60, s61, 1023\n\t" \
  "s_cmp_lg_u32 s60, 0\n\t" \
  "s_cbranch_scc1 LPH_%=\n\t" \
  "buffer_load_dword v4, off, %[X], s92 sc1\n\t" \
  "s_waitcnt vmcnt(0)\n\t" \
  "v_readfirstlane_b32 s60, v4\n\t" \
  "s_nop 0\n\t" \
  "s_cmp_eq_u32 s60, 0\n\t" \
  "s_cbranch_scc1 LPH_%=\n\t" \
  "LPH_%=g:\n\t" \
  "s_mov_b32 s84, 0\n\t" \
  "s_or_b32 s60, s48, 1610612736\n\t" \
  "v_mov_b32 v4, s60\n\t" \
  "s_mov_b64 exec, 1\n\t" \
  "buffer_store_dword v4, off, %[X], s92 sc1\n\t" \
  "s_mov_b64 exec, -1\n\t" \
  "LPH_%=x:\n\t" \
  "s_cmp_lt_i32 s57, 0\n\t" \
  "s_cbranch_scc1 LGO_%=\n\t" \
  "s_movk_i32 s63, 4000\n\t" \
  "s_branch LGC_%=u\n\t" \
  "LGC_%=:\n\t" \
  "s_sleep " TK_PSG "\n\t" \
  "buffer_load_dword v8, off, %[X], s57 sc1\n\t" \
  "s_waitcnt vmcnt(0)\n\t" \
  "LGC_%=u:\n\t" \
  "s_memrealtime s[52:53]\n\t" \
  "v_readfirstlane_b32 s60, v8\n\t" \
  "s_waitcnt lgkmcnt(0)\n\t" \
  "s_sub_u32 s63, s63, 1\n\t" \
  "s_cbranch_scc1 LGO_%=\n\t" \
  "s_cmp_eq_u32 s60, -1\n\t" \
  "s_cbranch_scc1 LGC_%=\n\t" \
  "s_not_b32 s60, s60\n\t" \
  "s_lshl_b32 s61, s52, 8\n\t" \
  "s_sub_u32 s60, s60, s61\n\t" \
  "s_ashr_i32 s60, s60, 8\n\t" \
  "s_sub_i32 s60, s60, " TK_QXC "\n\t" \
  "s_min_i32 s60, s60, 400\n\t" \
  "s_add_u32 s54, s52, s60\n\t" \
  "LGC_%=w:\n\t" \
  "s_sleep 1\n\t" \
  "s_memrealtime s[52:53]\n\t" \
  "s_waitcnt lgkmcnt(0)\n\t" \
  "s_sub_i32 s53, s52, s54\n\t" \
  "s_cmp_lt_i32 s53, 0\n\t" \
  "s_cbranch_scc1 LGC_%=w\n\t" \
  "LGO_%=:\n\t" \
  "s_barrier\n\t" \
  "s_add_u32 m0, s46, 0\n\t" \
  "s_add_u32 s47, s55, 0\n\t" \
  "buffer_load_dwordx4 v0, %[X], s47 offen sc1 lds\n\t" \
  "s_add_u32 m0, s46, 4096\n\t" \
  "s_add_u32 s47, s55, 4096\n\t" \
  "buffer_load_dwordx4 v0, %[X], s47 offen sc1 lds\n\t" \
  "s_add_u32 m0, s46, 8192\n\t" \
  "s_add_u32 s47, s55, 16384\n\t" \
  "buffer_load_dwordx4 v0, %[X], s47 offen sc1 lds\n\t" \
  "s_add_u32 m0, s46, 12288\n\t" \
  "s_add_u32 s47, s55, 20480\n\t" \
  "buffer_load_dwordx4 v0, %[X], s47 offen sc1 lds\n\t" \
  "s_add_u32 m0, s46, 16384\n\t" \
  "s_add_u32 s47, s55, 32768\n\t" \
  "buffer_load_dwordx4 v0, %[X], s47 offen sc1 lds\n\t" \
  "s_add_u32 m0, s46, 20480\n\t" \
  "s_add_u32 s47, s55, 36864\n\t" \
  "buffer_load_dwordx4 v0, %[X], s47 offen sc1 lds\n\t" \
  "s_add_u32 m0, s46, 24576\n\t" \
  "s_add_u32 s47, s55, 49152\n\t" \
  "buffer_load_dwordx4 v0, %[X], s47 offen sc1 lds\n\t" \
  "s_add_u32 m0, s46, 28672\n\t" \
  "s_add_u32 s47, s55, 53248\n\t" \
  "buffer_load_dwordx4 v0, %[X], s47 offen sc1 lds\n\t" \
  "s_add_u32 m0, s46, 32768\n\t" \
  "s_add_u32 s47, s55, 65536\n\t" \
  "buffer_load_dwordx4 v0, %[X], s47 offen sc1 lds\n\t" \
  "s_waitcnt vmcnt(5)\n\t" \
  "LTOP_%=:\n\t" \
  "s_barrier\n\t" \
  "ds_read_b128 v[64:67], v1 offset:0\n\t" \
  "ds_read_b128 v[68:71], v1 offset:2048\n\t" \
  "ds_read_b128 v[72:75], v2 offset:0\n\t" \
  "ds_read_b128 v[76:79], v2 offset:2048\n\t" \
  "ds_read_b128 v[80:83], v1 offset:4096\n\t" \
  "ds_read_b128 v[84:87], v1 offset:6144\n\t" \
  "s_mov_b32 s82, s83\n\t" \
  "s_xor_b32 s46, s46, 0x10000\n\t" \
  "s_waitcnt lgkmcnt(5)\n\t" \
  "v_mfma_f32_16x16x32_bf16 v[16:19], v[64:67], a[0:3], 0\n\t" \
  "ds_read_b128 v[88:91], v2 offset:4096\n\t" \
  "s_waitcnt lgkmcnt(4)\n\t" \
  "v_mfma_f32_16x16x32_bf16 v[16:19], v[72:75], a[4:7], v[16:19]\n\t" \
  "s_add_u32 s50, s48, 0\n\t" \
  "s_and_b32 s50, s50, " TK_TRM "\n\t" \
  "v_mfma_f32_16x16x32_bf16 v[20:23], v[68:71], a[0:3], 0\n\t" \
  "ds_read_b128 v[92:95], v2 offset:6144\n\t" \
  "s_waitcnt lgkmcnt(4)\n\t" \
  "v_mfma_f32_16x16x32_bf16 v[20:23], v[76:79], a[4:7], v[20:23]\n\t" \
  "s_lshr_b64 s[60:61], s[90:91], s50\n\t" \
  "s_and_b32 s60, s60, 1\n\t" \
  "v_mfma_f32_16x16x32_bf16 v[24:27], v[64:67], a[72:75], 0\n\t" \
  "ds_read_b128 v[96:99], v1 offset:8192\n\t" \
  "v_mfma_f32_16x16x32_bf16 v[24:27], v[72:75], a[76:79], v[24:27]\n\t" \
  "s_lshl_b32 s60, s60, 13\n\t" \
  "s_add_u32 s50, s50, " TK_SG0 "\n\t" \
  "v_mfma_f32_16x16x32_bf16 v[28:31], v[68:71], a[72:75], 0\n\t" \
  "ds_read_b128 v[100:103], v1 offset:10240\n\t" \
  "v_mfma_f32_16x16x32_bf16 v[28:31], v[76:79], a[76:79], v[28:31]\n\t" \
  "s_lshl_b32 s50, s50, 21\n\t" \
  "s_or_b32 s50, s50, s60\n\t" \
  "s_waitcnt lgkmcnt(5)\n\t" \
  "v_mfma_f32_16x16x32_bf16 v[16:19], v[80:83], a[8:11], v[16:19]\n\t" \
  "ds_read_b128 v[104:107], v2 offset:8192\n\t" \
  "s_waitcnt lgkmcnt(4)\n\t" \
  "v_mfma_f32_16x16x32_bf16 v[16:19], v[88:91], a[12:15], v[16:19]\n\t" \
  "s_add_u32 s50, s50, s37\n\t" \
  "s_add_u32 s51, s50, 4096\n\t" \
  "v_mfma_f32_16x16x32_bf16 v[20:23], v[84:87], a[8:11], v[20:23]\n\t" \
  "ds_read_b128 v[108:111], v2 offset:10240\n\t" \
  "s_waitcnt lgkmcnt(4)\n\t" \
  "v_mfma_f32_16x16x32_bf16 v[20:23], v[92:95], a[12:15], v[20:23]\n\t" \
  "s_add_u32 s83, s48, 1\n\t" \
  "s_lshr_b64 s[60:61], s[90:91], s83\n\t" \
  "v_mfma_f32_16x16x32_bf16 v[24:27], v[80:83], a[80:83], v[24:27]\n\t" \
  "ds_read_b128 v[112:115], v1 offset:12288\n\t" \
  "v_mfma_f32_16x16x32_bf16 v[24:27], v[88:91], a[84:87], v[24:27]\n\t" \
  "s_and_b32 s60, s60, 1\n\t" \
  "s_lshl_b32 s60, s60, 13\n\t" \
  "v_mfma_f32_16x16x32_bf16 v[28:31], v[84:87], a[80:83], v[28:31]\n\t" \
  "ds_read_b128 v[116:119], v1 offset:14336\n\t" \
  "v_mfma_f32_16x16x32_bf16 v[28:31], v[92:95], a[84:87], v[28:31]\n\t" \
  "s_add_u32 s83, s83, " TK_SG0 "\n\t" \
  "s_lshl_b32 s83, s83, 21\n\t" \
  "s_waitcnt lgkmcnt(5)\n\t" \
  "v_mfma_f32_16x16x32_bf16 v[16:19], v[96:99], a[16:19], v[16:19]\n\t" \
  "ds_read_b128 v[120:123], v2 offset:12288\n\t" \
  "s_waitcnt lgkmcnt(4)\n\t" \
  "v_mfma_f32_16x16x32_bf16 v[16:19], v[104:107], a[20:23], v[16:19]\n\t" \
  "s_or_b32 s83, s83, s60\n\t" \
  "s_cmp_lg_u32 s56, 0\n\t" \
  "v_mfma_f32_16x16x32_bf16 v[20:23], v[100:103], a[16:19], v[20:23]\n\t" \
  "ds_read_b128 v[124:127], v2 offset:14336\n\t" \
  "s_waitcnt lgkmcnt(4)\n\t" \
  "v_mfma_f32_16x16x32_bf16 v[20:23], v[108:111], a[20:23], v[20:23]\n\t" \
  "s_cbranch_scc1 LWARM_%=\n\t" \
  "s_waitcnt vmcnt(0)\n\t" \
  "LWARM_%=:\n\t" \
  "s_barrier\n\t" \
  "v_mfma_f32_16x16x32_bf16 v[24:27], v[96:99], a[88:91], v[24:27]\n\t" \
  "ds_read_b128 v[128:131], v1 offset:16384\n\t" \
  "v_mfma_f32_16x16x32_bf16 v[24:27], v[104:107], a[92:95], v[24:27]\n\t" \
  "v_mfma_f32_16x16x32_bf16 v[28:31], v[100:103], a[88:91], v[28:31]\n\t" \
  "ds_read_b128 v[132:135], v1 offset:18432\n\t" \
  "v_mfma_f32_16x16x32_bf16 v[28:31], v[108:111], a[92:95], v[28:31]\n\t" \
  "s_add_i32 s62, s48, s42\n\t" \
  "s_add_i32 s62, s62, 1\n\t" \
  "s_waitcnt lgkmcnt(5)\n\t" \
  "v_mfma_f32_16x16x32_bf16 v[16:19], v[112:115], a[24:27], v[16:19]\n\t" \
  "ds_read_b128 v[136:139], v2 offset:16384\n\t" \
  "s_waitcnt lgkmcnt(4)\n\t" \
  "v_mfma_f32_16x16x32_bf16 v[16:19], v[120:123], a[28:31], v[16:19]\n\t" \
  "s_mov_b32 s61, s62\n\t" \
  "v_mfma_f32_16x16x32_bf16 v[20:23], v[116:119], a[24:27], v[20:23]\n\t" \
  "ds_read_b128 v[140:143], v2 offset:18432\n\t" \
  "s_waitcnt lgkmcnt(4)\n\t" \
  "v_mfma_f32_16x16x32_bf16 v[20:23], v[124:127], a[28:31], v[20:23]\n\t" \
  "s_mul_i32 s63, s62, 43\n\t" \
  "s_lshr_b32 s63, s63, 9\n\t" \
  "v_mfma_f32_16x16x32_bf16 v[24:27], v[112:115], a[96:99], v[24:27]\n\t" \
  "ds_read_b128 v[144:147], v1 offset:20480\n\t" \
  "v_mfma_f32_16x16x32_bf16 v[24:27], v[120:123], a[100:103], v[24:27]\n\t" \
  "s_mul_i32 s63, s63, " TK_RM "\n\t" \
  "s_sub_u32 s62, s62, s63\n\t" \
  "v_mfma_f32_16x16x32_bf16 v[28:31], v[116:119], a[96:99], v[28:31]\n\t" \
  "ds_read_b128 v[148:151], v1 offset:22528\n\t" \
  "v_mfma_f32_16x16x32_bf16 v[28:31], v[124:127], a[100:103], v[28:31]\n\t" \
  "s_mul_i32 s63, s62, 43\n\t" \
  "s_lshr_b32 s63, s63, 7\n\t" \
  "s_waitcnt lgkmcnt(5)\n\t" \
  "v_mfma_f32_16x16x32_bf16 v[16:19], v[128:131], a[32:35], v[16:19]\n\t" \
  "ds_read_b128 v[152:155], v2 offset:20480\n\t" \
  "s_waitcnt lgkmcnt(4)\n\t" \
  "v_mfma_f32_16x16x32_bf16 v[16:19], v[136:139], a[36:39], v[16:19]\n\t" \
  "s_mul_i32 s57, s62, 688128\n\t" \
  "s_lshl_b32 s60, s63, 15\n\t" \
  "v_mfma_f32_16x16x32_bf16 v[20:23], v[132:135], a[32:35], v[20:23]\n\t" \
  "ds_read_b128 v[156:159], v2 offset:22528\n\t" \
  "s_waitcnt lgkmcnt(4)\n\t" \
  "v_mfma_f32_16x16x32_bf16 v[20:23], v[140:143], a[36:39], v[20:23]\n\t" \
  "s_add_u32 s57, s57, s60\n\t" \
  "s_lshr_b32 s60, s88, s63\n\t" \
  "v_mfma_f32_16x16x32_bf16 v[24:27], v[128:131], a[104:107], v[24:27]\n\t" \
  "ds_read_b128 v[160:163], v1 offset:24576\n\t" \
  "v_mfma_f32_16x16x32_bf16 v[24:27], v[136:139], a[108:111], v[24:27]\n\t" \
  "s_and_b32 s60, s60, 1\n\t" \
  "s_lshl_b32 s60, s60, 13\n\t" \
  "v_mfma_f32_16x16x32_bf16 v[28:31], v[132:135], a[104:107], v[28:31]\n\t" \
  "ds_read_b128 v[164:167], v1 offset:26624\n\t" \
  "v_mfma_f32_16x16x32_bf16 v[28:31], v[140:143], a[108:111], v[28:31]\n\t" \
  "s_add_u32 s57, s57, s60\n\t" \
  "s_add_u32 s57, s57, 656896\n\t" \
  "s_waitcnt lgkmcnt(5)\n\t" \
  "v_mfma_f32_16x16x32_bf16 v[16:19], v[144:147], a[40:43], v[16:19]\n\t" \
  "ds_read_b128 v[168:171], v2 offset:24576\n\t" \
  "s_waitcnt lgkmcnt(4)\n\t" \
  "v_mfma_f32_16x16x32_bf16 v[16:19], v[152:155], a[44:47], v[16:19]\n\t" \
  "s_cmp_lt_i32 s61, 1\n\t" \
  "s_cselect_b32 s57, 0xf0000000, s57\n\t" \
  "v_mfma_f32_16x16x32_bf16 v[20:23], v[148:151], a[40:43], v[20:23]\n\t" \
  "ds_read_b128 v[172:175], v2 offset:26624\n\t" \
  "s_waitcnt lgkmcnt(4)\n\t" \
  "v_mfma_f32_16x16x32_bf16 v[20:23], v[156:159], a[44:47], v[20:23]\n\t" \
  "s_cmp_lg_u32 s40, 0\n\t" \
  "s_cselect_b32 s57, 0xf0000000, s57\n\t" \
  "v_mfma_f32_16x16x32_bf16 v[24:27], v[144:147], a[112:115], v[24:27]\n\t" \
  "ds_read_b128 v[176:179], v1 offset:28672\n\t" \
  "v_mfma_f32_16x16x32_bf16 v[24:27], v[152:155], a[116:119], v[24:27]\n\t" \
  "s_add_u32 s80, s48, " TK_TRN "\n\t" \
  "s_lshr_b64 s[60:61], s[90:91], s80\n\t" \
  "s_and_b32 s60, s60, 1\n\t" \
  "v_mfma_f32_16x16x32_bf16 v[28:31], v[148:151], a[112:115], v[28:31]\n\t" \
  "ds_read_b128 v[180:183], v1 offset:30720\n\t" \
  "v_mfma_f32_16x16x32_bf16 v[28:31], v[156:159], a[116:119], v[28:31]\n\t" \
  "s_lshl_b32 s60, s60, 13\n\t" \
  "s_add_u32 s80, s80, " TK_SG0 "\n\t" \
  "s_lshl_b32 s80, s80, 21\n\t" \
  "s_waitcnt lgkmcnt(5)\n\t" \
  "v_mfma_f32_16x16x32_bf16 v[16:19], v[160:163], a[48:51], v[16:19]\n\t" \
  "ds_read_b128 v[184:187], v2 offset:28672\n\t" \
  "s_waitcnt lgkmcnt(4)\n\t" \
  "v_mfma_f32_16x16x32_bf16 v[16:19], v[168:171], a[52:55], v[16:19]\n\t" \
  "s_or_b32 s80, s80, s60\n\t" \
  "s_add_u32 s62, s83, s39\n\t" \
  "s_add_u32 s80, s80, s39\n\t" \
  "v_mfma_f32_16x16x32_bf16 v[20:23], v[164:167], a[48:51], v[20:23]\n\t" \
  "ds_read_b128 v[188:191], v2 offset:30720\n\t" \
  "s_waitcnt lgkmcnt(4)\n\t" \
  "v_mfma_f32_16x16x32_bf16 v[20:23], v[172:175], a[52:55], v[20:23]\n\t" \
  "s_add_u32 s80, s80, 2048\n\t" \
  "v_mfma_f32_16x16x32_bf16 v[24:27], v[160:163], a[120:123], v[24:27]\n\t" \
  "ds_read_b128 v[192:195], v1 offset:32768\n\t" \
  "v_mfma_f32_16x16x32_bf16 v[24:27], v[168:171], a[124:127], v[24:27]\n\t" \
  "v_mfma_f32_16x16x32_bf16 v[28:31], v[164:167], a[120:123], v[28:31]\n\t" \
  "ds_read_b128 v[196:199], v1 offset:34816\n\t" \
  "v_mfma_f32_16x16x32_bf16 v[28:31], v[172:175], a[124:127], v[28:31]\n\t" \
  "s_cmp_lt_u32 s48, " TK_TRM "\n\t" \
  "s_cselect_b32 s80, s62, s80\n\t" \
  "s_waitcnt lgkmcnt(5)\n\t" \
  "v_mfma_f32_16x16x32_bf16 v[16:19], v[176:179], a[56:59], v[16:19]\n\t" \
  "ds_read_b128 v[200:203], v2 offset:32768\n\t" \
  "s_waitcnt lgkmcnt(4)\n\t" \
  "v_mfma_f32_16x16x32_bf16 v[16:19], v[184:187], a[60:63], v[16:19]\n\t" \
  "v_add_u32 v7, s62, v3\n\t" \
  "s_mov_b32 exec_lo, 0\n\t" \
  "v_mov_b32 v7, s80\n\t" \
  "s_mov_b32 exec_lo, -1\n\t" \
  "v_mfma_f32_16x16x32_bf16 v[20:23], v[180:183], a[56:59], v[20:23]\n\t" \
  "ds_read_b128 v[204:207], v2 offset:34816\n\t" \
  "s_waitcnt lgkmcnt(4)\n\t" \
  "v_mfma_f32_16x16x32_bf16 v[20:23], v[188:191], a[60:63], v[20:23]\n\t" \
  "v_mfma_f32_16x16x32_bf16 v[24:27], v[176:179], a[128:131], v[24:27]\n\t" \
  "v_mfma_f32_16x16x32_bf16 v[24:27], v[184:187], a[132:135], v[24:27]\n\t" \
  "buffer_load_dword v4, v7, %[X], 0 offen sc1\n\t" \
  "v_mfma_f32_16x16x32_bf16 v[28:31], v[180:183], a[128:131], v[28:31]\n\t" \
  "v_mfma_f32_16x16x32_bf16 v[28:31], v[188:191], a[132:135], v[28:31]\n\t" \
  "s_waitcnt lgkmcnt(3)\n\t" \
  "v_mfma_f32_16x16x32_bf16 v[16:19], v[192:195], a[64:67], v[16:19]\n\t" \
  "v_xor_b32 v1, 0x10000, v1\n\t" \
  "v_xor_b32 v2, 0x10000, v2\n\t" \
  "s_waitcnt lgkmcnt(1)\n\t" \
  "v_mfma_f32_16x16x32_bf16 v[16:19], v[200:203], a[68:71], v[16:19]\n\t" \
  "v_mfma_f32_16x16x32_bf16 v[20:23], v[196:199], a[64:67], v[20:23]\n\t" \
  "s_waitcnt lgkmcnt(0)\n\t" \
  "v_mfma_f32_16x16x32_bf16 v[20:23], v[204:207], a[68:71], v[20:23]\n\t" \
  "v_mfma_f32_16x16x32_bf16 v[24:27], v[192:195], a[136:139], v[24:27]\n\t" \
  "v_mfma_f32_16x16x32_bf16 v[24:27], v[200:203], a[140:143], v[24:27]\n\t" \
  "v_mfma_f32_16x16x32_bf16 v[28:31], v[196:199], a[136:139], v[28:31]\n\t" \
  "v_mfma_f32_16x16x32_bf16 v[28:31], v[204:207], a[140:143], v[28:31]\n\t" \
  "v_mfma_f32_16x16x32_bf16 v[32:35], v[64:67], a[144:147], 0\n\t" \
  "v_cvt_pk_bf16_f32 v48, v16, v17\n\t" \
  "v_cvt_pk_bf16_f32 v49, v18, v19\n\t" \
  "v_mfma_f32_16x16x32_bf16 v[32:35], v[72:75], a[148:151], v[32:35]\n\t" \
  "v_cvt_pk_bf16_f32 v50, v20, v21\n\t" \
  "v_cvt_pk_bf16_f32 v51, v22, v23\n\t" \
  "v_mfma_f32_16x16x32_bf16 v[32:35], v[80:83], a[152:155], v[32:35]\n\t" \
  "v_cvt_pk_bf16_f32 v52, v24, v25\n\t" \
  "v_cvt_pk_bf16_f32 v53, v26, v27\n\t" \
  "v_mfma_f32_16x16x32_bf16 v[32:35], v[88:91], a[156:159], v[32:35]\n\t" \
  "v_cvt_pk_bf16_f32 v54, v28, v29\n\t" \
  "v_cvt_pk_bf16_f32 v55, v30, v31\n\t" \
  "v_mfma_f32_16x16x32_bf16 v[32:35], v[96:99], a[160:163], v[32:35]\n\t" \
  "buffer_store_dwordx4 v[48:51], v0, %[X], s50 offen offset:0 sc1\n\t" \
  "v_mfma_f32_16x16x32_bf16 v[32:35], v[104:107], a[164:167], v[32:35]\n\t" \
  "v_mfma_f32_16x16x32_bf16 v[32:35], v[112:115], a[168:171], v[32:35]\n\t" \
  "v_mfma_f32_16x16x32_bf16 v[32:35], v[120:123], a[172:175], v[32:35]\n\t" \
  "v_mfma_f32_16x16x32_bf16 v[32:35], v[128:131], a[176:179], v[32:35]\n\t" \
  "v_mfma_f32_16x16x32_bf16 v[32:35], v[136:139], a[180:183], v[32:35]\n\t" \
  "v_mfma_f32_16x16x32_bf16 v[32:35], v[144:147], a[184:187], v[32:35]\n\t" \
  "v_mfma_f32_16x16x32_bf16 v[32:35], v[152:155], a[188:191], v[32:35]\n\t" \
  "v_mfma_f32_16x16x32_bf16 v[32:35], v[160:163], a[192:195], v[32:35]\n\t" \
  "v_mfma_f32_16x16x32_bf16 v[32:35], v[168:171], a[196:199], v[32:35]\n\t" \
  "v_mfma_f32_16x16x32_bf16 v[32:35], v[176:179], a[200:203], v[32:35]\n\t" \
  "v_mfma_f32_16x16x32_bf16 v[32:35], v[184:187], a[204:207], v[32:35]\n\t" \
  "s_waitcnt vmcnt(1)\n\t" \
  "v_cmp_eq_u32 vcc, 0, v4\n\t" \
  "v_mfma_f32_16x16x32_bf16 v[32:35], v[192:195], a[208:211], v[32:35]\n\t" \
  "v_mfma_f32_16x16x32_bf16 v[32:35], v[200:203], a[212:215], v[32:35]\n\t" \
  "s_cmp_eq_u64 vcc, 0\n\t" \
  "s_cselect_b32 s60, 1, 0\n\t" \
  "v_mov_b32 v4, s60\n\t" \
  "v_mfma_f32_16x16x32_bf16 v[36:39], v[68:71], a[144:147], 0\n\t" \
  "ds_write_b32 v5, v4\n\t" \
  "v_mfma_f32_16x16x32_bf16 v[36:39], v[76:79], a[148:151], v[36:39]\n\t" \
  "v_mfma_f32_16x16x32_bf16 v[36:39], v[84:87], a[152:155], v[36:39]\n\t" \
  "v_cvt_pk_bf16_f32 v56, v32, v33\n\t" \
  "v_cvt_pk_bf16_f32 v57, v34, v35\n\t" \
  "v_mfma_f32_16x16x32_bf16 v[36:39], v[92:95], a[156:159], v[36:39]\n\t" \
  "buffer_store_dwordx4 v[52:55], v0, %[X], s50 offen offset:1024 sc1\n\t" \
  "v_mfma_f32_16x16x32_bf16 v[36:39], v[100:103], a[160:163], v[36:39]\n\t" \
  "v_mfma_f32_16x16x32_bf16 v[36:39], v[108:111], a[164:167], v[36:39]\n\t" \
  "s_waitcnt lgkmcnt(0)\n\t" \
  "s_barrier\n\t" \
  "v_mfma_f32_16x16x32_bf16 v[36:39], v[116:119], a[168:171], v[36:39]\n\t" \
  "ds_read_b32 v4, v6 offset:40959\n\t" \
  "v_mfma_f32_16x16x32_bf16 v[36:39], v[124:127], a[172:175], v[36:39]\n\t" \
  "v_mfma_f32_16x16x32_bf16 v[36:39], v[132:135], a[176:179], v[36:39]\n\t" \
  "v_mfma_f32_16x16x32_bf16 v[36:39], v[140:143], a[180:183], v[36:39]\n\t" \
  "v_mfma_f32_16x16x32_bf16 v[36:39], v[148:151], a[184:187], v[36:39]\n\t" \
  "v_mfma_f32_16x16x32_bf16 v[36:39], v[156:159], a[188:191], v[36:39]\n\t" \
  "s_waitcnt lgkmcnt(0)\n\t" \
  "v_readfirstlane_b32 s60, v4\n\t" \
  "s_add_u32 s55, s83, s36\n\t" \
  "v_mfma_f32_16x16x32_bf16 v[36:39], v[164:167], a[192:195], v[36:39]\n\t" \
  "s_cmp_eq_u32 s48, 63\n\t" \
  "s_cselect_b32 s56, 0, s60\n\t" \
  "v_mfma_f32_16x16x32_bf16 v[36:39], v[172:175], a[196:199], v[36:39]\n\t" \
  "s_cmp_eq_u32 s56, 0\n\t" \
  "s_cselect_b32 s55, 0xf0000000, s55\n\t" \
  "v_mfma_f32_16x16x32_bf16 v[36:39], v[180:183], a[200:203], v[36:39]\n\t" \
  "v_mfma_f32_16x16x32_bf16 v[36:39], v[188:191], a[204:207], v[36:39]\n\t" \
  "v_mfma_f32_16x16x32_bf16 v[36:39], v[196:199], a[208:211], v[36:39]\n\t" \
  "s_add_u32 m0, s46, 0\n\t" \
  "s_add_u32 s47, s55, 0\n\t" \
  "buffer_load_dwordx4 v0, %[X], s47 offen sc1 lds\n\t" \
  "v_mfma_f32_16x16x32_bf16 v[36:39], v[204:207], a[212:215], v[36:39]\n\t" \
  "v_mfma_f32_16x16x32_bf16 v[40:43], v[64:67], a[216:219], 0\n\t" \
  "v_mfma_f32_16x16x32_bf16 v[40:43], v[72:75], a[220:223], v[40:43]\n\t" \
  "v_mfma_f32_16x16x32_bf16 v[40:43], v[80:83], a[224:227], v[40:43]\n\t" \
  "s_add_u32 m0, s46, 4096\n\t" \
  "s_add_u32 s47, s55, 4096\n\t" \
  "buffer_load_dwordx4 v0, %[X], s47 offen sc1 lds\n\t" \
  "v_mfma_f32_16x16x32_bf16 v[40:43], v[88:91], a[228:231], v[40:43]\n\t" \
  "v_cvt_pk_bf16_f32 v58, v36, v37\n\t" \
  "v_cvt_pk_bf16_f32 v59, v38, v39\n\t" \
  "v_mfma_f32_16x16x32_bf16 v[40:43], v[96:99], a[232:235], v[40:43]\n\t" \
  "v_mfma_f32_16x16x32_bf16 v[40:43], v[104:107], a[236:239], v[40:43]\n\t" \
  "buffer_store_dwordx4 v[56:59], v0, %[X], s50 offen offset:2048 sc1\n\t" \
  "v_mfma_f32_16x16x32_bf16 v[40:43], v[112:115], a[240:243], v[40:43]\n\t" \
  "s_add_u32 m0, s46, 8192\n\t" \
  "s_add_u32 s47, s55, 16384\n\t" \
  "buffer_load_dwordx4 v0, %[X], s47 offen sc1 lds\n\t" \
  "v_mfma_f32_16x16x32_bf16 v[40:43], v[120:123], a[244:247], v[40:43]\n\t" \
  "v_mfma_f32_16x16x32_bf16 v[40:43], v[128:131], a[248:251], v[40:43]\n\t" \
  "v_mfma_f32_16x16x32_bf16 v[40:43], v[136:139], a[252:255], v[40:43]\n\t" \
  "v_mfma_f32_16x16x32_bf16 v[40:43], v[144:147], v[224:227], v[40:43]\n\t" \
  "s_add_u32 m0, s46, 12288\n\t" \
  "s_add_u32 s47, s55, 20480\n\t" \
  "buffer_load_dwordx4 v0, %[X], s47 offen sc1 lds\n\t" \
  "v_mfma_f32_16x16x32_bf16 v[40:43], v[152:155], v[228:231], v[40:43]\n\t" \
  "v_mfma_f32_16x16x32_bf16 v[40:43], v[160:163], v[232:235], v[40:43]\n\t" \
  "v_mfma_f32_16x16x32_bf16 v[40:43], v[168:171], v[236:239], v[40:43]\n\t" \
  "v_mfma_f32_16x16x32_bf16 v[40:43], v[176:179], v[240:243], v[40:43]\n\t" \
  "s_add_u32 m0, s46, 16384\n\t" \
  "s_add_u32 s47, s55, 32768\n\t" \
  "buffer_load_dwordx4 v0, %[X], s47 offen sc1 lds\n\t" \
  "v_mfma_f32_16x16x32_bf16 v[40:43], v[184:187], v[244:247], v[40:43]\n\t" \
  "v_mfma_f32_16x16x32_bf16 v[40:43], v[192:195], v[248:251], v[40:43]\n\t" \
  "v_mfma_f32_16x16x32_bf16 v[40:43], v[200:203], v[252:255], v[40:43]\n\t" \
  "v_mfma_f32_16x16x32_bf16 v[44:47], v[68:71], a[216:219], 0\n\t" \
  "s_add_u32 m0, s46, 20480\n\t" \
  "s_add_u32 s47, s55, 36864\n\t" \
  "buffer_load_dwordx4 v0, %[X], s47 offen sc1 lds\n\t" \
  "v_mfma_f32_16x16x32_bf16 v[44:47], v[76:79], a[220:223], v[44:47]\n\t" \
  "v_mfma_f32_16x16x32_bf16 v[44:47], v[84:87], a[224:227], v[44:47]\n\t" \
  "v_mfma_f32_16x16x32_bf16 v[44:47], v[92:95], a[228:231], v[44:47]\n\t" \
  "v_cvt_pk_bf16_f32 v60, v40, v41\n\t" \
  "v_cvt_pk_bf16_f32 v61, v42, v43\n\t" \
  "v_mfma_f32_16x16x32_bf16 v[44:47], v[100:103], a[232:235], v[44:47]\n\t" \
  "s_add_u32 m0, s46, 24576\n\t" \
  "s_add_u32 s47, s55, 49152\n\t" \
  "buffer_load_dwordx4 v0, %[X], s47 offen sc1 lds\n\t" \
  "v_mfma_f32_16x16x32_bf16 v[44:47], v[108:111], a[236:239], v[44:47]\n\t" \
  "v_mfma_f32_16x16x32_bf16 v[44:47], v[116:119], a[240:243], v[44:47]\n\t" \
  "v_mfma_f32_16x16x32_bf16 v[44:47], v[124:127], a[244:247], v[44:47]\n\t" \
  "v_mfma_f32_16x16x32_bf16 v[44:47], v[132:135], a[248:251], v[44:47]\n\t" \
  "s_add_u32 m0, s46, 28672\n\t" \
  "s_add_u32 s47, s55, 53248\n\t" \
  "buffer_load_dwordx4 v0, %[X], s47 offen sc1 lds\n\t" \
  "v_mfma_f32_16x16x32_bf16 v[44:47], v[140:143], a[252:255], v[44:47]\n\t" \
  "v_mfma_f32_16x16x32_bf16 v[44:47], v[148:151], v[224:227], v[44:47]\n\t" \
  "v_mfma_f32_16x16x32_bf16 v[44:47], v[156:159], v[228:231], v[44:47]\n\t" \
  "v_mfma_f32_16x16x32_bf16 v[44:47], v[164:167], v[232:235], v[44:47]\n\t" \
  "s_add_u32 m0, s46, 32768\n\t" \
  "s_add_u32 s47, s55, 65536\n\t" \
  "buffer_load_dwordx4 v0, %[X], s47 offen sc1 lds\n\t" \
  "v_mfma_f32_16x16x32_bf16 v[44:47], v[172:175], v[236:239], v[44:47]\n\t" \
  "v_mfma_f32_16x16x32_bf16 v[44:47], v[180:183], v[240:243], v[44:47]\n\t" \
  "v_mfma_f32_16x16x32_bf16 v[44:47], v[188:191], v[244:247], v[44:47]\n\t" \
  "buffer_load_dword v8, off, %[X], s57 sc1\n\t" \
  "v_mfma_f32_16x16x32_bf16 v[44:47], v[196:199], v[248:251], v[44:47]\n\t" \
  "v_mfma_f32_16x16x32_bf16 v[44:47], v[204:207], v[252:255], v[44:47]\n\t" \
  "s_nop 8\n\t" \
  "v_cvt_pk_bf16_f32 v62, v44, v45\n\t" \
  "v_cvt_pk_bf16_f32 v63, v46, v47\n\t" \
  "buffer_store_dwordx4 v[60:63], v0, %[X], s50 offen offset:3072 sc1\n\t" \
  "s_add_u32 s49, s82, s38\n\t" \
  "s_cmp_eq_u32 s48, 63\n\t" \
  "s_cbranch_scc1 LEND_%=\n\t" \
  "s_add_u32 s48, s48, 1\n\t" \
  "s_cmp_eq_u32 s56, 0\n\t" \
  "s_cbranch_scc1 LCOLD_%=\n\t" \
  "s_waitcnt vmcnt(0)\n\t" \
  "s_mov_b64 exec, 1\n\t" \
  "buffer_store_dword v6, off, %[X], s49 sc1\n\t" \
  "s_mov_b64 exec, -1\n\t" \
  "s_mov_b32 s49, 0xf0000000\n\t" \
  "s_cmp_lt_i32 s57, 0\n\t" \
  "s_cbranch_scc1 LTOP_%=\n\t" \
  "s_movk_i32 s63, 4000\n\t" \
  "s_branch LGW_%=u\n\t" \
  "LGW_%=:\n\t" \
  "s_sleep " TK_PSG "\n\t" \
  "buffer_load_dword v8, off, %[X], s57 sc1\n\t" \
  "s_waitcnt vmcnt(0)\n\t" \
  "LGW_%=u:\n\t" \
  "s_memrealtime s[52:53]\n\t" \
  "v_readfirstlane_b32 s60, v8\n\t" \
  "s_waitcnt lgkmcnt(0)\n\t" \
  "s_sub_u32 s63, s63, 1\n\t" \
  "s_cbranch_scc1 LTOP_%=\n\t" \
  "s_cmp_eq_u32 s60, -1\n\t" \
  "s_cbranch_scc1 LGW_%=\n\t" \
  "s_not_b32 s60, s60\n\t" \
  "s_lshl_b32 s61, s52, 8\n\t" \
  "s_sub_u32 s60, s60, s61\n\t" \
  "s_ashr_i32 s60, s60, 8\n\t" \
  "s_sub_i32 s60, s60, " TK_QXW "\n\t" \
  "s_min_i32 s60, s60, 400\n\t" \
  "s_add_u32 s54, s52, s60\n\t" \
  "LGW_%=w:\n\t" \
  "s_sleep 1\n\t" \
  "s_memrealtime s[52:53]\n\t" \
  "s_waitcnt lgkmcnt(0)\n\t" \
  "s_sub_i32 s53, s52, s54\n\t" \
  "s_cmp_lt_i32 s53, 0\n\t" \
  "s_cbranch_scc1 LGW_%=w\n\t" \
  "s_branch LTOP_%=\n\t" \
  "LEND_%=:\n\t" \
  "s_waitcnt vmcnt(0)\n\t" \
  "s_mov_b64 exec, 1\n\t" \
  "buffer_store_dword v6, off, %[X], s49 sc1\n\t" \
  "s_mov_b64 exec, -1\n\t" \
  "s_mov_b32 s49, 0xf0000000\n\t" \
  ""
__device__ __forceinline__ unsigned hoff(int r, int piece) {
  const int m = 4 * (r >> 3) + (r & 3), kp = piece >> 3;
  return (P_H + (unsigned)(kp >> 1)) * PAIR + (unsigned)((kp & 1) * 4096 + (16 * ((r >> 2) & 1) + m) * 128 + (((piece ^ m) & 7) << 4));
}
template <int SLEEP>
__device__ __forceinline__ bool wait_all(rsrc_t X, unsigned vo, unsigned so) {
  unsigned spins = 0;
  while (true) {
    asm volatile("" ::: "memory");
    if (__all(ld4(X, vo, so) != 0u)) break;
    if (spin_fail(spins, X)) break;
    __builtin_amdgcn_s_sleep(SLEEP);
  }
  asm volatile("" ::: "memory");
  return spins != 0u;
}
#define VQ_CLOB "v8", "v16", "v17", "v18", "v19", "v20", "v21", "v22", "v23", "v24", "v25", "v26", "v27", "v28", "v29", "v30", "v31", "v32", "v33", "v34", "v35", "v36", "v37", "v38", "v39", "v40", "v41", "v42", "v43", "v44", "v45", "v46", "v47", "v48", "v49", "v50", "v51", "v52", "v53", "v54", "v55", "v56", "v57", "v58", "v59", "v60", "v61", "v62", "v63", "v64", "v65", "v66", "v67", "v68", "v69", "v70", "v71", "v72", "v73", "v74", "v75", "v76", "v77", "v78", "v79", "v80", "v81", "v82", "v83", "v84", "v85", "v86", "v87", "v88", "v89", "v90", "v91", "v92", "v93", "v94", "v95", "v96", "v97", "v98", "v99", "v100", "v101", "v102", "v103", "v104", "v105", "v106", "v107", "v108", "v109", "v110", "v111", "v112", "v113", "v114", "v115", "v116", "v117", "v118", "v119", "v120", "v121", "v122", "v123", "v124", "v125", "v126", "v127", "v128", "v129", "v130", "v131", "v132", "v133", "v134", "v135", "v136", "v137", "v138", "v139", "v140", "v141", "v142", "v143", "v144", "v145", "v146", "v147", "v148", "v149", "v150", "v151", "v152", "v153", "v154", "v155", "v156", "v157", "v158", "v159", "v160", "v161", "v162", "v163", "v164", "v165", "v166", "v167", "v168", "v169", "v170", "v171", "v172", "v173", "v174", "v175", "v176", "v177", "v178", "v179", "v180", "v181", "v182", "v183", "v184", "v185", "v186", "v187", "v188", "v189", "v190", "v191", "v192", "v193", "v194", "v195", "v196", "v197", "v198", "v199", "v200", "v201", "v202", "v203", "v204", "v205", "v206", "v207", "v220", "v221"
#define VA_CLOB "v40", "v41", "v42", "v43", "v44", "v45", "v46", "v47", "v48", "v49", "v50", "v51", "v52", "v53", "v54", "v55", "v56", "v57", "v58", "v59", "v60", "v61", "v62", "v63", "v64", "v65", "v66", "v67", "v68", "v69", "v70", "v71", "v72", "v73", "v74", "v75", "v76", "v77", "v78", "v79", "v80", "v81", "v82", "v83", "v84", "v85", "v86", "v87", "v88", "v89", "v90", "v91", "v92", "v93", "v94", "v95", "v96", "v97", "v98", "v99", "v100", "v101", "v102", "v103", "v104", "v105", "v106", "v107", "v108", "v109", "v110", "v111", "v112", "v113", "v114", "v115", "v116", "v117", "v118", "v119", "v120", "v121", "v122", "v123", "v124", "v125", "v126", "v127", "v128", "v129", "v130", "v131", "v132", "v133", "v134", "v135", "v136", "v137", "v138", "v139", "v140", "v141", "v142", "v143", "v144", "v145", "v146", "v147", "v148", "v149", "v150", "v151", "v152", "v153", "v154", "v155", "v156", "v157", "v158", "v159", "v160", "v161", "v162", "v163", "v164", "v165", "v166", "v167", "v168", "v169", "v170", "v171", "v172", "v173", "v174", "v175", "v176", "v177", "v178", "v179", "v180", "v181", "v182", "v183", "v184", "v185", "v186", "v187", "v188", "v189", "v190", "v191", "v192", "v193", "v194", "v195", "v196", "v197", "v198", "v199", "v200", "v201", "v202", "v203", "v204", "v205", "v206", "v207", "v208", "v209", "v210", "v211", "v220", "v221"
#define TOK_COMMON() \
  const MP p = getp(); \
  const int c = (int)blockIdx.x - NCHW, xq = c & 3, L = (c >> 2) & 1, i = c >> 3; \
  const int tid = threadIdx.x, lane = tid & 63, w = __builtin_amdgcn_readfirstlane(tid >> 6); \
  const rsrc_t X = mkr(p.xb, XB_BYTES)
#define W_IN [W] "s"(WR)
#define W_CLOB "a0", "a1", "a2", "a3", "a4", "a5", "a6", "a7", "a8", "a9", "a10", "a11", "a12", "a13", "a14", "a15", "a16", "a17", "a18", "a19", "a20", "a21", "a22", "a23", "a24", "a25", "a26", "a27", "a28", "a29", "a30", "a31", "a32", "a33", "a34", "a35", "a36", "a37", "a38", "a39", "a40", "a41", "a42", "a43", "a44", "a45", "a46", "a47", "a48", "a49", "a50", "a51", "a52", "a53", "a54", "a55", "a56", "a57", "a58", "a59", "a60", "a61", "a62", "a63", "a64", "a65", "a66", "a67", "a68", "a69", "a70", "a71", "a72", "a73", "a74", "a75", "a76", "a77", "a78", "a79", "a80", "a81", "a82", "a83", "a84", "a85", "a86", "a87", "a88", "a89", "a90", "a91", "a92", "a93", "a94", "a95", "a96", "a97", "a98", "a99", "a100", "a101", "a102", "a103", "a104", "a105", "a106", "a107", "a108", "a109", "a110", "a111", "a112", "a113", "a114", "a115", "a116", "a117", "a118", "a119", "a120", "a121", "a122", "a123", "a124", "a125", "a126", "a127", "a128", "a129", "a130", "a131", "a132", "a133", "a134", "a135", "a136", "a137", "a138", "a139", "a140", "a141", "a142", "a143", "a144", "a145", "a146", "a147", "a148", "a149", "a150", "a151", "a152", "a153", "a154", "a155", "a156", "a157", "a158", "a159", "a160", "a161", "a162", "a163", "a164", "a165", "a166", "a167", "a168", "a169", "a170", "a171", "a172", "a173", "a174", "a175", "a176", "a177", "a178", "a179", "a180", "a181", "a182", "a183", "a184", "a185", "a186", "a187", "a188", "a189", "a190", "a191", "a192", "a193", "a194", "a195", "a196", "a197", "a198", "a199", "a200", "a201", "a202", "a203", "a204", "a205", "a206", "a207", "a208", "a209", "a210", "a211", "a212", "a213", "a214", "a215", "a216", "a217", "a218", "a219", "a220", "a221", "a222", "a223", "a224", "a225", "a226", "a227", "a228", "a229", "a230", "a231", "a232", "a233", "a234", "a235", "a236", "a237", "a238", "a239", "a240", "a241", "a242", "a243", "a244", "a245", "a246", "a247", "a248", "a249", "a250", "a251", "a252", "a253", "a254", "a255", "v224", "v225", "v226", "v227", "v228", "v229", "v230", "v231", "v232", "v233", "v234", "v235", "v236", "v237", "v238", "v239", "v240", "v241", "v242", "v243", "v244", "v245", "v246", "v247", "v248", "v249", "v250", "v251", "v252", "v253", "v254", "v255"
#define BOOT_IN "{s67}"(L), [ctab] "i"(CTAB_OFF), [rm] "i"(SRING == 12 ? 12 : 0), [trm] "i"(TRING - 1), [trn] "i"(1 - TRING)
#define S_CLOB "s87", "s80", "s81", "s76", "s78", "s79", "s86", "s88", "s89", "s90", "s91", "s46", "s47", "s48", "s49", "s50", "s51", "s52", "s53", "s54", "s55", "s56", "s57", "s58", "s59", "s60", "s61", "s62", "s63", "s68", "s69", "s70", "s71", "s72", "s73", "s82", "s83", "s84", "m0", "vcc", "scc", "memory"
typedef unsigned v8w __attribute__((ext_vector_type(8)));

__device__ __forceinline__ void qkv_role() {
  TOK_COMMON();
  const int role = i * 4 + w, hh = 8 * xq + role / 6, part = role % 6, which = part >> 1, half = part & 1;
  const rsrc_t WR = mkr((which == 0 ? p.Wq : (which == 1 ? p.Wk : p.Wv)) + hh * DH + 64 * half, DM * HD * 2u - (unsigned)(hh * DH + 64 * half) * 2u);
  const int m = lane & 15, g = lane >> 4;
  const unsigned a0 = (unsigned)(m * 128 + ((g ^ (m & 7)) << 4));
  v8w lv = {(unsigned)lane * 16u, a0, a0 ^ 64u, (unsigned)(lane & (L ? 15 : 3)) * 128u, 0u, 40960u + 4u * (unsigned)w, 1u, 0u};
  const unsigned c36 = P_H * PAIR + (unsigned)w * 1024u, c37 = (unsigned)(3 * hh + which) * PAIR + (unsigned)half * (which == 2 ? 4096u : 2048u);
  const unsigned c38 = P_FA * PAIR + (unsigned)(hh * 128 + part * 16), c39 = w == 0 ? P_FB * PAIR + 4096u : 0xF0000000u, c41 = (unsigned)w * 1024u;
  const int c42 = L ? 1 - QLEAD1 : 1 - QLEAD;
  unsigned d74 = TRC_OFF + 262144u + (unsigned)((c * 4 + w) * 66) * 8u;
#define Q_CALL(A) asm volatile("s_waitcnt vmcnt(0) lgkmcnt(0)\n\t" A : "+{v[0:7]}"(lv), "+{s74}"(d74) \
               : [X] "s"(X), "{s36}"(c36), "{s37}"(c37), "{s38}"(c38), "{s39}"(c39), "{s40}"(w), "{s41}"(c41), "{s42}"(c42), "{s45}"(i), "{s92}"(TMO_OFF), BOOT_IN, [cap] "i"(SPIN_CAP), [qxw] "i"(QXW), [qxc] "i"(QXC), W_IN : VQ_CLOB, W_CLOB, S_CLOB)
  if (which == 2) { Q_CALL(QV_ASM "LQVEND:"); __builtin_amdgcn_endpgm(); }
  Q_CALL(QK_ASM "LQKEND:");
  __builtin_amdgcn_endpgm();
}

__device__ __forceinline__ void ap_role() {
  TOK_COMMON();
  const int hp = 4 * xq + ((i - 12) >> 1), rt = i & 1, a = w >> 1, dh = w & 1, h = 2 * hp + a;
  const int n = lane & 15, g = lane >> 4;
  const rsrc_t WR = mkr(p.Wo + (long)hp * 2 * DH * DM, 2u * DH * DM * 2u);
  const unsigned l16 = (unsigned)lane * 16u, ab = (unsigned)a * 20480u + l16;
  v16u lv = {l16, ab, ab + 12288u + (unsigned)dh * 4096u, 49152u + (unsigned)w * 2048u + l16, 49152u + (unsigned)(w ^ 1) * 2048u + l16, 0u, (l16 >> 1) + (unsigned)dh * 1024u,
             (unsigned)(g * 262144 + n * 16), (unsigned)(n * 32 + g * 8), (unsigned)((lane & 7) % 6) * 16u, 0u, 0u, 0u, 0u, 0u, 0u};
  v8w mk;
  const int qi = 8 * (n >> 2) + (n & 3) + 4 * rt;
#pragma unroll
  for (int k = 0; k < 8; ++k) mk[k] = 8 * g + k > qi ? 0xFF800000u : 0u;
  v16u zz = {0u, 0u, 0u, 0u, 0u, 0u, 0u, 0u, 0u, 0u, 0u, 0u, 0u, 0u, 0u, 0u};
  const unsigned tb = (unsigned)(3 * h) * PAIR;
  const unsigned c36 = tb + (unsigned)(rt * 4096 + dh * 2048), c37 = tb + PAIR + (unsigned)dh * 4096u, c38 = c37 + PAIR;
  const unsigned c39 = (P_KV + (unsigned)(h >> 1)) * PAIR + (unsigned)(h & 1) * 4096u, c41 = P_FA * PAIR + (unsigned)h * 128u;
  const unsigned c42 = w == 0 ? P_FB * PAIR + (unsigned)(2 * hp + rt) * 128u : 0xF0000000u;
  const unsigned c43 = TP0 * XBLK + (unsigned)(8 * rt + (hp >> 3)) * PAIR + (unsigned)((hp & 7) * 1024 + w * 256);
  unsigned d44 = (TP0 + 8u) * XBLK + (unsigned)(hp >> 1) * PAIR + (unsigned)((((hp & 1) * 4 + w) * 2 + rt) * 512);
  const unsigned c45 = (unsigned)(a * 20480 + dh * 4096);
  unsigned d74 = TRC_OFF + 262144u + (unsigned)((c * 4 + w) * 66) * 8u;
  asm volatile("s_waitcnt vmcnt(0) lgkmcnt(0)\n\t" AP_ASM "LAPEND:" : "+{v[0:15]}"(lv), "+{v[24:39]}"(zz), "+{s74}"(d74), "+{s44}"(d44)
               : [X] "s"(X), "{s36}"(c36), "{s37}"(c37), "{s38}"(c38), "{s39}"(c39), "{s41}"(c41), "{s42}"(c42), "{s43}"(c43), "{s45}"(c45), "{s64}"(256 * w), "{s65}"(1024 + 32 * w), "{s66}"(2 * w), "{s85}"(a * 20480 + dh * 2048), "{s77}"(L ? AX1 : AX), BOOT_IN,
                 "{s92}"(TMO_OFF), [cap] "i"(SPIN_CAP), "{v[16:23]}"(mk), W_IN : VA_CLOB, W_CLOB, S_CLOB);
  __builtin_amdgcn_endpgm();
}

template <int N> __device__ __forceinline__ float rorN(float v) { return __uint_as_float((unsigned)__builtin_amdgcn_update_dpp(0, (int)__float_as_uint(v), 0x120 + N, 0xf, 0xf, false)); }
__device__ __forceinline__ void up8(v4u v, float* x) {
  x[0] = lo2f(v.x); x[1] = hi2f(v.x); x[2] = lo2f(v.y); x[3] = hi2f(v.y); x[4] = lo2f(v.z); x[5] = hi2f(v.z); x[6] = lo2f(v.w); x[7] = hi2f(v.w);
}
__device__ __forceinline__ v4u pk8(const float* x) { return (v4u){pk2(x[0], x[1]), pk2(x[2], x[3]), pk2(x[4], x[5]), pk2(x[6], x[7])}; }

__device__ __forceinline__ void rb9(float (&x)[9]) {
#pragma unroll
  for (int i = 0; i < 8; i += 2) { const unsigned u = pk2(x[i], x[i + 1]); x[i] = lo2f(u); x[i + 1] = hi2f(u); }
  x[8] = lo2f(pk2(x[8], x[8]));
}
template <int R>
__device__ __forceinline__ void lnR(const float (&x)[R][9], float (&o)[R][9]) {
  float s[2 * R], pv[R];
#pragma unroll
  for (int r = 0; r < R; ++r) {
    pv[r] = __uint_as_float(__builtin_amdgcn_readfirstlane(__float_as_uint(x[r][0])));
    const f32x2 p2 = {pv[r], pv[r]};
    f32x2 d[4];
#pragma unroll
    for (int i = 0; i < 4; ++i) d[i] = (f32x2){x[r][2 * i], x[r][2 * i + 1]} - p2;
    const float d8 = x[r][8] - pv[r];
    const f32x2 a = (d[0] + d[1]) + (d[2] + d[3]);
    const f32x2 q = __builtin_elementwise_fma(d[3], d[3], __builtin_elementwise_fma(d[2], d[2], __builtin_elementwise_fma(d[1], d[1], d[0] * d[0])));
    s[2 * r] = (a[0] + d8) + a[1];
    s[2 * r + 1] = __builtin_fmaf(d8, d8, q[0]) + q[1];
  }
#pragma unroll
  for (int k = 0; k < 2 * R; ++k) s[k] += rorN<8>(s[k]);
#pragma unroll
  for (int k = 0; k < 2 * R; ++k) s[k] += rorN<4>(s[k]);
#pragma unroll
  for (int k = 0; k < 2 * R; ++k) s[k] += rorN<2>(s[k]);
#pragma unroll
  for (int k = 0; k < 2 * R; ++k) s[k] += rorN<1>(s[k]);
#pragma unroll
  for (int r = 0; r < R; ++r) {
    auto u = __builtin_amdgcn_permlane16_swap(__float_as_uint(s[2 * r]), __float_as_uint(s[2 * r + 1]), false, false);
    s[2 * r] = __uint_as_float(u[0]) + __uint_as_float(u[1]);
  }
#pragma unroll
  for (int r = 0; r < R; ++r) {
    const unsigned u = __float_as_uint(s[2 * r]);
    const float md = (__uint_as_float(__builtin_amdgcn_readlane(u, 0)) + __uint_as_float(__builtin_amdgcn_readlane(u, 32))) * (1.0f / DM);
    const float sq = (__uint_as_float(__builtin_amdgcn_readlane(u, 16)) + __uint_as_float(__builtin_amdgcn_readlane(u, 48))) * (1.0f / DM);
    const unsigned mv = pk2(pv[r] + md, __builtin_fmaxf(__builtin_fmaf(-md, md, sq), 0.f));
    const float m = lo2f(mv), var = hi2f(mv);
    const float ve = lo2f(pk2(var + 1e-5f, 0.f));
    const float den = lo2f(pk2(__builtin_amdgcn_sqrtf(ve), 0.f));
    const float inv = __builtin_amdgcn_rcpf(den);
#pragma unroll
    for (int i = 0; i < 9; ++i) o[r][i] = (x[r][i] - m) * inv;
  }
}
__device__ __forceinline__ void ln9(const float (&x)[9], float (&o)[9]) { lnR<1>((const float (&)[1][9])x, (float (&)[1][9])o); }
__device__ __forceinline__ v4u gat8(float v) {
  const unsigned d = pk2(v, __uint_as_float((unsigned)__builtin_amdgcn_mov_dpp((int)__float_as_uint(v), 0xB1, 0xf, 0xf, true)));
  return (v4u){d, (unsigned)__builtin_amdgcn_mov_dpp((int)d, 0x102, 0xf, 0xf, true), (unsigned)__builtin_amdgcn_mov_dpp((int)d, 0x104, 0xf, 0xf, true),
               (unsigned)__builtin_amdgcn_mov_dpp((int)d, 0x106, 0xf, 0xf, true)};
}
__device__ __forceinline__ void tok_row(const MP& p, const rsrc_t X, unsigned sb1, int L, int s, int r, int lane, const v4u (&v)[18], v4u px, v4u py, unsigned px9, unsigned py9) {
  const long tok = (long)s * SEG + r;
  const int m = lane & 15, g = lane >> 4;
  const int xp = 4 * (g & 1) + 2 * (g >> 1) + (m >> 3);
  const unsigned zr = opq(0u);
  const unsigned one = (g == (m >> 2)) ? (opq(0x3F80u) << (16 * (m & 1))) : zr;
  const unsigned ax = (m & 2) ? zr : one, ay = (m & 2) ? one : zr;
  const v4u A1 = {ax, ay, zr, zr}, A2 = {zr, zr, ax, ay};
  const unsigned on2 = opq(0x3F80u) << (16 * (m & 1));
  const int q2 = (m >> 1) & 3;
  const v4u sel = {q2 == 0 ? on2 : zr, q2 == 1 ? on2 : zr, q2 == 2 ? on2 : zr, q2 == 3 ? on2 : zr};
  f32x4 d1, d2, e1;
  asm volatile("s_nop 1\n\t" MF "%0, %1, %3, 0\n\t" MF "%0, %2, %3, %0" : "=&v"(e1) : "v"(v[0]), "v"(v[1]), "v"(sel));
#pragma unroll
  for (int i = 0; i < 16; ++i) {
    if (i == 0) asm volatile("s_nop 1\n\t" MF "%0, %2, %4, 0\n\t" MF "%1, %3, %4, 0" : "=&v"(d1), "=&v"(d2) : "v"(A1), "v"(A2), "v"(v[2]));
    else asm volatile(MF "%0, %2, %4, %0\n\t" MF "%1, %3, %4, %1" : "+v"(d1), "+v"(d2) : "v"(A1), "v"(A2), "v"(v[2 + i]));
  }
  asm volatile("s_nop 7" : "+v"(d1), "+v"(d2), "+v"(e1));
  float o[9];
#pragma unroll
  for (int i = 0; i < 4; ++i) { o[i] = d1[i]; o[4 + i] = d2[i]; }
  {
    auto a02 = __builtin_amdgcn_permlane32_swap(__float_as_uint(e1[0]), __float_as_uint(e1[2]), false, false);
    auto a13 = __builtin_amdgcn_permlane32_swap(__float_as_uint(e1[1]), __float_as_uint(e1[3]), false, false);
    o[8] = m >= 8 ? __uint_as_float(a13[0]) + __uint_as_float(a13[1]) : __uint_as_float(a02[0]) + __uint_as_float(a02[1]);
  }
  float x[9];
  up8(px, x); x[8] = lo2f(px9);
#pragma unroll
  for (int i = 0; i < 9; ++i) x[i] += o[i];
  if (L == 0) {
    float y[9], ln[9];
    up8(py, y); y[8] = lo2f(py9);
    rb9(x);
    ln9(x, ln);
#pragma unroll
    for (int i = 0; i < 9; ++i) x[i] = __builtin_fmaf(ln[i], y[i], x[i]);
    rb9(x);
    st16(X, (unsigned)lane * 16u, sb1 + (P_X3 + (unsigned)(r >> 3)) * PAIR + (unsigned)(r & 7) * 1024u, pk8(x));
    const v4u x9 = gat8(x[8]);
    if ((lane & 7) == 0) st16(X, (unsigned)xp * 16u, sb1 + (P_H + 4u) * PAIR + 4096u + (unsigned)r * 128u, x9);
    ln9(x, ln);
    st16(X, hoff(r, lane), sb1, pk8(ln));
    const v4u h9 = gat8(ln[8]);
    if ((lane & 7) == 0) st16(X, hoff(r, 64 + xp), sb1, h9);
  } else {
    const rsrc_t O = mkr(p.out + tok * DM, DM * 2u);
    __builtin_amdgcn_raw_buffer_store_b128(pk8(x), O, (unsigned)lane * 16u, 0u, OAUX);
    __builtin_amdgcn_raw_buffer_store_b128(gat8(x[8]), O, (lane & 7) == 0 ? 1024u + (unsigned)xp * 16u : 0xF0000000u, 0u, OAUX);
  }
}

__device__ __forceinline__ void row_role(char* smem) {
  TOK_COMMON();
  const int rw = 4 * xq + (i - 20), r = 2 * rw + (w >> 1);
  const unsigned long long sm = die_mask(X, SG0, L, lane);
  const unsigned tm = (unsigned)die_mask(X, TP0, L, lane), dm = (unsigned)die_mask(X, 0u, L, lane);
  __attribute__((address_space(3))) unsigned* cnt = (__attribute__((address_space(3))) unsigned*)smem;
  BAR();
  if (tid < 64) cnt[tid] = 0u;
  BAR();
  bool have = false, early = false;
  for (int s = w & 1; s < NSEG; s += 2) {
    int ln_ = lane; asm volatile("" : "+v"(ln_));
    const int m = ln_ & 15, g = ln_ >> 4;
    const unsigned l16 = (unsigned)ln_ * 16u, xo = (unsigned)(8 * (4 * (g & 1) + 2 * (g >> 1) + (m >> 3)) + (m & 7)) * 2u;
    const unsigned sb = SEGB(sm, s), sb1 = sb ^ (L ? 0u : 8192u);
    const unsigned fho = P_FB * PAIR + 4096u + (unsigned)(L ? rw : r >> 3) * 128u;
    if (!have) wait_all<PSL>(X, 0u, sb + fho);
    const v4u px = ld16(X, l16, sb + (P_X3 + (unsigned)(r >> 3)) * PAIR + (unsigned)(r & 7) * 1024u);
    const unsigned px9 = __builtin_amdgcn_raw_buffer_load_b16(X, xo, sb + (P_H + 4u) * PAIR + 4096u + (unsigned)r * 128u, 16);
    const rsrc_t YX = mkr(p.yT + (2 * s + (r >> 4)) * 9216, L ? 0u : 18432u);
    const v2u ya = ld8(YX, (unsigned)(ln_ * 256 + ((r ^ m) & 15) * 8), 0u), yb = ld8(YX, (unsigned)(ln_ * 256 + 128 + ((r ^ m) & 15) * 8), 0u);
    const v4u py = {ya.x, ya.y, yb.x, yb.y};
    const unsigned py9 = __builtin_amdgcn_raw_buffer_load_b16(YX, 16384u + (xo >> 3) * 128u + (((unsigned)r ^ (xo >> 4)) & 15u) * 8u + (xo & 6u), 0u, 16);
    if (early && !L) __builtin_amdgcn_s_sleep(ARMD0);
    if (s + ARMK < NSEG) {
      const unsigned sv = opq(SENT), sl = SLOT_AT((s + ARMK) % SRING) + ((dm >> ((s + ARMK) % SRING / 3)) & 1u) * 8192u;
      for (int j = r; j < 84; j += 32) {
#pragma unroll
        for (int k = 0; k < 4; ++k) st16(X, l16 + (unsigned)k * 1024u, sl + (unsigned)(j >> 1) * PAIR + (unsigned)(j & 1) * 4096u, (v4u){sv, sv, sv, sv});
      }
    }
    if (s < NSEG - 1) {
      early = wait_all<PSP>(X, 0u, sb + P_FB * PAIR + (unsigned)((r & 24) + 2 * (r & 3) + ((r >> 2) & 1)) * 128u);
      __builtin_amdgcn_s_sleep(RONE);
    } else {
      wait_all<PSP>(X, 0u, sb + P_FA * PAIR + 4096u + (unsigned)r * 128u);
      wait_all<1>(X, (unsigned)(ln_ & 15) * 256u, sb + P_FB * PAIR + (unsigned)((r >> 2) & 1) * 128u);
    }
    if (s < NSEG - 1) wait_all<PSP>(X, (unsigned)(ln_ & 15) * 256u, sb + P_FB * PAIR + (unsigned)((r >> 2) & 1) * 128u);
    const unsigned j = (unsigned)s & 15u, to = TP0 * XBLK + (j << 20) + ((tm >> (j >> 1)) & 1u) * 8192u + (unsigned)r * 2u * PAIR;
    const unsigned t9 = (TP0 + 8u) * XBLK + ((tm >> 8) & 1u) * 8192u + (j << 17) + (unsigned)(((r >> 2) & 1) * 512 + (4 * (r >> 3) + (r & 3)) * 32);
    const unsigned v9 = (unsigned)(g * (int)PAIR + (m >> 1) * 1024 + (m & 1) * 16);
    v4u v[18];
#pragma unroll
    for (int k = 0; k < 2; ++k) v[k] = ld16(X, v9, t9 + (unsigned)k * 4u * PAIR);
#pragma unroll
    for (int k = 0; k < 16; ++k) v[2 + k] = ld16(X, l16 + (unsigned)(k & 3) * 1024u, to + (unsigned)(k >> 3) * PAIR + (unsigned)(k & 4) * 1024u);
    const unsigned nx = ld4(X, 0u, s + 2 < NSEG ? SEGB(sm, s + 2) + fho : 0xF0000000u);
    SB();
    tok_row(p, X, sb1, L, s, r, ln_, v, px, py, px9, py9);
    have = __builtin_amdgcn_readfirstlane(nx) != 0u;
    VM0();
    unsigned o1 = 0u;
    if (ln_ == 0) o1 = __hip_atomic_fetch_add(cnt + s, 1u, __ATOMIC_RELAXED, __HIP_MEMORY_SCOPE_WORKGROUP);
    if (__builtin_amdgcn_readfirstlane(o1) == 1u && ln_ < 2 - L) st4(X, ln_ ? sb1 : sb + 2048u, P_FB * PAIR + 4096u + (unsigned)rw * 128u, opq(1u));
  }
}

__device__ __forceinline__ void rows_task(const MP& p, const rsrc_t X, unsigned long long sm, u16* ys, int grp, int ng) {
  const int tid = threadIdx.x, lane = tid & 63, w = __builtin_amdgcn_readfirstlane(tid >> 6);
  const int m = lane & 15, g = lane >> 4;
  const int xp = 4 * (g & 1) + 2 * (g >> 1) + (m >> 3), c9 = 512 + 8 * xp + (m & 7);
  const unsigned sb = SEGB(sm, grp >> 2);
  v4u yv[5], xa[4];
  unsigned x9[4];
#pragma unroll
  for (int i = 0; i < 5; ++i) yv[i] = ld16(mkr(p.yT + (grp >> 1) * 9216, 18432u), (unsigned)(tid + 256 * i) * 16u, 0u);
#pragma unroll
  for (int rr = 0; rr < 4; ++rr) {
    const long t = grp * 8 + w + 4 * (rr & (2 * ng - 1));
    xa[rr] = *(const v4u*)&p.xin[t * DM + 8 * lane]; x9[rr] = p.xin[t * DM + c9];
  }
#pragma unroll
  for (int i = 0; i < 5; ++i) ((v4u*)ys)[tid + 256 * i] = yv[i];
  BAR();
  const unsigned o9 = (lane & 7) == 0 ? 0u : 0xF0000000u;
#pragma unroll 1
  for (int k = 0; k < ng; ++k) {
    float x[2][9], y[2][9], ln[2][9];
    const int t0 = (grp + k) * 8 + w;
#pragma unroll
    for (int rr = 0; rr < 2; ++rr) {
      const int j = (t0 + 4 * rr) & 15;
      const v2u ya = *(const v2u*)&ys[lane * 128 + (j ^ m) * 4], yb = *(const v2u*)&ys[lane * 128 + 64 + (j ^ m) * 4];
      up8(xa[rr], x[rr]); x[rr][8] = lo2f(x9[rr]); up8((v4u){ya.x, ya.y, yb.x, yb.y}, y[rr]); y[rr][8] = bf2f(ys[(c9 >> 2) * 64 + (j ^ xp) * 4 + (m & 3)]);
      xa[rr] = xa[2 + rr]; x9[rr] = x9[2 + rr];
    }
    lnR<2>(x, ln);
#pragma unroll
    for (int rr = 0; rr < 2; ++rr) {
#pragma unroll
      for (int i = 0; i < 9; ++i) x[rr][i] = __builtin_fmaf(ln[rr][i], y[rr][i], x[rr][i]);
      rb9(x[rr]);
      const int r = (t0 + 4 * rr) & 31;
      st16(X, (unsigned)lane * 16u, sb + (P_X3 + (unsigned)(r >> 3)) * PAIR + (unsigned)(r & 7) * 1024u, pk8(x[rr]));
      st16(X, (unsigned)xp * 16u + o9, sb + (P_H + 4u) * PAIR + 4096u + (unsigned)r * 128u, gat8(x[rr][8]));
    }
    lnR<2>(x, ln);
#pragma unroll
    for (int rr = 0; rr < 2; ++rr) {
      const int r = (t0 + 4 * rr) & 31;
      st16(X, hoff(r, lane), sb, pk8(ln[rr]));
      st16(X, hoff(r, 64 + xp) + o9, sb, gat8(ln[rr][8]));
    }
  }
  VM0();
  BAR();
  if (tid < ng) st4(X, (unsigned)tid * 128u, sb + P_FB * PAIR + 4096u + (unsigned)(grp & 3) * 128u, opq(1u));
}

__device__ __forceinline__ void arrive(const MP& p, int k, int grp) {
  VM0();
  BAR();
  if (threadIdx.x < 64) {
    unsigned old = 0u;
    if (threadIdx.x == 0) old = __hip_atomic_fetch_add(p.xb + MF_OFF / 4u + (k * 16 + grp) * 32, 1u, __ATOMIC_RELAXED, __HIP_MEMORY_SCOPE_AGENT);
    if (__builtin_amdgcn_readfirstlane(old) == 15u && threadIdx.x < 8) st4(mkr(p.xb, XB_BYTES), threadIdx.x * 128u, RDY_OFF(k) + (unsigned)grp * 1024u, opq(1u));
  }
}
__device__ __forceinline__ void wait_ready(const MP& p, int k) {
  if (threadIdx.x < 64) wait_all<PSL>(mkr(p.xb, XB_BYTES), (threadIdx.x % 9u) * 1024u, RDY_OFF(k) + (blockIdx.x & 7u) * 128u);
  BAR();
}
__device__ __forceinline__ void start_work(char* smem, int wk) {
  const MP p = getp();
  const int w = __builtin_amdgcn_readfirstlane(threadIdx.x >> 6);
#define NARM (ARMK - 3u)
  unsigned pf[PFM + 1];
  conv_task(p.kvT, p.ssm_k, p.ssm_D, p.yT, (u16*)(smem + w * 32768), 4 * wk + w, [&]() {
#pragma unroll
    for (int i = 0; i < PFM; ++i) {
      const int z = (i - 1) * 36864 + wk * 256 + threadIdx.x, a = z % 3;
      pf[i] = *(const unsigned*)(i == 0 ? p.Wo + (long)(z + 36864) * 64 : (a == 0 ? p.Wq : (a == 1 ? p.Wk : p.Wv)) + (long)(36863 - z / 3) * 64);
    }
  }, [&]() {
    const unsigned sv = opq(SENT);
    constexpr unsigned S16 = SLOT / 16u;
#pragma unroll
    for (unsigned k = 0; k < (NARM * S16 + 32767u) / 32768u; ++k) {
      const unsigned i = k * 32768u + wk * 256 + threadIdx.x;
      st16(mkr(p.xb, XB_BYTES), i < NARM * S16 && wk < 128 ? SLOT_AT(3u + i / S16) + (i % S16) * 16u : 0xF0000000u, 0u, (v4u){sv, sv, sv, sv});
    }
  });
#pragma unroll
  for (int i = 0; i < PFM; ++i) asm volatile("" :: "v"(pf[i]));
  arrive(p, 0, wk >> 4);
}
__device__ __forceinline__ void token_start(char* smem) {
  TOK_COMMON();
  (void)w;
  const int k = 4 * i + xq;
  if (L || (i >= 12 && i < 16)) start_work(smem, L ? 32 + k : 80 + k);
  if (i >= (L ? 12 : 20)) {
    const unsigned long long sm = die_mask(X, SG0, 0, lane);
    int g = L ? 111 - k : k - 96;
    while (g < 120) {
      if (g >= 0 && g < 64) {
        wait_ready(p, 0);
      }
      rows_task(p, X, sm, (u16*)smem, g < 0 ? g + 16 : 16 + 2 * g, g < 0 ? 1 : 2);
      g += g < 0 ? 16 : (L ? 48 : 120);
    }
  }
}
__device__ __forceinline__ void token_wg(char* smem) {
  if ((unsigned)(size_t)(__attribute__((address_space(3))) char*)smem != 0u) return;
  token_start(smem);
  const int i = ((int)blockIdx.x - NCHW) >> 3;
  if (i < 12) qkv_role();
  else if (i < 20) ap_role();
  else row_role(smem);
}

__global__ __launch_bounds__(256, 1) void k_main(MP) {
  extern __shared__ __attribute__((aligned(16))) char smem[];
  asm volatile("LKBEG:");
  if ((int)blockIdx.x < NCHW) chain_wg(smem);
  else token_wg(smem);
}

extern "C" void kernel_launch(void* const* d_in, const int* in_sizes, int n_in, void* d_out, int out_size, void* d_ws,
                              size_t ws_size, hipStream_t stream) {
  const u16* A_q = (const u16*)d_in[0];
  const u16* A_k = (const u16*)d_in[1];
  const u16* A_v = (const u16*)d_in[2];
  const u16* A_shift = (const u16*)d_in[3];
  const u16* A_ssmk = (const u16*)d_in[4];
  const u16* A_ssmD = (const u16*)d_in[5];
  const u16* B_mem0 = (const u16*)d_in[7];
  const u16* B_Wq = (const u16*)d_in[8];
  const u16* B_Wk = (const u16*)d_in[9];
  const u16* B_Wv = (const u16*)d_in[10];
  const u16* B_Wo = (const u16*)d_in[11];

  char* ws = (char*)d_ws;
  size_t off = (size_t)(-(uintptr_t)ws) & (XBLK - 1u);
  auto alloc = [&](size_t bytes) { char* q = ws + off; off += (bytes + 4095) & ~(size_t)4095; return q; };
  unsigned* xb = (unsigned*)alloc((size_t)XB_BYTES);
  u16* kvT = (u16*)alloc((size_t)DM * S_LEN * 2);
  u16* yT = (u16*)alloc((size_t)DM * S_LEN * 2);

  PreP pp;
  pp.kk = A_k; pp.vv = A_v; pp.shift_k = A_shift; pp.ssm_k = A_ssmk; pp.ssm_D = A_ssmD; pp.yT = yT; pp.kvT = kvT;
  pp.Wq = B_Wq; pp.Wk = B_Wk; pp.Wv = B_Wv; pp.Wo = B_Wo; pp.xb = xb; pp.mem0 = B_mem0;
  k_pre<<<240 + PRE_FILL + 256 + 288, 256, 0, stream>>>(pp);
  MP mp;
  mp.out = (u16*)d_out; mp.xb = xb;
  mp.xin = A_q; mp.yT = yT; mp.kvT = kvT; mp.ssm_k = A_ssmk; mp.ssm_D = A_ssmD;
  mp.Wq = B_Wq; mp.Wk = B_Wk; mp.Wv = B_Wv; mp.Wo = B_Wo;
  const int smem_bytes = 160 * 1024;
  (void)hipFuncSetAttribute((const void*)k_main, hipFuncAttributeMaxDynamicSharedMemorySize, smem_bytes);
  k_main<<<NCHW + NTOKW, 256, smem_bytes, stream>>>(mp);
}
```
